```python
import torch

def _softmax(x, axis=-1):
    return torch.softmax(x, dim=axis)

def _gelu(x):
    return torch.nn.functional.gelu(x, approximate='tanh')

def _silu(x):
    return torch.nn.functional.silu(x)

def _sigmoid(x):
    return torch.sigmoid(x)

def _layernorm(x, axis=-1, eps=1e-05):
    m = x.mean(dim=axis, keepdim=True)
    v = x.var(dim=axis, keepdim=True, unbiased=False)
    return (x - m) / torch.sqrt(v + eps)

def _scatter_add(tgt, idx, src, axis=0):
    return tgt.clone().index_add_(axis, idx, src)

def _segment_sum(src, seg, n):
    out = torch.zeros((n,) + src.shape[1:], dtype=src.dtype)
    return out.index_add_(0, seg, src)

def _pad(x, pw):
    flat = tuple((v for pair in reversed(pw) for v in pair))
    return torch.nn.functional.pad(x, flat)
kernel_name = 'adaLN_cond__sliding_block_cached__moe_arch_mod__prompt'

def setup_inputs():

    _w = lambda a: (lambda t: t.to(torch.bfloat16) if t.is_floating_point() else t)(torch.as_tensor(a))

    def _A_setup():
        B, Sq, D = (1, 2048, 1024)
        Hq, Hkv, Sk, Dh = (32, 4, 2048, 128)
        assert Hq % Hkv == 0, (Hq, Hkv)
        return {'x': _w((lambda *s: torch.randn(*s, dtype=torch.bfloat16) * 0.05)(B, Sq, D)), 'k_cache': _w((lambda *s: torch.randn(*s, dtype=torch.bfloat16) * 3.2)(B, Hkv, Sk, Dh)), 'v_cache': _w((lambda *s: torch.randn(*s, dtype=torch.bfloat16) * 0.4)(B, Hkv, Sk, Dh)), 'wq': _w((lambda *s: torch.randn(*s, dtype=torch.bfloat16) * 3.2)(D, Hq * Dh) * D ** (-0.5)), 'wo': _w((lambda *s: torch.randn(*s, dtype=torch.bfloat16) * 0.4)(Hq * Dh, D) * (Hq * Dh) ** (-0.5))}
    _A_in = _A_setup()

    def _B_setup():
        B, S, D, F, C = (1, 2048, 1024, 3584, 1)
        return {'x': _w((lambda *s: torch.randn(*s, dtype=torch.bfloat16) * 0.05)(B, S, D)), 'w_r': _w((lambda *s: torch.randn(*s, dtype=torch.bfloat16) * 0.05)(D)), 'w_gate': _w((lambda *s: torch.randn(*s, dtype=torch.bfloat16) * 0.05)(D, F)), 'w_up': _w((lambda *s: torch.randn(*s, dtype=torch.bfloat16) * 0.05)(D, F)), 'w_down': _w((lambda *s: torch.randn(*s, dtype=torch.bfloat16) * 0.05)(F, D))}
    _B_in = _B_setup()
    return {'A_x': _A_in['x'], 'A_k_cache': _A_in['k_cache'], 'A_v_cache': _A_in['v_cache'], 'A_wq': _A_in['wq'], 'A_wo': _A_in['wo'], 'B_x': _B_in['x'], 'B_w_r': _B_in['w_r'], 'B_w_gate': _B_in['w_gate'], 'B_w_up': _B_in['w_up'], 'B_w_down': _B_in['w_down'], 'c': _w((lambda *s: torch.randn(*s, dtype=torch.bfloat16) * 0.1)(1, 1024) * 8.0), 'w_mod': _w((lambda *s: torch.randn(*s, dtype=torch.bfloat16) * 0.1)(1024, 6 * 1024) * 1024 ** (-0.5) * 8.0)}

def reference(A_x, A_k_cache, A_v_cache, A_wq, A_wo, B_x, B_w_r, B_w_gate, B_w_up, B_w_down, c, w_mod):

    def _A_ref(x, k_cache, v_cache, wq, wo):
        B, Sq, D = x.shape
        Hq, Dh = (32, 128)
        Hkv, Sk = (k_cache.shape[1], k_cache.shape[2])
        W = min(4096, max(256, Sk // 4))
        rep = Hq // Hkv
        q = (lambda x, *d: x.permute(*d))((x @ wq).reshape(B, Sq, Hq, Dh), 0, 2, 1, 3)
        k = (lambda x, n, axis: torch.repeat_interleave(x, n, dim=axis))(k_cache[:, :, Sk - W:, :], rep, axis=1)
        v = (lambda x, n, axis: torch.repeat_interleave(x, n, dim=axis))(v_cache[:, :, Sk - W:, :], rep, axis=1)
        scores = torch.einsum('bhqd,bhkd->bhqk', q, k) * Dh ** (-0.5)
        p = _softmax(scores, axis=-1)
        o = torch.einsum('bhqk,bhkd->bhqd', p, v)
        return (lambda x, *d: x.permute(*d))(o, 0, 2, 1, 3).reshape(B, Sq, Hq * Dh) @ wo

    def _B_ref(x, w_r, w_gate, w_up, w_down):
        B, S, D = x.shape
        C = 1
        r = x @ w_r
        idx = torch.argsort(-r, axis=-1)[:, :C]
        idx_e = (lambda x, n, axis: torch.repeat_interleave(x, n, dim=axis))(idx.reshape(B, C, 1), D, axis=2)
        x_sel = (lambda x, i, axis: torch.take_along_dim(x, i, dim=axis))(x, idx_e, axis=1)
        g = _silu(x_sel @ w_gate)
        u = x_sel @ w_up
        y = g * u @ w_down
        r_sel = _sigmoid((lambda x, i, axis: torch.take_along_dim(x, i, dim=axis))(r, idx, axis=1))
        y = y * r_sel.reshape(B, C, 1)
        b_off = (torch.arange(B) * S).reshape(B, 1)
        flat_idx = (idx + b_off).reshape(B * C)
        zero = (x * 0.0).reshape(B * S, D)
        upd = _scatter_add(zero, flat_idx, y.reshape(B * C, D), axis=0)
        return x + upd.reshape(B, S, D)
    x = A_x
    mod = (_silu(c) @ w_mod).reshape(c.shape[0], 6, 1, -1)
    sa, ca, ga, sb, cb, gb = (mod[:, 0].clone(), mod[:, 1].clone(), mod[:, 2].clone(), mod[:, 3].clone(), mod[:, 4].clone(), mod[:, 5].clone())
    h = _layernorm(x, axis=-1) * (1 + ca) + sa
    x = x + ga * _A_ref(h, A_k_cache, A_v_cache, A_wq, A_wo)
    h = _layernorm(x, axis=-1) * (1 + cb) + sb
    x = x + gb * _B_ref(h, B_w_r, B_w_gate, B_w_up, B_w_down)
    return x
```

```cpp
#include <hip/hip_runtime.h>
#include <hip/hip_bf16.h>
#include <stdint.h>
#include <utility>

typedef unsigned short u16;
using bf16x8 = __attribute__((ext_vector_type(8))) short;
using s16x4  = __attribute__((ext_vector_type(4))) short;
using f32x16 = __attribute__((ext_vector_type(16))) float;
using u32x16 = __attribute__((ext_vector_type(16))) unsigned;
using f32x4  = __attribute__((ext_vector_type(4))) float;
using f32x2  = __attribute__((ext_vector_type(2))) float;
using u32x4  = __attribute__((ext_vector_type(4))) unsigned;
using u32x2  = __attribute__((ext_vector_type(2))) unsigned;
using u32x8 = __attribute__((ext_vector_type(8))) unsigned;
using f32x8 = __attribute__((ext_vector_type(8))) float;
using f32x32 = __attribute__((ext_vector_type(32))) float;
using u32x32 = __attribute__((ext_vector_type(32))) unsigned;
typedef __bf16 bf16x2_t __attribute__((ext_vector_type(2)));
#define DI __device__ __forceinline__
#define NTMASK 0
#define NT_SEL(bit) ((NTMASK >> (bit)) & 1)
#if NT_SEL(0)
#define NTX " nt"
#else
#define NTX ""
#endif
#if NT_SEL(1)
#define NTM " nt"
#else
#define NTM ""
#endif
#if NT_SEL(2)
#define NTQ " nt"
#else
#define NTQ ""
#endif
#if NT_SEL(3)
#define NTO " nt"
#else
#define NTO ""
#endif
#if NT_SEL(4)
#define NTG " nt"
#else
#define NTG ""
#endif
#if NT_SEL(5)
#define NTD " nt"
#else
#define NTD ""
#endif
#if NT_SEL(6)
#define NTT " nt"
#else
#define NTT ""
#endif
#if NT_SEL(7)
#define NTF " nt"
#else
#define NTF ""
#endif

constexpr int S = 2048, DM = 1024, HQ = 32, HKV = 4, DH = 128, SKV = 2048, WIN = 512, FF = 3584;
constexpr int QD = HQ * DH;
constexpr float QSCALE = 0.12751743082f;

DI float bf2f(u16 v) { return __uint_as_float(((unsigned)v) << 16); }
DI u16 f2bf(float x) { unsigned u = __float_as_uint(x); u += 0x7fffu + ((u >> 16) & 1u); return (u16)(u >> 16); }
DI unsigned cvtpk(float lo, float hi) { f32x2 v = {lo, hi}; bf16x2_t b = __builtin_convertvector(v, bf16x2_t); return __builtin_bit_cast(unsigned, b); }
DI float lo16f(unsigned w) { return __uint_as_float(w << 16); }
DI float hi16f(unsigned w) { return __uint_as_float(w & 0xffff0000u); }
DI float rbf(float x) { return lo16f(cvtpk(x, x)); }
DI f32x2 up2(unsigned w) { return f32x2{lo16f(w), hi16f(w)}; }
DI float dot2(unsigned a, unsigned b, float c) { return __builtin_amdgcn_fdot2_f32_bf16(__builtin_bit_cast(bf16x2_t, a), __builtin_bit_cast(bf16x2_t, b), c, false); }
DI unsigned plo(unsigned a, unsigned b) { return __builtin_amdgcn_perm(b, a, 0x05040100u); }
DI unsigned phi(unsigned a, unsigned b) { return __builtin_amdgcn_perm(b, a, 0x07060302u); }
DI unsigned rfl(unsigned x) { return __builtin_amdgcn_readfirstlane(x); }
template <int CTRL> DI float dpp_f(float v) { return __int_as_float(__builtin_amdgcn_update_dpp(0, __float_as_int(v), CTRL, 0xf, 0xf, true)); }
template <int CTRL> DI unsigned dpp_u(unsigned v) { return (unsigned)__builtin_amdgcn_update_dpp(0, (int)v, CTRL, 0xf, 0xf, true); }
DI float fold16(float a, float b) { auto r = __builtin_amdgcn_permlane16_swap(__float_as_uint(a), __float_as_uint(b), false, false); return __uint_as_float(r[0]) + __uint_as_float(r[1]); }
DI float fold32(float a, float b) { auto r = __builtin_amdgcn_permlane32_swap(__float_as_uint(a), __float_as_uint(b), false, false); return __uint_as_float(r[0]) + __uint_as_float(r[1]); }
DI float sum16(float v) { return fold16(v, v); }
DI float sum32(float v) { return fold32(v, v); }
DI float sum_row16(float v) { v += dpp_f<0xB1>(v); v += dpp_f<0x4E>(v); v += dpp_f<0x141>(v); v += dpp_f<0x140>(v); return v; }
DI float wave_sum(float v) { return sum32(sum16(sum_row16(v))); }
DI unsigned wave_maxu(unsigned v) {
  v = max(v, dpp_u<0xB1>(v)); v = max(v, dpp_u<0x4E>(v)); v = max(v, dpp_u<0x141>(v)); v = max(v, dpp_u<0x140>(v));
  { auto r = __builtin_amdgcn_permlane16_swap(v, v, false, false); v = max(r[0], r[1]); }
  { auto r = __builtin_amdgcn_permlane32_swap(v, v, false, false); v = max(r[0], r[1]); }
  return v;
}
#define GAP() __builtin_amdgcn_sched_barrier(0)
template <int N> using iseq = std::make_integer_sequence<int, N>;
template <int... Is> using ilist = std::integer_sequence<int, Is...>;
typedef __amdgpu_buffer_rsrc_t srd_t;
#define SRD3 0x00020000
DI srd_t mk_srd(const void* p, unsigned bytes) { return __builtin_amdgcn_make_buffer_rsrc((void*)p, (short)0, (int)bytes, SRD3); }
DI unsigned lane_id() { unsigned l; asm volatile("v_mbcnt_lo_u32_b32 %0, -1, 0\n\tv_mbcnt_hi_u32_b32 %0, -1, %0" : "=v"(l)); return l; }
struct Args { const u16 *x, *kc, *vc, *wq, *wo, *w_r, *wg, *wu, *wd, *c, *w_mod; u16* out; char* ws; };
typedef const __attribute__((address_space(4))) Args* argp_t;
DI argp_t args() { argp_t p = (argp_t)__builtin_amdgcn_kernarg_segment_ptr(); asm volatile("" : "+s"(p)); return p; }
#define BARRIER_LGKM() do { asm volatile("s_waitcnt lgkmcnt(0)\n\ts_barrier" ::: "memory"); } while (0)

constexpr unsigned W_EPOCH = 0x260000, W_TMO = 256;
constexpr unsigned W_FLAGH = 0x261000;
constexpr unsigned W_VOTE = 0x2000, W_PROBE = 0x4000;
constexpr unsigned W_GKEY = 0x10000;
constexpr unsigned W_FLAGR = 0x8000;
constexpr unsigned W_FLAGO = 0x20000;
constexpr unsigned W_CANDH = 0x40000;
constexpr unsigned W_STAT = 0x80000;
constexpr unsigned W_YG = 0x100000;
constexpr unsigned W_CANDX = 0x1E0000;
constexpr unsigned W_ST1 = 0x200000;
constexpr unsigned W_MODA = 0x210000;
constexpr unsigned W_MODB = 0x220000;
constexpr unsigned W_SIL = 0x240000;
constexpr unsigned W_HT = 4u << 20;
constexpr unsigned W_OT = 8u << 20;
constexpr unsigned W_XCH = 24u << 20;
constexpr unsigned W_END = 40u << 20;
constexpr unsigned TAGM = 0x40004000u, TAGC = 0x9E3739B9u;
#define SPINCAP 262144
constexpr unsigned kSpinCap = SPINCAP;
#define SL0 1
#define SL1 1
#define SL2 1
#define SL3 1
template <int SL = 1> DI bool spin_fail(unsigned& spins, srd_t ws, unsigned tag) {
  __builtin_amdgcn_s_sleep(SL);
  if (++spins > kSpinCap) { __builtin_amdgcn_raw_buffer_store_b32(tag, ws, W_TMO, 0, 16); return true; }
  return (spins & 255u) == 0u && rfl(__builtin_amdgcn_raw_buffer_load_b32(ws, W_TMO, 0, 16)) == tag;
}
DI u32x4 ld16(srd_t ws, unsigned off) { return __builtin_amdgcn_raw_buffer_load_b128(ws, off, 0, 16); }
DI u32x2 ld8(srd_t ws, unsigned off) { return __builtin_amdgcn_raw_buffer_load_b64(ws, off, 0, 16); }
DI void st16(srd_t ws, unsigned off, u32x4 v) { __builtin_amdgcn_raw_buffer_store_b128(v, ws, off, 0, 16); }
DI void st8(srd_t ws, unsigned off, u32x2 v) { __builtin_amdgcn_raw_buffer_store_b64(v, ws, off, 0, 16); }
DI void st4(srd_t ws, unsigned off, unsigned v) { __builtin_amdgcn_raw_buffer_store_b32(v, ws, off, 0, 16); }
DI void wait_flags(srd_t ws, unsigned off, unsigned tag) {
  const unsigned o = off + (lane_id() & 31u) * 4u;
  for (unsigned spins = 0;;) {
    const unsigned f = __builtin_amdgcn_raw_buffer_load_b32(ws, o, 0, 16);
    if (__all(f == tag)) break;
    if (spin_fail(spins, ws, tag)) break;
  }
  asm volatile("" ::: "memory");
}
template <int OFF, bool DEV = false> DI u32x4 ald16(srd_t s, unsigned vo, unsigned so) {
  u32x4 r;
  if constexpr (DEV) asm volatile("buffer_load_dwordx4 %0, %1, %2, %3 offen offset:%4 sc1" : "=v"(r) : "v"(vo), "s"(s), "s"(so), "i"(OFF) : "memory");
  else               asm volatile("buffer_load_dwordx4 %0, %1, %2, %3 offen offset:%4" NTF : "=v"(r) : "v"(vo), "s"(s), "s"(so), "i"(OFF) : "memory");
  return r;
}
template <int OFF> DI u32x2 ald8(srd_t s, unsigned vo, unsigned so) {
  u32x2 r; asm volatile("buffer_load_dwordx2 %0, %1, %2, %3 offen offset:%4" : "=v"(r) : "v"(vo), "s"(s), "s"(so), "i"(OFF) : "memory"); return r;
}
DI u32x2 ald8d(srd_t s, unsigned vo) { u32x2 r; asm volatile("buffer_load_dwordx2 %0, %1, %2, 0 offen sc1" : "=v"(r) : "v"(vo), "s"(s) : "memory"); return r; }
template <int OFF, bool DEV = false> DI unsigned ald4(srd_t s, unsigned vo, unsigned so) {
  unsigned r;
  if constexpr (DEV) asm volatile("buffer_load_dword %0, %1, %2, %3 offen offset:%4 sc1" : "=v"(r) : "v"(vo), "s"(s), "s"(so), "i"(OFF) : "memory");
  else               asm volatile("buffer_load_dword %0, %1, %2, %3 offen offset:%4" : "=v"(r) : "v"(vo), "s"(s), "s"(so), "i"(OFF) : "memory");
  return r;
}
DI void touch(unsigned& d, const void* base, unsigned vo) { asm volatile("global_load_dword %0, %1, %2" : "+v"(d) : "v"(vo), "s"(base) : "memory"); }
DI void touchq(unsigned& d, const void* base, unsigned vo) { asm volatile("global_load_dword %0, %1, %2" NTQ : "+v"(d) : "v"(vo), "s"(base) : "memory"); }
template <class T> DI T lds_ld(unsigned a) { return *reinterpret_cast<const __attribute__((address_space(3))) T*>(a); }
template <class T> DI void lds_st(unsigned a, T v) { *reinterpret_cast<__attribute__((address_space(3))) T*>(a) = v; }
DI float silu_bf(float v) { return rbf(v * __builtin_amdgcn_rcpf(1.f + __expf(-v))); }
struct Ctx { static constexpr unsigned ldsb = 0; unsigned pm, bid, wid, xcd, j, kvh, hl, rblk, tag, cs, nb; srd_t ws; };
#define TAIL2 1
DI unsigned ffn_j(const Ctx& k) { return TAIL2 ? (k.j < 24 ? k.j : 24 + (k.j & 3)) : min(k.j, 27u); }
DI bool ffn_idle(const Ctx& k) { return TAIL2 ? (k.j >> 2) == 6 + (k.xcd & 1) : k.j >= 28; }
constexpr unsigned UNIT = 16384;
#define STAMPA(I) do { } while (0)
#define STAMPO(I) do { } while (0)
#define STAMP_DECL
#define STAMP(I) do { } while (0)
#define STAMP_FLUSH() do { } while (0)
#define STAMP_ARG
#define STAMP_PASS
#define ACLOB_LO "a0","a1","a2","a3","a4","a5","a6","a7","a8","a9","a10","a11","a12","a13","a14","a15","a16","a17","a18","a19","a20","a21","a22","a23","a24","a25","a26","a27","a28","a29","a30","a31","a32","a33","a34","a35","a36","a37","a38","a39","a40","a41","a42","a43","a44","a45","a46","a47","a48","a49","a50","a51","a52","a53","a54","a55","a56","a57","a58","a59","a60","a61","a62","a63","a64","a65","a66","a67","a68","a69","a70","a71","a72","a73","a74","a75","a76","a77","a78","a79","a80","a81","a82","a83","a84","a85","a86","a87","a88","a89","a90","a91","a92","a93","a94","a95","a96","a97","a98","a99","a100","a101","a102","a103","a104","a105","a106","a107","a108","a109","a110","a111","a112","a113","a114","a115","a116","a117","a118","a119","a120","a121","a122","a123","a124","a125","a126","a127"
#define ACLOB_HI "a128","a129","a130","a131","a132","a133","a134","a135","a136","a137","a138","a139","a140","a141","a142","a143","a144","a145","a146","a147","a148","a149","a150","a151","a152","a153","a154","a155","a156","a157","a158","a159","a160","a161","a162","a163","a164","a165","a166","a167","a168","a169","a170","a171","a172","a173","a174","a175","a176","a177","a178","a179","a180","a181","a182","a183","a184","a185","a186","a187","a188","a189","a190","a191","a192","a193","a194","a195","a196","a197","a198","a199","a200","a201","a202","a203","a204","a205","a206","a207","a208","a209","a210","a211","a212","a213","a214","a215","a216","a217","a218","a219","a220","a221","a222","a223","a224","a225","a226","a227","a228","a229","a230","a231","a232","a233","a234","a235","a236","a237","a238","a239","a240","a241","a242","a243","a244","a245","a246","a247","a248","a249","a250","a251","a252","a253","a254","a255"
#define HOP_DEFS ".set .Lhc00, .Lhs0\n\t.set .Lhs0n, .Lhs4\n\t.set .Lhs4n, .Lhs8\n\t.set .Lhs8n, .Lhr0\n\t.set .Lhc01, .Lhs1\n\t.set .Lhs1n, .Lhs5\n\t.set .Lhs5n, .Lhs9\n\t.set .Lhs9n, .Lhr0\n\t.set .Lhc02, .Lhs2\n\t.set .Lhs2n, .Lhs6\n\t.set .Lhs6n, .Lhs10\n\t.set .Lhs10n, .Lhr0\n\t.set .Lhc03, .Lhs3\n\t.set .Lhs3n, .Lhs7\n\t.set .Lhs7n, .Lhr0\n\t.set .Lhc10, .Lhs11\n\t.set .Lhs11n, .Lht0\n\t.set .Lht0n, .Lhr1\n\t.set .Lhc11, .Lhs12\n\t.set .Lhs12n, .Lht1\n\t.set .Lht1n, .Lhr1\n\t.set .Lhc12, .Lhs13\n\t.set .Lhs13n, .Lhr1\n\t.set .Lhc13, .Lhs14\n\t.set .Lhs14n, .Lhr1\n\t.set .Lhc20, .Lhf\n\t.set .Lhfn, .Lhr2\n\t.set .Lhc21, .Lhr2\n\t.set .Lhc22, .Lhr2\n\t.set .Lhc23, .Lhr2"
#define HOP_PAGE(n) asm volatile("s_branch .Lht" #n "\n\t.p2align 12\n.Lht" #n ":\n\ts_cbranch_execz .Lht" #n "n")
#define PAGE_ALIGN() asm volatile("s_branch .Lpa%=\n\t.p2align 12\n.Lpa%=:" ::: "memory")
#define HOP_FRONT() asm volatile("s_branch .Lhf\n\t.p2align 12\n.Lhf:\n\ts_cbranch_execz .Lhfn")
#define HOP_GO(g, w) asm volatile("s_mov_b64 exec, 0\n\ts_cmp_eq_u32 %0, 0\n\ts_cbranch_scc1 .Lhc" #g "0\n\ts_cmp_eq_u32 %0, 1\n\ts_cbranch_scc1 .Lhc" #g "1\n\ts_cmp_eq_u32 %0, 2\n\ts_cbranch_scc1 .Lhc" #g "2\n\ts_branch .Lhc" #g "3\n.Lhr" #g ":\n\ts_mov_b64 exec, -1" :: "s"(w) : "scc", "memory")
constexpr int GSLOT = 49152;
constexpr unsigned KRS = 256;
struct KVP { srd_t srdK, srdV; unsigned vkb[4], vvb[2], vksrc[4], vvsrc[2], sofk; };
struct H4 {
  srd_t srdW;
  unsigned voffX, voffW[4];
  unsigned xb1, wb[2][8];
  unsigned mX, mW, soX, rot;
  unsigned pfd, fo, fl, fvo;
  u32x32 q[4];
};
#define W4(a) "+v"(a[0]), "+v"(a[1]), "+v"(a[2]), "+v"(a[3])
#define W8(a) W4(a), "+v"(a[4]), "+v"(a[5]), "+v"(a[6]), "+v"(a[7])
constexpr unsigned kXT = 65536, kWT = 64 * QD * 2;
DI void w_dma4(unsigned m0, srd_t srd, const unsigned (&vo)[4], unsigned so) {
  asm volatile("s_mov_b32 m0, %0\n\ts_nop 0\n\tbuffer_load_dwordx4 %1, %5, %6 offen lds\n\tbuffer_load_dwordx4 %2, %5, %6 offen offset:1024 lds\n\t"
               "buffer_load_dwordx4 %3, %5, %6 offen offset:2048 lds\n\tbuffer_load_dwordx4 %4, %5, %6 offen offset:3072 lds"
               :: "s"(m0), "v"(vo[0]), "v"(vo[1]), "v"(vo[2]), "v"(vo[3]), "s"(srd), "s"(so) : "memory");
}
DI void w_voffs(unsigned (&vo)[4], int ldw, unsigned wid, unsigned lane, unsigned k0) {
#pragma unroll
  for (int j = 0; j < 4; ++j) {
    const unsigned k = (4 * wid + j) * 4 + (lane >> 4), c16 = lane & 15, sw = (k & 3) | (((k >> 3) & 1) << 2);
    vo[j] = (k ^ (32 * k0)) * ldw * 2 + ((((c16 >> 1) ^ sw) << 1) + (c16 & 1)) * 16 - 1024 * j;
  }
}
DI void h4_init_addr(H4& c, unsigned ldsbase, unsigned wid, unsigned lane, unsigned k0) {
  const unsigned r16 = lane & 15, g = lane >> 4, q = (lane >> 2) & 3, p = lane & 3;
  c.xb1 = ldsbase + GSLOT + wid * 8192 + k0 * 4096 + g * 1024 + r16 * 16;
#pragma unroll
  for (int sl = 0; sl < 2; ++sl)
#pragma unroll
    for (int nb = 0; nb < 8; ++nb) { c.wb[sl][nb] = ldsbase + sl * 65536 + 32768 + (8 * g + q) * 256 + ((nb ^ (q | ((g & 1) << 2))) << 5) + p * 8; asm volatile("" : "+v"(c.wb[sl][nb])); }
}
#define FSTP(x)
#define FSTS(x)
#define FQ_STV
#define FQ_STC
#define FQ_ASM \
  "s_mov_b32 s98, 0xffff0000\n\t" \
  "ds_read_b128 v[128:131], v208 offset:0\n\t" \
  "ds_read_b128 v[132:135], v208 offset:16\n\t" \
  "ds_read_b128 v[136:139], v208 offset:256\n\t" \
  "ds_read_b128 v[140:143], v208 offset:272\n\t" \
  "ds_read_b128 v[24:27], v204 offset:49152\n\t" \
  "ds_read_b128 v[56:59], v204 offset:51200\n\t" \
  "ds_read_b128 v[88:91], v204 offset:53248\n\t" \
  "ds_read_b128 v[120:123], v204 offset:55296\n\t" \
  "ds_read_b128 v[144:147], v208 offset:128\n\t" \
  "ds_read_b128 v[148:151], v208 offset:144\n\t" \
  "ds_read_b128 v[152:155], v208 offset:384\n\t" \
  "ds_read_b128 v[156:159], v208 offset:400\n\t" \
  "ds_read_b128 v[28:31], v205 offset:49152\n\t" \
  "ds_read_b128 v[60:63], v205 offset:51200\n\t" \
  "ds_read_b128 v[92:95], v205 offset:53248\n\t" \
  "ds_read_b128 v[124:127], v205 offset:55296\n\t" \
  "v_lshlrev_b32 v176, 16, v16\n\t" \
  "v_and_b32 v177, s98, v16\n\t" \
  "v_lshlrev_b32 v178, 16, v17\n\t" \
  "v_and_b32 v179, s98, v17\n\t" \
  "v_lshlrev_b32 v180, 16, v18\n\t" \
  "v_and_b32 v181, s98, v18\n\t" \
  "v_lshlrev_b32 v182, 16, v19\n\t" \
  "v_and_b32 v183, s98, v19\n\t" \
  "v_pk_fma_f32 v[176:177], v[176:177], v[160:161], v[162:163]\n\t" \
  "v_pk_fma_f32 v[178:179], v[178:179], v[160:161], v[162:163]\n\t" \
  "v_pk_fma_f32 v[180:181], v[180:181], v[160:161], v[162:163]\n\t" \
  "v_pk_fma_f32 v[182:183], v[182:183], v[160:161], v[162:163]\n\t" \
  "s_waitcnt lgkmcnt(12)\n\t" \
  "s_xor_b32 s97, %[pso], 0\n\t" \
  "v_pk_fma_f32 v[176:177], v[176:177], v[128:129], v[136:137]\n\t" \
  "v_pk_fma_f32 v[178:179], v[178:179], v[130:131], v[138:139]\n\t" \
  "v_pk_fma_f32 v[180:181], v[180:181], v[132:133], v[140:141]\n\t" \
  "v_pk_fma_f32 v[182:183], v[182:183], v[134:135], v[142:143]\n\t" \
  "v_cvt_pk_bf16_f32 v16, v176, v177\n\t" \
  "v_cvt_pk_bf16_f32 v17, v178, v179\n\t" \
  "v_cvt_pk_bf16_f32 v18, v180, v181\n\t" \
  "v_cvt_pk_bf16_f32 v19, v182, v183\n\t" \
  "buffer_store_dwordx4 v[16:19], v214, %[ws], s97 offen sc1\n\t" \
  "v_lshlrev_b32 v176, 16, v48\n\t" \
  "v_and_b32 v177, s98, v48\n\t" \
  "v_lshlrev_b32 v178, 16, v49\n\t" \
  "v_and_b32 v179, s98, v49\n\t" \
  "v_lshlrev_b32 v180, 16, v50\n\t" \
  "v_and_b32 v181, s98, v50\n\t" \
  "v_lshlrev_b32 v182, 16, v51\n\t" \
  "v_and_b32 v183, s98, v51\n\t" \
  "v_pk_fma_f32 v[176:177], v[176:177], v[164:165], v[166:167]\n\t" \
  "v_pk_fma_f32 v[178:179], v[178:179], v[164:165], v[166:167]\n\t" \
  "v_pk_fma_f32 v[180:181], v[180:181], v[164:165], v[166:167]\n\t" \
  "v_pk_fma_f32 v[182:183], v[182:183], v[164:165], v[166:167]\n\t" \
  "s_xor_b32 s97, %[pso], 256\n\t" \
  "v_pk_fma_f32 v[176:177], v[176:177], v[128:129], v[136:137]\n\t" \
  "v_pk_fma_f32 v[178:179], v[178:179], v[130:131], v[138:139]\n\t" \
  "v_pk_fma_f32 v[180:181], v[180:181], v[132:133], v[140:141]\n\t" \
  "v_pk_fma_f32 v[182:183], v[182:183], v[134:135], v[142:143]\n\t" \
  "v_cvt_pk_bf16_f32 v48, v176, v177\n\t" \
  "v_cvt_pk_bf16_f32 v49, v178, v179\n\t" \
  "v_cvt_pk_bf16_f32 v50, v180, v181\n\t" \
  "v_cvt_pk_bf16_f32 v51, v182, v183\n\t" \
  "buffer_store_dwordx4 v[48:51], v214, %[ws], s97 offen sc1\n\t" \
  "v_lshlrev_b32 v176, 16, v80\n\t" \
  "v_and_b32 v177, s98, v80\n\t" \
  "v_lshlrev_b32 v178, 16, v81\n\t" \
  "v_and_b32 v179, s98, v81\n\t" \
  "v_lshlrev_b32 v180, 16, v82\n\t" \
  "v_and_b32 v181, s98, v82\n\t" \
  "v_lshlrev_b32 v182, 16, v83\n\t" \
  "v_and_b32 v183, s98, v83\n\t" \
  "v_pk_fma_f32 v[176:177], v[176:177], v[168:169], v[170:171]\n\t" \
  "v_pk_fma_f32 v[178:179], v[178:179], v[168:169], v[170:171]\n\t" \
  "v_pk_fma_f32 v[180:181], v[180:181], v[168:169], v[170:171]\n\t" \
  "v_pk_fma_f32 v[182:183], v[182:183], v[168:169], v[170:171]\n\t" \
  "s_xor_b32 s97, %[pso], 512\n\t" \
  "v_pk_fma_f32 v[176:177], v[176:177], v[128:129], v[136:137]\n\t" \
  "v_pk_fma_f32 v[178:179], v[178:179], v[130:131], v[138:139]\n\t" \
  "v_pk_fma_f32 v[180:181], v[180:181], v[132:133], v[140:141]\n\t" \
  "v_pk_fma_f32 v[182:183], v[182:183], v[134:135], v[142:143]\n\t" \
  "v_cvt_pk_bf16_f32 v80, v176, v177\n\t" \
  "v_cvt_pk_bf16_f32 v81, v178, v179\n\t" \
  "v_cvt_pk_bf16_f32 v82, v180, v181\n\t" \
  "v_cvt_pk_bf16_f32 v83, v182, v183\n\t" \
  "buffer_store_dwordx4 v[80:83], v214, %[ws], s97 offen sc1\n\t" \
  "v_lshlrev_b32 v176, 16, v112\n\t" \
  "v_and_b32 v177, s98, v112\n\t" \
  "v_lshlrev_b32 v178, 16, v113\n\t" \
  "v_and_b32 v179, s98, v113\n\t" \
  "v_lshlrev_b32 v180, 16, v114\n\t" \
  "v_and_b32 v181, s98, v114\n\t" \
  "v_lshlrev_b32 v182, 16, v115\n\t" \
  "v_and_b32 v183, s98, v115\n\t" \
  "v_pk_fma_f32 v[176:177], v[176:177], v[172:173], v[174:175]\n\t" \
  "v_pk_fma_f32 v[178:179], v[178:179], v[172:173], v[174:175]\n\t" \
  "v_pk_fma_f32 v[180:181], v[180:181], v[172:173], v[174:175]\n\t" \
  "v_pk_fma_f32 v[182:183], v[182:183], v[172:173], v[174:175]\n\t" \
  "s_xor_b32 s97, %[pso], 768\n\t" \
  "v_pk_fma_f32 v[176:177], v[176:177], v[128:129], v[136:137]\n\t" \
  "v_pk_fma_f32 v[178:179], v[178:179], v[130:131], v[138:139]\n\t" \
  "v_pk_fma_f32 v[180:181], v[180:181], v[132:133], v[140:141]\n\t" \
  "v_pk_fma_f32 v[182:183], v[182:183], v[134:135], v[142:143]\n\t" \
  "v_cvt_pk_bf16_f32 v112, v176, v177\n\t" \
  "v_cvt_pk_bf16_f32 v113, v178, v179\n\t" \
  "v_cvt_pk_bf16_f32 v114, v180, v181\n\t" \
  "v_cvt_pk_bf16_f32 v115, v182, v183\n\t" \
  "buffer_store_dwordx4 v[112:115], v214, %[ws], s97 offen sc1\n\t" \
  "ds_read_b128 v[128:131], v208 offset:512\n\t" \
  "ds_read_b128 v[132:135], v208 offset:528\n\t" \
  "ds_read_b128 v[136:139], v208 offset:768\n\t" \
  "ds_read_b128 v[140:143], v208 offset:784\n\t" \
  FSTP("s_memrealtime s[94:95]\n\t") \
  FSTP("s_memtime s[92:93]\n\t") \
  "s_add_u32 m0, %[mw], 49152\n\t" \
  "v_lshlrev_b32 v176, 16, v20\n\t" \
  "v_and_b32 v177, s98, v20\n\t" \
  "v_lshlrev_b32 v178, 16, v21\n\t" \
  "v_and_b32 v179, s98, v21\n\t" \
  "v_lshlrev_b32 v180, 16, v22\n\t" \
  "v_and_b32 v181, s98, v22\n\t" \
  "v_lshlrev_b32 v182, 16, v23\n\t" \
  "v_and_b32 v183, s98, v23\n\t" \
  "v_pk_fma_f32 v[176:177], v[176:177], v[160:161], v[162:163]\n\t" \
  "v_pk_fma_f32 v[178:179], v[178:179], v[160:161], v[162:163]\n\t" \
  "v_pk_fma_f32 v[180:181], v[180:181], v[160:161], v[162:163]\n\t" \
  "v_pk_fma_f32 v[182:183], v[182:183], v[160:161], v[162:163]\n\t" \
  "s_waitcnt lgkmcnt(8)\n\t" \
  "v_pk_fma_f32 v[176:177], v[176:177], v[144:145], v[152:153]\n\t" \
  "v_pk_fma_f32 v[178:179], v[178:179], v[146:147], v[154:155]\n\t" \
  "v_pk_fma_f32 v[180:181], v[180:181], v[148:149], v[156:157]\n\t" \
  "v_pk_fma_f32 v[182:183], v[182:183], v[150:151], v[158:159]\n\t" \
  "v_cvt_pk_bf16_f32 v20, v176, v177\n\t" \
  "v_cvt_pk_bf16_f32 v21, v178, v179\n\t" \
  "v_cvt_pk_bf16_f32 v22, v180, v181\n\t" \
  "v_cvt_pk_bf16_f32 v23, v182, v183\n\t" \
  "buffer_load_dwordx4 v200, %[sw], %[w1] offen offset:0 lds\n\t" \
  "v_lshlrev_b32 v176, 16, v52\n\t" \
  "v_and_b32 v177, s98, v52\n\t" \
  "v_lshlrev_b32 v178, 16, v53\n\t" \
  "v_and_b32 v179, s98, v53\n\t" \
  "v_lshlrev_b32 v180, 16, v54\n\t" \
  "v_and_b32 v181, s98, v54\n\t" \
  "v_lshlrev_b32 v182, 16, v55\n\t" \
  "v_and_b32 v183, s98, v55\n\t" \
  "v_pk_fma_f32 v[176:177], v[176:177], v[164:165], v[166:167]\n\t" \
  "v_pk_fma_f32 v[178:179], v[178:179], v[164:165], v[166:167]\n\t" \
  "v_pk_fma_f32 v[180:181], v[180:181], v[164:165], v[166:167]\n\t" \
  "v_pk_fma_f32 v[182:183], v[182:183], v[164:165], v[166:167]\n\t" \
  "v_pk_fma_f32 v[176:177], v[176:177], v[144:145], v[152:153]\n\t" \
  "v_pk_fma_f32 v[178:179], v[178:179], v[146:147], v[154:155]\n\t" \
  "v_pk_fma_f32 v[180:181], v[180:181], v[148:149], v[156:157]\n\t" \
  "v_pk_fma_f32 v[182:183], v[182:183], v[150:151], v[158:159]\n\t" \
  "v_cvt_pk_bf16_f32 v52, v176, v177\n\t" \
  "v_cvt_pk_bf16_f32 v53, v178, v179\n\t" \
  "v_cvt_pk_bf16_f32 v54, v180, v181\n\t" \
  "v_cvt_pk_bf16_f32 v55, v182, v183\n\t" \
  "buffer_load_dwordx4 v201, %[sw], %[w1] offen offset:1024 lds\n\t" \
  "v_lshlrev_b32 v176, 16, v84\n\t" \
  "v_and_b32 v177, s98, v84\n\t" \
  "v_lshlrev_b32 v178, 16, v85\n\t" \
  "v_and_b32 v179, s98, v85\n\t" \
  "v_lshlrev_b32 v180, 16, v86\n\t" \
  "v_and_b32 v181, s98, v86\n\t" \
  "v_lshlrev_b32 v182, 16, v87\n\t" \
  "v_and_b32 v183, s98, v87\n\t" \
  "v_pk_fma_f32 v[176:177], v[176:177], v[168:169], v[170:171]\n\t" \
  "v_pk_fma_f32 v[178:179], v[178:179], v[168:169], v[170:171]\n\t" \
  "v_pk_fma_f32 v[180:181], v[180:181], v[168:169], v[170:171]\n\t" \
  "v_pk_fma_f32 v[182:183], v[182:183], v[168:169], v[170:171]\n\t" \
  "v_pk_fma_f32 v[176:177], v[176:177], v[144:145], v[152:153]\n\t" \
  "v_pk_fma_f32 v[178:179], v[178:179], v[146:147], v[154:155]\n\t" \
  "v_pk_fma_f32 v[180:181], v[180:181], v[148:149], v[156:157]\n\t" \
  "v_pk_fma_f32 v[182:183], v[182:183], v[150:151], v[158:159]\n\t" \
  "v_cvt_pk_bf16_f32 v84, v176, v177\n\t" \
  "v_cvt_pk_bf16_f32 v85, v178, v179\n\t" \
  "v_cvt_pk_bf16_f32 v86, v180, v181\n\t" \
  "v_cvt_pk_bf16_f32 v87, v182, v183\n\t" \
  "buffer_load_dwordx4 v202, %[sw], %[w1] offen offset:2048 lds\n\t" \
  "v_lshlrev_b32 v176, 16, v116\n\t" \
  "v_and_b32 v177, s98, v116\n\t" \
  "v_lshlrev_b32 v178, 16, v117\n\t" \
  "v_and_b32 v179, s98, v117\n\t" \
  "v_lshlrev_b32 v180, 16, v118\n\t" \
  "v_and_b32 v181, s98, v118\n\t" \
  "v_lshlrev_b32 v182, 16, v119\n\t" \
  "v_and_b32 v183, s98, v119\n\t" \
  "v_pk_fma_f32 v[176:177], v[176:177], v[172:173], v[174:175]\n\t" \
  "v_pk_fma_f32 v[178:179], v[178:179], v[172:173], v[174:175]\n\t" \
  "v_pk_fma_f32 v[180:181], v[180:181], v[172:173], v[174:175]\n\t" \
  "v_pk_fma_f32 v[182:183], v[182:183], v[172:173], v[174:175]\n\t" \
  "v_pk_fma_f32 v[176:177], v[176:177], v[144:145], v[152:153]\n\t" \
  "v_pk_fma_f32 v[178:179], v[178:179], v[146:147], v[154:155]\n\t" \
  "v_pk_fma_f32 v[180:181], v[180:181], v[148:149], v[156:157]\n\t" \
  "v_pk_fma_f32 v[182:183], v[182:183], v[150:151], v[158:159]\n\t" \
  "v_cvt_pk_bf16_f32 v116, v176, v177\n\t" \
  "v_cvt_pk_bf16_f32 v117, v178, v179\n\t" \
  "v_cvt_pk_bf16_f32 v118, v180, v181\n\t" \
  "v_cvt_pk_bf16_f32 v119, v182, v183\n\t" \
  "buffer_load_dwordx4 v203, %[sw], %[w1] offen offset:3072 lds\n\t" \
  "ds_read_b128 v[144:147], v208 offset:640\n\t" \
  "ds_read_b128 v[148:151], v208 offset:656\n\t" \
  "ds_read_b128 v[152:155], v208 offset:896\n\t" \
  "ds_read_b128 v[156:159], v208 offset:912\n\t" \
  FSTP("v_writelane_b32 %[stv], s94, 28\n\t") \
  FSTP("v_writelane_b32 %[stv], s92, 29\n\t") \
  FSTP("s_memrealtime s[94:95]\n\t") \
  FSTP("s_memtime s[92:93]\n\t") \
  "s_add_u32 m0, %[mx], 98304\n\t" \
  "s_xor_b32 s96, %[c17], 0\n\t" \
  "s_add_u32 s96, s96, %[sl2]\n\t" \
  "v_lshlrev_b32 v176, 16, v24\n\t" \
  "v_and_b32 v177, s98, v24\n\t" \
  "v_lshlrev_b32 v178, 16, v25\n\t" \
  "v_and_b32 v179, s98, v25\n\t" \
  "v_lshlrev_b32 v180, 16, v26\n\t" \
  "v_and_b32 v181, s98, v26\n\t" \
  "v_lshlrev_b32 v182, 16, v27\n\t" \
  "v_and_b32 v183, s98, v27\n\t" \
  "v_pk_fma_f32 v[176:177], v[176:177], v[160:161], v[162:163]\n\t" \
  "v_pk_fma_f32 v[178:179], v[178:179], v[160:161], v[162:163]\n\t" \
  "v_pk_fma_f32 v[180:181], v[180:181], v[160:161], v[162:163]\n\t" \
  "v_pk_fma_f32 v[182:183], v[182:183], v[160:161], v[162:163]\n\t" \
  "s_waitcnt lgkmcnt(4)\n\t" \
  "v_pk_fma_f32 v[176:177], v[176:177], v[128:129], v[136:137]\n\t" \
  "v_pk_fma_f32 v[178:179], v[178:179], v[130:131], v[138:139]\n\t" \
  "v_pk_fma_f32 v[180:181], v[180:181], v[132:133], v[140:141]\n\t" \
  "v_pk_fma_f32 v[182:183], v[182:183], v[134:135], v[142:143]\n\t" \
  "v_cvt_pk_bf16_f32 v24, v176, v177\n\t" \
  "v_cvt_pk_bf16_f32 v25, v178, v179\n\t" \
  "v_cvt_pk_bf16_f32 v26, v180, v181\n\t" \
  "v_cvt_pk_bf16_f32 v27, v182, v183\n\t" \
  "buffer_load_dwordx4 v212, %[sx], s96 offen lds\n\t" \
  "v_lshlrev_b32 v176, 16, v56\n\t" \
  "v_and_b32 v177, s98, v56\n\t" \
  "v_lshlrev_b32 v178, 16, v57\n\t" \
  "v_and_b32 v179, s98, v57\n\t" \
  "v_lshlrev_b32 v180, 16, v58\n\t" \
  "v_and_b32 v181, s98, v58\n\t" \
  "v_lshlrev_b32 v182, 16, v59\n\t" \
  "v_and_b32 v183, s98, v59\n\t" \
  "v_pk_fma_f32 v[176:177], v[176:177], v[164:165], v[166:167]\n\t" \
  "v_pk_fma_f32 v[178:179], v[178:179], v[164:165], v[166:167]\n\t" \
  "v_pk_fma_f32 v[180:181], v[180:181], v[164:165], v[166:167]\n\t" \
  "v_pk_fma_f32 v[182:183], v[182:183], v[164:165], v[166:167]\n\t" \
  "v_pk_fma_f32 v[176:177], v[176:177], v[128:129], v[136:137]\n\t" \
  "v_pk_fma_f32 v[178:179], v[178:179], v[130:131], v[138:139]\n\t" \
  "v_pk_fma_f32 v[180:181], v[180:181], v[132:133], v[140:141]\n\t" \
  "v_pk_fma_f32 v[182:183], v[182:183], v[134:135], v[142:143]\n\t" \
  "v_cvt_pk_bf16_f32 v56, v176, v177\n\t" \
  "v_cvt_pk_bf16_f32 v57, v178, v179\n\t" \
  "v_cvt_pk_bf16_f32 v58, v180, v181\n\t" \
  "v_cvt_pk_bf16_f32 v59, v182, v183\n\t" \
  "buffer_load_dwordx4 v213, %[sx], s96 offen offset:1024 lds\n\t" \
  "s_add_u32 m0, %[mx], 100352\n\t" \
  "s_xor_b32 s96, %[c17], 131072\n\t" \
  "s_add_u32 s96, s96, %[sl2]\n\t" \
  "v_lshlrev_b32 v176, 16, v88\n\t" \
  "v_and_b32 v177, s98, v88\n\t" \
  "v_lshlrev_b32 v178, 16, v89\n\t" \
  "v_and_b32 v179, s98, v89\n\t" \
  "v_lshlrev_b32 v180, 16, v90\n\t" \
  "v_and_b32 v181, s98, v90\n\t" \
  "v_lshlrev_b32 v182, 16, v91\n\t" \
  "v_and_b32 v183, s98, v91\n\t" \
  "v_pk_fma_f32 v[176:177], v[176:177], v[168:169], v[170:171]\n\t" \
  "v_pk_fma_f32 v[178:179], v[178:179], v[168:169], v[170:171]\n\t" \
  "v_pk_fma_f32 v[180:181], v[180:181], v[168:169], v[170:171]\n\t" \
  "v_pk_fma_f32 v[182:183], v[182:183], v[168:169], v[170:171]\n\t" \
  "v_pk_fma_f32 v[176:177], v[176:177], v[128:129], v[136:137]\n\t" \
  "v_pk_fma_f32 v[178:179], v[178:179], v[130:131], v[138:139]\n\t" \
  "v_pk_fma_f32 v[180:181], v[180:181], v[132:133], v[140:141]\n\t" \
  "v_pk_fma_f32 v[182:183], v[182:183], v[134:135], v[142:143]\n\t" \
  "v_cvt_pk_bf16_f32 v88, v176, v177\n\t" \
  "v_cvt_pk_bf16_f32 v89, v178, v179\n\t" \
  "v_cvt_pk_bf16_f32 v90, v180, v181\n\t" \
  "v_cvt_pk_bf16_f32 v91, v182, v183\n\t" \
  "buffer_load_dwordx4 v212, %[sx], s96 offen lds\n\t" \
  "v_lshlrev_b32 v176, 16, v120\n\t" \
  "v_and_b32 v177, s98, v120\n\t" \
  "v_lshlrev_b32 v178, 16, v121\n\t" \
  "v_and_b32 v179, s98, v121\n\t" \
  "v_lshlrev_b32 v180, 16, v122\n\t" \
  "v_and_b32 v181, s98, v122\n\t" \
  "v_lshlrev_b32 v182, 16, v123\n\t" \
  "v_and_b32 v183, s98, v123\n\t" \
  "v_pk_fma_f32 v[176:177], v[176:177], v[172:173], v[174:175]\n\t" \
  "v_pk_fma_f32 v[178:179], v[178:179], v[172:173], v[174:175]\n\t" \
  "v_pk_fma_f32 v[180:181], v[180:181], v[172:173], v[174:175]\n\t" \
  "v_pk_fma_f32 v[182:183], v[182:183], v[172:173], v[174:175]\n\t" \
  "v_pk_fma_f32 v[176:177], v[176:177], v[128:129], v[136:137]\n\t" \
  "v_pk_fma_f32 v[178:179], v[178:179], v[130:131], v[138:139]\n\t" \
  "v_pk_fma_f32 v[180:181], v[180:181], v[132:133], v[140:141]\n\t" \
  "v_pk_fma_f32 v[182:183], v[182:183], v[134:135], v[142:143]\n\t" \
  "v_cvt_pk_bf16_f32 v120, v176, v177\n\t" \
  "v_cvt_pk_bf16_f32 v121, v178, v179\n\t" \
  "v_cvt_pk_bf16_f32 v122, v180, v181\n\t" \
  "v_cvt_pk_bf16_f32 v123, v182, v183\n\t" \
  "buffer_load_dwordx4 v213, %[sx], s96 offen offset:1024 lds\n\t" \
  "ds_read_b128 v[128:131], v208 offset:1024\n\t" \
  "ds_read_b128 v[132:135], v208 offset:1040\n\t" \
  "ds_read_b128 v[136:139], v208 offset:1280\n\t" \
  "ds_read_b128 v[140:143], v208 offset:1296\n\t" \
  FSTP("v_writelane_b32 %[stv], s94, 30\n\t") \
  FSTP("v_writelane_b32 %[stv], s92, 31\n\t") \
  FSTP("s_memrealtime s[94:95]\n\t") \
  FSTP("s_memtime s[92:93]\n\t") \
  "s_add_u32 m0, %[mx], 102400\n\t" \
  "s_xor_b32 s96, %[c17], 262144\n\t" \
  "s_add_u32 s96, s96, %[sl2]\n\t" \
  "v_lshlrev_b32 v176, 16, v28\n\t" \
  "v_and_b32 v177, s98, v28\n\t" \
  "v_lshlrev_b32 v178, 16, v29\n\t" \
  "v_and_b32 v179, s98, v29\n\t" \
  "v_lshlrev_b32 v180, 16, v30\n\t" \
  "v_and_b32 v181, s98, v30\n\t" \
  "v_lshlrev_b32 v182, 16, v31\n\t" \
  "v_and_b32 v183, s98, v31\n\t" \
  "v_pk_fma_f32 v[176:177], v[176:177], v[160:161], v[162:163]\n\t" \
  "v_pk_fma_f32 v[178:179], v[178:179], v[160:161], v[162:163]\n\t" \
  "v_pk_fma_f32 v[180:181], v[180:181], v[160:161], v[162:163]\n\t" \
  "v_pk_fma_f32 v[182:183], v[182:183], v[160:161], v[162:163]\n\t" \
  "s_waitcnt lgkmcnt(4)\n\t" \
  "v_pk_fma_f32 v[176:177], v[176:177], v[144:145], v[152:153]\n\t" \
  "v_pk_fma_f32 v[178:179], v[178:179], v[146:147], v[154:155]\n\t" \
  "v_pk_fma_f32 v[180:181], v[180:181], v[148:149], v[156:157]\n\t" \
  "v_pk_fma_f32 v[182:183], v[182:183], v[150:151], v[158:159]\n\t" \
  "v_cvt_pk_bf16_f32 v28, v176, v177\n\t" \
  "v_cvt_pk_bf16_f32 v29, v178, v179\n\t" \
  "v_cvt_pk_bf16_f32 v30, v180, v181\n\t" \
  "v_cvt_pk_bf16_f32 v31, v182, v183\n\t" \
  "buffer_load_dwordx4 v212, %[sx], s96 offen lds\n\t" \
  "ds_read_b64_tr_b16 a[128:129], v184 offset:0\n\t" \
  "ds_read_b64_tr_b16 a[130:131], v184 offset:1024\n\t" \
  "ds_read_b64_tr_b16 a[132:133], v185 offset:0\n\t" \
  "ds_read_b64_tr_b16 a[134:135], v185 offset:1024\n\t" \
  "v_lshlrev_b32 v176, 16, v60\n\t" \
  "v_and_b32 v177, s98, v60\n\t" \
  "v_lshlrev_b32 v178, 16, v61\n\t" \
  "v_and_b32 v179, s98, v61\n\t" \
  "v_lshlrev_b32 v180, 16, v62\n\t" \
  "v_and_b32 v181, s98, v62\n\t" \
  "v_lshlrev_b32 v182, 16, v63\n\t" \
  "v_and_b32 v183, s98, v63\n\t" \
  "v_pk_fma_f32 v[176:177], v[176:177], v[164:165], v[166:167]\n\t" \
  "v_pk_fma_f32 v[178:179], v[178:179], v[164:165], v[166:167]\n\t" \
  "v_pk_fma_f32 v[180:181], v[180:181], v[164:165], v[166:167]\n\t" \
  "v_pk_fma_f32 v[182:183], v[182:183], v[164:165], v[166:167]\n\t" \
  "v_pk_fma_f32 v[176:177], v[176:177], v[144:145], v[152:153]\n\t" \
  "v_pk_fma_f32 v[178:179], v[178:179], v[146:147], v[154:155]\n\t" \
  "v_pk_fma_f32 v[180:181], v[180:181], v[148:149], v[156:157]\n\t" \
  "v_pk_fma_f32 v[182:183], v[182:183], v[150:151], v[158:159]\n\t" \
  "v_cvt_pk_bf16_f32 v60, v176, v177\n\t" \
  "v_cvt_pk_bf16_f32 v61, v178, v179\n\t" \
  "v_cvt_pk_bf16_f32 v62, v180, v181\n\t" \
  "v_cvt_pk_bf16_f32 v63, v182, v183\n\t" \
  "buffer_load_dwordx4 v213, %[sx], s96 offen offset:1024 lds\n\t" \
  "ds_read_b64_tr_b16 a[136:137], v186 offset:0\n\t" \
  "ds_read_b64_tr_b16 a[138:139], v186 offset:1024\n\t" \
  "ds_read_b64_tr_b16 a[140:141], v187 offset:0\n\t" \
  "ds_read_b64_tr_b16 a[142:143], v187 offset:1024\n\t" \
  "s_add_u32 m0, %[mx], 104448\n\t" \
  "s_xor_b32 s96, %[c17], 393216\n\t" \
  "s_add_u32 s96, s96, %[sl2]\n\t" \
  "v_lshlrev_b32 v176, 16, v92\n\t" \
  "v_and_b32 v177, s98, v92\n\t" \
  "v_lshlrev_b32 v178, 16, v93\n\t" \
  "v_and_b32 v179, s98, v93\n\t" \
  "v_lshlrev_b32 v180, 16, v94\n\t" \
  "v_and_b32 v181, s98, v94\n\t" \
  "v_lshlrev_b32 v182, 16, v95\n\t" \
  "v_and_b32 v183, s98, v95\n\t" \
  "v_pk_fma_f32 v[176:177], v[176:177], v[168:169], v[170:171]\n\t" \
  "v_pk_fma_f32 v[178:179], v[178:179], v[168:169], v[170:171]\n\t" \
  "v_pk_fma_f32 v[180:181], v[180:181], v[168:169], v[170:171]\n\t" \
  "v_pk_fma_f32 v[182:183], v[182:183], v[168:169], v[170:171]\n\t" \
  "v_pk_fma_f32 v[176:177], v[176:177], v[144:145], v[152:153]\n\t" \
  "v_pk_fma_f32 v[178:179], v[178:179], v[146:147], v[154:155]\n\t" \
  "v_pk_fma_f32 v[180:181], v[180:181], v[148:149], v[156:157]\n\t" \
  "v_pk_fma_f32 v[182:183], v[182:183], v[150:151], v[158:159]\n\t" \
  "v_cvt_pk_bf16_f32 v92, v176, v177\n\t" \
  "v_cvt_pk_bf16_f32 v93, v178, v179\n\t" \
  "v_cvt_pk_bf16_f32 v94, v180, v181\n\t" \
  "v_cvt_pk_bf16_f32 v95, v182, v183\n\t" \
  "buffer_load_dwordx4 v212, %[sx], s96 offen lds\n\t" \
  "ds_read_b64_tr_b16 a[144:145], v188 offset:0\n\t" \
  "ds_read_b64_tr_b16 a[146:147], v188 offset:1024\n\t" \
  "ds_read_b64_tr_b16 a[148:149], v189 offset:0\n\t" \
  "ds_read_b64_tr_b16 a[150:151], v189 offset:1024\n\t" \
  "v_lshlrev_b32 v176, 16, v124\n\t" \
  "v_and_b32 v177, s98, v124\n\t" \
  "v_lshlrev_b32 v178, 16, v125\n\t" \
  "v_and_b32 v179, s98, v125\n\t" \
  "v_lshlrev_b32 v180, 16, v126\n\t" \
  "v_and_b32 v181, s98, v126\n\t" \
  "v_lshlrev_b32 v182, 16, v127\n\t" \
  "v_and_b32 v183, s98, v127\n\t" \
  "v_pk_fma_f32 v[176:177], v[176:177], v[172:173], v[174:175]\n\t" \
  "v_pk_fma_f32 v[178:179], v[178:179], v[172:173], v[174:175]\n\t" \
  "v_pk_fma_f32 v[180:181], v[180:181], v[172:173], v[174:175]\n\t" \
  "v_pk_fma_f32 v[182:183], v[182:183], v[172:173], v[174:175]\n\t" \
  "v_pk_fma_f32 v[176:177], v[176:177], v[144:145], v[152:153]\n\t" \
  "v_pk_fma_f32 v[178:179], v[178:179], v[146:147], v[154:155]\n\t" \
  "v_pk_fma_f32 v[180:181], v[180:181], v[148:149], v[156:157]\n\t" \
  "v_pk_fma_f32 v[182:183], v[182:183], v[150:151], v[158:159]\n\t" \
  "v_cvt_pk_bf16_f32 v124, v176, v177\n\t" \
  "v_cvt_pk_bf16_f32 v125, v178, v179\n\t" \
  "v_cvt_pk_bf16_f32 v126, v180, v181\n\t" \
  "v_cvt_pk_bf16_f32 v127, v182, v183\n\t" \
  "buffer_load_dwordx4 v213, %[sx], s96 offen offset:1024 lds\n\t" \
  "ds_read_b64_tr_b16 a[152:153], v190 offset:0\n\t" \
  "ds_read_b64_tr_b16 a[154:155], v190 offset:1024\n\t" \
  "ds_read_b64_tr_b16 a[156:157], v191 offset:0\n\t" \
  "ds_read_b64_tr_b16 a[158:159], v191 offset:1024\n\t" \
  "ds_read_b128 v[144:147], v208 offset:1152\n\t" \
  "ds_read_b128 v[148:151], v208 offset:1168\n\t" \
  "ds_read_b128 v[152:155], v208 offset:1408\n\t" \
  "ds_read_b128 v[156:159], v208 offset:1424\n\t" \
  FSTP("v_writelane_b32 %[stv], s94, 32\n\t") \
  FSTP("v_writelane_b32 %[stv], s92, 33\n\t") \
  FSTP("s_memrealtime s[94:95]\n\t") \
  FSTP("s_memtime s[92:93]\n\t") \
  "s_waitcnt vmcnt(12)\n\t" \
  "s_mov_b64 exec, 1\n\t" \
  "buffer_store_dword v211, off, %[ws], %[fw] sc1\n\t" \
  "s_mov_b64 exec, -1\n\t" \
  FSTP("v_writelane_b32 %[stv], s94, 34\n\t") \
  FSTP("v_writelane_b32 %[stv], s92, 35\n\t") \
  FSTP("s_memrealtime s[94:95]\n\t") \
  FSTP("s_memtime s[92:93]\n\t") \
  "s_waitcnt lgkmcnt(4)\n\t" \
  "v_mfma_f32_16x16x32_bf16 v[0:3], a[128:131], v[16:19], 0\n\t" \
  "ds_read_b64_tr_b16 a[160:161], v184 offset:8192\n\t" \
  "s_add_u32 m0, %[mw], 98304\n\t" \
  "v_mfma_f32_16x16x32_bf16 v[32:35], a[128:131], v[48:51], 0\n\t" \
  "buffer_load_dwordx4 v200, %[sw], %[w2] offen offset:0 lds\n\t" \
  "v_mfma_f32_16x16x32_bf16 v[64:67], a[128:131], v[80:83], 0\n\t" \
  "ds_read_b64_tr_b16 a[162:163], v184 offset:9216\n\t" \
  "v_mfma_f32_16x16x32_bf16 v[96:99], a[128:131], v[112:115], 0\n\t" \
  "buffer_load_dwordx4 v201, %[sw], %[w2] offen offset:1024 lds\n\t" \
  "v_mfma_f32_16x16x32_bf16 v[100:103], a[132:135], v[112:115], 0\n\t" \
  "ds_read_b64_tr_b16 a[164:165], v185 offset:8192\n\t" \
  "v_mfma_f32_16x16x32_bf16 v[68:71], a[132:135], v[80:83], 0\n\t" \
  "buffer_load_dwordx4 v202, %[sw], %[w2] offen offset:2048 lds\n\t" \
  "v_mfma_f32_16x16x32_bf16 v[36:39], a[132:135], v[48:51], 0\n\t" \
  "ds_read_b64_tr_b16 a[166:167], v185 offset:9216\n\t" \
  "v_mfma_f32_16x16x32_bf16 v[4:7], a[132:135], v[16:19], 0\n\t" \
  "ds_read_b64_tr_b16 a[168:169], v186 offset:8192\n\t" \
  "v_mfma_f32_16x16x32_bf16 v[8:11], a[136:139], v[16:19], 0\n\t" \
  "buffer_load_dwordx4 v203, %[sw], %[w2] offen offset:3072 lds\n\t" \
  "v_mfma_f32_16x16x32_bf16 v[40:43], a[136:139], v[48:51], 0\n\t" \
  "s_add_u32 m0, %[mx], 0\n\t" \
  "s_xor_b32 s96, %[c17], 0\n\t" \
  "s_add_u32 s96, s96, %[sl3]\n\t" \
  "v_mfma_f32_16x16x32_bf16 v[72:75], a[136:139], v[80:83], 0\n\t" \
  "buffer_load_dwordx4 v212, %[sx], s96 offen lds\n\t" \
  "v_mfma_f32_16x16x32_bf16 v[104:107], a[136:139], v[112:115], 0\n\t" \
  "ds_read_b64_tr_b16 a[170:171], v186 offset:9216\n\t" \
  "v_mfma_f32_16x16x32_bf16 v[108:111], a[140:143], v[112:115], 0\n\t" \
  "ds_read_b64_tr_b16 a[172:173], v187 offset:8192\n\t" \
  "v_mfma_f32_16x16x32_bf16 v[76:79], a[140:143], v[80:83], 0\n\t" \
  "buffer_load_dwordx4 v213, %[sx], s96 offen offset:1024 lds\n\t" \
  "v_mfma_f32_16x16x32_bf16 v[44:47], a[140:143], v[48:51], 0\n\t" \
  "s_add_u32 m0, %[mx], 2048\n\t" \
  "s_xor_b32 s96, %[c17], 131072\n\t" \
  "ds_read_b64_tr_b16 a[174:175], v187 offset:9216\n\t" \
  "s_add_u32 s96, s96, %[sl3]\n\t" \
  "v_mfma_f32_16x16x32_bf16 v[12:15], a[140:143], v[16:19], 0\n\t" \
  "buffer_load_dwordx4 v212, %[sx], s96 offen lds\n\t" \
  "v_mfma_f32_16x16x32_bf16 a[64:67], a[144:147], v[16:19], 0\n\t" \
  "ds_read_b64_tr_b16 a[176:177], v188 offset:8192\n\t" \
  "v_mfma_f32_16x16x32_bf16 a[68:71], a[144:147], v[48:51], 0\n\t" \
  "ds_read_b64_tr_b16 a[178:179], v188 offset:9216\n\t" \
  "v_mfma_f32_16x16x32_bf16 a[72:75], a[144:147], v[80:83], 0\n\t" \
  "buffer_load_dwordx4 v213, %[sx], s96 offen offset:1024 lds\n\t" \
  "v_mfma_f32_16x16x32_bf16 a[76:79], a[144:147], v[112:115], 0\n\t" \
  "s_add_u32 m0, %[mx], 4096\n\t" \
  "s_xor_b32 s96, %[c17], 262144\n\t" \
  "ds_read_b64_tr_b16 a[180:181], v189 offset:8192\n\t" \
  "s_add_u32 s96, s96, %[sl3]\n\t" \
  "v_mfma_f32_16x16x32_bf16 a[92:95], a[148:151], v[112:115], 0\n\t" \
  ".Lhs0:\n\ts_cbranch_execz .Lhs0n\n\t" \
  "buffer_load_dwordx4 v212, %[sx], s96 offen lds\n\t" \
  "v_mfma_f32_16x16x32_bf16 a[88:91], a[148:151], v[80:83], 0\n\t" \
  "ds_read_b64_tr_b16 a[182:183], v189 offset:9216\n\t" \
  "v_mfma_f32_16x16x32_bf16 a[84:87], a[148:151], v[48:51], 0\n\t" \
  "ds_read_b64_tr_b16 a[184:185], v190 offset:8192\n\t" \
  "v_mfma_f32_16x16x32_bf16 a[80:83], a[148:151], v[16:19], 0\n\t" \
  "buffer_load_dwordx4 v213, %[sx], s96 offen offset:1024 lds\n\t" \
  "v_mfma_f32_16x16x32_bf16 a[96:99], a[152:155], v[16:19], 0\n\t" \
  "s_add_u32 m0, %[mx], 6144\n\t" \
  "s_xor_b32 s96, %[c17], 393216\n\t" \
  "ds_read_b64_tr_b16 a[186:187], v190 offset:9216\n\t" \
  "s_add_u32 s96, s96, %[sl3]\n\t" \
  "v_mfma_f32_16x16x32_bf16 a[100:103], a[152:155], v[48:51], 0\n\t" \
  "buffer_load_dwordx4 v212, %[sx], s96 offen lds\n\t" \
  "v_mfma_f32_16x16x32_bf16 a[104:107], a[152:155], v[80:83], 0\n\t" \
  "ds_read_b64_tr_b16 a[188:189], v191 offset:8192\n\t" \
  "v_mfma_f32_16x16x32_bf16 a[108:111], a[152:155], v[112:115], 0\n\t" \
  "ds_read_b64_tr_b16 a[190:191], v191 offset:9216\n\t" \
  "v_mfma_f32_16x16x32_bf16 a[124:127], a[156:159], v[112:115], 0\n\t" \
  "buffer_load_dwordx4 v213, %[sx], s96 offen offset:1024 lds\n\t" \
  "v_mfma_f32_16x16x32_bf16 a[120:123], a[156:159], v[80:83], 0\n\t" \
  "s_add_u32 m0, %[mw], 0\n\t" \
  "v_mfma_f32_16x16x32_bf16 a[116:119], a[156:159], v[48:51], 0\n\t" \
  "s_waitcnt vmcnt(21) lgkmcnt(0)\n\t" \
  "v_mfma_f32_16x16x32_bf16 a[112:115], a[156:159], v[16:19], 0\n\t" \
  "s_waitcnt vmcnt(13)\n\t" \
  "ds_read_b128 v[16:19], v206 offset:0\n\t" \
  "ds_read_b128 v[48:51], v206 offset:2048\n\t" \
  "s_barrier\n\t" \
  FSTP("v_writelane_b32 %[stv], s94, 36\n\t") \
  FSTP("v_writelane_b32 %[stv], s92, 37\n\t") \
  FSTP("s_memrealtime s[94:95]\n\t") \
  FSTP("s_memtime s[92:93]\n\t") \
  "v_mfma_f32_16x16x32_bf16 v[0:3], a[160:163], v[20:23], v[0:3]\n\t" \
  "buffer_load_dwordx4 v200, %[sw], %[w3] offen offset:0 lds\n\t" \
  "ds_read_b128 v[80:83], v206 offset:4096\n\t" \
  "ds_read_b128 v[112:115], v206 offset:6144\n\t" \
  "v_mfma_f32_16x16x32_bf16 v[32:35], a[160:163], v[52:55], v[32:35]\n\t" \
  "ds_read_b64_tr_b16 a[128:129], v184 offset:49152\n\t" \
  "v_mfma_f32_16x16x32_bf16 v[64:67], a[160:163], v[84:87], v[64:67]\n\t" \
  "ds_read_b64_tr_b16 a[130:131], v184 offset:50176\n\t" \
  "v_mfma_f32_16x16x32_bf16 v[96:99], a[160:163], v[116:119], v[96:99]\n\t" \
  "buffer_load_dwordx4 v201, %[sw], %[w3] offen offset:1024 lds\n\t" \
  "v_mfma_f32_16x16x32_bf16 v[100:103], a[164:167], v[116:119], v[100:103]\n\t" \
  "ds_read_b64_tr_b16 a[132:133], v185 offset:49152\n\t" \
  "v_mfma_f32_16x16x32_bf16 v[68:71], a[164:167], v[84:87], v[68:71]\n\t" \
  "s_waitcnt lgkmcnt(3)\n\t" \
  "v_lshlrev_b32 v176, 16, v16\n\t" \
  "v_and_b32 v177, s98, v16\n\t" \
  "v_mfma_f32_16x16x32_bf16 v[36:39], a[164:167], v[52:55], v[36:39]\n\t" \
  "ds_read_b64_tr_b16 a[134:135], v185 offset:50176\n\t" \
  "v_lshlrev_b32 v178, 16, v17\n\t" \
  "v_mfma_f32_16x16x32_bf16 v[4:7], a[164:167], v[20:23], v[4:7]\n\t" \
  "v_and_b32 v179, s98, v17\n\t" \
  "v_lshlrev_b32 v180, 16, v18\n\t" \
  "v_mfma_f32_16x16x32_bf16 v[8:11], a[168:171], v[20:23], v[8:11]\n\t" \
  "buffer_load_dwordx4 v202, %[sw], %[w3] offen offset:2048 lds\n\t" \
  "v_mfma_f32_16x16x32_bf16 v[40:43], a[168:171], v[52:55], v[40:43]\n\t" \
  "ds_read_b64_tr_b16 a[136:137], v186 offset:49152\n\t" \
  "v_and_b32 v181, s98, v18\n\t" \
  "v_mfma_f32_16x16x32_bf16 v[72:75], a[168:171], v[84:87], v[72:75]\n\t" \
  "ds_read_b64_tr_b16 a[138:139], v186 offset:50176\n\t" \
  "v_lshlrev_b32 v182, 16, v19\n\t" \
  "v_mfma_f32_16x16x32_bf16 v[104:107], a[168:171], v[116:119], v[104:107]\n\t" \
  "ds_read_b64_tr_b16 a[140:141], v187 offset:49152\n\t" \
  "v_and_b32 v183, s98, v19\n\t" \
  "v_mfma_f32_16x16x32_bf16 v[108:111], a[172:175], v[116:119], v[108:111]\n\t" \
  "buffer_load_dwordx4 v203, %[sw], %[w3] offen offset:3072 lds\n\t" \
  "v_mfma_f32_16x16x32_bf16 v[76:79], a[172:175], v[84:87], v[76:79]\n\t" \
  "s_add_u32 s96, %[rot19], 2097152\n\t" \
  "s_and_b32 s96, s96, 0x780000\n\t" \
  "s_add_u32 m0, %[mw], 16384\n\t" \
  "v_mfma_f32_16x16x32_bf16 v[44:47], a[172:175], v[52:55], v[44:47]\n\t" \
  "ds_read_b64_tr_b16 a[142:143], v187 offset:50176\n\t" \
  "v_lshlrev_b32 v220, 16, v48\n\t" \
  "v_and_b32 v221, s98, v48\n\t" \
  "v_mfma_f32_16x16x32_bf16 v[12:15], a[172:175], v[20:23], v[12:15]\n\t" \
  "buffer_load_dwordx4 v200, %[sw], s96 offen offset:0 lds\n\t" \
  "v_mfma_f32_16x16x32_bf16 a[64:67], a[176:179], v[20:23], a[64:67]\n\t" \
  "ds_read_b64_tr_b16 a[144:145], v188 offset:49152\n\t" \
  "v_lshlrev_b32 v222, 16, v49\n\t" \
  "v_mfma_f32_16x16x32_bf16 a[68:71], a[176:179], v[52:55], a[68:71]\n\t" \
  "ds_read_b64_tr_b16 a[146:147], v188 offset:50176\n\t" \
  "v_and_b32 v223, s98, v49\n\t" \
  "v_mfma_f32_16x16x32_bf16 a[72:75], a[176:179], v[84:87], a[72:75]\n\t" \
  "ds_read_b64_tr_b16 a[148:149], v189 offset:49152\n\t" \
  "v_lshlrev_b32 v224, 16, v50\n\t" \
  "v_mfma_f32_16x16x32_bf16 a[76:79], a[176:179], v[116:119], a[76:79]\n\t" \
  "buffer_load_dwordx4 v201, %[sw], s96 offen offset:1024 lds\n\t" \
  "v_mfma_f32_16x16x32_bf16 a[92:95], a[180:183], v[116:119], a[92:95]\n\t" \
  "ds_read_b64_tr_b16 a[150:151], v189 offset:50176\n\t" \
  "v_and_b32 v225, s98, v50\n\t" \
  "v_mfma_f32_16x16x32_bf16 a[88:91], a[180:183], v[84:87], a[88:91]\n\t" \
  "ds_read_b64_tr_b16 a[152:153], v190 offset:49152\n\t" \
  "v_lshlrev_b32 v226, 16, v51\n\t" \
  "v_mfma_f32_16x16x32_bf16 a[84:87], a[180:183], v[52:55], a[84:87]\n\t" \
  "ds_read_b64_tr_b16 a[154:155], v190 offset:50176\n\t" \
  "v_and_b32 v227, s98, v51\n\t" \
  "v_mfma_f32_16x16x32_bf16 a[80:83], a[180:183], v[20:23], a[80:83]\n\t" \
  "buffer_load_dwordx4 v202, %[sw], s96 offen offset:2048 lds\n\t" \
  "ds_read_b64_tr_b16 a[156:157], v191 offset:49152\n\t" \
  "ds_read_b64_tr_b16 a[158:159], v191 offset:50176\n\t" \
  "v_lshlrev_b32 v228, 16, v80\n\t" \
  "v_pk_fma_f32 v[176:177], v[176:177], v[160:161], v[162:163]\n\t" \
  "v_pk_fma_f32 v[178:179], v[178:179], v[160:161], v[162:163]\n\t" \
  "v_pk_fma_f32 v[180:181], v[180:181], v[160:161], v[162:163]\n\t" \
  "v_pk_fma_f32 v[182:183], v[182:183], v[160:161], v[162:163]\n\t" \
  "v_pk_fma_f32 v[176:177], v[176:177], v[128:129], v[136:137]\n\t" \
  "v_pk_fma_f32 v[178:179], v[178:179], v[130:131], v[138:139]\n\t" \
  "v_pk_fma_f32 v[180:181], v[180:181], v[132:133], v[140:141]\n\t" \
  "v_pk_fma_f32 v[182:183], v[182:183], v[134:135], v[142:143]\n\t" \
  "v_pk_fma_f32 v[220:221], v[220:221], v[164:165], v[166:167]\n\t" \
  "v_pk_fma_f32 v[222:223], v[222:223], v[164:165], v[166:167]\n\t" \
  "v_pk_fma_f32 v[224:225], v[224:225], v[164:165], v[166:167]\n\t" \
  "v_pk_fma_f32 v[226:227], v[226:227], v[164:165], v[166:167]\n\t" \
  "v_pk_fma_f32 v[220:221], v[220:221], v[128:129], v[136:137]\n\t" \
  "v_pk_fma_f32 v[222:223], v[222:223], v[130:131], v[138:139]\n\t" \
  "v_pk_fma_f32 v[224:225], v[224:225], v[132:133], v[140:141]\n\t" \
  "v_pk_fma_f32 v[226:227], v[226:227], v[134:135], v[142:143]\n\t" \
  "v_cvt_pk_bf16_f32 v16, v176, v177\n\t" \
  "v_mfma_f32_16x16x32_bf16 a[96:99], a[184:187], v[20:23], a[96:99]\n\t" \
  "v_cvt_pk_bf16_f32 v17, v178, v179\n\t" \
  "v_cvt_pk_bf16_f32 v18, v180, v181\n\t" \
  "v_and_b32 v229, s98, v80\n\t" \
  "v_mfma_f32_16x16x32_bf16 a[100:103], a[184:187], v[52:55], a[100:103]\n\t" \
  "v_cvt_pk_bf16_f32 v19, v182, v183\n\t" \
  "v_cvt_pk_bf16_f32 v48, v220, v221\n\t" \
  "v_lshlrev_b32 v230, 16, v81\n\t" \
  "v_and_b32 v231, s98, v81\n\t" \
  "v_mfma_f32_16x16x32_bf16 a[104:107], a[184:187], v[84:87], a[104:107]\n\t" \
  "v_lshlrev_b32 v232, 16, v82\n\t" \
  "v_and_b32 v233, s98, v82\n\t" \
  "v_mfma_f32_16x16x32_bf16 a[108:111], a[184:187], v[116:119], a[108:111]\n\t" \
  "buffer_load_dwordx4 v203, %[sw], s96 offen offset:3072 lds\n\t" \
  "v_mfma_f32_16x16x32_bf16 a[124:127], a[188:191], v[116:119], a[124:127]\n\t" \
  "v_lshlrev_b32 v234, 16, v83\n\t" \
  "v_and_b32 v235, s98, v83\n\t" \
  "v_mfma_f32_16x16x32_bf16 a[120:123], a[188:191], v[84:87], a[120:123]\n\t" \
  "s_add_u32 s96, %[rot19], 2621440\n\t" \
  "v_lshlrev_b32 v236, 16, v112\n\t" \
  "v_and_b32 v237, s98, v112\n\t" \
  "v_mfma_f32_16x16x32_bf16 a[116:119], a[188:191], v[52:55], a[116:119]\n\t" \
  "s_and_b32 s96, s96, 0x780000\n\t" \
  "s_add_u32 m0, %[mw], 32768\n\t" \
  "v_lshlrev_b32 v238, 16, v113\n\t" \
  "v_mfma_f32_16x16x32_bf16 a[112:115], a[188:191], v[20:23], a[112:115]\n\t" \
  "s_waitcnt lgkmcnt(0)\n\t" \
  "ds_read_b128 v[20:23], v207 offset:0\n\t" \
  "ds_read_b128 v[52:55], v207 offset:2048\n\t" \
  "ds_read_b128 v[84:87], v207 offset:4096\n\t" \
  "v_and_b32 v239, s98, v113\n\t" \
  FSTP("v_writelane_b32 %[stv], s94, 38\n\t") \
  FSTP("v_writelane_b32 %[stv], s92, 39\n\t") \
  FSTP("s_memrealtime s[94:95]\n\t") \
  FSTP("s_memtime s[92:93]\n\t") \
  "v_mfma_f32_16x16x32_bf16 v[0:3], a[128:131], v[24:27], v[0:3]\n\t" \
  "buffer_load_dwordx4 v200, %[sw], s96 offen offset:0 lds\n\t" \
  "ds_read_b128 v[116:119], v207 offset:6144\n\t" \
  "v_mfma_f32_16x16x32_bf16 v[32:35], a[128:131], v[56:59], v[32:35]\n\t" \
  "v_lshlrev_b32 v240, 16, v114\n\t" \
  "v_and_b32 v241, s98, v114\n\t" \
  "v_mfma_f32_16x16x32_bf16 v[64:67], a[128:131], v[88:91], v[64:67]\n\t" \
  "v_lshlrev_b32 v242, 16, v115\n\t" \
  "v_and_b32 v243, s98, v115\n\t" \
  "v_mfma_f32_16x16x32_bf16 v[96:99], a[128:131], v[120:123], v[96:99]\n\t" \
  "ds_read_b64_tr_b16 a[160:161], v184 offset:57344\n\t" \
  "ds_read_b64_tr_b16 a[162:163], v184 offset:58368\n\t" \
  "v_cvt_pk_bf16_f32 v49, v222, v223\n\t" \
  "v_cvt_pk_bf16_f32 v50, v224, v225\n\t" \
  "v_cvt_pk_bf16_f32 v51, v226, v227\n\t" \
  "v_pk_fma_f32 v[228:229], v[228:229], v[168:169], v[170:171]\n\t" \
  "v_pk_fma_f32 v[230:231], v[230:231], v[168:169], v[170:171]\n\t" \
  "v_pk_fma_f32 v[232:233], v[232:233], v[168:169], v[170:171]\n\t" \
  "v_pk_fma_f32 v[234:235], v[234:235], v[168:169], v[170:171]\n\t" \
  "v_pk_fma_f32 v[228:229], v[228:229], v[128:129], v[136:137]\n\t" \
  "v_pk_fma_f32 v[230:231], v[230:231], v[130:131], v[138:139]\n\t" \
  "v_pk_fma_f32 v[232:233], v[232:233], v[132:133], v[140:141]\n\t" \
  "v_pk_fma_f32 v[234:235], v[234:235], v[134:135], v[142:143]\n\t" \
  "v_pk_fma_f32 v[236:237], v[236:237], v[172:173], v[174:175]\n\t" \
  "v_pk_fma_f32 v[238:239], v[238:239], v[172:173], v[174:175]\n\t" \
  "v_pk_fma_f32 v[240:241], v[240:241], v[172:173], v[174:175]\n\t" \
  "v_pk_fma_f32 v[242:243], v[242:243], v[172:173], v[174:175]\n\t" \
  "v_pk_fma_f32 v[236:237], v[236:237], v[128:129], v[136:137]\n\t" \
  "v_pk_fma_f32 v[238:239], v[238:239], v[130:131], v[138:139]\n\t" \
  "v_pk_fma_f32 v[240:241], v[240:241], v[132:133], v[140:141]\n\t" \
  "v_pk_fma_f32 v[242:243], v[242:243], v[134:135], v[142:143]\n\t" \
  "v_cvt_pk_bf16_f32 v80, v228, v229\n\t" \
  "v_mfma_f32_16x16x32_bf16 v[100:103], a[132:135], v[120:123], v[100:103]\n\t" \
  "ds_read_b64_tr_b16 a[164:165], v185 offset:57344\n\t" \
  "v_cvt_pk_bf16_f32 v81, v230, v231\n\t" \
  "v_mfma_f32_16x16x32_bf16 v[68:71], a[132:135], v[88:91], v[68:71]\n\t" \
  "s_waitcnt lgkmcnt(3)\n\t" \
  "v_lshlrev_b32 v176, 16, v20\n\t" \
  "v_and_b32 v177, s98, v20\n\t" \
  "v_mfma_f32_16x16x32_bf16 v[36:39], a[132:135], v[56:59], v[36:39]\n\t" \
  "buffer_load_dwordx4 v201, %[sw], s96 offen offset:1024 lds\n\t" \
  "v_mfma_f32_16x16x32_bf16 v[4:7], a[132:135], v[24:27], v[4:7]\n\t" \
  "ds_read_b64_tr_b16 a[166:167], v185 offset:58368\n\t" \
  "v_lshlrev_b32 v178, 16, v21\n\t" \
  "v_mfma_f32_16x16x32_bf16 v[8:11], a[136:139], v[24:27], v[8:11]\n\t" \
  "ds_read_b64_tr_b16 a[168:169], v186 offset:57344\n\t" \
  "v_and_b32 v179, s98, v21\n\t" \
  "v_mfma_f32_16x16x32_bf16 v[40:43], a[136:139], v[56:59], v[40:43]\n\t" \
  "v_lshlrev_b32 v180, 16, v22\n\t" \
  "v_and_b32 v181, s98, v22\n\t" \
  "v_mfma_f32_16x16x32_bf16 v[72:75], a[136:139], v[88:91], v[72:75]\n\t" \
  "ds_read_b64_tr_b16 a[170:171], v186 offset:58368\n\t" \
  "v_lshlrev_b32 v182, 16, v23\n\t" \
  "v_mfma_f32_16x16x32_bf16 v[104:107], a[136:139], v[120:123], v[104:107]\n\t" \
  "ds_read_b64_tr_b16 a[172:173], v187 offset:57344\n\t" \
  "v_and_b32 v183, s98, v23\n\t" \
  "v_mfma_f32_16x16x32_bf16 v[108:111], a[140:143], v[120:123], v[108:111]\n\t" \
  "buffer_load_dwordx4 v202, %[sw], s96 offen offset:2048 lds\n\t" \
  "v_mfma_f32_16x16x32_bf16 v[76:79], a[140:143], v[88:91], v[76:79]\n\t" \
  "ds_read_b64_tr_b16 a[174:175], v187 offset:58368\n\t" \
  "v_lshlrev_b32 v220, 16, v52\n\t" \
  "v_mfma_f32_16x16x32_bf16 v[44:47], a[140:143], v[56:59], v[44:47]\n\t" \
  "v_and_b32 v221, s98, v52\n\t" \
  "v_lshlrev_b32 v222, 16, v53\n\t" \
  "v_mfma_f32_16x16x32_bf16 v[12:15], a[140:143], v[24:27], v[12:15]\n\t" \
  "ds_read_b64_tr_b16 a[176:177], v188 offset:57344\n\t" \
  "v_and_b32 v223, s98, v53\n\t" \
  "v_mfma_f32_16x16x32_bf16 a[64:67], a[144:147], v[24:27], a[64:67]\n\t" \
  "ds_read_b64_tr_b16 a[178:179], v188 offset:58368\n\t" \
  "v_lshlrev_b32 v224, 16, v54\n\t" \
  "v_mfma_f32_16x16x32_bf16 a[68:71], a[144:147], v[56:59], a[68:71]\n\t" \
  "ds_read_b64_tr_b16 a[180:181], v189 offset:57344\n\t" \
  "v_and_b32 v225, s98, v54\n\t" \
  "v_mfma_f32_16x16x32_bf16 a[72:75], a[144:147], v[88:91], a[72:75]\n\t" \
  "buffer_load_dwordx4 v203, %[sw], s96 offen offset:3072 lds\n\t" \
  "v_mfma_f32_16x16x32_bf16 a[76:79], a[144:147], v[120:123], a[76:79]\n\t" \
  "ds_read_b64_tr_b16 a[182:183], v189 offset:58368\n\t" \
  "v_cvt_pk_bf16_f32 v82, v232, v233\n\t" \
  "v_mfma_f32_16x16x32_bf16 a[92:95], a[148:151], v[120:123], a[92:95]\n\t" \
  "v_lshlrev_b32 v226, 16, v55\n\t" \
  "v_and_b32 v227, s98, v55\n\t" \
  "v_mfma_f32_16x16x32_bf16 a[88:91], a[148:151], v[88:91], a[88:91]\n\t" \
  "ds_read_b64_tr_b16 a[184:185], v190 offset:57344\n\t" \
  "v_cvt_pk_bf16_f32 v83, v234, v235\n\t" \
  "v_cvt_pk_bf16_f32 v112, v236, v237\n\t" \
  "v_cvt_pk_bf16_f32 v113, v238, v239\n\t" \
  "v_pk_fma_f32 v[176:177], v[176:177], v[160:161], v[162:163]\n\t" \
  "v_pk_fma_f32 v[178:179], v[178:179], v[160:161], v[162:163]\n\t" \
  "v_pk_fma_f32 v[180:181], v[180:181], v[160:161], v[162:163]\n\t" \
  "v_pk_fma_f32 v[182:183], v[182:183], v[160:161], v[162:163]\n\t" \
  "v_pk_fma_f32 v[176:177], v[176:177], v[144:145], v[152:153]\n\t" \
  "v_pk_fma_f32 v[178:179], v[178:179], v[146:147], v[154:155]\n\t" \
  "v_pk_fma_f32 v[180:181], v[180:181], v[148:149], v[156:157]\n\t" \
  "v_pk_fma_f32 v[182:183], v[182:183], v[150:151], v[158:159]\n\t" \
  "v_pk_fma_f32 v[220:221], v[220:221], v[164:165], v[166:167]\n\t" \
  "v_pk_fma_f32 v[222:223], v[222:223], v[164:165], v[166:167]\n\t" \
  "v_pk_fma_f32 v[224:225], v[224:225], v[164:165], v[166:167]\n\t" \
  "v_pk_fma_f32 v[226:227], v[226:227], v[164:165], v[166:167]\n\t" \
  "v_pk_fma_f32 v[220:221], v[220:221], v[144:145], v[152:153]\n\t" \
  "v_pk_fma_f32 v[222:223], v[222:223], v[146:147], v[154:155]\n\t" \
  "v_pk_fma_f32 v[224:225], v[224:225], v[148:149], v[156:157]\n\t" \
  "v_pk_fma_f32 v[226:227], v[226:227], v[150:151], v[158:159]\n\t" \
  "v_cvt_pk_bf16_f32 v20, v176, v177\n\t" \
  "v_mfma_f32_16x16x32_bf16 a[84:87], a[148:151], v[56:59], a[84:87]\n\t" \
  "ds_read_b64_tr_b16 a[186:187], v190 offset:58368\n\t" \
  "v_lshlrev_b32 v228, 16, v84\n\t" \
  "v_mfma_f32_16x16x32_bf16 a[80:83], a[148:151], v[24:27], a[80:83]\n\t" \
  "ds_read_b64_tr_b16 a[188:189], v191 offset:57344\n\t" \
  "v_and_b32 v229, s98, v84\n\t" \
  "v_mfma_f32_16x16x32_bf16 a[96:99], a[152:155], v[24:27], a[96:99]\n\t" \
  "global_load_dword %[pf], v217, %[kv]\n\t" \
  "v_mfma_f32_16x16x32_bf16 a[100:103], a[152:155], v[56:59], a[100:103]\n\t" \
  "ds_read_b64_tr_b16 a[190:191], v191 offset:58368\n\t" \
  "v_lshlrev_b32 v230, 16, v85\n\t" \
  "v_mfma_f32_16x16x32_bf16 a[104:107], a[152:155], v[88:91], a[104:107]\n\t" \
  "v_and_b32 v231, s98, v85\n\t" \
  "v_lshlrev_b32 v232, 16, v86\n\t" \
  "v_mfma_f32_16x16x32_bf16 a[108:111], a[152:155], v[120:123], a[108:111]\n\t" \
  "v_and_b32 v233, s98, v86\n\t" \
  "v_lshlrev_b32 v234, 16, v87\n\t" \
  "v_mfma_f32_16x16x32_bf16 a[124:127], a[156:159], v[120:123], a[124:127]\n\t" \
  "v_cvt_pk_bf16_f32 v114, v240, v241\n\t" \
  "v_cvt_pk_bf16_f32 v115, v242, v243\n\t" \
  "v_mfma_f32_16x16x32_bf16 a[120:123], a[156:159], v[88:91], a[120:123]\n\t" \
  "s_add_u32 s96, %[rot19], 3145728\n\t" \
  "v_and_b32 v235, s98, v87\n\t" \
  "v_lshlrev_b32 v236, 16, v116\n\t" \
  "v_mfma_f32_16x16x32_bf16 a[116:119], a[156:159], v[56:59], a[116:119]\n\t" \
  "s_waitcnt vmcnt(21) lgkmcnt(0)\n\t" \
  "s_and_b32 s96, s96, 0x780000\n\t" \
  "s_add_u32 m0, %[mw], 49152\n\t" \
  "v_mfma_f32_16x16x32_bf16 a[112:115], a[156:159], v[24:27], a[112:115]\n\t" \
  "s_barrier\n\t" \
  FSTP("v_writelane_b32 %[stv], s94, 40\n\t") \
  FSTP("v_writelane_b32 %[stv], s92, 41\n\t") \
  FSTP("s_memrealtime s[94:95]\n\t") \
  FSTP("s_memtime s[92:93]\n\t") \
  "v_mfma_f32_16x16x32_bf16 v[0:3], a[160:163], v[28:31], v[0:3]\n\t" \
  "buffer_load_dwordx4 v200, %[sw], s96 offen offset:0 lds\n\t" \
  "v_mfma_f32_16x16x32_bf16 v[32:35], a[160:163], v[60:63], v[32:35]\n\t" \
  "s_waitcnt vmcnt(14)\n\t" \
  "ds_read_b128 v[24:27], v204 offset:0\n\t" \
  "ds_read_b64_tr_b16 a[128:129], v192 offset:32768\n\t" \
  "v_and_b32 v237, s98, v116\n\t" \
  "v_mfma_f32_16x16x32_bf16 v[64:67], a[160:163], v[92:95], v[64:67]\n\t" \
  "ds_read_b128 v[56:59], v204 offset:2048\n\t" \
  "ds_read_b64_tr_b16 a[130:131], v192 offset:33792\n\t" \
  "v_cvt_pk_bf16_f32 v21, v178, v179\n\t" \
  "v_mfma_f32_16x16x32_bf16 v[96:99], a[160:163], v[124:127], v[96:99]\n\t" \
  "ds_read_b128 v[88:91], v204 offset:4096\n\t" \
  "v_lshlrev_b32 v238, 16, v117\n\t" \
  "v_mfma_f32_16x16x32_bf16 v[100:103], a[164:167], v[124:127], v[100:103]\n\t" \
  "ds_read_b128 v[120:123], v204 offset:6144\n\t" \
  "v_cvt_pk_bf16_f32 v22, v180, v181\n\t" \
  "v_mfma_f32_16x16x32_bf16 v[68:71], a[164:167], v[92:95], v[68:71]\n\t" \
  "buffer_load_dwordx4 v201, %[sw], s96 offen offset:1024 lds\n\t" \
  "v_mfma_f32_16x16x32_bf16 v[36:39], a[164:167], v[60:63], v[36:39]\n\t" \
  "ds_read_b64_tr_b16 a[132:133], v193 offset:32768\n\t" \
  "v_and_b32 v239, s98, v117\n\t" \
  "v_mfma_f32_16x16x32_bf16 v[4:7], a[164:167], v[28:31], v[4:7]\n\t" \
  "ds_read_b64_tr_b16 a[134:135], v193 offset:33792\n\t" \
  "v_cvt_pk_bf16_f32 v23, v182, v183\n\t" \
  "v_mfma_f32_16x16x32_bf16 v[8:11], a[168:171], v[28:31], v[8:11]\n\t" \
  "buffer_load_dwordx4 v202, %[sw], s96 offen offset:2048 lds\n\t" \
  "v_mfma_f32_16x16x32_bf16 v[40:43], a[168:171], v[60:63], v[40:43]\n\t" \
  "ds_read_b64_tr_b16 a[136:137], v194 offset:32768\n\t" \
  "v_lshlrev_b32 v240, 16, v118\n\t" \
  "v_mfma_f32_16x16x32_bf16 v[72:75], a[168:171], v[92:95], v[72:75]\n\t" \
  "ds_read_b64_tr_b16 a[138:139], v194 offset:33792\n\t" \
  "v_and_b32 v241, s98, v118\n\t" \
  "v_mfma_f32_16x16x32_bf16 v[104:107], a[168:171], v[124:127], v[104:107]\n\t" \
  "ds_read_b64_tr_b16 a[140:141], v195 offset:32768\n\t" \
  "v_lshlrev_b32 v242, 16, v119\n\t" \
  "v_mfma_f32_16x16x32_bf16 v[108:111], a[172:175], v[124:127], v[108:111]\n\t" \
  "buffer_load_dwordx4 v203, %[sw], s96 offen offset:3072 lds\n\t" \
  "v_mfma_f32_16x16x32_bf16 v[76:79], a[172:175], v[92:95], v[76:79]\n\t" \
  "s_add_u32 s96, %[rot19], 3670016\n\t" \
  "s_and_b32 s96, s96, 0x780000\n\t" \
  "s_add_u32 m0, %[mw], 65536\n\t" \
  "v_mfma_f32_16x16x32_bf16 v[44:47], a[172:175], v[60:63], v[44:47]\n\t" \
  "ds_read_b64_tr_b16 a[142:143], v195 offset:33792\n\t" \
  "v_and_b32 v243, s98, v119\n\t" \
  "v_mfma_f32_16x16x32_bf16 v[12:15], a[172:175], v[28:31], v[12:15]\n\t" \
  "buffer_load_dwordx4 v200, %[sw], s96 offen offset:0 lds\n\t" \
  "ds_read_b64_tr_b16 a[144:145], v196 offset:32768\n\t" \
  "ds_read_b64_tr_b16 a[146:147], v196 offset:33792\n\t" \
  "v_cvt_pk_bf16_f32 v52, v220, v221\n\t" \
  "v_cvt_pk_bf16_f32 v53, v222, v223\n\t" \
  "v_pk_fma_f32 v[228:229], v[228:229], v[168:169], v[170:171]\n\t" \
  "v_pk_fma_f32 v[230:231], v[230:231], v[168:169], v[170:171]\n\t" \
  "v_pk_fma_f32 v[232:233], v[232:233], v[168:169], v[170:171]\n\t" \
  "v_pk_fma_f32 v[234:235], v[234:235], v[168:169], v[170:171]\n\t" \
  "v_pk_fma_f32 v[228:229], v[228:229], v[144:145], v[152:153]\n\t" \
  "v_pk_fma_f32 v[230:231], v[230:231], v[146:147], v[154:155]\n\t" \
  "v_pk_fma_f32 v[232:233], v[232:233], v[148:149], v[156:157]\n\t" \
  "v_pk_fma_f32 v[234:235], v[234:235], v[150:151], v[158:159]\n\t" \
  "v_pk_fma_f32 v[236:237], v[236:237], v[172:173], v[174:175]\n\t" \
  "v_pk_fma_f32 v[238:239], v[238:239], v[172:173], v[174:175]\n\t" \
  "v_pk_fma_f32 v[240:241], v[240:241], v[172:173], v[174:175]\n\t" \
  "v_pk_fma_f32 v[242:243], v[242:243], v[172:173], v[174:175]\n\t" \
  "v_pk_fma_f32 v[236:237], v[236:237], v[144:145], v[152:153]\n\t" \
  "v_pk_fma_f32 v[238:239], v[238:239], v[146:147], v[154:155]\n\t" \
  "v_pk_fma_f32 v[240:241], v[240:241], v[148:149], v[156:157]\n\t" \
  "v_pk_fma_f32 v[242:243], v[242:243], v[150:151], v[158:159]\n\t" \
  "v_cvt_pk_bf16_f32 v84, v228, v229\n\t" \
  "v_mfma_f32_16x16x32_bf16 a[64:67], a[176:179], v[28:31], a[64:67]\n\t" \
  "s_waitcnt lgkmcnt(8)\n\t" \
  "v_lshlrev_b32 v176, 16, v24\n\t" \
  "v_and_b32 v177, s98, v24\n\t" \
  "v_mfma_f32_16x16x32_bf16 a[68:71], a[176:179], v[60:63], a[68:71]\n\t" \
  "ds_read_b64_tr_b16 a[148:149], v197 offset:32768\n\t" \
  "v_lshlrev_b32 v178, 16, v25\n\t" \
  "v_mfma_f32_16x16x32_bf16 a[72:75], a[176:179], v[92:95], a[72:75]\n\t" \
  "ds_read_b64_tr_b16 a[150:151], v197 offset:33792\n\t" \
  "v_and_b32 v179, s98, v25\n\t" \
  "v_mfma_f32_16x16x32_bf16 a[76:79], a[176:179], v[124:127], a[76:79]\n\t" \
  "buffer_load_dwordx4 v201, %[sw], s96 offen offset:1024 lds\n\t" \
  "v_mfma_f32_16x16x32_bf16 a[92:95], a[180:183], v[124:127], a[92:95]\n\t" \
  "ds_read_b64_tr_b16 a[152:153], v198 offset:32768\n\t" \
  "v_lshlrev_b32 v180, 16, v26\n\t" \
  "v_mfma_f32_16x16x32_bf16 a[88:91], a[180:183], v[92:95], a[88:91]\n\t" \
  "v_and_b32 v181, s98, v26\n\t" \
  "v_lshlrev_b32 v182, 16, v27\n\t" \
  "v_mfma_f32_16x16x32_bf16 a[84:87], a[180:183], v[60:63], a[84:87]\n\t" \
  "ds_read_b64_tr_b16 a[154:155], v198 offset:33792\n\t" \
  "v_cvt_pk_bf16_f32 v54, v224, v225\n\t" \
  "v_mfma_f32_16x16x32_bf16 a[80:83], a[180:183], v[28:31], a[80:83]\n\t" \
  "buffer_load_dwordx4 v202, %[sw], s96 offen offset:2048 lds\n\t" \
  "v_mfma_f32_16x16x32_bf16 a[96:99], a[184:187], v[28:31], a[96:99]\n\t" \
  "ds_read_b64_tr_b16 a[156:157], v199 offset:32768\n\t" \
  "v_and_b32 v183, s98, v27\n\t" \
  "v_mfma_f32_16x16x32_bf16 a[100:103], a[184:187], v[60:63], a[100:103]\n\t" \
  "ds_read_b64_tr_b16 a[158:159], v199 offset:33792\n\t" \
  "v_cvt_pk_bf16_f32 v55, v226, v227\n\t" \
  "v_mfma_f32_16x16x32_bf16 a[104:107], a[184:187], v[92:95], a[104:107]\n\t" \
  "v_lshlrev_b32 v220, 16, v56\n\t" \
  "v_and_b32 v221, s98, v56\n\t" \
  "v_mfma_f32_16x16x32_bf16 a[108:111], a[184:187], v[124:127], a[108:111]\n\t" \
  "buffer_load_dwordx4 v203, %[sw], s96 offen offset:3072 lds\n\t" \
  "v_mfma_f32_16x16x32_bf16 a[124:127], a[188:191], v[124:127], a[124:127]\n\t" \
  "v_lshlrev_b32 v222, 16, v57\n\t" \
  "v_and_b32 v223, s98, v57\n\t" \
  "v_mfma_f32_16x16x32_bf16 a[120:123], a[188:191], v[92:95], a[120:123]\n\t" \
  "v_lshlrev_b32 v224, 16, v58\n\t" \
  "v_cvt_pk_bf16_f32 v85, v230, v231\n\t" \
  "v_mfma_f32_16x16x32_bf16 a[116:119], a[188:191], v[60:63], a[116:119]\n\t" \
  "v_and_b32 v225, s98, v58\n\t" \
  "v_cvt_pk_bf16_f32 v86, v232, v233\n\t" \
  "v_mfma_f32_16x16x32_bf16 a[112:115], a[188:191], v[28:31], a[112:115]\n\t" \
  "s_waitcnt lgkmcnt(0)\n\t" \
  "v_lshlrev_b32 v226, 16, v59\n\t" \
  "v_and_b32 v227, s98, v59\n\t" \
  FSTP("v_writelane_b32 %[stv], s94, 42\n\t") \
  FSTP("v_writelane_b32 %[stv], s92, 43\n\t") \
  FSTP("s_memrealtime s[94:95]\n\t") \
  FSTP("s_memtime s[92:93]\n\t") \
  "v_mfma_f32_16x16x32_bf16 v[0:3], a[128:131], v[16:19], v[0:3]\n\t" \
  "buffer_load_dword %[fl], v210, %[ws], %[fo] offen sc1\n\t" \
  "v_mfma_f32_16x16x32_bf16 v[32:35], a[128:131], v[48:51], v[32:35]\n\t" \
  "ds_read_b128 v[28:31], v205 offset:0\n\t" \
  "v_cvt_pk_bf16_f32 v87, v234, v235\n\t" \
  "v_mfma_f32_16x16x32_bf16 v[64:67], a[128:131], v[80:83], v[64:67]\n\t" \
  "s_lshr_b32 s99, %[c17], 9\n\t" \
  "ds_read_b128 v[60:63], v205 offset:2048\n\t" \
  "v_lshlrev_b32 v228, 16, v88\n\t" \
  "v_mfma_f32_16x16x32_bf16 v[96:99], a[128:131], v[112:115], v[96:99]\n\t" \
  "ds_read_b128 v[92:95], v205 offset:4096\n\t" \
  "ds_read_b64_tr_b16 a[160:161], v192 offset:40960\n\t" \
  "v_and_b32 v229, s98, v88\n\t" \
  "v_mfma_f32_16x16x32_bf16 v[100:103], a[132:135], v[112:115], v[100:103]\n\t" \
  "ds_read_b128 v[124:127], v205 offset:6144\n\t" \
  "v_xor_b32 v248, s99, v214\n\t" \
  "v_mfma_f32_16x16x32_bf16 v[68:71], a[132:135], v[80:83], v[68:71]\n\t" \
  "v_xor_b32 v249, 256, v248\n\t" \
  "v_xor_b32 v250, 512, v248\n\t" \
  "v_mfma_f32_16x16x32_bf16 v[36:39], a[132:135], v[48:51], v[36:39]\n\t" \
  "v_xor_b32 v251, 768, v248\n\t" \
  "s_add_u32 s96, %[rot19], 4194304\n\t" \
  "ds_read_b64_tr_b16 a[162:163], v192 offset:41984\n\t" \
  "s_and_b32 s96, s96, 0x780000\n\t" \
  "v_mfma_f32_16x16x32_bf16 v[4:7], a[132:135], v[16:19], v[4:7]\n\t" \
  "s_add_u32 m0, %[mw], 81920\n\t" \
  "ds_read_b128 v[128:131], v208 offset:1536\n\t" \
  "ds_read_b64_tr_b16 a[164:165], v193 offset:40960\n\t" \
  "v_lshlrev_b32 v230, 16, v89\n\t" \
  "v_mfma_f32_16x16x32_bf16 v[8:11], a[136:139], v[16:19], v[8:11]\n\t" \
  "buffer_load_dwordx4 v200, %[sw], s96 offen offset:0 lds\n\t" \
  "v_mfma_f32_16x16x32_bf16 v[40:43], a[136:139], v[48:51], v[40:43]\n\t" \
  "ds_read_b64_tr_b16 a[166:167], v193 offset:41984\n\t" \
  "v_and_b32 v231, s98, v89\n\t" \
  "v_mfma_f32_16x16x32_bf16 v[72:75], a[136:139], v[80:83], v[72:75]\n\t" \
  "ds_read_b64_tr_b16 a[168:169], v194 offset:40960\n\t" \
  "v_cvt_pk_bf16_f32 v116, v236, v237\n\t" \
  "v_mfma_f32_16x16x32_bf16 v[104:107], a[136:139], v[112:115], v[104:107]\n\t" \
  "ds_read_b64_tr_b16 a[170:171], v194 offset:41984\n\t" \
  "v_lshlrev_b32 v232, 16, v90\n\t" \
  "v_mfma_f32_16x16x32_bf16 v[108:111], a[140:143], v[112:115], v[108:111]\n\t" \
  "buffer_load_dwordx4 v201, %[sw], s96 offen offset:1024 lds\n\t" \
  "v_mfma_f32_16x16x32_bf16 v[76:79], a[140:143], v[80:83], v[76:79]\n\t" \
  "ds_read_b64_tr_b16 a[172:173], v195 offset:40960\n\t" \
  "v_cvt_pk_bf16_f32 v117, v238, v239\n\t" \
  "v_mfma_f32_16x16x32_bf16 v[44:47], a[140:143], v[48:51], v[44:47]\n\t" \
  "ds_read_b64_tr_b16 a[174:175], v195 offset:41984\n\t" \
  "v_and_b32 v233, s98, v90\n\t" \
  "v_mfma_f32_16x16x32_bf16 v[12:15], a[140:143], v[16:19], v[12:15]\n\t" \
  "ds_read_b64_tr_b16 a[176:177], v196 offset:40960\n\t" \
  "v_cvt_pk_bf16_f32 v118, v240, v241\n\t" \
  "v_mfma_f32_16x16x32_bf16 a[64:67], a[144:147], v[16:19], a[64:67]\n\t" \
  "ds_read_b64_tr_b16 a[178:179], v196 offset:41984\n\t" \
  "v_lshlrev_b32 v234, 16, v91\n\t" \
  "v_mfma_f32_16x16x32_bf16 a[68:71], a[144:147], v[48:51], a[68:71]\n\t" \
  "ds_read_b64_tr_b16 a[180:181], v197 offset:40960\n\t" \
  "v_cvt_pk_bf16_f32 v119, v242, v243\n\t" \
  "v_mfma_f32_16x16x32_bf16 a[72:75], a[144:147], v[80:83], a[72:75]\n\t" \
  "buffer_load_dwordx4 v202, %[sw], s96 offen offset:2048 lds\n\t" \
  "v_mfma_f32_16x16x32_bf16 a[76:79], a[144:147], v[112:115], a[76:79]\n\t" \
  "ds_read_b64_tr_b16 a[182:183], v197 offset:41984\n\t" \
  "v_and_b32 v235, s98, v91\n\t" \
  "v_mfma_f32_16x16x32_bf16 a[92:95], a[148:151], v[112:115], a[92:95]\n\t" \
  "ds_read_b64_tr_b16 a[184:185], v198 offset:40960\n\t" \
  "v_lshlrev_b32 v236, 16, v120\n\t" \
  "v_mfma_f32_16x16x32_bf16 a[88:91], a[148:151], v[80:83], a[88:91]\n\t" \
  "ds_read_b64_tr_b16 a[186:187], v198 offset:41984\n\t" \
  "v_and_b32 v237, s98, v120\n\t" \
  "v_mfma_f32_16x16x32_bf16 a[84:87], a[148:151], v[48:51], a[84:87]\n\t" \
  "ds_read_b128 v[132:135], v208 offset:1552\n\t" \
  "v_lshlrev_b32 v238, 16, v121\n\t" \
  "v_mfma_f32_16x16x32_bf16 a[80:83], a[148:151], v[16:19], a[80:83]\n\t" \
  "ds_read_b64_tr_b16 a[188:189], v199 offset:40960\n\t" \
  "v_and_b32 v239, s98, v121\n\t" \
  "v_mfma_f32_16x16x32_bf16 a[96:99], a[152:155], v[16:19], a[96:99]\n\t" \
  "buffer_load_dwordx4 v203, %[sw], s96 offen offset:3072 lds\n\t" \
  "v_mfma_f32_16x16x32_bf16 a[100:103], a[152:155], v[48:51], a[100:103]\n\t" \
  "ds_read_b64_tr_b16 a[190:191], v199 offset:41984\n\t" \
  "v_lshlrev_b32 v240, 16, v122\n\t" \
  "v_mfma_f32_16x16x32_bf16 a[104:107], a[152:155], v[80:83], a[104:107]\n\t" \
  "ds_read_b128 v[136:139], v208 offset:1792\n\t" \
  "v_and_b32 v241, s98, v122\n\t" \
  "v_mfma_f32_16x16x32_bf16 a[108:111], a[152:155], v[112:115], a[108:111]\n\t" \
  "ds_read_b128 v[140:143], v208 offset:1808\n\t" \
  "v_lshlrev_b32 v242, 16, v123\n\t" \
  "v_mfma_f32_16x16x32_bf16 a[124:127], a[156:159], v[112:115], a[124:127]\n\t" \
  "ds_read_b128 v[144:147], v208 offset:1664\n\t" \
  "ds_read_b128 v[148:151], v208 offset:1680\n\t" \
  "ds_read_b128 v[152:155], v208 offset:1920\n\t" \
  "v_and_b32 v243, s98, v123\n\t" \
  "s_waitcnt lgkmcnt(3)\n\t" \
  "v_pk_fma_f32 v[176:177], v[176:177], v[160:161], v[162:163]\n\t" \
  "v_pk_fma_f32 v[178:179], v[178:179], v[160:161], v[162:163]\n\t" \
  "v_pk_fma_f32 v[180:181], v[180:181], v[160:161], v[162:163]\n\t" \
  "v_pk_fma_f32 v[182:183], v[182:183], v[160:161], v[162:163]\n\t" \
  "v_pk_fma_f32 v[176:177], v[176:177], v[128:129], v[136:137]\n\t" \
  "v_pk_fma_f32 v[178:179], v[178:179], v[130:131], v[138:139]\n\t" \
  "v_pk_fma_f32 v[180:181], v[180:181], v[132:133], v[140:141]\n\t" \
  "v_pk_fma_f32 v[182:183], v[182:183], v[134:135], v[142:143]\n\t" \
  "v_pk_fma_f32 v[220:221], v[220:221], v[164:165], v[166:167]\n\t" \
  "v_pk_fma_f32 v[222:223], v[222:223], v[164:165], v[166:167]\n\t" \
  "v_pk_fma_f32 v[224:225], v[224:225], v[164:165], v[166:167]\n\t" \
  "v_pk_fma_f32 v[226:227], v[226:227], v[164:165], v[166:167]\n\t" \
  "v_pk_fma_f32 v[220:221], v[220:221], v[128:129], v[136:137]\n\t" \
  "v_pk_fma_f32 v[222:223], v[222:223], v[130:131], v[138:139]\n\t" \
  "v_pk_fma_f32 v[224:225], v[224:225], v[132:133], v[140:141]\n\t" \
  "v_pk_fma_f32 v[226:227], v[226:227], v[134:135], v[142:143]\n\t" \
  "v_cvt_pk_bf16_f32 v24, v176, v177\n\t" \
  "v_mfma_f32_16x16x32_bf16 a[120:123], a[156:159], v[80:83], a[120:123]\n\t" \
  "v_cvt_pk_bf16_f32 v25, v178, v179\n\t" \
  "v_cvt_pk_bf16_f32 v26, v180, v181\n\t" \
  "v_cvt_pk_bf16_f32 v27, v182, v183\n\t" \
  "v_cvt_pk_bf16_f32 v56, v220, v221\n\t" \
  "v_cvt_pk_bf16_f32 v57, v222, v223\n\t" \
  "v_pk_fma_f32 v[228:229], v[228:229], v[168:169], v[170:171]\n\t" \
  "v_pk_fma_f32 v[230:231], v[230:231], v[168:169], v[170:171]\n\t" \
  "v_pk_fma_f32 v[232:233], v[232:233], v[168:169], v[170:171]\n\t" \
  "v_pk_fma_f32 v[234:235], v[234:235], v[168:169], v[170:171]\n\t" \
  "v_pk_fma_f32 v[228:229], v[228:229], v[128:129], v[136:137]\n\t" \
  "v_pk_fma_f32 v[230:231], v[230:231], v[130:131], v[138:139]\n\t" \
  "v_pk_fma_f32 v[232:233], v[232:233], v[132:133], v[140:141]\n\t" \
  "v_pk_fma_f32 v[234:235], v[234:235], v[134:135], v[142:143]\n\t" \
  "v_pk_fma_f32 v[236:237], v[236:237], v[172:173], v[174:175]\n\t" \
  "v_pk_fma_f32 v[238:239], v[238:239], v[172:173], v[174:175]\n\t" \
  "v_pk_fma_f32 v[240:241], v[240:241], v[172:173], v[174:175]\n\t" \
  "v_pk_fma_f32 v[242:243], v[242:243], v[172:173], v[174:175]\n\t" \
  "v_pk_fma_f32 v[236:237], v[236:237], v[128:129], v[136:137]\n\t" \
  "v_pk_fma_f32 v[238:239], v[238:239], v[130:131], v[138:139]\n\t" \
  "v_pk_fma_f32 v[240:241], v[240:241], v[132:133], v[140:141]\n\t" \
  "v_pk_fma_f32 v[242:243], v[242:243], v[134:135], v[142:143]\n\t" \
  "v_cvt_pk_bf16_f32 v88, v228, v229\n\t" \
  "v_mfma_f32_16x16x32_bf16 a[116:119], a[156:159], v[48:51], a[116:119]\n\t" \
  "s_waitcnt vmcnt(22)\n\t" \
  "v_cvt_pk_bf16_f32 v58, v224, v225\n\t" \
  "v_cvt_pk_bf16_f32 v59, v226, v227\n\t" \
  "v_mfma_f32_16x16x32_bf16 a[112:115], a[156:159], v[16:19], a[112:115]\n\t" \
  "s_barrier\n\t" \
  FSTP("v_writelane_b32 %[stv], s94, 44\n\t") \
  FSTP("v_writelane_b32 %[stv], s92, 45\n\t") \
  FSTP("s_memrealtime s[94:95]\n\t") \
  FSTP("s_memtime s[92:93]\n\t") \
  "v_mfma_f32_16x16x32_bf16 v[0:3], a[160:163], v[20:23], v[0:3]\n\t" \
  "s_waitcnt vmcnt(4)\n\t" \
  "v_cmp_ne_u32 vcc, v211, %[fl]\n\t" \
  "v_cvt_pk_bf16_f32 v89, v230, v231\n\t" \
  "v_mfma_f32_16x16x32_bf16 v[32:35], a[160:163], v[52:55], v[32:35]\n\t" \
  "ds_read_b64_tr_b16 a[128:129], v184 offset:0\n\t" \
  "v_cvt_pk_bf16_f32 v90, v232, v233\n\t" \
  "v_mfma_f32_16x16x32_bf16 v[64:67], a[160:163], v[84:87], v[64:67]\n\t" \
  "s_cbranch_vccnz .Lfq_s\n\t" \
  ".Lfq_k:\n\t" \
  "v_mfma_f32_16x16x32_bf16 v[96:99], a[160:163], v[116:119], v[96:99]\n\t" \
  "s_add_u32 s97, %[rot19], 2097152\n\t" \
  "s_bfe_u32 s97, s97, 0x140003\n\t" \
  "s_add_u32 s97, s97, %[sox]\n\t" \
  "v_mfma_f32_16x16x32_bf16 v[100:103], a[164:167], v[116:119], v[100:103]\n\t" \
  "buffer_load_dwordx4 a[0:3], v248, %[ws], s97 offen sc1\n\t" \
  "v_mfma_f32_16x16x32_bf16 v[68:71], a[164:167], v[84:87], v[68:71]\n\t" \
  "buffer_load_dwordx4 a[4:7], v249, %[ws], s97 offen sc1\n\t" \
  "v_mfma_f32_16x16x32_bf16 v[36:39], a[164:167], v[52:55], v[36:39]\n\t" \
  "ds_read_b64_tr_b16 a[130:131], v184 offset:1024\n\t" \
  "v_cvt_pk_bf16_f32 v91, v234, v235\n\t" \
  "v_mfma_f32_16x16x32_bf16 v[4:7], a[164:167], v[20:23], v[4:7]\n\t" \
  "ds_read_b64_tr_b16 a[132:133], v185 offset:0\n\t" \
  "v_cvt_pk_bf16_f32 v120, v236, v237\n\t" \
  "v_mfma_f32_16x16x32_bf16 v[8:11], a[168:171], v[20:23], v[8:11]\n\t" \
  "buffer_load_dwordx4 a[8:11], v250, %[ws], s97 offen sc1\n\t" \
  "v_mfma_f32_16x16x32_bf16 v[40:43], a[168:171], v[52:55], v[40:43]\n\t" \
  "ds_read_b64_tr_b16 a[134:135], v185 offset:1024\n\t" \
  "v_cvt_pk_bf16_f32 v121, v238, v239\n\t" \
  "v_mfma_f32_16x16x32_bf16 v[72:75], a[168:171], v[84:87], v[72:75]\n\t" \
  "ds_read_b64_tr_b16 a[136:137], v186 offset:0\n\t" \
  "v_cvt_pk_bf16_f32 v122, v240, v241\n\t" \
  "v_mfma_f32_16x16x32_bf16 v[104:107], a[168:171], v[116:119], v[104:107]\n\t" \
  "ds_read_b64_tr_b16 a[138:139], v186 offset:1024\n\t" \
  "v_cvt_pk_bf16_f32 v123, v242, v243\n\t" \
  "v_mfma_f32_16x16x32_bf16 v[108:111], a[172:175], v[116:119], v[108:111]\n\t" \
  ".Lhs1:\n\ts_cbranch_execz .Lhs1n\n\t" \
  "buffer_load_dwordx4 a[12:15], v251, %[ws], s97 offen sc1\n\t" \
  "v_mfma_f32_16x16x32_bf16 v[76:79], a[172:175], v[84:87], v[76:79]\n\t" \
  "ds_read_b64_tr_b16 a[140:141], v187 offset:0\n\t" \
  "s_add_u32 s97, s97, %[dk]\n\t" \
  "v_mfma_f32_16x16x32_bf16 v[44:47], a[172:175], v[52:55], v[44:47]\n\t" \
  "v_lshlrev_b32 v176, 16, v28\n\t" \
  "v_and_b32 v177, s98, v28\n\t" \
  "v_mfma_f32_16x16x32_bf16 v[12:15], a[172:175], v[20:23], v[12:15]\n\t" \
  "buffer_load_dwordx4 a[16:19], v248, %[ws], s97 offen sc1\n\t" \
  "v_mfma_f32_16x16x32_bf16 a[64:67], a[176:179], v[20:23], a[64:67]\n\t" \
  "ds_read_b64_tr_b16 a[142:143], v187 offset:1024\n\t" \
  "v_lshlrev_b32 v178, 16, v29\n\t" \
  "v_mfma_f32_16x16x32_bf16 a[68:71], a[176:179], v[52:55], a[68:71]\n\t" \
  "ds_read_b64_tr_b16 a[144:145], v188 offset:0\n\t" \
  "v_and_b32 v179, s98, v29\n\t" \
  "v_mfma_f32_16x16x32_bf16 a[72:75], a[176:179], v[84:87], a[72:75]\n\t" \
  "ds_read_b64_tr_b16 a[146:147], v188 offset:1024\n\t" \
  "v_lshlrev_b32 v180, 16, v30\n\t" \
  "v_mfma_f32_16x16x32_bf16 a[76:79], a[176:179], v[116:119], a[76:79]\n\t" \
  "buffer_load_dwordx4 a[20:23], v249, %[ws], s97 offen sc1\n\t" \
  "v_mfma_f32_16x16x32_bf16 a[92:95], a[180:183], v[116:119], a[92:95]\n\t" \
  "ds_read_b64_tr_b16 a[148:149], v189 offset:0\n\t" \
  "v_and_b32 v181, s98, v30\n\t" \
  "v_mfma_f32_16x16x32_bf16 a[88:91], a[180:183], v[84:87], a[88:91]\n\t" \
  "ds_read_b64_tr_b16 a[150:151], v189 offset:1024\n\t" \
  "v_lshlrev_b32 v182, 16, v31\n\t" \
  "v_mfma_f32_16x16x32_bf16 a[84:87], a[180:183], v[52:55], a[84:87]\n\t" \
  "ds_read_b64_tr_b16 a[152:153], v190 offset:0\n\t" \
  "v_and_b32 v183, s98, v31\n\t" \
  "v_mfma_f32_16x16x32_bf16 a[80:83], a[180:183], v[20:23], a[80:83]\n\t" \
  "buffer_load_dwordx4 a[24:27], v250, %[ws], s97 offen sc1\n\t" \
  "v_mfma_f32_16x16x32_bf16 a[96:99], a[184:187], v[20:23], a[96:99]\n\t" \
  "ds_read_b64_tr_b16 a[154:155], v190 offset:1024\n\t" \
  "v_lshlrev_b32 v220, 16, v60\n\t" \
  "v_mfma_f32_16x16x32_bf16 a[100:103], a[184:187], v[52:55], a[100:103]\n\t" \
  "ds_read_b64_tr_b16 a[156:157], v191 offset:0\n\t" \
  "v_and_b32 v221, s98, v60\n\t" \
  "v_mfma_f32_16x16x32_bf16 a[104:107], a[184:187], v[84:87], a[104:107]\n\t" \
  "ds_read_b64_tr_b16 a[158:159], v191 offset:1024\n\t" \
  "v_lshlrev_b32 v222, 16, v61\n\t" \
  "v_mfma_f32_16x16x32_bf16 a[108:111], a[184:187], v[116:119], a[108:111]\n\t" \
  "buffer_load_dwordx4 a[28:31], v251, %[ws], s97 offen sc1\n\t" \
  "v_mfma_f32_16x16x32_bf16 a[124:127], a[188:191], v[116:119], a[124:127]\n\t" \
  "ds_read_b128 v[156:159], v208 offset:1936\n\t" \
  "v_and_b32 v223, s98, v61\n\t" \
  "v_mfma_f32_16x16x32_bf16 a[120:123], a[188:191], v[84:87], a[120:123]\n\t" \
  "s_add_u32 s97, %[rot19], 2621440\n\t" \
  "v_lshlrev_b32 v224, 16, v62\n\t" \
  "v_and_b32 v225, s98, v62\n\t" \
  "v_mfma_f32_16x16x32_bf16 a[116:119], a[188:191], v[52:55], a[116:119]\n\t" \
  "s_bfe_u32 s97, s97, 0x140003\n\t" \
  "s_add_u32 s97, s97, %[sox]\n\t" \
  "v_lshlrev_b32 v226, 16, v63\n\t" \
  "v_mfma_f32_16x16x32_bf16 a[112:115], a[188:191], v[20:23], a[112:115]\n\t" \
  "s_waitcnt lgkmcnt(1)\n\t" \
  "v_and_b32 v227, s98, v63\n\t" \
  "v_lshlrev_b32 v228, 16, v92\n\t" \
  FSTP("v_writelane_b32 %[stv], s94, 46\n\t") \
  FSTP("v_writelane_b32 %[stv], s92, 47\n\t") \
  FSTP("s_memrealtime s[94:95]\n\t") \
  FSTP("s_memtime s[92:93]\n\t") \
  "v_mfma_f32_16x16x32_bf16 v[0:3], a[128:131], v[24:27], v[0:3]\n\t" \
  "buffer_load_dwordx4 a[32:35], v248, %[ws], s97 offen sc1\n\t" \
  "ds_read_b64_tr_b16 a[160:161], v184 offset:8192\n\t" \
  "ds_read_b64_tr_b16 a[162:163], v184 offset:9216\n\t" \
  "v_and_b32 v229, s98, v92\n\t" \
  "s_waitcnt lgkmcnt(2)\n\t" \
  "v_pk_fma_f32 v[176:177], v[176:177], v[160:161], v[162:163]\n\t" \
  "v_pk_fma_f32 v[178:179], v[178:179], v[160:161], v[162:163]\n\t" \
  "v_pk_fma_f32 v[180:181], v[180:181], v[160:161], v[162:163]\n\t" \
  "v_pk_fma_f32 v[182:183], v[182:183], v[160:161], v[162:163]\n\t" \
  "v_pk_fma_f32 v[176:177], v[176:177], v[144:145], v[152:153]\n\t" \
  "v_pk_fma_f32 v[178:179], v[178:179], v[146:147], v[154:155]\n\t" \
  "v_pk_fma_f32 v[180:181], v[180:181], v[148:149], v[156:157]\n\t" \
  "v_pk_fma_f32 v[182:183], v[182:183], v[150:151], v[158:159]\n\t" \
  "v_pk_fma_f32 v[220:221], v[220:221], v[164:165], v[166:167]\n\t" \
  "v_pk_fma_f32 v[222:223], v[222:223], v[164:165], v[166:167]\n\t" \
  "v_pk_fma_f32 v[224:225], v[224:225], v[164:165], v[166:167]\n\t" \
  "v_pk_fma_f32 v[226:227], v[226:227], v[164:165], v[166:167]\n\t" \
  "v_pk_fma_f32 v[220:221], v[220:221], v[144:145], v[152:153]\n\t" \
  "v_pk_fma_f32 v[222:223], v[222:223], v[146:147], v[154:155]\n\t" \
  "v_pk_fma_f32 v[224:225], v[224:225], v[148:149], v[156:157]\n\t" \
  "v_pk_fma_f32 v[226:227], v[226:227], v[150:151], v[158:159]\n\t" \
  "v_cvt_pk_bf16_f32 v28, v176, v177\n\t" \
  "v_mfma_f32_16x16x32_bf16 v[32:35], a[128:131], v[56:59], v[32:35]\n\t" \
  "v_lshlrev_b32 v230, 16, v93\n\t" \
  "v_and_b32 v231, s98, v93\n\t" \
  "v_mfma_f32_16x16x32_bf16 v[64:67], a[128:131], v[88:91], v[64:67]\n\t" \
  "v_lshlrev_b32 v232, 16, v94\n\t" \
  "v_and_b32 v233, s98, v94\n\t" \
  "v_mfma_f32_16x16x32_bf16 v[96:99], a[128:131], v[120:123], v[96:99]\n\t" \
  "v_lshlrev_b32 v234, 16, v95\n\t" \
  "v_and_b32 v235, s98, v95\n\t" \
  "v_mfma_f32_16x16x32_bf16 v[100:103], a[132:135], v[120:123], v[100:103]\n\t" \
  "buffer_load_dwordx4 a[36:39], v249, %[ws], s97 offen sc1\n\t" \
  "v_mfma_f32_16x16x32_bf16 v[68:71], a[132:135], v[88:91], v[68:71]\n\t" \
  "ds_read_b64_tr_b16 a[164:165], v185 offset:8192\n\t" \
  "v_lshlrev_b32 v236, 16, v124\n\t" \
  "v_mfma_f32_16x16x32_bf16 v[36:39], a[132:135], v[56:59], v[36:39]\n\t" \
  "ds_read_b64_tr_b16 a[166:167], v185 offset:9216\n\t" \
  "v_and_b32 v237, s98, v124\n\t" \
  "v_mfma_f32_16x16x32_bf16 v[4:7], a[132:135], v[24:27], v[4:7]\n\t" \
  "ds_read_b64_tr_b16 a[168:169], v186 offset:8192\n\t" \
  "v_cvt_pk_bf16_f32 v29, v178, v179\n\t" \
  "v_mfma_f32_16x16x32_bf16 v[8:11], a[136:139], v[24:27], v[8:11]\n\t" \
  "buffer_load_dwordx4 a[40:43], v250, %[ws], s97 offen sc1\n\t" \
  "v_mfma_f32_16x16x32_bf16 v[40:43], a[136:139], v[56:59], v[40:43]\n\t" \
  "ds_read_b64_tr_b16 a[170:171], v186 offset:9216\n\t" \
  "v_lshlrev_b32 v238, 16, v125\n\t" \
  "v_mfma_f32_16x16x32_bf16 v[72:75], a[136:139], v[88:91], v[72:75]\n\t" \
  "ds_read_b64_tr_b16 a[172:173], v187 offset:8192\n\t" \
  "v_and_b32 v239, s98, v125\n\t" \
  "v_mfma_f32_16x16x32_bf16 v[104:107], a[136:139], v[120:123], v[104:107]\n\t" \
  "v_cvt_pk_bf16_f32 v30, v180, v181\n\t" \
  "v_cvt_pk_bf16_f32 v31, v182, v183\n\t" \
  "v_mfma_f32_16x16x32_bf16 v[108:111], a[140:143], v[120:123], v[108:111]\n\t" \
  "buffer_load_dwordx4 a[44:47], v251, %[ws], s97 offen sc1\n\t" \
  "v_mfma_f32_16x16x32_bf16 v[76:79], a[140:143], v[88:91], v[76:79]\n\t" \
  "s_add_u32 s97, s97, %[dk]\n\t" \
  "ds_read_b64_tr_b16 a[174:175], v187 offset:9216\n\t" \
  "v_lshlrev_b32 v240, 16, v126\n\t" \
  "v_mfma_f32_16x16x32_bf16 v[44:47], a[140:143], v[56:59], v[44:47]\n\t" \
  "ds_read_b64_tr_b16 a[176:177], v188 offset:8192\n\t" \
  "v_and_b32 v241, s98, v126\n\t" \
  "v_mfma_f32_16x16x32_bf16 v[12:15], a[140:143], v[24:27], v[12:15]\n\t" \
  "buffer_load_dwordx4 a[48:51], v248, %[ws], s97 offen sc1\n\t" \
  "v_mfma_f32_16x16x32_bf16 a[64:67], a[144:147], v[24:27], a[64:67]\n\t" \
  "ds_read_b64_tr_b16 a[178:179], v188 offset:9216\n\t" \
  "v_cvt_pk_bf16_f32 v60, v220, v221\n\t" \
  "v_mfma_f32_16x16x32_bf16 a[68:71], a[144:147], v[56:59], a[68:71]\n\t" \
  "ds_read_b64_tr_b16 a[180:181], v189 offset:8192\n\t" \
  "v_cvt_pk_bf16_f32 v61, v222, v223\n\t" \
  "v_mfma_f32_16x16x32_bf16 a[72:75], a[144:147], v[88:91], a[72:75]\n\t" \
  "ds_read_b64_tr_b16 a[182:183], v189 offset:9216\n\t" \
  "v_lshlrev_b32 v242, 16, v127\n\t" \
  "v_mfma_f32_16x16x32_bf16 a[76:79], a[144:147], v[120:123], a[76:79]\n\t" \
  "buffer_load_dwordx4 a[52:55], v249, %[ws], s97 offen sc1\n\t" \
  "v_mfma_f32_16x16x32_bf16 a[92:95], a[148:151], v[120:123], a[92:95]\n\t" \
  "ds_read_b64_tr_b16 a[184:185], v190 offset:8192\n\t" \
  "v_and_b32 v243, s98, v127\n\t" \
  "v_mfma_f32_16x16x32_bf16 a[88:91], a[148:151], v[88:91], a[88:91]\n\t" \
  "buffer_load_dwordx4 a[56:59], v250, %[ws], s97 offen sc1\n\t" \
  "ds_read_b64_tr_b16 a[186:187], v190 offset:9216\n\t" \
  "ds_read_b64_tr_b16 a[188:189], v191 offset:8192\n\t" \
  "v_cvt_pk_bf16_f32 v62, v224, v225\n\t" \
  "v_cvt_pk_bf16_f32 v63, v226, v227\n\t" \
  "v_pk_fma_f32 v[228:229], v[228:229], v[168:169], v[170:171]\n\t" \
  "v_pk_fma_f32 v[230:231], v[230:231], v[168:169], v[170:171]\n\t" \
  "v_pk_fma_f32 v[232:233], v[232:233], v[168:169], v[170:171]\n\t" \
  "v_pk_fma_f32 v[234:235], v[234:235], v[168:169], v[170:171]\n\t" \
  "v_pk_fma_f32 v[228:229], v[228:229], v[144:145], v[152:153]\n\t" \
  "v_pk_fma_f32 v[230:231], v[230:231], v[146:147], v[154:155]\n\t" \
  "v_pk_fma_f32 v[232:233], v[232:233], v[148:149], v[156:157]\n\t" \
  "v_pk_fma_f32 v[234:235], v[234:235], v[150:151], v[158:159]\n\t" \
  "v_pk_fma_f32 v[236:237], v[236:237], v[172:173], v[174:175]\n\t" \
  "v_pk_fma_f32 v[238:239], v[238:239], v[172:173], v[174:175]\n\t" \
  "v_pk_fma_f32 v[240:241], v[240:241], v[172:173], v[174:175]\n\t" \
  "v_pk_fma_f32 v[242:243], v[242:243], v[172:173], v[174:175]\n\t" \
  "v_pk_fma_f32 v[236:237], v[236:237], v[144:145], v[152:153]\n\t" \
  "v_pk_fma_f32 v[238:239], v[238:239], v[146:147], v[154:155]\n\t" \
  "v_pk_fma_f32 v[240:241], v[240:241], v[148:149], v[156:157]\n\t" \
  "v_pk_fma_f32 v[242:243], v[242:243], v[150:151], v[158:159]\n\t" \
  "v_cvt_pk_bf16_f32 v92, v228, v229\n\t" \
  "v_mfma_f32_16x16x32_bf16 a[84:87], a[148:151], v[56:59], a[84:87]\n\t" \
  "v_cvt_pk_bf16_f32 v93, v230, v231\n\t" \
  "v_cvt_pk_bf16_f32 v94, v232, v233\n\t" \
  "v_mfma_f32_16x16x32_bf16 a[80:83], a[148:151], v[24:27], a[80:83]\n\t" \
  "v_cvt_pk_bf16_f32 v95, v234, v235\n\t" \
  "v_cvt_pk_bf16_f32 v124, v236, v237\n\t" \
  "v_mfma_f32_16x16x32_bf16 a[96:99], a[152:155], v[24:27], a[96:99]\n\t" \
  "v_cvt_pk_bf16_f32 v125, v238, v239\n\t" \
  "v_cvt_pk_bf16_f32 v126, v240, v241\n\t" \
  "v_mfma_f32_16x16x32_bf16 a[100:103], a[152:155], v[56:59], a[100:103]\n\t" \
  "ds_read_b64_tr_b16 a[190:191], v191 offset:9216\n\t" \
  "v_cvt_pk_bf16_f32 v127, v242, v243\n\t" \
  "v_mfma_f32_16x16x32_bf16 a[104:107], a[152:155], v[88:91], a[104:107]\n\t" \
  "buffer_load_dwordx4 a[60:63], v251, %[ws], s97 offen sc1\n\t" \
  "v_mfma_f32_16x16x32_bf16 a[108:111], a[152:155], v[120:123], a[108:111]\n\t" \
  "v_mfma_f32_16x16x32_bf16 a[124:127], a[156:159], v[120:123], a[124:127]\n\t" \
  "v_mfma_f32_16x16x32_bf16 a[120:123], a[156:159], v[88:91], a[120:123]\n\t" \
  "s_add_u32 s97, %[rot19], 3145728\n\t" \
  "s_bfe_u32 s97, s97, 0x140003\n\t" \
  "s_add_u32 s97, s97, %[sox]\n\t" \
  "v_mfma_f32_16x16x32_bf16 a[116:119], a[156:159], v[56:59], a[116:119]\n\t" \
  "s_waitcnt lgkmcnt(0)\n\t" \
  "v_mfma_f32_16x16x32_bf16 a[112:115], a[156:159], v[24:27], a[112:115]\n\t" \
  "s_barrier\n\t" \
  FSTP("v_writelane_b32 %[stv], s94, 48\n\t") \
  FSTP("v_writelane_b32 %[stv], s92, 49\n\t") \
  FSTP("s_memrealtime s[94:95]\n\t") \
  FSTP("s_memtime s[92:93]\n\t") \
  "v_mfma_f32_16x16x32_bf16 v[0:3], a[160:163], v[28:31], v[0:3]\n\t" \
  "buffer_load_dwordx4 a[192:195], v248, %[ws], s97 offen sc1\n\t" \
  "v_mfma_f32_16x16x32_bf16 v[32:35], a[160:163], v[60:63], v[32:35]\n\t" \
  "ds_read_b64_tr_b16 a[128:129], v184 offset:16384\n\t" \
  "v_mfma_f32_16x16x32_bf16 v[64:67], a[160:163], v[92:95], v[64:67]\n\t" \
  "ds_read_b64_tr_b16 a[130:131], v184 offset:17408\n\t" \
  "v_mfma_f32_16x16x32_bf16 v[96:99], a[160:163], v[124:127], v[96:99]\n\t" \
  "ds_write_b32 v219, v218\n\t" \
  "v_mfma_f32_16x16x32_bf16 v[100:103], a[164:167], v[124:127], v[100:103]\n\t" \
  "buffer_load_dwordx4 a[196:199], v249, %[ws], s97 offen sc1\n\t" \
  "v_mfma_f32_16x16x32_bf16 v[68:71], a[164:167], v[92:95], v[68:71]\n\t" \
  "ds_read_b64_tr_b16 a[132:133], v185 offset:16384\n\t" \
  "v_and_b32 v219, 0x7fffffe0, v219\n\t" \
  "v_mfma_f32_16x16x32_bf16 v[36:39], a[164:167], v[60:63], v[36:39]\n\t" \
  "ds_read_b64_tr_b16 a[134:135], v185 offset:17408\n\t" \
  "v_mfma_f32_16x16x32_bf16 v[4:7], a[164:167], v[28:31], v[4:7]\n\t" \
  "ds_read_b64_tr_b16 a[136:137], v186 offset:16384\n\t" \
  "v_mfma_f32_16x16x32_bf16 v[8:11], a[168:171], v[28:31], v[8:11]\n\t" \
  "buffer_load_dwordx4 a[200:203], v250, %[ws], s97 offen sc1\n\t" \
  "v_mfma_f32_16x16x32_bf16 v[40:43], a[168:171], v[60:63], v[40:43]\n\t" \
  "ds_read_b64_tr_b16 a[138:139], v186 offset:17408\n\t" \
  "v_mfma_f32_16x16x32_bf16 v[72:75], a[168:171], v[92:95], v[72:75]\n\t" \
  "ds_read_b64_tr_b16 a[140:141], v187 offset:16384\n\t" \
  "v_mfma_f32_16x16x32_bf16 v[104:107], a[168:171], v[124:127], v[104:107]\n\t" \
  "ds_read_b128 v[168:171], v219 offset:0\n\t" \
  "v_mfma_f32_16x16x32_bf16 v[108:111], a[172:175], v[124:127], v[108:111]\n\t" \
  "buffer_load_dwordx4 a[204:207], v251, %[ws], s97 offen sc1\n\t" \
  "v_mfma_f32_16x16x32_bf16 v[76:79], a[172:175], v[92:95], v[76:79]\n\t" \
  "ds_read_b64_tr_b16 a[142:143], v187 offset:17408\n\t" \
  "s_add_u32 s97, s97, %[dk]\n\t" \
  "v_mfma_f32_16x16x32_bf16 v[44:47], a[172:175], v[60:63], v[44:47]\n\t" \
  "ds_read_b64_tr_b16 a[144:145], v188 offset:16384\n\t" \
  "v_mfma_f32_16x16x32_bf16 v[12:15], a[172:175], v[28:31], v[12:15]\n\t" \
  "buffer_load_dwordx4 a[208:211], v248, %[ws], s97 offen sc1\n\t" \
  "v_mfma_f32_16x16x32_bf16 a[64:67], a[176:179], v[28:31], a[64:67]\n\t" \
  "ds_read_b64_tr_b16 a[146:147], v188 offset:17408\n\t" \
  "v_mfma_f32_16x16x32_bf16 a[68:71], a[176:179], v[60:63], a[68:71]\n\t" \
  "ds_read_b64_tr_b16 a[148:149], v189 offset:16384\n\t" \
  "v_mfma_f32_16x16x32_bf16 a[72:75], a[176:179], v[92:95], a[72:75]\n\t" \
  "ds_read_b64_tr_b16 a[150:151], v189 offset:17408\n\t" \
  "v_mfma_f32_16x16x32_bf16 a[76:79], a[176:179], v[124:127], a[76:79]\n\t" \
  "buffer_load_dwordx4 a[212:215], v249, %[ws], s97 offen sc1\n\t" \
  "v_mfma_f32_16x16x32_bf16 a[92:95], a[180:183], v[124:127], a[92:95]\n\t" \
  "ds_read_b64_tr_b16 a[152:153], v190 offset:16384\n\t" \
  "v_mfma_f32_16x16x32_bf16 a[88:91], a[180:183], v[92:95], a[88:91]\n\t" \
  "ds_read_b128 v[172:175], v219 offset:16\n\t" \
  "v_mfma_f32_16x16x32_bf16 a[84:87], a[180:183], v[60:63], a[84:87]\n\t" \
  "ds_read_b64_tr_b16 a[154:155], v190 offset:17408\n\t" \
  "v_mfma_f32_16x16x32_bf16 a[80:83], a[180:183], v[28:31], a[80:83]\n\t" \
  "buffer_load_dwordx4 a[216:219], v250, %[ws], s97 offen sc1\n\t" \
  "v_mfma_f32_16x16x32_bf16 a[96:99], a[184:187], v[28:31], a[96:99]\n\t" \
  "ds_read_b64_tr_b16 a[156:157], v191 offset:16384\n\t" \
  "v_mfma_f32_16x16x32_bf16 a[100:103], a[184:187], v[60:63], a[100:103]\n\t" \
  "ds_read_b64_tr_b16 a[158:159], v191 offset:17408\n\t" \
  "v_mfma_f32_16x16x32_bf16 a[104:107], a[184:187], v[92:95], a[104:107]\n\t" \
  "buffer_load_dwordx4 a[220:223], v251, %[ws], s97 offen sc1\n\t" \
  "v_mfma_f32_16x16x32_bf16 a[108:111], a[184:187], v[124:127], a[108:111]\n\t" \
  "v_mfma_f32_16x16x32_bf16 a[124:127], a[188:191], v[124:127], a[124:127]\n\t" \
  "v_mfma_f32_16x16x32_bf16 a[120:123], a[188:191], v[92:95], a[120:123]\n\t" \
  "s_add_u32 s97, %[rot19], 3670016\n\t" \
  "s_bfe_u32 s97, s97, 0x140003\n\t" \
  "s_add_u32 s97, s97, %[sox]\n\t" \
  "v_mfma_f32_16x16x32_bf16 a[116:119], a[188:191], v[60:63], a[116:119]\n\t" \
  "v_mfma_f32_16x16x32_bf16 a[112:115], a[188:191], v[28:31], a[112:115]\n\t" \
  "s_waitcnt vmcnt(20) lgkmcnt(0)\n\t" \
  FSTP("v_writelane_b32 %[stv], s94, 50\n\t") \
  FSTP("v_writelane_b32 %[stv], s92, 51\n\t") \
  FSTP("s_memrealtime s[94:95]\n\t") \
  FSTP("s_memtime s[92:93]\n\t") \
  FSTS("s_memrealtime s[94:95]\n\t") \
  FSTS("s_waitcnt lgkmcnt(0)\n\t") \
  FSTS("v_writelane_b32 %[stv], s94, 14\n\t") \
  "v_mfma_f32_16x16x32_bf16 a[64:67], a[144:147], a[0:3], a[64:67]\n\t" \
  "buffer_load_dwordx4 a[224:227], v248, %[ws], s97 offen sc1\n\t" \
  "v_mfma_f32_16x16x32_bf16 a[68:71], a[144:147], a[4:7], a[68:71]\n\t" \
  "s_bfe_u32 %[hl], %[bid], 0x30003\n\t" \
  "ds_read_b64_tr_b16 a[160:161], v184 offset:24576\n\t" \
  "s_lshl_b32 %[hl18], %[hl], 18\n\t" \
  "v_mfma_f32_16x16x32_bf16 a[72:75], a[144:147], a[8:11], a[72:75]\n\t" \
  "s_lshr_b32 s98, %[bid], 3\n\t" \
  "ds_read_b64_tr_b16 a[162:163], v184 offset:25600\n\t" \
  "v_accvgpr_read_b32 v16, a64\n\t" \
  "v_mfma_f32_16x16x32_bf16 a[76:79], a[144:147], a[12:15], a[76:79]\n\t" \
  "s_lshl_b32 %[cto], %[wid], 5\n\t" \
  "v_accvgpr_read_b32 v17, a65\n\t" \
  "v_accvgpr_read_b32 v18, a66\n\t" \
  "v_mfma_f32_16x16x32_bf16 a[92:95], a[148:151], a[12:15], a[92:95]\n\t" \
  "buffer_load_dwordx4 a[228:231], v249, %[ws], s97 offen sc1\n\t" \
  "v_mfma_f32_16x16x32_bf16 a[88:91], a[148:151], a[8:11], a[88:91]\n\t" \
  "s_add_u32 %[cto], %[cto], s98\n\t" \
  "ds_read_b64_tr_b16 a[164:165], v185 offset:24576\n\t" \
  "v_accvgpr_read_b32 v19, a67\n\t" \
  "v_mfma_f32_16x16x32_bf16 a[84:87], a[148:151], a[4:7], a[84:87]\n\t" \
  "s_lshl_b32 %[cto], %[cto], 7\n\t" \
  "ds_read_b64_tr_b16 a[166:167], v185 offset:25600\n\t" \
  "v_accvgpr_read_b32 v48, a68\n\t" \
  "v_mfma_f32_16x16x32_bf16 a[80:83], a[148:151], a[0:3], a[80:83]\n\t" \
  "s_lshl_b32 %[mbp], %[wid], 7\n\t" \
  "ds_read_b64_tr_b16 a[168:169], v186 offset:24576\n\t" \
  "v_accvgpr_read_b32 v49, a69\n\t" \
  "v_mfma_f32_16x16x32_bf16 a[96:99], a[152:155], a[0:3], a[96:99]\n\t" \
  "buffer_load_dwordx4 a[232:235], v250, %[ws], s97 offen sc1\n\t" \
  "v_mfma_f32_16x16x32_bf16 a[100:103], a[152:155], a[4:7], a[100:103]\n\t" \
  "s_add_u32 %[mbp], %[mbp], 0x220000\n\t" \
  "ds_read_b64_tr_b16 a[170:171], v186 offset:25600\n\t" \
  "v_accvgpr_read_b32 v50, a70\n\t" \
  "v_mfma_f32_16x16x32_bf16 a[104:107], a[152:155], a[8:11], a[104:107]\n\t" \
  "s_and_b32 s98, %[bid], 0xc0\n\t" \
  "ds_read_b64_tr_b16 a[172:173], v187 offset:24576\n\t" \
  "v_accvgpr_read_b32 v51, a71\n\t" \
  "v_mfma_f32_16x16x32_bf16 a[108:111], a[152:155], a[12:15], a[108:111]\n\t" \
  "s_lshl_b32 s98, s98, 9\n\t" \
  "v_accvgpr_read_b32 v80, a72\n\t" \
  "v_accvgpr_read_b32 v81, a73\n\t" \
  "v_mfma_f32_16x16x32_bf16 a[124:127], a[156:159], a[12:15], a[124:127]\n\t" \
  "buffer_load_dwordx4 a[236:239], v251, %[ws], s97 offen sc1\n\t" \
  "v_mfma_f32_16x16x32_bf16 a[120:123], a[156:159], a[8:11], a[120:123]\n\t" \
  "s_add_u32 s96, %[rot19], 4718592\n\t" \
  "s_and_b32 s96, s96, 0x780000\n\t" \
  "s_add_u32 m0, %[mw], 98304\n\t" \
  "v_mfma_f32_16x16x32_bf16 a[116:119], a[156:159], a[4:7], a[116:119]\n\t" \
  "s_add_u32 %[mbp], %[mbp], s98\n\t" \
  "ds_read_b64_tr_b16 a[174:175], v187 offset:25600\n\t" \
  "v_accvgpr_read_b32 v82, a74\n\t" \
  "v_mfma_f32_16x16x32_bf16 a[112:115], a[156:159], a[0:3], a[112:115]\n\t" \
  "buffer_load_dwordx4 v200, %[sw], s96 offen offset:0 lds\n\t" \
  "v_mfma_f32_16x16x32_bf16 v[0:3], a[128:131], a[0:3], v[0:3]\n\t" \
  "s_and_b32 s98, %[bid], 0x3\n\t" \
  "ds_read_b64_tr_b16 a[176:177], v188 offset:24576\n\t" \
  "v_accvgpr_read_b32 v83, a75\n\t" \
  "v_mfma_f32_16x16x32_bf16 v[32:35], a[128:131], a[4:7], v[32:35]\n\t" \
  "s_lshl_b32 s98, s98, 13\n\t" \
  "ds_read_b64_tr_b16 a[178:179], v188 offset:25600\n\t" \
  "v_accvgpr_read_b32 v112, a76\n\t" \
  "v_mfma_f32_16x16x32_bf16 v[64:67], a[128:131], a[8:11], v[64:67]\n\t" \
  "s_add_u32 %[mbp], %[mbp], s98\n\t" \
  "ds_read_b64_tr_b16 a[180:181], v189 offset:24576\n\t" \
  "v_accvgpr_read_b32 v113, a77\n\t" \
  "v_mfma_f32_16x16x32_bf16 v[96:99], a[128:131], a[12:15], v[96:99]\n\t" \
  "buffer_load_dwordx4 v201, %[sw], s96 offen offset:1024 lds\n\t" \
  "v_mfma_f32_16x16x32_bf16 v[100:103], a[132:135], a[12:15], v[100:103]\n\t" \
  "s_and_b32 s98, %[bid], 0x4\n\t" \
  "ds_read_b64_tr_b16 a[182:183], v189 offset:25600\n\t" \
  "v_accvgpr_read_b32 v114, a78\n\t" \
  "v_mfma_f32_16x16x32_bf16 v[68:71], a[132:135], a[8:11], v[68:71]\n\t" \
  "s_lshl_b32 s98, s98, 10\n\t" \
  "ds_read_b64_tr_b16 a[184:185], v190 offset:24576\n\t" \
  "v_accvgpr_read_b32 v115, a79\n\t" \
  "v_mfma_f32_16x16x32_bf16 v[36:39], a[132:135], a[4:7], v[36:39]\n\t" \
  "s_add_u32 %[mbp], %[mbp], s98\n\t" \
  "ds_read_b64_tr_b16 a[186:187], v190 offset:25600\n\t" \
  "v_accvgpr_read_b32 v116, a92\n\t" \
  "v_mfma_f32_16x16x32_bf16 v[4:7], a[132:135], a[0:3], v[4:7]\n\t" \
  "buffer_load_dwordx4 v202, %[sw], s96 offen offset:2048 lds\n\t" \
  "v_mfma_f32_16x16x32_bf16 v[8:11], a[136:139], a[0:3], v[8:11]\n\t" \
  "s_and_b32 s98, %[bid], 0x38\n\t" \
  "ds_read_b64_tr_b16 a[188:189], v191 offset:24576\n\t" \
  "v_accvgpr_read_b32 v117, a93\n\t" \
  "v_mfma_f32_16x16x32_bf16 v[40:43], a[136:139], a[4:7], v[40:43]\n\t" \
  "s_lshl_b32 s98, s98, 6\n\t" \
  "ds_read_b64_tr_b16 a[190:191], v191 offset:25600\n\t" \
  "v_accvgpr_read_b32 v118, a94\n\t" \
  "v_mfma_f32_16x16x32_bf16 v[72:75], a[136:139], a[8:11], v[72:75]\n\t" \
  "s_add_u32 %[mbp], %[mbp], s98\n\t" \
  "v_accvgpr_read_b32 v119, a95\n\t" \
  "v_accvgpr_read_b32 v84, a88\n\t" \
  "v_mfma_f32_16x16x32_bf16 v[104:107], a[136:139], a[12:15], v[104:107]\n\t" \
  "buffer_load_dwordx4 v203, %[sw], s96 offen offset:3072 lds\n\t" \
  "v_mfma_f32_16x16x32_bf16 v[108:111], a[140:143], a[12:15], v[108:111]\n\t" \
  "s_lshr_b32 s98, %[bid], 2\n\t" \
  "v_accvgpr_read_b32 v85, a89\n\t" \
  "v_accvgpr_read_b32 v86, a90\n\t" \
  "v_mfma_f32_16x16x32_bf16 v[76:79], a[140:143], a[8:11], v[76:79]\n\t" \
  "s_xor_b32 s98, s98, %[wid]\n\t" \
  "v_accvgpr_read_b32 v87, a91\n\t" \
  "v_accvgpr_read_b32 v52, a84\n\t" \
  "v_mfma_f32_16x16x32_bf16 v[44:47], a[140:143], a[4:7], v[44:47]\n\t" \
  "s_waitcnt vmcnt(24) lgkmcnt(0)\n\t" \
  "v_accvgpr_read_b32 v53, a85\n\t" \
  "s_add_u32 s97, s97, %[dk]\n\t" \
  "v_mfma_f32_16x16x32_bf16 v[12:15], a[140:143], a[0:3], v[12:15]\n\t" \
  "s_barrier\n\t" \
  "v_mfma_f32_16x16x32_bf16 a[96:99], a[184:187], a[16:19], a[96:99]\n\t" \
  "buffer_load_dwordx4 a[240:243], v248, %[ws], s97 offen sc1\n\t" \
  "v_mfma_f32_16x16x32_bf16 a[100:103], a[184:187], a[20:23], a[100:103]\n\t" \
  "s_lshl_b32 s98, s98, 13\n\t" \
  "ds_read_b64_tr_b16 a[128:129], v184 offset:32768\n\t" \
  "v_accvgpr_read_b32 v54, a86\n\t" \
  "v_mfma_f32_16x16x32_bf16 a[104:107], a[184:187], a[24:27], a[104:107]\n\t" \
  "s_and_b32 %[po], s98, 0x2000\n\t" \
  "ds_read_b64_tr_b16 a[130:131], v184 offset:33792\n\t" \
  "v_accvgpr_read_b32 v55, a87\n\t" \
  "v_mfma_f32_16x16x32_bf16 a[108:111], a[184:187], a[28:31], a[108:111]\n\t" \
  "s_lshr_b32 s98, %[bid], 6\n\t" \
  "v_accvgpr_read_b32 v20, a80\n\t" \
  "v_accvgpr_read_b32 v21, a81\n\t" \
  "v_mfma_f32_16x16x32_bf16 a[124:127], a[188:191], a[28:31], a[124:127]\n\t" \
  "s_lshl_b32 s98, s98, 22\n\t" \
  "ds_read_b64_tr_b16 a[132:133], v185 offset:32768\n\t" \
  "v_accvgpr_read_b32 v22, a82\n\t" \
  "v_mfma_f32_16x16x32_bf16 a[120:123], a[188:191], a[24:27], a[120:123]\n\t" \
  "buffer_load_dwordx4 a[244:247], v249, %[ws], s97 offen sc1\n\t" \
  "v_mfma_f32_16x16x32_bf16 a[116:119], a[188:191], a[20:23], a[116:119]\n\t" \
  "s_add_u32 %[po], %[po], s98\n\t" \
  "ds_read_b64_tr_b16 a[134:135], v185 offset:33792\n\t" \
  "v_accvgpr_read_b32 v23, a83\n\t" \
  "v_mfma_f32_16x16x32_bf16 a[112:115], a[188:191], a[16:19], a[112:115]\n\t" \
  "s_lshl_b32 %[xcb], %[wid], 15\n\t" \
  "ds_read_b64_tr_b16 a[136:137], v186 offset:32768\n\t" \
  "v_accvgpr_read_b32 v24, a96\n\t" \
  "v_mfma_f32_16x16x32_bf16 v[0:3], a[160:163], a[16:19], v[0:3]\n\t" \
  "s_add_u32 %[xcb], %[xcb], 0x1800000\n\t" \
  "v_accvgpr_read_b32 v25, a97\n\t" \
  "v_accvgpr_read_b32 v26, a98\n\t" \
  "v_mfma_f32_16x16x32_bf16 v[32:35], a[160:163], a[20:23], v[32:35]\n\t" \
  "s_add_u32 %[xcb], %[xcb], %[po]\n\t" \
  "ds_read_b64_tr_b16 a[138:139], v186 offset:33792\n\t" \
  "v_accvgpr_read_b32 v27, a99\n\t" \
  "v_mfma_f32_16x16x32_bf16 v[64:67], a[160:163], a[24:27], v[64:67]\n\t" \
  "buffer_load_dwordx4 a[248:251], v250, %[ws], s97 offen sc1\n\t" \
  "v_mfma_f32_16x16x32_bf16 v[96:99], a[160:163], a[28:31], v[96:99]\n\t" \
  "s_and_b32 s98, %[bid], 0x38\n\t" \
  "ds_read_b64_tr_b16 a[140:141], v187 offset:32768\n\t" \
  "v_accvgpr_read_b32 v56, a100\n\t" \
  "v_mfma_f32_16x16x32_bf16 v[100:103], a[164:167], a[28:31], v[100:103]\n\t" \
  "s_lshl_b32 s98, s98, 16\n\t" \
  "ds_read_b64_tr_b16 a[142:143], v187 offset:33792\n\t" \
  "v_accvgpr_read_b32 v57, a101\n\t" \
  "v_mfma_f32_16x16x32_bf16 v[68:71], a[164:167], a[24:27], v[68:71]\n\t" \
  "s_add_u32 %[xcb], %[xcb], s98\n\t" \
  "ds_read_b64_tr_b16 a[144:145], v188 offset:32768\n\t" \
  "v_accvgpr_read_b32 v58, a102\n\t" \
  "v_mfma_f32_16x16x32_bf16 v[36:39], a[164:167], a[20:23], v[36:39]\n\t" \
  "s_add_u32 %[otb], %[po], 0x800000\n\t" \
  "v_accvgpr_read_b32 v59, a103\n\t" \
  "v_accvgpr_read_b32 v88, a104\n\t" \
  "v_mfma_f32_16x16x32_bf16 v[4:7], a[164:167], a[16:19], v[4:7]\n\t" \
  "buffer_load_dwordx4 a[252:255], v251, %[ws], s97 offen sc1\n\t" \
  "v_mfma_f32_16x16x32_bf16 v[8:11], a[168:171], a[16:19], v[8:11]\n\t" \
  "s_and_b32 s98, %[bid], 0x3\n\t" \
  "ds_read_b64_tr_b16 a[146:147], v188 offset:33792\n\t" \
  "v_accvgpr_read_b32 v89, a105\n\t" \
  "v_mfma_f32_16x16x32_bf16 v[40:43], a[168:171], a[20:23], v[40:43]\n\t" \
  "s_lshl_b32 s98, s98, 20\n\t" \
  "ds_read_b64_tr_b16 a[148:149], v189 offset:32768\n\t" \
  "v_accvgpr_read_b32 v90, a106\n\t" \
  "v_mfma_f32_16x16x32_bf16 v[72:75], a[168:171], a[24:27], v[72:75]\n\t" \
  "s_mov_b32 s99, 0\n\t" \
  "ds_read_b64_tr_b16 a[150:151], v189 offset:33792\n\t" \
  "v_accvgpr_read_b32 v91, a107\n\t" \
  "v_mfma_f32_16x16x32_bf16 v[104:107], a[168:171], a[28:31], v[104:107]\n\t" \
  "s_add_u32 %[otb], %[otb], s98\n\t" \
  "v_accvgpr_read_b32 v120, a108\n\t" \
  "v_accvgpr_read_b32 v121, a109\n\t" \
  "v_mfma_f32_16x16x32_bf16 v[108:111], a[172:175], a[28:31], v[108:111]\n\t" \
  "buffer_load_dwordx4 v[136:139], v209, %[sM], s99 offen\n\t" \
  "v_mfma_f32_16x16x32_bf16 v[76:79], a[172:175], a[24:27], v[76:79]\n\t" \
  "s_and_b32 s98, %[wid], 2\n\t" \
  "ds_read_b64_tr_b16 a[152:153], v190 offset:32768\n\t" \
  "v_accvgpr_read_b32 v122, a110\n\t" \
  "v_mfma_f32_16x16x32_bf16 v[44:47], a[172:175], a[20:23], v[44:47]\n\t" \
  "s_lshl_b32 s98, s98, 13\n\t" \
  "ds_read_b64_tr_b16 a[154:155], v190 offset:33792\n\t" \
  "v_accvgpr_read_b32 v123, a111\n\t" \
  "v_mfma_f32_16x16x32_bf16 v[12:15], a[172:175], a[16:19], v[12:15]\n\t" \
  "s_mov_b32 s99, 12288\n\t" \
  "ds_read_b64_tr_b16 a[156:157], v191 offset:32768\n\t" \
  "v_accvgpr_read_b32 v124, a124\n\t" \
  "v_mfma_f32_16x16x32_bf16 v[16:19], a[176:179], a[16:19], v[16:19]\n\t" \
  "s_add_u32 %[otb], %[otb], s98\n\t" \
  "v_accvgpr_read_b32 v125, a125\n\t" \
  "v_accvgpr_read_b32 v126, a126\n\t" \
  "v_mfma_f32_16x16x32_bf16 v[48:51], a[176:179], a[20:23], v[48:51]\n\t" \
  "buffer_load_dwordx4 v[140:143], v209, %[sM], s99 offen\n\t" \
  "v_mfma_f32_16x16x32_bf16 v[80:83], a[176:179], a[24:27], v[80:83]\n\t" \
  "s_and_b32 s98, %[wid], 1\n\t" \
  "ds_read_b64_tr_b16 a[158:159], v191 offset:33792\n\t" \
  "v_accvgpr_read_b32 v127, a127\n\t" \
  "v_mfma_f32_16x16x32_bf16 v[112:115], a[176:179], a[28:31], v[112:115]\n\t" \
  "s_lshl_b32 s98, s98, 12\n\t" \
  "v_accvgpr_read_b32 v92, a120\n\t" \
  "v_accvgpr_read_b32 v93, a121\n\t" \
  "v_mfma_f32_16x16x32_bf16 v[116:119], a[180:183], a[28:31], v[116:119]\n\t" \
  "s_add_u32 %[otb], %[otb], s98\n\t" \
  "v_accvgpr_read_b32 v94, a122\n\t" \
  "v_accvgpr_read_b32 v95, a123\n\t" \
  "v_mfma_f32_16x16x32_bf16 v[84:87], a[180:183], a[24:27], v[84:87]\n\t" \
  "s_lshr_b32 %[eso], %[c17], 2\n\t" \
  "v_accvgpr_read_b32 v60, a116\n\t" \
  "v_accvgpr_read_b32 v61, a117\n\t" \
  "v_mfma_f32_16x16x32_bf16 v[52:55], a[180:183], a[20:23], v[52:55]\n\t" \
  "s_add_u32 s97, %[rot19], 4194304\n\t" \
  "s_bfe_u32 s97, s97, 0x140003\n\t" \
  "s_add_u32 s97, s97, %[sox]\n\t" \
  "v_mfma_f32_16x16x32_bf16 v[20:23], a[180:183], a[16:19], v[20:23]\n\t" \
  "s_waitcnt vmcnt(26) lgkmcnt(0)\n\t" \
  "v_accvgpr_read_b32 v62, a118\n\t" \
  "v_accvgpr_read_b32 v63, a119\n\t" \
  "v_mfma_f32_16x16x32_bf16 v[0:3], a[128:131], a[32:35], v[0:3]\n\t" \
  "buffer_load_dwordx4 a[0:3], v248, %[ws], s97 offen sc1\n\t" \
  "v_mfma_f32_16x16x32_bf16 v[32:35], a[128:131], a[36:39], v[32:35]\n\t" \
  "s_add_u32 %[eso], %[eso], %[otb]\n\t" \
  "ds_read_b64_tr_b16 a[160:161], v184 offset:40960\n\t" \
  "v_accvgpr_read_b32 v28, a112\n\t" \
  "v_mfma_f32_16x16x32_bf16 v[64:67], a[128:131], a[40:43], v[64:67]\n\t" \
  "s_and_b32 s98, %[bid], 0x38\n\t" \
  "ds_read_b64_tr_b16 a[162:163], v184 offset:41984\n\t" \
  "v_accvgpr_read_b32 v29, a113\n\t" \
  "v_mfma_f32_16x16x32_bf16 v[96:99], a[128:131], a[44:47], v[96:99]\n\t" \
  "s_lshl_b32 s98, s98, 14\n\t" \
  "v_accvgpr_read_b32 v30, a114\n\t" \
  "v_accvgpr_read_b32 v31, a115\n\t" \
  "v_mfma_f32_16x16x32_bf16 v[100:103], a[132:135], a[44:47], v[100:103]\n\t" \
  "buffer_load_dwordx4 a[4:7], v249, %[ws], s97 offen sc1\n\t" \
  "v_mfma_f32_16x16x32_bf16 v[68:71], a[132:135], a[40:43], v[68:71]\n\t" \
  "s_add_u32 %[eso], %[eso], s98\n\t" \
  "ds_read_b64_tr_b16 a[164:165], v185 offset:40960\n\t" \
  "s_lshr_b32 s98, %[bid], 1\n\t" \
  "v_mfma_f32_16x16x32_bf16 v[36:39], a[132:135], a[36:39], v[36:39]\n\t" \
  "s_xor_b32 s98, s98, %[wid]\n\t" \
  "ds_read_b64_tr_b16 a[166:167], v185 offset:41984\n\t" \
  "s_lshl_b32 s98, s98, 13\n\t" \
  "v_mfma_f32_16x16x32_bf16 v[4:7], a[132:135], a[32:35], v[4:7]\n\t" \
  "s_and_b32 %[po], s98, 0x2000\n\t" \
  "ds_read_b64_tr_b16 a[168:169], v186 offset:40960\n\t" \
  "s_lshr_b32 s98, %[bid], 5\n\t" \
  "v_mfma_f32_16x16x32_bf16 v[8:11], a[136:139], a[32:35], v[8:11]\n\t" \
  "buffer_load_dwordx4 a[8:11], v250, %[ws], s97 offen sc1\n\t" \
  "v_mfma_f32_16x16x32_bf16 v[40:43], a[136:139], a[36:39], v[40:43]\n\t" \
  "s_xor_b32 s98, s98, %[wid]\n\t" \
  "ds_read_b64_tr_b16 a[170:171], v186 offset:41984\n\t" \
  "s_lshl_b32 s98, s98, 13\n\t" \
  "v_mfma_f32_16x16x32_bf16 v[72:75], a[136:139], a[40:43], v[72:75]\n\t" \
  "s_and_b32 %[px], s98, 0x2000\n\t" \
  "ds_read_b64_tr_b16 a[172:173], v187 offset:40960\n\t" \
  "s_lshl_b32 %[frb], %[wid], 2\n\t" \
  "v_mfma_f32_16x16x32_bf16 v[104:107], a[136:139], a[44:47], v[104:107]\n\t" \
  "s_add_u32 %[frb], %[frb], 0x8000\n\t" \
  "ds_read_b64_tr_b16 a[174:175], v187 offset:41984\n\t" \
  "s_and_b32 s98, %[bid], 0x80\n\t" \
  "v_mfma_f32_16x16x32_bf16 v[108:111], a[140:143], a[44:47], v[108:111]\n\t" \
  "buffer_load_dwordx4 a[12:15], v251, %[ws], s97 offen sc1\n\t" \
  "v_mfma_f32_16x16x32_bf16 v[76:79], a[140:143], a[40:43], v[76:79]\n\t" \
  "s_add_u32 s96, %[rot19], 5242880\n\t" \
  "s_and_b32 s96, s96, 0x780000\n\t" \
  "s_add_u32 m0, %[mw], 0\n\t" \
  "v_mfma_f32_16x16x32_bf16 v[44:47], a[140:143], a[36:39], v[44:47]\n\t" \
  "s_lshl_b32 s98, s98, 7\n\t" \
  "ds_read_b64_tr_b16 a[176:177], v188 offset:40960\n\t" \
  "s_add_u32 %[frb], %[frb], s98\n\t" \
  "v_mfma_f32_16x16x32_bf16 v[12:15], a[140:143], a[32:35], v[12:15]\n\t" \
  "buffer_load_dwordx4 v200, %[sw], s96 offen offset:0 lds\n\t" \
  "v_mfma_f32_16x16x32_bf16 v[16:19], a[144:147], a[32:35], v[16:19]\n\t" \
  "s_and_b32 s98, %[bid], 0x4\n\t" \
  "ds_read_b64_tr_b16 a[178:179], v188 offset:41984\n\t" \
  "s_lshl_b32 s98, s98, 11\n\t" \
  "v_mfma_f32_16x16x32_bf16 v[48:51], a[144:147], a[36:39], v[48:51]\n\t" \
  "s_add_u32 %[frb], %[frb], s98\n\t" \
  "ds_read_b64_tr_b16 a[180:181], v189 offset:40960\n\t" \
  "s_and_b32 s98, %[bid], 0x78\n\t" \
  "v_mfma_f32_16x16x32_bf16 v[80:83], a[144:147], a[40:43], v[80:83]\n\t" \
  "s_lshl_b32 s98, s98, 6\n\t" \
  "ds_read_b64_tr_b16 a[182:183], v189 offset:41984\n\t" \
  "s_add_u32 %[frb], %[frb], s98\n\t" \
  "v_mfma_f32_16x16x32_bf16 v[112:115], a[144:147], a[44:47], v[112:115]\n\t" \
  "buffer_load_dwordx4 v201, %[sw], s96 offen offset:1024 lds\n\t" \
  "v_mfma_f32_16x16x32_bf16 v[116:119], a[148:151], a[44:47], v[116:119]\n\t" \
  "s_lshl_b32 %[fw2], %[wid], 7\n\t" \
  "ds_read_b64_tr_b16 a[184:185], v190 offset:40960\n\t" \
  "s_add_u32 %[fw2], %[fw2], 0x20000\n\t" \
  "v_mfma_f32_16x16x32_bf16 v[84:87], a[148:151], a[40:43], v[84:87]\n\t" \
  "s_and_b32 s98, %[bid], 0xc0\n\t" \
  "ds_read_b64_tr_b16 a[186:187], v190 offset:41984\n\t" \
  "s_lshl_b32 s98, s98, 9\n\t" \
  "v_mfma_f32_16x16x32_bf16 v[52:55], a[148:151], a[36:39], v[52:55]\n\t" \
  "s_add_u32 %[fw2], %[fw2], s98\n\t" \
  "ds_read_b64_tr_b16 a[188:189], v191 offset:40960\n\t" \
  "s_and_b32 s98, %[bid], 0x2\n\t" \
  "v_mfma_f32_16x16x32_bf16 v[20:23], a[148:151], a[32:35], v[20:23]\n\t" \
  "buffer_load_dwordx4 v202, %[sw], s96 offen offset:2048 lds\n\t" \
  "v_mfma_f32_16x16x32_bf16 v[24:27], a[152:155], a[32:35], v[24:27]\n\t" \
  "s_lshl_b32 s98, s98, 13\n\t" \
  "ds_read_b64_tr_b16 a[190:191], v191 offset:41984\n\t" \
  "s_add_u32 %[fw2], %[fw2], s98\n\t" \
  "v_mfma_f32_16x16x32_bf16 v[56:59], a[152:155], a[36:39], v[56:59]\n\t" \
  "s_and_b32 s98, %[bid], 0x4\n\t" \
  "s_lshl_b32 s98, s98, 11\n\t" \
  "s_add_u32 %[fw2], %[fw2], s98\n\t" \
  "v_mfma_f32_16x16x32_bf16 v[88:91], a[152:155], a[40:43], v[88:91]\n\t" \
  "s_and_b32 s98, %[bid], 0x1\n\t" \
  "s_lshl_b32 s98, s98, 12\n\t" \
  "s_add_u32 %[fw2], %[fw2], s98\n\t" \
  "v_mfma_f32_16x16x32_bf16 v[120:123], a[152:155], a[44:47], v[120:123]\n\t" \
  "buffer_load_dwordx4 v203, %[sw], s96 offen offset:3072 lds\n\t" \
  "v_mfma_f32_16x16x32_bf16 v[124:127], a[156:159], a[44:47], v[124:127]\n\t" \
  ".Lhs2:\n\ts_cbranch_execz .Lhs2n\n\t" \
  "s_lshl2_add_u32 %[fw2], %[hl], %[fw2]\n\t" \
  "s_lshl_b32 %[xso], %[wid], 15\n\t" \
  "s_and_b32 s98, %[bid], 0x4\n\t" \
  "v_mfma_f32_16x16x32_bf16 v[92:95], a[156:159], a[40:43], v[92:95]\n\t" \
  "s_lshl_b32 s98, s98, 19\n\t" \
  "s_add_u32 %[xso], %[xso], s98\n\t" \
  "s_and_b32 s98, %[bid], 0xc0\n\t" \
  "v_mfma_f32_16x16x32_bf16 v[60:63], a[156:159], a[36:39], v[60:63]\n\t" \
  "s_waitcnt vmcnt(30) lgkmcnt(0)\n\t" \
  "s_add_u32 s97, s97, %[dk]\n\t" \
  "s_lshl_b32 s98, s98, 13\n\t" \
  "v_mfma_f32_16x16x32_bf16 v[28:31], a[156:159], a[32:35], v[28:31]\n\t" \
  "s_barrier\n\t" \
  "v_mfma_f32_16x16x32_bf16 v[0:3], a[160:163], a[48:51], v[0:3]\n\t" \
  "buffer_load_dwordx4 a[16:19], v248, %[ws], s97 offen sc1\n\t" \
  "v_mfma_f32_16x16x32_bf16 v[32:35], a[160:163], a[52:55], v[32:35]\n\t" \
  "s_add_u32 %[xso], %[xso], s98\n\t" \
  "ds_read_b64_tr_b16 a[128:129], v184 offset:49152\n\t" \
  "s_and_b32 s98, %[bid], 0x3\n\t" \
  "v_mfma_f32_16x16x32_bf16 v[64:67], a[160:163], a[56:59], v[64:67]\n\t" \
  "s_lshl_b32 s98, s98, 17\n\t" \
  "ds_read_b64_tr_b16 a[130:131], v184 offset:50176\n\t" \
  "s_add_u32 %[xso], %[xso], s98\n\t" \
  "v_mfma_f32_16x16x32_bf16 v[96:99], a[160:163], a[60:63], v[96:99]\n\t" \
  "s_and_b32 s98, %[bid], 0x38\n\t" \
  "ds_read_b64_tr_b16 a[132:133], v185 offset:49152\n\t" \
  "s_lshl_b32 s98, s98, 5\n\t" \
  "v_mfma_f32_16x16x32_bf16 v[100:103], a[164:167], a[60:63], v[100:103]\n\t" \
  "s_add_u32 %[xso], %[xso], s98\n\t" \
  "ds_read_b64_tr_b16 a[134:135], v185 offset:50176\n\t" \
  "s_lshr_b32 s98, %[bid], 2\n\t" \
  "v_mfma_f32_16x16x32_bf16 v[68:71], a[164:167], a[56:59], v[68:71]\n\t" \
  "s_xor_b32 s98, s98, %[bid]\n\t" \
  "ds_read_b64_tr_b16 a[136:137], v186 offset:49152\n\t" \
  "s_and_b32 s98, s98, 4\n\t" \
  "v_mfma_f32_16x16x32_bf16 v[36:39], a[164:167], a[52:55], v[36:39]\n\t" \
  "s_lshl_b32 %[vo], s98, 4\n\t" \
  "ds_read_b64_tr_b16 a[138:139], v186 offset:50176\n\t" \
  "s_and_b32 s98, %[bid], 0x8\n\t" \
  "v_mfma_f32_16x16x32_bf16 v[4:7], a[164:167], a[48:51], v[4:7]\n\t" \
  "s_lshl_b32 s98, s98, 2\n\t" \
  "ds_read_b64_tr_b16 a[140:141], v187 offset:49152\n\t" \
  "s_add_u32 %[vo], %[vo], s98\n\t" \
  "v_mfma_f32_16x16x32_bf16 v[8:11], a[168:171], a[48:51], v[8:11]\n\t" \
  "buffer_load_dwordx4 a[20:23], v249, %[ws], s97 offen sc1\n\t" \
  "v_mfma_f32_16x16x32_bf16 v[40:43], a[168:171], a[52:55], v[40:43]\n\t" \
  "s_and_b32 s98, %[bid], 0x3\n\t" \
  "ds_read_b64_tr_b16 a[142:143], v187 offset:50176\n\t" \
  "s_lshl_b32 s98, s98, 3\n\t" \
  "v_mfma_f32_16x16x32_bf16 v[72:75], a[168:171], a[56:59], v[72:75]\n\t" \
  "s_add_u32 %[vo], %[vo], s98\n\t" \
  "ds_read_b64_tr_b16 a[144:145], v188 offset:49152\n\t" \
  "s_and_b32 s98, %[bid], 0x4\n\t" \
  "v_mfma_f32_16x16x32_bf16 v[104:107], a[168:171], a[60:63], v[104:107]\n\t" \
  "s_lshl_b32 s98, s98, 0\n\t" \
  "ds_read_b64_tr_b16 a[146:147], v188 offset:50176\n\t" \
  "s_add_u32 %[vo], %[vo], s98\n\t" \
  "v_mfma_f32_16x16x32_bf16 v[108:111], a[172:175], a[60:63], v[108:111]\n\t" \
  "s_add_u32 %[vo], %[vo], 0x2000\n\t" \
  "s_cmp_lt_i32 %[prb], 0\n\t" \
  "s_cselect_b32 %[vo], %[prb], %[vo]\n\t" \
  "v_mfma_f32_16x16x32_bf16 v[76:79], a[172:175], a[56:59], v[76:79]\n\t" \
  "ds_read_b64_tr_b16 a[148:149], v189 offset:49152\n\t" \
  "v_mfma_f32_16x16x32_bf16 v[44:47], a[172:175], a[52:55], v[44:47]\n\t" \
  "buffer_load_dwordx4 a[24:27], v250, %[ws], s97 offen sc1\n\t" \
  "v_mfma_f32_16x16x32_bf16 v[12:15], a[172:175], a[48:51], v[12:15]\n\t" \
  "ds_read_b64_tr_b16 a[150:151], v189 offset:50176\n\t" \
  "v_mfma_f32_16x16x32_bf16 v[16:19], a[176:179], a[48:51], v[16:19]\n\t" \
  "ds_read_b64_tr_b16 a[152:153], v190 offset:49152\n\t" \
  "v_mfma_f32_16x16x32_bf16 v[48:51], a[176:179], a[52:55], v[48:51]\n\t" \
  "ds_read_b64_tr_b16 a[154:155], v190 offset:50176\n\t" \
  "v_mfma_f32_16x16x32_bf16 v[80:83], a[176:179], a[56:59], v[80:83]\n\t" \
  "ds_read_b64_tr_b16 a[156:157], v191 offset:49152\n\t" \
  "v_mfma_f32_16x16x32_bf16 v[112:115], a[176:179], a[60:63], v[112:115]\n\t" \
  "ds_read_b64_tr_b16 a[158:159], v191 offset:50176\n\t" \
  "v_mfma_f32_16x16x32_bf16 v[116:119], a[180:183], a[60:63], v[116:119]\n\t" \
  "v_mfma_f32_16x16x32_bf16 v[84:87], a[180:183], a[56:59], v[84:87]\n\t" \
  "buffer_load_dwordx4 a[28:31], v251, %[ws], s97 offen sc1\n\t" \
  "v_mfma_f32_16x16x32_bf16 v[52:55], a[180:183], a[52:55], v[52:55]\n\t" \
  "v_mbcnt_lo_u32_b32 v217, -1, 0\n\t" \
  "v_mbcnt_hi_u32_b32 v217, -1, v217\n\t" \
  "v_mfma_f32_16x16x32_bf16 v[20:23], a[180:183], a[48:51], v[20:23]\n\t" \
  "v_cmp_gt_u32 vcc, 16, v217\n\t" \
  "v_lshlrev_b32 v217, 7, v217\n\t" \
  "v_mfma_f32_16x16x32_bf16 v[24:27], a[184:187], a[48:51], v[24:27]\n\t" \
  "v_bfrev_b32 v215, 1\n\t" \
  "v_cndmask_b32 v217, v215, v217, vcc\n\t" \
  "v_mfma_f32_16x16x32_bf16 v[56:59], a[184:187], a[52:55], v[56:59]\n\t" \
  "v_mfma_f32_16x16x32_bf16 v[88:91], a[184:187], a[56:59], v[88:91]\n\t" \
  "s_waitcnt vmcnt(13)\n\t" \
  "v_lshlrev_b32 v128, 16, v136\n\t" \
  "v_and_b32 v136, 0xffff0000, v136\n\t" \
  "v_mfma_f32_16x16x32_bf16 v[120:123], a[184:187], a[60:63], v[120:123]\n\t" \
  "v_mul_f32 v176, v128, v168\n\t" \
  "v_mul_f32 v180, v136, v168\n\t" \
  "v_mfma_f32_16x16x32_bf16 v[124:127], a[188:191], a[60:63], v[124:127]\n\t" \
  "v_lshlrev_b32 v128, 16, v137\n\t" \
  "v_and_b32 v137, 0xffff0000, v137\n\t" \
  "v_mfma_f32_16x16x32_bf16 v[92:95], a[188:191], a[56:59], v[92:95]\n\t" \
  "s_add_u32 s97, %[rot19], 4718592\n\t" \
  "v_mul_f32 v177, v128, v168\n\t" \
  "v_mul_f32 v181, v137, v168\n\t" \
  "v_mfma_f32_16x16x32_bf16 v[60:63], a[188:191], a[52:55], v[60:63]\n\t" \
  "s_bfe_u32 s97, s97, 0x140003\n\t" \
  "s_add_u32 s97, s97, %[sox]\n\t" \
  "v_lshlrev_b32 v128, 16, v138\n\t" \
  "v_mfma_f32_16x16x32_bf16 v[28:31], a[188:191], a[48:51], v[28:31]\n\t" \
  "s_waitcnt lgkmcnt(0)\n\t" \
  "v_and_b32 v138, 0xffff0000, v138\n\t" \
  "v_mul_f32 v178, v128, v168\n\t" \
  "v_mfma_f32_16x16x32_bf16 v[0:3], a[128:131], a[192:195], v[0:3]\n\t" \
  "buffer_load_dwordx4 a[32:35], v248, %[ws], s97 offen sc1\n\t" \
  "v_mfma_f32_16x16x32_bf16 v[32:35], a[128:131], a[196:199], v[32:35]\n\t" \
  "ds_read_b64_tr_b16 a[160:161], v184 offset:57344\n\t" \
  "v_mul_f32 v182, v138, v168\n\t" \
  "v_mfma_f32_16x16x32_bf16 v[64:67], a[128:131], a[200:203], v[64:67]\n\t" \
  "ds_read_b64_tr_b16 a[162:163], v184 offset:58368\n\t" \
  "v_lshlrev_b32 v128, 16, v139\n\t" \
  "v_mfma_f32_16x16x32_bf16 v[96:99], a[128:131], a[204:207], v[96:99]\n\t" \
  "v_and_b32 v139, 0xffff0000, v139\n\t" \
  "v_mul_f32 v179, v128, v168\n\t" \
  "v_mfma_f32_16x16x32_bf16 v[100:103], a[132:135], a[204:207], v[100:103]\n\t" \
  "buffer_load_dwordx4 a[36:39], v249, %[ws], s97 offen sc1\n\t" \
  "v_mfma_f32_16x16x32_bf16 v[68:71], a[132:135], a[200:203], v[68:71]\n\t" \
  "ds_read_b64_tr_b16 a[164:165], v185 offset:57344\n\t" \
  "v_mul_f32 v183, v139, v168\n\t" \
  "v_mfma_f32_16x16x32_bf16 v[36:39], a[132:135], a[196:199], v[36:39]\n\t" \
  "ds_read_b64_tr_b16 a[166:167], v185 offset:58368\n\t" \
  "v_mfma_f32_16x16x32_bf16 v[4:7], a[132:135], a[192:195], v[4:7]\n\t" \
  "ds_read_b64_tr_b16 a[168:169], v186 offset:57344\n\t" \
  "v_mfma_f32_16x16x32_bf16 v[8:11], a[136:139], a[192:195], v[8:11]\n\t" \
  "buffer_load_dwordx4 a[40:43], v250, %[ws], s97 offen sc1\n\t" \
  "v_mfma_f32_16x16x32_bf16 v[40:43], a[136:139], a[196:199], v[40:43]\n\t" \
  "ds_read_b64_tr_b16 a[170:171], v186 offset:58368\n\t" \
  "v_mfma_f32_16x16x32_bf16 v[72:75], a[136:139], a[200:203], v[72:75]\n\t" \
  "ds_read_b64_tr_b16 a[172:173], v187 offset:57344\n\t" \
  "v_mfma_f32_16x16x32_bf16 v[104:107], a[136:139], a[204:207], v[104:107]\n\t" \
  "s_waitcnt vmcnt(15)\n\t" \
  "v_lshlrev_b32 v128, 16, v140\n\t" \
  "v_and_b32 v140, 0xffff0000, v140\n\t" \
  "v_mfma_f32_16x16x32_bf16 v[108:111], a[140:143], a[204:207], v[108:111]\n\t" \
  "buffer_load_dwordx4 a[44:47], v251, %[ws], s97 offen sc1\n\t" \
  "v_mfma_f32_16x16x32_bf16 v[76:79], a[140:143], a[200:203], v[76:79]\n\t" \
  "s_add_u32 s96, %[rot19], 5767168\n\t" \
  "s_and_b32 s96, s96, 0x780000\n\t" \
  "s_add_u32 m0, %[mw], 16384\n\t" \
  "v_mfma_f32_16x16x32_bf16 v[44:47], a[140:143], a[196:199], v[44:47]\n\t" \
  "ds_read_b64_tr_b16 a[174:175], v187 offset:58368\n\t" \
  "v_fma_f32 v176, v128, v169, v176\n\t" \
  "v_mfma_f32_16x16x32_bf16 v[12:15], a[140:143], a[192:195], v[12:15]\n\t" \
  "buffer_load_dwordx4 v200, %[sw], s96 offen offset:0 lds\n\t" \
  "v_mfma_f32_16x16x32_bf16 v[16:19], a[144:147], a[192:195], v[16:19]\n\t" \
  "ds_read_b64_tr_b16 a[176:177], v188 offset:57344\n\t" \
  "v_fma_f32 v180, v140, v169, v180\n\t" \
  "v_mfma_f32_16x16x32_bf16 v[48:51], a[144:147], a[196:199], v[48:51]\n\t" \
  "ds_read_b64_tr_b16 a[178:179], v188 offset:58368\n\t" \
  "v_lshlrev_b32 v128, 16, v141\n\t" \
  "v_mfma_f32_16x16x32_bf16 v[80:83], a[144:147], a[200:203], v[80:83]\n\t" \
  "ds_read_b64_tr_b16 a[180:181], v189 offset:57344\n\t" \
  "v_and_b32 v141, 0xffff0000, v141\n\t" \
  "v_mfma_f32_16x16x32_bf16 v[112:115], a[144:147], a[204:207], v[112:115]\n\t" \
  "buffer_load_dwordx4 v201, %[sw], s96 offen offset:1024 lds\n\t" \
  "v_mfma_f32_16x16x32_bf16 v[116:119], a[148:151], a[204:207], v[116:119]\n\t" \
  "ds_read_b64_tr_b16 a[182:183], v189 offset:58368\n\t" \
  "v_fma_f32 v177, v128, v169, v177\n\t" \
  "v_mfma_f32_16x16x32_bf16 v[84:87], a[148:151], a[200:203], v[84:87]\n\t" \
  "ds_read_b64_tr_b16 a[184:185], v190 offset:57344\n\t" \
  "v_fma_f32 v181, v141, v169, v181\n\t" \
  "v_mfma_f32_16x16x32_bf16 v[52:55], a[148:151], a[196:199], v[52:55]\n\t" \
  "ds_read_b64_tr_b16 a[186:187], v190 offset:58368\n\t" \
  "v_lshlrev_b32 v128, 16, v142\n\t" \
  "v_mfma_f32_16x16x32_bf16 v[20:23], a[148:151], a[192:195], v[20:23]\n\t" \
  "buffer_load_dwordx4 v202, %[sw], s96 offen offset:2048 lds\n\t" \
  "v_mfma_f32_16x16x32_bf16 v[24:27], a[152:155], a[192:195], v[24:27]\n\t" \
  "ds_read_b64_tr_b16 a[188:189], v191 offset:57344\n\t" \
  "v_and_b32 v142, 0xffff0000, v142\n\t" \
  "v_mfma_f32_16x16x32_bf16 v[56:59], a[152:155], a[196:199], v[56:59]\n\t" \
  "ds_read_b64_tr_b16 a[190:191], v191 offset:58368\n\t" \
  "v_fma_f32 v178, v128, v169, v178\n\t" \
  "v_mfma_f32_16x16x32_bf16 v[88:91], a[152:155], a[200:203], v[88:91]\n\t" \
  "v_fma_f32 v182, v142, v169, v182\n\t" \
  "v_lshlrev_b32 v128, 16, v143\n\t" \
  "v_mfma_f32_16x16x32_bf16 v[120:123], a[152:155], a[204:207], v[120:123]\n\t" \
  "buffer_load_dwordx4 v203, %[sw], s96 offen offset:3072 lds\n\t" \
  "v_mfma_f32_16x16x32_bf16 v[124:127], a[156:159], a[204:207], v[124:127]\n\t" \
  "v_and_b32 v143, 0xffff0000, v143\n\t" \
  "v_fma_f32 v179, v128, v169, v179\n\t" \
  "v_mfma_f32_16x16x32_bf16 v[92:95], a[156:159], a[200:203], v[92:95]\n\t" \
  "s_add_u32 s97, s97, %[dk]\n\t" \
  "v_fma_f32 v183, v143, v169, v183\n\t" \
  "v_mfma_f32_16x16x32_bf16 v[60:63], a[156:159], a[196:199], v[60:63]\n\t" \
  "s_waitcnt lgkmcnt(0)\n\t" \
  "v_mfma_f32_16x16x32_bf16 v[28:31], a[156:159], a[192:195], v[28:31]\n\t" \
  "s_barrier\n\t" \
  "v_mfma_f32_16x16x32_bf16 v[0:3], a[160:163], a[208:211], v[0:3]\n\t" \
  "buffer_load_dwordx4 a[48:51], v248, %[ws], s97 offen sc1\n\t" \
  "ds_read_b64_tr_b16 a[128:129], v192 offset:0\n\t" \
  "ds_read_b64_tr_b16 a[130:131], v192 offset:1024\n\t" \
  "ds_read_b64_tr_b16 a[132:133], v193 offset:0\n\t" \
  "v_mfma_f32_16x16x32_bf16 v[32:35], a[160:163], a[212:215], v[32:35]\n\t" \
  "ds_read_b64_tr_b16 a[134:135], v193 offset:1024\n\t" \
  "ds_read_b64_tr_b16 a[136:137], v194 offset:0\n\t" \
  "ds_read_b64_tr_b16 a[138:139], v194 offset:1024\n\t" \
  "ds_read_b64_tr_b16 a[140:141], v195 offset:0\n\t" \
  "v_mfma_f32_16x16x32_bf16 v[64:67], a[160:163], a[216:219], v[64:67]\n\t" \
  "ds_read_b64_tr_b16 a[142:143], v195 offset:1024\n\t" \
  "ds_read_b64_tr_b16 a[144:145], v196 offset:0\n\t" \
  "ds_read_b64_tr_b16 a[146:147], v196 offset:1024\n\t" \
  "ds_read_b64_tr_b16 a[148:149], v197 offset:0\n\t" \
  "v_mfma_f32_16x16x32_bf16 v[96:99], a[160:163], a[220:223], v[96:99]\n\t" \
  "ds_read_b64_tr_b16 a[150:151], v197 offset:1024\n\t" \
  "v_mbcnt_lo_u32_b32 v233, -1, 0\n\t" \
  "v_mfma_f32_16x16x32_bf16 v[100:103], a[164:167], a[220:223], v[100:103]\n\t" \
  "ds_read_b64_tr_b16 a[152:153], v198 offset:0\n\t" \
  "v_mbcnt_hi_u32_b32 v233, -1, v233\n\t" \
  "v_mfma_f32_16x16x32_bf16 v[68:71], a[164:167], a[216:219], v[68:71]\n\t" \
  "buffer_load_dwordx4 a[52:55], v249, %[ws], s97 offen sc1\n\t" \
  "v_mfma_f32_16x16x32_bf16 v[36:39], a[164:167], a[212:215], v[36:39]\n\t" \
  "ds_read_b64_tr_b16 a[154:155], v198 offset:1024\n\t" \
  "v_lshrrev_b32 v129, 4, v233\n\t" \
  "v_mfma_f32_16x16x32_bf16 v[4:7], a[164:167], a[208:211], v[4:7]\n\t" \
  "ds_read_b64_tr_b16 a[156:157], v199 offset:0\n\t" \
  "v_and_b32 v130, 15, v233\n\t" \
  "v_mfma_f32_16x16x32_bf16 v[8:11], a[168:171], a[208:211], v[8:11]\n\t" \
  "ds_read_b64_tr_b16 a[158:159], v199 offset:1024\n\t" \
  "v_bfe_u32 v131, v233, 2, 2\n\t" \
  "v_mfma_f32_16x16x32_bf16 v[40:43], a[168:171], a[212:215], v[40:43]\n\t" \
  "ds_read_b64_tr_b16 a[64:65], v192 offset:8192\n\t" \
  "v_and_b32 v132, 3, v233\n\t" \
  "v_mfma_f32_16x16x32_bf16 v[72:75], a[168:171], a[216:219], v[72:75]\n\t" \
  "buffer_load_dwordx4 a[56:59], v250, %[ws], s97 offen sc1\n\t" \
  "v_mfma_f32_16x16x32_bf16 v[104:107], a[168:171], a[220:223], v[104:107]\n\t" \
  "ds_read_b64_tr_b16 a[66:67], v192 offset:9216\n\t" \
  "v_cmp_lt_u32 vcc, 6, v233\n\t" \
  "v_mfma_f32_16x16x32_bf16 v[108:111], a[172:175], a[220:223], v[108:111]\n\t" \
  "ds_read_b64_tr_b16 a[68:69], v193 offset:8192\n\t" \
  "v_mov_b32 v133, 6272\n\t" \
  "v_mfma_f32_16x16x32_bf16 v[76:79], a[172:175], a[216:219], v[76:79]\n\t" \
  "ds_read_b64_tr_b16 a[70:71], v193 offset:9216\n\t" \
  "v_cndmask_b32 v133, 0, v133, vcc\n\t" \
  "v_mfma_f32_16x16x32_bf16 v[44:47], a[172:175], a[212:215], v[44:47]\n\t" \
  "ds_read_b64_tr_b16 a[72:73], v194 offset:8192\n\t" \
  "v_lshl_add_u32 v133, v233, 7, v133\n\t" \
  "v_mfma_f32_16x16x32_bf16 v[12:15], a[172:175], a[208:211], v[12:15]\n\t" \
  "buffer_load_dwordx4 a[60:63], v251, %[ws], s97 offen sc1\n\t" \
  "v_mfma_f32_16x16x32_bf16 v[16:19], a[176:179], a[208:211], v[16:19]\n\t" \
  "s_mov_b32 s99, 24576\n\t" \
  "ds_read_b64_tr_b16 a[74:75], v194 offset:9216\n\t" \
  "v_cmp_gt_u32 vcc, 14, v233\n\t" \
  "v_mfma_f32_16x16x32_bf16 v[48:51], a[176:179], a[212:215], v[48:51]\n\t" \
  "ds_read_b64_tr_b16 a[76:77], v195 offset:8192\n\t" \
  "v_bfrev_b32 v247, 1\n\t" \
  "v_mfma_f32_16x16x32_bf16 v[80:83], a[176:179], a[216:219], v[80:83]\n\t" \
  "ds_read_b64_tr_b16 a[78:79], v195 offset:9216\n\t" \
  "v_cndmask_b32 v245, v247, v133, vcc\n\t" \
  "v_mfma_f32_16x16x32_bf16 v[112:115], a[176:179], a[220:223], v[112:115]\n\t" \
  "ds_read_b64_tr_b16 a[80:81], v196 offset:8192\n\t" \
  "v_writelane_b32 v247, %[prb], 0\n\t" \
  "v_mfma_f32_16x16x32_bf16 v[116:119], a[180:183], a[220:223], v[116:119]\n\t" \
  "buffer_load_dwordx4 v[144:147], v209, %[sM], s99 offen\n\t" \
  "v_mfma_f32_16x16x32_bf16 v[84:87], a[180:183], a[216:219], v[84:87]\n\t" \
  "s_mov_b32 s99, 36864\n\t" \
  "ds_read_b64_tr_b16 a[82:83], v196 offset:9216\n\t" \
  "v_lshrrev_b32 v133, 1, v129\n\t" \
  "v_mfma_f32_16x16x32_bf16 v[52:55], a[180:183], a[212:215], v[52:55]\n\t" \
  "ds_read_b64_tr_b16 a[84:85], v197 offset:8192\n\t" \
  "v_and_b32 v134, 1, v129\n\t" \
  "v_mfma_f32_16x16x32_bf16 v[20:23], a[180:183], a[208:211], v[20:23]\n\t" \
  "ds_read_b64_tr_b16 a[86:87], v197 offset:9216\n\t" \
  "v_lshl_or_b32 v133, v134, 1, v133\n\t" \
  "v_mfma_f32_16x16x32_bf16 v[24:27], a[184:187], a[208:211], v[24:27]\n\t" \
  "ds_read_b64_tr_b16 a[88:89], v198 offset:8192\n\t" \
  "v_xor_b32 v133, v133, v130\n\t" \
  "v_mfma_f32_16x16x32_bf16 v[56:59], a[184:187], a[212:215], v[56:59]\n\t" \
  "buffer_load_dwordx4 v[148:151], v209, %[sM], s99 offen\n\t" \
  "v_mfma_f32_16x16x32_bf16 v[88:91], a[184:187], a[216:219], v[88:91]\n\t" \
  "ds_read_b64_tr_b16 a[90:91], v198 offset:9216\n\t" \
  "v_lshlrev_b32 v133, 4, v133\n\t" \
  "v_mfma_f32_16x16x32_bf16 v[120:123], a[184:187], a[220:223], v[120:123]\n\t" \
  "ds_read_b64_tr_b16 a[92:93], v199 offset:8192\n\t" \
  "v_lshl_add_u32 v133, v130, 8, v133\n\t" \
  "v_mfma_f32_16x16x32_bf16 v[124:127], a[188:191], a[220:223], v[124:127]\n\t" \
  "ds_read_b64_tr_b16 a[94:95], v199 offset:9216\n\t" \
  "v_add_u32 v220, 49152, v133\n\t" \
  "v_mfma_f32_16x16x32_bf16 v[92:95], a[188:191], a[216:219], v[92:95]\n\t" \
  "s_add_u32 s97, %[rot19], 5242880\n\t" \
  "v_xor_b32 v221, 64, v220\n\t" \
  "v_xor_b32 v222, 128, v220\n\t" \
  "v_mfma_f32_16x16x32_bf16 v[60:63], a[188:191], a[212:215], v[60:63]\n\t" \
  "s_waitcnt lgkmcnt(0)\n\t" \
  "s_bfe_u32 s97, s97, 0x140003\n\t" \
  "s_add_u32 s97, s97, %[sox]\n\t" \
  "v_mfma_f32_16x16x32_bf16 v[28:31], a[188:191], a[208:211], v[28:31]\n\t" \
  "s_barrier\n\t" \
  "v_mfma_f32_16x16x32_bf16 v[0:3], a[128:131], a[224:227], v[0:3]\n\t" \
  "buffer_load_dwordx4 a[192:195], v248, %[ws], s97 offen sc1\n\t" \
  "v_mfma_f32_16x16x32_bf16 v[0:3], a[64:67], a[240:243], v[0:3]\n\t" \
  "ds_read_b64_tr_b16 a[160:161], v192 offset:16384\n\t" \
  "v_xor_b32 v223, 192, v220\n\t" \
  "v_mfma_f32_16x16x32_bf16 v[32:35], a[64:67], a[244:247], v[32:35]\n\t" \
  "ds_read_b64_tr_b16 a[162:163], v192 offset:17408\n\t" \
  "v_xor_b32 v133, v130, v129\n\t" \
  "v_mfma_f32_16x16x32_bf16 v[32:35], a[128:131], a[228:231], v[32:35]\n\t" \
  "v_lshlrev_b32 v133, 4, v133\n\t" \
  "v_lshl_add_u32 v224, v129, 8, v133\n\t" \
  "v_mfma_f32_16x16x32_bf16 v[64:67], a[128:131], a[232:235], v[64:67]\n\t" \
  "buffer_load_dwordx4 a[196:199], v249, %[ws], s97 offen sc1\n\t" \
  "v_mfma_f32_16x16x32_bf16 v[64:67], a[64:67], a[248:251], v[64:67]\n\t" \
  "ds_read_b64_tr_b16 a[164:165], v193 offset:16384\n\t" \
  "v_xor_b32 v225, 64, v224\n\t" \
  "v_mfma_f32_16x16x32_bf16 v[96:99], a[64:67], a[252:255], v[96:99]\n\t" \
  "ds_read_b64_tr_b16 a[166:167], v193 offset:17408\n\t" \
  "v_xor_b32 v226, 128, v224\n\t" \
  "v_mfma_f32_16x16x32_bf16 v[96:99], a[128:131], a[236:239], v[96:99]\n\t" \
  "ds_read_b64_tr_b16 a[168:169], v194 offset:16384\n\t" \
  "v_xor_b32 v227, 192, v224\n\t" \
  "v_mfma_f32_16x16x32_bf16 v[4:7], a[132:135], a[224:227], v[4:7]\n\t" \
  "buffer_load_dwordx4 a[200:203], v250, %[ws], s97 offen sc1\n\t" \
  "v_mfma_f32_16x16x32_bf16 v[4:7], a[68:71], a[240:243], v[4:7]\n\t" \
  "ds_read_b64_tr_b16 a[170:171], v194 offset:17408\n\t" \
  "v_lshlrev_b32 v133, 3, v132\n\t" \
  "v_mfma_f32_16x16x32_bf16 v[36:39], a[68:71], a[244:247], v[36:39]\n\t" \
  "ds_read_b64_tr_b16 a[172:173], v195 offset:16384\n\t" \
  "v_lshl_add_u32 v133, v129, 10, v133\n\t" \
  "v_mfma_f32_16x16x32_bf16 v[36:39], a[132:135], a[228:231], v[36:39]\n\t" \
  "v_lshl_add_u32 v228, v131, 6, v133\n\t" \
  "v_lshl_add_u32 v228, v134, 5, v228\n\t" \
  "v_mfma_f32_16x16x32_bf16 v[68:71], a[132:135], a[232:235], v[68:71]\n\t" \
  "buffer_load_dwordx4 a[204:207], v251, %[ws], s97 offen sc1\n\t" \
  "v_mfma_f32_16x16x32_bf16 v[68:71], a[68:71], a[248:251], v[68:71]\n\t" \
  "s_add_u32 s96, %[rot19], 6291456\n\t" \
  "s_and_b32 s96, s96, 0x780000\n\t" \
  "s_add_u32 m0, %[mw], 32768\n\t" \
  "v_mfma_f32_16x16x32_bf16 v[100:103], a[68:71], a[252:255], v[100:103]\n\t" \
  "ds_read_b64_tr_b16 a[174:175], v195 offset:17408\n\t" \
  "v_add_u32 v228, 81920, v228\n\t" \
  "v_mfma_f32_16x16x32_bf16 v[100:103], a[132:135], a[236:239], v[100:103]\n\t" \
  "buffer_load_dwordx4 v200, %[sw], s96 offen offset:0 lds\n\t" \
  "v_mfma_f32_16x16x32_bf16 v[8:11], a[136:139], a[224:227], v[8:11]\n\t" \
  "ds_read_b64_tr_b16 a[176:177], v196 offset:16384\n\t" \
  "v_xor_b32 v229, 32, v228\n\t" \
  "v_mfma_f32_16x16x32_bf16 v[8:11], a[72:75], a[240:243], v[8:11]\n\t" \
  "ds_read_b64_tr_b16 a[178:179], v196 offset:17408\n\t" \
  "v_lshl_add_u32 v244, v131, 8, v133\n\t" \
  "v_mfma_f32_16x16x32_bf16 v[40:43], a[72:75], a[244:247], v[40:43]\n\t" \
  "ds_read_b64_tr_b16 a[180:181], v197 offset:16384\n\t" \
  "v_lshl_add_u32 v244, v131, 5, v244\n\t" \
  "v_mfma_f32_16x16x32_bf16 v[40:43], a[136:139], a[228:231], v[40:43]\n\t" \
  "buffer_load_dwordx4 v201, %[sw], s96 offen offset:1024 lds\n\t" \
  "v_mfma_f32_16x16x32_bf16 v[72:75], a[136:139], a[232:235], v[72:75]\n\t" \
  "ds_read_b64_tr_b16 a[182:183], v197 offset:17408\n\t" \
  "v_lshl_add_u32 v244, v134, 7, v244\n\t" \
  "v_mfma_f32_16x16x32_bf16 v[72:75], a[72:75], a[248:251], v[72:75]\n\t" \
  "ds_read_b64_tr_b16 a[184:185], v198 offset:16384\n\t" \
  "v_lshlrev_b32 v133, 4, v132\n\t" \
  "v_mfma_f32_16x16x32_bf16 v[104:107], a[72:75], a[252:255], v[104:107]\n\t" \
  "ds_read_b64_tr_b16 a[186:187], v198 offset:17408\n\t" \
  "v_lshl_add_u32 v133, v129, 6, v133\n\t" \
  "v_mfma_f32_16x16x32_bf16 v[104:107], a[136:139], a[236:239], v[104:107]\n\t" \
  "buffer_load_dwordx4 v202, %[sw], s96 offen offset:2048 lds\n\t" \
  "v_mfma_f32_16x16x32_bf16 v[12:15], a[140:143], a[224:227], v[12:15]\n\t" \
  "ds_read_b64_tr_b16 a[188:189], v199 offset:16384\n\t" \
  "v_lshl_add_u32 v230, v131, 8, v133\n\t" \
  "v_mfma_f32_16x16x32_bf16 v[12:15], a[76:79], a[240:243], v[12:15]\n\t" \
  "ds_read_b64_tr_b16 a[190:191], v199 offset:17408\n\t" \
  "v_xor_b32 v231, 32, v230\n\t" \
  "v_mfma_f32_16x16x32_bf16 v[44:47], a[76:79], a[244:247], v[44:47]\n\t" \
  "ds_read_b64_tr_b16 a[96:97], v192 offset:24576\n\t" \
  "v_lshlrev_b32 v133, 1, v129\n\t" \
  "v_mfma_f32_16x16x32_bf16 v[44:47], a[140:143], a[228:231], v[44:47]\n\t" \
  "buffer_load_dwordx4 v203, %[sw], s96 offen offset:3072 lds\n\t" \
  "v_mfma_f32_16x16x32_bf16 v[76:79], a[140:143], a[232:235], v[76:79]\n\t" \
  "ds_read_b64_tr_b16 a[98:99], v192 offset:25600\n\t" \
  "v_xor_b32 v133, v133, v130\n\t" \
  "v_mfma_f32_16x16x32_bf16 v[76:79], a[76:79], a[248:251], v[76:79]\n\t" \
  "s_add_u32 s97, s97, %[dk]\n\t" \
  "v_lshlrev_b32 v133, 4, v133\n\t" \
  "v_lshl_add_u32 v240, v129, 11, v133\n\t" \
  "v_mfma_f32_16x16x32_bf16 v[108:111], a[76:79], a[252:255], v[108:111]\n\t" \
  "ds_read_b64_tr_b16 a[100:101], v193 offset:24576\n\t" \
  "v_xor_b32 v241, 0x80, v240\n\t" \
  "v_mfma_f32_16x16x32_bf16 v[108:111], a[140:143], a[236:239], v[108:111]\n\t" \
  "ds_read_b64_tr_b16 a[102:103], v193 offset:25600\n\t" \
  "v_lshrrev_b32 v133, 1, v233\n\t" \
  "v_mfma_f32_16x16x32_bf16 v[16:19], a[144:147], a[224:227], v[16:19]\n\t" \
  "buffer_load_dwordx4 a[208:211], v248, %[ws], s97 offen sc1\n\t" \
  "v_mfma_f32_16x16x32_bf16 v[16:19], a[80:83], a[240:243], v[16:19]\n\t" \
  "ds_read_b64_tr_b16 a[104:105], v194 offset:24576\n\t" \
  "v_mul_u32_u24 v133, 57344, v133\n\t" \
  "v_mfma_f32_16x16x32_bf16 v[48:51], a[80:83], a[244:247], v[48:51]\n\t" \
  "v_and_b32 v134, 1, v233\n\t" \
  "v_lshl_add_u32 v133, v134, 4, v133\n\t" \
  "v_mfma_f32_16x16x32_bf16 v[48:51], a[144:147], a[228:231], v[48:51]\n\t" \
  "ds_read_b64_tr_b16 a[106:107], v194 offset:25600\n\t" \
  "v_or_b32 v232, %[idm], v133\n\t" \
  "v_mfma_f32_16x16x32_bf16 v[80:83], a[144:147], a[232:235], v[80:83]\n\t" \
  "ds_read_b64_tr_b16 a[108:109], v195 offset:24576\n\t" \
  "v_mfma_f32_16x16x32_bf16 v[80:83], a[80:83], a[248:251], v[80:83]\n\t" \
  "ds_read_b64_tr_b16 a[110:111], v195 offset:25600\n\t" \
  "v_mfma_f32_16x16x32_bf16 v[112:115], a[80:83], a[252:255], v[112:115]\n\t" \
  "buffer_load_dwordx4 a[212:215], v249, %[ws], s97 offen sc1\n\t" \
  "v_mfma_f32_16x16x32_bf16 v[112:115], a[144:147], a[236:239], v[112:115]\n\t" \
  "ds_read_b64_tr_b16 a[112:113], v196 offset:24576\n\t" \
  "v_mfma_f32_16x16x32_bf16 v[20:23], a[148:151], a[224:227], v[20:23]\n\t" \
  "ds_read_b64_tr_b16 a[114:115], v196 offset:25600\n\t" \
  "v_mfma_f32_16x16x32_bf16 v[20:23], a[84:87], a[240:243], v[20:23]\n\t" \
  "ds_read_b64_tr_b16 a[116:117], v197 offset:24576\n\t" \
  "v_mfma_f32_16x16x32_bf16 v[52:55], a[84:87], a[244:247], v[52:55]\n\t" \
  "ds_read_b64_tr_b16 a[118:119], v197 offset:25600\n\t" \
  "v_mfma_f32_16x16x32_bf16 v[52:55], a[148:151], a[228:231], v[52:55]\n\t" \
  "ds_read_b64_tr_b16 a[120:121], v198 offset:24576\n\t" \
  "v_mfma_f32_16x16x32_bf16 v[84:87], a[148:151], a[232:235], v[84:87]\n\t" \
  "ds_read_b64_tr_b16 a[122:123], v198 offset:25600\n\t" \
  "v_mfma_f32_16x16x32_bf16 v[84:87], a[84:87], a[248:251], v[84:87]\n\t" \
  "ds_read_b64_tr_b16 a[124:125], v199 offset:24576\n\t" \
  "v_mfma_f32_16x16x32_bf16 v[116:119], a[84:87], a[252:255], v[116:119]\n\t" \
  "buffer_load_dwordx4 a[216:219], v250, %[ws], s97 offen sc1\n\t" \
  "v_mfma_f32_16x16x32_bf16 v[116:119], a[148:151], a[236:239], v[116:119]\n\t" \
  "ds_read_b64_tr_b16 a[126:127], v199 offset:25600\n\t" \
  "v_mfma_f32_16x16x32_bf16 v[24:27], a[152:155], a[224:227], v[24:27]\n\t" \
  "v_mfma_f32_16x16x32_bf16 v[24:27], a[88:91], a[240:243], v[24:27]\n\t" \
  "v_mfma_f32_16x16x32_bf16 v[56:59], a[88:91], a[244:247], v[56:59]\n\t" \
  "v_mfma_f32_16x16x32_bf16 v[56:59], a[152:155], a[228:231], v[56:59]\n\t" \
  "v_mfma_f32_16x16x32_bf16 v[88:91], a[152:155], a[232:235], v[88:91]\n\t" \
  "v_mfma_f32_16x16x32_bf16 v[88:91], a[88:91], a[248:251], v[88:91]\n\t" \
  "buffer_load_dwordx4 a[220:223], v251, %[ws], s97 offen sc1\n\t" \
  "v_mfma_f32_16x16x32_bf16 v[120:123], a[88:91], a[252:255], v[120:123]\n\t" \
  "v_mfma_f32_16x16x32_bf16 v[120:123], a[152:155], a[236:239], v[120:123]\n\t" \
  "v_mfma_f32_16x16x32_bf16 v[28:31], a[156:159], a[224:227], v[28:31]\n\t" \
  "v_mfma_f32_16x16x32_bf16 v[28:31], a[92:95], a[240:243], v[28:31]\n\t" \
  "v_mfma_f32_16x16x32_bf16 v[60:63], a[92:95], a[244:247], v[60:63]\n\t" \
  "s_waitcnt vmcnt(13)\n\t" \
  "v_lshlrev_b32 v128, 16, v144\n\t" \
  "v_and_b32 v144, 0xffff0000, v144\n\t" \
  "v_mfma_f32_16x16x32_bf16 v[60:63], a[156:159], a[228:231], v[60:63]\n\t" \
  "v_fma_f32 v176, v128, v170, v176\n\t" \
  "v_fma_f32 v180, v144, v170, v180\n\t" \
  "v_mfma_f32_16x16x32_bf16 v[92:95], a[156:159], a[232:235], v[92:95]\n\t" \
  "v_lshlrev_b32 v128, 16, v145\n\t" \
  "v_and_b32 v145, 0xffff0000, v145\n\t" \
  "v_mfma_f32_16x16x32_bf16 v[92:95], a[92:95], a[248:251], v[92:95]\n\t" \
  "s_add_u32 s97, %[rot19], 5767168\n\t" \
  "v_fma_f32 v177, v128, v170, v177\n\t" \
  "v_fma_f32 v181, v145, v170, v181\n\t" \
  "v_mfma_f32_16x16x32_bf16 v[124:127], a[92:95], a[252:255], v[124:127]\n\t" \
  "s_waitcnt lgkmcnt(0)\n\t" \
  "s_bfe_u32 s97, s97, 0x140003\n\t" \
  "s_add_u32 s97, s97, %[sox]\n\t" \
  "v_mfma_f32_16x16x32_bf16 v[124:127], a[156:159], a[236:239], v[124:127]\n\t" \
  "s_barrier\n\t" \
  "v_mfma_f32_16x16x32_bf16 v[0:3], a[160:163], a[0:3], v[0:3]\n\t" \
  "buffer_load_dwordx4 a[224:227], v248, %[ws], s97 offen sc1\n\t" \
  "v_mfma_f32_16x16x32_bf16 v[0:3], a[96:99], a[16:19], v[0:3]\n\t" \
  "ds_read_b64_tr_b16 a[128:129], v192 offset:32768\n\t" \
  "v_lshlrev_b32 v128, 16, v146\n\t" \
  "v_mfma_f32_16x16x32_bf16 v[32:35], a[96:99], a[20:23], v[32:35]\n\t" \
  "ds_read_b64_tr_b16 a[130:131], v192 offset:33792\n\t" \
  "v_and_b32 v146, 0xffff0000, v146\n\t" \
  "v_mfma_f32_16x16x32_bf16 v[32:35], a[160:163], a[4:7], v[32:35]\n\t" \
  "v_fma_f32 v178, v128, v170, v178\n\t" \
  "v_fma_f32 v182, v146, v170, v182\n\t" \
  "v_mfma_f32_16x16x32_bf16 v[64:67], a[160:163], a[8:11], v[64:67]\n\t" \
  "buffer_load_dwordx4 a[228:231], v249, %[ws], s97 offen sc1\n\t" \
  "v_mfma_f32_16x16x32_bf16 v[64:67], a[96:99], a[24:27], v[64:67]\n\t" \
  "ds_read_b64_tr_b16 a[132:133], v193 offset:32768\n\t" \
  "v_lshlrev_b32 v128, 16, v147\n\t" \
  "v_mfma_f32_16x16x32_bf16 v[96:99], a[96:99], a[28:31], v[96:99]\n\t" \
  "ds_read_b64_tr_b16 a[134:135], v193 offset:33792\n\t" \
  "v_and_b32 v147, 0xffff0000, v147\n\t" \
  "v_mfma_f32_16x16x32_bf16 v[96:99], a[160:163], a[12:15], v[96:99]\n\t" \
  "ds_read_b64_tr_b16 a[136:137], v194 offset:32768\n\t" \
  "v_fma_f32 v179, v128, v170, v179\n\t" \
  "v_mfma_f32_16x16x32_bf16 v[4:7], a[164:167], a[0:3], v[4:7]\n\t" \
  "buffer_load_dwordx4 a[232:235], v250, %[ws], s97 offen sc1\n\t" \
  "v_mfma_f32_16x16x32_bf16 v[4:7], a[100:103], a[16:19], v[4:7]\n\t" \
  "ds_read_b64_tr_b16 a[138:139], v194 offset:33792\n\t" \
  "v_fma_f32 v183, v147, v170, v183\n\t" \
  "v_mfma_f32_16x16x32_bf16 v[36:39], a[100:103], a[20:23], v[36:39]\n\t" \
  "s_waitcnt vmcnt(15)\n\t" \
  "v_lshlrev_b32 v128, 16, v148\n\t" \
  "v_and_b32 v148, 0xffff0000, v148\n\t" \
  "v_mfma_f32_16x16x32_bf16 v[36:39], a[164:167], a[4:7], v[36:39]\n\t" \
  "ds_read_b64_tr_b16 a[140:141], v195 offset:32768\n\t" \
  "v_fma_f32 v176, v128, v171, v176\n\t" \
  "v_mfma_f32_16x16x32_bf16 v[68:71], a[164:167], a[8:11], v[68:71]\n\t" \
  "buffer_load_dwordx4 a[236:239], v251, %[ws], s97 offen sc1\n\t" \
  "v_mfma_f32_16x16x32_bf16 v[68:71], a[100:103], a[24:27], v[68:71]\n\t" \
  "s_add_u32 s96, %[rot19], 6815744\n\t" \
  "s_and_b32 s96, s96, 0x780000\n\t" \
  "s_add_u32 m0, %[mw], 65536\n\t" \
  "v_mfma_f32_16x16x32_bf16 v[100:103], a[100:103], a[28:31], v[100:103]\n\t" \
  "ds_read_b64_tr_b16 a[142:143], v195 offset:33792\n\t" \
  "v_fma_f32 v180, v148, v171, v180\n\t" \
  "v_mfma_f32_16x16x32_bf16 v[100:103], a[164:167], a[12:15], v[100:103]\n\t" \
  "buffer_load_dwordx4 v200, %[sw], s96 offen offset:0 lds\n\t" \
  "v_mfma_f32_16x16x32_bf16 v[8:11], a[168:171], a[0:3], v[8:11]\n\t" \
  "ds_read_b64_tr_b16 a[144:145], v196 offset:32768\n\t" \
  "v_lshlrev_b32 v128, 16, v149\n\t" \
  "v_mfma_f32_16x16x32_bf16 v[8:11], a[104:107], a[16:19], v[8:11]\n\t" \
  "ds_read_b64_tr_b16 a[146:147], v196 offset:33792\n\t" \
  "v_and_b32 v149, 0xffff0000, v149\n\t" \
  "v_mfma_f32_16x16x32_bf16 v[40:43], a[104:107], a[20:23], v[40:43]\n\t" \
  "ds_read_b64_tr_b16 a[148:149], v197 offset:32768\n\t" \
  "v_fma_f32 v177, v128, v171, v177\n\t" \
  "v_mfma_f32_16x16x32_bf16 v[40:43], a[168:171], a[4:7], v[40:43]\n\t" \
  "buffer_load_dwordx4 v201, %[sw], s96 offen offset:1024 lds\n\t" \
  "v_mfma_f32_16x16x32_bf16 v[72:75], a[168:171], a[8:11], v[72:75]\n\t" \
  "ds_read_b64_tr_b16 a[150:151], v197 offset:33792\n\t" \
  "v_fma_f32 v181, v149, v171, v181\n\t" \
  "v_mfma_f32_16x16x32_bf16 v[72:75], a[104:107], a[24:27], v[72:75]\n\t" \
  "ds_read_b64_tr_b16 a[152:153], v198 offset:32768\n\t" \
  "v_lshlrev_b32 v128, 16, v150\n\t" \
  "v_mfma_f32_16x16x32_bf16 v[104:107], a[104:107], a[28:31], v[104:107]\n\t" \
  "ds_read_b64_tr_b16 a[154:155], v198 offset:33792\n\t" \
  "v_and_b32 v150, 0xffff0000, v150\n\t" \
  "v_mfma_f32_16x16x32_bf16 v[104:107], a[168:171], a[12:15], v[104:107]\n\t" \
  "buffer_load_dwordx4 v202, %[sw], s96 offen offset:2048 lds\n\t" \
  "v_mfma_f32_16x16x32_bf16 v[12:15], a[172:175], a[0:3], v[12:15]\n\t" \
  "ds_read_b64_tr_b16 a[156:157], v199 offset:32768\n\t" \
  "v_fma_f32 v178, v128, v171, v178\n\t" \
  "v_mfma_f32_16x16x32_bf16 v[12:15], a[108:111], a[16:19], v[12:15]\n\t" \
  "ds_read_b64_tr_b16 a[158:159], v199 offset:33792\n\t" \
  "v_fma_f32 v182, v150, v171, v182\n\t" \
  "v_mfma_f32_16x16x32_bf16 v[44:47], a[108:111], a[20:23], v[44:47]\n\t" \
  "ds_read_b64_tr_b16 a[64:65], v192 offset:40960\n\t" \
  "v_lshlrev_b32 v128, 16, v151\n\t" \
  "v_mfma_f32_16x16x32_bf16 v[44:47], a[172:175], a[4:7], v[44:47]\n\t" \
  "buffer_load_dwordx4 v203, %[sw], s96 offen offset:3072 lds\n\t" \
  "v_mfma_f32_16x16x32_bf16 v[76:79], a[172:175], a[8:11], v[76:79]\n\t" \
  "ds_read_b64_tr_b16 a[66:67], v192 offset:41984\n\t" \
  "v_and_b32 v151, 0xffff0000, v151\n\t" \
  "v_mfma_f32_16x16x32_bf16 v[76:79], a[108:111], a[24:27], v[76:79]\n\t" \
  "s_add_u32 s97, s97, %[dk]\n\t" \
  "v_fma_f32 v179, v128, v171, v179\n\t" \
  "v_fma_f32 v183, v151, v171, v183\n\t" \
  "v_mfma_f32_16x16x32_bf16 v[108:111], a[108:111], a[28:31], v[108:111]\n\t" \
  "ds_read_b64_tr_b16 a[68:69], v193 offset:40960\n\t" \
  "v_mfma_f32_16x16x32_bf16 v[108:111], a[172:175], a[12:15], v[108:111]\n\t" \
  "ds_read_b64_tr_b16 a[70:71], v193 offset:41984\n\t" \
  "v_mfma_f32_16x16x32_bf16 v[16:19], a[176:179], a[0:3], v[16:19]\n\t" \
  "buffer_load_dwordx4 a[240:243], v248, %[ws], s97 offen sc1\n\t" \
  "v_mfma_f32_16x16x32_bf16 v[16:19], a[112:115], a[16:19], v[16:19]\n\t" \
  "ds_read_b64_tr_b16 a[72:73], v194 offset:40960\n\t" \
  "v_mfma_f32_16x16x32_bf16 v[48:51], a[112:115], a[20:23], v[48:51]\n\t" \
  "ds_read_b64_tr_b16 a[74:75], v194 offset:41984\n\t" \
  "v_mfma_f32_16x16x32_bf16 v[48:51], a[176:179], a[4:7], v[48:51]\n\t" \
  "ds_read_b64_tr_b16 a[76:77], v195 offset:40960\n\t" \
  "v_mfma_f32_16x16x32_bf16 v[80:83], a[176:179], a[8:11], v[80:83]\n\t" \
  "buffer_load_dwordx4 a[244:247], v249, %[ws], s97 offen sc1\n\t" \
  "v_mfma_f32_16x16x32_bf16 v[80:83], a[112:115], a[24:27], v[80:83]\n\t" \
  "ds_read_b64_tr_b16 a[78:79], v195 offset:41984\n\t" \
  "v_mfma_f32_16x16x32_bf16 v[112:115], a[112:115], a[28:31], v[112:115]\n\t" \
  "ds_read_b64_tr_b16 a[80:81], v196 offset:40960\n\t" \
  "v_mfma_f32_16x16x32_bf16 v[112:115], a[176:179], a[12:15], v[112:115]\n\t" \
  "ds_read_b64_tr_b16 a[82:83], v196 offset:41984\n\t" \
  "v_mfma_f32_16x16x32_bf16 v[20:23], a[180:183], a[0:3], v[20:23]\n\t" \
  "ds_read_b64_tr_b16 a[84:85], v197 offset:40960\n\t" \
  "v_mfma_f32_16x16x32_bf16 v[20:23], a[116:119], a[16:19], v[20:23]\n\t" \
  "buffer_load_dwordx4 a[248:251], v250, %[ws], s97 offen sc1\n\t" \
  "v_mfma_f32_16x16x32_bf16 v[52:55], a[116:119], a[20:23], v[52:55]\n\t" \
  "ds_read_b64_tr_b16 a[86:87], v197 offset:41984\n\t" \
  "v_mfma_f32_16x16x32_bf16 v[52:55], a[180:183], a[4:7], v[52:55]\n\t" \
  ".Lhs3:\n\ts_cbranch_execz .Lhs3n\n\t" \
  "ds_read_b64_tr_b16 a[88:89], v198 offset:40960\n\t" \
  "v_mfma_f32_16x16x32_bf16 v[84:87], a[180:183], a[8:11], v[84:87]\n\t" \
  "ds_read_b64_tr_b16 a[90:91], v198 offset:41984\n\t" \
  "v_mfma_f32_16x16x32_bf16 v[84:87], a[116:119], a[24:27], v[84:87]\n\t" \
  "ds_read_b64_tr_b16 a[92:93], v199 offset:40960\n\t" \
  "v_mfma_f32_16x16x32_bf16 v[116:119], a[116:119], a[28:31], v[116:119]\n\t" \
  "buffer_load_dwordx4 a[252:255], v251, %[ws], s97 offen sc1\n\t" \
  "v_mfma_f32_16x16x32_bf16 v[116:119], a[180:183], a[12:15], v[116:119]\n\t" \
  "ds_read_b64_tr_b16 a[94:95], v199 offset:41984\n\t" \
  "s_mov_b32 s99, 49152\n\t" \
  "v_mfma_f32_16x16x32_bf16 v[24:27], a[184:187], a[0:3], v[24:27]\n\t" \
  "v_mfma_f32_16x16x32_bf16 v[24:27], a[120:123], a[16:19], v[24:27]\n\t" \
  "v_mfma_f32_16x16x32_bf16 v[56:59], a[120:123], a[20:23], v[56:59]\n\t" \
  "v_mfma_f32_16x16x32_bf16 v[56:59], a[184:187], a[4:7], v[56:59]\n\t" \
  "buffer_load_dwordx4 v[152:155], v209, %[sM], s99 offen\n\t" \
  "v_mfma_f32_16x16x32_bf16 v[88:91], a[184:187], a[8:11], v[88:91]\n\t" \
  "s_mov_b32 s99, 61440\n\t" \
  "v_mfma_f32_16x16x32_bf16 v[88:91], a[120:123], a[24:27], v[88:91]\n\t" \
  "v_mfma_f32_16x16x32_bf16 v[120:123], a[120:123], a[28:31], v[120:123]\n\t" \
  "v_mfma_f32_16x16x32_bf16 v[120:123], a[184:187], a[12:15], v[120:123]\n\t" \
  "v_mfma_f32_16x16x32_bf16 v[28:31], a[188:191], a[0:3], v[28:31]\n\t" \
  "buffer_load_dwordx4 v[156:159], v209, %[sM], s99 offen\n\t" \
  "v_mfma_f32_16x16x32_bf16 v[28:31], a[124:127], a[16:19], v[28:31]\n\t" \
  "v_mfma_f32_16x16x32_bf16 v[60:63], a[124:127], a[20:23], v[60:63]\n\t" \
  "v_mfma_f32_16x16x32_bf16 v[60:63], a[188:191], a[4:7], v[60:63]\n\t" \
  "v_mfma_f32_16x16x32_bf16 v[92:95], a[188:191], a[8:11], v[92:95]\n\t" \
  "v_mfma_f32_16x16x32_bf16 v[92:95], a[124:127], a[24:27], v[92:95]\n\t" \
  "s_add_u32 s97, %[rot19], 6291456\n\t" \
  "s_bfe_u32 s97, s97, 0x140003\n\t" \
  "s_add_u32 s97, s97, %[sox]\n\t" \
  "v_mfma_f32_16x16x32_bf16 v[124:127], a[124:127], a[28:31], v[124:127]\n\t" \
  "s_waitcnt lgkmcnt(0)\n\t" \
  "v_mfma_f32_16x16x32_bf16 v[124:127], a[188:191], a[12:15], v[124:127]\n\t" \
  "s_barrier\n\t" \
  "v_mfma_f32_16x16x32_bf16 v[0:3], a[128:131], a[32:35], v[0:3]\n\t" \
  "buffer_load_dwordx4 a[0:3], v248, %[ws], s97 offen sc1\n\t" \
  "v_mfma_f32_16x16x32_bf16 v[0:3], a[64:67], a[48:51], v[0:3]\n\t" \
  "ds_read_b64_tr_b16 a[160:161], v184 offset:0\n\t" \
  "v_mfma_f32_16x16x32_bf16 v[32:35], a[64:67], a[52:55], v[32:35]\n\t" \
  "ds_read_b64_tr_b16 a[162:163], v184 offset:1024\n\t" \
  "v_mfma_f32_16x16x32_bf16 v[32:35], a[128:131], a[36:39], v[32:35]\n\t" \
  "buffer_load_dwordx4 a[4:7], v249, %[ws], s97 offen sc1\n\t" \
  "v_mfma_f32_16x16x32_bf16 v[64:67], a[128:131], a[40:43], v[64:67]\n\t" \
  "ds_read_b64_tr_b16 a[164:165], v185 offset:0\n\t" \
  "v_mfma_f32_16x16x32_bf16 v[64:67], a[64:67], a[56:59], v[64:67]\n\t" \
  "ds_read_b64_tr_b16 a[166:167], v185 offset:1024\n\t" \
  "v_mfma_f32_16x16x32_bf16 v[96:99], a[64:67], a[60:63], v[96:99]\n\t" \
  "buffer_load_dwordx4 a[8:11], v250, %[ws], s97 offen sc1\n\t" \
  "v_mfma_f32_16x16x32_bf16 v[96:99], a[128:131], a[44:47], v[96:99]\n\t" \
  "ds_read_b64_tr_b16 a[168:169], v186 offset:0\n\t" \
  "v_mfma_f32_16x16x32_bf16 v[4:7], a[132:135], a[32:35], v[4:7]\n\t" \
  "ds_read_b64_tr_b16 a[170:171], v186 offset:1024\n\t" \
  "v_mfma_f32_16x16x32_bf16 v[4:7], a[68:71], a[48:51], v[4:7]\n\t" \
  "ds_read_b64_tr_b16 a[172:173], v187 offset:0\n\t" \
  "v_mfma_f32_16x16x32_bf16 v[36:39], a[68:71], a[52:55], v[36:39]\n\t" \
  "buffer_load_dwordx4 a[12:15], v251, %[ws], s97 offen sc1\n\t" \
  "v_mfma_f32_16x16x32_bf16 v[36:39], a[132:135], a[36:39], v[36:39]\n\t" \
  "s_add_u32 s96, %[rot19], 7340032\n\t" \
  "ds_read_b64_tr_b16 a[174:175], v187 offset:1024\n\t" \
  "s_and_b32 s96, s96, 0x780000\n\t" \
  "v_mfma_f32_16x16x32_bf16 v[68:71], a[132:135], a[40:43], v[68:71]\n\t" \
  "ds_read_b64_tr_b16 a[176:177], v188 offset:0\n\t" \
  "s_add_u32 m0, %[mw], 81920\n\t" \
  "v_mfma_f32_16x16x32_bf16 v[68:71], a[68:71], a[56:59], v[68:71]\n\t" \
  "ds_read_b64_tr_b16 a[178:179], v188 offset:1024\n\t" \
  "v_mfma_f32_16x16x32_bf16 v[100:103], a[68:71], a[60:63], v[100:103]\n\t" \
  "buffer_load_dwordx4 v200, %[sw], s96 offen offset:0 lds\n\t" \
  "v_mfma_f32_16x16x32_bf16 v[100:103], a[132:135], a[44:47], v[100:103]\n\t" \
  "ds_read_b64_tr_b16 a[180:181], v189 offset:0\n\t" \
  "v_mfma_f32_16x16x32_bf16 v[8:11], a[136:139], a[32:35], v[8:11]\n\t" \
  "ds_read_b64_tr_b16 a[182:183], v189 offset:1024\n\t" \
  "v_mfma_f32_16x16x32_bf16 v[8:11], a[72:75], a[48:51], v[8:11]\n\t" \
  "buffer_load_dwordx4 v201, %[sw], s96 offen offset:1024 lds\n\t" \
  "v_mfma_f32_16x16x32_bf16 v[40:43], a[72:75], a[52:55], v[40:43]\n\t" \
  "ds_read_b64_tr_b16 a[184:185], v190 offset:0\n\t" \
  "v_mfma_f32_16x16x32_bf16 v[40:43], a[136:139], a[36:39], v[40:43]\n\t" \
  "ds_read_b64_tr_b16 a[186:187], v190 offset:1024\n\t" \
  "v_mfma_f32_16x16x32_bf16 v[72:75], a[136:139], a[40:43], v[72:75]\n\t" \
  "ds_read_b64_tr_b16 a[188:189], v191 offset:0\n\t" \
  "v_mfma_f32_16x16x32_bf16 v[72:75], a[72:75], a[56:59], v[72:75]\n\t" \
  "buffer_load_dwordx4 v202, %[sw], s96 offen offset:2048 lds\n\t" \
  "v_mfma_f32_16x16x32_bf16 v[104:107], a[72:75], a[60:63], v[104:107]\n\t" \
  "ds_read_b64_tr_b16 a[190:191], v191 offset:1024\n\t" \
  "v_mfma_f32_16x16x32_bf16 v[104:107], a[136:139], a[44:47], v[104:107]\n\t" \
  "ds_read_b64_tr_b16 a[96:97], v184 offset:8192\n\t" \
  "v_mfma_f32_16x16x32_bf16 v[12:15], a[140:143], a[32:35], v[12:15]\n\t" \
  "ds_read_b64_tr_b16 a[98:99], v184 offset:9216\n\t" \
  "v_mfma_f32_16x16x32_bf16 v[12:15], a[76:79], a[48:51], v[12:15]\n\t" \
  "buffer_load_dwordx4 v203, %[sw], s96 offen offset:3072 lds\n\t" \
  "v_mfma_f32_16x16x32_bf16 v[44:47], a[76:79], a[52:55], v[44:47]\n\t" \
  "ds_read_b64_tr_b16 a[100:101], v185 offset:8192\n\t" \
  "v_mfma_f32_16x16x32_bf16 v[44:47], a[140:143], a[36:39], v[44:47]\n\t" \
  "ds_read_b64_tr_b16 a[102:103], v185 offset:9216\n\t" \
  "v_mfma_f32_16x16x32_bf16 v[76:79], a[140:143], a[40:43], v[76:79]\n\t" \
  "ds_read_b64_tr_b16 a[104:105], v186 offset:8192\n\t" \
  "v_mfma_f32_16x16x32_bf16 v[76:79], a[76:79], a[56:59], v[76:79]\n\t" \
  "ds_read_b64_tr_b16 a[106:107], v186 offset:9216\n\t" \
  "s_add_u32 s97, s97, %[dk]\n\t" \
  "v_mfma_f32_16x16x32_bf16 v[108:111], a[76:79], a[60:63], v[108:111]\n\t" \
  "ds_read_b64_tr_b16 a[108:109], v187 offset:8192\n\t" \
  "v_mfma_f32_16x16x32_bf16 v[108:111], a[140:143], a[44:47], v[108:111]\n\t" \
  "buffer_load_dwordx4 a[16:19], v248, %[ws], s97 offen sc1\n\t" \
  "v_mfma_f32_16x16x32_bf16 v[16:19], a[144:147], a[32:35], v[16:19]\n\t" \
  "ds_read_b64_tr_b16 a[110:111], v187 offset:9216\n\t" \
  "v_mfma_f32_16x16x32_bf16 v[16:19], a[80:83], a[48:51], v[16:19]\n\t" \
  "ds_read_b64_tr_b16 a[112:113], v188 offset:8192\n\t" \
  "v_mfma_f32_16x16x32_bf16 v[48:51], a[80:83], a[52:55], v[48:51]\n\t" \
  "ds_read_b64_tr_b16 a[114:115], v188 offset:9216\n\t" \
  "v_mfma_f32_16x16x32_bf16 v[48:51], a[144:147], a[36:39], v[48:51]\n\t" \
  "ds_read_b64_tr_b16 a[116:117], v189 offset:8192\n\t" \
  "v_mfma_f32_16x16x32_bf16 v[80:83], a[144:147], a[40:43], v[80:83]\n\t" \
  "ds_read_b64_tr_b16 a[118:119], v189 offset:9216\n\t" \
  "v_mfma_f32_16x16x32_bf16 v[80:83], a[80:83], a[56:59], v[80:83]\n\t" \
  "ds_read_b64_tr_b16 a[120:121], v190 offset:8192\n\t" \
  "v_mfma_f32_16x16x32_bf16 v[112:115], a[80:83], a[60:63], v[112:115]\n\t" \
  "buffer_load_dwordx4 a[20:23], v249, %[ws], s97 offen sc1\n\t" \
  "v_mfma_f32_16x16x32_bf16 v[112:115], a[144:147], a[44:47], v[112:115]\n\t" \
  "ds_read_b64_tr_b16 a[122:123], v190 offset:9216\n\t" \
  "v_mfma_f32_16x16x32_bf16 v[20:23], a[148:151], a[32:35], v[20:23]\n\t" \
  "ds_read_b64_tr_b16 a[124:125], v191 offset:8192\n\t" \
  "v_mfma_f32_16x16x32_bf16 v[20:23], a[84:87], a[48:51], v[20:23]\n\t" \
  "ds_read_b64_tr_b16 a[126:127], v191 offset:9216\n\t" \
  "v_mfma_f32_16x16x32_bf16 v[52:55], a[84:87], a[52:55], v[52:55]\n\t" \
  "v_mfma_f32_16x16x32_bf16 v[52:55], a[148:151], a[36:39], v[52:55]\n\t" \
  "v_mfma_f32_16x16x32_bf16 v[84:87], a[148:151], a[40:43], v[84:87]\n\t" \
  "v_mfma_f32_16x16x32_bf16 v[84:87], a[84:87], a[56:59], v[84:87]\n\t" \
  "v_mfma_f32_16x16x32_bf16 v[116:119], a[84:87], a[60:63], v[116:119]\n\t" \
  "buffer_load_dwordx4 a[24:27], v250, %[ws], s97 offen sc1\n\t" \
  "v_mfma_f32_16x16x32_bf16 v[116:119], a[148:151], a[44:47], v[116:119]\n\t" \
  "v_mfma_f32_16x16x32_bf16 v[24:27], a[152:155], a[32:35], v[24:27]\n\t" \
  "v_mfma_f32_16x16x32_bf16 v[24:27], a[88:91], a[48:51], v[24:27]\n\t" \
  "v_mfma_f32_16x16x32_bf16 v[56:59], a[88:91], a[52:55], v[56:59]\n\t" \
  "v_mfma_f32_16x16x32_bf16 v[56:59], a[152:155], a[36:39], v[56:59]\n\t" \
  "v_mfma_f32_16x16x32_bf16 v[88:91], a[152:155], a[40:43], v[88:91]\n\t" \
  "v_mfma_f32_16x16x32_bf16 v[88:91], a[88:91], a[56:59], v[88:91]\n\t" \
  "buffer_load_dwordx4 a[28:31], v251, %[ws], s97 offen sc1\n\t" \
  "v_mfma_f32_16x16x32_bf16 v[120:123], a[88:91], a[60:63], v[120:123]\n\t" \
  "v_mfma_f32_16x16x32_bf16 v[120:123], a[152:155], a[44:47], v[120:123]\n\t" \
  "v_mfma_f32_16x16x32_bf16 v[28:31], a[156:159], a[32:35], v[28:31]\n\t" \
  "v_mfma_f32_16x16x32_bf16 v[28:31], a[92:95], a[48:51], v[28:31]\n\t" \
  "v_mfma_f32_16x16x32_bf16 v[60:63], a[92:95], a[52:55], v[60:63]\n\t" \
  "s_waitcnt vmcnt(13)\n\t" \
  "v_lshlrev_b32 v128, 16, v152\n\t" \
  "v_and_b32 v152, 0xffff0000, v152\n\t" \
  "v_mfma_f32_16x16x32_bf16 v[60:63], a[156:159], a[36:39], v[60:63]\n\t" \
  "v_fma_f32 v176, v128, v172, v176\n\t" \
  "v_fma_f32 v180, v152, v172, v180\n\t" \
  "v_mfma_f32_16x16x32_bf16 v[92:95], a[156:159], a[40:43], v[92:95]\n\t" \
  "v_lshlrev_b32 v128, 16, v153\n\t" \
  "v_and_b32 v153, 0xffff0000, v153\n\t" \
  "v_mfma_f32_16x16x32_bf16 v[92:95], a[92:95], a[56:59], v[92:95]\n\t" \
  "s_add_u32 s97, %[rot19], 6815744\n\t" \
  "v_fma_f32 v177, v128, v172, v177\n\t" \
  "v_fma_f32 v181, v153, v172, v181\n\t" \
  "v_mfma_f32_16x16x32_bf16 v[124:127], a[92:95], a[60:63], v[124:127]\n\t" \
  "s_waitcnt lgkmcnt(0)\n\t" \
  "s_bfe_u32 s97, s97, 0x140003\n\t" \
  "s_add_u32 s97, s97, %[sox]\n\t" \
  "v_mfma_f32_16x16x32_bf16 v[124:127], a[156:159], a[44:47], v[124:127]\n\t" \
  "s_barrier\n\t" \
  "v_mfma_f32_16x16x32_bf16 v[0:3], a[160:163], a[192:195], v[0:3]\n\t" \
  "buffer_load_dwordx4 a[32:35], v248, %[ws], s97 offen sc1\n\t" \
  "v_mfma_f32_16x16x32_bf16 v[0:3], a[96:99], a[208:211], v[0:3]\n\t" \
  "ds_read_b64_tr_b16 a[128:129], v184 offset:16384\n\t" \
  "v_lshlrev_b32 v128, 16, v154\n\t" \
  "v_mfma_f32_16x16x32_bf16 v[32:35], a[96:99], a[212:215], v[32:35]\n\t" \
  "ds_read_b64_tr_b16 a[130:131], v184 offset:17408\n\t" \
  "v_and_b32 v154, 0xffff0000, v154\n\t" \
  "v_mfma_f32_16x16x32_bf16 v[32:35], a[160:163], a[196:199], v[32:35]\n\t" \
  "v_fma_f32 v178, v128, v172, v178\n\t" \
  "v_fma_f32 v182, v154, v172, v182\n\t" \
  "v_mfma_f32_16x16x32_bf16 v[64:67], a[160:163], a[200:203], v[64:67]\n\t" \
  "buffer_load_dwordx4 a[36:39], v249, %[ws], s97 offen sc1\n\t" \
  "v_mfma_f32_16x16x32_bf16 v[64:67], a[96:99], a[216:219], v[64:67]\n\t" \
  "ds_read_b64_tr_b16 a[132:133], v185 offset:16384\n\t" \
  "v_lshlrev_b32 v128, 16, v155\n\t" \
  "v_mfma_f32_16x16x32_bf16 v[96:99], a[96:99], a[220:223], v[96:99]\n\t" \
  "ds_read_b64_tr_b16 a[134:135], v185 offset:17408\n\t" \
  "v_and_b32 v155, 0xffff0000, v155\n\t" \
  "v_mfma_f32_16x16x32_bf16 v[96:99], a[160:163], a[204:207], v[96:99]\n\t" \
  "ds_read_b64_tr_b16 a[136:137], v186 offset:16384\n\t" \
  "v_fma_f32 v179, v128, v172, v179\n\t" \
  "v_mfma_f32_16x16x32_bf16 v[4:7], a[164:167], a[192:195], v[4:7]\n\t" \
  "buffer_load_dwordx4 a[40:43], v250, %[ws], s97 offen sc1\n\t" \
  "v_mfma_f32_16x16x32_bf16 v[4:7], a[100:103], a[208:211], v[4:7]\n\t" \
  "ds_read_b64_tr_b16 a[138:139], v186 offset:17408\n\t" \
  "v_fma_f32 v183, v155, v172, v183\n\t" \
  "v_mfma_f32_16x16x32_bf16 v[36:39], a[100:103], a[212:215], v[36:39]\n\t" \
  "s_waitcnt vmcnt(15)\n\t" \
  "v_lshlrev_b32 v128, 16, v156\n\t" \
  "v_and_b32 v156, 0xffff0000, v156\n\t" \
  "v_mfma_f32_16x16x32_bf16 v[36:39], a[164:167], a[196:199], v[36:39]\n\t" \
  "ds_read_b64_tr_b16 a[140:141], v187 offset:16384\n\t" \
  "v_fma_f32 v176, v128, v173, v176\n\t" \
  "v_mfma_f32_16x16x32_bf16 v[68:71], a[164:167], a[200:203], v[68:71]\n\t" \
  "buffer_load_dwordx4 a[44:47], v251, %[ws], s97 offen sc1\n\t" \
  "v_mfma_f32_16x16x32_bf16 v[68:71], a[100:103], a[216:219], v[68:71]\n\t" \
  "s_add_u32 s96, %[rot19], 7864320\n\t" \
  "s_and_b32 s96, s96, 0x780000\n\t" \
  "s_add_u32 m0, %[mw], 98304\n\t" \
  "v_mfma_f32_16x16x32_bf16 v[100:103], a[100:103], a[220:223], v[100:103]\n\t" \
  "ds_read_b64_tr_b16 a[142:143], v187 offset:17408\n\t" \
  "v_fma_f32 v180, v156, v173, v180\n\t" \
  "v_mfma_f32_16x16x32_bf16 v[100:103], a[164:167], a[204:207], v[100:103]\n\t" \
  "buffer_load_dwordx4 v200, %[sw], s96 offen offset:0 lds\n\t" \
  "v_mfma_f32_16x16x32_bf16 v[8:11], a[168:171], a[192:195], v[8:11]\n\t" \
  "ds_read_b64_tr_b16 a[144:145], v188 offset:16384\n\t" \
  "v_lshlrev_b32 v128, 16, v157\n\t" \
  "v_mfma_f32_16x16x32_bf16 v[8:11], a[104:107], a[208:211], v[8:11]\n\t" \
  "ds_read_b64_tr_b16 a[146:147], v188 offset:17408\n\t" \
  "v_and_b32 v157, 0xffff0000, v157\n\t" \
  "v_mfma_f32_16x16x32_bf16 v[40:43], a[104:107], a[212:215], v[40:43]\n\t" \
  "ds_read_b64_tr_b16 a[148:149], v189 offset:16384\n\t" \
  "v_fma_f32 v177, v128, v173, v177\n\t" \
  "v_mfma_f32_16x16x32_bf16 v[40:43], a[168:171], a[196:199], v[40:43]\n\t" \
  "buffer_load_dwordx4 v201, %[sw], s96 offen offset:1024 lds\n\t" \
  "v_mfma_f32_16x16x32_bf16 v[72:75], a[168:171], a[200:203], v[72:75]\n\t" \
  "ds_read_b64_tr_b16 a[150:151], v189 offset:17408\n\t" \
  "v_fma_f32 v181, v157, v173, v181\n\t" \
  "v_mfma_f32_16x16x32_bf16 v[72:75], a[104:107], a[216:219], v[72:75]\n\t" \
  "ds_read_b64_tr_b16 a[152:153], v190 offset:16384\n\t" \
  "v_lshlrev_b32 v128, 16, v158\n\t" \
  "v_mfma_f32_16x16x32_bf16 v[104:107], a[104:107], a[220:223], v[104:107]\n\t" \
  "ds_read_b64_tr_b16 a[154:155], v190 offset:17408\n\t" \
  "v_and_b32 v158, 0xffff0000, v158\n\t" \
  "v_mfma_f32_16x16x32_bf16 v[104:107], a[168:171], a[204:207], v[104:107]\n\t" \
  "buffer_load_dwordx4 v202, %[sw], s96 offen offset:2048 lds\n\t" \
  "v_mfma_f32_16x16x32_bf16 v[12:15], a[172:175], a[192:195], v[12:15]\n\t" \
  "ds_read_b64_tr_b16 a[156:157], v191 offset:16384\n\t" \
  "v_fma_f32 v178, v128, v173, v178\n\t" \
  "v_mfma_f32_16x16x32_bf16 v[12:15], a[108:111], a[208:211], v[12:15]\n\t" \
  "ds_read_b64_tr_b16 a[158:159], v191 offset:17408\n\t" \
  "v_fma_f32 v182, v158, v173, v182\n\t" \
  "v_mfma_f32_16x16x32_bf16 v[44:47], a[108:111], a[212:215], v[44:47]\n\t" \
  "ds_read_b64_tr_b16 a[64:65], v184 offset:24576\n\t" \
  "v_lshlrev_b32 v128, 16, v159\n\t" \
  "v_mfma_f32_16x16x32_bf16 v[44:47], a[172:175], a[196:199], v[44:47]\n\t" \
  "buffer_load_dwordx4 v203, %[sw], s96 offen offset:3072 lds\n\t" \
  "v_mfma_f32_16x16x32_bf16 v[76:79], a[172:175], a[200:203], v[76:79]\n\t" \
  "ds_read_b64_tr_b16 a[66:67], v184 offset:25600\n\t" \
  "v_and_b32 v159, 0xffff0000, v159\n\t" \
  "v_mfma_f32_16x16x32_bf16 v[76:79], a[108:111], a[216:219], v[76:79]\n\t" \
  "s_add_u32 s97, s97, %[dk]\n\t" \
  "v_fma_f32 v179, v128, v173, v179\n\t" \
  "v_fma_f32 v183, v159, v173, v183\n\t" \
  "v_mfma_f32_16x16x32_bf16 v[108:111], a[108:111], a[220:223], v[108:111]\n\t" \
  "ds_read_b64_tr_b16 a[68:69], v185 offset:24576\n\t" \
  "v_mfma_f32_16x16x32_bf16 v[108:111], a[172:175], a[204:207], v[108:111]\n\t" \
  "ds_read_b64_tr_b16 a[70:71], v185 offset:25600\n\t" \
  "v_mfma_f32_16x16x32_bf16 v[16:19], a[176:179], a[192:195], v[16:19]\n\t" \
  "buffer_load_dwordx4 a[48:51], v248, %[ws], s97 offen sc1\n\t" \
  "v_mfma_f32_16x16x32_bf16 v[16:19], a[112:115], a[208:211], v[16:19]\n\t" \
  "ds_read_b64_tr_b16 a[72:73], v186 offset:24576\n\t" \
  "v_mfma_f32_16x16x32_bf16 v[48:51], a[112:115], a[212:215], v[48:51]\n\t" \
  "ds_read_b64_tr_b16 a[74:75], v186 offset:25600\n\t" \
  "v_mfma_f32_16x16x32_bf16 v[48:51], a[176:179], a[196:199], v[48:51]\n\t" \
  "ds_read_b64_tr_b16 a[76:77], v187 offset:24576\n\t" \
  "v_mfma_f32_16x16x32_bf16 v[80:83], a[176:179], a[200:203], v[80:83]\n\t" \
  "buffer_load_dwordx4 a[52:55], v249, %[ws], s97 offen sc1\n\t" \
  "v_mfma_f32_16x16x32_bf16 v[80:83], a[112:115], a[216:219], v[80:83]\n\t" \
  "ds_read_b64_tr_b16 a[78:79], v187 offset:25600\n\t" \
  "v_mfma_f32_16x16x32_bf16 v[112:115], a[112:115], a[220:223], v[112:115]\n\t" \
  "ds_read_b64_tr_b16 a[80:81], v188 offset:24576\n\t" \
  "v_mfma_f32_16x16x32_bf16 v[112:115], a[176:179], a[204:207], v[112:115]\n\t" \
  "ds_read_b64_tr_b16 a[82:83], v188 offset:25600\n\t" \
  "v_mfma_f32_16x16x32_bf16 v[20:23], a[180:183], a[192:195], v[20:23]\n\t" \
  "ds_read_b64_tr_b16 a[84:85], v189 offset:24576\n\t" \
  "v_mfma_f32_16x16x32_bf16 v[20:23], a[116:119], a[208:211], v[20:23]\n\t" \
  "buffer_load_dwordx4 a[56:59], v250, %[ws], s97 offen sc1\n\t" \
  "v_mfma_f32_16x16x32_bf16 v[52:55], a[116:119], a[212:215], v[52:55]\n\t" \
  "ds_read_b64_tr_b16 a[86:87], v189 offset:25600\n\t" \
  "v_mfma_f32_16x16x32_bf16 v[52:55], a[180:183], a[196:199], v[52:55]\n\t" \
  "ds_read_b64_tr_b16 a[88:89], v190 offset:24576\n\t" \
  "v_mfma_f32_16x16x32_bf16 v[84:87], a[180:183], a[200:203], v[84:87]\n\t" \
  "ds_read_b64_tr_b16 a[90:91], v190 offset:25600\n\t" \
  "v_mfma_f32_16x16x32_bf16 v[84:87], a[116:119], a[216:219], v[84:87]\n\t" \
  "ds_read_b64_tr_b16 a[92:93], v191 offset:24576\n\t" \
  "v_mfma_f32_16x16x32_bf16 v[116:119], a[116:119], a[220:223], v[116:119]\n\t" \
  "buffer_load_dwordx4 a[60:63], v251, %[ws], s97 offen sc1\n\t" \
  "v_mfma_f32_16x16x32_bf16 v[116:119], a[180:183], a[204:207], v[116:119]\n\t" \
  "ds_read_b64_tr_b16 a[94:95], v191 offset:25600\n\t" \
  "s_mov_b32 s99, 73728\n\t" \
  "v_mfma_f32_16x16x32_bf16 v[24:27], a[184:187], a[192:195], v[24:27]\n\t" \
  "v_mfma_f32_16x16x32_bf16 v[24:27], a[120:123], a[208:211], v[24:27]\n\t" \
  "v_mfma_f32_16x16x32_bf16 v[56:59], a[120:123], a[212:215], v[56:59]\n\t" \
  "v_mfma_f32_16x16x32_bf16 v[56:59], a[184:187], a[196:199], v[56:59]\n\t" \
  "buffer_load_dwordx4 v[160:163], v209, %[sM], s99 offen\n\t" \
  "v_mfma_f32_16x16x32_bf16 v[88:91], a[184:187], a[200:203], v[88:91]\n\t" \
  "s_mov_b32 s99, 86016\n\t" \
  "v_mfma_f32_16x16x32_bf16 v[88:91], a[120:123], a[216:219], v[88:91]\n\t" \
  "v_mfma_f32_16x16x32_bf16 v[120:123], a[120:123], a[220:223], v[120:123]\n\t" \
  "v_mfma_f32_16x16x32_bf16 v[120:123], a[184:187], a[204:207], v[120:123]\n\t" \
  "v_mfma_f32_16x16x32_bf16 v[28:31], a[188:191], a[192:195], v[28:31]\n\t" \
  "buffer_load_dwordx4 v[164:167], v209, %[sM], s99 offen\n\t" \
  "v_mfma_f32_16x16x32_bf16 v[28:31], a[124:127], a[208:211], v[28:31]\n\t" \
  "v_mfma_f32_16x16x32_bf16 v[60:63], a[124:127], a[212:215], v[60:63]\n\t" \
  "v_mfma_f32_16x16x32_bf16 v[60:63], a[188:191], a[196:199], v[60:63]\n\t" \
  "v_mfma_f32_16x16x32_bf16 v[92:95], a[188:191], a[200:203], v[92:95]\n\t" \
  "v_mfma_f32_16x16x32_bf16 v[92:95], a[124:127], a[216:219], v[92:95]\n\t" \
  "s_add_u32 s97, %[rot19], 7340032\n\t" \
  "s_bfe_u32 s97, s97, 0x140003\n\t" \
  "s_add_u32 s97, s97, %[sox]\n\t" \
  "v_mfma_f32_16x16x32_bf16 v[124:127], a[124:127], a[220:223], v[124:127]\n\t" \
  "s_waitcnt lgkmcnt(0)\n\t" \
  "v_mfma_f32_16x16x32_bf16 v[124:127], a[188:191], a[204:207], v[124:127]\n\t" \
  "s_barrier\n\t" \
  "v_mfma_f32_16x16x32_bf16 v[0:3], a[128:131], a[224:227], v[0:3]\n\t" \
  "buffer_load_dwordx4 a[192:195], v248, %[ws], s97 offen sc1\n\t" \
  "v_mfma_f32_16x16x32_bf16 v[0:3], a[64:67], a[240:243], v[0:3]\n\t" \
  "ds_read_b64_tr_b16 a[160:161], v184 offset:32768\n\t" \
  "v_mfma_f32_16x16x32_bf16 v[32:35], a[64:67], a[244:247], v[32:35]\n\t" \
  "ds_read_b64_tr_b16 a[162:163], v184 offset:33792\n\t" \
  "v_mfma_f32_16x16x32_bf16 v[32:35], a[128:131], a[228:231], v[32:35]\n\t" \
  "ds_read_b64_tr_b16 a[164:165], v185 offset:32768\n\t" \
  "v_mfma_f32_16x16x32_bf16 v[64:67], a[128:131], a[232:235], v[64:67]\n\t" \
  "ds_read_b64_tr_b16 a[166:167], v185 offset:33792\n\t" \
  "v_mfma_f32_16x16x32_bf16 v[64:67], a[64:67], a[248:251], v[64:67]\n\t" \
  "ds_read_b64_tr_b16 a[168:169], v186 offset:32768\n\t" \
  "v_mfma_f32_16x16x32_bf16 v[96:99], a[64:67], a[252:255], v[96:99]\n\t" \
  "buffer_load_dwordx4 a[196:199], v249, %[ws], s97 offen sc1\n\t" \
  "v_mfma_f32_16x16x32_bf16 v[96:99], a[128:131], a[236:239], v[96:99]\n\t" \
  "ds_read_b64_tr_b16 a[170:171], v186 offset:33792\n\t" \
  "v_mfma_f32_16x16x32_bf16 v[4:7], a[132:135], a[224:227], v[4:7]\n\t" \
  "ds_read_b64_tr_b16 a[172:173], v187 offset:32768\n\t" \
  "v_mfma_f32_16x16x32_bf16 v[4:7], a[68:71], a[240:243], v[4:7]\n\t" \
  "ds_read_b64_tr_b16 a[174:175], v187 offset:33792\n\t" \
  "v_mfma_f32_16x16x32_bf16 v[36:39], a[68:71], a[244:247], v[36:39]\n\t" \
  "ds_read_b64_tr_b16 a[176:177], v188 offset:32768\n\t" \
  "v_mfma_f32_16x16x32_bf16 v[36:39], a[132:135], a[228:231], v[36:39]\n\t" \
  "ds_read_b64_tr_b16 a[178:179], v188 offset:33792\n\t" \
  "v_mfma_f32_16x16x32_bf16 v[68:71], a[132:135], a[232:235], v[68:71]\n\t" \
  "ds_read_b64_tr_b16 a[180:181], v189 offset:32768\n\t" \
  "v_mfma_f32_16x16x32_bf16 v[68:71], a[68:71], a[248:251], v[68:71]\n\t" \
  "ds_read_b64_tr_b16 a[182:183], v189 offset:33792\n\t" \
  "v_mfma_f32_16x16x32_bf16 v[100:103], a[68:71], a[252:255], v[100:103]\n\t" \
  "buffer_load_dwordx4 a[200:203], v250, %[ws], s97 offen sc1\n\t" \
  "v_mfma_f32_16x16x32_bf16 v[100:103], a[132:135], a[236:239], v[100:103]\n\t" \
  "ds_read_b64_tr_b16 a[184:185], v190 offset:32768\n\t" \
  "v_mfma_f32_16x16x32_bf16 v[8:11], a[136:139], a[224:227], v[8:11]\n\t" \
  "ds_read_b64_tr_b16 a[186:187], v190 offset:33792\n\t" \
  "v_mfma_f32_16x16x32_bf16 v[8:11], a[72:75], a[240:243], v[8:11]\n\t" \
  "ds_read_b64_tr_b16 a[188:189], v191 offset:32768\n\t" \
  "v_mfma_f32_16x16x32_bf16 v[40:43], a[72:75], a[244:247], v[40:43]\n\t" \
  "ds_read_b64_tr_b16 a[190:191], v191 offset:33792\n\t" \
  "v_mfma_f32_16x16x32_bf16 v[40:43], a[136:139], a[228:231], v[40:43]\n\t" \
  "ds_read_b64_tr_b16 a[96:97], v184 offset:40960\n\t" \
  "v_mfma_f32_16x16x32_bf16 v[72:75], a[136:139], a[232:235], v[72:75]\n\t" \
  "ds_read_b64_tr_b16 a[98:99], v184 offset:41984\n\t" \
  "v_mfma_f32_16x16x32_bf16 v[72:75], a[72:75], a[248:251], v[72:75]\n\t" \
  "buffer_load_dwordx4 a[204:207], v251, %[ws], s97 offen sc1\n\t" \
  "v_mfma_f32_16x16x32_bf16 v[104:107], a[72:75], a[252:255], v[104:107]\n\t" \
  "ds_read_b64_tr_b16 a[100:101], v185 offset:40960\n\t" \
  "v_mfma_f32_16x16x32_bf16 v[104:107], a[136:139], a[236:239], v[104:107]\n\t" \
  "ds_read_b64_tr_b16 a[102:103], v185 offset:41984\n\t" \
  "v_mfma_f32_16x16x32_bf16 v[12:15], a[140:143], a[224:227], v[12:15]\n\t" \
  "ds_read_b64_tr_b16 a[104:105], v186 offset:40960\n\t" \
  "v_mfma_f32_16x16x32_bf16 v[12:15], a[76:79], a[240:243], v[12:15]\n\t" \
  "ds_read_b64_tr_b16 a[106:107], v186 offset:41984\n\t" \
  "v_mfma_f32_16x16x32_bf16 v[44:47], a[76:79], a[244:247], v[44:47]\n\t" \
  "ds_read_b64_tr_b16 a[108:109], v187 offset:40960\n\t" \
  "v_mfma_f32_16x16x32_bf16 v[44:47], a[140:143], a[228:231], v[44:47]\n\t" \
  "v_mfma_f32_16x16x32_bf16 v[76:79], a[140:143], a[232:235], v[76:79]\n\t" \
  "ds_read_b64_tr_b16 a[110:111], v187 offset:41984\n\t" \
  "v_mfma_f32_16x16x32_bf16 v[76:79], a[76:79], a[248:251], v[76:79]\n\t" \
  "ds_read_b64_tr_b16 a[112:113], v188 offset:40960\n\t" \
  "s_add_u32 s97, s97, %[dk]\n\t" \
  "v_mfma_f32_16x16x32_bf16 v[108:111], a[76:79], a[252:255], v[108:111]\n\t" \
  "v_mfma_f32_16x16x32_bf16 v[108:111], a[140:143], a[236:239], v[108:111]\n\t" \
  "buffer_load_dwordx4 a[208:211], v248, %[ws], s97 offen sc1\n\t" \
  "v_mfma_f32_16x16x32_bf16 v[16:19], a[144:147], a[224:227], v[16:19]\n\t" \
  "ds_read_b64_tr_b16 a[114:115], v188 offset:41984\n\t" \
  "v_mfma_f32_16x16x32_bf16 v[16:19], a[80:83], a[240:243], v[16:19]\n\t" \
  "ds_read_b64_tr_b16 a[116:117], v189 offset:40960\n\t" \
  "v_mfma_f32_16x16x32_bf16 v[48:51], a[80:83], a[244:247], v[48:51]\n\t" \
  "ds_read_b64_tr_b16 a[118:119], v189 offset:41984\n\t" \
  "v_mfma_f32_16x16x32_bf16 v[48:51], a[144:147], a[228:231], v[48:51]\n\t" \
  "v_mfma_f32_16x16x32_bf16 v[80:83], a[144:147], a[232:235], v[80:83]\n\t" \
  "ds_read_b64_tr_b16 a[120:121], v190 offset:40960\n\t" \
  "v_mfma_f32_16x16x32_bf16 v[80:83], a[80:83], a[248:251], v[80:83]\n\t" \
  "buffer_load_dwordx4 a[212:215], v249, %[ws], s97 offen sc1\n\t" \
  "v_mfma_f32_16x16x32_bf16 v[112:115], a[80:83], a[252:255], v[112:115]\n\t" \
  "ds_read_b64_tr_b16 a[122:123], v190 offset:41984\n\t" \
  "v_mfma_f32_16x16x32_bf16 v[112:115], a[144:147], a[236:239], v[112:115]\n\t" \
  "ds_read_b64_tr_b16 a[124:125], v191 offset:40960\n\t" \
  "v_mfma_f32_16x16x32_bf16 v[20:23], a[148:151], a[224:227], v[20:23]\n\t" \
  "v_mfma_f32_16x16x32_bf16 v[20:23], a[84:87], a[240:243], v[20:23]\n\t" \
  "ds_read_b64_tr_b16 a[126:127], v191 offset:41984\n\t" \
  "v_mfma_f32_16x16x32_bf16 v[52:55], a[84:87], a[244:247], v[52:55]\n\t" \
  "v_mfma_f32_16x16x32_bf16 v[52:55], a[148:151], a[228:231], v[52:55]\n\t" \
  "buffer_load_dwordx4 a[216:219], v250, %[ws], s97 offen sc1\n\t" \
  "v_mfma_f32_16x16x32_bf16 v[84:87], a[148:151], a[232:235], v[84:87]\n\t" \
  "v_mfma_f32_16x16x32_bf16 v[84:87], a[84:87], a[248:251], v[84:87]\n\t" \
  "v_mfma_f32_16x16x32_bf16 v[116:119], a[84:87], a[252:255], v[116:119]\n\t" \
  "v_mfma_f32_16x16x32_bf16 v[116:119], a[148:151], a[236:239], v[116:119]\n\t" \
  "v_mfma_f32_16x16x32_bf16 v[24:27], a[152:155], a[224:227], v[24:27]\n\t" \
  "v_mfma_f32_16x16x32_bf16 v[24:27], a[88:91], a[240:243], v[24:27]\n\t" \
  "buffer_load_dwordx4 a[220:223], v251, %[ws], s97 offen sc1\n\t" \
  "v_mfma_f32_16x16x32_bf16 v[56:59], a[88:91], a[244:247], v[56:59]\n\t" \
  "v_mfma_f32_16x16x32_bf16 v[56:59], a[152:155], a[228:231], v[56:59]\n\t" \
  "v_mfma_f32_16x16x32_bf16 v[88:91], a[152:155], a[232:235], v[88:91]\n\t" \
  "v_mfma_f32_16x16x32_bf16 v[88:91], a[88:91], a[248:251], v[88:91]\n\t" \
  "v_mfma_f32_16x16x32_bf16 v[120:123], a[88:91], a[252:255], v[120:123]\n\t" \
  "v_mfma_f32_16x16x32_bf16 v[120:123], a[152:155], a[236:239], v[120:123]\n\t" \
  "buffer_load_dword %[pf], v217, %[ws], %[pto] offen sc1\n\t" \
  "v_mfma_f32_16x16x32_bf16 v[28:31], a[156:159], a[224:227], v[28:31]\n\t" \
  "v_mfma_f32_16x16x32_bf16 v[28:31], a[92:95], a[240:243], v[28:31]\n\t" \
  "v_mfma_f32_16x16x32_bf16 v[60:63], a[92:95], a[244:247], v[60:63]\n\t" \
  "s_waitcnt vmcnt(10)\n\t" \
  "v_lshlrev_b32 v128, 16, v160\n\t" \
  "v_and_b32 v160, 0xffff0000, v160\n\t" \
  "v_mfma_f32_16x16x32_bf16 v[60:63], a[156:159], a[228:231], v[60:63]\n\t" \
  "v_fma_f32 v176, v128, v174, v176\n\t" \
  "v_fma_f32 v180, v160, v174, v180\n\t" \
  "v_mfma_f32_16x16x32_bf16 v[92:95], a[156:159], a[232:235], v[92:95]\n\t" \
  "v_lshlrev_b32 v128, 16, v161\n\t" \
  "v_and_b32 v161, 0xffff0000, v161\n\t" \
  "v_mfma_f32_16x16x32_bf16 v[92:95], a[92:95], a[248:251], v[92:95]\n\t" \
  "s_add_u32 s97, %[rot19], 7864320\n\t" \
  "v_fma_f32 v177, v128, v174, v177\n\t" \
  "v_fma_f32 v181, v161, v174, v181\n\t" \
  "v_mfma_f32_16x16x32_bf16 v[124:127], a[92:95], a[252:255], v[124:127]\n\t" \
  "s_waitcnt lgkmcnt(0)\n\t" \
  "s_bfe_u32 s97, s97, 0x140003\n\t" \
  "s_add_u32 s97, s97, %[sox]\n\t" \
  "v_mfma_f32_16x16x32_bf16 v[124:127], a[156:159], a[236:239], v[124:127]\n\t" \
  "s_barrier\n\t" \
  "v_mfma_f32_16x16x32_bf16 v[0:3], a[160:163], a[0:3], v[0:3]\n\t" \
  "buffer_load_dwordx4 a[224:227], v248, %[ws], s97 offen sc1\n\t" \
  "v_mfma_f32_16x16x32_bf16 v[0:3], a[96:99], a[16:19], v[0:3]\n\t" \
  "ds_read_b64_tr_b16 a[128:129], v192 offset:0\n\t" \
  "v_lshlrev_b32 v128, 16, v162\n\t" \
  "v_mfma_f32_16x16x32_bf16 v[32:35], a[96:99], a[20:23], v[32:35]\n\t" \
  "ds_read_b64_tr_b16 a[130:131], v192 offset:1024\n\t" \
  "v_and_b32 v162, 0xffff0000, v162\n\t" \
  "v_mfma_f32_16x16x32_bf16 v[32:35], a[160:163], a[4:7], v[32:35]\n\t" \
  "v_fma_f32 v178, v128, v174, v178\n\t" \
  "v_fma_f32 v182, v162, v174, v182\n\t" \
  "v_mfma_f32_16x16x32_bf16 v[64:67], a[160:163], a[8:11], v[64:67]\n\t" \
  "ds_read_b64_tr_b16 a[132:133], v193 offset:0\n\t" \
  "v_lshlrev_b32 v128, 16, v163\n\t" \
  "v_mfma_f32_16x16x32_bf16 v[64:67], a[96:99], a[24:27], v[64:67]\n\t" \
  "ds_read_b64_tr_b16 a[134:135], v193 offset:1024\n\t" \
  "v_and_b32 v163, 0xffff0000, v163\n\t" \
  "v_mfma_f32_16x16x32_bf16 v[96:99], a[96:99], a[28:31], v[96:99]\n\t" \
  "v_fma_f32 v179, v128, v174, v179\n\t" \
  "v_fma_f32 v183, v163, v174, v183\n\t" \
  "v_mfma_f32_16x16x32_bf16 v[96:99], a[160:163], a[12:15], v[96:99]\n\t" \
  "s_waitcnt vmcnt(10)\n\t" \
  "v_lshlrev_b32 v128, 16, v164\n\t" \
  "v_and_b32 v164, 0xffff0000, v164\n\t" \
  "v_mfma_f32_16x16x32_bf16 v[4:7], a[164:167], a[0:3], v[4:7]\n\t" \
  "buffer_load_dwordx4 a[228:231], v249, %[ws], s97 offen sc1\n\t" \
  "v_mfma_f32_16x16x32_bf16 v[4:7], a[100:103], a[16:19], v[4:7]\n\t" \
  "ds_read_b64_tr_b16 a[136:137], v194 offset:0\n\t" \
  "v_fma_f32 v176, v128, v175, v176\n\t" \
  "v_mfma_f32_16x16x32_bf16 v[36:39], a[100:103], a[20:23], v[36:39]\n\t" \
  "ds_read_b64_tr_b16 a[138:139], v194 offset:1024\n\t" \
  "v_fma_f32 v180, v164, v175, v180\n\t" \
  "v_mfma_f32_16x16x32_bf16 v[36:39], a[164:167], a[4:7], v[36:39]\n\t" \
  "ds_read_b64_tr_b16 a[140:141], v195 offset:0\n\t" \
  "v_lshlrev_b32 v128, 16, v165\n\t" \
  "v_mfma_f32_16x16x32_bf16 v[68:71], a[164:167], a[8:11], v[68:71]\n\t" \
  "ds_read_b64_tr_b16 a[142:143], v195 offset:1024\n\t" \
  "v_and_b32 v165, 0xffff0000, v165\n\t" \
  "v_mfma_f32_16x16x32_bf16 v[68:71], a[100:103], a[24:27], v[68:71]\n\t" \
  "ds_read_b64_tr_b16 a[144:145], v196 offset:0\n\t" \
  "v_fma_f32 v177, v128, v175, v177\n\t" \
  "v_mfma_f32_16x16x32_bf16 v[100:103], a[100:103], a[28:31], v[100:103]\n\t" \
  "v_fma_f32 v181, v165, v175, v181\n\t" \
  "v_lshlrev_b32 v128, 16, v166\n\t" \
  "v_mfma_f32_16x16x32_bf16 v[100:103], a[164:167], a[12:15], v[100:103]\n\t" \
  "buffer_load_dwordx4 a[232:235], v250, %[ws], s97 offen sc1\n\t" \
  "v_mfma_f32_16x16x32_bf16 v[8:11], a[168:171], a[0:3], v[8:11]\n\t" \
  "ds_read_b64_tr_b16 a[146:147], v196 offset:1024\n\t" \
  "v_and_b32 v166, 0xffff0000, v166\n\t" \
  "v_mfma_f32_16x16x32_bf16 v[8:11], a[104:107], a[16:19], v[8:11]\n\t" \
  "ds_read_b64_tr_b16 a[148:149], v197 offset:0\n\t" \
  "v_fma_f32 v178, v128, v175, v178\n\t" \
  "v_mfma_f32_16x16x32_bf16 v[40:43], a[104:107], a[20:23], v[40:43]\n\t" \
  "ds_read_b64_tr_b16 a[150:151], v197 offset:1024\n\t" \
  "v_fma_f32 v182, v166, v175, v182\n\t" \
  "v_mfma_f32_16x16x32_bf16 v[40:43], a[168:171], a[4:7], v[40:43]\n\t" \
  "v_lshlrev_b32 v128, 16, v167\n\t" \
  "v_and_b32 v167, 0xffff0000, v167\n\t" \
  "v_mfma_f32_16x16x32_bf16 v[72:75], a[168:171], a[8:11], v[72:75]\n\t" \
  "ds_read_b64_tr_b16 a[152:153], v198 offset:0\n\t" \
  "v_fma_f32 v179, v128, v175, v179\n\t" \
  "v_mfma_f32_16x16x32_bf16 v[72:75], a[104:107], a[24:27], v[72:75]\n\t" \
  "v_fma_f32 v183, v167, v175, v183\n\t" \
  "v_and_b32 v138, 7, v233\n\t" \
  "v_mfma_f32_16x16x32_bf16 v[104:107], a[104:107], a[28:31], v[104:107]\n\t" \
  "ds_read_b64_tr_b16 a[154:155], v198 offset:1024\n\t" \
  "v_lshlrev_b32 v138, 5, v138\n\t" \
  "v_mfma_f32_16x16x32_bf16 v[104:107], a[168:171], a[12:15], v[104:107]\n\t" \
  "buffer_load_dwordx4 a[236:239], v251, %[ws], s97 offen sc1\n\t" \
  "v_mfma_f32_16x16x32_bf16 v[12:15], a[172:175], a[0:3], v[12:15]\n\t" \
  "v_permlane32_swap_b32 v176, v178\n\t" \
  "v_mfma_f32_16x16x32_bf16 v[12:15], a[108:111], a[16:19], v[12:15]\n\t" \
  "ds_read_b64_tr_b16 a[156:157], v199 offset:0\n\t" \
  "v_mfma_f32_16x16x32_bf16 v[44:47], a[108:111], a[20:23], v[44:47]\n\t" \
  "v_permlane32_swap_b32 v180, v182\n\t" \
  "v_mfma_f32_16x16x32_bf16 v[44:47], a[172:175], a[4:7], v[44:47]\n\t" \
  "ds_read_b64_tr_b16 a[158:159], v199 offset:1024\n\t" \
  "v_mfma_f32_16x16x32_bf16 v[76:79], a[172:175], a[8:11], v[76:79]\n\t" \
  "ds_read_b64_tr_b16 a[64:65], v192 offset:8192\n\t" \
  "v_mfma_f32_16x16x32_bf16 v[76:79], a[108:111], a[24:27], v[76:79]\n\t" \
  "s_add_u32 s97, s97, %[dk]\n\t" \
  "v_permlane32_swap_b32 v177, v179\n\t" \
  "v_mfma_f32_16x16x32_bf16 v[108:111], a[108:111], a[28:31], v[108:111]\n\t" \
  "ds_read_b64_tr_b16 a[66:67], v192 offset:9216\n\t" \
  "v_mfma_f32_16x16x32_bf16 v[108:111], a[172:175], a[12:15], v[108:111]\n\t" \
  "ds_read_b64_tr_b16 a[68:69], v193 offset:8192\n\t" \
  "v_mfma_f32_16x16x32_bf16 v[16:19], a[176:179], a[0:3], v[16:19]\n\t" \
  "buffer_load_dwordx4 a[240:243], v248, %[ws], s97 offen sc1\n\t" \
  "v_mfma_f32_16x16x32_bf16 v[16:19], a[112:115], a[16:19], v[16:19]\n\t" \
  "ds_read_b64_tr_b16 a[70:71], v193 offset:9216\n\t" \
  "v_mfma_f32_16x16x32_bf16 v[48:51], a[112:115], a[20:23], v[48:51]\n\t" \
  "ds_read_b64_tr_b16 a[72:73], v194 offset:8192\n\t" \
  "v_mfma_f32_16x16x32_bf16 v[48:51], a[176:179], a[4:7], v[48:51]\n\t" \
  "v_permlane32_swap_b32 v181, v183\n\t" \
  "v_mfma_f32_16x16x32_bf16 v[80:83], a[176:179], a[8:11], v[80:83]\n\t" \
  "ds_read_b64_tr_b16 a[74:75], v194 offset:9216\n\t" \
  "v_add_f32 v176, v176, v178\n\t" \
  "v_mfma_f32_16x16x32_bf16 v[80:83], a[112:115], a[24:27], v[80:83]\n\t" \
  "ds_read_b64_tr_b16 a[76:77], v195 offset:8192\n\t" \
  "v_add_f32 v177, v177, v179\n\t" \
  "v_mfma_f32_16x16x32_bf16 v[112:115], a[112:115], a[28:31], v[112:115]\n\t" \
  "buffer_load_dwordx4 a[244:247], v249, %[ws], s97 offen sc1\n\t" \
  "v_mfma_f32_16x16x32_bf16 v[112:115], a[176:179], a[12:15], v[112:115]\n\t" \
  "ds_read_b64_tr_b16 a[78:79], v195 offset:9216\n\t" \
  "v_add_f32 v180, v180, v182\n\t" \
  "v_mfma_f32_16x16x32_bf16 v[20:23], a[180:183], a[0:3], v[20:23]\n\t" \
  "ds_read_b64_tr_b16 a[80:81], v196 offset:8192\n\t" \
  "v_add_f32 v181, v181, v183\n\t" \
  "v_mfma_f32_16x16x32_bf16 v[20:23], a[116:119], a[16:19], v[20:23]\n\t" \
  ".Lhs4:\n\ts_cbranch_execz .Lhs4n\n\t" \
  "ds_read_b64_tr_b16 a[82:83], v196 offset:9216\n\t" \
  "v_lshrrev_b32 v139, 4, v233\n\t" \
  "v_mfma_f32_16x16x32_bf16 v[52:55], a[116:119], a[20:23], v[52:55]\n\t" \
  "v_permlane16_swap_b32 v176, v177\n\t" \
  "v_mfma_f32_16x16x32_bf16 v[52:55], a[180:183], a[4:7], v[52:55]\n\t" \
  "v_permlane16_swap_b32 v180, v181\n\t" \
  "v_mfma_f32_16x16x32_bf16 v[84:87], a[180:183], a[8:11], v[84:87]\n\t" \
  "buffer_load_dwordx4 a[248:251], v250, %[ws], s97 offen sc1\n\t" \
  "v_mfma_f32_16x16x32_bf16 v[84:87], a[116:119], a[24:27], v[84:87]\n\t" \
  "ds_read_b64_tr_b16 a[84:85], v197 offset:8192\n\t" \
  "v_add_f32 v176, v176, v177\n\t" \
  "v_mfma_f32_16x16x32_bf16 v[116:119], a[116:119], a[28:31], v[116:119]\n\t" \
  "ds_read_b64_tr_b16 a[86:87], v197 offset:9216\n\t" \
  "v_add_f32 v180, v180, v181\n\t" \
  "v_mfma_f32_16x16x32_bf16 v[116:119], a[180:183], a[12:15], v[116:119]\n\t" \
  "ds_read_b64_tr_b16 a[88:89], v198 offset:8192\n\t" \
  "v_lshl_add_u32 v138, v139, 3, v138\n\t" \
  "v_mfma_f32_16x16x32_bf16 v[24:27], a[184:187], a[0:3], v[24:27]\n\t" \
  "ds_read_b64_tr_b16 a[90:91], v198 offset:9216\n\t" \
  "v_and_b32 v139, 0x7fffff00, v219\n\t" \
  "v_mfma_f32_16x16x32_bf16 v[24:27], a[120:123], a[16:19], v[24:27]\n\t" \
  "ds_read_b64_tr_b16 a[92:93], v199 offset:8192\n\t" \
  "v_add_f32_dpp v136, v176, v176 row_ror:8 row_mask:0xf bank_mask:0xf\n\t" \
  "v_mfma_f32_16x16x32_bf16 v[56:59], a[120:123], a[20:23], v[56:59]\n\t" \
  "buffer_load_dwordx4 a[252:255], v251, %[ws], s97 offen sc1\n\t" \
  "v_mfma_f32_16x16x32_bf16 v[56:59], a[184:187], a[4:7], v[56:59]\n\t" \
  "s_add_u32 s99, %[pf4], 0\n\t" \
  "ds_read_b64_tr_b16 a[94:95], v199 offset:9216\n\t" \
  "v_add_f32_dpp v137, v180, v180 row_ror:8 row_mask:0xf bank_mask:0xf\n\t" \
  "v_mfma_f32_16x16x32_bf16 v[88:91], a[184:187], a[8:11], v[88:91]\n\t" \
  "v_add_u32 v138, v139, v138\n\t" \
  "v_mfma_f32_16x16x32_bf16 v[88:91], a[120:123], a[24:27], v[88:91]\n\t" \
  "ds_write_b64 v138, v[136:137] offset:1024\n\t" \
  "v_mfma_f32_16x16x32_bf16 v[120:123], a[120:123], a[28:31], v[120:123]\n\t" \
  "v_and_b32 v140, 3, v233\n\t" \
  "v_bfe_u32 v141, v233, 4, 1\n\t" \
  "v_mfma_f32_16x16x32_bf16 v[120:123], a[184:187], a[12:15], v[120:123]\n\t" \
  "v_lshl_add_u32 v140, v141, 2, v140\n\t" \
  "v_bfe_u32 v141, v233, 2, 2\n\t" \
  "v_mfma_f32_16x16x32_bf16 v[28:31], a[188:191], a[0:3], v[28:31]\n\t" \
  "buffer_load_dword %[pf], v245, %[sG], s99 offen\n\t" \
  "v_mfma_f32_16x16x32_bf16 v[28:31], a[124:127], a[16:19], v[28:31]\n\t" \
  "v_lshlrev_b32 v141, 8, v141\n\t" \
  "v_lshl_add_u32 v141, v140, 3, v141\n\t" \
  "v_mfma_f32_16x16x32_bf16 v[60:63], a[124:127], a[20:23], v[60:63]\n\t" \
  "v_lshl_add_u32 v141, %[wid], 6, v141\n\t" \
  "v_lshlrev_b32 v140, 4, v140\n\t" \
  "v_mfma_f32_16x16x32_bf16 v[60:63], a[188:191], a[4:7], v[60:63]\n\t" \
  "v_and_b32 v142, 0x2c, v233\n\t" \
  "v_lshl_or_b32 v140, v142, 26, v140\n\t" \
  "v_mfma_f32_16x16x32_bf16 v[92:95], a[188:191], a[8:11], v[92:95]\n\t" \
  "v_add_u32 v141, 151552, v141\n\t" \
  "v_mov_b32 v145, v211\n\t" \
  "v_mfma_f32_16x16x32_bf16 v[92:95], a[124:127], a[24:27], v[92:95]\n\t" \
  "s_add_u32 s99, %[pf4], 14336\n\t" \
  "v_mov_b32 v147, v211\n\t" \
  "v_mfma_f32_16x16x32_bf16 v[124:127], a[124:127], a[28:31], v[124:127]\n\t" \
  "s_waitcnt lgkmcnt(1)\n\t" \
  "v_mfma_f32_16x16x32_bf16 v[124:127], a[188:191], a[12:15], v[124:127]\n\t" \
  "s_barrier\n\t" \
  "v_mfma_f32_16x16x32_bf16 v[0:3], a[128:131], a[32:35], v[0:3]\n\t" \
  "buffer_load_dword %[pf], v245, %[sG], s99 offen\n\t" \
  "v_mfma_f32_16x16x32_bf16 v[0:3], a[64:67], a[48:51], v[0:3]\n\t" \
  "ds_read_b64_tr_b16 a[160:161], v192 offset:16384\n\t" \
  "v_mfma_f32_16x16x32_bf16 v[32:35], a[64:67], a[52:55], v[32:35]\n\t" \
  "ds_read_b64_tr_b16 a[162:163], v192 offset:17408\n\t" \
  "v_mfma_f32_16x16x32_bf16 v[32:35], a[128:131], a[36:39], v[32:35]\n\t" \
  "ds_read_b64_tr_b16 a[164:165], v193 offset:16384\n\t" \
  "v_mfma_f32_16x16x32_bf16 v[64:67], a[128:131], a[40:43], v[64:67]\n\t" \
  "ds_read_b64_tr_b16 a[166:167], v193 offset:17408\n\t" \
  "v_mfma_f32_16x16x32_bf16 v[64:67], a[64:67], a[56:59], v[64:67]\n\t" \
  "ds_read_b64_tr_b16 a[168:169], v194 offset:16384\n\t" \
  "v_mfma_f32_16x16x32_bf16 v[96:99], a[64:67], a[60:63], v[96:99]\n\t" \
  "ds_read_b64_tr_b16 a[170:171], v194 offset:17408\n\t" \
  "v_mfma_f32_16x16x32_bf16 v[96:99], a[128:131], a[44:47], v[96:99]\n\t" \
  "ds_read_b64_tr_b16 a[172:173], v195 offset:16384\n\t" \
  "v_mfma_f32_16x16x32_bf16 v[4:7], a[132:135], a[32:35], v[4:7]\n\t" \
  "ds_read_b64_tr_b16 a[174:175], v195 offset:17408\n\t" \
  "v_mfma_f32_16x16x32_bf16 v[4:7], a[68:71], a[48:51], v[4:7]\n\t" \
  "ds_read_b64_tr_b16 a[176:177], v196 offset:16384\n\t" \
  "v_mfma_f32_16x16x32_bf16 v[36:39], a[68:71], a[52:55], v[36:39]\n\t" \
  "ds_read_b64_tr_b16 a[178:179], v196 offset:17408\n\t" \
  "v_mfma_f32_16x16x32_bf16 v[36:39], a[132:135], a[36:39], v[36:39]\n\t" \
  "ds_read_b64_tr_b16 a[180:181], v197 offset:16384\n\t" \
  "v_mfma_f32_16x16x32_bf16 v[68:71], a[132:135], a[40:43], v[68:71]\n\t" \
  "ds_read_b64_tr_b16 a[182:183], v197 offset:17408\n\t" \
  "v_mfma_f32_16x16x32_bf16 v[68:71], a[68:71], a[56:59], v[68:71]\n\t" \
  "ds_read_b64_tr_b16 a[184:185], v198 offset:16384\n\t" \
  "v_mfma_f32_16x16x32_bf16 v[100:103], a[68:71], a[60:63], v[100:103]\n\t" \
  "ds_read_b64_tr_b16 a[186:187], v198 offset:17408\n\t" \
  "v_mfma_f32_16x16x32_bf16 v[100:103], a[132:135], a[44:47], v[100:103]\n\t" \
  "ds_read_b64_tr_b16 a[188:189], v199 offset:16384\n\t" \
  "v_mfma_f32_16x16x32_bf16 v[8:11], a[136:139], a[32:35], v[8:11]\n\t" \
  "ds_read_b64_tr_b16 a[190:191], v199 offset:17408\n\t" \
  "v_mfma_f32_16x16x32_bf16 v[8:11], a[72:75], a[48:51], v[8:11]\n\t" \
  "ds_read_b64_tr_b16 a[96:97], v192 offset:24576\n\t" \
  "v_mfma_f32_16x16x32_bf16 v[40:43], a[72:75], a[52:55], v[40:43]\n\t" \
  "ds_read_b64_tr_b16 a[98:99], v192 offset:25600\n\t" \
  "v_mfma_f32_16x16x32_bf16 v[40:43], a[136:139], a[36:39], v[40:43]\n\t" \
  "v_mfma_f32_16x16x32_bf16 v[72:75], a[136:139], a[40:43], v[72:75]\n\t" \
  "ds_read_b64_tr_b16 a[100:101], v193 offset:24576\n\t" \
  "v_mfma_f32_16x16x32_bf16 v[72:75], a[72:75], a[56:59], v[72:75]\n\t" \
  "ds_read_b64_tr_b16 a[102:103], v193 offset:25600\n\t" \
  "v_mfma_f32_16x16x32_bf16 v[104:107], a[72:75], a[60:63], v[104:107]\n\t" \
  "v_mfma_f32_16x16x32_bf16 v[104:107], a[136:139], a[44:47], v[104:107]\n\t" \
  "ds_read_b64_tr_b16 a[104:105], v194 offset:24576\n\t" \
  "v_mfma_f32_16x16x32_bf16 v[12:15], a[140:143], a[32:35], v[12:15]\n\t" \
  "v_mfma_f32_16x16x32_bf16 v[12:15], a[76:79], a[48:51], v[12:15]\n\t" \
  "ds_read_b64_tr_b16 a[106:107], v194 offset:25600\n\t" \
  "v_mfma_f32_16x16x32_bf16 v[44:47], a[76:79], a[52:55], v[44:47]\n\t" \
  "ds_read_b64_tr_b16 a[108:109], v195 offset:24576\n\t" \
  "v_mfma_f32_16x16x32_bf16 v[44:47], a[140:143], a[36:39], v[44:47]\n\t" \
  "v_mfma_f32_16x16x32_bf16 v[76:79], a[140:143], a[40:43], v[76:79]\n\t" \
  "ds_read_b64_tr_b16 a[110:111], v195 offset:25600\n\t" \
  "v_mfma_f32_16x16x32_bf16 v[76:79], a[76:79], a[56:59], v[76:79]\n\t" \
  "ds_read_b64_tr_b16 a[112:113], v196 offset:24576\n\t" \
  "s_add_u32 m0, %[mw], 16384\n\t" \
  "v_mfma_f32_16x16x32_bf16 v[108:111], a[76:79], a[60:63], v[108:111]\n\t" \
  "v_mfma_f32_16x16x32_bf16 v[108:111], a[140:143], a[44:47], v[108:111]\n\t" \
  "buffer_load_dwordx4 v224, %[sK], %[sofk] offen offset:0 lds\n\t" \
  "v_mfma_f32_16x16x32_bf16 v[16:19], a[144:147], a[32:35], v[16:19]\n\t" \
  "ds_read_b64_tr_b16 a[114:115], v196 offset:25600\n\t" \
  "v_mfma_f32_16x16x32_bf16 v[16:19], a[80:83], a[48:51], v[16:19]\n\t" \
  "ds_read_b64_tr_b16 a[116:117], v197 offset:24576\n\t" \
  "v_mfma_f32_16x16x32_bf16 v[48:51], a[80:83], a[52:55], v[48:51]\n\t" \
  "ds_read_b64_tr_b16 a[118:119], v197 offset:25600\n\t" \
  "v_mfma_f32_16x16x32_bf16 v[48:51], a[144:147], a[36:39], v[48:51]\n\t" \
  "v_mfma_f32_16x16x32_bf16 v[80:83], a[144:147], a[40:43], v[80:83]\n\t" \
  "buffer_load_dwordx4 v225, %[sK], %[sofk] offen offset:1024 lds\n\t" \
  "v_mfma_f32_16x16x32_bf16 v[80:83], a[80:83], a[56:59], v[80:83]\n\t" \
  "ds_read_b64_tr_b16 a[120:121], v198 offset:24576\n\t" \
  "v_mfma_f32_16x16x32_bf16 v[112:115], a[80:83], a[60:63], v[112:115]\n\t" \
  "ds_read_b64_tr_b16 a[122:123], v198 offset:25600\n\t" \
  "v_mfma_f32_16x16x32_bf16 v[112:115], a[144:147], a[44:47], v[112:115]\n\t" \
  "ds_read_b64_tr_b16 a[124:125], v199 offset:24576\n\t" \
  "v_mfma_f32_16x16x32_bf16 v[20:23], a[148:151], a[32:35], v[20:23]\n\t" \
  "v_mfma_f32_16x16x32_bf16 v[20:23], a[84:87], a[48:51], v[20:23]\n\t" \
  "buffer_load_dwordx4 v226, %[sK], %[sofk] offen offset:2048 lds\n\t" \
  "v_mfma_f32_16x16x32_bf16 v[52:55], a[84:87], a[52:55], v[52:55]\n\t" \
  "ds_read_b64_tr_b16 a[126:127], v199 offset:25600\n\t" \
  "v_mfma_f32_16x16x32_bf16 v[52:55], a[148:151], a[36:39], v[52:55]\n\t" \
  "v_mfma_f32_16x16x32_bf16 v[84:87], a[148:151], a[40:43], v[84:87]\n\t" \
  "v_mfma_f32_16x16x32_bf16 v[84:87], a[84:87], a[56:59], v[84:87]\n\t" \
  "v_mfma_f32_16x16x32_bf16 v[116:119], a[84:87], a[60:63], v[116:119]\n\t" \
  "buffer_load_dwordx4 v227, %[sK], %[sofk] offen offset:3072 lds\n\t" \
  "v_mfma_f32_16x16x32_bf16 v[116:119], a[148:151], a[44:47], v[116:119]\n\t" \
  "s_add_u32 m0, %[mw], 49152\n\t" \
  "v_mfma_f32_16x16x32_bf16 v[24:27], a[152:155], a[32:35], v[24:27]\n\t" \
  "v_mfma_f32_16x16x32_bf16 v[24:27], a[88:91], a[48:51], v[24:27]\n\t" \
  "v_mfma_f32_16x16x32_bf16 v[56:59], a[88:91], a[52:55], v[56:59]\n\t" \
  "v_mfma_f32_16x16x32_bf16 v[56:59], a[152:155], a[36:39], v[56:59]\n\t" \
  "buffer_load_dwordx4 v230, %[sV], %[sofk] offen offset:0 lds\n\t" \
  "v_mfma_f32_16x16x32_bf16 v[88:91], a[152:155], a[40:43], v[88:91]\n\t" \
  "v_mfma_f32_16x16x32_bf16 v[88:91], a[88:91], a[56:59], v[88:91]\n\t" \
  "v_mfma_f32_16x16x32_bf16 v[120:123], a[88:91], a[60:63], v[120:123]\n\t" \
  "v_mfma_f32_16x16x32_bf16 v[120:123], a[152:155], a[44:47], v[120:123]\n\t" \
  "v_mfma_f32_16x16x32_bf16 v[28:31], a[156:159], a[32:35], v[28:31]\n\t" \
  "buffer_load_dwordx4 v231, %[sV], %[sofk] offen offset:1024 lds\n\t" \
  "v_mfma_f32_16x16x32_bf16 v[28:31], a[92:95], a[48:51], v[28:31]\n\t" \
  "v_mfma_f32_16x16x32_bf16 v[60:63], a[92:95], a[52:55], v[60:63]\n\t" \
  "v_mfma_f32_16x16x32_bf16 v[60:63], a[156:159], a[36:39], v[60:63]\n\t" \
  "v_mfma_f32_16x16x32_bf16 v[92:95], a[156:159], a[40:43], v[92:95]\n\t" \
  "v_mfma_f32_16x16x32_bf16 v[92:95], a[92:95], a[56:59], v[92:95]\n\t" \
  "v_mfma_f32_16x16x32_bf16 v[124:127], a[92:95], a[60:63], v[124:127]\n\t" \
  "s_waitcnt vmcnt(17) lgkmcnt(0)\n\t" \
  "v_mfma_f32_16x16x32_bf16 v[124:127], a[156:159], a[44:47], v[124:127]\n\t" \
  "s_barrier\n\t" \
  "v_mfma_f32_16x16x32_bf16 v[0:3], a[160:163], a[192:195], v[0:3]\n\t" \
  "buffer_load_dwordx4 v230, %[sV], %[sofk] offen offset:2048 lds\n\t" \
  "v_mfma_f32_16x16x32_bf16 v[0:3], a[96:99], a[208:211], v[0:3]\n\t" \
  "ds_read_b64_tr_b16 a[128:129], v192 offset:32768\n\t" \
  "v_mfma_f32_16x16x32_bf16 v[32:35], a[96:99], a[212:215], v[32:35]\n\t" \
  "ds_read_b64_tr_b16 a[130:131], v192 offset:33792\n\t" \
  "v_mfma_f32_16x16x32_bf16 v[32:35], a[160:163], a[196:199], v[32:35]\n\t" \
  "ds_read_b64 v[142:143], v141 offset:1024\n\t" \
  "v_mfma_f32_16x16x32_bf16 v[64:67], a[160:163], a[200:203], v[64:67]\n\t" \
  "ds_read_b64_tr_b16 a[132:133], v193 offset:32768\n\t" \
  "v_mfma_f32_16x16x32_bf16 v[64:67], a[96:99], a[216:219], v[64:67]\n\t" \
  "buffer_load_dwordx4 v231, %[sV], %[sofk] offen offset:3072 lds\n\t" \
  "v_mfma_f32_16x16x32_bf16 v[96:99], a[96:99], a[220:223], v[96:99]\n\t" \
  "s_add_u32 m0, %[mw], 32768\n\t" \
  "ds_read_b64_tr_b16 a[134:135], v193 offset:33792\n\t" \
  "s_add_u32 s96, %[sofk], 16384\n\t" \
  "v_mfma_f32_16x16x32_bf16 v[96:99], a[160:163], a[204:207], v[96:99]\n\t" \
  "ds_read_b64_tr_b16 a[136:137], v194 offset:32768\n\t" \
  "v_mfma_f32_16x16x32_bf16 v[4:7], a[164:167], a[192:195], v[4:7]\n\t" \
  "buffer_load_dwordx4 v224, %[sK], s96 offen offset:0 lds\n\t" \
  "v_mfma_f32_16x16x32_bf16 v[4:7], a[100:103], a[208:211], v[4:7]\n\t" \
  "s_waitcnt lgkmcnt(3)\n\t" \
  "v_add_f32_dpp v142, v142, v142 row_ror:4 row_mask:0xf bank_mask:0xf\n\t" \
  "v_add_f32_dpp v143, v143, v143 row_ror:4 row_mask:0xf bank_mask:0xf\n\t" \
  "v_mfma_f32_16x16x32_bf16 v[36:39], a[100:103], a[212:215], v[36:39]\n\t" \
  "ds_read_b64_tr_b16 a[138:139], v194 offset:33792\n\t" \
  "v_add_f32_dpp v144, v142, v142 row_ror:8 row_mask:0xf bank_mask:0xf\n\t" \
  "v_mfma_f32_16x16x32_bf16 v[36:39], a[164:167], a[196:199], v[36:39]\n\t" \
  "ds_read_b64_tr_b16 a[140:141], v195 offset:32768\n\t" \
  "v_add_f32_dpp v146, v143, v143 row_ror:8 row_mask:0xf bank_mask:0xf\n\t" \
  "v_mfma_f32_16x16x32_bf16 v[68:71], a[164:167], a[200:203], v[68:71]\n\t" \
  "buffer_store_dwordx4 v[144:147], v140, %[ws], %[mbp] offen sc1\n\t" \
  "ds_read_b64_tr_b16 a[142:143], v195 offset:33792\n\t" \
  "ds_read_b64_tr_b16 a[144:145], v196 offset:32768\n\t" \
  "ds_read_b64_tr_b16 a[146:147], v196 offset:33792\n\t" \
  "v_mfma_f32_16x16x32_bf16 v[68:71], a[100:103], a[216:219], v[68:71]\n\t" \
  "buffer_load_dwordx4 v225, %[sK], s96 offen offset:1024 lds\n\t" \
  "v_mfma_f32_16x16x32_bf16 v[100:103], a[100:103], a[220:223], v[100:103]\n\t" \
  "ds_read_b64_tr_b16 a[148:149], v197 offset:32768\n\t" \
  "v_mfma_f32_16x16x32_bf16 v[100:103], a[164:167], a[204:207], v[100:103]\n\t" \
  "ds_read_b64_tr_b16 a[150:151], v197 offset:33792\n\t" \
  "v_mfma_f32_16x16x32_bf16 v[8:11], a[168:171], a[192:195], v[8:11]\n\t" \
  "buffer_load_dwordx4 v226, %[sK], s96 offen offset:2048 lds\n\t" \
  "v_mfma_f32_16x16x32_bf16 v[8:11], a[104:107], a[208:211], v[8:11]\n\t" \
  "ds_read_b64_tr_b16 a[152:153], v198 offset:32768\n\t" \
  "v_mfma_f32_16x16x32_bf16 v[40:43], a[104:107], a[212:215], v[40:43]\n\t" \
  "ds_read_b64_tr_b16 a[154:155], v198 offset:33792\n\t" \
  "v_mfma_f32_16x16x32_bf16 v[40:43], a[168:171], a[196:199], v[40:43]\n\t" \
  "ds_read_b64_tr_b16 a[156:157], v199 offset:32768\n\t" \
  "v_mfma_f32_16x16x32_bf16 v[72:75], a[168:171], a[200:203], v[72:75]\n\t" \
  "buffer_load_dwordx4 v227, %[sK], s96 offen offset:3072 lds\n\t" \
  "v_mfma_f32_16x16x32_bf16 v[72:75], a[104:107], a[216:219], v[72:75]\n\t" \
  "ds_read_b64_tr_b16 a[158:159], v199 offset:33792\n\t" \
  "s_add_u32 s99, %[pf4], 28672\n\t" \
  "v_mfma_f32_16x16x32_bf16 v[104:107], a[104:107], a[220:223], v[104:107]\n\t" \
  "ds_read_b64_tr_b16 a[64:65], v192 offset:40960\n\t" \
  "v_mfma_f32_16x16x32_bf16 v[104:107], a[168:171], a[204:207], v[104:107]\n\t" \
  "ds_read_b64_tr_b16 a[66:67], v192 offset:41984\n\t" \
  "v_mfma_f32_16x16x32_bf16 v[12:15], a[172:175], a[192:195], v[12:15]\n\t" \
  "buffer_load_dword %[pf], v245, %[sG], s99 offen\n\t" \
  "v_mfma_f32_16x16x32_bf16 v[12:15], a[108:111], a[208:211], v[12:15]\n\t" \
  "ds_read_b64_tr_b16 a[68:69], v193 offset:40960\n\t" \
  "v_mfma_f32_16x16x32_bf16 v[44:47], a[108:111], a[212:215], v[44:47]\n\t" \
  "ds_read_b64_tr_b16 a[70:71], v193 offset:41984\n\t" \
  "v_mfma_f32_16x16x32_bf16 v[44:47], a[172:175], a[196:199], v[44:47]\n\t" \
  "ds_read_b64_tr_b16 a[72:73], v194 offset:40960\n\t" \
  "v_mfma_f32_16x16x32_bf16 v[76:79], a[172:175], a[200:203], v[76:79]\n\t" \
  "ds_read_b64_tr_b16 a[74:75], v194 offset:41984\n\t" \
  "v_mfma_f32_16x16x32_bf16 v[76:79], a[108:111], a[216:219], v[76:79]\n\t" \
  "ds_read_b64_tr_b16 a[76:77], v195 offset:40960\n\t" \
  "s_add_u32 s99, %[pf4], 43008\n\t" \
  "v_mfma_f32_16x16x32_bf16 v[108:111], a[108:111], a[220:223], v[108:111]\n\t" \
  "ds_read_b64_tr_b16 a[78:79], v195 offset:41984\n\t" \
  "v_mfma_f32_16x16x32_bf16 v[108:111], a[172:175], a[204:207], v[108:111]\n\t" \
  "buffer_load_dword %[pf], v245, %[sG], s99 offen\n\t" \
  "v_mfma_f32_16x16x32_bf16 v[16:19], a[176:179], a[192:195], v[16:19]\n\t" \
  "ds_read_b64_tr_b16 a[80:81], v196 offset:40960\n\t" \
  "v_mfma_f32_16x16x32_bf16 v[16:19], a[112:115], a[208:211], v[16:19]\n\t" \
  "ds_read_b64_tr_b16 a[82:83], v196 offset:41984\n\t" \
  "v_mfma_f32_16x16x32_bf16 v[48:51], a[112:115], a[212:215], v[48:51]\n\t" \
  "ds_read_b64_tr_b16 a[84:85], v197 offset:40960\n\t" \
  "v_mfma_f32_16x16x32_bf16 v[48:51], a[176:179], a[196:199], v[48:51]\n\t" \
  "ds_read_b64_tr_b16 a[86:87], v197 offset:41984\n\t" \
  "v_mfma_f32_16x16x32_bf16 v[80:83], a[176:179], a[200:203], v[80:83]\n\t" \
  "ds_read_b64_tr_b16 a[88:89], v198 offset:40960\n\t" \
  "v_mfma_f32_16x16x32_bf16 v[80:83], a[112:115], a[216:219], v[80:83]\n\t" \
  "v_mfma_f32_16x16x32_bf16 v[112:115], a[112:115], a[220:223], v[112:115]\n\t" \
  "ds_read_b64_tr_b16 a[90:91], v198 offset:41984\n\t" \
  "v_mfma_f32_16x16x32_bf16 v[112:115], a[176:179], a[204:207], v[112:115]\n\t" \
  "ds_read_b64_tr_b16 a[92:93], v199 offset:40960\n\t" \
  "v_mfma_f32_16x16x32_bf16 v[20:23], a[180:183], a[192:195], v[20:23]\n\t" \
  "v_mfma_f32_16x16x32_bf16 v[20:23], a[116:119], a[208:211], v[20:23]\n\t" \
  "ds_read_b64_tr_b16 a[94:95], v199 offset:41984\n\t" \
  "v_mfma_f32_16x16x32_bf16 v[52:55], a[116:119], a[212:215], v[52:55]\n\t" \
  "v_mfma_f32_16x16x32_bf16 v[52:55], a[180:183], a[196:199], v[52:55]\n\t" \
  "v_mfma_f32_16x16x32_bf16 v[84:87], a[180:183], a[200:203], v[84:87]\n\t" \
  "v_mfma_f32_16x16x32_bf16 v[84:87], a[116:119], a[216:219], v[84:87]\n\t" \
  "v_mfma_f32_16x16x32_bf16 v[116:119], a[116:119], a[220:223], v[116:119]\n\t" \
  "v_mfma_f32_16x16x32_bf16 v[116:119], a[180:183], a[204:207], v[116:119]\n\t" \
  "v_mfma_f32_16x16x32_bf16 v[24:27], a[184:187], a[192:195], v[24:27]\n\t" \
  "v_mfma_f32_16x16x32_bf16 v[24:27], a[120:123], a[208:211], v[24:27]\n\t" \
  "v_mfma_f32_16x16x32_bf16 v[56:59], a[120:123], a[212:215], v[56:59]\n\t" \
  "v_mfma_f32_16x16x32_bf16 v[56:59], a[184:187], a[196:199], v[56:59]\n\t" \
  "v_mfma_f32_16x16x32_bf16 v[88:91], a[184:187], a[200:203], v[88:91]\n\t" \
  "v_mfma_f32_16x16x32_bf16 v[88:91], a[120:123], a[216:219], v[88:91]\n\t" \
  "v_mfma_f32_16x16x32_bf16 v[120:123], a[120:123], a[220:223], v[120:123]\n\t" \
  "v_mfma_f32_16x16x32_bf16 v[120:123], a[184:187], a[204:207], v[120:123]\n\t" \
  "v_mfma_f32_16x16x32_bf16 v[28:31], a[188:191], a[192:195], v[28:31]\n\t" \
  "v_mfma_f32_16x16x32_bf16 v[28:31], a[124:127], a[208:211], v[28:31]\n\t" \
  "v_mfma_f32_16x16x32_bf16 v[60:63], a[124:127], a[212:215], v[60:63]\n\t" \
  "v_mfma_f32_16x16x32_bf16 v[60:63], a[188:191], a[196:199], v[60:63]\n\t" \
  "v_mfma_f32_16x16x32_bf16 v[92:95], a[188:191], a[200:203], v[92:95]\n\t" \
  "v_mfma_f32_16x16x32_bf16 v[92:95], a[124:127], a[216:219], v[92:95]\n\t" \
  "v_mfma_f32_16x16x32_bf16 v[124:127], a[124:127], a[220:223], v[124:127]\n\t" \
  "v_mfma_f32_16x16x32_bf16 v[124:127], a[188:191], a[204:207], v[124:127]\n\t" \
  "s_waitcnt vmcnt(17) lgkmcnt(0)\n\t" \
  FSTP("s_waitcnt lgkmcnt(0)\n\t") \
  FSTP("v_writelane_b32 %[stv], s94, 52\n\t") \
  FSTP("v_writelane_b32 %[stv], s92, 53\n\t") \
  ".subsection 1\n\t" \
  ".Lfq_s:\n\t" \
  "s_mov_b32 s97, 0\n\t" \
  ".Lfq_r:\n\t" \
  "buffer_load_dword %[fl], v210, %[ws], %[fo] offen sc1\n\t" \
  "s_waitcnt vmcnt(0)\n\t" \
  "v_cmp_ne_u32 vcc, v211, %[fl]\n\t" \
  "s_cbranch_vccz .Lfq_k\n\t" \
  "s_add_u32 s97, s97, 1\n\t" \
  "s_cmp_lt_u32 s97, " FQ_CAP "\n\t" \
  "s_cbranch_scc1 .Lfq_r\n\t" \
  "buffer_store_dword v211, off, %[ws], 0 offset:256 sc1\n\t" \
  "s_branch .Lfq_k\n\t" \
  ".subsection 0\n\t"
#define FQ_CL "memory", "scc", "vcc", "s96", "s97", "s98", "s99", "v128", "v129", "v130", "v131", "v132", "v133", "v134", "v135", "v136", "v137", "v138", "v139", "v140", "v141", "v142", "v143", "v144", "v145", "v146", "v147", "v148", "v149", "v150", "v151", "v152", "v153", "v154", "v155", "v156", "v157", "v158", "v159", "v176", "v177", "v178", "v179", "v180", "v181", "v182", "v183", "v234", "v235", "v236", "v237", "v238", "v239", "v242", "v243", "v248", "v249", "v250", "v251" FQ_STC
#define FQ_EB 0
static_assert(W_VOTE == 8192 && W_FLAGR == 32768 && W_FLAGO == 131072 && W_MODB == 2228224 && W_OT == 8388608 && W_XCH == 25165824);
#define STS(x)
#define AO_STV
#define STA(x)
#define STP(x)
#define AT_ASM \
  "s_cmp_eq_u32 %[pm], 0\n\t" \
  "s_cbranch_scc1 .Lsf_go\n\t" \
  "v_mfma_f32_16x16x32_bf16 v[28:31], a[156:159], a[224:227], v[28:31]\n\t" \
  "s_add_u32 s77, %[wob], 131072\n\t" \
  "v_add_u32 v210, 65536, v228\n\t" \
  "v_add_u32 v248, 14336, v240\n\t" \
  "v_mfma_f32_16x16x32_bf16 v[28:31], a[92:95], a[240:243], v[28:31]\n\t" \
  "v_add_u32 v211, 65536, v229\n\t" \
  "v_add_u32 v249, 21504, v241\n\t" \
  "v_mfma_f32_16x16x32_bf16 v[24:27], a[152:155], a[224:227], v[24:27]\n\t" \
  "v_mov_b32 v216, 0x3f803f80\n\t" \
  "v_mov_b32 v217, 0x3f803f80\n\t" \
  "v_mfma_f32_16x16x32_bf16 v[24:27], a[88:91], a[240:243], v[24:27]\n\t" \
  "v_mov_b32 v218, 0x3f803f80\n\t" \
  "v_add_u32 v241, 7168, v241\n\t" \
  "v_mfma_f32_16x16x32_bf16 v[20:23], a[148:151], a[224:227], v[20:23]\n\t" \
  "s_mov_b32 s89, 0\n\t" \
  "v_cvt_pk_bf16_f32 v31, v30, v31\n\t" \
  "v_cvt_pk_bf16_f32 v30, v28, v29\n\t" \
  "v_mfma_f32_16x16x32_bf16 v[20:23], a[84:87], a[240:243], v[20:23]\n\t" \
  "v_mov_b32 v219, 0x3f803f80\n\t" \
  "v_cvt_pk_bf16_f32 v28, v24, v25\n\t" \
  "v_mfma_f32_16x16x32_bf16 v[16:19], a[144:147], a[224:227], v[16:19]\n\t" \
  "v_cvt_pk_bf16_f32 v29, v26, v27\n\t" \
  "v_lshlrev_b32 v212, 7, v233\n\t" \
  "v_mfma_f32_16x16x32_bf16 v[16:19], a[80:83], a[240:243], v[16:19]\n\t" \
  "v_permlane16_swap_b32 v28, v30\n\t" \
  "v_mfma_f32_16x16x32_bf16 v[12:15], a[140:143], a[224:227], v[12:15]\n\t" \
  "v_cvt_pk_bf16_f32 v27, v22, v23\n\t" \
  "v_cvt_pk_bf16_f32 v26, v20, v21\n\t" \
  "v_mfma_f32_16x16x32_bf16 v[12:15], a[76:79], a[240:243], v[12:15]\n\t" \
  "v_permlane16_swap_b32 v29, v31\n\t" \
  "v_mfma_f32_16x16x32_bf16 v[8:11], a[136:139], a[224:227], v[8:11]\n\t" \
  "s_mov_b32 %[na], 0\n\t" \
  "v_cvt_pk_bf16_f32 v24, v16, v17\n\t" \
  "v_cvt_pk_bf16_f32 v25, v18, v19\n\t" \
  "v_mfma_f32_16x16x32_bf16 v[8:11], a[72:75], a[240:243], v[8:11]\n\t" \
  "v_permlane16_swap_b32 v24, v26\n\t" \
  "v_mfma_f32_16x16x32_bf16 v[4:7], a[132:135], a[224:227], v[4:7]\n\t" \
  "v_cvt_pk_bf16_f32 v23, v14, v15\n\t" \
  "v_cvt_pk_bf16_f32 v22, v12, v13\n\t" \
  "v_mfma_f32_16x16x32_bf16 v[4:7], a[68:71], a[240:243], v[4:7]\n\t" \
  "v_permlane16_swap_b32 v25, v27\n\t" \
  "v_mfma_f32_16x16x32_bf16 v[0:3], a[128:131], a[224:227], v[0:3]\n\t" \
  "s_waitcnt vmcnt(2)\n\t" \
  "v_cvt_pk_bf16_f32 v20, v8, v9\n\t" \
  "v_cvt_pk_bf16_f32 v21, v10, v11\n\t" \
  "v_mfma_f32_16x16x32_bf16 v[0:3], a[64:67], a[240:243], v[0:3]\n\t" \
  "s_barrier\n\t" \
  "v_mfma_f32_16x16x32_bf16 v[60:63], a[156:159], a[228:231], v[60:63]\n\t" \
  "v_cvt_pk_bf16_f32 v19, v6, v7\n\t" \
  "v_cvt_pk_bf16_f32 v18, v4, v5\n\t" \
  "v_mfma_f32_16x16x32_bf16 v[60:63], a[92:95], a[244:247], v[60:63]\n\t" \
  "v_permlane16_swap_b32 v20, v22\n\t" \
  "v_mfma_f32_16x16x32_bf16 v[56:59], a[152:155], a[228:231], v[56:59]\n\t" \
  "ds_read_b128 a[0:3], v220 offset:0\n\t" \
  "v_cvt_pk_bf16_f32 v16, v0, v1\n\t" \
  "v_mfma_f32_16x16x32_bf16 v[56:59], a[88:91], a[244:247], v[56:59]\n\t" \
  "ds_read_b128 a[4:7], v221 offset:0\n\t" \
  "v_cvt_pk_bf16_f32 v17, v2, v3\n\t" \
  "v_mfma_f32_16x16x32_bf16 v[52:55], a[148:151], a[228:231], v[52:55]\n\t" \
  "v_cvt_pk_bf16_f32 v63, v62, v63\n\t" \
  "v_cvt_pk_bf16_f32 v62, v60, v61\n\t" \
  "v_mfma_f32_16x16x32_bf16 v[52:55], a[84:87], a[244:247], v[52:55]\n\t" \
  "v_permlane16_swap_b32 v21, v23\n\t" \
  "v_mfma_f32_16x16x32_bf16 v[48:51], a[144:147], a[228:231], v[48:51]\n\t" \
  "v_cvt_pk_bf16_f32 v60, v56, v57\n\t" \
  "v_cvt_pk_bf16_f32 v61, v58, v59\n\t" \
  "v_mfma_f32_16x16x32_bf16 v[48:51], a[80:83], a[244:247], v[48:51]\n\t" \
  "ds_read_b128 a[8:11], v222 offset:0\n\t" \
  "v_cmp_gt_u32 vcc, 16, v233\n\t" \
  "v_mfma_f32_16x16x32_bf16 v[44:47], a[140:143], a[228:231], v[44:47]\n\t" \
  "v_cvt_pk_bf16_f32 v59, v54, v55\n\t" \
  "v_cvt_pk_bf16_f32 v58, v52, v53\n\t" \
  "v_mfma_f32_16x16x32_bf16 v[44:47], a[76:79], a[244:247], v[44:47]\n\t" \
  "ds_read_b128 a[12:15], v223 offset:0\n\t" \
  "v_bfrev_b32 v214, 1\n\t" \
  "v_mfma_f32_16x16x32_bf16 v[40:43], a[136:139], a[228:231], v[40:43]\n\t" \
  "v_cvt_pk_bf16_f32 v56, v48, v49\n\t" \
  "v_cvt_pk_bf16_f32 v57, v50, v51\n\t" \
  "v_mfma_f32_16x16x32_bf16 v[40:43], a[72:75], a[244:247], v[40:43]\n\t" \
  "s_add_u32 s99, %[pf4], 0\n\t" \
  "ds_read_b128 a[16:19], v220 offset:4096\n\t" \
  "v_cvt_pk_bf16_f32 v55, v46, v47\n\t" \
  "v_mfma_f32_16x16x32_bf16 v[36:39], a[132:135], a[228:231], v[36:39]\n\t" \
  "ds_read_b128 a[20:23], v221 offset:4096\n\t" \
  "v_cvt_pk_bf16_f32 v54, v44, v45\n\t" \
  "v_mfma_f32_16x16x32_bf16 v[36:39], a[68:71], a[244:247], v[36:39]\n\t" \
  "ds_read_b128 a[24:27], v222 offset:4096\n\t" \
  "v_cvt_pk_bf16_f32 v52, v40, v41\n\t" \
  "v_mfma_f32_16x16x32_bf16 v[32:35], a[128:131], a[228:231], v[32:35]\n\t" \
  "buffer_load_dword v246, v245, s[72:75], s99 offen\n\t" \
  "v_mfma_f32_16x16x32_bf16 v[32:35], a[64:67], a[244:247], v[32:35]\n\t" \
  "v_cvt_pk_bf16_f32 v53, v42, v43\n\t" \
  "v_permlane16_swap_b32 v16, v18\n\t" \
  "v_mfma_f32_16x16x32_bf16 v[92:95], a[156:159], a[232:235], v[92:95]\n\t" \
  "v_cvt_pk_bf16_f32 v51, v38, v39\n\t" \
  "v_cvt_pk_bf16_f32 v50, v36, v37\n\t" \
  "v_mfma_f32_16x16x32_bf16 v[92:95], a[92:95], a[248:251], v[92:95]\n\t" \
  "ds_read_b128 a[28:31], v223 offset:4096\n\t" \
  "v_permlane16_swap_b32 v17, v19\n\t" \
  "v_mfma_f32_16x16x32_bf16 v[88:91], a[152:155], a[232:235], v[88:91]\n\t" \
  "v_cvt_pk_bf16_f32 v48, v32, v33\n\t" \
  "v_permlane16_swap_b32 v60, v62\n\t" \
  "v_mfma_f32_16x16x32_bf16 v[88:91], a[88:91], a[248:251], v[88:91]\n\t" \
  "ds_read_b128 a[32:35], v220 offset:8192\n\t" \
  "v_cvt_pk_bf16_f32 v49, v34, v35\n\t" \
  "v_mfma_f32_16x16x32_bf16 v[84:87], a[148:151], a[232:235], v[84:87]\n\t" \
  "v_cvt_pk_bf16_f32 v95, v94, v95\n\t" \
  "v_cvt_pk_bf16_f32 v94, v92, v93\n\t" \
  "v_mfma_f32_16x16x32_bf16 v[84:87], a[84:87], a[248:251], v[84:87]\n\t" \
  "ds_read_b128 a[36:39], v221 offset:8192\n\t" \
  "v_permlane16_swap_b32 v61, v63\n\t" \
  "v_mfma_f32_16x16x32_bf16 v[80:83], a[144:147], a[232:235], v[80:83]\n\t" \
  "v_cvt_pk_bf16_f32 v92, v88, v89\n\t" \
  "v_cvt_pk_bf16_f32 v93, v90, v91\n\t" \
  "v_mfma_f32_16x16x32_bf16 v[80:83], a[80:83], a[248:251], v[80:83]\n\t" \
  "ds_read_b128 a[40:43], v222 offset:8192\n\t" \
  "v_permlane16_swap_b32 v56, v58\n\t" \
  "v_cvt_pk_bf16_f32 v91, v86, v87\n\t" \
  "v_mfma_f32_16x16x32_bf16 v[76:79], a[140:143], a[232:235], v[76:79]\n\t" \
  "buffer_load_dword v246, v247, %[ws], 0 offen sc1\n\t" \
  "v_mfma_f32_16x16x32_bf16 v[76:79], a[76:79], a[248:251], v[76:79]\n\t" \
  "ds_read_b128 a[44:47], v223 offset:8192\n\t" \
  "v_permlane16_swap_b32 v57, v59\n\t" \
  "v_mfma_f32_16x16x32_bf16 v[72:75], a[136:139], a[232:235], v[72:75]\n\t" \
  "v_cvt_pk_bf16_f32 v90, v84, v85\n\t" \
  "v_cvt_pk_bf16_f32 v88, v80, v81\n\t" \
  "v_mfma_f32_16x16x32_bf16 v[72:75], a[72:75], a[248:251], v[72:75]\n\t" \
  "ds_read_b128 a[48:51], v220 offset:12288\n\t" \
  "v_permlane16_swap_b32 v52, v54\n\t" \
  "v_mfma_f32_16x16x32_bf16 v[68:71], a[132:135], a[232:235], v[68:71]\n\t" \
  "ds_read_b128 a[52:55], v221 offset:12288\n\t" \
  "v_cvt_pk_bf16_f32 v89, v82, v83\n\t" \
  "v_mfma_f32_16x16x32_bf16 v[68:71], a[68:71], a[248:251], v[68:71]\n\t" \
  "v_cvt_pk_bf16_f32 v87, v78, v79\n\t" \
  "v_permlane16_swap_b32 v53, v55\n\t" \
  "v_mfma_f32_16x16x32_bf16 v[64:67], a[128:131], a[232:235], v[64:67]\n\t" \
  "ds_read_b128 a[56:59], v222 offset:12288\n\t" \
  "v_cvt_pk_bf16_f32 v86, v76, v77\n\t" \
  "v_mfma_f32_16x16x32_bf16 v[64:67], a[64:67], a[248:251], v[64:67]\n\t" \
  "v_cvt_pk_bf16_f32 v84, v72, v73\n\t" \
  "v_permlane16_swap_b32 v48, v50\n\t" \
  "v_mfma_f32_16x16x32_bf16 v[124:127], a[156:159], a[236:239], v[124:127]\n\t" \
  "ds_read_b128 a[60:63], v223 offset:12288\n\t" \
  "v_cvt_pk_bf16_f32 v85, v74, v75\n\t" \
  "v_mfma_f32_16x16x32_bf16 v[124:127], a[92:95], a[252:255], v[124:127]\n\t" \
  "v_cvt_pk_bf16_f32 v83, v70, v71\n\t" \
  "v_permlane16_swap_b32 v49, v51\n\t" \
  "v_mfma_f32_16x16x32_bf16 v[120:123], a[152:155], a[236:239], v[120:123]\n\t" \
  "v_cvt_pk_bf16_f32 v82, v68, v69\n\t" \
  "v_cvt_pk_bf16_f32 v80, v64, v65\n\t" \
  "v_mfma_f32_16x16x32_bf16 v[120:123], a[88:91], a[252:255], v[120:123]\n\t" \
  "v_cvt_pk_bf16_f32 v81, v66, v67\n\t" \
  "v_permlane16_swap_b32 v92, v94\n\t" \
  "v_mfma_f32_16x16x32_bf16 v[116:119], a[148:151], a[236:239], v[116:119]\n\t" \
  "v_cvt_pk_bf16_f32 v127, v126, v127\n\t" \
  "v_cvt_pk_bf16_f32 v126, v124, v125\n\t" \
  "v_mfma_f32_16x16x32_bf16 v[116:119], a[84:87], a[252:255], v[116:119]\n\t" \
  "v_permlane16_swap_b32 v93, v95\n\t" \
  "v_mfma_f32_16x16x32_bf16 v[112:115], a[144:147], a[236:239], v[112:115]\n\t" \
  "v_cvt_pk_bf16_f32 v124, v120, v121\n\t" \
  "v_permlane16_swap_b32 v88, v90\n\t" \
  "v_mfma_f32_16x16x32_bf16 v[112:115], a[80:83], a[252:255], v[112:115]\n\t" \
  "v_permlane16_swap_b32 v89, v91\n\t" \
  "v_mfma_f32_16x16x32_bf16 v[108:111], a[140:143], a[236:239], v[108:111]\n\t" \
  "v_cvt_pk_bf16_f32 v125, v122, v123\n\t" \
  "v_cvt_pk_bf16_f32 v123, v118, v119\n\t" \
  "v_mfma_f32_16x16x32_bf16 v[108:111], a[76:79], a[252:255], v[108:111]\n\t" \
  "v_cvt_pk_bf16_f32 v122, v116, v117\n\t" \
  "v_permlane16_swap_b32 v84, v86\n\t" \
  "v_mfma_f32_16x16x32_bf16 v[104:107], a[136:139], a[236:239], v[104:107]\n\t" \
  "v_cvt_pk_bf16_f32 v120, v112, v113\n\t" \
  "v_cvt_pk_bf16_f32 v121, v114, v115\n\t" \
  "v_mfma_f32_16x16x32_bf16 v[104:107], a[72:75], a[252:255], v[104:107]\n\t" \
  "v_permlane16_swap_b32 v85, v87\n\t" \
  "v_mfma_f32_16x16x32_bf16 v[100:103], a[132:135], a[236:239], v[100:103]\n\t" \
  "s_add_u32 m0, %[m], 49152\n\t" \
  "v_permlane16_swap_b32 v80, v82\n\t" \
  "v_mfma_f32_16x16x32_bf16 v[100:103], a[68:71], a[252:255], v[100:103]\n\t" \
  "s_add_u32 s96, %[m], 32768\n\t" \
  "v_permlane16_swap_b32 v81, v83\n\t" \
  "v_mfma_f32_16x16x32_bf16 v[96:99], a[128:131], a[236:239], v[96:99]\n\t" \
  "s_waitcnt lgkmcnt(0)\n\t" \
  "v_cvt_pk_bf16_f32 v119, v110, v111\n\t" \
  "v_cvt_pk_bf16_f32 v118, v108, v109\n\t" \
  "v_mfma_f32_16x16x32_bf16 v[96:99], a[64:67], a[252:255], v[96:99]\n\t" \
  "s_barrier\n\t" \
  STS("s_memrealtime s[94:95]\n\t") \
  STS("s_memtime s[92:93]\n\t") \
  "v_mfma_f32_16x16x32_bf16 v[0:3], a[0:3], v[16:19], 0\n\t" \
  "buffer_load_dwordx4 v224, s[60:63], s96 offen offset:0 lds\n\t" \
  "v_mfma_f32_16x16x32_bf16 v[0:3], a[4:7], v[20:23], v[0:3]\n\t" \
  "v_permlane16_swap_b32 v124, v126\n\t" \
  "v_mfma_f32_16x16x32_bf16 v[0:3], a[8:11], v[24:27], v[0:3]\n\t" \
  "ds_read_b128 a[192:195], v220 offset:16384\n\t" \
  "v_cvt_pk_bf16_f32 v116, v104, v105\n\t" \
  "v_mfma_f32_16x16x32_bf16 v[0:3], a[12:15], v[28:31], v[0:3]\n\t" \
  "v_permlane16_swap_b32 v125, v127\n\t" \
  "v_mfma_f32_16x16x32_bf16 v[32:35], a[0:3], v[48:51], 0\n\t" \
  "ds_read_b128 a[196:199], v221 offset:16384\n\t" \
  "v_cvt_pk_bf16_f32 v117, v106, v107\n\t" \
  "v_mfma_f32_16x16x32_bf16 v[32:35], a[4:7], v[52:55], v[32:35]\n\t" \
  "v_cvt_pk_bf16_f32 v115, v102, v103\n\t" \
  "v_cvt_pk_bf16_f32 v114, v100, v101\n\t" \
  "s_nop 0\n\t" \
  "v_max3_f32 v194, v0, v1, v2\n\t" \
  "v_max_f32 v194, v194, v3\n\t" \
  "v_mfma_f32_16x16x32_bf16 v[32:35], a[8:11], v[56:59], v[32:35]\n\t" \
  "v_mov_b32 v195, v194\n\t" \
  "v_cvt_pk_bf16_f32 v112, v96, v97\n\t" \
  "s_nop 0\n\t" \
  "v_permlane32_swap_b32 v194, v195\n\t" \
  "v_mfma_f32_16x16x32_bf16 v[32:35], a[12:15], v[60:63], v[32:35]\n\t" \
  "v_permlane16_swap_b32 v120, v122\n\t" \
  "v_max_f32 v194, v194, v195\n\t" \
  "v_mov_b32 v195, v194\n\t" \
  "s_nop 1\n\t" \
  "v_permlane16_swap_b32 v194, v195\n\t" \
  "v_max_f32 v194, v194, v195\n\t" \
  "v_sub_f32 v176, 0xc2000000, v194\n\t" \
  "v_sub_f32 v177, 0xc2000000, v194\n\t" \
  "v_sub_f32 v178, 0xc2000000, v194\n\t" \
  "v_sub_f32 v179, 0xc2000000, v194\n\t" \
  "s_nop 1\n\t" \
  "v_mfma_f32_16x16x32_bf16 v[4:7], a[16:19], v[16:19], v[176:179]\n\t" \
  "v_add_f32 v0, v0, v176\n\t" \
  "v_add_f32 v1, v1, v176\n\t" \
  "v_add_f32 v2, v2, v176\n\t" \
  "v_add_f32 v3, v3, v176\n\t" \
  "v_mfma_f32_16x16x32_bf16 v[4:7], a[20:23], v[20:23], v[4:7]\n\t" \
  "v_max3_f32 v194, v32, v33, v34\n\t" \
  "v_max_f32 v194, v194, v35\n\t" \
  "v_mov_b32 v195, v194\n\t" \
  "v_exp_f32 v0, v0\n\t" \
  "v_mfma_f32_16x16x32_bf16 v[4:7], a[24:27], v[24:27], v[4:7]\n\t" \
  "buffer_load_dwordx4 v225, s[60:63], s96 offen offset:1024 lds\n\t" \
  "v_permlane32_swap_b32 v194, v195\n\t" \
  "v_max_f32 v194, v194, v195\n\t" \
  "v_mfma_f32_16x16x32_bf16 v[4:7], a[28:31], v[28:31], v[4:7]\n\t" \
  "v_mov_b32 v195, v194\n\t" \
  "v_exp_f32 v1, v1\n\t" \
  "v_exp_f32 v2, v2\n\t" \
  "v_mfma_f32_16x16x32_bf16 v[8:11], a[32:35], v[16:19], v[176:179]\n\t" \
  STS("v_writelane_b32 %[stv], s94, 18\n\t") \
  STS("v_writelane_b32 %[stv], s92, 19\n\t") \
  "v_permlane16_swap_b32 v194, v195\n\t" \
  "v_max_f32 v194, v194, v195\n\t" \
  "v_sub_f32 v180, 0xc2000000, v194\n\t" \
  "v_sub_f32 v181, 0xc2000000, v194\n\t" \
  "v_mfma_f32_16x16x32_bf16 v[8:11], a[36:39], v[20:23], v[8:11]\n\t" \
  "v_sub_f32 v182, 0xc2000000, v194\n\t" \
  "v_sub_f32 v183, 0xc2000000, v194\n\t" \
  "v_add_f32 v32, v32, v180\n\t" \
  "v_exp_f32 v3, v3\n\t" \
  "v_mfma_f32_16x16x32_bf16 v[8:11], a[40:43], v[24:27], v[8:11]\n\t" \
  "s_add_u32 s99, %[pf4], 14336\n\t" \
  "v_add_f32 v33, v33, v180\n\t" \
  "v_exp_f32 v4, v4\n\t" \
  "v_exp_f32 v5, v5\n\t" \
  "v_mfma_f32_16x16x32_bf16 v[8:11], a[44:47], v[28:31], v[8:11]\n\t" \
  "v_exp_f32 v6, v6\n\t" \
  "v_exp_f32 v7, v7\n\t" \
  "v_add_f32 v34, v34, v180\n\t" \
  "v_mfma_f32_16x16x32_bf16 v[12:15], a[48:51], v[16:19], v[176:179]\n\t" \
  "buffer_load_dword v246, v245, s[72:75], s99 offen\n\t" \
  "v_mfma_f32_16x16x32_bf16 v[12:15], a[52:55], v[20:23], v[12:15]\n\t" \
  "ds_read_b128 a[200:203], v222 offset:16384\n\t" \
  "v_cvt_pk_bf16_f32 v113, v98, v99\n\t" \
  "v_add_f32 v35, v35, v180\n\t" \
  "v_exp_f32 v8, v8\n\t" \
  "v_mfma_f32_16x16x32_bf16 v[12:15], a[56:59], v[24:27], v[12:15]\n\t" \
  "ds_read_b128 a[204:207], v223 offset:16384\n\t" \
  "v_exp_f32 v9, v9\n\t" \
  "v_exp_f32 v10, v10\n\t" \
  "v_mfma_f32_16x16x32_bf16 v[12:15], a[60:63], v[28:31], v[12:15]\n\t" \
  "buffer_load_dwordx4 v226, s[60:63], s96 offen offset:2048 lds\n\t" \
  "v_exp_f32 v11, v11\n\t" \
  "v_mfma_f32_16x16x32_bf16 v[64:67], a[0:3], v[80:83], 0\n\t" \
  ".Lhs5:\n\ts_cbranch_execz .Lhs5n\n\t" \
  "ds_read_b128 a[208:211], v220 offset:20480\n\t" \
  "v_permlane16_swap_b32 v121, v123\n\t" \
  "v_mfma_f32_16x16x32_bf16 v[64:67], a[4:7], v[84:87], v[64:67]\n\t" \
  "ds_read_b128 a[212:215], v221 offset:20480\n\t" \
  "v_permlane16_swap_b32 v116, v118\n\t" \
  "v_exp_f32 v12, v12\n\t" \
  "v_mfma_f32_16x16x32_bf16 v[64:67], a[8:11], v[88:91], v[64:67]\n\t" \
  "ds_read_b128 a[216:219], v222 offset:20480\n\t" \
  "v_exp_f32 v13, v13\n\t" \
  "v_exp_f32 v14, v14\n\t" \
  "v_mfma_f32_16x16x32_bf16 v[64:67], a[12:15], v[92:95], v[64:67]\n\t" \
  "ds_read_b128 a[220:223], v223 offset:20480\n\t" \
  "v_exp_f32 v15, v15\n\t" \
  "v_exp_f32 v32, v32\n\t" \
  "v_mfma_f32_16x16x32_bf16 v[36:39], a[16:19], v[48:51], v[180:183]\n\t" \
  "v_exp_f32 v33, v33\n\t" \
  "v_exp_f32 v34, v34\n\t" \
  "v_mfma_f32_16x16x32_bf16 v[36:39], a[20:23], v[52:55], v[36:39]\n\t" \
  "s_nop 0\n\t" \
  "v_max3_f32 v194, v64, v65, v66\n\t" \
  "v_max_f32 v194, v194, v67\n\t" \
  "v_mov_b32 v195, v194\n\t" \
  "v_exp_f32 v35, v35\n\t" \
  "v_mfma_f32_16x16x32_bf16 v[36:39], a[24:27], v[56:59], v[36:39]\n\t" \
  "ds_read_b128 a[224:227], v220 offset:24576\n\t" \
  "v_permlane32_swap_b32 v194, v195\n\t" \
  "v_max_f32 v194, v194, v195\n\t" \
  "v_mov_b32 v195, v194\n\t" \
  "v_mfma_f32_16x16x32_bf16 v[36:39], a[28:31], v[60:63], v[36:39]\n\t" \
  "s_nop 0\n\t" \
  "v_permlane16_swap_b32 v194, v195\n\t" \
  "v_max_f32 v194, v194, v195\n\t" \
  "v_sub_f32 v184, 0xc2000000, v194\n\t" \
  "v_sub_f32 v185, 0xc2000000, v194\n\t" \
  "v_mfma_f32_16x16x32_bf16 v[40:43], a[32:35], v[48:51], v[180:183]\n\t" \
  "buffer_load_dwordx4 v227, s[60:63], s96 offen offset:3072 lds\n\t" \
  "v_sub_f32 v186, 0xc2000000, v194\n\t" \
  "v_sub_f32 v187, 0xc2000000, v194\n\t" \
  "v_mfma_f32_16x16x32_bf16 v[40:43], a[36:39], v[52:55], v[40:43]\n\t" \
  "v_add_f32 v64, v64, v184\n\t" \
  "v_permlane16_swap_b32 v117, v119\n\t" \
  "v_exp_f32 v36, v36\n\t" \
  "v_mfma_f32_16x16x32_bf16 v[40:43], a[40:43], v[56:59], v[40:43]\n\t" \
  "v_add_f32 v65, v65, v184\n\t" \
  "v_exp_f32 v37, v37\n\t" \
  "v_exp_f32 v38, v38\n\t" \
  "v_mfma_f32_16x16x32_bf16 v[40:43], a[44:47], v[60:63], v[40:43]\n\t" \
  "v_add_f32 v66, v66, v184\n\t" \
  "v_exp_f32 v39, v39\n\t" \
  "v_permlane16_swap_b32 v112, v114\n\t" \
  "v_mfma_f32_16x16x32_bf16 v[44:47], a[48:51], v[48:51], v[180:183]\n\t" \
  "s_add_u32 m0, %[m], 147456\n\t" \
  "v_add_f32 v67, v67, v184\n\t" \
  "v_permlane16_swap_b32 v113, v115\n\t" \
  "v_mfma_f32_16x16x32_bf16 v[44:47], a[52:55], v[52:55], v[44:47]\n\t" \
  "s_add_u32 s96, %[m], 16384\n\t" \
  "ds_read_b128 a[228:231], v221 offset:24576\n\t" \
  "v_exp_f32 v40, v40\n\t" \
  "v_mfma_f32_16x16x32_bf16 v[44:47], a[56:59], v[56:59], v[44:47]\n\t" \
  "ds_read_b128 a[232:235], v222 offset:24576\n\t" \
  "v_exp_f32 v41, v41\n\t" \
  "v_mfma_f32_16x16x32_bf16 v[44:47], a[60:63], v[60:63], v[44:47]\n\t" \
  "v_exp_f32 v42, v42\n\t" \
  "v_mfma_f32_16x16x32_bf16 v[96:99], a[0:3], v[112:115], 0\n\t" \
  "buffer_load_dwordx4 v230, s[64:67], s96 offen offset:0 lds\n\t" \
  "v_mfma_f32_16x16x32_bf16 v[96:99], a[4:7], v[116:119], v[96:99]\n\t" \
  "ds_read_b128 a[236:239], v223 offset:24576\n\t" \
  "v_exp_f32 v43, v43\n\t" \
  "v_mfma_f32_16x16x32_bf16 v[96:99], a[8:11], v[120:123], v[96:99]\n\t" \
  "v_cvt_pk_bf16_f32 v128, v0, v1\n\t" \
  "v_exp_f32 v44, v44\n\t" \
  "v_mfma_f32_16x16x32_bf16 v[96:99], a[12:15], v[124:127], v[96:99]\n\t" \
  "v_exp_f32 v45, v45\n\t" \
  "v_mfma_f32_16x16x32_bf16 v[68:71], a[16:19], v[80:83], v[184:187]\n\t" \
  "v_exp_f32 v46, v46\n\t" \
  "v_mfma_f32_16x16x32_bf16 v[68:71], a[20:23], v[84:87], v[68:71]\n\t" \
  "s_nop 3\n\t" \
  "v_max3_f32 v194, v96, v97, v98\n\t" \
  "v_mfma_f32_16x16x32_bf16 v[68:71], a[24:27], v[88:91], v[68:71]\n\t" \
  "ds_read_b128 a[240:243], v220 offset:28672\n\t" \
  "v_cvt_pk_bf16_f32 v129, v2, v3\n\t" \
  "v_max_f32 v194, v194, v99\n\t" \
  "v_mov_b32 v195, v194\n\t" \
  "v_mfma_f32_16x16x32_bf16 v[68:71], a[28:31], v[92:95], v[68:71]\n\t" \
  "v_exp_f32 v47, v47\n\t" \
  "v_permlane32_swap_b32 v194, v195\n\t" \
  "v_mfma_f32_16x16x32_bf16 v[72:75], a[32:35], v[80:83], v[184:187]\n\t" \
  "v_max_f32 v194, v194, v195\n\t" \
  "v_mov_b32 v195, v194\n\t" \
  "v_mfma_f32_16x16x32_bf16 v[72:75], a[36:39], v[84:87], v[72:75]\n\t" \
  "ds_read_b128 a[244:247], v221 offset:28672\n\t" \
  "v_permlane16_swap_b32 v194, v195\n\t" \
  "v_mfma_f32_16x16x32_bf16 v[72:75], a[40:43], v[88:91], v[72:75]\n\t" \
  "v_max_f32 v194, v194, v195\n\t" \
  "v_sub_f32 v188, 0xc2000000, v194\n\t" \
  "v_mfma_f32_16x16x32_bf16 v[72:75], a[44:47], v[92:95], v[72:75]\n\t" \
  "v_sub_f32 v189, 0xc2000000, v194\n\t" \
  "v_sub_f32 v190, 0xc2000000, v194\n\t" \
  "v_mfma_f32_16x16x32_bf16 v[76:79], a[48:51], v[80:83], v[184:187]\n\t" \
  "buffer_load_dwordx4 v231, s[64:67], s96 offen offset:1024 lds\n\t" \
  "v_mfma_f32_16x16x32_bf16 v[76:79], a[52:55], v[84:87], v[76:79]\n\t" \
  "v_sub_f32 v191, 0xc2000000, v194\n\t" \
  "v_add_f32 v96, v96, v188\n\t" \
  "v_mfma_f32_16x16x32_bf16 v[76:79], a[56:59], v[88:91], v[76:79]\n\t" \
  "v_add_f32 v97, v97, v188\n\t" \
  "v_exp_f32 v64, v64\n\t" \
  "v_mfma_f32_16x16x32_bf16 v[76:79], a[60:63], v[92:95], v[76:79]\n\t" \
  "ds_read_b128 a[248:251], v222 offset:28672\n\t" \
  "v_exp_f32 v65, v65\n\t" \
  "v_mfma_f32_16x16x32_bf16 v[100:103], a[16:19], v[112:115], v[188:191]\n\t" \
  "v_add_f32 v98, v98, v188\n\t" \
  "v_add_f32 v99, v99, v188\n\t" \
  "v_mfma_f32_16x16x32_bf16 v[100:103], a[20:23], v[116:119], v[100:103]\n\t" \
  "ds_read_b128 a[252:255], v223 offset:28672\n\t" \
  "v_exp_f32 v66, v66\n\t" \
  "v_mfma_f32_16x16x32_bf16 v[100:103], a[24:27], v[120:123], v[100:103]\n\t" \
  "buffer_load_dwordx4 v230, s[64:67], s96 offen offset:2048 lds\n\t" \
  "v_mfma_f32_16x16x32_bf16 v[100:103], a[28:31], v[124:127], v[100:103]\n\t" \
  "v_cvt_pk_bf16_f32 v130, v4, v5\n\t" \
  "v_exp_f32 v67, v67\n\t" \
  "v_mfma_f32_16x16x32_bf16 v[104:107], a[32:35], v[112:115], v[188:191]\n\t" \
  "v_cvt_pk_bf16_f32 v131, v6, v7\n\t" \
  "v_cndmask_b32 v212, v214, v212, vcc\n\t" \
  "v_mfma_f32_16x16x32_bf16 v[104:107], a[36:39], v[116:119], v[104:107]\n\t" \
  "v_cvt_pk_bf16_f32 v132, v8, v9\n\t" \
  "v_exp_f32 v68, v68\n\t" \
  "v_mfma_f32_16x16x32_bf16 v[104:107], a[40:43], v[120:123], v[104:107]\n\t" \
  "v_exp_f32 v69, v69\n\t" \
  "v_mfma_f32_16x16x32_bf16 v[104:107], a[44:47], v[124:127], v[104:107]\n\t" \
  "v_cvt_pk_bf16_f32 v133, v10, v11\n\t" \
  "v_cvt_pk_bf16_f32 v134, v12, v13\n\t" \
  "v_mfma_f32_16x16x32_bf16 v[108:111], a[48:51], v[112:115], v[188:191]\n\t" \
  "v_exp_f32 v70, v70\n\t" \
  "v_mfma_f32_16x16x32_bf16 v[108:111], a[52:55], v[116:119], v[108:111]\n\t" \
  "v_exp_f32 v71, v71\n\t" \
  "v_mfma_f32_16x16x32_bf16 v[108:111], a[56:59], v[120:123], v[108:111]\n\t" \
  "v_cvt_pk_bf16_f32 v135, v14, v15\n\t" \
  "v_cvt_pk_bf16_f32 v136, v32, v33\n\t" \
  "v_mfma_f32_16x16x32_bf16 v[108:111], a[60:63], v[124:127], v[108:111]\n\t" \
  "s_waitcnt lgkmcnt(0)\n\t" \
  "buffer_load_dwordx4 v231, s[64:67], s96 offen offset:3072 lds\n\t" \
  STA("s_memrealtime s[94:95]\n\t") \
  STA("s_memtime s[92:93]\n\t") \
  "v_mfma_f32_16x16x32_bf16 v[0:3], a[192:195], v[16:19], v[176:179]\n\t" \
  "s_add_u32 s99, %[pf4], 28672\n\t" \
  "ds_read_b64_tr_b16 a[128:129], v228 offset:0\n\t" \
  "v_cvt_pk_bf16_f32 v137, v34, v35\n\t" \
  "v_mfma_f32_16x16x32_bf16 v[0:3], a[196:199], v[20:23], v[0:3]\n\t" \
  "v_exp_f32 v72, v72\n\t" \
  "v_mfma_f32_16x16x32_bf16 v[0:3], a[200:203], v[24:27], v[0:3]\n\t" \
  "buffer_load_dword v246, v245, s[72:75], s99 offen\n\t" \
  "v_mfma_f32_16x16x32_bf16 v[0:3], a[204:207], v[28:31], v[0:3]\n\t" \
  "ds_read_b64_tr_b16 a[130:131], v228 offset:4096\n\t" \
  "v_cvt_pk_bf16_f32 v138, v36, v37\n\t" \
  "v_mfma_f32_16x16x32_bf16 v[4:7], a[220:223], v[28:31], v[176:179]\n\t" \
  "ds_read_b64_tr_b16 a[132:133], v228 offset:8192\n\t" \
  "v_cvt_pk_bf16_f32 v139, v38, v39\n\t" \
  "v_mfma_f32_16x16x32_bf16 v[4:7], a[216:219], v[24:27], v[4:7]\n\t" \
  "v_exp_f32 v73, v73\n\t" \
  "v_mfma_f32_16x16x32_bf16 v[4:7], a[212:215], v[20:23], v[4:7]\n\t" \
  "ds_read_b64_tr_b16 a[134:135], v228 offset:12288\n\t" \
  "v_cvt_pk_bf16_f32 v140, v40, v41\n\t" \
  "v_mfma_f32_16x16x32_bf16 v[4:7], a[208:211], v[16:19], v[4:7]\n\t" \
  "ds_read_b64_tr_b16 a[136:137], v229 offset:0\n\t" \
  "v_cvt_pk_bf16_f32 v141, v42, v43\n\t" \
  "v_mfma_f32_16x16x32_bf16 v[8:11], a[224:227], v[16:19], v[176:179]\n\t" \
  "v_exp_f32 v74, v74\n\t" \
  "v_mfma_f32_16x16x32_bf16 v[8:11], a[228:231], v[20:23], v[8:11]\n\t" \
  "ds_read_b64_tr_b16 a[138:139], v229 offset:4096\n\t" \
  "v_cvt_pk_bf16_f32 v142, v44, v45\n\t" \
  "v_mfma_f32_16x16x32_bf16 v[8:11], a[232:235], v[24:27], v[8:11]\n\t" \
  "v_exp_f32 v75, v75\n\t" \
  "v_mfma_f32_16x16x32_bf16 v[8:11], a[236:239], v[28:31], v[8:11]\n\t" \
  "ds_read_b64_tr_b16 a[140:141], v229 offset:8192\n\t" \
  "v_lshl_add_u32 v214, v233, 14, %[cto]\n\t" \
  "v_mfma_f32_16x16x32_bf16 v[12:15], a[252:255], v[28:31], v[176:179]\n\t" \
  STA("v_writelane_b32 %[stv], s94, 26\n\t") \
  STA("v_writelane_b32 %[stv], s92, 27\n\t") \
  "v_exp_f32 v76, v76\n\t" \
  "v_mfma_f32_16x16x32_bf16 v[12:15], a[248:251], v[24:27], v[12:15]\n\t" \
  "ds_read_b64_tr_b16 a[142:143], v229 offset:12288\n\t" \
  "v_min_u32 v214, .Lfz_z-.Lat_t0-4, v214\n\t" \
  "v_mfma_f32_16x16x32_bf16 v[12:15], a[244:247], v[20:23], v[12:15]\n\t" \
  "s_getpc_b64 s[84:85]\n\t" \
  ".Lat_t0:\n\t" \
  "ds_read_b64_tr_b16 a[144:145], v228 offset:256\n\t" \
  "v_cvt_pk_bf16_f32 v143, v46, v47\n\t" \
  "v_mfma_f32_16x16x32_bf16 v[12:15], a[240:243], v[16:19], v[12:15]\n\t" \
  "global_load_dword v246, v214, s[84:85]\n\t" \
  "v_mfma_f32_16x16x32_bf16 v[32:35], a[192:195], v[48:51], v[180:183]\n\t" \
  "v_exp_f32 v77, v77\n\t" \
  "v_mfma_f32_16x16x32_bf16 v[32:35], a[196:199], v[52:55], v[32:35]\n\t" \
  "ds_read_b64_tr_b16 a[146:147], v228 offset:4352\n\t" \
  "v_cvt_pk_bf16_f32 v144, v64, v65\n\t" \
  "v_mfma_f32_16x16x32_bf16 v[32:35], a[200:203], v[56:59], v[32:35]\n\t" \
  "ds_read_b64_tr_b16 a[148:149], v228 offset:8448\n\t" \
  "v_exp_f32 v78, v78\n\t" \
  "v_mfma_f32_16x16x32_bf16 v[32:35], a[204:207], v[60:63], v[32:35]\n\t" \
  "v_exp_f32 v79, v79\n\t" \
  "v_mfma_f32_16x16x32_bf16 v[36:39], a[220:223], v[60:63], v[180:183]\n\t" \
  "ds_read_b64_tr_b16 a[150:151], v228 offset:12544\n\t" \
  "v_cvt_pk_bf16_f32 v145, v66, v67\n\t" \
  "v_mfma_f32_16x16x32_bf16 v[36:39], a[216:219], v[56:59], v[36:39]\n\t" \
  "v_exp_f32 v96, v96\n\t" \
  "v_mfma_f32_16x16x32_bf16 v[36:39], a[212:215], v[52:55], v[36:39]\n\t" \
  "ds_read_b64_tr_b16 a[152:153], v229 offset:256\n\t" \
  "v_cvt_pk_bf16_f32 v146, v68, v69\n\t" \
  "v_mfma_f32_16x16x32_bf16 v[36:39], a[208:211], v[48:51], v[36:39]\n\t" \
  "ds_read_b64_tr_b16 a[154:155], v229 offset:4352\n\t" \
  "v_exp_f32 v97, v97\n\t" \
  "v_mfma_f32_16x16x32_bf16 v[40:43], a[224:227], v[48:51], v[180:183]\n\t" \
  "v_exp_f32 v98, v98\n\t" \
  "v_mfma_f32_16x16x32_bf16 v[40:43], a[228:231], v[52:55], v[40:43]\n\t" \
  "ds_read_b64_tr_b16 a[156:157], v229 offset:8448\n\t" \
  "v_cvt_pk_bf16_f32 v147, v70, v71\n\t" \
  "v_mfma_f32_16x16x32_bf16 v[40:43], a[232:235], v[56:59], v[40:43]\n\t" \
  "v_exp_f32 v99, v99\n\t" \
  "v_mfma_f32_16x16x32_bf16 v[40:43], a[236:239], v[60:63], v[40:43]\n\t" \
  "ds_read_b64_tr_b16 a[158:159], v229 offset:12544\n\t" \
  "v_cvt_pk_bf16_f32 v148, v72, v73\n\t" \
  "v_mfma_f32_16x16x32_bf16 v[44:47], a[252:255], v[60:63], v[180:183]\n\t" \
  "ds_read_b64_tr_b16 a[160:161], v228 offset:512\n\t" \
  "v_cvt_pk_bf16_f32 v149, v74, v75\n\t" \
  "v_mfma_f32_16x16x32_bf16 v[44:47], a[248:251], v[56:59], v[44:47]\n\t" \
  "v_exp_f32 v100, v100\n\t" \
  "v_mfma_f32_16x16x32_bf16 v[44:47], a[244:247], v[52:55], v[44:47]\n\t" \
  "v_exp_f32 v101, v101\n\t" \
  "v_mfma_f32_16x16x32_bf16 v[44:47], a[240:243], v[48:51], v[44:47]\n\t" \
  "ds_read_b64_tr_b16 a[162:163], v228 offset:4608\n\t" \
  "v_cvt_pk_bf16_f32 v150, v76, v77\n\t" \
  "v_mfma_f32_16x16x32_bf16 v[64:67], a[192:195], v[80:83], v[184:187]\n\t" \
  "ds_read_b64_tr_b16 a[164:165], v228 offset:8704\n\t" \
  "v_cvt_pk_bf16_f32 v151, v78, v79\n\t" \
  "v_mfma_f32_16x16x32_bf16 v[64:67], a[196:199], v[84:87], v[64:67]\n\t" \
  "v_exp_f32 v102, v102\n\t" \
  "v_mfma_f32_16x16x32_bf16 v[64:67], a[200:203], v[88:91], v[64:67]\n\t" \
  "ds_read_b64_tr_b16 a[166:167], v228 offset:12800\n\t" \
  "v_cvt_pk_bf16_f32 v152, v96, v97\n\t" \
  "v_mfma_f32_16x16x32_bf16 v[64:67], a[204:207], v[92:95], v[64:67]\n\t" \
  "ds_read_b64_tr_b16 a[168:169], v229 offset:512\n\t" \
  "v_exp_f32 v103, v103\n\t" \
  "v_mfma_f32_16x16x32_bf16 v[68:71], a[220:223], v[92:95], v[184:187]\n\t" \
  "v_exp_f32 v104, v104\n\t" \
  "v_mfma_f32_16x16x32_bf16 v[68:71], a[216:219], v[88:91], v[68:71]\n\t" \
  "ds_read_b64_tr_b16 a[170:171], v229 offset:4608\n\t" \
  "v_cvt_pk_bf16_f32 v153, v98, v99\n\t" \
  "v_mfma_f32_16x16x32_bf16 v[68:71], a[212:215], v[84:87], v[68:71]\n\t" \
  "v_exp_f32 v105, v105\n\t" \
  "v_mfma_f32_16x16x32_bf16 v[68:71], a[208:211], v[80:83], v[68:71]\n\t" \
  "ds_read_b64_tr_b16 a[172:173], v229 offset:8704\n\t" \
  "v_cvt_pk_bf16_f32 v154, v100, v101\n\t" \
  "v_mfma_f32_16x16x32_bf16 v[72:75], a[224:227], v[80:83], v[184:187]\n\t" \
  "v_exp_f32 v106, v106\n\t" \
  "v_mfma_f32_16x16x32_bf16 v[72:75], a[228:231], v[84:87], v[72:75]\n\t" \
  "ds_read_b64_tr_b16 a[174:175], v229 offset:12800\n\t" \
  "v_cvt_pk_bf16_f32 v155, v102, v103\n\t" \
  "v_mfma_f32_16x16x32_bf16 v[72:75], a[232:235], v[88:91], v[72:75]\n\t" \
  "v_exp_f32 v107, v107\n\t" \
  "v_mfma_f32_16x16x32_bf16 v[72:75], a[236:239], v[92:95], v[72:75]\n\t" \
  "ds_read_b64_tr_b16 a[176:177], v228 offset:768\n\t" \
  "v_cvt_pk_bf16_f32 v156, v104, v105\n\t" \
  "v_mfma_f32_16x16x32_bf16 v[76:79], a[252:255], v[92:95], v[184:187]\n\t" \
  "v_exp_f32 v108, v108\n\t" \
  "v_mfma_f32_16x16x32_bf16 v[76:79], a[248:251], v[88:91], v[76:79]\n\t" \
  "ds_read_b64_tr_b16 a[178:179], v228 offset:4864\n\t" \
  "v_cvt_pk_bf16_f32 v157, v106, v107\n\t" \
  "v_mfma_f32_16x16x32_bf16 v[76:79], a[244:247], v[84:87], v[76:79]\n\t" \
  "v_exp_f32 v109, v109\n\t" \
  "v_mfma_f32_16x16x32_bf16 v[76:79], a[240:243], v[80:83], v[76:79]\n\t" \
  "ds_read_b64_tr_b16 a[180:181], v228 offset:8960\n\t" \
  "v_cvt_pk_bf16_f32 v158, v108, v109\n\t" \
  "v_mfma_f32_16x16x32_bf16 v[96:99], a[192:195], v[112:115], v[188:191]\n\t" \
  "v_exp_f32 v110, v110\n\t" \
  "v_mfma_f32_16x16x32_bf16 v[96:99], a[196:199], v[116:119], v[96:99]\n\t" \
  "s_add_u32 s99, %[pf4], 43008\n\t" \
  "v_exp_f32 v111, v111\n\t" \
  "v_mfma_f32_16x16x32_bf16 v[96:99], a[200:203], v[120:123], v[96:99]\n\t" \
  "ds_read_b64_tr_b16 a[182:183], v228 offset:13056\n\t" \
  "v_cvt_pk_bf16_f32 v159, v110, v111\n\t" \
  "v_mfma_f32_16x16x32_bf16 v[96:99], a[204:207], v[124:127], v[96:99]\n\t" \
  "v_exp_f32 v0, v0\n\t" \
  "v_mfma_f32_16x16x32_bf16 v[100:103], a[220:223], v[124:127], v[188:191]\n\t" \
  "buffer_load_dword v246, v245, s[72:75], s99 offen\n\t" \
  "v_mfma_f32_16x16x32_bf16 v[100:103], a[216:219], v[120:123], v[100:103]\n\t" \
  "ds_read_b64_tr_b16 a[184:185], v229 offset:768\n\t" \
  "v_mfma_f32_16x16x32_bf16 v[100:103], a[212:215], v[116:119], v[100:103]\n\t" \
  "ds_read_b64_tr_b16 a[186:187], v229 offset:4864\n\t" \
  "v_mfma_f32_16x16x32_bf16 v[100:103], a[208:211], v[112:115], v[100:103]\n\t" \
  "ds_read_b64_tr_b16 a[188:189], v229 offset:8960\n\t" \
  "v_mfma_f32_16x16x32_bf16 v[104:107], a[224:227], v[112:115], v[188:191]\n\t" \
  "v_exp_f32 v1, v1\n\t" \
  "v_mfma_f32_16x16x32_bf16 v[104:107], a[228:231], v[116:119], v[104:107]\n\t" \
  "ds_read_b64_tr_b16 a[190:191], v229 offset:13056\n\t" \
  "v_mfma_f32_16x16x32_bf16 v[104:107], a[232:235], v[120:123], v[104:107]\n\t" \
  "v_exp_f32 v2, v2\n\t" \
  "v_mfma_f32_16x16x32_bf16 v[104:107], a[236:239], v[124:127], v[104:107]\n\t" \
  "v_exp_f32 v3, v3\n\t" \
  "v_mfma_f32_16x16x32_bf16 v[108:111], a[252:255], v[124:127], v[188:191]\n\t" \
  "v_exp_f32 v4, v4\n\t" \
  "v_mfma_f32_16x16x32_bf16 v[108:111], a[248:251], v[120:123], v[108:111]\n\t" \
  "v_exp_f32 v5, v5\n\t" \
  "v_mfma_f32_16x16x32_bf16 v[108:111], a[244:247], v[116:119], v[108:111]\n\t" \
  "s_waitcnt vmcnt(3) lgkmcnt(0)\n\t" \
  "v_exp_f32 v6, v6\n\t" \
  "v_mfma_f32_16x16x32_bf16 v[108:111], a[240:243], v[112:115], v[108:111]\n\t" \
  "s_barrier\n\t" \
  STA("s_memrealtime s[94:95]\n\t") \
  STA("s_memtime s[92:93]\n\t") \
  "v_mfma_f32_16x16x32_bf16 a[0:3], a[128:131], v[128:131], 0\n\t" \
  "v_exp_f32 v7, v7\n\t" \
  "v_mfma_f32_16x16x32_bf16 a[0:3], a[132:135], v[132:135], a[0:3]\n\t" \
  "v_exp_f32 v8, v8\n\t" \
  "v_mfma_f32_16x16x32_bf16 a[4:7], a[136:139], v[128:131], 0\n\t" \
  "v_exp_f32 v9, v9\n\t" \
  "v_mfma_f32_16x16x32_bf16 a[4:7], a[140:143], v[132:135], a[4:7]\n\t" \
  "v_exp_f32 v10, v10\n\t" \
  "v_mfma_f32_16x16x32_bf16 a[8:11], a[144:147], v[128:131], 0\n\t" \
  "v_exp_f32 v11, v11\n\t" \
  "v_mfma_f32_16x16x32_bf16 a[8:11], a[148:151], v[132:135], a[8:11]\n\t" \
  "v_exp_f32 v12, v12\n\t" \
  "v_mfma_f32_16x16x32_bf16 a[12:15], a[152:155], v[128:131], 0\n\t" \
  "v_exp_f32 v13, v13\n\t" \
  "v_mfma_f32_16x16x32_bf16 a[12:15], a[156:159], v[132:135], a[12:15]\n\t" \
  "v_exp_f32 v14, v14\n\t" \
  "v_mfma_f32_16x16x32_bf16 a[16:19], a[160:163], v[128:131], 0\n\t" \
  "v_exp_f32 v15, v15\n\t" \
  "v_mfma_f32_16x16x32_bf16 a[16:19], a[164:167], v[132:135], a[16:19]\n\t" \
  "v_exp_f32 v32, v32\n\t" \
  "v_mfma_f32_16x16x32_bf16 a[20:23], a[168:171], v[128:131], 0\n\t" \
  "v_exp_f32 v33, v33\n\t" \
  "v_mfma_f32_16x16x32_bf16 a[20:23], a[172:175], v[132:135], a[20:23]\n\t" \
  "v_exp_f32 v34, v34\n\t" \
  "v_mfma_f32_16x16x32_bf16 a[24:27], a[176:179], v[128:131], 0\n\t" \
  STA("v_writelane_b32 %[stv], s94, 28\n\t") \
  STA("v_writelane_b32 %[stv], s92, 29\n\t") \
  "v_exp_f32 v35, v35\n\t" \
  "v_mfma_f32_16x16x32_bf16 a[24:27], a[180:183], v[132:135], a[24:27]\n\t" \
  "v_exp_f32 v36, v36\n\t" \
  "v_mfma_f32_16x16x32_bf16 a[28:31], a[184:187], v[128:131], 0\n\t" \
  "v_exp_f32 v37, v37\n\t" \
  "v_mfma_f32_16x16x32_bf16 a[28:31], a[188:191], v[132:135], a[28:31]\n\t" \
  "v_exp_f32 v38, v38\n\t" \
  "v_mfma_f32_16x16x32_bf16 v[160:163], v[216:219], v[128:131], 0\n\t" \
  "s_add_u32 m0, %[m], 65536\n\t" \
  "v_exp_f32 v39, v39\n\t" \
  "v_mfma_f32_16x16x32_bf16 v[160:163], v[216:219], v[132:135], v[160:163]\n\t" \
  "s_add_u32 s96, %[m], 49152\n\t" \
  "ds_read_b128 a[192:195], v220 offset:0\n\t" \
  "v_cvt_pk_bf16_f32 v128, v0, v1\n\t" \
  "v_mfma_f32_16x16x32_bf16 a[32:35], a[128:131], v[136:139], 0\n\t" \
  "ds_read_b128 a[196:199], v221 offset:0\n\t" \
  "v_cvt_pk_bf16_f32 v129, v2, v3\n\t" \
  "v_mfma_f32_16x16x32_bf16 a[32:35], a[132:135], v[140:143], a[32:35]\n\t" \
  "v_exp_f32 v40, v40\n\t" \
  "v_mfma_f32_16x16x32_bf16 a[36:39], a[136:139], v[136:139], 0\n\t" \
  "buffer_load_dwordx4 v224, s[60:63], s96 offen offset:0 lds\n\t" \
  "v_mfma_f32_16x16x32_bf16 a[36:39], a[140:143], v[140:143], a[36:39]\n\t" \
  "ds_read_b128 a[200:203], v222 offset:0\n\t" \
  "v_cvt_pk_bf16_f32 v130, v4, v5\n\t" \
  "v_mfma_f32_16x16x32_bf16 a[40:43], a[144:147], v[136:139], 0\n\t" \
  "v_exp_f32 v41, v41\n\t" \
  "v_mfma_f32_16x16x32_bf16 a[40:43], a[148:151], v[140:143], a[40:43]\n\t" \
  "ds_read_b128 a[204:207], v223 offset:0\n\t" \
  "v_cvt_pk_bf16_f32 v131, v6, v7\n\t" \
  "v_mfma_f32_16x16x32_bf16 a[44:47], a[152:155], v[136:139], 0\n\t" \
  "v_exp_f32 v42, v42\n\t" \
  "v_mfma_f32_16x16x32_bf16 a[44:47], a[156:159], v[140:143], a[44:47]\n\t" \
  "v_exp_f32 v43, v43\n\t" \
  "v_mfma_f32_16x16x32_bf16 a[48:51], a[160:163], v[136:139], 0\n\t" \
  "buffer_load_dwordx4 v225, s[60:63], s96 offen offset:1024 lds\n\t" \
  "v_mfma_f32_16x16x32_bf16 a[48:51], a[164:167], v[140:143], a[48:51]\n\t" \
  "ds_read_b128 a[208:211], v220 offset:4096\n\t" \
  "v_cvt_pk_bf16_f32 v132, v8, v9\n\t" \
  "v_mfma_f32_16x16x32_bf16 a[52:55], a[168:171], v[136:139], 0\n\t" \
  "v_exp_f32 v44, v44\n\t" \
  "v_mfma_f32_16x16x32_bf16 a[52:55], a[172:175], v[140:143], a[52:55]\n\t" \
  "ds_read_b128 a[212:215], v221 offset:4096\n\t" \
  "v_cvt_pk_bf16_f32 v133, v10, v11\n\t" \
  "v_mfma_f32_16x16x32_bf16 a[56:59], a[176:179], v[136:139], 0\n\t" \
  "v_exp_f32 v45, v45\n\t" \
  "v_mfma_f32_16x16x32_bf16 a[56:59], a[180:183], v[140:143], a[56:59]\n\t" \
  "v_exp_f32 v46, v46\n\t" \
  "v_mfma_f32_16x16x32_bf16 a[60:63], a[184:187], v[136:139], 0\n\t" \
  "buffer_load_dwordx4 v226, s[60:63], s96 offen offset:2048 lds\n\t" \
  "v_mfma_f32_16x16x32_bf16 a[60:63], a[188:191], v[140:143], a[60:63]\n\t" \
  "ds_read_b128 a[216:219], v222 offset:4096\n\t" \
  "v_cvt_pk_bf16_f32 v134, v12, v13\n\t" \
  "v_mfma_f32_16x16x32_bf16 v[164:167], v[216:219], v[136:139], 0\n\t" \
  "v_exp_f32 v47, v47\n\t" \
  "v_mfma_f32_16x16x32_bf16 v[164:167], v[216:219], v[140:143], v[164:167]\n\t" \
  "ds_read_b128 a[220:223], v223 offset:4096\n\t" \
  "v_cvt_pk_bf16_f32 v135, v14, v15\n\t" \
  "v_mfma_f32_16x16x32_bf16 a[64:67], a[128:131], v[144:147], 0\n\t" \
  "v_exp_f32 v64, v64\n\t" \
  "v_mfma_f32_16x16x32_bf16 a[64:67], a[132:135], v[148:151], a[64:67]\n\t" \
  "v_exp_f32 v65, v65\n\t" \
  "v_mfma_f32_16x16x32_bf16 a[68:71], a[136:139], v[144:147], 0\n\t" \
  "buffer_load_dwordx4 v227, s[60:63], s96 offen offset:3072 lds\n\t" \
  "v_mfma_f32_16x16x32_bf16 a[68:71], a[140:143], v[148:151], a[68:71]\n\t" \
  "ds_read_b128 a[224:227], v220 offset:8192\n\t" \
  "v_cvt_pk_bf16_f32 v136, v32, v33\n\t" \
  "v_mfma_f32_16x16x32_bf16 a[72:75], a[144:147], v[144:147], 0\n\t" \
  "s_add_u32 m0, %[m], 81920\n\t" \
  "v_exp_f32 v66, v66\n\t" \
  "v_mfma_f32_16x16x32_bf16 a[72:75], a[148:151], v[148:151], a[72:75]\n\t" \
  "s_add_u32 s96, %[m], 32768\n\t" \
  "ds_read_b128 a[228:231], v221 offset:8192\n\t" \
  "v_cvt_pk_bf16_f32 v137, v34, v35\n\t" \
  "v_mfma_f32_16x16x32_bf16 a[76:79], a[152:155], v[144:147], 0\n\t" \
  "v_exp_f32 v67, v67\n\t" \
  "v_mfma_f32_16x16x32_bf16 a[76:79], a[156:159], v[148:151], a[76:79]\n\t" \
  "v_exp_f32 v68, v68\n\t" \
  "v_mfma_f32_16x16x32_bf16 a[80:83], a[160:163], v[144:147], 0\n\t" \
  "buffer_load_dwordx4 v230, s[64:67], s96 offen offset:0 lds\n\t" \
  "v_mfma_f32_16x16x32_bf16 a[80:83], a[164:167], v[148:151], a[80:83]\n\t" \
  "ds_read_b128 a[232:235], v222 offset:8192\n\t" \
  "v_cvt_pk_bf16_f32 v138, v36, v37\n\t" \
  "v_mfma_f32_16x16x32_bf16 a[84:87], a[168:171], v[144:147], 0\n\t" \
  "v_exp_f32 v69, v69\n\t" \
  "v_mfma_f32_16x16x32_bf16 a[84:87], a[172:175], v[148:151], a[84:87]\n\t" \
  "ds_read_b128 a[236:239], v223 offset:8192\n\t" \
  "v_cvt_pk_bf16_f32 v139, v38, v39\n\t" \
  "v_mfma_f32_16x16x32_bf16 a[88:91], a[176:179], v[144:147], 0\n\t" \
  "v_exp_f32 v70, v70\n\t" \
  "v_mfma_f32_16x16x32_bf16 a[88:91], a[180:183], v[148:151], a[88:91]\n\t" \
  "v_exp_f32 v71, v71\n\t" \
  "v_mfma_f32_16x16x32_bf16 a[92:95], a[184:187], v[144:147], 0\n\t" \
  "buffer_load_dwordx4 v231, s[64:67], s96 offen offset:1024 lds\n\t" \
  "v_mfma_f32_16x16x32_bf16 a[92:95], a[188:191], v[148:151], a[92:95]\n\t" \
  "ds_read_b128 a[240:243], v220 offset:12288\n\t" \
  "v_cvt_pk_bf16_f32 v140, v40, v41\n\t" \
  "v_mfma_f32_16x16x32_bf16 v[168:171], v[216:219], v[144:147], 0\n\t" \
  "v_exp_f32 v72, v72\n\t" \
  "v_mfma_f32_16x16x32_bf16 v[168:171], v[216:219], v[148:151], v[168:171]\n\t" \
  "ds_read_b128 a[244:247], v221 offset:12288\n\t" \
  "v_cvt_pk_bf16_f32 v141, v42, v43\n\t" \
  "v_mfma_f32_16x16x32_bf16 a[96:99], a[128:131], v[152:155], 0\n\t" \
  "v_exp_f32 v73, v73\n\t" \
  "v_mfma_f32_16x16x32_bf16 a[96:99], a[132:135], v[156:159], a[96:99]\n\t" \
  "v_exp_f32 v74, v74\n\t" \
  "v_mfma_f32_16x16x32_bf16 a[100:103], a[136:139], v[152:155], 0\n\t" \
  "buffer_load_dwordx4 v230, s[64:67], s96 offen offset:2048 lds\n\t" \
  "v_mfma_f32_16x16x32_bf16 a[100:103], a[140:143], v[156:159], a[100:103]\n\t" \
  "ds_read_b128 a[248:251], v222 offset:12288\n\t" \
  "v_cvt_pk_bf16_f32 v142, v44, v45\n\t" \
  "v_mfma_f32_16x16x32_bf16 a[104:107], a[144:147], v[152:155], 0\n\t" \
  "v_exp_f32 v75, v75\n\t" \
  "v_mfma_f32_16x16x32_bf16 a[104:107], a[148:151], v[156:159], a[104:107]\n\t" \
  "ds_read_b128 a[252:255], v223 offset:12288\n\t" \
  "v_cvt_pk_bf16_f32 v143, v46, v47\n\t" \
  "v_mfma_f32_16x16x32_bf16 a[108:111], a[152:155], v[152:155], 0\n\t" \
  "v_exp_f32 v76, v76\n\t" \
  "v_mfma_f32_16x16x32_bf16 a[108:111], a[156:159], v[156:159], a[108:111]\n\t" \
  "v_exp_f32 v77, v77\n\t" \
  "v_mfma_f32_16x16x32_bf16 a[112:115], a[160:163], v[152:155], 0\n\t" \
  "buffer_load_dwordx4 v231, s[64:67], s96 offen offset:3072 lds\n\t" \
  "v_mfma_f32_16x16x32_bf16 a[112:115], a[164:167], v[156:159], a[112:115]\n\t" \
  "v_exp_f32 v78, v78\n\t" \
  "v_mfma_f32_16x16x32_bf16 a[116:119], a[168:171], v[152:155], 0\n\t" \
  "v_exp_f32 v79, v79\n\t" \
  "v_mfma_f32_16x16x32_bf16 a[116:119], a[172:175], v[156:159], a[116:119]\n\t" \
  "v_exp_f32 v96, v96\n\t" \
  "v_mfma_f32_16x16x32_bf16 a[120:123], a[176:179], v[152:155], 0\n\t" \
  "v_cvt_pk_bf16_f32 v144, v64, v65\n\t" \
  "v_cvt_pk_bf16_f32 v145, v66, v67\n\t" \
  "v_mfma_f32_16x16x32_bf16 a[120:123], a[180:183], v[156:159], a[120:123]\n\t" \
  "v_exp_f32 v97, v97\n\t" \
  "v_mfma_f32_16x16x32_bf16 a[124:127], a[184:187], v[152:155], 0\n\t" \
  "v_exp_f32 v98, v98\n\t" \
  "v_mfma_f32_16x16x32_bf16 a[124:127], a[188:191], v[156:159], a[124:127]\n\t" \
  "v_cvt_pk_bf16_f32 v146, v68, v69\n\t" \
  "v_cvt_pk_bf16_f32 v147, v70, v71\n\t" \
  "v_mfma_f32_16x16x32_bf16 v[172:175], v[216:219], v[152:155], 0\n\t" \
  "v_exp_f32 v99, v99\n\t" \
  "v_mfma_f32_16x16x32_bf16 v[172:175], v[216:219], v[156:159], v[172:175]\n\t" \
  "s_waitcnt lgkmcnt(12)\n\t" \
  "v_exp_f32 v100, v100\n\t" \
  STA("s_memrealtime s[94:95]\n\t") \
  STA("s_memtime s[92:93]\n\t") \
  "v_mfma_f32_16x16x32_bf16 v[0:3], a[192:195], v[16:19], v[176:179]\n\t" \
  "ds_read_b64_tr_b16 a[128:129], v210 offset:0\n\t" \
  "v_cvt_pk_bf16_f32 v148, v72, v73\n\t" \
  "v_mfma_f32_16x16x32_bf16 v[0:3], a[196:199], v[20:23], v[0:3]\n\t" \
  "v_exp_f32 v101, v101\n\t" \
  "v_mfma_f32_16x16x32_bf16 v[0:3], a[200:203], v[24:27], v[0:3]\n\t" \
  "ds_read_b64_tr_b16 a[130:131], v210 offset:4096\n\t" \
  "v_cvt_pk_bf16_f32 v149, v74, v75\n\t" \
  "v_mfma_f32_16x16x32_bf16 v[0:3], a[204:207], v[28:31], v[0:3]\n\t" \
  "s_waitcnt lgkmcnt(10)\n\t" \
  "v_exp_f32 v102, v102\n\t" \
  "v_mfma_f32_16x16x32_bf16 v[4:7], a[220:223], v[28:31], v[176:179]\n\t" \
  "ds_read_b64_tr_b16 a[132:133], v210 offset:8192\n\t" \
  "v_cvt_pk_bf16_f32 v150, v76, v77\n\t" \
  "v_mfma_f32_16x16x32_bf16 v[4:7], a[216:219], v[24:27], v[4:7]\n\t" \
  "v_exp_f32 v103, v103\n\t" \
  "v_mfma_f32_16x16x32_bf16 v[4:7], a[212:215], v[20:23], v[4:7]\n\t" \
  "ds_read_b64_tr_b16 a[134:135], v210 offset:12288\n\t" \
  "v_cvt_pk_bf16_f32 v151, v78, v79\n\t" \
  "v_mfma_f32_16x16x32_bf16 v[4:7], a[208:211], v[16:19], v[4:7]\n\t" \
  "s_waitcnt lgkmcnt(8)\n\t" \
  "ds_read_b64_tr_b16 a[136:137], v211 offset:0\n\t" \
  "v_cvt_pk_bf16_f32 v152, v96, v97\n\t" \
  "v_mfma_f32_16x16x32_bf16 v[8:11], a[224:227], v[16:19], v[176:179]\n\t" \
  "v_exp_f32 v104, v104\n\t" \
  "v_mfma_f32_16x16x32_bf16 v[8:11], a[228:231], v[20:23], v[8:11]\n\t" \
  "ds_read_b64_tr_b16 a[138:139], v211 offset:4096\n\t" \
  "v_cvt_pk_bf16_f32 v153, v98, v99\n\t" \
  "v_mfma_f32_16x16x32_bf16 v[8:11], a[232:235], v[24:27], v[8:11]\n\t" \
  "v_exp_f32 v105, v105\n\t" \
  "v_mfma_f32_16x16x32_bf16 v[8:11], a[236:239], v[28:31], v[8:11]\n\t" \
  "s_waitcnt lgkmcnt(6)\n\t" \
  "ds_read_b64_tr_b16 a[140:141], v211 offset:8192\n\t" \
  "v_cvt_pk_bf16_f32 v154, v100, v101\n\t" \
  "v_mfma_f32_16x16x32_bf16 v[12:15], a[252:255], v[28:31], v[176:179]\n\t" \
  STA("v_writelane_b32 %[stv], s94, 30\n\t") \
  STA("v_writelane_b32 %[stv], s92, 31\n\t") \
  "v_exp_f32 v106, v106\n\t" \
  "v_mfma_f32_16x16x32_bf16 v[12:15], a[248:251], v[24:27], v[12:15]\n\t" \
  "ds_read_b64_tr_b16 a[142:143], v211 offset:12288\n\t" \
  "v_cvt_pk_bf16_f32 v155, v102, v103\n\t" \
  "v_mfma_f32_16x16x32_bf16 v[12:15], a[244:247], v[20:23], v[12:15]\n\t" \
  "ds_read_b64_tr_b16 a[144:145], v210 offset:256\n\t" \
  "v_cvt_pk_bf16_f32 v156, v104, v105\n\t" \
  "v_mfma_f32_16x16x32_bf16 v[12:15], a[240:243], v[16:19], v[12:15]\n\t" \
  "v_exp_f32 v107, v107\n\t" \
  "v_mfma_f32_16x16x32_bf16 v[32:35], a[192:195], v[48:51], v[180:183]\n\t" \
  "s_add_u32 m0, %[m], 16384\n\t" \
  "ds_read_b64_tr_b16 a[146:147], v210 offset:4352\n\t" \
  "v_cvt_pk_bf16_f32 v157, v106, v107\n\t" \
  "v_mfma_f32_16x16x32_bf16 v[32:35], a[196:199], v[52:55], v[32:35]\n\t" \
  "s_mov_b32 s96, 0\n\t" \
  "v_exp_f32 v108, v108\n\t" \
  "v_mfma_f32_16x16x32_bf16 v[32:35], a[200:203], v[56:59], v[32:35]\n\t" \
  "v_exp_f32 v109, v109\n\t" \
  "v_mfma_f32_16x16x32_bf16 v[32:35], a[204:207], v[60:63], v[32:35]\n\t" \
  "ds_read_b64_tr_b16 a[148:149], v210 offset:8448\n\t" \
  "v_cvt_pk_bf16_f32 v158, v108, v109\n\t" \
  "v_mfma_f32_16x16x32_bf16 v[36:39], a[220:223], v[60:63], v[180:183]\n\t" \
  "buffer_load_dwordx4 v232, s[68:71], s96 offen offset:0 lds\n\t" \
  "v_mfma_f32_16x16x32_bf16 v[36:39], a[216:219], v[56:59], v[36:39]\n\t" \
  "v_exp_f32 v110, v110\n\t" \
  "v_mfma_f32_16x16x32_bf16 v[36:39], a[212:215], v[52:55], v[36:39]\n\t" \
  "v_exp_f32 v111, v111\n\t" \
  "v_mfma_f32_16x16x32_bf16 v[36:39], a[208:211], v[48:51], v[36:39]\n\t" \
  "ds_read_b64_tr_b16 a[150:151], v210 offset:12544\n\t" \
  "v_cvt_pk_bf16_f32 v159, v110, v111\n\t" \
  "v_mfma_f32_16x16x32_bf16 v[40:43], a[224:227], v[48:51], v[180:183]\n\t" \
  "v_exp_f32 v0, v0\n\t" \
  "v_mfma_f32_16x16x32_bf16 v[40:43], a[228:231], v[52:55], v[40:43]\n\t" \
  "ds_read_b64_tr_b16 a[152:153], v211 offset:256\n\t" \
  "v_mfma_f32_16x16x32_bf16 v[40:43], a[232:235], v[56:59], v[40:43]\n\t" \
  "ds_read_b64_tr_b16 a[154:155], v211 offset:4352\n\t" \
  "s_mov_b32 s96, 6144\n\t" \
  "v_mfma_f32_16x16x32_bf16 v[40:43], a[236:239], v[60:63], v[40:43]\n\t" \
  "v_exp_f32 v1, v1\n\t" \
  "v_mfma_f32_16x16x32_bf16 v[44:47], a[252:255], v[60:63], v[180:183]\n\t" \
  "ds_read_b64_tr_b16 a[156:157], v211 offset:8448\n\t" \
  "v_mfma_f32_16x16x32_bf16 v[44:47], a[248:251], v[56:59], v[44:47]\n\t" \
  "v_exp_f32 v2, v2\n\t" \
  "v_mfma_f32_16x16x32_bf16 v[44:47], a[244:247], v[52:55], v[44:47]\n\t" \
  "buffer_load_dwordx4 v232, s[68:71], s96 offen offset:1024 lds\n\t" \
  "v_mfma_f32_16x16x32_bf16 v[44:47], a[240:243], v[48:51], v[44:47]\n\t" \
  "ds_read_b64_tr_b16 a[158:159], v211 offset:12544\n\t" \
  "v_mfma_f32_16x16x32_bf16 v[64:67], a[192:195], v[80:83], v[184:187]\n\t" \
  "ds_read_b64_tr_b16 a[160:161], v210 offset:512\n\t" \
  "v_mfma_f32_16x16x32_bf16 v[64:67], a[196:199], v[84:87], v[64:67]\n\t" \
  "ds_read_b64_tr_b16 a[162:163], v210 offset:4608\n\t" \
  "v_mfma_f32_16x16x32_bf16 v[64:67], a[200:203], v[88:91], v[64:67]\n\t" \
  "v_exp_f32 v3, v3\n\t" \
  "v_mfma_f32_16x16x32_bf16 v[64:67], a[204:207], v[92:95], v[64:67]\n\t" \
  "ds_read_b64_tr_b16 a[164:165], v210 offset:8704\n\t" \
  "v_mfma_f32_16x16x32_bf16 v[68:71], a[220:223], v[92:95], v[184:187]\n\t" \
  "ds_read_b64_tr_b16 a[166:167], v210 offset:12800\n\t" \
  "s_mov_b32 s96, 12288\n\t" \
  "v_mfma_f32_16x16x32_bf16 v[68:71], a[216:219], v[88:91], v[68:71]\n\t" \
  "v_exp_f32 v4, v4\n\t" \
  "v_mfma_f32_16x16x32_bf16 v[68:71], a[212:215], v[84:87], v[68:71]\n\t" \
  "ds_read_b64_tr_b16 a[168:169], v211 offset:512\n\t" \
  "v_mfma_f32_16x16x32_bf16 v[68:71], a[208:211], v[80:83], v[68:71]\n\t" \
  "v_exp_f32 v5, v5\n\t" \
  "v_mfma_f32_16x16x32_bf16 v[72:75], a[224:227], v[80:83], v[184:187]\n\t" \
  "buffer_load_dwordx4 v232, s[68:71], s96 offen offset:2048 lds\n\t" \
  "v_mfma_f32_16x16x32_bf16 v[72:75], a[228:231], v[84:87], v[72:75]\n\t" \
  "ds_read_b64_tr_b16 a[170:171], v211 offset:4608\n\t" \
  "v_mfma_f32_16x16x32_bf16 v[72:75], a[232:235], v[88:91], v[72:75]\n\t" \
  "ds_read_b64_tr_b16 a[172:173], v211 offset:8704\n\t" \
  "v_mfma_f32_16x16x32_bf16 v[72:75], a[236:239], v[92:95], v[72:75]\n\t" \
  "ds_read_b64_tr_b16 a[174:175], v211 offset:12800\n\t" \
  "v_mfma_f32_16x16x32_bf16 v[76:79], a[252:255], v[92:95], v[184:187]\n\t" \
  "v_exp_f32 v6, v6\n\t" \
  "v_mfma_f32_16x16x32_bf16 v[76:79], a[248:251], v[88:91], v[76:79]\n\t" \
  "ds_read_b64_tr_b16 a[176:177], v210 offset:768\n\t" \
  "v_mfma_f32_16x16x32_bf16 v[76:79], a[244:247], v[84:87], v[76:79]\n\t" \
  "s_mov_b32 s96, 18432\n\t" \
  "v_exp_f32 v7, v7\n\t" \
  "v_mfma_f32_16x16x32_bf16 v[76:79], a[240:243], v[80:83], v[76:79]\n\t" \
  "ds_read_b64_tr_b16 a[178:179], v210 offset:4864\n\t" \
  "v_mfma_f32_16x16x32_bf16 v[96:99], a[192:195], v[112:115], v[188:191]\n\t" \
  "ds_read_b64_tr_b16 a[180:181], v210 offset:8960\n\t" \
  "v_mfma_f32_16x16x32_bf16 v[96:99], a[196:199], v[116:119], v[96:99]\n\t" \
  "v_exp_f32 v8, v8\n\t" \
  "v_mfma_f32_16x16x32_bf16 v[96:99], a[200:203], v[120:123], v[96:99]\n\t" \
  "buffer_load_dwordx4 v232, s[68:71], s96 offen offset:3072 lds\n\t" \
  "v_mfma_f32_16x16x32_bf16 v[96:99], a[204:207], v[124:127], v[96:99]\n\t" \
  "ds_read_b64_tr_b16 a[182:183], v210 offset:13056\n\t" \
  "v_mfma_f32_16x16x32_bf16 v[100:103], a[220:223], v[124:127], v[188:191]\n\t" \
  "ds_read_b64_tr_b16 a[184:185], v211 offset:768\n\t" \
  ".Lhs6:\n\ts_cbranch_execz .Lhs6n\n\t" \
  "v_mfma_f32_16x16x32_bf16 v[100:103], a[216:219], v[120:123], v[100:103]\n\t" \
  "ds_read_b64_tr_b16 a[186:187], v211 offset:4864\n\t" \
  "v_mfma_f32_16x16x32_bf16 v[100:103], a[212:215], v[116:119], v[100:103]\n\t" \
  "ds_read_b64_tr_b16 a[188:189], v211 offset:8960\n\t" \
  "v_mfma_f32_16x16x32_bf16 v[100:103], a[208:211], v[112:115], v[100:103]\n\t" \
  "v_exp_f32 v9, v9\n\t" \
  "v_mfma_f32_16x16x32_bf16 v[104:107], a[224:227], v[112:115], v[188:191]\n\t" \
  "ds_read_b64_tr_b16 a[190:191], v211 offset:13056\n\t" \
  "v_mfma_f32_16x16x32_bf16 v[104:107], a[228:231], v[116:119], v[104:107]\n\t" \
  "v_exp_f32 v10, v10\n\t" \
  "v_mfma_f32_16x16x32_bf16 v[104:107], a[232:235], v[120:123], v[104:107]\n\t" \
  "v_exp_f32 v11, v11\n\t" \
  "v_mfma_f32_16x16x32_bf16 v[104:107], a[236:239], v[124:127], v[104:107]\n\t" \
  "v_exp_f32 v12, v12\n\t" \
  "v_mfma_f32_16x16x32_bf16 v[108:111], a[252:255], v[124:127], v[188:191]\n\t" \
  "v_exp_f32 v13, v13\n\t" \
  "v_mfma_f32_16x16x32_bf16 v[108:111], a[248:251], v[120:123], v[108:111]\n\t" \
  "v_exp_f32 v14, v14\n\t" \
  "v_mfma_f32_16x16x32_bf16 v[108:111], a[244:247], v[116:119], v[108:111]\n\t" \
  "s_waitcnt vmcnt(4) lgkmcnt(0)\n\t" \
  "v_exp_f32 v15, v15\n\t" \
  "v_mfma_f32_16x16x32_bf16 v[108:111], a[240:243], v[112:115], v[108:111]\n\t" \
  "s_barrier\n\t" \
  STA("s_memrealtime s[94:95]\n\t") \
  STA("s_memtime s[92:93]\n\t") \
  "v_mfma_f32_16x16x32_bf16 a[0:3], a[128:131], v[128:131], a[0:3]\n\t" \
  "v_exp_f32 v32, v32\n\t" \
  "v_mfma_f32_16x16x32_bf16 a[0:3], a[132:135], v[132:135], a[0:3]\n\t" \
  "v_exp_f32 v33, v33\n\t" \
  "v_mfma_f32_16x16x32_bf16 a[4:7], a[136:139], v[128:131], a[4:7]\n\t" \
  "v_exp_f32 v34, v34\n\t" \
  "v_mfma_f32_16x16x32_bf16 a[4:7], a[140:143], v[132:135], a[4:7]\n\t" \
  "v_exp_f32 v35, v35\n\t" \
  "v_mfma_f32_16x16x32_bf16 a[8:11], a[144:147], v[128:131], a[8:11]\n\t" \
  "v_exp_f32 v36, v36\n\t" \
  "v_mfma_f32_16x16x32_bf16 a[8:11], a[148:151], v[132:135], a[8:11]\n\t" \
  "v_exp_f32 v37, v37\n\t" \
  "v_mfma_f32_16x16x32_bf16 a[12:15], a[152:155], v[128:131], a[12:15]\n\t" \
  "v_exp_f32 v38, v38\n\t" \
  "v_mfma_f32_16x16x32_bf16 a[12:15], a[156:159], v[132:135], a[12:15]\n\t" \
  "v_exp_f32 v39, v39\n\t" \
  "v_mfma_f32_16x16x32_bf16 a[16:19], a[160:163], v[128:131], a[16:19]\n\t" \
  "v_exp_f32 v40, v40\n\t" \
  "v_mfma_f32_16x16x32_bf16 a[16:19], a[164:167], v[132:135], a[16:19]\n\t" \
  "v_exp_f32 v41, v41\n\t" \
  "v_mfma_f32_16x16x32_bf16 a[20:23], a[168:171], v[128:131], a[20:23]\n\t" \
  "v_exp_f32 v42, v42\n\t" \
  "v_mfma_f32_16x16x32_bf16 a[20:23], a[172:175], v[132:135], a[20:23]\n\t" \
  "v_exp_f32 v43, v43\n\t" \
  "v_mfma_f32_16x16x32_bf16 a[24:27], a[176:179], v[128:131], a[24:27]\n\t" \
  STA("v_writelane_b32 %[stv], s94, 32\n\t") \
  STA("v_writelane_b32 %[stv], s92, 33\n\t") \
  "buffer_store_dword v233, v247, %[ws], 0 offen sc1\n\t" \
  "ds_read_b128 a[192:195], v220 offset:16384\n\t" \
  "v_mfma_f32_16x16x32_bf16 a[24:27], a[180:183], v[132:135], a[24:27]\n\t" \
  "v_exp_f32 v44, v44\n\t" \
  "v_mfma_f32_16x16x32_bf16 a[28:31], a[184:187], v[128:131], a[28:31]\n\t" \
  "v_exp_f32 v45, v45\n\t" \
  "v_mfma_f32_16x16x32_bf16 a[28:31], a[188:191], v[132:135], a[28:31]\n\t" \
  "v_exp_f32 v46, v46\n\t" \
  "v_mfma_f32_16x16x32_bf16 v[160:163], v[216:219], v[128:131], v[160:163]\n\t" \
  "s_add_u32 m0, %[m], 49152\n\t" \
  "v_exp_f32 v47, v47\n\t" \
  "v_mfma_f32_16x16x32_bf16 v[160:163], v[216:219], v[132:135], v[160:163]\n\t" \
  "s_add_u32 s96, %[m], 65536\n\t" \
  "ds_read_b128 a[196:199], v221 offset:16384\n\t" \
  "v_cvt_pk_bf16_f32 v128, v0, v1\n\t" \
  "v_mfma_f32_16x16x32_bf16 a[32:35], a[128:131], v[136:139], a[32:35]\n\t" \
  "v_exp_f32 v64, v64\n\t" \
  "v_mfma_f32_16x16x32_bf16 a[32:35], a[132:135], v[140:143], a[32:35]\n\t" \
  "v_exp_f32 v65, v65\n\t" \
  "v_mfma_f32_16x16x32_bf16 a[36:39], a[136:139], v[136:139], a[36:39]\n\t" \
  "buffer_load_dwordx4 v224, s[60:63], s96 offen offset:0 lds\n\t" \
  "v_mfma_f32_16x16x32_bf16 a[36:39], a[140:143], v[140:143], a[36:39]\n\t" \
  "ds_read_b128 a[200:203], v222 offset:16384\n\t" \
  "v_cvt_pk_bf16_f32 v129, v2, v3\n\t" \
  "v_mfma_f32_16x16x32_bf16 a[40:43], a[144:147], v[136:139], a[40:43]\n\t" \
  "v_exp_f32 v66, v66\n\t" \
  "v_mfma_f32_16x16x32_bf16 a[40:43], a[148:151], v[140:143], a[40:43]\n\t" \
  "ds_read_b128 a[204:207], v223 offset:16384\n\t" \
  "v_cvt_pk_bf16_f32 v130, v4, v5\n\t" \
  "v_mfma_f32_16x16x32_bf16 a[44:47], a[152:155], v[136:139], a[44:47]\n\t" \
  "buffer_load_dwordx4 v225, s[60:63], s96 offen offset:1024 lds\n\t" \
  "v_mfma_f32_16x16x32_bf16 a[44:47], a[156:159], v[140:143], a[44:47]\n\t" \
  "v_exp_f32 v67, v67\n\t" \
  "v_mfma_f32_16x16x32_bf16 a[48:51], a[160:163], v[136:139], a[48:51]\n\t" \
  "ds_read_b128 a[208:211], v220 offset:20480\n\t" \
  "v_cvt_pk_bf16_f32 v131, v6, v7\n\t" \
  "v_mfma_f32_16x16x32_bf16 a[48:51], a[164:167], v[140:143], a[48:51]\n\t" \
  "v_exp_f32 v68, v68\n\t" \
  "v_mfma_f32_16x16x32_bf16 a[52:55], a[168:171], v[136:139], a[52:55]\n\t" \
  "buffer_load_dwordx4 v226, s[60:63], s96 offen offset:2048 lds\n\t" \
  "v_mfma_f32_16x16x32_bf16 a[52:55], a[172:175], v[140:143], a[52:55]\n\t" \
  "ds_read_b128 a[212:215], v221 offset:20480\n\t" \
  "v_cvt_pk_bf16_f32 v132, v8, v9\n\t" \
  "v_mfma_f32_16x16x32_bf16 a[56:59], a[176:179], v[136:139], a[56:59]\n\t" \
  "v_exp_f32 v69, v69\n\t" \
  "v_mfma_f32_16x16x32_bf16 a[56:59], a[180:183], v[140:143], a[56:59]\n\t" \
  "v_cvt_pk_bf16_f32 v133, v10, v11\n\t" \
  "v_cvt_pk_bf16_f32 v134, v12, v13\n\t" \
  "v_mfma_f32_16x16x32_bf16 a[60:63], a[184:187], v[136:139], a[60:63]\n\t" \
  "buffer_load_dwordx4 v227, s[60:63], s96 offen offset:3072 lds\n\t" \
  "v_mfma_f32_16x16x32_bf16 a[60:63], a[188:191], v[140:143], a[60:63]\n\t" \
  "s_add_u32 m0, %[m], 147456\n\t" \
  "ds_read_b128 a[216:219], v222 offset:20480\n\t" \
  "v_cvt_pk_bf16_f32 v135, v14, v15\n\t" \
  "v_mfma_f32_16x16x32_bf16 v[164:167], v[216:219], v[136:139], v[164:167]\n\t" \
  "s_add_u32 s96, %[m], 49152\n\t" \
  "v_exp_f32 v70, v70\n\t" \
  "v_mfma_f32_16x16x32_bf16 v[164:167], v[216:219], v[140:143], v[164:167]\n\t" \
  "ds_read_b128 a[220:223], v223 offset:20480\n\t" \
  "v_cvt_pk_bf16_f32 v136, v32, v33\n\t" \
  "v_mfma_f32_16x16x32_bf16 a[64:67], a[128:131], v[144:147], a[64:67]\n\t" \
  "buffer_load_dwordx4 v230, s[64:67], s96 offen offset:0 lds\n\t" \
  "v_mfma_f32_16x16x32_bf16 a[64:67], a[132:135], v[148:151], a[64:67]\n\t" \
  "v_exp_f32 v71, v71\n\t" \
  "v_mfma_f32_16x16x32_bf16 a[68:71], a[136:139], v[144:147], a[68:71]\n\t" \
  "ds_read_b128 a[224:227], v220 offset:24576\n\t" \
  "v_cvt_pk_bf16_f32 v137, v34, v35\n\t" \
  "v_mfma_f32_16x16x32_bf16 a[68:71], a[140:143], v[148:151], a[68:71]\n\t" \
  "v_exp_f32 v72, v72\n\t" \
  "v_mfma_f32_16x16x32_bf16 a[72:75], a[144:147], v[144:147], a[72:75]\n\t" \
  "buffer_load_dwordx4 v231, s[64:67], s96 offen offset:1024 lds\n\t" \
  "v_mfma_f32_16x16x32_bf16 a[72:75], a[148:151], v[148:151], a[72:75]\n\t" \
  "ds_read_b128 a[228:231], v221 offset:24576\n\t" \
  "v_cvt_pk_bf16_f32 v138, v36, v37\n\t" \
  "v_mfma_f32_16x16x32_bf16 a[76:79], a[152:155], v[144:147], a[76:79]\n\t" \
  "v_exp_f32 v73, v73\n\t" \
  "v_mfma_f32_16x16x32_bf16 a[76:79], a[156:159], v[148:151], a[76:79]\n\t" \
  "v_exp_f32 v74, v74\n\t" \
  "v_mfma_f32_16x16x32_bf16 a[80:83], a[160:163], v[144:147], a[80:83]\n\t" \
  "buffer_load_dwordx4 v230, s[64:67], s96 offen offset:2048 lds\n\t" \
  "v_mfma_f32_16x16x32_bf16 a[80:83], a[164:167], v[148:151], a[80:83]\n\t" \
  "ds_read_b128 a[232:235], v222 offset:24576\n\t" \
  "v_cvt_pk_bf16_f32 v139, v38, v39\n\t" \
  "v_mfma_f32_16x16x32_bf16 a[84:87], a[168:171], v[144:147], a[84:87]\n\t" \
  "v_exp_f32 v75, v75\n\t" \
  "v_mfma_f32_16x16x32_bf16 a[84:87], a[172:175], v[148:151], a[84:87]\n\t" \
  "ds_read_b128 a[236:239], v223 offset:24576\n\t" \
  "v_cvt_pk_bf16_f32 v140, v40, v41\n\t" \
  "v_mfma_f32_16x16x32_bf16 a[88:91], a[176:179], v[144:147], a[88:91]\n\t" \
  "buffer_load_dwordx4 v231, s[64:67], s96 offen offset:3072 lds\n\t" \
  "v_mfma_f32_16x16x32_bf16 a[88:91], a[180:183], v[148:151], a[88:91]\n\t" \
  "s_add_u32 m0, %[m], 32768\n\t" \
  "v_exp_f32 v76, v76\n\t" \
  "v_mfma_f32_16x16x32_bf16 a[92:95], a[184:187], v[144:147], a[92:95]\n\t" \
  "s_mov_b32 s96, 28672\n\t" \
  "ds_read_b128 a[240:243], v220 offset:28672\n\t" \
  "v_cvt_pk_bf16_f32 v141, v42, v43\n\t" \
  "v_mfma_f32_16x16x32_bf16 a[92:95], a[188:191], v[148:151], a[92:95]\n\t" \
  "v_exp_f32 v77, v77\n\t" \
  "v_mfma_f32_16x16x32_bf16 v[168:171], v[216:219], v[144:147], v[168:171]\n\t" \
  "buffer_load_dwordx4 v232, s[68:71], s96 offen offset:0 lds\n\t" \
  "v_mfma_f32_16x16x32_bf16 v[168:171], v[216:219], v[148:151], v[168:171]\n\t" \
  "s_mov_b32 s96, 34816\n\t" \
  "ds_read_b128 a[244:247], v221 offset:28672\n\t" \
  "v_cvt_pk_bf16_f32 v142, v44, v45\n\t" \
  "v_mfma_f32_16x16x32_bf16 a[96:99], a[128:131], v[152:155], a[96:99]\n\t" \
  "v_exp_f32 v78, v78\n\t" \
  "v_mfma_f32_16x16x32_bf16 a[96:99], a[132:135], v[156:159], a[96:99]\n\t" \
  "v_exp_f32 v79, v79\n\t" \
  "v_mfma_f32_16x16x32_bf16 a[100:103], a[136:139], v[152:155], a[100:103]\n\t" \
  "buffer_load_dwordx4 v232, s[68:71], s96 offen offset:1024 lds\n\t" \
  "v_mfma_f32_16x16x32_bf16 a[100:103], a[140:143], v[156:159], a[100:103]\n\t" \
  "s_mov_b32 s96, 40960\n\t" \
  "ds_read_b128 a[248:251], v222 offset:28672\n\t" \
  "v_cvt_pk_bf16_f32 v143, v46, v47\n\t" \
  "v_mfma_f32_16x16x32_bf16 a[104:107], a[144:147], v[152:155], a[104:107]\n\t" \
  "v_exp_f32 v96, v96\n\t" \
  "v_mfma_f32_16x16x32_bf16 a[104:107], a[148:151], v[156:159], a[104:107]\n\t" \
  "ds_read_b128 a[252:255], v223 offset:28672\n\t" \
  "v_cvt_pk_bf16_f32 v144, v64, v65\n\t" \
  "v_mfma_f32_16x16x32_bf16 a[108:111], a[152:155], v[152:155], a[108:111]\n\t" \
  "buffer_load_dwordx4 v232, s[68:71], s96 offen offset:2048 lds\n\t" \
  "v_mfma_f32_16x16x32_bf16 a[108:111], a[156:159], v[156:159], a[108:111]\n\t" \
  "s_mov_b32 s96, 47104\n\t" \
  "v_exp_f32 v97, v97\n\t" \
  "v_mfma_f32_16x16x32_bf16 a[112:115], a[160:163], v[152:155], a[112:115]\n\t" \
  "v_cvt_pk_bf16_f32 v145, v66, v67\n\t" \
  "v_cvt_pk_bf16_f32 v146, v68, v69\n\t" \
  "v_mfma_f32_16x16x32_bf16 a[112:115], a[164:167], v[156:159], a[112:115]\n\t" \
  "v_exp_f32 v98, v98\n\t" \
  "v_mfma_f32_16x16x32_bf16 a[116:119], a[168:171], v[152:155], a[116:119]\n\t" \
  "buffer_load_dwordx4 v232, s[68:71], s96 offen offset:3072 lds\n\t" \
  "v_mfma_f32_16x16x32_bf16 a[116:119], a[172:175], v[156:159], a[116:119]\n\t" \
  "v_exp_f32 v99, v99\n\t" \
  "v_mfma_f32_16x16x32_bf16 a[120:123], a[176:179], v[152:155], a[120:123]\n\t" \
  "v_cvt_pk_bf16_f32 v147, v70, v71\n\t" \
  "v_cvt_pk_bf16_f32 v148, v72, v73\n\t" \
  "v_mfma_f32_16x16x32_bf16 a[120:123], a[180:183], v[156:159], a[120:123]\n\t" \
  "v_exp_f32 v100, v100\n\t" \
  "v_mfma_f32_16x16x32_bf16 a[124:127], a[184:187], v[152:155], a[124:127]\n\t" \
  "v_exp_f32 v101, v101\n\t" \
  "v_mfma_f32_16x16x32_bf16 a[124:127], a[188:191], v[156:159], a[124:127]\n\t" \
  "v_cvt_pk_bf16_f32 v149, v74, v75\n\t" \
  "v_cvt_pk_bf16_f32 v150, v76, v77\n\t" \
  "v_mfma_f32_16x16x32_bf16 v[172:175], v[216:219], v[152:155], v[172:175]\n\t" \
  "v_exp_f32 v102, v102\n\t" \
  "v_mfma_f32_16x16x32_bf16 v[172:175], v[216:219], v[156:159], v[172:175]\n\t" \
  "s_waitcnt lgkmcnt(12)\n\t" \
  "v_exp_f32 v103, v103\n\t" \
  STA("s_memrealtime s[94:95]\n\t") \
  STA("s_memtime s[92:93]\n\t") \
  "v_mfma_f32_16x16x32_bf16 v[0:3], a[192:195], v[16:19], v[176:179]\n\t" \
  "ds_read_b64_tr_b16 a[128:129], v228 offset:0\n\t" \
  "v_cvt_pk_bf16_f32 v151, v78, v79\n\t" \
  "v_mfma_f32_16x16x32_bf16 v[0:3], a[196:199], v[20:23], v[0:3]\n\t" \
  "v_exp_f32 v104, v104\n\t" \
  "v_mfma_f32_16x16x32_bf16 v[0:3], a[200:203], v[24:27], v[0:3]\n\t" \
  "ds_read_b64_tr_b16 a[130:131], v228 offset:4096\n\t" \
  "v_cvt_pk_bf16_f32 v152, v96, v97\n\t" \
  "v_mfma_f32_16x16x32_bf16 v[0:3], a[204:207], v[28:31], v[0:3]\n\t" \
  "s_waitcnt lgkmcnt(10)\n\t" \
  "v_exp_f32 v105, v105\n\t" \
  "v_mfma_f32_16x16x32_bf16 v[4:7], a[220:223], v[28:31], v[176:179]\n\t" \
  "ds_read_b64_tr_b16 a[132:133], v228 offset:8192\n\t" \
  "v_cvt_pk_bf16_f32 v153, v98, v99\n\t" \
  "v_mfma_f32_16x16x32_bf16 v[4:7], a[216:219], v[24:27], v[4:7]\n\t" \
  "v_exp_f32 v106, v106\n\t" \
  "v_mfma_f32_16x16x32_bf16 v[4:7], a[212:215], v[20:23], v[4:7]\n\t" \
  "ds_read_b64_tr_b16 a[134:135], v228 offset:12288\n\t" \
  "v_cvt_pk_bf16_f32 v154, v100, v101\n\t" \
  "v_mfma_f32_16x16x32_bf16 v[4:7], a[208:211], v[16:19], v[4:7]\n\t" \
  "s_waitcnt lgkmcnt(8)\n\t" \
  "ds_read_b64_tr_b16 a[136:137], v229 offset:0\n\t" \
  "v_cvt_pk_bf16_f32 v155, v102, v103\n\t" \
  "v_mfma_f32_16x16x32_bf16 v[8:11], a[224:227], v[16:19], v[176:179]\n\t" \
  "v_exp_f32 v107, v107\n\t" \
  "v_mfma_f32_16x16x32_bf16 v[8:11], a[228:231], v[20:23], v[8:11]\n\t" \
  "ds_read_b64_tr_b16 a[138:139], v229 offset:4096\n\t" \
  "v_cvt_pk_bf16_f32 v156, v104, v105\n\t" \
  "v_mfma_f32_16x16x32_bf16 v[8:11], a[232:235], v[24:27], v[8:11]\n\t" \
  "v_exp_f32 v108, v108\n\t" \
  "v_mfma_f32_16x16x32_bf16 v[8:11], a[236:239], v[28:31], v[8:11]\n\t" \
  "s_waitcnt lgkmcnt(6)\n\t" \
  "ds_read_b64_tr_b16 a[140:141], v229 offset:8192\n\t" \
  "v_cvt_pk_bf16_f32 v157, v106, v107\n\t" \
  "v_mfma_f32_16x16x32_bf16 v[12:15], a[252:255], v[28:31], v[176:179]\n\t" \
  STA("v_writelane_b32 %[stv], s94, 34\n\t") \
  STA("v_writelane_b32 %[stv], s92, 35\n\t") \
  "v_exp_f32 v109, v109\n\t" \
  "v_mfma_f32_16x16x32_bf16 v[12:15], a[248:251], v[24:27], v[12:15]\n\t" \
  "ds_read_b64_tr_b16 a[142:143], v229 offset:12288\n\t" \
  "v_cvt_pk_bf16_f32 v158, v108, v109\n\t" \
  "v_mfma_f32_16x16x32_bf16 v[12:15], a[244:247], v[20:23], v[12:15]\n\t" \
  "v_exp_f32 v110, v110\n\t" \
  "v_mfma_f32_16x16x32_bf16 v[12:15], a[240:243], v[16:19], v[12:15]\n\t" \
  "v_exp_f32 v111, v111\n\t" \
  "v_mfma_f32_16x16x32_bf16 v[32:35], a[192:195], v[48:51], v[180:183]\n\t" \
  "s_add_u32 m0, %[m], 114688\n\t" \
  "ds_read_b64_tr_b16 a[144:145], v228 offset:256\n\t" \
  "v_cvt_pk_bf16_f32 v159, v110, v111\n\t" \
  "v_mfma_f32_16x16x32_bf16 v[32:35], a[196:199], v[52:55], v[32:35]\n\t" \
  "s_mov_b32 s96, 0\n\t" \
  "v_exp_f32 v0, v0\n\t" \
  "v_mfma_f32_16x16x32_bf16 v[32:35], a[200:203], v[56:59], v[32:35]\n\t" \
  "v_exp_f32 v1, v1\n\t" \
  "v_mfma_f32_16x16x32_bf16 v[32:35], a[204:207], v[60:63], v[32:35]\n\t" \
  "ds_read_b64_tr_b16 a[146:147], v228 offset:4352\n\t" \
  "v_mfma_f32_16x16x32_bf16 v[36:39], a[220:223], v[60:63], v[180:183]\n\t" \
  "buffer_load_dwordx4 v232, s[72:75], s96 offen offset:0 lds\n\t" \
  "v_mfma_f32_16x16x32_bf16 v[36:39], a[216:219], v[56:59], v[36:39]\n\t" \
  "ds_read_b64_tr_b16 a[148:149], v228 offset:8448\n\t" \
  "buffer_load_dword v246, v247, %[ws], 0 offen sc1\n\t" \
  "v_mfma_f32_16x16x32_bf16 v[36:39], a[212:215], v[52:55], v[36:39]\n\t" \
  "v_exp_f32 v2, v2\n\t" \
  "v_mfma_f32_16x16x32_bf16 v[36:39], a[208:211], v[48:51], v[36:39]\n\t" \
  "ds_read_b64_tr_b16 a[150:151], v228 offset:12544\n\t" \
  "v_mfma_f32_16x16x32_bf16 v[40:43], a[224:227], v[48:51], v[180:183]\n\t" \
  "ds_read_b64_tr_b16 a[152:153], v229 offset:256\n\t" \
  "v_mfma_f32_16x16x32_bf16 v[40:43], a[228:231], v[52:55], v[40:43]\n\t" \
  "v_exp_f32 v3, v3\n\t" \
  "v_mfma_f32_16x16x32_bf16 v[40:43], a[232:235], v[56:59], v[40:43]\n\t" \
  "ds_read_b64_tr_b16 a[154:155], v229 offset:4352\n\t" \
  "s_mov_b32 s96, 6144\n\t" \
  "v_mfma_f32_16x16x32_bf16 v[40:43], a[236:239], v[60:63], v[40:43]\n\t" \
  "v_exp_f32 v4, v4\n\t" \
  "v_mfma_f32_16x16x32_bf16 v[44:47], a[252:255], v[60:63], v[180:183]\n\t" \
  "ds_read_b64_tr_b16 a[156:157], v229 offset:8448\n\t" \
  "v_mfma_f32_16x16x32_bf16 v[44:47], a[248:251], v[56:59], v[44:47]\n\t" \
  "ds_read_b64_tr_b16 a[158:159], v229 offset:12544\n\t" \
  "v_mfma_f32_16x16x32_bf16 v[44:47], a[244:247], v[52:55], v[44:47]\n\t" \
  "buffer_load_dwordx4 v232, s[72:75], s96 offen offset:1024 lds\n\t" \
  "v_mfma_f32_16x16x32_bf16 v[44:47], a[240:243], v[48:51], v[44:47]\n\t" \
  "ds_read_b64_tr_b16 a[160:161], v228 offset:512\n\t" \
  "v_mfma_f32_16x16x32_bf16 v[64:67], a[192:195], v[80:83], v[184:187]\n\t" \
  "v_exp_f32 v5, v5\n\t" \
  "v_mfma_f32_16x16x32_bf16 v[64:67], a[196:199], v[84:87], v[64:67]\n\t" \
  "ds_read_b64_tr_b16 a[162:163], v228 offset:4608\n\t" \
  "v_mfma_f32_16x16x32_bf16 v[64:67], a[200:203], v[88:91], v[64:67]\n\t" \
  "ds_read_b64_tr_b16 a[164:165], v228 offset:8704\n\t" \
  "v_mfma_f32_16x16x32_bf16 v[64:67], a[204:207], v[92:95], v[64:67]\n\t" \
  "v_exp_f32 v6, v6\n\t" \
  "v_mfma_f32_16x16x32_bf16 v[68:71], a[220:223], v[92:95], v[184:187]\n\t" \
  "ds_read_b64_tr_b16 a[166:167], v228 offset:12800\n\t" \
  "s_mov_b32 s96, 12288\n\t" \
  "v_mfma_f32_16x16x32_bf16 v[68:71], a[216:219], v[88:91], v[68:71]\n\t" \
  "v_exp_f32 v7, v7\n\t" \
  "v_mfma_f32_16x16x32_bf16 v[68:71], a[212:215], v[84:87], v[68:71]\n\t" \
  "ds_read_b64_tr_b16 a[168:169], v229 offset:512\n\t" \
  "v_mfma_f32_16x16x32_bf16 v[68:71], a[208:211], v[80:83], v[68:71]\n\t" \
  "ds_read_b64_tr_b16 a[170:171], v229 offset:4608\n\t" \
  "v_mfma_f32_16x16x32_bf16 v[72:75], a[224:227], v[80:83], v[184:187]\n\t" \
  "buffer_load_dwordx4 v232, s[72:75], s96 offen offset:2048 lds\n\t" \
  "v_mfma_f32_16x16x32_bf16 v[72:75], a[228:231], v[84:87], v[72:75]\n\t" \
  "ds_read_b64_tr_b16 a[172:173], v229 offset:8704\n\t" \
  "v_mfma_f32_16x16x32_bf16 v[72:75], a[232:235], v[88:91], v[72:75]\n\t" \
  "v_exp_f32 v8, v8\n\t" \
  "v_mfma_f32_16x16x32_bf16 v[72:75], a[236:239], v[92:95], v[72:75]\n\t" \
  "ds_read_b64_tr_b16 a[174:175], v229 offset:12800\n\t" \
  "v_mfma_f32_16x16x32_bf16 v[76:79], a[252:255], v[92:95], v[184:187]\n\t" \
  "ds_read_b64_tr_b16 a[176:177], v228 offset:768\n\t" \
  "v_mfma_f32_16x16x32_bf16 v[76:79], a[248:251], v[88:91], v[76:79]\n\t" \
  "v_exp_f32 v9, v9\n\t" \
  "v_mfma_f32_16x16x32_bf16 v[76:79], a[244:247], v[84:87], v[76:79]\n\t" \
  "ds_read_b64_tr_b16 a[178:179], v228 offset:4864\n\t" \
  "s_mov_b32 s96, 18432\n\t" \
  "v_mfma_f32_16x16x32_bf16 v[76:79], a[240:243], v[80:83], v[76:79]\n\t" \
  "v_exp_f32 v10, v10\n\t" \
  "v_mfma_f32_16x16x32_bf16 v[96:99], a[192:195], v[112:115], v[188:191]\n\t" \
  "ds_read_b64_tr_b16 a[180:181], v228 offset:8960\n\t" \
  "v_mfma_f32_16x16x32_bf16 v[96:99], a[196:199], v[116:119], v[96:99]\n\t" \
  "ds_read_b64_tr_b16 a[182:183], v228 offset:13056\n\t" \
  "v_mfma_f32_16x16x32_bf16 v[96:99], a[200:203], v[120:123], v[96:99]\n\t" \
  "buffer_load_dwordx4 v232, s[72:75], s96 offen offset:3072 lds\n\t" \
  "v_mfma_f32_16x16x32_bf16 v[96:99], a[204:207], v[124:127], v[96:99]\n\t" \
  "ds_read_b64_tr_b16 a[184:185], v229 offset:768\n\t" \
  "v_mfma_f32_16x16x32_bf16 v[100:103], a[220:223], v[124:127], v[188:191]\n\t" \
  "ds_read_b64_tr_b16 a[186:187], v229 offset:4864\n\t" \
  "v_mfma_f32_16x16x32_bf16 v[100:103], a[216:219], v[120:123], v[100:103]\n\t" \
  "v_exp_f32 v11, v11\n\t" \
  "v_mfma_f32_16x16x32_bf16 v[100:103], a[212:215], v[116:119], v[100:103]\n\t" \
  "ds_read_b64_tr_b16 a[188:189], v229 offset:8960\n\t" \
  "v_mfma_f32_16x16x32_bf16 v[100:103], a[208:211], v[112:115], v[100:103]\n\t" \
  "ds_read_b64_tr_b16 a[190:191], v229 offset:13056\n\t" \
  "v_mfma_f32_16x16x32_bf16 v[104:107], a[224:227], v[112:115], v[188:191]\n\t" \
  "v_exp_f32 v12, v12\n\t" \
  "v_mfma_f32_16x16x32_bf16 v[104:107], a[228:231], v[116:119], v[104:107]\n\t" \
  "s_getreg_b32 s98, hwreg(HW_REG_IB_STS, 0, 4)\n\t" \
  "s_cmp_lg_u32 s98, 0\n\t" \
  "s_addc_u32 %[na], %[na], 0\n\t" \
  "v_mfma_f32_16x16x32_bf16 v[104:107], a[232:235], v[120:123], v[104:107]\n\t" \
  "v_exp_f32 v13, v13\n\t" \
  "v_mfma_f32_16x16x32_bf16 v[104:107], a[236:239], v[124:127], v[104:107]\n\t" \
  "s_getreg_b32 s98, hwreg(HW_REG_IB_STS, 0, 4)\n\t" \
  "s_cmp_lg_u32 s98, 0\n\t" \
  "s_addc_u32 %[na], %[na], 0\n\t" \
  "v_mfma_f32_16x16x32_bf16 v[108:111], a[252:255], v[124:127], v[188:191]\n\t" \
  "v_exp_f32 v14, v14\n\t" \
  "v_mfma_f32_16x16x32_bf16 v[108:111], a[248:251], v[120:123], v[108:111]\n\t" \
  "s_getreg_b32 s98, hwreg(HW_REG_IB_STS, 0, 4)\n\t" \
  "s_cmp_lg_u32 s98, 0\n\t" \
  "s_addc_u32 %[na], %[na], 0\n\t" \
  "v_mfma_f32_16x16x32_bf16 v[108:111], a[244:247], v[116:119], v[108:111]\n\t" \
  "s_waitcnt vmcnt(9) lgkmcnt(0)\n\t" \
  "s_getreg_b32 s98, hwreg(HW_REG_IB_STS, 0, 4)\n\t" \
  "s_cmp_lg_u32 s98, 0\n\t" \
  "s_addc_u32 %[na], %[na], 0\n\t" \
  "v_mfma_f32_16x16x32_bf16 v[108:111], a[240:243], v[112:115], v[108:111]\n\t" \
  "s_barrier\n\t" \
  STA("s_memrealtime s[94:95]\n\t") \
  STA("s_memtime s[92:93]\n\t") \
  "v_mfma_f32_16x16x32_bf16 a[0:3], a[128:131], v[128:131], a[0:3]\n\t" \
  "v_exp_f32 v15, v15\n\t" \
  "v_mfma_f32_16x16x32_bf16 a[0:3], a[132:135], v[132:135], a[0:3]\n\t" \
  "v_exp_f32 v32, v32\n\t" \
  "v_mfma_f32_16x16x32_bf16 a[4:7], a[136:139], v[128:131], a[4:7]\n\t" \
  "v_exp_f32 v33, v33\n\t" \
  "v_mfma_f32_16x16x32_bf16 a[4:7], a[140:143], v[132:135], a[4:7]\n\t" \
  "v_exp_f32 v34, v34\n\t" \
  "v_mfma_f32_16x16x32_bf16 a[8:11], a[144:147], v[128:131], a[8:11]\n\t" \
  STS("v_writelane_b32 %[stv], %[na], 58\n\t") \
  "s_cmp_ge_u32 %[na], 2\n\t" \
  "s_cselect_b32 s98, 0x4000, 0\n\t" \
  "s_or_b32 s98, s98, %[tag]\n\t" \
  "v_mfma_f32_16x16x32_bf16 a[8:11], a[148:151], v[132:135], a[8:11]\n\t" \
  "s_cmp_ge_u32 %[na], 3\n\t" \
  "s_cselect_b32 %[na], 2.0, 0\n\t" \
  "s_or_b32 s98, s98, %[na]\n\t" \
  "v_mfma_f32_16x16x32_bf16 a[12:15], a[152:155], v[128:131], a[12:15]\n\t" \
  "ds_read_b128 a[192:195], v220 offset:0\n\t" \
  "v_mov_b32 v209, s98\n\t" \
  "v_mfma_f32_16x16x32_bf16 a[12:15], a[156:159], v[132:135], a[12:15]\n\t" \
  "ds_read_b128 a[196:199], v221 offset:0\n\t" \
  "v_bfrev_b32 v250, 1\n\t" \
  "v_mfma_f32_16x16x32_bf16 a[16:19], a[160:163], v[128:131], a[16:19]\n\t" \
  "v_writelane_b32 v250, %[vo], 0\n\t" \
  "v_mfma_f32_16x16x32_bf16 a[16:19], a[164:167], v[132:135], a[16:19]\n\t" \
  "v_exp_f32 v35, v35\n\t" \
  "v_mfma_f32_16x16x32_bf16 a[20:23], a[168:171], v[128:131], a[20:23]\n\t" \
  "buffer_store_dword v209, v250, %[ws], 0 offen sc1\n\t" \
  "ds_read_b128 a[200:203], v222 offset:0\n\t" \
  "v_mfma_f32_16x16x32_bf16 a[20:23], a[172:175], v[132:135], a[20:23]\n\t" \
  "v_exp_f32 v36, v36\n\t" \
  "v_mfma_f32_16x16x32_bf16 a[24:27], a[176:179], v[128:131], a[24:27]\n\t" \
  STA("v_writelane_b32 %[stv], s94, 36\n\t") \
  STA("v_writelane_b32 %[stv], s92, 37\n\t") \
  "v_exp_f32 v37, v37\n\t" \
  "v_mfma_f32_16x16x32_bf16 a[24:27], a[180:183], v[132:135], a[24:27]\n\t" \
  "v_exp_f32 v38, v38\n\t" \
  "v_mfma_f32_16x16x32_bf16 a[28:31], a[184:187], v[128:131], a[28:31]\n\t" \
  "v_exp_f32 v39, v39\n\t" \
  "v_mfma_f32_16x16x32_bf16 a[28:31], a[188:191], v[132:135], a[28:31]\n\t" \
  "v_exp_f32 v40, v40\n\t" \
  "v_mfma_f32_16x16x32_bf16 v[160:163], v[216:219], v[128:131], v[160:163]\n\t" \
  "s_add_u32 m0, %[m], 65536\n\t" \
  "v_exp_f32 v41, v41\n\t" \
  "v_mfma_f32_16x16x32_bf16 v[160:163], v[216:219], v[132:135], v[160:163]\n\t" \
  "s_add_u32 s96, %[m], 81920\n\t" \
  "v_exp_f32 v42, v42\n\t" \
  "v_mfma_f32_16x16x32_bf16 a[32:35], a[128:131], v[136:139], a[32:35]\n\t" \
  "v_exp_f32 v43, v43\n\t" \
  "v_mfma_f32_16x16x32_bf16 a[32:35], a[132:135], v[140:143], a[32:35]\n\t" \
  "v_exp_f32 v44, v44\n\t" \
  "v_mfma_f32_16x16x32_bf16 a[36:39], a[136:139], v[136:139], a[36:39]\n\t" \
  "buffer_load_dwordx4 v224, s[60:63], s96 offen offset:0 lds\n\t" \
  "v_mfma_f32_16x16x32_bf16 a[36:39], a[140:143], v[140:143], a[36:39]\n\t" \
  "v_exp_f32 v45, v45\n\t" \
  "v_mfma_f32_16x16x32_bf16 a[40:43], a[144:147], v[136:139], a[40:43]\n\t" \
  "v_exp_f32 v46, v46\n\t" \
  "v_mfma_f32_16x16x32_bf16 a[40:43], a[148:151], v[140:143], a[40:43]\n\t" \
  "ds_read_b128 a[204:207], v223 offset:0\n\t" \
  "v_cvt_pk_bf16_f32 v128, v0, v1\n\t" \
  "v_mfma_f32_16x16x32_bf16 a[44:47], a[152:155], v[136:139], a[44:47]\n\t" \
  "buffer_load_dwordx4 v225, s[60:63], s96 offen offset:1024 lds\n\t" \
  "v_mfma_f32_16x16x32_bf16 a[44:47], a[156:159], v[140:143], a[44:47]\n\t" \
  "v_exp_f32 v47, v47\n\t" \
  "v_mfma_f32_16x16x32_bf16 a[48:51], a[160:163], v[136:139], a[48:51]\n\t" \
  "ds_read_b128 a[208:211], v220 offset:4096\n\t" \
  "v_cvt_pk_bf16_f32 v129, v2, v3\n\t" \
  "v_mfma_f32_16x16x32_bf16 a[48:51], a[164:167], v[140:143], a[48:51]\n\t" \
  "v_exp_f32 v64, v64\n\t" \
  "v_mfma_f32_16x16x32_bf16 a[52:55], a[168:171], v[136:139], a[52:55]\n\t" \
  "buffer_load_dwordx4 v226, s[60:63], s96 offen offset:2048 lds\n\t" \
  "v_mfma_f32_16x16x32_bf16 a[52:55], a[172:175], v[140:143], a[52:55]\n\t" \
  "ds_read_b128 a[212:215], v221 offset:4096\n\t" \
  "v_cvt_pk_bf16_f32 v130, v4, v5\n\t" \
  "v_mfma_f32_16x16x32_bf16 a[56:59], a[176:179], v[136:139], a[56:59]\n\t" \
  "v_exp_f32 v65, v65\n\t" \
  "v_mfma_f32_16x16x32_bf16 a[56:59], a[180:183], v[140:143], a[56:59]\n\t" \
  "v_exp_f32 v66, v66\n\t" \
  "v_mfma_f32_16x16x32_bf16 a[60:63], a[184:187], v[136:139], a[60:63]\n\t" \
  "buffer_load_dwordx4 v227, s[60:63], s96 offen offset:3072 lds\n\t" \
  "v_mfma_f32_16x16x32_bf16 a[60:63], a[188:191], v[140:143], a[60:63]\n\t" \
  "s_add_u32 m0, %[m], 81920\n\t" \
  "ds_read_b128 a[216:219], v222 offset:4096\n\t" \
  "v_cvt_pk_bf16_f32 v131, v6, v7\n\t" \
  "v_mfma_f32_16x16x32_bf16 v[164:167], v[216:219], v[136:139], v[164:167]\n\t" \
  "s_add_u32 s96, %[m], 65536\n\t" \
  "v_exp_f32 v67, v67\n\t" \
  "v_mfma_f32_16x16x32_bf16 v[164:167], v[216:219], v[140:143], v[164:167]\n\t" \
  "ds_read_b128 a[220:223], v223 offset:4096\n\t" \
  "v_cvt_pk_bf16_f32 v132, v8, v9\n\t" \
  "v_mfma_f32_16x16x32_bf16 a[64:67], a[128:131], v[144:147], a[64:67]\n\t" \
  "buffer_load_dwordx4 v230, s[64:67], s96 offen offset:0 lds\n\t" \
  "v_mfma_f32_16x16x32_bf16 a[64:67], a[132:135], v[148:151], a[64:67]\n\t" \
  "v_exp_f32 v68, v68\n\t" \
  "v_mfma_f32_16x16x32_bf16 a[68:71], a[136:139], v[144:147], a[68:71]\n\t" \
  "ds_read_b128 a[224:227], v220 offset:8192\n\t" \
  "v_cvt_pk_bf16_f32 v133, v10, v11\n\t" \
  "v_mfma_f32_16x16x32_bf16 a[68:71], a[140:143], v[148:151], a[68:71]\n\t" \
  "v_exp_f32 v69, v69\n\t" \
  "v_mfma_f32_16x16x32_bf16 a[72:75], a[144:147], v[144:147], a[72:75]\n\t" \
  "buffer_load_dwordx4 v231, s[64:67], s96 offen offset:1024 lds\n\t" \
  "v_mfma_f32_16x16x32_bf16 a[72:75], a[148:151], v[148:151], a[72:75]\n\t" \
  "ds_read_b128 a[228:231], v221 offset:8192\n\t" \
  "v_cvt_pk_bf16_f32 v134, v12, v13\n\t" \
  "v_mfma_f32_16x16x32_bf16 a[76:79], a[152:155], v[144:147], a[76:79]\n\t" \
  "v_exp_f32 v70, v70\n\t" \
  "v_mfma_f32_16x16x32_bf16 a[76:79], a[156:159], v[148:151], a[76:79]\n\t" \
  "v_exp_f32 v71, v71\n\t" \
  "v_mfma_f32_16x16x32_bf16 a[80:83], a[160:163], v[144:147], a[80:83]\n\t" \
  "buffer_load_dwordx4 v230, s[64:67], s96 offen offset:2048 lds\n\t" \
  "v_mfma_f32_16x16x32_bf16 a[80:83], a[164:167], v[148:151], a[80:83]\n\t" \
  "ds_read_b128 a[232:235], v222 offset:8192\n\t" \
  "v_cvt_pk_bf16_f32 v135, v14, v15\n\t" \
  "v_mfma_f32_16x16x32_bf16 a[84:87], a[168:171], v[144:147], a[84:87]\n\t" \
  "v_exp_f32 v72, v72\n\t" \
  "v_mfma_f32_16x16x32_bf16 a[84:87], a[172:175], v[148:151], a[84:87]\n\t" \
  "ds_read_b128 a[236:239], v223 offset:8192\n\t" \
  "v_cvt_pk_bf16_f32 v136, v32, v33\n\t" \
  "v_mfma_f32_16x16x32_bf16 a[88:91], a[176:179], v[144:147], a[88:91]\n\t" \
  "buffer_load_dwordx4 v231, s[64:67], s96 offen offset:3072 lds\n\t" \
  "v_mfma_f32_16x16x32_bf16 a[88:91], a[180:183], v[148:151], a[88:91]\n\t" \
  "s_add_u32 m0, %[m], 131072\n\t" \
  "v_exp_f32 v73, v73\n\t" \
  "v_mfma_f32_16x16x32_bf16 a[92:95], a[184:187], v[144:147], a[92:95]\n\t" \
  "s_mov_b32 s96, 28672\n\t" \
  "ds_read_b128 a[240:243], v220 offset:12288\n\t" \
  "v_cvt_pk_bf16_f32 v137, v34, v35\n\t" \
  "v_mfma_f32_16x16x32_bf16 a[92:95], a[188:191], v[148:151], a[92:95]\n\t" \
  "v_exp_f32 v74, v74\n\t" \
  "v_mfma_f32_16x16x32_bf16 v[168:171], v[216:219], v[144:147], v[168:171]\n\t" \
  "buffer_load_dwordx4 v232, s[72:75], s96 offen offset:0 lds\n\t" \
  "v_mfma_f32_16x16x32_bf16 v[168:171], v[216:219], v[148:151], v[168:171]\n\t" \
  "s_mov_b32 s96, 34816\n\t" \
  "ds_read_b128 a[244:247], v221 offset:12288\n\t" \
  "v_cvt_pk_bf16_f32 v138, v36, v37\n\t" \
  "v_mfma_f32_16x16x32_bf16 a[96:99], a[128:131], v[152:155], a[96:99]\n\t" \
  "v_exp_f32 v75, v75\n\t" \
  "v_mfma_f32_16x16x32_bf16 a[96:99], a[132:135], v[156:159], a[96:99]\n\t" \
  "v_cvt_pk_bf16_f32 v139, v38, v39\n\t" \
  "v_cvt_pk_bf16_f32 v140, v40, v41\n\t" \
  "v_mfma_f32_16x16x32_bf16 a[100:103], a[136:139], v[152:155], a[100:103]\n\t" \
  "buffer_load_dwordx4 v232, s[72:75], s96 offen offset:1024 lds\n\t" \
  "v_mfma_f32_16x16x32_bf16 a[100:103], a[140:143], v[156:159], a[100:103]\n\t" \
  "s_mov_b32 s96, 40960\n\t" \
  "ds_read_b128 a[248:251], v222 offset:12288\n\t" \
  "v_cvt_pk_bf16_f32 v141, v42, v43\n\t" \
  "v_mfma_f32_16x16x32_bf16 a[104:107], a[144:147], v[152:155], a[104:107]\n\t" \
  "v_exp_f32 v76, v76\n\t" \
  "v_mfma_f32_16x16x32_bf16 a[104:107], a[148:151], v[156:159], a[104:107]\n\t" \
  "ds_read_b128 a[252:255], v223 offset:12288\n\t" \
  "v_cvt_pk_bf16_f32 v142, v44, v45\n\t" \
  "v_mfma_f32_16x16x32_bf16 a[108:111], a[152:155], v[152:155], a[108:111]\n\t" \
  "buffer_load_dwordx4 v232, s[72:75], s96 offen offset:2048 lds\n\t" \
  "v_mfma_f32_16x16x32_bf16 a[108:111], a[156:159], v[156:159], a[108:111]\n\t" \
  "s_mov_b32 s96, 47104\n\t" \
  "v_exp_f32 v77, v77\n\t" \
  "v_mfma_f32_16x16x32_bf16 a[112:115], a[160:163], v[152:155], a[112:115]\n\t" \
  "v_exp_f32 v78, v78\n\t" \
  "v_mfma_f32_16x16x32_bf16 a[112:115], a[164:167], v[156:159], a[112:115]\n\t" \
  "v_exp_f32 v79, v79\n\t" \
  "v_mfma_f32_16x16x32_bf16 a[116:119], a[168:171], v[152:155], a[116:119]\n\t" \
  "buffer_load_dwordx4 v232, s[72:75], s96 offen offset:3072 lds\n\t" \
  "v_mfma_f32_16x16x32_bf16 a[116:119], a[172:175], v[156:159], a[116:119]\n\t" \
  "v_cvt_pk_bf16_f32 v143, v46, v47\n\t" \
  "v_cvt_pk_bf16_f32 v144, v64, v65\n\t" \
  "v_mfma_f32_16x16x32_bf16 a[120:123], a[176:179], v[152:155], a[120:123]\n\t" \
  "v_exp_f32 v96, v96\n\t" \
  "v_mfma_f32_16x16x32_bf16 a[120:123], a[180:183], v[156:159], a[120:123]\n\t" \
  "s_add_u32 s99, %[pf1], 0\n\t" \
  "v_exp_f32 v97, v97\n\t" \
  "v_mfma_f32_16x16x32_bf16 a[124:127], a[184:187], v[152:155], a[124:127]\n\t" \
  "v_cvt_pk_bf16_f32 v145, v66, v67\n\t" \
  "v_cvt_pk_bf16_f32 v146, v68, v69\n\t" \
  "v_mfma_f32_16x16x32_bf16 a[124:127], a[188:191], v[156:159], a[124:127]\n\t" \
  "v_exp_f32 v98, v98\n\t" \
  "v_mfma_f32_16x16x32_bf16 v[172:175], v[216:219], v[152:155], v[172:175]\n\t" \
  "buffer_load_dword v246, v212, %[wo], s99 offen\n\t" \
  "v_mfma_f32_16x16x32_bf16 v[172:175], v[216:219], v[156:159], v[172:175]\n\t" \
  "s_waitcnt lgkmcnt(12)\n\t" \
  "v_exp_f32 v99, v99\n\t" \
  STA("s_memrealtime s[94:95]\n\t") \
  STA("s_memtime s[92:93]\n\t") \
  "v_mfma_f32_16x16x32_bf16 v[0:3], a[192:195], v[16:19], v[176:179]\n\t" \
  "s_lshr_b32 s78, %[m], 6\n\t" \
  "ds_read_b64_tr_b16 a[128:129], v210 offset:0\n\t" \
  "v_cvt_pk_bf16_f32 v147, v70, v71\n\t" \
  "v_mfma_f32_16x16x32_bf16 v[0:3], a[196:199], v[20:23], v[0:3]\n\t" \
  "v_exp_f32 v100, v100\n\t" \
  "v_mfma_f32_16x16x32_bf16 v[0:3], a[200:203], v[24:27], v[0:3]\n\t" \
  "ds_read_b64_tr_b16 a[130:131], v210 offset:4096\n\t" \
  "v_cvt_pk_bf16_f32 v148, v72, v73\n\t" \
  "v_mfma_f32_16x16x32_bf16 v[0:3], a[204:207], v[28:31], v[0:3]\n\t" \
  "s_waitcnt lgkmcnt(10)\n\t" \
  "v_exp_f32 v101, v101\n\t" \
  "v_mfma_f32_16x16x32_bf16 v[4:7], a[220:223], v[28:31], v[176:179]\n\t" \
  "ds_read_b64_tr_b16 a[132:133], v210 offset:8192\n\t" \
  "v_cvt_pk_bf16_f32 v149, v74, v75\n\t" \
  "v_mfma_f32_16x16x32_bf16 v[4:7], a[216:219], v[24:27], v[4:7]\n\t" \
  "v_exp_f32 v102, v102\n\t" \
  "v_mfma_f32_16x16x32_bf16 v[4:7], a[212:215], v[20:23], v[4:7]\n\t" \
  "ds_read_b64_tr_b16 a[134:135], v210 offset:12288\n\t" \
  "v_cvt_pk_bf16_f32 v150, v76, v77\n\t" \
  "v_mfma_f32_16x16x32_bf16 v[4:7], a[208:211], v[16:19], v[4:7]\n\t" \
  "s_waitcnt lgkmcnt(8)\n\t" \
  "ds_read_b64_tr_b16 a[136:137], v211 offset:0\n\t" \
  "v_cvt_pk_bf16_f32 v151, v78, v79\n\t" \
  "v_mfma_f32_16x16x32_bf16 v[8:11], a[224:227], v[16:19], v[176:179]\n\t" \
  "v_exp_f32 v103, v103\n\t" \
  "v_mfma_f32_16x16x32_bf16 v[8:11], a[228:231], v[20:23], v[8:11]\n\t" \
  "s_add_u32 s99, %[pf1], 2048\n\t" \
  "ds_read_b64_tr_b16 a[138:139], v211 offset:4096\n\t" \
  "v_cvt_pk_bf16_f32 v152, v96, v97\n\t" \
  "v_mfma_f32_16x16x32_bf16 v[8:11], a[232:235], v[24:27], v[8:11]\n\t" \
  "v_exp_f32 v104, v104\n\t" \
  "v_mfma_f32_16x16x32_bf16 v[8:11], a[236:239], v[28:31], v[8:11]\n\t" \
  "s_waitcnt lgkmcnt(6)\n\t" \
  "ds_read_b64_tr_b16 a[140:141], v211 offset:8192\n\t" \
  "v_cvt_pk_bf16_f32 v153, v98, v99\n\t" \
  "v_mfma_f32_16x16x32_bf16 v[12:15], a[252:255], v[28:31], v[176:179]\n\t" \
  STA("v_writelane_b32 %[stv], s94, 38\n\t") \
  STA("v_writelane_b32 %[stv], s92, 39\n\t") \
  "buffer_load_dword v246, v212, %[wo], s99 offen\n\t" \
  "v_mfma_f32_16x16x32_bf16 v[12:15], a[248:251], v[24:27], v[12:15]\n\t" \
  "ds_read_b64_tr_b16 a[142:143], v211 offset:12288\n\t" \
  "v_cvt_pk_bf16_f32 v154, v100, v101\n\t" \
  "v_mfma_f32_16x16x32_bf16 v[12:15], a[244:247], v[20:23], v[12:15]\n\t" \
  "ds_read_b64_tr_b16 a[144:145], v210 offset:256\n\t" \
  "v_cvt_pk_bf16_f32 v155, v102, v103\n\t" \
  "v_mfma_f32_16x16x32_bf16 v[12:15], a[240:243], v[16:19], v[12:15]\n\t" \
  "v_exp_f32 v105, v105\n\t" \
  "v_mfma_f32_16x16x32_bf16 v[32:35], a[192:195], v[48:51], v[180:183]\n\t" \
  "ds_read_b64_tr_b16 a[146:147], v210 offset:4352\n\t" \
  "v_cvt_pk_bf16_f32 v156, v104, v105\n\t" \
  "v_mfma_f32_16x16x32_bf16 v[32:35], a[196:199], v[52:55], v[32:35]\n\t" \
  "v_exp_f32 v106, v106\n\t" \
  "v_mfma_f32_16x16x32_bf16 v[32:35], a[200:203], v[56:59], v[32:35]\n\t" \
  "ds_read_b64_tr_b16 a[148:149], v210 offset:8448\n\t" \
  "v_xor_b32 v234, s78, v244\n\t" \
  "v_mfma_f32_16x16x32_bf16 v[32:35], a[204:207], v[60:63], v[32:35]\n\t" \
  "v_exp_f32 v107, v107\n\t" \
  "v_mfma_f32_16x16x32_bf16 v[36:39], a[220:223], v[60:63], v[180:183]\n\t" \
  ".Lhs7:\n\ts_cbranch_execz .Lhs7n\n\t" \
  "ds_read_b64_tr_b16 a[150:151], v210 offset:12544\n\t" \
  "v_cvt_pk_bf16_f32 v157, v106, v107\n\t" \
  "v_mfma_f32_16x16x32_bf16 v[36:39], a[216:219], v[56:59], v[36:39]\n\t" \
  "ds_read_b64_tr_b16 a[152:153], v211 offset:256\n\t" \
  "v_xor_b32 v235, 32, v234\n\t" \
  "v_mfma_f32_16x16x32_bf16 v[36:39], a[212:215], v[52:55], v[36:39]\n\t" \
  "v_exp_f32 v108, v108\n\t" \
  "v_mfma_f32_16x16x32_bf16 v[36:39], a[208:211], v[48:51], v[36:39]\n\t" \
  "ds_read_b64_tr_b16 a[154:155], v211 offset:4352\n\t" \
  "v_add_u32 v236, 65536, v234\n\t" \
  "v_mfma_f32_16x16x32_bf16 v[40:43], a[224:227], v[48:51], v[180:183]\n\t" \
  "v_exp_f32 v109, v109\n\t" \
  "v_mfma_f32_16x16x32_bf16 v[40:43], a[228:231], v[52:55], v[40:43]\n\t" \
  "ds_read_b64_tr_b16 a[156:157], v211 offset:8448\n\t" \
  "v_cvt_pk_bf16_f32 v158, v108, v109\n\t" \
  "v_mfma_f32_16x16x32_bf16 v[40:43], a[232:235], v[56:59], v[40:43]\n\t" \
  "v_exp_f32 v110, v110\n\t" \
  "v_mfma_f32_16x16x32_bf16 v[40:43], a[236:239], v[60:63], v[40:43]\n\t" \
  "ds_read_b64_tr_b16 a[158:159], v211 offset:12544\n\t" \
  "v_add_u32 v237, 65536, v235\n\t" \
  "v_mfma_f32_16x16x32_bf16 v[44:47], a[252:255], v[60:63], v[180:183]\n\t" \
  "v_exp_f32 v111, v111\n\t" \
  "v_mfma_f32_16x16x32_bf16 v[44:47], a[248:251], v[56:59], v[44:47]\n\t" \
  "ds_read_b64_tr_b16 a[160:161], v210 offset:512\n\t" \
  "v_cvt_pk_bf16_f32 v159, v110, v111\n\t" \
  "v_mfma_f32_16x16x32_bf16 v[44:47], a[244:247], v[52:55], v[44:47]\n\t" \
  "ds_read_b64_tr_b16 a[162:163], v210 offset:4608\n\t" \
  "v_add_u32 v238, 131072, v234\n\t" \
  "v_mfma_f32_16x16x32_bf16 v[44:47], a[240:243], v[48:51], v[44:47]\n\t" \
  "v_exp_f32 v0, v0\n\t" \
  "v_mfma_f32_16x16x32_bf16 v[64:67], a[192:195], v[80:83], v[184:187]\n\t" \
  "ds_read_b64_tr_b16 a[164:165], v210 offset:8704\n\t" \
  "v_add_u32 v239, 131072, v235\n\t" \
  "v_mfma_f32_16x16x32_bf16 v[64:67], a[196:199], v[84:87], v[64:67]\n\t" \
  "v_exp_f32 v1, v1\n\t" \
  "v_mfma_f32_16x16x32_bf16 v[64:67], a[200:203], v[88:91], v[64:67]\n\t" \
  "ds_read_b64_tr_b16 a[166:167], v210 offset:12800\n\t" \
  "v_lshlrev_b32 v242, 4, v233\n\t" \
  "v_mfma_f32_16x16x32_bf16 v[64:67], a[204:207], v[92:95], v[64:67]\n\t" \
  "ds_read_b64_tr_b16 a[168:169], v211 offset:512\n\t" \
  "v_add_u32 v213, %[m], v242\n\t" \
  "v_mfma_f32_16x16x32_bf16 v[68:71], a[220:223], v[92:95], v[184:187]\n\t" \
  "v_exp_f32 v2, v2\n\t" \
  "v_mfma_f32_16x16x32_bf16 v[68:71], a[216:219], v[88:91], v[68:71]\n\t" \
  "ds_read_b64_tr_b16 a[170:171], v211 offset:4608\n\t" \
  "v_add_u32 v215, 98304, v213\n\t" \
  "v_mfma_f32_16x16x32_bf16 v[68:71], a[212:215], v[84:87], v[68:71]\n\t" \
  "v_exp_f32 v3, v3\n\t" \
  "v_mfma_f32_16x16x32_bf16 v[68:71], a[208:211], v[80:83], v[68:71]\n\t" \
  "ds_read_b64_tr_b16 a[172:173], v211 offset:8704\n\t" \
  "v_mov_b32 v243, %[tag]\n\t" \
  "v_mfma_f32_16x16x32_bf16 v[72:75], a[224:227], v[80:83], v[184:187]\n\t" \
  "v_exp_f32 v4, v4\n\t" \
  "v_mfma_f32_16x16x32_bf16 v[72:75], a[228:231], v[84:87], v[72:75]\n\t" \
  "ds_read_b64_tr_b16 a[174:175], v211 offset:12800\n\t" \
  "v_lshlrev_b32 v254, 9, v233\n\t" \
  "v_mfma_f32_16x16x32_bf16 v[72:75], a[232:235], v[88:91], v[72:75]\n\t" \
  "ds_read_b64_tr_b16 a[176:177], v210 offset:768\n\t" \
  "v_and_b32 v244, 0x38, v233\n\t" \
  "v_mfma_f32_16x16x32_bf16 v[72:75], a[236:239], v[92:95], v[72:75]\n\t" \
  "v_exp_f32 v5, v5\n\t" \
  "v_mfma_f32_16x16x32_bf16 v[76:79], a[252:255], v[92:95], v[184:187]\n\t" \
  "ds_read_b64_tr_b16 a[178:179], v210 offset:4864\n\t" \
  "v_lshl_or_b32 v254, v244, 26, v254\n\t" \
  "v_mfma_f32_16x16x32_bf16 v[76:79], a[248:251], v[88:91], v[76:79]\n\t" \
  "s_brev_b32 s78, 1\n\t" \
  "v_exp_f32 v6, v6\n\t" \
  "v_mfma_f32_16x16x32_bf16 v[76:79], a[244:247], v[84:87], v[76:79]\n\t" \
  "ds_read_b64_tr_b16 a[180:181], v210 offset:8960\n\t" \
  "v_mov_b32 v244, s78\n\t" \
  "v_mfma_f32_16x16x32_bf16 v[76:79], a[240:243], v[80:83], v[76:79]\n\t" \
  "v_exp_f32 v7, v7\n\t" \
  "v_mfma_f32_16x16x32_bf16 v[96:99], a[192:195], v[112:115], v[188:191]\n\t" \
  "ds_read_b64_tr_b16 a[182:183], v210 offset:13056\n\t" \
  "v_cmp_eq_u32 vcc, %[hl], v233\n\t" \
  "v_mfma_f32_16x16x32_bf16 v[96:99], a[196:199], v[116:119], v[96:99]\n\t" \
  "ds_read_b64_tr_b16 a[184:185], v211 offset:768\n\t" \
  "v_add_u32 v245, %[hl], v233\n\t" \
  "v_mfma_f32_16x16x32_bf16 v[96:99], a[200:203], v[120:123], v[96:99]\n\t" \
  "v_exp_f32 v8, v8\n\t" \
  "v_mfma_f32_16x16x32_bf16 v[96:99], a[204:207], v[124:127], v[96:99]\n\t" \
  "ds_read_b64_tr_b16 a[186:187], v211 offset:4864\n\t" \
  "v_bfi_b32 v245, 7, v245, v233\n\t" \
  "v_mfma_f32_16x16x32_bf16 v[100:103], a[220:223], v[124:127], v[188:191]\n\t" \
  "v_exp_f32 v9, v9\n\t" \
  "v_mfma_f32_16x16x32_bf16 v[100:103], a[216:219], v[120:123], v[100:103]\n\t" \
  "ds_read_b64_tr_b16 a[188:189], v211 offset:8960\n\t" \
  "v_cndmask_b32 v254, v254, v244, vcc\n\t" \
  "v_mfma_f32_16x16x32_bf16 v[100:103], a[212:215], v[116:119], v[100:103]\n\t" \
  "v_exp_f32 v10, v10\n\t" \
  "v_mfma_f32_16x16x32_bf16 v[100:103], a[208:211], v[112:115], v[100:103]\n\t" \
  "ds_read_b64_tr_b16 a[190:191], v211 offset:13056\n\t" \
  "v_and_b32 v245, 31, v245\n\t" \
  "v_mfma_f32_16x16x32_bf16 v[104:107], a[224:227], v[112:115], v[188:191]\n\t" \
  "v_exp_f32 v11, v11\n\t" \
  "v_mfma_f32_16x16x32_bf16 v[104:107], a[228:231], v[116:119], v[104:107]\n\t" \
  "v_exp_f32 v12, v12\n\t" \
  "v_mfma_f32_16x16x32_bf16 v[104:107], a[232:235], v[120:123], v[104:107]\n\t" \
  "v_exp_f32 v13, v13\n\t" \
  "v_mfma_f32_16x16x32_bf16 v[104:107], a[236:239], v[124:127], v[104:107]\n\t" \
  "v_exp_f32 v14, v14\n\t" \
  "v_mfma_f32_16x16x32_bf16 v[108:111], a[252:255], v[124:127], v[188:191]\n\t" \
  "v_exp_f32 v15, v15\n\t" \
  "v_mfma_f32_16x16x32_bf16 v[108:111], a[248:251], v[120:123], v[108:111]\n\t" \
  "v_exp_f32 v32, v32\n\t" \
  "v_mfma_f32_16x16x32_bf16 v[108:111], a[244:247], v[116:119], v[108:111]\n\t" \
  "s_waitcnt vmcnt(2) lgkmcnt(0)\n\t" \
  "v_exp_f32 v33, v33\n\t" \
  "v_mfma_f32_16x16x32_bf16 v[108:111], a[240:243], v[112:115], v[108:111]\n\t" \
  "s_barrier\n\t" \
  STA("s_memrealtime s[94:95]\n\t") \
  STA("s_memtime s[92:93]\n\t") \
  "v_mfma_f32_16x16x32_bf16 a[0:3], a[128:131], v[128:131], a[0:3]\n\t" \
  "v_exp_f32 v34, v34\n\t" \
  "v_mfma_f32_16x16x32_bf16 a[0:3], a[132:135], v[132:135], a[0:3]\n\t" \
  "v_exp_f32 v35, v35\n\t" \
  "v_mfma_f32_16x16x32_bf16 a[4:7], a[136:139], v[128:131], a[4:7]\n\t" \
  "v_exp_f32 v36, v36\n\t" \
  "v_mfma_f32_16x16x32_bf16 a[4:7], a[140:143], v[132:135], a[4:7]\n\t" \
  "v_exp_f32 v37, v37\n\t" \
  "v_mfma_f32_16x16x32_bf16 a[8:11], a[144:147], v[128:131], a[8:11]\n\t" \
  "v_exp_f32 v38, v38\n\t" \
  "v_mfma_f32_16x16x32_bf16 a[8:11], a[148:151], v[132:135], a[8:11]\n\t" \
  "v_exp_f32 v39, v39\n\t" \
  "v_mfma_f32_16x16x32_bf16 a[12:15], a[152:155], v[128:131], a[12:15]\n\t" \
  "v_exp_f32 v40, v40\n\t" \
  "v_mfma_f32_16x16x32_bf16 a[12:15], a[156:159], v[132:135], a[12:15]\n\t" \
  "v_exp_f32 v41, v41\n\t" \
  "v_mfma_f32_16x16x32_bf16 a[16:19], a[160:163], v[128:131], a[16:19]\n\t" \
  "v_exp_f32 v42, v42\n\t" \
  "v_mfma_f32_16x16x32_bf16 a[16:19], a[164:167], v[132:135], a[16:19]\n\t" \
  "v_exp_f32 v43, v43\n\t" \
  "v_mfma_f32_16x16x32_bf16 a[20:23], a[168:171], v[128:131], a[20:23]\n\t" \
  "v_exp_f32 v44, v44\n\t" \
  "v_mfma_f32_16x16x32_bf16 a[20:23], a[172:175], v[132:135], a[20:23]\n\t" \
  "v_exp_f32 v45, v45\n\t" \
  "v_mfma_f32_16x16x32_bf16 a[24:27], a[176:179], v[128:131], a[24:27]\n\t" \
  STA("v_writelane_b32 %[stv], s94, 40\n\t") \
  STA("v_writelane_b32 %[stv], s92, 41\n\t") \
  "v_exp_f32 v46, v46\n\t" \
  "v_mfma_f32_16x16x32_bf16 a[24:27], a[180:183], v[132:135], a[24:27]\n\t" \
  "v_exp_f32 v47, v47\n\t" \
  "v_mfma_f32_16x16x32_bf16 a[28:31], a[184:187], v[128:131], a[28:31]\n\t" \
  "ds_read_b128 a[192:195], v220 offset:16384\n\t" \
  "v_lshlrev_b32 v245, 2, v245\n\t" \
  "v_mfma_f32_16x16x32_bf16 a[28:31], a[188:191], v[132:135], a[28:31]\n\t" \
  "v_exp_f32 v64, v64\n\t" \
  "v_mfma_f32_16x16x32_bf16 v[160:163], v[216:219], v[128:131], v[160:163]\n\t" \
  "s_add_u32 m0, %[m], 49152\n\t" \
  "v_exp_f32 v65, v65\n\t" \
  "v_mfma_f32_16x16x32_bf16 v[160:163], v[216:219], v[132:135], v[160:163]\n\t" \
  "s_add_u32 s96, %[m], 98304\n\t" \
  "ds_read_b128 a[196:199], v221 offset:16384\n\t" \
  "v_cvt_pk_bf16_f32 v128, v0, v1\n\t" \
  "v_mfma_f32_16x16x32_bf16 a[32:35], a[128:131], v[136:139], a[32:35]\n\t" \
  "v_cvt_pk_bf16_f32 v129, v2, v3\n\t" \
  "v_cvt_pk_bf16_f32 v130, v4, v5\n\t" \
  "v_mfma_f32_16x16x32_bf16 a[32:35], a[132:135], v[140:143], a[32:35]\n\t" \
  "v_exp_f32 v66, v66\n\t" \
  "v_mfma_f32_16x16x32_bf16 a[36:39], a[136:139], v[136:139], a[36:39]\n\t" \
  "buffer_load_dwordx4 v224, s[60:63], s96 offen offset:0 lds\n\t" \
  "v_mfma_f32_16x16x32_bf16 a[36:39], a[140:143], v[140:143], a[36:39]\n\t" \
  "ds_read_b128 a[200:203], v222 offset:16384\n\t" \
  "v_cvt_pk_bf16_f32 v131, v6, v7\n\t" \
  "v_mfma_f32_16x16x32_bf16 a[40:43], a[144:147], v[136:139], a[40:43]\n\t" \
  "v_exp_f32 v67, v67\n\t" \
  "v_mfma_f32_16x16x32_bf16 a[40:43], a[148:151], v[140:143], a[40:43]\n\t" \
  "ds_read_b128 a[204:207], v223 offset:16384\n\t" \
  "v_cvt_pk_bf16_f32 v132, v8, v9\n\t" \
  "v_mfma_f32_16x16x32_bf16 a[44:47], a[152:155], v[136:139], a[44:47]\n\t" \
  "v_exp_f32 v68, v68\n\t" \
  "v_mfma_f32_16x16x32_bf16 a[44:47], a[156:159], v[140:143], a[44:47]\n\t" \
  "v_exp_f32 v69, v69\n\t" \
  "v_mfma_f32_16x16x32_bf16 a[48:51], a[160:163], v[136:139], a[48:51]\n\t" \
  "buffer_load_dwordx4 v225, s[60:63], s96 offen offset:1024 lds\n\t" \
  "v_mfma_f32_16x16x32_bf16 a[48:51], a[164:167], v[140:143], a[48:51]\n\t" \
  "ds_read_b128 a[208:211], v220 offset:20480\n\t" \
  "v_cvt_pk_bf16_f32 v133, v10, v11\n\t" \
  "v_mfma_f32_16x16x32_bf16 a[52:55], a[168:171], v[136:139], a[52:55]\n\t" \
  "v_exp_f32 v70, v70\n\t" \
  "v_mfma_f32_16x16x32_bf16 a[52:55], a[172:175], v[140:143], a[52:55]\n\t" \
  "ds_read_b128 a[212:215], v221 offset:20480\n\t" \
  "v_cvt_pk_bf16_f32 v134, v12, v13\n\t" \
  "v_mfma_f32_16x16x32_bf16 a[56:59], a[176:179], v[136:139], a[56:59]\n\t" \
  "v_exp_f32 v71, v71\n\t" \
  "v_mfma_f32_16x16x32_bf16 a[56:59], a[180:183], v[140:143], a[56:59]\n\t" \
  "v_exp_f32 v72, v72\n\t" \
  "v_mfma_f32_16x16x32_bf16 a[60:63], a[184:187], v[136:139], a[60:63]\n\t" \
  "buffer_load_dwordx4 v226, s[60:63], s96 offen offset:2048 lds\n\t" \
  "v_mfma_f32_16x16x32_bf16 a[60:63], a[188:191], v[140:143], a[60:63]\n\t" \
  "ds_read_b128 a[216:219], v222 offset:20480\n\t" \
  "v_cvt_pk_bf16_f32 v135, v14, v15\n\t" \
  "v_mfma_f32_16x16x32_bf16 v[164:167], v[216:219], v[136:139], v[164:167]\n\t" \
  "v_exp_f32 v73, v73\n\t" \
  "v_mfma_f32_16x16x32_bf16 v[164:167], v[216:219], v[140:143], v[164:167]\n\t" \
  "ds_read_b128 a[220:223], v223 offset:20480\n\t" \
  "v_cvt_pk_bf16_f32 v136, v32, v33\n\t" \
  "v_mfma_f32_16x16x32_bf16 a[64:67], a[128:131], v[144:147], a[64:67]\n\t" \
  "v_cvt_pk_bf16_f32 v137, v34, v35\n\t" \
  "v_cvt_pk_bf16_f32 v138, v36, v37\n\t" \
  "v_mfma_f32_16x16x32_bf16 a[64:67], a[132:135], v[148:151], a[64:67]\n\t" \
  "v_exp_f32 v74, v74\n\t" \
  "v_mfma_f32_16x16x32_bf16 a[68:71], a[136:139], v[144:147], a[68:71]\n\t" \
  "buffer_load_dwordx4 v227, s[60:63], s96 offen offset:3072 lds\n\t" \
  "v_mfma_f32_16x16x32_bf16 a[68:71], a[140:143], v[148:151], a[68:71]\n\t" \
  "ds_read_b128 a[224:227], v220 offset:24576\n\t" \
  "v_cvt_pk_bf16_f32 v139, v38, v39\n\t" \
  "v_mfma_f32_16x16x32_bf16 a[72:75], a[144:147], v[144:147], a[72:75]\n\t" \
  "s_add_u32 m0, %[m], 147456\n\t" \
  "v_exp_f32 v75, v75\n\t" \
  "v_mfma_f32_16x16x32_bf16 a[72:75], a[148:151], v[148:151], a[72:75]\n\t" \
  "s_add_u32 s96, %[m], 81920\n\t" \
  "ds_read_b128 a[228:231], v221 offset:24576\n\t" \
  "v_cvt_pk_bf16_f32 v140, v40, v41\n\t" \
  "v_mfma_f32_16x16x32_bf16 a[76:79], a[152:155], v[144:147], a[76:79]\n\t" \
  "v_exp_f32 v76, v76\n\t" \
  "v_mfma_f32_16x16x32_bf16 a[76:79], a[156:159], v[148:151], a[76:79]\n\t" \
  "v_exp_f32 v77, v77\n\t" \
  "v_mfma_f32_16x16x32_bf16 a[80:83], a[160:163], v[144:147], a[80:83]\n\t" \
  "buffer_load_dwordx4 v230, s[64:67], s96 offen offset:0 lds\n\t" \
  "v_mfma_f32_16x16x32_bf16 a[80:83], a[164:167], v[148:151], a[80:83]\n\t" \
  "ds_read_b128 a[232:235], v222 offset:24576\n\t" \
  "v_cvt_pk_bf16_f32 v141, v42, v43\n\t" \
  "v_mfma_f32_16x16x32_bf16 a[84:87], a[168:171], v[144:147], a[84:87]\n\t" \
  "v_exp_f32 v78, v78\n\t" \
  "v_mfma_f32_16x16x32_bf16 a[84:87], a[172:175], v[148:151], a[84:87]\n\t" \
  "ds_read_b128 a[236:239], v223 offset:24576\n\t" \
  "v_cvt_pk_bf16_f32 v142, v44, v45\n\t" \
  "v_mfma_f32_16x16x32_bf16 a[88:91], a[176:179], v[144:147], a[88:91]\n\t" \
  "v_exp_f32 v79, v79\n\t" \
  "v_mfma_f32_16x16x32_bf16 a[88:91], a[180:183], v[148:151], a[88:91]\n\t" \
  "v_exp_f32 v96, v96\n\t" \
  "v_mfma_f32_16x16x32_bf16 a[92:95], a[184:187], v[144:147], a[92:95]\n\t" \
  "buffer_load_dwordx4 v231, s[64:67], s96 offen offset:1024 lds\n\t" \
  "v_mfma_f32_16x16x32_bf16 a[92:95], a[188:191], v[148:151], a[92:95]\n\t" \
  "ds_read_b128 a[240:243], v220 offset:28672\n\t" \
  "v_cvt_pk_bf16_f32 v143, v46, v47\n\t" \
  "v_mfma_f32_16x16x32_bf16 v[168:171], v[216:219], v[144:147], v[168:171]\n\t" \
  "v_exp_f32 v97, v97\n\t" \
  "v_mfma_f32_16x16x32_bf16 v[168:171], v[216:219], v[148:151], v[168:171]\n\t" \
  "ds_read_b128 a[244:247], v221 offset:28672\n\t" \
  "v_cvt_pk_bf16_f32 v144, v64, v65\n\t" \
  "v_mfma_f32_16x16x32_bf16 a[96:99], a[128:131], v[152:155], a[96:99]\n\t" \
  "v_cvt_pk_bf16_f32 v145, v66, v67\n\t" \
  "v_cvt_pk_bf16_f32 v146, v68, v69\n\t" \
  "v_mfma_f32_16x16x32_bf16 a[96:99], a[132:135], v[156:159], a[96:99]\n\t" \
  "v_exp_f32 v98, v98\n\t" \
  "v_mfma_f32_16x16x32_bf16 a[100:103], a[136:139], v[152:155], a[100:103]\n\t" \
  "buffer_load_dwordx4 v230, s[64:67], s96 offen offset:2048 lds\n\t" \
  "v_mfma_f32_16x16x32_bf16 a[100:103], a[140:143], v[156:159], a[100:103]\n\t" \
  "ds_read_b128 a[248:251], v222 offset:28672\n\t" \
  "v_cvt_pk_bf16_f32 v147, v70, v71\n\t" \
  "v_mfma_f32_16x16x32_bf16 a[104:107], a[144:147], v[152:155], a[104:107]\n\t" \
  "v_exp_f32 v99, v99\n\t" \
  "v_mfma_f32_16x16x32_bf16 a[104:107], a[148:151], v[156:159], a[104:107]\n\t" \
  "ds_read_b128 a[252:255], v223 offset:28672\n\t" \
  "v_cvt_pk_bf16_f32 v148, v72, v73\n\t" \
  "v_mfma_f32_16x16x32_bf16 a[108:111], a[152:155], v[152:155], a[108:111]\n\t" \
  "v_exp_f32 v100, v100\n\t" \
  "v_mfma_f32_16x16x32_bf16 a[108:111], a[156:159], v[156:159], a[108:111]\n\t" \
  "v_exp_f32 v101, v101\n\t" \
  "v_mfma_f32_16x16x32_bf16 a[112:115], a[160:163], v[152:155], a[112:115]\n\t" \
  "buffer_load_dwordx4 v231, s[64:67], s96 offen offset:3072 lds\n\t" \
  "v_mfma_f32_16x16x32_bf16 a[112:115], a[164:167], v[156:159], a[112:115]\n\t" \
  "v_cvt_pk_bf16_f32 v149, v74, v75\n\t" \
  "v_cvt_pk_bf16_f32 v150, v76, v77\n\t" \
  "v_mfma_f32_16x16x32_bf16 a[116:119], a[168:171], v[152:155], a[116:119]\n\t" \
  "v_exp_f32 v102, v102\n\t" \
  "v_mfma_f32_16x16x32_bf16 a[116:119], a[172:175], v[156:159], a[116:119]\n\t" \
  "v_exp_f32 v103, v103\n\t" \
  "v_mfma_f32_16x16x32_bf16 a[120:123], a[176:179], v[152:155], a[120:123]\n\t" \
  "v_exp_f32 v104, v104\n\t" \
  "v_mfma_f32_16x16x32_bf16 a[120:123], a[180:183], v[156:159], a[120:123]\n\t" \
  "s_add_u32 s99, %[pf1], 4096\n\t" \
  "v_exp_f32 v105, v105\n\t" \
  "v_mfma_f32_16x16x32_bf16 a[124:127], a[184:187], v[152:155], a[124:127]\n\t" \
  "v_exp_f32 v106, v106\n\t" \
  "v_mfma_f32_16x16x32_bf16 a[124:127], a[188:191], v[156:159], a[124:127]\n\t" \
  "v_exp_f32 v107, v107\n\t" \
  "v_mfma_f32_16x16x32_bf16 v[172:175], v[216:219], v[152:155], v[172:175]\n\t" \
  "buffer_load_dword v246, v212, %[wo], s99 offen\n\t" \
  "v_mfma_f32_16x16x32_bf16 v[172:175], v[216:219], v[156:159], v[172:175]\n\t" \
  "s_waitcnt lgkmcnt(12)\n\t" \
  "v_cvt_pk_bf16_f32 v151, v78, v79\n\t" \
  "v_cvt_pk_bf16_f32 v152, v96, v97\n\t" \
  STA("s_memrealtime s[94:95]\n\t") \
  STA("s_memtime s[92:93]\n\t") \
  "v_mfma_f32_16x16x32_bf16 v[0:3], a[192:195], v[16:19], v[176:179]\n\t" \
  "ds_read_b64_tr_b16 a[128:129], v228 offset:0\n\t" \
  "v_cvt_pk_bf16_f32 v153, v98, v99\n\t" \
  "v_mfma_f32_16x16x32_bf16 v[0:3], a[196:199], v[20:23], v[0:3]\n\t" \
  "v_cvt_pk_bf16_f32 v154, v100, v101\n\t" \
  "v_cvt_pk_bf16_f32 v155, v102, v103\n\t" \
  "v_mfma_f32_16x16x32_bf16 v[0:3], a[200:203], v[24:27], v[0:3]\n\t" \
  "ds_read_b64_tr_b16 a[130:131], v228 offset:4096\n\t" \
  "v_cvt_pk_bf16_f32 v156, v104, v105\n\t" \
  "v_mfma_f32_16x16x32_bf16 v[0:3], a[204:207], v[28:31], v[0:3]\n\t" \
  "s_waitcnt lgkmcnt(10)\n\t" \
  "v_exp_f32 v108, v108\n\t" \
  "v_mfma_f32_16x16x32_bf16 v[4:7], a[220:223], v[28:31], v[176:179]\n\t" \
  "ds_read_b64_tr_b16 a[132:133], v228 offset:8192\n\t" \
  "v_cvt_pk_bf16_f32 v157, v106, v107\n\t" \
  "v_mfma_f32_16x16x32_bf16 v[4:7], a[216:219], v[24:27], v[4:7]\n\t" \
  "v_exp_f32 v109, v109\n\t" \
  "v_mfma_f32_16x16x32_bf16 v[4:7], a[212:215], v[20:23], v[4:7]\n\t" \
  "ds_read_b64_tr_b16 a[134:135], v228 offset:12288\n\t" \
  "v_cvt_pk_bf16_f32 v158, v108, v109\n\t" \
  "v_mfma_f32_16x16x32_bf16 v[4:7], a[208:211], v[16:19], v[4:7]\n\t" \
  "s_waitcnt lgkmcnt(8)\n\t" \
  "ds_read_b64_tr_b16 a[136:137], v229 offset:0\n\t" \
  "v_writelane_b32 v244, 0, 0\n\t" \
  "v_mfma_f32_16x16x32_bf16 v[8:11], a[224:227], v[16:19], v[176:179]\n\t" \
  "v_exp_f32 v110, v110\n\t" \
  "v_mfma_f32_16x16x32_bf16 v[8:11], a[228:231], v[20:23], v[8:11]\n\t" \
  "s_add_u32 s99, %[pf1], 6144\n\t" \
  "v_exp_f32 v111, v111\n\t" \
  "v_mfma_f32_16x16x32_bf16 v[8:11], a[232:235], v[24:27], v[8:11]\n\t" \
  "ds_read_b64_tr_b16 a[138:139], v229 offset:4096\n\t" \
  "v_cvt_pk_bf16_f32 v159, v110, v111\n\t" \
  "v_mfma_f32_16x16x32_bf16 v[8:11], a[236:239], v[28:31], v[8:11]\n\t" \
  "s_waitcnt lgkmcnt(6)\n\t" \
  "v_exp_f32 v0, v0\n\t" \
  "v_mfma_f32_16x16x32_bf16 v[12:15], a[252:255], v[28:31], v[176:179]\n\t" \
  STA("v_writelane_b32 %[stv], s94, 42\n\t") \
  STA("v_writelane_b32 %[stv], s92, 43\n\t") \
  "buffer_load_dword v246, v212, %[wo], s99 offen\n\t" \
  "v_mfma_f32_16x16x32_bf16 v[12:15], a[248:251], v[24:27], v[12:15]\n\t" \
  "v_exp_f32 v1, v1\n\t" \
  "v_mfma_f32_16x16x32_bf16 v[12:15], a[244:247], v[20:23], v[12:15]\n\t" \
  "v_exp_f32 v2, v2\n\t" \
  "v_mfma_f32_16x16x32_bf16 v[12:15], a[240:243], v[16:19], v[12:15]\n\t" \
  "ds_read_b64_tr_b16 a[140:141], v229 offset:8192\n\t" \
  "v_mfma_f32_16x16x32_bf16 v[32:35], a[192:195], v[48:51], v[180:183]\n\t" \
  "ds_read_b64_tr_b16 a[142:143], v229 offset:12288\n\t" \
  "v_mfma_f32_16x16x32_bf16 v[32:35], a[196:199], v[52:55], v[32:35]\n\t" \
  "v_exp_f32 v3, v3\n\t" \
  "v_mfma_f32_16x16x32_bf16 v[32:35], a[200:203], v[56:59], v[32:35]\n\t" \
  "ds_read_b64_tr_b16 a[144:145], v228 offset:256\n\t" \
  "v_mfma_f32_16x16x32_bf16 v[32:35], a[204:207], v[60:63], v[32:35]\n\t" \
  "v_exp_f32 v4, v4\n\t" \
  "v_mfma_f32_16x16x32_bf16 v[36:39], a[220:223], v[60:63], v[180:183]\n\t" \
  "ds_read_b64_tr_b16 a[146:147], v228 offset:4352\n\t" \
  "v_mfma_f32_16x16x32_bf16 v[36:39], a[216:219], v[56:59], v[36:39]\n\t" \
  "ds_read_b64_tr_b16 a[148:149], v228 offset:8448\n\t" \
  "v_mfma_f32_16x16x32_bf16 v[36:39], a[212:215], v[52:55], v[36:39]\n\t" \
  "v_exp_f32 v5, v5\n\t" \
  "v_mfma_f32_16x16x32_bf16 v[36:39], a[208:211], v[48:51], v[36:39]\n\t" \
  "ds_read_b64_tr_b16 a[150:151], v228 offset:12544\n\t" \
  "v_mfma_f32_16x16x32_bf16 v[40:43], a[224:227], v[48:51], v[180:183]\n\t" \
  "v_exp_f32 v6, v6\n\t" \
  "v_mfma_f32_16x16x32_bf16 v[40:43], a[228:231], v[52:55], v[40:43]\n\t" \
  "ds_read_b64_tr_b16 a[152:153], v229 offset:256\n\t" \
  "v_mfma_f32_16x16x32_bf16 v[40:43], a[232:235], v[56:59], v[40:43]\n\t" \
  "v_exp_f32 v7, v7\n\t" \
  "v_mfma_f32_16x16x32_bf16 v[40:43], a[236:239], v[60:63], v[40:43]\n\t" \
  "ds_read_b64_tr_b16 a[154:155], v229 offset:4352\n\t" \
  "v_mfma_f32_16x16x32_bf16 v[44:47], a[252:255], v[60:63], v[180:183]\n\t" \
  "ds_read_b64_tr_b16 a[156:157], v229 offset:8448\n\t" \
  "v_mfma_f32_16x16x32_bf16 v[44:47], a[248:251], v[56:59], v[44:47]\n\t" \
  "v_exp_f32 v8, v8\n\t" \
  "v_mfma_f32_16x16x32_bf16 v[44:47], a[244:247], v[52:55], v[44:47]\n\t" \
  "ds_read_b64_tr_b16 a[158:159], v229 offset:12544\n\t" \
  "v_mfma_f32_16x16x32_bf16 v[44:47], a[240:243], v[48:51], v[44:47]\n\t" \
  "v_exp_f32 v9, v9\n\t" \
  "v_mfma_f32_16x16x32_bf16 v[64:67], a[192:195], v[80:83], v[184:187]\n\t" \
  "ds_read_b64_tr_b16 a[160:161], v228 offset:512\n\t" \
  "v_mfma_f32_16x16x32_bf16 v[64:67], a[196:199], v[84:87], v[64:67]\n\t" \
  "ds_read_b64_tr_b16 a[162:163], v228 offset:4608\n\t" \
  "v_mfma_f32_16x16x32_bf16 v[64:67], a[200:203], v[88:91], v[64:67]\n\t" \
  "v_exp_f32 v10, v10\n\t" \
  "v_mfma_f32_16x16x32_bf16 v[64:67], a[204:207], v[92:95], v[64:67]\n\t" \
  "ds_read_b64_tr_b16 a[164:165], v228 offset:8704\n\t" \
  "v_mfma_f32_16x16x32_bf16 v[68:71], a[220:223], v[92:95], v[184:187]\n\t" \
  "v_exp_f32 v11, v11\n\t" \
  "v_mfma_f32_16x16x32_bf16 v[68:71], a[216:219], v[88:91], v[68:71]\n\t" \
  "ds_read_b64_tr_b16 a[166:167], v228 offset:12800\n\t" \
  "v_mfma_f32_16x16x32_bf16 v[68:71], a[212:215], v[84:87], v[68:71]\n\t" \
  "ds_read_b64_tr_b16 a[168:169], v229 offset:512\n\t" \
  "v_mfma_f32_16x16x32_bf16 v[68:71], a[208:211], v[80:83], v[68:71]\n\t" \
  "v_exp_f32 v12, v12\n\t" \
  "v_mfma_f32_16x16x32_bf16 v[72:75], a[224:227], v[80:83], v[184:187]\n\t" \
  "ds_read_b64_tr_b16 a[170:171], v229 offset:4608\n\t" \
  "v_mfma_f32_16x16x32_bf16 v[72:75], a[228:231], v[84:87], v[72:75]\n\t" \
  "ds_read_b64_tr_b16 a[172:173], v229 offset:8704\n\t" \
  "v_mfma_f32_16x16x32_bf16 v[72:75], a[232:235], v[88:91], v[72:75]\n\t" \
  "v_exp_f32 v13, v13\n\t" \
  "v_mfma_f32_16x16x32_bf16 v[72:75], a[236:239], v[92:95], v[72:75]\n\t" \
  "ds_read_b64_tr_b16 a[174:175], v229 offset:12800\n\t" \
  "v_mfma_f32_16x16x32_bf16 v[76:79], a[252:255], v[92:95], v[184:187]\n\t" \
  "v_exp_f32 v14, v14\n\t" \
  "v_mfma_f32_16x16x32_bf16 v[76:79], a[248:251], v[88:91], v[76:79]\n\t" \
  "ds_read_b64_tr_b16 a[176:177], v228 offset:768\n\t" \
  "v_mfma_f32_16x16x32_bf16 v[76:79], a[244:247], v[84:87], v[76:79]\n\t" \
  "ds_read_b64_tr_b16 a[178:179], v228 offset:4864\n\t" \
  "v_mfma_f32_16x16x32_bf16 v[76:79], a[240:243], v[80:83], v[76:79]\n\t" \
  "v_exp_f32 v15, v15\n\t" \
  "v_mfma_f32_16x16x32_bf16 v[96:99], a[192:195], v[112:115], v[188:191]\n\t" \
  "ds_read_b64_tr_b16 a[180:181], v228 offset:8960\n\t" \
  "v_mfma_f32_16x16x32_bf16 v[96:99], a[196:199], v[116:119], v[96:99]\n\t" \
  "ds_read_b64_tr_b16 a[182:183], v228 offset:13056\n\t" \
  "v_mfma_f32_16x16x32_bf16 v[96:99], a[200:203], v[120:123], v[96:99]\n\t" \
  "v_exp_f32 v32, v32\n\t" \
  "v_mfma_f32_16x16x32_bf16 v[96:99], a[204:207], v[124:127], v[96:99]\n\t" \
  "ds_read_b64_tr_b16 a[184:185], v229 offset:768\n\t" \
  "v_mfma_f32_16x16x32_bf16 v[100:103], a[220:223], v[124:127], v[188:191]\n\t" \
  "v_exp_f32 v33, v33\n\t" \
  "v_mfma_f32_16x16x32_bf16 v[100:103], a[216:219], v[120:123], v[100:103]\n\t" \
  "ds_read_b64_tr_b16 a[186:187], v229 offset:4864\n\t" \
  "v_mfma_f32_16x16x32_bf16 v[100:103], a[212:215], v[116:119], v[100:103]\n\t" \
  "ds_read_b64_tr_b16 a[188:189], v229 offset:8960\n\t" \
  "v_mfma_f32_16x16x32_bf16 v[100:103], a[208:211], v[112:115], v[100:103]\n\t" \
  "v_exp_f32 v34, v34\n\t" \
  "v_mfma_f32_16x16x32_bf16 v[104:107], a[224:227], v[112:115], v[188:191]\n\t" \
  "ds_read_b64_tr_b16 a[190:191], v229 offset:13056\n\t" \
  "v_mfma_f32_16x16x32_bf16 v[104:107], a[228:231], v[116:119], v[104:107]\n\t" \
  "v_exp_f32 v35, v35\n\t" \
  "v_mfma_f32_16x16x32_bf16 v[104:107], a[232:235], v[120:123], v[104:107]\n\t" \
  "v_exp_f32 v36, v36\n\t" \
  "v_mfma_f32_16x16x32_bf16 v[104:107], a[236:239], v[124:127], v[104:107]\n\t" \
  "v_exp_f32 v37, v37\n\t" \
  "v_mfma_f32_16x16x32_bf16 v[108:111], a[252:255], v[124:127], v[188:191]\n\t" \
  "v_exp_f32 v38, v38\n\t" \
  "v_mfma_f32_16x16x32_bf16 v[108:111], a[248:251], v[120:123], v[108:111]\n\t" \
  "v_exp_f32 v39, v39\n\t" \
  "v_mfma_f32_16x16x32_bf16 v[108:111], a[244:247], v[116:119], v[108:111]\n\t" \
  "s_waitcnt vmcnt(2) lgkmcnt(0)\n\t" \
  "v_exp_f32 v40, v40\n\t" \
  "v_mfma_f32_16x16x32_bf16 v[108:111], a[240:243], v[112:115], v[108:111]\n\t" \
  "s_barrier\n\t" \
  STA("s_memrealtime s[94:95]\n\t") \
  STA("s_memtime s[92:93]\n\t") \
  "v_mfma_f32_16x16x32_bf16 a[0:3], a[128:131], v[128:131], a[0:3]\n\t" \
  "v_exp_f32 v41, v41\n\t" \
  "v_mfma_f32_16x16x32_bf16 a[0:3], a[132:135], v[132:135], a[0:3]\n\t" \
  "v_exp_f32 v42, v42\n\t" \
  "v_mfma_f32_16x16x32_bf16 a[4:7], a[136:139], v[128:131], a[4:7]\n\t" \
  "v_exp_f32 v43, v43\n\t" \
  "v_mfma_f32_16x16x32_bf16 a[4:7], a[140:143], v[132:135], a[4:7]\n\t" \
  "v_exp_f32 v44, v44\n\t" \
  "v_mfma_f32_16x16x32_bf16 a[8:11], a[144:147], v[128:131], a[8:11]\n\t" \
  "v_exp_f32 v45, v45\n\t" \
  "v_mfma_f32_16x16x32_bf16 a[8:11], a[148:151], v[132:135], a[8:11]\n\t" \
  "v_exp_f32 v46, v46\n\t" \
  "v_mfma_f32_16x16x32_bf16 a[12:15], a[152:155], v[128:131], a[12:15]\n\t" \
  "v_exp_f32 v47, v47\n\t" \
  "v_mfma_f32_16x16x32_bf16 a[12:15], a[156:159], v[132:135], a[12:15]\n\t" \
  "v_exp_f32 v64, v64\n\t" \
  "v_mfma_f32_16x16x32_bf16 a[16:19], a[160:163], v[128:131], a[16:19]\n\t" \
  "v_exp_f32 v65, v65\n\t" \
  "v_mfma_f32_16x16x32_bf16 a[16:19], a[164:167], v[132:135], a[16:19]\n\t" \
  "v_exp_f32 v66, v66\n\t" \
  "v_mfma_f32_16x16x32_bf16 a[20:23], a[168:171], v[128:131], a[20:23]\n\t" \
  "v_exp_f32 v67, v67\n\t" \
  "v_mfma_f32_16x16x32_bf16 a[20:23], a[172:175], v[132:135], a[20:23]\n\t" \
  "v_exp_f32 v68, v68\n\t" \
  "v_mfma_f32_16x16x32_bf16 a[24:27], a[176:179], v[128:131], a[24:27]\n\t" \
  STA("v_writelane_b32 %[stv], s94, 44\n\t") \
  STA("v_writelane_b32 %[stv], s92, 45\n\t") \
  "v_exp_f32 v69, v69\n\t" \
  "v_mfma_f32_16x16x32_bf16 a[24:27], a[180:183], v[132:135], a[24:27]\n\t" \
  "v_exp_f32 v70, v70\n\t" \
  "v_mfma_f32_16x16x32_bf16 a[28:31], a[184:187], v[128:131], a[28:31]\n\t" \
  "v_exp_f32 v71, v71\n\t" \
  "v_mfma_f32_16x16x32_bf16 a[28:31], a[188:191], v[132:135], a[28:31]\n\t" \
  "v_exp_f32 v72, v72\n\t" \
  "v_mfma_f32_16x16x32_bf16 v[160:163], v[216:219], v[128:131], v[160:163]\n\t" \
  "s_add_u32 m0, %[m], 65536\n\t" \
  "v_exp_f32 v73, v73\n\t" \
  "v_mfma_f32_16x16x32_bf16 v[160:163], v[216:219], v[132:135], v[160:163]\n\t" \
  "s_add_u32 s96, %[m], 114688\n\t" \
  "ds_read_b128 a[192:195], v220 offset:0\n\t" \
  "v_cvt_pk_bf16_f32 v128, v0, v1\n\t" \
  "v_mfma_f32_16x16x32_bf16 a[32:35], a[128:131], v[136:139], a[32:35]\n\t" \
  "ds_read_b128 a[196:199], v221 offset:0\n\t" \
  "v_cvt_pk_bf16_f32 v129, v2, v3\n\t" \
  "v_mfma_f32_16x16x32_bf16 a[32:35], a[132:135], v[140:143], a[32:35]\n\t" \
  "ds_read_b128 v[192:195], v215 offset:32768\n\t" \
  "v_cvt_pk_bf16_f32 v130, v4, v5\n\t" \
  "v_mfma_f32_16x16x32_bf16 a[36:39], a[136:139], v[136:139], a[36:39]\n\t" \
  "buffer_load_dwordx4 v224, s[60:63], s96 offen offset:0 lds\n\t" \
  "v_mfma_f32_16x16x32_bf16 a[36:39], a[140:143], v[140:143], a[36:39]\n\t" \
  "ds_read_b128 a[200:203], v222 offset:0\n\t" \
  "v_cvt_pk_bf16_f32 v131, v6, v7\n\t" \
  "v_mfma_f32_16x16x32_bf16 a[40:43], a[144:147], v[136:139], a[40:43]\n\t" \
  "ds_read_b128 v[196:199], v215 offset:33792\n\t" \
  "v_cvt_pk_bf16_f32 v132, v8, v9\n\t" \
  "v_mfma_f32_16x16x32_bf16 a[40:43], a[148:151], v[140:143], a[40:43]\n\t" \
  "ds_read_b128 v[200:203], v215 offset:34816\n\t" \
  "v_cvt_pk_bf16_f32 v133, v10, v11\n\t" \
  "v_mfma_f32_16x16x32_bf16 a[44:47], a[152:155], v[136:139], a[44:47]\n\t" \
  "buffer_load_dwordx4 v225, s[60:63], s96 offen offset:1024 lds\n\t" \
  "v_mfma_f32_16x16x32_bf16 a[44:47], a[156:159], v[140:143], a[44:47]\n\t" \
  "ds_read_b128 a[204:207], v223 offset:0\n\t" \
  "v_cvt_pk_bf16_f32 v134, v12, v13\n\t" \
  "v_mfma_f32_16x16x32_bf16 a[48:51], a[160:163], v[136:139], a[48:51]\n\t" \
  "ds_read_b128 v[204:207], v215 offset:35840\n\t" \
  "v_cvt_pk_bf16_f32 v135, v14, v15\n\t" \
  "v_mfma_f32_16x16x32_bf16 a[48:51], a[164:167], v[140:143], a[48:51]\n\t" \
  "v_exp_f32 v74, v74\n\t" \
  "v_mfma_f32_16x16x32_bf16 a[52:55], a[168:171], v[136:139], a[52:55]\n\t" \
  "buffer_load_dwordx4 v226, s[60:63], s96 offen offset:2048 lds\n\t" \
  "v_mfma_f32_16x16x32_bf16 a[52:55], a[172:175], v[140:143], a[52:55]\n\t" \
  "v_exp_f32 v75, v75\n\t" \
  "v_mfma_f32_16x16x32_bf16 a[56:59], a[176:179], v[136:139], a[56:59]\n\t" \
  "v_exp_f32 v76, v76\n\t" \
  "v_mfma_f32_16x16x32_bf16 a[56:59], a[180:183], v[140:143], a[56:59]\n\t" \
  "ds_read_b128 a[208:211], v220 offset:4096\n\t" \
  "v_mfma_f32_16x16x32_bf16 a[60:63], a[184:187], v[136:139], a[60:63]\n\t" \
  "buffer_load_dwordx4 v227, s[60:63], s96 offen offset:3072 lds\n\t" \
  "v_mfma_f32_16x16x32_bf16 a[60:63], a[188:191], v[140:143], a[60:63]\n\t" \
  "s_add_u32 m0, %[m], 81920\n\t" \
  "ds_read_b128 a[212:215], v221 offset:4096\n\t" \
  "s_add_u32 s96, %[m], 98304\n\t" \
  "v_mfma_f32_16x16x32_bf16 v[164:167], v[216:219], v[136:139], v[164:167]\n\t" \
  "v_exp_f32 v77, v77\n\t" \
  "v_mfma_f32_16x16x32_bf16 v[164:167], v[216:219], v[140:143], v[164:167]\n\t" \
  "ds_read_b128 a[216:219], v222 offset:4096\n\t" \
  "v_cvt_pk_bf16_f32 v136, v32, v33\n\t" \
  "v_mfma_f32_16x16x32_bf16 a[64:67], a[128:131], v[144:147], a[64:67]\n\t" \
  "buffer_load_dwordx4 v230, s[64:67], s96 offen offset:0 lds\n\t" \
  "v_mfma_f32_16x16x32_bf16 a[64:67], a[132:135], v[148:151], a[64:67]\n\t" \
  "ds_read_b128 a[220:223], v223 offset:4096\n\t" \
  "v_cvt_pk_bf16_f32 v137, v34, v35\n\t" \
  "v_mfma_f32_16x16x32_bf16 a[68:71], a[136:139], v[144:147], a[68:71]\n\t" \
  "ds_read_b128 a[224:227], v220 offset:8192\n\t" \
  "v_cvt_pk_bf16_f32 v138, v36, v37\n\t" \
  "v_mfma_f32_16x16x32_bf16 a[68:71], a[140:143], v[148:151], a[68:71]\n\t" \
  "v_cvt_pk_bf16_f32 v139, v38, v39\n\t" \
  "v_cvt_pk_bf16_f32 v140, v40, v41\n\t" \
  "v_mfma_f32_16x16x32_bf16 a[72:75], a[144:147], v[144:147], a[72:75]\n\t" \
  "buffer_load_dwordx4 v231, s[64:67], s96 offen offset:1024 lds\n\t" \
  "v_mfma_f32_16x16x32_bf16 a[72:75], a[148:151], v[148:151], a[72:75]\n\t" \
  "ds_read_b128 a[228:231], v221 offset:8192\n\t" \
  "v_cvt_pk_bf16_f32 v141, v42, v43\n\t" \
  "v_mfma_f32_16x16x32_bf16 a[76:79], a[152:155], v[144:147], a[76:79]\n\t" \
  "v_exp_f32 v78, v78\n\t" \
  "v_mfma_f32_16x16x32_bf16 a[76:79], a[156:159], v[148:151], a[76:79]\n\t" \
  "v_cvt_pk_bf16_f32 v142, v44, v45\n\t" \
  "v_cvt_pk_bf16_f32 v143, v46, v47\n\t" \
  "v_mfma_f32_16x16x32_bf16 a[80:83], a[160:163], v[144:147], a[80:83]\n\t" \
  "buffer_load_dwordx4 v230, s[64:67], s96 offen offset:2048 lds\n\t" \
  "v_mfma_f32_16x16x32_bf16 a[80:83], a[164:167], v[148:151], a[80:83]\n\t" \
  "v_exp_f32 v79, v79\n\t" \
  "v_mfma_f32_16x16x32_bf16 a[84:87], a[168:171], v[144:147], a[84:87]\n\t" \
  "v_exp_f32 v96, v96\n\t" \
  "v_mfma_f32_16x16x32_bf16 a[84:87], a[172:175], v[148:151], a[84:87]\n\t" \
  "v_exp_f32 v97, v97\n\t" \
  "v_mfma_f32_16x16x32_bf16 a[88:91], a[176:179], v[144:147], a[88:91]\n\t" \
  "buffer_load_dwordx4 v231, s[64:67], s96 offen offset:3072 lds\n\t" \
  "v_mfma_f32_16x16x32_bf16 a[88:91], a[180:183], v[148:151], a[88:91]\n\t" \
  "s_add_u32 s96, %[hl18], 524288\n\t" \
  "ds_read_b128 a[232:235], v222 offset:8192\n\t" \
  "s_and_b32 s96, s96, 0x1c0000\n\t" \
  "v_mfma_f32_16x16x32_bf16 a[92:95], a[184:187], v[144:147], a[92:95]\n\t" \
  "s_add_u32 s96, s96, %[wob]\n\t" \
  "ds_read_b128 a[236:239], v223 offset:8192\n\t" \
  "s_add_u32 m0, %[m], 131072\n\t" \
  "v_mfma_f32_16x16x32_bf16 a[92:95], a[188:191], v[148:151], a[92:95]\n\t" \
  "v_exp_f32 v98, v98\n\t" \
  "v_mfma_f32_16x16x32_bf16 v[168:171], v[216:219], v[144:147], v[168:171]\n\t" \
  "s_waitcnt lgkmcnt(8)\n\t" \
  "buffer_load_dwordx4 v240, %[wo], s96 offen offset:0 lds\n\t" \
  "v_mfma_f32_16x16x32_bf16 v[168:171], v[216:219], v[148:151], v[168:171]\n\t" \
  "ds_read_b128 a[240:243], v220 offset:12288\n\t" \
  "v_cvt_pk_bf16_f32 v144, v64, v65\n\t" \
  "v_mfma_f32_16x16x32_bf16 a[96:99], a[128:131], v[152:155], a[96:99]\n\t" \
  "ds_read_b128 a[244:247], v221 offset:12288\n\t" \
  "v_cvt_pk_bf16_f32 v145, v66, v67\n\t" \
  "v_mfma_f32_16x16x32_bf16 a[96:99], a[132:135], v[156:159], a[96:99]\n\t" \
  "v_cvt_pk_bf16_f32 v146, v68, v69\n\t" \
  "v_cvt_pk_bf16_f32 v147, v70, v71\n\t" \
  "v_mfma_f32_16x16x32_bf16 a[100:103], a[136:139], v[152:155], a[100:103]\n\t" \
  "buffer_load_dwordx4 v241, %[wo], s96 offen offset:1024 lds\n\t" \
  "v_mfma_f32_16x16x32_bf16 a[100:103], a[140:143], v[156:159], a[100:103]\n\t" \
  "ds_read_b128 a[248:251], v222 offset:12288\n\t" \
  "v_cvt_pk_bf16_f32 v148, v72, v73\n\t" \
  "v_mfma_f32_16x16x32_bf16 a[104:107], a[144:147], v[152:155], a[104:107]\n\t" \
  "v_exp_f32 v99, v99\n\t" \
  "v_mfma_f32_16x16x32_bf16 a[104:107], a[148:151], v[156:159], a[104:107]\n\t" \
  "ds_read_b128 a[252:255], v223 offset:12288\n\t" \
  "v_cvt_pk_bf16_f32 v149, v74, v75\n\t" \
  "v_mfma_f32_16x16x32_bf16 a[108:111], a[152:155], v[152:155], a[108:111]\n\t" \
  "buffer_load_dwordx4 v248, %[wo], s96 offen offset:2048 lds\n\t" \
  "v_mfma_f32_16x16x32_bf16 a[108:111], a[156:159], v[156:159], a[108:111]\n\t" \
  "v_exp_f32 v100, v100\n\t" \
  "v_mfma_f32_16x16x32_bf16 a[112:115], a[160:163], v[152:155], a[112:115]\n\t" \
  "v_exp_f32 v101, v101\n\t" \
  "v_mfma_f32_16x16x32_bf16 a[112:115], a[164:167], v[156:159], a[112:115]\n\t" \
  "v_exp_f32 v102, v102\n\t" \
  "v_mfma_f32_16x16x32_bf16 a[116:119], a[168:171], v[152:155], a[116:119]\n\t" \
  "buffer_load_dwordx4 v249, %[wo], s96 offen offset:3072 lds\n\t" \
  "v_mfma_f32_16x16x32_bf16 a[116:119], a[172:175], v[156:159], a[116:119]\n\t" \
  "v_cvt_pk_bf16_f32 v150, v76, v77\n\t" \
  "v_cvt_pk_bf16_f32 v151, v78, v79\n\t" \
  "v_mfma_f32_16x16x32_bf16 a[120:123], a[176:179], v[152:155], a[120:123]\n\t" \
  "v_exp_f32 v103, v103\n\t" \
  "v_mfma_f32_16x16x32_bf16 a[120:123], a[180:183], v[156:159], a[120:123]\n\t" \
  "s_lshl1_add_u32 s99, %[m], 0\n\t" \
  "v_exp_f32 v104, v104\n\t" \
  "v_mfma_f32_16x16x32_bf16 a[124:127], a[184:187], v[152:155], a[124:127]\n\t" \
  ".Lhs8:\n\ts_cbranch_execz .Lhs8n\n\t" \
  "v_exp_f32 v105, v105\n\t" \
  "v_mfma_f32_16x16x32_bf16 a[124:127], a[188:191], v[156:159], a[124:127]\n\t" \
  "v_exp_f32 v106, v106\n\t" \
  "v_mfma_f32_16x16x32_bf16 v[172:175], v[216:219], v[152:155], v[172:175]\n\t" \
  "buffer_load_dword v246, v212, %[sD], s99 offen\n\t" \
  "v_mfma_f32_16x16x32_bf16 v[172:175], v[216:219], v[156:159], v[172:175]\n\t" \
  "v_cvt_pk_bf16_f32 v152, v96, v97\n\t" \
  "v_cvt_pk_bf16_f32 v153, v98, v99\n\t" \
  STA("s_memrealtime s[94:95]\n\t") \
  STA("s_memtime s[92:93]\n\t") \
  "v_mfma_f32_16x16x32_bf16 v[0:3], a[192:195], v[16:19], v[176:179]\n\t" \
  "ds_read_b64_tr_b16 a[128:129], v210 offset:0\n\t" \
  "v_cvt_pk_bf16_f32 v154, v100, v101\n\t" \
  "v_mfma_f32_16x16x32_bf16 v[0:3], a[196:199], v[20:23], v[0:3]\n\t" \
  "v_exp_f32 v107, v107\n\t" \
  "v_mfma_f32_16x16x32_bf16 v[0:3], a[200:203], v[24:27], v[0:3]\n\t" \
  "ds_read_b64_tr_b16 a[130:131], v210 offset:4096\n\t" \
  "v_cvt_pk_bf16_f32 v155, v102, v103\n\t" \
  "v_mfma_f32_16x16x32_bf16 v[0:3], a[204:207], v[28:31], v[0:3]\n\t" \
  "s_waitcnt lgkmcnt(10)\n\t" \
  "v_exp_f32 v108, v108\n\t" \
  "v_mfma_f32_16x16x32_bf16 v[4:7], a[220:223], v[28:31], v[176:179]\n\t" \
  "ds_read_b64_tr_b16 a[132:133], v210 offset:8192\n\t" \
  "v_lshrrev_b32 v250, 1, v233\n\t" \
  "v_mfma_f32_16x16x32_bf16 v[4:7], a[216:219], v[24:27], v[4:7]\n\t" \
  "v_lshl_add_u32 v214, v233, 14, %[cto]\n\t" \
  "v_lshlrev_b32 v250, 2, v250\n\t" \
  "v_mfma_f32_16x16x32_bf16 v[4:7], a[212:215], v[20:23], v[4:7]\n\t" \
  "v_min_u32 v214, .Lfz_z-.Lat_t1-4, v214\n\t" \
  "v_add_u32 v250, 8192, v250\n\t" \
  "v_mfma_f32_16x16x32_bf16 v[4:7], a[208:211], v[16:19], v[4:7]\n\t" \
  "s_waitcnt lgkmcnt(7)\n\t" \
  "ds_read_b64_tr_b16 a[134:135], v210 offset:12288\n\t" \
  "v_cvt_pk_bf16_f32 v156, v104, v105\n\t" \
  "v_mfma_f32_16x16x32_bf16 v[8:11], a[224:227], v[16:19], v[176:179]\n\t" \
  "buffer_load_dword v209, v250, %[ws], 0 offen sc1\n\t" \
  "v_mfma_f32_16x16x32_bf16 v[8:11], a[228:231], v[20:23], v[8:11]\n\t" \
  "s_getpc_b64 s[84:85]\n\t" \
  ".Lat_t1:\n\t" \
  "ds_read_b64_tr_b16 a[136:137], v211 offset:0\n\t" \
  "v_cvt_pk_bf16_f32 v157, v106, v107\n\t" \
  "v_mfma_f32_16x16x32_bf16 v[8:11], a[232:235], v[24:27], v[8:11]\n\t" \
  "global_load_dword v246, v214, s[84:85]\n\t" \
  "v_mfma_f32_16x16x32_bf16 v[8:11], a[236:239], v[28:31], v[8:11]\n\t" \
  "s_waitcnt lgkmcnt(5)\n\t" \
  "ds_read_b64_tr_b16 a[138:139], v211 offset:4096\n\t" \
  "s_lshl1_add_u32 s99, %[m], 2048\n\t" \
  "v_mfma_f32_16x16x32_bf16 v[12:15], a[252:255], v[28:31], v[176:179]\n\t" \
  STA("v_writelane_b32 %[stv], s94, 46\n\t") \
  STA("v_writelane_b32 %[stv], s92, 47\n\t") \
  "buffer_load_dword v246, v212, %[sD], s99 offen\n\t" \
  "v_mfma_f32_16x16x32_bf16 v[12:15], a[248:251], v[24:27], v[12:15]\n\t" \
  "v_exp_f32 v109, v109\n\t" \
  "v_mfma_f32_16x16x32_bf16 v[12:15], a[244:247], v[20:23], v[12:15]\n\t" \
  "ds_read_b64_tr_b16 a[140:141], v211 offset:8192\n\t" \
  "v_cvt_pk_bf16_f32 v158, v108, v109\n\t" \
  "v_mfma_f32_16x16x32_bf16 v[12:15], a[240:243], v[16:19], v[12:15]\n\t" \
  "v_exp_f32 v110, v110\n\t" \
  "v_mfma_f32_16x16x32_bf16 v[32:35], a[192:195], v[48:51], v[180:183]\n\t" \
  "s_add_u32 s96, %[hl18], 0\n\t" \
  "v_exp_f32 v111, v111\n\t" \
  "v_mfma_f32_16x16x32_bf16 v[32:35], a[196:199], v[52:55], v[32:35]\n\t" \
  "s_and_b32 s96, s96, 0x1c0000\n\t" \
  "ds_read_b64_tr_b16 a[142:143], v211 offset:12288\n\t" \
  "v_cvt_pk_bf16_f32 v159, v110, v111\n\t" \
  "v_mfma_f32_16x16x32_bf16 v[32:35], a[200:203], v[56:59], v[32:35]\n\t" \
  "s_add_u32 s96, s96, %[wob]\n\t" \
  "ds_read_b64_tr_b16 a[144:145], v210 offset:256\n\t" \
  "s_add_u32 m0, %[m], 0\n\t" \
  "v_mfma_f32_16x16x32_bf16 v[32:35], a[204:207], v[60:63], v[32:35]\n\t" \
  "v_exp_f32 v0, v0\n\t" \
  "v_mfma_f32_16x16x32_bf16 v[36:39], a[220:223], v[60:63], v[180:183]\n\t" \
  "buffer_load_dwordx4 v240, %[wo], s96 offen offset:0 lds\n\t" \
  "v_mfma_f32_16x16x32_bf16 v[36:39], a[216:219], v[56:59], v[36:39]\n\t" \
  "ds_read_b64_tr_b16 a[146:147], v210 offset:4352\n\t" \
  "v_mfma_f32_16x16x32_bf16 v[36:39], a[212:215], v[52:55], v[36:39]\n\t" \
  "ds_read_b64_tr_b16 a[148:149], v210 offset:8448\n\t" \
  "v_mfma_f32_16x16x32_bf16 v[36:39], a[208:211], v[48:51], v[36:39]\n\t" \
  "ds_read_b64_tr_b16 a[150:151], v210 offset:12544\n\t" \
  "v_mfma_f32_16x16x32_bf16 v[40:43], a[224:227], v[48:51], v[180:183]\n\t" \
  "v_exp_f32 v1, v1\n\t" \
  "v_mfma_f32_16x16x32_bf16 v[40:43], a[228:231], v[52:55], v[40:43]\n\t" \
  "ds_read_b64_tr_b16 a[152:153], v211 offset:256\n\t" \
  "v_mfma_f32_16x16x32_bf16 v[40:43], a[232:235], v[56:59], v[40:43]\n\t" \
  "v_exp_f32 v2, v2\n\t" \
  "v_mfma_f32_16x16x32_bf16 v[40:43], a[236:239], v[60:63], v[40:43]\n\t" \
  "ds_read_b64_tr_b16 a[154:155], v211 offset:4352\n\t" \
  "v_mfma_f32_16x16x32_bf16 v[44:47], a[252:255], v[60:63], v[180:183]\n\t" \
  "v_exp_f32 v3, v3\n\t" \
  "v_mfma_f32_16x16x32_bf16 v[44:47], a[248:251], v[56:59], v[44:47]\n\t" \
  "ds_read_b64_tr_b16 a[156:157], v211 offset:8448\n\t" \
  "v_mfma_f32_16x16x32_bf16 v[44:47], a[244:247], v[52:55], v[44:47]\n\t" \
  "buffer_load_dwordx4 v241, %[wo], s96 offen offset:1024 lds\n\t" \
  "v_mfma_f32_16x16x32_bf16 v[44:47], a[240:243], v[48:51], v[44:47]\n\t" \
  "ds_read_b64_tr_b16 a[158:159], v211 offset:12544\n\t" \
  "v_mfma_f32_16x16x32_bf16 v[64:67], a[192:195], v[80:83], v[184:187]\n\t" \
  "ds_read_b64_tr_b16 a[160:161], v210 offset:512\n\t" \
  "v_mfma_f32_16x16x32_bf16 v[64:67], a[196:199], v[84:87], v[64:67]\n\t" \
  "v_exp_f32 v4, v4\n\t" \
  "v_mfma_f32_16x16x32_bf16 v[64:67], a[200:203], v[88:91], v[64:67]\n\t" \
  "ds_read_b64_tr_b16 a[162:163], v210 offset:4608\n\t" \
  "v_mfma_f32_16x16x32_bf16 v[64:67], a[204:207], v[92:95], v[64:67]\n\t" \
  "ds_read_b64_tr_b16 a[164:165], v210 offset:8704\n\t" \
  "v_mfma_f32_16x16x32_bf16 v[68:71], a[220:223], v[92:95], v[184:187]\n\t" \
  "v_exp_f32 v5, v5\n\t" \
  "v_mfma_f32_16x16x32_bf16 v[68:71], a[216:219], v[88:91], v[68:71]\n\t" \
  "ds_read_b64_tr_b16 a[166:167], v210 offset:12800\n\t" \
  "v_mfma_f32_16x16x32_bf16 v[68:71], a[212:215], v[84:87], v[68:71]\n\t" \
  "v_exp_f32 v6, v6\n\t" \
  "v_mfma_f32_16x16x32_bf16 v[68:71], a[208:211], v[80:83], v[68:71]\n\t" \
  "ds_read_b64_tr_b16 a[168:169], v211 offset:512\n\t" \
  "v_mfma_f32_16x16x32_bf16 v[72:75], a[224:227], v[80:83], v[184:187]\n\t" \
  "buffer_load_dwordx4 v248, %[wo], s96 offen offset:2048 lds\n\t" \
  "v_mfma_f32_16x16x32_bf16 v[72:75], a[228:231], v[84:87], v[72:75]\n\t" \
  "ds_read_b64_tr_b16 a[170:171], v211 offset:4608\n\t" \
  "v_mfma_f32_16x16x32_bf16 v[72:75], a[232:235], v[88:91], v[72:75]\n\t" \
  "ds_read_b64_tr_b16 a[172:173], v211 offset:8704\n\t" \
  "v_mfma_f32_16x16x32_bf16 v[72:75], a[236:239], v[92:95], v[72:75]\n\t" \
  "v_exp_f32 v7, v7\n\t" \
  "v_mfma_f32_16x16x32_bf16 v[76:79], a[252:255], v[92:95], v[184:187]\n\t" \
  "ds_read_b64_tr_b16 a[174:175], v211 offset:12800\n\t" \
  "v_mfma_f32_16x16x32_bf16 v[76:79], a[248:251], v[88:91], v[76:79]\n\t" \
  "v_exp_f32 v8, v8\n\t" \
  "v_mfma_f32_16x16x32_bf16 v[76:79], a[244:247], v[84:87], v[76:79]\n\t" \
  "ds_read_b64_tr_b16 a[176:177], v210 offset:768\n\t" \
  "v_mfma_f32_16x16x32_bf16 v[76:79], a[240:243], v[80:83], v[76:79]\n\t" \
  "ds_read_b64_tr_b16 a[178:179], v210 offset:4864\n\t" \
  "v_mfma_f32_16x16x32_bf16 v[96:99], a[192:195], v[112:115], v[188:191]\n\t" \
  "v_exp_f32 v9, v9\n\t" \
  "v_mfma_f32_16x16x32_bf16 v[96:99], a[196:199], v[116:119], v[96:99]\n\t" \
  "ds_read_b64_tr_b16 a[180:181], v210 offset:8960\n\t" \
  "v_mfma_f32_16x16x32_bf16 v[96:99], a[200:203], v[120:123], v[96:99]\n\t" \
  "buffer_load_dwordx4 v249, %[wo], s96 offen offset:3072 lds\n\t" \
  "v_mfma_f32_16x16x32_bf16 v[96:99], a[204:207], v[124:127], v[96:99]\n\t" \
  "ds_read_b64_tr_b16 a[182:183], v210 offset:13056\n\t" \
  "v_mfma_f32_16x16x32_bf16 v[100:103], a[220:223], v[124:127], v[188:191]\n\t" \
  "v_exp_f32 v10, v10\n\t" \
  "v_mfma_f32_16x16x32_bf16 v[100:103], a[216:219], v[120:123], v[100:103]\n\t" \
  "ds_read_b64_tr_b16 a[184:185], v211 offset:768\n\t" \
  "v_mfma_f32_16x16x32_bf16 v[100:103], a[212:215], v[116:119], v[100:103]\n\t" \
  "ds_read_b64_tr_b16 a[186:187], v211 offset:4864\n\t" \
  "v_mfma_f32_16x16x32_bf16 v[100:103], a[208:211], v[112:115], v[100:103]\n\t" \
  "v_exp_f32 v11, v11\n\t" \
  "v_mfma_f32_16x16x32_bf16 v[104:107], a[224:227], v[112:115], v[188:191]\n\t" \
  "ds_read_b64_tr_b16 a[188:189], v211 offset:8960\n\t" \
  "v_mfma_f32_16x16x32_bf16 v[104:107], a[228:231], v[116:119], v[104:107]\n\t" \
  "v_exp_f32 v12, v12\n\t" \
  "v_mfma_f32_16x16x32_bf16 v[104:107], a[232:235], v[120:123], v[104:107]\n\t" \
  "ds_read_b64_tr_b16 a[190:191], v211 offset:13056\n\t" \
  "v_mfma_f32_16x16x32_bf16 v[104:107], a[236:239], v[124:127], v[104:107]\n\t" \
  "v_exp_f32 v13, v13\n\t" \
  "v_mfma_f32_16x16x32_bf16 v[108:111], a[252:255], v[124:127], v[188:191]\n\t" \
  "v_exp_f32 v14, v14\n\t" \
  "v_mfma_f32_16x16x32_bf16 v[108:111], a[248:251], v[120:123], v[108:111]\n\t" \
  "v_exp_f32 v15, v15\n\t" \
  "v_mfma_f32_16x16x32_bf16 v[108:111], a[244:247], v[116:119], v[108:111]\n\t" \
  "s_waitcnt vmcnt(12) lgkmcnt(0)\n\t" \
  "v_exp_f32 v32, v32\n\t" \
  "v_mfma_f32_16x16x32_bf16 v[108:111], a[240:243], v[112:115], v[108:111]\n\t" \
  "s_barrier\n\t" \
  STA("s_memrealtime s[94:95]\n\t") \
  STA("s_memtime s[92:93]\n\t") \
  "v_mfma_f32_16x16x32_bf16 a[0:3], a[128:131], v[128:131], a[0:3]\n\t" \
  "v_exp_f32 v33, v33\n\t" \
  "v_mfma_f32_16x16x32_bf16 a[0:3], a[132:135], v[132:135], a[0:3]\n\t" \
  "v_exp_f32 v34, v34\n\t" \
  "v_mfma_f32_16x16x32_bf16 a[4:7], a[136:139], v[128:131], a[4:7]\n\t" \
  "v_exp_f32 v35, v35\n\t" \
  "v_mfma_f32_16x16x32_bf16 a[4:7], a[140:143], v[132:135], a[4:7]\n\t" \
  "v_exp_f32 v36, v36\n\t" \
  "v_mfma_f32_16x16x32_bf16 a[8:11], a[144:147], v[128:131], a[8:11]\n\t" \
  "v_exp_f32 v37, v37\n\t" \
  "v_mfma_f32_16x16x32_bf16 a[8:11], a[148:151], v[132:135], a[8:11]\n\t" \
  "v_exp_f32 v38, v38\n\t" \
  "v_mfma_f32_16x16x32_bf16 a[12:15], a[152:155], v[128:131], a[12:15]\n\t" \
  "v_exp_f32 v39, v39\n\t" \
  "v_mfma_f32_16x16x32_bf16 a[12:15], a[156:159], v[132:135], a[12:15]\n\t" \
  "v_exp_f32 v40, v40\n\t" \
  "v_mfma_f32_16x16x32_bf16 a[16:19], a[160:163], v[128:131], a[16:19]\n\t" \
  "v_exp_f32 v41, v41\n\t" \
  "v_mfma_f32_16x16x32_bf16 a[16:19], a[164:167], v[132:135], a[16:19]\n\t" \
  "v_exp_f32 v42, v42\n\t" \
  "v_mfma_f32_16x16x32_bf16 a[20:23], a[168:171], v[128:131], a[20:23]\n\t" \
  "v_exp_f32 v43, v43\n\t" \
  "v_mfma_f32_16x16x32_bf16 a[20:23], a[172:175], v[132:135], a[20:23]\n\t" \
  "v_exp_f32 v44, v44\n\t" \
  "v_mfma_f32_16x16x32_bf16 a[24:27], a[176:179], v[128:131], a[24:27]\n\t" \
  STA("v_writelane_b32 %[stv], s94, 48\n\t") \
  STA("v_writelane_b32 %[stv], s92, 49\n\t") \
  "v_exp_f32 v45, v45\n\t" \
  "v_mfma_f32_16x16x32_bf16 a[24:27], a[180:183], v[132:135], a[24:27]\n\t" \
  "v_exp_f32 v46, v46\n\t" \
  "v_mfma_f32_16x16x32_bf16 a[28:31], a[184:187], v[128:131], a[28:31]\n\t" \
  "v_exp_f32 v47, v47\n\t" \
  "v_mfma_f32_16x16x32_bf16 a[28:31], a[188:191], v[132:135], a[28:31]\n\t" \
  "v_exp_f32 v64, v64\n\t" \
  "v_mfma_f32_16x16x32_bf16 v[160:163], v[216:219], v[128:131], v[160:163]\n\t" \
  "s_add_u32 m0, %[m], 147456\n\t" \
  "v_exp_f32 v65, v65\n\t" \
  "v_mfma_f32_16x16x32_bf16 v[160:163], v[216:219], v[132:135], v[160:163]\n\t" \
  "s_add_u32 s96, %[m], 114688\n\t" \
  "ds_read_b128 a[192:195], v220 offset:16384\n\t" \
  "v_cvt_pk_bf16_f32 v128, v0, v1\n\t" \
  "v_mfma_f32_16x16x32_bf16 a[32:35], a[128:131], v[136:139], a[32:35]\n\t" \
  "v_exp_f32 v66, v66\n\t" \
  "v_mfma_f32_16x16x32_bf16 a[32:35], a[132:135], v[140:143], a[32:35]\n\t" \
  "ds_read_b128 a[196:199], v221 offset:16384\n\t" \
  "v_cvt_pk_bf16_f32 v129, v2, v3\n\t" \
  "v_mfma_f32_16x16x32_bf16 a[36:39], a[136:139], v[136:139], a[36:39]\n\t" \
  "buffer_load_dwordx4 v230, s[64:67], s96 offen offset:0 lds\n\t" \
  "v_mfma_f32_16x16x32_bf16 a[36:39], a[140:143], v[140:143], a[36:39]\n\t" \
  "v_exp_f32 v67, v67\n\t" \
  "v_mfma_f32_16x16x32_bf16 a[40:43], a[144:147], v[136:139], a[40:43]\n\t" \
  "ds_read_b128 a[200:203], v222 offset:16384\n\t" \
  "v_cvt_pk_bf16_f32 v130, v4, v5\n\t" \
  "v_mfma_f32_16x16x32_bf16 a[40:43], a[148:151], v[140:143], a[40:43]\n\t" \
  "v_exp_f32 v68, v68\n\t" \
  "v_mfma_f32_16x16x32_bf16 a[44:47], a[152:155], v[136:139], a[44:47]\n\t" \
  "buffer_load_dwordx4 v231, s[64:67], s96 offen offset:1024 lds\n\t" \
  "v_mfma_f32_16x16x32_bf16 a[44:47], a[156:159], v[140:143], a[44:47]\n\t" \
  "ds_read_b128 a[204:207], v223 offset:16384\n\t" \
  "v_cvt_pk_bf16_f32 v131, v6, v7\n\t" \
  "v_mfma_f32_16x16x32_bf16 a[48:51], a[160:163], v[136:139], a[48:51]\n\t" \
  "v_exp_f32 v69, v69\n\t" \
  "v_mfma_f32_16x16x32_bf16 a[48:51], a[164:167], v[140:143], a[48:51]\n\t" \
  "ds_read_b128 a[208:211], v220 offset:20480\n\t" \
  "v_cvt_pk_bf16_f32 v132, v8, v9\n\t" \
  "v_mfma_f32_16x16x32_bf16 a[52:55], a[168:171], v[136:139], a[52:55]\n\t" \
  "buffer_load_dwordx4 v230, s[64:67], s96 offen offset:2048 lds\n\t" \
  "v_mfma_f32_16x16x32_bf16 a[52:55], a[172:175], v[140:143], a[52:55]\n\t" \
  "v_exp_f32 v70, v70\n\t" \
  "v_mfma_f32_16x16x32_bf16 a[56:59], a[176:179], v[136:139], a[56:59]\n\t" \
  "ds_read_b128 a[212:215], v221 offset:20480\n\t" \
  "v_cvt_pk_bf16_f32 v133, v10, v11\n\t" \
  "v_mfma_f32_16x16x32_bf16 a[56:59], a[180:183], v[140:143], a[56:59]\n\t" \
  "v_exp_f32 v71, v71\n\t" \
  "v_mfma_f32_16x16x32_bf16 a[60:63], a[184:187], v[136:139], a[60:63]\n\t" \
  "buffer_load_dwordx4 v231, s[64:67], s96 offen offset:3072 lds\n\t" \
  "v_mfma_f32_16x16x32_bf16 a[60:63], a[188:191], v[140:143], a[60:63]\n\t" \
  "s_add_u32 s96, %[hl18], 0\n\t" \
  "v_exp_f32 v72, v72\n\t" \
  "v_mfma_f32_16x16x32_bf16 v[164:167], v[216:219], v[136:139], v[164:167]\n\t" \
  "s_and_b32 s96, s96, 0x1c0000\n\t" \
  "s_add_u32 s96, s96, s77\n\t" \
  "s_add_u32 m0, %[m], 98304\n\t" \
  "v_mfma_f32_16x16x32_bf16 v[164:167], v[216:219], v[140:143], v[164:167]\n\t" \
  "ds_read_b128 a[216:219], v222 offset:20480\n\t" \
  "v_cvt_pk_bf16_f32 v134, v12, v13\n\t" \
  "v_mfma_f32_16x16x32_bf16 a[64:67], a[128:131], v[144:147], a[64:67]\n\t" \
  "buffer_load_dwordx4 v240, %[wo], s96 offen offset:0 lds\n\t" \
  "v_mfma_f32_16x16x32_bf16 a[64:67], a[132:135], v[148:151], a[64:67]\n\t" \
  "ds_read_b128 a[220:223], v223 offset:20480\n\t" \
  "v_cvt_pk_bf16_f32 v135, v14, v15\n\t" \
  "v_mfma_f32_16x16x32_bf16 a[68:71], a[136:139], v[144:147], a[68:71]\n\t" \
  "v_exp_f32 v73, v73\n\t" \
  "v_mfma_f32_16x16x32_bf16 a[68:71], a[140:143], v[148:151], a[68:71]\n\t" \
  "v_exp_f32 v74, v74\n\t" \
  "v_mfma_f32_16x16x32_bf16 a[72:75], a[144:147], v[144:147], a[72:75]\n\t" \
  "buffer_load_dwordx4 v241, %[wo], s96 offen offset:1024 lds\n\t" \
  "v_mfma_f32_16x16x32_bf16 a[72:75], a[148:151], v[148:151], a[72:75]\n\t" \
  "s_waitcnt vmcnt(12)\n\t" \
  "v_and_b32 v251, 0xbfffbfff, v209\n\t" \
  "v_cmp_ne_u32_e64 s[84:85], %[tag], v251\n\t" \
  "v_mfma_f32_16x16x32_bf16 a[76:79], a[152:155], v[144:147], a[76:79]\n\t" \
  "s_cmp_lg_u64 s[84:85], 0\n\t" \
  "s_cbranch_scc1 .Lat_v\n\t" \
  ".Lat_w:\n\t" \
  "v_mfma_f32_16x16x32_bf16 a[76:79], a[156:159], v[148:151], a[76:79]\n\t" \
  "v_and_b32 v251, 1, v233\n\t" \
  "v_lshlrev_b32 v251, 4, v251\n\t" \
  "v_mfma_f32_16x16x32_bf16 a[80:83], a[160:163], v[144:147], a[80:83]\n\t" \
  "buffer_load_dwordx4 v248, %[wo], s96 offen offset:2048 lds\n\t" \
  "v_mfma_f32_16x16x32_bf16 a[80:83], a[164:167], v[148:151], a[80:83]\n\t" \
  "v_lshrrev_b32 v251, v251, v209\n\t" \
  "v_and_b32 v251, 0x4000, v251\n\t" \
  "v_mfma_f32_16x16x32_bf16 a[84:87], a[168:171], v[144:147], a[84:87]\n\t" \
  "ds_read_b128 a[224:227], v220 offset:24576\n\t" \
  "v_cmp_ne_u32_e64 s[84:85], 0, v251\n\t" \
  STS("s_nop 4\n\t") \
  STS("v_writelane_b32 %[stv], s84, 60\n\t") \
  STS("v_writelane_b32 %[stv], s85, 61\n\t") \
  "v_mfma_f32_16x16x32_bf16 a[84:87], a[172:175], v[148:151], a[84:87]\n\t" \
  "s_bcnt1_i32_b32 s98, s84\n\t" \
  "ds_read_b128 a[228:231], v221 offset:24576\n\t" \
  "s_bcnt1_i32_b32 %[na], s85\n\t" \
  "v_mfma_f32_16x16x32_bf16 a[88:91], a[176:179], v[144:147], a[88:91]\n\t" \
  "buffer_load_dwordx4 v249, %[wo], s96 offen offset:3072 lds\n\t" \
  "v_mfma_f32_16x16x32_bf16 a[88:91], a[180:183], v[148:151], a[88:91]\n\t" \
  "s_cmp_gt_u32 s98, %[na]\n\t" \
  "s_cselect_b32 %[nb], 0x2000, 0\n\t" \
  "s_cselect_b32 s98, s84, s85\n\t" \
  "v_mfma_f32_16x16x32_bf16 a[92:95], a[184:187], v[144:147], a[92:95]\n\t" \
  "s_add_u32 s96, %[hl18], 262144\n\t" \
  "s_and_b32 s96, s96, 0x1c0000\n\t" \
  "s_add_u32 s96, s96, %[wob]\n\t" \
  "v_mfma_f32_16x16x32_bf16 a[92:95], a[188:191], v[148:151], a[92:95]\n\t" \
  "s_add_u32 m0, %[m], 49152\n\t" \
  "s_cmp_lg_u32 %[nb], 0\n\t" \
  "s_cselect_b32 s84, s85, s84\n\t" \
  "v_mfma_f32_16x16x32_bf16 v[168:171], v[216:219], v[144:147], v[168:171]\n\t" \
  "s_lshr_b32 s85, s98, 16\n\t" \
  "s_bcnt1_i32_b32 s85, s85\n\t" \
  "s_lshr_b32 s84, s84, 16\n\t" \
  "v_mfma_f32_16x16x32_bf16 v[168:171], v[216:219], v[148:151], v[168:171]\n\t" \
  "buffer_load_dwordx4 v240, %[wo], s96 offen offset:0 lds\n\t" \
  "v_mfma_f32_16x16x32_bf16 a[96:99], a[128:131], v[152:155], a[96:99]\n\t" \
  "s_bcnt1_i32_b32 s84, s84\n\t" \
  "ds_read_b128 a[232:235], v222 offset:24576\n\t" \
  "s_add_u32 s84, s84, 7\n\t" \
  "v_mfma_f32_16x16x32_bf16 a[96:99], a[132:135], v[156:159], a[96:99]\n\t" \
  "s_cmp_ge_u32 s85, s84\n\t" \
  "s_cselect_b32 %[na], -1, 0\n\t" \
  "s_xor_b32 s98, %[po], %[nb]\n\t" \
  "v_mfma_f32_16x16x32_bf16 a[100:103], a[136:139], v[152:155], a[100:103]\n\t" \
  "s_and_b32 s98, s98, %[na]\n\t" \
  "s_xor_b32 %[eso], %[eso], s98\n\t" \
  "s_xor_b32 %[otb], %[otb], s98\n\t" \
  STS("v_writelane_b32 %[stv], %[na], 57\n\t") \
  STS("v_writelane_b32 %[stv], %[nb], 59\n\t") \
  "v_mfma_f32_16x16x32_bf16 a[100:103], a[140:143], v[156:159], a[100:103]\n\t" \
  "buffer_load_dwordx4 v241, %[wo], s96 offen offset:1024 lds\n\t" \
  "v_mfma_f32_16x16x32_bf16 a[104:107], a[144:147], v[152:155], a[104:107]\n\t" \
  "s_xor_b32 s98, %[px], %[nb]\n\t" \
  "s_and_b32 s98, s98, %[na]\n\t" \
  "s_xor_b32 %[xcb], %[xcb], s98\n\t" \
  "v_mfma_f32_16x16x32_bf16 a[104:107], a[148:151], v[156:159], a[104:107]\n\t" \
  "s_xor_b32 %[frb], %[frb], %[nb]\n\t" \
  "ds_read_b128 a[236:239], v223 offset:24576\n\t" \
  "s_xor_b32 %[fw2], %[fw2], %[nb]\n\t" \
  "v_mfma_f32_16x16x32_bf16 a[108:111], a[152:155], v[152:155], a[108:111]\n\t" \
  "buffer_load_dwordx4 v248, %[wo], s96 offen offset:2048 lds\n\t" \
  "v_mfma_f32_16x16x32_bf16 a[108:111], a[156:159], v[156:159], a[108:111]\n\t" \
  "s_xor_b32 s98, %[eso], 0\n\t" \
  "ds_read_b128 a[240:243], v220 offset:28672\n\t" \
  "v_exp_f32 v75, v75\n\t" \
  "v_mfma_f32_16x16x32_bf16 a[112:115], a[160:163], v[152:155], a[112:115]\n\t" \
  "s_lshl_b32 s64, %[hl], 17\n\t" \
  "ds_read_b128 a[244:247], v221 offset:28672\n\t" \
  "v_exp_f32 v76, v76\n\t" \
  "v_mfma_f32_16x16x32_bf16 a[112:115], a[164:167], v[156:159], a[112:115]\n\t" \
  "s_bfe_u32 s70, %[otb], 0x20014\n\t" \
  "ds_read_b128 a[248:251], v222 offset:28672\n\t" \
  "v_exp_f32 v77, v77\n\t" \
  "v_mfma_f32_16x16x32_bf16 a[116:119], a[168:171], v[152:155], a[116:119]\n\t" \
  "buffer_load_dwordx4 v249, %[wo], s96 offen offset:3072 lds\n\t" \
  "v_mfma_f32_16x16x32_bf16 a[116:119], a[172:175], v[156:159], a[116:119]\n\t" \
  "s_lshl_b32 s70, s70, 14\n\t" \
  "ds_read_b128 a[252:255], v223 offset:28672\n\t" \
  "v_exp_f32 v78, v78\n\t" \
  "v_mfma_f32_16x16x32_bf16 a[120:123], a[176:179], v[152:155], a[120:123]\n\t" \
  "s_xor_b32 s67, s70, 49152\n\t" \
  "v_cvt_pk_bf16_f32 v136, v32, v33\n\t" \
  "v_exp_f32 v79, v79\n\t" \
  "v_mfma_f32_16x16x32_bf16 a[120:123], a[180:183], v[156:159], a[120:123]\n\t" \
  "s_lshl1_add_u32 s60, s67, %[otb]\n\t" \
  "v_exp_f32 v96, v96\n\t" \
  "v_mfma_f32_16x16x32_bf16 a[124:127], a[184:187], v[152:155], a[124:127]\n\t" \
  "s_lshl1_add_u32 s99, %[m], 4096\n\t" \
  "v_exp_f32 v97, v97\n\t" \
  "v_mfma_f32_16x16x32_bf16 a[124:127], a[188:191], v[156:159], a[124:127]\n\t" \
  "s_xor_b32 s68, s70, 32768\n\t" \
  "v_exp_f32 v98, v98\n\t" \
  "v_mfma_f32_16x16x32_bf16 v[172:175], v[216:219], v[152:155], v[172:175]\n\t" \
  "buffer_load_dword v246, v212, %[sD], s99 offen\n\t" \
  "v_mfma_f32_16x16x32_bf16 v[172:175], v[216:219], v[156:159], v[172:175]\n\t" \
  "s_waitcnt lgkmcnt(12)\n\t" \
  "v_exp_f32 v99, v99\n\t" \
  STA("s_memrealtime s[94:95]\n\t") \
  STA("s_memtime s[92:93]\n\t") \
  "v_mfma_f32_16x16x32_bf16 v[0:3], a[192:195], v[16:19], v[176:179]\n\t" \
  "s_lshl1_add_u32 s61, s68, %[otb]\n\t" \
  "ds_read_b64_tr_b16 a[128:129], v228 offset:0\n\t" \
  "v_cvt_pk_bf16_f32 v137, v34, v35\n\t" \
  "v_mfma_f32_16x16x32_bf16 v[0:3], a[196:199], v[20:23], v[0:3]\n\t" \
  "s_xor_b32 s69, s70, 16384\n\t" \
  "ds_read_b64_tr_b16 a[130:131], v228 offset:4096\n\t" \
  "v_cvt_pk_bf16_f32 v138, v36, v37\n\t" \
  "v_mfma_f32_16x16x32_bf16 v[0:3], a[200:203], v[24:27], v[0:3]\n\t" \
  "s_lshl1_add_u32 s62, s69, %[otb]\n\t" \
  "ds_read_b64_tr_b16 a[132:133], v228 offset:8192\n\t" \
  "v_exp_f32 v100, v100\n\t" \
  "v_mfma_f32_16x16x32_bf16 v[0:3], a[204:207], v[28:31], v[0:3]\n\t" \
  "s_waitcnt lgkmcnt(11)\n\t" \
  "ds_read_b64_tr_b16 a[134:135], v228 offset:12288\n\t" \
  "v_cvt_pk_bf16_f32 v139, v38, v39\n\t" \
  "v_mfma_f32_16x16x32_bf16 v[4:7], a[220:223], v[28:31], v[176:179]\n\t" \
  "s_lshl1_add_u32 s63, s70, %[otb]\n\t" \
  "ds_read_b64_tr_b16 a[136:137], v229 offset:0\n\t" \
  "v_cvt_pk_bf16_f32 v140, v40, v41\n\t" \
  "v_mfma_f32_16x16x32_bf16 v[4:7], a[216:219], v[24:27], v[4:7]\n\t" \
  "s_mul_i32 s65, %[hl], 508\n\t" \
  "ds_read_b64_tr_b16 a[138:139], v229 offset:4096\n\t" \
  "v_exp_f32 v101, v101\n\t" \
  "v_mfma_f32_16x16x32_bf16 v[4:7], a[212:215], v[20:23], v[4:7]\n\t" \
  "s_add_u32 s65, s65, %[fw2]\n\t" \
  "ds_read_b64_tr_b16 a[140:141], v229 offset:8192\n\t" \
  "v_cvt_pk_bf16_f32 v141, v42, v43\n\t" \
  "v_mfma_f32_16x16x32_bf16 v[4:7], a[208:211], v[16:19], v[4:7]\n\t" \
  "s_waitcnt lgkmcnt(11)\n\t" \
  "ds_read_b64_tr_b16 a[142:143], v229 offset:12288\n\t" \
  "v_cvt_pk_bf16_f32 v142, v44, v45\n\t" \
  "v_mfma_f32_16x16x32_bf16 v[8:11], a[224:227], v[16:19], v[176:179]\n\t" \
  "s_lshl_b32 s80, %[hl], 2\n\t" \
  "v_exp_f32 v102, v102\n\t" \
  "v_mfma_f32_16x16x32_bf16 v[8:11], a[228:231], v[20:23], v[8:11]\n\t" \
  "s_add_u32 s80, s80, %[wid]\n\t" \
  "v_exp_f32 v103, v103\n\t" \
  "v_mfma_f32_16x16x32_bf16 v[8:11], a[232:235], v[24:27], v[8:11]\n\t" \
  "s_lshl1_add_u32 s99, %[m], 6144\n\t" \
  "v_exp_f32 v104, v104\n\t" \
  "v_mfma_f32_16x16x32_bf16 v[8:11], a[236:239], v[28:31], v[8:11]\n\t" \
  "s_waitcnt lgkmcnt(8)\n\t" \
  "v_exp_f32 v105, v105\n\t" \
  "v_mfma_f32_16x16x32_bf16 v[12:15], a[252:255], v[28:31], v[176:179]\n\t" \
  STA("v_writelane_b32 %[stv], s94, 50\n\t") \
  STA("v_writelane_b32 %[stv], s92, 51\n\t") \
  "buffer_load_dword v246, v212, %[sD], s99 offen\n\t" \
  "v_mfma_f32_16x16x32_bf16 v[12:15], a[248:251], v[24:27], v[12:15]\n\t" \
  "s_barrier\n\t" \
  "v_mfma_f32_16x16x32_bf16 v[12:15], a[244:247], v[20:23], v[12:15]\n\t" \
  "s_lshl_b32 s80, s80, 6\n\t" \
  "ds_read_b64_tr_b16 a[144:145], v228 offset:256\n\t" \
  "v_cvt_pk_bf16_f32 v143, v46, v47\n\t" \
  "v_mfma_f32_16x16x32_bf16 v[12:15], a[240:243], v[16:19], v[12:15]\n\t" \
  "s_add_u32 s76, %[m], 147456\n\t" \
  "ds_read_b64_tr_b16 a[146:147], v228 offset:4352\n\t" \
  "v_cvt_pk_bf16_f32 v144, v64, v65\n\t" \
  "v_mfma_f32_16x16x32_bf16 v[32:35], a[192:195], v[48:51], v[180:183]\n\t" \
  "s_lshl_b32 s75, s80, 2\n\t" \
  "v_exp_f32 v106, v106\n\t" \
  "v_mfma_f32_16x16x32_bf16 v[32:35], a[196:199], v[52:55], v[32:35]\n\t" \
  "s_add_u32 s75, s75, 2228224\n\t" \
  "ds_read_b64_tr_b16 a[148:149], v228 offset:8448\n\t" \
  "v_cvt_pk_bf16_f32 v145, v66, v67\n\t" \
  "v_mfma_f32_16x16x32_bf16 v[32:35], a[200:203], v[56:59], v[32:35]\n\t" \
  "s_add_u32 s96, %[hl18], 262144\n\t" \
  "s_and_b32 s96, s96, 0x1c0000\n\t" \
  "s_add_u32 s96, s96, s77\n\t" \
  "v_mfma_f32_16x16x32_bf16 v[32:35], a[204:207], v[60:63], v[32:35]\n\t" \
  "s_add_u32 m0, %[m], 65536\n\t" \
  "ds_read_b64_tr_b16 a[150:151], v228 offset:12544\n\t" \
  "v_cvt_pk_bf16_f32 v146, v68, v69\n\t" \
  "v_mfma_f32_16x16x32_bf16 v[36:39], a[220:223], v[60:63], v[180:183]\n\t" \
  "buffer_load_dwordx4 v240, %[wo], s96 offen offset:0 lds\n\t" \
  "v_mfma_f32_16x16x32_bf16 v[36:39], a[216:219], v[56:59], v[36:39]\n\t" \
  "s_lshr_b32 s71, s67, 7\n\t" \
  "ds_read_b64_tr_b16 a[152:153], v229 offset:256\n\t" \
  "v_exp_f32 v107, v107\n\t" \
  "v_mfma_f32_16x16x32_bf16 v[36:39], a[212:215], v[52:55], v[36:39]\n\t" \
  "s_add_u32 s71, s71, %[frb]\n\t" \
  "ds_read_b64_tr_b16 a[154:155], v229 offset:4352\n\t" \
  "v_exp_f32 v108, v108\n\t" \
  "v_mfma_f32_16x16x32_bf16 v[36:39], a[208:211], v[48:51], v[36:39]\n\t" \
  "s_lshr_b32 s72, s68, 7\n\t" \
  "ds_read_b64_tr_b16 a[156:157], v229 offset:8448\n\t" \
  "v_cvt_pk_bf16_f32 v147, v70, v71\n\t" \
  "v_mfma_f32_16x16x32_bf16 v[40:43], a[224:227], v[48:51], v[180:183]\n\t" \
  "s_add_u32 s72, s72, %[frb]\n\t" \
  "v_cvt_pk_bf16_f32 v148, v72, v73\n\t" \
  "v_exp_f32 v109, v109\n\t" \
  "v_mfma_f32_16x16x32_bf16 v[40:43], a[228:231], v[52:55], v[40:43]\n\t" \
  "s_lshr_b32 s66, s69, 7\n\t" \
  "v_exp_f32 v110, v110\n\t" \
  "v_mfma_f32_16x16x32_bf16 v[40:43], a[232:235], v[56:59], v[40:43]\n\t" \
  "s_add_u32 s66, s66, %[frb]\n\t" \
  "ds_read_b64_tr_b16 a[158:159], v229 offset:12544\n\t" \
  "v_cvt_pk_bf16_f32 v149, v74, v75\n\t" \
  "v_mfma_f32_16x16x32_bf16 v[40:43], a[236:239], v[60:63], v[40:43]\n\t" \
  "s_lshr_b32 s73, s70, 7\n\t" \
  "ds_read_b64_tr_b16 a[160:161], v228 offset:512\n\t" \
  "v_cvt_pk_bf16_f32 v150, v76, v77\n\t" \
  "v_mfma_f32_16x16x32_bf16 v[44:47], a[252:255], v[60:63], v[180:183]\n\t" \
  "s_add_u32 s73, s73, %[frb]\n\t" \
  "v_exp_f32 v111, v111\n\t" \
  "v_mfma_f32_16x16x32_bf16 v[44:47], a[248:251], v[56:59], v[44:47]\n\t" \
  "s_lshl3_add_u32 s67, s67, %[xcb]\n\t" \
  "v_exp_f32 v0, v0\n\t" \
  "v_mfma_f32_16x16x32_bf16 v[44:47], a[244:247], v[52:55], v[44:47]\n\t" \
  "buffer_load_dwordx4 v241, %[wo], s96 offen offset:1024 lds\n\t" \
  "v_mfma_f32_16x16x32_bf16 v[44:47], a[240:243], v[48:51], v[44:47]\n\t" \
  "s_lshl3_add_u32 s68, s68, %[xcb]\n\t" \
  "ds_read_b64_tr_b16 a[162:163], v228 offset:4608\n\t" \
  "v_cvt_pk_bf16_f32 v151, v78, v79\n\t" \
  "v_mfma_f32_16x16x32_bf16 v[64:67], a[192:195], v[80:83], v[184:187]\n\t" \
  "s_add_u32 s68, s68, 4096\n\t" \
  "ds_read_b64_tr_b16 a[164:165], v228 offset:8704\n\t" \
  "v_cvt_pk_bf16_f32 v152, v96, v97\n\t" \
  "v_mfma_f32_16x16x32_bf16 v[64:67], a[196:199], v[84:87], v[64:67]\n\t" \
  "s_lshl3_add_u32 s69, s69, %[xcb]\n\t" \
  "v_cvt_pk_bf16_f32 v153, v98, v99\n\t" \
  "v_exp_f32 v1, v1\n\t" \
  "v_mfma_f32_16x16x32_bf16 v[64:67], a[200:203], v[88:91], v[64:67]\n\t" \
  "s_add_u32 s69, s69, 16384\n\t" \
  "ds_read_b64_tr_b16 a[166:167], v228 offset:12800\n\t" \
  "v_cvt_pk_bf16_f32 v154, v100, v101\n\t" \
  "v_mfma_f32_16x16x32_bf16 v[64:67], a[204:207], v[92:95], v[64:67]\n\t" \
  "s_lshl3_add_u32 s70, s70, %[xcb]\n\t" \
  "ds_read_b64_tr_b16 a[168:169], v229 offset:512\n\t" \
  "v_cvt_pk_bf16_f32 v155, v102, v103\n\t" \
  "v_mfma_f32_16x16x32_bf16 v[68:71], a[220:223], v[92:95], v[184:187]\n\t" \
  "v_exp_f32 v2, v2\n\t" \
  "v_mfma_f32_16x16x32_bf16 v[68:71], a[216:219], v[88:91], v[68:71]\n\t" \
  "v_exp_f32 v3, v3\n\t" \
  "v_mfma_f32_16x16x32_bf16 v[68:71], a[212:215], v[84:87], v[68:71]\n\t" \
  "ds_read_b64_tr_b16 a[170:171], v229 offset:4608\n\t" \
  "v_cvt_pk_bf16_f32 v156, v104, v105\n\t" \
  "v_mfma_f32_16x16x32_bf16 v[68:71], a[208:211], v[80:83], v[68:71]\n\t" \
  "v_exp_f32 v4, v4\n\t" \
  "v_mfma_f32_16x16x32_bf16 v[72:75], a[224:227], v[80:83], v[184:187]\n\t" \
  "buffer_load_dwordx4 v248, %[wo], s96 offen offset:2048 lds\n\t" \
  "v_mfma_f32_16x16x32_bf16 v[72:75], a[228:231], v[84:87], v[72:75]\n\t" \
  "ds_read_b64_tr_b16 a[172:173], v229 offset:8704\n\t" \
  "v_cvt_pk_bf16_f32 v157, v106, v107\n\t" \
  "v_mfma_f32_16x16x32_bf16 v[72:75], a[232:235], v[88:91], v[72:75]\n\t" \
  "ds_read_b64_tr_b16 a[174:175], v229 offset:12800\n\t" \
  "v_cvt_pk_bf16_f32 v158, v108, v109\n\t" \
  "v_mfma_f32_16x16x32_bf16 v[72:75], a[236:239], v[92:95], v[72:75]\n\t" \
  "ds_read_b64_tr_b16 a[176:177], v228 offset:768\n\t" \
  "v_exp_f32 v5, v5\n\t" \
  "v_mfma_f32_16x16x32_bf16 v[76:79], a[252:255], v[92:95], v[184:187]\n\t" \
  "v_exp_f32 v6, v6\n\t" \
  "v_mfma_f32_16x16x32_bf16 v[76:79], a[248:251], v[88:91], v[76:79]\n\t" \
  "ds_read_b64_tr_b16 a[178:179], v228 offset:4864\n\t" \
  "v_cvt_pk_bf16_f32 v159, v110, v111\n\t" \
  "v_mfma_f32_16x16x32_bf16 v[76:79], a[244:247], v[84:87], v[76:79]\n\t" \
  "v_exp_f32 v7, v7\n\t" \
  "v_mfma_f32_16x16x32_bf16 v[76:79], a[240:243], v[80:83], v[76:79]\n\t" \
  "v_exp_f32 v8, v8\n\t" \
  "v_mfma_f32_16x16x32_bf16 v[96:99], a[192:195], v[112:115], v[188:191]\n\t" \
  "v_exp_f32 v9, v9\n\t" \
  "v_mfma_f32_16x16x32_bf16 v[96:99], a[196:199], v[116:119], v[96:99]\n\t" \
  "ds_read_b64_tr_b16 a[180:181], v228 offset:8960\n\t" \
  "v_cvt_pk_bf16_f32 v0, v0, v1\n\t" \
  "v_mfma_f32_16x16x32_bf16 v[96:99], a[200:203], v[120:123], v[96:99]\n\t" \
  "buffer_load_dwordx4 v249, %[wo], s96 offen offset:3072 lds\n\t" \
  "v_mfma_f32_16x16x32_bf16 v[96:99], a[204:207], v[124:127], v[96:99]\n\t" \
  "ds_read_b64_tr_b16 a[182:183], v228 offset:13056\n\t" \
  "v_cvt_pk_bf16_f32 v1, v2, v3\n\t" \
  "v_mfma_f32_16x16x32_bf16 v[100:103], a[220:223], v[124:127], v[188:191]\n\t" \
  "v_exp_f32 v10, v10\n\t" \
  "v_mfma_f32_16x16x32_bf16 v[100:103], a[216:219], v[120:123], v[100:103]\n\t" \
  "ds_read_b64_tr_b16 a[184:185], v229 offset:768\n\t" \
  "v_cvt_pk_bf16_f32 v2, v4, v5\n\t" \
  "v_mfma_f32_16x16x32_bf16 v[100:103], a[212:215], v[116:119], v[100:103]\n\t" \
  "v_exp_f32 v11, v11\n\t" \
  "v_mfma_f32_16x16x32_bf16 v[100:103], a[208:211], v[112:115], v[100:103]\n\t" \
  "ds_read_b64_tr_b16 a[186:187], v229 offset:4864\n\t" \
  "v_cvt_pk_bf16_f32 v3, v6, v7\n\t" \
  "v_mfma_f32_16x16x32_bf16 v[104:107], a[224:227], v[112:115], v[188:191]\n\t" \
  "ds_read_b64_tr_b16 a[188:189], v229 offset:8960\n\t" \
  "v_cvt_pk_bf16_f32 v8, v8, v9\n\t" \
  "v_mfma_f32_16x16x32_bf16 v[104:107], a[228:231], v[116:119], v[104:107]\n\t" \
  "ds_read_b64_tr_b16 a[190:191], v229 offset:13056\n\t" \
  "v_exp_f32 v12, v12\n\t" \
  "v_mfma_f32_16x16x32_bf16 v[104:107], a[232:235], v[120:123], v[104:107]\n\t" \
  "s_add_u32 s96, %[hl18], 524288\n\t" \
  "v_exp_f32 v13, v13\n\t" \
  "v_mfma_f32_16x16x32_bf16 v[104:107], a[236:239], v[124:127], v[104:107]\n\t" \
  "s_and_b32 s96, s96, 0x1c0000\n\t" \
  "v_exp_f32 v14, v14\n\t" \
  "v_mfma_f32_16x16x32_bf16 v[108:111], a[252:255], v[124:127], v[188:191]\n\t" \
  "s_add_u32 s96, s96, s77\n\t" \
  "v_exp_f32 v15, v15\n\t" \
  "v_mfma_f32_16x16x32_bf16 v[108:111], a[248:251], v[120:123], v[108:111]\n\t" \
  "s_add_u32 m0, %[m], 81920\n\t" \
  "v_exp_f32 v32, v32\n\t" \
  "v_mfma_f32_16x16x32_bf16 v[108:111], a[244:247], v[116:119], v[108:111]\n\t" \
  "s_waitcnt vmcnt(6) lgkmcnt(0)\n\t" \
  "v_exp_f32 v33, v33\n\t" \
  "v_mfma_f32_16x16x32_bf16 v[108:111], a[240:243], v[112:115], v[108:111]\n\t" \
  "s_barrier\n\t" \
  STS("s_memrealtime s[94:95]\n\t") \
  STS("s_memtime s[92:93]\n\t") \
  "v_mfma_f32_16x16x32_bf16 a[0:3], a[128:131], v[128:131], a[0:3]\n\t" \
  "buffer_load_dwordx4 v240, %[wo], s96 offen offset:0 lds\n\t" \
  "ds_read_b64_tr_b16 v[16:17], v210 offset:0\n\t" \
  "ds_read_b128 v[176:179], v215 offset:16384\n\t" \
  "ds_read_b128 v[180:183], v215 offset:17408\n\t" \
  "v_mfma_f32_16x16x32_bf16 a[0:3], a[132:135], v[132:135], a[0:3]\n\t" \
  "ds_read_b64_tr_b16 v[18:19], v210 offset:4096\n\t" \
  "ds_read_b64_tr_b16 v[20:21], v210 offset:8192\n\t" \
  "ds_read_b64_tr_b16 v[22:23], v210 offset:12288\n\t" \
  "ds_read_b64_tr_b16 v[24:25], v211 offset:0\n\t" \
  "v_mfma_f32_16x16x32_bf16 a[4:7], a[136:139], v[128:131], a[4:7]\n\t" \
  "ds_read_b64_tr_b16 v[26:27], v211 offset:4096\n\t" \
  "ds_read_b64_tr_b16 v[28:29], v211 offset:8192\n\t" \
  "ds_read_b64_tr_b16 v[30:31], v211 offset:12288\n\t" \
  "ds_read_b128 v[184:187], v215 offset:18432\n\t" \
  "v_mfma_f32_16x16x32_bf16 a[4:7], a[140:143], v[132:135], a[4:7]\n\t" \
  "buffer_load_dwordx4 v241, %[wo], s96 offen offset:1024 lds\n\t" \
  "v_mfma_f32_16x16x32_bf16 a[8:11], a[144:147], v[128:131], a[8:11]\n\t" \
  "ds_read_b128 v[188:191], v215 offset:19456\n\t" \
  "ds_read_b64_tr_b16 v[48:49], v210 offset:256\n\t" \
  "v_cvt_pk_bf16_f32 v9, v10, v11\n\t" \
  "v_mfma_f32_16x16x32_bf16 a[8:11], a[148:151], v[132:135], a[8:11]\n\t" \
  "ds_read_b64_tr_b16 v[50:51], v210 offset:4352\n\t" \
  "v_cvt_pk_bf16_f32 v10, v12, v13\n\t" \
  "v_mfma_f32_16x16x32_bf16 a[12:15], a[152:155], v[128:131], a[12:15]\n\t" \
  "buffer_load_dwordx4 v248, %[wo], s96 offen offset:2048 lds\n\t" \
  "v_mfma_f32_16x16x32_bf16 a[12:15], a[156:159], v[132:135], a[12:15]\n\t" \
  "v_exp_f32 v34, v34\n\t" \
  "v_mfma_f32_16x16x32_bf16 a[16:19], a[160:163], v[128:131], a[16:19]\n\t" \
  "ds_read_b64_tr_b16 v[52:53], v210 offset:8448\n\t" \
  "v_cvt_pk_bf16_f32 v11, v14, v15\n\t" \
  "v_mfma_f32_16x16x32_bf16 a[16:19], a[164:167], v[132:135], a[16:19]\n\t" \
  "buffer_load_dwordx4 v249, %[wo], s96 offen offset:3072 lds\n\t" \
  "v_mfma_f32_16x16x32_bf16 a[20:23], a[168:171], v[128:131], a[20:23]\n\t" \
  "s_add_u32 s96, %[hl18], 786432\n\t" \
  "ds_read_b64_tr_b16 v[54:55], v210 offset:12544\n\t" \
  "v_cvt_pk_bf16_f32 v32, v32, v33\n\t" \
  "v_mfma_f32_16x16x32_bf16 a[20:23], a[172:175], v[132:135], a[20:23]\n\t" \
  "s_and_b32 s96, s96, 0x1c0000\n\t" \
  "v_exp_f32 v35, v35\n\t" \
  "v_mfma_f32_16x16x32_bf16 a[24:27], a[176:179], v[128:131], a[24:27]\n\t" \
  STS("v_writelane_b32 %[stv], s94, 20\n\t") \
  STS("v_writelane_b32 %[stv], s92, 21\n\t") \
  "s_add_u32 s96, s96, %[wob]\n\t" \
  "v_cvt_pk_bf16_f32 v33, v34, v35\n\t" \
  "s_add_u32 m0, %[m], 114688\n\t" \
  "v_mfma_f32_16x16x32_bf16 a[24:27], a[180:183], v[132:135], a[24:27]\n\t" \
  ".Lhs9:\n\ts_cbranch_execz .Lhs9n\n\t" \
  "v_exp_f32 v36, v36\n\t" \
  "v_mfma_f32_16x16x32_bf16 a[28:31], a[184:187], v[128:131], a[28:31]\n\t" \
  "s_waitcnt lgkmcnt(4)\n\t" \
  "buffer_load_dwordx4 v240, %[wo], s96 offen offset:0 lds\n\t" \
  "v_mfma_f32_16x16x32_bf16 a[28:31], a[188:191], v[132:135], a[28:31]\n\t" \
  "ds_read_b64_tr_b16 v[56:57], v211 offset:256\n\t" \
  "v_add_u32 v230, 0x10000, v242\n\t" \
  "v_mfma_f32_16x16x32_bf16 v[160:163], v[216:219], v[128:131], v[160:163]\n\t" \
  "ds_read_b64_tr_b16 v[58:59], v211 offset:4352\n\t" \
  "v_add_u32 v232, 0x20000, v242\n\t" \
  "v_mfma_f32_16x16x32_bf16 v[160:163], v[216:219], v[132:135], v[160:163]\n\t" \
  "buffer_load_dwordx4 v241, %[wo], s96 offen offset:1024 lds\n\t" \
  "v_mfma_f32_16x16x32_bf16 a[32:35], a[128:131], v[136:139], a[32:35]\n\t" \
  "v_exp_f32 v37, v37\n\t" \
  "v_mfma_f32_16x16x32_bf16 a[32:35], a[132:135], v[140:143], a[32:35]\n\t" \
  "ds_read_b64_tr_b16 v[60:61], v211 offset:8448\n\t" \
  "v_cvt_pk_bf16_f32 v34, v36, v37\n\t" \
  "v_mfma_f32_16x16x32_bf16 a[36:39], a[136:139], v[136:139], a[36:39]\n\t" \
  "buffer_load_dwordx4 v248, %[wo], s96 offen offset:2048 lds\n\t" \
  "v_mfma_f32_16x16x32_bf16 a[36:39], a[140:143], v[140:143], a[36:39]\n\t" \
  "v_exp_f32 v38, v38\n\t" \
  "v_mfma_f32_16x16x32_bf16 a[40:43], a[144:147], v[136:139], a[40:43]\n\t" \
  "ds_read_b64_tr_b16 v[62:63], v211 offset:12544\n\t" \
  "v_mfma_f32_16x16x32_bf16 a[40:43], a[148:151], v[140:143], a[40:43]\n\t" \
  "buffer_load_dwordx4 v249, %[wo], s96 offen offset:3072 lds\n\t" \
  "v_mfma_f32_16x16x32_bf16 a[44:47], a[152:155], v[136:139], a[44:47]\n\t" \
  "ds_read_b64_tr_b16 v[80:81], v210 offset:512\n\t" \
  "v_mfma_f32_16x16x32_bf16 a[44:47], a[156:159], v[140:143], a[44:47]\n\t" \
  "ds_read_b64_tr_b16 a[192:193], v234 offset:0\n\t" \
  "ds_read_b64_tr_b16 v[82:83], v210 offset:4608\n\t" \
  "v_exp_f32 v39, v39\n\t" \
  "v_mfma_f32_16x16x32_bf16 a[48:51], a[160:163], v[136:139], a[48:51]\n\t" \
  "ds_read_b64_tr_b16 v[84:85], v210 offset:8704\n\t" \
  "v_exp_f32 v40, v40\n\t" \
  "v_mfma_f32_16x16x32_bf16 a[48:51], a[164:167], v[140:143], a[48:51]\n\t" \
  "ds_read_b64_tr_b16 a[194:195], v234 offset:4096\n\t" \
  "ds_read_b64_tr_b16 v[86:87], v210 offset:12800\n\t" \
  "v_cvt_pk_bf16_f32 v35, v38, v39\n\t" \
  "v_mfma_f32_16x16x32_bf16 a[52:55], a[168:171], v[136:139], a[52:55]\n\t" \
  "v_exp_f32 v41, v41\n\t" \
  "v_mfma_f32_16x16x32_bf16 a[52:55], a[172:175], v[140:143], a[52:55]\n\t" \
  "ds_read_b64_tr_b16 v[88:89], v211 offset:512\n\t" \
  "v_cvt_pk_bf16_f32 v40, v40, v41\n\t" \
  "v_mfma_f32_16x16x32_bf16 a[56:59], a[176:179], v[136:139], a[56:59]\n\t" \
  "v_exp_f32 v42, v42\n\t" \
  "v_mfma_f32_16x16x32_bf16 a[56:59], a[180:183], v[140:143], a[56:59]\n\t" \
  "ds_read_b64_tr_b16 v[90:91], v211 offset:4608\n\t" \
  "v_mfma_f32_16x16x32_bf16 a[60:63], a[184:187], v[136:139], a[60:63]\n\t" \
  "ds_read_b64_tr_b16 v[92:93], v211 offset:8704\n\t" \
  "v_mfma_f32_16x16x32_bf16 a[60:63], a[188:191], v[140:143], a[60:63]\n\t" \
  "v_exp_f32 v43, v43\n\t" \
  "v_mfma_f32_16x16x32_bf16 v[164:167], v[216:219], v[136:139], v[164:167]\n\t" \
  "ds_read_b64_tr_b16 a[196:197], v235 offset:0\n\t" \
  "ds_read_b64_tr_b16 v[94:95], v211 offset:12800\n\t" \
  "v_cvt_pk_bf16_f32 v41, v42, v43\n\t" \
  "v_mfma_f32_16x16x32_bf16 v[164:167], v[216:219], v[140:143], v[164:167]\n\t" \
  "ds_read_b64_tr_b16 a[198:199], v235 offset:4096\n\t" \
  "ds_read_b64_tr_b16 v[112:113], v210 offset:768\n\t" \
  "v_exp_f32 v44, v44\n\t" \
  "v_mfma_f32_16x16x32_bf16 a[0:3], v[16:19], v[0:3], a[0:3]\n\t" \
  "ds_read_b64_tr_b16 v[114:115], v210 offset:4864\n\t" \
  "v_mfma_f32_16x16x32_bf16 a[0:3], v[20:23], v[8:11], a[0:3]\n\t" \
  "v_exp_f32 v45, v45\n\t" \
  "v_mfma_f32_16x16x32_bf16 v[160:163], v[216:219], v[0:3], v[160:163]\n\t" \
  "ds_read_b64_tr_b16 v[116:117], v210 offset:8960\n\t" \
  "v_cvt_pk_bf16_f32 v42, v44, v45\n\t" \
  "v_mfma_f32_16x16x32_bf16 v[160:163], v[216:219], v[8:11], v[160:163]\n\t" \
  "ds_read_b64_tr_b16 v[118:119], v210 offset:13056\n\t" \
  "v_mfma_f32_16x16x32_bf16 a[4:7], v[24:27], v[0:3], a[4:7]\n\t" \
  "v_exp_f32 v46, v46\n\t" \
  "v_mfma_f32_16x16x32_bf16 a[4:7], v[28:31], v[8:11], a[4:7]\n\t" \
  "s_waitcnt lgkmcnt(15)\n\t" \
  "ds_read_b64_tr_b16 v[120:121], v211 offset:768\n\t" \
  "v_mfma_f32_16x16x32_bf16 a[8:11], v[48:51], v[0:3], a[8:11]\n\t" \
  "ds_read_b64_tr_b16 v[122:123], v211 offset:4864\n\t" \
  "v_exp_f32 v47, v47\n\t" \
  "v_mfma_f32_16x16x32_bf16 a[8:11], v[52:55], v[8:11], a[8:11]\n\t" \
  "v_rcp_f32 v224, v160\n\t" \
  "v_mfma_f32_16x16x32_bf16 a[12:15], v[56:59], v[0:3], a[12:15]\n\t" \
  "ds_read_b64_tr_b16 v[124:125], v211 offset:8960\n\t" \
  "v_accvgpr_read_b32 v128, a0\n\t" \
  "v_accvgpr_read_b32 v129, a1\n\t" \
  "v_accvgpr_read_b32 v130, a2\n\t" \
  "v_accvgpr_read_b32 v131, a3\n\t" \
  "v_accvgpr_read_b32 v132, a4\n\t" \
  "v_accvgpr_read_b32 v133, a5\n\t" \
  "v_accvgpr_read_b32 v134, a6\n\t" \
  "v_accvgpr_read_b32 v135, a7\n\t" \
  "v_pk_mul_f32 v[128:129], v[128:129], v[224:225] op_sel_hi:[1,0]\n\t" \
  "v_pk_mul_f32 v[130:131], v[130:131], v[224:225] op_sel_hi:[1,0]\n\t" \
  "v_pk_mul_f32 v[132:133], v[132:133], v[224:225] op_sel_hi:[1,0]\n\t" \
  "v_pk_mul_f32 v[134:135], v[134:135], v[224:225] op_sel_hi:[1,0]\n\t" \
  "v_cvt_pk_bf16_f32 v128, v128, v129\n\t" \
  "v_cvt_pk_bf16_f32 v129, v130, v131\n\t" \
  "v_cvt_pk_bf16_f32 v130, v132, v133\n\t" \
  "v_cvt_pk_bf16_f32 v131, v134, v135\n\t" \
  "buffer_store_dwordx4 v[128:131], v242, %[ws], s98 offen offset:0 sc1\n\t" \
  "v_mfma_f32_16x16x32_bf16 a[12:15], v[60:63], v[8:11], a[12:15]\n\t" \
  "s_waitcnt lgkmcnt(8)\n\t" \
  "ds_read_b64_tr_b16 a[200:201], v234 offset:8192\n\t" \
  "ds_read_b64_tr_b16 v[126:127], v211 offset:13056\n\t" \
  "v_cmp_lt_f32 vcc, 0x5f800000, v160\n\t" \
  "v_mfma_f32_16x16x32_bf16 a[16:19], v[80:83], v[0:3], a[16:19]\n\t" \
  "s_cmp_lg_u64 vcc, 0\n\t" \
  "s_cselect_b32 s89, s78, s89\n\t" \
  "v_or_b32 v254, s89, v254\n\t" \
  "v_mfma_f32_16x16x32_bf16 a[16:19], v[84:87], v[8:11], a[16:19]\n\t" \
  "ds_read_b64_tr_b16 a[202:203], v234 offset:12288\n\t" \
  "v_exp_f32 v64, v64\n\t" \
  "v_mfma_f32_16x16x32_bf16 a[20:23], v[88:91], v[0:3], a[20:23]\n\t" \
  "v_accvgpr_read_b32 v136, a8\n\t" \
  "v_accvgpr_read_b32 v137, a9\n\t" \
  "v_accvgpr_read_b32 v138, a10\n\t" \
  "v_accvgpr_read_b32 v139, a11\n\t" \
  "v_accvgpr_read_b32 v140, a12\n\t" \
  "v_accvgpr_read_b32 v141, a13\n\t" \
  "v_accvgpr_read_b32 v142, a14\n\t" \
  "v_accvgpr_read_b32 v143, a15\n\t" \
  "v_pk_mul_f32 v[136:137], v[136:137], v[224:225] op_sel_hi:[1,0]\n\t" \
  "v_pk_mul_f32 v[138:139], v[138:139], v[224:225] op_sel_hi:[1,0]\n\t" \
  "v_pk_mul_f32 v[140:141], v[140:141], v[224:225] op_sel_hi:[1,0]\n\t" \
  "v_pk_mul_f32 v[142:143], v[142:143], v[224:225] op_sel_hi:[1,0]\n\t" \
  "v_cvt_pk_bf16_f32 v136, v136, v137\n\t" \
  "v_cvt_pk_bf16_f32 v137, v138, v139\n\t" \
  "v_cvt_pk_bf16_f32 v138, v140, v141\n\t" \
  "v_cvt_pk_bf16_f32 v139, v142, v143\n\t" \
  "buffer_store_dwordx4 v[136:139], v242, %[ws], s98 offen offset:1024 sc1\n\t" \
  "v_mfma_f32_16x16x32_bf16 a[20:23], v[92:95], v[8:11], a[20:23]\n\t" \
  "s_waitcnt lgkmcnt(1)\n\t" \
  "ds_read_b64_tr_b16 a[204:205], v235 offset:8192\n\t" \
  "v_exp_f32 v65, v65\n\t" \
  "v_mfma_f32_16x16x32_bf16 a[24:27], v[112:115], v[0:3], a[24:27]\n\t" \
  "ds_read_b128 a[224:227], v213 offset:16384\n\t" \
  "v_exp_f32 v66, v66\n\t" \
  "v_mfma_f32_16x16x32_bf16 a[24:27], v[116:119], v[8:11], a[24:27]\n\t" \
  "ds_read_b64_tr_b16 a[206:207], v235 offset:12288\n\t" \
  "v_exp_f32 v67, v67\n\t" \
  "v_mfma_f32_16x16x32_bf16 a[28:31], v[120:123], v[0:3], a[28:31]\n\t" \
  "ds_read_b128 a[228:231], v213 offset:17408\n\t" \
  "v_exp_f32 v68, v68\n\t" \
  "v_mfma_f32_16x16x32_bf16 a[28:31], v[124:127], v[8:11], a[28:31]\n\t" \
  "ds_read_b128 a[232:235], v213 offset:18432\n\t" \
  "v_exp_f32 v69, v69\n\t" \
  "v_mfma_f32_16x16x32_bf16 a[64:67], a[128:131], v[144:147], a[64:67]\n\t" \
  "ds_read_b128 a[236:239], v213 offset:19456\n\t" \
  "v_exp_f32 v70, v70\n\t" \
  "v_mfma_f32_16x16x32_bf16 a[64:67], a[132:135], v[148:151], a[64:67]\n\t" \
  "ds_write_b128 v213, v[128:131] offset:16384\n\t" \
  "s_barrier\n\t" \
  "v_mfma_f32_16x16x32_bf16 a[68:71], a[136:139], v[144:147], a[68:71]\n\t" \
  "ds_write_b128 v213, v[136:139] offset:17408\n\t" \
  "v_accvgpr_read_b32 v0, a16\n\t" \
  "v_accvgpr_read_b32 v1, a17\n\t" \
  "v_accvgpr_read_b32 v2, a18\n\t" \
  "v_accvgpr_read_b32 v3, a19\n\t" \
  "v_accvgpr_read_b32 v4, a20\n\t" \
  "v_accvgpr_read_b32 v5, a21\n\t" \
  "v_accvgpr_read_b32 v6, a22\n\t" \
  "v_accvgpr_read_b32 v7, a23\n\t" \
  "v_pk_mul_f32 v[0:1], v[0:1], v[224:225] op_sel_hi:[1,0]\n\t" \
  "v_pk_mul_f32 v[2:3], v[2:3], v[224:225] op_sel_hi:[1,0]\n\t" \
  "v_pk_mul_f32 v[4:5], v[4:5], v[224:225] op_sel_hi:[1,0]\n\t" \
  "v_pk_mul_f32 v[6:7], v[6:7], v[224:225] op_sel_hi:[1,0]\n\t" \
  "v_cvt_pk_bf16_f32 v0, v0, v1\n\t" \
  "v_cvt_pk_bf16_f32 v1, v2, v3\n\t" \
  "v_cvt_pk_bf16_f32 v2, v4, v5\n\t" \
  "v_cvt_pk_bf16_f32 v3, v6, v7\n\t" \
  "buffer_store_dwordx4 v[0:3], v242, %[ws], s98 offen offset:2048 sc1\n\t" \
  "v_mfma_f32_16x16x32_bf16 a[68:71], a[140:143], v[148:151], a[68:71]\n\t" \
  "ds_write_b128 v213, v[0:3] offset:18432\n\t" \
  "v_accvgpr_read_b32 v8, a24\n\t" \
  "v_accvgpr_read_b32 v9, a25\n\t" \
  "v_accvgpr_read_b32 v10, a26\n\t" \
  "v_accvgpr_read_b32 v11, a27\n\t" \
  "v_accvgpr_read_b32 v12, a28\n\t" \
  "v_accvgpr_read_b32 v13, a29\n\t" \
  "v_accvgpr_read_b32 v14, a30\n\t" \
  "v_accvgpr_read_b32 v15, a31\n\t" \
  "v_pk_mul_f32 v[8:9], v[8:9], v[224:225] op_sel_hi:[1,0]\n\t" \
  "v_pk_mul_f32 v[10:11], v[10:11], v[224:225] op_sel_hi:[1,0]\n\t" \
  "v_pk_mul_f32 v[12:13], v[12:13], v[224:225] op_sel_hi:[1,0]\n\t" \
  "v_pk_mul_f32 v[14:15], v[14:15], v[224:225] op_sel_hi:[1,0]\n\t" \
  "v_cvt_pk_bf16_f32 v8, v8, v9\n\t" \
  "v_cvt_pk_bf16_f32 v9, v10, v11\n\t" \
  "v_cvt_pk_bf16_f32 v10, v12, v13\n\t" \
  "v_cvt_pk_bf16_f32 v11, v14, v15\n\t" \
  "buffer_store_dwordx4 v[8:11], v242, %[ws], s98 offen offset:3072 sc1\n\t" \
  "v_mfma_f32_16x16x32_bf16 a[72:75], a[144:147], v[144:147], a[72:75]\n\t" \
  "ds_write_b128 v213, v[8:11] offset:19456\n\t" \
  "v_mfma_f32_16x16x32_bf16 a[72:75], a[148:151], v[148:151], a[72:75]\n\t" \
  "s_xor_b32 s98, %[eso], 32768\n\t" \
  "ds_read_b64_tr_b16 a[208:209], v236 offset:32768\n\t" \
  "v_exp_f32 v71, v71\n\t" \
  "v_mfma_f32_16x16x32_bf16 a[76:79], a[152:155], v[144:147], a[76:79]\n\t" \
  "v_exp_f32 v72, v72\n\t" \
  "v_mfma_f32_16x16x32_bf16 a[76:79], a[156:159], v[148:151], a[76:79]\n\t" \
  "v_exp_f32 v73, v73\n\t" \
  "v_mfma_f32_16x16x32_bf16 a[80:83], a[160:163], v[144:147], a[80:83]\n\t" \
  "ds_read_b64_tr_b16 a[210:211], v236 offset:36864\n\t" \
  "v_cvt_pk_bf16_f32 v43, v46, v47\n\t" \
  "v_mfma_f32_16x16x32_bf16 a[80:83], a[164:167], v[148:151], a[80:83]\n\t" \
  "v_exp_f32 v74, v74\n\t" \
  "v_mfma_f32_16x16x32_bf16 a[84:87], a[168:171], v[144:147], a[84:87]\n\t" \
  "v_exp_f32 v75, v75\n\t" \
  "v_mfma_f32_16x16x32_bf16 a[84:87], a[172:175], v[148:151], a[84:87]\n\t" \
  "v_exp_f32 v76, v76\n\t" \
  "v_mfma_f32_16x16x32_bf16 a[88:91], a[176:179], v[144:147], a[88:91]\n\t" \
  "v_exp_f32 v77, v77\n\t" \
  "v_mfma_f32_16x16x32_bf16 a[88:91], a[180:183], v[148:151], a[88:91]\n\t" \
  "ds_read_b64_tr_b16 a[212:213], v237 offset:32768\n\t" \
  "v_cvt_pk_bf16_f32 v64, v64, v65\n\t" \
  "v_mfma_f32_16x16x32_bf16 a[92:95], a[184:187], v[144:147], a[92:95]\n\t" \
  "v_exp_f32 v78, v78\n\t" \
  "v_mfma_f32_16x16x32_bf16 a[92:95], a[188:191], v[148:151], a[92:95]\n\t" \
  "ds_read_b64_tr_b16 a[214:215], v237 offset:36864\n\t" \
  "v_cvt_pk_bf16_f32 v65, v66, v67\n\t" \
  "v_mfma_f32_16x16x32_bf16 v[168:171], v[216:219], v[144:147], v[168:171]\n\t" \
  "v_exp_f32 v79, v79\n\t" \
  "v_mfma_f32_16x16x32_bf16 v[168:171], v[216:219], v[148:151], v[168:171]\n\t" \
  "s_add_u32 s96, %[hl18], 786432\n\t" \
  "v_exp_f32 v96, v96\n\t" \
  "v_mfma_f32_16x16x32_bf16 a[32:35], v[16:19], v[32:35], a[32:35]\n\t" \
  "s_and_b32 s96, s96, 0x1c0000\n\t" \
  "ds_read_b64_tr_b16 a[216:217], v236 offset:40960\n\t" \
  "v_cvt_pk_bf16_f32 v66, v68, v69\n\t" \
  "v_mfma_f32_16x16x32_bf16 a[32:35], v[20:23], v[40:43], a[32:35]\n\t" \
  "s_add_u32 s96, s96, s77\n\t" \
  "v_exp_f32 v97, v97\n\t" \
  "v_mfma_f32_16x16x32_bf16 v[164:167], v[216:219], v[32:35], v[164:167]\n\t" \
  "s_add_u32 m0, %[m], 147456\n\t" \
  "v_exp_f32 v98, v98\n\t" \
  "v_mfma_f32_16x16x32_bf16 v[164:167], v[216:219], v[40:43], v[164:167]\n\t" \
  "ds_read_b64_tr_b16 a[218:219], v236 offset:45056\n\t" \
  "v_cvt_pk_bf16_f32 v67, v70, v71\n\t" \
  "v_mfma_f32_16x16x32_bf16 a[36:39], v[24:27], v[32:35], a[36:39]\n\t" \
  "v_exp_f32 v99, v99\n\t" \
  "v_mfma_f32_16x16x32_bf16 a[36:39], v[28:31], v[40:43], a[36:39]\n\t" \
  "v_cvt_pk_bf16_f32 v72, v72, v73\n\t" \
  "v_cvt_pk_bf16_f32 v73, v74, v75\n\t" \
  "s_nop 0\n\t" \
  "v_rcp_f32 v226, v164\n\t" \
  "v_mfma_f32_16x16x32_bf16 a[40:43], v[48:51], v[32:35], a[40:43]\n\t" \
  "buffer_load_dwordx4 v240, %[wo], s96 offen offset:0 lds\n\t" \
  "v_accvgpr_read_b32 v0, a32\n\t" \
  "v_accvgpr_read_b32 v1, a33\n\t" \
  "v_mfma_f32_16x16x32_bf16 a[40:43], v[52:55], v[40:43], a[40:43]\n\t" \
  "v_cmp_lt_f32 vcc, 0x5f800000, v164\n\t" \
  "v_accvgpr_read_b32 v2, a34\n\t" \
  "v_accvgpr_read_b32 v3, a35\n\t" \
  "v_mfma_f32_16x16x32_bf16 a[44:47], v[56:59], v[32:35], a[44:47]\n\t" \
  "s_cmp_lg_u64 vcc, 0\n\t" \
  "s_cselect_b32 s89, s78, s89\n\t" \
  "v_or_b32 v254, s89, v254\n\t" \
  "v_mfma_f32_16x16x32_bf16 a[44:47], v[60:63], v[40:43], a[44:47]\n\t" \
  "buffer_load_dwordx4 v241, %[wo], s96 offen offset:1024 lds\n\t" \
  "v_accvgpr_read_b32 v4, a36\n\t" \
  "v_accvgpr_read_b32 v5, a37\n\t" \
  "v_mfma_f32_16x16x32_bf16 a[48:51], v[80:83], v[32:35], a[48:51]\n\t" \
  "ds_read_b64_tr_b16 a[220:221], v237 offset:40960\n\t" \
  "v_accvgpr_read_b32 v6, a38\n\t" \
  "v_accvgpr_read_b32 v7, a39\n\t" \
  "v_mfma_f32_16x16x32_bf16 a[48:51], v[84:87], v[40:43], a[48:51]\n\t" \
  "buffer_load_dwordx4 v248, %[wo], s96 offen offset:2048 lds\n\t" \
  "v_mfma_f32_16x16x32_bf16 a[52:55], v[88:91], v[32:35], a[52:55]\n\t" \
  "ds_read_b64_tr_b16 a[222:223], v237 offset:45056\n\t" \
  "v_mul_f32 v0, v0, v226\n\t" \
  "v_mul_f32 v1, v1, v226\n\t" \
  "v_mfma_f32_16x16x32_bf16 a[52:55], v[92:95], v[40:43], a[52:55]\n\t" \
  "v_exp_f32 v100, v100\n\t" \
  "v_mul_f32 v2, v2, v226\n\t" \
  "v_mul_f32 v3, v3, v226\n\t" \
  "v_mfma_f32_16x16x32_bf16 a[56:59], v[112:115], v[32:35], a[56:59]\n\t" \
  "buffer_load_dwordx4 v249, %[wo], s96 offen offset:3072 lds\n\t" \
  "v_mfma_f32_16x16x32_bf16 a[56:59], v[116:119], v[40:43], a[56:59]\n\t" \
  "v_exp_f32 v101, v101\n\t" \
  "v_mul_f32 v4, v4, v226\n\t" \
  "v_mul_f32 v5, v5, v226\n\t" \
  "v_mfma_f32_16x16x32_bf16 a[60:63], v[120:123], v[32:35], a[60:63]\n\t" \
  "v_exp_f32 v102, v102\n\t" \
  "v_mul_f32 v6, v6, v226\n\t" \
  "v_mul_f32 v7, v7, v226\n\t" \
  "v_mfma_f32_16x16x32_bf16 a[60:63], v[124:127], v[40:43], a[60:63]\n\t" \
  "v_cvt_pk_bf16_f32 v32, v0, v1\n\t" \
  "v_cvt_pk_bf16_f32 v33, v2, v3\n\t" \
  "v_cvt_pk_bf16_f32 v34, v4, v5\n\t" \
  "v_cvt_pk_bf16_f32 v35, v6, v7\n\t" \
  "v_mfma_f32_16x16x32_bf16 a[96:99], a[128:131], v[152:155], a[96:99]\n\t" \
  "v_accvgpr_read_b32 v8, a40\n\t" \
  "v_accvgpr_read_b32 v9, a41\n\t" \
  "v_accvgpr_read_b32 v10, a42\n\t" \
  "v_accvgpr_read_b32 v11, a43\n\t" \
  "v_mfma_f32_16x16x32_bf16 a[96:99], a[132:135], v[156:159], a[96:99]\n\t" \
  "v_accvgpr_read_b32 v12, a44\n\t" \
  "v_accvgpr_read_b32 v13, a45\n\t" \
  "v_accvgpr_read_b32 v14, a46\n\t" \
  "v_accvgpr_read_b32 v15, a47\n\t" \
  "v_mfma_f32_16x16x32_bf16 a[100:103], a[136:139], v[152:155], a[100:103]\n\t" \
  "v_mul_f32 v8, v8, v226\n\t" \
  "v_mul_f32 v9, v9, v226\n\t" \
  "v_mul_f32 v10, v10, v226\n\t" \
  "v_mul_f32 v11, v11, v226\n\t" \
  "v_mfma_f32_16x16x32_bf16 a[100:103], a[140:143], v[156:159], a[100:103]\n\t" \
  "v_mul_f32 v12, v12, v226\n\t" \
  "v_mul_f32 v13, v13, v226\n\t" \
  "v_mul_f32 v14, v14, v226\n\t" \
  "v_mul_f32 v15, v15, v226\n\t" \
  "v_mfma_f32_16x16x32_bf16 a[104:107], a[144:147], v[152:155], a[104:107]\n\t" \
  "s_waitcnt vmcnt(8) lgkmcnt(0)\n\t" \
  "v_cvt_pk_bf16_f32 v36, v8, v9\n\t" \
  "v_cvt_pk_bf16_f32 v37, v10, v11\n\t" \
  "v_cvt_pk_bf16_f32 v38, v12, v13\n\t" \
  "v_cvt_pk_bf16_f32 v39, v14, v15\n\t" \
  "v_mfma_f32_16x16x32_bf16 a[104:107], a[148:151], v[156:159], a[104:107]\n\t" \
  "v_accvgpr_read_b32 v0, a48\n\t" \
  "v_accvgpr_read_b32 v1, a49\n\t" \
  "s_barrier\n\t" \
  "v_mfma_f32_16x16x32_bf16 a[108:111], a[152:155], v[152:155], a[108:111]\n\t" \
  "buffer_store_dwordx4 v[32:35], v242, %[ws], s98 offen offset:0 sc1\n\t" \
  "v_accvgpr_read_b32 v2, a50\n\t" \
  "v_accvgpr_read_b32 v3, a51\n\t" \
  "v_accvgpr_read_b32 v4, a52\n\t" \
  "v_mfma_f32_16x16x32_bf16 a[108:111], a[156:159], v[156:159], a[108:111]\n\t" \
  "s_add_u32 s96, %[hl18], 1048576\n\t" \
  "s_and_b32 s96, s96, 0x1c0000\n\t" \
  "s_add_u32 s96, s96, %[wob]\n\t" \
  "v_accvgpr_read_b32 v5, a53\n\t" \
  "v_mfma_f32_16x16x32_bf16 a[112:115], a[160:163], v[152:155], a[112:115]\n\t" \
  "s_add_u32 m0, %[m], 0\n\t" \
  "v_accvgpr_read_b32 v6, a54\n\t" \
  "v_accvgpr_read_b32 v7, a55\n\t" \
  "v_mul_f32 v0, v0, v226\n\t" \
  "v_mfma_f32_16x16x32_bf16 a[112:115], a[164:167], v[156:159], a[112:115]\n\t" \
  "buffer_load_dwordx4 v240, %[wo], s96 offen offset:0 lds\n\t" \
  "v_mul_f32 v1, v1, v226\n\t" \
  "v_mul_f32 v2, v2, v226\n\t" \
  "v_mul_f32 v3, v3, v226\n\t" \
  "v_mfma_f32_16x16x32_bf16 a[116:119], a[168:171], v[152:155], a[116:119]\n\t" \
  "v_mul_f32 v4, v4, v226\n\t" \
  "v_mul_f32 v5, v5, v226\n\t" \
  "v_mul_f32 v6, v6, v226\n\t" \
  "v_mul_f32 v7, v7, v226\n\t" \
  "v_mfma_f32_16x16x32_bf16 a[116:119], a[172:175], v[156:159], a[116:119]\n\t" \
  "buffer_load_dwordx4 v241, %[wo], s96 offen offset:1024 lds\n\t" \
  "v_cvt_pk_bf16_f32 v40, v0, v1\n\t" \
  "v_cvt_pk_bf16_f32 v41, v2, v3\n\t" \
  "v_cvt_pk_bf16_f32 v42, v4, v5\n\t" \
  "v_mfma_f32_16x16x32_bf16 a[120:123], a[176:179], v[152:155], a[120:123]\n\t" \
  "v_cvt_pk_bf16_f32 v74, v76, v77\n\t" \
  "v_cvt_pk_bf16_f32 v43, v6, v7\n\t" \
  "v_accvgpr_read_b32 v8, a56\n\t" \
  "v_accvgpr_read_b32 v9, a57\n\t" \
  "v_mfma_f32_16x16x32_bf16 a[120:123], a[180:183], v[156:159], a[120:123]\n\t" \
  "v_accvgpr_read_b32 v10, a58\n\t" \
  "v_accvgpr_read_b32 v11, a59\n\t" \
  "v_accvgpr_read_b32 v12, a60\n\t" \
  "v_exp_f32 v103, v103\n\t" \
  "v_mfma_f32_16x16x32_bf16 a[124:127], a[184:187], v[152:155], a[124:127]\n\t" \
  "buffer_load_dwordx4 v248, %[wo], s96 offen offset:2048 lds\n\t" \
  "v_accvgpr_read_b32 v13, a61\n\t" \
  "v_accvgpr_read_b32 v14, a62\n\t" \
  "v_accvgpr_read_b32 v15, a63\n\t" \
  "v_mfma_f32_16x16x32_bf16 a[124:127], a[188:191], v[156:159], a[124:127]\n\t" \
  "v_cvt_pk_bf16_f32 v75, v78, v79\n\t" \
  "v_mul_f32 v8, v8, v226\n\t" \
  "v_mul_f32 v9, v9, v226\n\t" \
  "v_mul_f32 v10, v10, v226\n\t" \
  "v_mfma_f32_16x16x32_bf16 v[172:175], v[216:219], v[152:155], v[172:175]\n\t" \
  "v_cvt_pk_bf16_f32 v96, v96, v97\n\t" \
  "v_exp_f32 v104, v104\n\t" \
  "v_mul_f32 v11, v11, v226\n\t" \
  "v_mul_f32 v12, v12, v226\n\t" \
  "v_mfma_f32_16x16x32_bf16 v[172:175], v[216:219], v[156:159], v[172:175]\n\t" \
  "buffer_store_dwordx4 v[36:39], v242, %[ws], s98 offen offset:1024 sc1\n\t" \
  "v_exp_f32 v105, v105\n\t" \
  "v_mul_f32 v13, v13, v226\n\t" \
  "v_exp_f32 v106, v106\n\t" \
  "v_mfma_f32_16x16x32_bf16 a[64:67], v[16:19], v[64:67], a[64:67]\n\t" \
  "v_cvt_pk_bf16_f32 v97, v98, v99\n\t" \
  "v_exp_f32 v107, v107\n\t" \
  "v_mfma_f32_16x16x32_bf16 a[64:67], v[20:23], v[72:75], a[64:67]\n\t" \
  "v_cvt_pk_bf16_f32 v98, v100, v101\n\t" \
  "v_mul_f32 v14, v14, v226\n\t" \
  "v_mfma_f32_16x16x32_bf16 v[168:171], v[216:219], v[64:67], v[168:171]\n\t" \
  "v_mul_f32 v15, v15, v226\n\t" \
  "v_exp_f32 v108, v108\n\t" \
  "v_mfma_f32_16x16x32_bf16 v[168:171], v[216:219], v[72:75], v[168:171]\n\t" \
  "v_cvt_pk_bf16_f32 v99, v102, v103\n\t" \
  "v_cvt_pk_bf16_f32 v44, v8, v9\n\t" \
  "v_mfma_f32_16x16x32_bf16 a[68:71], v[24:27], v[64:67], a[68:71]\n\t" \
  "v_cvt_pk_bf16_f32 v104, v104, v105\n\t" \
  "v_cvt_pk_bf16_f32 v45, v10, v11\n\t" \
  "v_mfma_f32_16x16x32_bf16 a[68:71], v[28:31], v[72:75], a[68:71]\n\t" \
  "v_cvt_pk_bf16_f32 v105, v106, v107\n\t" \
  "v_exp_f32 v109, v109\n\t" \
  "v_mfma_f32_16x16x32_bf16 a[72:75], v[48:51], v[64:67], a[72:75]\n\t" \
  "v_rcp_f32 v220, v168\n\t" \
  "v_mfma_f32_16x16x32_bf16 a[72:75], v[52:55], v[72:75], a[72:75]\n\t" \
  "v_cmp_lt_f32 vcc, 0x5f800000, v168\n\t" \
  "v_exp_f32 v110, v110\n\t" \
  "v_mfma_f32_16x16x32_bf16 a[76:79], v[56:59], v[64:67], a[76:79]\n\t" \
  "s_cmp_lg_u64 vcc, 0\n\t" \
  "s_cselect_b32 s89, s78, s89\n\t" \
  "v_or_b32 v254, s89, v254\n\t" \
  "v_mfma_f32_16x16x32_bf16 a[76:79], v[60:63], v[72:75], a[76:79]\n\t" \
  "buffer_load_dwordx4 v249, %[wo], s96 offen offset:3072 lds\n\t" \
  "v_mfma_f32_16x16x32_bf16 a[80:83], v[80:83], v[64:67], a[80:83]\n\t" \
  "ds_read_b64_tr_b16 a[128:129], v234 offset:49152\n\t" \
  "v_cvt_pk_bf16_f32 v46, v12, v13\n\t" \
  "v_mfma_f32_16x16x32_bf16 a[80:83], v[84:87], v[72:75], a[80:83]\n\t" \
  "v_cvt_pk_bf16_f32 v106, v108, v109\n\t" \
  "v_exp_f32 v111, v111\n\t" \
  "v_mfma_f32_16x16x32_bf16 a[84:87], v[88:91], v[64:67], a[84:87]\n\t" \
  "ds_read_b64_tr_b16 a[130:131], v234 offset:53248\n\t" \
  "v_cvt_pk_bf16_f32 v47, v14, v15\n\t" \
  "v_mfma_f32_16x16x32_bf16 a[84:87], v[92:95], v[72:75], a[84:87]\n\t" \
  "ds_read_b64_tr_b16 a[132:133], v235 offset:49152\n\t" \
  "v_cvt_pk_bf16_f32 v107, v110, v111\n\t" \
  "v_mfma_f32_16x16x32_bf16 a[88:91], v[112:115], v[64:67], a[88:91]\n\t" \
  "ds_read_b128 a[240:243], v213 offset:32768\n\t" \
  "v_mfma_f32_16x16x32_bf16 a[88:91], v[116:119], v[72:75], a[88:91]\n\t" \
  "s_waitcnt vmcnt(10)\n\t" \
  "buffer_store_dword v243, v254, %[ws], %[fw2] offen offset:0 sc1\n\t" \
  "ds_read_b64_tr_b16 a[134:135], v235 offset:53248\n\t" \
  "v_mfma_f32_16x16x32_bf16 a[92:95], v[120:123], v[64:67], a[92:95]\n\t" \
  "ds_read_b128 a[244:247], v213 offset:33792\n\t" \
  "v_mfma_f32_16x16x32_bf16 a[92:95], v[124:127], v[72:75], a[92:95]\n\t" \
  "buffer_store_dwordx4 v[40:43], v242, %[ws], s98 offen offset:2048 sc1\n\t" \
  "v_accvgpr_read_b32 v64, a64\n\t" \
  "v_accvgpr_read_b32 v65, a65\n\t" \
  "v_accvgpr_read_b32 v66, a66\n\t" \
  "v_mfma_f32_16x16x32_bf16 a[96:99], v[16:19], v[96:99], a[96:99]\n\t" \
  "v_accvgpr_read_b32 v67, a67\n\t" \
  "v_accvgpr_read_b32 v68, a68\n\t" \
  "v_mfma_f32_16x16x32_bf16 a[96:99], v[20:23], v[104:107], a[96:99]\n\t" \
  "s_barrier\n\t" \
  "v_mfma_f32_16x16x32_bf16 v[172:175], v[216:219], v[96:99], v[172:175]\n\t" \
  "ds_read_b128 v[128:131], v242 offset:16384\n\t" \
  "v_accvgpr_read_b32 v69, a69\n\t" \
  "v_mfma_f32_16x16x32_bf16 v[172:175], v[216:219], v[104:107], v[172:175]\n\t" \
  "v_accvgpr_read_b32 v70, a70\n\t" \
  "v_accvgpr_read_b32 v71, a71\n\t" \
  "v_mfma_f32_16x16x32_bf16 a[100:103], v[24:27], v[96:99], a[100:103]\n\t" \
  "buffer_store_dwordx4 v[44:47], v242, %[ws], s98 offen offset:3072 sc1\n\t" \
  "v_mul_f32 v64, v64, v220\n\t" \
  "ds_read_b128 v[132:135], v242 offset:17408\n\t" \
  "v_mul_f32 v65, v65, v220\n\t" \
  "v_mfma_f32_16x16x32_bf16 a[100:103], v[28:31], v[104:107], a[100:103]\n\t" \
  "s_xor_b32 s98, %[eso], 65536\n\t" \
  "v_rcp_f32 v222, v172\n\t" \
  "v_mfma_f32_16x16x32_bf16 a[104:107], v[48:51], v[96:99], a[104:107]\n\t" \
  "ds_read_b128 v[136:139], v242 offset:18432\n\t" \
  "v_cmp_lt_f32 vcc, 0x5f800000, v172\n\t" \
  "v_mfma_f32_16x16x32_bf16 a[104:107], v[52:55], v[104:107], a[104:107]\n\t" \
  "s_cmp_lg_u64 vcc, 0\n\t" \
  "s_cselect_b32 s89, s78, s89\n\t" \
  "v_or_b32 v254, s89, v254\n\t" \
  "v_mfma_f32_16x16x32_bf16 a[108:111], v[56:59], v[96:99], a[108:111]\n\t" \
  "s_cmp_lg_u32 s89, 0\n\t" \
  "s_cbranch_scc1 .Lat_x\n\t" \
  ".Lat_y:\n\t" \
  "v_mfma_f32_16x16x32_bf16 a[108:111], v[60:63], v[104:107], a[108:111]\n\t" \
  "ds_read_b128 v[140:143], v242 offset:19456\n\t" \
  "v_mul_f32 v66, v66, v220\n\t" \
  "v_mfma_f32_16x16x32_bf16 a[112:115], v[80:83], v[96:99], a[112:115]\n\t" \
  "ds_read_b128 v[144:147], v242 offset:20480\n\t" \
  "v_mul_f32 v67, v67, v220\n\t" \
  "v_mfma_f32_16x16x32_bf16 a[112:115], v[84:87], v[104:107], a[112:115]\n\t" \
  "ds_read_b64_tr_b16 a[136:137], v234 offset:57344\n\t" \
  "v_mul_f32 v68, v68, v220\n\t" \
  "v_mfma_f32_16x16x32_bf16 a[116:119], v[88:91], v[96:99], a[116:119]\n\t" \
  "ds_read_b128 v[148:151], v242 offset:21504\n\t" \
  "v_mul_f32 v69, v69, v220\n\t" \
  "v_mfma_f32_16x16x32_bf16 a[116:119], v[92:95], v[104:107], a[116:119]\n\t" \
  "ds_read_b64_tr_b16 a[138:139], v234 offset:61440\n\t" \
  "v_mul_f32 v70, v70, v220\n\t" \
  "v_mfma_f32_16x16x32_bf16 a[120:123], v[112:115], v[96:99], a[120:123]\n\t" \
  "ds_read_b128 v[152:155], v242 offset:22528\n\t" \
  "v_mul_f32 v71, v71, v220\n\t" \
  "v_mfma_f32_16x16x32_bf16 a[120:123], v[116:119], v[104:107], a[120:123]\n\t" \
  "ds_read_b128 v[156:159], v242 offset:23552\n\t" \
  "v_mfma_f32_16x16x32_bf16 a[124:127], v[120:123], v[96:99], a[124:127]\n\t" \
  "ds_read_b64_tr_b16 a[140:141], v235 offset:57344\n\t" \
  "v_mfma_f32_16x16x32_bf16 a[124:127], v[124:127], v[104:107], a[124:127]\n\t" \
  "s_waitcnt lgkmcnt(7)\n\t" \
  "ds_read_b64_tr_b16 a[142:143], v235 offset:61440\n\t" \
  "ds_read_b128 a[248:251], v213 offset:34816\n\t" \
  ".Lat_join:\n\t" \
  STS("s_memrealtime s[94:95]\n\t") \
  STS("s_memtime s[92:93]\n\t") \
  "v_mfma_f32_16x16x32_bf16 v[0:3], a[192:195], v[128:131], 0\n\t" \
  "ds_read_b128 v[160:163], v242 offset:24576\n\t" \
  "v_cvt_pk_bf16_f32 v112, v64, v65\n\t" \
  "v_cvt_pk_bf16_f32 v113, v66, v67\n\t" \
  "v_cvt_pk_bf16_f32 v114, v68, v69\n\t" \
  "v_mfma_f32_16x16x32_bf16 v[0:3], a[200:203], v[132:135], v[0:3]\n\t" \
  "ds_read_b128 a[252:255], v213 offset:35840\n\t" \
  "v_cvt_pk_bf16_f32 v115, v70, v71\n\t" \
  "v_accvgpr_read_b32 v72, a72\n\t" \
  "v_accvgpr_read_b32 v73, a73\n\t" \
  "v_mfma_f32_16x16x32_bf16 v[0:3], a[208:211], v[136:139], v[0:3]\n\t" \
  "ds_read_b128 v[164:167], v242 offset:25600\n\t" \
  "v_accvgpr_read_b32 v74, a74\n\t" \
  "v_accvgpr_read_b32 v75, a75\n\t" \
  "v_accvgpr_read_b32 v76, a76\n\t" \
  "v_mfma_f32_16x16x32_bf16 v[0:3], a[216:219], v[140:143], v[0:3]\n\t" \
  "v_accvgpr_read_b32 v77, a77\n\t" \
  "v_accvgpr_read_b32 v78, a78\n\t" \
  "v_accvgpr_read_b32 v79, a79\n\t" \
  "v_mul_f32 v72, v72, v220\n\t" \
  "v_mfma_f32_16x16x32_bf16 v[16:19], a[220:223], v[140:143], 0\n\t" \
  "v_mul_f32 v73, v73, v220\n\t" \
  "ds_read_b128 v[168:171], v242 offset:26624\n\t" \
  "ds_write_b128 v213, v[32:35] offset:32768\n\t" \
  "v_mfma_f32_16x16x32_bf16 v[16:19], a[212:215], v[136:139], v[16:19]\n\t" \
  "v_mul_f32 v74, v74, v220\n\t" \
  "v_mul_f32 v75, v75, v220\n\t" \
  "v_mul_f32 v76, v76, v220\n\t" \
  "v_mul_f32 v77, v77, v220\n\t" \
  "v_mfma_f32_16x16x32_bf16 v[16:19], a[204:207], v[132:135], v[16:19]\n\t" \
  "v_mul_f32 v78, v78, v220\n\t" \
  "ds_read_b128 v[172:175], v242 offset:27648\n\t" \
  "ds_write_b128 v213, v[36:39] offset:33792\n\t" \
  "v_mfma_f32_16x16x32_bf16 v[16:19], a[196:199], v[128:131], v[16:19]\n\t" \
  "s_waitcnt lgkmcnt(10)\n\t" \
  "v_mul_f32 v79, v79, v220\n\t" \
  "v_cvt_pk_bf16_f32 v116, v72, v73\n\t" \
  "v_cvt_pk_bf16_f32 v117, v74, v75\n\t" \
  "v_cvt_pk_bf16_f32 v118, v76, v77\n\t" \
  "v_mfma_f32_16x16x32_bf16 v[4:7], a[192:195], v[144:147], 0\n\t" \
  "v_cvt_pk_bf16_f32 v119, v78, v79\n\t" \
  "ds_read_b128 v[128:131], v242 offset:28672\n\t" \
  "ds_read_b128 v[132:135], v242 offset:29696\n\t" \
  "ds_write_b128 v213, v[40:43] offset:34816\n\t" \
  "v_mfma_f32_16x16x32_bf16 v[4:7], a[200:203], v[148:151], v[4:7]\n\t" \
  "v_accvgpr_read_b32 v64, a80\n\t" \
  "ds_read_b128 v[136:139], v242 offset:30720\n\t" \
  "ds_write_b128 v213, v[44:47] offset:35840\n\t" \
  "v_mfma_f32_16x16x32_bf16 v[4:7], a[208:211], v[152:155], v[4:7]\n\t" \
  "ds_read_b64_tr_b16 a[144:145], v236 offset:0\n\t" \
  "v_accvgpr_read_b32 v65, a81\n\t" \
  "v_accvgpr_read_b32 v66, a82\n\t" \
  "v_accvgpr_read_b32 v67, a83\n\t" \
  "v_mfma_f32_16x16x32_bf16 v[4:7], a[216:219], v[156:159], v[4:7]\n\t" \
  "v_accvgpr_read_b32 v68, a84\n\t" \
  "v_accvgpr_read_b32 v69, a85\n\t" \
  "v_accvgpr_read_b32 v70, a86\n\t" \
  "v_accvgpr_read_b32 v71, a87\n\t" \
  "v_mfma_f32_16x16x32_bf16 v[20:23], a[220:223], v[156:159], 0\n\t" \
  STS("v_writelane_b32 %[stv], s94, 22\n\t") \
  STS("v_writelane_b32 %[stv], s92, 23\n\t") \
  "buffer_load_dword v246, v245, %[ws], s65 offen sc1\n\t" \
  "v_mul_f32 v64, v64, v220\n\t" \
  "v_mul_f32 v65, v65, v220\n\t" \
  "v_mul_f32 v66, v66, v220\n\t" \
  "v_mfma_f32_16x16x32_bf16 v[20:23], a[212:215], v[152:155], v[20:23]\n\t" \
  "ds_read_b64_tr_b16 a[146:147], v236 offset:4096\n\t" \
  "v_mul_f32 v67, v67, v220\n\t" \
  "v_mul_f32 v68, v68, v220\n\t" \
  "v_mul_f32 v69, v69, v220\n\t" \
  "v_mfma_f32_16x16x32_bf16 v[20:23], a[204:207], v[148:151], v[20:23]\n\t" \
  "s_waitcnt vmcnt(10) lgkmcnt(2)\n\t" \
  "buffer_store_dwordx4 v[112:115], v242, %[ws], s98 offen offset:0 sc1\n\t" \
  "v_mul_f32 v70, v70, v220\n\t" \
  "ds_read_b128 v[140:143], v242 offset:31744\n\t" \
  "v_mul_f32 v71, v71, v220\n\t" \
  "v_mfma_f32_16x16x32_bf16 v[20:23], a[196:199], v[144:147], v[20:23]\n\t" \
  "v_cvt_pk_bf16_f32 v120, v64, v65\n\t" \
  "v_cvt_pk_bf16_f32 v121, v66, v67\n\t" \
  "s_barrier\n\t" \
  "v_mfma_f32_16x16x32_bf16 v[8:11], a[192:195], v[160:163], 0\n\t" \
  "ds_read_b128 v[144:147], v242 offset:32768\n\t" \
  "v_cvt_pk_bf16_f32 v122, v68, v69\n\t" \
  "v_cvt_pk_bf16_f32 v123, v70, v71\n\t" \
  "v_accvgpr_read_b32 v72, a88\n\t" \
  "v_mfma_f32_16x16x32_bf16 v[8:11], a[200:203], v[164:167], v[8:11]\n\t" \
  "ds_read_b64_tr_b16 a[148:149], v237 offset:0\n\t" \
  "v_accvgpr_read_b32 v73, a89\n\t" \
  "v_accvgpr_read_b32 v74, a90\n\t" \
  "v_accvgpr_read_b32 v75, a91\n\t" \
  "v_mfma_f32_16x16x32_bf16 v[8:11], a[208:211], v[168:171], v[8:11]\n\t" \
  ".Lhs10:\n\ts_cbranch_execz .Lhs10n\n\t" \
  "buffer_store_dwordx4 v[116:119], v242, %[ws], s98 offen offset:1024 sc1\n\t" \
  "ds_read_b32 v209, v215 offset:0\n\t" \
  "ds_read_b128 v[148:151], v242 offset:33792\n\t" \
  "v_accvgpr_read_b32 v76, a92\n\t" \
  "v_mfma_f32_16x16x32_bf16 v[8:11], a[216:219], v[172:175], v[8:11]\n\t" \
  "ds_read_b64_tr_b16 a[150:151], v237 offset:4096\n\t" \
  "v_accvgpr_read_b32 v77, a93\n\t" \
  "v_mfma_f32_16x16x32_bf16 v[24:27], a[220:223], v[172:175], 0\n\t" \
  "ds_read_b64_tr_b16 a[152:153], v236 offset:8192\n\t" \
  "ds_read_b128 v[152:155], v242 offset:34816\n\t" \
  "v_accvgpr_read_b32 v78, a94\n\t" \
  "v_mfma_f32_16x16x32_bf16 v[24:27], a[212:215], v[168:171], v[24:27]\n\t" \
  "ds_read_b64_tr_b16 a[154:155], v236 offset:12288\n\t" \
  "v_accvgpr_read_b32 v79, a95\n\t" \
  "v_mfma_f32_16x16x32_bf16 v[24:27], a[204:207], v[164:167], v[24:27]\n\t" \
  "ds_read_b64_tr_b16 a[156:157], v237 offset:8192\n\t" \
  "ds_read_b128 v[156:159], v242 offset:35840\n\t" \
  "v_mul_f32 v72, v72, v220\n\t" \
  "v_mfma_f32_16x16x32_bf16 v[24:27], a[196:199], v[160:163], v[24:27]\n\t" \
  "s_waitcnt lgkmcnt(10)\n\t" \
  "s_waitcnt lgkmcnt(7)\n\t" \
  "v_cmp_eq_u32 vcc, %[tag], v209\n\t" \
  "v_mfma_f32_16x16x32_bf16 v[12:15], a[192:195], v[128:131], 0\n\t" \
  "s_and_b32 vcc_lo, vcc_lo, %[pm]\n\t" \
  "s_cbranch_scc1 .Lat_rd\n\t" \
  "v_mfma_f32_16x16x32_bf16 v[12:15], a[200:203], v[132:135], v[12:15]\n\t" \
  "ds_read_b128 v[160:163], v242 offset:36864\n\t" \
  "v_mul_f32 v73, v73, v220\n\t" \
  "v_mul_f32 v74, v74, v220\n\t" \
  "v_mfma_f32_16x16x32_bf16 v[12:15], a[208:211], v[136:139], v[12:15]\n\t" \
  "ds_write_b128 v215, v[112:115] offset:0\n\t" \
  "v_mfma_f32_16x16x32_bf16 v[12:15], a[216:219], v[140:143], v[12:15]\n\t" \
  "ds_read_b64_tr_b16 a[158:159], v237 offset:12288\n\t" \
  "ds_read_b128 v[164:167], v242 offset:37888\n\t" \
  "v_mul_f32 v75, v75, v220\n\t" \
  "v_mul_f32 v76, v76, v220\n\t" \
  "v_mfma_f32_16x16x32_bf16 v[28:31], a[220:223], v[140:143], 0\n\t" \
  "ds_write_b128 v215, v[116:119] offset:1024\n\t" \
  "v_mfma_f32_16x16x32_bf16 v[28:31], a[212:215], v[136:139], v[28:31]\n\t" \
  "ds_read_b64_tr_b16 a[160:161], v238 offset:0\n\t" \
  "ds_read_b128 v[168:171], v242 offset:38912\n\t" \
  "v_mul_f32 v77, v77, v220\n\t" \
  "v_mfma_f32_16x16x32_bf16 v[28:31], a[204:207], v[132:135], v[28:31]\n\t" \
  "ds_read_b64_tr_b16 a[162:163], v238 offset:4096\n\t" \
  "ds_read_b128 v[172:175], v242 offset:39936\n\t" \
  "v_mul_f32 v78, v78, v220\n\t" \
  "v_mfma_f32_16x16x32_bf16 v[28:31], a[196:199], v[128:131], v[28:31]\n\t" \
  "s_waitcnt lgkmcnt(9)\n\t" \
  "ds_write_b128 v215, v[120:123] offset:2048\n\t" \
  STP("s_memrealtime s[94:95]\n\t") \
  STP("s_memtime s[92:93]\n\t") \
  "v_mfma_f32_16x16x32_bf16 v[32:35], a[192:195], v[144:147], 0\n\t" \
  "ds_read_b128 v[128:131], v242 offset:40960\n\t" \
  "v_mul_f32 v79, v79, v220\n\t" \
  "v_mfma_f32_16x16x32_bf16 v[32:35], a[200:203], v[148:151], v[32:35]\n\t" \
  "ds_read_b64_tr_b16 a[164:165], v239 offset:0\n\t" \
  "v_cvt_pk_bf16_f32 v124, v72, v73\n\t" \
  "v_mfma_f32_16x16x32_bf16 v[32:35], a[208:211], v[152:155], v[32:35]\n\t" \
  "ds_read_b64_tr_b16 a[166:167], v239 offset:4096\n\t" \
  "ds_read_b128 v[132:135], v242 offset:41984\n\t" \
  "v_cvt_pk_bf16_f32 v125, v74, v75\n\t" \
  "v_mfma_f32_16x16x32_bf16 v[32:35], a[216:219], v[156:159], v[32:35]\n\t" \
  "ds_read_b64_tr_b16 a[168:169], v238 offset:8192\n\t" \
  "ds_read_b128 v[136:139], v242 offset:43008\n\t" \
  "v_cvt_pk_bf16_f32 v126, v76, v77\n\t" \
  "v_mfma_f32_16x16x32_bf16 v[48:51], a[220:223], v[156:159], 0\n\t" \
  "ds_read_b64_tr_b16 a[170:171], v238 offset:12288\n\t" \
  "v_cvt_pk_bf16_f32 v127, v78, v79\n\t" \
  "v_mfma_f32_16x16x32_bf16 v[48:51], a[212:215], v[152:155], v[48:51]\n\t" \
  "ds_write_b128 v215, v[124:127] offset:3072\n\t" \
  "v_mfma_f32_16x16x32_bf16 v[48:51], a[204:207], v[148:151], v[48:51]\n\t" \
  "s_waitcnt vmcnt(3)\n\t" \
  "buffer_store_dword v243, v254, %[ws], %[fw2] offen offset:32 sc1\n\t" \
  "ds_read_b128 v[140:143], v242 offset:44032\n\t" \
  "v_mfma_f32_16x16x32_bf16 v[48:51], a[196:199], v[144:147], v[48:51]\n\t" \
  "s_waitcnt lgkmcnt(10)\n\t" \
  "ds_read_b64_tr_b16 a[172:173], v239 offset:8192\n\t" \
  "s_add_u32 s97, s64, 131072\n\t" \
  "v_mfma_f32_16x16x32_bf16 v[36:39], a[192:195], v[160:163], 0\n\t" \
  "s_and_b32 s97, s97, 0xe0000\n\t" \
  "ds_read_b128 v[144:147], v242 offset:45056\n\t" \
  "s_add_u32 s97, s97, s60\n\t" \
  "v_mfma_f32_16x16x32_bf16 v[36:39], a[200:203], v[164:167], v[36:39]\n\t" \
  "s_add_u32 m0, %[m], 49152\n\t" \
  "s_waitcnt vmcnt(3)\n\t" \
  "v_cmp_ne_u32 vcc, %[tag], v246\n\t" \
  "v_mfma_f32_16x16x32_bf16 v[36:39], a[208:211], v[168:171], v[36:39]\n\t" \
  "s_and_b32 vcc_lo, vcc_lo, 0xe\n\t" \
  "s_cbranch_scc1 .Lat_s0\n\t" \
  ".Lat_k0:\n\t" \
  "v_mfma_f32_16x16x32_bf16 v[36:39], a[216:219], v[172:175], v[36:39]\n\t" \
  "ds_read_b64_tr_b16 a[174:175], v239 offset:12288\n\t" \
  "ds_read_b128 v[148:151], v242 offset:46080\n\t" \
  "v_mfma_f32_16x16x32_bf16 v[52:55], a[220:223], v[172:175], 0\n\t" \
  STP("v_writelane_b32 %[stv], s94, 24\n\t") \
  STP("v_writelane_b32 %[stv], s92, 25\n\t") \
  "buffer_load_dwordx4 v242, %[ws], s97 offen offset:0 sc1 lds\n\t" \
  "v_mfma_f32_16x16x32_bf16 v[52:55], a[212:215], v[168:171], v[52:55]\n\t" \
  "ds_read_b64_tr_b16 a[176:177], v236 offset:16384\n\t" \
  "ds_read_b128 v[152:155], v242 offset:47104\n\t" \
  "v_mfma_f32_16x16x32_bf16 v[52:55], a[204:207], v[164:167], v[52:55]\n\t" \
  "s_waitcnt lgkmcnt(6)\n\t" \
  "ds_read_b64_tr_b16 a[178:179], v236 offset:20480\n\t" \
  "ds_read_b128 v[156:159], v242 offset:48128\n\t" \
  "v_mfma_f32_16x16x32_bf16 v[52:55], a[196:199], v[160:163], v[52:55]\n\t" \
  "s_barrier\n\t" \
  "v_mfma_f32_16x16x32_bf16 v[40:43], a[192:195], v[128:131], 0\n\t" \
  "buffer_load_dwordx4 v242, %[ws], s97 offen offset:1024 sc1 lds\n\t" \
  "v_mfma_f32_16x16x32_bf16 v[40:43], a[200:203], v[132:135], v[40:43]\n\t" \
  "ds_read_b128 v[160:163], v230 offset:32768\n\t" \
  "v_mfma_f32_16x16x32_bf16 v[40:43], a[208:211], v[136:139], v[40:43]\n\t" \
  "ds_read_b128 v[164:167], v230 offset:33792\n\t" \
  "v_mfma_f32_16x16x32_bf16 v[40:43], a[216:219], v[140:143], v[40:43]\n\t" \
  "buffer_load_dwordx4 v242, %[ws], s97 offen offset:2048 sc1 lds\n\t" \
  "v_mfma_f32_16x16x32_bf16 v[56:59], a[220:223], v[140:143], 0\n\t" \
  "ds_read_b128 v[168:171], v230 offset:34816\n\t" \
  "v_mfma_f32_16x16x32_bf16 v[56:59], a[212:215], v[136:139], v[56:59]\n\t" \
  "ds_read_b64_tr_b16 a[180:181], v237 offset:16384\n\t" \
  "v_mfma_f32_16x16x32_bf16 v[56:59], a[204:207], v[132:135], v[56:59]\n\t" \
  "buffer_load_dwordx4 v242, %[ws], s97 offen offset:3072 sc1 lds\n\t" \
  "v_mfma_f32_16x16x32_bf16 v[56:59], a[196:199], v[128:131], v[56:59]\n\t" \
  "s_waitcnt lgkmcnt(4)\n\t" \
  "s_add_u32 s97, s64, 262144\n\t" \
  "s_and_b32 s97, s97, 0xe0000\n\t" \
  "ds_read_b128 v[172:175], v230 offset:35840\n\t" \
  "s_add_u32 s97, s97, s60\n\t" \
  "v_mfma_f32_16x16x32_bf16 v[44:47], a[192:195], v[144:147], 0\n\t" \
  "s_add_u32 m0, %[m], 65536\n\t" \
  "ds_read_b64_tr_b16 a[182:183], v237 offset:20480\n\t" \
  "ds_read_b128 v[128:131], v230 offset:36864\n\t" \
  "ds_read_b128 v[132:135], v230 offset:37888\n\t" \
  "v_mfma_f32_16x16x32_bf16 v[44:47], a[200:203], v[148:151], v[44:47]\n\t" \
  "buffer_load_dwordx4 v242, %[ws], s97 offen offset:0 sc1 lds\n\t" \
  "v_mfma_f32_16x16x32_bf16 v[44:47], a[208:211], v[152:155], v[44:47]\n\t" \
  "ds_read_b64_tr_b16 a[184:185], v236 offset:24576\n\t" \
  "ds_read_b128 v[136:139], v230 offset:38912\n\t" \
  "v_mfma_f32_16x16x32_bf16 v[44:47], a[216:219], v[156:159], v[44:47]\n\t" \
  "ds_read_b64_tr_b16 a[186:187], v236 offset:28672\n\t" \
  "v_mfma_f32_16x16x32_bf16 v[60:63], a[220:223], v[156:159], 0\n\t" \
  "buffer_load_dwordx4 v242, %[ws], s97 offen offset:1024 sc1 lds\n\t" \
  "v_mfma_f32_16x16x32_bf16 v[60:63], a[212:215], v[152:155], v[60:63]\n\t" \
  "ds_read_b128 v[140:143], v230 offset:39936\n\t" \
  "v_mfma_f32_16x16x32_bf16 v[60:63], a[204:207], v[148:151], v[60:63]\n\t" \
  "ds_read_b64_tr_b16 a[188:189], v237 offset:24576\n\t" \
  "v_mfma_f32_16x16x32_bf16 v[60:63], a[196:199], v[144:147], v[60:63]\n\t" \
  "s_waitcnt lgkmcnt(8)\n\t" \
  "buffer_load_dwordx4 v242, %[ws], s97 offen offset:2048 sc1 lds\n\t" \
  STP("s_memrealtime s[94:95]\n\t") \
  STP("s_memtime s[92:93]\n\t") \
  "v_mfma_f32_16x16x32_bf16 v[64:67], a[192:195], v[160:163], 0\n\t" \
  "ds_read_b128 v[144:147], v230 offset:40960\n\t" \
  "v_mfma_f32_16x16x32_bf16 v[64:67], a[200:203], v[164:167], v[64:67]\n\t" \
  "ds_read_b64_tr_b16 a[190:191], v237 offset:28672\n\t" \
  "v_mfma_f32_16x16x32_bf16 v[64:67], a[208:211], v[168:171], v[64:67]\n\t" \
  "buffer_load_dwordx4 v242, %[ws], s97 offen offset:3072 sc1 lds\n\t" \
  "v_mfma_f32_16x16x32_bf16 v[64:67], a[216:219], v[172:175], v[64:67]\n\t" \
  "s_add_u32 s97, s64, 393216\n\t" \
  "s_and_b32 s97, s97, 0xe0000\n\t" \
  "ds_read_b128 v[148:151], v230 offset:41984\n\t" \
  "s_add_u32 s97, s97, s60\n\t" \
  "v_mfma_f32_16x16x32_bf16 v[80:83], a[220:223], v[172:175], 0\n\t" \
  "s_add_u32 m0, %[m], 16384\n\t" \
  "ds_read_b128 v[152:155], v230 offset:43008\n\t" \
  "v_mfma_f32_16x16x32_bf16 v[80:83], a[212:215], v[168:171], v[80:83]\n\t" \
  "buffer_load_dwordx4 v242, %[ws], s97 offen offset:0 sc1 lds\n\t" \
  "v_mfma_f32_16x16x32_bf16 v[80:83], a[204:207], v[164:167], v[80:83]\n\t" \
  "ds_read_b128 v[156:159], v230 offset:44032\n\t" \
  "v_mfma_f32_16x16x32_bf16 v[80:83], a[196:199], v[160:163], v[80:83]\n\t" \
  "s_waitcnt lgkmcnt(6)\n\t" \
  "ds_read_b64_tr_b16 a[0:1], v236 offset:49152\n\t" \
  "v_mfma_f32_16x16x32_bf16 v[68:71], a[192:195], v[128:131], 0\n\t" \
  "s_mov_b64 exec, 0\n\t" \
  "s_cmp_eq_u32 %[wid], 0\n\t" \
  "s_cbranch_scc1 .Lhc10\n\t" \
  "s_cmp_eq_u32 %[wid], 1\n\t" \
  "s_cbranch_scc1 .Lhc11\n\t" \
  "s_cmp_eq_u32 %[wid], 2\n\t" \
  "s_cbranch_scc1 .Lhc12\n\t" \
  "s_branch .Lhc13\n\t" \
  ".Lhr1:\n\t" \
  "s_mov_b64 exec, -1\n\t" \
  "v_mfma_f32_16x16x32_bf16 v[68:71], a[200:203], v[132:135], v[68:71]\n\t" \
  "buffer_load_dwordx4 v242, %[ws], s97 offen offset:1024 sc1 lds\n\t" \
  "v_mfma_f32_16x16x32_bf16 v[68:71], a[208:211], v[136:139], v[68:71]\n\t" \
  "ds_read_b64_tr_b16 a[2:3], v236 offset:53248\n\t" \
  "ds_read_b128 v[160:163], v230 offset:45056\n\t" \
  "v_mfma_f32_16x16x32_bf16 v[68:71], a[216:219], v[140:143], v[68:71]\n\t" \
  "buffer_load_dwordx4 v242, %[ws], s97 offen offset:2048 sc1 lds\n\t" \
  "v_mfma_f32_16x16x32_bf16 v[84:87], a[220:223], v[140:143], 0\n\t" \
  STP("v_writelane_b32 %[stv], s94, 26\n\t") \
  STP("v_writelane_b32 %[stv], s92, 27\n\t") \
  "ds_read_b128 v[164:167], v230 offset:46080\n\t" \
  "v_mfma_f32_16x16x32_bf16 v[84:87], a[212:215], v[136:139], v[84:87]\n\t" \
  "ds_read_b128 v[168:171], v230 offset:47104\n\t" \
  "v_mfma_f32_16x16x32_bf16 v[84:87], a[204:207], v[132:135], v[84:87]\n\t" \
  "s_waitcnt vmcnt(7) lgkmcnt(5)\n\t" \
  "buffer_load_dwordx4 v242, %[ws], s97 offen offset:3072 sc1 lds\n\t" \
  "v_mfma_f32_16x16x32_bf16 v[84:87], a[196:199], v[128:131], v[84:87]\n\t" \
  "s_barrier\n\t" \
  "v_mfma_f32_16x16x32_bf16 v[72:75], a[192:195], v[144:147], 0\n\t" \
  "ds_read_b128 v[172:175], v230 offset:48128\n\t" \
  "v_mfma_f32_16x16x32_bf16 v[72:75], a[200:203], v[148:151], v[72:75]\n\t" \
  "ds_read_b128 v[128:131], v242 offset:49152\n\t" \
  "v_mfma_f32_16x16x32_bf16 v[72:75], a[208:211], v[152:155], v[72:75]\n\t" \
  "ds_read_b64_tr_b16 a[4:5], v237 offset:49152\n\t" \
  "v_mfma_f32_16x16x32_bf16 v[72:75], a[216:219], v[156:159], v[72:75]\n\t" \
  "ds_read_b64_tr_b16 a[6:7], v237 offset:53248\n\t" \
  "ds_read_b128 v[132:135], v242 offset:50176\n\t" \
  "v_mfma_f32_16x16x32_bf16 v[88:91], a[220:223], v[156:159], 0\n\t" \
  "s_add_u32 s96, %[hl18], 1048576\n\t" \
  "ds_read_b64_tr_b16 a[8:9], v236 offset:57344\n\t" \
  "s_and_b32 s96, s96, 0x1c0000\n\t" \
  "v_mfma_f32_16x16x32_bf16 v[88:91], a[212:215], v[152:155], v[88:91]\n\t" \
  "s_add_u32 s96, s96, s77\n\t" \
  "ds_read_b128 v[136:139], v242 offset:51200\n\t" \
  "s_add_u32 m0, %[m], 81920\n\t" \
  "v_mfma_f32_16x16x32_bf16 v[88:91], a[204:207], v[148:151], v[88:91]\n\t" \
  "ds_read_b128 v[140:143], v242 offset:52224\n\t" \
  "v_mfma_f32_16x16x32_bf16 v[88:91], a[196:199], v[144:147], v[88:91]\n\t" \
  "s_waitcnt lgkmcnt(7)\n\t" \
  "ds_read_b64_tr_b16 a[10:11], v236 offset:61440\n\t" \
  "v_mfma_f32_16x16x32_bf16 v[76:79], a[192:195], v[160:163], 0\n\t" \
  "buffer_load_dword v246, v245, %[ws], s65 offen sc1\n\t" \
  "v_mfma_f32_16x16x32_bf16 v[76:79], a[200:203], v[164:167], v[76:79]\n\t" \
  "ds_read_b64_tr_b16 a[12:13], v237 offset:57344\n\t" \
  "ds_read_b128 v[144:147], v242 offset:53248\n\t" \
  "v_mfma_f32_16x16x32_bf16 v[76:79], a[208:211], v[168:171], v[76:79]\n\t" \
  "buffer_load_dwordx4 v240, %[wo], s96 offen offset:0 lds\n\t" \
  "v_mfma_f32_16x16x32_bf16 v[76:79], a[216:219], v[172:175], v[76:79]\n\t" \
  "ds_read_b64_tr_b16 a[14:15], v237 offset:61440\n\t" \
  "ds_read_b128 v[148:151], v242 offset:54272\n\t" \
  "v_mfma_f32_16x16x32_bf16 v[92:95], a[220:223], v[172:175], 0\n\t" \
  "ds_read_b128 v[152:155], v242 offset:55296\n\t" \
  "v_mfma_f32_16x16x32_bf16 v[92:95], a[212:215], v[168:171], v[92:95]\n\t" \
  "buffer_load_dwordx4 v241, %[wo], s96 offen offset:1024 lds\n\t" \
  "v_mfma_f32_16x16x32_bf16 v[92:95], a[204:207], v[164:167], v[92:95]\n\t" \
  "ds_read_b128 v[156:159], v242 offset:56320\n\t" \
  "v_mfma_f32_16x16x32_bf16 v[92:95], a[196:199], v[160:163], v[92:95]\n\t" \
  "s_waitcnt lgkmcnt(7)\n\t" \
  "ds_read_b64_tr_b16 a[16:17], v238 offset:16384\n\t" \
  STP("s_memrealtime s[94:95]\n\t") \
  STP("s_memtime s[92:93]\n\t") \
  "v_mfma_f32_16x16x32_bf16 v[0:3], a[128:131], v[128:131], v[0:3]\n\t" \
  "buffer_load_dwordx4 v248, %[wo], s96 offen offset:2048 lds\n\t" \
  "v_mfma_f32_16x16x32_bf16 v[0:3], a[136:139], v[132:135], v[0:3]\n\t" \
  "ds_read_b64_tr_b16 a[18:19], v238 offset:20480\n\t" \
  "ds_read_b128 v[160:163], v242 offset:57344\n\t" \
  "v_accvgpr_read_b32 v96, a96\n\t" \
  "v_mfma_f32_16x16x32_bf16 v[0:3], a[144:147], v[136:139], v[0:3]\n\t" \
  "ds_read_b64_tr_b16 a[20:21], v239 offset:16384\n\t" \
  "ds_read_b128 v[164:167], v242 offset:58368\n\t" \
  "v_accvgpr_read_b32 v97, a97\n\t" \
  "v_mfma_f32_16x16x32_bf16 v[0:3], a[152:155], v[140:143], v[0:3]\n\t" \
  "buffer_load_dwordx4 v249, %[wo], s96 offen offset:3072 lds\n\t" \
  "v_mfma_f32_16x16x32_bf16 v[16:19], a[156:159], v[140:143], v[16:19]\n\t" \
  "s_add_u32 s96, %[hl18], 1310720\n\t" \
  "ds_read_b128 v[168:171], v242 offset:59392\n\t" \
  "v_accvgpr_read_b32 v98, a98\n\t" \
  "v_mfma_f32_16x16x32_bf16 v[16:19], a[148:151], v[136:139], v[16:19]\n\t" \
  "s_and_b32 s96, s96, 0x1c0000\n\t" \
  "ds_read_b64_tr_b16 a[22:23], v239 offset:20480\n\t" \
  "v_accvgpr_read_b32 v99, a99\n\t" \
  "v_mfma_f32_16x16x32_bf16 v[16:19], a[140:143], v[132:135], v[16:19]\n\t" \
  "buffer_store_dwordx4 v[120:123], v242, %[ws], s98 offen offset:2048 sc1\n\t" \
  "ds_read_b64_tr_b16 a[24:25], v238 offset:24576\n\t" \
  "ds_read_b64_tr_b16 a[26:27], v238 offset:28672\n\t" \
  "ds_read_b128 v[172:175], v242 offset:60416\n\t" \
  "v_mfma_f32_16x16x32_bf16 v[16:19], a[132:135], v[128:131], v[16:19]\n\t" \
  "s_waitcnt lgkmcnt(10)\n\t" \
  "ds_read_b64_tr_b16 a[28:29], v239 offset:24576\n\t" \
  "v_accvgpr_read_b32 v100, a100\n\t" \
  "v_mfma_f32_16x16x32_bf16 v[4:7], a[128:131], v[144:147], v[4:7]\n\t" \
  "s_add_u32 s96, s96, %[wob]\n\t" \
  "ds_read_b128 v[128:131], v242 offset:61440\n\t" \
  "s_add_u32 m0, %[m], 131072\n\t" \
  "v_mfma_f32_16x16x32_bf16 v[4:7], a[136:139], v[148:151], v[4:7]\n\t" \
  "ds_read_b64_tr_b16 a[30:31], v239 offset:28672\n\t" \
  "v_accvgpr_read_b32 v101, a101\n\t" \
  "v_mfma_f32_16x16x32_bf16 v[4:7], a[144:147], v[152:155], v[4:7]\n\t" \
  "buffer_load_dwordx4 v240, %[wo], s96 offen offset:0 lds\n\t" \
  "v_mfma_f32_16x16x32_bf16 v[4:7], a[152:155], v[156:159], v[4:7]\n\t" \
  "ds_read_b128 v[132:135], v242 offset:62464\n\t" \
  "v_accvgpr_read_b32 v102, a102\n\t" \
  "v_mfma_f32_16x16x32_bf16 v[20:23], a[156:159], v[156:159], v[20:23]\n\t" \
  STP("v_writelane_b32 %[stv], s94, 28\n\t") \
  STP("v_writelane_b32 %[stv], s92, 29\n\t") \
  "ds_read_b128 v[136:139], v242 offset:63488\n\t" \
  "v_accvgpr_read_b32 v103, a103\n\t" \
  "v_mfma_f32_16x16x32_bf16 v[20:23], a[148:151], v[152:155], v[20:23]\n\t" \
  "buffer_load_dwordx4 v241, %[wo], s96 offen offset:1024 lds\n\t" \
  "v_mfma_f32_16x16x32_bf16 v[20:23], a[140:143], v[148:151], v[20:23]\n\t" \
  "s_waitcnt vmcnt(12) lgkmcnt(5)\n\t" \
  "ds_read_b128 v[140:143], v242 offset:64512\n\t" \
  "v_mul_f32 v96, v96, v222\n\t" \
  "v_mfma_f32_16x16x32_bf16 v[20:23], a[132:135], v[144:147], v[20:23]\n\t" \
  "s_barrier\n\t" \
  "v_mfma_f32_16x16x32_bf16 v[8:11], a[128:131], v[160:163], v[8:11]\n\t" \
  "buffer_load_dwordx4 v248, %[wo], s96 offen offset:2048 lds\n\t" \
  "v_mfma_f32_16x16x32_bf16 v[8:11], a[136:139], v[164:167], v[8:11]\n\t" \
  "ds_read_b128 v[144:147], v230 offset:0\n\t" \
  "v_mul_f32 v97, v97, v222\n\t" \
  "v_mfma_f32_16x16x32_bf16 v[8:11], a[144:147], v[168:171], v[8:11]\n\t" \
  "ds_read_b64_tr_b16 a[32:33], v234 offset:0\n\t" \
  "v_mul_f32 v98, v98, v222\n\t" \
  "v_mfma_f32_16x16x32_bf16 v[8:11], a[152:155], v[172:175], v[8:11]\n\t" \
  "ds_read_b128 v[148:151], v230 offset:1024\n\t" \
  "v_mul_f32 v99, v99, v222\n\t" \
  "v_mfma_f32_16x16x32_bf16 v[24:27], a[156:159], v[172:175], v[24:27]\n\t" \
  "buffer_load_dwordx4 v249, %[wo], s96 offen offset:3072 lds\n\t" \
  "v_mfma_f32_16x16x32_bf16 v[24:27], a[148:151], v[168:171], v[24:27]\n\t" \
  "ds_read_b128 v[152:155], v230 offset:2048\n\t" \
  "v_mul_f32 v100, v100, v222\n\t" \
  "v_mfma_f32_16x16x32_bf16 v[24:27], a[140:143], v[164:167], v[24:27]\n\t" \
  "s_add_u32 s97, s64, 131072\n\t" \
  "ds_read_b128 v[156:159], v230 offset:3072\n\t" \
  "v_mul_f32 v101, v101, v222\n\t" \
  "v_mfma_f32_16x16x32_bf16 v[24:27], a[132:135], v[160:163], v[24:27]\n\t" \
  "s_waitcnt lgkmcnt(5)\n\t" \
  "ds_read_b64_tr_b16 a[34:35], v234 offset:4096\n\t" \
  "v_mul_f32 v102, v102, v222\n\t" \
  "v_mfma_f32_16x16x32_bf16 v[12:15], a[128:131], v[128:131], v[12:15]\n\t" \
  "s_and_b32 s97, s97, 0xe0000\n\t" \
  "ds_read_b128 v[160:163], v230 offset:4096\n\t" \
  "v_mul_f32 v103, v103, v222\n\t" \
  "v_mfma_f32_16x16x32_bf16 v[12:15], a[136:139], v[132:135], v[12:15]\n\t" \
  "s_waitcnt vmcnt(9)\n\t" \
  "v_cmp_ne_u32 vcc, %[tag], v246\n\t" \
  "v_cvt_pk_bf16_f32 v96, v96, v97\n\t" \
  "v_mfma_f32_16x16x32_bf16 v[12:15], a[144:147], v[136:139], v[12:15]\n\t" \
  "s_add_u32 s97, s97, s61\n\t" \
  "ds_read_b64_tr_b16 a[36:37], v235 offset:0\n\t" \
  "s_add_u32 m0, %[m], 98304\n\t" \
  "v_mfma_f32_16x16x32_bf16 v[12:15], a[152:155], v[140:143], v[12:15]\n\t" \
  "s_and_b32 vcc_lo, vcc_lo, 0xfefe\n\t" \
  "s_cbranch_scc1 .Lat_s1\n\t" \
  ".Lat_k1:\n\t" \
  "ds_read_b128 v[164:167], v230 offset:5120\n\t" \
  "v_cvt_pk_bf16_f32 v97, v98, v99\n\t" \
  "v_mfma_f32_16x16x32_bf16 v[28:31], a[156:159], v[140:143], v[28:31]\n\t" \
  "buffer_load_dwordx4 v242, %[ws], s97 offen offset:0 sc1 lds\n\t" \
  "v_mfma_f32_16x16x32_bf16 v[28:31], a[148:151], v[136:139], v[28:31]\n\t" \
  "ds_read_b128 v[168:171], v230 offset:6144\n\t" \
  "v_cvt_pk_bf16_f32 v98, v100, v101\n\t" \
  "v_mfma_f32_16x16x32_bf16 v[28:31], a[140:143], v[132:135], v[28:31]\n\t" \
  "ds_read_b64_tr_b16 a[38:39], v235 offset:4096\n\t" \
  "v_cvt_pk_bf16_f32 v99, v102, v103\n\t" \
  "v_mfma_f32_16x16x32_bf16 v[28:31], a[132:135], v[128:131], v[28:31]\n\t" \
  "s_waitcnt lgkmcnt(6)\n\t" \
  "buffer_load_dwordx4 v242, %[ws], s97 offen offset:1024 sc1 lds\n\t" \
  STP("s_memrealtime s[94:95]\n\t") \
  STP("s_memtime s[92:93]\n\t") \
  "v_mfma_f32_16x16x32_bf16 v[0:3], a[160:163], v[144:147], v[0:3]\n\t" \
  "ds_write_b128 v213, v[96:99] offset:32768\n\t" \
  "v_mfma_f32_16x16x32_bf16 v[0:3], a[168:171], v[148:151], v[0:3]\n\t" \
  "ds_read_b64_tr_b16 a[40:41], v234 offset:8192\n\t" \
  "ds_read_b128 v[172:175], v230 offset:7168\n\t" \
  "v_accvgpr_read_b32 v104, a104\n\t" \
  "v_mfma_f32_16x16x32_bf16 v[0:3], a[176:179], v[152:155], v[0:3]\n\t" \
  "ds_read_b64_tr_b16 a[42:43], v234 offset:12288\n\t" \
  "ds_read_b128 v[128:131], v230 offset:8192\n\t" \
  "v_accvgpr_read_b32 v105, a105\n\t" \
  "v_mfma_f32_16x16x32_bf16 v[0:3], a[184:187], v[156:159], v[0:3]\n\t" \
  "ds_read_b128 v[132:135], v230 offset:9216\n\t" \
  "v_accvgpr_read_b32 v106, a106\n\t" \
  "v_mfma_f32_16x16x32_bf16 v[16:19], a[188:191], v[156:159], v[16:19]\n\t" \
  "ds_read_b64_tr_b16 a[44:45], v235 offset:8192\n\t" \
  "v_accvgpr_read_b32 v107, a107\n\t" \
  "v_mfma_f32_16x16x32_bf16 v[16:19], a[180:183], v[152:155], v[16:19]\n\t" \
  "ds_read_b128 v[136:139], v230 offset:10240\n\t" \
  "v_accvgpr_read_b32 v108, a108\n\t" \
  "v_mfma_f32_16x16x32_bf16 v[16:19], a[172:175], v[148:151], v[16:19]\n\t" \
  "ds_read_b64_tr_b16 a[46:47], v235 offset:12288\n\t" \
  "v_accvgpr_read_b32 v109, a109\n\t" \
  "v_mfma_f32_16x16x32_bf16 v[16:19], a[164:167], v[144:147], v[16:19]\n\t" \
  "s_waitcnt lgkmcnt(6)\n\t" \
  "ds_read_b128 v[140:143], v230 offset:11264\n\t" \
  "v_accvgpr_read_b32 v110, a110\n\t" \
  "v_mfma_f32_16x16x32_bf16 v[4:7], a[160:163], v[160:163], v[4:7]\n\t" \
  "buffer_load_dwordx4 v242, %[ws], s97 offen offset:2048 sc1 lds\n\t" \
  "v_mfma_f32_16x16x32_bf16 v[4:7], a[168:171], v[164:167], v[4:7]\n\t" \
  "ds_read_b128 v[144:147], v230 offset:12288\n\t" \
  "v_accvgpr_read_b32 v111, a111\n\t" \
  "v_mfma_f32_16x16x32_bf16 v[4:7], a[176:179], v[168:171], v[4:7]\n\t" \
  "ds_read_b128 v[148:151], v230 offset:13312\n\t" \
  "v_mul_f32 v104, v104, v222\n\t" \
  "v_mfma_f32_16x16x32_bf16 v[4:7], a[184:187], v[172:175], v[4:7]\n\t" \
  "buffer_load_dwordx4 v242, %[ws], s97 offen offset:3072 sc1 lds\n\t" \
  "v_mfma_f32_16x16x32_bf16 v[20:23], a[188:191], v[172:175], v[20:23]\n\t" \
  STP("v_writelane_b32 %[stv], s94, 30\n\t") \
  STP("v_writelane_b32 %[stv], s92, 31\n\t") \
  "ds_read_b128 v[152:155], v230 offset:14336\n\t" \
  "v_mul_f32 v105, v105, v222\n\t" \
  "v_mfma_f32_16x16x32_bf16 v[20:23], a[180:183], v[168:171], v[20:23]\n\t" \
  "v_mul_f32 v106, v106, v222\n\t" \
  "v_mul_f32 v107, v107, v222\n\t" \
  "v_mfma_f32_16x16x32_bf16 v[20:23], a[172:175], v[164:167], v[20:23]\n\t" \
  "s_waitcnt lgkmcnt(3)\n\t" \
  "ds_read_b128 v[156:159], v230 offset:15360\n\t" \
  "v_mul_f32 v108, v108, v222\n\t" \
  "v_mfma_f32_16x16x32_bf16 v[20:23], a[164:167], v[160:163], v[20:23]\n\t" \
  "s_barrier\n\t" \
  "v_mfma_f32_16x16x32_bf16 v[8:11], a[160:163], v[128:131], v[8:11]\n\t" \
  "buffer_store_dwordx4 v[124:127], v242, %[ws], s98 offen offset:3072 sc1\n\t" \
  "v_mul_f32 v109, v109, v222\n\t" \
  "ds_read_b128 v[160:163], v242 offset:16384\n\t" \
  "v_mul_f32 v110, v110, v222\n\t" \
  "v_mfma_f32_16x16x32_bf16 v[8:11], a[168:171], v[132:135], v[8:11]\n\t" \
  "s_xor_b32 s98, %[eso], 98304\n\t" \
  "v_mul_f32 v111, v111, v222\n\t" \
  "v_cvt_pk_bf16_f32 v104, v104, v105\n\t" \
  "v_mfma_f32_16x16x32_bf16 v[8:11], a[176:179], v[136:139], v[8:11]\n\t" \
  "s_add_u32 s97, s64, 524288\n\t" \
  "ds_read_b128 v[164:167], v242 offset:17408\n\t" \
  "v_cvt_pk_bf16_f32 v105, v106, v107\n\t" \
  "v_mfma_f32_16x16x32_bf16 v[8:11], a[184:187], v[140:143], v[8:11]\n\t" \
  "s_and_b32 s97, s97, 0xe0000\n\t" \
  "v_cvt_pk_bf16_f32 v106, v108, v109\n\t" \
  "v_cvt_pk_bf16_f32 v107, v110, v111\n\t" \
  "v_mfma_f32_16x16x32_bf16 v[24:27], a[188:191], v[140:143], v[24:27]\n\t" \
  "s_add_u32 s97, s97, s60\n\t" \
  "ds_read_b128 v[168:171], v242 offset:18432\n\t" \
  "s_add_u32 m0, %[m], 0\n\t" \
  "v_mfma_f32_16x16x32_bf16 v[24:27], a[180:183], v[136:139], v[24:27]\n\t" \
  "ds_write_b128 v213, v[104:107] offset:33792\n\t" \
  "v_mfma_f32_16x16x32_bf16 v[24:27], a[172:175], v[132:135], v[24:27]\n\t" \
  "buffer_load_dwordx4 v242, %[ws], s97 offen offset:0 sc1 lds\n\t" \
  "v_mfma_f32_16x16x32_bf16 v[24:27], a[164:167], v[128:131], v[24:27]\n\t" \
  "s_waitcnt lgkmcnt(4)\n\t" \
  "ds_read_b128 v[172:175], v242 offset:19456\n\t" \
  "v_mfma_f32_16x16x32_bf16 v[12:15], a[160:163], v[144:147], v[12:15]\n\t" \
  "ds_read_b128 v[128:131], v242 offset:20480\n\t" \
  "v_mfma_f32_16x16x32_bf16 v[12:15], a[168:171], v[148:151], v[12:15]\n\t" \
  "buffer_load_dwordx4 v242, %[ws], s97 offen offset:1024 sc1 lds\n\t" \
  "v_mfma_f32_16x16x32_bf16 v[12:15], a[176:179], v[152:155], v[12:15]\n\t" \
  "ds_read_b128 v[132:135], v242 offset:21504\n\t" \
  "v_mfma_f32_16x16x32_bf16 v[12:15], a[184:187], v[156:159], v[12:15]\n\t" \
  "ds_read_b128 v[136:139], v242 offset:22528\n\t" \
  "v_mfma_f32_16x16x32_bf16 v[28:31], a[188:191], v[156:159], v[28:31]\n\t" \
  "ds_read_b128 v[140:143], v242 offset:23552\n\t" \
  "v_mfma_f32_16x16x32_bf16 v[28:31], a[180:183], v[152:155], v[28:31]\n\t" \
  "v_mfma_f32_16x16x32_bf16 v[28:31], a[172:175], v[148:151], v[28:31]\n\t" \
  "v_mfma_f32_16x16x32_bf16 v[28:31], a[164:167], v[144:147], v[28:31]\n\t" \
  "s_waitcnt lgkmcnt(4)\n\t" \
  "v_mfma_f32_16x16x32_bf16 v[0:3], a[0:3], v[160:163], v[0:3]\n\t" \
  "ds_read_b128 v[144:147], v242 offset:24576\n\t" \
  "v_mfma_f32_16x16x32_bf16 v[0:3], a[8:11], v[164:167], v[0:3]\n\t" \
  "ds_read_b128 v[148:151], v242 offset:25600\n\t" \
  "v_mfma_f32_16x16x32_bf16 v[0:3], a[16:19], v[168:171], v[0:3]\n\t" \
  "buffer_load_dwordx4 v242, %[ws], s97 offen offset:2048 sc1 lds\n\t" \
  "v_mfma_f32_16x16x32_bf16 v[0:3], a[24:27], v[172:175], v[0:3]\n\t" \
  "ds_read_b128 v[152:155], v242 offset:26624\n\t" \
  "v_mfma_f32_16x16x32_bf16 v[16:19], a[28:31], v[172:175], v[16:19]\n\t" \
  "ds_read_b128 v[156:159], v242 offset:27648\n\t" \
  "v_mfma_f32_16x16x32_bf16 v[16:19], a[20:23], v[168:171], v[16:19]\n\t" \
  "buffer_load_dwordx4 v242, %[ws], s97 offen offset:3072 sc1 lds\n\t" \
  "v_mfma_f32_16x16x32_bf16 v[16:19], a[12:15], v[164:167], v[16:19]\n\t" \
  "v_mfma_f32_16x16x32_bf16 v[16:19], a[4:7], v[160:163], v[16:19]\n\t" \
  "s_waitcnt lgkmcnt(4)\n\t" \
  "v_mfma_f32_16x16x32_bf16 v[4:7], a[0:3], v[128:131], v[4:7]\n\t" \
  "buffer_store_dwordx4 v[96:99], v242, %[ws], s98 offen offset:0 sc1\n\t" \
  "ds_read_b128 v[160:163], v242 offset:28672\n\t" \
  "ds_read_b128 v[164:167], v242 offset:29696\n\t" \
  "ds_read_b128 v[168:171], v242 offset:30720\n\t" \
  "v_mfma_f32_16x16x32_bf16 v[4:7], a[8:11], v[132:135], v[4:7]\n\t" \
  "s_add_u32 s97, s64, 262144\n\t" \
  "v_accvgpr_read_b32 v96, a112\n\t" \
  "v_accvgpr_read_b32 v97, a113\n\t" \
  "v_mfma_f32_16x16x32_bf16 v[4:7], a[16:19], v[136:139], v[4:7]\n\t" \
  "s_and_b32 s97, s97, 0xe0000\n\t" \
  "v_accvgpr_read_b32 v98, a114\n\t" \
  "v_accvgpr_read_b32 v99, a115\n\t" \
  "v_mfma_f32_16x16x32_bf16 v[4:7], a[24:27], v[140:143], v[4:7]\n\t" \
  "s_add_u32 s97, s97, s61\n\t" \
  "ds_read_b128 v[172:175], v242 offset:31744\n\t" \
  "v_accvgpr_read_b32 v100, a116\n\t" \
  "v_mfma_f32_16x16x32_bf16 v[20:23], a[28:31], v[140:143], v[20:23]\n\t" \
  "s_add_u32 m0, %[m], 147456\n\t" \
  "v_accvgpr_read_b32 v101, a117\n\t" \
  "v_accvgpr_read_b32 v102, a118\n\t" \
  "v_mfma_f32_16x16x32_bf16 v[20:23], a[20:23], v[136:139], v[20:23]\n\t" \
  "v_accvgpr_read_b32 v103, a119\n\t" \
  "v_mul_f32 v96, v96, v222\n\t" \
  "v_mfma_f32_16x16x32_bf16 v[20:23], a[12:15], v[132:135], v[20:23]\n\t" \
  "s_waitcnt vmcnt(6) lgkmcnt(4)\n\t" \
  "buffer_load_dwordx4 v242, %[ws], s97 offen offset:0 sc1 lds\n\t" \
  "v_mfma_f32_16x16x32_bf16 v[20:23], a[4:7], v[128:131], v[20:23]\n\t" \
  "s_barrier\n\t" \
  "v_mfma_f32_16x16x32_bf16 v[8:11], a[0:3], v[144:147], v[8:11]\n\t" \
  "ds_read_b64_tr_b16 a[48:49], v236 offset:16384\n\t" \
  "ds_read_b128 v[128:131], v230 offset:32768\n\t" \
  "v_mul_f32 v97, v97, v222\n\t" \
  "v_mfma_f32_16x16x32_bf16 v[8:11], a[8:11], v[148:151], v[8:11]\n\t" \
  "buffer_load_dwordx4 v242, %[ws], s97 offen offset:1024 sc1 lds\n\t" \
  "v_mfma_f32_16x16x32_bf16 v[8:11], a[16:19], v[152:155], v[8:11]\n\t" \
  "ds_read_b128 v[132:135], v230 offset:33792\n\t" \
  "v_mul_f32 v98, v98, v222\n\t" \
  "v_mfma_f32_16x16x32_bf16 v[8:11], a[24:27], v[156:159], v[8:11]\n\t" \
  "ds_read_b64_tr_b16 a[50:51], v236 offset:20480\n\t" \
  "ds_read_b128 v[136:139], v230 offset:34816\n\t" \
  "v_mul_f32 v99, v99, v222\n\t" \
  "v_mfma_f32_16x16x32_bf16 v[24:27], a[28:31], v[156:159], v[24:27]\n\t" \
  "buffer_load_dwordx4 v242, %[ws], s97 offen offset:2048 sc1 lds\n\t" \
  "v_mfma_f32_16x16x32_bf16 v[24:27], a[20:23], v[152:155], v[24:27]\n\t" \
  "ds_read_b128 v[140:143], v230 offset:35840\n\t" \
  "v_mul_f32 v100, v100, v222\n\t" \
  "v_mfma_f32_16x16x32_bf16 v[24:27], a[12:15], v[148:151], v[24:27]\n\t" \
  "ds_read_b64_tr_b16 a[52:53], v237 offset:16384\n\t" \
  "v_mul_f32 v101, v101, v222\n\t" \
  "v_mfma_f32_16x16x32_bf16 v[24:27], a[4:7], v[144:147], v[24:27]\n\t" \
  "s_waitcnt lgkmcnt(7)\n\t" \
  "buffer_load_dwordx4 v242, %[ws], s97 offen offset:3072 sc1 lds\n\t" \
  "v_mfma_f32_16x16x32_bf16 v[12:15], a[0:3], v[160:163], v[12:15]\n\t" \
  "s_add_u32 s96, %[hl18], 1310720\n\t" \
  "ds_read_b128 v[144:147], v230 offset:36864\n\t" \
  "v_mul_f32 v102, v102, v222\n\t" \
  "v_mfma_f32_16x16x32_bf16 v[12:15], a[8:11], v[164:167], v[12:15]\n\t" \
  "s_and_b32 s96, s96, 0x1c0000\n\t" \
  "ds_read_b128 v[148:151], v230 offset:37888\n\t" \
  "v_mul_f32 v103, v103, v222\n\t" \
  "v_mfma_f32_16x16x32_bf16 v[12:15], a[16:19], v[168:171], v[12:15]\n\t" \
  "buffer_store_dwordx4 v[104:107], v242, %[ws], s98 offen offset:1024 sc1\n\t" \
  "ds_read_b64_tr_b16 a[54:55], v237 offset:20480\n\t" \
  "ds_read_b128 v[152:155], v230 offset:38912\n\t" \
  "v_cvt_pk_bf16_f32 v96, v96, v97\n\t" \
  "v_mfma_f32_16x16x32_bf16 v[12:15], a[24:27], v[172:175], v[12:15]\n\t" \
  "s_add_u32 s96, s96, s77\n\t" \
  "ds_read_b128 v[156:159], v230 offset:39936\n\t" \
  "v_cvt_pk_bf16_f32 v97, v98, v99\n\t" \
  "v_mfma_f32_16x16x32_bf16 v[28:31], a[28:31], v[172:175], v[28:31]\n\t" \
  "s_add_u32 m0, %[m], 65536\n\t" \
  "ds_read_b64_tr_b16 a[56:57], v236 offset:24576\n\t" \
  "v_cvt_pk_bf16_f32 v98, v100, v101\n\t" \
  "v_mfma_f32_16x16x32_bf16 v[28:31], a[20:23], v[168:171], v[28:31]\n\t" \
  "v_cvt_pk_bf16_f32 v99, v102, v103\n\t" \
  "v_accvgpr_read_b32 v104, a120\n\t" \
  "v_mfma_f32_16x16x32_bf16 v[28:31], a[12:15], v[164:167], v[28:31]\n\t" \
  "buffer_load_dwordx4 v240, %[wo], s96 offen offset:0 lds\n\t" \
  "v_mfma_f32_16x16x32_bf16 v[28:31], a[4:7], v[160:163], v[28:31]\n\t" \
  "s_waitcnt lgkmcnt(7)\n\t" \
  "ds_write_b128 v213, v[96:99] offset:34816\n\t" \
  "v_mfma_f32_16x16x32_bf16 v[32:35], a[128:131], v[128:131], v[32:35]\n\t" \
  "ds_read_b128 v[160:163], v230 offset:40960\n\t" \
  "v_accvgpr_read_b32 v105, a121\n\t" \
  "v_mfma_f32_16x16x32_bf16 v[32:35], a[136:139], v[132:135], v[32:35]\n\t" \
  "buffer_load_dwordx4 v241, %[wo], s96 offen offset:1024 lds\n\t" \
  "v_mfma_f32_16x16x32_bf16 v[32:35], a[144:147], v[136:139], v[32:35]\n\t" \
  "ds_read_b128 v[164:167], v230 offset:41984\n\t" \
  "v_accvgpr_read_b32 v106, a122\n\t" \
  "v_mfma_f32_16x16x32_bf16 v[32:35], a[152:155], v[140:143], v[32:35]\n\t" \
  "ds_read_b64_tr_b16 a[58:59], v236 offset:28672\n\t" \
  "v_accvgpr_read_b32 v107, a123\n\t" \
  "v_mfma_f32_16x16x32_bf16 v[48:51], a[156:159], v[140:143], v[48:51]\n\t" \
  "buffer_load_dwordx4 v248, %[wo], s96 offen offset:2048 lds\n\t" \
  "v_mfma_f32_16x16x32_bf16 v[48:51], a[148:151], v[136:139], v[48:51]\n\t" \
  "ds_read_b128 v[168:171], v230 offset:43008\n\t" \
  "v_accvgpr_read_b32 v108, a124\n\t" \
  "v_mfma_f32_16x16x32_bf16 v[48:51], a[140:143], v[132:135], v[48:51]\n\t" \
  "ds_read_b64_tr_b16 a[60:61], v237 offset:24576\n\t" \
  "v_accvgpr_read_b32 v109, a125\n\t" \
  "v_mfma_f32_16x16x32_bf16 v[48:51], a[132:135], v[128:131], v[48:51]\n\t" \
  "s_waitcnt lgkmcnt(7)\n\t" \
  "ds_read_b128 v[172:175], v230 offset:44032\n\t" \
  "v_accvgpr_read_b32 v110, a126\n\t" \
  "v_mfma_f32_16x16x32_bf16 v[36:39], a[128:131], v[144:147], v[36:39]\n\t" \
  "buffer_load_dwordx4 v249, %[wo], s96 offen offset:3072 lds\n\t" \
  "v_mfma_f32_16x16x32_bf16 v[36:39], a[136:139], v[148:151], v[36:39]\n\t" \
  "ds_read_b64_tr_b16 a[62:63], v237 offset:28672\n\t" \
  "v_accvgpr_read_b32 v111, a127\n\t" \
  "v_mfma_f32_16x16x32_bf16 v[36:39], a[144:147], v[152:155], v[36:39]\n\t" \
  "ds_read_b128 v[128:131], v230 offset:45056\n\t" \
  "v_mul_f32 v104, v104, v222\n\t" \
  "v_mfma_f32_16x16x32_bf16 v[36:39], a[152:155], v[156:159], v[36:39]\n\t" \
  "buffer_store_dwordx4 v[96:99], v242, %[ws], s98 offen offset:2048 sc1\n\t" \
  "ds_read_b64_tr_b16 a[64:65], v238 offset:0\n\t" \
  "ds_read_b64_tr_b16 a[66:67], v238 offset:4096\n\t" \
  "ds_read_b128 v[132:135], v230 offset:46080\n\t" \
  "v_mfma_f32_16x16x32_bf16 v[52:55], a[156:159], v[156:159], v[52:55]\n\t" \
  ".Lhs11:\n\ts_cbranch_execz .Lhs11n\n\t" \
  "ds_read_b128 v[136:139], v230 offset:47104\n\t" \
  "v_mul_f32 v105, v105, v222\n\t" \
  "v_mfma_f32_16x16x32_bf16 v[52:55], a[148:151], v[152:155], v[52:55]\n\t" \
  "ds_read_b64_tr_b16 a[68:69], v239 offset:0\n\t" \
  "v_mul_f32 v106, v106, v222\n\t" \
  "v_mfma_f32_16x16x32_bf16 v[52:55], a[140:143], v[148:151], v[52:55]\n\t" \
  "s_waitcnt vmcnt(11) lgkmcnt(6)\n\t" \
  "ds_read_b128 v[140:143], v230 offset:48128\n\t" \
  "v_mul_f32 v107, v107, v222\n\t" \
  "v_mfma_f32_16x16x32_bf16 v[52:55], a[132:135], v[144:147], v[52:55]\n\t" \
  "s_barrier\n\t" \
  "v_mfma_f32_16x16x32_bf16 v[40:43], a[128:131], v[160:163], v[40:43]\n\t" \
  "ds_read_b128 v[144:147], v242 offset:0\n\t" \
  "v_mul_f32 v108, v108, v222\n\t" \
  "v_mfma_f32_16x16x32_bf16 v[40:43], a[136:139], v[164:167], v[40:43]\n\t" \
  "s_add_u32 s96, %[hl18], 1572864\n\t" \
  "ds_read_b64_tr_b16 a[70:71], v239 offset:4096\n\t" \
  "v_mul_f32 v109, v109, v222\n\t" \
  "v_mfma_f32_16x16x32_bf16 v[40:43], a[144:147], v[168:171], v[40:43]\n\t" \
  "s_and_b32 s96, s96, 0x1c0000\n\t" \
  "ds_read_b128 v[148:151], v242 offset:1024\n\t" \
  "v_mul_f32 v110, v110, v222\n\t" \
  "v_mfma_f32_16x16x32_bf16 v[40:43], a[152:155], v[172:175], v[40:43]\n\t" \
  "s_add_u32 s96, s96, %[wob]\n\t" \
  "ds_read_b64_tr_b16 a[72:73], v238 offset:8192\n\t" \
  "v_mul_f32 v111, v111, v222\n\t" \
  "v_mfma_f32_16x16x32_bf16 v[56:59], a[156:159], v[172:175], v[56:59]\n\t" \
  "s_add_u32 m0, %[m], 81920\n\t" \
  "ds_read_b128 v[152:155], v242 offset:2048\n\t" \
  "v_cvt_pk_bf16_f32 v104, v104, v105\n\t" \
  "v_mfma_f32_16x16x32_bf16 v[56:59], a[148:151], v[168:171], v[56:59]\n\t" \
  "ds_read_b64_tr_b16 a[74:75], v238 offset:12288\n\t" \
  "v_cvt_pk_bf16_f32 v105, v106, v107\n\t" \
  "v_mfma_f32_16x16x32_bf16 v[56:59], a[140:143], v[164:167], v[56:59]\n\t" \
  "ds_read_b128 v[156:159], v242 offset:3072\n\t" \
  "v_cvt_pk_bf16_f32 v106, v108, v109\n\t" \
  "v_mfma_f32_16x16x32_bf16 v[56:59], a[132:135], v[160:163], v[56:59]\n\t" \
  "s_waitcnt lgkmcnt(7)\n\t" \
  "buffer_load_dwordx4 v240, %[wo], s96 offen offset:0 lds\n\t" \
  "v_mfma_f32_16x16x32_bf16 v[44:47], a[128:131], v[128:131], v[44:47]\n\t" \
  "ds_read_b128 v[160:163], v242 offset:4096\n\t" \
  "v_cvt_pk_bf16_f32 v107, v110, v111\n\t" \
  "v_mfma_f32_16x16x32_bf16 v[44:47], a[136:139], v[132:135], v[44:47]\n\t" \
  "ds_write_b128 v213, v[104:107] offset:35840\n\t" \
  "v_mfma_f32_16x16x32_bf16 v[44:47], a[144:147], v[136:139], v[44:47]\n\t" \
  "buffer_load_dwordx4 v241, %[wo], s96 offen offset:1024 lds\n\t" \
  "v_mfma_f32_16x16x32_bf16 v[44:47], a[152:155], v[140:143], v[44:47]\n\t" \
  "ds_read_b128 v[164:167], v242 offset:5120\n\t" \
  "v_mfma_f32_16x16x32_bf16 v[60:63], a[156:159], v[140:143], v[60:63]\n\t" \
  "ds_read_b128 v[168:171], v242 offset:6144\n\t" \
  "v_mfma_f32_16x16x32_bf16 v[60:63], a[148:151], v[136:139], v[60:63]\n\t" \
  "ds_read_b64_tr_b16 a[76:77], v239 offset:8192\n\t" \
  "v_mfma_f32_16x16x32_bf16 v[60:63], a[140:143], v[132:135], v[60:63]\n\t" \
  "buffer_load_dwordx4 v248, %[wo], s96 offen offset:2048 lds\n\t" \
  "v_mfma_f32_16x16x32_bf16 v[60:63], a[132:135], v[128:131], v[60:63]\n\t" \
  "s_waitcnt lgkmcnt(5)\n\t" \
  "ds_read_b128 v[172:175], v242 offset:7168\n\t" \
  "v_mfma_f32_16x16x32_bf16 v[0:3], a[32:35], v[144:147], v[0:3]\n\t" \
  "ds_read_b64_tr_b16 a[78:79], v239 offset:12288\n\t" \
  "v_mfma_f32_16x16x32_bf16 v[0:3], a[40:43], v[148:151], v[0:3]\n\t" \
  "buffer_load_dwordx4 v249, %[wo], s96 offen offset:3072 lds\n\t" \
  "v_mfma_f32_16x16x32_bf16 v[0:3], a[48:51], v[152:155], v[0:3]\n\t" \
  "ds_read_b128 v[128:131], v242 offset:8192\n\t" \
  "v_mfma_f32_16x16x32_bf16 v[0:3], a[56:59], v[156:159], v[0:3]\n\t" \
  "ds_read_b128 v[132:135], v242 offset:9216\n\t" \
  "v_mfma_f32_16x16x32_bf16 v[16:19], a[60:63], v[156:159], v[16:19]\n\t" \
  "ds_read_b128 v[136:139], v242 offset:10240\n\t" \
  "v_mfma_f32_16x16x32_bf16 v[16:19], a[52:55], v[152:155], v[16:19]\n\t" \
  "buffer_store_dwordx4 v[104:107], v242, %[ws], s98 offen offset:3072 sc1\n\t" \
  "ds_read_b128 v[140:143], v242 offset:11264\n\t" \
  "v_mfma_f32_16x16x32_bf16 v[16:19], a[44:47], v[148:151], v[16:19]\n\t" \
  "v_mfma_f32_16x16x32_bf16 v[16:19], a[36:39], v[144:147], v[16:19]\n\t" \
  "s_waitcnt lgkmcnt(5)\n\t" \
  "v_mfma_f32_16x16x32_bf16 v[4:7], a[32:35], v[160:163], v[4:7]\n\t" \
  "ds_read_b128 v[144:147], v242 offset:12288\n\t" \
  "v_mfma_f32_16x16x32_bf16 v[4:7], a[40:43], v[164:167], v[4:7]\n\t" \
  "ds_read_b128 v[148:151], v242 offset:13312\n\t" \
  "v_mfma_f32_16x16x32_bf16 v[4:7], a[48:51], v[168:171], v[4:7]\n\t" \
  "ds_read_b128 v[152:155], v242 offset:14336\n\t" \
  "v_mfma_f32_16x16x32_bf16 v[4:7], a[56:59], v[172:175], v[4:7]\n\t" \
  "s_add_u32 s97, s64, 655360\n\t" \
  "ds_read_b128 v[156:159], v242 offset:15360\n\t" \
  "s_and_b32 s97, s97, 0xe0000\n\t" \
  "v_mfma_f32_16x16x32_bf16 v[20:23], a[60:63], v[172:175], v[20:23]\n\t" \
  "s_add_u32 s97, s97, s60\n\t" \
  "s_add_u32 m0, %[m], 131072\n\t" \
  "v_mfma_f32_16x16x32_bf16 v[20:23], a[52:55], v[168:171], v[20:23]\n\t" \
  "v_mfma_f32_16x16x32_bf16 v[20:23], a[44:47], v[164:167], v[20:23]\n\t" \
  "s_waitcnt vmcnt(11) lgkmcnt(4)\n\t" \
  "v_mfma_f32_16x16x32_bf16 v[20:23], a[36:39], v[160:163], v[20:23]\n\t" \
  "s_barrier\n\t" \
  "v_mfma_f32_16x16x32_bf16 v[8:11], a[32:35], v[128:131], v[8:11]\n\t" \
  "ds_read_b128 v[160:163], v232 offset:16384\n\t" \
  "v_mfma_f32_16x16x32_bf16 v[8:11], a[40:43], v[132:135], v[8:11]\n\t" \
  "buffer_load_dwordx4 v242, %[ws], s97 offen offset:0 sc1 lds\n\t" \
  "v_mfma_f32_16x16x32_bf16 v[8:11], a[48:51], v[136:139], v[8:11]\n\t" \
  "ds_read_b128 v[164:167], v232 offset:17408\n\t" \
  "v_mfma_f32_16x16x32_bf16 v[8:11], a[56:59], v[140:143], v[8:11]\n\t" \
  "ds_read_b128 v[168:171], v232 offset:18432\n\t" \
  "v_mfma_f32_16x16x32_bf16 v[24:27], a[60:63], v[140:143], v[24:27]\n\t" \
  "ds_read_b128 v[172:175], v232 offset:19456\n\t" \
  "v_mfma_f32_16x16x32_bf16 v[24:27], a[52:55], v[136:139], v[24:27]\n\t" \
  "v_mfma_f32_16x16x32_bf16 v[24:27], a[44:47], v[132:135], v[24:27]\n\t" \
  "buffer_load_dwordx4 v242, %[ws], s97 offen offset:1024 sc1 lds\n\t" \
  "v_mfma_f32_16x16x32_bf16 v[24:27], a[36:39], v[128:131], v[24:27]\n\t" \
  "s_waitcnt lgkmcnt(4)\n\t" \
  "v_mfma_f32_16x16x32_bf16 v[12:15], a[32:35], v[144:147], v[12:15]\n\t" \
  "ds_read_b128 v[128:131], v232 offset:20480\n\t" \
  "v_mfma_f32_16x16x32_bf16 v[12:15], a[40:43], v[148:151], v[12:15]\n\t" \
  "buffer_load_dwordx4 v242, %[ws], s97 offen offset:2048 sc1 lds\n\t" \
  "v_mfma_f32_16x16x32_bf16 v[12:15], a[48:51], v[152:155], v[12:15]\n\t" \
  "ds_read_b128 v[132:135], v232 offset:21504\n\t" \
  "v_mfma_f32_16x16x32_bf16 v[12:15], a[56:59], v[156:159], v[12:15]\n\t" \
  "ds_read_b128 v[136:139], v232 offset:22528\n\t" \
  "v_mfma_f32_16x16x32_bf16 v[28:31], a[60:63], v[156:159], v[28:31]\n\t" \
  "buffer_load_dwordx4 v242, %[ws], s97 offen offset:3072 sc1 lds\n\t" \
  "v_mfma_f32_16x16x32_bf16 v[28:31], a[52:55], v[152:155], v[28:31]\n\t" \
  "s_add_u32 s96, %[hl18], 1572864\n\t" \
  "ds_read_b128 v[140:143], v232 offset:23552\n\t" \
  "s_and_b32 s96, s96, 0x1c0000\n\t" \
  "v_mfma_f32_16x16x32_bf16 v[28:31], a[44:47], v[148:151], v[28:31]\n\t" \
  "s_add_u32 s96, s96, s77\n\t" \
  "s_add_u32 m0, %[m], 16384\n\t" \
  "v_mfma_f32_16x16x32_bf16 v[28:31], a[36:39], v[144:147], v[28:31]\n\t" \
  "s_waitcnt lgkmcnt(4)\n\t" \
  "v_mfma_f32_16x16x32_bf16 v[32:35], a[160:163], v[160:163], v[32:35]\n\t" \
  "buffer_load_dwordx4 v240, %[wo], s96 offen offset:0 lds\n\t" \
  "v_mfma_f32_16x16x32_bf16 v[32:35], a[168:171], v[164:167], v[32:35]\n\t" \
  "ds_read_b128 v[144:147], v232 offset:24576\n\t" \
  "v_mfma_f32_16x16x32_bf16 v[32:35], a[176:179], v[168:171], v[32:35]\n\t" \
  "ds_read_b128 v[148:151], v232 offset:25600\n\t" \
  "v_mfma_f32_16x16x32_bf16 v[32:35], a[184:187], v[172:175], v[32:35]\n\t" \
  "ds_read_b128 v[152:155], v232 offset:26624\n\t" \
  "v_mfma_f32_16x16x32_bf16 v[48:51], a[188:191], v[172:175], v[48:51]\n\t" \
  "buffer_load_dwordx4 v241, %[wo], s96 offen offset:1024 lds\n\t" \
  "v_mfma_f32_16x16x32_bf16 v[48:51], a[180:183], v[168:171], v[48:51]\n\t" \
  "ds_read_b128 v[156:159], v232 offset:27648\n\t" \
  "v_mfma_f32_16x16x32_bf16 v[48:51], a[172:175], v[164:167], v[48:51]\n\t" \
  "v_mfma_f32_16x16x32_bf16 v[48:51], a[164:167], v[160:163], v[48:51]\n\t" \
  "s_waitcnt lgkmcnt(4)\n\t" \
  "v_mfma_f32_16x16x32_bf16 v[36:39], a[160:163], v[128:131], v[36:39]\n\t" \
  "ds_read_b128 v[160:163], v232 offset:28672\n\t" \
  "v_mfma_f32_16x16x32_bf16 v[36:39], a[168:171], v[132:135], v[36:39]\n\t" \
  "ds_read_b128 v[164:167], v232 offset:29696\n\t" \
  "v_mfma_f32_16x16x32_bf16 v[36:39], a[176:179], v[136:139], v[36:39]\n\t" \
  "ds_read_b128 v[168:171], v232 offset:30720\n\t" \
  "v_mfma_f32_16x16x32_bf16 v[36:39], a[184:187], v[140:143], v[36:39]\n\t" \
  "ds_read_b128 v[172:175], v232 offset:31744\n\t" \
  "v_mfma_f32_16x16x32_bf16 v[52:55], a[188:191], v[140:143], v[52:55]\n\t" \
  "v_mfma_f32_16x16x32_bf16 v[52:55], a[180:183], v[136:139], v[52:55]\n\t" \
  "v_mfma_f32_16x16x32_bf16 v[52:55], a[172:175], v[132:135], v[52:55]\n\t" \
  "s_waitcnt vmcnt(12) lgkmcnt(4)\n\t" \
  "v_mfma_f32_16x16x32_bf16 v[52:55], a[164:167], v[128:131], v[52:55]\n\t" \
  "s_barrier\n\t" \
  "v_mfma_f32_16x16x32_bf16 v[40:43], a[160:163], v[144:147], v[40:43]\n\t" \
  "buffer_load_dwordx4 v248, %[wo], s96 offen offset:2048 lds\n\t" \
  "v_mfma_f32_16x16x32_bf16 v[40:43], a[168:171], v[148:151], v[40:43]\n\t" \
  "ds_read_b128 v[128:131], v242 offset:32768\n\t" \
  "v_mfma_f32_16x16x32_bf16 v[40:43], a[176:179], v[152:155], v[40:43]\n\t" \
  "ds_read_b128 v[132:135], v242 offset:33792\n\t" \
  "v_mfma_f32_16x16x32_bf16 v[40:43], a[184:187], v[156:159], v[40:43]\n\t" \
  "ds_read_b64_tr_b16 a[80:81], v236 offset:0\n\t" \
  "v_mfma_f32_16x16x32_bf16 v[56:59], a[188:191], v[156:159], v[56:59]\n\t" \
  "buffer_load_dwordx4 v249, %[wo], s96 offen offset:3072 lds\n\t" \
  "v_mfma_f32_16x16x32_bf16 v[56:59], a[180:183], v[152:155], v[56:59]\n\t" \
  "s_add_u32 s97, s64, 393216\n\t" \
  "ds_read_b128 v[136:139], v242 offset:34816\n\t" \
  "s_and_b32 s97, s97, 0xe0000\n\t" \
  "v_mfma_f32_16x16x32_bf16 v[56:59], a[172:175], v[148:151], v[56:59]\n\t" \
  "s_add_u32 s97, s97, s61\n\t" \
  "ds_read_b128 v[140:143], v242 offset:35840\n\t" \
  "s_add_u32 m0, %[m], 0\n\t" \
  "v_mfma_f32_16x16x32_bf16 v[56:59], a[164:167], v[144:147], v[56:59]\n\t" \
  "s_waitcnt lgkmcnt(5)\n\t" \
  "ds_read_b64_tr_b16 a[82:83], v236 offset:4096\n\t" \
  "v_mfma_f32_16x16x32_bf16 v[44:47], a[160:163], v[160:163], v[44:47]\n\t" \
  "buffer_load_dwordx4 v242, %[ws], s97 offen offset:0 sc1 lds\n\t" \
  "v_mfma_f32_16x16x32_bf16 v[44:47], a[168:171], v[164:167], v[44:47]\n\t" \
  "ds_read_b64_tr_b16 a[84:85], v237 offset:0\n\t" \
  "ds_read_b128 v[144:147], v242 offset:36864\n\t" \
  "v_mfma_f32_16x16x32_bf16 v[44:47], a[176:179], v[168:171], v[44:47]\n\t" \
  "ds_read_b64_tr_b16 a[86:87], v237 offset:4096\n\t" \
  "ds_read_b128 v[148:151], v242 offset:37888\n\t" \
  "v_mfma_f32_16x16x32_bf16 v[44:47], a[184:187], v[172:175], v[44:47]\n\t" \
  "buffer_load_dwordx4 v242, %[ws], s97 offen offset:1024 sc1 lds\n\t" \
  "v_mfma_f32_16x16x32_bf16 v[60:63], a[188:191], v[172:175], v[60:63]\n\t" \
  "ds_read_b128 v[152:155], v242 offset:38912\n\t" \
  "v_mfma_f32_16x16x32_bf16 v[60:63], a[180:183], v[168:171], v[60:63]\n\t" \
  "ds_read_b64_tr_b16 a[88:89], v236 offset:8192\n\t" \
  "v_mfma_f32_16x16x32_bf16 v[60:63], a[172:175], v[164:167], v[60:63]\n\t" \
  "ds_read_b128 v[156:159], v242 offset:39936\n\t" \
  "v_mfma_f32_16x16x32_bf16 v[60:63], a[164:167], v[160:163], v[60:63]\n\t" \
  "s_waitcnt lgkmcnt(8)\n\t" \
  "ds_read_b64_tr_b16 a[90:91], v236 offset:12288\n\t" \
  "v_mfma_f32_16x16x32_bf16 v[96:99], a[192:195], v[128:131], 0\n\t" \
  "buffer_load_dwordx4 v242, %[ws], s97 offen offset:2048 sc1 lds\n\t" \
  "v_mfma_f32_16x16x32_bf16 v[96:99], a[200:203], v[132:135], v[96:99]\n\t" \
  "ds_read_b128 v[160:163], v242 offset:40960\n\t" \
  "v_mfma_f32_16x16x32_bf16 v[96:99], a[208:211], v[136:139], v[96:99]\n\t" \
  "ds_read_b128 v[164:167], v242 offset:41984\n\t" \
  "v_mfma_f32_16x16x32_bf16 v[96:99], a[216:219], v[140:143], v[96:99]\n\t" \
  "buffer_load_dwordx4 v242, %[ws], s97 offen offset:3072 sc1 lds\n\t" \
  "v_mfma_f32_16x16x32_bf16 v[112:115], a[220:223], v[140:143], 0\n\t" \
  "s_add_u32 s96, %[hl18], 1835008\n\t" \
  "ds_read_b128 v[168:171], v242 offset:43008\n\t" \
  "s_and_b32 s96, s96, 0x1c0000\n\t" \
  "v_mfma_f32_16x16x32_bf16 v[112:115], a[212:215], v[136:139], v[112:115]\n\t" \
  "s_add_u32 s96, s96, %[wob]\n\t" \
  "ds_read_b64_tr_b16 a[92:93], v237 offset:8192\n\t" \
  "s_add_u32 m0, %[m], 49152\n\t" \
  "v_mfma_f32_16x16x32_bf16 v[112:115], a[204:207], v[132:135], v[112:115]\n\t" \
  "s_waitcnt vmcnt(12)\n\t" \
  "buffer_store_dword v243, v254, %[ws], %[fw2] offen offset:64 sc1\n\t" \
  "ds_read_b128 v[172:175], v242 offset:44032\n\t" \
  "v_mfma_f32_16x16x32_bf16 v[112:115], a[196:199], v[128:131], v[112:115]\n\t" \
  "s_waitcnt lgkmcnt(6)\n\t" \
  "ds_read_b64_tr_b16 a[94:95], v237 offset:12288\n\t" \
  "v_mfma_f32_16x16x32_bf16 v[100:103], a[192:195], v[144:147], 0\n\t" \
  "buffer_load_dwordx4 v240, %[wo], s96 offen offset:0 lds\n\t" \
  "v_mfma_f32_16x16x32_bf16 v[100:103], a[200:203], v[148:151], v[100:103]\n\t" \
  "ds_read_b128 v[128:131], v242 offset:45056\n\t" \
  "v_mfma_f32_16x16x32_bf16 v[100:103], a[208:211], v[152:155], v[100:103]\n\t" \
  "ds_read_b128 v[132:135], v242 offset:46080\n\t" \
  "v_mfma_f32_16x16x32_bf16 v[100:103], a[216:219], v[156:159], v[100:103]\n\t" \
  "buffer_load_dwordx4 v241, %[wo], s96 offen offset:1024 lds\n\t" \
  "v_mfma_f32_16x16x32_bf16 v[116:119], a[220:223], v[156:159], 0\n\t" \
  "ds_read_b128 v[136:139], v242 offset:47104\n\t" \
  "v_mfma_f32_16x16x32_bf16 v[116:119], a[212:215], v[152:155], v[116:119]\n\t" \
  "ds_read_b128 v[140:143], v242 offset:48128\n\t" \
  "v_mfma_f32_16x16x32_bf16 v[116:119], a[204:207], v[148:151], v[116:119]\n\t" \
  "s_waitcnt vmcnt(11) lgkmcnt(4)\n\t" \
  "v_mfma_f32_16x16x32_bf16 v[116:119], a[196:199], v[144:147], v[116:119]\n\t" \
  "s_barrier\n\t" \
  "v_mfma_f32_16x16x32_bf16 v[104:107], a[192:195], v[160:163], 0\n\t" \
  "ds_read_b128 v[144:147], v232 offset:0\n\t" \
  "v_mfma_f32_16x16x32_bf16 v[104:107], a[200:203], v[164:167], v[104:107]\n\t" \
  "ds_read_b64_tr_b16 a[96:97], v236 offset:16384\n\t" \
  "v_mfma_f32_16x16x32_bf16 v[104:107], a[208:211], v[168:171], v[104:107]\n\t" \
  "ds_read_b128 v[148:151], v232 offset:1024\n\t" \
  "v_mfma_f32_16x16x32_bf16 v[104:107], a[216:219], v[172:175], v[104:107]\n\t" \
  "ds_read_b64_tr_b16 a[98:99], v236 offset:20480\n\t" \
  "v_mfma_f32_16x16x32_bf16 v[120:123], a[220:223], v[172:175], 0\n\t" \
  "ds_read_b128 v[152:155], v232 offset:2048\n\t" \
  "v_mfma_f32_16x16x32_bf16 v[120:123], a[212:215], v[168:171], v[120:123]\n\t" \
  "ds_read_b128 v[156:159], v232 offset:3072\n\t" \
  "v_mfma_f32_16x16x32_bf16 v[120:123], a[204:207], v[164:167], v[120:123]\n\t" \
  "ds_read_b64_tr_b16 a[100:101], v237 offset:16384\n\t" \
  "v_mfma_f32_16x16x32_bf16 v[120:123], a[196:199], v[160:163], v[120:123]\n\t" \
  "s_waitcnt lgkmcnt(7)\n\t" \
  "ds_read_b64_tr_b16 a[102:103], v237 offset:20480\n\t" \
  "v_mfma_f32_16x16x32_bf16 v[108:111], a[192:195], v[128:131], 0\n\t" \
  "ds_read_b128 v[160:163], v232 offset:4096\n\t" \
  "v_mfma_f32_16x16x32_bf16 v[108:111], a[200:203], v[132:135], v[108:111]\n\t" \
  "ds_read_b128 v[164:167], v232 offset:5120\n\t" \
  "v_mfma_f32_16x16x32_bf16 v[108:111], a[208:211], v[136:139], v[108:111]\n\t" \
  "ds_read_b128 v[168:171], v232 offset:6144\n\t" \
  "v_mfma_f32_16x16x32_bf16 v[108:111], a[216:219], v[140:143], v[108:111]\n\t" \
  "buffer_load_dwordx4 v248, %[wo], s96 offen offset:2048 lds\n\t" \
  "v_mfma_f32_16x16x32_bf16 v[124:127], a[220:223], v[140:143], 0\n\t" \
  "ds_read_b64_tr_b16 a[104:105], v236 offset:24576\n\t" \
  "v_mfma_f32_16x16x32_bf16 v[124:127], a[212:215], v[136:139], v[124:127]\n\t" \
  "ds_read_b128 v[172:175], v232 offset:7168\n\t" \
  "v_mfma_f32_16x16x32_bf16 v[124:127], a[204:207], v[132:135], v[124:127]\n\t" \
  "buffer_load_dwordx4 v249, %[wo], s96 offen offset:3072 lds\n\t" \
  "v_mfma_f32_16x16x32_bf16 v[124:127], a[196:199], v[128:131], v[124:127]\n\t" \
  "s_waitcnt lgkmcnt(7)\n\t" \
  "ds_read_b64_tr_b16 a[106:107], v236 offset:28672\n\t" \
  "s_add_u32 s97, s64, 786432\n\t" \
  "v_mfma_f32_16x16x32_bf16 v[0:3], a[64:67], v[144:147], v[0:3]\n\t" \
  "s_and_b32 s97, s97, 0xe0000\n\t" \
  "s_add_u32 s97, s97, s60\n\t" \
  "s_add_u32 m0, %[m], 65536\n\t" \
  "v_mfma_f32_16x16x32_bf16 v[0:3], a[72:75], v[148:151], v[0:3]\n\t" \
  "ds_read_b128 v[128:131], v232 offset:8192\n\t" \
  "v_lshrrev_b32 v252, 5, v233\n\t" \
  "v_mfma_f32_16x16x32_bf16 v[0:3], a[80:83], v[152:155], v[0:3]\n\t" \
  "buffer_load_dwordx4 v242, %[ws], s97 offen offset:0 sc1 lds\n\t" \
  "v_mfma_f32_16x16x32_bf16 v[0:3], a[88:91], v[156:159], v[0:3]\n\t" \
  "ds_read_b128 v[132:135], v232 offset:9216\n\t" \
  "v_mul_u32_u24 v252, 1536, v252\n\t" \
  "v_mfma_f32_16x16x32_bf16 v[16:19], a[92:95], v[156:159], v[16:19]\n\t" \
  "ds_read_b128 v[136:139], v232 offset:10240\n\t" \
  "v_lshl_add_u32 v252, %[wid], 9, v252\n\t" \
  "v_mfma_f32_16x16x32_bf16 v[16:19], a[84:87], v[152:155], v[16:19]\n\t" \
  "ds_read_b64_tr_b16 a[108:109], v237 offset:24576\n\t" \
  "v_add_u32 v252, v252, v242\n\t" \
  "v_mfma_f32_16x16x32_bf16 v[16:19], a[76:79], v[148:151], v[16:19]\n\t" \
  "buffer_load_dwordx4 v242, %[ws], s97 offen offset:1024 sc1 lds\n\t" \
  "v_mfma_f32_16x16x32_bf16 v[16:19], a[68:71], v[144:147], v[16:19]\n\t" \
  "s_waitcnt lgkmcnt(5)\n\t" \
  "ds_read_b128 v[140:143], v232 offset:11264\n\t" \
  "v_lshrrev_b32 v247, 1, v242\n\t" \
  "v_mfma_f32_16x16x32_bf16 v[4:7], a[64:67], v[160:163], v[4:7]\n\t" \
  "ds_read_b64_tr_b16 a[110:111], v237 offset:28672\n\t" \
  "v_add_u32 v247, %[m], v247\n\t" \
  "v_mfma_f32_16x16x32_bf16 v[4:7], a[72:75], v[164:167], v[4:7]\n\t" \
  "buffer_load_dwordx4 v242, %[ws], s97 offen offset:2048 sc1 lds\n\t" \
  "v_mfma_f32_16x16x32_bf16 v[4:7], a[80:83], v[168:171], v[4:7]\n\t" \
  "ds_read_b128 v[144:147], v232 offset:12288\n\t" \
  "v_mfma_f32_16x16x32_bf16 v[4:7], a[88:91], v[172:175], v[4:7]\n\t" \
  "ds_read_b128 v[148:151], v232 offset:13312\n\t" \
  "v_mfma_f32_16x16x32_bf16 v[20:23], a[92:95], v[172:175], v[20:23]\n\t" \
  "buffer_load_dwordx4 v242, %[ws], s97 offen offset:3072 sc1 lds\n\t" \
  "v_mfma_f32_16x16x32_bf16 v[20:23], a[84:87], v[168:171], v[20:23]\n\t" \
  "s_add_u32 s96, %[hl18], 1835008\n\t" \
  "ds_read_b128 v[152:155], v232 offset:14336\n\t" \
  "s_and_b32 s96, s96, 0x1c0000\n\t" \
  "v_mfma_f32_16x16x32_bf16 v[20:23], a[76:79], v[164:167], v[20:23]\n\t" \
  "s_waitcnt vmcnt(9) lgkmcnt(3)\n\t" \
  "s_add_u32 s96, s96, s77\n\t" \
  "s_add_u32 m0, %[m], 81920\n\t" \
  "v_mfma_f32_16x16x32_bf16 v[20:23], a[68:71], v[160:163], v[20:23]\n\t" \
  "s_barrier\n\t" \
  "v_mfma_f32_16x16x32_bf16 v[8:11], a[64:67], v[128:131], v[8:11]\n\t" \
  "buffer_load_dwordx4 v240, %[wo], s96 offen offset:0 lds\n\t" \
  "v_mfma_f32_16x16x32_bf16 v[8:11], a[72:75], v[132:135], v[8:11]\n\t" \
  "ds_read_b128 v[156:159], v232 offset:15360\n\t" \
  "v_mfma_f32_16x16x32_bf16 v[8:11], a[80:83], v[136:139], v[8:11]\n\t" \
  "ds_read_b128 v[160:163], v242 offset:0\n\t" \
  "v_mfma_f32_16x16x32_bf16 v[8:11], a[88:91], v[140:143], v[8:11]\n\t" \
  "buffer_load_dwordx4 v241, %[wo], s96 offen offset:1024 lds\n\t" \
  "v_mfma_f32_16x16x32_bf16 v[24:27], a[92:95], v[140:143], v[24:27]\n\t" \
  "ds_read_b128 v[164:167], v242 offset:1024\n\t" \
  "v_mfma_f32_16x16x32_bf16 v[24:27], a[84:87], v[136:139], v[24:27]\n\t" \
  "ds_read_b128 v[168:171], v242 offset:2048\n\t" \
  "v_mfma_f32_16x16x32_bf16 v[24:27], a[76:79], v[132:135], v[24:27]\n\t" \
  "buffer_load_dwordx4 v248, %[wo], s96 offen offset:2048 lds\n\t" \
  "v_mfma_f32_16x16x32_bf16 v[24:27], a[68:71], v[128:131], v[24:27]\n\t" \
  "s_waitcnt lgkmcnt(3)\n\t" \
  "ds_read_b128 v[172:175], v242 offset:3072\n\t" \
  "v_mfma_f32_16x16x32_bf16 v[12:15], a[64:67], v[144:147], v[12:15]\n\t" \
  "ds_read_b64_tr_b16 a[112:113], v234 offset:16384\n\t" \
  "v_mfma_f32_16x16x32_bf16 v[12:15], a[72:75], v[148:151], v[12:15]\n\t" \
  "ds_read_b128 v[128:131], v242 offset:4096\n\t" \
  "v_mfma_f32_16x16x32_bf16 v[12:15], a[80:83], v[152:155], v[12:15]\n\t" \
  "ds_read_b128 v[132:135], v242 offset:5120\n\t" \
  "v_mfma_f32_16x16x32_bf16 v[12:15], a[88:91], v[156:159], v[12:15]\n\t" \
  "buffer_load_dwordx4 v249, %[wo], s96 offen offset:3072 lds\n\t" \
  "v_mfma_f32_16x16x32_bf16 v[28:31], a[92:95], v[156:159], v[28:31]\n\t" \
  "s_add_u32 s97, s64, 524288\n\t" \
  "s_and_b32 s97, s97, 0xe0000\n\t" \
  "s_add_u32 s97, s97, s61\n\t" \
  "v_mfma_f32_16x16x32_bf16 v[28:31], a[84:87], v[152:155], v[28:31]\n\t" \
  "s_add_u32 m0, %[m], 147456\n\t" \
  "ds_read_b128 v[136:139], v242 offset:6144\n\t" \
  "v_mfma_f32_16x16x32_bf16 v[28:31], a[76:79], v[148:151], v[28:31]\n\t" \
  "buffer_load_dwordx4 v242, %[ws], s97 offen offset:0 sc1 lds\n\t" \
  "v_mfma_f32_16x16x32_bf16 v[28:31], a[68:71], v[144:147], v[28:31]\n\t" \
  "s_waitcnt lgkmcnt(4)\n\t" \
  "ds_read_b64_tr_b16 a[114:115], v234 offset:20480\n\t" \
  STP("s_memrealtime s[94:95]\n\t") \
  STP("s_memtime s[92:93]\n\t") \
  "v_mfma_f32_16x16x32_bf16 v[32:35], a[0:3], v[160:163], v[32:35]\n\t" \
  "ds_read_b128 v[140:143], v242 offset:7168\n\t" \
  "v_mfma_f32_16x16x32_bf16 v[32:35], a[8:11], v[164:167], v[32:35]\n\t" \
  "ds_read_b64_tr_b16 a[116:117], v235 offset:16384\n\t" \
  "v_mfma_f32_16x16x32_bf16 v[32:35], a[16:19], v[168:171], v[32:35]\n\t" \
  "ds_read_b128 v[144:147], v242 offset:8192\n\t" \
  "v_mfma_f32_16x16x32_bf16 v[32:35], a[24:27], v[172:175], v[32:35]\n\t" \
  "ds_read_b64_tr_b16 a[118:119], v235 offset:20480\n\t" \
  "v_mfma_f32_16x16x32_bf16 v[48:51], a[28:31], v[172:175], v[48:51]\n\t" \
  "ds_read_b128 v[148:151], v242 offset:9216\n\t" \
  "v_mfma_f32_16x16x32_bf16 v[48:51], a[20:23], v[168:171], v[48:51]\n\t" \
  "ds_read_b64_tr_b16 a[120:121], v234 offset:24576\n\t" \
  "v_mfma_f32_16x16x32_bf16 v[48:51], a[12:15], v[164:167], v[48:51]\n\t" \
  "ds_read_b128 v[152:155], v242 offset:10240\n\t" \
  "v_mfma_f32_16x16x32_bf16 v[48:51], a[4:7], v[160:163], v[48:51]\n\t" \
  "s_waitcnt lgkmcnt(6)\n\t" \
  "ds_read_b64_tr_b16 a[122:123], v234 offset:28672\n\t" \
  "v_mfma_f32_16x16x32_bf16 v[36:39], a[0:3], v[128:131], v[36:39]\n\t" \
  "ds_read_b128 v[156:159], v242 offset:11264\n\t" \
  "v_mfma_f32_16x16x32_bf16 v[36:39], a[8:11], v[132:135], v[36:39]\n\t" \
  "ds_read_b64_tr_b16 a[124:125], v235 offset:24576\n\t" \
  "v_mfma_f32_16x16x32_bf16 v[36:39], a[16:19], v[136:139], v[36:39]\n\t" \
  "ds_read_b128 v[160:163], v242 offset:12288\n\t" \
  "v_mfma_f32_16x16x32_bf16 v[36:39], a[24:27], v[140:143], v[36:39]\n\t" \
  "ds_read_b128 v[164:167], v242 offset:13312\n\t" \
  "ds_read_b64_tr_b16 a[126:127], v235 offset:28672\n\t" \
  "v_mfma_f32_16x16x32_bf16 v[52:55], a[28:31], v[140:143], v[52:55]\n\t" \
  STP("v_writelane_b32 %[stv], s94, 32\n\t") \
  STP("v_writelane_b32 %[stv], s92, 33\n\t") \
  "ds_read_b128 v[168:171], v242 offset:14336\n\t" \
  "v_mfma_f32_16x16x32_bf16 v[52:55], a[20:23], v[136:139], v[52:55]\n\t" \
  "buffer_load_dwordx4 v242, %[ws], s97 offen offset:1024 sc1 lds\n\t" \
  "v_mfma_f32_16x16x32_bf16 v[52:55], a[12:15], v[132:135], v[52:55]\n\t" \
  "s_waitcnt vmcnt(6) lgkmcnt(1)\n\t" \
  "ds_read_b128 v[172:175], v242 offset:15360\n\t" \
  "v_mfma_f32_16x16x32_bf16 v[52:55], a[4:7], v[128:131], v[52:55]\n\t" \
  "s_barrier\n\t" \
  "v_mfma_f32_16x16x32_bf16 v[40:43], a[0:3], v[144:147], v[40:43]\n\t" \
  "buffer_load_dwordx4 v242, %[ws], s97 offen offset:2048 sc1 lds\n\t" \
  "v_mfma_f32_16x16x32_bf16 v[40:43], a[8:11], v[148:151], v[40:43]\n\t" \
  "ds_read_b128 v[128:131], v230 offset:0\n\t" \
  "v_mfma_f32_16x16x32_bf16 v[40:43], a[16:19], v[152:155], v[40:43]\n\t" \
  "ds_read_b128 v[132:135], v230 offset:1024\n\t" \
  "v_mfma_f32_16x16x32_bf16 v[40:43], a[24:27], v[156:159], v[40:43]\n\t" \
  "buffer_load_dwordx4 v242, %[ws], s97 offen offset:3072 sc1 lds\n\t" \
  "v_mfma_f32_16x16x32_bf16 v[56:59], a[28:31], v[156:159], v[56:59]\n\t" \
  "s_add_u32 s97, s64, 917504\n\t" \
  "s_and_b32 s97, s97, 0xe0000\n\t" \
  "s_add_u32 s97, s97, s60\n\t" \
  "v_mfma_f32_16x16x32_bf16 v[56:59], a[20:23], v[152:155], v[56:59]\n\t" \
  "s_add_u32 m0, %[m], 131072\n\t" \
  "ds_read_b128 v[136:139], v230 offset:2048\n\t" \
  "v_mfma_f32_16x16x32_bf16 v[56:59], a[12:15], v[148:151], v[56:59]\n\t" \
  "buffer_load_dwordx4 v242, %[ws], s97 offen offset:0 sc1 lds\n\t" \
  "v_mfma_f32_16x16x32_bf16 v[56:59], a[4:7], v[144:147], v[56:59]\n\t" \
  "s_waitcnt lgkmcnt(3)\n\t" \
  "ds_read_b128 v[140:143], v230 offset:3072\n\t" \
  "v_mfma_f32_16x16x32_bf16 v[44:47], a[0:3], v[160:163], v[44:47]\n\t" \
  "ds_read_b128 v[144:147], v230 offset:4096\n\t" \
  "v_mfma_f32_16x16x32_bf16 v[44:47], a[8:11], v[164:167], v[44:47]\n\t" \
  "buffer_load_dwordx4 v242, %[ws], s97 offen offset:1024 sc1 lds\n\t" \
  "v_mfma_f32_16x16x32_bf16 v[44:47], a[16:19], v[168:171], v[44:47]\n\t" \
  "ds_read_b128 v[148:151], v230 offset:5120\n\t" \
  "v_mfma_f32_16x16x32_bf16 v[44:47], a[24:27], v[172:175], v[44:47]\n\t" \
  "ds_read_b128 v[152:155], v230 offset:6144\n\t" \
  "v_mfma_f32_16x16x32_bf16 v[60:63], a[28:31], v[172:175], v[60:63]\n\t" \
  "buffer_load_dwordx4 v242, %[ws], s97 offen offset:2048 sc1 lds\n\t" \
  "v_mfma_f32_16x16x32_bf16 v[60:63], a[20:23], v[168:171], v[60:63]\n\t" \
  "ds_read_b128 v[156:159], v230 offset:7168\n\t" \
  "v_mfma_f32_16x16x32_bf16 v[60:63], a[12:15], v[164:167], v[60:63]\n\t" \
  "v_mfma_f32_16x16x32_bf16 v[60:63], a[4:7], v[160:163], v[60:63]\n\t" \
  "s_waitcnt lgkmcnt(4)\n\t" \
  "v_mfma_f32_16x16x32_bf16 v[0:3], a[96:99], v[128:131], v[0:3]\n\t" \
  "buffer_load_dwordx4 v242, %[ws], s97 offen offset:3072 sc1 lds\n\t" \
  "v_mfma_f32_16x16x32_bf16 v[0:3], a[104:107], v[132:135], v[0:3]\n\t" \
  "s_add_u32 s97, s64, 655360\n\t" \
  "ds_read_b128 v[160:163], v230 offset:8192\n\t" \
  "s_and_b32 s97, s97, 0xe0000\n\t" \
  "v_mfma_f32_16x16x32_bf16 v[0:3], a[112:115], v[136:139], v[0:3]\n\t" \
  "s_add_u32 s97, s97, s61\n\t" \
  "ds_read_b128 v[164:167], v230 offset:9216\n\t" \
  "s_add_u32 m0, %[m], 16384\n\t" \
  "v_mfma_f32_16x16x32_bf16 v[0:3], a[120:123], v[140:143], v[0:3]\n\t" \
  "ds_read_b128 v[168:171], v230 offset:10240\n\t" \
  "v_mfma_f32_16x16x32_bf16 v[16:19], a[124:127], v[140:143], v[16:19]\n\t" \
  "buffer_load_dwordx4 v242, %[ws], s97 offen offset:0 sc1 lds\n\t" \
  "v_mfma_f32_16x16x32_bf16 v[16:19], a[116:119], v[136:139], v[16:19]\n\t" \
  "ds_read_b128 v[172:175], v230 offset:11264\n\t" \
  "v_mfma_f32_16x16x32_bf16 v[16:19], a[108:111], v[132:135], v[16:19]\n\t" \
  "v_mfma_f32_16x16x32_bf16 v[16:19], a[100:103], v[128:131], v[16:19]\n\t" \
  "s_waitcnt lgkmcnt(4)\n\t" \
  "buffer_load_dword v246, v245, %[ws], s65 offen sc1\n\t" \
  "v_mfma_f32_16x16x32_bf16 v[4:7], a[96:99], v[144:147], v[4:7]\n\t" \
  "ds_read_b128 v[128:131], v230 offset:12288\n\t" \
  "v_mfma_f32_16x16x32_bf16 v[4:7], a[104:107], v[148:151], v[4:7]\n\t" \
  "ds_read_b128 v[132:135], v230 offset:13312\n\t" \
  "v_mfma_f32_16x16x32_bf16 v[4:7], a[112:115], v[152:155], v[4:7]\n\t" \
  "ds_read_b128 v[136:139], v230 offset:14336\n\t" \
  "v_mfma_f32_16x16x32_bf16 v[4:7], a[120:123], v[156:159], v[4:7]\n\t" \
  "buffer_load_dwordx4 v242, %[ws], s97 offen offset:1024 sc1 lds\n\t" \
  "v_mfma_f32_16x16x32_bf16 v[20:23], a[124:127], v[156:159], v[20:23]\n\t" \
  "ds_read_b128 v[140:143], v230 offset:15360\n\t" \
  "v_mfma_f32_16x16x32_bf16 v[20:23], a[116:119], v[152:155], v[20:23]\n\t" \
  "v_mfma_f32_16x16x32_bf16 v[20:23], a[108:111], v[148:151], v[20:23]\n\t" \
  "s_waitcnt vmcnt(7) lgkmcnt(4)\n\t" \
  "buffer_load_dwordx4 v242, %[ws], s97 offen offset:2048 sc1 lds\n\t" \
  "v_mfma_f32_16x16x32_bf16 v[20:23], a[100:103], v[144:147], v[20:23]\n\t" \
  "s_barrier\n\t" \
  "v_mfma_f32_16x16x32_bf16 v[8:11], a[96:99], v[160:163], v[8:11]\n\t" \
  "ds_read_b64_tr_b16 a[192:193], v234 offset:49152\n\t" \
  "ds_read_b128 v[144:147], v232 offset:16384\n\t" \
  "ds_read_b128 v[148:151], v232 offset:17408\n\t" \
  "v_mfma_f32_16x16x32_bf16 v[8:11], a[104:107], v[164:167], v[8:11]\n\t" \
  "buffer_load_dwordx4 v242, %[ws], s97 offen offset:3072 sc1 lds\n\t" \
  "v_mfma_f32_16x16x32_bf16 v[8:11], a[112:115], v[168:171], v[8:11]\n\t" \
  "s_add_u32 s97, s64, 786432\n\t" \
  "s_and_b32 s97, s97, 0xe0000\n\t" \
  "ds_read_b128 v[152:155], v232 offset:18432\n\t" \
  "s_add_u32 s97, s97, s61\n\t" \
  "v_mfma_f32_16x16x32_bf16 v[8:11], a[120:123], v[172:175], v[8:11]\n\t" \
  "s_add_u32 m0, %[m], 0\n\t" \
  "ds_read_b128 v[156:159], v232 offset:19456\n\t" \
  "v_mfma_f32_16x16x32_bf16 v[24:27], a[124:127], v[172:175], v[24:27]\n\t" \
  "buffer_load_dwordx4 v242, %[ws], s97 offen offset:0 sc1 lds\n\t" \
  "v_mfma_f32_16x16x32_bf16 v[24:27], a[116:119], v[168:171], v[24:27]\n\t" \
  "ds_read_b64_tr_b16 a[194:195], v234 offset:53248\n\t" \
  "v_mfma_f32_16x16x32_bf16 v[24:27], a[108:111], v[164:167], v[24:27]\n\t" \
  "ds_read_b64_tr_b16 a[196:197], v235 offset:49152\n\t" \
  "v_mfma_f32_16x16x32_bf16 v[24:27], a[100:103], v[160:163], v[24:27]\n\t" \
  "s_waitcnt lgkmcnt(7)\n\t" \
  "buffer_load_dwordx4 v242, %[ws], s97 offen offset:1024 sc1 lds\n\t" \
  "v_mfma_f32_16x16x32_bf16 v[12:15], a[96:99], v[128:131], v[12:15]\n\t" \
  "ds_read_b64_tr_b16 a[198:199], v235 offset:53248\n\t" \
  "ds_read_b128 v[160:163], v232 offset:20480\n\t" \
  "v_mfma_f32_16x16x32_bf16 v[12:15], a[104:107], v[132:135], v[12:15]\n\t" \
  "ds_read_b128 v[164:167], v232 offset:21504\n\t" \
  "v_mfma_f32_16x16x32_bf16 v[12:15], a[112:115], v[136:139], v[12:15]\n\t" \
  "buffer_load_dwordx4 v242, %[ws], s97 offen offset:2048 sc1 lds\n\t" \
  "v_mfma_f32_16x16x32_bf16 v[12:15], a[120:123], v[140:143], v[12:15]\n\t" \
  "ds_read_b64_tr_b16 a[200:201], v234 offset:57344\n\t" \
  "ds_read_b128 v[168:171], v232 offset:22528\n\t" \
  "v_mfma_f32_16x16x32_bf16 v[28:31], a[124:127], v[140:143], v[28:31]\n\t" \
  "ds_read_b128 v[172:175], v232 offset:23552\n\t" \
  "v_mfma_f32_16x16x32_bf16 v[28:31], a[116:119], v[136:139], v[28:31]\n\t" \
  "buffer_load_dwordx4 v242, %[ws], s97 offen offset:3072 sc1 lds\n\t" \
  "v_mfma_f32_16x16x32_bf16 v[28:31], a[108:111], v[132:135], v[28:31]\n\t" \
  "s_add_u32 s97, s64, 917504\n\t" \
  "s_and_b32 s97, s97, 0xe0000\n\t" \
  "s_add_u32 s97, s97, s61\n\t" \
  "v_mfma_f32_16x16x32_bf16 v[28:31], a[100:103], v[128:131], v[28:31]\n\t" \
  "s_waitcnt lgkmcnt(8)\n\t" \
  "ds_read_b64_tr_b16 a[202:203], v234 offset:61440\n\t" \
  "s_add_u32 m0, %[m], 32768\n\t" \
  "v_mfma_f32_16x16x32_bf16 v[32:35], a[32:35], v[144:147], v[32:35]\n\t" \
  "buffer_load_dwordx4 v242, %[ws], s97 offen offset:0 sc1 lds\n\t" \
  "ds_read_b128 v[128:131], v232 offset:24576\n\t" \
  "v_mfma_f32_16x16x32_bf16 v[32:35], a[40:43], v[148:151], v[32:35]\n\t" \
  "ds_read_b64_tr_b16 a[204:205], v235 offset:57344\n\t" \
  "ds_read_b128 v[132:135], v232 offset:25600\n\t" \
  "v_mfma_f32_16x16x32_bf16 v[32:35], a[48:51], v[152:155], v[32:35]\n\t" \
  "ds_read_b128 v[136:139], v232 offset:26624\n\t" \
  "v_mfma_f32_16x16x32_bf16 v[32:35], a[56:59], v[156:159], v[32:35]\n\t" \
  "buffer_load_dwordx4 v242, %[ws], s97 offen offset:1024 sc1 lds\n\t" \
  "v_mfma_f32_16x16x32_bf16 v[48:51], a[60:63], v[156:159], v[48:51]\n\t" \
  "ds_read_b128 v[140:143], v232 offset:27648\n\t" \
  "v_mfma_f32_16x16x32_bf16 v[48:51], a[52:55], v[152:155], v[48:51]\n\t" \
  "ds_read_b64_tr_b16 a[206:207], v235 offset:61440\n\t" \
  "v_mfma_f32_16x16x32_bf16 v[48:51], a[44:47], v[148:151], v[48:51]\n\t" \
  "buffer_load_dwordx4 v242, %[ws], s97 offen offset:2048 sc1 lds\n\t" \
  "v_mfma_f32_16x16x32_bf16 v[48:51], a[36:39], v[144:147], v[48:51]\n\t" \
  "s_waitcnt lgkmcnt(7)\n\t" \
  "ds_read_b64_tr_b16 a[208:209], v236 offset:16384\n\t" \
  "v_mfma_f32_16x16x32_bf16 v[36:39], a[32:35], v[160:163], v[36:39]\n\t" \
  "ds_read_b64_tr_b16 a[210:211], v236 offset:20480\n\t" \
  "ds_read_b128 v[144:147], v232 offset:28672\n\t" \
  "ds_read_b128 v[148:151], v232 offset:29696\n\t" \
  "v_mfma_f32_16x16x32_bf16 v[36:39], a[40:43], v[164:167], v[36:39]\n\t" \
  "buffer_load_dwordx4 v242, %[ws], s97 offen offset:3072 sc1 lds\n\t" \
  "v_mfma_f32_16x16x32_bf16 v[36:39], a[48:51], v[168:171], v[36:39]\n\t" \
  "s_add_u32 s97, s64, 131072\n\t" \
  "ds_read_b128 v[152:155], v232 offset:30720\n\t" \
  "s_and_b32 s97, s97, 0xe0000\n\t" \
  "v_mfma_f32_16x16x32_bf16 v[36:39], a[56:59], v[172:175], v[36:39]\n\t" \
  "s_add_u32 s97, s97, s62\n\t" \
  "ds_read_b128 v[156:159], v232 offset:31744\n\t" \
  "s_add_u32 m0, %[m], 65536\n\t" \
  "v_mfma_f32_16x16x32_bf16 v[52:55], a[60:63], v[172:175], v[52:55]\n\t" \
  "ds_read_b64_tr_b16 a[212:213], v237 offset:16384\n\t" \
  "v_mfma_f32_16x16x32_bf16 v[52:55], a[52:55], v[168:171], v[52:55]\n\t" \
  "s_waitcnt vmcnt(11)\n\t" \
  "v_cmp_ne_u32 vcc, %[tag], v246\n\t" \
  "v_mfma_f32_16x16x32_bf16 v[52:55], a[44:47], v[164:167], v[52:55]\n\t" \
  "s_waitcnt lgkmcnt(7)\n\t" \
  "s_and_b32 vcc_lo, vcc_lo, 0xfe0000\n\t" \
  "s_cbranch_scc1 .Lat_s2\n\t" \
  ".Lat_k2:\n\t" \
  "v_mfma_f32_16x16x32_bf16 v[52:55], a[36:39], v[160:163], v[52:55]\n\t" \
  "s_barrier\n\t" \
  ".Lhs12:\n\ts_cbranch_execz .Lhs12n\n\t" \
  "v_mfma_f32_16x16x32_bf16 v[40:43], a[32:35], v[128:131], v[40:43]\n\t" \
  "buffer_load_dwordx4 v242, %[ws], s97 offen offset:0 sc1 lds\n\t" \
  "ds_read_b128 v[160:163], v232 offset:0\n\t" \
  "v_mfma_f32_16x16x32_bf16 v[40:43], a[40:43], v[132:135], v[40:43]\n\t" \
  "ds_read_b128 v[164:167], v232 offset:1024\n\t" \
  "ds_read_b64_tr_b16 a[214:215], v237 offset:20480\n\t" \
  "v_mfma_f32_16x16x32_bf16 v[40:43], a[48:51], v[136:139], v[40:43]\n\t" \
  "ds_read_b64_tr_b16 a[216:217], v236 offset:24576\n\t" \
  "v_mfma_f32_16x16x32_bf16 v[40:43], a[56:59], v[140:143], v[40:43]\n\t" \
  "buffer_load_dwordx4 v242, %[ws], s97 offen offset:1024 sc1 lds\n\t" \
  "v_mfma_f32_16x16x32_bf16 v[56:59], a[60:63], v[140:143], v[56:59]\n\t" \
  "ds_read_b128 v[168:171], v232 offset:2048\n\t" \
  "v_mfma_f32_16x16x32_bf16 v[56:59], a[52:55], v[136:139], v[56:59]\n\t" \
  "ds_read_b64_tr_b16 a[218:219], v236 offset:28672\n\t" \
  "v_mfma_f32_16x16x32_bf16 v[56:59], a[44:47], v[132:135], v[56:59]\n\t" \
  "buffer_load_dwordx4 v242, %[ws], s97 offen offset:2048 sc1 lds\n\t" \
  "v_mfma_f32_16x16x32_bf16 v[56:59], a[36:39], v[128:131], v[56:59]\n\t" \
  "s_waitcnt lgkmcnt(7)\n\t" \
  "ds_read_b128 v[172:175], v232 offset:3072\n\t" \
  "v_mfma_f32_16x16x32_bf16 v[44:47], a[32:35], v[144:147], v[44:47]\n\t" \
  "ds_read_b64_tr_b16 a[220:221], v237 offset:24576\n\t" \
  "v_mfma_f32_16x16x32_bf16 v[44:47], a[40:43], v[148:151], v[44:47]\n\t" \
  "buffer_load_dwordx4 v242, %[ws], s97 offen offset:3072 sc1 lds\n\t" \
  "v_mfma_f32_16x16x32_bf16 v[44:47], a[48:51], v[152:155], v[44:47]\n\t" \
  "s_add_u32 s97, s64, 262144\n\t" \
  "ds_read_b128 v[128:131], v232 offset:4096\n\t" \
  "s_and_b32 s97, s97, 0xe0000\n\t" \
  "v_mfma_f32_16x16x32_bf16 v[44:47], a[56:59], v[156:159], v[44:47]\n\t" \
  "s_add_u32 s97, s97, s62\n\t" \
  "ds_read_b128 v[132:135], v232 offset:5120\n\t" \
  "ds_read_b64_tr_b16 a[222:223], v237 offset:28672\n\t" \
  "s_add_u32 m0, %[m], 49152\n\t" \
  "v_mfma_f32_16x16x32_bf16 v[60:63], a[60:63], v[156:159], v[60:63]\n\t" \
  "ds_read_b128 v[136:139], v232 offset:6144\n\t" \
  "v_mfma_f32_16x16x32_bf16 v[60:63], a[52:55], v[152:155], v[60:63]\n\t" \
  "buffer_load_dwordx4 v242, %[ws], s97 offen offset:0 sc1 lds\n\t" \
  "v_mfma_f32_16x16x32_bf16 v[60:63], a[44:47], v[148:151], v[60:63]\n\t" \
  "ds_read_b128 v[140:143], v232 offset:7168\n\t" \
  "v_mfma_f32_16x16x32_bf16 v[60:63], a[36:39], v[144:147], v[60:63]\n\t" \
  "s_waitcnt lgkmcnt(2)\n\t" \
  "v_mfma_f32_16x16x32_bf16 v[0:3], a[192:195], v[160:163], v[0:3]\n\t" \
  "buffer_load_dwordx4 v242, %[ws], s97 offen offset:1024 sc1 lds\n\t" \
  "v_mfma_f32_16x16x32_bf16 v[0:3], a[200:203], v[164:167], v[0:3]\n\t" \
  "ds_read_b128 v[144:147], v232 offset:8192\n\t" \
  "v_mfma_f32_16x16x32_bf16 v[0:3], a[208:211], v[168:171], v[0:3]\n\t" \
  "ds_read_b128 v[148:151], v232 offset:9216\n\t" \
  "v_mfma_f32_16x16x32_bf16 v[0:3], a[216:219], v[172:175], v[0:3]\n\t" \
  "ds_read_b128 v[152:155], v232 offset:10240\n\t" \
  "v_mfma_f32_16x16x32_bf16 v[16:19], a[220:223], v[172:175], v[16:19]\n\t" \
  "buffer_load_dwordx4 v242, %[ws], s97 offen offset:2048 sc1 lds\n\t" \
  "v_mfma_f32_16x16x32_bf16 v[16:19], a[212:215], v[168:171], v[16:19]\n\t" \
  "ds_read_b128 v[156:159], v232 offset:11264\n\t" \
  "v_mfma_f32_16x16x32_bf16 v[16:19], a[204:207], v[164:167], v[16:19]\n\t" \
  "v_mfma_f32_16x16x32_bf16 v[16:19], a[196:199], v[160:163], v[16:19]\n\t" \
  "s_waitcnt lgkmcnt(4)\n\t" \
  "v_cvt_pk_bf16_f32 v208, v0, v1\n\t" \
  "v_cvt_pk_bf16_f32 v209, v2, v3\n\t" \
  "v_mfma_f32_16x16x32_bf16 v[4:7], a[192:195], v[128:131], v[4:7]\n\t" \
  "buffer_load_dwordx4 v242, %[ws], s97 offen offset:3072 sc1 lds\n\t" \
  "v_mfma_f32_16x16x32_bf16 v[4:7], a[200:203], v[132:135], v[4:7]\n\t" \
  "s_add_u32 s97, s64, 393216\n\t" \
  "s_and_b32 s97, s97, 0xe0000\n\t" \
  "s_add_u32 s97, s97, s62\n\t" \
  "v_mfma_f32_16x16x32_bf16 v[4:7], a[208:211], v[136:139], v[4:7]\n\t" \
  "s_add_u32 m0, %[m], 98304\n\t" \
  "ds_read_b128 v[160:163], v232 offset:12288\n\t" \
  "v_cvt_pk_bf16_f32 v210, v16, v17\n\t" \
  "v_mfma_f32_16x16x32_bf16 v[4:7], a[216:219], v[140:143], v[4:7]\n\t" \
  "buffer_load_dwordx4 v242, %[ws], s97 offen offset:0 sc1 lds\n\t" \
  "v_mfma_f32_16x16x32_bf16 v[20:23], a[220:223], v[140:143], v[20:23]\n\t" \
  "ds_read_b128 v[164:167], v232 offset:13312\n\t" \
  "v_cvt_pk_bf16_f32 v211, v18, v19\n\t" \
  "v_mfma_f32_16x16x32_bf16 v[20:23], a[212:215], v[136:139], v[20:23]\n\t" \
  "ds_read_b128 v[168:171], v232 offset:14336\n\t" \
  "v_mfma_f32_16x16x32_bf16 v[20:23], a[204:207], v[132:135], v[20:23]\n\t" \
  "s_waitcnt vmcnt(17) lgkmcnt(3)\n\t" \
  "ds_read_b128 v[172:175], v232 offset:15360\n\t" \
  "v_mfma_f32_16x16x32_bf16 v[20:23], a[196:199], v[128:131], v[20:23]\n\t" \
  "s_barrier\n\t" \
  "v_mfma_f32_16x16x32_bf16 v[8:11], a[192:195], v[144:147], v[8:11]\n\t" \
  "buffer_store_dwordx4 v[208:211], v242, %[ws], s67 offen offset:0 sc1\n\t" \
  "ds_read_b128 v[128:131], v242 offset:16384\n\t" \
  "v_cvt_pk_bf16_f32 v212, v4, v5\n\t" \
  "v_cvt_pk_bf16_f32 v213, v6, v7\n\t" \
  "v_mfma_f32_16x16x32_bf16 v[8:11], a[200:203], v[148:151], v[8:11]\n\t" \
  "ds_read_b128 v[132:135], v242 offset:17408\n\t" \
  "v_cvt_pk_bf16_f32 v214, v20, v21\n\t" \
  "v_mfma_f32_16x16x32_bf16 v[8:11], a[208:211], v[152:155], v[8:11]\n\t" \
  "ds_read_b128 v[136:139], v242 offset:18432\n\t" \
  "v_cvt_pk_bf16_f32 v215, v22, v23\n\t" \
  "v_mfma_f32_16x16x32_bf16 v[8:11], a[216:219], v[156:159], v[8:11]\n\t" \
  "ds_read_b128 v[140:143], v242 offset:19456\n\t" \
  "v_mfma_f32_16x16x32_bf16 v[24:27], a[220:223], v[156:159], v[24:27]\n\t" \
  "v_mfma_f32_16x16x32_bf16 v[24:27], a[212:215], v[152:155], v[24:27]\n\t" \
  "v_mfma_f32_16x16x32_bf16 v[24:27], a[204:207], v[148:151], v[24:27]\n\t" \
  "v_mfma_f32_16x16x32_bf16 v[24:27], a[196:199], v[144:147], v[24:27]\n\t" \
  "s_waitcnt lgkmcnt(4)\n\t" \
  "v_mfma_f32_16x16x32_bf16 v[12:15], a[192:195], v[160:163], v[12:15]\n\t" \
  "ds_read_b128 v[144:147], v242 offset:20480\n\t" \
  "v_cvt_pk_bf16_f32 v216, v8, v9\n\t" \
  "v_mfma_f32_16x16x32_bf16 v[12:15], a[200:203], v[164:167], v[12:15]\n\t" \
  "ds_read_b128 v[148:151], v242 offset:21504\n\t" \
  "v_cvt_pk_bf16_f32 v217, v10, v11\n\t" \
  "v_mfma_f32_16x16x32_bf16 v[12:15], a[208:211], v[168:171], v[12:15]\n\t" \
  "v_cvt_pk_bf16_f32 v218, v24, v25\n\t" \
  "v_cvt_pk_bf16_f32 v219, v26, v27\n\t" \
  "v_mfma_f32_16x16x32_bf16 v[12:15], a[216:219], v[172:175], v[12:15]\n\t" \
  "ds_read_b128 v[152:155], v242 offset:22528\n\t" \
  "v_mfma_f32_16x16x32_bf16 v[28:31], a[220:223], v[172:175], v[28:31]\n\t" \
  "buffer_store_dwordx4 v[212:215], v242, %[ws], s67 offen offset:1024 sc1\n\t" \
  "ds_read_b128 v[156:159], v242 offset:23552\n\t" \
  "v_mfma_f32_16x16x32_bf16 v[28:31], a[212:215], v[168:171], v[28:31]\n\t" \
  "v_mfma_f32_16x16x32_bf16 v[28:31], a[204:207], v[164:167], v[28:31]\n\t" \
  "v_mfma_f32_16x16x32_bf16 v[28:31], a[196:199], v[160:163], v[28:31]\n\t" \
  "s_waitcnt lgkmcnt(4)\n\t" \
  "v_cvt_pk_bf16_f32 v220, v12, v13\n\t" \
  "v_cvt_pk_bf16_f32 v221, v14, v15\n\t" \
  "v_mfma_f32_16x16x32_bf16 v[32:35], a[64:67], v[128:131], v[32:35]\n\t" \
  "ds_read_b128 v[160:163], v242 offset:24576\n\t" \
  "v_mfma_f32_16x16x32_bf16 v[32:35], a[72:75], v[132:135], v[32:35]\n\t" \
  "ds_read_b128 v[164:167], v242 offset:25600\n\t" \
  "v_mfma_f32_16x16x32_bf16 v[32:35], a[80:83], v[136:139], v[32:35]\n\t" \
  "v_cvt_pk_bf16_f32 v222, v28, v29\n\t" \
  "v_cvt_pk_bf16_f32 v223, v30, v31\n\t" \
  "v_mfma_f32_16x16x32_bf16 v[32:35], a[88:91], v[140:143], v[32:35]\n\t" \
  "ds_read_b128 v[168:171], v242 offset:26624\n\t" \
  "v_mfma_f32_16x16x32_bf16 v[48:51], a[92:95], v[140:143], v[48:51]\n\t" \
  "ds_read_b128 v[172:175], v242 offset:27648\n\t" \
  "v_mfma_f32_16x16x32_bf16 v[48:51], a[84:87], v[136:139], v[48:51]\n\t" \
  "v_mfma_f32_16x16x32_bf16 v[48:51], a[76:79], v[132:135], v[48:51]\n\t" \
  "v_mfma_f32_16x16x32_bf16 v[48:51], a[68:71], v[128:131], v[48:51]\n\t" \
  "s_waitcnt lgkmcnt(4)\n\t" \
  "v_mfma_f32_16x16x32_bf16 v[36:39], a[64:67], v[144:147], v[36:39]\n\t" \
  "ds_read_b128 v[128:131], v242 offset:28672\n\t" \
  "v_mfma_f32_16x16x32_bf16 v[36:39], a[72:75], v[148:151], v[36:39]\n\t" \
  "buffer_store_dwordx4 v[216:219], v242, %[ws], s67 offen offset:2048 sc1\n\t" \
  "ds_read_b128 v[132:135], v242 offset:29696\n\t" \
  "ds_read_b128 v[136:139], v242 offset:30720\n\t" \
  "ds_read_b128 v[140:143], v242 offset:31744\n\t" \
  "v_mfma_f32_16x16x32_bf16 v[36:39], a[80:83], v[152:155], v[36:39]\n\t" \
  "v_mfma_f32_16x16x32_bf16 v[36:39], a[88:91], v[156:159], v[36:39]\n\t" \
  "v_mfma_f32_16x16x32_bf16 v[52:55], a[92:95], v[156:159], v[52:55]\n\t" \
  "v_mfma_f32_16x16x32_bf16 v[52:55], a[84:87], v[152:155], v[52:55]\n\t" \
  "v_mfma_f32_16x16x32_bf16 v[52:55], a[76:79], v[148:151], v[52:55]\n\t" \
  "s_waitcnt vmcnt(16) lgkmcnt(4)\n\t" \
  "v_mfma_f32_16x16x32_bf16 v[52:55], a[68:71], v[144:147], v[52:55]\n\t" \
  "s_barrier\n\t" \
  "v_mfma_f32_16x16x32_bf16 v[40:43], a[64:67], v[160:163], v[40:43]\n\t" \
  "ds_read_b128 v[144:147], v242 offset:0\n\t" \
  "v_mfma_f32_16x16x32_bf16 v[40:43], a[72:75], v[164:167], v[40:43]\n\t" \
  "ds_read_b128 v[148:151], v242 offset:1024\n\t" \
  "v_mfma_f32_16x16x32_bf16 v[40:43], a[80:83], v[168:171], v[40:43]\n\t" \
  "ds_read_b128 v[152:155], v242 offset:2048\n\t" \
  "v_mfma_f32_16x16x32_bf16 v[40:43], a[88:91], v[172:175], v[40:43]\n\t" \
  "ds_read_b128 v[156:159], v242 offset:3072\n\t" \
  "v_mfma_f32_16x16x32_bf16 v[56:59], a[92:95], v[172:175], v[56:59]\n\t" \
  "v_mfma_f32_16x16x32_bf16 v[56:59], a[84:87], v[168:171], v[56:59]\n\t" \
  "v_mfma_f32_16x16x32_bf16 v[56:59], a[76:79], v[164:167], v[56:59]\n\t" \
  "buffer_store_dwordx4 v[220:223], v242, %[ws], s67 offen offset:3072 sc1\n\t" \
  "v_mfma_f32_16x16x32_bf16 v[56:59], a[68:71], v[160:163], v[56:59]\n\t" \
  "s_waitcnt lgkmcnt(4)\n\t" \
  "v_mfma_f32_16x16x32_bf16 v[44:47], a[64:67], v[128:131], v[44:47]\n\t" \
  "ds_read_b128 v[160:163], v242 offset:4096\n\t" \
  "v_mfma_f32_16x16x32_bf16 v[44:47], a[72:75], v[132:135], v[44:47]\n\t" \
  "ds_read_b128 v[164:167], v242 offset:5120\n\t" \
  "v_mfma_f32_16x16x32_bf16 v[44:47], a[80:83], v[136:139], v[44:47]\n\t" \
  "ds_read_b128 v[168:171], v242 offset:6144\n\t" \
  "v_mfma_f32_16x16x32_bf16 v[44:47], a[88:91], v[140:143], v[44:47]\n\t" \
  "ds_read_b128 v[172:175], v242 offset:7168\n\t" \
  "v_mfma_f32_16x16x32_bf16 v[60:63], a[92:95], v[140:143], v[60:63]\n\t" \
  "v_mfma_f32_16x16x32_bf16 v[60:63], a[84:87], v[136:139], v[60:63]\n\t" \
  "v_mfma_f32_16x16x32_bf16 v[60:63], a[76:79], v[132:135], v[60:63]\n\t" \
  "v_mfma_f32_16x16x32_bf16 v[60:63], a[68:71], v[128:131], v[60:63]\n\t" \
  "s_waitcnt lgkmcnt(4)\n\t" \
  "v_mfma_f32_16x16x32_bf16 v[32:35], a[96:99], v[144:147], v[32:35]\n\t" \
  "ds_read_b128 v[128:131], v242 offset:8192\n\t" \
  "v_mfma_f32_16x16x32_bf16 v[32:35], a[104:107], v[148:151], v[32:35]\n\t" \
  "ds_read_b128 v[132:135], v242 offset:9216\n\t" \
  "v_mfma_f32_16x16x32_bf16 v[32:35], a[112:115], v[152:155], v[32:35]\n\t" \
  "ds_read_b128 v[136:139], v242 offset:10240\n\t" \
  "v_mfma_f32_16x16x32_bf16 v[32:35], a[120:123], v[156:159], v[32:35]\n\t" \
  "buffer_load_dwordx4 v242, %[ws], s97 offen offset:1024 sc1 lds\n\t" \
  "v_mfma_f32_16x16x32_bf16 v[48:51], a[124:127], v[156:159], v[48:51]\n\t" \
  "ds_read_b128 v[140:143], v242 offset:11264\n\t" \
  "v_mfma_f32_16x16x32_bf16 v[48:51], a[116:119], v[152:155], v[48:51]\n\t" \
  "v_mfma_f32_16x16x32_bf16 v[48:51], a[108:111], v[148:151], v[48:51]\n\t" \
  "v_mfma_f32_16x16x32_bf16 v[48:51], a[100:103], v[144:147], v[48:51]\n\t" \
  "s_waitcnt lgkmcnt(4)\n\t" \
  "buffer_load_dwordx4 v242, %[ws], s97 offen offset:2048 sc1 lds\n\t" \
  "v_mfma_f32_16x16x32_bf16 v[36:39], a[96:99], v[160:163], v[36:39]\n\t" \
  "ds_read_b128 v[144:147], v242 offset:12288\n\t" \
  "v_mfma_f32_16x16x32_bf16 v[36:39], a[104:107], v[164:167], v[36:39]\n\t" \
  "ds_read_b128 v[148:151], v242 offset:13312\n\t" \
  "v_mfma_f32_16x16x32_bf16 v[36:39], a[112:115], v[168:171], v[36:39]\n\t" \
  "buffer_load_dwordx4 v242, %[ws], s97 offen offset:3072 sc1 lds\n\t" \
  "v_mfma_f32_16x16x32_bf16 v[36:39], a[120:123], v[172:175], v[36:39]\n\t" \
  "s_add_u32 s97, s64, 524288\n\t" \
  "ds_read_b128 v[152:155], v242 offset:14336\n\t" \
  "s_and_b32 s97, s97, 0xe0000\n\t" \
  "v_mfma_f32_16x16x32_bf16 v[52:55], a[124:127], v[172:175], v[52:55]\n\t" \
  "s_add_u32 s97, s97, s62\n\t" \
  "ds_read_b128 v[156:159], v242 offset:15360\n\t" \
  "s_add_u32 m0, %[m], 81920\n\t" \
  "v_mfma_f32_16x16x32_bf16 v[52:55], a[116:119], v[168:171], v[52:55]\n\t" \
  "v_mfma_f32_16x16x32_bf16 v[52:55], a[108:111], v[164:167], v[52:55]\n\t" \
  "s_waitcnt vmcnt(16) lgkmcnt(4)\n\t" \
  "buffer_load_dwordx4 v242, %[ws], s97 offen offset:0 sc1 lds\n\t" \
  "v_mfma_f32_16x16x32_bf16 v[52:55], a[100:103], v[160:163], v[52:55]\n\t" \
  "s_barrier\n\t" \
  "v_mfma_f32_16x16x32_bf16 v[40:43], a[96:99], v[128:131], v[40:43]\n\t" \
  "ds_read_b128 v[160:163], v242 offset:32768\n\t" \
  "v_mfma_f32_16x16x32_bf16 v[40:43], a[104:107], v[132:135], v[40:43]\n\t" \
  "ds_read_b128 v[164:167], v242 offset:33792\n\t" \
  "v_mfma_f32_16x16x32_bf16 v[40:43], a[112:115], v[136:139], v[40:43]\n\t" \
  "buffer_load_dwordx4 v242, %[ws], s97 offen offset:1024 sc1 lds\n\t" \
  "v_mfma_f32_16x16x32_bf16 v[40:43], a[120:123], v[140:143], v[40:43]\n\t" \
  "ds_read_b128 v[168:171], v242 offset:34816\n\t" \
  "v_mfma_f32_16x16x32_bf16 v[56:59], a[124:127], v[140:143], v[56:59]\n\t" \
  "ds_read_b128 v[172:175], v242 offset:35840\n\t" \
  "v_mfma_f32_16x16x32_bf16 v[56:59], a[116:119], v[136:139], v[56:59]\n\t" \
  "buffer_load_dwordx4 v242, %[ws], s97 offen offset:2048 sc1 lds\n\t" \
  "v_mfma_f32_16x16x32_bf16 v[56:59], a[108:111], v[132:135], v[56:59]\n\t" \
  "v_mfma_f32_16x16x32_bf16 v[56:59], a[100:103], v[128:131], v[56:59]\n\t" \
  "s_waitcnt lgkmcnt(4)\n\t" \
  "v_mfma_f32_16x16x32_bf16 v[44:47], a[96:99], v[144:147], v[44:47]\n\t" \
  "ds_read_b128 v[128:131], v242 offset:36864\n\t" \
  "v_mfma_f32_16x16x32_bf16 v[44:47], a[104:107], v[148:151], v[44:47]\n\t" \
  "buffer_load_dwordx4 v242, %[ws], s97 offen offset:3072 sc1 lds\n\t" \
  "v_mfma_f32_16x16x32_bf16 v[44:47], a[112:115], v[152:155], v[44:47]\n\t" \
  "s_add_u32 s97, s64, 655360\n\t" \
  "ds_read_b128 v[132:135], v242 offset:37888\n\t" \
  "s_and_b32 s97, s97, 0xe0000\n\t" \
  "v_mfma_f32_16x16x32_bf16 v[44:47], a[120:123], v[156:159], v[44:47]\n\t" \
  "s_add_u32 s97, s97, s62\n\t" \
  "ds_read_b128 v[136:139], v242 offset:38912\n\t" \
  "s_add_u32 m0, %[m], 131072\n\t" \
  "v_mfma_f32_16x16x32_bf16 v[60:63], a[124:127], v[156:159], v[60:63]\n\t" \
  "ds_read_b128 v[140:143], v242 offset:39936\n\t" \
  "v_mfma_f32_16x16x32_bf16 v[60:63], a[116:119], v[152:155], v[60:63]\n\t" \
  "buffer_load_dwordx4 v242, %[ws], s97 offen offset:0 sc1 lds\n\t" \
  "v_mfma_f32_16x16x32_bf16 v[60:63], a[108:111], v[148:151], v[60:63]\n\t" \
  "v_mfma_f32_16x16x32_bf16 v[60:63], a[100:103], v[144:147], v[60:63]\n\t" \
  "s_waitcnt lgkmcnt(4)\n\t" \
  "v_mfma_f32_16x16x32_bf16 v[32:35], a[192:195], v[160:163], v[32:35]\n\t" \
  "buffer_load_dwordx4 v242, %[ws], s97 offen offset:1024 sc1 lds\n\t" \
  "v_mfma_f32_16x16x32_bf16 v[32:35], a[200:203], v[164:167], v[32:35]\n\t" \
  "ds_read_b128 v[144:147], v242 offset:40960\n\t" \
  "v_mfma_f32_16x16x32_bf16 v[32:35], a[208:211], v[168:171], v[32:35]\n\t" \
  "ds_read_b128 v[148:151], v242 offset:41984\n\t" \
  "v_mfma_f32_16x16x32_bf16 v[32:35], a[216:219], v[172:175], v[32:35]\n\t" \
  "ds_read_b128 v[152:155], v242 offset:43008\n\t" \
  "v_mfma_f32_16x16x32_bf16 v[48:51], a[220:223], v[172:175], v[48:51]\n\t" \
  "ds_read_b128 v[156:159], v242 offset:44032\n\t" \
  "v_mfma_f32_16x16x32_bf16 v[48:51], a[212:215], v[168:171], v[48:51]\n\t" \
  "v_mfma_f32_16x16x32_bf16 v[48:51], a[204:207], v[164:167], v[48:51]\n\t" \
  "buffer_load_dwordx4 v242, %[ws], s97 offen offset:2048 sc1 lds\n\t" \
  "v_mfma_f32_16x16x32_bf16 v[48:51], a[196:199], v[160:163], v[48:51]\n\t" \
  "s_waitcnt lgkmcnt(4)\n\t" \
  "v_cvt_pk_bf16_f32 v0, v32, v33\n\t" \
  "v_cvt_pk_bf16_f32 v1, v34, v35\n\t" \
  "v_mfma_f32_16x16x32_bf16 v[36:39], a[192:195], v[128:131], v[36:39]\n\t" \
  "ds_read_b128 v[160:163], v242 offset:45056\n\t" \
  "v_mfma_f32_16x16x32_bf16 v[36:39], a[200:203], v[132:135], v[36:39]\n\t" \
  "ds_read_b128 v[164:167], v242 offset:46080\n\t" \
  "v_mfma_f32_16x16x32_bf16 v[36:39], a[208:211], v[136:139], v[36:39]\n\t" \
  "v_cvt_pk_bf16_f32 v2, v48, v49\n\t" \
  "v_cvt_pk_bf16_f32 v3, v50, v51\n\t" \
  "v_mfma_f32_16x16x32_bf16 v[36:39], a[216:219], v[140:143], v[36:39]\n\t" \
  "ds_read_b128 v[168:171], v242 offset:47104\n\t" \
  "v_mfma_f32_16x16x32_bf16 v[52:55], a[220:223], v[140:143], v[52:55]\n\t" \
  "ds_read_b128 v[172:175], v242 offset:48128\n\t" \
  "v_mfma_f32_16x16x32_bf16 v[52:55], a[212:215], v[136:139], v[52:55]\n\t" \
  "v_mfma_f32_16x16x32_bf16 v[52:55], a[204:207], v[132:135], v[52:55]\n\t" \
  "s_waitcnt vmcnt(15) lgkmcnt(4)\n\t" \
  "v_mfma_f32_16x16x32_bf16 v[52:55], a[196:199], v[128:131], v[52:55]\n\t" \
  "s_barrier\n\t" \
  "v_mfma_f32_16x16x32_bf16 v[40:43], a[192:195], v[144:147], v[40:43]\n\t" \
  "ds_read_b128 v[128:131], v230 offset:0\n\t" \
  "v_cvt_pk_bf16_f32 v4, v36, v37\n\t" \
  "v_mfma_f32_16x16x32_bf16 v[40:43], a[200:203], v[148:151], v[40:43]\n\t" \
  "v_cvt_pk_bf16_f32 v5, v38, v39\n\t" \
  "v_and_b32 v235, 15, v233\n\t" \
  "v_mfma_f32_16x16x32_bf16 v[40:43], a[208:211], v[152:155], v[40:43]\n\t" \
  "v_cvt_pk_bf16_f32 v6, v52, v53\n\t" \
  "v_cvt_pk_bf16_f32 v7, v54, v55\n\t" \
  "v_mfma_f32_16x16x32_bf16 v[40:43], a[216:219], v[156:159], v[40:43]\n\t" \
  "ds_read_b128 v[132:135], v230 offset:1024\n\t" \
  "v_lshrrev_b32 v236, 4, v233\n\t" \
  "v_mfma_f32_16x16x32_bf16 v[56:59], a[220:223], v[156:159], v[56:59]\n\t" \
  "buffer_store_dwordx4 v[0:3], v242, %[ws], s68 offen offset:0 sc1\n\t" \
  "ds_read_b128 v[136:139], v230 offset:2048\n\t" \
  "v_lshlrev_b32 v234, 13, v236\n\t" \
  "v_lshl_add_u32 v234, v235, 4, v234\n\t" \
  "v_mfma_f32_16x16x32_bf16 v[56:59], a[212:215], v[152:155], v[56:59]\n\t" \
  "v_lshlrev_b32 v240, 7, v236\n\t" \
  "v_lshl_add_u32 v240, v235, 3, v240\n\t" \
  "v_mfma_f32_16x16x32_bf16 v[56:59], a[204:207], v[148:151], v[56:59]\n\t" \
  "buffer_load_dwordx4 v242, %[ws], s97 offen offset:3072 sc1 lds\n\t" \
  "v_mfma_f32_16x16x32_bf16 v[56:59], a[196:199], v[144:147], v[56:59]\n\t" \
  "s_waitcnt lgkmcnt(3)\n\t" \
  "ds_read_b128 v[140:143], v230 offset:3072\n\t" \
  "v_cvt_pk_bf16_f32 v8, v40, v41\n\t" \
  "v_mfma_f32_16x16x32_bf16 v[44:47], a[192:195], v[160:163], v[44:47]\n\t" \
  "ds_read_b128 v[144:147], v230 offset:4096\n\t" \
  "v_cvt_pk_bf16_f32 v9, v42, v43\n\t" \
  "v_mfma_f32_16x16x32_bf16 v[44:47], a[200:203], v[164:167], v[44:47]\n\t" \
  "s_waitcnt vmcnt(12)\n\t" \
  "buffer_store_dword v243, v244, %[ws], s71 offen offset:0 sc1\n\t" \
  "ds_read_b128 v[148:151], v230 offset:5120\n\t" \
  "v_mfma_f32_16x16x32_bf16 v[44:47], a[208:211], v[168:171], v[44:47]\n\t" \
  "v_cvt_pk_bf16_f32 v10, v56, v57\n\t" \
  "v_cvt_pk_bf16_f32 v11, v58, v59\n\t" \
  "v_mfma_f32_16x16x32_bf16 v[44:47], a[216:219], v[172:175], v[44:47]\n\t" \
  "buffer_store_dwordx4 v[4:7], v242, %[ws], s68 offen offset:1024 sc1\n\t" \
  "ds_read_b128 v[152:155], v230 offset:6144\n\t" \
  "v_add_u32 v240, s76, v240\n\t" \
  "v_lshlrev_b32 v235, 2, v235\n\t" \
  "v_mfma_f32_16x16x32_bf16 v[60:63], a[220:223], v[172:175], v[60:63]\n\t" \
  "ds_read_b128 v[156:159], v230 offset:7168\n\t" \
  "v_mfma_f32_16x16x32_bf16 v[60:63], a[212:215], v[168:171], v[60:63]\n\t" \
  "v_mfma_f32_16x16x32_bf16 v[60:63], a[204:207], v[164:167], v[60:63]\n\t" \
  "v_mfma_f32_16x16x32_bf16 v[60:63], a[196:199], v[160:163], v[60:63]\n\t" \
  "s_waitcnt lgkmcnt(4)\n\t" \
  "v_cvt_pk_bf16_f32 v12, v44, v45\n\t" \
  "v_cvt_pk_bf16_f32 v13, v46, v47\n\t" \
  STP("s_memrealtime s[94:95]\n\t") \
  STP("s_memtime s[92:93]\n\t") \
  "v_mfma_f32_16x16x32_bf16 v[64:67], a[128:131], v[128:131], v[64:67]\n\t" \
  "ds_read_b128 v[160:163], v230 offset:8192\n\t" \
  "v_mfma_f32_16x16x32_bf16 v[64:67], a[136:139], v[132:135], v[64:67]\n\t" \
  "ds_read_b128 v[164:167], v230 offset:9216\n\t" \
  "v_mfma_f32_16x16x32_bf16 v[64:67], a[144:147], v[136:139], v[64:67]\n\t" \
  "v_cvt_pk_bf16_f32 v14, v60, v61\n\t" \
  "v_cvt_pk_bf16_f32 v15, v62, v63\n\t" \
  "v_mfma_f32_16x16x32_bf16 v[64:67], a[152:155], v[140:143], v[64:67]\n\t" \
  "ds_read_b128 v[168:171], v230 offset:10240\n\t" \
  "v_mfma_f32_16x16x32_bf16 v[80:83], a[156:159], v[140:143], v[80:83]\n\t" \
  "ds_read_b128 v[172:175], v230 offset:11264\n\t" \
  "s_add_u32 s78, s75, 0\n\t" \
  "v_mfma_f32_16x16x32_bf16 v[80:83], a[148:151], v[136:139], v[80:83]\n\t" \
  "v_mfma_f32_16x16x32_bf16 v[80:83], a[140:143], v[132:135], v[80:83]\n\t" \
  "v_mfma_f32_16x16x32_bf16 v[80:83], a[132:135], v[128:131], v[80:83]\n\t" \
  "s_waitcnt lgkmcnt(4)\n\t" \
  "v_mfma_f32_16x16x32_bf16 v[68:71], a[128:131], v[144:147], v[68:71]\n\t" \
  "buffer_load_dwordx4 v[208:211], v234, %[ws], s78 offen sc1\n\t" \
  "v_mfma_f32_16x16x32_bf16 v[68:71], a[136:139], v[148:151], v[68:71]\n\t" \
  "ds_read_b128 v[128:131], v230 offset:12288\n\t" \
  "v_mfma_f32_16x16x32_bf16 v[68:71], a[144:147], v[152:155], v[68:71]\n\t" \
  "ds_read_b128 v[132:135], v230 offset:13312\n\t" \
  "s_add_u32 s78, s75, 32768\n\t" \
  "v_mfma_f32_16x16x32_bf16 v[68:71], a[152:155], v[156:159], v[68:71]\n\t" \
  "ds_read_b128 v[136:139], v230 offset:14336\n\t" \
  "v_mfma_f32_16x16x32_bf16 v[84:87], a[156:159], v[156:159], v[84:87]\n\t" \
  STP("v_writelane_b32 %[stv], s94, 34\n\t") \
  STP("v_writelane_b32 %[stv], s92, 35\n\t") \
  "ds_read_b128 v[140:143], v230 offset:15360\n\t" \
  "v_mfma_f32_16x16x32_bf16 v[84:87], a[148:151], v[152:155], v[84:87]\n\t" \
  "v_mfma_f32_16x16x32_bf16 v[84:87], a[140:143], v[148:151], v[84:87]\n\t" \
  "buffer_load_dwordx4 v[212:215], v234, %[ws], s78 offen sc1\n\t" \
  "v_mfma_f32_16x16x32_bf16 v[84:87], a[132:135], v[144:147], v[84:87]\n\t" \
  "s_waitcnt lgkmcnt(4)\n\t" \
  "s_add_u32 s78, s75, 65536\n\t" \
  "v_mfma_f32_16x16x32_bf16 v[72:75], a[128:131], v[160:163], v[72:75]\n\t" \
  "ds_read_b128 v[144:147], v242 offset:49152\n\t" \
  "v_mfma_f32_16x16x32_bf16 v[72:75], a[136:139], v[164:167], v[72:75]\n\t" \
  "ds_read_b128 v[148:151], v242 offset:50176\n\t" \
  "v_mfma_f32_16x16x32_bf16 v[72:75], a[144:147], v[168:171], v[72:75]\n\t" \
  "ds_read_b128 v[152:155], v242 offset:51200\n\t" \
  "v_mfma_f32_16x16x32_bf16 v[72:75], a[152:155], v[172:175], v[72:75]\n\t" \
  "buffer_load_dwordx4 v[216:219], v234, %[ws], s78 offen sc1\n\t" \
  "v_mfma_f32_16x16x32_bf16 v[88:91], a[156:159], v[172:175], v[88:91]\n\t" \
  "ds_read_b128 v[156:159], v242 offset:52224\n\t" \
  "s_add_u32 s78, s75, 98304\n\t" \
  "v_mfma_f32_16x16x32_bf16 v[88:91], a[148:151], v[168:171], v[88:91]\n\t" \
  "v_mfma_f32_16x16x32_bf16 v[88:91], a[140:143], v[164:167], v[88:91]\n\t" \
  "v_mfma_f32_16x16x32_bf16 v[88:91], a[132:135], v[160:163], v[88:91]\n\t" \
  "s_waitcnt lgkmcnt(4)\n\t" \
  "v_mfma_f32_16x16x32_bf16 v[76:79], a[128:131], v[128:131], v[76:79]\n\t" \
  "buffer_load_dwordx4 v[220:223], v234, %[ws], s78 offen sc1\n\t" \
  "v_mfma_f32_16x16x32_bf16 v[76:79], a[136:139], v[132:135], v[76:79]\n\t" \
  "ds_read_b128 v[160:163], v242 offset:53248\n\t" \
  "v_mfma_f32_16x16x32_bf16 v[76:79], a[144:147], v[136:139], v[76:79]\n\t" \
  "ds_read_b128 v[164:167], v242 offset:54272\n\t" \
  "v_mfma_f32_16x16x32_bf16 v[76:79], a[152:155], v[140:143], v[76:79]\n\t" \
  "ds_read_b128 v[168:171], v242 offset:55296\n\t" \
  "v_mfma_f32_16x16x32_bf16 v[92:95], a[156:159], v[140:143], v[92:95]\n\t" \
  "ds_read_b128 v[172:175], v242 offset:56320\n\t" \
  "v_mfma_f32_16x16x32_bf16 v[92:95], a[148:151], v[136:139], v[92:95]\n\t" \
  "buffer_load_dword v224, v235, %[sR], s80 offen\n\t" \
  "v_mfma_f32_16x16x32_bf16 v[92:95], a[140:143], v[132:135], v[92:95]\n\t" \
  "v_lshrrev_b32 v250, 4, v233\n\t" \
  "v_and_b32 v238, 15, v233\n\t" \
  "v_mfma_f32_16x16x32_bf16 v[92:95], a[132:135], v[128:131], v[92:95]\n\t" \
  "s_waitcnt lgkmcnt(4)\n\t" \
  "v_xor_b32 v26, v238, v250\n\t" \
  "v_lshlrev_b32 v26, 4, v26\n\t" \
  "v_mfma_f32_16x16x32_bf16 v[64:67], a[160:163], v[144:147], v[64:67]\n\t" \
  "ds_read_b128 v[128:131], v242 offset:57344\n\t" \
  "v_lshl_add_u32 v26, v250, 11, v26\n\t" \
  "v_mfma_f32_16x16x32_bf16 v[64:67], a[168:171], v[148:151], v[64:67]\n\t" \
  "v_xor_b32 v27, 64, v26\n\t" \
  "v_xor_b32 v28, 0x80, v26\n\t" \
  "v_mfma_f32_16x16x32_bf16 v[64:67], a[176:179], v[152:155], v[64:67]\n\t" \
  "buffer_store_dwordx4 v[8:11], v242, %[ws], s68 offen offset:2048 sc1\n\t" \
  "ds_read_b128 v[132:135], v242 offset:58368\n\t" \
  "v_xor_b32 v29, 0xc0, v26\n\t" \
  "v_lshrrev_b32 v30, 5, v233\n\t" \
  "v_mfma_f32_16x16x32_bf16 v[64:67], a[184:187], v[156:159], v[64:67]\n\t" \
  "v_lshl_add_u32 v30, %[wid], 2, v30\n\t" \
  "v_xor_b32 v30, v30, v238\n\t" \
  "v_mfma_f32_16x16x32_bf16 v[80:83], a[188:191], v[156:159], v[80:83]\n\t" \
  "ds_read_b128 v[136:139], v242 offset:59392\n\t" \
  "v_lshlrev_b32 v30, 4, v30\n\t" \
  "v_mfma_f32_16x16x32_bf16 v[80:83], a[180:183], v[152:155], v[80:83]\n\t" \
  "v_lshl_add_u32 v30, v238, 8, v30\n\t" \
  "v_and_b32 v250, 1, v250\n\t" \
  "v_mfma_f32_16x16x32_bf16 v[80:83], a[172:175], v[148:151], v[80:83]\n\t" \
  "ds_read_b128 v[140:143], v242 offset:60416\n\t" \
  "v_lshl_add_u32 v30, v250, 3, v30\n\t" \
  "v_mfma_f32_16x16x32_bf16 v[80:83], a[164:167], v[144:147], v[80:83]\n\t" \
  "s_waitcnt lgkmcnt(4)\n\t" \
  "v_add_u32 v238, 0, v30\n\t" \
  "v_xor_b32 v239, 32, v238\n\t" \
  "v_mfma_f32_16x16x32_bf16 v[68:71], a[160:163], v[160:163], v[68:71]\n\t" \
  "ds_read_b128 v[144:147], v242 offset:61440\n\t" \
  "v_bfe_u32 v237, v233, 1, 2\n\t" \
  "v_mfma_f32_16x16x32_bf16 v[68:71], a[168:171], v[164:167], v[68:71]\n\t" \
  "v_bfe_u32 v16, v233, 3, 1\n\t" \
  "v_lshlrev_b32 v237, 4, v237\n\t" \
  "v_mfma_f32_16x16x32_bf16 v[68:71], a[176:179], v[168:171], v[68:71]\n\t" \
  "s_add_u32 s97, s64, 786432\n\t" \
  "ds_read_b128 v[148:151], v242 offset:62464\n\t" \
  "v_lshl_add_u32 v237, v16, 3, v237\n\t" \
  "v_mfma_f32_16x16x32_bf16 v[68:71], a[184:187], v[172:175], v[68:71]\n\t" \
  "s_and_b32 s97, s97, 0xe0000\n\t" \
  "v_and_b32 v16, 1, v233\n\t" \
  "v_lshl_add_u32 v237, v16, 2, v237\n\t" \
  "v_mfma_f32_16x16x32_bf16 v[84:87], a[188:191], v[172:175], v[84:87]\n\t" \
  "s_add_u32 s97, s97, s62\n\t" \
  "ds_read_b128 v[152:155], v242 offset:63488\n\t" \
  "v_add_u32 v237, s76, v237\n\t" \
  "v_mfma_f32_16x16x32_bf16 v[84:87], a[180:183], v[168:171], v[84:87]\n\t" \
  "s_add_u32 m0, %[m], 65536\n\t" \
  "v_cmp_eq_u32 vcc, 2, v236\n\t" \
  "v_mov_b32 v16, 0\n\t" \
  "v_mfma_f32_16x16x32_bf16 v[84:87], a[172:175], v[164:167], v[84:87]\n\t" \
  "s_waitcnt vmcnt(13) lgkmcnt(3)\n\t" \
  "ds_read_b128 v[156:159], v242 offset:64512\n\t" \
  "v_mov_b32 v17, 0\n\t" \
  "v_mfma_f32_16x16x32_bf16 v[84:87], a[164:167], v[160:163], v[84:87]\n\t" \
  "s_barrier\n\t" \
  "v_mfma_f32_16x16x32_bf16 v[72:75], a[160:163], v[128:131], v[72:75]\n\t" \
  "buffer_load_dwordx4 v242, %[ws], s97 offen offset:0 sc1 lds\n\t" \
  "v_mfma_f32_16x16x32_bf16 v[72:75], a[168:171], v[132:135], v[72:75]\n\t" \
  "ds_read_b128 v[160:163], v230 offset:32768\n\t" \
  "v_mov_b32 v18, 0\n\t" \
  "v_mfma_f32_16x16x32_bf16 v[72:75], a[176:179], v[136:139], v[72:75]\n\t" \
  "ds_read_b128 v[164:167], v230 offset:33792\n\t" \
  "v_mov_b32 v19, 0\n\t" \
  "v_mfma_f32_16x16x32_bf16 v[72:75], a[184:187], v[140:143], v[72:75]\n\t" \
  "buffer_load_dwordx4 v242, %[ws], s97 offen offset:1024 sc1 lds\n\t" \
  "v_mfma_f32_16x16x32_bf16 v[88:91], a[188:191], v[140:143], v[88:91]\n\t" \
  "ds_read_b128 v[168:171], v230 offset:34816\n\t" \
  "v_cndmask_b32 v236, 0, 1.0, vcc\n\t" \
  "v_mfma_f32_16x16x32_bf16 v[88:91], a[180:183], v[136:139], v[88:91]\n\t" \
  "v_mov_b32 v20, 0x3f803f80\n\t" \
  "v_add_u32 v21, s76, v242\n\t" \
  "v_mfma_f32_16x16x32_bf16 v[88:91], a[172:175], v[132:135], v[88:91]\n\t" \
  "ds_write_b128 v21, v[16:19] offset:0\n\t" \
  "v_mfma_f32_16x16x32_bf16 v[88:91], a[164:167], v[128:131], v[88:91]\n\t" \
  "s_waitcnt lgkmcnt(4)\n\t" \
  "buffer_load_dwordx4 v242, %[ws], s97 offen offset:2048 sc1 lds\n\t" \
  "v_mfma_f32_16x16x32_bf16 v[76:79], a[160:163], v[144:147], v[76:79]\n\t" \
  "ds_read_b128 v[172:175], v230 offset:35840\n\t" \
  "v_mfma_f32_16x16x32_bf16 v[76:79], a[168:171], v[148:151], v[76:79]\n\t" \
  "ds_write_b128 v21, v[16:19] offset:1024\n\t" \
  "v_mfma_f32_16x16x32_bf16 v[76:79], a[176:179], v[152:155], v[76:79]\n\t" \
  "buffer_load_dwordx4 v242, %[ws], s97 offen offset:3072 sc1 lds\n\t" \
  "v_mfma_f32_16x16x32_bf16 v[76:79], a[184:187], v[156:159], v[76:79]\n\t" \
  "ds_read_b128 v[128:131], v230 offset:36864\n\t" \
  "v_mfma_f32_16x16x32_bf16 v[92:95], a[188:191], v[156:159], v[92:95]\n\t" \
  "ds_read_b128 v[132:135], v230 offset:37888\n\t" \
  "v_mfma_f32_16x16x32_bf16 v[92:95], a[180:183], v[152:155], v[92:95]\n\t" \
  "ds_write_b128 v21, v[16:19] offset:1536\n\t" \
  "v_mfma_f32_16x16x32_bf16 v[92:95], a[172:175], v[148:151], v[92:95]\n\t" \
  "buffer_store_dwordx4 v[12:15], v242, %[ws], s68 offen offset:3072 sc1\n\t" \
  "ds_read_b128 v[136:139], v230 offset:38912\n\t" \
  "ds_read_b128 v[140:143], v230 offset:39936\n\t" \
  "v_mfma_f32_16x16x32_bf16 v[92:95], a[164:167], v[144:147], v[92:95]\n\t" \
  "s_waitcnt lgkmcnt(6)\n\t" \
  "s_mov_b64 exec, 0xffff\n\t" \
  "ds_write_b32 v237, v20 offset:960\n\t" \
  "s_mov_b64 exec, -1\n\t" \
  "v_mfma_f32_16x16x32_bf16 v[64:67], a[0:3], v[160:163], v[64:67]\n\t" \
  "ds_read_b128 v[144:147], v230 offset:40960\n\t" \
  "v_mfma_f32_16x16x32_bf16 v[64:67], a[8:11], v[164:167], v[64:67]\n\t" \
  "ds_read_b128 v[148:151], v230 offset:41984\n\t" \
  "v_mfma_f32_16x16x32_bf16 v[64:67], a[16:19], v[168:171], v[64:67]\n\t" \
  "ds_read_b128 v[152:155], v230 offset:43008\n\t" \
  "v_mfma_f32_16x16x32_bf16 v[64:67], a[24:27], v[172:175], v[64:67]\n\t" \
  "ds_read_b128 v[156:159], v230 offset:44032\n\t" \
  "v_mfma_f32_16x16x32_bf16 v[80:83], a[28:31], v[172:175], v[80:83]\n\t" \
  "v_mfma_f32_16x16x32_bf16 v[80:83], a[20:23], v[168:171], v[80:83]\n\t" \
  "s_add_u32 m0, %[m], 0\n\t" \
  "s_add_u32 s96, %[xso], 0\n\t" \
  "v_mfma_f32_16x16x32_bf16 v[80:83], a[12:15], v[164:167], v[80:83]\n\t" \
  "v_mfma_f32_16x16x32_bf16 v[80:83], a[4:7], v[160:163], v[80:83]\n\t" \
  "s_waitcnt lgkmcnt(5)\n\t" \
  "v_mfma_f32_16x16x32_bf16 v[68:71], a[0:3], v[128:131], v[68:71]\n\t" \
  "ds_read_b128 v[160:163], v230 offset:45056\n\t" \
  "v_mfma_f32_16x16x32_bf16 v[68:71], a[8:11], v[132:135], v[68:71]\n\t" \
  "ds_read_b128 v[164:167], v230 offset:46080\n\t" \
  "v_mfma_f32_16x16x32_bf16 v[68:71], a[16:19], v[136:139], v[68:71]\n\t" \
  "ds_read_b128 v[168:171], v230 offset:47104\n\t" \
  "v_mfma_f32_16x16x32_bf16 v[68:71], a[24:27], v[140:143], v[68:71]\n\t" \
  "buffer_load_dwordx4 v26, %[sX], s96 offen offset:0 lds\n\t" \
  "v_mfma_f32_16x16x32_bf16 v[84:87], a[28:31], v[140:143], v[84:87]\n\t" \
  "s_add_u32 s96, %[xso], 7168\n\t" \
  "ds_read_b128 v[172:175], v230 offset:48128\n\t" \
  "v_mfma_f32_16x16x32_bf16 v[84:87], a[20:23], v[136:139], v[84:87]\n\t" \
  "v_mfma_f32_16x16x32_bf16 v[84:87], a[12:15], v[132:135], v[84:87]\n\t" \
  "v_mfma_f32_16x16x32_bf16 v[84:87], a[4:7], v[128:131], v[84:87]\n\t" \
  "s_waitcnt lgkmcnt(4)\n\t" \
  "v_mfma_f32_16x16x32_bf16 v[72:75], a[0:3], v[144:147], v[72:75]\n\t" \
  "buffer_load_dwordx4 v27, %[sX], s96 offen offset:1024 lds\n\t" \
  "v_mfma_f32_16x16x32_bf16 v[72:75], a[8:11], v[148:151], v[72:75]\n\t" \
  "s_add_u32 s96, %[xso], 14336\n\t" \
  "ds_read_b128 v[128:131], v230 offset:16384\n\t" \
  "v_mfma_f32_16x16x32_bf16 v[72:75], a[16:19], v[152:155], v[72:75]\n\t" \
  "ds_read_b128 v[132:135], v230 offset:17408\n\t" \
  "v_mfma_f32_16x16x32_bf16 v[72:75], a[24:27], v[156:159], v[72:75]\n\t" \
  "ds_read_b128 v[136:139], v230 offset:18432\n\t" \
  "v_mfma_f32_16x16x32_bf16 v[88:91], a[28:31], v[156:159], v[88:91]\n\t" \
  "ds_read_b128 v[140:143], v230 offset:19456\n\t" \
  "v_mfma_f32_16x16x32_bf16 v[88:91], a[20:23], v[152:155], v[88:91]\n\t" \
  "buffer_load_dwordx4 v28, %[sX], s96 offen offset:2048 lds\n\t" \
  "v_mfma_f32_16x16x32_bf16 v[88:91], a[12:15], v[148:151], v[88:91]\n\t" \
  "s_add_u32 s96, %[xso], 21504\n\t" \
  "v_mfma_f32_16x16x32_bf16 v[88:91], a[4:7], v[144:147], v[88:91]\n\t" \
  "s_waitcnt lgkmcnt(4)\n\t" \
  "v_mfma_f32_16x16x32_bf16 v[76:79], a[0:3], v[160:163], v[76:79]\n\t" \
  "ds_read_b128 v[144:147], v230 offset:20480\n\t" \
  "v_mfma_f32_16x16x32_bf16 v[76:79], a[8:11], v[164:167], v[76:79]\n\t" \
  "ds_read_b128 v[148:151], v230 offset:21504\n\t" \
  "v_mfma_f32_16x16x32_bf16 v[76:79], a[16:19], v[168:171], v[76:79]\n\t" \
  "buffer_load_dwordx4 v29, %[sX], s96 offen offset:3072 lds\n\t" \
  "v_mfma_f32_16x16x32_bf16 v[76:79], a[24:27], v[172:175], v[76:79]\n\t" \
  "s_add_u32 s97, s64, 917504\n\t" \
  "ds_read_b128 v[152:155], v230 offset:22528\n\t" \
  "s_and_b32 s97, s97, 0xe0000\n\t" \
  "v_mfma_f32_16x16x32_bf16 v[92:95], a[28:31], v[172:175], v[92:95]\n\t" \
  "s_add_u32 s97, s97, s62\n\t" \
  "ds_read_b128 v[156:159], v230 offset:23552\n\t" \
  "s_add_u32 m0, %[m], 16384\n\t" \
  "v_mfma_f32_16x16x32_bf16 v[92:95], a[20:23], v[168:171], v[92:95]\n\t" \
  ".Lhs13:\n\ts_cbranch_execz .Lhs13n\n\t" \
  "v_mfma_f32_16x16x32_bf16 v[92:95], a[12:15], v[164:167], v[92:95]\n\t" \
  "s_waitcnt vmcnt(10)\n\t" \
  "v_xor_b32 v246, v243, v209\n\t" \
  "v_bitop3_b32 v246, v246, v211, v243 bitop3:0xf6\n\t" \
  "v_mfma_f32_16x16x32_bf16 v[92:95], a[4:7], v[160:163], v[92:95]\n\t" \
  "s_waitcnt lgkmcnt(4)\n\t" \
  "v_bitop3_b32 v246, v246, v213, v243 bitop3:0xf6\n\t" \
  "v_bitop3_b32 v246, v246, v215, v243 bitop3:0xf6\n\t" \
  "v_mfma_f32_16x16x32_bf16 v[64:67], a[32:35], v[128:131], v[64:67]\n\t" \
  "v_bitop3_b32 v246, v246, v217, v243 bitop3:0xf6\n\t" \
  "v_bitop3_b32 v246, v246, v219, v243 bitop3:0xf6\n\t" \
  "v_mfma_f32_16x16x32_bf16 v[64:67], a[40:43], v[132:135], v[64:67]\n\t" \
  "v_bitop3_b32 v246, v246, v221, v243 bitop3:0xf6\n\t" \
  "v_bitop3_b32 v246, v246, v223, v243 bitop3:0xf6\n\t" \
  "v_mfma_f32_16x16x32_bf16 v[64:67], a[48:51], v[136:139], v[64:67]\n\t" \
  "buffer_load_dwordx4 v242, %[ws], s97 offen offset:0 sc1 lds\n\t" \
  "v_mfma_f32_16x16x32_bf16 v[64:67], a[56:59], v[140:143], v[64:67]\n\t" \
  "ds_read_b128 v[160:163], v230 offset:24576\n\t" \
  "v_cmp_ne_u32 vcc, 0, v246\n\t" \
  "v_mfma_f32_16x16x32_bf16 v[80:83], a[60:63], v[140:143], v[80:83]\n\t" \
  "buffer_load_dwordx4 v242, %[ws], s97 offen offset:1024 sc1 lds\n\t" \
  "v_mfma_f32_16x16x32_bf16 v[80:83], a[52:55], v[136:139], v[80:83]\n\t" \
  "s_cbranch_vccnz .Lat_s3\n\t" \
  ".Lat_k3:\n\t" \
  "v_mfma_f32_16x16x32_bf16 v[80:83], a[44:47], v[132:135], v[80:83]\n\t" \
  "ds_read_b128 v[164:167], v230 offset:25600\n\t" \
  "v_add_f32 v208, v208, v212\n\t" \
  "v_mfma_f32_16x16x32_bf16 v[80:83], a[36:39], v[128:131], v[80:83]\n\t" \
  "s_waitcnt lgkmcnt(2)\n\t" \
  "buffer_load_dwordx4 v242, %[ws], s97 offen offset:2048 sc1 lds\n\t" \
  "v_mfma_f32_16x16x32_bf16 v[68:71], a[32:35], v[144:147], v[68:71]\n\t" \
  "ds_read_b128 v[168:171], v230 offset:26624\n\t" \
  "v_add_f32 v216, v216, v220\n\t" \
  "v_mfma_f32_16x16x32_bf16 v[68:71], a[40:43], v[148:151], v[68:71]\n\t" \
  "ds_read_b128 v[172:175], v230 offset:27648\n\t" \
  "v_add_f32 v210, v210, v214\n\t" \
  "v_mfma_f32_16x16x32_bf16 v[68:71], a[48:51], v[152:155], v[68:71]\n\t" \
  "ds_read_b128 v[128:131], v230 offset:28672\n\t" \
  "v_add_f32 v218, v218, v222\n\t" \
  "v_mfma_f32_16x16x32_bf16 v[68:71], a[56:59], v[156:159], v[68:71]\n\t" \
  "buffer_load_dwordx4 v242, %[ws], s97 offen offset:3072 sc1 lds\n\t" \
  "v_mfma_f32_16x16x32_bf16 v[84:87], a[60:63], v[156:159], v[84:87]\n\t" \
  "s_add_u32 s97, s64, 131072\n\t" \
  "s_and_b32 s97, s97, 0xe0000\n\t" \
  "s_add_u32 s97, s97, s63\n\t" \
  "v_mfma_f32_16x16x32_bf16 v[84:87], a[52:55], v[152:155], v[84:87]\n\t" \
  "s_add_u32 m0, %[m], 32768\n\t" \
  "ds_read_b128 v[132:135], v230 offset:29696\n\t" \
  "v_add_f32 v208, v208, v216\n\t" \
  "v_mfma_f32_16x16x32_bf16 v[84:87], a[44:47], v[148:151], v[84:87]\n\t" \
  "s_waitcnt vmcnt(9) lgkmcnt(2)\n\t" \
  "buffer_load_dwordx4 v242, %[ws], s97 offen offset:0 sc1 lds\n\t" \
  "v_mfma_f32_16x16x32_bf16 v[84:87], a[36:39], v[144:147], v[84:87]\n\t" \
  "s_barrier\n\t" \
  "v_mfma_f32_16x16x32_bf16 v[72:75], a[32:35], v[160:163], v[72:75]\n\t" \
  "ds_read_b128 v[136:139], v230 offset:30720\n\t" \
  "v_add_f32 v210, v210, v218\n\t" \
  "v_mfma_f32_16x16x32_bf16 v[72:75], a[40:43], v[164:167], v[72:75]\n\t" \
  "ds_read_b128 v[140:143], v230 offset:31744\n\t" \
  "v_lshlrev_b32 v19, 16, v224\n\t" \
  "v_mfma_f32_16x16x32_bf16 v[72:75], a[48:51], v[168:171], v[72:75]\n\t" \
  "ds_read_b128 v[144:147], v232 offset:0\n\t" \
  "v_and_b32 v20, 0xffff0000, v224\n\t" \
  "v_mfma_f32_16x16x32_bf16 v[72:75], a[56:59], v[172:175], v[72:75]\n\t" \
  "buffer_load_dwordx4 v242, %[ws], s97 offen offset:1024 sc1 lds\n\t" \
  "v_mfma_f32_16x16x32_bf16 v[88:91], a[60:63], v[172:175], v[88:91]\n\t" \
  "ds_read_b128 v[148:151], v232 offset:1024\n\t" \
  "v_cvt_pk_bf16_f32 v18, v208, v210\n\t" \
  "v_mfma_f32_16x16x32_bf16 v[88:91], a[52:55], v[168:171], v[88:91]\n\t" \
  "ds_read_b128 v[152:155], v232 offset:2048\n\t" \
  "v_lshlrev_b32 v16, 16, v18\n\t" \
  "v_mfma_f32_16x16x32_bf16 v[88:91], a[44:47], v[164:167], v[88:91]\n\t" \
  "ds_read_b128 v[156:159], v232 offset:3072\n\t" \
  "v_and_b32 v17, 0xffff0000, v18\n\t" \
  "v_mfma_f32_16x16x32_bf16 v[88:91], a[36:39], v[160:163], v[88:91]\n\t" \
  "s_waitcnt lgkmcnt(4)\n\t" \
  "v_add_f32 v16, v16, v236\n\t" \
  "v_add_f32 v17, v17, v236\n\t" \
  "v_mfma_f32_16x16x32_bf16 v[76:79], a[32:35], v[128:131], v[76:79]\n\t" \
  "ds_read_b128 v[160:163], v232 offset:4096\n\t" \
  "v_cvt_pk_bf16_f32 v18, v16, v17\n\t" \
  "v_mfma_f32_16x16x32_bf16 v[76:79], a[40:43], v[132:135], v[76:79]\n\t" \
  "v_lshlrev_b32 v16, 16, v18\n\t" \
  "v_and_b32 v17, 0xffff0000, v18\n\t" \
  "v_mfma_f32_16x16x32_bf16 v[76:79], a[48:51], v[136:139], v[76:79]\n\t" \
  "ds_read_b128 v[164:167], v232 offset:5120\n\t" \
  "v_mfma_f32_16x16x32_bf16 v[76:79], a[56:59], v[140:143], v[76:79]\n\t" \
  "ds_write_b64 v240, v[16:17] offset:2560\n\t" \
  "v_mfma_f32_16x16x32_bf16 v[92:95], a[60:63], v[140:143], v[92:95]\n\t" \
  "buffer_load_dwordx4 v242, %[ws], s97 offen offset:2048 sc1 lds\n\t" \
  "v_mfma_f32_16x16x32_bf16 v[92:95], a[52:55], v[136:139], v[92:95]\n\t" \
  "ds_read_b128 v[168:171], v232 offset:6144\n\t" \
  "v_mul_f32 v22, v16, v19\n\t" \
  "v_mfma_f32_16x16x32_bf16 v[92:95], a[44:47], v[132:135], v[92:95]\n\t" \
  "ds_read_b128 v[172:175], v232 offset:7168\n\t" \
  "v_mul_f32 v23, v17, v20\n\t" \
  "v_mfma_f32_16x16x32_bf16 v[92:95], a[36:39], v[128:131], v[92:95]\n\t" \
  "s_waitcnt lgkmcnt(5)\n\t" \
  "v_cvt_pk_bf16_f32 v24, v22, v23\n\t" \
  "v_add_f32 v18, v22, v23\n\t" \
  "v_mfma_f32_16x16x32_bf16 v[64:67], a[64:67], v[144:147], v[64:67]\n\t" \
  "ds_read_b128 v[128:131], v232 offset:8192\n\t" \
  "v_lshlrev_b32 v19, 16, v24\n\t" \
  "v_mfma_f32_16x16x32_bf16 v[64:67], a[72:75], v[148:151], v[64:67]\n\t" \
  "s_waitcnt vmcnt(11)\n\t" \
  "buffer_store_dword v243, v244, %[ws], s72 offen offset:16 sc1\n\t" \
  "ds_read_b128 v[132:135], v232 offset:9216\n\t" \
  "v_mfma_f32_16x16x32_bf16 v[64:67], a[80:83], v[152:155], v[64:67]\n\t" \
  "v_and_b32 v20, 0xffff0000, v24\n\t" \
  "v_add_f32_dpp v18, v18, v18 row_ror:8 row_mask:0xf bank_mask:0xf\n\t" \
  "v_mfma_f32_16x16x32_bf16 v[64:67], a[88:91], v[156:159], v[64:67]\n\t" \
  "v_sub_f32 v22, v22, v19\n\t" \
  "v_sub_f32 v23, v23, v20\n\t" \
  "v_mfma_f32_16x16x32_bf16 v[80:83], a[92:95], v[156:159], v[80:83]\n\t" \
  "ds_read_b128 v[136:139], v232 offset:10240\n\t" \
  "v_add_f32_dpp v18, v18, v18 row_ror:4 row_mask:0xf bank_mask:0xf\n\t" \
  "v_mfma_f32_16x16x32_bf16 v[80:83], a[84:87], v[152:155], v[80:83]\n\t" \
  "ds_read_b128 v[140:143], v232 offset:11264\n\t" \
  "v_cvt_pk_bf16_f32 v25, v22, v23\n\t" \
  "v_mfma_f32_16x16x32_bf16 v[80:83], a[76:79], v[148:151], v[80:83]\n\t" \
  "s_mov_b32 s84, 0\n\t" \
  "v_add_f32_dpp v18, v18, v18 row_ror:2 row_mask:0xf bank_mask:0xf\n\t" \
  "s_mov_b32 s85, 0xffff\n\t" \
  "v_mfma_f32_16x16x32_bf16 v[80:83], a[68:71], v[144:147], v[80:83]\n\t" \
  "s_waitcnt lgkmcnt(4)\n\t" \
  "v_add_f32_dpp v18, v18, v18 row_ror:1 row_mask:0xf bank_mask:0xf\n\t" \
  "v_mfma_f32_16x16x32_bf16 v[68:71], a[64:67], v[160:163], v[68:71]\n\t" \
  "s_mov_b64 exec, s[84:85]\n\t" \
  "ds_write_b32 v237, v24 offset:1040\n\t" \
  "ds_write_b32 v237, v25 offset:1120\n\t" \
  "s_mov_b64 exec, -1\n\t" \
  "v_mfma_f32_16x16x32_bf16 v[68:71], a[72:75], v[164:167], v[68:71]\n\t" \
  "ds_read_b128 v[144:147], v232 offset:12288\n\t" \
  "v_readlane_b32 s82, v18, 32\n\t" \
  "v_mfma_f32_16x16x32_bf16 v[68:71], a[80:83], v[168:171], v[68:71]\n\t" \
  "ds_read_b128 v[148:151], v232 offset:13312\n\t" \
  "v_readlane_b32 s83, v18, 16\n\t" \
  "v_mfma_f32_16x16x32_bf16 v[68:71], a[88:91], v[172:175], v[68:71]\n\t" \
  "ds_read_b128 v[152:155], v232 offset:14336\n\t" \
  "v_and_b32 v235, 16, v242\n\t" \
  "v_mfma_f32_16x16x32_bf16 v[84:87], a[92:95], v[172:175], v[84:87]\n\t" \
  "buffer_load_dwordx4 v242, %[ws], s97 offen offset:3072 sc1 lds\n\t" \
  "v_mfma_f32_16x16x32_bf16 v[84:87], a[84:87], v[168:171], v[84:87]\n\t" \
  "s_add_u32 s97, s64, 262144\n\t" \
  "ds_read_b128 v[156:159], v232 offset:15360\n\t" \
  "s_and_b32 s97, s97, 0xe0000\n\t" \
  "v_mfma_f32_16x16x32_bf16 v[84:87], a[76:79], v[164:167], v[84:87]\n\t" \
  "s_add_u32 s97, s97, s63\n\t" \
  "s_add_u32 m0, %[m], 49152\n\t" \
  "v_mfma_f32_16x16x32_bf16 v[84:87], a[68:71], v[160:163], v[84:87]\n\t" \
  "s_waitcnt lgkmcnt(6)\n\t" \
  "v_mfma_f32_16x16x32_bf16 v[72:75], a[64:67], v[128:131], v[72:75]\n\t" \
  "ds_read_b128 v[160:163], v230 offset:0\n\t" \
  "v_mfma_f32_16x16x32_bf16 v[72:75], a[72:75], v[132:135], v[72:75]\n\t" \
  "buffer_load_dwordx4 v242, %[ws], s97 offen offset:0 sc1 lds\n\t" \
  "v_mfma_f32_16x16x32_bf16 v[72:75], a[80:83], v[136:139], v[72:75]\n\t" \
  "ds_read_b128 v[164:167], v230 offset:1024\n\t" \
  "v_mfma_f32_16x16x32_bf16 v[72:75], a[88:91], v[140:143], v[72:75]\n\t" \
  "ds_read_b128 v[168:171], v230 offset:2048\n\t" \
  "v_mfma_f32_16x16x32_bf16 v[88:91], a[92:95], v[140:143], v[88:91]\n\t" \
  "ds_read_b128 v[172:175], v230 offset:3072\n\t" \
  "v_mfma_f32_16x16x32_bf16 v[88:91], a[84:87], v[136:139], v[88:91]\n\t" \
  "v_mfma_f32_16x16x32_bf16 v[88:91], a[76:79], v[132:135], v[88:91]\n\t" \
  "buffer_load_dwordx4 v242, %[ws], s97 offen offset:1024 sc1 lds\n\t" \
  "v_mfma_f32_16x16x32_bf16 v[88:91], a[68:71], v[128:131], v[88:91]\n\t" \
  "s_waitcnt lgkmcnt(4)\n\t" \
  "v_mfma_f32_16x16x32_bf16 v[76:79], a[64:67], v[144:147], v[76:79]\n\t" \
  "ds_read_b128 v[128:131], v230 offset:4096\n\t" \
  "v_mfma_f32_16x16x32_bf16 v[76:79], a[72:75], v[148:151], v[76:79]\n\t" \
  "buffer_load_dwordx4 v242, %[ws], s97 offen offset:2048 sc1 lds\n\t" \
  "v_mfma_f32_16x16x32_bf16 v[76:79], a[80:83], v[152:155], v[76:79]\n\t" \
  "ds_read_b128 v[132:135], v230 offset:5120\n\t" \
  "v_mfma_f32_16x16x32_bf16 v[76:79], a[88:91], v[156:159], v[76:79]\n\t" \
  "ds_read_b128 v[136:139], v230 offset:6144\n\t" \
  "v_mfma_f32_16x16x32_bf16 v[92:95], a[92:95], v[156:159], v[92:95]\n\t" \
  "buffer_load_dwordx4 v242, %[ws], s97 offen offset:3072 sc1 lds\n\t" \
  "v_mfma_f32_16x16x32_bf16 v[92:95], a[84:87], v[152:155], v[92:95]\n\t" \
  "s_add_u32 s97, s64, 393216\n\t" \
  "s_and_b32 s97, s97, 0xe0000\n\t" \
  "s_add_u32 s97, s97, s63\n\t" \
  "v_mfma_f32_16x16x32_bf16 v[92:95], a[76:79], v[148:151], v[92:95]\n\t" \
  "s_add_u32 m0, %[m], 98304\n\t" \
  "ds_read_b128 v[140:143], v230 offset:7168\n\t" \
  "v_mfma_f32_16x16x32_bf16 v[92:95], a[68:71], v[144:147], v[92:95]\n\t" \
  "s_waitcnt lgkmcnt(4)\n\t" \
  "buffer_load_dwordx4 v242, %[ws], s97 offen offset:0 sc1 lds\n\t" \
  "v_mfma_f32_16x16x32_bf16 v[64:67], a[96:99], v[160:163], v[64:67]\n\t" \
  "ds_read_b128 v[144:147], v230 offset:8192\n\t" \
  "v_mfma_f32_16x16x32_bf16 v[64:67], a[104:107], v[164:167], v[64:67]\n\t" \
  "ds_read_b128 v[148:151], v230 offset:9216\n\t" \
  "v_mfma_f32_16x16x32_bf16 v[64:67], a[112:115], v[168:171], v[64:67]\n\t" \
  "buffer_load_dwordx4 v242, %[ws], s97 offen offset:1024 sc1 lds\n\t" \
  "v_mfma_f32_16x16x32_bf16 v[64:67], a[120:123], v[172:175], v[64:67]\n\t" \
  "ds_read_b128 v[152:155], v230 offset:10240\n\t" \
  "v_mfma_f32_16x16x32_bf16 v[80:83], a[124:127], v[172:175], v[80:83]\n\t" \
  "ds_read_b128 v[156:159], v230 offset:11264\n\t" \
  "v_mfma_f32_16x16x32_bf16 v[80:83], a[116:119], v[168:171], v[80:83]\n\t" \
  "v_mfma_f32_16x16x32_bf16 v[80:83], a[108:111], v[164:167], v[80:83]\n\t" \
  "v_mfma_f32_16x16x32_bf16 v[80:83], a[100:103], v[160:163], v[80:83]\n\t" \
  "s_waitcnt lgkmcnt(4)\n\t" \
  "v_mfma_f32_16x16x32_bf16 v[68:71], a[96:99], v[128:131], v[68:71]\n\t" \
  "ds_read_b128 v[160:163], v230 offset:12288\n\t" \
  "v_mfma_f32_16x16x32_bf16 v[68:71], a[104:107], v[132:135], v[68:71]\n\t" \
  "ds_read_b128 v[164:167], v230 offset:13312\n\t" \
  "v_mfma_f32_16x16x32_bf16 v[68:71], a[112:115], v[136:139], v[68:71]\n\t" \
  "buffer_load_dwordx4 v242, %[ws], s97 offen offset:2048 sc1 lds\n\t" \
  "v_mfma_f32_16x16x32_bf16 v[68:71], a[120:123], v[140:143], v[68:71]\n\t" \
  "ds_read_b128 v[168:171], v230 offset:14336\n\t" \
  "v_mfma_f32_16x16x32_bf16 v[84:87], a[124:127], v[140:143], v[84:87]\n\t" \
  "ds_read_b128 v[172:175], v230 offset:15360\n\t" \
  "v_mfma_f32_16x16x32_bf16 v[84:87], a[116:119], v[136:139], v[84:87]\n\t" \
  "v_mfma_f32_16x16x32_bf16 v[84:87], a[108:111], v[132:135], v[84:87]\n\t" \
  "s_waitcnt vmcnt(7) lgkmcnt(4)\n\t" \
  "buffer_load_dwordx4 v242, %[ws], s97 offen offset:3072 sc1 lds\n\t" \
  "v_mfma_f32_16x16x32_bf16 v[84:87], a[100:103], v[128:131], v[84:87]\n\t" \
  "s_barrier\n\t" \
  "v_mfma_f32_16x16x32_bf16 v[72:75], a[96:99], v[144:147], v[72:75]\n\t" \
  "s_add_u32 s97, s64, 524288\n\t" \
  "s_and_b32 s97, s97, 0xe0000\n\t" \
  "s_add_u32 s97, s97, s63\n\t" \
  "v_mfma_f32_16x16x32_bf16 v[72:75], a[104:107], v[148:151], v[72:75]\n\t" \
  "s_add_u32 m0, %[m], 81920\n\t" \
  "ds_read_b128 v[128:131], v242 offset:16384\n\t" \
  "v_mfma_f32_16x16x32_bf16 v[72:75], a[112:115], v[152:155], v[72:75]\n\t" \
  "buffer_load_dwordx4 v242, %[ws], s97 offen offset:0 sc1 lds\n\t" \
  "v_mfma_f32_16x16x32_bf16 v[72:75], a[120:123], v[156:159], v[72:75]\n\t" \
  "ds_read_b128 v[132:135], v242 offset:17408\n\t" \
  "v_mfma_f32_16x16x32_bf16 v[88:91], a[124:127], v[156:159], v[88:91]\n\t" \
  "buffer_load_dwordx4 v242, %[ws], s97 offen offset:1024 sc1 lds\n\t" \
  "v_mfma_f32_16x16x32_bf16 v[88:91], a[116:119], v[152:155], v[88:91]\n\t" \
  "ds_read_b128 v[136:139], v242 offset:18432\n\t" \
  "v_mfma_f32_16x16x32_bf16 v[88:91], a[108:111], v[148:151], v[88:91]\n\t" \
  "ds_read_b128 v[140:143], v242 offset:19456\n\t" \
  "v_mfma_f32_16x16x32_bf16 v[88:91], a[100:103], v[144:147], v[88:91]\n\t" \
  "s_waitcnt lgkmcnt(4)\n\t" \
  "buffer_load_dwordx4 v242, %[ws], s97 offen offset:2048 sc1 lds\n\t" \
  "v_mfma_f32_16x16x32_bf16 v[76:79], a[96:99], v[160:163], v[76:79]\n\t" \
  "ds_read_b128 v[144:147], v242 offset:20480\n\t" \
  "v_mfma_f32_16x16x32_bf16 v[76:79], a[104:107], v[164:167], v[76:79]\n\t" \
  "ds_read_b128 v[148:151], v242 offset:21504\n\t" \
  "v_mfma_f32_16x16x32_bf16 v[76:79], a[112:115], v[168:171], v[76:79]\n\t" \
  "buffer_load_dwordx4 v242, %[ws], s97 offen offset:3072 sc1 lds\n\t" \
  "v_mfma_f32_16x16x32_bf16 v[76:79], a[120:123], v[172:175], v[76:79]\n\t" \
  "s_add_u32 s97, s64, 655360\n\t" \
  "s_and_b32 s97, s97, 0xe0000\n\t" \
  "s_add_u32 s97, s97, s63\n\t" \
  "v_mfma_f32_16x16x32_bf16 v[92:95], a[124:127], v[172:175], v[92:95]\n\t" \
  "s_add_u32 m0, %[m], 131072\n\t" \
  "ds_read_b128 v[152:155], v242 offset:22528\n\t" \
  "v_mfma_f32_16x16x32_bf16 v[92:95], a[116:119], v[168:171], v[92:95]\n\t" \
  "buffer_load_dwordx4 v242, %[ws], s97 offen offset:0 sc1 lds\n\t" \
  "v_mfma_f32_16x16x32_bf16 v[92:95], a[108:111], v[164:167], v[92:95]\n\t" \
  "ds_read_b128 v[156:159], v242 offset:23552\n\t" \
  "v_mfma_f32_16x16x32_bf16 v[92:95], a[100:103], v[160:163], v[92:95]\n\t" \
  "s_waitcnt lgkmcnt(4)\n\t" \
  "v_mfma_f32_16x16x32_bf16 v[64:67], a[192:195], v[128:131], v[64:67]\n\t" \
  "buffer_load_dwordx4 v242, %[ws], s97 offen offset:1024 sc1 lds\n\t" \
  "v_mfma_f32_16x16x32_bf16 v[64:67], a[200:203], v[132:135], v[64:67]\n\t" \
  "ds_read_b128 v[160:163], v242 offset:24576\n\t" \
  "v_mfma_f32_16x16x32_bf16 v[64:67], a[208:211], v[136:139], v[64:67]\n\t" \
  "ds_read_b128 v[164:167], v242 offset:25600\n\t" \
  "v_mfma_f32_16x16x32_bf16 v[64:67], a[216:219], v[140:143], v[64:67]\n\t" \
  "buffer_load_dwordx4 v242, %[ws], s97 offen offset:2048 sc1 lds\n\t" \
  "v_mfma_f32_16x16x32_bf16 v[80:83], a[220:223], v[140:143], v[80:83]\n\t" \
  "ds_read_b128 v[168:171], v242 offset:26624\n\t" \
  "v_mfma_f32_16x16x32_bf16 v[80:83], a[212:215], v[136:139], v[80:83]\n\t" \
  "ds_read_b128 v[172:175], v242 offset:27648\n\t" \
  "v_mfma_f32_16x16x32_bf16 v[80:83], a[204:207], v[132:135], v[80:83]\n\t" \
  "buffer_load_dwordx4 v242, %[ws], s97 offen offset:3072 sc1 lds\n\t" \
  "v_mfma_f32_16x16x32_bf16 v[80:83], a[196:199], v[128:131], v[80:83]\n\t" \
  "s_waitcnt lgkmcnt(4)\n\t" \
  "v_cvt_pk_bf16_f32 v0, v64, v65\n\t" \
  "v_cvt_pk_bf16_f32 v1, v66, v67\n\t" \
  "v_mfma_f32_16x16x32_bf16 v[68:71], a[192:195], v[144:147], v[68:71]\n\t" \
  "buffer_load_dword v246, v235, %[ws], s73 offen sc1\n\t" \
  "v_mfma_f32_16x16x32_bf16 v[68:71], a[200:203], v[148:151], v[68:71]\n\t" \
  "ds_read_b128 v[128:131], v242 offset:28672\n\t" \
  "v_mov_b32 v250, 0x3f80\n\t" \
  "v_mfma_f32_16x16x32_bf16 v[68:71], a[208:211], v[152:155], v[68:71]\n\t" \
  "v_cvt_pk_bf16_f32 v2, v80, v81\n\t" \
  "v_cvt_pk_bf16_f32 v3, v82, v83\n\t" \
  "v_mfma_f32_16x16x32_bf16 v[68:71], a[216:219], v[156:159], v[68:71]\n\t" \
  "ds_read_b128 v[132:135], v242 offset:29696\n\t" \
  "v_lshlrev_b32 v250, v235, v250\n\t" \
  "v_mfma_f32_16x16x32_bf16 v[84:87], a[220:223], v[156:159], v[84:87]\n\t" \
  "s_mov_b32 s84, 0x300003\n\t" \
  "ds_read_b128 v[136:139], v242 offset:30720\n\t" \
  "s_mov_b32 s85, 0x30000300\n\t" \
  "v_mfma_f32_16x16x32_bf16 v[84:87], a[212:215], v[152:155], v[84:87]\n\t" \
  "s_mov_b32 s86, 0xc0000c\n\t" \
  "s_mov_b32 s87, 0xc0000c00\n\t" \
  "v_cndmask_b32 v248, 0, v250, s[84:85]\n\t" \
  "v_mfma_f32_16x16x32_bf16 v[84:87], a[204:207], v[148:151], v[84:87]\n\t" \
  "ds_read_b128 v[140:143], v242 offset:31744\n\t" \
  "v_cndmask_b32 v249, 0, v250, s[86:87]\n\t" \
  "v_mfma_f32_16x16x32_bf16 v[84:87], a[196:199], v[144:147], v[84:87]\n\t" \
  "s_waitcnt lgkmcnt(4)\n\t" \
  "v_cvt_pk_bf16_f32 v4, v68, v69\n\t" \
  "v_cvt_pk_bf16_f32 v5, v70, v71\n\t" \
  "v_mfma_f32_16x16x32_bf16 v[72:75], a[192:195], v[160:163], v[72:75]\n\t" \
  "buffer_store_dwordx4 v[0:3], v242, %[ws], s69 offen offset:0 sc1\n\t" \
  "ds_read_b128 v[144:147], v242 offset:32768\n\t" \
  "v_and_b32 v236, 15, v233\n\t" \
  "v_lshrrev_b32 v250, 4, v233\n\t" \
  "v_mfma_f32_16x16x32_bf16 v[72:75], a[200:203], v[164:167], v[72:75]\n\t" \
  "v_cvt_pk_bf16_f32 v6, v84, v85\n\t" \
  "v_cvt_pk_bf16_f32 v7, v86, v87\n\t" \
  "v_mfma_f32_16x16x32_bf16 v[72:75], a[208:211], v[168:171], v[72:75]\n\t" \
  "ds_read_b128 v[148:151], v242 offset:33792\n\t" \
  "v_mul_u32_u24 v236, 80, v236\n\t" \
  "v_mfma_f32_16x16x32_bf16 v[72:75], a[216:219], v[172:175], v[72:75]\n\t" \
  "v_lshlrev_b32 v237, 4, v250\n\t" \
  "v_add_u32 v237, s76, v237\n\t" \
  "v_mfma_f32_16x16x32_bf16 v[88:91], a[220:223], v[172:175], v[88:91]\n\t" \
  "ds_read_b128 v[152:155], v242 offset:34816\n\t" \
  "v_add_u32 v236, v237, v236\n\t" \
  "v_mfma_f32_16x16x32_bf16 v[88:91], a[212:215], v[168:171], v[88:91]\n\t" \
  "s_add_u32 s78, s70, 4096\n\t" \
  "ds_read_b128 v[156:159], v242 offset:35840\n\t" \
  "s_add_u32 s79, s70, 16384\n\t" \
  "v_mfma_f32_16x16x32_bf16 v[88:91], a[204:207], v[164:167], v[88:91]\n\t" \
  "v_mfma_f32_16x16x32_bf16 v[88:91], a[196:199], v[160:163], v[88:91]\n\t" \
  "s_waitcnt lgkmcnt(4)\n\t" \
  "v_cvt_pk_bf16_f32 v8, v72, v73\n\t" \
  "v_cvt_pk_bf16_f32 v9, v74, v75\n\t" \
  "v_mfma_f32_16x16x32_bf16 v[76:79], a[192:195], v[128:131], v[76:79]\n\t" \
  "buffer_store_dwordx4 v[4:7], v242, %[ws], s69 offen offset:1024 sc1\n\t" \
  "ds_read_b128 v[160:163], v242 offset:36864\n\t" \
  "ds_read_b128 v[164:167], v242 offset:37888\n\t" \
  "ds_read_b128 v[168:171], v242 offset:38912\n\t" \
  "v_mfma_f32_16x16x32_bf16 v[76:79], a[200:203], v[132:135], v[76:79]\n\t" \
  "v_cvt_pk_bf16_f32 v10, v88, v89\n\t" \
  "v_cvt_pk_bf16_f32 v11, v90, v91\n\t" \
  "v_mfma_f32_16x16x32_bf16 v[76:79], a[208:211], v[136:139], v[76:79]\n\t" \
  "ds_read_b128 v[172:175], v242 offset:39936\n\t" \
  "v_mfma_f32_16x16x32_bf16 v[76:79], a[216:219], v[140:143], v[76:79]\n\t" \
  "v_mfma_f32_16x16x32_bf16 v[92:95], a[220:223], v[140:143], v[92:95]\n\t" \
  "v_mfma_f32_16x16x32_bf16 v[92:95], a[212:215], v[136:139], v[92:95]\n\t" \
  "v_mfma_f32_16x16x32_bf16 v[92:95], a[204:207], v[132:135], v[92:95]\n\t" \
  "v_mfma_f32_16x16x32_bf16 v[92:95], a[196:199], v[128:131], v[92:95]\n\t" \
  "s_waitcnt lgkmcnt(4)\n\t" \
  STP("s_memrealtime s[94:95]\n\t") \
  STP("s_memtime s[92:93]\n\t") \
  "v_mfma_f32_16x16x32_bf16 v[96:99], a[128:131], v[144:147], v[96:99]\n\t" \
  "buffer_store_dwordx4 v[8:11], v242, %[ws], s69 offen offset:2048 sc1\n\t" \
  "ds_read_b128 v[128:131], v242 offset:40960\n\t" \
  "v_cvt_pk_bf16_f32 v12, v76, v77\n\t" \
  "v_cvt_pk_bf16_f32 v13, v78, v79\n\t" \
  "v_mfma_f32_16x16x32_bf16 v[96:99], a[136:139], v[148:151], v[96:99]\n\t" \
  "ds_read_b64 v[208:209], v238 offset:0\n\t" \
  "v_cvt_pk_bf16_f32 v14, v92, v93\n\t" \
  "v_mfma_f32_16x16x32_bf16 v[96:99], a[144:147], v[152:155], v[96:99]\n\t" \
  "ds_read_b128 v[132:135], v242 offset:41984\n\t" \
  "v_cvt_pk_bf16_f32 v15, v94, v95\n\t" \
  "v_mfma_f32_16x16x32_bf16 v[96:99], a[152:155], v[156:159], v[96:99]\n\t" \
  "ds_read_b64 v[210:211], v238 offset:4096\n\t" \
  "v_mfma_f32_16x16x32_bf16 v[112:115], a[156:159], v[156:159], v[112:115]\n\t" \
  "ds_read_b128 v[136:139], v242 offset:43008\n\t" \
  "v_mfma_f32_16x16x32_bf16 v[112:115], a[148:151], v[152:155], v[112:115]\n\t" \
  "ds_read_b64 v[212:213], v238 offset:8192\n\t" \
  "v_mfma_f32_16x16x32_bf16 v[112:115], a[140:143], v[148:151], v[112:115]\n\t" \
  "ds_read_b128 v[140:143], v242 offset:44032\n\t" \
  "v_mfma_f32_16x16x32_bf16 v[112:115], a[132:135], v[144:147], v[112:115]\n\t" \
  "s_waitcnt lgkmcnt(7)\n\t" \
  "ds_read_b64 v[214:215], v238 offset:12288\n\t" \
  "v_mfma_f32_16x16x32_bf16 v[100:103], a[128:131], v[160:163], v[100:103]\n\t" \
  "buffer_store_dwordx4 v[12:15], v242, %[ws], s69 offen offset:3072 sc1\n\t" \
  "ds_read_b128 v[144:147], v242 offset:45056\n\t" \
  "ds_read_b64 v[216:217], v239 offset:0\n\t" \
  "ds_read_b128 v[148:151], v242 offset:46080\n\t" \
  "v_mfma_f32_16x16x32_bf16 v[100:103], a[136:139], v[164:167], v[100:103]\n\t" \
  "ds_read_b128 v[152:155], v242 offset:47104\n\t" \
  "v_mfma_f32_16x16x32_bf16 v[100:103], a[144:147], v[168:171], v[100:103]\n\t" \
  "ds_read_b64 v[218:219], v239 offset:4096\n\t" \
  "v_mfma_f32_16x16x32_bf16 v[100:103], a[152:155], v[172:175], v[100:103]\n\t" \
  "ds_read_b128 v[156:159], v242 offset:48128\n\t" \
  "v_mfma_f32_16x16x32_bf16 v[116:119], a[156:159], v[172:175], v[116:119]\n\t" \
  STP("v_writelane_b32 %[stv], s94, 36\n\t") \
  STP("v_writelane_b32 %[stv], s92, 37\n\t") \
  "ds_read_b64 v[220:221], v239 offset:8192\n\t" \
  "v_mfma_f32_16x16x32_bf16 v[116:119], a[148:151], v[168:171], v[116:119]\n\t" \
  "ds_read_b64 v[222:223], v239 offset:12288\n\t" \
  "v_mfma_f32_16x16x32_bf16 v[116:119], a[140:143], v[164:167], v[116:119]\n\t" \
  "s_waitcnt vmcnt(13) lgkmcnt(9)\n\t" \
  "s_waitcnt lgkmcnt(0)\n\t" \
  "v_lshlrev_b32 v0, 16, v208\n\t" \
  "v_mfma_f32_16x16x32_bf16 v[116:119], a[132:135], v[160:163], v[116:119]\n\t" \
  "s_barrier\n\t" \
  "v_mfma_f32_16x16x32_bf16 v[104:107], a[128:131], v[128:131], v[104:107]\n\t" \
  "ds_read_b128 v[160:163], v242 offset:49152\n\t" \
  "v_and_b32 v1, 0xffff0000, v208\n\t" \
  "v_mfma_f32_16x16x32_bf16 v[104:107], a[136:139], v[132:135], v[104:107]\n\t" \
  "v_lshlrev_b32 v2, 16, v209\n\t" \
  "v_and_b32 v3, 0xffff0000, v209\n\t" \
  "v_mfma_f32_16x16x32_bf16 v[104:107], a[144:147], v[136:139], v[104:107]\n\t" \
  "ds_read_b128 v[164:167], v242 offset:50176\n\t" \
  "v_lshlrev_b32 v4, 16, v210\n\t" \
  "v_mfma_f32_16x16x32_bf16 v[104:107], a[152:155], v[140:143], v[104:107]\n\t" \
  "v_and_b32 v5, 0xffff0000, v210\n\t" \
  "v_lshlrev_b32 v6, 16, v211\n\t" \
  "v_mfma_f32_16x16x32_bf16 v[120:123], a[156:159], v[140:143], v[120:123]\n\t" \
  "s_add_u32 s97, s64, 786432\n\t" \
  "ds_read_b128 v[168:171], v242 offset:51200\n\t" \
  "v_and_b32 v7, 0xffff0000, v211\n\t" \
  "v_mfma_f32_16x16x32_bf16 v[120:123], a[148:151], v[136:139], v[120:123]\n\t" \
  "s_and_b32 s97, s97, 0xe0000\n\t" \
  "v_lshlrev_b32 v8, 16, v212\n\t" \
  "v_and_b32 v9, 0xffff0000, v212\n\t" \
  "v_mfma_f32_16x16x32_bf16 v[120:123], a[140:143], v[132:135], v[120:123]\n\t" \
  "s_add_u32 s97, s97, s63\n\t" \
  "ds_read_b128 v[172:175], v242 offset:52224\n\t" \
  "v_lshlrev_b32 v10, 16, v213\n\t" \
  "v_mfma_f32_16x16x32_bf16 v[120:123], a[132:135], v[128:131], v[120:123]\n\t" \
  "s_add_u32 m0, %[m], 65536\n\t" \
  "v_and_b32 v11, 0xffff0000, v213\n\t" \
  "v_lshlrev_b32 v12, 16, v214\n\t" \
  "v_mfma_f32_16x16x32_bf16 v[108:111], a[128:131], v[144:147], v[108:111]\n\t" \
  "ds_read_b128 v[128:131], v242 offset:53248\n\t" \
  "v_and_b32 v13, 0xffff0000, v214\n\t" \
  "v_mfma_f32_16x16x32_bf16 v[108:111], a[136:139], v[148:151], v[108:111]\n\t" \
  "v_lshlrev_b32 v14, 16, v215\n\t" \
  "v_and_b32 v15, 0xffff0000, v215\n\t" \
  "v_mfma_f32_16x16x32_bf16 v[108:111], a[144:147], v[152:155], v[108:111]\n\t" \
  "buffer_load_dwordx4 v242, %[ws], s97 offen offset:0 sc1 lds\n\t" \
  "v_mfma_f32_16x16x32_bf16 v[108:111], a[152:155], v[156:159], v[108:111]\n\t" \
  "ds_read_b128 v[132:135], v242 offset:54272\n\t" \
  "v_lshlrev_b32 v16, 16, v216\n\t" \
  "v_mfma_f32_16x16x32_bf16 v[124:127], a[156:159], v[156:159], v[124:127]\n\t" \
  "ds_read_b128 v[136:139], v242 offset:55296\n\t" \
  "v_and_b32 v17, 0xffff0000, v216\n\t" \
  "v_mfma_f32_16x16x32_bf16 v[124:127], a[148:151], v[152:155], v[124:127]\n\t" \
  "buffer_load_dwordx4 v242, %[ws], s97 offen offset:1024 sc1 lds\n\t" \
  "v_mfma_f32_16x16x32_bf16 v[124:127], a[140:143], v[148:151], v[124:127]\n\t" \
  "ds_read_b128 v[140:143], v242 offset:56320\n\t" \
  "v_lshlrev_b32 v18, 16, v217\n\t" \
  "v_mfma_f32_16x16x32_bf16 v[124:127], a[132:135], v[144:147], v[124:127]\n\t" \
  "s_waitcnt lgkmcnt(4)\n\t" \
  "v_and_b32 v19, 0xffff0000, v217\n\t" \
  "v_lshlrev_b32 v20, 16, v218\n\t" \
  "v_mfma_f32_16x16x32_bf16 v[96:99], a[160:163], v[160:163], v[96:99]\n\t" \
  "ds_read_b128 v[144:147], v242 offset:57344\n\t" \
  "v_and_b32 v21, 0xffff0000, v218\n\t" \
  "v_mfma_f32_16x16x32_bf16 v[96:99], a[168:171], v[164:167], v[96:99]\n\t" \
  "buffer_load_dwordx4 v242, %[ws], s97 offen offset:2048 sc1 lds\n\t" \
  "v_mfma_f32_16x16x32_bf16 v[96:99], a[176:179], v[168:171], v[96:99]\n\t" \
  "ds_read_b128 v[148:151], v242 offset:58368\n\t" \
  "v_lshlrev_b32 v22, 16, v219\n\t" \
  "v_mfma_f32_16x16x32_bf16 v[96:99], a[184:187], v[172:175], v[96:99]\n\t" \
  "v_and_b32 v23, 0xffff0000, v219\n\t" \
  "v_lshlrev_b32 v24, 16, v220\n\t" \
  "v_mfma_f32_16x16x32_bf16 v[112:115], a[188:191], v[172:175], v[112:115]\n\t" \
  "ds_read_b128 v[152:155], v242 offset:59392\n\t" \
  "v_and_b32 v25, 0xffff0000, v220\n\t" \
  "v_mfma_f32_16x16x32_bf16 v[112:115], a[180:183], v[168:171], v[112:115]\n\t" \
  "buffer_load_dwordx4 v242, %[ws], s97 offen offset:3072 sc1 lds\n\t" \
  "v_mfma_f32_16x16x32_bf16 v[112:115], a[172:175], v[164:167], v[112:115]\n\t" \
  "s_add_u32 s97, s64, 917504\n\t" \
  "s_and_b32 s97, s97, 0xe0000\n\t" \
  "s_add_u32 s97, s97, s63\n\t" \
  "v_mfma_f32_16x16x32_bf16 v[112:115], a[164:167], v[160:163], v[112:115]\n\t" \
  "s_waitcnt lgkmcnt(3)\n\t" \
  "ds_read_b128 v[156:159], v242 offset:60416\n\t" \
  "s_add_u32 m0, %[m], 16384\n\t" \
  "v_mfma_f32_16x16x32_bf16 v[100:103], a[160:163], v[128:131], v[100:103]\n\t" \
  "buffer_load_dwordx4 v242, %[ws], s97 offen offset:0 sc1 lds\n\t" \
  "v_mfma_f32_16x16x32_bf16 v[100:103], a[168:171], v[132:135], v[100:103]\n\t" \
  "ds_read_b128 v[160:163], v242 offset:61440\n\t" \
  "v_lshlrev_b32 v26, 16, v221\n\t" \
  "v_mfma_f32_16x16x32_bf16 v[100:103], a[176:179], v[136:139], v[100:103]\n\t" \
  "ds_read_b128 v[164:167], v242 offset:62464\n\t" \
  "v_and_b32 v27, 0xffff0000, v221\n\t" \
  "v_mfma_f32_16x16x32_bf16 v[100:103], a[184:187], v[140:143], v[100:103]\n\t" \
  "v_lshlrev_b32 v28, 16, v222\n\t" \
  "v_and_b32 v29, 0xffff0000, v222\n\t" \
  "v_mfma_f32_16x16x32_bf16 v[116:119], a[188:191], v[140:143], v[116:119]\n\t" \
  "buffer_load_dwordx4 v242, %[ws], s97 offen offset:1024 sc1 lds\n\t" \
  "v_mfma_f32_16x16x32_bf16 v[116:119], a[180:183], v[136:139], v[116:119]\n\t" \
  "ds_read_b128 v[168:171], v242 offset:63488\n\t" \
  "v_lshlrev_b32 v30, 16, v223\n\t" \
  "v_mfma_f32_16x16x32_bf16 v[116:119], a[172:175], v[132:135], v[116:119]\n\t" \
  "ds_read_b128 v[172:175], v242 offset:64512\n\t" \
  "v_and_b32 v31, 0xffff0000, v223\n\t" \
  "v_mfma_f32_16x16x32_bf16 v[116:119], a[164:167], v[128:131], v[116:119]\n\t" \
  "s_waitcnt lgkmcnt(4)\n\t" \
  "v_mfma_f32_16x16x32_bf16 v[104:107], a[160:163], v[144:147], v[104:107]\n\t" \
  "buffer_load_dwordx4 v242, %[ws], s97 offen offset:2048 sc1 lds\n\t" \
  "v_mfma_f32_16x16x32_bf16 v[104:107], a[168:171], v[148:151], v[104:107]\n\t" \
  "s_waitcnt vmcnt(11)\n\t" \
  "v_cmp_ne_u32 vcc, %[tag], v246\n\t" \
  "v_mfma_f32_16x16x32_bf16 v[104:107], a[176:179], v[152:155], v[104:107]\n\t" \
  "ds_read_b128 v[128:131], v230 offset:32768\n\t" \
  "v_mfma_f32_16x16x32_bf16 v[104:107], a[184:187], v[156:159], v[104:107]\n\t" \
  "s_cbranch_vccnz .Lat_s4\n\t" \
  ".Lat_k4:\n\t" \
  "v_mfma_f32_16x16x32_bf16 v[120:123], a[188:191], v[156:159], v[120:123]\n\t" \
  "buffer_load_dwordx4 v[32:35], v242, %[ws], s70 offen offset:0 sc1\n\t" \
  "v_mfma_f32_16x16x32_bf16 v[120:123], a[180:183], v[152:155], v[120:123]\n\t" \
  "ds_read_b128 v[132:135], v230 offset:33792\n\t" \
  "v_mfma_f32_16x16x32_bf16 v[120:123], a[172:175], v[148:151], v[120:123]\n\t" \
  "ds_read_b128 v[136:139], v230 offset:34816\n\t" \
  "v_mfma_f32_16x16x32_bf16 v[120:123], a[164:167], v[144:147], v[120:123]\n\t" \
  "s_waitcnt lgkmcnt(3)\n\t" \
  "ds_read_b128 v[140:143], v230 offset:35840\n\t" \
  "v_mfma_f32_16x16x32_bf16 v[108:111], a[160:163], v[160:163], v[108:111]\n\t" \
  "buffer_load_dwordx4 v242, %[ws], s97 offen offset:3072 sc1 lds\n\t" \
  "v_mfma_f32_16x16x32_bf16 v[108:111], a[168:171], v[164:167], v[108:111]\n\t" \
  "ds_read_b128 v[144:147], v230 offset:36864\n\t" \
  "v_mfma_f32_16x16x32_bf16 v[108:111], a[176:179], v[168:171], v[108:111]\n\t" \
  "ds_read_b128 v[148:151], v230 offset:37888\n\t" \
  "v_mfma_f32_16x16x32_bf16 v[108:111], a[184:187], v[172:175], v[108:111]\n\t" \
  "ds_read_b128 v[152:155], v230 offset:38912\n\t" \
  "v_mfma_f32_16x16x32_bf16 v[124:127], a[188:191], v[172:175], v[124:127]\n\t" \
  "ds_read_b128 v[156:159], v230 offset:39936\n\t" \
  "v_mfma_f32_16x16x32_bf16 v[124:127], a[180:183], v[168:171], v[124:127]\n\t" \
  "buffer_load_dwordx4 v[36:39], v242, %[ws], s70 offen offset:1024 sc1\n\t" \
  "v_mfma_f32_16x16x32_bf16 v[124:127], a[172:175], v[164:167], v[124:127]\n\t" \
  "v_mfma_f32_16x16x32_bf16 v[124:127], a[164:167], v[160:163], v[124:127]\n\t" \
  "s_waitcnt lgkmcnt(4)\n\t" \
  "v_mfma_f32_16x16x32_bf16 v[96:99], a[0:3], v[128:131], v[96:99]\n\t" \
  "ds_read_b128 v[160:163], v230 offset:40960\n\t" \
  "v_mfma_f32_16x16x32_bf16 v[96:99], a[8:11], v[132:135], v[96:99]\n\t" \
  "ds_read_b128 v[164:167], v230 offset:41984\n\t" \
  "v_mfma_f32_16x16x32_bf16 v[96:99], a[16:19], v[136:139], v[96:99]\n\t" \
  "ds_read_b128 v[168:171], v230 offset:43008\n\t" \
  "v_mfma_f32_16x16x32_bf16 v[96:99], a[24:27], v[140:143], v[96:99]\n\t" \
  "ds_read_b128 v[172:175], v230 offset:44032\n\t" \
  "v_mfma_f32_16x16x32_bf16 v[112:115], a[28:31], v[140:143], v[112:115]\n\t" \
  "v_mfma_f32_16x16x32_bf16 v[112:115], a[20:23], v[136:139], v[112:115]\n\t" \
  "v_mfma_f32_16x16x32_bf16 v[112:115], a[12:15], v[132:135], v[112:115]\n\t" \
  "v_mfma_f32_16x16x32_bf16 v[112:115], a[4:7], v[128:131], v[112:115]\n\t" \
  "s_waitcnt lgkmcnt(4)\n\t" \
  "v_mfma_f32_16x16x32_bf16 v[100:103], a[0:3], v[144:147], v[100:103]\n\t" \
  "ds_read_b128 v[128:131], v230 offset:45056\n\t" \
  "v_mfma_f32_16x16x32_bf16 v[100:103], a[8:11], v[148:151], v[100:103]\n\t" \
  "ds_read_b128 v[132:135], v230 offset:46080\n\t" \
  "v_mfma_f32_16x16x32_bf16 v[100:103], a[16:19], v[152:155], v[100:103]\n\t" \
  "buffer_load_dwordx4 v[40:43], v242, %[ws], s70 offen offset:2048 sc1\n\t" \
  "v_mfma_f32_16x16x32_bf16 v[100:103], a[24:27], v[156:159], v[100:103]\n\t" \
  "ds_read_b128 v[136:139], v230 offset:47104\n\t" \
  "v_mfma_f32_16x16x32_bf16 v[116:119], a[28:31], v[156:159], v[116:119]\n\t" \
  "ds_read_b128 v[140:143], v230 offset:48128\n\t" \
  "v_mfma_f32_16x16x32_bf16 v[116:119], a[20:23], v[152:155], v[116:119]\n\t" \
  "buffer_load_dwordx4 v[44:47], v242, %[ws], s70 offen offset:3072 sc1\n\t" \
  "v_mfma_f32_16x16x32_bf16 v[116:119], a[12:15], v[148:151], v[116:119]\n\t" \
  "s_waitcnt lgkmcnt(4)\n\t" \
  "v_mfma_f32_16x16x32_bf16 v[116:119], a[4:7], v[144:147], v[116:119]\n\t" \
  "s_barrier\n\t" \
  "v_mfma_f32_16x16x32_bf16 v[104:107], a[0:3], v[160:163], v[104:107]\n\t" \
  "s_waitcnt vmcnt(12)\n\t" \
  "buffer_store_dword v243, v244, %[ws], s66 offen offset:32 sc1\n\t" \
  "ds_read_b128 v[144:147], v230 offset:16384\n\t" \
  "v_mfma_f32_16x16x32_bf16 v[104:107], a[8:11], v[164:167], v[104:107]\n\t" \
  "ds_read_b128 v[148:151], v230 offset:17408\n\t" \
  "v_mfma_f32_16x16x32_bf16 v[104:107], a[16:19], v[168:171], v[104:107]\n\t" \
  "buffer_load_dwordx4 v[48:51], v242, %[ws], s78 offen offset:0 sc1\n\t" \
  "v_mfma_f32_16x16x32_bf16 v[104:107], a[24:27], v[172:175], v[104:107]\n\t" \
  "ds_read_b128 v[152:155], v230 offset:18432\n\t" \
  "v_mfma_f32_16x16x32_bf16 v[120:123], a[28:31], v[172:175], v[120:123]\n\t" \
  "ds_read_b128 v[156:159], v230 offset:19456\n\t" \
  "v_mfma_f32_16x16x32_bf16 v[120:123], a[20:23], v[168:171], v[120:123]\n\t" \
  "buffer_load_dwordx4 v[52:55], v242, %[ws], s78 offen offset:1024 sc1\n\t" \
  "v_mfma_f32_16x16x32_bf16 v[120:123], a[12:15], v[164:167], v[120:123]\n\t" \
  ".Lhs14:\n\ts_cbranch_execz .Lhs14n\n\t" \
  "v_mfma_f32_16x16x32_bf16 v[120:123], a[4:7], v[160:163], v[120:123]\n\t" \
  "s_waitcnt lgkmcnt(4)\n\t" \
  "v_mfma_f32_16x16x32_bf16 v[108:111], a[0:3], v[128:131], v[108:111]\n\t" \
  "buffer_load_dwordx4 v[56:59], v242, %[ws], s78 offen offset:2048 sc1\n\t" \
  "v_mfma_f32_16x16x32_bf16 v[108:111], a[8:11], v[132:135], v[108:111]\n\t" \
  "ds_read_b128 v[160:163], v230 offset:20480\n\t" \
  "v_mfma_f32_16x16x32_bf16 v[108:111], a[16:19], v[136:139], v[108:111]\n\t" \
  "ds_read_b128 v[164:167], v230 offset:21504\n\t" \
  "v_mfma_f32_16x16x32_bf16 v[108:111], a[24:27], v[140:143], v[108:111]\n\t" \
  "ds_read_b128 v[168:171], v230 offset:22528\n\t" \
  "v_mfma_f32_16x16x32_bf16 v[124:127], a[28:31], v[140:143], v[124:127]\n\t" \
  "ds_read_b128 v[172:175], v230 offset:23552\n\t" \
  "v_mfma_f32_16x16x32_bf16 v[124:127], a[20:23], v[136:139], v[124:127]\n\t" \
  "buffer_load_dwordx4 v[60:63], v242, %[ws], s78 offen offset:3072 sc1\n\t" \
  "v_mfma_f32_16x16x32_bf16 v[124:127], a[12:15], v[132:135], v[124:127]\n\t" \
  "v_accvgpr_write_b32 a128, 0\n\t" \
  "v_accvgpr_write_b32 a129, 0\n\t" \
  "v_mfma_f32_16x16x32_bf16 v[124:127], a[4:7], v[128:131], v[124:127]\n\t" \
  "s_waitcnt lgkmcnt(4)\n\t" \
  "v_accvgpr_write_b32 a130, v248\n\t" \
  "v_accvgpr_write_b32 a131, v249\n\t" \
  "v_mfma_f32_16x16x32_bf16 v[96:99], a[32:35], v[144:147], v[96:99]\n\t" \
  "ds_read_b128 v[128:131], v230 offset:24576\n\t" \
  "v_accvgpr_write_b32 a132, 0\n\t" \
  "v_mfma_f32_16x16x32_bf16 v[96:99], a[40:43], v[148:151], v[96:99]\n\t" \
  "ds_read_b128 v[132:135], v230 offset:25600\n\t" \
  "v_accvgpr_write_b32 a133, 0\n\t" \
  "v_mfma_f32_16x16x32_bf16 v[96:99], a[48:51], v[152:155], v[96:99]\n\t" \
  "ds_read_b128 a[176:179], v236 offset:960\n\t" \
  "v_mfma_f32_16x16x32_bf16 v[96:99], a[56:59], v[156:159], v[96:99]\n\t" \
  "ds_read_b128 a[180:183], v236 offset:640\n\t" \
  "v_mfma_f32_16x16x32_bf16 v[112:115], a[60:63], v[156:159], v[112:115]\n\t" \
  "ds_read_b128 v[136:139], v230 offset:26624\n\t" \
  "v_mfma_f32_16x16x32_bf16 v[112:115], a[52:55], v[152:155], v[112:115]\n\t" \
  "ds_read_b128 a[184:187], v236 offset:320\n\t" \
  "v_mfma_f32_16x16x32_bf16 v[112:115], a[44:47], v[148:151], v[112:115]\n\t" \
  "ds_read_b128 v[140:143], v230 offset:27648\n\t" \
  "v_mfma_f32_16x16x32_bf16 v[112:115], a[36:39], v[144:147], v[112:115]\n\t" \
  "s_waitcnt lgkmcnt(7)\n\t" \
  "ds_read_b128 a[188:191], v236 offset:0\n\t" \
  "v_mfma_f32_16x16x32_bf16 v[100:103], a[32:35], v[160:163], v[100:103]\n\t" \
  "ds_read_b128 v[144:147], v230 offset:28672\n\t" \
  "v_mfma_f32_16x16x32_bf16 v[100:103], a[40:43], v[164:167], v[100:103]\n\t" \
  "ds_read_b128 v[148:151], v230 offset:29696\n\t" \
  "v_mfma_f32_16x16x32_bf16 v[100:103], a[48:51], v[168:171], v[100:103]\n\t" \
  "ds_read_b128 v[152:155], v230 offset:30720\n\t" \
  "v_mfma_f32_16x16x32_bf16 v[100:103], a[56:59], v[172:175], v[100:103]\n\t" \
  "ds_read_b128 v[156:159], v230 offset:31744\n\t" \
  "v_mfma_f32_16x16x32_bf16 v[116:119], a[60:63], v[172:175], v[116:119]\n\t" \
  "v_mfma_f32_16x16x32_bf16 v[116:119], a[52:55], v[168:171], v[116:119]\n\t" \
  "v_mfma_f32_16x16x32_bf16 v[116:119], a[44:47], v[164:167], v[116:119]\n\t" \
  "v_mfma_f32_16x16x32_bf16 v[116:119], a[36:39], v[160:163], v[116:119]\n\t" \
  "s_waitcnt lgkmcnt(5)\n\t" \
  "v_mfma_f32_16x16x32_bf16 v[104:107], a[32:35], v[128:131], v[104:107]\n\t" \
  "ds_read_b128 v[160:163], v232 offset:0\n\t" \
  "v_mfma_f32_16x16x32_bf16 v[104:107], a[40:43], v[132:135], v[104:107]\n\t" \
  "ds_read_b128 v[164:167], v232 offset:1024\n\t" \
  "v_mfma_f32_16x16x32_bf16 v[104:107], a[48:51], v[136:139], v[104:107]\n\t" \
  "ds_read_b128 v[168:171], v232 offset:2048\n\t" \
  "v_mfma_f32_16x16x32_bf16 v[104:107], a[56:59], v[140:143], v[104:107]\n\t" \
  "ds_read_b128 v[172:175], v232 offset:3072\n\t" \
  "v_mfma_f32_16x16x32_bf16 v[120:123], a[60:63], v[140:143], v[120:123]\n\t" \
  "v_mfma_f32_16x16x32_bf16 v[120:123], a[52:55], v[136:139], v[120:123]\n\t" \
  "v_mfma_f32_16x16x32_bf16 v[120:123], a[44:47], v[132:135], v[120:123]\n\t" \
  "v_mfma_f32_16x16x32_bf16 v[120:123], a[36:39], v[128:131], v[120:123]\n\t" \
  "s_waitcnt lgkmcnt(4)\n\t" \
  "v_mfma_f32_16x16x32_bf16 v[108:111], a[32:35], v[144:147], v[108:111]\n\t" \
  "ds_read_b128 v[128:131], v232 offset:4096\n\t" \
  "v_mfma_f32_16x16x32_bf16 v[108:111], a[40:43], v[148:151], v[108:111]\n\t" \
  "ds_read_b128 v[132:135], v232 offset:5120\n\t" \
  "v_mfma_f32_16x16x32_bf16 v[108:111], a[48:51], v[152:155], v[108:111]\n\t" \
  "ds_read_b128 v[136:139], v232 offset:6144\n\t" \
  "v_mfma_f32_16x16x32_bf16 v[108:111], a[56:59], v[156:159], v[108:111]\n\t" \
  "ds_read_b128 v[140:143], v232 offset:7168\n\t" \
  "v_mfma_f32_16x16x32_bf16 v[124:127], a[60:63], v[156:159], v[124:127]\n\t" \
  "buffer_load_dword v246, off, %[ws], s73 offset:32 sc1\n\t" \
  "v_mfma_f32_16x16x32_bf16 v[124:127], a[52:55], v[152:155], v[124:127]\n\t" \
  "v_mfma_f32_16x16x32_bf16 v[124:127], a[44:47], v[148:151], v[124:127]\n\t" \
  "v_mfma_f32_16x16x32_bf16 v[124:127], a[36:39], v[144:147], v[124:127]\n\t" \
  "s_waitcnt lgkmcnt(4)\n\t" \
  "v_mfma_f32_16x16x32_bf16 v[96:99], a[64:67], v[160:163], v[96:99]\n\t" \
  "ds_read_b128 v[144:147], v232 offset:8192\n\t" \
  "v_mfma_f32_16x16x32_bf16 v[96:99], a[72:75], v[164:167], v[96:99]\n\t" \
  "ds_read_b128 v[148:151], v232 offset:9216\n\t" \
  "v_mfma_f32_16x16x32_bf16 v[96:99], a[80:83], v[168:171], v[96:99]\n\t" \
  "ds_read_b128 v[152:155], v232 offset:10240\n\t" \
  "v_mfma_f32_16x16x32_bf16 v[96:99], a[88:91], v[172:175], v[96:99]\n\t" \
  "ds_read_b128 v[156:159], v232 offset:11264\n\t" \
  "v_mfma_f32_16x16x32_bf16 v[112:115], a[92:95], v[172:175], v[112:115]\n\t" \
  "v_mfma_f32_16x16x32_bf16 v[112:115], a[84:87], v[168:171], v[112:115]\n\t" \
  "v_mfma_f32_16x16x32_bf16 v[112:115], a[76:79], v[164:167], v[112:115]\n\t" \
  "v_mfma_f32_16x16x32_bf16 v[112:115], a[68:71], v[160:163], v[112:115]\n\t" \
  "s_waitcnt lgkmcnt(4)\n\t" \
  "v_mfma_f32_16x16x32_bf16 v[100:103], a[64:67], v[128:131], v[100:103]\n\t" \
  "ds_read_b128 v[160:163], v232 offset:12288\n\t" \
  "v_mfma_f32_16x16x32_bf16 v[100:103], a[72:75], v[132:135], v[100:103]\n\t" \
  "ds_read_b128 v[164:167], v232 offset:13312\n\t" \
  "v_mfma_f32_16x16x32_bf16 v[100:103], a[80:83], v[136:139], v[100:103]\n\t" \
  "ds_read_b128 v[168:171], v232 offset:14336\n\t" \
  "v_mfma_f32_16x16x32_bf16 v[100:103], a[88:91], v[140:143], v[100:103]\n\t" \
  "ds_read_b128 v[172:175], v232 offset:15360\n\t" \
  "v_mfma_f32_16x16x32_bf16 v[116:119], a[92:95], v[140:143], v[116:119]\n\t" \
  "v_mfma_f32_16x16x32_bf16 v[116:119], a[84:87], v[136:139], v[116:119]\n\t" \
  "v_mfma_f32_16x16x32_bf16 v[116:119], a[76:79], v[132:135], v[116:119]\n\t" \
  "s_waitcnt vmcnt(9) lgkmcnt(4)\n\t" \
  "v_mfma_f32_16x16x32_bf16 v[116:119], a[68:71], v[128:131], v[116:119]\n\t" \
  "s_barrier\n\t" \
  "v_mfma_f32_16x16x32_bf16 v[104:107], a[64:67], v[144:147], v[104:107]\n\t" \
  "ds_read_b128 v[128:131], v230 offset:0\n\t" \
  "v_mfma_f32_16x16x32_bf16 v[104:107], a[72:75], v[148:151], v[104:107]\n\t" \
  "ds_read_b128 v[132:135], v230 offset:1024\n\t" \
  "v_mfma_f32_16x16x32_bf16 v[104:107], a[80:83], v[152:155], v[104:107]\n\t" \
  "ds_read_b128 v[136:139], v230 offset:2048\n\t" \
  "v_mfma_f32_16x16x32_bf16 v[104:107], a[88:91], v[156:159], v[104:107]\n\t" \
  "ds_read_b128 v[140:143], v230 offset:3072\n\t" \
  "v_mfma_f32_16x16x32_bf16 v[120:123], a[92:95], v[156:159], v[120:123]\n\t" \
  "v_mfma_f32_16x16x32_bf16 v[120:123], a[84:87], v[152:155], v[120:123]\n\t" \
  "v_mfma_f32_16x16x32_bf16 v[120:123], a[76:79], v[148:151], v[120:123]\n\t" \
  "v_mfma_f32_16x16x32_bf16 v[120:123], a[68:71], v[144:147], v[120:123]\n\t" \
  "s_waitcnt lgkmcnt(4)\n\t" \
  "v_mfma_f32_16x16x32_bf16 v[108:111], a[64:67], v[160:163], v[108:111]\n\t" \
  "ds_read_b128 v[144:147], v230 offset:4096\n\t" \
  "v_mfma_f32_16x16x32_bf16 v[108:111], a[72:75], v[164:167], v[108:111]\n\t" \
  "ds_read_b128 v[148:151], v230 offset:5120\n\t" \
  "v_mfma_f32_16x16x32_bf16 v[108:111], a[80:83], v[168:171], v[108:111]\n\t" \
  "ds_read_b128 v[152:155], v230 offset:6144\n\t" \
  "v_mfma_f32_16x16x32_bf16 v[108:111], a[88:91], v[172:175], v[108:111]\n\t" \
  "buffer_load_dword v251, off, %[ws], s73 offset:32 sc1\n\t" \
  "v_mfma_f32_16x16x32_bf16 v[124:127], a[92:95], v[172:175], v[124:127]\n\t" \
  "ds_read_b128 v[156:159], v230 offset:7168\n\t" \
  "v_mfma_f32_16x16x32_bf16 v[124:127], a[84:87], v[168:171], v[124:127]\n\t" \
  "v_mfma_f32_16x16x32_bf16 v[124:127], a[76:79], v[164:167], v[124:127]\n\t" \
  "v_mfma_f32_16x16x32_bf16 v[124:127], a[68:71], v[160:163], v[124:127]\n\t" \
  "s_waitcnt lgkmcnt(4)\n\t" \
  "v_mfma_f32_16x16x32_bf16 v[96:99], a[96:99], v[128:131], v[96:99]\n\t" \
  "s_waitcnt vmcnt(1)\n\t" \
  "v_cmp_ne_u32 vcc, %[tag], v246\n\t" \
  "v_mfma_f32_16x16x32_bf16 v[96:99], a[104:107], v[132:135], v[96:99]\n\t" \
  "ds_read_b128 v[160:163], v230 offset:8192\n\t" \
  "v_mfma_f32_16x16x32_bf16 v[96:99], a[112:115], v[136:139], v[96:99]\n\t" \
  "ds_read_b128 v[164:167], v230 offset:9216\n\t" \
  "s_mov_b32 s88, vcc_lo\n\t" \
  "s_cbranch_vccnz .Lat_n2\n\t" \
  "buffer_load_dwordx4 v[64:67], v242, %[ws], s79 offen offset:0 sc1\n\t" \
  "buffer_load_dwordx4 v[68:71], v242, %[ws], s79 offen offset:1024 sc1\n\t" \
  "buffer_load_dwordx4 v[72:75], v242, %[ws], s79 offen offset:2048 sc1\n\t" \
  "buffer_load_dwordx4 v[76:79], v242, %[ws], s79 offen offset:3072 sc1\n\t" \
  ".Lat_n2:\n\t" \
  "v_mfma_f32_16x16x32_bf16 v[96:99], a[120:123], v[140:143], v[96:99]\n\t" \
  "ds_read_b128 v[168:171], v230 offset:10240\n\t" \
  "v_mfma_f32_16x16x32_bf16 v[112:115], a[124:127], v[140:143], v[112:115]\n\t" \
  "ds_read_b128 v[172:175], v230 offset:11264\n\t" \
  "v_mfma_f32_16x16x32_bf16 v[112:115], a[116:119], v[136:139], v[112:115]\n\t" \
  "v_mfma_f32_16x16x32_bf16 v[112:115], a[108:111], v[132:135], v[112:115]\n\t" \
  "v_mfma_f32_16x16x32_bf16 v[112:115], a[100:103], v[128:131], v[112:115]\n\t" \
  "s_waitcnt lgkmcnt(4)\n\t" \
  "v_mfma_f32_16x16x32_bf16 v[100:103], a[96:99], v[144:147], v[100:103]\n\t" \
  "ds_read_b128 v[128:131], v230 offset:12288\n\t" \
  "v_mfma_f32_16x16x32_bf16 v[100:103], a[104:107], v[148:151], v[100:103]\n\t" \
  "ds_read_b128 v[132:135], v230 offset:13312\n\t" \
  "v_mfma_f32_16x16x32_bf16 v[100:103], a[112:115], v[152:155], v[100:103]\n\t" \
  "ds_read_b128 v[136:139], v230 offset:14336\n\t" \
  "v_mfma_f32_16x16x32_bf16 v[100:103], a[120:123], v[156:159], v[100:103]\n\t" \
  "ds_read_b128 v[140:143], v230 offset:15360\n\t" \
  "v_mfma_f32_16x16x32_bf16 v[116:119], a[124:127], v[156:159], v[116:119]\n\t" \
  "v_mfma_f32_16x16x32_bf16 v[116:119], a[116:119], v[152:155], v[116:119]\n\t" \
  "v_mfma_f32_16x16x32_bf16 v[116:119], a[108:111], v[148:151], v[116:119]\n\t" \
  "v_mfma_f32_16x16x32_bf16 v[116:119], a[100:103], v[144:147], v[116:119]\n\t" \
  "s_waitcnt lgkmcnt(4)\n\t" \
  "v_mfma_f32_16x16x32_bf16 v[104:107], a[96:99], v[160:163], v[104:107]\n\t" \
  "ds_read_b128 v[144:147], v242 offset:16384\n\t" \
  "v_mfma_f32_16x16x32_bf16 v[104:107], a[104:107], v[164:167], v[104:107]\n\t" \
  "ds_read_b128 v[148:151], v242 offset:17408\n\t" \
  "v_mfma_f32_16x16x32_bf16 v[104:107], a[112:115], v[168:171], v[104:107]\n\t" \
  "ds_read_b128 v[152:155], v242 offset:18432\n\t" \
  "v_mfma_f32_16x16x32_bf16 v[104:107], a[120:123], v[172:175], v[104:107]\n\t" \
  "ds_read_b128 v[156:159], v242 offset:19456\n\t" \
  "v_mfma_f32_16x16x32_bf16 v[120:123], a[124:127], v[172:175], v[120:123]\n\t" \
  "v_mfma_f32_16x16x32_bf16 v[120:123], a[116:119], v[168:171], v[120:123]\n\t" \
  "v_mfma_f32_16x16x32_bf16 v[120:123], a[108:111], v[164:167], v[120:123]\n\t" \
  "v_mfma_f32_16x16x32_bf16 v[120:123], a[100:103], v[160:163], v[120:123]\n\t" \
  "s_waitcnt lgkmcnt(4)\n\t" \
  "v_mfma_f32_16x16x32_bf16 v[108:111], a[96:99], v[128:131], v[108:111]\n\t" \
  "ds_read_b128 v[160:163], v242 offset:20480\n\t" \
  "v_mfma_f32_16x16x32_bf16 v[108:111], a[104:107], v[132:135], v[108:111]\n\t" \
  "ds_read_b128 v[164:167], v242 offset:21504\n\t" \
  "v_mfma_f32_16x16x32_bf16 v[108:111], a[112:115], v[136:139], v[108:111]\n\t" \
  "ds_read_b128 v[168:171], v242 offset:22528\n\t" \
  "v_mfma_f32_16x16x32_bf16 v[108:111], a[120:123], v[140:143], v[108:111]\n\t" \
  "ds_read_b128 v[172:175], v242 offset:23552\n\t" \
  "v_mfma_f32_16x16x32_bf16 v[124:127], a[124:127], v[140:143], v[124:127]\n\t" \
  "v_mfma_f32_16x16x32_bf16 v[124:127], a[116:119], v[136:139], v[124:127]\n\t" \
  "v_mfma_f32_16x16x32_bf16 v[124:127], a[108:111], v[132:135], v[124:127]\n\t" \
  "v_mfma_f32_16x16x32_bf16 v[124:127], a[100:103], v[128:131], v[124:127]\n\t" \
  "v_mfma_f32_16x16x32_bf16 v[96:99], a[130:133], v[32:35], v[96:99]\n\t" \
  "v_mfma_f32_16x16x32_bf16 v[100:103], a[130:133], v[36:39], v[100:103]\n\t" \
  "v_mfma_f32_16x16x32_bf16 v[104:107], a[130:133], v[40:43], v[104:107]\n\t" \
  "v_mfma_f32_16x16x32_bf16 v[108:111], a[130:133], v[44:47], v[108:111]\n\t" \
  "v_mfma_f32_16x16x32_bf16 v[112:115], a[128:131], v[32:35], v[112:115]\n\t" \
  "v_mfma_f32_16x16x32_bf16 v[116:119], a[128:131], v[36:39], v[116:119]\n\t" \
  "v_mfma_f32_16x16x32_bf16 v[120:123], a[128:131], v[40:43], v[120:123]\n\t" \
  "v_mfma_f32_16x16x32_bf16 v[124:127], a[128:131], v[44:47], v[124:127]\n\t" \
  "s_waitcnt lgkmcnt(4)\n\t" \
  "v_mfma_f32_16x16x32_bf16 v[96:99], a[192:195], v[144:147], v[96:99]\n\t" \
  "s_cmp_lg_u32 s88, 0\n\t" \
  "s_cbranch_scc1 .Lat_m3\n\t" \
  ".Lat_n3:\n\t" \
  "v_mfma_f32_16x16x32_bf16 v[96:99], a[200:203], v[148:151], v[96:99]\n\t" \
  "s_add_u32 m0, %[m], 32768\n\t" \
  "ds_read_b128 v[128:131], v242 offset:24576\n\t" \
  "s_mov_b32 s96, 0\n\t" \
  "v_mfma_f32_16x16x32_bf16 v[96:99], a[208:211], v[152:155], v[96:99]\n\t" \
  "buffer_load_dwordx4 v252, %[sD], s96 offen offset:0 lds\n\t" \
  "v_mfma_f32_16x16x32_bf16 v[96:99], a[216:219], v[156:159], v[96:99]\n\t" \
  "ds_read_b128 v[132:135], v242 offset:25600\n\t" \
  "s_mov_b32 s96, 3072\n\t" \
  "v_mfma_f32_16x16x32_bf16 v[112:115], a[220:223], v[156:159], v[112:115]\n\t" \
  "buffer_load_dwordx4 v252, %[sD], s96 offen offset:1024 lds\n\t" \
  "v_mfma_f32_16x16x32_bf16 v[112:115], a[212:215], v[152:155], v[112:115]\n\t" \
  "ds_read_b128 v[136:139], v242 offset:26624\n\t" \
  "s_mov_b32 s96, 6144\n\t" \
  "v_mfma_f32_16x16x32_bf16 v[112:115], a[204:207], v[148:151], v[112:115]\n\t" \
  "buffer_load_dwordx4 v252, %[sD], s96 offen offset:2048 lds\n\t" \
  "v_mfma_f32_16x16x32_bf16 v[112:115], a[196:199], v[144:147], v[112:115]\n\t" \
  "ds_read_b128 v[140:143], v242 offset:27648\n\t" \
  "s_mov_b32 s96, 9216\n\t" \
  "v_mfma_f32_16x16x32_bf16 v[96:99], a[130:133], v[48:51], v[96:99]\n\t" \
  "buffer_load_dwordx4 v252, %[sD], s96 offen offset:3072 lds\n\t" \
  "v_mfma_f32_16x16x32_bf16 v[100:103], a[130:133], v[52:55], v[100:103]\n\t" \
  "s_add_u32 m0, %[m], 49152\n\t" \
  "s_mov_b32 s96, 16384\n\t" \
  "v_mfma_f32_16x16x32_bf16 v[104:107], a[130:133], v[56:59], v[104:107]\n\t" \
  "buffer_load_dwordx4 v252, %[sD], s96 offen offset:0 lds\n\t" \
  "v_mfma_f32_16x16x32_bf16 v[108:111], a[130:133], v[60:63], v[108:111]\n\t" \
  "s_mov_b32 s96, 19456\n\t" \
  "v_mfma_f32_16x16x32_bf16 v[112:115], a[128:131], v[48:51], v[112:115]\n\t" \
  "buffer_load_dwordx4 v252, %[sD], s96 offen offset:1024 lds\n\t" \
  "v_mfma_f32_16x16x32_bf16 v[116:119], a[128:131], v[52:55], v[116:119]\n\t" \
  "s_mov_b32 s96, 22528\n\t" \
  "v_mfma_f32_16x16x32_bf16 v[120:123], a[128:131], v[56:59], v[120:123]\n\t" \
  "buffer_load_dwordx4 v252, %[sD], s96 offen offset:2048 lds\n\t" \
  "v_mfma_f32_16x16x32_bf16 v[124:127], a[128:131], v[60:63], v[124:127]\n\t" \
  "s_waitcnt lgkmcnt(4)\n\t" \
  "s_mov_b32 s96, 25600\n\t" \
  "v_mfma_f32_16x16x32_bf16 v[100:103], a[192:195], v[160:163], v[100:103]\n\t" \
  "buffer_load_dwordx4 v252, %[sD], s96 offen offset:3072 lds\n\t" \
  "v_mfma_f32_16x16x32_bf16 v[100:103], a[200:203], v[164:167], v[100:103]\n\t" \
  "ds_read_b128 v[144:147], v242 offset:28672\n\t" \
  "v_mfma_f32_16x16x32_bf16 v[100:103], a[208:211], v[168:171], v[100:103]\n\t" \
  "ds_read_b128 v[148:151], v242 offset:29696\n\t" \
  "v_mfma_f32_16x16x32_bf16 v[100:103], a[216:219], v[172:175], v[100:103]\n\t" \
  "ds_read_b128 v[152:155], v242 offset:30720\n\t" \
  "v_mfma_f32_16x16x32_bf16 v[116:119], a[220:223], v[172:175], v[116:119]\n\t" \
  "ds_read_b128 v[156:159], v242 offset:31744\n\t" \
  "v_mfma_f32_16x16x32_bf16 v[116:119], a[212:215], v[168:171], v[116:119]\n\t" \
  "v_mfma_f32_16x16x32_bf16 v[116:119], a[204:207], v[164:167], v[116:119]\n\t" \
  "v_mfma_f32_16x16x32_bf16 v[116:119], a[196:199], v[160:163], v[116:119]\n\t" \
  "s_waitcnt lgkmcnt(4)\n\t" \
  "v_mfma_f32_16x16x32_bf16 v[104:107], a[192:195], v[128:131], v[104:107]\n\t" \
  "ds_read_b128 v[160:163], v237 offset:2560\n\t" \
  "v_mfma_f32_16x16x32_bf16 v[104:107], a[200:203], v[132:135], v[104:107]\n\t" \
  "ds_read_b128 v[164:167], v237 offset:2624\n\t" \
  "v_mfma_f32_16x16x32_bf16 v[104:107], a[208:211], v[136:139], v[104:107]\n\t" \
  "v_mfma_f32_16x16x32_bf16 v[104:107], a[216:219], v[140:143], v[104:107]\n\t" \
  "v_mfma_f32_16x16x32_bf16 v[120:123], a[220:223], v[140:143], v[120:123]\n\t" \
  "v_mfma_f32_16x16x32_bf16 v[120:123], a[212:215], v[136:139], v[120:123]\n\t" \
  "v_mfma_f32_16x16x32_bf16 v[120:123], a[204:207], v[132:135], v[120:123]\n\t" \
  "v_mfma_f32_16x16x32_bf16 v[120:123], a[196:199], v[128:131], v[120:123]\n\t" \
  "s_waitcnt lgkmcnt(2)\n\t" \
  "v_mfma_f32_16x16x32_bf16 v[108:111], a[192:195], v[144:147], v[108:111]\n\t" \
  "v_mfma_f32_16x16x32_bf16 v[108:111], a[200:203], v[148:151], v[108:111]\n\t" \
  "v_mfma_f32_16x16x32_bf16 v[108:111], a[208:211], v[152:155], v[108:111]\n\t" \
  "v_mfma_f32_16x16x32_bf16 v[108:111], a[216:219], v[156:159], v[108:111]\n\t" \
  "v_mfma_f32_16x16x32_bf16 v[124:127], a[220:223], v[156:159], v[124:127]\n\t" \
  "v_mfma_f32_16x16x32_bf16 v[124:127], a[212:215], v[152:155], v[124:127]\n\t" \
  "v_mfma_f32_16x16x32_bf16 v[124:127], a[204:207], v[148:151], v[124:127]\n\t" \
  "v_mfma_f32_16x16x32_bf16 v[124:127], a[196:199], v[144:147], v[124:127]\n\t" \
  STP("s_memrealtime s[94:95]\n\t") \
  STP("s_memtime s[92:93]\n\t") \
  "s_cmp_lg_u32 s88, 0\n\t" \
  "s_cbranch_scc1 .Lat_s99\n\t" \
  ".Lat_d2:\n\t" \
  "s_waitcnt vmcnt(8)\n\t" \
  "v_mfma_f32_16x16x32_bf16 v[96:99], a[130:133], v[64:67], v[96:99]\n\t" \
  "v_mfma_f32_16x16x32_bf16 v[100:103], a[130:133], v[68:71], v[100:103]\n\t" \
  "v_mfma_f32_16x16x32_bf16 v[104:107], a[130:133], v[72:75], v[104:107]\n\t" \
  "v_mfma_f32_16x16x32_bf16 v[108:111], a[130:133], v[76:79], v[108:111]\n\t" \
  "v_mfma_f32_16x16x32_bf16 v[112:115], a[128:131], v[64:67], v[112:115]\n\t" \
  "v_mfma_f32_16x16x32_bf16 v[116:119], a[128:131], v[68:71], v[116:119]\n\t" \
  "v_mfma_f32_16x16x32_bf16 v[120:123], a[128:131], v[72:75], v[120:123]\n\t" \
  "v_mfma_f32_16x16x32_bf16 v[124:127], a[128:131], v[76:79], v[124:127]\n\t" \
  "s_waitcnt lgkmcnt(0)\n\t" \
  STP("v_writelane_b32 %[stv], s94, 38\n\t") \
  STP("v_writelane_b32 %[stv], s92, 39\n\t") \
  "s_nop 7\n\t" \
  ".subsection 1\n\t" \
  ".Lat_x:\n\t" \
  "v_lshlrev_b32 v209, 12, v233\n\t" \
  "v_add_u32 v209, 98304, v209\n\t" \
  "s_mov_b64 exec, 15\n\t" \
  "ds_write_b32 v209, v243\n\t" \
  "s_mov_b64 exec, -1\n\t" \
  "s_waitcnt lgkmcnt(0)\n\t" \
  "s_branch .Lat_y\n\t" \
  ".Lat_rd:\n\t" \
  "s_waitcnt vmcnt(0) lgkmcnt(0)\n\t" \
  "s_barrier\n\t" \
  "s_mov_b32 %[pm], 2\n\t" \
  "s_branch .Lop_end\n\t" \
  ".Lat_s0:\n\t" \
  "s_mov_b32 s81, 0\n\t" \
  ".Lat_r0:\n\t" \
  "s_sleep 4\n\t" \
  "buffer_load_dword v246, v245, %[ws], s65 offen sc1\n\t" \
  "s_waitcnt vmcnt(0)\n\t" \
  "v_cmp_ne_u32 vcc, %[tag], v246\n\t" \
  "s_and_b32 vcc_lo, vcc_lo, 0xe\n\t" \
  "s_cbranch_scc0 .Lat_k0\n\t" \
  "s_add_u32 s81, s81, 1\n\t" \
  "s_cmp_lt_u32 s81, %[cap]\n\t" \
  "s_cbranch_scc1 .Lat_r0\n\t" \
  "buffer_store_dword v243, off, %[ws], 0 offset:256 sc1\n\t" \
  "s_branch .Lat_k0\n\t" \
  ".Lat_s1:\n\t" \
  "s_mov_b32 s81, 0\n\t" \
  ".Lat_r1:\n\t" \
  "s_sleep 4\n\t" \
  "buffer_load_dword v246, v245, %[ws], s65 offen sc1\n\t" \
  "s_waitcnt vmcnt(0)\n\t" \
  "v_cmp_ne_u32 vcc, %[tag], v246\n\t" \
  "s_and_b32 vcc_lo, vcc_lo, 0xfefe\n\t" \
  "s_cbranch_scc0 .Lat_k1\n\t" \
  "s_add_u32 s81, s81, 1\n\t" \
  "s_cmp_lt_u32 s81, %[cap]\n\t" \
  "s_cbranch_scc1 .Lat_r1\n\t" \
  "buffer_store_dword v243, off, %[ws], 0 offset:256 sc1\n\t" \
  "s_branch .Lat_k1\n\t" \
  ".Lat_s2:\n\t" \
  "s_mov_b32 s81, 0\n\t" \
  ".Lat_r2:\n\t" \
  "s_sleep 4\n\t" \
  "buffer_load_dword v246, v245, %[ws], s65 offen sc1\n\t" \
  "s_waitcnt vmcnt(0)\n\t" \
  "v_cmp_ne_u32 vcc, %[tag], v246\n\t" \
  "s_and_b32 vcc_lo, vcc_lo, 0xfe0000\n\t" \
  "s_cbranch_scc0 .Lat_k2\n\t" \
  "s_add_u32 s81, s81, 1\n\t" \
  "s_cmp_lt_u32 s81, %[cap]\n\t" \
  "s_cbranch_scc1 .Lat_r2\n\t" \
  "buffer_store_dword v243, off, %[ws], 0 offset:256 sc1\n\t" \
  "s_branch .Lat_k2\n\t" \
  ".Lat_s3:\n\t" \
  "s_mov_b32 s81, 0\n\t" \
  ".Lat_r3:\n\t" \
  "s_sleep 4\n\t" \
  "s_add_u32 s78, s75, 0\n\t" \
  "buffer_load_dwordx4 v[208:211], v234, %[ws], s78 offen sc1\n\t" \
  "s_add_u32 s78, s75, 32768\n\t" \
  "buffer_load_dwordx4 v[212:215], v234, %[ws], s78 offen sc1\n\t" \
  "s_add_u32 s78, s75, 65536\n\t" \
  "buffer_load_dwordx4 v[216:219], v234, %[ws], s78 offen sc1\n\t" \
  "s_add_u32 s78, s75, 98304\n\t" \
  "buffer_load_dwordx4 v[220:223], v234, %[ws], s78 offen sc1\n\t" \
  "s_waitcnt vmcnt(0)\n\t" \
  "v_xor_b32 v246, v243, v209\n\t" \
  "v_bitop3_b32 v246, v246, v211, v243 bitop3:0xf6\n\t" \
  "v_bitop3_b32 v246, v246, v213, v243 bitop3:0xf6\n\t" \
  "v_bitop3_b32 v246, v246, v215, v243 bitop3:0xf6\n\t" \
  "v_bitop3_b32 v246, v246, v217, v243 bitop3:0xf6\n\t" \
  "v_bitop3_b32 v246, v246, v219, v243 bitop3:0xf6\n\t" \
  "v_bitop3_b32 v246, v246, v221, v243 bitop3:0xf6\n\t" \
  "v_bitop3_b32 v246, v246, v223, v243 bitop3:0xf6\n\t" \
  "v_cmp_ne_u32 vcc, 0, v246\n\t" \
  "s_cbranch_vccz .Lat_k3\n\t" \
  "s_add_u32 s81, s81, 1\n\t" \
  "s_cmp_lt_u32 s81, %[cap]\n\t" \
  "s_cbranch_scc1 .Lat_r3\n\t" \
  "buffer_store_dword v243, off, %[ws], 0 offset:256 sc1\n\t" \
  "s_branch .Lat_k3\n\t" \
  ".Lat_s4:\n\t" \
  "s_mov_b32 s81, 0\n\t" \
  ".Lat_r4:\n\t" \
  "s_sleep 4\n\t" \
  "buffer_load_dword v246, v235, %[ws], s73 offen sc1\n\t" \
  "s_waitcnt vmcnt(0)\n\t" \
  "v_cmp_ne_u32 vcc, %[tag], v246\n\t" \
  "s_cbranch_vccz .Lat_k4\n\t" \
  "s_add_u32 s81, s81, 1\n\t" \
  "s_cmp_lt_u32 s81, %[cap]\n\t" \
  "s_cbranch_scc1 .Lat_r4\n\t" \
  "buffer_store_dword v243, off, %[ws], 0 offset:256 sc1\n\t" \
  "s_branch .Lat_k4\n\t" \
  ".Lat_m3:\n\t" \
  "s_waitcnt vmcnt(0)\n\t" \
  "v_cmp_ne_u32 vcc, %[tag], v251\n\t" \
  "s_mov_b32 s88, vcc_lo\n\t" \
  "s_cbranch_vccnz .Lat_n3\n\t" \
  "buffer_load_dwordx4 v[64:67], v242, %[ws], s79 offen offset:0 sc1\n\t" \
  "buffer_load_dwordx4 v[68:71], v242, %[ws], s79 offen offset:1024 sc1\n\t" \
  "buffer_load_dwordx4 v[72:75], v242, %[ws], s79 offen offset:2048 sc1\n\t" \
  "buffer_load_dwordx4 v[76:79], v242, %[ws], s79 offen offset:3072 sc1\n\t" \
  "s_branch .Lat_n3\n\t" \
  ".Lat_v:\n\t" \
  "s_mov_b32 s98, 0\n\t" \
  ".Lat_v1:\n\t" \
  "s_sleep 4\n\t" \
  "buffer_load_dword v209, v250, %[ws], 0 offen sc1\n\t" \
  "s_waitcnt vmcnt(0)\n\t" \
  "v_and_b32 v251, 0xbfffbfff, v209\n\t" \
  "v_cmp_ne_u32_e64 s[84:85], %[tag], v251\n\t" \
  "s_cmp_lg_u64 s[84:85], 0\n\t" \
  "s_cbranch_scc0 .Lat_w\n\t" \
  "s_add_u32 s98, s98, 1\n\t" \
  "s_cmp_lt_u32 s98, %[cap]\n\t" \
  "s_cbranch_scc1 .Lat_v1\n\t" \
  "v_mov_b32 v251, %[tag]\n\t" \
  "buffer_store_dword v251, off, %[ws], 0 offset:256 sc1\n\t" \
  "s_branch .Lat_w\n\t" \
  ".Lat_s99:\n\t" \
  "s_mov_b32 s81, 0\n\t" \
  ".Lat_r99:\n\t" \
  "buffer_load_dword v246, off, %[ws], s73 offset:32 sc1\n\t" \
  "s_waitcnt vmcnt(0)\n\t" \
  "v_cmp_ne_u32 vcc, %[tag], v246\n\t" \
  "s_cbranch_vccz .Lat_k99\n\t" \
  "s_add_u32 s81, s81, 1\n\t" \
  "s_cmp_lt_u32 s81, %[cap]\n\t" \
  "s_cbranch_scc1 .Lat_r99\n\t" \
  "buffer_store_dword v243, off, %[ws], 0 offset:256 sc1\n\t" \
  ".Lat_k99:\n\t" \
  "buffer_load_dwordx4 v[64:67], v242, %[ws], s79 offen offset:0 sc1\n\t" \
  "buffer_load_dwordx4 v[68:71], v242, %[ws], s79 offen offset:1024 sc1\n\t" \
  "buffer_load_dwordx4 v[72:75], v242, %[ws], s79 offen offset:2048 sc1\n\t" \
  "buffer_load_dwordx4 v[76:79], v242, %[ws], s79 offen offset:3072 sc1\n\t" \
  "s_waitcnt vmcnt(0)\n\t" \
  "s_branch .Lat_d2\n\t" \
  "s_endpgm\n\t" \
  ".Lfz_z:\n\t" \
  ".fill 128, 4, 0xBF800000\n\t" \
  ".subsection 2\n\t" \
  ".Lsf_go:\n\t" \
  "v_mfma_f32_16x16x32_bf16 v[28:31], a[156:159], a[224:227], v[28:31]\n\t" \
  "v_mov_b32 v196, 1.0\n\t" \
  "v_mov_b32 v197, 1.0\n\t" \
  "v_mfma_f32_16x16x32_bf16 v[28:31], a[92:95], a[240:243], v[28:31]\n\t" \
  "v_mov_b32 v198, 1.0\n\t" \
  "v_mov_b32 v199, 1.0\n\t" \
  "v_mfma_f32_16x16x32_bf16 v[24:27], a[152:155], a[224:227], v[24:27]\n\t" \
  "s_mov_b32 s99, 0\n\t" \
  "v_add_u32 v210, 65536, v228\n\t" \
  "v_add_u32 v211, 65536, v229\n\t" \
  "v_mfma_f32_16x16x32_bf16 v[24:27], a[88:91], a[240:243], v[24:27]\n\t" \
  "v_mov_b32 v216, 0x3f803f80\n\t" \
  "v_cvt_pk_bf16_f32 v31, v30, v31\n\t" \
  "v_mfma_f32_16x16x32_bf16 v[20:23], a[148:151], a[224:227], v[20:23]\n\t" \
  "v_mov_b32 v217, 0x3f803f80\n\t" \
  "v_cvt_pk_bf16_f32 v30, v28, v29\n\t" \
  "v_mfma_f32_16x16x32_bf16 v[20:23], a[84:87], a[240:243], v[20:23]\n\t" \
  "v_mov_b32 v218, 0x3f803f80\n\t" \
  "v_mov_b32 v219, 0x3f803f80\n\t" \
  "v_mfma_f32_16x16x32_bf16 v[16:19], a[144:147], a[224:227], v[16:19]\n\t" \
  "v_cvt_pk_bf16_f32 v28, v24, v25\n\t" \
  "v_cvt_pk_bf16_f32 v29, v26, v27\n\t" \
  "v_mfma_f32_16x16x32_bf16 v[16:19], a[80:83], a[240:243], v[16:19]\n\t" \
  "v_permlane16_swap_b32 v28, v30\n\t" \
  "v_mfma_f32_16x16x32_bf16 v[12:15], a[140:143], a[224:227], v[12:15]\n\t" \
  "v_cvt_pk_bf16_f32 v27, v22, v23\n\t" \
  "v_cvt_pk_bf16_f32 v26, v20, v21\n\t" \
  "v_mfma_f32_16x16x32_bf16 v[12:15], a[76:79], a[240:243], v[12:15]\n\t" \
  "v_permlane16_swap_b32 v29, v31\n\t" \
  "v_mfma_f32_16x16x32_bf16 v[8:11], a[136:139], a[224:227], v[8:11]\n\t" \
  "v_mfma_f32_16x16x32_bf16 v[8:11], a[72:75], a[240:243], v[8:11]\n\t" \
  "v_cvt_pk_bf16_f32 v24, v16, v17\n\t" \
  "v_cvt_pk_bf16_f32 v25, v18, v19\n\t" \
  "v_mfma_f32_16x16x32_bf16 v[4:7], a[132:135], a[224:227], v[4:7]\n\t" \
  "v_permlane16_swap_b32 v24, v26\n\t" \
  "v_mfma_f32_16x16x32_bf16 v[4:7], a[68:71], a[240:243], v[4:7]\n\t" \
  "v_cvt_pk_bf16_f32 v23, v14, v15\n\t" \
  "v_cvt_pk_bf16_f32 v22, v12, v13\n\t" \
  "v_mfma_f32_16x16x32_bf16 v[0:3], a[128:131], a[224:227], v[0:3]\n\t" \
  "s_waitcnt vmcnt(2)\n\t" \
  "v_cvt_pk_bf16_f32 v20, v8, v9\n\t" \
  "v_cvt_pk_bf16_f32 v21, v10, v11\n\t" \
  "v_mfma_f32_16x16x32_bf16 v[0:3], a[64:67], a[240:243], v[0:3]\n\t" \
  "s_barrier\n\t" \
  "v_mfma_f32_16x16x32_bf16 v[60:63], a[156:159], a[228:231], v[60:63]\n\t" \
  "v_cvt_pk_bf16_f32 v19, v6, v7\n\t" \
  "v_cvt_pk_bf16_f32 v18, v4, v5\n\t" \
  "v_mfma_f32_16x16x32_bf16 v[60:63], a[92:95], a[244:247], v[60:63]\n\t" \
  "v_permlane16_swap_b32 v25, v27\n\t" \
  "v_mfma_f32_16x16x32_bf16 v[56:59], a[152:155], a[228:231], v[56:59]\n\t" \
  "ds_read_b128 a[0:3], v220 offset:0\n\t" \
  "v_cvt_pk_bf16_f32 v16, v0, v1\n\t" \
  "v_mfma_f32_16x16x32_bf16 v[56:59], a[88:91], a[244:247], v[56:59]\n\t" \
  "ds_read_b128 a[4:7], v221 offset:0\n\t" \
  "v_cvt_pk_bf16_f32 v17, v2, v3\n\t" \
  "v_mfma_f32_16x16x32_bf16 v[52:55], a[148:151], a[228:231], v[52:55]\n\t" \
  "v_cvt_pk_bf16_f32 v63, v62, v63\n\t" \
  "v_cvt_pk_bf16_f32 v62, v60, v61\n\t" \
  "v_mfma_f32_16x16x32_bf16 v[52:55], a[84:87], a[244:247], v[52:55]\n\t" \
  "v_permlane16_swap_b32 v20, v22\n\t" \
  "v_mfma_f32_16x16x32_bf16 v[48:51], a[144:147], a[228:231], v[48:51]\n\t" \
  "ds_read_b128 a[8:11], v222 offset:0\n\t" \
  "v_cvt_pk_bf16_f32 v60, v56, v57\n\t" \
  "v_mfma_f32_16x16x32_bf16 v[48:51], a[80:83], a[244:247], v[48:51]\n\t" \
  "ds_read_b128 a[12:15], v223 offset:0\n\t" \
  "v_cvt_pk_bf16_f32 v61, v58, v59\n\t" \
  "v_mfma_f32_16x16x32_bf16 v[44:47], a[140:143], a[228:231], v[44:47]\n\t" \
  "v_cvt_pk_bf16_f32 v59, v54, v55\n\t" \
  "v_cvt_pk_bf16_f32 v58, v52, v53\n\t" \
  "v_mfma_f32_16x16x32_bf16 v[44:47], a[76:79], a[244:247], v[44:47]\n\t" \
  "v_permlane16_swap_b32 v21, v23\n\t" \
  "v_mfma_f32_16x16x32_bf16 v[40:43], a[136:139], a[228:231], v[40:43]\n\t" \
  "ds_read_b128 a[16:19], v220 offset:4096\n\t" \
  "v_cvt_pk_bf16_f32 v56, v48, v49\n\t" \
  "v_mfma_f32_16x16x32_bf16 v[40:43], a[72:75], a[244:247], v[40:43]\n\t" \
  "ds_read_b128 a[20:23], v221 offset:4096\n\t" \
  "v_cvt_pk_bf16_f32 v57, v50, v51\n\t" \
  "v_mfma_f32_16x16x32_bf16 v[36:39], a[132:135], a[228:231], v[36:39]\n\t" \
  "v_cvt_pk_bf16_f32 v55, v46, v47\n\t" \
  "v_cvt_pk_bf16_f32 v54, v44, v45\n\t" \
  "v_mfma_f32_16x16x32_bf16 v[36:39], a[68:71], a[244:247], v[36:39]\n\t" \
  "v_permlane16_swap_b32 v16, v18\n\t" \
  "v_mfma_f32_16x16x32_bf16 v[32:35], a[128:131], a[228:231], v[32:35]\n\t" \
  "ds_read_b128 a[24:27], v222 offset:4096\n\t" \
  "v_cvt_pk_bf16_f32 v52, v40, v41\n\t" \
  "v_mfma_f32_16x16x32_bf16 v[32:35], a[64:67], a[244:247], v[32:35]\n\t" \
  "ds_read_b128 a[28:31], v223 offset:4096\n\t" \
  "v_cvt_pk_bf16_f32 v53, v42, v43\n\t" \
  "v_mfma_f32_16x16x32_bf16 v[92:95], a[156:159], a[232:235], v[92:95]\n\t" \
  "v_cvt_pk_bf16_f32 v51, v38, v39\n\t" \
  "v_cvt_pk_bf16_f32 v50, v36, v37\n\t" \
  "v_mfma_f32_16x16x32_bf16 v[92:95], a[92:95], a[248:251], v[92:95]\n\t" \
  "v_permlane16_swap_b32 v17, v19\n\t" \
  "v_mfma_f32_16x16x32_bf16 v[88:91], a[152:155], a[232:235], v[88:91]\n\t" \
  "v_cvt_pk_bf16_f32 v48, v32, v33\n\t" \
  "v_cvt_pk_bf16_f32 v49, v34, v35\n\t" \
  "v_mfma_f32_16x16x32_bf16 v[88:91], a[88:91], a[248:251], v[88:91]\n\t" \
  "v_permlane16_swap_b32 v60, v62\n\t" \
  "v_mfma_f32_16x16x32_bf16 v[84:87], a[148:151], a[232:235], v[84:87]\n\t" \
  "ds_read_b128 a[32:35], v220 offset:8192\n\t" \
  "v_cvt_pk_bf16_f32 v95, v94, v95\n\t" \
  "v_mfma_f32_16x16x32_bf16 v[84:87], a[84:87], a[248:251], v[84:87]\n\t" \
  "ds_read_b128 a[36:39], v221 offset:8192\n\t" \
  "v_cvt_pk_bf16_f32 v94, v92, v93\n\t" \
  "v_mfma_f32_16x16x32_bf16 v[80:83], a[144:147], a[232:235], v[80:83]\n\t" \
  "v_cvt_pk_bf16_f32 v92, v88, v89\n\t" \
  "v_permlane16_swap_b32 v61, v63\n\t" \
  "v_mfma_f32_16x16x32_bf16 v[80:83], a[80:83], a[248:251], v[80:83]\n\t" \
  "ds_read_b128 a[40:43], v222 offset:8192\n\t" \
  "v_cvt_pk_bf16_f32 v93, v90, v91\n\t" \
  "v_mfma_f32_16x16x32_bf16 v[76:79], a[140:143], a[232:235], v[76:79]\n\t" \
  "v_cvt_pk_bf16_f32 v91, v86, v87\n\t" \
  "v_cvt_pk_bf16_f32 v90, v84, v85\n\t" \
  "v_mfma_f32_16x16x32_bf16 v[76:79], a[76:79], a[248:251], v[76:79]\n\t" \
  "ds_read_b128 a[44:47], v223 offset:8192\n\t" \
  "v_permlane16_swap_b32 v56, v58\n\t" \
  "v_mfma_f32_16x16x32_bf16 v[72:75], a[136:139], a[232:235], v[72:75]\n\t" \
  "v_cvt_pk_bf16_f32 v88, v80, v81\n\t" \
  "v_permlane16_swap_b32 v57, v59\n\t" \
  "v_mfma_f32_16x16x32_bf16 v[72:75], a[72:75], a[248:251], v[72:75]\n\t" \
  "ds_read_b128 a[48:51], v220 offset:12288\n\t" \
  "v_cvt_pk_bf16_f32 v89, v82, v83\n\t" \
  "v_mfma_f32_16x16x32_bf16 v[68:71], a[132:135], a[232:235], v[68:71]\n\t" \
  "ds_read_b128 a[52:55], v221 offset:12288\n\t" \
  "v_cvt_pk_bf16_f32 v87, v78, v79\n\t" \
  "v_mfma_f32_16x16x32_bf16 v[68:71], a[68:71], a[248:251], v[68:71]\n\t" \
  "v_cvt_pk_bf16_f32 v86, v76, v77\n\t" \
  "v_permlane16_swap_b32 v52, v54\n\t" \
  "v_mfma_f32_16x16x32_bf16 v[64:67], a[128:131], a[232:235], v[64:67]\n\t" \
  "ds_read_b128 a[56:59], v222 offset:12288\n\t" \
  "v_permlane16_swap_b32 v53, v55\n\t" \
  "v_mfma_f32_16x16x32_bf16 v[64:67], a[64:67], a[248:251], v[64:67]\n\t" \
  "v_cvt_pk_bf16_f32 v84, v72, v73\n\t" \
  "v_cvt_pk_bf16_f32 v85, v74, v75\n\t" \
  "v_mfma_f32_16x16x32_bf16 v[124:127], a[156:159], a[236:239], v[124:127]\n\t" \
  "ds_read_b128 a[60:63], v223 offset:12288\n\t" \
  "v_cvt_pk_bf16_f32 v83, v70, v71\n\t" \
  "v_mfma_f32_16x16x32_bf16 v[124:127], a[92:95], a[252:255], v[124:127]\n\t" \
  "v_cvt_pk_bf16_f32 v82, v68, v69\n\t" \
  "v_permlane16_swap_b32 v48, v50\n\t" \
  "v_mfma_f32_16x16x32_bf16 v[120:123], a[152:155], a[236:239], v[120:123]\n\t" \
  "v_cvt_pk_bf16_f32 v80, v64, v65\n\t" \
  "v_permlane16_swap_b32 v49, v51\n\t" \
  "v_mfma_f32_16x16x32_bf16 v[120:123], a[88:91], a[252:255], v[120:123]\n\t" \
  "v_permlane16_swap_b32 v92, v94\n\t" \
  "v_mfma_f32_16x16x32_bf16 v[116:119], a[148:151], a[236:239], v[116:119]\n\t" \
  "v_cvt_pk_bf16_f32 v81, v66, v67\n\t" \
  "v_cvt_pk_bf16_f32 v127, v126, v127\n\t" \
  "v_mfma_f32_16x16x32_bf16 v[116:119], a[84:87], a[252:255], v[116:119]\n\t" \
  "v_permlane16_swap_b32 v93, v95\n\t" \
  "v_mfma_f32_16x16x32_bf16 v[112:115], a[144:147], a[236:239], v[112:115]\n\t" \
  "v_cvt_pk_bf16_f32 v126, v124, v125\n\t" \
  "v_cvt_pk_bf16_f32 v124, v120, v121\n\t" \
  "v_mfma_f32_16x16x32_bf16 v[112:115], a[80:83], a[252:255], v[112:115]\n\t" \
  "v_cvt_pk_bf16_f32 v125, v122, v123\n\t" \
  "v_permlane16_swap_b32 v88, v90\n\t" \
  "v_mfma_f32_16x16x32_bf16 v[108:111], a[140:143], a[236:239], v[108:111]\n\t" \
  "v_permlane16_swap_b32 v89, v91\n\t" \
  "v_mfma_f32_16x16x32_bf16 v[108:111], a[76:79], a[252:255], v[108:111]\n\t" \
  "v_cvt_pk_bf16_f32 v123, v118, v119\n\t" \
  "v_cvt_pk_bf16_f32 v122, v116, v117\n\t" \
  "v_mfma_f32_16x16x32_bf16 v[104:107], a[136:139], a[236:239], v[104:107]\n\t" \
  "v_permlane16_swap_b32 v84, v86\n\t" \
  "v_mfma_f32_16x16x32_bf16 v[104:107], a[72:75], a[252:255], v[104:107]\n\t" \
  "v_cvt_pk_bf16_f32 v120, v112, v113\n\t" \
  "v_cvt_pk_bf16_f32 v121, v114, v115\n\t" \
  "v_mfma_f32_16x16x32_bf16 v[100:103], a[132:135], a[236:239], v[100:103]\n\t" \
  "s_add_u32 m0, %[m], 49152\n\t" \
  "v_permlane16_swap_b32 v85, v87\n\t" \
  "v_mfma_f32_16x16x32_bf16 v[100:103], a[68:71], a[252:255], v[100:103]\n\t" \
  "s_add_u32 s96, %[m], 32768\n\t" \
  "v_permlane16_swap_b32 v80, v82\n\t" \
  "v_mfma_f32_16x16x32_bf16 v[96:99], a[128:131], a[236:239], v[96:99]\n\t" \
  "s_waitcnt lgkmcnt(0)\n\t" \
  "v_cvt_pk_bf16_f32 v119, v110, v111\n\t" \
  "v_cvt_pk_bf16_f32 v118, v108, v109\n\t" \
  "v_mfma_f32_16x16x32_bf16 v[96:99], a[64:67], a[252:255], v[96:99]\n\t" \
  "s_barrier\n\t" \
  "v_mfma_f32_16x16x32_bf16 v[0:3], a[0:3], v[16:19], 0\n\t" \
  "buffer_load_dwordx4 v224, s[60:63], s96 offen offset:0 lds\n\t" \
  "v_mfma_f32_16x16x32_bf16 v[0:3], a[4:7], v[20:23], v[0:3]\n\t" \
  "v_permlane16_swap_b32 v81, v83\n\t" \
  "v_mfma_f32_16x16x32_bf16 v[0:3], a[8:11], v[24:27], v[0:3]\n\t" \
  "ds_read_b128 a[192:195], v220 offset:16384\n\t" \
  "v_cvt_pk_bf16_f32 v116, v104, v105\n\t" \
  "v_mfma_f32_16x16x32_bf16 v[0:3], a[12:15], v[28:31], v[0:3]\n\t" \
  "v_permlane16_swap_b32 v124, v126\n\t" \
  "v_mfma_f32_16x16x32_bf16 v[32:35], a[0:3], v[48:51], 0\n\t" \
  "v_permlane16_swap_b32 v125, v127\n\t" \
  "v_mfma_f32_16x16x32_bf16 v[32:35], a[4:7], v[52:55], v[32:35]\n\t" \
  "s_nop 3\n\t" \
  "v_max3_f32 v194, v0, v1, v2\n\t" \
  "v_mfma_f32_16x16x32_bf16 v[32:35], a[8:11], v[56:59], v[32:35]\n\t" \
  "ds_read_b128 a[196:199], v221 offset:16384\n\t" \
  "v_cvt_pk_bf16_f32 v117, v106, v107\n\t" \
  "v_max_f32 v194, v194, v3\n\t" \
  "v_mov_b32 v195, v194\n\t" \
  "v_mfma_f32_16x16x32_bf16 v[32:35], a[12:15], v[60:63], v[32:35]\n\t" \
  "v_cvt_pk_bf16_f32 v115, v102, v103\n\t" \
  "v_permlane32_swap_b32 v194, v195\n\t" \
  "v_max_f32 v194, v194, v195\n\t" \
  "v_mov_b32 v195, v194\n\t" \
  "s_nop 1\n\t" \
  "v_permlane16_swap_b32 v194, v195\n\t" \
  "v_max_f32 v194, v194, v195\n\t" \
  "v_sub_f32 v176, 0xc2000000, v194\n\t" \
  "v_sub_f32 v177, 0xc2000000, v194\n\t" \
  "v_sub_f32 v178, 0xc2000000, v194\n\t" \
  "v_sub_f32 v179, 0xc2000000, v194\n\t" \
  "s_nop 1\n\t" \
  "v_mfma_f32_16x16x32_bf16 v[4:7], a[16:19], v[16:19], v[176:179]\n\t" \
  "v_add_f32 v0, v0, v176\n\t" \
  "v_add_f32 v1, v1, v176\n\t" \
  "v_add_f32 v2, v2, v176\n\t" \
  "v_add_f32 v3, v3, v176\n\t" \
  "v_mfma_f32_16x16x32_bf16 v[4:7], a[20:23], v[20:23], v[4:7]\n\t" \
  "buffer_load_dwordx4 v225, s[60:63], s96 offen offset:1024 lds\n\t" \
  "v_max3_f32 v194, v32, v33, v34\n\t" \
  "v_max_f32 v194, v194, v35\n\t" \
  "v_mov_b32 v195, v194\n\t" \
  "v_mfma_f32_16x16x32_bf16 v[4:7], a[24:27], v[24:27], v[4:7]\n\t" \
  "ds_read_b128 a[200:203], v222 offset:16384\n\t" \
  "v_permlane32_swap_b32 v194, v195\n\t" \
  "v_max_f32 v194, v194, v195\n\t" \
  "v_mov_b32 v195, v194\n\t" \
  "v_mfma_f32_16x16x32_bf16 v[4:7], a[28:31], v[28:31], v[4:7]\n\t" \
  "v_permlane16_swap_b32 v120, v122\n\t" \
  "v_permlane16_swap_b32 v194, v195\n\t" \
  "v_max_f32 v194, v194, v195\n\t" \
  "v_mfma_f32_16x16x32_bf16 v[8:11], a[32:35], v[16:19], v[176:179]\n\t" \
  "v_sub_f32 v180, 0xc2000000, v194\n\t" \
  "v_sub_f32 v181, 0xc2000000, v194\n\t" \
  "v_mfma_f32_16x16x32_bf16 v[8:11], a[36:39], v[20:23], v[8:11]\n\t" \
  "v_sub_f32 v182, 0xc2000000, v194\n\t" \
  "v_sub_f32 v183, 0xc2000000, v194\n\t" \
  "v_mfma_f32_16x16x32_bf16 v[8:11], a[40:43], v[24:27], v[8:11]\n\t" \
  "v_add_f32 v32, v32, v180\n\t" \
  "v_add_f32 v33, v33, v180\n\t" \
  "v_max3_f32 v192, v4, v5, v6\n\t" \
  "v_mfma_f32_16x16x32_bf16 v[8:11], a[44:47], v[28:31], v[8:11]\n\t" \
  "v_add_f32 v34, v34, v180\n\t" \
  "v_permlane16_swap_b32 v121, v123\n\t" \
  "v_mfma_f32_16x16x32_bf16 v[12:15], a[48:51], v[16:19], v[176:179]\n\t" \
  "v_add_f32 v35, v35, v180\n\t" \
  "v_permlane16_swap_b32 v116, v118\n\t" \
  "v_mfma_f32_16x16x32_bf16 v[12:15], a[52:55], v[20:23], v[12:15]\n\t" \
  "ds_read_b128 a[204:207], v223 offset:16384\n\t" \
  "v_cvt_pk_bf16_f32 v114, v100, v101\n\t" \
  "v_max3_f32 v203, v7, v8, v9\n\t" \
  "v_max3_f32 v192, v192, v10, v11\n\t" \
  "v_mfma_f32_16x16x32_bf16 v[12:15], a[56:59], v[24:27], v[12:15]\n\t" \
  "buffer_load_dwordx4 v226, s[60:63], s96 offen offset:2048 lds\n\t" \
  "v_mfma_f32_16x16x32_bf16 v[12:15], a[60:63], v[28:31], v[12:15]\n\t" \
  "ds_read_b128 a[208:211], v220 offset:20480\n\t" \
  "v_permlane16_swap_b32 v117, v119\n\t" \
  "v_mfma_f32_16x16x32_bf16 v[64:67], a[0:3], v[80:83], 0\n\t" \
  "ds_read_b128 a[212:215], v221 offset:20480\n\t" \
  "v_cvt_pk_bf16_f32 v112, v96, v97\n\t" \
  "v_mfma_f32_16x16x32_bf16 v[64:67], a[4:7], v[84:87], v[64:67]\n\t" \
  "v_cvt_pk_bf16_f32 v113, v98, v99\n\t" \
  "s_nop 0\n\t" \
  "v_max3_f32 v203, v203, v12, v13\n\t" \
  "v_max3_f32 v192, v192, v14, v15\n\t" \
  "v_max_f32 v192, v192, v203\n\t" \
  "v_mfma_f32_16x16x32_bf16 v[64:67], a[8:11], v[88:91], v[64:67]\n\t" \
  "v_permlane16_swap_b32 v112, v114\n\t" \
  "v_cmp_lt_f32 vcc, 0x42800000, v192\n\t" \
  "v_mfma_f32_16x16x32_bf16 v[64:67], a[12:15], v[92:95], v[64:67]\n\t" \
  "v_permlane16_swap_b32 v113, v115\n\t" \
  "v_mfma_f32_16x16x32_bf16 v[36:39], a[16:19], v[48:51], v[180:183]\n\t" \
  "s_cbranch_vccnz .Lsf_c00\n\t" \
  ".Lsf_r00:\n\t" \
  "v_mfma_f32_16x16x32_bf16 v[36:39], a[20:23], v[52:55], v[36:39]\n\t" \
  "ds_read_b128 a[216:219], v222 offset:20480\n\t" \
  "v_exp_f32 v0, v0\n\t" \
  "v_exp_f32 v1, v1\n\t" \
  "v_mfma_f32_16x16x32_bf16 v[36:39], a[24:27], v[56:59], v[36:39]\n\t" \
  "v_max3_f32 v194, v64, v65, v66\n\t" \
  "v_max_f32 v194, v194, v67\n\t" \
  "v_mov_b32 v195, v194\n\t" \
  "v_exp_f32 v2, v2\n\t" \
  "v_mfma_f32_16x16x32_bf16 v[36:39], a[28:31], v[60:63], v[36:39]\n\t" \
  "ds_read_b128 a[220:223], v223 offset:20480\n\t" \
  "v_permlane32_swap_b32 v194, v195\n\t" \
  "v_max_f32 v194, v194, v195\n\t" \
  "v_mov_b32 v195, v194\n\t" \
  "v_mfma_f32_16x16x32_bf16 v[40:43], a[32:35], v[48:51], v[180:183]\n\t" \
  "s_nop 0\n\t" \
  "v_permlane16_swap_b32 v194, v195\n\t" \
  "v_max_f32 v194, v194, v195\n\t" \
  "v_sub_f32 v184, 0xc2000000, v194\n\t" \
  "v_sub_f32 v185, 0xc2000000, v194\n\t" \
  "v_mfma_f32_16x16x32_bf16 v[40:43], a[36:39], v[52:55], v[40:43]\n\t" \
  "buffer_load_dwordx4 v227, s[60:63], s96 offen offset:3072 lds\n\t" \
  "v_exp_f32 v3, v3\n\t" \
  "v_sub_f32 v186, 0xc2000000, v194\n\t" \
  "v_mfma_f32_16x16x32_bf16 v[40:43], a[40:43], v[56:59], v[40:43]\n\t" \
  "v_sub_f32 v187, 0xc2000000, v194\n\t" \
  "v_add_f32 v64, v64, v184\n\t" \
  "v_max3_f32 v192, v36, v37, v38\n\t" \
  "v_exp_f32 v4, v4\n\t" \
  "v_mfma_f32_16x16x32_bf16 v[40:43], a[44:47], v[60:63], v[40:43]\n\t" \
  "v_add_f32 v65, v65, v184\n\t" \
  "v_exp_f32 v5, v5\n\t" \
  "v_exp_f32 v6, v6\n\t" \
  "v_mfma_f32_16x16x32_bf16 v[44:47], a[48:51], v[48:51], v[180:183]\n\t" \
  "s_add_u32 m0, %[m], 147456\n\t" \
  "v_add_f32 v66, v66, v184\n\t" \
  "v_exp_f32 v7, v7\n\t" \
  "v_exp_f32 v8, v8\n\t" \
  "v_mfma_f32_16x16x32_bf16 v[44:47], a[52:55], v[52:55], v[44:47]\n\t" \
  "s_add_u32 s96, %[m], 16384\n\t" \
  "v_max3_f32 v203, v39, v40, v41\n\t" \
  "v_exp_f32 v9, v9\n\t" \
  "v_exp_f32 v10, v10\n\t" \
  "v_mfma_f32_16x16x32_bf16 v[44:47], a[56:59], v[56:59], v[44:47]\n\t" \
  "v_max3_f32 v192, v192, v42, v43\n\t" \
  "v_exp_f32 v11, v11\n\t" \
  "v_exp_f32 v12, v12\n\t" \
  "v_mfma_f32_16x16x32_bf16 v[44:47], a[60:63], v[60:63], v[44:47]\n\t" \
  "v_add_f32 v67, v67, v184\n\t" \
  "v_exp_f32 v13, v13\n\t" \
  "v_exp_f32 v14, v14\n\t" \
  "v_mfma_f32_16x16x32_bf16 v[96:99], a[0:3], v[112:115], 0\n\t" \
  "buffer_load_dwordx4 v230, s[64:67], s96 offen offset:0 lds\n\t" \
  "v_exp_f32 v15, v15\n\t" \
  "v_mfma_f32_16x16x32_bf16 v[96:99], a[4:7], v[116:119], v[96:99]\n\t" \
  "s_nop 0\n\t" \
  "v_max3_f32 v203, v203, v44, v45\n\t" \
  "v_max3_f32 v192, v192, v46, v47\n\t" \
  "v_max_f32 v192, v192, v203\n\t" \
  "v_cmp_lt_f32 vcc, 0x42800000, v192\n\t" \
  "v_mfma_f32_16x16x32_bf16 v[96:99], a[8:11], v[120:123], v[96:99]\n\t" \
  "ds_read_b128 a[224:227], v220 offset:24576\n\t" \
  "v_cvt_pk_bf16_f32 v128, v0, v1\n\t" \
  "v_mfma_f32_16x16x32_bf16 v[96:99], a[12:15], v[124:127], v[96:99]\n\t" \
  "ds_read_b128 a[228:231], v221 offset:24576\n\t" \
  "s_cbranch_vccnz .Lsf_c01\n\t" \
  ".Lsf_r01:\n\t" \
  "v_mfma_f32_16x16x32_bf16 v[68:71], a[16:19], v[80:83], v[184:187]\n\t" \
  "ds_read_b128 a[232:235], v222 offset:24576\n\t" \
  "v_exp_f32 v32, v32\n\t" \
  "v_exp_f32 v33, v33\n\t" \
  "v_mfma_f32_16x16x32_bf16 v[68:71], a[20:23], v[84:87], v[68:71]\n\t" \
  "s_nop 0\n\t" \
  "v_max3_f32 v194, v96, v97, v98\n\t" \
  "v_max_f32 v194, v194, v99\n\t" \
  "v_mov_b32 v195, v194\n\t" \
  "v_exp_f32 v34, v34\n\t" \
  "v_mfma_f32_16x16x32_bf16 v[68:71], a[24:27], v[88:91], v[68:71]\n\t" \
  "ds_read_b128 a[236:239], v223 offset:24576\n\t" \
  "v_permlane32_swap_b32 v194, v195\n\t" \
  "v_max_f32 v194, v194, v195\n\t" \
  "v_mov_b32 v195, v194\n\t" \
  "v_mfma_f32_16x16x32_bf16 v[68:71], a[28:31], v[92:95], v[68:71]\n\t" \
  "ds_read_b128 a[240:243], v220 offset:28672\n\t" \
  "v_permlane16_swap_b32 v194, v195\n\t" \
  "v_max_f32 v194, v194, v195\n\t" \
  "v_sub_f32 v188, 0xc2000000, v194\n\t" \
  "v_mfma_f32_16x16x32_bf16 v[72:75], a[32:35], v[80:83], v[184:187]\n\t" \
  "v_sub_f32 v189, 0xc2000000, v194\n\t" \
  "v_sub_f32 v190, 0xc2000000, v194\n\t" \
  "v_exp_f32 v35, v35\n\t" \
  "v_max3_f32 v192, v68, v69, v70\n\t" \
  "v_mfma_f32_16x16x32_bf16 v[72:75], a[36:39], v[84:87], v[72:75]\n\t" \
  "v_sub_f32 v191, 0xc2000000, v194\n\t" \
  "v_exp_f32 v36, v36\n\t" \
  "v_exp_f32 v37, v37\n\t" \
  "v_mfma_f32_16x16x32_bf16 v[72:75], a[40:43], v[88:91], v[72:75]\n\t" \
  "buffer_load_dwordx4 v231, s[64:67], s96 offen offset:1024 lds\n\t" \
  "v_exp_f32 v38, v38\n\t" \
  "v_mfma_f32_16x16x32_bf16 v[72:75], a[44:47], v[92:95], v[72:75]\n\t" \
  "v_add_f32 v96, v96, v188\n\t" \
  "v_exp_f32 v39, v39\n\t" \
  "v_exp_f32 v40, v40\n\t" \
  "v_mfma_f32_16x16x32_bf16 v[76:79], a[48:51], v[80:83], v[184:187]\n\t" \
  "ds_read_b128 a[244:247], v221 offset:28672\n\t" \
  "v_exp_f32 v41, v41\n\t" \
  "v_exp_f32 v42, v42\n\t" \
  "v_mfma_f32_16x16x32_bf16 v[76:79], a[52:55], v[84:87], v[76:79]\n\t" \
  "v_add_f32 v97, v97, v188\n\t" \
  "v_max3_f32 v203, v71, v72, v73\n\t" \
  "v_exp_f32 v43, v43\n\t" \
  "v_max3_f32 v192, v192, v74, v75\n\t" \
  "v_mfma_f32_16x16x32_bf16 v[76:79], a[56:59], v[88:91], v[76:79]\n\t" \
  "v_add_f32 v98, v98, v188\n\t" \
  "v_exp_f32 v44, v44\n\t" \
  "v_exp_f32 v45, v45\n\t" \
  "v_mfma_f32_16x16x32_bf16 v[76:79], a[60:63], v[92:95], v[76:79]\n\t" \
  "ds_read_b128 a[248:251], v222 offset:28672\n\t" \
  "v_exp_f32 v46, v46\n\t" \
  "v_exp_f32 v47, v47\n\t" \
  "v_mfma_f32_16x16x32_bf16 v[100:103], a[16:19], v[112:115], v[188:191]\n\t" \
  "s_nop 3\n\t" \
  "v_max3_f32 v203, v203, v76, v77\n\t" \
  "v_mfma_f32_16x16x32_bf16 v[100:103], a[20:23], v[116:119], v[100:103]\n\t" \
  "ds_read_b128 a[252:255], v223 offset:28672\n\t" \
  "v_max3_f32 v192, v192, v78, v79\n\t" \
  "v_max_f32 v192, v192, v203\n\t" \
  "v_cmp_lt_f32 vcc, 0x42800000, v192\n\t" \
  "v_mfma_f32_16x16x32_bf16 v[100:103], a[24:27], v[120:123], v[100:103]\n\t" \
  "buffer_load_dwordx4 v230, s[64:67], s96 offen offset:2048 lds\n\t" \
  "v_mfma_f32_16x16x32_bf16 v[100:103], a[28:31], v[124:127], v[100:103]\n\t" \
  "v_add_f32 v99, v99, v188\n\t" \
  "v_cvt_pk_bf16_f32 v129, v2, v3\n\t" \
  "s_cbranch_vccnz .Lsf_c02\n\t" \
  ".Lsf_r02:\n\t" \
  "v_mfma_f32_16x16x32_bf16 v[104:107], a[32:35], v[112:115], v[188:191]\n\t" \
  "v_cvt_pk_bf16_f32 v130, v4, v5\n\t" \
  "v_cvt_pk_bf16_f32 v131, v6, v7\n\t" \
  "v_exp_f32 v64, v64\n\t" \
  "v_mfma_f32_16x16x32_bf16 v[104:107], a[36:39], v[116:119], v[104:107]\n\t" \
  "v_cvt_pk_bf16_f32 v132, v8, v9\n\t" \
  "v_exp_f32 v65, v65\n\t" \
  "v_max3_f32 v192, v100, v101, v102\n\t" \
  "v_mfma_f32_16x16x32_bf16 v[104:107], a[40:43], v[120:123], v[104:107]\n\t" \
  "v_cvt_pk_bf16_f32 v133, v10, v11\n\t" \
  "v_exp_f32 v66, v66\n\t" \
  "v_mfma_f32_16x16x32_bf16 v[104:107], a[44:47], v[124:127], v[104:107]\n\t" \
  "v_cvt_pk_bf16_f32 v134, v12, v13\n\t" \
  "v_exp_f32 v67, v67\n\t" \
  "v_mfma_f32_16x16x32_bf16 v[108:111], a[48:51], v[112:115], v[188:191]\n\t" \
  "v_cvt_pk_bf16_f32 v135, v14, v15\n\t" \
  "v_exp_f32 v68, v68\n\t" \
  "v_mfma_f32_16x16x32_bf16 v[108:111], a[52:55], v[116:119], v[108:111]\n\t" \
  "v_exp_f32 v69, v69\n\t" \
  "v_mfma_f32_16x16x32_bf16 v[108:111], a[56:59], v[120:123], v[108:111]\n\t" \
  "v_max3_f32 v203, v103, v104, v105\n\t" \
  "v_max3_f32 v192, v192, v106, v107\n\t" \
  "v_mfma_f32_16x16x32_bf16 v[108:111], a[60:63], v[124:127], v[108:111]\n\t" \
  "s_waitcnt lgkmcnt(0)\n\t" \
  "buffer_load_dwordx4 v231, s[64:67], s96 offen offset:3072 lds\n\t" \
  "v_mfma_f32_16x16x32_bf16 v[0:3], a[192:195], v[16:19], v[176:179]\n\t" \
  "ds_read_b64_tr_b16 a[128:129], v228 offset:0\n\t" \
  "v_cvt_pk_bf16_f32 v136, v32, v33\n\t" \
  "v_mfma_f32_16x16x32_bf16 v[0:3], a[196:199], v[20:23], v[0:3]\n\t" \
  "v_cvt_pk_bf16_f32 v137, v34, v35\n\t" \
  "v_exp_f32 v70, v70\n\t" \
  "v_mfma_f32_16x16x32_bf16 v[0:3], a[200:203], v[24:27], v[0:3]\n\t" \
  "v_max3_f32 v203, v203, v108, v109\n\t" \
  "v_max3_f32 v192, v192, v110, v111\n\t" \
  "v_mfma_f32_16x16x32_bf16 v[0:3], a[204:207], v[28:31], v[0:3]\n\t" \
  "v_max_f32 v192, v192, v203\n\t" \
  "v_cmp_lt_f32 vcc, 0x42800000, v192\n\t" \
  "v_mfma_f32_16x16x32_bf16 v[4:7], a[220:223], v[28:31], v[176:179]\n\t" \
  "ds_read_b64_tr_b16 a[130:131], v228 offset:4096\n\t" \
  "v_exp_f32 v71, v71\n\t" \
  "v_mfma_f32_16x16x32_bf16 v[4:7], a[216:219], v[24:27], v[4:7]\n\t" \
  "s_cbranch_vccnz .Lsf_c03\n\t" \
  ".Lsf_r03:\n\t" \
  "v_mfma_f32_16x16x32_bf16 v[4:7], a[212:215], v[20:23], v[4:7]\n\t" \
  "ds_read_b64_tr_b16 a[132:133], v228 offset:8192\n\t" \
  "v_exp_f32 v72, v72\n\t" \
  "v_mfma_f32_16x16x32_bf16 v[4:7], a[208:211], v[16:19], v[4:7]\n\t" \
  "s_cmp_lg_u32 s99, 0\n\t" \
  "s_cbranch_scc1 .Lsf_p0\n\t" \
  ".Lsf_q0:\n\t" \
  "v_mfma_f32_16x16x32_bf16 v[8:11], a[224:227], v[16:19], v[176:179]\n\t" \
  "ds_read_b64_tr_b16 a[134:135], v228 offset:12288\n\t" \
  "v_max3_f32 v192, v0, v1, v2\n\t" \
  "v_mfma_f32_16x16x32_bf16 v[8:11], a[228:231], v[20:23], v[8:11]\n\t" \
  "ds_read_b64_tr_b16 a[136:137], v229 offset:0\n\t" \
  "v_exp_f32 v73, v73\n\t" \
  "v_mfma_f32_16x16x32_bf16 v[8:11], a[232:235], v[24:27], v[8:11]\n\t" \
  "v_max3_f32 v203, v3, v4, v5\n\t" \
  "v_max3_f32 v192, v192, v6, v7\n\t" \
  "v_mfma_f32_16x16x32_bf16 v[8:11], a[236:239], v[28:31], v[8:11]\n\t" \
  "ds_read_b64_tr_b16 a[138:139], v229 offset:4096\n\t" \
  "v_exp_f32 v74, v74\n\t" \
  "v_mfma_f32_16x16x32_bf16 v[12:15], a[252:255], v[28:31], v[176:179]\n\t" \
  "ds_read_b64_tr_b16 a[140:141], v229 offset:8192\n\t" \
  "v_exp_f32 v75, v75\n\t" \
  "v_mfma_f32_16x16x32_bf16 v[12:15], a[248:251], v[24:27], v[12:15]\n\t" \
  "ds_read_b64_tr_b16 a[142:143], v229 offset:12288\n\t" \
  "v_cvt_pk_bf16_f32 v138, v36, v37\n\t" \
  "v_mfma_f32_16x16x32_bf16 v[12:15], a[244:247], v[20:23], v[12:15]\n\t" \
  "v_max3_f32 v203, v203, v8, v9\n\t" \
  "v_max3_f32 v192, v192, v10, v11\n\t" \
  "v_mfma_f32_16x16x32_bf16 v[12:15], a[240:243], v[16:19], v[12:15]\n\t" \
  "ds_read_b64_tr_b16 a[144:145], v228 offset:256\n\t" \
  "v_exp_f32 v76, v76\n\t" \
  "v_mfma_f32_16x16x32_bf16 v[32:35], a[192:195], v[48:51], v[180:183]\n\t" \
  "v_cvt_pk_bf16_f32 v139, v38, v39\n\t" \
  "v_exp_f32 v77, v77\n\t" \
  "v_mfma_f32_16x16x32_bf16 v[32:35], a[196:199], v[52:55], v[32:35]\n\t" \
  "ds_read_b64_tr_b16 a[146:147], v228 offset:4352\n\t" \
  "v_cvt_pk_bf16_f32 v140, v40, v41\n\t" \
  "v_mfma_f32_16x16x32_bf16 v[32:35], a[200:203], v[56:59], v[32:35]\n\t" \
  "v_max3_f32 v203, v203, v12, v13\n\t" \
  "v_max3_f32 v192, v192, v14, v15\n\t" \
  "v_mfma_f32_16x16x32_bf16 v[32:35], a[204:207], v[60:63], v[32:35]\n\t" \
  "v_max_f32 v192, v192, v203\n\t" \
  "v_cmp_lt_f32 vcc, 0x42800000, v192\n\t" \
  "v_mfma_f32_16x16x32_bf16 v[36:39], a[220:223], v[60:63], v[180:183]\n\t" \
  "ds_read_b64_tr_b16 a[148:149], v228 offset:8448\n\t" \
  "v_exp_f32 v78, v78\n\t" \
  "v_mfma_f32_16x16x32_bf16 v[36:39], a[216:219], v[56:59], v[36:39]\n\t" \
  "s_cbranch_vccnz .Lsf_c10\n\t" \
  ".Lsf_r10:\n\t" \
  "v_mfma_f32_16x16x32_bf16 v[36:39], a[212:215], v[52:55], v[36:39]\n\t" \
  "v_max3_f32 v192, v32, v33, v34\n\t" \
  "v_exp_f32 v79, v79\n\t" \
  "v_cvt_pk_bf16_f32 v141, v42, v43\n\t" \
  "v_mfma_f32_16x16x32_bf16 v[36:39], a[208:211], v[48:51], v[36:39]\n\t" \
  "ds_read_b64_tr_b16 a[150:151], v228 offset:12544\n\t" \
  "v_exp_f32 v96, v96\n\t" \
  "v_mfma_f32_16x16x32_bf16 v[40:43], a[224:227], v[48:51], v[180:183]\n\t" \
  "ds_read_b64_tr_b16 a[152:153], v229 offset:256\n\t" \
  "v_exp_f32 v97, v97\n\t" \
  "v_mfma_f32_16x16x32_bf16 v[40:43], a[228:231], v[52:55], v[40:43]\n\t" \
  "ds_read_b64_tr_b16 a[154:155], v229 offset:4352\n\t" \
  "v_cvt_pk_bf16_f32 v142, v44, v45\n\t" \
  "v_mfma_f32_16x16x32_bf16 v[40:43], a[232:235], v[56:59], v[40:43]\n\t" \
  "v_max3_f32 v203, v35, v36, v37\n\t" \
  "v_max3_f32 v192, v192, v38, v39\n\t" \
  "v_mfma_f32_16x16x32_bf16 v[40:43], a[236:239], v[60:63], v[40:43]\n\t" \
  "ds_read_b64_tr_b16 a[156:157], v229 offset:8448\n\t" \
  "v_exp_f32 v98, v98\n\t" \
  "v_cvt_pk_bf16_f32 v143, v46, v47\n\t" \
  "v_mfma_f32_16x16x32_bf16 v[44:47], a[252:255], v[60:63], v[180:183]\n\t" \
  "ds_read_b64_tr_b16 a[158:159], v229 offset:12544\n\t" \
  "v_exp_f32 v99, v99\n\t" \
  "v_mfma_f32_16x16x32_bf16 v[44:47], a[248:251], v[56:59], v[44:47]\n\t" \
  "v_cvt_pk_bf16_f32 v144, v64, v65\n\t" \
  "v_max3_f32 v203, v203, v40, v41\n\t" \
  "v_mfma_f32_16x16x32_bf16 v[44:47], a[244:247], v[52:55], v[44:47]\n\t" \
  "v_max3_f32 v192, v192, v42, v43\n\t" \
  "v_exp_f32 v100, v100\n\t" \
  "v_mfma_f32_16x16x32_bf16 v[44:47], a[240:243], v[48:51], v[44:47]\n\t" \
  "ds_read_b64_tr_b16 a[160:161], v228 offset:512\n\t" \
  "v_cvt_pk_bf16_f32 v145, v66, v67\n\t" \
  "v_mfma_f32_16x16x32_bf16 v[64:67], a[192:195], v[80:83], v[184:187]\n\t" \
  "ds_read_b64_tr_b16 a[162:163], v228 offset:4608\n\t" \
  "v_exp_f32 v101, v101\n\t" \
  "v_mfma_f32_16x16x32_bf16 v[64:67], a[196:199], v[84:87], v[64:67]\n\t" \
  "v_cvt_pk_bf16_f32 v146, v68, v69\n\t" \
  "v_cvt_pk_bf16_f32 v147, v70, v71\n\t" \
  "v_mfma_f32_16x16x32_bf16 v[64:67], a[200:203], v[88:91], v[64:67]\n\t" \
  "v_max3_f32 v203, v203, v44, v45\n\t" \
  "v_max3_f32 v192, v192, v46, v47\n\t" \
  "v_mfma_f32_16x16x32_bf16 v[64:67], a[204:207], v[92:95], v[64:67]\n\t" \
  "v_max_f32 v192, v192, v203\n\t" \
  "v_cmp_lt_f32 vcc, 0x42800000, v192\n\t" \
  "v_mfma_f32_16x16x32_bf16 v[68:71], a[220:223], v[92:95], v[184:187]\n\t" \
  "ds_read_b64_tr_b16 a[164:165], v228 offset:8704\n\t" \
  "v_exp_f32 v102, v102\n\t" \
  "v_mfma_f32_16x16x32_bf16 v[68:71], a[216:219], v[88:91], v[68:71]\n\t" \
  "s_cbranch_vccnz .Lsf_c11\n\t" \
  ".Lsf_r11:\n\t" \
  "v_mfma_f32_16x16x32_bf16 v[68:71], a[212:215], v[84:87], v[68:71]\n\t" \
  "v_max3_f32 v192, v64, v65, v66\n\t" \
  "v_exp_f32 v103, v103\n\t" \
  "v_cvt_pk_bf16_f32 v148, v72, v73\n\t" \
  "v_cvt_pk_bf16_f32 v149, v74, v75\n\t" \
  "v_mfma_f32_16x16x32_bf16 v[68:71], a[208:211], v[80:83], v[68:71]\n\t" \
  "ds_read_b64_tr_b16 a[166:167], v228 offset:12800\n\t" \
  "v_exp_f32 v104, v104\n\t" \
  "v_mfma_f32_16x16x32_bf16 v[72:75], a[224:227], v[80:83], v[184:187]\n\t" \
  "ds_read_b64_tr_b16 a[168:169], v229 offset:512\n\t" \
  "v_exp_f32 v105, v105\n\t" \
  "v_mfma_f32_16x16x32_bf16 v[72:75], a[228:231], v[84:87], v[72:75]\n\t" \
  "ds_read_b64_tr_b16 a[170:171], v229 offset:4608\n\t" \
  "v_cvt_pk_bf16_f32 v150, v76, v77\n\t" \
  "v_mfma_f32_16x16x32_bf16 v[72:75], a[232:235], v[88:91], v[72:75]\n\t" \
  "v_max3_f32 v203, v67, v68, v69\n\t" \
  "v_max3_f32 v192, v192, v70, v71\n\t" \
  "v_mfma_f32_16x16x32_bf16 v[72:75], a[236:239], v[92:95], v[72:75]\n\t" \
  "ds_read_b64_tr_b16 a[172:173], v229 offset:8704\n\t" \
  "v_exp_f32 v106, v106\n\t" \
  "v_cvt_pk_bf16_f32 v151, v78, v79\n\t" \
  "v_mfma_f32_16x16x32_bf16 v[76:79], a[252:255], v[92:95], v[184:187]\n\t" \
  "ds_read_b64_tr_b16 a[174:175], v229 offset:12800\n\t" \
  "v_exp_f32 v107, v107\n\t" \
  "v_mfma_f32_16x16x32_bf16 v[76:79], a[248:251], v[88:91], v[76:79]\n\t" \
  "ds_read_b64_tr_b16 a[176:177], v228 offset:768\n\t" \
  "v_max3_f32 v203, v203, v72, v73\n\t" \
  "v_mfma_f32_16x16x32_bf16 v[76:79], a[244:247], v[84:87], v[76:79]\n\t" \
  "v_max3_f32 v192, v192, v74, v75\n\t" \
  "v_exp_f32 v108, v108\n\t" \
  "v_mfma_f32_16x16x32_bf16 v[76:79], a[240:243], v[80:83], v[76:79]\n\t" \
  "ds_read_b64_tr_b16 a[178:179], v228 offset:4864\n\t" \
  "v_cvt_pk_bf16_f32 v152, v96, v97\n\t" \
  "v_cvt_pk_bf16_f32 v153, v98, v99\n\t" \
  "v_mfma_f32_16x16x32_bf16 v[96:99], a[192:195], v[112:115], v[188:191]\n\t" \
  "ds_read_b64_tr_b16 a[180:181], v228 offset:8960\n\t" \
  "v_exp_f32 v109, v109\n\t" \
  "v_mfma_f32_16x16x32_bf16 v[96:99], a[196:199], v[116:119], v[96:99]\n\t" \
  "v_cvt_pk_bf16_f32 v154, v100, v101\n\t" \
  "v_max3_f32 v203, v203, v76, v77\n\t" \
  "v_mfma_f32_16x16x32_bf16 v[96:99], a[200:203], v[120:123], v[96:99]\n\t" \
  "v_max3_f32 v192, v192, v78, v79\n\t" \
  "v_max_f32 v192, v192, v203\n\t" \
  "v_mfma_f32_16x16x32_bf16 v[96:99], a[204:207], v[124:127], v[96:99]\n\t" \
  "v_cmp_lt_f32 vcc, 0x42800000, v192\n\t" \
  "v_exp_f32 v110, v110\n\t" \
  "v_cvt_pk_bf16_f32 v155, v102, v103\n\t" \
  "v_mfma_f32_16x16x32_bf16 v[100:103], a[220:223], v[124:127], v[188:191]\n\t" \
  "ds_read_b64_tr_b16 a[182:183], v228 offset:13056\n\t" \
  "v_exp_f32 v111, v111\n\t" \
  "v_mfma_f32_16x16x32_bf16 v[100:103], a[216:219], v[120:123], v[100:103]\n\t" \
  "s_cbranch_vccnz .Lsf_c12\n\t" \
  ".Lsf_r12:\n\t" \
  "v_mfma_f32_16x16x32_bf16 v[100:103], a[212:215], v[116:119], v[100:103]\n\t" \
  "ds_read_b64_tr_b16 a[184:185], v229 offset:768\n\t" \
  "v_max3_f32 v192, v96, v97, v98\n\t" \
  "v_mfma_f32_16x16x32_bf16 v[100:103], a[208:211], v[112:115], v[100:103]\n\t" \
  "ds_read_b64_tr_b16 a[186:187], v229 offset:4864\n\t" \
  "v_cvt_pk_bf16_f32 v156, v104, v105\n\t" \
  "v_cvt_pk_bf16_f32 v157, v106, v107\n\t" \
  "v_mfma_f32_16x16x32_bf16 v[104:107], a[224:227], v[112:115], v[188:191]\n\t" \
  "ds_read_b64_tr_b16 a[188:189], v229 offset:8960\n\t" \
  "v_cvt_pk_bf16_f32 v158, v108, v109\n\t" \
  "v_mfma_f32_16x16x32_bf16 v[104:107], a[228:231], v[116:119], v[104:107]\n\t" \
  "ds_read_b64_tr_b16 a[190:191], v229 offset:13056\n\t" \
  "v_cvt_pk_bf16_f32 v159, v110, v111\n\t" \
  "v_mfma_f32_16x16x32_bf16 v[104:107], a[232:235], v[120:123], v[104:107]\n\t" \
  "v_max3_f32 v203, v99, v100, v101\n\t" \
  "v_max3_f32 v192, v192, v102, v103\n\t" \
  "v_mfma_f32_16x16x32_bf16 v[104:107], a[236:239], v[124:127], v[104:107]\n\t" \
  "v_exp_f32 v0, v0\n\t" \
  "v_mfma_f32_16x16x32_bf16 v[108:111], a[252:255], v[124:127], v[188:191]\n\t" \
  "v_exp_f32 v1, v1\n\t" \
  "v_mfma_f32_16x16x32_bf16 v[108:111], a[248:251], v[120:123], v[108:111]\n\t" \
  "v_exp_f32 v2, v2\n\t" \
  "v_mfma_f32_16x16x32_bf16 v[108:111], a[244:247], v[116:119], v[108:111]\n\t" \
  "s_waitcnt vmcnt(0) lgkmcnt(0)\n\t" \
  "v_exp_f32 v3, v3\n\t" \
  "v_mfma_f32_16x16x32_bf16 v[108:111], a[240:243], v[112:115], v[108:111]\n\t" \
  "s_barrier\n\t" \
  "v_mfma_f32_16x16x32_bf16 a[0:3], a[128:131], v[128:131], 0\n\t" \
  "v_max3_f32 v203, v203, v104, v105\n\t" \
  "v_max3_f32 v192, v192, v106, v107\n\t" \
  "v_mfma_f32_16x16x32_bf16 a[0:3], a[132:135], v[132:135], a[0:3]\n\t" \
  "v_exp_f32 v4, v4\n\t" \
  "v_mfma_f32_16x16x32_bf16 a[4:7], a[136:139], v[128:131], 0\n\t" \
  "v_exp_f32 v5, v5\n\t" \
  "v_mfma_f32_16x16x32_bf16 a[4:7], a[140:143], v[132:135], a[4:7]\n\t" \
  "v_max3_f32 v203, v203, v108, v109\n\t" \
  "v_max3_f32 v192, v192, v110, v111\n\t" \
  "v_mfma_f32_16x16x32_bf16 a[8:11], a[144:147], v[128:131], 0\n\t" \
  "v_max_f32 v192, v192, v203\n\t" \
  "v_cmp_lt_f32 vcc, 0x42800000, v192\n\t" \
  "v_mfma_f32_16x16x32_bf16 a[8:11], a[148:151], v[132:135], a[8:11]\n\t" \
  "v_exp_f32 v6, v6\n\t" \
  "v_mfma_f32_16x16x32_bf16 a[12:15], a[152:155], v[128:131], 0\n\t" \
  "s_cbranch_vccnz .Lsf_c13\n\t" \
  ".Lsf_r13:\n\t" \
  "v_mfma_f32_16x16x32_bf16 a[12:15], a[156:159], v[132:135], a[12:15]\n\t" \
  "s_add_u32 m0, %[m], 65536\n\t" \
  "v_exp_f32 v7, v7\n\t" \
  "v_mfma_f32_16x16x32_bf16 a[16:19], a[160:163], v[128:131], 0\n\t" \
  "s_add_u32 s96, %[m], 49152\n\t" \
  "v_exp_f32 v8, v8\n\t" \
  "v_mfma_f32_16x16x32_bf16 a[16:19], a[164:167], v[132:135], a[16:19]\n\t" \
  "v_exp_f32 v9, v9\n\t" \
  "v_mfma_f32_16x16x32_bf16 a[20:23], a[168:171], v[128:131], 0\n\t" \
  "buffer_load_dwordx4 v224, s[60:63], s96 offen offset:0 lds\n\t" \
  "v_mfma_f32_16x16x32_bf16 a[20:23], a[172:175], v[132:135], a[20:23]\n\t" \
  "v_exp_f32 v10, v10\n\t" \
  "v_mfma_f32_16x16x32_bf16 a[24:27], a[176:179], v[128:131], 0\n\t" \
  "v_exp_f32 v11, v11\n\t" \
  "v_mfma_f32_16x16x32_bf16 a[24:27], a[180:183], v[132:135], a[24:27]\n\t" \
  "v_exp_f32 v12, v12\n\t" \
  "v_mfma_f32_16x16x32_bf16 a[28:31], a[184:187], v[128:131], 0\n\t" \
  "v_exp_f32 v13, v13\n\t" \
  "v_mfma_f32_16x16x32_bf16 a[28:31], a[188:191], v[132:135], a[28:31]\n\t" \
  "v_exp_f32 v14, v14\n\t" \
  "v_mfma_f32_16x16x32_bf16 v[160:163], v[216:219], v[128:131], 0\n\t" \
  "v_exp_f32 v15, v15\n\t" \
  "v_mfma_f32_16x16x32_bf16 v[160:163], v[216:219], v[132:135], v[160:163]\n\t" \
  "buffer_load_dwordx4 v225, s[60:63], s96 offen offset:1024 lds\n\t" \
  "v_mfma_f32_16x16x32_bf16 a[32:35], a[128:131], v[136:139], 0\n\t" \
  "ds_read_b128 a[192:195], v220 offset:0\n\t" \
  "v_cvt_pk_bf16_f32 v128, v0, v1\n\t" \
  "v_mfma_f32_16x16x32_bf16 a[32:35], a[132:135], v[140:143], a[32:35]\n\t" \
  "ds_read_b128 a[196:199], v221 offset:0\n\t" \
  "v_cvt_pk_bf16_f32 v129, v2, v3\n\t" \
  "v_mfma_f32_16x16x32_bf16 a[36:39], a[136:139], v[136:139], 0\n\t" \
  "v_exp_f32 v32, v32\n\t" \
  "v_mfma_f32_16x16x32_bf16 a[36:39], a[140:143], v[140:143], a[36:39]\n\t" \
  "ds_read_b128 a[200:203], v222 offset:0\n\t" \
  "v_cvt_pk_bf16_f32 v130, v4, v5\n\t" \
  "v_mfma_f32_16x16x32_bf16 a[40:43], a[144:147], v[136:139], 0\n\t" \
  "v_exp_f32 v33, v33\n\t" \
  "v_mfma_f32_16x16x32_bf16 a[40:43], a[148:151], v[140:143], a[40:43]\n\t" \
  "buffer_load_dwordx4 v226, s[60:63], s96 offen offset:2048 lds\n\t" \
  "v_mfma_f32_16x16x32_bf16 a[44:47], a[152:155], v[136:139], 0\n\t" \
  "ds_read_b128 a[204:207], v223 offset:0\n\t" \
  "v_cvt_pk_bf16_f32 v131, v6, v7\n\t" \
  "v_mfma_f32_16x16x32_bf16 a[44:47], a[156:159], v[140:143], a[44:47]\n\t" \
  "v_exp_f32 v34, v34\n\t" \
  "v_mfma_f32_16x16x32_bf16 a[48:51], a[160:163], v[136:139], 0\n\t" \
  "v_exp_f32 v35, v35\n\t" \
  "v_mfma_f32_16x16x32_bf16 a[48:51], a[164:167], v[140:143], a[48:51]\n\t" \
  "ds_read_b128 a[208:211], v220 offset:4096\n\t" \
  "v_cvt_pk_bf16_f32 v132, v8, v9\n\t" \
  "v_mfma_f32_16x16x32_bf16 a[52:55], a[168:171], v[136:139], 0\n\t" \
  "v_exp_f32 v36, v36\n\t" \
  "v_mfma_f32_16x16x32_bf16 a[52:55], a[172:175], v[140:143], a[52:55]\n\t" \
  "buffer_load_dwordx4 v227, s[60:63], s96 offen offset:3072 lds\n\t" \
  "v_mfma_f32_16x16x32_bf16 a[56:59], a[176:179], v[136:139], 0\n\t" \
  "ds_read_b128 a[212:215], v221 offset:4096\n\t" \
  "v_cvt_pk_bf16_f32 v133, v10, v11\n\t" \
  "v_mfma_f32_16x16x32_bf16 a[56:59], a[180:183], v[140:143], a[56:59]\n\t" \
  "s_add_u32 m0, %[m], 81920\n\t" \
  "v_exp_f32 v37, v37\n\t" \
  "v_mfma_f32_16x16x32_bf16 a[60:63], a[184:187], v[136:139], 0\n\t" \
  "s_add_u32 s96, %[m], 32768\n\t" \
  "v_exp_f32 v38, v38\n\t" \
  "v_mfma_f32_16x16x32_bf16 a[60:63], a[188:191], v[140:143], a[60:63]\n\t" \
  "ds_read_b128 a[216:219], v222 offset:4096\n\t" \
  "v_cvt_pk_bf16_f32 v134, v12, v13\n\t" \
  "v_mfma_f32_16x16x32_bf16 v[164:167], v[216:219], v[136:139], 0\n\t" \
  "v_exp_f32 v39, v39\n\t" \
  "v_mfma_f32_16x16x32_bf16 v[164:167], v[216:219], v[140:143], v[164:167]\n\t" \
  "buffer_load_dwordx4 v230, s[64:67], s96 offen offset:0 lds\n\t" \
  "v_mfma_f32_16x16x32_bf16 a[64:67], a[128:131], v[144:147], 0\n\t" \
  "ds_read_b128 a[220:223], v223 offset:4096\n\t" \
  "v_cvt_pk_bf16_f32 v135, v14, v15\n\t" \
  "v_mfma_f32_16x16x32_bf16 a[64:67], a[132:135], v[148:151], a[64:67]\n\t" \
  "v_exp_f32 v40, v40\n\t" \
  "v_mfma_f32_16x16x32_bf16 a[68:71], a[136:139], v[144:147], 0\n\t" \
  "ds_read_b128 a[224:227], v220 offset:8192\n\t" \
  "v_cvt_pk_bf16_f32 v136, v32, v33\n\t" \
  "v_mfma_f32_16x16x32_bf16 a[68:71], a[140:143], v[148:151], a[68:71]\n\t" \
  "v_exp_f32 v41, v41\n\t" \
  "v_mfma_f32_16x16x32_bf16 a[72:75], a[144:147], v[144:147], 0\n\t" \
  "v_exp_f32 v42, v42\n\t" \
  "v_mfma_f32_16x16x32_bf16 a[72:75], a[148:151], v[148:151], a[72:75]\n\t" \
  "ds_read_b128 a[228:231], v221 offset:8192\n\t" \
  "v_cvt_pk_bf16_f32 v137, v34, v35\n\t" \
  "v_mfma_f32_16x16x32_bf16 a[76:79], a[152:155], v[144:147], 0\n\t" \
  "buffer_load_dwordx4 v231, s[64:67], s96 offen offset:1024 lds\n\t" \
  "v_mfma_f32_16x16x32_bf16 a[76:79], a[156:159], v[148:151], a[76:79]\n\t" \
  "v_exp_f32 v43, v43\n\t" \
  "v_mfma_f32_16x16x32_bf16 a[80:83], a[160:163], v[144:147], 0\n\t" \
  "ds_read_b128 a[232:235], v222 offset:8192\n\t" \
  "v_cvt_pk_bf16_f32 v138, v36, v37\n\t" \
  "v_mfma_f32_16x16x32_bf16 a[80:83], a[164:167], v[148:151], a[80:83]\n\t" \
  "v_exp_f32 v44, v44\n\t" \
  "v_mfma_f32_16x16x32_bf16 a[84:87], a[168:171], v[144:147], 0\n\t" \
  "v_exp_f32 v45, v45\n\t" \
  "v_mfma_f32_16x16x32_bf16 a[84:87], a[172:175], v[148:151], a[84:87]\n\t" \
  "ds_read_b128 a[236:239], v223 offset:8192\n\t" \
  "v_cvt_pk_bf16_f32 v139, v38, v39\n\t" \
  "v_mfma_f32_16x16x32_bf16 a[88:91], a[176:179], v[144:147], 0\n\t" \
  "buffer_load_dwordx4 v230, s[64:67], s96 offen offset:2048 lds\n\t" \
  "v_mfma_f32_16x16x32_bf16 a[88:91], a[180:183], v[148:151], a[88:91]\n\t" \
  "v_exp_f32 v46, v46\n\t" \
  "v_mfma_f32_16x16x32_bf16 a[92:95], a[184:187], v[144:147], 0\n\t" \
  "ds_read_b128 a[240:243], v220 offset:12288\n\t" \
  "v_cvt_pk_bf16_f32 v140, v40, v41\n\t" \
  "v_mfma_f32_16x16x32_bf16 a[92:95], a[188:191], v[148:151], a[92:95]\n\t" \
  "v_exp_f32 v47, v47\n\t" \
  "v_mfma_f32_16x16x32_bf16 v[168:171], v[216:219], v[144:147], 0\n\t" \
  "v_exp_f32 v64, v64\n\t" \
  "v_mfma_f32_16x16x32_bf16 v[168:171], v[216:219], v[148:151], v[168:171]\n\t" \
  "ds_read_b128 a[244:247], v221 offset:12288\n\t" \
  "v_cvt_pk_bf16_f32 v141, v42, v43\n\t" \
  "v_mfma_f32_16x16x32_bf16 a[96:99], a[128:131], v[152:155], 0\n\t" \
  "buffer_load_dwordx4 v231, s[64:67], s96 offen offset:3072 lds\n\t" \
  "v_mfma_f32_16x16x32_bf16 a[96:99], a[132:135], v[156:159], a[96:99]\n\t" \
  "v_exp_f32 v65, v65\n\t" \
  "v_mfma_f32_16x16x32_bf16 a[100:103], a[136:139], v[152:155], 0\n\t" \
  "ds_read_b128 a[248:251], v222 offset:12288\n\t" \
  "v_cvt_pk_bf16_f32 v142, v44, v45\n\t" \
  "v_mfma_f32_16x16x32_bf16 a[100:103], a[140:143], v[156:159], a[100:103]\n\t" \
  "v_exp_f32 v66, v66\n\t" \
  "v_mfma_f32_16x16x32_bf16 a[104:107], a[144:147], v[152:155], 0\n\t" \
  "v_exp_f32 v67, v67\n\t" \
  "v_mfma_f32_16x16x32_bf16 a[104:107], a[148:151], v[156:159], a[104:107]\n\t" \
  "ds_read_b128 a[252:255], v223 offset:12288\n\t" \
  "v_cvt_pk_bf16_f32 v143, v46, v47\n\t" \
  "v_mfma_f32_16x16x32_bf16 a[108:111], a[152:155], v[152:155], 0\n\t" \
  "v_exp_f32 v68, v68\n\t" \
  "v_mfma_f32_16x16x32_bf16 a[108:111], a[156:159], v[156:159], a[108:111]\n\t" \
  "v_exp_f32 v69, v69\n\t" \
  "v_mfma_f32_16x16x32_bf16 a[112:115], a[160:163], v[152:155], 0\n\t" \
  "v_exp_f32 v70, v70\n\t" \
  "v_mfma_f32_16x16x32_bf16 a[112:115], a[164:167], v[156:159], a[112:115]\n\t" \
  "v_exp_f32 v71, v71\n\t" \
  "v_mfma_f32_16x16x32_bf16 a[116:119], a[168:171], v[152:155], 0\n\t" \
  "v_exp_f32 v72, v72\n\t" \
  "v_mfma_f32_16x16x32_bf16 a[116:119], a[172:175], v[156:159], a[116:119]\n\t" \
  "v_exp_f32 v73, v73\n\t" \
  "v_mfma_f32_16x16x32_bf16 a[120:123], a[176:179], v[152:155], 0\n\t" \
  "v_exp_f32 v74, v74\n\t" \
  "v_mfma_f32_16x16x32_bf16 a[120:123], a[180:183], v[156:159], a[120:123]\n\t" \
  "v_exp_f32 v75, v75\n\t" \
  "v_mfma_f32_16x16x32_bf16 a[124:127], a[184:187], v[152:155], 0\n\t" \
  "v_exp_f32 v76, v76\n\t" \
  "v_mfma_f32_16x16x32_bf16 a[124:127], a[188:191], v[156:159], a[124:127]\n\t" \
  "v_exp_f32 v77, v77\n\t" \
  "v_mfma_f32_16x16x32_bf16 v[172:175], v[216:219], v[152:155], 0\n\t" \
  "v_exp_f32 v78, v78\n\t" \
  "v_mfma_f32_16x16x32_bf16 v[172:175], v[216:219], v[156:159], v[172:175]\n\t" \
  "s_waitcnt lgkmcnt(12)\n\t" \
  "s_cmp_lg_u32 s99, 0\n\t" \
  "s_cbranch_scc1 .Lsf_p1\n\t" \
  ".Lsf_q1:\n\t" \
  "v_mfma_f32_16x16x32_bf16 v[0:3], a[192:195], v[16:19], v[176:179]\n\t" \
  "ds_read_b64_tr_b16 a[128:129], v210 offset:0\n\t" \
  "v_cvt_pk_bf16_f32 v144, v64, v65\n\t" \
  "v_mfma_f32_16x16x32_bf16 v[0:3], a[196:199], v[20:23], v[0:3]\n\t" \
  "v_exp_f32 v79, v79\n\t" \
  "v_mfma_f32_16x16x32_bf16 v[0:3], a[200:203], v[24:27], v[0:3]\n\t" \
  "ds_read_b64_tr_b16 a[130:131], v210 offset:4096\n\t" \
  "v_cvt_pk_bf16_f32 v145, v66, v67\n\t" \
  "v_mfma_f32_16x16x32_bf16 v[0:3], a[204:207], v[28:31], v[0:3]\n\t" \
  "s_waitcnt lgkmcnt(10)\n\t" \
  "v_exp_f32 v96, v96\n\t" \
  "v_mfma_f32_16x16x32_bf16 v[4:7], a[220:223], v[28:31], v[176:179]\n\t" \
  "ds_read_b64_tr_b16 a[132:133], v210 offset:8192\n\t" \
  "v_cvt_pk_bf16_f32 v146, v68, v69\n\t" \
  "v_mfma_f32_16x16x32_bf16 v[4:7], a[216:219], v[24:27], v[4:7]\n\t" \
  "v_exp_f32 v97, v97\n\t" \
  "v_mfma_f32_16x16x32_bf16 v[4:7], a[212:215], v[20:23], v[4:7]\n\t" \
  "ds_read_b64_tr_b16 a[134:135], v210 offset:12288\n\t" \
  "v_max3_f32 v192, v0, v1, v2\n\t" \
  "v_mfma_f32_16x16x32_bf16 v[4:7], a[208:211], v[16:19], v[4:7]\n\t" \
  "s_waitcnt lgkmcnt(8)\n\t" \
  "ds_read_b64_tr_b16 a[136:137], v211 offset:0\n\t" \
  "v_cvt_pk_bf16_f32 v147, v70, v71\n\t" \
  "v_mfma_f32_16x16x32_bf16 v[8:11], a[224:227], v[16:19], v[176:179]\n\t" \
  "v_exp_f32 v98, v98\n\t" \
  "v_mfma_f32_16x16x32_bf16 v[8:11], a[228:231], v[20:23], v[8:11]\n\t" \
  "ds_read_b64_tr_b16 a[138:139], v211 offset:4096\n\t" \
  "v_cvt_pk_bf16_f32 v148, v72, v73\n\t" \
  "v_mfma_f32_16x16x32_bf16 v[8:11], a[232:235], v[24:27], v[8:11]\n\t" \
  "v_max3_f32 v203, v3, v4, v5\n\t" \
  "v_max3_f32 v192, v192, v6, v7\n\t" \
  "v_mfma_f32_16x16x32_bf16 v[8:11], a[236:239], v[28:31], v[8:11]\n\t" \
  "s_waitcnt lgkmcnt(6)\n\t" \
  "ds_read_b64_tr_b16 a[140:141], v211 offset:8192\n\t" \
  "v_cvt_pk_bf16_f32 v149, v74, v75\n\t" \
  "v_mfma_f32_16x16x32_bf16 v[12:15], a[252:255], v[28:31], v[176:179]\n\t" \
  "v_exp_f32 v99, v99\n\t" \
  "v_mfma_f32_16x16x32_bf16 v[12:15], a[248:251], v[24:27], v[12:15]\n\t" \
  "ds_read_b64_tr_b16 a[142:143], v211 offset:12288\n\t" \
  "v_cvt_pk_bf16_f32 v150, v76, v77\n\t" \
  "v_mfma_f32_16x16x32_bf16 v[12:15], a[244:247], v[20:23], v[12:15]\n\t" \
  "v_max3_f32 v203, v203, v8, v9\n\t" \
  "v_max3_f32 v192, v192, v10, v11\n\t" \
  "v_mfma_f32_16x16x32_bf16 v[12:15], a[240:243], v[16:19], v[12:15]\n\t" \
  "ds_read_b64_tr_b16 a[144:145], v210 offset:256\n\t" \
  "v_cvt_pk_bf16_f32 v151, v78, v79\n\t" \
  "v_mfma_f32_16x16x32_bf16 v[32:35], a[192:195], v[48:51], v[180:183]\n\t" \
  "ds_read_b64_tr_b16 a[146:147], v210 offset:4352\n\t" \
  "v_cvt_pk_bf16_f32 v152, v96, v97\n\t" \
  "v_mfma_f32_16x16x32_bf16 v[32:35], a[196:199], v[52:55], v[32:35]\n\t" \
  "v_exp_f32 v100, v100\n\t" \
  "v_mfma_f32_16x16x32_bf16 v[32:35], a[200:203], v[56:59], v[32:35]\n\t" \
  "v_max3_f32 v203, v203, v12, v13\n\t" \
  "v_max3_f32 v192, v192, v14, v15\n\t" \
  "v_mfma_f32_16x16x32_bf16 v[32:35], a[204:207], v[60:63], v[32:35]\n\t" \
  "v_max_f32 v192, v192, v203\n\t" \
  "v_cmp_lt_f32 vcc, 0x42800000, v192\n\t" \
  "v_mfma_f32_16x16x32_bf16 v[36:39], a[220:223], v[60:63], v[180:183]\n\t" \
  "ds_read_b64_tr_b16 a[148:149], v210 offset:8448\n\t" \
  "v_cvt_pk_bf16_f32 v153, v98, v99\n\t" \
  "v_mfma_f32_16x16x32_bf16 v[36:39], a[216:219], v[56:59], v[36:39]\n\t" \
  "s_cbranch_vccnz .Lsf_c20\n\t" \
  ".Lsf_r20:\n\t" \
  "v_mfma_f32_16x16x32_bf16 v[36:39], a[212:215], v[52:55], v[36:39]\n\t" \
  "ds_read_b64_tr_b16 a[150:151], v210 offset:12544\n\t" \
  "v_max3_f32 v192, v32, v33, v34\n\t" \
  "v_mfma_f32_16x16x32_bf16 v[36:39], a[208:211], v[48:51], v[36:39]\n\t" \
  "v_exp_f32 v101, v101\n\t" \
  "v_mfma_f32_16x16x32_bf16 v[40:43], a[224:227], v[48:51], v[180:183]\n\t" \
  "ds_read_b64_tr_b16 a[152:153], v211 offset:256\n\t" \
  "v_cvt_pk_bf16_f32 v154, v100, v101\n\t" \
  "v_mfma_f32_16x16x32_bf16 v[40:43], a[228:231], v[52:55], v[40:43]\n\t" \
  "v_exp_f32 v102, v102\n\t" \
  "v_mfma_f32_16x16x32_bf16 v[40:43], a[232:235], v[56:59], v[40:43]\n\t" \
  "ds_read_b64_tr_b16 a[154:155], v211 offset:4352\n\t" \
  "v_max3_f32 v203, v35, v36, v37\n\t" \
  "v_mfma_f32_16x16x32_bf16 v[40:43], a[236:239], v[60:63], v[40:43]\n\t" \
  "ds_read_b64_tr_b16 a[156:157], v211 offset:8448\n\t" \
  "v_max3_f32 v192, v192, v38, v39\n\t" \
  "v_mfma_f32_16x16x32_bf16 v[44:47], a[252:255], v[60:63], v[180:183]\n\t" \
  "v_exp_f32 v103, v103\n\t" \
  "v_mfma_f32_16x16x32_bf16 v[44:47], a[248:251], v[56:59], v[44:47]\n\t" \
  "ds_read_b64_tr_b16 a[158:159], v211 offset:12544\n\t" \
  "v_cvt_pk_bf16_f32 v155, v102, v103\n\t" \
  "v_mfma_f32_16x16x32_bf16 v[44:47], a[244:247], v[52:55], v[44:47]\n\t" \
  "v_max3_f32 v203, v203, v40, v41\n\t" \
  "v_max3_f32 v192, v192, v42, v43\n\t" \
  "v_mfma_f32_16x16x32_bf16 v[44:47], a[240:243], v[48:51], v[44:47]\n\t" \
  "v_exp_f32 v104, v104\n\t" \
  "v_mfma_f32_16x16x32_bf16 v[64:67], a[192:195], v[80:83], v[184:187]\n\t" \
  "v_exp_f32 v105, v105\n\t" \
  "v_mfma_f32_16x16x32_bf16 v[64:67], a[196:199], v[84:87], v[64:67]\n\t" \
  "ds_read_b64_tr_b16 a[160:161], v210 offset:512\n\t" \
  "v_cvt_pk_bf16_f32 v156, v104, v105\n\t" \
  "v_mfma_f32_16x16x32_bf16 v[64:67], a[200:203], v[88:91], v[64:67]\n\t" \
  "ds_read_b64_tr_b16 a[162:163], v210 offset:4608\n\t" \
  "v_max3_f32 v203, v203, v44, v45\n\t" \
  "v_mfma_f32_16x16x32_bf16 v[64:67], a[204:207], v[92:95], v[64:67]\n\t" \
  "v_max3_f32 v192, v192, v46, v47\n\t" \
  "v_max_f32 v192, v192, v203\n\t" \
  "v_mfma_f32_16x16x32_bf16 v[68:71], a[220:223], v[92:95], v[184:187]\n\t" \
  "ds_read_b64_tr_b16 a[164:165], v210 offset:8704\n\t" \
  "v_cmp_lt_f32 vcc, 0x42800000, v192\n\t" \
  "v_mfma_f32_16x16x32_bf16 v[68:71], a[216:219], v[88:91], v[68:71]\n\t" \
  "v_exp_f32 v106, v106\n\t" \
  "v_mfma_f32_16x16x32_bf16 v[68:71], a[212:215], v[84:87], v[68:71]\n\t" \
  "s_cbranch_vccnz .Lsf_c21\n\t" \
  ".Lsf_r21:\n\t" \
  "v_mfma_f32_16x16x32_bf16 v[68:71], a[208:211], v[80:83], v[68:71]\n\t" \
  "ds_read_b64_tr_b16 a[166:167], v210 offset:12800\n\t" \
  "v_max3_f32 v192, v64, v65, v66\n\t" \
  "v_mfma_f32_16x16x32_bf16 v[72:75], a[224:227], v[80:83], v[184:187]\n\t" \
  "v_exp_f32 v107, v107\n\t" \
  "v_mfma_f32_16x16x32_bf16 v[72:75], a[228:231], v[84:87], v[72:75]\n\t" \
  "ds_read_b64_tr_b16 a[168:169], v211 offset:512\n\t" \
  "v_cvt_pk_bf16_f32 v157, v106, v107\n\t" \
  "v_mfma_f32_16x16x32_bf16 v[72:75], a[232:235], v[88:91], v[72:75]\n\t" \
  "v_max3_f32 v203, v67, v68, v69\n\t" \
  "v_max3_f32 v192, v192, v70, v71\n\t" \
  "v_mfma_f32_16x16x32_bf16 v[72:75], a[236:239], v[92:95], v[72:75]\n\t" \
  "v_exp_f32 v108, v108\n\t" \
  "v_mfma_f32_16x16x32_bf16 v[76:79], a[252:255], v[92:95], v[184:187]\n\t" \
  "v_exp_f32 v109, v109\n\t" \
  "v_mfma_f32_16x16x32_bf16 v[76:79], a[248:251], v[88:91], v[76:79]\n\t" \
  "ds_read_b64_tr_b16 a[170:171], v211 offset:4608\n\t" \
  "v_cvt_pk_bf16_f32 v158, v108, v109\n\t" \
  "v_mfma_f32_16x16x32_bf16 v[76:79], a[244:247], v[84:87], v[76:79]\n\t" \
  "ds_read_b64_tr_b16 a[172:173], v211 offset:8704\n\t" \
  "v_max3_f32 v203, v203, v72, v73\n\t" \
  "v_mfma_f32_16x16x32_bf16 v[76:79], a[240:243], v[80:83], v[76:79]\n\t" \
  "ds_read_b64_tr_b16 a[174:175], v211 offset:12800\n\t" \
  "v_max3_f32 v192, v192, v74, v75\n\t" \
  "v_mfma_f32_16x16x32_bf16 v[96:99], a[192:195], v[112:115], v[188:191]\n\t" \
  "v_exp_f32 v110, v110\n\t" \
  "v_mfma_f32_16x16x32_bf16 v[96:99], a[196:199], v[116:119], v[96:99]\n\t" \
  "v_exp_f32 v111, v111\n\t" \
  "v_mfma_f32_16x16x32_bf16 v[96:99], a[200:203], v[120:123], v[96:99]\n\t" \
  "ds_read_b64_tr_b16 a[176:177], v210 offset:768\n\t" \
  "v_max3_f32 v203, v203, v76, v77\n\t" \
  "v_mfma_f32_16x16x32_bf16 v[96:99], a[204:207], v[124:127], v[96:99]\n\t" \
  "v_max3_f32 v192, v192, v78, v79\n\t" \
  "v_max_f32 v192, v192, v203\n\t" \
  "v_mfma_f32_16x16x32_bf16 v[100:103], a[220:223], v[124:127], v[188:191]\n\t" \
  "ds_read_b64_tr_b16 a[178:179], v210 offset:4864\n\t" \
  "v_cmp_lt_f32 vcc, 0x42800000, v192\n\t" \
  "v_mfma_f32_16x16x32_bf16 v[100:103], a[216:219], v[120:123], v[100:103]\n\t" \
  "ds_read_b64_tr_b16 a[180:181], v210 offset:8960\n\t" \
  "v_cvt_pk_bf16_f32 v159, v110, v111\n\t" \
  "v_mfma_f32_16x16x32_bf16 v[100:103], a[212:215], v[116:119], v[100:103]\n\t" \
  "s_cbranch_vccnz .Lsf_c22\n\t" \
  ".Lsf_r22:\n\t" \
  "v_mfma_f32_16x16x32_bf16 v[100:103], a[208:211], v[112:115], v[100:103]\n\t" \
  "ds_read_b64_tr_b16 a[182:183], v210 offset:13056\n\t" \
  "v_max3_f32 v192, v96, v97, v98\n\t" \
  "v_mfma_f32_16x16x32_bf16 v[104:107], a[224:227], v[112:115], v[188:191]\n\t" \
  "ds_read_b64_tr_b16 a[184:185], v211 offset:768\n\t" \
  "v_mfma_f32_16x16x32_bf16 v[104:107], a[228:231], v[116:119], v[104:107]\n\t" \
  "ds_read_b64_tr_b16 a[186:187], v211 offset:4864\n\t" \
  "v_mfma_f32_16x16x32_bf16 v[104:107], a[232:235], v[120:123], v[104:107]\n\t" \
  "ds_read_b64_tr_b16 a[188:189], v211 offset:8960\n\t" \
  "v_max3_f32 v203, v99, v100, v101\n\t" \
  "v_mfma_f32_16x16x32_bf16 v[104:107], a[236:239], v[124:127], v[104:107]\n\t" \
  "ds_read_b64_tr_b16 a[190:191], v211 offset:13056\n\t" \
  "v_max3_f32 v192, v192, v102, v103\n\t" \
  "v_mfma_f32_16x16x32_bf16 v[108:111], a[252:255], v[124:127], v[188:191]\n\t" \
  "v_exp_f32 v0, v0\n\t" \
  "v_mfma_f32_16x16x32_bf16 v[108:111], a[248:251], v[120:123], v[108:111]\n\t" \
  "v_exp_f32 v1, v1\n\t" \
  "v_mfma_f32_16x16x32_bf16 v[108:111], a[244:247], v[116:119], v[108:111]\n\t" \
  "s_waitcnt vmcnt(0) lgkmcnt(0)\n\t" \
  "v_max3_f32 v203, v203, v104, v105\n\t" \
  "v_max3_f32 v192, v192, v106, v107\n\t" \
  "v_mfma_f32_16x16x32_bf16 v[108:111], a[240:243], v[112:115], v[108:111]\n\t" \
  "s_barrier\n\t" \
  "v_mfma_f32_16x16x32_bf16 a[0:3], a[128:131], v[128:131], a[0:3]\n\t" \
  "v_exp_f32 v2, v2\n\t" \
  "v_mfma_f32_16x16x32_bf16 a[0:3], a[132:135], v[132:135], a[0:3]\n\t" \
  "v_exp_f32 v3, v3\n\t" \
  "v_mfma_f32_16x16x32_bf16 a[4:7], a[136:139], v[128:131], a[4:7]\n\t" \
  "v_exp_f32 v4, v4\n\t" \
  "v_mfma_f32_16x16x32_bf16 a[4:7], a[140:143], v[132:135], a[4:7]\n\t" \
  "v_max3_f32 v203, v203, v108, v109\n\t" \
  "v_max3_f32 v192, v192, v110, v111\n\t" \
  "v_mfma_f32_16x16x32_bf16 a[8:11], a[144:147], v[128:131], a[8:11]\n\t" \
  "v_max_f32 v192, v192, v203\n\t" \
  "v_cmp_lt_f32 vcc, 0x42800000, v192\n\t" \
  "v_mfma_f32_16x16x32_bf16 a[8:11], a[148:151], v[132:135], a[8:11]\n\t" \
  "v_exp_f32 v5, v5\n\t" \
  "v_mfma_f32_16x16x32_bf16 a[12:15], a[152:155], v[128:131], a[12:15]\n\t" \
  "s_cbranch_vccnz .Lsf_c23\n\t" \
  ".Lsf_r23:\n\t" \
  "v_mfma_f32_16x16x32_bf16 a[12:15], a[156:159], v[132:135], a[12:15]\n\t" \
  "s_add_u32 m0, %[m], 49152\n\t" \
  "v_exp_f32 v6, v6\n\t" \
  "v_mfma_f32_16x16x32_bf16 a[16:19], a[160:163], v[128:131], a[16:19]\n\t" \
  "s_add_u32 s96, %[m], 65536\n\t" \
  "v_exp_f32 v7, v7\n\t" \
  "v_mfma_f32_16x16x32_bf16 a[16:19], a[164:167], v[132:135], a[16:19]\n\t" \
  "v_exp_f32 v8, v8\n\t" \
  "v_mfma_f32_16x16x32_bf16 a[20:23], a[168:171], v[128:131], a[20:23]\n\t" \
  "buffer_load_dwordx4 v224, s[60:63], s96 offen offset:0 lds\n\t" \
  "v_mfma_f32_16x16x32_bf16 a[20:23], a[172:175], v[132:135], a[20:23]\n\t" \
  "v_exp_f32 v9, v9\n\t" \
  "v_mfma_f32_16x16x32_bf16 a[24:27], a[176:179], v[128:131], a[24:27]\n\t" \
  "v_exp_f32 v10, v10\n\t" \
  "v_mfma_f32_16x16x32_bf16 a[24:27], a[180:183], v[132:135], a[24:27]\n\t" \
  "v_exp_f32 v11, v11\n\t" \
  "v_mfma_f32_16x16x32_bf16 a[28:31], a[184:187], v[128:131], a[28:31]\n\t" \
  "v_exp_f32 v12, v12\n\t" \
  "v_mfma_f32_16x16x32_bf16 a[28:31], a[188:191], v[132:135], a[28:31]\n\t" \
  "v_exp_f32 v13, v13\n\t" \
  "v_mfma_f32_16x16x32_bf16 v[160:163], v[216:219], v[128:131], v[160:163]\n\t" \
  "v_exp_f32 v14, v14\n\t" \
  "v_mfma_f32_16x16x32_bf16 v[160:163], v[216:219], v[132:135], v[160:163]\n\t" \
  "buffer_load_dwordx4 v225, s[60:63], s96 offen offset:1024 lds\n\t" \
  "v_mfma_f32_16x16x32_bf16 a[32:35], a[128:131], v[136:139], a[32:35]\n\t" \
  "ds_read_b128 a[192:195], v220 offset:16384\n\t" \
  "v_cvt_pk_bf16_f32 v128, v0, v1\n\t" \
  "v_mfma_f32_16x16x32_bf16 a[32:35], a[132:135], v[140:143], a[32:35]\n\t" \
  "ds_read_b128 a[196:199], v221 offset:16384\n\t" \
  "v_cvt_pk_bf16_f32 v129, v2, v3\n\t" \
  "v_mfma_f32_16x16x32_bf16 a[36:39], a[136:139], v[136:139], a[36:39]\n\t" \
  "v_exp_f32 v15, v15\n\t" \
  "v_mfma_f32_16x16x32_bf16 a[36:39], a[140:143], v[140:143], a[36:39]\n\t" \
  "v_exp_f32 v32, v32\n\t" \
  "v_mfma_f32_16x16x32_bf16 a[40:43], a[144:147], v[136:139], a[40:43]\n\t" \
  "ds_read_b128 a[200:203], v222 offset:16384\n\t" \
  "v_cvt_pk_bf16_f32 v130, v4, v5\n\t" \
  "v_mfma_f32_16x16x32_bf16 a[40:43], a[148:151], v[140:143], a[40:43]\n\t" \
  "buffer_load_dwordx4 v226, s[60:63], s96 offen offset:2048 lds\n\t" \
  "v_mfma_f32_16x16x32_bf16 a[44:47], a[152:155], v[136:139], a[44:47]\n\t" \
  "ds_read_b128 a[204:207], v223 offset:16384\n\t" \
  "v_cvt_pk_bf16_f32 v131, v6, v7\n\t" \
  "v_mfma_f32_16x16x32_bf16 a[44:47], a[156:159], v[140:143], a[44:47]\n\t" \
  "v_exp_f32 v33, v33\n\t" \
  "v_mfma_f32_16x16x32_bf16 a[48:51], a[160:163], v[136:139], a[48:51]\n\t" \
  "v_exp_f32 v34, v34\n\t" \
  "v_mfma_f32_16x16x32_bf16 a[48:51], a[164:167], v[140:143], a[48:51]\n\t" \
  "ds_read_b128 a[208:211], v220 offset:20480\n\t" \
  "v_cvt_pk_bf16_f32 v132, v8, v9\n\t" \
  "v_mfma_f32_16x16x32_bf16 a[52:55], a[168:171], v[136:139], a[52:55]\n\t" \
  "v_exp_f32 v35, v35\n\t" \
  "v_mfma_f32_16x16x32_bf16 a[52:55], a[172:175], v[140:143], a[52:55]\n\t" \
  "buffer_load_dwordx4 v227, s[60:63], s96 offen offset:3072 lds\n\t" \
  "v_mfma_f32_16x16x32_bf16 a[56:59], a[176:179], v[136:139], a[56:59]\n\t" \
  "ds_read_b128 a[212:215], v221 offset:20480\n\t" \
  "v_cvt_pk_bf16_f32 v133, v10, v11\n\t" \
  "v_mfma_f32_16x16x32_bf16 a[56:59], a[180:183], v[140:143], a[56:59]\n\t" \
  "s_add_u32 m0, %[m], 147456\n\t" \
  "v_exp_f32 v36, v36\n\t" \
  "v_mfma_f32_16x16x32_bf16 a[60:63], a[184:187], v[136:139], a[60:63]\n\t" \
  "s_add_u32 s96, %[m], 49152\n\t" \
  "v_exp_f32 v37, v37\n\t" \
  "v_mfma_f32_16x16x32_bf16 a[60:63], a[188:191], v[140:143], a[60:63]\n\t" \
  "ds_read_b128 a[216:219], v222 offset:20480\n\t" \
  "v_cvt_pk_bf16_f32 v134, v12, v13\n\t" \
  "v_mfma_f32_16x16x32_bf16 v[164:167], v[216:219], v[136:139], v[164:167]\n\t" \
  "v_exp_f32 v38, v38\n\t" \
  "v_mfma_f32_16x16x32_bf16 v[164:167], v[216:219], v[140:143], v[164:167]\n\t" \
  "buffer_load_dwordx4 v230, s[64:67], s96 offen offset:0 lds\n\t" \
  "v_mfma_f32_16x16x32_bf16 a[64:67], a[128:131], v[144:147], a[64:67]\n\t" \
  "ds_read_b128 a[220:223], v223 offset:20480\n\t" \
  "v_cvt_pk_bf16_f32 v135, v14, v15\n\t" \
  "v_mfma_f32_16x16x32_bf16 a[64:67], a[132:135], v[148:151], a[64:67]\n\t" \
  "v_exp_f32 v39, v39\n\t" \
  "v_mfma_f32_16x16x32_bf16 a[68:71], a[136:139], v[144:147], a[68:71]\n\t" \
  "v_exp_f32 v40, v40\n\t" \
  "v_mfma_f32_16x16x32_bf16 a[68:71], a[140:143], v[148:151], a[68:71]\n\t" \
  "ds_read_b128 a[224:227], v220 offset:24576\n\t" \
  "v_cvt_pk_bf16_f32 v136, v32, v33\n\t" \
  "v_mfma_f32_16x16x32_bf16 a[72:75], a[144:147], v[144:147], a[72:75]\n\t" \
  "v_exp_f32 v41, v41\n\t" \
  "v_mfma_f32_16x16x32_bf16 a[72:75], a[148:151], v[148:151], a[72:75]\n\t" \
  "ds_read_b128 a[228:231], v221 offset:24576\n\t" \
  "v_cvt_pk_bf16_f32 v137, v34, v35\n\t" \
  "v_mfma_f32_16x16x32_bf16 a[76:79], a[152:155], v[144:147], a[76:79]\n\t" \
  "buffer_load_dwordx4 v231, s[64:67], s96 offen offset:1024 lds\n\t" \
  "v_mfma_f32_16x16x32_bf16 a[76:79], a[156:159], v[148:151], a[76:79]\n\t" \
  "v_exp_f32 v42, v42\n\t" \
  "v_mfma_f32_16x16x32_bf16 a[80:83], a[160:163], v[144:147], a[80:83]\n\t" \
  "ds_read_b128 a[232:235], v222 offset:24576\n\t" \
  "v_cvt_pk_bf16_f32 v138, v36, v37\n\t" \
  "v_mfma_f32_16x16x32_bf16 a[80:83], a[164:167], v[148:151], a[80:83]\n\t" \
  "v_exp_f32 v43, v43\n\t" \
  "v_mfma_f32_16x16x32_bf16 a[84:87], a[168:171], v[144:147], a[84:87]\n\t" \
  "v_exp_f32 v44, v44\n\t" \
  "v_mfma_f32_16x16x32_bf16 a[84:87], a[172:175], v[148:151], a[84:87]\n\t" \
  "ds_read_b128 a[236:239], v223 offset:24576\n\t" \
  "v_cvt_pk_bf16_f32 v139, v38, v39\n\t" \
  "v_mfma_f32_16x16x32_bf16 a[88:91], a[176:179], v[144:147], a[88:91]\n\t" \
  "buffer_load_dwordx4 v230, s[64:67], s96 offen offset:2048 lds\n\t" \
  "v_mfma_f32_16x16x32_bf16 a[88:91], a[180:183], v[148:151], a[88:91]\n\t" \
  "v_exp_f32 v45, v45\n\t" \
  "v_mfma_f32_16x16x32_bf16 a[92:95], a[184:187], v[144:147], a[92:95]\n\t" \
  "ds_read_b128 a[240:243], v220 offset:28672\n\t" \
  "v_cvt_pk_bf16_f32 v140, v40, v41\n\t" \
  "v_mfma_f32_16x16x32_bf16 a[92:95], a[188:191], v[148:151], a[92:95]\n\t" \
  "v_exp_f32 v46, v46\n\t" \
  "v_mfma_f32_16x16x32_bf16 v[168:171], v[216:219], v[144:147], v[168:171]\n\t" \
  "v_exp_f32 v47, v47\n\t" \
  "v_mfma_f32_16x16x32_bf16 v[168:171], v[216:219], v[148:151], v[168:171]\n\t" \
  "ds_read_b128 a[244:247], v221 offset:28672\n\t" \
  "v_cvt_pk_bf16_f32 v141, v42, v43\n\t" \
  "v_mfma_f32_16x16x32_bf16 a[96:99], a[128:131], v[152:155], a[96:99]\n\t" \
  "buffer_load_dwordx4 v231, s[64:67], s96 offen offset:3072 lds\n\t" \
  "v_mfma_f32_16x16x32_bf16 a[96:99], a[132:135], v[156:159], a[96:99]\n\t" \
  "v_exp_f32 v64, v64\n\t" \
  "v_mfma_f32_16x16x32_bf16 a[100:103], a[136:139], v[152:155], a[100:103]\n\t" \
  "ds_read_b128 a[248:251], v222 offset:28672\n\t" \
  "v_cvt_pk_bf16_f32 v142, v44, v45\n\t" \
  "v_mfma_f32_16x16x32_bf16 a[100:103], a[140:143], v[156:159], a[100:103]\n\t" \
  "v_exp_f32 v65, v65\n\t" \
  "v_mfma_f32_16x16x32_bf16 a[104:107], a[144:147], v[152:155], a[104:107]\n\t" \
  "v_exp_f32 v66, v66\n\t" \
  "v_mfma_f32_16x16x32_bf16 a[104:107], a[148:151], v[156:159], a[104:107]\n\t" \
  "ds_read_b128 a[252:255], v223 offset:28672\n\t" \
  "v_cvt_pk_bf16_f32 v143, v46, v47\n\t" \
  "v_mfma_f32_16x16x32_bf16 a[108:111], a[152:155], v[152:155], a[108:111]\n\t" \
  "v_exp_f32 v67, v67\n\t" \
  "v_mfma_f32_16x16x32_bf16 a[108:111], a[156:159], v[156:159], a[108:111]\n\t" \
  "v_exp_f32 v68, v68\n\t" \
  "v_mfma_f32_16x16x32_bf16 a[112:115], a[160:163], v[152:155], a[112:115]\n\t" \
  "v_exp_f32 v69, v69\n\t" \
  "v_mfma_f32_16x16x32_bf16 a[112:115], a[164:167], v[156:159], a[112:115]\n\t" \
  "v_exp_f32 v70, v70\n\t" \
  "v_mfma_f32_16x16x32_bf16 a[116:119], a[168:171], v[152:155], a[116:119]\n\t" \
  "v_exp_f32 v71, v71\n\t" \
  "v_mfma_f32_16x16x32_bf16 a[116:119], a[172:175], v[156:159], a[116:119]\n\t" \
  "v_exp_f32 v72, v72\n\t" \
  "v_mfma_f32_16x16x32_bf16 a[120:123], a[176:179], v[152:155], a[120:123]\n\t" \
  "v_exp_f32 v73, v73\n\t" \
  "v_mfma_f32_16x16x32_bf16 a[120:123], a[180:183], v[156:159], a[120:123]\n\t" \
  "v_exp_f32 v74, v74\n\t" \
  "v_mfma_f32_16x16x32_bf16 a[124:127], a[184:187], v[152:155], a[124:127]\n\t" \
  "v_exp_f32 v75, v75\n\t" \
  "v_mfma_f32_16x16x32_bf16 a[124:127], a[188:191], v[156:159], a[124:127]\n\t" \
  "v_exp_f32 v76, v76\n\t" \
  "v_mfma_f32_16x16x32_bf16 v[172:175], v[216:219], v[152:155], v[172:175]\n\t" \
  "v_exp_f32 v77, v77\n\t" \
  "v_mfma_f32_16x16x32_bf16 v[172:175], v[216:219], v[156:159], v[172:175]\n\t" \
  "s_waitcnt lgkmcnt(12)\n\t" \
  "s_cmp_lg_u32 s99, 0\n\t" \
  "s_cbranch_scc1 .Lsf_p2\n\t" \
  ".Lsf_q2:\n\t" \
  "v_mfma_f32_16x16x32_bf16 v[0:3], a[192:195], v[16:19], v[176:179]\n\t" \
  "ds_read_b64_tr_b16 a[128:129], v228 offset:0\n\t" \
  "v_cvt_pk_bf16_f32 v144, v64, v65\n\t" \
  "v_mfma_f32_16x16x32_bf16 v[0:3], a[196:199], v[20:23], v[0:3]\n\t" \
  "v_exp_f32 v78, v78\n\t" \
  "v_mfma_f32_16x16x32_bf16 v[0:3], a[200:203], v[24:27], v[0:3]\n\t" \
  "ds_read_b64_tr_b16 a[130:131], v228 offset:4096\n\t" \
  "v_cvt_pk_bf16_f32 v145, v66, v67\n\t" \
  "v_mfma_f32_16x16x32_bf16 v[0:3], a[204:207], v[28:31], v[0:3]\n\t" \
  "s_waitcnt lgkmcnt(10)\n\t" \
  "v_exp_f32 v79, v79\n\t" \
  "v_mfma_f32_16x16x32_bf16 v[4:7], a[220:223], v[28:31], v[176:179]\n\t" \
  "ds_read_b64_tr_b16 a[132:133], v228 offset:8192\n\t" \
  "v_cvt_pk_bf16_f32 v146, v68, v69\n\t" \
  "v_mfma_f32_16x16x32_bf16 v[4:7], a[216:219], v[24:27], v[4:7]\n\t" \
  "v_exp_f32 v96, v96\n\t" \
  "v_mfma_f32_16x16x32_bf16 v[4:7], a[212:215], v[20:23], v[4:7]\n\t" \
  "ds_read_b64_tr_b16 a[134:135], v228 offset:12288\n\t" \
  "v_max3_f32 v192, v0, v1, v2\n\t" \
  "v_mfma_f32_16x16x32_bf16 v[4:7], a[208:211], v[16:19], v[4:7]\n\t" \
  "s_waitcnt lgkmcnt(8)\n\t" \
  "ds_read_b64_tr_b16 a[136:137], v229 offset:0\n\t" \
  "v_cvt_pk_bf16_f32 v147, v70, v71\n\t" \
  "v_mfma_f32_16x16x32_bf16 v[8:11], a[224:227], v[16:19], v[176:179]\n\t" \
  "v_exp_f32 v97, v97\n\t" \
  "v_mfma_f32_16x16x32_bf16 v[8:11], a[228:231], v[20:23], v[8:11]\n\t" \
  "ds_read_b64_tr_b16 a[138:139], v229 offset:4096\n\t" \
  "v_cvt_pk_bf16_f32 v148, v72, v73\n\t" \
  "v_mfma_f32_16x16x32_bf16 v[8:11], a[232:235], v[24:27], v[8:11]\n\t" \
  "v_max3_f32 v203, v3, v4, v5\n\t" \
  "v_max3_f32 v192, v192, v6, v7\n\t" \
  "v_mfma_f32_16x16x32_bf16 v[8:11], a[236:239], v[28:31], v[8:11]\n\t" \
  "s_waitcnt lgkmcnt(6)\n\t" \
  "ds_read_b64_tr_b16 a[140:141], v229 offset:8192\n\t" \
  "v_cvt_pk_bf16_f32 v149, v74, v75\n\t" \
  "v_mfma_f32_16x16x32_bf16 v[12:15], a[252:255], v[28:31], v[176:179]\n\t" \
  "v_exp_f32 v98, v98\n\t" \
  "v_mfma_f32_16x16x32_bf16 v[12:15], a[248:251], v[24:27], v[12:15]\n\t" \
  "ds_read_b64_tr_b16 a[142:143], v229 offset:12288\n\t" \
  "v_cvt_pk_bf16_f32 v150, v76, v77\n\t" \
  "v_mfma_f32_16x16x32_bf16 v[12:15], a[244:247], v[20:23], v[12:15]\n\t" \
  "v_max3_f32 v203, v203, v8, v9\n\t" \
  "v_max3_f32 v192, v192, v10, v11\n\t" \
  "v_mfma_f32_16x16x32_bf16 v[12:15], a[240:243], v[16:19], v[12:15]\n\t" \
  "ds_read_b64_tr_b16 a[144:145], v228 offset:256\n\t" \
  "v_cvt_pk_bf16_f32 v151, v78, v79\n\t" \
  "v_mfma_f32_16x16x32_bf16 v[32:35], a[192:195], v[48:51], v[180:183]\n\t" \
  "ds_read_b64_tr_b16 a[146:147], v228 offset:4352\n\t" \
  "v_cvt_pk_bf16_f32 v152, v96, v97\n\t" \
  "v_mfma_f32_16x16x32_bf16 v[32:35], a[196:199], v[52:55], v[32:35]\n\t" \
  "v_exp_f32 v99, v99\n\t" \
  "v_mfma_f32_16x16x32_bf16 v[32:35], a[200:203], v[56:59], v[32:35]\n\t" \
  "v_max3_f32 v203, v203, v12, v13\n\t" \
  "v_max3_f32 v192, v192, v14, v15\n\t" \
  "v_mfma_f32_16x16x32_bf16 v[32:35], a[204:207], v[60:63], v[32:35]\n\t" \
  "v_max_f32 v192, v192, v203\n\t" \
  "v_cmp_lt_f32 vcc, 0x42800000, v192\n\t" \
  "v_mfma_f32_16x16x32_bf16 v[36:39], a[220:223], v[60:63], v[180:183]\n\t" \
  "ds_read_b64_tr_b16 a[148:149], v228 offset:8448\n\t" \
  "v_cvt_pk_bf16_f32 v153, v98, v99\n\t" \
  "v_mfma_f32_16x16x32_bf16 v[36:39], a[216:219], v[56:59], v[36:39]\n\t" \
  "s_cbranch_vccnz .Lsf_c30\n\t" \
  ".Lsf_r30:\n\t" \
  "v_mfma_f32_16x16x32_bf16 v[36:39], a[212:215], v[52:55], v[36:39]\n\t" \
  "ds_read_b64_tr_b16 a[150:151], v228 offset:12544\n\t" \
  "v_max3_f32 v192, v32, v33, v34\n\t" \
  "v_mfma_f32_16x16x32_bf16 v[36:39], a[208:211], v[48:51], v[36:39]\n\t" \
  "v_exp_f32 v100, v100\n\t" \
  "v_mfma_f32_16x16x32_bf16 v[40:43], a[224:227], v[48:51], v[180:183]\n\t" \
  "v_exp_f32 v101, v101\n\t" \
  "v_mfma_f32_16x16x32_bf16 v[40:43], a[228:231], v[52:55], v[40:43]\n\t" \
  "ds_read_b64_tr_b16 a[152:153], v229 offset:256\n\t" \
  "v_cvt_pk_bf16_f32 v154, v100, v101\n\t" \
  "v_mfma_f32_16x16x32_bf16 v[40:43], a[232:235], v[56:59], v[40:43]\n\t" \
  "ds_read_b64_tr_b16 a[154:155], v229 offset:4352\n\t" \
  "v_max3_f32 v203, v35, v36, v37\n\t" \
  "v_mfma_f32_16x16x32_bf16 v[40:43], a[236:239], v[60:63], v[40:43]\n\t" \
  "ds_read_b64_tr_b16 a[156:157], v229 offset:8448\n\t" \
  "v_max3_f32 v192, v192, v38, v39\n\t" \
  "v_mfma_f32_16x16x32_bf16 v[44:47], a[252:255], v[60:63], v[180:183]\n\t" \
  "v_exp_f32 v102, v102\n\t" \
  "v_mfma_f32_16x16x32_bf16 v[44:47], a[248:251], v[56:59], v[44:47]\n\t" \
  "v_exp_f32 v103, v103\n\t" \
  "v_mfma_f32_16x16x32_bf16 v[44:47], a[244:247], v[52:55], v[44:47]\n\t" \
  "ds_read_b64_tr_b16 a[158:159], v229 offset:12544\n\t" \
  "v_max3_f32 v203, v203, v40, v41\n\t" \
  "v_mfma_f32_16x16x32_bf16 v[44:47], a[240:243], v[48:51], v[44:47]\n\t" \
  "ds_read_b64_tr_b16 a[160:161], v228 offset:512\n\t" \
  "v_max3_f32 v192, v192, v42, v43\n\t" \
  "v_mfma_f32_16x16x32_bf16 v[64:67], a[192:195], v[80:83], v[184:187]\n\t" \
  "ds_read_b64_tr_b16 a[162:163], v228 offset:4608\n\t" \
  "v_cvt_pk_bf16_f32 v155, v102, v103\n\t" \
  "v_mfma_f32_16x16x32_bf16 v[64:67], a[196:199], v[84:87], v[64:67]\n\t" \
  "v_exp_f32 v104, v104\n\t" \
  "v_mfma_f32_16x16x32_bf16 v[64:67], a[200:203], v[88:91], v[64:67]\n\t" \
  "v_max3_f32 v203, v203, v44, v45\n\t" \
  "v_max3_f32 v192, v192, v46, v47\n\t" \
  "v_mfma_f32_16x16x32_bf16 v[64:67], a[204:207], v[92:95], v[64:67]\n\t" \
  "v_max_f32 v192, v192, v203\n\t" \
  "v_cmp_lt_f32 vcc, 0x42800000, v192\n\t" \
  "v_mfma_f32_16x16x32_bf16 v[68:71], a[220:223], v[92:95], v[184:187]\n\t" \
  "v_exp_f32 v105, v105\n\t" \
  "v_mfma_f32_16x16x32_bf16 v[68:71], a[216:219], v[88:91], v[68:71]\n\t" \
  "s_cbranch_vccnz .Lsf_c31\n\t" \
  ".Lsf_r31:\n\t" \
  "v_mfma_f32_16x16x32_bf16 v[68:71], a[212:215], v[84:87], v[68:71]\n\t" \
  "ds_read_b64_tr_b16 a[164:165], v228 offset:8704\n\t" \
  "v_max3_f32 v192, v64, v65, v66\n\t" \
  "v_mfma_f32_16x16x32_bf16 v[68:71], a[208:211], v[80:83], v[68:71]\n\t" \
  "ds_read_b64_tr_b16 a[166:167], v228 offset:12800\n\t" \
  "v_cvt_pk_bf16_f32 v156, v104, v105\n\t" \
  "v_mfma_f32_16x16x32_bf16 v[72:75], a[224:227], v[80:83], v[184:187]\n\t" \
  "v_exp_f32 v106, v106\n\t" \
  "v_mfma_f32_16x16x32_bf16 v[72:75], a[228:231], v[84:87], v[72:75]\n\t" \
  "v_exp_f32 v107, v107\n\t" \
  "v_mfma_f32_16x16x32_bf16 v[72:75], a[232:235], v[88:91], v[72:75]\n\t" \
  "ds_read_b64_tr_b16 a[168:169], v229 offset:512\n\t" \
  "v_max3_f32 v203, v67, v68, v69\n\t" \
  "v_mfma_f32_16x16x32_bf16 v[72:75], a[236:239], v[92:95], v[72:75]\n\t" \
  "ds_read_b64_tr_b16 a[170:171], v229 offset:4608\n\t" \
  "v_max3_f32 v192, v192, v70, v71\n\t" \
  "v_mfma_f32_16x16x32_bf16 v[76:79], a[252:255], v[92:95], v[184:187]\n\t" \
  "ds_read_b64_tr_b16 a[172:173], v229 offset:8704\n\t" \
  "v_cvt_pk_bf16_f32 v157, v106, v107\n\t" \
  "v_mfma_f32_16x16x32_bf16 v[76:79], a[248:251], v[88:91], v[76:79]\n\t" \
  "v_exp_f32 v108, v108\n\t" \
  "v_mfma_f32_16x16x32_bf16 v[76:79], a[244:247], v[84:87], v[76:79]\n\t" \
  "v_max3_f32 v203, v203, v72, v73\n\t" \
  "v_max3_f32 v192, v192, v74, v75\n\t" \
  "v_mfma_f32_16x16x32_bf16 v[76:79], a[240:243], v[80:83], v[76:79]\n\t" \
  "v_exp_f32 v109, v109\n\t" \
  "v_mfma_f32_16x16x32_bf16 v[96:99], a[192:195], v[112:115], v[188:191]\n\t" \
  "ds_read_b64_tr_b16 a[174:175], v229 offset:12800\n\t" \
  "v_cvt_pk_bf16_f32 v158, v108, v109\n\t" \
  "v_mfma_f32_16x16x32_bf16 v[96:99], a[196:199], v[116:119], v[96:99]\n\t" \
  "v_exp_f32 v110, v110\n\t" \
  "v_mfma_f32_16x16x32_bf16 v[96:99], a[200:203], v[120:123], v[96:99]\n\t" \
  "ds_read_b64_tr_b16 a[176:177], v228 offset:768\n\t" \
  "v_max3_f32 v203, v203, v76, v77\n\t" \
  "v_mfma_f32_16x16x32_bf16 v[96:99], a[204:207], v[124:127], v[96:99]\n\t" \
  "v_max3_f32 v192, v192, v78, v79\n\t" \
  "v_max_f32 v192, v192, v203\n\t" \
  "v_mfma_f32_16x16x32_bf16 v[100:103], a[220:223], v[124:127], v[188:191]\n\t" \
  "ds_read_b64_tr_b16 a[178:179], v228 offset:4864\n\t" \
  "v_cmp_lt_f32 vcc, 0x42800000, v192\n\t" \
  "v_mfma_f32_16x16x32_bf16 v[100:103], a[216:219], v[120:123], v[100:103]\n\t" \
  "ds_read_b64_tr_b16 a[180:181], v228 offset:8960\n\t" \
  "v_exp_f32 v111, v111\n\t" \
  "v_mfma_f32_16x16x32_bf16 v[100:103], a[212:215], v[116:119], v[100:103]\n\t" \
  "s_cbranch_vccnz .Lsf_c32\n\t" \
  ".Lsf_r32:\n\t" \
  "v_mfma_f32_16x16x32_bf16 v[100:103], a[208:211], v[112:115], v[100:103]\n\t" \
  "ds_read_b64_tr_b16 a[182:183], v228 offset:13056\n\t" \
  "v_max3_f32 v192, v96, v97, v98\n\t" \
  "v_mfma_f32_16x16x32_bf16 v[104:107], a[224:227], v[112:115], v[188:191]\n\t" \
  "ds_read_b64_tr_b16 a[184:185], v229 offset:768\n\t" \
  "v_cvt_pk_bf16_f32 v159, v110, v111\n\t" \
  "v_mfma_f32_16x16x32_bf16 v[104:107], a[228:231], v[116:119], v[104:107]\n\t" \
  "ds_read_b64_tr_b16 a[186:187], v229 offset:4864\n\t" \
  "v_mfma_f32_16x16x32_bf16 v[104:107], a[232:235], v[120:123], v[104:107]\n\t" \
  "ds_read_b64_tr_b16 a[188:189], v229 offset:8960\n\t" \
  "v_max3_f32 v203, v99, v100, v101\n\t" \
  "v_mfma_f32_16x16x32_bf16 v[104:107], a[236:239], v[124:127], v[104:107]\n\t" \
  "ds_read_b64_tr_b16 a[190:191], v229 offset:13056\n\t" \
  "v_max3_f32 v192, v192, v102, v103\n\t" \
  "v_mfma_f32_16x16x32_bf16 v[108:111], a[252:255], v[124:127], v[188:191]\n\t" \
  "v_exp_f32 v0, v0\n\t" \
  "v_mfma_f32_16x16x32_bf16 v[108:111], a[248:251], v[120:123], v[108:111]\n\t" \
  "v_exp_f32 v1, v1\n\t" \
  "v_mfma_f32_16x16x32_bf16 v[108:111], a[244:247], v[116:119], v[108:111]\n\t" \
  "s_waitcnt vmcnt(0) lgkmcnt(0)\n\t" \
  "v_max3_f32 v203, v203, v104, v105\n\t" \
  "v_max3_f32 v192, v192, v106, v107\n\t" \
  "v_mfma_f32_16x16x32_bf16 v[108:111], a[240:243], v[112:115], v[108:111]\n\t" \
  "s_barrier\n\t" \
  "v_mfma_f32_16x16x32_bf16 a[0:3], a[128:131], v[128:131], a[0:3]\n\t" \
  "v_exp_f32 v2, v2\n\t" \
  "v_mfma_f32_16x16x32_bf16 a[0:3], a[132:135], v[132:135], a[0:3]\n\t" \
  "v_exp_f32 v3, v3\n\t" \
  "v_mfma_f32_16x16x32_bf16 a[4:7], a[136:139], v[128:131], a[4:7]\n\t" \
  "v_exp_f32 v4, v4\n\t" \
  "v_mfma_f32_16x16x32_bf16 a[4:7], a[140:143], v[132:135], a[4:7]\n\t" \
  "v_max3_f32 v203, v203, v108, v109\n\t" \
  "v_max3_f32 v192, v192, v110, v111\n\t" \
  "v_mfma_f32_16x16x32_bf16 a[8:11], a[144:147], v[128:131], a[8:11]\n\t" \
  "v_max_f32 v192, v192, v203\n\t" \
  "v_cmp_lt_f32 vcc, 0x42800000, v192\n\t" \
  "v_mfma_f32_16x16x32_bf16 a[8:11], a[148:151], v[132:135], a[8:11]\n\t" \
  "v_exp_f32 v5, v5\n\t" \
  "v_mfma_f32_16x16x32_bf16 a[12:15], a[152:155], v[128:131], a[12:15]\n\t" \
  "s_cbranch_vccnz .Lsf_c33\n\t" \
  ".Lsf_r33:\n\t" \
  "v_mfma_f32_16x16x32_bf16 a[12:15], a[156:159], v[132:135], a[12:15]\n\t" \
  "s_add_u32 m0, %[m], 65536\n\t" \
  "v_exp_f32 v6, v6\n\t" \
  "v_mfma_f32_16x16x32_bf16 a[16:19], a[160:163], v[128:131], a[16:19]\n\t" \
  "s_add_u32 s96, %[m], 81920\n\t" \
  "v_exp_f32 v7, v7\n\t" \
  "v_mfma_f32_16x16x32_bf16 a[16:19], a[164:167], v[132:135], a[16:19]\n\t" \
  "v_exp_f32 v8, v8\n\t" \
  "v_mfma_f32_16x16x32_bf16 a[20:23], a[168:171], v[128:131], a[20:23]\n\t" \
  "buffer_load_dwordx4 v224, s[60:63], s96 offen offset:0 lds\n\t" \
  "v_mfma_f32_16x16x32_bf16 a[20:23], a[172:175], v[132:135], a[20:23]\n\t" \
  "v_exp_f32 v9, v9\n\t" \
  "v_mfma_f32_16x16x32_bf16 a[24:27], a[176:179], v[128:131], a[24:27]\n\t" \
  "v_exp_f32 v10, v10\n\t" \
  "v_mfma_f32_16x16x32_bf16 a[24:27], a[180:183], v[132:135], a[24:27]\n\t" \
  "v_exp_f32 v11, v11\n\t" \
  "v_mfma_f32_16x16x32_bf16 a[28:31], a[184:187], v[128:131], a[28:31]\n\t" \
  "v_exp_f32 v12, v12\n\t" \
  "v_mfma_f32_16x16x32_bf16 a[28:31], a[188:191], v[132:135], a[28:31]\n\t" \
  "v_exp_f32 v13, v13\n\t" \
  "v_mfma_f32_16x16x32_bf16 v[160:163], v[216:219], v[128:131], v[160:163]\n\t" \
  "v_exp_f32 v14, v14\n\t" \
  "v_mfma_f32_16x16x32_bf16 v[160:163], v[216:219], v[132:135], v[160:163]\n\t" \
  "buffer_load_dwordx4 v225, s[60:63], s96 offen offset:1024 lds\n\t" \
  "v_mfma_f32_16x16x32_bf16 a[32:35], a[128:131], v[136:139], a[32:35]\n\t" \
  "ds_read_b128 a[192:195], v220 offset:0\n\t" \
  "v_cvt_pk_bf16_f32 v128, v0, v1\n\t" \
  "v_mfma_f32_16x16x32_bf16 a[32:35], a[132:135], v[140:143], a[32:35]\n\t" \
  "ds_read_b128 a[196:199], v221 offset:0\n\t" \
  "v_cvt_pk_bf16_f32 v129, v2, v3\n\t" \
  "v_mfma_f32_16x16x32_bf16 a[36:39], a[136:139], v[136:139], a[36:39]\n\t" \
  "v_exp_f32 v15, v15\n\t" \
  "v_mfma_f32_16x16x32_bf16 a[36:39], a[140:143], v[140:143], a[36:39]\n\t" \
  "v_exp_f32 v32, v32\n\t" \
  "v_mfma_f32_16x16x32_bf16 a[40:43], a[144:147], v[136:139], a[40:43]\n\t" \
  "ds_read_b128 a[200:203], v222 offset:0\n\t" \
  "v_cvt_pk_bf16_f32 v130, v4, v5\n\t" \
  "v_mfma_f32_16x16x32_bf16 a[40:43], a[148:151], v[140:143], a[40:43]\n\t" \
  "buffer_load_dwordx4 v226, s[60:63], s96 offen offset:2048 lds\n\t" \
  "v_mfma_f32_16x16x32_bf16 a[44:47], a[152:155], v[136:139], a[44:47]\n\t" \
  "ds_read_b128 a[204:207], v223 offset:0\n\t" \
  "v_cvt_pk_bf16_f32 v131, v6, v7\n\t" \
  "v_mfma_f32_16x16x32_bf16 a[44:47], a[156:159], v[140:143], a[44:47]\n\t" \
  "v_exp_f32 v33, v33\n\t" \
  "v_mfma_f32_16x16x32_bf16 a[48:51], a[160:163], v[136:139], a[48:51]\n\t" \
  "v_exp_f32 v34, v34\n\t" \
  "v_mfma_f32_16x16x32_bf16 a[48:51], a[164:167], v[140:143], a[48:51]\n\t" \
  "ds_read_b128 a[208:211], v220 offset:4096\n\t" \
  "v_cvt_pk_bf16_f32 v132, v8, v9\n\t" \
  "v_mfma_f32_16x16x32_bf16 a[52:55], a[168:171], v[136:139], a[52:55]\n\t" \
  "v_exp_f32 v35, v35\n\t" \
  "v_mfma_f32_16x16x32_bf16 a[52:55], a[172:175], v[140:143], a[52:55]\n\t" \
  "buffer_load_dwordx4 v227, s[60:63], s96 offen offset:3072 lds\n\t" \
  "v_mfma_f32_16x16x32_bf16 a[56:59], a[176:179], v[136:139], a[56:59]\n\t" \
  "ds_read_b128 a[212:215], v221 offset:4096\n\t" \
  "v_cvt_pk_bf16_f32 v133, v10, v11\n\t" \
  "v_mfma_f32_16x16x32_bf16 a[56:59], a[180:183], v[140:143], a[56:59]\n\t" \
  "s_add_u32 m0, %[m], 81920\n\t" \
  "v_exp_f32 v36, v36\n\t" \
  "v_mfma_f32_16x16x32_bf16 a[60:63], a[184:187], v[136:139], a[60:63]\n\t" \
  "s_add_u32 s96, %[m], 65536\n\t" \
  "v_exp_f32 v37, v37\n\t" \
  "v_mfma_f32_16x16x32_bf16 a[60:63], a[188:191], v[140:143], a[60:63]\n\t" \
  "ds_read_b128 a[216:219], v222 offset:4096\n\t" \
  "v_cvt_pk_bf16_f32 v134, v12, v13\n\t" \
  "v_mfma_f32_16x16x32_bf16 v[164:167], v[216:219], v[136:139], v[164:167]\n\t" \
  "v_exp_f32 v38, v38\n\t" \
  "v_mfma_f32_16x16x32_bf16 v[164:167], v[216:219], v[140:143], v[164:167]\n\t" \
  "buffer_load_dwordx4 v230, s[64:67], s96 offen offset:0 lds\n\t" \
  "v_mfma_f32_16x16x32_bf16 a[64:67], a[128:131], v[144:147], a[64:67]\n\t" \
  "ds_read_b128 a[220:223], v223 offset:4096\n\t" \
  "v_cvt_pk_bf16_f32 v135, v14, v15\n\t" \
  "v_mfma_f32_16x16x32_bf16 a[64:67], a[132:135], v[148:151], a[64:67]\n\t" \
  "v_exp_f32 v39, v39\n\t" \
  "v_mfma_f32_16x16x32_bf16 a[68:71], a[136:139], v[144:147], a[68:71]\n\t" \
  "v_exp_f32 v40, v40\n\t" \
  "v_mfma_f32_16x16x32_bf16 a[68:71], a[140:143], v[148:151], a[68:71]\n\t" \
  "ds_read_b128 a[224:227], v220 offset:8192\n\t" \
  "v_cvt_pk_bf16_f32 v136, v32, v33\n\t" \
  "v_mfma_f32_16x16x32_bf16 a[72:75], a[144:147], v[144:147], a[72:75]\n\t" \
  "v_exp_f32 v41, v41\n\t" \
  "v_mfma_f32_16x16x32_bf16 a[72:75], a[148:151], v[148:151], a[72:75]\n\t" \
  "ds_read_b128 a[228:231], v221 offset:8192\n\t" \
  "v_cvt_pk_bf16_f32 v137, v34, v35\n\t" \
  "v_mfma_f32_16x16x32_bf16 a[76:79], a[152:155], v[144:147], a[76:79]\n\t" \
  "buffer_load_dwordx4 v231, s[64:67], s96 offen offset:1024 lds\n\t" \
  "v_mfma_f32_16x16x32_bf16 a[76:79], a[156:159], v[148:151], a[76:79]\n\t" \
  "v_exp_f32 v42, v42\n\t" \
  "v_mfma_f32_16x16x32_bf16 a[80:83], a[160:163], v[144:147], a[80:83]\n\t" \
  "ds_read_b128 a[232:235], v222 offset:8192\n\t" \
  "v_cvt_pk_bf16_f32 v138, v36, v37\n\t" \
  "v_mfma_f32_16x16x32_bf16 a[80:83], a[164:167], v[148:151], a[80:83]\n\t" \
  "v_exp_f32 v43, v43\n\t" \
  "v_mfma_f32_16x16x32_bf16 a[84:87], a[168:171], v[144:147], a[84:87]\n\t" \
  "v_exp_f32 v44, v44\n\t" \
  "v_mfma_f32_16x16x32_bf16 a[84:87], a[172:175], v[148:151], a[84:87]\n\t" \
  "ds_read_b128 a[236:239], v223 offset:8192\n\t" \
  "v_cvt_pk_bf16_f32 v139, v38, v39\n\t" \
  "v_mfma_f32_16x16x32_bf16 a[88:91], a[176:179], v[144:147], a[88:91]\n\t" \
  "buffer_load_dwordx4 v230, s[64:67], s96 offen offset:2048 lds\n\t" \
  "v_mfma_f32_16x16x32_bf16 a[88:91], a[180:183], v[148:151], a[88:91]\n\t" \
  "v_exp_f32 v45, v45\n\t" \
  "v_mfma_f32_16x16x32_bf16 a[92:95], a[184:187], v[144:147], a[92:95]\n\t" \
  "ds_read_b128 a[240:243], v220 offset:12288\n\t" \
  "v_cvt_pk_bf16_f32 v140, v40, v41\n\t" \
  "v_mfma_f32_16x16x32_bf16 a[92:95], a[188:191], v[148:151], a[92:95]\n\t" \
  "v_exp_f32 v46, v46\n\t" \
  "v_mfma_f32_16x16x32_bf16 v[168:171], v[216:219], v[144:147], v[168:171]\n\t" \
  "v_exp_f32 v47, v47\n\t" \
  "v_mfma_f32_16x16x32_bf16 v[168:171], v[216:219], v[148:151], v[168:171]\n\t" \
  "ds_read_b128 a[244:247], v221 offset:12288\n\t" \
  "v_cvt_pk_bf16_f32 v141, v42, v43\n\t" \
  "v_mfma_f32_16x16x32_bf16 a[96:99], a[128:131], v[152:155], a[96:99]\n\t" \
  "buffer_load_dwordx4 v231, s[64:67], s96 offen offset:3072 lds\n\t" \
  "v_mfma_f32_16x16x32_bf16 a[96:99], a[132:135], v[156:159], a[96:99]\n\t" \
  "v_exp_f32 v64, v64\n\t" \
  "v_mfma_f32_16x16x32_bf16 a[100:103], a[136:139], v[152:155], a[100:103]\n\t" \
  "ds_read_b128 a[248:251], v222 offset:12288\n\t" \
  "v_cvt_pk_bf16_f32 v142, v44, v45\n\t" \
  "v_mfma_f32_16x16x32_bf16 a[100:103], a[140:143], v[156:159], a[100:103]\n\t" \
  "v_exp_f32 v65, v65\n\t" \
  "v_mfma_f32_16x16x32_bf16 a[104:107], a[144:147], v[152:155], a[104:107]\n\t" \
  "v_exp_f32 v66, v66\n\t" \
  "v_mfma_f32_16x16x32_bf16 a[104:107], a[148:151], v[156:159], a[104:107]\n\t" \
  "ds_read_b128 a[252:255], v223 offset:12288\n\t" \
  "v_cvt_pk_bf16_f32 v143, v46, v47\n\t" \
  "v_mfma_f32_16x16x32_bf16 a[108:111], a[152:155], v[152:155], a[108:111]\n\t" \
  "v_exp_f32 v67, v67\n\t" \
  "v_mfma_f32_16x16x32_bf16 a[108:111], a[156:159], v[156:159], a[108:111]\n\t" \
  "v_exp_f32 v68, v68\n\t" \
  "v_mfma_f32_16x16x32_bf16 a[112:115], a[160:163], v[152:155], a[112:115]\n\t" \
  "v_exp_f32 v69, v69\n\t" \
  "v_mfma_f32_16x16x32_bf16 a[112:115], a[164:167], v[156:159], a[112:115]\n\t" \
  "v_exp_f32 v70, v70\n\t" \
  "v_mfma_f32_16x16x32_bf16 a[116:119], a[168:171], v[152:155], a[116:119]\n\t" \
  "v_exp_f32 v71, v71\n\t" \
  "v_mfma_f32_16x16x32_bf16 a[116:119], a[172:175], v[156:159], a[116:119]\n\t" \
  "v_exp_f32 v72, v72\n\t" \
  "v_mfma_f32_16x16x32_bf16 a[120:123], a[176:179], v[152:155], a[120:123]\n\t" \
  "v_exp_f32 v73, v73\n\t" \
  "v_mfma_f32_16x16x32_bf16 a[120:123], a[180:183], v[156:159], a[120:123]\n\t" \
  "v_exp_f32 v74, v74\n\t" \
  "v_mfma_f32_16x16x32_bf16 a[124:127], a[184:187], v[152:155], a[124:127]\n\t" \
  "v_exp_f32 v75, v75\n\t" \
  "v_mfma_f32_16x16x32_bf16 a[124:127], a[188:191], v[156:159], a[124:127]\n\t" \
  "v_exp_f32 v76, v76\n\t" \
  "v_mfma_f32_16x16x32_bf16 v[172:175], v[216:219], v[152:155], v[172:175]\n\t" \
  "v_exp_f32 v77, v77\n\t" \
  "v_mfma_f32_16x16x32_bf16 v[172:175], v[216:219], v[156:159], v[172:175]\n\t" \
  "s_waitcnt lgkmcnt(12)\n\t" \
  "s_cmp_lg_u32 s99, 0\n\t" \
  "s_cbranch_scc1 .Lsf_p3\n\t" \
  ".Lsf_q3:\n\t" \
  "v_mfma_f32_16x16x32_bf16 v[0:3], a[192:195], v[16:19], v[176:179]\n\t" \
  "ds_read_b64_tr_b16 a[128:129], v210 offset:0\n\t" \
  "v_cvt_pk_bf16_f32 v144, v64, v65\n\t" \
  "v_mfma_f32_16x16x32_bf16 v[0:3], a[196:199], v[20:23], v[0:3]\n\t" \
  "v_exp_f32 v78, v78\n\t" \
  "v_mfma_f32_16x16x32_bf16 v[0:3], a[200:203], v[24:27], v[0:3]\n\t" \
  "ds_read_b64_tr_b16 a[130:131], v210 offset:4096\n\t" \
  "v_cvt_pk_bf16_f32 v145, v66, v67\n\t" \
  "v_mfma_f32_16x16x32_bf16 v[0:3], a[204:207], v[28:31], v[0:3]\n\t" \
  "s_waitcnt lgkmcnt(10)\n\t" \
  "v_exp_f32 v79, v79\n\t" \
  "v_mfma_f32_16x16x32_bf16 v[4:7], a[220:223], v[28:31], v[176:179]\n\t" \
  "ds_read_b64_tr_b16 a[132:133], v210 offset:8192\n\t" \
  "v_cvt_pk_bf16_f32 v146, v68, v69\n\t" \
  "v_mfma_f32_16x16x32_bf16 v[4:7], a[216:219], v[24:27], v[4:7]\n\t" \
  "v_exp_f32 v96, v96\n\t" \
  "v_mfma_f32_16x16x32_bf16 v[4:7], a[212:215], v[20:23], v[4:7]\n\t" \
  "ds_read_b64_tr_b16 a[134:135], v210 offset:12288\n\t" \
  "v_max3_f32 v192, v0, v1, v2\n\t" \
  "v_mfma_f32_16x16x32_bf16 v[4:7], a[208:211], v[16:19], v[4:7]\n\t" \
  "s_waitcnt lgkmcnt(8)\n\t" \
  "ds_read_b64_tr_b16 a[136:137], v211 offset:0\n\t" \
  "v_cvt_pk_bf16_f32 v147, v70, v71\n\t" \
  "v_mfma_f32_16x16x32_bf16 v[8:11], a[224:227], v[16:19], v[176:179]\n\t" \
  "v_exp_f32 v97, v97\n\t" \
  "v_mfma_f32_16x16x32_bf16 v[8:11], a[228:231], v[20:23], v[8:11]\n\t" \
  "ds_read_b64_tr_b16 a[138:139], v211 offset:4096\n\t" \
  "v_cvt_pk_bf16_f32 v148, v72, v73\n\t" \
  "v_mfma_f32_16x16x32_bf16 v[8:11], a[232:235], v[24:27], v[8:11]\n\t" \
  "v_max3_f32 v203, v3, v4, v5\n\t" \
  "v_max3_f32 v192, v192, v6, v7\n\t" \
  "v_mfma_f32_16x16x32_bf16 v[8:11], a[236:239], v[28:31], v[8:11]\n\t" \
  "s_waitcnt lgkmcnt(6)\n\t" \
  "ds_read_b64_tr_b16 a[140:141], v211 offset:8192\n\t" \
  "v_cvt_pk_bf16_f32 v149, v74, v75\n\t" \
  "v_mfma_f32_16x16x32_bf16 v[12:15], a[252:255], v[28:31], v[176:179]\n\t" \
  "v_exp_f32 v98, v98\n\t" \
  "v_mfma_f32_16x16x32_bf16 v[12:15], a[248:251], v[24:27], v[12:15]\n\t" \
  "ds_read_b64_tr_b16 a[142:143], v211 offset:12288\n\t" \
  "v_cvt_pk_bf16_f32 v150, v76, v77\n\t" \
  "v_mfma_f32_16x16x32_bf16 v[12:15], a[244:247], v[20:23], v[12:15]\n\t" \
  "v_max3_f32 v203, v203, v8, v9\n\t" \
  "v_max3_f32 v192, v192, v10, v11\n\t" \
  "v_mfma_f32_16x16x32_bf16 v[12:15], a[240:243], v[16:19], v[12:15]\n\t" \
  "ds_read_b64_tr_b16 a[144:145], v210 offset:256\n\t" \
  "v_cvt_pk_bf16_f32 v151, v78, v79\n\t" \
  "v_mfma_f32_16x16x32_bf16 v[32:35], a[192:195], v[48:51], v[180:183]\n\t" \
  "ds_read_b64_tr_b16 a[146:147], v210 offset:4352\n\t" \
  "v_cvt_pk_bf16_f32 v152, v96, v97\n\t" \
  "v_mfma_f32_16x16x32_bf16 v[32:35], a[196:199], v[52:55], v[32:35]\n\t" \
  "v_exp_f32 v99, v99\n\t" \
  "v_mfma_f32_16x16x32_bf16 v[32:35], a[200:203], v[56:59], v[32:35]\n\t" \
  "v_max3_f32 v203, v203, v12, v13\n\t" \
  "v_max3_f32 v192, v192, v14, v15\n\t" \
  "v_mfma_f32_16x16x32_bf16 v[32:35], a[204:207], v[60:63], v[32:35]\n\t" \
  "v_max_f32 v192, v192, v203\n\t" \
  "v_cmp_lt_f32 vcc, 0x42800000, v192\n\t" \
  "v_mfma_f32_16x16x32_bf16 v[36:39], a[220:223], v[60:63], v[180:183]\n\t" \
  "ds_read_b64_tr_b16 a[148:149], v210 offset:8448\n\t" \
  "v_cvt_pk_bf16_f32 v153, v98, v99\n\t" \
  "v_mfma_f32_16x16x32_bf16 v[36:39], a[216:219], v[56:59], v[36:39]\n\t" \
  "s_cbranch_vccnz .Lsf_c40\n\t" \
  ".Lsf_r40:\n\t" \
  "v_mfma_f32_16x16x32_bf16 v[36:39], a[212:215], v[52:55], v[36:39]\n\t" \
  "ds_read_b64_tr_b16 a[150:151], v210 offset:12544\n\t" \
  "v_max3_f32 v192, v32, v33, v34\n\t" \
  "v_mfma_f32_16x16x32_bf16 v[36:39], a[208:211], v[48:51], v[36:39]\n\t" \
  "v_exp_f32 v100, v100\n\t" \
  "v_mfma_f32_16x16x32_bf16 v[40:43], a[224:227], v[48:51], v[180:183]\n\t" \
  "v_exp_f32 v101, v101\n\t" \
  "v_mfma_f32_16x16x32_bf16 v[40:43], a[228:231], v[52:55], v[40:43]\n\t" \
  "ds_read_b64_tr_b16 a[152:153], v211 offset:256\n\t" \
  "v_cvt_pk_bf16_f32 v154, v100, v101\n\t" \
  "v_mfma_f32_16x16x32_bf16 v[40:43], a[232:235], v[56:59], v[40:43]\n\t" \
  "ds_read_b64_tr_b16 a[154:155], v211 offset:4352\n\t" \
  "v_max3_f32 v203, v35, v36, v37\n\t" \
  "v_mfma_f32_16x16x32_bf16 v[40:43], a[236:239], v[60:63], v[40:43]\n\t" \
  "ds_read_b64_tr_b16 a[156:157], v211 offset:8448\n\t" \
  "v_max3_f32 v192, v192, v38, v39\n\t" \
  "v_mfma_f32_16x16x32_bf16 v[44:47], a[252:255], v[60:63], v[180:183]\n\t" \
  "v_exp_f32 v102, v102\n\t" \
  "v_mfma_f32_16x16x32_bf16 v[44:47], a[248:251], v[56:59], v[44:47]\n\t" \
  "v_exp_f32 v103, v103\n\t" \
  "v_mfma_f32_16x16x32_bf16 v[44:47], a[244:247], v[52:55], v[44:47]\n\t" \
  "ds_read_b64_tr_b16 a[158:159], v211 offset:12544\n\t" \
  "v_max3_f32 v203, v203, v40, v41\n\t" \
  "v_mfma_f32_16x16x32_bf16 v[44:47], a[240:243], v[48:51], v[44:47]\n\t" \
  "ds_read_b64_tr_b16 a[160:161], v210 offset:512\n\t" \
  "v_max3_f32 v192, v192, v42, v43\n\t" \
  "v_mfma_f32_16x16x32_bf16 v[64:67], a[192:195], v[80:83], v[184:187]\n\t" \
  "ds_read_b64_tr_b16 a[162:163], v210 offset:4608\n\t" \
  "v_cvt_pk_bf16_f32 v155, v102, v103\n\t" \
  "v_mfma_f32_16x16x32_bf16 v[64:67], a[196:199], v[84:87], v[64:67]\n\t" \
  "v_exp_f32 v104, v104\n\t" \
  "v_mfma_f32_16x16x32_bf16 v[64:67], a[200:203], v[88:91], v[64:67]\n\t" \
  "v_max3_f32 v203, v203, v44, v45\n\t" \
  "v_max3_f32 v192, v192, v46, v47\n\t" \
  "v_mfma_f32_16x16x32_bf16 v[64:67], a[204:207], v[92:95], v[64:67]\n\t" \
  "v_max_f32 v192, v192, v203\n\t" \
  "v_cmp_lt_f32 vcc, 0x42800000, v192\n\t" \
  "v_mfma_f32_16x16x32_bf16 v[68:71], a[220:223], v[92:95], v[184:187]\n\t" \
  "v_exp_f32 v105, v105\n\t" \
  "v_mfma_f32_16x16x32_bf16 v[68:71], a[216:219], v[88:91], v[68:71]\n\t" \
  "s_cbranch_vccnz .Lsf_c41\n\t" \
  ".Lsf_r41:\n\t" \
  "v_mfma_f32_16x16x32_bf16 v[68:71], a[212:215], v[84:87], v[68:71]\n\t" \
  "ds_read_b64_tr_b16 a[164:165], v210 offset:8704\n\t" \
  "v_max3_f32 v192, v64, v65, v66\n\t" \
  "v_mfma_f32_16x16x32_bf16 v[68:71], a[208:211], v[80:83], v[68:71]\n\t" \
  "ds_read_b64_tr_b16 a[166:167], v210 offset:12800\n\t" \
  "v_cvt_pk_bf16_f32 v156, v104, v105\n\t" \
  "v_mfma_f32_16x16x32_bf16 v[72:75], a[224:227], v[80:83], v[184:187]\n\t" \
  "v_exp_f32 v106, v106\n\t" \
  "v_mfma_f32_16x16x32_bf16 v[72:75], a[228:231], v[84:87], v[72:75]\n\t" \
  "v_exp_f32 v107, v107\n\t" \
  "v_mfma_f32_16x16x32_bf16 v[72:75], a[232:235], v[88:91], v[72:75]\n\t" \
  "ds_read_b64_tr_b16 a[168:169], v211 offset:512\n\t" \
  "v_max3_f32 v203, v67, v68, v69\n\t" \
  "v_mfma_f32_16x16x32_bf16 v[72:75], a[236:239], v[92:95], v[72:75]\n\t" \
  "ds_read_b64_tr_b16 a[170:171], v211 offset:4608\n\t" \
  "v_max3_f32 v192, v192, v70, v71\n\t" \
  "v_mfma_f32_16x16x32_bf16 v[76:79], a[252:255], v[92:95], v[184:187]\n\t" \
  "ds_read_b64_tr_b16 a[172:173], v211 offset:8704\n\t" \
  "v_cvt_pk_bf16_f32 v157, v106, v107\n\t" \
  "v_mfma_f32_16x16x32_bf16 v[76:79], a[248:251], v[88:91], v[76:79]\n\t" \
  "v_exp_f32 v108, v108\n\t" \
  "v_mfma_f32_16x16x32_bf16 v[76:79], a[244:247], v[84:87], v[76:79]\n\t" \
  "v_max3_f32 v203, v203, v72, v73\n\t" \
  "v_max3_f32 v192, v192, v74, v75\n\t" \
  "v_mfma_f32_16x16x32_bf16 v[76:79], a[240:243], v[80:83], v[76:79]\n\t" \
  "v_exp_f32 v109, v109\n\t" \
  "v_mfma_f32_16x16x32_bf16 v[96:99], a[192:195], v[112:115], v[188:191]\n\t" \
  "ds_read_b64_tr_b16 a[174:175], v211 offset:12800\n\t" \
  "v_cvt_pk_bf16_f32 v158, v108, v109\n\t" \
  "v_mfma_f32_16x16x32_bf16 v[96:99], a[196:199], v[116:119], v[96:99]\n\t" \
  "v_exp_f32 v110, v110\n\t" \
  "v_mfma_f32_16x16x32_bf16 v[96:99], a[200:203], v[120:123], v[96:99]\n\t" \
  "ds_read_b64_tr_b16 a[176:177], v210 offset:768\n\t" \
  "v_max3_f32 v203, v203, v76, v77\n\t" \
  "v_mfma_f32_16x16x32_bf16 v[96:99], a[204:207], v[124:127], v[96:99]\n\t" \
  "v_max3_f32 v192, v192, v78, v79\n\t" \
  "v_max_f32 v192, v192, v203\n\t" \
  "v_mfma_f32_16x16x32_bf16 v[100:103], a[220:223], v[124:127], v[188:191]\n\t" \
  "ds_read_b64_tr_b16 a[178:179], v210 offset:4864\n\t" \
  "v_cmp_lt_f32 vcc, 0x42800000, v192\n\t" \
  "v_mfma_f32_16x16x32_bf16 v[100:103], a[216:219], v[120:123], v[100:103]\n\t" \
  "ds_read_b64_tr_b16 a[180:181], v210 offset:8960\n\t" \
  "v_exp_f32 v111, v111\n\t" \
  "v_mfma_f32_16x16x32_bf16 v[100:103], a[212:215], v[116:119], v[100:103]\n\t" \
  "s_cbranch_vccnz .Lsf_c42\n\t" \
  ".Lsf_r42:\n\t" \
  "v_mfma_f32_16x16x32_bf16 v[100:103], a[208:211], v[112:115], v[100:103]\n\t" \
  "ds_read_b64_tr_b16 a[182:183], v210 offset:13056\n\t" \
  "v_max3_f32 v192, v96, v97, v98\n\t" \
  "v_mfma_f32_16x16x32_bf16 v[104:107], a[224:227], v[112:115], v[188:191]\n\t" \
  "ds_read_b64_tr_b16 a[184:185], v211 offset:768\n\t" \
  "v_cvt_pk_bf16_f32 v159, v110, v111\n\t" \
  "v_mfma_f32_16x16x32_bf16 v[104:107], a[228:231], v[116:119], v[104:107]\n\t" \
  "ds_read_b64_tr_b16 a[186:187], v211 offset:4864\n\t" \
  "v_mfma_f32_16x16x32_bf16 v[104:107], a[232:235], v[120:123], v[104:107]\n\t" \
  "ds_read_b64_tr_b16 a[188:189], v211 offset:8960\n\t" \
  "v_max3_f32 v203, v99, v100, v101\n\t" \
  "v_mfma_f32_16x16x32_bf16 v[104:107], a[236:239], v[124:127], v[104:107]\n\t" \
  "ds_read_b64_tr_b16 a[190:191], v211 offset:13056\n\t" \
  "v_max3_f32 v192, v192, v102, v103\n\t" \
  "v_mfma_f32_16x16x32_bf16 v[108:111], a[252:255], v[124:127], v[188:191]\n\t" \
  "v_exp_f32 v0, v0\n\t" \
  "v_mfma_f32_16x16x32_bf16 v[108:111], a[248:251], v[120:123], v[108:111]\n\t" \
  "v_exp_f32 v1, v1\n\t" \
  "v_mfma_f32_16x16x32_bf16 v[108:111], a[244:247], v[116:119], v[108:111]\n\t" \
  "s_waitcnt vmcnt(0) lgkmcnt(0)\n\t" \
  "v_max3_f32 v203, v203, v104, v105\n\t" \
  "v_max3_f32 v192, v192, v106, v107\n\t" \
  "v_mfma_f32_16x16x32_bf16 v[108:111], a[240:243], v[112:115], v[108:111]\n\t" \
  "s_barrier\n\t" \
  "v_mfma_f32_16x16x32_bf16 a[0:3], a[128:131], v[128:131], a[0:3]\n\t" \
  "v_exp_f32 v2, v2\n\t" \
  "v_mfma_f32_16x16x32_bf16 a[0:3], a[132:135], v[132:135], a[0:3]\n\t" \
  "v_exp_f32 v3, v3\n\t" \
  "v_mfma_f32_16x16x32_bf16 a[4:7], a[136:139], v[128:131], a[4:7]\n\t" \
  "v_exp_f32 v4, v4\n\t" \
  "v_mfma_f32_16x16x32_bf16 a[4:7], a[140:143], v[132:135], a[4:7]\n\t" \
  "v_max3_f32 v203, v203, v108, v109\n\t" \
  "v_max3_f32 v192, v192, v110, v111\n\t" \
  "v_mfma_f32_16x16x32_bf16 a[8:11], a[144:147], v[128:131], a[8:11]\n\t" \
  "v_max_f32 v192, v192, v203\n\t" \
  "v_cmp_lt_f32 vcc, 0x42800000, v192\n\t" \
  "v_mfma_f32_16x16x32_bf16 a[8:11], a[148:151], v[132:135], a[8:11]\n\t" \
  "v_exp_f32 v5, v5\n\t" \
  "v_mfma_f32_16x16x32_bf16 a[12:15], a[152:155], v[128:131], a[12:15]\n\t" \
  "s_cbranch_vccnz .Lsf_c43\n\t" \
  ".Lsf_r43:\n\t" \
  "v_mfma_f32_16x16x32_bf16 a[12:15], a[156:159], v[132:135], a[12:15]\n\t" \
  "s_add_u32 m0, %[m], 49152\n\t" \
  "v_exp_f32 v6, v6\n\t" \
  "v_mfma_f32_16x16x32_bf16 a[16:19], a[160:163], v[128:131], a[16:19]\n\t" \
  "s_add_u32 s96, %[m], 98304\n\t" \
  "v_exp_f32 v7, v7\n\t" \
  "v_mfma_f32_16x16x32_bf16 a[16:19], a[164:167], v[132:135], a[16:19]\n\t" \
  "v_exp_f32 v8, v8\n\t" \
  "v_mfma_f32_16x16x32_bf16 a[20:23], a[168:171], v[128:131], a[20:23]\n\t" \
  "buffer_load_dwordx4 v224, s[60:63], s96 offen offset:0 lds\n\t" \
  "v_mfma_f32_16x16x32_bf16 a[20:23], a[172:175], v[132:135], a[20:23]\n\t" \
  "v_exp_f32 v9, v9\n\t" \
  "v_mfma_f32_16x16x32_bf16 a[24:27], a[176:179], v[128:131], a[24:27]\n\t" \
  "v_exp_f32 v10, v10\n\t" \
  "v_mfma_f32_16x16x32_bf16 a[24:27], a[180:183], v[132:135], a[24:27]\n\t" \
  "v_exp_f32 v11, v11\n\t" \
  "v_mfma_f32_16x16x32_bf16 a[28:31], a[184:187], v[128:131], a[28:31]\n\t" \
  "v_exp_f32 v12, v12\n\t" \
  "v_mfma_f32_16x16x32_bf16 a[28:31], a[188:191], v[132:135], a[28:31]\n\t" \
  "v_exp_f32 v13, v13\n\t" \
  "v_mfma_f32_16x16x32_bf16 v[160:163], v[216:219], v[128:131], v[160:163]\n\t" \
  "v_exp_f32 v14, v14\n\t" \
  "v_mfma_f32_16x16x32_bf16 v[160:163], v[216:219], v[132:135], v[160:163]\n\t" \
  "buffer_load_dwordx4 v225, s[60:63], s96 offen offset:1024 lds\n\t" \
  "v_mfma_f32_16x16x32_bf16 a[32:35], a[128:131], v[136:139], a[32:35]\n\t" \
  "ds_read_b128 a[192:195], v220 offset:16384\n\t" \
  "v_cvt_pk_bf16_f32 v128, v0, v1\n\t" \
  "v_mfma_f32_16x16x32_bf16 a[32:35], a[132:135], v[140:143], a[32:35]\n\t" \
  "ds_read_b128 a[196:199], v221 offset:16384\n\t" \
  "v_cvt_pk_bf16_f32 v129, v2, v3\n\t" \
  "v_mfma_f32_16x16x32_bf16 a[36:39], a[136:139], v[136:139], a[36:39]\n\t" \
  "v_exp_f32 v15, v15\n\t" \
  "v_mfma_f32_16x16x32_bf16 a[36:39], a[140:143], v[140:143], a[36:39]\n\t" \
  "v_exp_f32 v32, v32\n\t" \
  "v_mfma_f32_16x16x32_bf16 a[40:43], a[144:147], v[136:139], a[40:43]\n\t" \
  "ds_read_b128 a[200:203], v222 offset:16384\n\t" \
  "v_cvt_pk_bf16_f32 v130, v4, v5\n\t" \
  "v_mfma_f32_16x16x32_bf16 a[40:43], a[148:151], v[140:143], a[40:43]\n\t" \
  "buffer_load_dwordx4 v226, s[60:63], s96 offen offset:2048 lds\n\t" \
  "v_mfma_f32_16x16x32_bf16 a[44:47], a[152:155], v[136:139], a[44:47]\n\t" \
  "ds_read_b128 a[204:207], v223 offset:16384\n\t" \
  "v_cvt_pk_bf16_f32 v131, v6, v7\n\t" \
  "v_mfma_f32_16x16x32_bf16 a[44:47], a[156:159], v[140:143], a[44:47]\n\t" \
  "v_exp_f32 v33, v33\n\t" \
  "v_mfma_f32_16x16x32_bf16 a[48:51], a[160:163], v[136:139], a[48:51]\n\t" \
  "v_exp_f32 v34, v34\n\t" \
  "v_mfma_f32_16x16x32_bf16 a[48:51], a[164:167], v[140:143], a[48:51]\n\t" \
  "ds_read_b128 a[208:211], v220 offset:20480\n\t" \
  "v_cvt_pk_bf16_f32 v132, v8, v9\n\t" \
  "v_mfma_f32_16x16x32_bf16 a[52:55], a[168:171], v[136:139], a[52:55]\n\t" \
  "v_exp_f32 v35, v35\n\t" \
  "v_mfma_f32_16x16x32_bf16 a[52:55], a[172:175], v[140:143], a[52:55]\n\t" \
  "buffer_load_dwordx4 v227, s[60:63], s96 offen offset:3072 lds\n\t" \
  "v_mfma_f32_16x16x32_bf16 a[56:59], a[176:179], v[136:139], a[56:59]\n\t" \
  "ds_read_b128 a[212:215], v221 offset:20480\n\t" \
  "v_cvt_pk_bf16_f32 v133, v10, v11\n\t" \
  "v_mfma_f32_16x16x32_bf16 a[56:59], a[180:183], v[140:143], a[56:59]\n\t" \
  "s_add_u32 m0, %[m], 147456\n\t" \
  "v_exp_f32 v36, v36\n\t" \
  "v_mfma_f32_16x16x32_bf16 a[60:63], a[184:187], v[136:139], a[60:63]\n\t" \
  "s_add_u32 s96, %[m], 81920\n\t" \
  "v_exp_f32 v37, v37\n\t" \
  "v_mfma_f32_16x16x32_bf16 a[60:63], a[188:191], v[140:143], a[60:63]\n\t" \
  "ds_read_b128 a[216:219], v222 offset:20480\n\t" \
  "v_cvt_pk_bf16_f32 v134, v12, v13\n\t" \
  "v_mfma_f32_16x16x32_bf16 v[164:167], v[216:219], v[136:139], v[164:167]\n\t" \
  "v_exp_f32 v38, v38\n\t" \
  "v_mfma_f32_16x16x32_bf16 v[164:167], v[216:219], v[140:143], v[164:167]\n\t" \
  "buffer_load_dwordx4 v230, s[64:67], s96 offen offset:0 lds\n\t" \
  "v_mfma_f32_16x16x32_bf16 a[64:67], a[128:131], v[144:147], a[64:67]\n\t" \
  "ds_read_b128 a[220:223], v223 offset:20480\n\t" \
  "v_cvt_pk_bf16_f32 v135, v14, v15\n\t" \
  "v_mfma_f32_16x16x32_bf16 a[64:67], a[132:135], v[148:151], a[64:67]\n\t" \
  "v_exp_f32 v39, v39\n\t" \
  "v_mfma_f32_16x16x32_bf16 a[68:71], a[136:139], v[144:147], a[68:71]\n\t" \
  "v_exp_f32 v40, v40\n\t" \
  "v_mfma_f32_16x16x32_bf16 a[68:71], a[140:143], v[148:151], a[68:71]\n\t" \
  "ds_read_b128 a[224:227], v220 offset:24576\n\t" \
  "v_cvt_pk_bf16_f32 v136, v32, v33\n\t" \
  "v_mfma_f32_16x16x32_bf16 a[72:75], a[144:147], v[144:147], a[72:75]\n\t" \
  "v_exp_f32 v41, v41\n\t" \
  "v_mfma_f32_16x16x32_bf16 a[72:75], a[148:151], v[148:151], a[72:75]\n\t" \
  "ds_read_b128 a[228:231], v221 offset:24576\n\t" \
  "v_cvt_pk_bf16_f32 v137, v34, v35\n\t" \
  "v_mfma_f32_16x16x32_bf16 a[76:79], a[152:155], v[144:147], a[76:79]\n\t" \
  "buffer_load_dwordx4 v231, s[64:67], s96 offen offset:1024 lds\n\t" \
  "v_mfma_f32_16x16x32_bf16 a[76:79], a[156:159], v[148:151], a[76:79]\n\t" \
  "v_exp_f32 v42, v42\n\t" \
  "v_mfma_f32_16x16x32_bf16 a[80:83], a[160:163], v[144:147], a[80:83]\n\t" \
  "ds_read_b128 a[232:235], v222 offset:24576\n\t" \
  "v_cvt_pk_bf16_f32 v138, v36, v37\n\t" \
  "v_mfma_f32_16x16x32_bf16 a[80:83], a[164:167], v[148:151], a[80:83]\n\t" \
  "v_exp_f32 v43, v43\n\t" \
  "v_mfma_f32_16x16x32_bf16 a[84:87], a[168:171], v[144:147], a[84:87]\n\t" \
  "v_exp_f32 v44, v44\n\t" \
  "v_mfma_f32_16x16x32_bf16 a[84:87], a[172:175], v[148:151], a[84:87]\n\t" \
  "ds_read_b128 a[236:239], v223 offset:24576\n\t" \
  "v_cvt_pk_bf16_f32 v139, v38, v39\n\t" \
  "v_mfma_f32_16x16x32_bf16 a[88:91], a[176:179], v[144:147], a[88:91]\n\t" \
  "buffer_load_dwordx4 v230, s[64:67], s96 offen offset:2048 lds\n\t" \
  "v_mfma_f32_16x16x32_bf16 a[88:91], a[180:183], v[148:151], a[88:91]\n\t" \
  "v_exp_f32 v45, v45\n\t" \
  "v_mfma_f32_16x16x32_bf16 a[92:95], a[184:187], v[144:147], a[92:95]\n\t" \
  "ds_read_b128 a[240:243], v220 offset:28672\n\t" \
  "v_cvt_pk_bf16_f32 v140, v40, v41\n\t" \
  "v_mfma_f32_16x16x32_bf16 a[92:95], a[188:191], v[148:151], a[92:95]\n\t" \
  "v_exp_f32 v46, v46\n\t" \
  "v_mfma_f32_16x16x32_bf16 v[168:171], v[216:219], v[144:147], v[168:171]\n\t" \
  "v_exp_f32 v47, v47\n\t" \
  "v_mfma_f32_16x16x32_bf16 v[168:171], v[216:219], v[148:151], v[168:171]\n\t" \
  "ds_read_b128 a[244:247], v221 offset:28672\n\t" \
  "v_cvt_pk_bf16_f32 v141, v42, v43\n\t" \
  "v_mfma_f32_16x16x32_bf16 a[96:99], a[128:131], v[152:155], a[96:99]\n\t" \
  "buffer_load_dwordx4 v231, s[64:67], s96 offen offset:3072 lds\n\t" \
  "v_mfma_f32_16x16x32_bf16 a[96:99], a[132:135], v[156:159], a[96:99]\n\t" \
  "v_exp_f32 v64, v64\n\t" \
  "v_mfma_f32_16x16x32_bf16 a[100:103], a[136:139], v[152:155], a[100:103]\n\t" \
  "ds_read_b128 a[248:251], v222 offset:28672\n\t" \
  "v_cvt_pk_bf16_f32 v142, v44, v45\n\t" \
  "v_mfma_f32_16x16x32_bf16 a[100:103], a[140:143], v[156:159], a[100:103]\n\t" \
  "v_exp_f32 v65, v65\n\t" \
  "v_mfma_f32_16x16x32_bf16 a[104:107], a[144:147], v[152:155], a[104:107]\n\t" \
  "v_exp_f32 v66, v66\n\t" \
  "v_mfma_f32_16x16x32_bf16 a[104:107], a[148:151], v[156:159], a[104:107]\n\t" \
  "ds_read_b128 a[252:255], v223 offset:28672\n\t" \
  "v_cvt_pk_bf16_f32 v143, v46, v47\n\t" \
  "v_mfma_f32_16x16x32_bf16 a[108:111], a[152:155], v[152:155], a[108:111]\n\t" \
  "v_exp_f32 v67, v67\n\t" \
  "v_mfma_f32_16x16x32_bf16 a[108:111], a[156:159], v[156:159], a[108:111]\n\t" \
  "v_exp_f32 v68, v68\n\t" \
  "v_mfma_f32_16x16x32_bf16 a[112:115], a[160:163], v[152:155], a[112:115]\n\t" \
  "v_exp_f32 v69, v69\n\t" \
  "v_mfma_f32_16x16x32_bf16 a[112:115], a[164:167], v[156:159], a[112:115]\n\t" \
  "v_exp_f32 v70, v70\n\t" \
  "v_mfma_f32_16x16x32_bf16 a[116:119], a[168:171], v[152:155], a[116:119]\n\t" \
  "v_exp_f32 v71, v71\n\t" \
  "v_mfma_f32_16x16x32_bf16 a[116:119], a[172:175], v[156:159], a[116:119]\n\t" \
  "v_exp_f32 v72, v72\n\t" \
  "v_mfma_f32_16x16x32_bf16 a[120:123], a[176:179], v[152:155], a[120:123]\n\t" \
  "v_exp_f32 v73, v73\n\t" \
  "v_mfma_f32_16x16x32_bf16 a[120:123], a[180:183], v[156:159], a[120:123]\n\t" \
  "v_exp_f32 v74, v74\n\t" \
  "v_mfma_f32_16x16x32_bf16 a[124:127], a[184:187], v[152:155], a[124:127]\n\t" \
  "v_exp_f32 v75, v75\n\t" \
  "v_mfma_f32_16x16x32_bf16 a[124:127], a[188:191], v[156:159], a[124:127]\n\t" \
  "v_exp_f32 v76, v76\n\t" \
  "v_mfma_f32_16x16x32_bf16 v[172:175], v[216:219], v[152:155], v[172:175]\n\t" \
  "v_exp_f32 v77, v77\n\t" \
  "v_mfma_f32_16x16x32_bf16 v[172:175], v[216:219], v[156:159], v[172:175]\n\t" \
  "s_waitcnt lgkmcnt(12)\n\t" \
  "s_cmp_lg_u32 s99, 0\n\t" \
  "s_cbranch_scc1 .Lsf_p4\n\t" \
  ".Lsf_q4:\n\t" \
  "v_mfma_f32_16x16x32_bf16 v[0:3], a[192:195], v[16:19], v[176:179]\n\t" \
  "ds_read_b64_tr_b16 a[128:129], v228 offset:0\n\t" \
  "v_cvt_pk_bf16_f32 v144, v64, v65\n\t" \
  "v_mfma_f32_16x16x32_bf16 v[0:3], a[196:199], v[20:23], v[0:3]\n\t" \
  "v_exp_f32 v78, v78\n\t" \
  "v_mfma_f32_16x16x32_bf16 v[0:3], a[200:203], v[24:27], v[0:3]\n\t" \
  "ds_read_b64_tr_b16 a[130:131], v228 offset:4096\n\t" \
  "v_cvt_pk_bf16_f32 v145, v66, v67\n\t" \
  "v_mfma_f32_16x16x32_bf16 v[0:3], a[204:207], v[28:31], v[0:3]\n\t" \
  "s_waitcnt lgkmcnt(10)\n\t" \
  "v_exp_f32 v79, v79\n\t" \
  "v_mfma_f32_16x16x32_bf16 v[4:7], a[220:223], v[28:31], v[176:179]\n\t" \
  "ds_read_b64_tr_b16 a[132:133], v228 offset:8192\n\t" \
  "v_cvt_pk_bf16_f32 v146, v68, v69\n\t" \
  "v_mfma_f32_16x16x32_bf16 v[4:7], a[216:219], v[24:27], v[4:7]\n\t" \
  "v_exp_f32 v96, v96\n\t" \
  "v_mfma_f32_16x16x32_bf16 v[4:7], a[212:215], v[20:23], v[4:7]\n\t" \
  "ds_read_b64_tr_b16 a[134:135], v228 offset:12288\n\t" \
  "v_max3_f32 v192, v0, v1, v2\n\t" \
  "v_mfma_f32_16x16x32_bf16 v[4:7], a[208:211], v[16:19], v[4:7]\n\t" \
  "s_waitcnt lgkmcnt(8)\n\t" \
  "ds_read_b64_tr_b16 a[136:137], v229 offset:0\n\t" \
  "v_cvt_pk_bf16_f32 v147, v70, v71\n\t" \
  "v_mfma_f32_16x16x32_bf16 v[8:11], a[224:227], v[16:19], v[176:179]\n\t" \
  "v_exp_f32 v97, v97\n\t" \
  "v_mfma_f32_16x16x32_bf16 v[8:11], a[228:231], v[20:23], v[8:11]\n\t" \
  "ds_read_b64_tr_b16 a[138:139], v229 offset:4096\n\t" \
  "v_cvt_pk_bf16_f32 v148, v72, v73\n\t" \
  "v_mfma_f32_16x16x32_bf16 v[8:11], a[232:235], v[24:27], v[8:11]\n\t" \
  "v_max3_f32 v203, v3, v4, v5\n\t" \
  "v_max3_f32 v192, v192, v6, v7\n\t" \
  "v_mfma_f32_16x16x32_bf16 v[8:11], a[236:239], v[28:31], v[8:11]\n\t" \
  "s_waitcnt lgkmcnt(6)\n\t" \
  "ds_read_b64_tr_b16 a[140:141], v229 offset:8192\n\t" \
  "v_cvt_pk_bf16_f32 v149, v74, v75\n\t" \
  "v_mfma_f32_16x16x32_bf16 v[12:15], a[252:255], v[28:31], v[176:179]\n\t" \
  "v_exp_f32 v98, v98\n\t" \
  "v_mfma_f32_16x16x32_bf16 v[12:15], a[248:251], v[24:27], v[12:15]\n\t" \
  "ds_read_b64_tr_b16 a[142:143], v229 offset:12288\n\t" \
  "v_cvt_pk_bf16_f32 v150, v76, v77\n\t" \
  "v_mfma_f32_16x16x32_bf16 v[12:15], a[244:247], v[20:23], v[12:15]\n\t" \
  "v_max3_f32 v203, v203, v8, v9\n\t" \
  "v_max3_f32 v192, v192, v10, v11\n\t" \
  "v_mfma_f32_16x16x32_bf16 v[12:15], a[240:243], v[16:19], v[12:15]\n\t" \
  "ds_read_b64_tr_b16 a[144:145], v228 offset:256\n\t" \
  "v_cvt_pk_bf16_f32 v151, v78, v79\n\t" \
  "v_mfma_f32_16x16x32_bf16 v[32:35], a[192:195], v[48:51], v[180:183]\n\t" \
  "ds_read_b64_tr_b16 a[146:147], v228 offset:4352\n\t" \
  "v_cvt_pk_bf16_f32 v152, v96, v97\n\t" \
  "v_mfma_f32_16x16x32_bf16 v[32:35], a[196:199], v[52:55], v[32:35]\n\t" \
  "v_exp_f32 v99, v99\n\t" \
  "v_mfma_f32_16x16x32_bf16 v[32:35], a[200:203], v[56:59], v[32:35]\n\t" \
  "v_max3_f32 v203, v203, v12, v13\n\t" \
  "v_max3_f32 v192, v192, v14, v15\n\t" \
  "v_mfma_f32_16x16x32_bf16 v[32:35], a[204:207], v[60:63], v[32:35]\n\t" \
  "v_max_f32 v192, v192, v203\n\t" \
  "v_cmp_lt_f32 vcc, 0x42800000, v192\n\t" \
  "v_mfma_f32_16x16x32_bf16 v[36:39], a[220:223], v[60:63], v[180:183]\n\t" \
  "ds_read_b64_tr_b16 a[148:149], v228 offset:8448\n\t" \
  "v_cvt_pk_bf16_f32 v153, v98, v99\n\t" \
  "v_mfma_f32_16x16x32_bf16 v[36:39], a[216:219], v[56:59], v[36:39]\n\t" \
  "s_cbranch_vccnz .Lsf_c50\n\t" \
  ".Lsf_r50:\n\t" \
  "v_mfma_f32_16x16x32_bf16 v[36:39], a[212:215], v[52:55], v[36:39]\n\t" \
  "ds_read_b64_tr_b16 a[150:151], v228 offset:12544\n\t" \
  "v_max3_f32 v192, v32, v33, v34\n\t" \
  "v_mfma_f32_16x16x32_bf16 v[36:39], a[208:211], v[48:51], v[36:39]\n\t" \
  "v_exp_f32 v100, v100\n\t" \
  "v_mfma_f32_16x16x32_bf16 v[40:43], a[224:227], v[48:51], v[180:183]\n\t" \
  "v_exp_f32 v101, v101\n\t" \
  "v_mfma_f32_16x16x32_bf16 v[40:43], a[228:231], v[52:55], v[40:43]\n\t" \
  "ds_read_b64_tr_b16 a[152:153], v229 offset:256\n\t" \
  "v_cvt_pk_bf16_f32 v154, v100, v101\n\t" \
  "v_mfma_f32_16x16x32_bf16 v[40:43], a[232:235], v[56:59], v[40:43]\n\t" \
  "ds_read_b64_tr_b16 a[154:155], v229 offset:4352\n\t" \
  "v_max3_f32 v203, v35, v36, v37\n\t" \
  "v_mfma_f32_16x16x32_bf16 v[40:43], a[236:239], v[60:63], v[40:43]\n\t" \
  "ds_read_b64_tr_b16 a[156:157], v229 offset:8448\n\t" \
  "v_max3_f32 v192, v192, v38, v39\n\t" \
  "v_mfma_f32_16x16x32_bf16 v[44:47], a[252:255], v[60:63], v[180:183]\n\t" \
  "v_exp_f32 v102, v102\n\t" \
  "v_mfma_f32_16x16x32_bf16 v[44:47], a[248:251], v[56:59], v[44:47]\n\t" \
  "v_exp_f32 v103, v103\n\t" \
  "v_mfma_f32_16x16x32_bf16 v[44:47], a[244:247], v[52:55], v[44:47]\n\t" \
  "ds_read_b64_tr_b16 a[158:159], v229 offset:12544\n\t" \
  "v_max3_f32 v203, v203, v40, v41\n\t" \
  "v_mfma_f32_16x16x32_bf16 v[44:47], a[240:243], v[48:51], v[44:47]\n\t" \
  "ds_read_b64_tr_b16 a[160:161], v228 offset:512\n\t" \
  "v_max3_f32 v192, v192, v42, v43\n\t" \
  "v_mfma_f32_16x16x32_bf16 v[64:67], a[192:195], v[80:83], v[184:187]\n\t" \
  "ds_read_b64_tr_b16 a[162:163], v228 offset:4608\n\t" \
  "v_cvt_pk_bf16_f32 v155, v102, v103\n\t" \
  "v_mfma_f32_16x16x32_bf16 v[64:67], a[196:199], v[84:87], v[64:67]\n\t" \
  "v_exp_f32 v104, v104\n\t" \
  "v_mfma_f32_16x16x32_bf16 v[64:67], a[200:203], v[88:91], v[64:67]\n\t" \
  "v_max3_f32 v203, v203, v44, v45\n\t" \
  "v_max3_f32 v192, v192, v46, v47\n\t" \
  "v_mfma_f32_16x16x32_bf16 v[64:67], a[204:207], v[92:95], v[64:67]\n\t" \
  "v_max_f32 v192, v192, v203\n\t" \
  "v_cmp_lt_f32 vcc, 0x42800000, v192\n\t" \
  "v_mfma_f32_16x16x32_bf16 v[68:71], a[220:223], v[92:95], v[184:187]\n\t" \
  "v_exp_f32 v105, v105\n\t" \
  "v_mfma_f32_16x16x32_bf16 v[68:71], a[216:219], v[88:91], v[68:71]\n\t" \
  "s_cbranch_vccnz .Lsf_c51\n\t" \
  ".Lsf_r51:\n\t" \
  "v_mfma_f32_16x16x32_bf16 v[68:71], a[212:215], v[84:87], v[68:71]\n\t" \
  "ds_read_b64_tr_b16 a[164:165], v228 offset:8704\n\t" \
  "v_max3_f32 v192, v64, v65, v66\n\t" \
  "v_mfma_f32_16x16x32_bf16 v[68:71], a[208:211], v[80:83], v[68:71]\n\t" \
  "ds_read_b64_tr_b16 a[166:167], v228 offset:12800\n\t" \
  "v_cvt_pk_bf16_f32 v156, v104, v105\n\t" \
  "v_mfma_f32_16x16x32_bf16 v[72:75], a[224:227], v[80:83], v[184:187]\n\t" \
  "v_exp_f32 v106, v106\n\t" \
  "v_mfma_f32_16x16x32_bf16 v[72:75], a[228:231], v[84:87], v[72:75]\n\t" \
  "v_exp_f32 v107, v107\n\t" \
  "v_mfma_f32_16x16x32_bf16 v[72:75], a[232:235], v[88:91], v[72:75]\n\t" \
  "ds_read_b64_tr_b16 a[168:169], v229 offset:512\n\t" \
  "v_max3_f32 v203, v67, v68, v69\n\t" \
  "v_mfma_f32_16x16x32_bf16 v[72:75], a[236:239], v[92:95], v[72:75]\n\t" \
  "ds_read_b64_tr_b16 a[170:171], v229 offset:4608\n\t" \
  "v_max3_f32 v192, v192, v70, v71\n\t" \
  "v_mfma_f32_16x16x32_bf16 v[76:79], a[252:255], v[92:95], v[184:187]\n\t" \
  "ds_read_b64_tr_b16 a[172:173], v229 offset:8704\n\t" \
  "v_cvt_pk_bf16_f32 v157, v106, v107\n\t" \
  "v_mfma_f32_16x16x32_bf16 v[76:79], a[248:251], v[88:91], v[76:79]\n\t" \
  "v_exp_f32 v108, v108\n\t" \
  "v_mfma_f32_16x16x32_bf16 v[76:79], a[244:247], v[84:87], v[76:79]\n\t" \
  "v_max3_f32 v203, v203, v72, v73\n\t" \
  "v_max3_f32 v192, v192, v74, v75\n\t" \
  "v_mfma_f32_16x16x32_bf16 v[76:79], a[240:243], v[80:83], v[76:79]\n\t" \
  "v_exp_f32 v109, v109\n\t" \
  "v_mfma_f32_16x16x32_bf16 v[96:99], a[192:195], v[112:115], v[188:191]\n\t" \
  "ds_read_b64_tr_b16 a[174:175], v229 offset:12800\n\t" \
  "v_cvt_pk_bf16_f32 v158, v108, v109\n\t" \
  "v_mfma_f32_16x16x32_bf16 v[96:99], a[196:199], v[116:119], v[96:99]\n\t" \
  "v_exp_f32 v110, v110\n\t" \
  "v_mfma_f32_16x16x32_bf16 v[96:99], a[200:203], v[120:123], v[96:99]\n\t" \
  "ds_read_b64_tr_b16 a[176:177], v228 offset:768\n\t" \
  "v_max3_f32 v203, v203, v76, v77\n\t" \
  "v_mfma_f32_16x16x32_bf16 v[96:99], a[204:207], v[124:127], v[96:99]\n\t" \
  "v_max3_f32 v192, v192, v78, v79\n\t" \
  "v_max_f32 v192, v192, v203\n\t" \
  "v_mfma_f32_16x16x32_bf16 v[100:103], a[220:223], v[124:127], v[188:191]\n\t" \
  "ds_read_b64_tr_b16 a[178:179], v228 offset:4864\n\t" \
  "v_cmp_lt_f32 vcc, 0x42800000, v192\n\t" \
  "v_mfma_f32_16x16x32_bf16 v[100:103], a[216:219], v[120:123], v[100:103]\n\t" \
  "ds_read_b64_tr_b16 a[180:181], v228 offset:8960\n\t" \
  "v_exp_f32 v111, v111\n\t" \
  "v_mfma_f32_16x16x32_bf16 v[100:103], a[212:215], v[116:119], v[100:103]\n\t" \
  "s_cbranch_vccnz .Lsf_c52\n\t" \
  ".Lsf_r52:\n\t" \
  "v_mfma_f32_16x16x32_bf16 v[100:103], a[208:211], v[112:115], v[100:103]\n\t" \
  "ds_read_b64_tr_b16 a[182:183], v228 offset:13056\n\t" \
  "v_max3_f32 v192, v96, v97, v98\n\t" \
  "v_mfma_f32_16x16x32_bf16 v[104:107], a[224:227], v[112:115], v[188:191]\n\t" \
  "ds_read_b64_tr_b16 a[184:185], v229 offset:768\n\t" \
  "v_cvt_pk_bf16_f32 v159, v110, v111\n\t" \
  "v_mfma_f32_16x16x32_bf16 v[104:107], a[228:231], v[116:119], v[104:107]\n\t" \
  "ds_read_b64_tr_b16 a[186:187], v229 offset:4864\n\t" \
  "v_mfma_f32_16x16x32_bf16 v[104:107], a[232:235], v[120:123], v[104:107]\n\t" \
  "ds_read_b64_tr_b16 a[188:189], v229 offset:8960\n\t" \
  "v_max3_f32 v203, v99, v100, v101\n\t" \
  "v_mfma_f32_16x16x32_bf16 v[104:107], a[236:239], v[124:127], v[104:107]\n\t" \
  "ds_read_b64_tr_b16 a[190:191], v229 offset:13056\n\t" \
  "v_max3_f32 v192, v192, v102, v103\n\t" \
  "v_mfma_f32_16x16x32_bf16 v[108:111], a[252:255], v[124:127], v[188:191]\n\t" \
  "v_exp_f32 v0, v0\n\t" \
  "v_mfma_f32_16x16x32_bf16 v[108:111], a[248:251], v[120:123], v[108:111]\n\t" \
  "v_exp_f32 v1, v1\n\t" \
  "v_mfma_f32_16x16x32_bf16 v[108:111], a[244:247], v[116:119], v[108:111]\n\t" \
  "s_waitcnt vmcnt(0) lgkmcnt(0)\n\t" \
  "v_max3_f32 v203, v203, v104, v105\n\t" \
  "v_max3_f32 v192, v192, v106, v107\n\t" \
  "v_mfma_f32_16x16x32_bf16 v[108:111], a[240:243], v[112:115], v[108:111]\n\t" \
  "s_barrier\n\t" \
  "v_mfma_f32_16x16x32_bf16 a[0:3], a[128:131], v[128:131], a[0:3]\n\t" \
  "v_exp_f32 v2, v2\n\t" \
  "v_mfma_f32_16x16x32_bf16 a[0:3], a[132:135], v[132:135], a[0:3]\n\t" \
  "v_exp_f32 v3, v3\n\t" \
  "v_mfma_f32_16x16x32_bf16 a[4:7], a[136:139], v[128:131], a[4:7]\n\t" \
  "v_exp_f32 v4, v4\n\t" \
  "v_mfma_f32_16x16x32_bf16 a[4:7], a[140:143], v[132:135], a[4:7]\n\t" \
  "v_max3_f32 v203, v203, v108, v109\n\t" \
  "v_max3_f32 v192, v192, v110, v111\n\t" \
  "v_mfma_f32_16x16x32_bf16 a[8:11], a[144:147], v[128:131], a[8:11]\n\t" \
  "v_max_f32 v192, v192, v203\n\t" \
  "v_cmp_lt_f32 vcc, 0x42800000, v192\n\t" \
  "v_mfma_f32_16x16x32_bf16 a[8:11], a[148:151], v[132:135], a[8:11]\n\t" \
  "v_exp_f32 v5, v5\n\t" \
  "v_mfma_f32_16x16x32_bf16 a[12:15], a[152:155], v[128:131], a[12:15]\n\t" \
  "s_cbranch_vccnz .Lsf_c53\n\t" \
  ".Lsf_r53:\n\t" \
  "v_mfma_f32_16x16x32_bf16 a[12:15], a[156:159], v[132:135], a[12:15]\n\t" \
  "s_add_u32 m0, %[m], 65536\n\t" \
  "v_exp_f32 v6, v6\n\t" \
  "v_mfma_f32_16x16x32_bf16 a[16:19], a[160:163], v[128:131], a[16:19]\n\t" \
  "s_add_u32 s96, %[m], 114688\n\t" \
  "v_exp_f32 v7, v7\n\t" \
  "v_mfma_f32_16x16x32_bf16 a[16:19], a[164:167], v[132:135], a[16:19]\n\t" \
  "v_exp_f32 v8, v8\n\t" \
  "v_mfma_f32_16x16x32_bf16 a[20:23], a[168:171], v[128:131], a[20:23]\n\t" \
  "buffer_load_dwordx4 v224, s[60:63], s96 offen offset:0 lds\n\t" \
  "v_mfma_f32_16x16x32_bf16 a[20:23], a[172:175], v[132:135], a[20:23]\n\t" \
  "v_exp_f32 v9, v9\n\t" \
  "v_mfma_f32_16x16x32_bf16 a[24:27], a[176:179], v[128:131], a[24:27]\n\t" \
  "v_exp_f32 v10, v10\n\t" \
  "v_mfma_f32_16x16x32_bf16 a[24:27], a[180:183], v[132:135], a[24:27]\n\t" \
  "v_exp_f32 v11, v11\n\t" \
  "v_mfma_f32_16x16x32_bf16 a[28:31], a[184:187], v[128:131], a[28:31]\n\t" \
  "v_exp_f32 v12, v12\n\t" \
  "v_mfma_f32_16x16x32_bf16 a[28:31], a[188:191], v[132:135], a[28:31]\n\t" \
  "v_exp_f32 v13, v13\n\t" \
  "v_mfma_f32_16x16x32_bf16 v[160:163], v[216:219], v[128:131], v[160:163]\n\t" \
  "v_exp_f32 v14, v14\n\t" \
  "v_mfma_f32_16x16x32_bf16 v[160:163], v[216:219], v[132:135], v[160:163]\n\t" \
  "buffer_load_dwordx4 v225, s[60:63], s96 offen offset:1024 lds\n\t" \
  "v_mfma_f32_16x16x32_bf16 a[32:35], a[128:131], v[136:139], a[32:35]\n\t" \
  "ds_read_b128 a[192:195], v220 offset:0\n\t" \
  "v_cvt_pk_bf16_f32 v128, v0, v1\n\t" \
  "v_mfma_f32_16x16x32_bf16 a[32:35], a[132:135], v[140:143], a[32:35]\n\t" \
  "ds_read_b128 a[196:199], v221 offset:0\n\t" \
  "v_cvt_pk_bf16_f32 v129, v2, v3\n\t" \
  "v_mfma_f32_16x16x32_bf16 a[36:39], a[136:139], v[136:139], a[36:39]\n\t" \
  "v_exp_f32 v15, v15\n\t" \
  "v_mfma_f32_16x16x32_bf16 a[36:39], a[140:143], v[140:143], a[36:39]\n\t" \
  "v_exp_f32 v32, v32\n\t" \
  "v_mfma_f32_16x16x32_bf16 a[40:43], a[144:147], v[136:139], a[40:43]\n\t" \
  "ds_read_b128 a[200:203], v222 offset:0\n\t" \
  "v_cvt_pk_bf16_f32 v130, v4, v5\n\t" \
  "v_mfma_f32_16x16x32_bf16 a[40:43], a[148:151], v[140:143], a[40:43]\n\t" \
  "buffer_load_dwordx4 v226, s[60:63], s96 offen offset:2048 lds\n\t" \
  "v_mfma_f32_16x16x32_bf16 a[44:47], a[152:155], v[136:139], a[44:47]\n\t" \
  "ds_read_b128 a[204:207], v223 offset:0\n\t" \
  "v_cvt_pk_bf16_f32 v131, v6, v7\n\t" \
  "v_mfma_f32_16x16x32_bf16 a[44:47], a[156:159], v[140:143], a[44:47]\n\t" \
  "v_exp_f32 v33, v33\n\t" \
  "v_mfma_f32_16x16x32_bf16 a[48:51], a[160:163], v[136:139], a[48:51]\n\t" \
  "v_exp_f32 v34, v34\n\t" \
  "v_mfma_f32_16x16x32_bf16 a[48:51], a[164:167], v[140:143], a[48:51]\n\t" \
  "ds_read_b128 a[208:211], v220 offset:4096\n\t" \
  "v_cvt_pk_bf16_f32 v132, v8, v9\n\t" \
  "v_mfma_f32_16x16x32_bf16 a[52:55], a[168:171], v[136:139], a[52:55]\n\t" \
  "v_exp_f32 v35, v35\n\t" \
  "v_mfma_f32_16x16x32_bf16 a[52:55], a[172:175], v[140:143], a[52:55]\n\t" \
  "buffer_load_dwordx4 v227, s[60:63], s96 offen offset:3072 lds\n\t" \
  "v_mfma_f32_16x16x32_bf16 a[56:59], a[176:179], v[136:139], a[56:59]\n\t" \
  "ds_read_b128 a[212:215], v221 offset:4096\n\t" \
  "v_cvt_pk_bf16_f32 v133, v10, v11\n\t" \
  "v_mfma_f32_16x16x32_bf16 a[56:59], a[180:183], v[140:143], a[56:59]\n\t" \
  "s_add_u32 m0, %[m], 81920\n\t" \
  "v_exp_f32 v36, v36\n\t" \
  "v_mfma_f32_16x16x32_bf16 a[60:63], a[184:187], v[136:139], a[60:63]\n\t" \
  "s_add_u32 s96, %[m], 98304\n\t" \
  "v_exp_f32 v37, v37\n\t" \
  "v_mfma_f32_16x16x32_bf16 a[60:63], a[188:191], v[140:143], a[60:63]\n\t" \
  "ds_read_b128 a[216:219], v222 offset:4096\n\t" \
  "v_cvt_pk_bf16_f32 v134, v12, v13\n\t" \
  "v_mfma_f32_16x16x32_bf16 v[164:167], v[216:219], v[136:139], v[164:167]\n\t" \
  "v_exp_f32 v38, v38\n\t" \
  "v_mfma_f32_16x16x32_bf16 v[164:167], v[216:219], v[140:143], v[164:167]\n\t" \
  "buffer_load_dwordx4 v230, s[64:67], s96 offen offset:0 lds\n\t" \
  "v_mfma_f32_16x16x32_bf16 a[64:67], a[128:131], v[144:147], a[64:67]\n\t" \
  "ds_read_b128 a[220:223], v223 offset:4096\n\t" \
  "v_cvt_pk_bf16_f32 v135, v14, v15\n\t" \
  "v_mfma_f32_16x16x32_bf16 a[64:67], a[132:135], v[148:151], a[64:67]\n\t" \
  "v_exp_f32 v39, v39\n\t" \
  "v_mfma_f32_16x16x32_bf16 a[68:71], a[136:139], v[144:147], a[68:71]\n\t" \
  "v_exp_f32 v40, v40\n\t" \
  "v_mfma_f32_16x16x32_bf16 a[68:71], a[140:143], v[148:151], a[68:71]\n\t" \
  "ds_read_b128 a[224:227], v220 offset:8192\n\t" \
  "v_cvt_pk_bf16_f32 v136, v32, v33\n\t" \
  "v_mfma_f32_16x16x32_bf16 a[72:75], a[144:147], v[144:147], a[72:75]\n\t" \
  "v_exp_f32 v41, v41\n\t" \
  "v_mfma_f32_16x16x32_bf16 a[72:75], a[148:151], v[148:151], a[72:75]\n\t" \
  "ds_read_b128 a[228:231], v221 offset:8192\n\t" \
  "v_cvt_pk_bf16_f32 v137, v34, v35\n\t" \
  "v_mfma_f32_16x16x32_bf16 a[76:79], a[152:155], v[144:147], a[76:79]\n\t" \
  "buffer_load_dwordx4 v231, s[64:67], s96 offen offset:1024 lds\n\t" \
  "v_mfma_f32_16x16x32_bf16 a[76:79], a[156:159], v[148:151], a[76:79]\n\t" \
  "v_exp_f32 v42, v42\n\t" \
  "v_mfma_f32_16x16x32_bf16 a[80:83], a[160:163], v[144:147], a[80:83]\n\t" \
  "ds_read_b128 a[232:235], v222 offset:8192\n\t" \
  "v_cvt_pk_bf16_f32 v138, v36, v37\n\t" \
  "v_mfma_f32_16x16x32_bf16 a[80:83], a[164:167], v[148:151], a[80:83]\n\t" \
  "v_exp_f32 v43, v43\n\t" \
  "v_mfma_f32_16x16x32_bf16 a[84:87], a[168:171], v[144:147], a[84:87]\n\t" \
  "v_exp_f32 v44, v44\n\t" \
  "v_mfma_f32_16x16x32_bf16 a[84:87], a[172:175], v[148:151], a[84:87]\n\t" \
  "ds_read_b128 a[236:239], v223 offset:8192\n\t" \
  "v_cvt_pk_bf16_f32 v139, v38, v39\n\t" \
  "v_mfma_f32_16x16x32_bf16 a[88:91], a[176:179], v[144:147], a[88:91]\n\t" \
  "buffer_load_dwordx4 v230, s[64:67], s96 offen offset:2048 lds\n\t" \
  "v_mfma_f32_16x16x32_bf16 a[88:91], a[180:183], v[148:151], a[88:91]\n\t" \
  "v_exp_f32 v45, v45\n\t" \
  "v_mfma_f32_16x16x32_bf16 a[92:95], a[184:187], v[144:147], a[92:95]\n\t" \
  "ds_read_b128 a[240:243], v220 offset:12288\n\t" \
  "v_cvt_pk_bf16_f32 v140, v40, v41\n\t" \
  "v_mfma_f32_16x16x32_bf16 a[92:95], a[188:191], v[148:151], a[92:95]\n\t" \
  "v_exp_f32 v46, v46\n\t" \
  "v_mfma_f32_16x16x32_bf16 v[168:171], v[216:219], v[144:147], v[168:171]\n\t" \
  "v_exp_f32 v47, v47\n\t" \
  "v_mfma_f32_16x16x32_bf16 v[168:171], v[216:219], v[148:151], v[168:171]\n\t" \
  "ds_read_b128 a[244:247], v221 offset:12288\n\t" \
  "v_cvt_pk_bf16_f32 v141, v42, v43\n\t" \
  "v_mfma_f32_16x16x32_bf16 a[96:99], a[128:131], v[152:155], a[96:99]\n\t" \
  "buffer_load_dwordx4 v231, s[64:67], s96 offen offset:3072 lds\n\t" \
  "v_mfma_f32_16x16x32_bf16 a[96:99], a[132:135], v[156:159], a[96:99]\n\t" \
  "v_exp_f32 v64, v64\n\t" \
  "v_mfma_f32_16x16x32_bf16 a[100:103], a[136:139], v[152:155], a[100:103]\n\t" \
  "ds_read_b128 a[248:251], v222 offset:12288\n\t" \
  "v_cvt_pk_bf16_f32 v142, v44, v45\n\t" \
  "v_mfma_f32_16x16x32_bf16 a[100:103], a[140:143], v[156:159], a[100:103]\n\t" \
  "v_exp_f32 v65, v65\n\t" \
  "v_mfma_f32_16x16x32_bf16 a[104:107], a[144:147], v[152:155], a[104:107]\n\t" \
  "v_exp_f32 v66, v66\n\t" \
  "v_mfma_f32_16x16x32_bf16 a[104:107], a[148:151], v[156:159], a[104:107]\n\t" \
  "ds_read_b128 a[252:255], v223 offset:12288\n\t" \
  "v_cvt_pk_bf16_f32 v143, v46, v47\n\t" \
  "v_mfma_f32_16x16x32_bf16 a[108:111], a[152:155], v[152:155], a[108:111]\n\t" \
  "v_exp_f32 v67, v67\n\t" \
  "v_mfma_f32_16x16x32_bf16 a[108:111], a[156:159], v[156:159], a[108:111]\n\t" \
  "v_exp_f32 v68, v68\n\t" \
  "v_mfma_f32_16x16x32_bf16 a[112:115], a[160:163], v[152:155], a[112:115]\n\t" \
  "v_exp_f32 v69, v69\n\t" \
  "v_mfma_f32_16x16x32_bf16 a[112:115], a[164:167], v[156:159], a[112:115]\n\t" \
  "v_exp_f32 v70, v70\n\t" \
  "v_mfma_f32_16x16x32_bf16 a[116:119], a[168:171], v[152:155], a[116:119]\n\t" \
  "v_exp_f32 v71, v71\n\t" \
  "v_mfma_f32_16x16x32_bf16 a[116:119], a[172:175], v[156:159], a[116:119]\n\t" \
  "v_exp_f32 v72, v72\n\t" \
  "v_mfma_f32_16x16x32_bf16 a[120:123], a[176:179], v[152:155], a[120:123]\n\t" \
  "v_exp_f32 v73, v73\n\t" \
  "v_mfma_f32_16x16x32_bf16 a[120:123], a[180:183], v[156:159], a[120:123]\n\t" \
  "v_exp_f32 v74, v74\n\t" \
  "v_mfma_f32_16x16x32_bf16 a[124:127], a[184:187], v[152:155], a[124:127]\n\t" \
  "v_exp_f32 v75, v75\n\t" \
  "v_mfma_f32_16x16x32_bf16 a[124:127], a[188:191], v[156:159], a[124:127]\n\t" \
  "v_exp_f32 v76, v76\n\t" \
  "v_mfma_f32_16x16x32_bf16 v[172:175], v[216:219], v[152:155], v[172:175]\n\t" \
  "v_exp_f32 v77, v77\n\t" \
  "v_mfma_f32_16x16x32_bf16 v[172:175], v[216:219], v[156:159], v[172:175]\n\t" \
  "s_waitcnt lgkmcnt(12)\n\t" \
  "s_cmp_lg_u32 s99, 0\n\t" \
  "s_cbranch_scc1 .Lsf_p5\n\t" \
  ".Lsf_q5:\n\t" \
  "v_mfma_f32_16x16x32_bf16 v[0:3], a[192:195], v[16:19], v[176:179]\n\t" \
  "ds_read_b64_tr_b16 a[128:129], v210 offset:0\n\t" \
  "v_cvt_pk_bf16_f32 v144, v64, v65\n\t" \
  "v_mfma_f32_16x16x32_bf16 v[0:3], a[196:199], v[20:23], v[0:3]\n\t" \
  "v_exp_f32 v78, v78\n\t" \
  "v_mfma_f32_16x16x32_bf16 v[0:3], a[200:203], v[24:27], v[0:3]\n\t" \
  "ds_read_b64_tr_b16 a[130:131], v210 offset:4096\n\t" \
  "v_cvt_pk_bf16_f32 v145, v66, v67\n\t" \
  "v_mfma_f32_16x16x32_bf16 v[0:3], a[204:207], v[28:31], v[0:3]\n\t" \
  "s_waitcnt lgkmcnt(10)\n\t" \
  "v_exp_f32 v79, v79\n\t" \
  "v_mfma_f32_16x16x32_bf16 v[4:7], a[220:223], v[28:31], v[176:179]\n\t" \
  "ds_read_b64_tr_b16 a[132:133], v210 offset:8192\n\t" \
  "v_cvt_pk_bf16_f32 v146, v68, v69\n\t" \
  "v_mfma_f32_16x16x32_bf16 v[4:7], a[216:219], v[24:27], v[4:7]\n\t" \
  "v_exp_f32 v96, v96\n\t" \
  "v_mfma_f32_16x16x32_bf16 v[4:7], a[212:215], v[20:23], v[4:7]\n\t" \
  "ds_read_b64_tr_b16 a[134:135], v210 offset:12288\n\t" \
  "v_max3_f32 v192, v0, v1, v2\n\t" \
  "v_mfma_f32_16x16x32_bf16 v[4:7], a[208:211], v[16:19], v[4:7]\n\t" \
  "s_waitcnt lgkmcnt(8)\n\t" \
  "ds_read_b64_tr_b16 a[136:137], v211 offset:0\n\t" \
  "v_cvt_pk_bf16_f32 v147, v70, v71\n\t" \
  "v_mfma_f32_16x16x32_bf16 v[8:11], a[224:227], v[16:19], v[176:179]\n\t" \
  "v_exp_f32 v97, v97\n\t" \
  "v_mfma_f32_16x16x32_bf16 v[8:11], a[228:231], v[20:23], v[8:11]\n\t" \
  "ds_read_b64_tr_b16 a[138:139], v211 offset:4096\n\t" \
  "v_cvt_pk_bf16_f32 v148, v72, v73\n\t" \
  "v_mfma_f32_16x16x32_bf16 v[8:11], a[232:235], v[24:27], v[8:11]\n\t" \
  "v_max3_f32 v203, v3, v4, v5\n\t" \
  "v_max3_f32 v192, v192, v6, v7\n\t" \
  "v_mfma_f32_16x16x32_bf16 v[8:11], a[236:239], v[28:31], v[8:11]\n\t" \
  "s_waitcnt lgkmcnt(6)\n\t" \
  "ds_read_b64_tr_b16 a[140:141], v211 offset:8192\n\t" \
  "v_cvt_pk_bf16_f32 v149, v74, v75\n\t" \
  "v_mfma_f32_16x16x32_bf16 v[12:15], a[252:255], v[28:31], v[176:179]\n\t" \
  "v_exp_f32 v98, v98\n\t" \
  "v_mfma_f32_16x16x32_bf16 v[12:15], a[248:251], v[24:27], v[12:15]\n\t" \
  "ds_read_b64_tr_b16 a[142:143], v211 offset:12288\n\t" \
  "v_cvt_pk_bf16_f32 v150, v76, v77\n\t" \
  "v_mfma_f32_16x16x32_bf16 v[12:15], a[244:247], v[20:23], v[12:15]\n\t" \
  "v_max3_f32 v203, v203, v8, v9\n\t" \
  "v_max3_f32 v192, v192, v10, v11\n\t" \
  "v_mfma_f32_16x16x32_bf16 v[12:15], a[240:243], v[16:19], v[12:15]\n\t" \
  "ds_read_b64_tr_b16 a[144:145], v210 offset:256\n\t" \
  "v_cvt_pk_bf16_f32 v151, v78, v79\n\t" \
  "v_mfma_f32_16x16x32_bf16 v[32:35], a[192:195], v[48:51], v[180:183]\n\t" \
  "ds_read_b64_tr_b16 a[146:147], v210 offset:4352\n\t" \
  "v_cvt_pk_bf16_f32 v152, v96, v97\n\t" \
  "v_mfma_f32_16x16x32_bf16 v[32:35], a[196:199], v[52:55], v[32:35]\n\t" \
  "v_exp_f32 v99, v99\n\t" \
  "v_mfma_f32_16x16x32_bf16 v[32:35], a[200:203], v[56:59], v[32:35]\n\t" \
  "v_max3_f32 v203, v203, v12, v13\n\t" \
  "v_max3_f32 v192, v192, v14, v15\n\t" \
  "v_mfma_f32_16x16x32_bf16 v[32:35], a[204:207], v[60:63], v[32:35]\n\t" \
  "v_max_f32 v192, v192, v203\n\t" \
  "v_cmp_lt_f32 vcc, 0x42800000, v192\n\t" \
  "v_mfma_f32_16x16x32_bf16 v[36:39], a[220:223], v[60:63], v[180:183]\n\t" \
  "ds_read_b64_tr_b16 a[148:149], v210 offset:8448\n\t" \
  "v_cvt_pk_bf16_f32 v153, v98, v99\n\t" \
  "v_mfma_f32_16x16x32_bf16 v[36:39], a[216:219], v[56:59], v[36:39]\n\t" \
  "s_cbranch_vccnz .Lsf_c60\n\t" \
  ".Lsf_r60:\n\t" \
  "v_mfma_f32_16x16x32_bf16 v[36:39], a[212:215], v[52:55], v[36:39]\n\t" \
  "ds_read_b64_tr_b16 a[150:151], v210 offset:12544\n\t" \
  "v_max3_f32 v192, v32, v33, v34\n\t" \
  "v_mfma_f32_16x16x32_bf16 v[36:39], a[208:211], v[48:51], v[36:39]\n\t" \
  "v_exp_f32 v100, v100\n\t" \
  "v_mfma_f32_16x16x32_bf16 v[40:43], a[224:227], v[48:51], v[180:183]\n\t" \
  "v_exp_f32 v101, v101\n\t" \
  "v_mfma_f32_16x16x32_bf16 v[40:43], a[228:231], v[52:55], v[40:43]\n\t" \
  "ds_read_b64_tr_b16 a[152:153], v211 offset:256\n\t" \
  "v_cvt_pk_bf16_f32 v154, v100, v101\n\t" \
  "v_mfma_f32_16x16x32_bf16 v[40:43], a[232:235], v[56:59], v[40:43]\n\t" \
  "ds_read_b64_tr_b16 a[154:155], v211 offset:4352\n\t" \
  "v_max3_f32 v203, v35, v36, v37\n\t" \
  "v_mfma_f32_16x16x32_bf16 v[40:43], a[236:239], v[60:63], v[40:43]\n\t" \
  "ds_read_b64_tr_b16 a[156:157], v211 offset:8448\n\t" \
  "v_max3_f32 v192, v192, v38, v39\n\t" \
  "v_mfma_f32_16x16x32_bf16 v[44:47], a[252:255], v[60:63], v[180:183]\n\t" \
  "v_exp_f32 v102, v102\n\t" \
  "v_mfma_f32_16x16x32_bf16 v[44:47], a[248:251], v[56:59], v[44:47]\n\t" \
  "v_exp_f32 v103, v103\n\t" \
  "v_mfma_f32_16x16x32_bf16 v[44:47], a[244:247], v[52:55], v[44:47]\n\t" \
  "ds_read_b64_tr_b16 a[158:159], v211 offset:12544\n\t" \
  "v_max3_f32 v203, v203, v40, v41\n\t" \
  "v_mfma_f32_16x16x32_bf16 v[44:47], a[240:243], v[48:51], v[44:47]\n\t" \
  "ds_read_b64_tr_b16 a[160:161], v210 offset:512\n\t" \
  "v_max3_f32 v192, v192, v42, v43\n\t" \
  "v_mfma_f32_16x16x32_bf16 v[64:67], a[192:195], v[80:83], v[184:187]\n\t" \
  "ds_read_b64_tr_b16 a[162:163], v210 offset:4608\n\t" \
  "v_cvt_pk_bf16_f32 v155, v102, v103\n\t" \
  "v_mfma_f32_16x16x32_bf16 v[64:67], a[196:199], v[84:87], v[64:67]\n\t" \
  "v_exp_f32 v104, v104\n\t" \
  "v_mfma_f32_16x16x32_bf16 v[64:67], a[200:203], v[88:91], v[64:67]\n\t" \
  "v_max3_f32 v203, v203, v44, v45\n\t" \
  "v_max3_f32 v192, v192, v46, v47\n\t" \
  "v_mfma_f32_16x16x32_bf16 v[64:67], a[204:207], v[92:95], v[64:67]\n\t" \
  "v_max_f32 v192, v192, v203\n\t" \
  "v_cmp_lt_f32 vcc, 0x42800000, v192\n\t" \
  "v_mfma_f32_16x16x32_bf16 v[68:71], a[220:223], v[92:95], v[184:187]\n\t" \
  "v_exp_f32 v105, v105\n\t" \
  "v_mfma_f32_16x16x32_bf16 v[68:71], a[216:219], v[88:91], v[68:71]\n\t" \
  "s_cbranch_vccnz .Lsf_c61\n\t" \
  ".Lsf_r61:\n\t" \
  "v_mfma_f32_16x16x32_bf16 v[68:71], a[212:215], v[84:87], v[68:71]\n\t" \
  "ds_read_b64_tr_b16 a[164:165], v210 offset:8704\n\t" \
  "v_max3_f32 v192, v64, v65, v66\n\t" \
  "v_mfma_f32_16x16x32_bf16 v[68:71], a[208:211], v[80:83], v[68:71]\n\t" \
  "ds_read_b64_tr_b16 a[166:167], v210 offset:12800\n\t" \
  "v_cvt_pk_bf16_f32 v156, v104, v105\n\t" \
  "v_mfma_f32_16x16x32_bf16 v[72:75], a[224:227], v[80:83], v[184:187]\n\t" \
  "v_exp_f32 v106, v106\n\t" \
  "v_mfma_f32_16x16x32_bf16 v[72:75], a[228:231], v[84:87], v[72:75]\n\t" \
  "v_exp_f32 v107, v107\n\t" \
  "v_mfma_f32_16x16x32_bf16 v[72:75], a[232:235], v[88:91], v[72:75]\n\t" \
  "ds_read_b64_tr_b16 a[168:169], v211 offset:512\n\t" \
  "v_max3_f32 v203, v67, v68, v69\n\t" \
  "v_mfma_f32_16x16x32_bf16 v[72:75], a[236:239], v[92:95], v[72:75]\n\t" \
  "ds_read_b64_tr_b16 a[170:171], v211 offset:4608\n\t" \
  "v_max3_f32 v192, v192, v70, v71\n\t" \
  "v_mfma_f32_16x16x32_bf16 v[76:79], a[252:255], v[92:95], v[184:187]\n\t" \
  "ds_read_b64_tr_b16 a[172:173], v211 offset:8704\n\t" \
  "v_cvt_pk_bf16_f32 v157, v106, v107\n\t" \
  "v_mfma_f32_16x16x32_bf16 v[76:79], a[248:251], v[88:91], v[76:79]\n\t" \
  "v_exp_f32 v108, v108\n\t" \
  "v_mfma_f32_16x16x32_bf16 v[76:79], a[244:247], v[84:87], v[76:79]\n\t" \
  "v_max3_f32 v203, v203, v72, v73\n\t" \
  "v_max3_f32 v192, v192, v74, v75\n\t" \
  "v_mfma_f32_16x16x32_bf16 v[76:79], a[240:243], v[80:83], v[76:79]\n\t" \
  "v_exp_f32 v109, v109\n\t" \
  "v_mfma_f32_16x16x32_bf16 v[96:99], a[192:195], v[112:115], v[188:191]\n\t" \
  "ds_read_b64_tr_b16 a[174:175], v211 offset:12800\n\t" \
  "v_cvt_pk_bf16_f32 v158, v108, v109\n\t" \
  "v_mfma_f32_16x16x32_bf16 v[96:99], a[196:199], v[116:119], v[96:99]\n\t" \
  "v_exp_f32 v110, v110\n\t" \
  "v_mfma_f32_16x16x32_bf16 v[96:99], a[200:203], v[120:123], v[96:99]\n\t" \
  "ds_read_b64_tr_b16 a[176:177], v210 offset:768\n\t" \
  "v_max3_f32 v203, v203, v76, v77\n\t" \
  "v_mfma_f32_16x16x32_bf16 v[96:99], a[204:207], v[124:127], v[96:99]\n\t" \
  "v_max3_f32 v192, v192, v78, v79\n\t" \
  "v_max_f32 v192, v192, v203\n\t" \
  "v_mfma_f32_16x16x32_bf16 v[100:103], a[220:223], v[124:127], v[188:191]\n\t" \
  "ds_read_b64_tr_b16 a[178:179], v210 offset:4864\n\t" \
  "v_cmp_lt_f32 vcc, 0x42800000, v192\n\t" \
  "v_mfma_f32_16x16x32_bf16 v[100:103], a[216:219], v[120:123], v[100:103]\n\t" \
  "ds_read_b64_tr_b16 a[180:181], v210 offset:8960\n\t" \
  "v_exp_f32 v111, v111\n\t" \
  "v_mfma_f32_16x16x32_bf16 v[100:103], a[212:215], v[116:119], v[100:103]\n\t" \
  "s_cbranch_vccnz .Lsf_c62\n\t" \
  ".Lsf_r62:\n\t" \
  "v_mfma_f32_16x16x32_bf16 v[100:103], a[208:211], v[112:115], v[100:103]\n\t" \
  "ds_read_b64_tr_b16 a[182:183], v210 offset:13056\n\t" \
  "v_max3_f32 v192, v96, v97, v98\n\t" \
  "v_mfma_f32_16x16x32_bf16 v[104:107], a[224:227], v[112:115], v[188:191]\n\t" \
  "ds_read_b64_tr_b16 a[184:185], v211 offset:768\n\t" \
  "v_cvt_pk_bf16_f32 v159, v110, v111\n\t" \
  "v_mfma_f32_16x16x32_bf16 v[104:107], a[228:231], v[116:119], v[104:107]\n\t" \
  "ds_read_b64_tr_b16 a[186:187], v211 offset:4864\n\t" \
  "v_mfma_f32_16x16x32_bf16 v[104:107], a[232:235], v[120:123], v[104:107]\n\t" \
  "ds_read_b64_tr_b16 a[188:189], v211 offset:8960\n\t" \
  "v_max3_f32 v203, v99, v100, v101\n\t" \
  "v_mfma_f32_16x16x32_bf16 v[104:107], a[236:239], v[124:127], v[104:107]\n\t" \
  "ds_read_b64_tr_b16 a[190:191], v211 offset:13056\n\t" \
  "v_max3_f32 v192, v192, v102, v103\n\t" \
  "v_mfma_f32_16x16x32_bf16 v[108:111], a[252:255], v[124:127], v[188:191]\n\t" \
  "v_exp_f32 v0, v0\n\t" \
  "v_mfma_f32_16x16x32_bf16 v[108:111], a[248:251], v[120:123], v[108:111]\n\t" \
  "v_exp_f32 v1, v1\n\t" \
  "v_mfma_f32_16x16x32_bf16 v[108:111], a[244:247], v[116:119], v[108:111]\n\t" \
  "s_waitcnt vmcnt(0) lgkmcnt(0)\n\t" \
  "v_max3_f32 v203, v203, v104, v105\n\t" \
  "v_max3_f32 v192, v192, v106, v107\n\t" \
  "v_mfma_f32_16x16x32_bf16 v[108:111], a[240:243], v[112:115], v[108:111]\n\t" \
  "s_barrier\n\t" \
  "v_mfma_f32_16x16x32_bf16 a[0:3], a[128:131], v[128:131], a[0:3]\n\t" \
  "v_exp_f32 v2, v2\n\t" \
  "v_mfma_f32_16x16x32_bf16 a[0:3], a[132:135], v[132:135], a[0:3]\n\t" \
  "v_exp_f32 v3, v3\n\t" \
  "v_mfma_f32_16x16x32_bf16 a[4:7], a[136:139], v[128:131], a[4:7]\n\t" \
  "v_exp_f32 v4, v4\n\t" \
  "v_mfma_f32_16x16x32_bf16 a[4:7], a[140:143], v[132:135], a[4:7]\n\t" \
  "v_max3_f32 v203, v203, v108, v109\n\t" \
  "v_max3_f32 v192, v192, v110, v111\n\t" \
  "v_mfma_f32_16x16x32_bf16 a[8:11], a[144:147], v[128:131], a[8:11]\n\t" \
  "v_max_f32 v192, v192, v203\n\t" \
  "v_cmp_lt_f32 vcc, 0x42800000, v192\n\t" \
  "v_mfma_f32_16x16x32_bf16 a[8:11], a[148:151], v[132:135], a[8:11]\n\t" \
  "v_exp_f32 v5, v5\n\t" \
  "v_mfma_f32_16x16x32_bf16 a[12:15], a[152:155], v[128:131], a[12:15]\n\t" \
  "s_cbranch_vccnz .Lsf_c63\n\t" \
  ".Lsf_r63:\n\t" \
  "v_mfma_f32_16x16x32_bf16 a[12:15], a[156:159], v[132:135], a[12:15]\n\t" \
  "s_add_u32 m0, %[m], 147456\n\t" \
  "v_exp_f32 v6, v6\n\t" \
  "v_mfma_f32_16x16x32_bf16 a[16:19], a[160:163], v[128:131], a[16:19]\n\t" \
  "s_add_u32 s96, %[m], 114688\n\t" \
  "v_exp_f32 v7, v7\n\t" \
  "v_mfma_f32_16x16x32_bf16 a[16:19], a[164:167], v[132:135], a[16:19]\n\t" \
  "v_exp_f32 v8, v8\n\t" \
  "v_mfma_f32_16x16x32_bf16 a[20:23], a[168:171], v[128:131], a[20:23]\n\t" \
  "buffer_load_dwordx4 v230, s[64:67], s96 offen offset:0 lds\n\t" \
  "v_mfma_f32_16x16x32_bf16 a[20:23], a[172:175], v[132:135], a[20:23]\n\t" \
  "v_exp_f32 v9, v9\n\t" \
  "v_mfma_f32_16x16x32_bf16 a[24:27], a[176:179], v[128:131], a[24:27]\n\t" \
  "v_exp_f32 v10, v10\n\t" \
  "v_mfma_f32_16x16x32_bf16 a[24:27], a[180:183], v[132:135], a[24:27]\n\t" \
  "v_exp_f32 v11, v11\n\t" \
  "v_mfma_f32_16x16x32_bf16 a[28:31], a[184:187], v[128:131], a[28:31]\n\t" \
  "v_exp_f32 v12, v12\n\t" \
  "v_mfma_f32_16x16x32_bf16 a[28:31], a[188:191], v[132:135], a[28:31]\n\t" \
  "v_exp_f32 v13, v13\n\t" \
  "v_mfma_f32_16x16x32_bf16 v[160:163], v[216:219], v[128:131], v[160:163]\n\t" \
  "v_exp_f32 v14, v14\n\t" \
  "v_mfma_f32_16x16x32_bf16 v[160:163], v[216:219], v[132:135], v[160:163]\n\t" \
  "ds_read_b128 a[192:195], v220 offset:16384\n\t" \
  "v_cvt_pk_bf16_f32 v128, v0, v1\n\t" \
  "v_mfma_f32_16x16x32_bf16 a[32:35], a[128:131], v[136:139], a[32:35]\n\t" \
  "ds_read_b128 a[196:199], v221 offset:16384\n\t" \
  "v_cvt_pk_bf16_f32 v129, v2, v3\n\t" \
  "v_mfma_f32_16x16x32_bf16 a[32:35], a[132:135], v[140:143], a[32:35]\n\t" \
  "v_exp_f32 v15, v15\n\t" \
  "v_mfma_f32_16x16x32_bf16 a[36:39], a[136:139], v[136:139], a[36:39]\n\t" \
  "v_exp_f32 v32, v32\n\t" \
  "v_mfma_f32_16x16x32_bf16 a[36:39], a[140:143], v[140:143], a[36:39]\n\t" \
  "ds_read_b128 a[200:203], v222 offset:16384\n\t" \
  "v_cvt_pk_bf16_f32 v130, v4, v5\n\t" \
  "v_mfma_f32_16x16x32_bf16 a[40:43], a[144:147], v[136:139], a[40:43]\n\t" \
  "v_exp_f32 v33, v33\n\t" \
  "v_mfma_f32_16x16x32_bf16 a[40:43], a[148:151], v[140:143], a[40:43]\n\t" \
  "buffer_load_dwordx4 v231, s[64:67], s96 offen offset:1024 lds\n\t" \
  "v_mfma_f32_16x16x32_bf16 a[44:47], a[152:155], v[136:139], a[44:47]\n\t" \
  "ds_read_b128 a[204:207], v223 offset:16384\n\t" \
  "v_cvt_pk_bf16_f32 v131, v6, v7\n\t" \
  "v_mfma_f32_16x16x32_bf16 a[44:47], a[156:159], v[140:143], a[44:47]\n\t" \
  "v_exp_f32 v34, v34\n\t" \
  "v_mfma_f32_16x16x32_bf16 a[48:51], a[160:163], v[136:139], a[48:51]\n\t" \
  "v_exp_f32 v35, v35\n\t" \
  "v_mfma_f32_16x16x32_bf16 a[48:51], a[164:167], v[140:143], a[48:51]\n\t" \
  "ds_read_b128 a[208:211], v220 offset:20480\n\t" \
  "v_cvt_pk_bf16_f32 v132, v8, v9\n\t" \
  "v_mfma_f32_16x16x32_bf16 a[52:55], a[168:171], v[136:139], a[52:55]\n\t" \
  "v_exp_f32 v36, v36\n\t" \
  "v_mfma_f32_16x16x32_bf16 a[52:55], a[172:175], v[140:143], a[52:55]\n\t" \
  "v_exp_f32 v37, v37\n\t" \
  "v_mfma_f32_16x16x32_bf16 a[56:59], a[176:179], v[136:139], a[56:59]\n\t" \
  "ds_read_b128 a[212:215], v221 offset:20480\n\t" \
  "v_cvt_pk_bf16_f32 v133, v10, v11\n\t" \
  "v_mfma_f32_16x16x32_bf16 a[56:59], a[180:183], v[140:143], a[56:59]\n\t" \
  "v_exp_f32 v38, v38\n\t" \
  "v_mfma_f32_16x16x32_bf16 a[60:63], a[184:187], v[136:139], a[60:63]\n\t" \
  "ds_read_b128 a[216:219], v222 offset:20480\n\t" \
  "v_cvt_pk_bf16_f32 v134, v12, v13\n\t" \
  "v_mfma_f32_16x16x32_bf16 a[60:63], a[188:191], v[140:143], a[60:63]\n\t" \
  "v_exp_f32 v39, v39\n\t" \
  "v_mfma_f32_16x16x32_bf16 v[164:167], v[216:219], v[136:139], v[164:167]\n\t" \
  "v_exp_f32 v40, v40\n\t" \
  "v_mfma_f32_16x16x32_bf16 v[164:167], v[216:219], v[140:143], v[164:167]\n\t" \
  "buffer_load_dwordx4 v230, s[64:67], s96 offen offset:2048 lds\n\t" \
  "v_mfma_f32_16x16x32_bf16 a[64:67], a[128:131], v[144:147], a[64:67]\n\t" \
  "ds_read_b128 a[220:223], v223 offset:20480\n\t" \
  "v_cvt_pk_bf16_f32 v135, v14, v15\n\t" \
  "v_mfma_f32_16x16x32_bf16 a[64:67], a[132:135], v[148:151], a[64:67]\n\t" \
  "v_exp_f32 v41, v41\n\t" \
  "v_mfma_f32_16x16x32_bf16 a[68:71], a[136:139], v[144:147], a[68:71]\n\t" \
  "ds_read_b128 a[224:227], v220 offset:24576\n\t" \
  "v_cvt_pk_bf16_f32 v136, v32, v33\n\t" \
  "v_mfma_f32_16x16x32_bf16 a[68:71], a[140:143], v[148:151], a[68:71]\n\t" \
  "v_exp_f32 v42, v42\n\t" \
  "v_mfma_f32_16x16x32_bf16 a[72:75], a[144:147], v[144:147], a[72:75]\n\t" \
  "v_exp_f32 v43, v43\n\t" \
  "v_mfma_f32_16x16x32_bf16 a[72:75], a[148:151], v[148:151], a[72:75]\n\t" \
  "ds_read_b128 a[228:231], v221 offset:24576\n\t" \
  "v_cvt_pk_bf16_f32 v137, v34, v35\n\t" \
  "v_mfma_f32_16x16x32_bf16 a[76:79], a[152:155], v[144:147], a[76:79]\n\t" \
  "v_exp_f32 v44, v44\n\t" \
  "v_mfma_f32_16x16x32_bf16 a[76:79], a[156:159], v[148:151], a[76:79]\n\t" \
  "v_exp_f32 v45, v45\n\t" \
  "v_mfma_f32_16x16x32_bf16 a[80:83], a[160:163], v[144:147], a[80:83]\n\t" \
  "ds_read_b128 a[232:235], v222 offset:24576\n\t" \
  "v_cvt_pk_bf16_f32 v138, v36, v37\n\t" \
  "v_mfma_f32_16x16x32_bf16 a[80:83], a[164:167], v[148:151], a[80:83]\n\t" \
  "v_exp_f32 v46, v46\n\t" \
  "v_mfma_f32_16x16x32_bf16 a[84:87], a[168:171], v[144:147], a[84:87]\n\t" \
  "v_exp_f32 v47, v47\n\t" \
  "v_mfma_f32_16x16x32_bf16 a[84:87], a[172:175], v[148:151], a[84:87]\n\t" \
  "ds_read_b128 a[236:239], v223 offset:24576\n\t" \
  "v_cvt_pk_bf16_f32 v139, v38, v39\n\t" \
  "v_mfma_f32_16x16x32_bf16 a[88:91], a[176:179], v[144:147], a[88:91]\n\t" \
  "buffer_load_dwordx4 v231, s[64:67], s96 offen offset:3072 lds\n\t" \
  "v_mfma_f32_16x16x32_bf16 a[88:91], a[180:183], v[148:151], a[88:91]\n\t" \
  "v_exp_f32 v64, v64\n\t" \
  "v_mfma_f32_16x16x32_bf16 a[92:95], a[184:187], v[144:147], a[92:95]\n\t" \
  "ds_read_b128 a[240:243], v220 offset:28672\n\t" \
  "v_cvt_pk_bf16_f32 v140, v40, v41\n\t" \
  "v_mfma_f32_16x16x32_bf16 a[92:95], a[188:191], v[148:151], a[92:95]\n\t" \
  "v_exp_f32 v65, v65\n\t" \
  "v_mfma_f32_16x16x32_bf16 v[168:171], v[216:219], v[144:147], v[168:171]\n\t" \
  "v_exp_f32 v66, v66\n\t" \
  "v_mfma_f32_16x16x32_bf16 v[168:171], v[216:219], v[148:151], v[168:171]\n\t" \
  "ds_read_b128 a[244:247], v221 offset:28672\n\t" \
  "v_cvt_pk_bf16_f32 v141, v42, v43\n\t" \
  "v_mfma_f32_16x16x32_bf16 a[96:99], a[128:131], v[152:155], a[96:99]\n\t" \
  "v_exp_f32 v67, v67\n\t" \
  "v_mfma_f32_16x16x32_bf16 a[96:99], a[132:135], v[156:159], a[96:99]\n\t" \
  "v_exp_f32 v68, v68\n\t" \
  "v_mfma_f32_16x16x32_bf16 a[100:103], a[136:139], v[152:155], a[100:103]\n\t" \
  "ds_read_b128 a[248:251], v222 offset:28672\n\t" \
  "v_cvt_pk_bf16_f32 v142, v44, v45\n\t" \
  "v_mfma_f32_16x16x32_bf16 a[100:103], a[140:143], v[156:159], a[100:103]\n\t" \
  "v_exp_f32 v69, v69\n\t" \
  "v_mfma_f32_16x16x32_bf16 a[104:107], a[144:147], v[152:155], a[104:107]\n\t" \
  "v_exp_f32 v70, v70\n\t" \
  "v_mfma_f32_16x16x32_bf16 a[104:107], a[148:151], v[156:159], a[104:107]\n\t" \
  "ds_read_b128 a[252:255], v223 offset:28672\n\t" \
  "v_cvt_pk_bf16_f32 v143, v46, v47\n\t" \
  "v_mfma_f32_16x16x32_bf16 a[108:111], a[152:155], v[152:155], a[108:111]\n\t" \
  "v_exp_f32 v71, v71\n\t" \
  "v_mfma_f32_16x16x32_bf16 a[108:111], a[156:159], v[156:159], a[108:111]\n\t" \
  "v_exp_f32 v72, v72\n\t" \
  "v_mfma_f32_16x16x32_bf16 a[112:115], a[160:163], v[152:155], a[112:115]\n\t" \
  "v_exp_f32 v73, v73\n\t" \
  "v_mfma_f32_16x16x32_bf16 a[112:115], a[164:167], v[156:159], a[112:115]\n\t" \
  "v_exp_f32 v74, v74\n\t" \
  "v_mfma_f32_16x16x32_bf16 a[116:119], a[168:171], v[152:155], a[116:119]\n\t" \
  "v_exp_f32 v75, v75\n\t" \
  "v_mfma_f32_16x16x32_bf16 a[116:119], a[172:175], v[156:159], a[116:119]\n\t" \
  "v_exp_f32 v76, v76\n\t" \
  "v_mfma_f32_16x16x32_bf16 a[120:123], a[176:179], v[152:155], a[120:123]\n\t" \
  "v_exp_f32 v77, v77\n\t" \
  "v_mfma_f32_16x16x32_bf16 a[120:123], a[180:183], v[156:159], a[120:123]\n\t" \
  "v_exp_f32 v78, v78\n\t" \
  "v_mfma_f32_16x16x32_bf16 a[124:127], a[184:187], v[152:155], a[124:127]\n\t" \
  "v_exp_f32 v79, v79\n\t" \
  "v_mfma_f32_16x16x32_bf16 a[124:127], a[188:191], v[156:159], a[124:127]\n\t" \
  "v_exp_f32 v96, v96\n\t" \
  "v_mfma_f32_16x16x32_bf16 v[172:175], v[216:219], v[152:155], v[172:175]\n\t" \
  "v_cvt_pk_bf16_f32 v144, v64, v65\n\t" \
  "v_cvt_pk_bf16_f32 v145, v66, v67\n\t" \
  "v_mfma_f32_16x16x32_bf16 v[172:175], v[216:219], v[156:159], v[172:175]\n\t" \
  "s_waitcnt lgkmcnt(12)\n\t" \
  "s_cmp_lg_u32 s99, 0\n\t" \
  "s_cbranch_scc1 .Lsf_p6\n\t" \
  ".Lsf_q6:\n\t" \
  "v_mfma_f32_16x16x32_bf16 v[0:3], a[192:195], v[16:19], v[176:179]\n\t" \
  "ds_read_b64_tr_b16 a[128:129], v228 offset:0\n\t" \
  "v_cvt_pk_bf16_f32 v146, v68, v69\n\t" \
  "v_mfma_f32_16x16x32_bf16 v[0:3], a[196:199], v[20:23], v[0:3]\n\t" \
  "v_exp_f32 v97, v97\n\t" \
  "v_mfma_f32_16x16x32_bf16 v[0:3], a[200:203], v[24:27], v[0:3]\n\t" \
  "ds_read_b64_tr_b16 a[130:131], v228 offset:4096\n\t" \
  "v_cvt_pk_bf16_f32 v147, v70, v71\n\t" \
  "v_mfma_f32_16x16x32_bf16 v[0:3], a[204:207], v[28:31], v[0:3]\n\t" \
  "s_waitcnt lgkmcnt(10)\n\t" \
  "v_exp_f32 v98, v98\n\t" \
  "v_mfma_f32_16x16x32_bf16 v[4:7], a[220:223], v[28:31], v[176:179]\n\t" \
  "ds_read_b64_tr_b16 a[132:133], v228 offset:8192\n\t" \
  "v_cvt_pk_bf16_f32 v148, v72, v73\n\t" \
  "v_mfma_f32_16x16x32_bf16 v[4:7], a[216:219], v[24:27], v[4:7]\n\t" \
  "v_exp_f32 v99, v99\n\t" \
  "v_mfma_f32_16x16x32_bf16 v[4:7], a[212:215], v[20:23], v[4:7]\n\t" \
  "ds_read_b64_tr_b16 a[134:135], v228 offset:12288\n\t" \
  "v_max3_f32 v192, v0, v1, v2\n\t" \
  "v_mfma_f32_16x16x32_bf16 v[4:7], a[208:211], v[16:19], v[4:7]\n\t" \
  "s_waitcnt lgkmcnt(8)\n\t" \
  "ds_read_b64_tr_b16 a[136:137], v229 offset:0\n\t" \
  "v_cvt_pk_bf16_f32 v149, v74, v75\n\t" \
  "v_mfma_f32_16x16x32_bf16 v[8:11], a[224:227], v[16:19], v[176:179]\n\t" \
  "v_exp_f32 v100, v100\n\t" \
  "v_mfma_f32_16x16x32_bf16 v[8:11], a[228:231], v[20:23], v[8:11]\n\t" \
  "ds_read_b64_tr_b16 a[138:139], v229 offset:4096\n\t" \
  "v_cvt_pk_bf16_f32 v150, v76, v77\n\t" \
  "v_mfma_f32_16x16x32_bf16 v[8:11], a[232:235], v[24:27], v[8:11]\n\t" \
  "v_max3_f32 v203, v3, v4, v5\n\t" \
  "v_max3_f32 v192, v192, v6, v7\n\t" \
  "v_mfma_f32_16x16x32_bf16 v[8:11], a[236:239], v[28:31], v[8:11]\n\t" \
  "s_waitcnt lgkmcnt(6)\n\t" \
  "ds_read_b64_tr_b16 a[140:141], v229 offset:8192\n\t" \
  "v_cvt_pk_bf16_f32 v151, v78, v79\n\t" \
  "v_mfma_f32_16x16x32_bf16 v[12:15], a[252:255], v[28:31], v[176:179]\n\t" \
  "v_exp_f32 v101, v101\n\t" \
  "v_mfma_f32_16x16x32_bf16 v[12:15], a[248:251], v[24:27], v[12:15]\n\t" \
  "ds_read_b64_tr_b16 a[142:143], v229 offset:12288\n\t" \
  "v_cvt_pk_bf16_f32 v152, v96, v97\n\t" \
  "v_mfma_f32_16x16x32_bf16 v[12:15], a[244:247], v[20:23], v[12:15]\n\t" \
  "v_max3_f32 v203, v203, v8, v9\n\t" \
  "v_max3_f32 v192, v192, v10, v11\n\t" \
  "v_mfma_f32_16x16x32_bf16 v[12:15], a[240:243], v[16:19], v[12:15]\n\t" \
  "ds_read_b64_tr_b16 a[144:145], v228 offset:256\n\t" \
  "v_cvt_pk_bf16_f32 v153, v98, v99\n\t" \
  "v_mfma_f32_16x16x32_bf16 v[32:35], a[192:195], v[48:51], v[180:183]\n\t" \
  "ds_read_b64_tr_b16 a[146:147], v228 offset:4352\n\t" \
  "v_cvt_pk_bf16_f32 v154, v100, v101\n\t" \
  "v_mfma_f32_16x16x32_bf16 v[32:35], a[196:199], v[52:55], v[32:35]\n\t" \
  "v_exp_f32 v102, v102\n\t" \
  "v_mfma_f32_16x16x32_bf16 v[32:35], a[200:203], v[56:59], v[32:35]\n\t" \
  "v_max3_f32 v203, v203, v12, v13\n\t" \
  "v_max3_f32 v192, v192, v14, v15\n\t" \
  "v_mfma_f32_16x16x32_bf16 v[32:35], a[204:207], v[60:63], v[32:35]\n\t" \
  "v_max_f32 v192, v192, v203\n\t" \
  "v_cmp_lt_f32 vcc, 0x42800000, v192\n\t" \
  "v_mfma_f32_16x16x32_bf16 v[36:39], a[220:223], v[60:63], v[180:183]\n\t" \
  "v_exp_f32 v103, v103\n\t" \
  "v_mfma_f32_16x16x32_bf16 v[36:39], a[216:219], v[56:59], v[36:39]\n\t" \
  "s_cbranch_vccnz .Lsf_c70\n\t" \
  ".Lsf_r70:\n\t" \
  "v_mfma_f32_16x16x32_bf16 v[36:39], a[212:215], v[52:55], v[36:39]\n\t" \
  "ds_read_b64_tr_b16 a[148:149], v228 offset:8448\n\t" \
  "v_max3_f32 v192, v32, v33, v34\n\t" \
  "v_mfma_f32_16x16x32_bf16 v[36:39], a[208:211], v[48:51], v[36:39]\n\t" \
  "ds_read_b64_tr_b16 a[150:151], v228 offset:12544\n\t" \
  "v_cvt_pk_bf16_f32 v155, v102, v103\n\t" \
  "v_mfma_f32_16x16x32_bf16 v[40:43], a[224:227], v[48:51], v[180:183]\n\t" \
  "v_exp_f32 v104, v104\n\t" \
  "v_mfma_f32_16x16x32_bf16 v[40:43], a[228:231], v[52:55], v[40:43]\n\t" \
  "v_exp_f32 v105, v105\n\t" \
  "v_mfma_f32_16x16x32_bf16 v[40:43], a[232:235], v[56:59], v[40:43]\n\t" \
  "ds_read_b64_tr_b16 a[152:153], v229 offset:256\n\t" \
  "v_max3_f32 v203, v35, v36, v37\n\t" \
  "v_mfma_f32_16x16x32_bf16 v[40:43], a[236:239], v[60:63], v[40:43]\n\t" \
  "ds_read_b64_tr_b16 a[154:155], v229 offset:4352\n\t" \
  "v_max3_f32 v192, v192, v38, v39\n\t" \
  "v_mfma_f32_16x16x32_bf16 v[44:47], a[252:255], v[60:63], v[180:183]\n\t" \
  "ds_read_b64_tr_b16 a[156:157], v229 offset:8448\n\t" \
  "v_cvt_pk_bf16_f32 v156, v104, v105\n\t" \
  "v_mfma_f32_16x16x32_bf16 v[44:47], a[248:251], v[56:59], v[44:47]\n\t" \
  "v_exp_f32 v106, v106\n\t" \
  "v_mfma_f32_16x16x32_bf16 v[44:47], a[244:247], v[52:55], v[44:47]\n\t" \
  "v_max3_f32 v203, v203, v40, v41\n\t" \
  "v_max3_f32 v192, v192, v42, v43\n\t" \
  "v_mfma_f32_16x16x32_bf16 v[44:47], a[240:243], v[48:51], v[44:47]\n\t" \
  "v_exp_f32 v107, v107\n\t" \
  "v_mfma_f32_16x16x32_bf16 v[64:67], a[192:195], v[80:83], v[184:187]\n\t" \
  "ds_read_b64_tr_b16 a[158:159], v229 offset:12544\n\t" \
  "v_cvt_pk_bf16_f32 v157, v106, v107\n\t" \
  "v_mfma_f32_16x16x32_bf16 v[64:67], a[196:199], v[84:87], v[64:67]\n\t" \
  "v_exp_f32 v108, v108\n\t" \
  "v_mfma_f32_16x16x32_bf16 v[64:67], a[200:203], v[88:91], v[64:67]\n\t" \
  "ds_read_b64_tr_b16 a[160:161], v228 offset:512\n\t" \
  "v_max3_f32 v203, v203, v44, v45\n\t" \
  "v_mfma_f32_16x16x32_bf16 v[64:67], a[204:207], v[92:95], v[64:67]\n\t" \
  "v_max3_f32 v192, v192, v46, v47\n\t" \
  "v_max_f32 v192, v192, v203\n\t" \
  "v_mfma_f32_16x16x32_bf16 v[68:71], a[220:223], v[92:95], v[184:187]\n\t" \
  "ds_read_b64_tr_b16 a[162:163], v228 offset:4608\n\t" \
  "v_cmp_lt_f32 vcc, 0x42800000, v192\n\t" \
  "v_mfma_f32_16x16x32_bf16 v[68:71], a[216:219], v[88:91], v[68:71]\n\t" \
  "v_exp_f32 v109, v109\n\t" \
  "v_mfma_f32_16x16x32_bf16 v[68:71], a[212:215], v[84:87], v[68:71]\n\t" \
  "s_cbranch_vccnz .Lsf_c71\n\t" \
  ".Lsf_r71:\n\t" \
  "v_mfma_f32_16x16x32_bf16 v[68:71], a[208:211], v[80:83], v[68:71]\n\t" \
  "ds_read_b64_tr_b16 a[164:165], v228 offset:8704\n\t" \
  "v_max3_f32 v192, v64, v65, v66\n\t" \
  "v_mfma_f32_16x16x32_bf16 v[72:75], a[224:227], v[80:83], v[184:187]\n\t" \
  "ds_read_b64_tr_b16 a[166:167], v228 offset:12800\n\t" \
  "v_cvt_pk_bf16_f32 v158, v108, v109\n\t" \
  "v_mfma_f32_16x16x32_bf16 v[72:75], a[228:231], v[84:87], v[72:75]\n\t" \
  "v_exp_f32 v110, v110\n\t" \
  "v_mfma_f32_16x16x32_bf16 v[72:75], a[232:235], v[88:91], v[72:75]\n\t" \
  "v_max3_f32 v203, v67, v68, v69\n\t" \
  "v_max3_f32 v192, v192, v70, v71\n\t" \
  "v_mfma_f32_16x16x32_bf16 v[72:75], a[236:239], v[92:95], v[72:75]\n\t" \
  "ds_read_b64_tr_b16 a[168:169], v229 offset:512\n\t" \
  "v_mfma_f32_16x16x32_bf16 v[76:79], a[252:255], v[92:95], v[184:187]\n\t" \
  "v_exp_f32 v111, v111\n\t" \
  "v_mfma_f32_16x16x32_bf16 v[76:79], a[248:251], v[88:91], v[76:79]\n\t" \
  "ds_read_b64_tr_b16 a[170:171], v229 offset:4608\n\t" \
  "v_cvt_pk_bf16_f32 v159, v110, v111\n\t" \
  "v_mfma_f32_16x16x32_bf16 v[76:79], a[244:247], v[84:87], v[76:79]\n\t" \
  "ds_read_b64_tr_b16 a[172:173], v229 offset:8704\n\t" \
  "v_max3_f32 v203, v203, v72, v73\n\t" \
  "v_mfma_f32_16x16x32_bf16 v[76:79], a[240:243], v[80:83], v[76:79]\n\t" \
  "ds_read_b64_tr_b16 a[174:175], v229 offset:12800\n\t" \
  "v_max3_f32 v192, v192, v74, v75\n\t" \
  "v_mfma_f32_16x16x32_bf16 v[96:99], a[192:195], v[112:115], v[188:191]\n\t" \
  "ds_read_b64_tr_b16 a[176:177], v228 offset:768\n\t" \
  "v_mfma_f32_16x16x32_bf16 v[96:99], a[196:199], v[116:119], v[96:99]\n\t" \
  "ds_read_b64_tr_b16 a[178:179], v228 offset:4864\n\t" \
  "v_mfma_f32_16x16x32_bf16 v[96:99], a[200:203], v[120:123], v[96:99]\n\t" \
  "ds_read_b64_tr_b16 a[180:181], v228 offset:8960\n\t" \
  "v_max3_f32 v203, v203, v76, v77\n\t" \
  "v_mfma_f32_16x16x32_bf16 v[96:99], a[204:207], v[124:127], v[96:99]\n\t" \
  "v_max3_f32 v192, v192, v78, v79\n\t" \
  "v_max_f32 v192, v192, v203\n\t" \
  "v_mfma_f32_16x16x32_bf16 v[100:103], a[220:223], v[124:127], v[188:191]\n\t" \
  "ds_read_b64_tr_b16 a[182:183], v228 offset:13056\n\t" \
  "v_cmp_lt_f32 vcc, 0x42800000, v192\n\t" \
  "v_mfma_f32_16x16x32_bf16 v[100:103], a[216:219], v[120:123], v[100:103]\n\t" \
  "ds_read_b64_tr_b16 a[184:185], v229 offset:768\n\t" \
  "v_mfma_f32_16x16x32_bf16 v[100:103], a[212:215], v[116:119], v[100:103]\n\t" \
  "s_cbranch_vccnz .Lsf_c72\n\t" \
  ".Lsf_r72:\n\t" \
  "v_mfma_f32_16x16x32_bf16 v[100:103], a[208:211], v[112:115], v[100:103]\n\t" \
  "ds_read_b64_tr_b16 a[186:187], v229 offset:4864\n\t" \
  "v_max3_f32 v192, v96, v97, v98\n\t" \
  "v_mfma_f32_16x16x32_bf16 v[104:107], a[224:227], v[112:115], v[188:191]\n\t" \
  "ds_read_b64_tr_b16 a[188:189], v229 offset:8960\n\t" \
  "v_mfma_f32_16x16x32_bf16 v[104:107], a[228:231], v[116:119], v[104:107]\n\t" \
  "ds_read_b64_tr_b16 a[190:191], v229 offset:13056\n\t" \
  "v_mfma_f32_16x16x32_bf16 v[104:107], a[232:235], v[120:123], v[104:107]\n\t" \
  "v_exp_f32 v0, v0\n\t" \
  "v_mfma_f32_16x16x32_bf16 v[104:107], a[236:239], v[124:127], v[104:107]\n\t" \
  "v_max3_f32 v203, v99, v100, v101\n\t" \
  "v_max3_f32 v192, v192, v102, v103\n\t" \
  "v_mfma_f32_16x16x32_bf16 v[108:111], a[252:255], v[124:127], v[188:191]\n\t" \
  "v_exp_f32 v1, v1\n\t" \
  "v_mfma_f32_16x16x32_bf16 v[108:111], a[248:251], v[120:123], v[108:111]\n\t" \
  "v_exp_f32 v2, v2\n\t" \
  "v_mfma_f32_16x16x32_bf16 v[108:111], a[244:247], v[116:119], v[108:111]\n\t" \
  "s_waitcnt vmcnt(0) lgkmcnt(0)\n\t" \
  "v_max3_f32 v203, v203, v104, v105\n\t" \
  "v_max3_f32 v192, v192, v106, v107\n\t" \
  "v_mfma_f32_16x16x32_bf16 v[108:111], a[240:243], v[112:115], v[108:111]\n\t" \
  "s_barrier\n\t" \
  "v_mfma_f32_16x16x32_bf16 a[0:3], a[128:131], v[128:131], a[0:3]\n\t" \
  "ds_read_b64_tr_b16 v[16:17], v210 offset:0\n\t" \
  "v_cvt_pk_bf16_f32 v0, v0, v1\n\t" \
  "v_mfma_f32_16x16x32_bf16 a[0:3], a[132:135], v[132:135], a[0:3]\n\t" \
  "v_exp_f32 v3, v3\n\t" \
  "v_mfma_f32_16x16x32_bf16 a[4:7], a[136:139], v[128:131], a[4:7]\n\t" \
  "ds_read_b64_tr_b16 v[18:19], v210 offset:4096\n\t" \
  "v_max3_f32 v203, v203, v108, v109\n\t" \
  "v_mfma_f32_16x16x32_bf16 a[4:7], a[140:143], v[132:135], a[4:7]\n\t" \
  "v_max3_f32 v192, v192, v110, v111\n\t" \
  "v_max_f32 v192, v192, v203\n\t" \
  "v_mfma_f32_16x16x32_bf16 a[8:11], a[144:147], v[128:131], a[8:11]\n\t" \
  "ds_read_b64_tr_b16 v[20:21], v210 offset:8192\n\t" \
  "v_cmp_lt_f32 vcc, 0x42800000, v192\n\t" \
  "v_mfma_f32_16x16x32_bf16 a[8:11], a[148:151], v[132:135], a[8:11]\n\t" \
  "ds_read_b64_tr_b16 v[22:23], v210 offset:12288\n\t" \
  "v_cvt_pk_bf16_f32 v1, v2, v3\n\t" \
  "v_mfma_f32_16x16x32_bf16 a[12:15], a[152:155], v[128:131], a[12:15]\n\t" \
  "s_cbranch_vccnz .Lsf_c73\n\t" \
  ".Lsf_r73:\n\t" \
  "v_mfma_f32_16x16x32_bf16 a[12:15], a[156:159], v[132:135], a[12:15]\n\t" \
  "v_exp_f32 v4, v4\n\t" \
  "v_mfma_f32_16x16x32_bf16 a[16:19], a[160:163], v[128:131], a[16:19]\n\t" \
  "v_exp_f32 v5, v5\n\t" \
  "v_mfma_f32_16x16x32_bf16 a[16:19], a[164:167], v[132:135], a[16:19]\n\t" \
  "ds_read_b64_tr_b16 v[24:25], v211 offset:0\n\t" \
  "v_cvt_pk_bf16_f32 v2, v4, v5\n\t" \
  "v_mfma_f32_16x16x32_bf16 a[20:23], a[168:171], v[128:131], a[20:23]\n\t" \
  "v_exp_f32 v6, v6\n\t" \
  "v_mfma_f32_16x16x32_bf16 a[20:23], a[172:175], v[132:135], a[20:23]\n\t" \
  "v_exp_f32 v7, v7\n\t" \
  "v_mfma_f32_16x16x32_bf16 a[24:27], a[176:179], v[128:131], a[24:27]\n\t" \
  "ds_read_b64_tr_b16 v[26:27], v211 offset:4096\n\t" \
  "v_cvt_pk_bf16_f32 v3, v6, v7\n\t" \
  "v_mfma_f32_16x16x32_bf16 a[24:27], a[180:183], v[132:135], a[24:27]\n\t" \
  "v_exp_f32 v8, v8\n\t" \
  "v_mfma_f32_16x16x32_bf16 a[28:31], a[184:187], v[128:131], a[28:31]\n\t" \
  "ds_read_b64_tr_b16 v[28:29], v211 offset:8192\n\t" \
  "v_mfma_f32_16x16x32_bf16 a[28:31], a[188:191], v[132:135], a[28:31]\n\t" \
  "ds_read_b64_tr_b16 v[30:31], v211 offset:12288\n\t" \
  "v_mfma_f32_16x16x32_bf16 v[160:163], v[216:219], v[128:131], v[160:163]\n\t" \
  "ds_read_b64_tr_b16 v[48:49], v210 offset:256\n\t" \
  "v_mfma_f32_16x16x32_bf16 v[160:163], v[216:219], v[132:135], v[160:163]\n\t" \
  "ds_read_b64_tr_b16 v[50:51], v210 offset:4352\n\t" \
  "v_mfma_f32_16x16x32_bf16 a[32:35], a[128:131], v[136:139], a[32:35]\n\t" \
  "v_exp_f32 v9, v9\n\t" \
  "v_mfma_f32_16x16x32_bf16 a[32:35], a[132:135], v[140:143], a[32:35]\n\t" \
  "ds_read_b64_tr_b16 v[52:53], v210 offset:8448\n\t" \
  "v_cvt_pk_bf16_f32 v8, v8, v9\n\t" \
  "v_mfma_f32_16x16x32_bf16 a[36:39], a[136:139], v[136:139], a[36:39]\n\t" \
  "ds_read_b64_tr_b16 v[54:55], v210 offset:12544\n\t" \
  "v_mfma_f32_16x16x32_bf16 a[36:39], a[140:143], v[140:143], a[36:39]\n\t" \
  "ds_read_b64_tr_b16 v[56:57], v211 offset:256\n\t" \
  "v_mfma_f32_16x16x32_bf16 a[40:43], a[144:147], v[136:139], a[40:43]\n\t" \
  "v_exp_f32 v10, v10\n\t" \
  "v_mfma_f32_16x16x32_bf16 a[40:43], a[148:151], v[140:143], a[40:43]\n\t" \
  "ds_read_b64_tr_b16 v[58:59], v211 offset:4352\n\t" \
  "v_mfma_f32_16x16x32_bf16 a[44:47], a[152:155], v[136:139], a[44:47]\n\t" \
  "ds_read_b64_tr_b16 v[60:61], v211 offset:8448\n\t" \
  "v_mfma_f32_16x16x32_bf16 a[44:47], a[156:159], v[140:143], a[44:47]\n\t" \
  "ds_read_b64_tr_b16 v[62:63], v211 offset:12544\n\t" \
  "v_mfma_f32_16x16x32_bf16 a[48:51], a[160:163], v[136:139], a[48:51]\n\t" \
  "v_exp_f32 v11, v11\n\t" \
  "v_mfma_f32_16x16x32_bf16 a[48:51], a[164:167], v[140:143], a[48:51]\n\t" \
  "ds_read_b64_tr_b16 v[80:81], v210 offset:512\n\t" \
  "v_cvt_pk_bf16_f32 v9, v10, v11\n\t" \
  "v_mfma_f32_16x16x32_bf16 a[52:55], a[168:171], v[136:139], a[52:55]\n\t" \
  "ds_read_b64_tr_b16 v[82:83], v210 offset:4608\n\t" \
  "v_mfma_f32_16x16x32_bf16 a[52:55], a[172:175], v[140:143], a[52:55]\n\t" \
  "v_exp_f32 v12, v12\n\t" \
  "v_mfma_f32_16x16x32_bf16 a[56:59], a[176:179], v[136:139], a[56:59]\n\t" \
  "v_exp_f32 v13, v13\n\t" \
  "v_mfma_f32_16x16x32_bf16 a[56:59], a[180:183], v[140:143], a[56:59]\n\t" \
  "ds_read_b64_tr_b16 v[84:85], v210 offset:8704\n\t" \
  "v_cvt_pk_bf16_f32 v10, v12, v13\n\t" \
  "v_mfma_f32_16x16x32_bf16 a[60:63], a[184:187], v[136:139], a[60:63]\n\t" \
  "ds_read_b64_tr_b16 v[86:87], v210 offset:12800\n\t" \
  "v_mfma_f32_16x16x32_bf16 a[60:63], a[188:191], v[140:143], a[60:63]\n\t" \
  "v_exp_f32 v14, v14\n\t" \
  "v_mfma_f32_16x16x32_bf16 v[164:167], v[216:219], v[136:139], v[164:167]\n\t" \
  "ds_read_b64_tr_b16 v[88:89], v211 offset:512\n\t" \
  "v_mfma_f32_16x16x32_bf16 v[164:167], v[216:219], v[140:143], v[164:167]\n\t" \
  "v_exp_f32 v15, v15\n\t" \
  "v_mfma_f32_16x16x32_bf16 a[64:67], a[128:131], v[144:147], a[64:67]\n\t" \
  "ds_read_b64_tr_b16 v[90:91], v211 offset:4608\n\t" \
  "v_cvt_pk_bf16_f32 v11, v14, v15\n\t" \
  "v_mfma_f32_16x16x32_bf16 a[64:67], a[132:135], v[148:151], a[64:67]\n\t" \
  "v_exp_f32 v32, v32\n\t" \
  "v_mfma_f32_16x16x32_bf16 a[68:71], a[136:139], v[144:147], a[68:71]\n\t" \
  "v_exp_f32 v33, v33\n\t" \
  "v_mfma_f32_16x16x32_bf16 a[68:71], a[140:143], v[148:151], a[68:71]\n\t" \
  "ds_read_b64_tr_b16 v[92:93], v211 offset:8704\n\t" \
  "v_cvt_pk_bf16_f32 v32, v32, v33\n\t" \
  "v_mfma_f32_16x16x32_bf16 a[72:75], a[144:147], v[144:147], a[72:75]\n\t" \
  "ds_read_b64_tr_b16 v[94:95], v211 offset:12800\n\t" \
  "v_mfma_f32_16x16x32_bf16 a[72:75], a[148:151], v[148:151], a[72:75]\n\t" \
  "v_exp_f32 v34, v34\n\t" \
  "v_mfma_f32_16x16x32_bf16 a[76:79], a[152:155], v[144:147], a[76:79]\n\t" \
  "v_exp_f32 v35, v35\n\t" \
  "v_mfma_f32_16x16x32_bf16 a[76:79], a[156:159], v[148:151], a[76:79]\n\t" \
  "ds_read_b64_tr_b16 v[112:113], v210 offset:768\n\t" \
  "v_cvt_pk_bf16_f32 v33, v34, v35\n\t" \
  "v_mfma_f32_16x16x32_bf16 a[80:83], a[160:163], v[144:147], a[80:83]\n\t" \
  "v_exp_f32 v36, v36\n\t" \
  "v_mfma_f32_16x16x32_bf16 a[80:83], a[164:167], v[148:151], a[80:83]\n\t" \
  "ds_read_b64_tr_b16 v[114:115], v210 offset:4864\n\t" \
  "v_mfma_f32_16x16x32_bf16 a[84:87], a[168:171], v[144:147], a[84:87]\n\t" \
  "v_exp_f32 v37, v37\n\t" \
  "v_mfma_f32_16x16x32_bf16 a[84:87], a[172:175], v[148:151], a[84:87]\n\t" \
  "ds_read_b64_tr_b16 v[116:117], v210 offset:8960\n\t" \
  "v_cvt_pk_bf16_f32 v34, v36, v37\n\t" \
  "v_mfma_f32_16x16x32_bf16 a[88:91], a[176:179], v[144:147], a[88:91]\n\t" \
  "v_exp_f32 v38, v38\n\t" \
  "v_mfma_f32_16x16x32_bf16 a[88:91], a[180:183], v[148:151], a[88:91]\n\t" \
  "ds_read_b64_tr_b16 v[118:119], v210 offset:13056\n\t" \
  "v_mfma_f32_16x16x32_bf16 a[92:95], a[184:187], v[144:147], a[92:95]\n\t" \
  "v_exp_f32 v39, v39\n\t" \
  "v_mfma_f32_16x16x32_bf16 a[92:95], a[188:191], v[148:151], a[92:95]\n\t" \
  "ds_read_b64_tr_b16 v[120:121], v211 offset:768\n\t" \
  "v_cvt_pk_bf16_f32 v35, v38, v39\n\t" \
  "v_mfma_f32_16x16x32_bf16 v[168:171], v[216:219], v[144:147], v[168:171]\n\t" \
  "v_exp_f32 v40, v40\n\t" \
  "v_mfma_f32_16x16x32_bf16 v[168:171], v[216:219], v[148:151], v[168:171]\n\t" \
  "ds_read_b64_tr_b16 v[122:123], v211 offset:4864\n\t" \
  "v_mfma_f32_16x16x32_bf16 a[96:99], a[128:131], v[152:155], a[96:99]\n\t" \
  "v_exp_f32 v41, v41\n\t" \
  "v_mfma_f32_16x16x32_bf16 a[96:99], a[132:135], v[156:159], a[96:99]\n\t" \
  "ds_read_b64_tr_b16 v[124:125], v211 offset:8960\n\t" \
  "v_cvt_pk_bf16_f32 v40, v40, v41\n\t" \
  "v_mfma_f32_16x16x32_bf16 a[100:103], a[136:139], v[152:155], a[100:103]\n\t" \
  "v_exp_f32 v42, v42\n\t" \
  "v_mfma_f32_16x16x32_bf16 a[100:103], a[140:143], v[156:159], a[100:103]\n\t" \
  "ds_read_b64_tr_b16 v[126:127], v211 offset:13056\n\t" \
  "v_mfma_f32_16x16x32_bf16 a[104:107], a[144:147], v[152:155], a[104:107]\n\t" \
  "v_exp_f32 v43, v43\n\t" \
  "v_mfma_f32_16x16x32_bf16 a[104:107], a[148:151], v[156:159], a[104:107]\n\t" \
  "v_exp_f32 v44, v44\n\t" \
  "v_mfma_f32_16x16x32_bf16 a[108:111], a[152:155], v[152:155], a[108:111]\n\t" \
  "v_exp_f32 v45, v45\n\t" \
  "v_mfma_f32_16x16x32_bf16 a[108:111], a[156:159], v[156:159], a[108:111]\n\t" \
  "v_exp_f32 v46, v46\n\t" \
  "v_mfma_f32_16x16x32_bf16 a[112:115], a[160:163], v[152:155], a[112:115]\n\t" \
  "v_exp_f32 v47, v47\n\t" \
  "v_mfma_f32_16x16x32_bf16 a[112:115], a[164:167], v[156:159], a[112:115]\n\t" \
  "v_exp_f32 v64, v64\n\t" \
  "v_mfma_f32_16x16x32_bf16 a[116:119], a[168:171], v[152:155], a[116:119]\n\t" \
  "v_cvt_pk_bf16_f32 v41, v42, v43\n\t" \
  "v_cvt_pk_bf16_f32 v42, v44, v45\n\t" \
  "v_mfma_f32_16x16x32_bf16 a[116:119], a[172:175], v[156:159], a[116:119]\n\t" \
  "v_exp_f32 v65, v65\n\t" \
  "v_mfma_f32_16x16x32_bf16 a[120:123], a[176:179], v[152:155], a[120:123]\n\t" \
  "v_exp_f32 v66, v66\n\t" \
  "v_mfma_f32_16x16x32_bf16 a[120:123], a[180:183], v[156:159], a[120:123]\n\t" \
  "v_exp_f32 v67, v67\n\t" \
  "v_mfma_f32_16x16x32_bf16 a[124:127], a[184:187], v[152:155], a[124:127]\n\t" \
  "v_exp_f32 v68, v68\n\t" \
  "v_mfma_f32_16x16x32_bf16 a[124:127], a[188:191], v[156:159], a[124:127]\n\t" \
  "v_exp_f32 v69, v69\n\t" \
  "v_mfma_f32_16x16x32_bf16 v[172:175], v[216:219], v[152:155], v[172:175]\n\t" \
  "v_cvt_pk_bf16_f32 v43, v46, v47\n\t" \
  "v_cvt_pk_bf16_f32 v64, v64, v65\n\t" \
  "v_mfma_f32_16x16x32_bf16 v[172:175], v[216:219], v[156:159], v[172:175]\n\t" \
  "s_waitcnt lgkmcnt(15)\n\t" \
  "s_cmp_lg_u32 s99, 0\n\t" \
  "s_cbranch_scc1 .Lsf_p7\n\t" \
  ".Lsf_q7:\n\t" \
  "v_mfma_f32_16x16x32_bf16 a[0:3], v[16:19], v[0:3], a[0:3]\n\t" \
  "v_exp_f32 v70, v70\n\t" \
  "v_mfma_f32_16x16x32_bf16 a[0:3], v[20:23], v[8:11], a[0:3]\n\t" \
  "v_exp_f32 v71, v71\n\t" \
  "v_mfma_f32_16x16x32_bf16 v[160:163], v[216:219], v[0:3], v[160:163]\n\t" \
  "v_exp_f32 v72, v72\n\t" \
  "v_mfma_f32_16x16x32_bf16 v[160:163], v[216:219], v[8:11], v[160:163]\n\t" \
  "v_exp_f32 v73, v73\n\t" \
  "v_mfma_f32_16x16x32_bf16 a[4:7], v[24:27], v[0:3], a[4:7]\n\t" \
  "v_exp_f32 v74, v74\n\t" \
  "v_mfma_f32_16x16x32_bf16 a[4:7], v[28:31], v[8:11], a[4:7]\n\t" \
  "v_cvt_pk_bf16_f32 v65, v66, v67\n\t" \
  "v_cvt_pk_bf16_f32 v66, v68, v69\n\t" \
  "v_mfma_f32_16x16x32_bf16 a[8:11], v[48:51], v[0:3], a[8:11]\n\t" \
  "v_exp_f32 v75, v75\n\t" \
  "v_mfma_f32_16x16x32_bf16 a[8:11], v[52:55], v[8:11], a[8:11]\n\t" \
  "v_exp_f32 v76, v76\n\t" \
  "v_mfma_f32_16x16x32_bf16 a[12:15], v[56:59], v[0:3], a[12:15]\n\t" \
  "v_cvt_pk_bf16_f32 v67, v70, v71\n\t" \
  "v_cvt_pk_bf16_f32 v72, v72, v73\n\t" \
  "v_mfma_f32_16x16x32_bf16 a[12:15], v[60:63], v[8:11], a[12:15]\n\t" \
  "s_waitcnt lgkmcnt(8)\n\t" \
  "v_exp_f32 v77, v77\n\t" \
  "v_mfma_f32_16x16x32_bf16 a[16:19], v[80:83], v[0:3], a[16:19]\n\t" \
  "v_exp_f32 v78, v78\n\t" \
  "v_mfma_f32_16x16x32_bf16 a[16:19], v[84:87], v[8:11], a[16:19]\n\t" \
  "v_cvt_pk_bf16_f32 v73, v74, v75\n\t" \
  "v_cvt_pk_bf16_f32 v74, v76, v77\n\t" \
  "v_mfma_f32_16x16x32_bf16 a[20:23], v[88:91], v[0:3], a[20:23]\n\t" \
  "v_exp_f32 v79, v79\n\t" \
  "v_mfma_f32_16x16x32_bf16 a[20:23], v[92:95], v[8:11], a[20:23]\n\t" \
  "s_waitcnt lgkmcnt(0)\n\t" \
  "v_exp_f32 v96, v96\n\t" \
  "v_mfma_f32_16x16x32_bf16 a[24:27], v[112:115], v[0:3], a[24:27]\n\t" \
  "v_exp_f32 v97, v97\n\t" \
  "v_mfma_f32_16x16x32_bf16 a[24:27], v[116:119], v[8:11], a[24:27]\n\t" \
  "v_exp_f32 v98, v98\n\t" \
  "v_mfma_f32_16x16x32_bf16 a[28:31], v[120:123], v[0:3], a[28:31]\n\t" \
  "v_exp_f32 v99, v99\n\t" \
  "v_mfma_f32_16x16x32_bf16 a[28:31], v[124:127], v[8:11], a[28:31]\n\t" \
  "v_exp_f32 v100, v100\n\t" \
  "v_mfma_f32_16x16x32_bf16 a[32:35], v[16:19], v[32:35], a[32:35]\n\t" \
  "v_cvt_pk_bf16_f32 v75, v78, v79\n\t" \
  "v_cvt_pk_bf16_f32 v96, v96, v97\n\t" \
  "v_mfma_f32_16x16x32_bf16 a[32:35], v[20:23], v[40:43], a[32:35]\n\t" \
  "v_exp_f32 v101, v101\n\t" \
  "v_mfma_f32_16x16x32_bf16 v[164:167], v[216:219], v[32:35], v[164:167]\n\t" \
  "v_exp_f32 v102, v102\n\t" \
  "v_mfma_f32_16x16x32_bf16 v[164:167], v[216:219], v[40:43], v[164:167]\n\t" \
  "v_exp_f32 v103, v103\n\t" \
  "v_mfma_f32_16x16x32_bf16 a[36:39], v[24:27], v[32:35], a[36:39]\n\t" \
  "v_exp_f32 v104, v104\n\t" \
  "v_mfma_f32_16x16x32_bf16 a[36:39], v[28:31], v[40:43], a[36:39]\n\t" \
  "v_exp_f32 v105, v105\n\t" \
  "v_mfma_f32_16x16x32_bf16 a[40:43], v[48:51], v[32:35], a[40:43]\n\t" \
  "v_cvt_pk_bf16_f32 v97, v98, v99\n\t" \
  "v_cvt_pk_bf16_f32 v98, v100, v101\n\t" \
  "v_mfma_f32_16x16x32_bf16 a[40:43], v[52:55], v[40:43], a[40:43]\n\t" \
  "v_exp_f32 v106, v106\n\t" \
  "v_mfma_f32_16x16x32_bf16 a[44:47], v[56:59], v[32:35], a[44:47]\n\t" \
  "v_exp_f32 v107, v107\n\t" \
  "v_mfma_f32_16x16x32_bf16 a[44:47], v[60:63], v[40:43], a[44:47]\n\t" \
  "v_cvt_pk_bf16_f32 v99, v102, v103\n\t" \
  "v_cvt_pk_bf16_f32 v104, v104, v105\n\t" \
  "v_mfma_f32_16x16x32_bf16 a[48:51], v[80:83], v[32:35], a[48:51]\n\t" \
  "v_exp_f32 v108, v108\n\t" \
  "v_mfma_f32_16x16x32_bf16 a[48:51], v[84:87], v[40:43], a[48:51]\n\t" \
  "v_exp_f32 v109, v109\n\t" \
  "v_mfma_f32_16x16x32_bf16 a[52:55], v[88:91], v[32:35], a[52:55]\n\t" \
  "v_cvt_pk_bf16_f32 v105, v106, v107\n\t" \
  "v_cvt_pk_bf16_f32 v106, v108, v109\n\t" \
  "v_mfma_f32_16x16x32_bf16 a[52:55], v[92:95], v[40:43], a[52:55]\n\t" \
  "v_exp_f32 v110, v110\n\t" \
  "v_mfma_f32_16x16x32_bf16 a[56:59], v[112:115], v[32:35], a[56:59]\n\t" \
  "v_exp_f32 v111, v111\n\t" \
  "v_mfma_f32_16x16x32_bf16 a[56:59], v[116:119], v[40:43], a[56:59]\n\t" \
  "v_cvt_pk_bf16_f32 v107, v110, v111\n\t" \
  "v_mfma_f32_16x16x32_bf16 a[60:63], v[120:123], v[32:35], a[60:63]\n\t" \
  "v_mfma_f32_16x16x32_bf16 a[60:63], v[124:127], v[40:43], a[60:63]\n\t" \
  "v_mfma_f32_16x16x32_bf16 a[64:67], v[16:19], v[64:67], a[64:67]\n\t" \
  "v_mfma_f32_16x16x32_bf16 a[64:67], v[20:23], v[72:75], a[64:67]\n\t" \
  "v_mfma_f32_16x16x32_bf16 v[168:171], v[216:219], v[64:67], v[168:171]\n\t" \
  "v_mfma_f32_16x16x32_bf16 v[168:171], v[216:219], v[72:75], v[168:171]\n\t" \
  "v_mfma_f32_16x16x32_bf16 a[68:71], v[24:27], v[64:67], a[68:71]\n\t" \
  "v_mfma_f32_16x16x32_bf16 a[68:71], v[28:31], v[72:75], a[68:71]\n\t" \
  "v_mfma_f32_16x16x32_bf16 a[72:75], v[48:51], v[64:67], a[72:75]\n\t" \
  "v_mfma_f32_16x16x32_bf16 a[72:75], v[52:55], v[72:75], a[72:75]\n\t" \
  "v_mfma_f32_16x16x32_bf16 a[76:79], v[56:59], v[64:67], a[76:79]\n\t" \
  "v_mfma_f32_16x16x32_bf16 a[76:79], v[60:63], v[72:75], a[76:79]\n\t" \
  "v_mfma_f32_16x16x32_bf16 a[80:83], v[80:83], v[64:67], a[80:83]\n\t" \
  "v_mfma_f32_16x16x32_bf16 a[80:83], v[84:87], v[72:75], a[80:83]\n\t" \
  "v_mfma_f32_16x16x32_bf16 a[84:87], v[88:91], v[64:67], a[84:87]\n\t" \
  "v_mfma_f32_16x16x32_bf16 a[84:87], v[92:95], v[72:75], a[84:87]\n\t" \
  "v_mfma_f32_16x16x32_bf16 a[88:91], v[112:115], v[64:67], a[88:91]\n\t" \
  "v_mfma_f32_16x16x32_bf16 a[88:91], v[116:119], v[72:75], a[88:91]\n\t" \
  "v_mfma_f32_16x16x32_bf16 a[92:95], v[120:123], v[64:67], a[92:95]\n\t" \
  "v_mfma_f32_16x16x32_bf16 a[92:95], v[124:127], v[72:75], a[92:95]\n\t" \
  "v_mfma_f32_16x16x32_bf16 a[96:99], v[16:19], v[96:99], a[96:99]\n\t" \
  "v_mfma_f32_16x16x32_bf16 a[96:99], v[20:23], v[104:107], a[96:99]\n\t" \
  "v_mfma_f32_16x16x32_bf16 v[172:175], v[216:219], v[96:99], v[172:175]\n\t" \
  "v_mfma_f32_16x16x32_bf16 v[172:175], v[216:219], v[104:107], v[172:175]\n\t" \
  "v_mfma_f32_16x16x32_bf16 a[100:103], v[24:27], v[96:99], a[100:103]\n\t" \
  "v_mfma_f32_16x16x32_bf16 a[100:103], v[28:31], v[104:107], a[100:103]\n\t" \
  "v_mfma_f32_16x16x32_bf16 a[104:107], v[48:51], v[96:99], a[104:107]\n\t" \
  "v_mfma_f32_16x16x32_bf16 a[104:107], v[52:55], v[104:107], a[104:107]\n\t" \
  "v_mfma_f32_16x16x32_bf16 a[108:111], v[56:59], v[96:99], a[108:111]\n\t" \
  "v_mfma_f32_16x16x32_bf16 a[108:111], v[60:63], v[104:107], a[108:111]\n\t" \
  "v_mfma_f32_16x16x32_bf16 a[112:115], v[80:83], v[96:99], a[112:115]\n\t" \
  "v_mfma_f32_16x16x32_bf16 a[112:115], v[84:87], v[104:107], a[112:115]\n\t" \
  "v_mfma_f32_16x16x32_bf16 a[116:119], v[88:91], v[96:99], a[116:119]\n\t" \
  "v_mfma_f32_16x16x32_bf16 a[116:119], v[92:95], v[104:107], a[116:119]\n\t" \
  "v_mfma_f32_16x16x32_bf16 a[120:123], v[112:115], v[96:99], a[120:123]\n\t" \
  "v_mfma_f32_16x16x32_bf16 a[120:123], v[116:119], v[104:107], a[120:123]\n\t" \
  "v_mfma_f32_16x16x32_bf16 a[124:127], v[120:123], v[96:99], a[124:127]\n\t" \
  "v_mfma_f32_16x16x32_bf16 a[124:127], v[124:127], v[104:107], a[124:127]\n\t" \
  "s_nop 7\n\t" \
  "s_waitcnt vmcnt(0)\n\t" \
  "s_barrier\n\t" \
  "s_add_u32 s77, %[wob], 131072\n\t" \
  "v_add_u32 v248, 14336, v240\n\t" \
  "v_add_u32 v249, 21504, v241\n\t" \
  "v_add_u32 v241, 7168, v241\n\t" \
  "s_mov_b32 s89, 0\n\t" \
  "v_lshlrev_b32 v212, 7, v233\n\t" \
  "s_waitcnt vmcnt(0) lgkmcnt(0)\n\t" \
  "s_barrier\n\t" \
  "v_cmp_gt_u32 vcc, 16, v233\n\t" \
  "v_bfrev_b32 v214, 1\n\t" \
  "s_waitcnt vmcnt(0) lgkmcnt(0)\n\t" \
  "s_barrier\n\t" \
  "v_cndmask_b32 v212, v214, v212, vcc\n\t" \
  "s_waitcnt vmcnt(0) lgkmcnt(0)\n\t" \
  "s_barrier\n\t" \
  "s_add_u32 m0, %[m], 16384\n\t" \
  "s_mov_b32 s96, 0\n\t" \
  "buffer_load_dwordx4 v232, s[68:71], s96 offen offset:0 lds\n\t" \
  "s_mov_b32 s96, 6144\n\t" \
  "buffer_load_dwordx4 v232, s[68:71], s96 offen offset:1024 lds\n\t" \
  "s_mov_b32 s96, 12288\n\t" \
  "buffer_load_dwordx4 v232, s[68:71], s96 offen offset:2048 lds\n\t" \
  "s_mov_b32 s96, 18432\n\t" \
  "buffer_load_dwordx4 v232, s[68:71], s96 offen offset:3072 lds\n\t" \
  "s_waitcnt vmcnt(0) lgkmcnt(0)\n\t" \
  "s_barrier\n\t" \
  "s_add_u32 m0, %[m], 32768\n\t" \
  "s_mov_b32 s96, 28672\n\t" \
  "buffer_load_dwordx4 v232, s[68:71], s96 offen offset:0 lds\n\t" \
  "s_mov_b32 s96, 34816\n\t" \
  "buffer_load_dwordx4 v232, s[68:71], s96 offen offset:1024 lds\n\t" \
  "s_mov_b32 s96, 40960\n\t" \
  "buffer_load_dwordx4 v232, s[68:71], s96 offen offset:2048 lds\n\t" \
  "s_mov_b32 s96, 47104\n\t" \
  "buffer_load_dwordx4 v232, s[68:71], s96 offen offset:3072 lds\n\t" \
  "s_add_u32 m0, %[m], 114688\n\t" \
  "s_mov_b32 s96, 0\n\t" \
  "buffer_load_dwordx4 v232, s[72:75], s96 offen offset:0 lds\n\t" \
  "s_mov_b32 s96, 6144\n\t" \
  "buffer_load_dwordx4 v232, s[72:75], s96 offen offset:1024 lds\n\t" \
  "s_mov_b32 s96, 12288\n\t" \
  "buffer_load_dwordx4 v232, s[72:75], s96 offen offset:2048 lds\n\t" \
  "s_mov_b32 s96, 18432\n\t" \
  "buffer_load_dwordx4 v232, s[72:75], s96 offen offset:3072 lds\n\t" \
  "s_waitcnt vmcnt(0) lgkmcnt(0)\n\t" \
  "s_barrier\n\t" \
  "s_add_u32 m0, %[m], 131072\n\t" \
  "s_mov_b32 s96, 28672\n\t" \
  "buffer_load_dwordx4 v232, s[72:75], s96 offen offset:0 lds\n\t" \
  "s_mov_b32 s96, 34816\n\t" \
  "buffer_load_dwordx4 v232, s[72:75], s96 offen offset:1024 lds\n\t" \
  "s_mov_b32 s96, 40960\n\t" \
  "buffer_load_dwordx4 v232, s[72:75], s96 offen offset:2048 lds\n\t" \
  "s_mov_b32 s96, 47104\n\t" \
  "buffer_load_dwordx4 v232, s[72:75], s96 offen offset:3072 lds\n\t" \
  "s_lshr_b32 s78, %[m], 6\n\t" \
  "v_xor_b32 v234, s78, v244\n\t" \
  "v_xor_b32 v235, 32, v234\n\t" \
  "v_add_u32 v236, 65536, v234\n\t" \
  "v_add_u32 v237, 65536, v235\n\t" \
  "v_add_u32 v238, 131072, v234\n\t" \
  "v_add_u32 v239, 131072, v235\n\t" \
  "v_lshlrev_b32 v242, 4, v233\n\t" \
  "v_add_u32 v213, %[m], v242\n\t" \
  "v_add_u32 v215, 98304, v213\n\t" \
  "v_mov_b32 v243, %[tag]\n\t" \
  "v_lshlrev_b32 v254, 9, v233\n\t" \
  "v_and_b32 v244, 0x38, v233\n\t" \
  "v_lshl_or_b32 v254, v244, 26, v254\n\t" \
  "s_brev_b32 s78, 1\n\t" \
  "v_mov_b32 v244, s78\n\t" \
  "v_cmp_eq_u32 vcc, %[hl], v233\n\t" \
  "v_add_u32 v245, %[hl], v233\n\t" \
  "v_bfi_b32 v245, 7, v245, v233\n\t" \
  "v_cndmask_b32 v254, v254, v244, vcc\n\t" \
  "v_and_b32 v245, 31, v245\n\t" \
  "s_waitcnt vmcnt(0) lgkmcnt(0)\n\t" \
  "s_barrier\n\t" \
  "v_lshlrev_b32 v245, 2, v245\n\t" \
  "v_writelane_b32 v244, 0, 0\n\t" \
  "s_waitcnt vmcnt(0) lgkmcnt(0)\n\t" \
  "s_barrier\n\t" \
  "ds_read_b128 v[192:195], v215 offset:32768\n\t" \
  "ds_read_b128 v[196:199], v215 offset:33792\n\t" \
  "ds_read_b128 v[200:203], v215 offset:34816\n\t" \
  "ds_read_b128 v[204:207], v215 offset:35840\n\t" \
  "s_add_u32 s96, %[hl18], 524288\n\t" \
  "s_and_b32 s96, s96, 0x1c0000\n\t" \
  "s_add_u32 s96, s96, %[wob]\n\t" \
  "s_add_u32 m0, %[m], 131072\n\t" \
  "s_waitcnt vmcnt(0) lgkmcnt(0)\n\t" \
  "buffer_load_dwordx4 v240, %[wo], s96 offen offset:0 lds\n\t" \
  "buffer_load_dwordx4 v241, %[wo], s96 offen offset:1024 lds\n\t" \
  "buffer_load_dwordx4 v248, %[wo], s96 offen offset:2048 lds\n\t" \
  "buffer_load_dwordx4 v249, %[wo], s96 offen offset:3072 lds\n\t" \
  "v_lshrrev_b32 v250, 1, v233\n\t" \
  "v_lshlrev_b32 v250, 2, v250\n\t" \
  "v_add_u32 v250, 8192, v250\n\t" \
  "buffer_load_dword v209, v250, %[ws], 0 offen sc1\n\t" \
  "s_add_u32 s96, %[hl18], 0\n\t" \
  "s_and_b32 s96, s96, 0x1c0000\n\t" \
  "s_add_u32 s96, s96, %[wob]\n\t" \
  "s_add_u32 m0, %[m], 0\n\t" \
  "s_nop 0\n\t" \
  "buffer_load_dwordx4 v240, %[wo], s96 offen offset:0 lds\n\t" \
  "buffer_load_dwordx4 v241, %[wo], s96 offen offset:1024 lds\n\t" \
  "buffer_load_dwordx4 v248, %[wo], s96 offen offset:2048 lds\n\t" \
  "buffer_load_dwordx4 v249, %[wo], s96 offen offset:3072 lds\n\t" \
  "s_waitcnt vmcnt(0) lgkmcnt(0)\n\t" \
  "s_barrier\n\t" \
  "s_add_u32 s96, %[hl18], 0\n\t" \
  "s_and_b32 s96, s96, 0x1c0000\n\t" \
  "s_add_u32 s96, s96, s77\n\t" \
  "s_add_u32 m0, %[m], 98304\n\t" \
  "s_nop 0\n\t" \
  "buffer_load_dwordx4 v240, %[wo], s96 offen offset:0 lds\n\t" \
  "buffer_load_dwordx4 v241, %[wo], s96 offen offset:1024 lds\n\t" \
  "s_waitcnt vmcnt(0) lgkmcnt(0)\n\t" \
  "v_and_b32 v251, 0xbfffbfff, v209\n\t" \
  "v_cmp_ne_u32_e64 s[84:85], %[tag], v251\n\t" \
  "s_cmp_lg_u64 s[84:85], 0\n\t" \
  "s_cbranch_scc1 .Lsf_v\n\t" \
  ".Lsf_w:\n\t" \
  "v_and_b32 v251, 1, v233\n\t" \
  "v_lshlrev_b32 v251, 4, v251\n\t" \
  "buffer_load_dwordx4 v248, %[wo], s96 offen offset:2048 lds\n\t" \
  "v_lshrrev_b32 v251, v251, v209\n\t" \
  "v_and_b32 v251, 0x4000, v251\n\t" \
  "v_cmp_ne_u32_e64 s[84:85], 0, v251\n\t" \
  "s_bcnt1_i32_b32 s98, s84\n\t" \
  "s_bcnt1_i32_b32 %[na], s85\n\t" \
  "buffer_load_dwordx4 v249, %[wo], s96 offen offset:3072 lds\n\t" \
  "s_cmp_gt_u32 s98, %[na]\n\t" \
  "s_cselect_b32 %[nb], 0x2000, 0\n\t" \
  "s_cselect_b32 s98, s84, s85\n\t" \
  "s_add_u32 s96, %[hl18], 262144\n\t" \
  "s_and_b32 s96, s96, 0x1c0000\n\t" \
  "s_add_u32 s96, s96, %[wob]\n\t" \
  "s_add_u32 m0, %[m], 49152\n\t" \
  "s_cmp_lg_u32 %[nb], 0\n\t" \
  "s_cselect_b32 s84, s85, s84\n\t" \
  "s_lshr_b32 s85, s98, 16\n\t" \
  "s_bcnt1_i32_b32 s85, s85\n\t" \
  "s_lshr_b32 s84, s84, 16\n\t" \
  "buffer_load_dwordx4 v240, %[wo], s96 offen offset:0 lds\n\t" \
  "s_bcnt1_i32_b32 s84, s84\n\t" \
  "s_add_u32 s84, s84, 7\n\t" \
  "s_cmp_ge_u32 s85, s84\n\t" \
  "s_cselect_b32 %[na], -1, 0\n\t" \
  "s_xor_b32 s98, %[po], %[nb]\n\t" \
  "s_and_b32 s98, s98, %[na]\n\t" \
  "s_xor_b32 %[eso], %[eso], s98\n\t" \
  "s_xor_b32 %[otb], %[otb], s98\n\t" \
  "buffer_load_dwordx4 v241, %[wo], s96 offen offset:1024 lds\n\t" \
  "s_xor_b32 s98, %[px], %[nb]\n\t" \
  "s_and_b32 s98, s98, %[na]\n\t" \
  "s_xor_b32 %[xcb], %[xcb], s98\n\t" \
  "s_xor_b32 %[frb], %[frb], %[nb]\n\t" \
  "s_xor_b32 %[fw2], %[fw2], %[nb]\n\t" \
  "buffer_load_dwordx4 v248, %[wo], s96 offen offset:2048 lds\n\t" \
  "s_xor_b32 s98, %[eso], 0\n\t" \
  "s_lshl_b32 s64, %[hl], 17\n\t" \
  "s_bfe_u32 s70, %[otb], 0x20014\n\t" \
  "buffer_load_dwordx4 v249, %[wo], s96 offen offset:3072 lds\n\t" \
  "s_lshl_b32 s70, s70, 14\n\t" \
  "s_xor_b32 s67, s70, 49152\n\t" \
  "s_lshl1_add_u32 s60, s67, %[otb]\n\t" \
  "s_xor_b32 s68, s70, 32768\n\t" \
  "s_lshl1_add_u32 s61, s68, %[otb]\n\t" \
  "s_xor_b32 s69, s70, 16384\n\t" \
  "s_lshl1_add_u32 s62, s69, %[otb]\n\t" \
  "s_lshl1_add_u32 s63, s70, %[otb]\n\t" \
  "s_mul_i32 s65, %[hl], 508\n\t" \
  "s_add_u32 s65, s65, %[fw2]\n\t" \
  "s_lshl_b32 s80, %[hl], 2\n\t" \
  "s_add_u32 s80, s80, %[wid]\n\t" \
  "s_waitcnt vmcnt(0) lgkmcnt(0)\n\t" \
  "s_barrier\n\t" \
  "s_lshl_b32 s80, s80, 6\n\t" \
  "s_add_u32 s76, %[m], 147456\n\t" \
  "s_lshl_b32 s75, s80, 2\n\t" \
  "s_add_u32 s75, s75, 2228224\n\t" \
  "s_add_u32 s96, %[hl18], 262144\n\t" \
  "s_and_b32 s96, s96, 0x1c0000\n\t" \
  "s_add_u32 s96, s96, s77\n\t" \
  "s_add_u32 m0, %[m], 65536\n\t" \
  "s_nop 0\n\t" \
  "buffer_load_dwordx4 v240, %[wo], s96 offen offset:0 lds\n\t" \
  "s_lshr_b32 s71, s67, 7\n\t" \
  "s_add_u32 s71, s71, %[frb]\n\t" \
  "s_lshr_b32 s72, s68, 7\n\t" \
  "s_add_u32 s72, s72, %[frb]\n\t" \
  "s_lshr_b32 s66, s69, 7\n\t" \
  "s_add_u32 s66, s66, %[frb]\n\t" \
  "s_lshr_b32 s73, s70, 7\n\t" \
  "s_add_u32 s73, s73, %[frb]\n\t" \
  "s_lshl3_add_u32 s67, s67, %[xcb]\n\t" \
  "buffer_load_dwordx4 v241, %[wo], s96 offen offset:1024 lds\n\t" \
  "s_lshl3_add_u32 s68, s68, %[xcb]\n\t" \
  "s_add_u32 s68, s68, 4096\n\t" \
  "s_lshl3_add_u32 s69, s69, %[xcb]\n\t" \
  "s_add_u32 s69, s69, 16384\n\t" \
  "s_lshl3_add_u32 s70, s70, %[xcb]\n\t" \
  "buffer_load_dwordx4 v248, %[wo], s96 offen offset:2048 lds\n\t" \
  "buffer_load_dwordx4 v249, %[wo], s96 offen offset:3072 lds\n\t" \
  "s_add_u32 s96, %[hl18], 524288\n\t" \
  "s_and_b32 s96, s96, 0x1c0000\n\t" \
  "s_add_u32 s96, s96, s77\n\t" \
  "s_add_u32 m0, %[m], 81920\n\t" \
  "s_waitcnt vmcnt(0) lgkmcnt(0)\n\t" \
  "s_barrier\n\t" \
  "buffer_load_dwordx4 v240, %[wo], s96 offen offset:0 lds\n\t" \
  "ds_read_b128 v[176:179], v215 offset:16384\n\t" \
  "ds_read_b128 v[180:183], v215 offset:17408\n\t" \
  "ds_read_b128 v[184:187], v215 offset:18432\n\t" \
  "buffer_load_dwordx4 v241, %[wo], s96 offen offset:1024 lds\n\t" \
  "ds_read_b128 v[188:191], v215 offset:19456\n\t" \
  "buffer_load_dwordx4 v248, %[wo], s96 offen offset:2048 lds\n\t" \
  "buffer_load_dwordx4 v249, %[wo], s96 offen offset:3072 lds\n\t" \
  "s_add_u32 s96, %[hl18], 786432\n\t" \
  "s_and_b32 s96, s96, 0x1c0000\n\t" \
  "s_add_u32 s96, s96, %[wob]\n\t" \
  "s_add_u32 m0, %[m], 114688\n\t" \
  "s_waitcnt vmcnt(0) lgkmcnt(0)\n\t" \
  "buffer_load_dwordx4 v240, %[wo], s96 offen offset:0 lds\n\t" \
  "v_add_u32 v230, 0x10000, v242\n\t" \
  "v_add_u32 v232, 0x20000, v242\n\t" \
  "buffer_load_dwordx4 v241, %[wo], s96 offen offset:1024 lds\n\t" \
  "buffer_load_dwordx4 v248, %[wo], s96 offen offset:2048 lds\n\t" \
  "buffer_load_dwordx4 v249, %[wo], s96 offen offset:3072 lds\n\t" \
  "ds_read_b64_tr_b16 a[192:193], v234 offset:0\n\t" \
  "ds_read_b64_tr_b16 a[194:195], v234 offset:4096\n\t" \
  "ds_read_b64_tr_b16 a[196:197], v235 offset:0\n\t" \
  "ds_read_b64_tr_b16 a[198:199], v235 offset:4096\n\t" \
  "v_rcp_f32 v224, v160\n\t" \
  "v_accvgpr_read_b32 v128, a0\n\t" \
  "v_accvgpr_read_b32 v129, a1\n\t" \
  "v_accvgpr_read_b32 v130, a2\n\t" \
  "v_accvgpr_read_b32 v131, a3\n\t" \
  "v_accvgpr_read_b32 v132, a4\n\t" \
  "v_accvgpr_read_b32 v133, a5\n\t" \
  "v_accvgpr_read_b32 v134, a6\n\t" \
  "v_accvgpr_read_b32 v135, a7\n\t" \
  "v_pk_mul_f32 v[128:129], v[128:129], v[224:225] op_sel_hi:[1,0]\n\t" \
  "v_pk_mul_f32 v[130:131], v[130:131], v[224:225] op_sel_hi:[1,0]\n\t" \
  "v_pk_mul_f32 v[132:133], v[132:133], v[224:225] op_sel_hi:[1,0]\n\t" \
  "v_pk_mul_f32 v[134:135], v[134:135], v[224:225] op_sel_hi:[1,0]\n\t" \
  "v_cvt_pk_bf16_f32 v128, v128, v129\n\t" \
  "v_cvt_pk_bf16_f32 v129, v130, v131\n\t" \
  "v_cvt_pk_bf16_f32 v130, v132, v133\n\t" \
  "v_cvt_pk_bf16_f32 v131, v134, v135\n\t" \
  "buffer_store_dwordx4 v[128:131], v242, %[ws], s98 offen offset:0 sc1\n\t" \
  "ds_read_b64_tr_b16 a[200:201], v234 offset:8192\n\t" \
  "ds_read_b64_tr_b16 a[202:203], v234 offset:12288\n\t" \
  "v_accvgpr_read_b32 v136, a8\n\t" \
  "v_accvgpr_read_b32 v137, a9\n\t" \
  "v_accvgpr_read_b32 v138, a10\n\t" \
  "v_accvgpr_read_b32 v139, a11\n\t" \
  "v_accvgpr_read_b32 v140, a12\n\t" \
  "v_accvgpr_read_b32 v141, a13\n\t" \
  "v_accvgpr_read_b32 v142, a14\n\t" \
  "v_accvgpr_read_b32 v143, a15\n\t" \
  "v_pk_mul_f32 v[136:137], v[136:137], v[224:225] op_sel_hi:[1,0]\n\t" \
  "v_pk_mul_f32 v[138:139], v[138:139], v[224:225] op_sel_hi:[1,0]\n\t" \
  "v_pk_mul_f32 v[140:141], v[140:141], v[224:225] op_sel_hi:[1,0]\n\t" \
  "v_pk_mul_f32 v[142:143], v[142:143], v[224:225] op_sel_hi:[1,0]\n\t" \
  "v_cvt_pk_bf16_f32 v136, v136, v137\n\t" \
  "v_cvt_pk_bf16_f32 v137, v138, v139\n\t" \
  "v_cvt_pk_bf16_f32 v138, v140, v141\n\t" \
  "v_cvt_pk_bf16_f32 v139, v142, v143\n\t" \
  "buffer_store_dwordx4 v[136:139], v242, %[ws], s98 offen offset:1024 sc1\n\t" \
  "ds_read_b64_tr_b16 a[204:205], v235 offset:8192\n\t" \
  "ds_read_b128 a[224:227], v213 offset:16384\n\t" \
  "ds_read_b64_tr_b16 a[206:207], v235 offset:12288\n\t" \
  "ds_read_b128 a[228:231], v213 offset:17408\n\t" \
  "ds_read_b128 a[232:235], v213 offset:18432\n\t" \
  "ds_read_b128 a[236:239], v213 offset:19456\n\t" \
  "ds_write_b128 v213, v[128:131] offset:16384\n\t" \
  "s_waitcnt vmcnt(0) lgkmcnt(0)\n\t" \
  "s_barrier\n\t" \
  "ds_write_b128 v213, v[136:139] offset:17408\n\t" \
  "v_accvgpr_read_b32 v0, a16\n\t" \
  "v_accvgpr_read_b32 v1, a17\n\t" \
  "v_accvgpr_read_b32 v2, a18\n\t" \
  "v_accvgpr_read_b32 v3, a19\n\t" \
  "v_accvgpr_read_b32 v4, a20\n\t" \
  "v_accvgpr_read_b32 v5, a21\n\t" \
  "v_accvgpr_read_b32 v6, a22\n\t" \
  "v_accvgpr_read_b32 v7, a23\n\t" \
  "v_pk_mul_f32 v[0:1], v[0:1], v[224:225] op_sel_hi:[1,0]\n\t" \
  "v_pk_mul_f32 v[2:3], v[2:3], v[224:225] op_sel_hi:[1,0]\n\t" \
  "v_pk_mul_f32 v[4:5], v[4:5], v[224:225] op_sel_hi:[1,0]\n\t" \
  "v_pk_mul_f32 v[6:7], v[6:7], v[224:225] op_sel_hi:[1,0]\n\t" \
  "v_cvt_pk_bf16_f32 v0, v0, v1\n\t" \
  "v_cvt_pk_bf16_f32 v1, v2, v3\n\t" \
  "v_cvt_pk_bf16_f32 v2, v4, v5\n\t" \
  "v_cvt_pk_bf16_f32 v3, v6, v7\n\t" \
  "buffer_store_dwordx4 v[0:3], v242, %[ws], s98 offen offset:2048 sc1\n\t" \
  "ds_write_b128 v213, v[0:3] offset:18432\n\t" \
  "v_accvgpr_read_b32 v8, a24\n\t" \
  "v_accvgpr_read_b32 v9, a25\n\t" \
  "v_accvgpr_read_b32 v10, a26\n\t" \
  "v_accvgpr_read_b32 v11, a27\n\t" \
  "v_accvgpr_read_b32 v12, a28\n\t" \
  "v_accvgpr_read_b32 v13, a29\n\t" \
  "v_accvgpr_read_b32 v14, a30\n\t" \
  "v_accvgpr_read_b32 v15, a31\n\t" \
  "v_pk_mul_f32 v[8:9], v[8:9], v[224:225] op_sel_hi:[1,0]\n\t" \
  "v_pk_mul_f32 v[10:11], v[10:11], v[224:225] op_sel_hi:[1,0]\n\t" \
  "v_pk_mul_f32 v[12:13], v[12:13], v[224:225] op_sel_hi:[1,0]\n\t" \
  "v_pk_mul_f32 v[14:15], v[14:15], v[224:225] op_sel_hi:[1,0]\n\t" \
  "v_cvt_pk_bf16_f32 v8, v8, v9\n\t" \
  "v_cvt_pk_bf16_f32 v9, v10, v11\n\t" \
  "v_cvt_pk_bf16_f32 v10, v12, v13\n\t" \
  "v_cvt_pk_bf16_f32 v11, v14, v15\n\t" \
  "buffer_store_dwordx4 v[8:11], v242, %[ws], s98 offen offset:3072 sc1\n\t" \
  "ds_write_b128 v213, v[8:11] offset:19456\n\t" \
  "s_xor_b32 s98, %[eso], 32768\n\t" \
  "ds_read_b64_tr_b16 a[208:209], v236 offset:32768\n\t" \
  "ds_read_b64_tr_b16 a[210:211], v236 offset:36864\n\t" \
  "ds_read_b64_tr_b16 a[212:213], v237 offset:32768\n\t" \
  "ds_read_b64_tr_b16 a[214:215], v237 offset:36864\n\t" \
  "s_add_u32 s96, %[hl18], 786432\n\t" \
  "s_and_b32 s96, s96, 0x1c0000\n\t" \
  "ds_read_b64_tr_b16 a[216:217], v236 offset:40960\n\t" \
  "s_add_u32 s96, s96, s77\n\t" \
  "s_add_u32 m0, %[m], 147456\n\t" \
  "ds_read_b64_tr_b16 a[218:219], v236 offset:45056\n\t" \
  "v_rcp_f32 v226, v164\n\t" \
  "buffer_load_dwordx4 v240, %[wo], s96 offen offset:0 lds\n\t" \
  "v_accvgpr_read_b32 v0, a32\n\t" \
  "v_accvgpr_read_b32 v1, a33\n\t" \
  "v_accvgpr_read_b32 v2, a34\n\t" \
  "v_accvgpr_read_b32 v3, a35\n\t" \
  "buffer_load_dwordx4 v241, %[wo], s96 offen offset:1024 lds\n\t" \
  "v_accvgpr_read_b32 v4, a36\n\t" \
  "v_accvgpr_read_b32 v5, a37\n\t" \
  "ds_read_b64_tr_b16 a[220:221], v237 offset:40960\n\t" \
  "v_accvgpr_read_b32 v6, a38\n\t" \
  "v_accvgpr_read_b32 v7, a39\n\t" \
  "buffer_load_dwordx4 v248, %[wo], s96 offen offset:2048 lds\n\t" \
  "ds_read_b64_tr_b16 a[222:223], v237 offset:45056\n\t" \
  "v_mul_f32 v0, v0, v226\n\t" \
  "v_mul_f32 v1, v1, v226\n\t" \
  "v_mul_f32 v2, v2, v226\n\t" \
  "v_mul_f32 v3, v3, v226\n\t" \
  "buffer_load_dwordx4 v249, %[wo], s96 offen offset:3072 lds\n\t" \
  "v_mul_f32 v4, v4, v226\n\t" \
  "v_mul_f32 v5, v5, v226\n\t" \
  "v_mul_f32 v6, v6, v226\n\t" \
  "v_mul_f32 v7, v7, v226\n\t" \
  "v_cvt_pk_bf16_f32 v32, v0, v1\n\t" \
  "v_cvt_pk_bf16_f32 v33, v2, v3\n\t" \
  "v_cvt_pk_bf16_f32 v34, v4, v5\n\t" \
  "v_cvt_pk_bf16_f32 v35, v6, v7\n\t" \
  "v_accvgpr_read_b32 v8, a40\n\t" \
  "v_accvgpr_read_b32 v9, a41\n\t" \
  "v_accvgpr_read_b32 v10, a42\n\t" \
  "v_accvgpr_read_b32 v11, a43\n\t" \
  "v_accvgpr_read_b32 v12, a44\n\t" \
  "v_accvgpr_read_b32 v13, a45\n\t" \
  "v_accvgpr_read_b32 v14, a46\n\t" \
  "v_accvgpr_read_b32 v15, a47\n\t" \
  "v_mul_f32 v8, v8, v226\n\t" \
  "v_mul_f32 v9, v9, v226\n\t" \
  "v_mul_f32 v10, v10, v226\n\t" \
  "v_mul_f32 v11, v11, v226\n\t" \
  "v_mul_f32 v12, v12, v226\n\t" \
  "v_mul_f32 v13, v13, v226\n\t" \
  "v_mul_f32 v14, v14, v226\n\t" \
  "v_mul_f32 v15, v15, v226\n\t" \
  "v_cvt_pk_bf16_f32 v36, v8, v9\n\t" \
  "v_cvt_pk_bf16_f32 v37, v10, v11\n\t" \
  "v_cvt_pk_bf16_f32 v38, v12, v13\n\t" \
  "v_cvt_pk_bf16_f32 v39, v14, v15\n\t" \
  "v_accvgpr_read_b32 v0, a48\n\t" \
  "v_accvgpr_read_b32 v1, a49\n\t" \
  "s_waitcnt vmcnt(0) lgkmcnt(0)\n\t" \
  "s_barrier\n\t" \
  "buffer_store_dwordx4 v[32:35], v242, %[ws], s98 offen offset:0 sc1\n\t" \
  "v_accvgpr_read_b32 v2, a50\n\t" \
  "v_accvgpr_read_b32 v3, a51\n\t" \
  "v_accvgpr_read_b32 v4, a52\n\t" \
  "s_add_u32 s96, %[hl18], 1048576\n\t" \
  "s_and_b32 s96, s96, 0x1c0000\n\t" \
  "s_add_u32 s96, s96, %[wob]\n\t" \
  "v_accvgpr_read_b32 v5, a53\n\t" \
  "s_add_u32 m0, %[m], 0\n\t" \
  "v_accvgpr_read_b32 v6, a54\n\t" \
  "v_accvgpr_read_b32 v7, a55\n\t" \
  "v_mul_f32 v0, v0, v226\n\t" \
  "buffer_load_dwordx4 v240, %[wo], s96 offen offset:0 lds\n\t" \
  "v_mul_f32 v1, v1, v226\n\t" \
  "v_mul_f32 v2, v2, v226\n\t" \
  "v_mul_f32 v3, v3, v226\n\t" \
  "v_mul_f32 v4, v4, v226\n\t" \
  "v_mul_f32 v5, v5, v226\n\t" \
  "v_mul_f32 v6, v6, v226\n\t" \
  "v_mul_f32 v7, v7, v226\n\t" \
  "buffer_load_dwordx4 v241, %[wo], s96 offen offset:1024 lds\n\t" \
  "v_cvt_pk_bf16_f32 v40, v0, v1\n\t" \
  "v_cvt_pk_bf16_f32 v41, v2, v3\n\t" \
  "v_cvt_pk_bf16_f32 v42, v4, v5\n\t" \
  "v_cvt_pk_bf16_f32 v43, v6, v7\n\t" \
  "v_accvgpr_read_b32 v8, a56\n\t" \
  "v_accvgpr_read_b32 v9, a57\n\t" \
  "v_accvgpr_read_b32 v10, a58\n\t" \
  "v_accvgpr_read_b32 v11, a59\n\t" \
  "v_accvgpr_read_b32 v12, a60\n\t" \
  "buffer_load_dwordx4 v248, %[wo], s96 offen offset:2048 lds\n\t" \
  "v_accvgpr_read_b32 v13, a61\n\t" \
  "v_accvgpr_read_b32 v14, a62\n\t" \
  "v_accvgpr_read_b32 v15, a63\n\t" \
  "v_mul_f32 v8, v8, v226\n\t" \
  "v_mul_f32 v9, v9, v226\n\t" \
  "v_mul_f32 v10, v10, v226\n\t" \
  "v_mul_f32 v11, v11, v226\n\t" \
  "v_mul_f32 v12, v12, v226\n\t" \
  "buffer_store_dwordx4 v[36:39], v242, %[ws], s98 offen offset:1024 sc1\n\t" \
  "v_mul_f32 v13, v13, v226\n\t" \
  "v_mul_f32 v14, v14, v226\n\t" \
  "v_mul_f32 v15, v15, v226\n\t" \
  "v_cvt_pk_bf16_f32 v44, v8, v9\n\t" \
  "v_cvt_pk_bf16_f32 v45, v10, v11\n\t" \
  "v_rcp_f32 v220, v168\n\t" \
  "buffer_load_dwordx4 v249, %[wo], s96 offen offset:3072 lds\n\t" \
  "ds_read_b64_tr_b16 a[128:129], v234 offset:49152\n\t" \
  "v_cvt_pk_bf16_f32 v46, v12, v13\n\t" \
  "ds_read_b64_tr_b16 a[130:131], v234 offset:53248\n\t" \
  "v_cvt_pk_bf16_f32 v47, v14, v15\n\t" \
  "ds_read_b64_tr_b16 a[132:133], v235 offset:49152\n\t" \
  "ds_read_b128 a[240:243], v213 offset:32768\n\t" \
  "s_waitcnt vmcnt(0) lgkmcnt(0)\n\t" \
  "buffer_store_dword v243, v254, %[ws], %[fw2] offen offset:0 sc1\n\t" \
  "ds_read_b64_tr_b16 a[134:135], v235 offset:53248\n\t" \
  "ds_read_b128 a[244:247], v213 offset:33792\n\t" \
  "buffer_store_dwordx4 v[40:43], v242, %[ws], s98 offen offset:2048 sc1\n\t" \
  "v_accvgpr_read_b32 v64, a64\n\t" \
  "v_accvgpr_read_b32 v65, a65\n\t" \
  "v_accvgpr_read_b32 v66, a66\n\t" \
  "v_accvgpr_read_b32 v67, a67\n\t" \
  "v_accvgpr_read_b32 v68, a68\n\t" \
  "s_waitcnt vmcnt(0) lgkmcnt(0)\n\t" \
  "s_barrier\n\t" \
  "ds_read_b128 v[128:131], v242 offset:16384\n\t" \
  "v_accvgpr_read_b32 v69, a69\n\t" \
  "v_accvgpr_read_b32 v70, a70\n\t" \
  "v_accvgpr_read_b32 v71, a71\n\t" \
  "buffer_store_dwordx4 v[44:47], v242, %[ws], s98 offen offset:3072 sc1\n\t" \
  "v_mul_f32 v64, v64, v220\n\t" \
  "ds_read_b128 v[132:135], v242 offset:17408\n\t" \
  "v_mul_f32 v65, v65, v220\n\t" \
  "s_xor_b32 s98, %[eso], 65536\n\t" \
  "v_rcp_f32 v222, v172\n\t" \
  "ds_read_b128 v[136:139], v242 offset:18432\n\t" \
  "ds_read_b128 v[140:143], v242 offset:19456\n\t" \
  "v_mul_f32 v66, v66, v220\n\t" \
  "ds_read_b128 v[144:147], v242 offset:20480\n\t" \
  "v_mul_f32 v67, v67, v220\n\t" \
  "ds_read_b64_tr_b16 a[136:137], v234 offset:57344\n\t" \
  "v_mul_f32 v68, v68, v220\n\t" \
  "ds_read_b128 v[148:151], v242 offset:21504\n\t" \
  "v_mul_f32 v69, v69, v220\n\t" \
  "ds_read_b64_tr_b16 a[138:139], v234 offset:61440\n\t" \
  "v_mul_f32 v70, v70, v220\n\t" \
  "ds_read_b128 v[152:155], v242 offset:22528\n\t" \
  "v_mul_f32 v71, v71, v220\n\t" \
  "ds_read_b128 v[156:159], v242 offset:23552\n\t" \
  "ds_read_b64_tr_b16 a[140:141], v235 offset:57344\n\t" \
  "ds_read_b64_tr_b16 a[142:143], v235 offset:61440\n\t" \
  "ds_read_b128 a[248:251], v213 offset:34816\n\t" \
  "s_waitcnt vmcnt(0) lgkmcnt(0)\n\t" \
  "s_nop 1\n\t" \
  "s_branch .Lat_join\n\t" \
  ".Lsf_c00:\n\t" \
  "s_mov_b32 vcc_lo, 0\n\t" \
  "s_branch .Lsf_s0\n\t" \
  ".Lsf_c10:\n\t" \
  "s_mov_b32 vcc_lo, 1\n\t" \
  "s_branch .Lsf_s0\n\t" \
  ".Lsf_c20:\n\t" \
  "s_mov_b32 vcc_lo, 2\n\t" \
  "s_branch .Lsf_s0\n\t" \
  ".Lsf_c30:\n\t" \
  "s_mov_b32 vcc_lo, 3\n\t" \
  "s_branch .Lsf_s0\n\t" \
  ".Lsf_c40:\n\t" \
  "s_mov_b32 vcc_lo, 4\n\t" \
  "s_branch .Lsf_s0\n\t" \
  ".Lsf_c50:\n\t" \
  "s_mov_b32 vcc_lo, 5\n\t" \
  "s_branch .Lsf_s0\n\t" \
  ".Lsf_c60:\n\t" \
  "s_mov_b32 vcc_lo, 6\n\t" \
  "s_branch .Lsf_s0\n\t" \
  ".Lsf_c70:\n\t" \
  "s_mov_b32 vcc_lo, 7\n\t" \
  "s_branch .Lsf_s0\n\t" \
  ".Lsf_s0:\n\t" \
  "s_nop 7\n\t" \
  "s_nop 1\n\t" \
  "v_mov_b32 v193, v192\n\t" \
  "s_nop 1\n\t" \
  "v_permlane32_swap_b32 v192, v193\n\t" \
  "s_nop 1\n\t" \
  "v_max_f32 v192, v192, v193\n\t" \
  "s_nop 1\n\t" \
  "v_mov_b32 v193, v192\n\t" \
  "s_nop 1\n\t" \
  "v_permlane16_swap_b32 v192, v193\n\t" \
  "s_nop 1\n\t" \
  "v_max_f32 v192, v192, v193\n\t" \
  "v_max_f32 v192, 0, v192\n\t" \
  "v_sub_f32 v0, v0, v192\n\t" \
  "v_sub_f32 v1, v1, v192\n\t" \
  "v_sub_f32 v2, v2, v192\n\t" \
  "v_sub_f32 v3, v3, v192\n\t" \
  "v_sub_f32 v4, v4, v192\n\t" \
  "v_sub_f32 v5, v5, v192\n\t" \
  "v_sub_f32 v6, v6, v192\n\t" \
  "v_sub_f32 v7, v7, v192\n\t" \
  "v_sub_f32 v8, v8, v192\n\t" \
  "v_sub_f32 v9, v9, v192\n\t" \
  "v_sub_f32 v10, v10, v192\n\t" \
  "v_sub_f32 v11, v11, v192\n\t" \
  "v_sub_f32 v12, v12, v192\n\t" \
  "v_sub_f32 v13, v13, v192\n\t" \
  "v_sub_f32 v14, v14, v192\n\t" \
  "v_sub_f32 v15, v15, v192\n\t" \
  "v_sub_f32 v176, v176, v192\n\t" \
  "v_sub_f32 v177, v177, v192\n\t" \
  "v_sub_f32 v178, v178, v192\n\t" \
  "v_sub_f32 v179, v179, v192\n\t" \
  "v_exp_f32 v196, -v192\n\t" \
  "s_mov_b32 s99, 1\n\t" \
  "s_nop 1\n\t" \
  "s_cmp_eq_u32 vcc_lo, 0\n\t" \
  "s_cbranch_scc1 .Lsf_r00\n\t" \
  "s_cmp_eq_u32 vcc_lo, 1\n\t" \
  "s_cbranch_scc1 .Lsf_r10\n\t" \
  "s_cmp_eq_u32 vcc_lo, 2\n\t" \
  "s_cbranch_scc1 .Lsf_r20\n\t" \
  "s_cmp_eq_u32 vcc_lo, 3\n\t" \
  "s_cbranch_scc1 .Lsf_r30\n\t" \
  "s_cmp_eq_u32 vcc_lo, 4\n\t" \
  "s_cbranch_scc1 .Lsf_r40\n\t" \
  "s_cmp_eq_u32 vcc_lo, 5\n\t" \
  "s_cbranch_scc1 .Lsf_r50\n\t" \
  "s_cmp_eq_u32 vcc_lo, 6\n\t" \
  "s_cbranch_scc1 .Lsf_r60\n\t" \
  "s_cmp_eq_u32 vcc_lo, 7\n\t" \
  "s_cbranch_scc1 .Lsf_r70\n\t" \
  ".Lsf_c01:\n\t" \
  "s_mov_b32 vcc_lo, 0\n\t" \
  "s_branch .Lsf_s1\n\t" \
  ".Lsf_c11:\n\t" \
  "s_mov_b32 vcc_lo, 1\n\t" \
  "s_branch .Lsf_s1\n\t" \
  ".Lsf_c21:\n\t" \
  "s_mov_b32 vcc_lo, 2\n\t" \
  "s_branch .Lsf_s1\n\t" \
  ".Lsf_c31:\n\t" \
  "s_mov_b32 vcc_lo, 3\n\t" \
  "s_branch .Lsf_s1\n\t" \
  ".Lsf_c41:\n\t" \
  "s_mov_b32 vcc_lo, 4\n\t" \
  "s_branch .Lsf_s1\n\t" \
  ".Lsf_c51:\n\t" \
  "s_mov_b32 vcc_lo, 5\n\t" \
  "s_branch .Lsf_s1\n\t" \
  ".Lsf_c61:\n\t" \
  "s_mov_b32 vcc_lo, 6\n\t" \
  "s_branch .Lsf_s1\n\t" \
  ".Lsf_c71:\n\t" \
  "s_mov_b32 vcc_lo, 7\n\t" \
  "s_branch .Lsf_s1\n\t" \
  ".Lsf_s1:\n\t" \
  "s_nop 7\n\t" \
  "s_nop 1\n\t" \
  "v_mov_b32 v193, v192\n\t" \
  "s_nop 1\n\t" \
  "v_permlane32_swap_b32 v192, v193\n\t" \
  "s_nop 1\n\t" \
  "v_max_f32 v192, v192, v193\n\t" \
  "s_nop 1\n\t" \
  "v_mov_b32 v193, v192\n\t" \
  "s_nop 1\n\t" \
  "v_permlane16_swap_b32 v192, v193\n\t" \
  "s_nop 1\n\t" \
  "v_max_f32 v192, v192, v193\n\t" \
  "v_max_f32 v192, 0, v192\n\t" \
  "v_sub_f32 v32, v32, v192\n\t" \
  "v_sub_f32 v33, v33, v192\n\t" \
  "v_sub_f32 v34, v34, v192\n\t" \
  "v_sub_f32 v35, v35, v192\n\t" \
  "v_sub_f32 v36, v36, v192\n\t" \
  "v_sub_f32 v37, v37, v192\n\t" \
  "v_sub_f32 v38, v38, v192\n\t" \
  "v_sub_f32 v39, v39, v192\n\t" \
  "v_sub_f32 v40, v40, v192\n\t" \
  "v_sub_f32 v41, v41, v192\n\t" \
  "v_sub_f32 v42, v42, v192\n\t" \
  "v_sub_f32 v43, v43, v192\n\t" \
  "v_sub_f32 v44, v44, v192\n\t" \
  "v_sub_f32 v45, v45, v192\n\t" \
  "v_sub_f32 v46, v46, v192\n\t" \
  "v_sub_f32 v47, v47, v192\n\t" \
  "v_sub_f32 v180, v180, v192\n\t" \
  "v_sub_f32 v181, v181, v192\n\t" \
  "v_sub_f32 v182, v182, v192\n\t" \
  "v_sub_f32 v183, v183, v192\n\t" \
  "v_exp_f32 v197, -v192\n\t" \
  "s_mov_b32 s99, 1\n\t" \
  "s_nop 1\n\t" \
  "s_cmp_eq_u32 vcc_lo, 0\n\t" \
  "s_cbranch_scc1 .Lsf_r01\n\t" \
  "s_cmp_eq_u32 vcc_lo, 1\n\t" \
  "s_cbranch_scc1 .Lsf_r11\n\t" \
  "s_cmp_eq_u32 vcc_lo, 2\n\t" \
  "s_cbranch_scc1 .Lsf_r21\n\t" \
  "s_cmp_eq_u32 vcc_lo, 3\n\t" \
  "s_cbranch_scc1 .Lsf_r31\n\t" \
  "s_cmp_eq_u32 vcc_lo, 4\n\t" \
  "s_cbranch_scc1 .Lsf_r41\n\t" \
  "s_cmp_eq_u32 vcc_lo, 5\n\t" \
  "s_cbranch_scc1 .Lsf_r51\n\t" \
  "s_cmp_eq_u32 vcc_lo, 6\n\t" \
  "s_cbranch_scc1 .Lsf_r61\n\t" \
  "s_cmp_eq_u32 vcc_lo, 7\n\t" \
  "s_cbranch_scc1 .Lsf_r71\n\t" \
  ".Lsf_c02:\n\t" \
  "s_mov_b32 vcc_lo, 0\n\t" \
  "s_branch .Lsf_s2\n\t" \
  ".Lsf_c12:\n\t" \
  "s_mov_b32 vcc_lo, 1\n\t" \
  "s_branch .Lsf_s2\n\t" \
  ".Lsf_c22:\n\t" \
  "s_mov_b32 vcc_lo, 2\n\t" \
  "s_branch .Lsf_s2\n\t" \
  ".Lsf_c32:\n\t" \
  "s_mov_b32 vcc_lo, 3\n\t" \
  "s_branch .Lsf_s2\n\t" \
  ".Lsf_c42:\n\t" \
  "s_mov_b32 vcc_lo, 4\n\t" \
  "s_branch .Lsf_s2\n\t" \
  ".Lsf_c52:\n\t" \
  "s_mov_b32 vcc_lo, 5\n\t" \
  "s_branch .Lsf_s2\n\t" \
  ".Lsf_c62:\n\t" \
  "s_mov_b32 vcc_lo, 6\n\t" \
  "s_branch .Lsf_s2\n\t" \
  ".Lsf_c72:\n\t" \
  "s_mov_b32 vcc_lo, 7\n\t" \
  "s_branch .Lsf_s2\n\t" \
  ".Lsf_s2:\n\t" \
  "s_nop 7\n\t" \
  "s_nop 1\n\t" \
  "v_mov_b32 v193, v192\n\t" \
  "s_nop 1\n\t" \
  "v_permlane32_swap_b32 v192, v193\n\t" \
  "s_nop 1\n\t" \
  "v_max_f32 v192, v192, v193\n\t" \
  "s_nop 1\n\t" \
  "v_mov_b32 v193, v192\n\t" \
  "s_nop 1\n\t" \
  "v_permlane16_swap_b32 v192, v193\n\t" \
  "s_nop 1\n\t" \
  "v_max_f32 v192, v192, v193\n\t" \
  "v_max_f32 v192, 0, v192\n\t" \
  "v_sub_f32 v64, v64, v192\n\t" \
  "v_sub_f32 v65, v65, v192\n\t" \
  "v_sub_f32 v66, v66, v192\n\t" \
  "v_sub_f32 v67, v67, v192\n\t" \
  "v_sub_f32 v68, v68, v192\n\t" \
  "v_sub_f32 v69, v69, v192\n\t" \
  "v_sub_f32 v70, v70, v192\n\t" \
  "v_sub_f32 v71, v71, v192\n\t" \
  "v_sub_f32 v72, v72, v192\n\t" \
  "v_sub_f32 v73, v73, v192\n\t" \
  "v_sub_f32 v74, v74, v192\n\t" \
  "v_sub_f32 v75, v75, v192\n\t" \
  "v_sub_f32 v76, v76, v192\n\t" \
  "v_sub_f32 v77, v77, v192\n\t" \
  "v_sub_f32 v78, v78, v192\n\t" \
  "v_sub_f32 v79, v79, v192\n\t" \
  "v_sub_f32 v184, v184, v192\n\t" \
  "v_sub_f32 v185, v185, v192\n\t" \
  "v_sub_f32 v186, v186, v192\n\t" \
  "v_sub_f32 v187, v187, v192\n\t" \
  "v_exp_f32 v198, -v192\n\t" \
  "s_mov_b32 s99, 1\n\t" \
  "s_nop 1\n\t" \
  "s_cmp_eq_u32 vcc_lo, 0\n\t" \
  "s_cbranch_scc1 .Lsf_r02\n\t" \
  "s_cmp_eq_u32 vcc_lo, 1\n\t" \
  "s_cbranch_scc1 .Lsf_r12\n\t" \
  "s_cmp_eq_u32 vcc_lo, 2\n\t" \
  "s_cbranch_scc1 .Lsf_r22\n\t" \
  "s_cmp_eq_u32 vcc_lo, 3\n\t" \
  "s_cbranch_scc1 .Lsf_r32\n\t" \
  "s_cmp_eq_u32 vcc_lo, 4\n\t" \
  "s_cbranch_scc1 .Lsf_r42\n\t" \
  "s_cmp_eq_u32 vcc_lo, 5\n\t" \
  "s_cbranch_scc1 .Lsf_r52\n\t" \
  "s_cmp_eq_u32 vcc_lo, 6\n\t" \
  "s_cbranch_scc1 .Lsf_r62\n\t" \
  "s_cmp_eq_u32 vcc_lo, 7\n\t" \
  "s_cbranch_scc1 .Lsf_r72\n\t" \
  ".Lsf_c03:\n\t" \
  "s_mov_b32 vcc_lo, 0\n\t" \
  "s_branch .Lsf_s3\n\t" \
  ".Lsf_c13:\n\t" \
  "s_mov_b32 vcc_lo, 1\n\t" \
  "s_branch .Lsf_s3\n\t" \
  ".Lsf_c23:\n\t" \
  "s_mov_b32 vcc_lo, 2\n\t" \
  "s_branch .Lsf_s3\n\t" \
  ".Lsf_c33:\n\t" \
  "s_mov_b32 vcc_lo, 3\n\t" \
  "s_branch .Lsf_s3\n\t" \
  ".Lsf_c43:\n\t" \
  "s_mov_b32 vcc_lo, 4\n\t" \
  "s_branch .Lsf_s3\n\t" \
  ".Lsf_c53:\n\t" \
  "s_mov_b32 vcc_lo, 5\n\t" \
  "s_branch .Lsf_s3\n\t" \
  ".Lsf_c63:\n\t" \
  "s_mov_b32 vcc_lo, 6\n\t" \
  "s_branch .Lsf_s3\n\t" \
  ".Lsf_c73:\n\t" \
  "s_mov_b32 vcc_lo, 7\n\t" \
  "s_branch .Lsf_s3\n\t" \
  ".Lsf_s3:\n\t" \
  "s_nop 7\n\t" \
  "s_nop 1\n\t" \
  "v_mov_b32 v193, v192\n\t" \
  "s_nop 1\n\t" \
  "v_permlane32_swap_b32 v192, v193\n\t" \
  "s_nop 1\n\t" \
  "v_max_f32 v192, v192, v193\n\t" \
  "s_nop 1\n\t" \
  "v_mov_b32 v193, v192\n\t" \
  "s_nop 1\n\t" \
  "v_permlane16_swap_b32 v192, v193\n\t" \
  "s_nop 1\n\t" \
  "v_max_f32 v192, v192, v193\n\t" \
  "v_max_f32 v192, 0, v192\n\t" \
  "v_sub_f32 v96, v96, v192\n\t" \
  "v_sub_f32 v97, v97, v192\n\t" \
  "v_sub_f32 v98, v98, v192\n\t" \
  "v_sub_f32 v99, v99, v192\n\t" \
  "v_sub_f32 v100, v100, v192\n\t" \
  "v_sub_f32 v101, v101, v192\n\t" \
  "v_sub_f32 v102, v102, v192\n\t" \
  "v_sub_f32 v103, v103, v192\n\t" \
  "v_sub_f32 v104, v104, v192\n\t" \
  "v_sub_f32 v105, v105, v192\n\t" \
  "v_sub_f32 v106, v106, v192\n\t" \
  "v_sub_f32 v107, v107, v192\n\t" \
  "v_sub_f32 v108, v108, v192\n\t" \
  "v_sub_f32 v109, v109, v192\n\t" \
  "v_sub_f32 v110, v110, v192\n\t" \
  "v_sub_f32 v111, v111, v192\n\t" \
  "v_sub_f32 v188, v188, v192\n\t" \
  "v_sub_f32 v189, v189, v192\n\t" \
  "v_sub_f32 v190, v190, v192\n\t" \
  "v_sub_f32 v191, v191, v192\n\t" \
  "v_exp_f32 v199, -v192\n\t" \
  "s_mov_b32 s99, 1\n\t" \
  "s_nop 1\n\t" \
  "s_cmp_eq_u32 vcc_lo, 0\n\t" \
  "s_cbranch_scc1 .Lsf_r03\n\t" \
  "s_cmp_eq_u32 vcc_lo, 1\n\t" \
  "s_cbranch_scc1 .Lsf_r13\n\t" \
  "s_cmp_eq_u32 vcc_lo, 2\n\t" \
  "s_cbranch_scc1 .Lsf_r23\n\t" \
  "s_cmp_eq_u32 vcc_lo, 3\n\t" \
  "s_cbranch_scc1 .Lsf_r33\n\t" \
  "s_cmp_eq_u32 vcc_lo, 4\n\t" \
  "s_cbranch_scc1 .Lsf_r43\n\t" \
  "s_cmp_eq_u32 vcc_lo, 5\n\t" \
  "s_cbranch_scc1 .Lsf_r53\n\t" \
  "s_cmp_eq_u32 vcc_lo, 6\n\t" \
  "s_cbranch_scc1 .Lsf_r63\n\t" \
  "s_cmp_eq_u32 vcc_lo, 7\n\t" \
  "s_cbranch_scc1 .Lsf_r73\n\t" \
  ".Lsf_p0:\n\t" \
  "s_mov_b32 vcc_lo, 0\n\t" \
  "s_branch .Lsf_rs\n\t" \
  ".Lsf_p1:\n\t" \
  "s_mov_b32 vcc_lo, 1\n\t" \
  "s_branch .Lsf_rs\n\t" \
  ".Lsf_p2:\n\t" \
  "s_mov_b32 vcc_lo, 2\n\t" \
  "s_branch .Lsf_rs\n\t" \
  ".Lsf_p3:\n\t" \
  "s_mov_b32 vcc_lo, 3\n\t" \
  "s_branch .Lsf_rs\n\t" \
  ".Lsf_p4:\n\t" \
  "s_mov_b32 vcc_lo, 4\n\t" \
  "s_branch .Lsf_rs\n\t" \
  ".Lsf_p5:\n\t" \
  "s_mov_b32 vcc_lo, 5\n\t" \
  "s_branch .Lsf_rs\n\t" \
  ".Lsf_p6:\n\t" \
  "s_mov_b32 vcc_lo, 6\n\t" \
  "s_branch .Lsf_rs\n\t" \
  ".Lsf_p7:\n\t" \
  "s_mov_b32 vcc_lo, 7\n\t" \
  "s_branch .Lsf_rs\n\t" \
  ".Lsf_rs:\n\t" \
  "s_nop 7\n\t" \
  "s_nop 7\n\t" \
  "v_accvgpr_read_b32 v193, a0\n\t" \
  "v_mul_f32 v193, v196, v193\n\t" \
  "v_accvgpr_write_b32 a0, v193\n\t" \
  "v_accvgpr_read_b32 v193, a1\n\t" \
  "v_mul_f32 v193, v196, v193\n\t" \
  "v_accvgpr_write_b32 a1, v193\n\t" \
  "v_accvgpr_read_b32 v193, a2\n\t" \
  "v_mul_f32 v193, v196, v193\n\t" \
  "v_accvgpr_write_b32 a2, v193\n\t" \
  "v_accvgpr_read_b32 v193, a3\n\t" \
  "v_mul_f32 v193, v196, v193\n\t" \
  "v_accvgpr_write_b32 a3, v193\n\t" \
  "v_accvgpr_read_b32 v193, a4\n\t" \
  "v_mul_f32 v193, v196, v193\n\t" \
  "v_accvgpr_write_b32 a4, v193\n\t" \
  "v_accvgpr_read_b32 v193, a5\n\t" \
  "v_mul_f32 v193, v196, v193\n\t" \
  "v_accvgpr_write_b32 a5, v193\n\t" \
  "v_accvgpr_read_b32 v193, a6\n\t" \
  "v_mul_f32 v193, v196, v193\n\t" \
  "v_accvgpr_write_b32 a6, v193\n\t" \
  "v_accvgpr_read_b32 v193, a7\n\t" \
  "v_mul_f32 v193, v196, v193\n\t" \
  "v_accvgpr_write_b32 a7, v193\n\t" \
  "v_accvgpr_read_b32 v193, a8\n\t" \
  "v_mul_f32 v193, v196, v193\n\t" \
  "v_accvgpr_write_b32 a8, v193\n\t" \
  "v_accvgpr_read_b32 v193, a9\n\t" \
  "v_mul_f32 v193, v196, v193\n\t" \
  "v_accvgpr_write_b32 a9, v193\n\t" \
  "v_accvgpr_read_b32 v193, a10\n\t" \
  "v_mul_f32 v193, v196, v193\n\t" \
  "v_accvgpr_write_b32 a10, v193\n\t" \
  "v_accvgpr_read_b32 v193, a11\n\t" \
  "v_mul_f32 v193, v196, v193\n\t" \
  "v_accvgpr_write_b32 a11, v193\n\t" \
  "v_accvgpr_read_b32 v193, a12\n\t" \
  "v_mul_f32 v193, v196, v193\n\t" \
  "v_accvgpr_write_b32 a12, v193\n\t" \
  "v_accvgpr_read_b32 v193, a13\n\t" \
  "v_mul_f32 v193, v196, v193\n\t" \
  "v_accvgpr_write_b32 a13, v193\n\t" \
  "v_accvgpr_read_b32 v193, a14\n\t" \
  "v_mul_f32 v193, v196, v193\n\t" \
  "v_accvgpr_write_b32 a14, v193\n\t" \
  "v_accvgpr_read_b32 v193, a15\n\t" \
  "v_mul_f32 v193, v196, v193\n\t" \
  "v_accvgpr_write_b32 a15, v193\n\t" \
  "v_accvgpr_read_b32 v193, a16\n\t" \
  "v_mul_f32 v193, v196, v193\n\t" \
  "v_accvgpr_write_b32 a16, v193\n\t" \
  "v_accvgpr_read_b32 v193, a17\n\t" \
  "v_mul_f32 v193, v196, v193\n\t" \
  "v_accvgpr_write_b32 a17, v193\n\t" \
  "v_accvgpr_read_b32 v193, a18\n\t" \
  "v_mul_f32 v193, v196, v193\n\t" \
  "v_accvgpr_write_b32 a18, v193\n\t" \
  "v_accvgpr_read_b32 v193, a19\n\t" \
  "v_mul_f32 v193, v196, v193\n\t" \
  "v_accvgpr_write_b32 a19, v193\n\t" \
  "v_accvgpr_read_b32 v193, a20\n\t" \
  "v_mul_f32 v193, v196, v193\n\t" \
  "v_accvgpr_write_b32 a20, v193\n\t" \
  "v_accvgpr_read_b32 v193, a21\n\t" \
  "v_mul_f32 v193, v196, v193\n\t" \
  "v_accvgpr_write_b32 a21, v193\n\t" \
  "v_accvgpr_read_b32 v193, a22\n\t" \
  "v_mul_f32 v193, v196, v193\n\t" \
  "v_accvgpr_write_b32 a22, v193\n\t" \
  "v_accvgpr_read_b32 v193, a23\n\t" \
  "v_mul_f32 v193, v196, v193\n\t" \
  "v_accvgpr_write_b32 a23, v193\n\t" \
  "v_accvgpr_read_b32 v193, a24\n\t" \
  "v_mul_f32 v193, v196, v193\n\t" \
  "v_accvgpr_write_b32 a24, v193\n\t" \
  "v_accvgpr_read_b32 v193, a25\n\t" \
  "v_mul_f32 v193, v196, v193\n\t" \
  "v_accvgpr_write_b32 a25, v193\n\t" \
  "v_accvgpr_read_b32 v193, a26\n\t" \
  "v_mul_f32 v193, v196, v193\n\t" \
  "v_accvgpr_write_b32 a26, v193\n\t" \
  "v_accvgpr_read_b32 v193, a27\n\t" \
  "v_mul_f32 v193, v196, v193\n\t" \
  "v_accvgpr_write_b32 a27, v193\n\t" \
  "v_accvgpr_read_b32 v193, a28\n\t" \
  "v_mul_f32 v193, v196, v193\n\t" \
  "v_accvgpr_write_b32 a28, v193\n\t" \
  "v_accvgpr_read_b32 v193, a29\n\t" \
  "v_mul_f32 v193, v196, v193\n\t" \
  "v_accvgpr_write_b32 a29, v193\n\t" \
  "v_accvgpr_read_b32 v193, a30\n\t" \
  "v_mul_f32 v193, v196, v193\n\t" \
  "v_accvgpr_write_b32 a30, v193\n\t" \
  "v_accvgpr_read_b32 v193, a31\n\t" \
  "v_mul_f32 v193, v196, v193\n\t" \
  "v_accvgpr_write_b32 a31, v193\n\t" \
  "v_accvgpr_read_b32 v193, a32\n\t" \
  "v_mul_f32 v193, v197, v193\n\t" \
  "v_accvgpr_write_b32 a32, v193\n\t" \
  "v_accvgpr_read_b32 v193, a33\n\t" \
  "v_mul_f32 v193, v197, v193\n\t" \
  "v_accvgpr_write_b32 a33, v193\n\t" \
  "v_accvgpr_read_b32 v193, a34\n\t" \
  "v_mul_f32 v193, v197, v193\n\t" \
  "v_accvgpr_write_b32 a34, v193\n\t" \
  "v_accvgpr_read_b32 v193, a35\n\t" \
  "v_mul_f32 v193, v197, v193\n\t" \
  "v_accvgpr_write_b32 a35, v193\n\t" \
  "v_accvgpr_read_b32 v193, a36\n\t" \
  "v_mul_f32 v193, v197, v193\n\t" \
  "v_accvgpr_write_b32 a36, v193\n\t" \
  "v_accvgpr_read_b32 v193, a37\n\t" \
  "v_mul_f32 v193, v197, v193\n\t" \
  "v_accvgpr_write_b32 a37, v193\n\t" \
  "v_accvgpr_read_b32 v193, a38\n\t" \
  "v_mul_f32 v193, v197, v193\n\t" \
  "v_accvgpr_write_b32 a38, v193\n\t" \
  "v_accvgpr_read_b32 v193, a39\n\t" \
  "v_mul_f32 v193, v197, v193\n\t" \
  "v_accvgpr_write_b32 a39, v193\n\t" \
  "v_accvgpr_read_b32 v193, a40\n\t" \
  "v_mul_f32 v193, v197, v193\n\t" \
  "v_accvgpr_write_b32 a40, v193\n\t" \
  "v_accvgpr_read_b32 v193, a41\n\t" \
  "v_mul_f32 v193, v197, v193\n\t" \
  "v_accvgpr_write_b32 a41, v193\n\t" \
  "v_accvgpr_read_b32 v193, a42\n\t" \
  "v_mul_f32 v193, v197, v193\n\t" \
  "v_accvgpr_write_b32 a42, v193\n\t" \
  "v_accvgpr_read_b32 v193, a43\n\t" \
  "v_mul_f32 v193, v197, v193\n\t" \
  "v_accvgpr_write_b32 a43, v193\n\t" \
  "v_accvgpr_read_b32 v193, a44\n\t" \
  "v_mul_f32 v193, v197, v193\n\t" \
  "v_accvgpr_write_b32 a44, v193\n\t" \
  "v_accvgpr_read_b32 v193, a45\n\t" \
  "v_mul_f32 v193, v197, v193\n\t" \
  "v_accvgpr_write_b32 a45, v193\n\t" \
  "v_accvgpr_read_b32 v193, a46\n\t" \
  "v_mul_f32 v193, v197, v193\n\t" \
  "v_accvgpr_write_b32 a46, v193\n\t" \
  "v_accvgpr_read_b32 v193, a47\n\t" \
  "v_mul_f32 v193, v197, v193\n\t" \
  "v_accvgpr_write_b32 a47, v193\n\t" \
  "v_accvgpr_read_b32 v193, a48\n\t" \
  "v_mul_f32 v193, v197, v193\n\t" \
  "v_accvgpr_write_b32 a48, v193\n\t" \
  "v_accvgpr_read_b32 v193, a49\n\t" \
  "v_mul_f32 v193, v197, v193\n\t" \
  "v_accvgpr_write_b32 a49, v193\n\t" \
  "v_accvgpr_read_b32 v193, a50\n\t" \
  "v_mul_f32 v193, v197, v193\n\t" \
  "v_accvgpr_write_b32 a50, v193\n\t" \
  "v_accvgpr_read_b32 v193, a51\n\t" \
  "v_mul_f32 v193, v197, v193\n\t" \
  "v_accvgpr_write_b32 a51, v193\n\t" \
  "v_accvgpr_read_b32 v193, a52\n\t" \
  "v_mul_f32 v193, v197, v193\n\t" \
  "v_accvgpr_write_b32 a52, v193\n\t" \
  "v_accvgpr_read_b32 v193, a53\n\t" \
  "v_mul_f32 v193, v197, v193\n\t" \
  "v_accvgpr_write_b32 a53, v193\n\t" \
  "v_accvgpr_read_b32 v193, a54\n\t" \
  "v_mul_f32 v193, v197, v193\n\t" \
  "v_accvgpr_write_b32 a54, v193\n\t" \
  "v_accvgpr_read_b32 v193, a55\n\t" \
  "v_mul_f32 v193, v197, v193\n\t" \
  "v_accvgpr_write_b32 a55, v193\n\t" \
  "v_accvgpr_read_b32 v193, a56\n\t" \
  "v_mul_f32 v193, v197, v193\n\t" \
  "v_accvgpr_write_b32 a56, v193\n\t" \
  "v_accvgpr_read_b32 v193, a57\n\t" \
  "v_mul_f32 v193, v197, v193\n\t" \
  "v_accvgpr_write_b32 a57, v193\n\t" \
  "v_accvgpr_read_b32 v193, a58\n\t" \
  "v_mul_f32 v193, v197, v193\n\t" \
  "v_accvgpr_write_b32 a58, v193\n\t" \
  "v_accvgpr_read_b32 v193, a59\n\t" \
  "v_mul_f32 v193, v197, v193\n\t" \
  "v_accvgpr_write_b32 a59, v193\n\t" \
  "v_accvgpr_read_b32 v193, a60\n\t" \
  "v_mul_f32 v193, v197, v193\n\t" \
  "v_accvgpr_write_b32 a60, v193\n\t" \
  "v_accvgpr_read_b32 v193, a61\n\t" \
  "v_mul_f32 v193, v197, v193\n\t" \
  "v_accvgpr_write_b32 a61, v193\n\t" \
  "v_accvgpr_read_b32 v193, a62\n\t" \
  "v_mul_f32 v193, v197, v193\n\t" \
  "v_accvgpr_write_b32 a62, v193\n\t" \
  "v_accvgpr_read_b32 v193, a63\n\t" \
  "v_mul_f32 v193, v197, v193\n\t" \
  "v_accvgpr_write_b32 a63, v193\n\t" \
  "v_accvgpr_read_b32 v193, a64\n\t" \
  "v_mul_f32 v193, v198, v193\n\t" \
  "v_accvgpr_write_b32 a64, v193\n\t" \
  "v_accvgpr_read_b32 v193, a65\n\t" \
  "v_mul_f32 v193, v198, v193\n\t" \
  "v_accvgpr_write_b32 a65, v193\n\t" \
  "v_accvgpr_read_b32 v193, a66\n\t" \
  "v_mul_f32 v193, v198, v193\n\t" \
  "v_accvgpr_write_b32 a66, v193\n\t" \
  "v_accvgpr_read_b32 v193, a67\n\t" \
  "v_mul_f32 v193, v198, v193\n\t" \
  "v_accvgpr_write_b32 a67, v193\n\t" \
  "v_accvgpr_read_b32 v193, a68\n\t" \
  "v_mul_f32 v193, v198, v193\n\t" \
  "v_accvgpr_write_b32 a68, v193\n\t" \
  "v_accvgpr_read_b32 v193, a69\n\t" \
  "v_mul_f32 v193, v198, v193\n\t" \
  "v_accvgpr_write_b32 a69, v193\n\t" \
  "v_accvgpr_read_b32 v193, a70\n\t" \
  "v_mul_f32 v193, v198, v193\n\t" \
  "v_accvgpr_write_b32 a70, v193\n\t" \
  "v_accvgpr_read_b32 v193, a71\n\t" \
  "v_mul_f32 v193, v198, v193\n\t" \
  "v_accvgpr_write_b32 a71, v193\n\t" \
  "v_accvgpr_read_b32 v193, a72\n\t" \
  "v_mul_f32 v193, v198, v193\n\t" \
  "v_accvgpr_write_b32 a72, v193\n\t" \
  "v_accvgpr_read_b32 v193, a73\n\t" \
  "v_mul_f32 v193, v198, v193\n\t" \
  "v_accvgpr_write_b32 a73, v193\n\t" \
  "v_accvgpr_read_b32 v193, a74\n\t" \
  "v_mul_f32 v193, v198, v193\n\t" \
  "v_accvgpr_write_b32 a74, v193\n\t" \
  "v_accvgpr_read_b32 v193, a75\n\t" \
  "v_mul_f32 v193, v198, v193\n\t" \
  "v_accvgpr_write_b32 a75, v193\n\t" \
  "v_accvgpr_read_b32 v193, a76\n\t" \
  "v_mul_f32 v193, v198, v193\n\t" \
  "v_accvgpr_write_b32 a76, v193\n\t" \
  "v_accvgpr_read_b32 v193, a77\n\t" \
  "v_mul_f32 v193, v198, v193\n\t" \
  "v_accvgpr_write_b32 a77, v193\n\t" \
  "v_accvgpr_read_b32 v193, a78\n\t" \
  "v_mul_f32 v193, v198, v193\n\t" \
  "v_accvgpr_write_b32 a78, v193\n\t" \
  "v_accvgpr_read_b32 v193, a79\n\t" \
  "v_mul_f32 v193, v198, v193\n\t" \
  "v_accvgpr_write_b32 a79, v193\n\t" \
  "v_accvgpr_read_b32 v193, a80\n\t" \
  "v_mul_f32 v193, v198, v193\n\t" \
  "v_accvgpr_write_b32 a80, v193\n\t" \
  "v_accvgpr_read_b32 v193, a81\n\t" \
  "v_mul_f32 v193, v198, v193\n\t" \
  "v_accvgpr_write_b32 a81, v193\n\t" \
  "v_accvgpr_read_b32 v193, a82\n\t" \
  "v_mul_f32 v193, v198, v193\n\t" \
  "v_accvgpr_write_b32 a82, v193\n\t" \
  "v_accvgpr_read_b32 v193, a83\n\t" \
  "v_mul_f32 v193, v198, v193\n\t" \
  "v_accvgpr_write_b32 a83, v193\n\t" \
  "v_accvgpr_read_b32 v193, a84\n\t" \
  "v_mul_f32 v193, v198, v193\n\t" \
  "v_accvgpr_write_b32 a84, v193\n\t" \
  "v_accvgpr_read_b32 v193, a85\n\t" \
  "v_mul_f32 v193, v198, v193\n\t" \
  "v_accvgpr_write_b32 a85, v193\n\t" \
  "v_accvgpr_read_b32 v193, a86\n\t" \
  "v_mul_f32 v193, v198, v193\n\t" \
  "v_accvgpr_write_b32 a86, v193\n\t" \
  "v_accvgpr_read_b32 v193, a87\n\t" \
  "v_mul_f32 v193, v198, v193\n\t" \
  "v_accvgpr_write_b32 a87, v193\n\t" \
  "v_accvgpr_read_b32 v193, a88\n\t" \
  "v_mul_f32 v193, v198, v193\n\t" \
  "v_accvgpr_write_b32 a88, v193\n\t" \
  "v_accvgpr_read_b32 v193, a89\n\t" \
  "v_mul_f32 v193, v198, v193\n\t" \
  "v_accvgpr_write_b32 a89, v193\n\t" \
  "v_accvgpr_read_b32 v193, a90\n\t" \
  "v_mul_f32 v193, v198, v193\n\t" \
  "v_accvgpr_write_b32 a90, v193\n\t" \
  "v_accvgpr_read_b32 v193, a91\n\t" \
  "v_mul_f32 v193, v198, v193\n\t" \
  "v_accvgpr_write_b32 a91, v193\n\t" \
  "v_accvgpr_read_b32 v193, a92\n\t" \
  "v_mul_f32 v193, v198, v193\n\t" \
  "v_accvgpr_write_b32 a92, v193\n\t" \
  "v_accvgpr_read_b32 v193, a93\n\t" \
  "v_mul_f32 v193, v198, v193\n\t" \
  "v_accvgpr_write_b32 a93, v193\n\t" \
  "v_accvgpr_read_b32 v193, a94\n\t" \
  "v_mul_f32 v193, v198, v193\n\t" \
  "v_accvgpr_write_b32 a94, v193\n\t" \
  "v_accvgpr_read_b32 v193, a95\n\t" \
  "v_mul_f32 v193, v198, v193\n\t" \
  "v_accvgpr_write_b32 a95, v193\n\t" \
  "v_accvgpr_read_b32 v193, a96\n\t" \
  "v_mul_f32 v193, v199, v193\n\t" \
  "v_accvgpr_write_b32 a96, v193\n\t" \
  "v_accvgpr_read_b32 v193, a97\n\t" \
  "v_mul_f32 v193, v199, v193\n\t" \
  "v_accvgpr_write_b32 a97, v193\n\t" \
  "v_accvgpr_read_b32 v193, a98\n\t" \
  "v_mul_f32 v193, v199, v193\n\t" \
  "v_accvgpr_write_b32 a98, v193\n\t" \
  "v_accvgpr_read_b32 v193, a99\n\t" \
  "v_mul_f32 v193, v199, v193\n\t" \
  "v_accvgpr_write_b32 a99, v193\n\t" \
  "v_accvgpr_read_b32 v193, a100\n\t" \
  "v_mul_f32 v193, v199, v193\n\t" \
  "v_accvgpr_write_b32 a100, v193\n\t" \
  "v_accvgpr_read_b32 v193, a101\n\t" \
  "v_mul_f32 v193, v199, v193\n\t" \
  "v_accvgpr_write_b32 a101, v193\n\t" \
  "v_accvgpr_read_b32 v193, a102\n\t" \
  "v_mul_f32 v193, v199, v193\n\t" \
  "v_accvgpr_write_b32 a102, v193\n\t" \
  "v_accvgpr_read_b32 v193, a103\n\t" \
  "v_mul_f32 v193, v199, v193\n\t" \
  "v_accvgpr_write_b32 a103, v193\n\t" \
  "v_accvgpr_read_b32 v193, a104\n\t" \
  "v_mul_f32 v193, v199, v193\n\t" \
  "v_accvgpr_write_b32 a104, v193\n\t" \
  "v_accvgpr_read_b32 v193, a105\n\t" \
  "v_mul_f32 v193, v199, v193\n\t" \
  "v_accvgpr_write_b32 a105, v193\n\t" \
  "v_accvgpr_read_b32 v193, a106\n\t" \
  "v_mul_f32 v193, v199, v193\n\t" \
  "v_accvgpr_write_b32 a106, v193\n\t" \
  "v_accvgpr_read_b32 v193, a107\n\t" \
  "v_mul_f32 v193, v199, v193\n\t" \
  "v_accvgpr_write_b32 a107, v193\n\t" \
  "v_accvgpr_read_b32 v193, a108\n\t" \
  "v_mul_f32 v193, v199, v193\n\t" \
  "v_accvgpr_write_b32 a108, v193\n\t" \
  "v_accvgpr_read_b32 v193, a109\n\t" \
  "v_mul_f32 v193, v199, v193\n\t" \
  "v_accvgpr_write_b32 a109, v193\n\t" \
  "v_accvgpr_read_b32 v193, a110\n\t" \
  "v_mul_f32 v193, v199, v193\n\t" \
  "v_accvgpr_write_b32 a110, v193\n\t" \
  "v_accvgpr_read_b32 v193, a111\n\t" \
  "v_mul_f32 v193, v199, v193\n\t" \
  "v_accvgpr_write_b32 a111, v193\n\t" \
  "v_accvgpr_read_b32 v193, a112\n\t" \
  "v_mul_f32 v193, v199, v193\n\t" \
  "v_accvgpr_write_b32 a112, v193\n\t" \
  "v_accvgpr_read_b32 v193, a113\n\t" \
  "v_mul_f32 v193, v199, v193\n\t" \
  "v_accvgpr_write_b32 a113, v193\n\t" \
  "v_accvgpr_read_b32 v193, a114\n\t" \
  "v_mul_f32 v193, v199, v193\n\t" \
  "v_accvgpr_write_b32 a114, v193\n\t" \
  "v_accvgpr_read_b32 v193, a115\n\t" \
  "v_mul_f32 v193, v199, v193\n\t" \
  "v_accvgpr_write_b32 a115, v193\n\t" \
  "v_accvgpr_read_b32 v193, a116\n\t" \
  "v_mul_f32 v193, v199, v193\n\t" \
  "v_accvgpr_write_b32 a116, v193\n\t" \
  "v_accvgpr_read_b32 v193, a117\n\t" \
  "v_mul_f32 v193, v199, v193\n\t" \
  "v_accvgpr_write_b32 a117, v193\n\t" \
  "v_accvgpr_read_b32 v193, a118\n\t" \
  "v_mul_f32 v193, v199, v193\n\t" \
  "v_accvgpr_write_b32 a118, v193\n\t" \
  "v_accvgpr_read_b32 v193, a119\n\t" \
  "v_mul_f32 v193, v199, v193\n\t" \
  "v_accvgpr_write_b32 a119, v193\n\t" \
  "v_accvgpr_read_b32 v193, a120\n\t" \
  "v_mul_f32 v193, v199, v193\n\t" \
  "v_accvgpr_write_b32 a120, v193\n\t" \
  "v_accvgpr_read_b32 v193, a121\n\t" \
  "v_mul_f32 v193, v199, v193\n\t" \
  "v_accvgpr_write_b32 a121, v193\n\t" \
  "v_accvgpr_read_b32 v193, a122\n\t" \
  "v_mul_f32 v193, v199, v193\n\t" \
  "v_accvgpr_write_b32 a122, v193\n\t" \
  "v_accvgpr_read_b32 v193, a123\n\t" \
  "v_mul_f32 v193, v199, v193\n\t" \
  "v_accvgpr_write_b32 a123, v193\n\t" \
  "v_accvgpr_read_b32 v193, a124\n\t" \
  "v_mul_f32 v193, v199, v193\n\t" \
  "v_accvgpr_write_b32 a124, v193\n\t" \
  "v_accvgpr_read_b32 v193, a125\n\t" \
  "v_mul_f32 v193, v199, v193\n\t" \
  "v_accvgpr_write_b32 a125, v193\n\t" \
  "v_accvgpr_read_b32 v193, a126\n\t" \
  "v_mul_f32 v193, v199, v193\n\t" \
  "v_accvgpr_write_b32 a126, v193\n\t" \
  "v_accvgpr_read_b32 v193, a127\n\t" \
  "v_mul_f32 v193, v199, v193\n\t" \
  "v_accvgpr_write_b32 a127, v193\n\t" \
  "v_mul_f32 v160, v196, v160\n\t" \
  "v_mul_f32 v161, v196, v161\n\t" \
  "v_mul_f32 v162, v196, v162\n\t" \
  "v_mul_f32 v163, v196, v163\n\t" \
  "v_mov_b32 v196, 1.0\n\t" \
  "v_mul_f32 v164, v197, v164\n\t" \
  "v_mul_f32 v165, v197, v165\n\t" \
  "v_mul_f32 v166, v197, v166\n\t" \
  "v_mul_f32 v167, v197, v167\n\t" \
  "v_mov_b32 v197, 1.0\n\t" \
  "v_mul_f32 v168, v198, v168\n\t" \
  "v_mul_f32 v169, v198, v169\n\t" \
  "v_mul_f32 v170, v198, v170\n\t" \
  "v_mul_f32 v171, v198, v171\n\t" \
  "v_mov_b32 v198, 1.0\n\t" \
  "v_mul_f32 v172, v199, v172\n\t" \
  "v_mul_f32 v173, v199, v173\n\t" \
  "v_mul_f32 v174, v199, v174\n\t" \
  "v_mul_f32 v175, v199, v175\n\t" \
  "v_mov_b32 v199, 1.0\n\t" \
  "s_mov_b32 s99, 0\n\t" \
  "s_nop 1\n\t" \
  "s_cmp_eq_u32 vcc_lo, 0\n\t" \
  "s_cbranch_scc1 .Lsf_q0\n\t" \
  "s_cmp_eq_u32 vcc_lo, 1\n\t" \
  "s_cbranch_scc1 .Lsf_q1\n\t" \
  "s_cmp_eq_u32 vcc_lo, 2\n\t" \
  "s_cbranch_scc1 .Lsf_q2\n\t" \
  "s_cmp_eq_u32 vcc_lo, 3\n\t" \
  "s_cbranch_scc1 .Lsf_q3\n\t" \
  "s_cmp_eq_u32 vcc_lo, 4\n\t" \
  "s_cbranch_scc1 .Lsf_q4\n\t" \
  "s_cmp_eq_u32 vcc_lo, 5\n\t" \
  "s_cbranch_scc1 .Lsf_q5\n\t" \
  "s_cmp_eq_u32 vcc_lo, 6\n\t" \
  "s_cbranch_scc1 .Lsf_q6\n\t" \
  "s_cmp_eq_u32 vcc_lo, 7\n\t" \
  "s_cbranch_scc1 .Lsf_q7\n\t" \
  "s_endpgm\n\t" \
  ".Lsf_v:\n\t" \
  "s_mov_b32 s98, 0\n\t" \
  ".Lsf_v1:\n\t" \
  "s_sleep 4\n\t" \
  "buffer_load_dword v209, v250, %[ws], 0 offen sc1\n\t" \
  "s_waitcnt vmcnt(0)\n\t" \
  "v_and_b32 v251, 0xbfffbfff, v209\n\t" \
  "v_cmp_ne_u32_e64 s[84:85], %[tag], v251\n\t" \
  "s_cmp_lg_u64 s[84:85], 0\n\t" \
  "s_cbranch_scc0 .Lsf_w\n\t" \
  "s_add_u32 s98, s98, 1\n\t" \
  "s_cmp_lt_u32 s98, %[cap]\n\t" \
  "s_cbranch_scc1 .Lsf_v1\n\t" \
  "v_mov_b32 v251, %[tag]\n\t" \
  "buffer_store_dword v251, off, %[ws], 0 offset:256 sc1\n\t" \
  "s_branch .Lsf_w\n\t" \
  ".subsection 0\n\t" \
  ".Lop_end:\n\t" \
  ""
#define OP_GA0 "=&{v[160:163]}"
#define OP_GA1 "=&{v[164:167]}"
#define OP_CW "=&{s82}"
#define OP_BB "=&{s83}"
#define AO_CLOB "v128", "v129", "v130", "v131", "v132", "v133", "v134", "v135", "v136", "v137", "v138", "v139", "v140", "v141", "v142", "v143", "v144", "v145", "v146", "v147", "v148", "v149", "v150", "v151", "v152", "v153", "v154", "v155", "v156", "v157", "v158", "v159", "v168", "v169", "v170", "v171", "v172", "v173", "v174", "v175", "v208", "v209", "v210", "v211", "v212", "v213", "v214", "v215", "v216", "v217", "v218", "v219", "v234", "v235", "v236", "v237", "v238", "v239", "v242", "v243", "v248", "v249", "v250", "v251", "v252", "v254", "s76", "s77", "s78", "s79", "s80", "s81", "s84", "s85", "s86", "s87", "s88", "s89", "s90", "s91", "s92", "s93", "s94", "s95", "s96", "s97", "s98", "s99", "vcc", "scc", "memory"
#define OP_DRD "s_waitcnt vmcnt(5)\n\t" "ds_read2st64_b64 v[32:35], %1 offset0:64 offset1:65\n\t" "ds_read2st64_b64 v[36:39], %1 offset0:66 offset1:67\n\t" "ds_read2st64_b64 v[40:43], %1 offset0:68 offset1:69\n\t" "ds_read2st64_b64 v[44:47], %1 offset0:70 offset1:71\n\t" "ds_read2st64_b64 v[48:51], %1 offset0:96 offset1:97\n\t" "ds_read2st64_b64 v[52:55], %1 offset0:98 offset1:99\n\t" "ds_read2st64_b64 v[56:59], %1 offset0:100 offset1:101\n\t" "ds_read2st64_b64 v[60:63], %1 offset0:102 offset1:103\n\t" "s_waitcnt lgkmcnt(0)"
#define ST_ASM "s_nop 1\n\t" "v_mfma_f32_16x16x32_bf16 v[80:83], a[176:179], v[160:163], 0\n\t" "v_mfma_f32_16x16x32_bf16 v[80:83], a[180:183], v[164:167], v[80:83]\n\t" "v_mfma_f32_16x16x32_bf16 v[80:83], a[184:187], v[168:171], v[80:83]\n\t" "v_mfma_f32_16x16x32_bf16 v[80:83], a[188:191], v[172:175], v[80:83]\n\t" "v_dot2_f32_bf16 v64, v160, v160, 0\n\t" "v_dot2_f32_bf16 v65, v164, v164, 0\n\t" "v_dot2_f32_bf16 v66, v168, v168, 0\n\t" "v_dot2_f32_bf16 v67, v172, v172, 0\n\t" "v_dot2_f32_bf16 v64, v161, v161, v64\n\t" "v_dot2_f32_bf16 v65, v165, v165, v65\n\t" "v_dot2_f32_bf16 v66, v169, v169, v66\n\t" "v_dot2_f32_bf16 v67, v173, v173, v67\n\t" "v_dot2_f32_bf16 v64, v162, v162, v64\n\t" "v_dot2_f32_bf16 v65, v166, v166, v65\n\t" "v_dot2_f32_bf16 v66, v170, v170, v66\n\t" "v_dot2_f32_bf16 v67, v174, v174, v67\n\t" "v_dot2_f32_bf16 v64, v163, v163, v64\n\t" "v_dot2_f32_bf16 v65, v167, v167, v65\n\t" "v_dot2_f32_bf16 v66, v171, v171, v66\n\t" "v_dot2_f32_bf16 v67, v175, v175, v67\n\t" "s_nop 2\n\t"
static_assert(W_MODB == 2228224 && W_VOTE == 8192);
struct WoP { srd_t srd; unsigned wv[2], m, hl, base, wa[3][2]; };
struct W4C {
  KVP kv;
  unsigned pf1, pf2, pf4, cto, po, px, vo, eso, hl, hl18, frb, xcb, xso, ss, mbp, gvo, l16, gvm, vs, c3;
  WoP wo;
  srd_t sG, sU, sD, sX, sR, sM;
  unsigned gv, wa0, fw2, tagv, fbv, tlb, pre, otb; u32x32 up;
};
struct Ptrs { const u16 *wo, *w_r, *wg, *wu, *wd; };
DI void attn_setup(const Ctx& k, W4C& c, const u16* kc, const u16* vc, const u16* pm, srd_t srdx, const Ptrs& a) {
  const unsigned wid = k.wid, fb = k.xcd * 28 + ffn_j(k);
  c.kv.sofk = KRS * 16u * wid;
  const size_t kvoff = ((size_t)k.kvh * SKV + (SKV - WIN)) * DH;
  c.kv.srdK = mk_srd(kc + kvoff, WIN * DH * 2); c.kv.srdV = mk_srd(vc + kvoff, WIN * DH * 2);
  const unsigned go = wid * 256 * FF + fb * 16, nG = (DM * FF - go) * 2;
  c.sG = mk_srd(a.wg + go, nG); c.sU = mk_srd(a.wu + go, nG);
  c.sM = mk_srd(pm + 2048 + k.xcd * 512, DM * 6 * DM * 2 - 4096 - k.xcd * 1024);
  c.wo.srd = mk_srd(a.wo, QD * DM * 2); c.sX = srdx;
  c.sD = mk_srd(a.wd + (size_t)fb * 16 * DM, ffn_idle(k) ? 0u : 32768u); c.sR = mk_srd(a.w_r, DM * 2);
  asm volatile("" : "+s"(c.kv.srdK), "+s"(c.kv.srdV), "+s"(c.sG), "+s"(c.sU), "+s"(c.sM), "+s"(c.wo.srd), "+s"(c.sD), "+s"(c.sR));
  c.wo.m = wid * 4096; c.wo.base = (k.kvh * 1024 + wid * 16) * (DM * 2) + k.hl * 256;
  c.pf1 = (k.kvh * 1024 + k.j * 32 + wid * 8) * (DM * 2) + (k.xcd & 1) * 8192;
  c.pf4 = k.j * (8 * FF * 2) - ffn_j(k) * 32;
}
constexpr unsigned SCR = 9 * UNIT;
#define STAMPF(I) do { } while (0)
#define FQ_STR2(x) #x
#define FQ_STR(x) FQ_STR2(x)
#define FQ_CAP FQ_STR(SPINCAP)
DI bool tags2(const u32x4& a, unsigned tag) { return (a[1] == tag) & (a[3] == tag); }
DI void bcast4(float v, float (&o)[4]) {
  auto a = __builtin_amdgcn_permlane32_swap(__float_as_uint(v), __float_as_uint(v), false, false);
  auto b = __builtin_amdgcn_permlane16_swap(a[0], a[0], false, false);
  auto c = __builtin_amdgcn_permlane16_swap(a[1], a[1], false, false);
  o[0] = __uint_as_float(b[0]); o[1] = __uint_as_float(b[1]); o[2] = __uint_as_float(c[0]); o[3] = __uint_as_float(c[1]);
}
DI void x_raw2(unsigned m0, srd_t srd, unsigned v0, unsigned v1, unsigned so) {
  asm volatile("s_mov_b32 m0, %0\n\ts_nop 0\n\tbuffer_load_dwordx4 %1, %3, %4 offen lds\n\tbuffer_load_dwordx4 %2, %3, %4 offen offset:1024 lds"
               :: "s"(m0), "v"(v0), "v"(v1), "s"(srd), "s"(so) : "memory");
}
DI void w_touch2(H4& g, const u16* wq, unsigned base, unsigned t) {
  touch(g.pfd, wq, base + ((g.rot + t) & 15u) * kWT);
}
constexpr unsigned TB0 = 6, TBN = 6;
#define SE 0
#define F1 0
#define FT 0
#define FQ_XD 0
#define MK 0
#define PGA 0
#define WT 0
#define LT 0
#define EL 0
#define PT0 0
#define XE 0
#define PPOS 2
DI void front(Ctx& k, H4& g, W4C& c STAMP_ARG) {
  argp_t ap = args();
  const u16 *px = ap->x, *kc = ap->kc, *vc = ap->vc, *wqp = ap->wq, *pc_ = ap->c, *pm = ap->w_mod; char* pw = ap->ws;
  GAP();
  const unsigned lane = lane_id(), wid = k.wid, tid = wid * 64 + lane;
  g.pfd = 0;
  {
    unsigned long long pc; unsigned len;
    asm volatile("s_getpc_b64 %0\n.Lfz_a:\n\ts_mov_b32 %1, .Lfz_z-.Lfz_a-4" : "=s"(pc), "=s"(len));
    touch(g.pfd, (const void*)pc, min(((lane * 4 + wid) * 32 + k.j) * 128, len));
  }
  GAP();
  k.ws = mk_srd((char*)(((uintptr_t)pw + 0x1fffff) & ~(uintptr_t)0x1fffff), W_END);
  const srd_t srdx = mk_srd(px, S * DM * 2), srdm = mk_srd(pm, DM * 6 * DM * 2), srdc = mk_srd(pc_, DM * 2);
  asm volatile("" : "+s"(kc), "+s"(vc));
  const unsigned head = k.kvh * 8 + k.hl, row64 = k.rblk * 256 + k.kvh * 64, rot = k.kvh * 4, u0 = k.hl >> 1, kt = rot + ((u0 + wid) & 3), k0 = k.hl & 1, cq = k.kvh ^ 3;
  g.srdW = mk_srd(wqp + head * DH, (DM * QD - head * DH) * 2); g.rot = rot;
  const unsigned st1 = W_ST1 + (k.rblk << 12);
  u32x4 xs[4], wm[4];
  const unsigned cbk = (k.xcd & 1) * 32 + k.j, kp = k.kvh;
  u32x2 cv = ald8<0>(srdc, (tid >> 2) * 8, kp * 512);
  { const unsigned vo = (tid >> 2) * (4 * 6 * DM * 2) + (tid & 3) * 16, so = kp * (256 * 6 * DM * 2) + cbk * 64;
#pragma unroll
    for (int i = 0; i < 4; ++i) wm[i] = ald16<0>(srdm, vo, so + i * (6 * DM * 2)); }
  unsigned E = ald4<0, true>(k.ws, EL == 0 || lane == 0 ? W_EPOCH : lane == 1 ? W_HT + ((k.rblk & 3) << 20) : lane == 2 ? st1 + (k.kvh * 8 + k.hl) * 128 : W_FLAGH + (k.rblk * 4 + wid) * 128, 0u);
  { const unsigned so = (row64 + k.hl * 8 + 2 * wid) * (DM * 2);
    xs[0] = ald16<0>(srdx, lane * 16, so); xs[1] = ald16<1024>(srdx, lane * 16, so); xs[2] = ald16<2048>(srdx, lane * 16, so); xs[3] = ald16<3072>(srdx, lane * 16, so); }
  unsigned vx = (lane >> 3) * (DM * 2) + (((lane & 7) ^ (lane >> 4)) << 4), vx1 = (vx ^ 64) + 15360, sl[4];
  g.mX = k.ldsb + wid * 8192;
#pragma unroll
  for (int u = 0; u < 4; ++u) sl[u] = (k.rblk * 256 + wid * 16) * (DM * 2) + (rot + ((u0 + u) & 3)) * 128;
#define XRAW(u, rb) x_raw2(g.mX + (u) * GSLOT + (rb) * 2048, srdx, vx, vx1, sl[u] + (((rb) ^ cq) << 17))
  const unsigned r0 = XE ? 2 * k0 : 0;
  XRAW(0, r0); XRAW(0, r0 + 1);
  const unsigned tw2 = (k.j * 2 + (wid >> 1)) * (QD * 2) + k.kvh * 2048 + (lane & 7) * 256 + (wid & 1) * 128;
  if (!(LT & 1)) w_touch2(g, wqp, tw2, (lane >> 3) & 3);
  constexpr int NL = (LT & 1) + 2 * (LT & 2);
  GAP();
  g.mW = k.ldsb + 32768 + wid * 4096;
#define X01() do { XRAW(0, r0 ^ 2); XRAW(0, r0 ^ 3); XRAW(1, 0); XRAW(1, 1); XRAW(1, 2); XRAW(1, 3); } while (0)
#define X2() do { XRAW(2, 0); XRAW(2, 1); XRAW(2, 2); XRAW(2, 3); } while (0)
  const unsigned tb = k.ldsb + SCR + 1024;
  u32x16 wbs; u32x8 vw, ma; u32x4 mb; const u16* kvt; unsigned pso, fw, w2, w3, gso, gpo;
  auto setup_w = [&]() {
    w_voffs(g.voffW, QD, wid, lane, k0);
    gso = st1 + (((lane >> 4) ^ cq) * 64 + wid * 16 + (lane & 15)) * 16; gpo = W_MODA + ((lane >> 5) * 1024 + kt * 64 + (lane & 31) * 2) * 8;
    asm volatile("" : W4(g.voffW), "+v"(gso), "+v"(gpo));
    GAP();
  };
  auto setup_b = [&]() {
    g.soX = W_HT + ((k.rblk & 3) << 20) + (((k.xcd ^ wid) & 1) << 13) + wid * 8192; g.fo = W_FLAGH + (k.rblk * 4 + wid) * 128;
    h4_init_addr(g, k.ldsb, wid, lane, k0);
    const unsigned xr = g.mX + (lane & 15) * 128 + (((4 * k0 + (lane >> 4)) ^ ((lane >> 1) & 7)) << 4);
    vw = u32x8{g.voffW[0], g.voffW[1], g.voffW[2], g.voffW[3], xr, xr ^ 64, xr + 2 * GSLOT, (xr ^ 64) + 2 * GSLOT};
#pragma unroll
    for (int i = 0; i < 8; ++i) { wbs[i] = g.wb[0][i]; wbs[8 + i] = g.wb[1][i]; }
    ma = u32x8{tb + (lane >> 4) * 32, ((tid >> 3) + (k.j >> 3) * 32) * (8 * 6 * DM * 2) + (lane & 7) * 16 + k.hl * 128, (lane & 31) * 4, 0u, vx, vx1, g.xb1,
               WT == 1 ? tw2 + ((rot + 4 + (k.xcd & 1) + 2 * min(lane >> 3, 5u)) & 15u) * kWT : WT == 2 ? tw2 + ((rot + 4 + (k.xcd & 1) + 2 * min(lane >> 3, 2u)) & 15u) * kWT
                       : tw2 - (lane & 7) * 256 + (1 + 4 * (lane & 1)) * 256 + ((rot + 4 + min(lane >> 1, 11u)) & 15u) * kWT};
    mb = u32x4{WT == 2 ? tw2 + ((rot + 10 + (k.xcd & 1) + 2 * min(lane >> 3, 2u)) & 15u) * kWT : (tw2 & ~(((lane & 3) == 1) ? 256u : 0u)) + ((rot + TB0 + min(lane >> 3, TBN - 1u)) & 15u) * kWT, ((k.j & 15) * 64 + wid * 16 + (lane & 15)) * 128, 0u, k.ldsb + SCR + 4096 + tid * 4};
    kvt = ((k.j & 16) ? vc : kc) + ((size_t)k.kvh * SKV + (SKV - WIN)) * DH;
    pso = g.soX + (rot + u0) * kXT + cq * 256 - GSLOT - k.ldsb; fw = g.fo + head * 4; w2 = (rot + ((u0 + 2) & 3)) * kWT; w3 = (rot + ((u0 + 3) & 3)) * kWT;
    asm volatile("" : "+{v[184:199]}"(wbs), "+{v[200:207]}"(vw), "+{v[208:215]}"(ma), "+{v[216:219]}"(mb), "+s"(kvt), "+s"(pso), "+s"(fw), "+s"(w2), "+s"(w3));
    GAP();
  };
  Ptrs pa;
  if constexpr (SE & 1) { argp_t aq = args(); pa = Ptrs{aq->wo, aq->w_r, aq->wg, aq->wu, aq->wd}; }
  if constexpr (SE & 2) { setup_w(); setup_b(); }
  if constexpr (SE & 1) { attn_setup(k, c, kc, vc, pm, srdx, pa); GAP(); }
  unsigned tag;
  HOP_GO(2, wid);
  asm volatile("s_waitcnt vmcnt(%1)" : "+v"(cv) : "i"(14 + PT0 - NL) : "memory");
  float sc[4];
#pragma unroll
  for (int i = 0; i < 4; ++i) sc[i] = silu_bf((i & 1) ? hi16f(cv[i >> 1]) : lo16f(cv[i >> 1]));
  asm volatile("" : W4(sc));
  GAP();
  STAMPF(27);
  asm volatile("s_waitcnt vmcnt(%4)" : W4(wm) : "i"(10 + PT0 - NL) : "memory");
  STAMP(1);
  if (LT & 1) w_touch2(g, wqp, tw2, (lane >> 3) & 3);
  GAP();
  {
    f32x2 a[4] = {};
#pragma unroll
    for (int i = 0; i < 4; ++i)
#pragma unroll
      for (int e = 0; e < 4; ++e) a[e] = __builtin_elementwise_fma(f32x2{sc[i], sc[i]}, up2(wm[i][e]), a[e]);
    const float b0 = fold32(a[0][0], a[2][0]), b1 = fold32(a[0][1], a[2][1]), b2 = fold32(a[1][0], a[3][0]), b3 = fold32(a[1][1], a[3][1]);
    float c0 = fold16(b0, b2), c1 = fold16(b1, b3);
    c0 += dpp_f<0x128>(c0); c1 += dpp_f<0x128>(c1);
    c0 += dpp_f<0x124>(c0); c1 += dpp_f<0x124>(c1);
    const unsigned p = 4 * (lane & 3) + 2 * (lane >> 5) + ((lane >> 4) & 1);
    if (!(lane & 12)) lds_st(k.ldsb + SCR + wid * 256 + p * 8, f32x2{c0, c1});
    asm volatile("s_waitcnt vmcnt(%1) lgkmcnt(0)\n\ts_barrier" : "+v"(E) : "i"(9 + PT0) : "memory");
    tag = ((rfl(E) | TAGM) + TAGC) & ~TAGM;
    k.tag = tag;
    if (wid == 0 && lane < 16) {
      const unsigned a0 = k.ldsb + SCR + lane * 8;
      const f32x2 s = (lds_ld<f32x2>(a0) + lds_ld<f32x2>(a0 + 256)) + (lds_ld<f32x2>(a0 + 512) + lds_ld<f32x2>(a0 + 768));
      st16(k.ws, W_MODA + (kp * 2048 + cbk * 32 + lane * 2) * 8, u32x4{__float_as_uint(s[0]), tag, __float_as_uint(s[1]), tag});
    }
  }
  GAP();
  asm volatile("s_waitcnt vmcnt(%5)\n\ts_cmp_eq_u32 %4, 0\n\ts_cbranch_scc1 .Lxs%=\n\ts_waitcnt vmcnt(%6)\n.Lxs%=:" : W4(xs) : "s"(wid), "i"(6 + PT0), "i"(5 + PT0) : "memory", "scc");
  STAMP(2);
  {
    float sm[2][2] = {}, sq[2][2] = {};
#pragma unroll
    for (int e = 0; e < 4; ++e)
#pragma unroll
      for (int i = 0; i < 4; ++i) { sm[i >> 1][i & 1] = dot2(xs[i][e], 0x3f803f80u, sm[i >> 1][i & 1]); sq[i >> 1][i & 1] = dot2(xs[i][e], xs[i][e], sq[i >> 1][i & 1]); }
    const float z = sum_row16(fold16(fold32(sm[0][0] + sm[0][1], sm[1][0] + sm[1][1]), fold32(sq[0][0] + sq[0][1], sq[1][0] + sq[1][1]))) * (1.f / DM);
    auto zz = __builtin_amdgcn_permlane16_swap(__float_as_uint(z), __float_as_uint(z), false, false);
    const float mean = __uint_as_float(zz[0]);
    const float rstd = rsqrtf(fmaxf(fmaf(-mean, mean, __uint_as_float(zz[1])), 0.f) + 1e-5f);
    if ((lane & 31) == 0) st16(k.ws, st1 + (k.kvh * 64 + k.hl * 8 + 2 * wid + (lane >> 5)) * 16, u32x4{__float_as_uint(rstd), tag, __float_as_uint(-mean * rstd), tag});
  }
  GAP();
  STAMP(3);
  if constexpr (!(SE & 1)) { argp_t aq = args(); pa = Ptrs{aq->wo, aq->w_r, aq->wg, aq->wu, aq->wd}; }
  X01();
  unsigned cb;
  asm volatile("buffer_load_ushort %0, %1, %2, %3 offen" : "=v"(cb) : "v"(tid * 2), "s"(srdc), "s"((k.j >> 3) * 512) : "memory");
  if constexpr (!(SE & 2)) setup_w();
  w_dma4(g.mW, g.srdW, g.voffW, (rot + u0) * kWT);
#define W1() w_dma4(g.mW + GSLOT, g.srdW, g.voffW, (rot + ((u0 + 1) & 3)) * kWT)
#define W2() w_dma4(g.mW + 2 * GSLOT, g.srdW, g.voffW, (rot + ((u0 + 2) & 3)) * kWT)
  u32x4 st, p[4];
#define POLL() do { st = ald16<0, true>(k.ws, gso, 0u); _Pragma("unroll") for (int i = 0; i < 4; ++i) p[i] = ald16<0, true>(k.ws, gpo, i * 16384u); GAP(); } while (0)
  GAP();
  if constexpr (FQ_EB & 1) W1();
  if constexpr (PPOS == 0) POLL();
  if constexpr (!(SE & 2)) setup_b();
  if constexpr (FQ_EB & 2) X2();
  if constexpr (FQ_EB & 4) W2();
  if constexpr (PPOS == 2) POLL();
  if constexpr (FQ_EB & 8) W1();
  if constexpr (FQ_EB & 16) X2();
  if constexpr (FQ_EB & 32) W2();
  if constexpr (!(SE & 1)) attn_setup(k, c, kc, vc, pm, srdx, pa);
  GAP();
  ma[3] = tag;
  asm volatile("" : "+{v[208:215]}"(ma));
  GAP();
  STAMPF(28);
  if (PGA & 1) PAGE_ALIGN();
  HOP_GO(0, wid);
  if (PGA & 2) PAGE_ALIGN();
  HOP_FRONT();
  f32x16 rs;
  {
    asm volatile("s_waitcnt vmcnt(%7)" : W4(p), "+v"(st), "+v"(g.pfd), "+v"(cb) : "i"((FT & 1) + (FT >> 1) + ((FQ_EB & 8) ? 4 : 0) + ((FQ_EB & 16) ? 8 : 0) + ((FQ_EB & 32) ? 4 : 0)) : "memory");
    asm volatile("ds_read_b128 v[16:19], %4\n\tds_read_b128 v[48:51], %4 offset:2048\n\tds_read_b128 v[80:83], %4 offset:4096\n\tds_read_b128 v[112:115], %4 offset:6144\n\t"
                 "ds_read_b128 v[20:23], %5\n\tds_read_b128 v[52:55], %5 offset:2048\n\tds_read_b128 v[84:87], %5 offset:4096\n\tds_read_b128 v[116:119], %5 offset:6144"
                 : "=&{v[0:31]}"(g.q[0]), "=&{v[32:63]}"(g.q[1]), "=&{v[64:95]}"(g.q[2]), "=&{v[96:127]}"(g.q[3]) : "v"(vw[4]), "v"(vw[5]) : "memory");
    mb[2] = __float_as_uint(silu_bf(lo16f(cb)));
    asm volatile("" : "+{v[216:219]}"(mb));
    STAMPF(29);
    for (unsigned spins = 0;;) {
      if (__all(tags2(st, tag) & tags2(p[0], tag) & tags2(p[1], tag) & tags2(p[2], tag) & tags2(p[3], tag))) break;
      if (spin_fail<SL0>(spins, k.ws, tag)) break;
      st = ld16(k.ws, gso);
#pragma unroll
      for (int i = 0; i < 4; ++i) p[i] = ld16(k.ws, gpo + i * 16384);
    }
    STAMP(4);
    f32x2 m[4];
#pragma unroll
    for (int i = 0; i < 4; ++i) m[i] = f32x2{__uint_as_float(p[i][0]), __uint_as_float(p[i][2])};
    f32x2 s = (m[0] + m[1]) + (m[2] + m[3]);
    s = up2(cvtpk(s[0], s[1]));
    if (lane >= 32) { s += 1.f; s = up2(cvtpk(s[0], s[1])); }
    lds_st(tb + wid * 512 + (((lane ^ 32) * 8) ^ (k0 * 128)), s * QSCALE);
    GAP();
    float ra[4], rn[4];
    bcast4(__uint_as_float(st[0]), ra); bcast4(__uint_as_float(st[2]), rn);
    rs = f32x16{ra[0], ra[0], rn[0], rn[0], ra[1], ra[1], rn[1], rn[1], ra[2], ra[2], rn[2], rn[2], ra[3], ra[3], rn[3], rn[3]};
    asm volatile("" : "+{v[160:175]}"(rs));
    STAMPF(12);
    BARRIER_LGKM();
    STAMPF(13);
  }
  static_assert(W_XCH == W_OT + (16u << 20));
  const unsigned vp = k.xcd * 2 + ((k.j >> 1) & 1);
  asm volatile(FQ_ASM : "+{v[0:31]}"(g.q[0]), "+{v[32:63]}"(g.q[1]), "+{v[64:95]}"(g.q[2]), "+{v[96:127]}"(g.q[3]), [fl] "=&v"(g.fl), [pf] "+{v246}"(g.pfd), "+{v[200:207]}"(vw), "+{v[208:215]}"(ma), "+{v[216:219]}"(mb),
                 "=&{v220}"(c.kv.vkb[0]), "=&{v221}"(c.kv.vkb[1]), "=&{v222}"(c.kv.vkb[2]), "=&{v223}"(c.kv.vkb[3]), "=&{v224}"(c.kv.vksrc[0]), "=&{v225}"(c.kv.vksrc[1]), "=&{v226}"(c.kv.vksrc[2]), "=&{v227}"(c.kv.vksrc[3]),
                 "=&{v228}"(c.kv.vvb[0]), "=&{v229}"(c.kv.vvb[1]), "=&{v230}"(c.kv.vvsrc[0]), "=&{v231}"(c.kv.vvsrc[1]), "=&{v232}"(c.gv), "=&{v240}"(c.wo.wv[0]), "=&{v241}"(c.wo.wv[1]), "=&{v244}"(c.wa0),
                 "=&{v245}"(g.fvo), "=&{v247}"(c.tlb), "=&{v233}"(c.gvm), "+{v[160:175]}"(rs),
                 [hl] "=&s"(c.hl), [hl18] "=&s"(c.hl18), [cto] "=&s"(c.cto), [po] "=&s"(c.po), [px] "=&s"(c.px), [vo] "=&s"(c.vo), [eso] "=&s"(c.eso), [otb] "=&s"(c.otb),
                 [xcb] "=&s"(c.xcb), [frb] "=&s"(c.frb), [fw2] "=&s"(c.fw2), [xso] "=&s"(c.xso), [mbp] "=&s"(c.mbp) FQ_STV
               : "{v[184:199]}"(wbs), [sM] "s"(c.sM),
                 [ws] "s"(k.ws), [sw] "s"(g.srdW), [sx] "s"(srdx), [sK] "s"(c.kv.srdK), [sV] "s"(c.kv.srdV), [sG] "s"(c.sG), [sU] "s"(c.sU), [mw] "s"(g.mW), [mx] "s"(g.mX),
                 [rot19] "s"(rot << 19), [w2] "s"(w2), [w3] "s"(w3), [sox] "s"(g.soX - GSLOT - k.ldsb), [dk] "s"(4096u - k0 * 8192u), [fo] "s"(g.fo), [fw] "s"(fw), [pso] "s"(pso), [c17] "s"(cq << 17), [sl2] "s"(sl[2]), [w1] "s"((rot + ((u0 + 1) & 3)) * kWT),
                 [sl3] "s"(sl[3]), [wq] "s"(wqp), [kv] "s"(kvt), [sofk] "s"(c.wo.m), [pf4] "s"(c.pf4), [pf1] "s"(c.pf1), [wo] "s"(c.wo.srd),
                 [bid] "s"(k.bid), [wid] "s"(wid), [idm] "s"(ffn_idle(k) ? 0x80000000u : 0u), [prb] "s"(ffn_idle(k) && wid == 0 ? ((k.j & 1) ? W_OT + (vp << 21) : W_PROBE + ((vp & 1) << 13) + vp * 128) : 0x80000000u), [pto] "s"((blockIdx.x * 4 + wid) * 2048)
               : FQ_CL, ACLOB_LO, ACLOB_HI);
  GAP();
}
constexpr unsigned TABU = 9 * UNIT;
#define OAUX 17
DI int key_row(unsigned k) { return S - 1 - (int)(k & 0xffffu); }
DI float key_val(unsigned k) { unsigned b = k >> 16; b = (b & 0x8000u) ? (b & 0x7fffu) : (~b & 0xffffu); return bf2f((u16)b); }
template <int R> DI unsigned acc_readu() { unsigned r; asm volatile("v_accvgpr_read_b32 %0, a%c1" : "=v"(r) : "i"(R)); return r; }
template <int... Is> DI void gate_read(u32x32& g, ilist<Is...>) { ((g[Is] = acc_readu<224 + Is>()), ...); }
#define RC 16
#define TP 1
#define TA 0
#define TB 33
#define TE 0
#define OP_FX
DI void oproj(Ctx& k, H4& h, W4C& c STAMP_ARG) {
  const unsigned wid = k.wid, tag = k.tag, bid = k.bid, grp = k.rblk * 4 + k.kvh, cblk = k.hl;
  u32x32 UP; f32x4 ga0, ga1; float cww, bbw; unsigned na, nb;
  asm volatile(AT_ASM
               : "+{v[0:31]}"(h.q[0]), "+{v[32:63]}"(h.q[1]), "+{v[64:95]}"(h.q[2]), "+{v[96:127]}"(h.q[3]), "=&{v[176:207]}"(UP), OP_GA0(ga0), OP_GA1(ga1), OP_CW(cww), OP_BB(bbw),
                 "+{v220}"(c.kv.vkb[0]), "+{v221}"(c.kv.vkb[1]), "+{v222}"(c.kv.vkb[2]), "+{v223}"(c.kv.vkb[3]), "+{v224}"(c.kv.vksrc[0]), "+{v225}"(c.kv.vksrc[1]), "+{v226}"(c.kv.vksrc[2]), "+{v227}"(c.kv.vksrc[3]),
                 "+{v228}"(c.kv.vvb[0]), "+{v229}"(c.kv.vvb[1]), "+{v230}"(c.kv.vvsrc[0]), "+{v231}"(c.kv.vvsrc[1]), "+{v232}"(c.gv), "+{v233}"(c.gvm), "+{v240}"(c.wo.wv[0]), "+{v241}"(c.wo.wv[1]), "+{v244}"(c.wa0), "+{v245}"(h.fvo),
                 "+{v246}"(h.pfd), "+{v247}"(c.tlb), "+{s[60:63]}"(c.kv.srdK), "+{s[64:67]}"(c.kv.srdV), "+{s[68:71]}"(c.sG), "+{s[72:75]}"(c.sU),
                 [na] "=&s"(na), [nb] "=&s"(nb), [eso] "+s"(c.eso), [otb] "+s"(c.otb), [xcb] "+s"(c.xcb), [frb] "+s"(c.frb), [fw2] "+s"(c.fw2), [pm] "+s"(k.pm) AO_STV
               : [ws] "s"(k.ws), [wo] "s"(c.wo.srd), [sD] "s"(c.sD), [sX] "s"(c.sX), [sR] "s"(c.sR), [m] "s"(c.wo.m), [hl] "s"(c.hl), [hl18] "s"(c.hl18), [wob] "s"(c.wo.base), [pf1] "s"(c.pf1), [pf4] "s"(c.pf4),
                 [tag] "s"(tag), [wid] "s"(wid), [xso] "s"(c.xso), [cap] "i"(kSpinCap), [cto] "s"(c.cto), [po] "s"(c.po), [px] "s"(c.px), [vo] "s"(c.vo) OP_FX
               : AO_CLOB, ACLOB_LO, ACLOB_HI);
  k.cs ^= nb; k.nb = nb;
  u32x32 DN; f32x32 A3 = __builtin_bit_cast(f32x32, h.q[3]), XT = __builtin_bit_cast(f32x32, h.q[0]);
  if (__builtin_expect(k.pm == 2, 0)) {
    asm volatile("s_getpc_b64 vcc\n.Lrs_a:\n\ts_sub_u32 vcc_lo, vcc_lo, .Lrs_a-.Lfz_a\n\ts_subb_u32 vcc_hi, vcc_hi, 0\n\ts_and_b32 vcc_lo, vcc_lo, 0xfffff000\n\ts_setpc_b64 vcc"
                 :: "{s[0:1]}"(__builtin_amdgcn_kernarg_segment_ptr()), "{s2}"(blockIdx.x), "{v0}"((wid * 64 + lane_id()) | 0x10000u) : "vcc", "scc", "memory");
    __builtin_unreachable();
  }
  const unsigned lane = lane_id(), r16 = lane & 15, g = lane >> 4, tid = wid * 64 + lane;
  const unsigned sc = k.ldsb, sto = W_STAT + ((grp & 15) << 15) + k.cs, stw = ((wid >> 1) << 14) + ((wid & 1) << 12);
  const bool own0 = TA && cblk == 2 * wid, own1 = TA && cblk == 2 * wid + 1;
  const unsigned o0 = (sto + stw + lane * 16) | (own0 ? 0x80000000u : 0u), o1 = (sto + stw + 2048 + lane * 16) | (own1 ? 0x80000000u : 0u);
  u32x4 a0, a1, b0, b1;
#define STATS() do { a0 = ld16(k.ws, o0); b0 = ld16(k.ws, o0 + 1024); a1 = ld16(k.ws, o1); b1 = ld16(k.ws, o1 + 1024); } while (0)
  u32x16 xp; f32x4 q, sr;
#pragma unroll
  for (int rb = 0; rb < 4; ++rb)
#pragma unroll
    for (int e = 0; e < 4; ++e) {
      const int i = ((e >> 1) * 4 + rb) * 4 + (e & 1) * 2;
      const f32x4 gq = (e >> 1) ? ga1 : ga0;
      const f32x2 t = __builtin_elementwise_fma(f32x2{gq[(e & 1) * 2], gq[(e & 1) * 2 + 1]}, f32x2{A3[i], A3[i + 1]}, f32x2{XT[i], XT[i + 1]});
      xp[4 * rb + e] = cvtpk(t[0], t[1]);
    }
  asm volatile(ST_ASM : "=&{v[64:67]}"(q), "=&{v[80:83]}"(sr) : "{v[160:175]}"(xp));
  lds_st(sc + wid * 1024 + lane * 16, f32x4{sr[0], fold16(fold32(q[0], q[2]), fold32(q[1], q[3])), sr[1] + sr[2], (lane & 1) ? bbw : cww});
  BARRIER_LGKM();
  f32x4 so;
  {
    f32x4 p[4];
#pragma unroll
    for (int w = 0; w < 4; ++w) p[w] = lds_ld<f32x4>(sc + w * 1024 + lane * 16);
    so = (p[0] + p[1]) + (p[2] + p[3]);
    st16(k.ws, (sto + ((cblk >> 2) << 14) + ((cblk & 3) << 11) + wid * 1024 + lane * 16) | (wid << 30 & 0x80000000u), u32x4{__float_as_uint(wid ? so[2] : so[0]), tag, __float_as_uint(wid ? so[3] : so[1]), tag});
  }
  STAMPO(20);
  f32x2 xq[4][4];
#define XQ(rb, e) xq[rb][e]
  f32x4 tsb[2], tc2[2], tgb[2];
#pragma unroll
  for (int nb = 0; nb < 2; ++nb) {
    const unsigned ta = k.ldsb + TABU + wid * 4096 + 2560 + nb * 64 + g * 16;
    tsb[nb] = lds_ld<f32x4>(ta + 128); tc2[nb] = lds_ld<f32x4>(ta + 256); tgb[nb] = lds_ld<f32x4>(ta + 384);
  }
  float mu_d, rstd; unsigned gbest;
  srd_t srdo = mk_srd(args()->out, S * DM * 2);
  asm volatile("" : "+s"(srdo));
  const unsigned ri = ((bid & 3) | ((bid >> 3) << 2)) & (RC - 1);
  const unsigned ko = W_GKEY + ((ri >> 5) << 14) + k.cs + (ri & 31) * 256 + (lane & 31) * 8, ho = W_CANDH + k.cs + (tid >> 1) * 32;
  u32x2 kk; u32x4 h0, h1; unsigned spec;
#define KEY1() asm volatile("buffer_load_dwordx2 %0, %1, %2, 0 offen sc1" : "={v[224:225]}"(kk) : "v"(ko), "s"(k.ws) : "memory")
#define KEYS() asm volatile("buffer_load_dwordx2 %0, %3, %5, 0 offen sc1\n\tbuffer_load_dwordx4 %1, %4, %5, %6 offen sc1\n\tbuffer_load_dwordx4 %2, %4, %5, %6 offen offset:16 sc1" \
                            : "=&{v[224:225]}"(kk), "=&{v[208:211]}"(h0), "=&{v[212:215]}"(h1) : "v"(ko), "v"(ho), "s"(k.ws), "s"(((spec >> 1) << 14) + ((spec & 1) << 12)) : "memory")
#define KEYS_WAIT(N) asm volatile("s_waitcnt vmcnt(%3)" : "+{v[224:225]}"(kk), "+{v[208:211]}"(h0), "+{v[212:215]}"(h1) : "i"(N) : "memory")
#define PERMS() do { \
      u32x32 gt; gate_read(gt, iseq<32>{}); \
      _Pragma("unroll") for (int rp = 0; rp < 4; ++rp) _Pragma("unroll") for (int e = 0; e < 4; ++e) { \
          const unsigned u0 = UP[8 * rp + e], u1 = UP[8 * rp + 4 + e]; \
          UP[8 * rp + e] = plo(u0, u1); UP[8 * rp + 4 + e] = phi(u0, u1); \
          A3[8 * rp + e] = __uint_as_float(plo(gt[8 * rp + e], gt[8 * rp + 4 + e])); A3[8 * rp + 4 + e] = __uint_as_float(phi(gt[8 * rp + e], gt[8 * rp + 4 + e])); } \
      asm volatile("" : "+v"(UP), "+v"(A3)); \
      asm volatile(OP_DRD : "=&{v[32:63]}"(DN) : "v"(c.tlb) : "memory"); \
      _Pragma("unroll") for (int rp = 0; rp < 8; ++rp) _Pragma("unroll") for (int e = 0; e < 2; ++e) { \
          const unsigned u0 = DN[4 * rp + e], u1 = DN[4 * rp + 2 + e]; DN[4 * rp + e] = plo(u0, u1); DN[4 * rp + 2 + e] = phi(u0, u1); } \
      asm volatile("" : "+v"(UP), "+v"(DN), "+v"(A3)); } while (0)
  {
    unsigned spins = 0;
    STATS();
    GAP();
    HOP_PAGE(1);
    GAP();
    PERMS();
#pragma unroll
    for (int i = 0; i < 16; ++i) { xq[i >> 2][i & 3] = up2(xp[i]); asm volatile("" : "+v"(xq[i >> 2][i & 3])); }
    while (!__all((own0 | (tags2(a0, tag) & tags2(b0, tag))) & (own1 | (tags2(a1, tag) & tags2(b1, tag))))) {
      if (spin_fail<SL1>(spins, k.ws, tag)) break;
      STATS();
    }
    const f32x4 s0 = own0 ? so : f32x4{__uint_as_float(a0[0]), __uint_as_float(a0[2]), __uint_as_float(b0[0]), __uint_as_float(b0[2])};
    const f32x4 s1 = own1 ? so : f32x4{__uint_as_float(a1[0]), __uint_as_float(a1[2]), __uint_as_float(b1[0]), __uint_as_float(b1[2])};
    lds_st(sc + 4096 + wid * 1024 + lane * 16, s0 + s1);
    BARRIER_LGKM();
    KEY1();
    GAP();
    STAMPO(21);
    f32x4 p[4];
#pragma unroll
    for (int w = 0; w < 4; ++w) p[w] = lds_ld<f32x4>(sc + 4096 + w * 1024 + lane * 16);
    const f32x4 s = (p[0] + p[1]) + (p[2] + p[3]);
    const float mu = s[0] * (1.f / DM);
    rstd = rsqrtf(fmaxf(fmaf(-mu, mu, s[1] * (1.f / DM)), 0.f) + 1e-5f);
    const float cw = __uint_as_float(__builtin_amdgcn_readlane(__float_as_uint(s[3]), 0)), bb = __uint_as_float(__builtin_amdgcn_readlane(__float_as_uint(s[3]), 1));
    const float r = fmaf(rstd, fmaf(-mu, cw, s[2]), bb);
    unsigned kb = cvtpk(r, r) & 0xffffu; kb = (kb & 0x8000u) ? (~kb & 0xffffu) : (kb | 0x8000u);
    gbest = rfl(wave_maxu((kb << 16) | (unsigned)(S - 1 - (grp * 64 + lane))));
    const unsigned rp = cblk * 4 + wid + 32 * lane, wi = rp >> 1;
    st8(k.ws, (W_GKEY + ((wi >> 5) << 14) + (((rp & 1) << 13) ^ k.nb) + (wi & 31) * 256 + grp * 8) | (lane < RC / 16 ? 0u : 0x80000000u), u32x2{gbest, tag});
    mu_d = -mu * rstd;
  }
  STAMPO(22);
  const unsigned lrow = (unsigned)key_row(gbest) & 63u;
  const unsigned oo = (grp * 64 + r16) * (DM * 2) + cblk * 256 + wid * 64 + (g & 1) * 32 + (g >> 1) * 16;
  u32x4 hp[4], ow[4]; unsigned best; bool hold, early;
  {
    float ra[4], rb_[4];
    bcast4(rstd, ra); bcast4(mu_d, rb_);
#pragma unroll
    for (int rb = 0; rb < 4; ++rb)
#pragma unroll
      for (int e = 0; e < 4; ++e) {
        const int nb = e >> 1, h = (e & 1) * 2;
        const f32x2 n = __builtin_elementwise_fma(XQ(rb, e), f32x2{ra[rb], ra[rb]}, f32x2{rb_[rb], rb_[rb]});
        const f32x2 hh = __builtin_elementwise_fma(n, f32x2{tc2[nb][h], tc2[nb][h + 1]}, f32x2{tsb[nb][h], tsb[nb][h + 1]});
        hp[rb][e] = cvtpk(hh[0], hh[1]);
      }
    u32x4 hc = hp[0], xc = {xp[0], xp[1], xp[2], xp[3]};
#pragma unroll
    for (int rb = 1; rb < 4; ++rb) if ((lrow >> 4) == (unsigned)rb) { hc = hp[rb]; xc = u32x4{xp[4 * rb], xp[4 * rb + 1], xp[4 * rb + 2], xp[4 * rb + 3]}; }
    GAP();
#define CAND(H) do { const unsigned off = ((H) && r16 == (lrow & 15) ? 0u : 0x80000000u) | ((wid * 16 + g * 2) * 8); \
    const unsigned co = (W_CANDH + ((grp >> 1) << 14) + ((grp & 1) << 12) + cblk * 512 + k.cs) + off, cx = (W_CANDX + ((grp >> 2) << 14) + (((cblk >> 2) << 13) ^ k.nb) + ((grp & 3) << 11) + (cblk & 3) * 512) + off; \
    st16(k.ws, co, u32x4{hc[0], tag, hc[1], tag}); st16(k.ws, co + 64, u32x4{hc[2], tag, hc[3], tag}); \
    st16(k.ws, co ^ 8192, u32x4{hc[0], tag, hc[1], tag}); st16(k.ws, (co ^ 8192) + 64, u32x4{hc[2], tag, hc[3], tag}); \
    st16(k.ws, cx, u32x4{xc[0], tag, xc[1], tag}); st16(k.ws, cx + 64, u32x4{xc[2], tag, xc[3], tag}); } while (0)
    asm volatile("s_waitcnt vmcnt(%1)" : "+{v[224:225]}"(kk) : "i"(TE ? 7 : 1) : "memory");
    best = max(rfl(wave_maxu(kk[1] == tag ? kk[0] : 0u)), gbest);
    hold = best == gbest;
    early = __builtin_popcountll(__ballot(kk[1] == tag)) < 2 * TB;
    spec = ((unsigned)key_row(best) >> 6) & 31u;
    KEYS();
    GAP();
    CAND(hold);
    GAP();
#pragma unroll
    for (int rb = 0; rb < 4; ++rb) {
      u32x4 op;
#pragma unroll
      for (int e = 0; e < 4; ++e) {
        const int nb = e >> 1, h = (e & 1) * 2;
        const f32x2 o = __builtin_elementwise_fma(f32x2{tgb[nb][h], tgb[nb][h + 1]}, up2(hp[rb][e]), XQ(rb, e));
        op[e] = cvtpk(o[0], o[1]);
      }
      auto s0 = __builtin_amdgcn_permlane16_swap(op[0], op[2], false, false);
      auto s1 = __builtin_amdgcn_permlane16_swap(op[1], op[3], false, false);
      ow[rb] = u32x4{s0[0], s1[0], s0[1], s1[1]};
      __builtin_amdgcn_raw_buffer_store_b128(ow[rb], srdo, (oo + rb * (16 * DM * 2)) | (!early || (hold && lrow == 16 * rb + r16) ? 0x80000000u : 0u), 0, OAUX);
    }
    GAP();
  }
  STAMPO(23);
  const unsigned fb = k.xcd * 28 + ffn_j(k);
  unsigned ga_ = sc + 8192 + wid * 128 + (lane >> 5) * 64 + (lane & 1) * 32 + ((lane >> 4) & 1) * 16, gr_ = sc + 8192 + (lane & 31) * 4, gy_ = W_YG + ((fb >> 2) << 14) + (((wid >> 1) << 13) ^ k.nb) + ((fb & 3) << 11) + (tid & 127) * 16;
  asm volatile("" : "+v"(ga_), "+v"(gr_), "+v"(gy_));
  HOP_PAGE(0);
  KEYS_WAIT(TE ? 4 : 10);
  for (unsigned spins = 0;;) {
    if ((lane & 31) == grp) kk = u32x2{gbest, tag};
    const bool kv = kk[1] == tag;
    best = rfl(wave_maxu(kv ? kk[0] : 0u));
    const unsigned w = ((unsigned)key_row(best) >> 6) & 31u;
    if (__all(kv & tags2(h0, tag) & tags2(h1, tag)) && w == spec) break;
    spec = w;
    if (spin_fail<SL2>(spins, k.ws, tag)) break;
    KEYS(); KEYS_WAIT(0);
  }
  STAMPO(24);
  const unsigned idx = (unsigned)key_row(best) & (S - 1), wgrp = idx >> 6;
  if (!ffn_idle(k)) {
    float a[16];
    const unsigned hk[4] = {h0[0], h0[2], h1[0], h1[2]};
#pragma unroll
    for (int e = 0; e < 8; ++e) {
      float sg = 0.f, su = 0.f;
#pragma unroll
      for (int rp = 0; rp < 4; ++rp) { sg = dot2(__float_as_uint(A3[8 * rp + (e & 1) * 4 + (e >> 1)]), hk[rp], sg); su = dot2(UP[8 * rp + (e & 1) * 4 + (e >> 1)], hk[rp], su); }
      a[e] = sg; a[8 + e] = su;
    }
    float b[8], cc[4];
#pragma unroll
    for (int e = 0; e < 8; ++e) b[e] = fold32(a[e], a[8 + e]);
#pragma unroll
    for (int e = 0; e < 4; ++e) cc[e] = fold16(b[e], b[4 + e]);
#pragma unroll
    for (int e = 0; e < 4; ++e) { cc[e] += dpp_f<0x128>(cc[e]); cc[e] += dpp_f<0x124>(cc[e]); cc[e] += dpp_f<0x122>(cc[e]); }
    if ((lane & 14) == 0) lds_st(ga_, f32x4{cc[0], cc[1], cc[2], cc[3]});
    BARRIER_LGKM();
    const unsigned ra = gr_;
    const float s = rbf((lds_ld<float>(ra) + lds_ld<float>(ra + 128)) + (lds_ld<float>(ra + 256) + lds_ld<float>(ra + 384)));
    auto su = __builtin_amdgcn_permlane16_swap(__float_as_uint(s), __float_as_uint(s), false, false);
    const float gg = __uint_as_float(su[0]);
    const float act = silu_bf(gg) * __uint_as_float(su[1]);
    const unsigned ap = cvtpk(act, dpp_f<0xB1>(act));
    f32x4 y = {0.f, 0.f, 0.f, 0.f};
#pragma unroll
    for (int rp = 0; rp < 8; ++rp) {
      const unsigned w = __builtin_amdgcn_readlane(ap, 2 * rp);
#pragma unroll
      for (int e = 0; e < 4; ++e) y[e] = dot2(DN[4 * rp + (e & 1) * 2 + (e >> 1)], w, y[e]);
    }
    st16(k.ws, gy_, u32x4{cvtpk(y[0], y[1]), tag, cvtpk(y[2], y[3]), tag});
  }
  STAMPO(25);
  GAP();
  if (!early || hold) {
#pragma unroll
    for (int rb = 0; rb < 4; ++rb) {
      const bool cr = lrow == 16 * rb + r16;
      __builtin_amdgcn_raw_buffer_store_b128(ow[rb], srdo, (oo + rb * (16 * DM * 2)) | ((wgrp == grp && cr) || (early && !cr) ? 0x80000000u : 0u), 0, OAUX);
    }
  }
  GAP();
  if (bid == 0 && tid == 0) st4(k.ws, W_EPOCH, tag);
  constexpr unsigned NFIN = 16, FST = 128;
  if (ffn_idle(k)) {
    const unsigned cl = 32 * k.kvh + 2 * (tid & 15), col = cblk * 128 + cl;
    const unsigned yo = W_YG + ((tid >> 5) << 14) + k.cs + (((tid >> 3) & 3) << 11) + (cblk & 3) * 512 + k.kvh * 128 + (tid & 7) * 16;
    const unsigned co = W_CANDH + ((wgrp >> 1) << 14) + k.cs + ((wgrp & 1) << 12) + col * 4, cx = W_CANDX + ((wgrp >> 2) << 14) + k.cs + ((wgrp & 3) << 11) + (col & 511) * 4;
    f32x2 gb2 = lds_ld<f32x2>(k.ldsb + TABU + k.kvh * 4096 + 2560 + 384 + (cl & 31) * 4);
    float rs = rbf(__builtin_amdgcn_rcpf(1.f + __expf(-key_val(best))));
    unsigned oaddr = idx * (DM * 2) + col * 2, fa = sc + 12288 + tid * 8, fw = sc + 12288 + wid * 128 + (lane & 7) * 16 + (lane >> 4) * 4;
    u32x4 p[7]; u32x2 hh, xx;
    asm volatile("" : "+v"(rs), "+v"(oaddr), "+v"(fa), "+v"(fw), "+v"(gb2));
    for (unsigned spins = 0;;) {
      hh = ld8(k.ws, co); xx = ld8(k.ws, cx);
#pragma unroll
      for (int r = 0; r < 7; ++r) p[r] = __builtin_amdgcn_raw_buffer_load_b128(k.ws, yo, r * 131072, 16);
      bool ok = (hh[1] == tag) & (xx[1] == tag);
#pragma unroll
      for (int r = 0; r < 7; ++r) ok &= tags2(p[r], tag);
      if (__all(ok)) break;
      if (spin_fail<SL3>(spins, k.ws, tag)) break;
    }
    f32x2 ya = up2(p[0][0]), yb = up2(p[0][2]);
#pragma unroll
    for (int r = 1; r < 7; ++r) { ya += up2(p[r][0]); yb += up2(p[r][2]); }
    float z = fold16(fold32(ya[0], yb[0]), fold32(ya[1], yb[1]));
    z += dpp_f<0x128>(z);
    if (!(lane & 8)) lds_st(fw, z);
    BARRIER_LGKM();
    if (tid < NFIN) {
      f32x2 yy = (lds_ld<f32x2>(fa) + lds_ld<f32x2>(fa + FST)) + (lds_ld<f32x2>(fa + 2 * FST) + lds_ld<f32x2>(fa + 3 * FST));
      yy = up2(cvtpk(yy[0], yy[1])) * rs;
      yy = up2(cvtpk(yy[0], yy[1])) + up2(hh[0]);
      yy = up2(cvtpk(yy[0], yy[1])) * gb2;
      yy = up2(cvtpk(yy[0], yy[1])) + up2(xx[0]);
      __builtin_amdgcn_raw_buffer_store_b32(cvtpk(yy[0], yy[1]), srdo, oaddr, 0, OAUX);
    }
  }
  STAMPO(26);
}
#define PERM 0
__global__ __launch_bounds__(256, 1) __attribute__((aligned(4096))) void k_fused(Args a) {
  Ctx k;
  const unsigned bid = PERM ? ((blockIdx.x << 3) | (blockIdx.x >> 5)) & 255u : blockIdx.x;
  unsigned t0;
  asm volatile("v_readfirstlane_b32 %0, %1" : "=s"(t0) : "v"(threadIdx.x));
  const unsigned wid = (t0 >> 6) & 3;
  k.pm = 1 - (t0 >> 16);
  k.wid = wid; k.bid = bid; k.xcd = ((bid & 3) << 1) | ((bid >> 2) & 1); k.j = bid >> 3; k.kvh = k.xcd >> 1; k.hl = k.j & 7; k.rblk = (k.xcd & 1) * 4 + (k.j >> 3);
  k.cs = (k.xcd & 1) << 13; k.nb = 0;
  STAMP_DECL;
  STAMP(0);
#define SPINTO(i) do { } while (0)
  W4C c;
  SPINTO(0);
  H4 g;
  front(k, g, c STAMP_PASS);
  STAMP(8);
  oproj(k, g, c STAMP_PASS);
  SPINTO(3);
  STAMP_FLUSH();
  asm volatile(HOP_DEFS);
}

extern "C" void kernel_launch(void* const* d_in, const int* in_sizes, int n_in, void* d_out, int out_size, void* d_ws, size_t ws_size,
                              hipStream_t stream) {
  Args a;
  a.x = (const u16*)d_in[0]; a.kc = (const u16*)d_in[1]; a.vc = (const u16*)d_in[2]; a.wq = (const u16*)d_in[3]; a.wo = (const u16*)d_in[4];
  a.w_r = (const u16*)d_in[6]; a.wg = (const u16*)d_in[7]; a.wu = (const u16*)d_in[8]; a.wd = (const u16*)d_in[9]; a.c = (const u16*)d_in[10];
  a.w_mod = (const u16*)d_in[11]; a.out = (u16*)d_out; a.ws = (char*)d_ws;
  (void)hipFuncSetAttribute((const void*)k_fused, hipFuncAttributeMaxDynamicSharedMemorySize, 163840);
  k_fused<<<256, 256, 163840, stream>>>(a);
}
```

```cpp
#include <hip/hip_runtime.h>
#include <hip/hip_bf16.h>
#include <stdint.h>
#include <utility>

typedef unsigned short u16;
using bf16x8 = __attribute__((ext_vector_type(8))) short;
using s16x4  = __attribute__((ext_vector_type(4))) short;
using f32x16 = __attribute__((ext_vector_type(16))) float;
using u32x16 = __attribute__((ext_vector_type(16))) unsigned;
using f32x4  = __attribute__((ext_vector_type(4))) float;
using f32x2  = __attribute__((ext_vector_type(2))) float;
using u32x4  = __attribute__((ext_vector_type(4))) unsigned;
using u32x2  = __attribute__((ext_vector_type(2))) unsigned;
using u32x8 = __attribute__((ext_vector_type(8))) unsigned;
using f32x8 = __attribute__((ext_vector_type(8))) float;
using f32x32 = __attribute__((ext_vector_type(32))) float;
using u32x32 = __attribute__((ext_vector_type(32))) unsigned;
typedef __bf16 bf16x2_t __attribute__((ext_vector_type(2)));
#define DI __device__ __forceinline__
#define NTMASK 0
#define NT_SEL(bit) ((NTMASK >> (bit)) & 1)
#if NT_SEL(0)
#define NTX " nt"
#else
#define NTX ""
#endif
#if NT_SEL(1)
#define NTM " nt"
#else
#define NTM ""
#endif
#if NT_SEL(2)
#define NTQ " nt"
#else
#define NTQ ""
#endif
#if NT_SEL(3)
#define NTO " nt"
#else
#define NTO ""
#endif
#if NT_SEL(4)
#define NTG " nt"
#else
#define NTG ""
#endif
#if NT_SEL(5)
#define NTD " nt"
#else
#define NTD ""
#endif
#if NT_SEL(6)
#define NTT " nt"
#else
#define NTT ""
#endif
#if NT_SEL(7)
#define NTF " nt"
#else
#define NTF ""
#endif

constexpr int S = 2048, DM = 1024, HQ = 32, HKV = 4, DH = 128, SKV = 2048, WIN = 512, FF = 3584;
constexpr int QD = HQ * DH;
constexpr float QSCALE = 0.12751743082f;

DI float bf2f(u16 v) { return __uint_as_float(((unsigned)v) << 16); }
DI u16 f2bf(float x) { unsigned u = __float_as_uint(x); u += 0x7fffu + ((u >> 16) & 1u); return (u16)(u >> 16); }
DI unsigned cvtpk(float lo, float hi) { f32x2 v = {lo, hi}; bf16x2_t b = __builtin_convertvector(v, bf16x2_t); return __builtin_bit_cast(unsigned, b); }
DI float lo16f(unsigned w) { return __uint_as_float(w << 16); }
DI float hi16f(unsigned w) { return __uint_as_float(w & 0xffff0000u); }
DI float rbf(float x) { return lo16f(cvtpk(x, x)); }
DI f32x2 up2(unsigned w) { return f32x2{lo16f(w), hi16f(w)}; }
DI float dot2(unsigned a, unsigned b, float c) { return __builtin_amdgcn_fdot2_f32_bf16(__builtin_bit_cast(bf16x2_t, a), __builtin_bit_cast(bf16x2_t, b), c, false); }
DI unsigned plo(unsigned a, unsigned b) { return __builtin_amdgcn_perm(b, a, 0x05040100u); }
DI unsigned phi(unsigned a, unsigned b) { return __builtin_amdgcn_perm(b, a, 0x07060302u); }
DI unsigned rfl(unsigned x) { return __builtin_amdgcn_readfirstlane(x); }
template <int CTRL> DI float dpp_f(float v) { return __int_as_float(__builtin_amdgcn_update_dpp(0, __float_as_int(v), CTRL, 0xf, 0xf, true)); }
template <int CTRL> DI unsigned dpp_u(unsigned v) { return (unsigned)__builtin_amdgcn_update_dpp(0, (int)v, CTRL, 0xf, 0xf, true); }
DI float fold16(float a, float b) { auto r = __builtin_amdgcn_permlane16_swap(__float_as_uint(a), __float_as_uint(b), false, false); return __uint_as_float(r[0]) + __uint_as_float(r[1]); }
DI float fold32(float a, float b) { auto r = __builtin_amdgcn_permlane32_swap(__float_as_uint(a), __float_as_uint(b), false, false); return __uint_as_float(r[0]) + __uint_as_float(r[1]); }
DI float sum16(float v) { return fold16(v, v); }
DI float sum32(float v) { return fold32(v, v); }
DI float sum_row16(float v) { v += dpp_f<0xB1>(v); v += dpp_f<0x4E>(v); v += dpp_f<0x141>(v); v += dpp_f<0x140>(v); return v; }
DI float wave_sum(float v) { return sum32(sum16(sum_row16(v))); }
DI unsigned wave_maxu(unsigned v) {
  v = max(v, dpp_u<0xB1>(v)); v = max(v, dpp_u<0x4E>(v)); v = max(v, dpp_u<0x141>(v)); v = max(v, dpp_u<0x140>(v));
  { auto r = __builtin_amdgcn_permlane16_swap(v, v, false, false); v = max(r[0], r[1]); }
  { auto r = __builtin_amdgcn_permlane32_swap(v, v, false, false); v = max(r[0], r[1]); }
  return v;
}
#define GAP() __builtin_amdgcn_sched_barrier(0)
template <int N> using iseq = std::make_integer_sequence<int, N>;
template <int... Is> using ilist = std::integer_sequence<int, Is...>;
typedef __amdgpu_buffer_rsrc_t srd_t;
#define SRD3 0x00020000
DI srd_t mk_srd(const void* p, unsigned bytes) { return __builtin_amdgcn_make_buffer_rsrc((void*)p, (short)0, (int)bytes, SRD3); }
DI unsigned lane_id() { unsigned l; asm volatile("v_mbcnt_lo_u32_b32 %0, -1, 0\n\tv_mbcnt_hi_u32_b32 %0, -1, %0" : "=v"(l)); return l; }
struct Desc { const void* p; unsigned bytes, w3; };
struct Args { Desc x, w_mod, c, ws, wq, wo, wd, w_r, kc, vc, wg, wu, out; };
typedef const __attribute__((address_space(4))) Args* argp_t;
DI argp_t args() { argp_t p = (argp_t)__builtin_amdgcn_kernarg_segment_ptr(); asm volatile("" : "+s"(p)); return p; }
#define BARRIER_LGKM() do { asm volatile("s_waitcnt lgkmcnt(0)\n\ts_barrier" ::: "memory"); } while (0)

constexpr unsigned W_EPOCH = 0x260000, W_TMO = 256;
constexpr unsigned W_FLAGH = 0x261000;
constexpr unsigned W_VOTE = 0x2000, W_PROBE = 0x4000;
constexpr unsigned W_GKEY = 0x10000;
constexpr unsigned W_FLAGR = 0x8000;
constexpr unsigned W_FLAGO = 0x20000;
constexpr unsigned W_CANDH = 0x40000;
constexpr unsigned W_STAT = 0x80000;
constexpr unsigned W_YG = 0x100000;
constexpr unsigned W_CANDX = 0x1E0000;
constexpr unsigned W_ST1 = 0x200000;
constexpr unsigned W_MODA = 0x210000;
constexpr unsigned W_MODB = 0x220000;
constexpr unsigned W_SIL = 0x240000;
constexpr unsigned W_HT = 4u << 20;
constexpr unsigned W_OT = 8u << 20;
constexpr unsigned W_XCH = 24u << 20;
constexpr unsigned W_END = 40u << 20;
constexpr unsigned TAGM = 0x40004000u, TAGC = 0x9E3739B9u;
#define SPINCAP 262144
constexpr unsigned kSpinCap = SPINCAP;
#define SL0 1
#define SL1 1
#define SL2 1
#define SL3 1
template <int SL = 1> DI bool spin_fail(unsigned& spins, srd_t ws, unsigned tag) {
  __builtin_amdgcn_s_sleep(SL);
  if (++spins > kSpinCap) { __builtin_amdgcn_raw_buffer_store_b32(tag, ws, W_TMO, 0, 16); return true; }
  return (spins & 255u) == 0u && rfl(__builtin_amdgcn_raw_buffer_load_b32(ws, W_TMO, 0, 16)) == tag;
}
DI u32x4 ld16(srd_t ws, unsigned off) { return __builtin_amdgcn_raw_buffer_load_b128(ws, off, 0, 16); }
DI u32x2 ld8(srd_t ws, unsigned off) { return __builtin_amdgcn_raw_buffer_load_b64(ws, off, 0, 16); }
DI void st16(srd_t ws, unsigned off, u32x4 v) { __builtin_amdgcn_raw_buffer_store_b128(v, ws, off, 0, 16); }
DI void st8(srd_t ws, unsigned off, u32x2 v) { __builtin_amdgcn_raw_buffer_store_b64(v, ws, off, 0, 16); }
DI void st4(srd_t ws, unsigned off, unsigned v) { __builtin_amdgcn_raw_buffer_store_b32(v, ws, off, 0, 16); }
DI void wait_flags(srd_t ws, unsigned off, unsigned tag) {
  const unsigned o = off + (lane_id() & 31u) * 4u;
  for (unsigned spins = 0;;) {
    const unsigned f = __builtin_amdgcn_raw_buffer_load_b32(ws, o, 0, 16);
    if (__all(f == tag)) break;
    if (spin_fail(spins, ws, tag)) break;
  }
  asm volatile("" ::: "memory");
}
template <int OFF, bool DEV = false> DI u32x4 ald16(srd_t s, unsigned vo, unsigned so) {
  u32x4 r;
  if constexpr (DEV) asm volatile("buffer_load_dwordx4 %0, %1, %2, %3 offen offset:%4 sc1" : "=v"(r) : "v"(vo), "s"(s), "s"(so), "i"(OFF) : "memory");
  else               asm volatile("buffer_load_dwordx4 %0, %1, %2, %3 offen offset:%4" NTF : "=v"(r) : "v"(vo), "s"(s), "s"(so), "i"(OFF) : "memory");
  return r;
}
template <int OFF> DI u32x2 ald8(srd_t s, unsigned vo, unsigned so) {
  u32x2 r; asm volatile("buffer_load_dwordx2 %0, %1, %2, %3 offen offset:%4" : "=v"(r) : "v"(vo), "s"(s), "s"(so), "i"(OFF) : "memory"); return r;
}
DI u32x2 ald8d(srd_t s, unsigned vo) { u32x2 r; asm volatile("buffer_load_dwordx2 %0, %1, %2, 0 offen sc1" : "=v"(r) : "v"(vo), "s"(s) : "memory"); return r; }
template <int OFF, bool DEV = false> DI unsigned ald4(srd_t s, unsigned vo, unsigned so) {
  unsigned r;
  if constexpr (DEV) asm volatile("buffer_load_dword %0, %1, %2, %3 offen offset:%4 sc1" : "=v"(r) : "v"(vo), "s"(s), "s"(so), "i"(OFF) : "memory");
  else               asm volatile("buffer_load_dword %0, %1, %2, %3 offen offset:%4" : "=v"(r) : "v"(vo), "s"(s), "s"(so), "i"(OFF) : "memory");
  return r;
}
DI void touch(unsigned& d, const void* base, unsigned vo) { asm volatile("global_load_dword %0, %1, %2" : "+v"(d) : "v"(vo), "s"(base) : "memory"); }
DI void touchq(unsigned& d, const void* base, unsigned vo) { asm volatile("global_load_dword %0, %1, %2" NTQ : "+v"(d) : "v"(vo), "s"(base) : "memory"); }
template <class T> DI T lds_ld(unsigned a) { return *reinterpret_cast<const __attribute__((address_space(3))) T*>(a); }
template <class T> DI void lds_st(unsigned a, T v) { *reinterpret_cast<__attribute__((address_space(3))) T*>(a) = v; }
DI float silu_bf(float v) { return rbf(v * __builtin_amdgcn_rcpf(1.f + __expf(-v))); }
struct Ctx { static constexpr unsigned ldsb = 0; unsigned t0, pm, bid, wid, xcd, j, kvh, hl, rblk, tag, cs, nb; srd_t ws; };
#define TAIL2 1
DI unsigned ffn_j(const Ctx& k) { return TAIL2 ? (k.j < 24 ? k.j : 24 + (k.j & 3)) : min(k.j, 27u); }
DI bool ffn_idle(const Ctx& k) { return TAIL2 ? (k.j >> 2) == 6 + (k.xcd & 1) : k.j >= 28; }
constexpr unsigned UNIT = 16384;
#define STAMPA(I) do { } while (0)
#define STAMPO(I) do { } while (0)
#define STAMP_DECL
#define STAMP(I) do { } while (0)
#define STAMP_FLUSH() do { } while (0)
#define STAMP_ARG
#define STAMP_PASS
#define ACLOB_LO "a0","a1","a2","a3","a4","a5","a6","a7","a8","a9","a10","a11","a12","a13","a14","a15","a16","a17","a18","a19","a20","a21","a22","a23","a24","a25","a26","a27","a28","a29","a30","a31","a32","a33","a34","a35","a36","a37","a38","a39","a40","a41","a42","a43","a44","a45","a46","a47","a48","a49","a50","a51","a52","a53","a54","a55","a56","a57","a58","a59","a60","a61","a62","a63","a64","a65","a66","a67","a68","a69","a70","a71","a72","a73","a74","a75","a76","a77","a78","a79","a80","a81","a82","a83","a84","a85","a86","a87","a88","a89","a90","a91","a92","a93","a94","a95","a96","a97","a98","a99","a100","a101","a102","a103","a104","a105","a106","a107","a108","a109","a110","a111","a112","a113","a114","a115","a116","a117","a118","a119","a120","a121","a122","a123","a124","a125","a126","a127"
#define ACLOB_HI "a128","a129","a130","a131","a132","a133","a134","a135","a136","a137","a138","a139","a140","a141","a142","a143","a144","a145","a146","a147","a148","a149","a150","a151","a152","a153","a154","a155","a156","a157","a158","a159","a160","a161","a162","a163","a164","a165","a166","a167","a168","a169","a170","a171","a172","a173","a174","a175","a176","a177","a178","a179","a180","a181","a182","a183","a184","a185","a186","a187","a188","a189","a190","a191","a192","a193","a194","a195","a196","a197","a198","a199","a200","a201","a202","a203","a204","a205","a206","a207","a208","a209","a210","a211","a212","a213","a214","a215","a216","a217","a218","a219","a220","a221","a222","a223","a224","a225","a226","a227","a228","a229","a230","a231","a232","a233","a234","a235","a236","a237","a238","a239","a240","a241","a242","a243","a244","a245","a246","a247","a248","a249","a250","a251","a252","a253","a254","a255"
#define HOP_DEFS ".set .Lhc00, .Lhs0\n\t.set .Lhs0n, .Lhs4\n\t.set .Lhs4n, .Lhs8\n\t.set .Lhs8n, .Lhr0\n\t.set .Lhc01, .Lhs1\n\t.set .Lhs1n, .Lhs5\n\t.set .Lhs5n, .Lhs9\n\t.set .Lhs9n, .Lhr0\n\t.set .Lhc02, .Lhs2\n\t.set .Lhs2n, .Lhs6\n\t.set .Lhs6n, .Lhs10\n\t.set .Lhs10n, .Lhr0\n\t.set .Lhc03, .Lhs3\n\t.set .Lhs3n, .Lhs7\n\t.set .Lhs7n, .Lhr0\n\t.set .Lhc10, .Lhs11\n\t.set .Lhs11n, .Lhs15\n\t.set .Lhs15n, .Lhr1\n\t.set .Lhc11, .Lhs12\n\t.set .Lhs12n, .Lht0\n\t.set .Lht0n, .Lhr1\n\t.set .Lhc12, .Lhs13\n\t.set .Lhs13n, .Lht1\n\t.set .Lht1n, .Lhr1\n\t.set .Lhc13, .Lhs14\n\t.set .Lhs14n, .Lhr1"
#define HOP_PAGE(n) asm volatile("s_branch .Lht" #n "\n\t.p2align 12\n.Lht" #n ":\n\ts_cbranch_execz .Lht" #n "n")
#define PAGE_ALIGN() asm volatile("s_branch .Lpa%=\n\t.p2align 12\n.Lpa%=:" ::: "memory")
#define HOP_FRONT() asm volatile("s_branch .Lhf\n\t.p2align 12\n.Lhf:\n\ts_cbranch_execz .Lhfn")
#define HOP_GO(g, w) asm volatile("s_mov_b64 exec, 0\n\ts_cmp_eq_u32 %0, 0\n\ts_cbranch_scc1 .Lhc" #g "0\n\ts_cmp_eq_u32 %0, 1\n\ts_cbranch_scc1 .Lhc" #g "1\n\ts_cmp_eq_u32 %0, 2\n\ts_cbranch_scc1 .Lhc" #g "2\n\ts_branch .Lhc" #g "3\n.Lhr" #g ":\n\ts_mov_b64 exec, -1" :: "s"(w) : "scc", "memory")
#define FQ_STR2(x) #x
#define FQ_STR(x) FQ_STR2(x)
#define FQ_CAP FQ_STR(SPINCAP)
DI bool tags2(const u32x4& a, unsigned tag) { return (a[1] == tag) & (a[3] == tag); }
DI void bcast4(float v, float (&o)[4]) {
  auto a = __builtin_amdgcn_permlane32_swap(__float_as_uint(v), __float_as_uint(v), false, false);
  auto b = __builtin_amdgcn_permlane16_swap(a[0], a[0], false, false);
  auto c = __builtin_amdgcn_permlane16_swap(a[1], a[1], false, false);
  o[0] = __uint_as_float(b[0]); o[1] = __uint_as_float(b[1]); o[2] = __uint_as_float(c[0]); o[3] = __uint_as_float(c[1]);
}
#define FSTP(x)
#define FSTS(x)
#define FSTA(x)
#define FQ_ASM \
  FSTA("v_mov_b32 v255, 0\n\t") \
  FSTA("s_memrealtime s[94:95]\n\t") \
  FSTA("s_memtime s[92:93]\n\t") \
  FSTA("s_waitcnt lgkmcnt(0)\n\t") \
  FSTA("s_or_b32 s94, s94, 1\n\t") \
  FSTA("v_writelane_b32 v255, s94, 0\n\t") \
  FSTA("v_writelane_b32 v255, s92, 1\n\t") \
  "s_mov_b32 s37, 1\n\t" \
  ".Lfr_again:\n\t" \
  "s_load_dwordx16 s[4:19], s[0:1], 0x0\n\t" \
  "s_load_dwordx16 s[20:35], s[0:1], 0x40\n\t" \
  "s_load_dwordx16 s[60:75], s[0:1], 0x80\n\t" \
  "v_readfirstlane_b32 s3, v0\n\t" \
  "v_and_b32 v254, 63, v0\n\t" \
  "s_lshr_b32 s3, s3, 6\n\t" \
  "s_lshr_b32 s39, s2, 3\n\t" \
  "s_lshl_b32 s54, s3, 5\n\t" \
  "s_add_u32 s54, s54, s39\n\t" \
  "s_lshl_b32 s54, s54, 7\n\t" \
  "v_lshl_add_u32 v48, v254, 14, s54\n\t" \
  "v_min_u32 v48, .Lfz_z-.Lfz_a-4, v48\n\t" \
  "s_getpc_b64 s[96:97]\n\t" \
  ".Lfz_a:\n\t" \
  "global_load_dword v246, v48, s[96:97]\n\t" \
  "s_and_b32 s40, s2, 3\n\t" \
  "v_lshrrev_b32 v52, 2, v0\n\t" \
  "v_lshlrev_b32 v244, 4, v254\n\t" \
  "v_lshlrev_b32 v53, 3, v52\n\t" \
  "v_mul_u32_u24 v52, 49152, v52\n\t" \
  "v_and_or_b32 v52, v244, 48, v52\n\t" \
  "s_lshl_b32 s44, s40, 9\n\t" \
  "s_mul_i32 s45, s40, 3145728\n\t" \
  "s_bfe_u32 s52, s2, 0x10002\n\t" \
  "s_lshl_b32 s52, s52, 5\n\t" \
  "s_add_u32 s52, s52, s39\n\t" \
  "s_lshl_b32 s54, s52, 6\n\t" \
  "s_add_u32 s45, s45, s54\n\t" \
  "s_add_u32 s96, s45, 12288\n\t" \
  "s_add_u32 s97, s45, 24576\n\t" \
  "s_add_u32 s99, s45, 36864\n\t" \
  "v_mov_b32 v1, 2490368\n\t" \
  "s_and_b32 s98, s2, 4\n\t" \
  "s_lshr_b32 s54, s2, 6\n\t" \
  "s_or_b32 s98, s98, s54\n\t" \
  "s_lshl_b32 s41, s98, 19\n\t" \
  "s_lshl_b32 s38, s3, 12\n\t" \
  "s_lshl_b32 s77, s3, 13\n\t" \
  "s_and_b32 s54, s2, 0x38\n\t" \
  "s_lshl_b32 s54, s54, 11\n\t" \
  "s_lshl_b32 s55, s40, 17\n\t" \
  "s_or_b32 s46, s41, s38\n\t" \
  "s_or_b32 s46, s46, s54\n\t" \
  "s_or_b32 s46, s46, s55\n\t" \
  "s_xor_b32 s87, s55, 393216\n\t" \
  "v_lshrrev_b32 v245, 4, v254\n\t" \
  "v_lshrrev_b32 v49, 3, v254\n\t" \
  "v_and_b32 v50, 7, v254\n\t" \
  "v_xor_b32 v51, v50, v245\n\t" \
  "v_lshlrev_b32 v51, 4, v51\n\t" \
  "v_lshl_add_u32 v212, v49, 11, v51\n\t" \
  "v_xor_b32 v213, 64, v212\n\t" \
  "v_add_u32 v213, 15360, v213\n\t" \
  "s_bfe_u32 s42, s2, 0x20004\n\t" \
  "s_lshl_b32 s54, s3, 15\n\t" \
  "s_or_b32 s47, s41, s54\n\t" \
  "s_or_b32 s47, s47, s44\n\t" \
  "s_lshl_b32 s54, s42, 7\n\t" \
  "s_or_b32 s47, s47, s54\n\t" \
  "s_and_b32 s43, s2, 8\n\t" \
  "s_lshl_b32 s54, s43, 15\n\t" \
  "s_or_b32 s57, s47, s87\n\t" \
  "s_xor_b32 s57, s57, s54\n\t" \
  "s_xor_b32 s58, s57, 131072\n\t" \
  "s_lshl_b32 s54, s43, 9\n\t" \
  "s_add_u32 s90, s77, s54\n\t" \
  "s_lshl_b32 s78, s40, 21\n\t" \
  "s_lshl_b32 s50, s39, 14\n\t" \
  "s_lshl2_add_u32 s50, s44, s50\n\t" \
  "s_mul_i32 s54, s3, 0x1080\n\t" \
  "s_and_b32 s54, s54, 0x2080\n\t" \
  "s_or_b32 s50, s50, s54\n\t" \
  "v_lshl_add_u32 v54, v50, 8, s50\n\t" \
  "v_and_b32 v51, 3, v49\n\t" \
  "v_lshl_add_u32 v51, v51, 19, v54\n\t" \
  "s_mov_b32 m0, s90\n\t" \
  "s_waitcnt lgkmcnt(0)\n\t" \
  "buffer_load_dwordx2 v[2:3], v53, s[12:15], s44 offen\n\t" \
  "buffer_load_dwordx4 v[4:7], v52, s[8:11], s45 offen\n\t" \
  "buffer_load_dwordx4 v[8:11], v52, s[8:11], s96 offen\n\t" \
  "buffer_load_dwordx4 v[12:15], v52, s[8:11], s97 offen\n\t" \
  "buffer_load_dwordx4 v[16:19], v52, s[8:11], s99 offen\n\t" \
  "s_add_u32 s16, s16, 0x1fffff\n\t" \
  "s_addc_u32 s17, s17, 0\n\t" \
  "s_and_b32 s16, s16, 0xffe00000\n\t" \
  "buffer_load_dword v1, v1, s[16:19], 0 offen sc1\n\t" \
  "s_mov_b32 s96, s46\n\t" \
  "v_mov_b32 v253, v0\n\t" \
  "v_and_b32 v247, 15, v254\n\t" \
  "s_bfe_u32 s43, s2, 0x10003\n\t" \
  "v_and_b32 v48, 3, v254\n\t" \
  "v_lshl_add_u32 v48, v48, 2, v245\n\t" \
  "s_lshl_b32 s54, s3, 8\n\t" \
  "s_add_u32 s55, s54, 147456\n\t" \
  "v_lshl_add_u32 v55, v48, 3, s55\n\t" \
  "v_lshlrev_b32 v56, 3, v247\n\t" \
  "v_add_u32 v56, 147456, v56\n\t" \
  "v_and_b32 v48, 0xf0, v0\n\t" \
  "v_lshl_or_b32 v57, v48, 24, v244\n\t" \
  "s_lshl_b32 s52, s52, 8\n\t" \
  "s_lshl_b32 s55, s40, 14\n\t" \
  "s_add_u32 s52, s52, s55\n\t" \
  "s_add_u32 s52, s52, 2162688\n\t" \
  "v_and_b32 v210, 31, v254\n\t" \
  "v_lshrrev_b32 v48, 5, v254\n\t" \
  "v_lshlrev_b32 v58, 4, v48\n\t" \
  "v_lshl_or_b32 v58, v210, 26, v58\n\t" \
  "s_lshr_b32 s46, s46, 7\n\t" \
  "s_add_u32 s46, s46, 2097152\n\t" \
  "v_cvt_f32_u32 v251, v48\n\t" \
  "v_lshlrev_b32 v248, 4, v210\n\t" \
  "v_lshl_or_b32 v248, v48, 13, v248\n\t" \
  "v_lshlrev_b32 v210, 2, v210\n\t" \
  "v_lshlrev_b32 v249, 4, v247\n\t" \
  "s_lshl_b32 s55, s3, 17\n\t" \
  "s_lshl_b32 s49, s43, 18\n\t" \
  "s_xor_b32 s55, s55, s49\n\t" \
  "buffer_load_dwordx4 v[32:35], v244, s[4:7], s96 offen offset:0\n\t" \
  "buffer_load_dwordx4 v[36:39], v244, s[4:7], s96 offen offset:1024\n\t" \
  "buffer_load_dwordx4 v[40:43], v244, s[4:7], s96 offen offset:2048\n\t" \
  "buffer_load_dwordx4 v[44:47], v244, s[4:7], s96 offen offset:3072\n\t" \
  "s_and_b32 s49, s2, 0x38\n\t" \
  "s_lshl2_add_u32 s55, s44, s55\n\t" \
  "s_lshl_b32 s49, s49, 5\n\t" \
  "s_add_u32 s55, s55, s49\n\t" \
  "v_lshlrev_b32 v48, 5, v245\n\t" \
  "v_xor_b32 v48, v48, v249\n\t" \
  "v_lshl_add_u32 v48, v245, 13, v48\n\t" \
  "v_add_u32 v200, s55, v48\n\t" \
  "v_xor_b32 v48, 0x80, v200\n\t" \
  "v_add_u32 v201, 31744, v200\n\t" \
  "v_add_u32 v202, 63488, v48\n\t" \
  "v_add_u32 v203, 95232, v48\n\t" \
  "s_add_u32 s76, s38, 32768\n\t" \
  "s_lshl_b32 s49, s42, 19\n\t" \
  "s_or_b32 s49, s49, s78\n\t" \
  "s_xor_b32 s80, s49, 1048576\n\t" \
  "s_add_u32 s55, s42, 1\n\t" \
  "s_and_b32 s55, s55, 3\n\t" \
  "s_xor_b32 s55, s55, s42\n\t" \
  "s_lshl_b32 s79, s55, 19\n\t" \
  "s_xor_b32 s79, s79, s49\n\t" \
  "s_xor_b32 s81, s79, 1048576\n\t" \
  "s_lshl_b32 s48, s55, 7\n\t" \
  "s_xor_b32 s48, s48, s47\n\t" \
  "s_xor_b32 s88, s47, 256\n\t" \
  "s_xor_b32 s89, s48, 256\n\t" \
  "v_lshl_add_u32 v48, s43, 2, v245\n\t" \
  "v_bfe_u32 v50, v254, 1, 3\n\t" \
  "v_xor_b32 v48, v48, v50\n\t" \
  "v_lshlrev_b32 v48, 4, v48\n\t" \
  "v_lshl_add_u32 v48, v247, 7, v48\n\t" \
  "v_add_u32 v204, s77, v48\n\t" \
  "v_xor_b32 v205, 64, v204\n\t" \
  "v_add_u32 v206, 98304, v204\n\t" \
  "v_add_u32 v207, 98304, v205\n\t" \
  "v_lshlrev_b32 v208, 5, v245\n\t" \
  "v_add_u32 v208, 148480, v208\n\t" \
  "v_lshlrev_b32 v50, 3, v254\n\t" \
  "s_lshl_b32 s55, s43, 7\n\t" \
  "s_or_b32 s55, s55, 256\n\t" \
  "v_xor_b32 v250, s55, v50\n\t" \
  "s_lshl1_add_u32 s55, s54, 148480\n\t" \
  "v_add_u32 v250, s55, v250\n\t" \
  "v_lshl_add_u32 v214, v245, 10, v249\n\t" \
  "v_lshl_add_u32 v214, s43, 12, v214\n\t" \
  "s_lshr_b32 s55, s2, 2\n\t" \
  "s_xor_b32 s55, s55, s3\n\t" \
  "s_and_b32 s55, s55, 1\n\t" \
  "s_lshl1_add_u32 s55, s3, s55\n\t" \
  "s_lshl_b32 s82, s55, 13\n\t" \
  "s_lshr_b32 s55, s2, 6\n\t" \
  "s_lshl_b32 s55, s55, 20\n\t" \
  "s_add_u32 s82, s82, s55\n\t" \
  "s_add_u32 s82, s82, 4194304\n\t" \
  "s_lshr_b32 s86, s49, 3\n\t" \
  "s_add_u32 s86, s86, s82\n\t" \
  "s_lshr_b32 s55, s87, 9\n\t" \
  "s_add_u32 s86, s86, s55\n\t" \
  "s_lshl2_add_u32 s84, s98, s3\n\t" \
  "s_lshl_b32 s84, s84, 7\n\t" \
  "s_add_u32 s84, s84, 2494464\n\t" \
  "s_bfe_u32 s55, s2, 0x30003\n\t" \
  "s_lshl3_add_u32 s55, s40, s55\n\t" \
  "s_lshl2_add_u32 s85, s55, s84\n\t" \
  "s_lshl_b32 s55, s43, 13\n\t" \
  "s_sub_u32 s83, 4096, s55\n\t" \
  "v_and_b32 v50, 0xf8, v50\n\t" \
  "v_bfe_u32 v48, v254, 2, 2\n\t" \
  "v_lshl_add_u32 v50, v48, 8, v50\n\t" \
  "v_lshl_add_u32 v50, v245, 11, v50\n\t" \
  "v_add_u32 v184, 32768, v50\n\t" \
  "v_xor_b32 v185, 32, v184\n\t" \
  "v_xor_b32 v186, 64, v184\n\t" \
  "v_xor_b32 v187, 96, v184\n\t" \
  "v_xor_b32 v188, 128, v184\n\t" \
  "v_xor_b32 v189, 160, v184\n\t" \
  "v_xor_b32 v190, 192, v184\n\t" \
  "v_xor_b32 v191, 224, v184\n\t" \
  "s_lshl_b32 s51, s98, 12\n\t" \
  "s_add_u32 s51, s51, s54\n\t" \
  "s_add_u32 s51, s51, 2097152\n\t" \
  "s_lshr_b32 s55, s87, 7\n\t" \
  "s_add_u32 s51, s51, s55\n\t" \
  "s_add_u32 s55, s42, s3\n\t" \
  "s_and_b32 s55, s55, 3\n\t" \
  "s_lshl2_add_u32 s55, s40, s55\n\t" \
  "s_lshl_b32 s55, s55, 9\n\t" \
  "s_add_u32 s53, s55, 2162688\n\t" \
  "v_min_u32 v49, 2, v49\n\t" \
  "s_bfe_u32 s55, s2, 0x10002\n\t" \
  "s_lshl2_add_u32 s55, s40, s55\n\t" \
  "s_add_u32 s55, s55, 4\n\t" \
  "v_lshl_add_u32 v49, v49, 1, s55\n\t" \
  "v_lshl_add_u32 v215, v49, 19, v54\n\t" \
  "v_add_u32 v216, 3145728, v215\n\t" \
  "v_and_b32 v215, 0x7fffff, v215\n\t" \
  "v_and_b32 v216, 0x7fffff, v216\n\t" \
  "s_mov_b32 s98, 0xffff0000\n\t" \
  "s_mov_b32 s45, 0x05040100\n\t" \
  "s_mov_b32 s50, 0x07060302\n\t" \
  "s_lshr_b32 s44, s2, 6\n\t" \
  "s_lshl_b32 s44, s44, 9\n\t" \
  "v_lshlrev_b32 v53, 1, v0\n\t" \
  "s_waitcnt vmcnt(9)\n\t" \
  "v_lshlrev_b32 v20, 16, v2\n\t" \
  "v_and_b32 v21, s98, v2\n\t" \
  "v_lshlrev_b32 v22, 16, v3\n\t" \
  "v_and_b32 v23, s98, v3\n\t" \
  "v_mul_f32 v24, 0xbfb8aa3b, v20\n\t" \
  "v_mul_f32 v25, 0xbfb8aa3b, v21\n\t" \
  "v_mul_f32 v26, 0xbfb8aa3b, v22\n\t" \
  "v_mul_f32 v27, 0xbfb8aa3b, v23\n\t" \
  "v_exp_f32 v24, v24\n\t" \
  "v_exp_f32 v25, v25\n\t" \
  "v_exp_f32 v26, v26\n\t" \
  "v_exp_f32 v27, v27\n\t" \
  "v_add_f32 v24, 1.0, v24\n\t" \
  "v_add_f32 v25, 1.0, v25\n\t" \
  "v_add_f32 v26, 1.0, v26\n\t" \
  "v_add_f32 v27, 1.0, v27\n\t" \
  "v_rcp_f32 v24, v24\n\t" \
  "v_rcp_f32 v25, v25\n\t" \
  "v_rcp_f32 v26, v26\n\t" \
  "v_rcp_f32 v27, v27\n\t" \
  "v_mul_f32 v20, v24, v20\n\t" \
  "v_mul_f32 v21, v25, v21\n\t" \
  "v_mul_f32 v22, v26, v22\n\t" \
  "v_mul_f32 v23, v27, v23\n\t" \
  "v_cvt_pk_bf16_f32 v2, v20, v21\n\t" \
  "v_cvt_pk_bf16_f32 v3, v22, v23\n\t" \
  FSTP("s_memrealtime s[94:95]\n\t") \
  FSTP("s_memtime s[92:93]\n\t") \
  FSTP("s_waitcnt lgkmcnt(0)\n\t") \
  FSTP("s_or_b32 s94, s94, 1\n\t") \
  FSTP("v_writelane_b32 v255, s94, 54\n\t") \
  FSTP("v_writelane_b32 v255, s92, 55\n\t") \
  "s_waitcnt vmcnt(5)\n\t" \
  FSTA("s_memrealtime s[94:95]\n\t") \
  FSTA("s_memtime s[92:93]\n\t") \
  FSTA("s_waitcnt lgkmcnt(0)\n\t") \
  FSTA("s_or_b32 s94, s94, 1\n\t") \
  FSTA("v_writelane_b32 v255, s94, 2\n\t") \
  FSTA("v_writelane_b32 v255, s92, 3\n\t") \
  "v_perm_b32 v28, v8, v4, s45\n\t" \
  "v_perm_b32 v29, v9, v5, s45\n\t" \
  "v_perm_b32 v30, v10, v6, s45\n\t" \
  "v_perm_b32 v31, v11, v7, s45\n\t" \
  "v_perm_b32 v4, v8, v4, s50\n\t" \
  "v_perm_b32 v5, v9, v5, s50\n\t" \
  "v_perm_b32 v6, v10, v6, s50\n\t" \
  "v_perm_b32 v7, v11, v7, s50\n\t" \
  "v_dot2_f32_bf16 v20, v28, v2, 0\n\t" \
  "v_dot2_f32_bf16 v21, v29, v2, 0\n\t" \
  "v_dot2_f32_bf16 v22, v30, v2, 0\n\t" \
  "v_dot2_f32_bf16 v23, v31, v2, 0\n\t" \
  "v_dot2_f32_bf16 v24, v4, v2, 0\n\t" \
  "v_dot2_f32_bf16 v25, v5, v2, 0\n\t" \
  "v_dot2_f32_bf16 v26, v6, v2, 0\n\t" \
  "v_dot2_f32_bf16 v27, v7, v2, 0\n\t" \
  "v_perm_b32 v28, v16, v12, s45\n\t" \
  "v_perm_b32 v29, v17, v13, s45\n\t" \
  "v_perm_b32 v30, v18, v14, s45\n\t" \
  "v_perm_b32 v31, v19, v15, s45\n\t" \
  "v_perm_b32 v12, v16, v12, s50\n\t" \
  "v_perm_b32 v13, v17, v13, s50\n\t" \
  "v_perm_b32 v14, v18, v14, s50\n\t" \
  "v_perm_b32 v15, v19, v15, s50\n\t" \
  "v_dot2_f32_bf16 v20, v28, v3, v20\n\t" \
  "v_dot2_f32_bf16 v21, v29, v3, v21\n\t" \
  "v_dot2_f32_bf16 v22, v30, v3, v22\n\t" \
  "v_dot2_f32_bf16 v23, v31, v3, v23\n\t" \
  "v_dot2_f32_bf16 v24, v12, v3, v24\n\t" \
  "v_dot2_f32_bf16 v25, v13, v3, v25\n\t" \
  "v_dot2_f32_bf16 v26, v14, v3, v26\n\t" \
  "v_dot2_f32_bf16 v27, v15, v3, v27\n\t" \
  "v_permlane32_swap_b32 v20, v22\n\t" \
  "v_permlane32_swap_b32 v21, v23\n\t" \
  "v_permlane32_swap_b32 v24, v26\n\t" \
  "v_permlane32_swap_b32 v25, v27\n\t" \
  "v_add_f32 v20, v20, v22\n\t" \
  "v_add_f32 v21, v21, v23\n\t" \
  "v_add_f32 v24, v24, v26\n\t" \
  "v_add_f32 v25, v25, v27\n\t" \
  "v_permlane16_swap_b32 v20, v21\n\t" \
  "s_nop 0\n\t" \
  "v_permlane16_swap_b32 v24, v25\n\t" \
  "v_add_f32 v20, v20, v21\n\t" \
  "v_add_f32 v21, v24, v25\n\t" \
  "s_nop 0\n\t" \
  "v_add_f32_dpp v20, v20, v20 row_ror:8 row_mask:0xf bank_mask:0xf\n\t" \
  "v_add_f32_dpp v21, v21, v21 row_ror:8 row_mask:0xf bank_mask:0xf\n\t" \
  "s_nop 0\n\t" \
  "v_add_f32_dpp v20, v20, v20 row_ror:4 row_mask:0xf bank_mask:0xf\n\t" \
  "v_add_f32_dpp v21, v21, v21 row_ror:4 row_mask:0xf bank_mask:0xf\n\t" \
  "ds_write_b64 v55, v[20:21]\n\t" \
  "buffer_load_dwordx4 v212, s[4:7], s57 offen lds\n\t" \
  "buffer_load_dwordx4 v213, s[4:7], s57 offen offset:1024 lds\n\t" \
  "s_add_u32 m0, s90, 2048\n\t" \
  "s_nop 0\n\t" \
  "buffer_load_dwordx4 v212, s[4:7], s58 offen lds\n\t" \
  "buffer_load_dwordx4 v213, s[4:7], s58 offen offset:1024 lds\n\t" \
  "s_xor_b32 m0, s90, 4096\n\t" \
  "s_xor_b32 s97, s57, 262144\n\t" \
  "buffer_load_dwordx4 v212, s[4:7], s97 offen lds\n\t" \
  "buffer_load_dwordx4 v213, s[4:7], s97 offen offset:1024 lds\n\t" \
  "s_xor_b32 m0, s90, 6144\n\t" \
  "s_xor_b32 s97, s57, 393216\n\t" \
  "buffer_load_dwordx4 v212, s[4:7], s97 offen lds\n\t" \
  "buffer_load_dwordx4 v213, s[4:7], s97 offen offset:1024 lds\n\t" \
  "v_mov_b32 v4, 0\n\t" \
  "v_mov_b32 v5, 0\n\t" \
  "v_mov_b32 v6, 0\n\t" \
  "v_mov_b32 v7, 0\n\t" \
  "s_waitcnt vmcnt(12) lgkmcnt(0)\n\t" \
  "s_barrier\n\t" \
  "ds_read_b64 v[8:9], v56 offset:0\n\t" \
  "ds_read_b64 v[10:11], v56 offset:256\n\t" \
  "ds_read_b64 v[12:13], v56 offset:512\n\t" \
  "ds_read_b64 v[14:15], v56 offset:768\n\t" \
  "v_readfirstlane_b32 s36, v1\n\t" \
  "s_or_b32 s36, s36, 0x40004000\n\t" \
  "s_add_u32 s36, s36, 0x9e3739b9\n\t" \
  "s_and_b32 s36, s36, 0xbfffbfff\n\t" \
  "v_mov_b32 v211, s36\n\t" \
  "v_mov_b32 v17, s36\n\t" \
  "v_mov_b32 v19, s36\n\t" \
  "s_waitcnt lgkmcnt(0)\n\t" \
  "v_add_f32 v8, v8, v10\n\t" \
  "v_add_f32 v12, v12, v14\n\t" \
  "v_add_f32 v9, v9, v11\n\t" \
  "v_add_f32 v13, v13, v15\n\t" \
  "v_add_f32 v16, v8, v12\n\t" \
  "v_add_f32 v18, v9, v13\n\t" \
  "buffer_store_dwordx4 v[16:19], v57, s[16:19], s52 offen sc1\n\t" \
  "s_waitcnt vmcnt(9)\n\t" \
  FSTA("s_memrealtime s[94:95]\n\t") \
  FSTA("s_memtime s[92:93]\n\t") \
  FSTA("s_waitcnt lgkmcnt(0)\n\t") \
  FSTA("s_or_b32 s94, s94, 1\n\t") \
  FSTA("v_writelane_b32 v255, s94, 4\n\t") \
  FSTA("v_writelane_b32 v255, s92, 5\n\t") \
  "v_dot2c_f32_bf16 v4, 0x3f803f80, v32\n\t" \
  "v_dot2c_f32_bf16 v6, v32, v32\n\t" \
  "v_dot2c_f32_bf16 v5, 0x3f803f80, v40\n\t" \
  "v_dot2c_f32_bf16 v7, v40, v40\n\t" \
  "v_dot2c_f32_bf16 v4, 0x3f803f80, v33\n\t" \
  "v_dot2c_f32_bf16 v6, v33, v33\n\t" \
  "v_dot2c_f32_bf16 v5, 0x3f803f80, v41\n\t" \
  "v_dot2c_f32_bf16 v7, v41, v41\n\t" \
  "v_dot2c_f32_bf16 v4, 0x3f803f80, v34\n\t" \
  "v_dot2c_f32_bf16 v6, v34, v34\n\t" \
  "v_dot2c_f32_bf16 v5, 0x3f803f80, v42\n\t" \
  "v_dot2c_f32_bf16 v7, v42, v42\n\t" \
  "v_dot2c_f32_bf16 v4, 0x3f803f80, v35\n\t" \
  "v_dot2c_f32_bf16 v6, v35, v35\n\t" \
  "v_dot2c_f32_bf16 v5, 0x3f803f80, v43\n\t" \
  "v_dot2c_f32_bf16 v7, v43, v43\n\t" \
  "v_dot2c_f32_bf16 v4, 0x3f803f80, v36\n\t" \
  "v_dot2c_f32_bf16 v6, v36, v36\n\t" \
  "v_dot2c_f32_bf16 v5, 0x3f803f80, v44\n\t" \
  "v_dot2c_f32_bf16 v7, v44, v44\n\t" \
  "v_dot2c_f32_bf16 v4, 0x3f803f80, v37\n\t" \
  "v_dot2c_f32_bf16 v6, v37, v37\n\t" \
  "v_dot2c_f32_bf16 v5, 0x3f803f80, v45\n\t" \
  "v_dot2c_f32_bf16 v7, v45, v45\n\t" \
  "v_dot2c_f32_bf16 v4, 0x3f803f80, v38\n\t" \
  "v_dot2c_f32_bf16 v6, v38, v38\n\t" \
  "v_dot2c_f32_bf16 v5, 0x3f803f80, v46\n\t" \
  "v_dot2c_f32_bf16 v7, v46, v46\n\t" \
  "v_dot2c_f32_bf16 v4, 0x3f803f80, v39\n\t" \
  "v_dot2c_f32_bf16 v6, v39, v39\n\t" \
  "v_dot2c_f32_bf16 v5, 0x3f803f80, v47\n\t" \
  "v_dot2c_f32_bf16 v7, v47, v47\n\t" \
  "v_mov_b32 v21, s36\n\t" \
  "v_mov_b32 v23, s36\n\t" \
  "v_permlane32_swap_b32 v4, v5\n\t" \
  "v_permlane32_swap_b32 v6, v7\n\t" \
  "v_add_f32 v4, v4, v5\n\t" \
  "v_add_f32 v6, v6, v7\n\t" \
  "s_nop 1\n\t" \
  "v_permlane16_swap_b32 v4, v6\n\t" \
  "v_add_f32 v4, v4, v6\n\t" \
  "s_nop 1\n\t" \
  "v_add_f32_dpp v4, v4, v4 quad_perm:[1,0,3,2] row_mask:0xf bank_mask:0xf\n\t" \
  "s_nop 1\n\t" \
  "v_add_f32_dpp v4, v4, v4 quad_perm:[2,3,0,1] row_mask:0xf bank_mask:0xf\n\t" \
  "s_nop 1\n\t" \
  "v_add_f32_dpp v4, v4, v4 row_half_mirror row_mask:0xf bank_mask:0xf\n\t" \
  "s_nop 1\n\t" \
  "v_add_f32_dpp v4, v4, v4 row_mirror row_mask:0xf bank_mask:0xf\n\t" \
  "v_mul_f32 v4, 0x3a800000, v4\n\t" \
  "v_mov_b32 v5, v4\n\t" \
  "s_nop 1\n\t" \
  "v_permlane16_swap_b32 v4, v5\n\t" \
  "v_fma_f32 v5, -v4, v4, v5\n\t" \
  "v_max_f32 v5, 0, v5\n\t" \
  "v_add_f32 v5, 0x3727c5ac, v5\n\t" \
  "v_rsq_f32 v20, v5\n\t" \
  "s_nop 0\n\t" \
  "v_mul_f32_e64 v22, v20, -v4\n\t" \
  "buffer_store_dwordx4 v[20:23], v58, s[16:19], s46 offen sc1\n\t" \
  FSTA("s_memrealtime s[94:95]\n\t") \
  FSTA("s_memtime s[92:93]\n\t") \
  FSTA("s_waitcnt lgkmcnt(0)\n\t") \
  FSTA("s_or_b32 s94, s94, 1\n\t") \
  FSTA("v_writelane_b32 v255, s94, 6\n\t") \
  FSTA("v_writelane_b32 v255, s92, 7\n\t") \
  "buffer_load_ushort v218, v53, s[12:15], s44 offen\n\t" \
  "s_add_u32 m0, s76, 0\n\t" \
  "s_nop 0\n\t" \
  "buffer_load_dwordx4 v200, s[20:23], s49 offen offset:0 lds\n\t" \
  "buffer_load_dwordx4 v201, s[20:23], s49 offen offset:1024 lds\n\t" \
  "buffer_load_dwordx4 v202, s[20:23], s49 offen offset:2048 lds\n\t" \
  "buffer_load_dwordx4 v203, s[20:23], s49 offen offset:3072 lds\n\t" \
  "s_add_u32 m0, s77, 49152\n\t" \
  "s_xor_b32 s54, s87, 0\n\t" \
  "s_or_b32 s54, s54, s48\n\t" \
  "buffer_load_dwordx4 v212, s[4:7], s54 offen lds\n\t" \
  "buffer_load_dwordx4 v213, s[4:7], s54 offen offset:1024 lds\n\t" \
  "s_add_u32 m0, s77, 51200\n\t" \
  "s_xor_b32 s54, s87, 131072\n\t" \
  "s_or_b32 s54, s54, s48\n\t" \
  "buffer_load_dwordx4 v212, s[4:7], s54 offen lds\n\t" \
  "buffer_load_dwordx4 v213, s[4:7], s54 offen offset:1024 lds\n\t" \
  "s_add_u32 m0, s77, 53248\n\t" \
  "s_xor_b32 s54, s87, 262144\n\t" \
  "s_or_b32 s54, s54, s48\n\t" \
  "buffer_load_dwordx4 v212, s[4:7], s54 offen lds\n\t" \
  "buffer_load_dwordx4 v213, s[4:7], s54 offen offset:1024 lds\n\t" \
  "s_add_u32 m0, s77, 55296\n\t" \
  "s_xor_b32 s54, s87, 393216\n\t" \
  "s_or_b32 s54, s54, s48\n\t" \
  "buffer_load_dwordx4 v212, s[4:7], s54 offen lds\n\t" \
  "buffer_load_dwordx4 v213, s[4:7], s54 offen offset:1024 lds\n\t" \
  "s_add_u32 m0, s76, 49152\n\t" \
  "s_nop 0\n\t" \
  "buffer_load_dwordx4 v200, s[20:23], s79 offen offset:0 lds\n\t" \
  "buffer_load_dwordx4 v201, s[20:23], s79 offen offset:1024 lds\n\t" \
  "buffer_load_dwordx4 v202, s[20:23], s79 offen offset:2048 lds\n\t" \
  "buffer_load_dwordx4 v203, s[20:23], s79 offen offset:3072 lds\n\t" \
  "v_add_u32 v192, 65536, v184\n\t" \
  "v_add_u32 v193, 65536, v185\n\t" \
  "v_add_u32 v194, 65536, v186\n\t" \
  "v_add_u32 v195, 65536, v187\n\t" \
  "v_add_u32 v196, 65536, v188\n\t" \
  "v_add_u32 v197, 65536, v189\n\t" \
  "v_add_u32 v198, 65536, v190\n\t" \
  "v_add_u32 v199, 65536, v191\n\t" \
  "v_lshrrev_b32 v48, 3, v253\n\t" \
  "s_lshr_b32 s54, s2, 6\n\t" \
  "v_lshl_add_u32 v48, s54, 5, v48\n\t" \
  "v_mul_u32_u24 v48, 98304, v48\n\t" \
  "v_and_b32 v49, 0x70, v244\n\t" \
  "s_bfe_u32 s54, s2, 0x10002\n\t" \
  "s_lshl1_add_u32 s54, s40, s54\n\t" \
  "s_lshl_b32 s54, s54, 10\n\t" \
  "s_and_b32 s55, s2, 0x38\n\t" \
  "s_lshl4_add_u32 s54, s55, s54\n\t" \
  "s_add_u32 s54, s54, 4096\n\t" \
  "v_add3_u32 v209, v48, v49, s54\n\t" \
  "v_lshlrev_b32 v219, 2, v253\n\t" \
  "v_add_u32 v219, 151552, v219\n\t" \
  "s_and_b32 s54, s39, 15\n\t" \
  "s_lshl2_add_u32 s54, s54, s3\n\t" \
  "s_lshl_b32 s54, s54, 11\n\t" \
  "s_lshl_b32 s55, s40, 19\n\t" \
  "s_add_u32 s54, s54, s55\n\t" \
  "v_lshl_add_u32 v217, v247, 7, s54\n\t" \
  "s_mov_b64 exec, 0\n\t" \
  "s_cmp_eq_u32 s3, 0\n\t" \
  "s_cbranch_scc1 .Lhc00\n\t" \
  "s_cmp_eq_u32 s3, 1\n\t" \
  "s_cbranch_scc1 .Lhc01\n\t" \
  "s_cmp_eq_u32 s3, 2\n\t" \
  "s_cbranch_scc1 .Lhc02\n\t" \
  "s_branch .Lhc03\n\t" \
  ".Lhr0:\n\t" \
  "s_mov_b64 exec, -1\n\t" \
  "s_sleep 6\n\t" \
  "s_add_u32 s58, s53, 16384\n\t" \
  "s_add_u32 s90, s53, 32768\n\t" \
  "s_add_u32 s91, s53, 49152\n\t" \
  "s_xor_b32 s96, s51, 0\n\t" \
  "s_xor_b32 s97, s51, 1024\n\t" \
  "s_xor_b32 s99, s51, 2048\n\t" \
  "s_xor_b32 s57, s51, 3072\n\t" \
  "buffer_load_dwordx4 v[128:131], v248, s[16:19], s53 offen sc1\n\t" \
  "buffer_load_dwordx4 v[132:135], v248, s[16:19], s58 offen sc1\n\t" \
  "buffer_load_dwordx4 v[136:139], v248, s[16:19], s90 offen sc1\n\t" \
  "buffer_load_dwordx4 v[140:143], v248, s[16:19], s91 offen sc1\n\t" \
  "buffer_load_dwordx4 v[160:163], v249, s[16:19], s96 offen sc1\n\t" \
  "buffer_load_dwordx4 v[164:167], v249, s[16:19], s97 offen sc1\n\t" \
  "buffer_load_dwordx4 v[168:171], v249, s[16:19], s99 offen sc1\n\t" \
  "buffer_load_dwordx4 v[172:175], v249, s[16:19], s57 offen sc1\n\t" \
  "s_bitcmp1_b32 s2, 7\n\t" \
  "s_cselect_b64 s[12:13], s[64:65], s[60:61]\n\t" \
  FSTP("s_memrealtime s[94:95]\n\t") \
  FSTP("s_memtime s[92:93]\n\t") \
  FSTP("s_waitcnt lgkmcnt(0)\n\t") \
  FSTP("s_or_b32 s94, s94, 1\n\t") \
  FSTP("v_writelane_b32 v255, s94, 56\n\t") \
  FSTP("v_writelane_b32 v255, s92, 57\n\t") \
  "s_waitcnt vmcnt(12)\n\t" \
  "ds_read_b128 v[16:19], v204 offset:0\n\t" \
  "ds_read_b128 v[48:51], v204 offset:2048\n\t" \
  "ds_read_b128 v[80:83], v204 offset:4096\n\t" \
  "ds_read_b128 v[112:115], v204 offset:6144\n\t" \
  "ds_read_b128 v[20:23], v205 offset:0\n\t" \
  "ds_read_b128 v[52:55], v205 offset:2048\n\t" \
  "ds_read_b128 v[84:87], v205 offset:4096\n\t" \
  "ds_read_b128 v[116:119], v205 offset:6144\n\t" \
  "ds_read_b128 v[24:27], v204 offset:49152\n\t" \
  "ds_read_b128 v[56:59], v204 offset:51200\n\t" \
  "ds_read_b128 v[88:91], v204 offset:53248\n\t" \
  "ds_read_b128 v[120:123], v204 offset:55296\n\t" \
  "ds_read_b128 v[28:31], v205 offset:49152\n\t" \
  "ds_read_b128 v[60:63], v205 offset:51200\n\t" \
  "ds_read_b128 v[92:95], v205 offset:53248\n\t" \
  "ds_read_b128 v[124:127], v205 offset:55296\n\t" \
  "v_lshlrev_b32 v218, 16, v218\n\t" \
  "v_mul_f32 v145, 0xbfb8aa3b, v218\n\t" \
  "v_exp_f32 v145, v145\n\t" \
  "s_nop 0\n\t" \
  "v_add_f32 v145, 1.0, v145\n\t" \
  "v_rcp_f32 v145, v145\n\t" \
  "s_nop 0\n\t" \
  "v_mul_f32 v218, v145, v218\n\t" \
  "v_cvt_pk_bf16_f32 v218, v218, v218\n\t" \
  "v_lshlrev_b32 v218, 16, v218\n\t" \
  "s_waitcnt lgkmcnt(12)\n\t" \
  "v_lshlrev_b32 v0, 16, v16\n\t" \
  "v_and_b32 v1, s98, v16\n\t" \
  "v_lshlrev_b32 v2, 16, v17\n\t" \
  "v_and_b32 v3, s98, v17\n\t" \
  "v_lshlrev_b32 v4, 16, v18\n\t" \
  "v_and_b32 v5, s98, v18\n\t" \
  "v_lshlrev_b32 v6, 16, v19\n\t" \
  "v_and_b32 v7, s98, v19\n\t" \
  "v_lshlrev_b32 v32, 16, v48\n\t" \
  "v_and_b32 v33, s98, v48\n\t" \
  "v_lshlrev_b32 v34, 16, v49\n\t" \
  "v_and_b32 v35, s98, v49\n\t" \
  "v_lshlrev_b32 v36, 16, v50\n\t" \
  "v_and_b32 v37, s98, v50\n\t" \
  "v_lshlrev_b32 v38, 16, v51\n\t" \
  "v_and_b32 v39, s98, v51\n\t" \
  "v_lshlrev_b32 v64, 16, v80\n\t" \
  "v_and_b32 v65, s98, v80\n\t" \
  "v_lshlrev_b32 v66, 16, v81\n\t" \
  "v_and_b32 v67, s98, v81\n\t" \
  "v_lshlrev_b32 v68, 16, v82\n\t" \
  "v_and_b32 v69, s98, v82\n\t" \
  "v_lshlrev_b32 v70, 16, v83\n\t" \
  "v_and_b32 v71, s98, v83\n\t" \
  "v_lshlrev_b32 v96, 16, v112\n\t" \
  "v_and_b32 v97, s98, v112\n\t" \
  "v_lshlrev_b32 v98, 16, v113\n\t" \
  "v_and_b32 v99, s98, v113\n\t" \
  "v_lshlrev_b32 v100, 16, v114\n\t" \
  "v_and_b32 v101, s98, v114\n\t" \
  "v_lshlrev_b32 v102, 16, v115\n\t" \
  "v_and_b32 v103, s98, v115\n\t" \
  "s_waitcnt lgkmcnt(8)\n\t" \
  "v_lshlrev_b32 v8, 16, v20\n\t" \
  "v_and_b32 v9, s98, v20\n\t" \
  "v_lshlrev_b32 v10, 16, v21\n\t" \
  "v_and_b32 v11, s98, v21\n\t" \
  "v_lshlrev_b32 v12, 16, v22\n\t" \
  "v_and_b32 v13, s98, v22\n\t" \
  "v_lshlrev_b32 v14, 16, v23\n\t" \
  "v_and_b32 v15, s98, v23\n\t" \
  "v_lshlrev_b32 v40, 16, v52\n\t" \
  "v_and_b32 v41, s98, v52\n\t" \
  "v_lshlrev_b32 v42, 16, v53\n\t" \
  "v_and_b32 v43, s98, v53\n\t" \
  "v_lshlrev_b32 v44, 16, v54\n\t" \
  "v_and_b32 v45, s98, v54\n\t" \
  "v_lshlrev_b32 v46, 16, v55\n\t" \
  "v_and_b32 v47, s98, v55\n\t" \
  "v_lshlrev_b32 v72, 16, v84\n\t" \
  "v_and_b32 v73, s98, v84\n\t" \
  "v_lshlrev_b32 v74, 16, v85\n\t" \
  "v_and_b32 v75, s98, v85\n\t" \
  "v_lshlrev_b32 v76, 16, v86\n\t" \
  "v_and_b32 v77, s98, v86\n\t" \
  "v_lshlrev_b32 v78, 16, v87\n\t" \
  "v_and_b32 v79, s98, v87\n\t" \
  "v_lshlrev_b32 v104, 16, v116\n\t" \
  "v_and_b32 v105, s98, v116\n\t" \
  "v_lshlrev_b32 v106, 16, v117\n\t" \
  "v_and_b32 v107, s98, v117\n\t" \
  "v_lshlrev_b32 v108, 16, v118\n\t" \
  "v_and_b32 v109, s98, v118\n\t" \
  "v_lshlrev_b32 v110, 16, v119\n\t" \
  "v_and_b32 v111, s98, v119\n\t" \
  FSTP("s_memrealtime s[94:95]\n\t") \
  FSTP("s_memtime s[92:93]\n\t") \
  FSTP("s_waitcnt lgkmcnt(0)\n\t") \
  FSTP("s_or_b32 s94, s94, 1\n\t") \
  FSTP("v_writelane_b32 v255, s94, 58\n\t") \
  FSTP("v_writelane_b32 v255, s92, 59\n\t") \
  "s_waitcnt vmcnt(0)\n\t" \
  "v_xor_b32 v144, v211, v129\n\t" \
  "v_bitop3_b32 v144, v144, v131, v211 bitop3:0xf6\n\t" \
  "v_bitop3_b32 v144, v144, v133, v211 bitop3:0xf6\n\t" \
  "v_bitop3_b32 v144, v144, v135, v211 bitop3:0xf6\n\t" \
  "v_bitop3_b32 v144, v144, v137, v211 bitop3:0xf6\n\t" \
  "v_bitop3_b32 v144, v144, v139, v211 bitop3:0xf6\n\t" \
  "v_bitop3_b32 v144, v144, v141, v211 bitop3:0xf6\n\t" \
  "v_bitop3_b32 v144, v144, v143, v211 bitop3:0xf6\n\t" \
  "v_bitop3_b32 v144, v144, v161, v211 bitop3:0xf6\n\t" \
  "v_bitop3_b32 v144, v144, v163, v211 bitop3:0xf6\n\t" \
  "v_bitop3_b32 v144, v144, v165, v211 bitop3:0xf6\n\t" \
  "v_bitop3_b32 v144, v144, v167, v211 bitop3:0xf6\n\t" \
  "v_bitop3_b32 v144, v144, v169, v211 bitop3:0xf6\n\t" \
  "v_bitop3_b32 v144, v144, v171, v211 bitop3:0xf6\n\t" \
  "v_bitop3_b32 v144, v144, v173, v211 bitop3:0xf6\n\t" \
  "v_bitop3_b32 v144, v144, v175, v211 bitop3:0xf6\n\t" \
  "v_cmp_ne_u32 vcc, 0, v144\n\t" \
  "v_add_f32 v146, v128, v132\n\t" \
  "v_add_f32 v148, v136, v140\n\t" \
  "v_add_f32 v147, v130, v134\n\t" \
  "v_add_f32 v149, v138, v142\n\t" \
  "v_add_f32 v146, v146, v148\n\t" \
  "v_add_f32 v147, v147, v149\n\t" \
  "s_cbranch_vccnz .Lfr_s\n\t" \
  ".Lfr_k:\n\t" \
  FSTA("s_memrealtime s[94:95]\n\t") \
  FSTA("s_memtime s[92:93]\n\t") \
  FSTA("s_waitcnt lgkmcnt(0)\n\t") \
  FSTA("s_or_b32 s94, s94, 1\n\t") \
  FSTA("v_writelane_b32 v255, s94, 8\n\t") \
  FSTA("v_writelane_b32 v255, s92, 9\n\t") \
  "v_cvt_pk_bf16_f32 v147, v146, v147\n\t" \
  "v_lshlrev_b32 v146, 16, v147\n\t" \
  "v_and_b32 v147, s98, v147\n\t" \
  "v_add_f32 v146, v146, v251\n\t" \
  "v_add_f32 v147, v147, v251\n\t" \
  "v_cvt_pk_bf16_f32 v147, v146, v147\n\t" \
  "v_lshlrev_b32 v146, 16, v147\n\t" \
  "v_and_b32 v147, s98, v147\n\t" \
  "v_mul_f32 v146, 0x3e0293ee, v146\n\t" \
  "v_mul_f32 v147, 0x3e0293ee, v147\n\t" \
  "ds_write_b64 v250, v[146:147]\n\t" \
  "v_pk_fma_f32 v[0:1], v[0:1], v[160:161], v[162:163] op_sel_hi:[1,0,0]\n\t" \
  "v_pk_fma_f32 v[2:3], v[2:3], v[160:161], v[162:163] op_sel_hi:[1,0,0]\n\t" \
  "v_pk_fma_f32 v[4:5], v[4:5], v[160:161], v[162:163] op_sel_hi:[1,0,0]\n\t" \
  "v_pk_fma_f32 v[6:7], v[6:7], v[160:161], v[162:163] op_sel_hi:[1,0,0]\n\t" \
  "v_pk_fma_f32 v[32:33], v[32:33], v[164:165], v[166:167] op_sel_hi:[1,0,0]\n\t" \
  "v_pk_fma_f32 v[34:35], v[34:35], v[164:165], v[166:167] op_sel_hi:[1,0,0]\n\t" \
  "v_pk_fma_f32 v[36:37], v[36:37], v[164:165], v[166:167] op_sel_hi:[1,0,0]\n\t" \
  "v_pk_fma_f32 v[38:39], v[38:39], v[164:165], v[166:167] op_sel_hi:[1,0,0]\n\t" \
  "v_pk_fma_f32 v[64:65], v[64:65], v[168:169], v[170:171] op_sel_hi:[1,0,0]\n\t" \
  "v_pk_fma_f32 v[66:67], v[66:67], v[168:169], v[170:171] op_sel_hi:[1,0,0]\n\t" \
  "v_pk_fma_f32 v[68:69], v[68:69], v[168:169], v[170:171] op_sel_hi:[1,0,0]\n\t" \
  "v_pk_fma_f32 v[70:71], v[70:71], v[168:169], v[170:171] op_sel_hi:[1,0,0]\n\t" \
  "v_pk_fma_f32 v[96:97], v[96:97], v[172:173], v[174:175] op_sel_hi:[1,0,0]\n\t" \
  "v_pk_fma_f32 v[98:99], v[98:99], v[172:173], v[174:175] op_sel_hi:[1,0,0]\n\t" \
  "v_pk_fma_f32 v[100:101], v[100:101], v[172:173], v[174:175] op_sel_hi:[1,0,0]\n\t" \
  "v_pk_fma_f32 v[102:103], v[102:103], v[172:173], v[174:175] op_sel_hi:[1,0,0]\n\t" \
  FSTP("s_memrealtime s[94:95]\n\t") \
  FSTP("s_memtime s[92:93]\n\t") \
  FSTP("s_waitcnt lgkmcnt(0)\n\t") \
  FSTP("s_or_b32 s94, s94, 1\n\t") \
  FSTP("v_writelane_b32 v255, s94, 24\n\t") \
  FSTP("v_writelane_b32 v255, s92, 25\n\t") \
  "s_waitcnt lgkmcnt(0)\n\t" \
  "s_barrier\n\t" \
  FSTP("s_memrealtime s[94:95]\n\t") \
  FSTP("s_memtime s[92:93]\n\t") \
  FSTP("s_waitcnt lgkmcnt(0)\n\t") \
  FSTP("s_or_b32 s94, s94, 1\n\t") \
  FSTP("v_writelane_b32 v255, s94, 26\n\t") \
  FSTP("v_writelane_b32 v255, s92, 27\n\t") \
  "ds_read_b128 v[128:131], v208 offset:0\n\t" \
  "ds_read_b128 v[132:135], v208 offset:16\n\t" \
  "ds_read_b128 v[136:139], v208 offset:256\n\t" \
  "ds_read_b128 v[140:143], v208 offset:272\n\t" \
  "ds_read_b128 v[144:147], v208 offset:128\n\t" \
  "ds_read_b128 v[148:151], v208 offset:144\n\t" \
  "ds_read_b128 v[152:155], v208 offset:384\n\t" \
  "ds_read_b128 v[156:159], v208 offset:400\n\t" \
  "global_load_dword v246, v215, s[20:21]\n\t" \
  "v_pk_fma_f32 v[8:9], v[8:9], v[160:161], v[162:163] op_sel_hi:[1,0,0]\n\t" \
  "v_pk_fma_f32 v[10:11], v[10:11], v[160:161], v[162:163] op_sel_hi:[1,0,0]\n\t" \
  "v_pk_fma_f32 v[12:13], v[12:13], v[160:161], v[162:163] op_sel_hi:[1,0,0]\n\t" \
  "v_pk_fma_f32 v[14:15], v[14:15], v[160:161], v[162:163] op_sel_hi:[1,0,0]\n\t" \
  "v_pk_fma_f32 v[40:41], v[40:41], v[164:165], v[166:167] op_sel_hi:[1,0,0]\n\t" \
  "v_pk_fma_f32 v[42:43], v[42:43], v[164:165], v[166:167] op_sel_hi:[1,0,0]\n\t" \
  "v_pk_fma_f32 v[44:45], v[44:45], v[164:165], v[166:167] op_sel_hi:[1,0,0]\n\t" \
  "v_pk_fma_f32 v[46:47], v[46:47], v[164:165], v[166:167] op_sel_hi:[1,0,0]\n\t" \
  "v_pk_fma_f32 v[72:73], v[72:73], v[168:169], v[170:171] op_sel_hi:[1,0,0]\n\t" \
  "v_pk_fma_f32 v[74:75], v[74:75], v[168:169], v[170:171] op_sel_hi:[1,0,0]\n\t" \
  "v_pk_fma_f32 v[76:77], v[76:77], v[168:169], v[170:171] op_sel_hi:[1,0,0]\n\t" \
  "v_pk_fma_f32 v[78:79], v[78:79], v[168:169], v[170:171] op_sel_hi:[1,0,0]\n\t" \
  "v_pk_fma_f32 v[104:105], v[104:105], v[172:173], v[174:175] op_sel_hi:[1,0,0]\n\t" \
  "v_pk_fma_f32 v[106:107], v[106:107], v[172:173], v[174:175] op_sel_hi:[1,0,0]\n\t" \
  "v_pk_fma_f32 v[108:109], v[108:109], v[172:173], v[174:175] op_sel_hi:[1,0,0]\n\t" \
  "v_pk_fma_f32 v[110:111], v[110:111], v[172:173], v[174:175] op_sel_hi:[1,0,0]\n\t" \
  "s_xor_b32 s97, s86, 0\n\t" \
  "s_waitcnt lgkmcnt(4)\n\t" \
  "v_pk_fma_f32 v[0:1], v[0:1], v[128:129], v[136:137]\n\t" \
  "v_pk_fma_f32 v[2:3], v[2:3], v[130:131], v[138:139]\n\t" \
  "v_pk_fma_f32 v[4:5], v[4:5], v[132:133], v[140:141]\n\t" \
  "v_pk_fma_f32 v[6:7], v[6:7], v[134:135], v[142:143]\n\t" \
  "v_cvt_pk_bf16_f32 v16, v0, v1\n\t" \
  "v_cvt_pk_bf16_f32 v17, v2, v3\n\t" \
  "v_cvt_pk_bf16_f32 v18, v4, v5\n\t" \
  "v_cvt_pk_bf16_f32 v19, v6, v7\n\t" \
  "buffer_store_dwordx4 v[16:19], v214, s[16:19], s97 offen sc1\n\t" \
  "s_xor_b32 s97, s86, 256\n\t" \
  "v_pk_fma_f32 v[32:33], v[32:33], v[128:129], v[136:137]\n\t" \
  "v_pk_fma_f32 v[34:35], v[34:35], v[130:131], v[138:139]\n\t" \
  "v_pk_fma_f32 v[36:37], v[36:37], v[132:133], v[140:141]\n\t" \
  "v_pk_fma_f32 v[38:39], v[38:39], v[134:135], v[142:143]\n\t" \
  "v_cvt_pk_bf16_f32 v48, v32, v33\n\t" \
  "v_cvt_pk_bf16_f32 v49, v34, v35\n\t" \
  "v_cvt_pk_bf16_f32 v50, v36, v37\n\t" \
  "v_cvt_pk_bf16_f32 v51, v38, v39\n\t" \
  "buffer_store_dwordx4 v[48:51], v214, s[16:19], s97 offen sc1\n\t" \
  "s_xor_b32 s97, s86, 512\n\t" \
  "v_pk_fma_f32 v[64:65], v[64:65], v[128:129], v[136:137]\n\t" \
  "v_pk_fma_f32 v[66:67], v[66:67], v[130:131], v[138:139]\n\t" \
  "v_pk_fma_f32 v[68:69], v[68:69], v[132:133], v[140:141]\n\t" \
  "v_pk_fma_f32 v[70:71], v[70:71], v[134:135], v[142:143]\n\t" \
  "v_cvt_pk_bf16_f32 v80, v64, v65\n\t" \
  "v_cvt_pk_bf16_f32 v81, v66, v67\n\t" \
  "v_cvt_pk_bf16_f32 v82, v68, v69\n\t" \
  "v_cvt_pk_bf16_f32 v83, v70, v71\n\t" \
  ".Lhs0:\n\ts_cbranch_execz .Lhs0n\n\t" \
  "buffer_store_dwordx4 v[80:83], v214, s[16:19], s97 offen sc1\n\t" \
  "s_xor_b32 s97, s86, 768\n\t" \
  "v_pk_fma_f32 v[96:97], v[96:97], v[128:129], v[136:137]\n\t" \
  "v_pk_fma_f32 v[98:99], v[98:99], v[130:131], v[138:139]\n\t" \
  "v_pk_fma_f32 v[100:101], v[100:101], v[132:133], v[140:141]\n\t" \
  "v_pk_fma_f32 v[102:103], v[102:103], v[134:135], v[142:143]\n\t" \
  "v_cvt_pk_bf16_f32 v112, v96, v97\n\t" \
  "v_cvt_pk_bf16_f32 v113, v98, v99\n\t" \
  "v_cvt_pk_bf16_f32 v114, v100, v101\n\t" \
  "v_cvt_pk_bf16_f32 v115, v102, v103\n\t" \
  "buffer_store_dwordx4 v[112:115], v214, s[16:19], s97 offen sc1\n\t" \
  "ds_read_b128 v[128:131], v208 offset:512\n\t" \
  "ds_read_b128 v[132:135], v208 offset:528\n\t" \
  "ds_read_b128 v[136:139], v208 offset:768\n\t" \
  "ds_read_b128 v[140:143], v208 offset:784\n\t" \
  FSTP("s_memrealtime s[94:95]\n\t") \
  FSTP("s_memtime s[92:93]\n\t") \
  "s_add_u32 m0, s77, 98304\n\t" \
  "s_xor_b32 s96, s87, 0\n\t" \
  "s_add_u32 s96, s96, s88\n\t" \
  "s_waitcnt lgkmcnt(4)\n\t" \
  "v_pk_fma_f32 v[8:9], v[8:9], v[144:145], v[152:153]\n\t" \
  "v_pk_fma_f32 v[10:11], v[10:11], v[146:147], v[154:155]\n\t" \
  "v_pk_fma_f32 v[12:13], v[12:13], v[148:149], v[156:157]\n\t" \
  "v_pk_fma_f32 v[14:15], v[14:15], v[150:151], v[158:159]\n\t" \
  "v_cvt_pk_bf16_f32 v20, v8, v9\n\t" \
  "v_cvt_pk_bf16_f32 v21, v10, v11\n\t" \
  "v_cvt_pk_bf16_f32 v22, v12, v13\n\t" \
  "v_cvt_pk_bf16_f32 v23, v14, v15\n\t" \
  "buffer_load_dwordx4 v212, s[4:7], s96 offen lds\n\t" \
  "v_pk_fma_f32 v[40:41], v[40:41], v[144:145], v[152:153]\n\t" \
  "v_pk_fma_f32 v[42:43], v[42:43], v[146:147], v[154:155]\n\t" \
  "v_pk_fma_f32 v[44:45], v[44:45], v[148:149], v[156:157]\n\t" \
  "v_pk_fma_f32 v[46:47], v[46:47], v[150:151], v[158:159]\n\t" \
  "v_cvt_pk_bf16_f32 v52, v40, v41\n\t" \
  "v_cvt_pk_bf16_f32 v53, v42, v43\n\t" \
  "v_cvt_pk_bf16_f32 v54, v44, v45\n\t" \
  "v_cvt_pk_bf16_f32 v55, v46, v47\n\t" \
  "buffer_load_dwordx4 v213, s[4:7], s96 offen offset:1024 lds\n\t" \
  "s_add_u32 m0, s77, 100352\n\t" \
  "s_xor_b32 s96, s87, 131072\n\t" \
  "s_add_u32 s96, s96, s88\n\t" \
  "v_pk_fma_f32 v[72:73], v[72:73], v[144:145], v[152:153]\n\t" \
  "v_pk_fma_f32 v[74:75], v[74:75], v[146:147], v[154:155]\n\t" \
  "v_pk_fma_f32 v[76:77], v[76:77], v[148:149], v[156:157]\n\t" \
  "v_pk_fma_f32 v[78:79], v[78:79], v[150:151], v[158:159]\n\t" \
  "v_cvt_pk_bf16_f32 v84, v72, v73\n\t" \
  "v_cvt_pk_bf16_f32 v85, v74, v75\n\t" \
  "v_cvt_pk_bf16_f32 v86, v76, v77\n\t" \
  "v_cvt_pk_bf16_f32 v87, v78, v79\n\t" \
  "buffer_load_dwordx4 v212, s[4:7], s96 offen lds\n\t" \
  "v_pk_fma_f32 v[104:105], v[104:105], v[144:145], v[152:153]\n\t" \
  "v_pk_fma_f32 v[106:107], v[106:107], v[146:147], v[154:155]\n\t" \
  "v_pk_fma_f32 v[108:109], v[108:109], v[148:149], v[156:157]\n\t" \
  "v_pk_fma_f32 v[110:111], v[110:111], v[150:151], v[158:159]\n\t" \
  "v_cvt_pk_bf16_f32 v116, v104, v105\n\t" \
  "v_cvt_pk_bf16_f32 v117, v106, v107\n\t" \
  "v_cvt_pk_bf16_f32 v118, v108, v109\n\t" \
  "v_cvt_pk_bf16_f32 v119, v110, v111\n\t" \
  "buffer_load_dwordx4 v213, s[4:7], s96 offen offset:1024 lds\n\t" \
  "ds_read_b128 v[144:147], v208 offset:640\n\t" \
  "ds_read_b128 v[148:151], v208 offset:656\n\t" \
  "ds_read_b128 v[152:155], v208 offset:896\n\t" \
  "ds_read_b128 v[156:159], v208 offset:912\n\t" \
  FSTP("v_writelane_b32 v255, s94, 28\n\t") \
  FSTP("v_writelane_b32 v255, s92, 29\n\t") \
  FSTP("s_memrealtime s[94:95]\n\t") \
  FSTP("s_memtime s[92:93]\n\t") \
  "s_add_u32 m0, s77, 102400\n\t" \
  "s_xor_b32 s96, s87, 262144\n\t" \
  "s_add_u32 s96, s96, s88\n\t" \
  "v_lshlrev_b32 v176, 16, v24\n\t" \
  "v_and_b32 v177, s98, v24\n\t" \
  "v_lshlrev_b32 v178, 16, v25\n\t" \
  "v_and_b32 v179, s98, v25\n\t" \
  "v_lshlrev_b32 v180, 16, v26\n\t" \
  "v_and_b32 v181, s98, v26\n\t" \
  "v_lshlrev_b32 v182, 16, v27\n\t" \
  "v_and_b32 v183, s98, v27\n\t" \
  "v_pk_fma_f32 v[176:177], v[176:177], v[160:161], v[162:163] op_sel_hi:[1,0,0]\n\t" \
  "v_pk_fma_f32 v[178:179], v[178:179], v[160:161], v[162:163] op_sel_hi:[1,0,0]\n\t" \
  "v_pk_fma_f32 v[180:181], v[180:181], v[160:161], v[162:163] op_sel_hi:[1,0,0]\n\t" \
  "v_pk_fma_f32 v[182:183], v[182:183], v[160:161], v[162:163] op_sel_hi:[1,0,0]\n\t" \
  "s_waitcnt lgkmcnt(4)\n\t" \
  "v_pk_fma_f32 v[176:177], v[176:177], v[128:129], v[136:137]\n\t" \
  "v_pk_fma_f32 v[178:179], v[178:179], v[130:131], v[138:139]\n\t" \
  "v_pk_fma_f32 v[180:181], v[180:181], v[132:133], v[140:141]\n\t" \
  "v_pk_fma_f32 v[182:183], v[182:183], v[134:135], v[142:143]\n\t" \
  "v_cvt_pk_bf16_f32 v24, v176, v177\n\t" \
  "v_cvt_pk_bf16_f32 v25, v178, v179\n\t" \
  "v_cvt_pk_bf16_f32 v26, v180, v181\n\t" \
  "v_cvt_pk_bf16_f32 v27, v182, v183\n\t" \
  "buffer_load_dwordx4 v212, s[4:7], s96 offen lds\n\t" \
  "v_lshlrev_b32 v176, 16, v56\n\t" \
  "v_and_b32 v177, s98, v56\n\t" \
  "v_lshlrev_b32 v178, 16, v57\n\t" \
  "v_and_b32 v179, s98, v57\n\t" \
  "v_lshlrev_b32 v180, 16, v58\n\t" \
  "v_and_b32 v181, s98, v58\n\t" \
  "v_lshlrev_b32 v182, 16, v59\n\t" \
  "v_and_b32 v183, s98, v59\n\t" \
  "v_pk_fma_f32 v[176:177], v[176:177], v[164:165], v[166:167] op_sel_hi:[1,0,0]\n\t" \
  "v_pk_fma_f32 v[178:179], v[178:179], v[164:165], v[166:167] op_sel_hi:[1,0,0]\n\t" \
  "v_pk_fma_f32 v[180:181], v[180:181], v[164:165], v[166:167] op_sel_hi:[1,0,0]\n\t" \
  "v_pk_fma_f32 v[182:183], v[182:183], v[164:165], v[166:167] op_sel_hi:[1,0,0]\n\t" \
  "v_pk_fma_f32 v[176:177], v[176:177], v[128:129], v[136:137]\n\t" \
  "v_pk_fma_f32 v[178:179], v[178:179], v[130:131], v[138:139]\n\t" \
  "v_pk_fma_f32 v[180:181], v[180:181], v[132:133], v[140:141]\n\t" \
  "v_pk_fma_f32 v[182:183], v[182:183], v[134:135], v[142:143]\n\t" \
  "v_cvt_pk_bf16_f32 v56, v176, v177\n\t" \
  "v_cvt_pk_bf16_f32 v57, v178, v179\n\t" \
  "v_cvt_pk_bf16_f32 v58, v180, v181\n\t" \
  "v_cvt_pk_bf16_f32 v59, v182, v183\n\t" \
  "buffer_load_dwordx4 v213, s[4:7], s96 offen offset:1024 lds\n\t" \
  "s_add_u32 m0, s77, 104448\n\t" \
  "s_xor_b32 s96, s87, 393216\n\t" \
  "s_add_u32 s96, s96, s88\n\t" \
  "v_lshlrev_b32 v176, 16, v88\n\t" \
  "v_and_b32 v177, s98, v88\n\t" \
  "v_lshlrev_b32 v178, 16, v89\n\t" \
  "v_and_b32 v179, s98, v89\n\t" \
  "v_lshlrev_b32 v180, 16, v90\n\t" \
  "v_and_b32 v181, s98, v90\n\t" \
  "v_lshlrev_b32 v182, 16, v91\n\t" \
  "v_and_b32 v183, s98, v91\n\t" \
  "v_pk_fma_f32 v[176:177], v[176:177], v[168:169], v[170:171] op_sel_hi:[1,0,0]\n\t" \
  "v_pk_fma_f32 v[178:179], v[178:179], v[168:169], v[170:171] op_sel_hi:[1,0,0]\n\t" \
  "v_pk_fma_f32 v[180:181], v[180:181], v[168:169], v[170:171] op_sel_hi:[1,0,0]\n\t" \
  "v_pk_fma_f32 v[182:183], v[182:183], v[168:169], v[170:171] op_sel_hi:[1,0,0]\n\t" \
  "v_pk_fma_f32 v[176:177], v[176:177], v[128:129], v[136:137]\n\t" \
  "v_pk_fma_f32 v[178:179], v[178:179], v[130:131], v[138:139]\n\t" \
  "v_pk_fma_f32 v[180:181], v[180:181], v[132:133], v[140:141]\n\t" \
  "v_pk_fma_f32 v[182:183], v[182:183], v[134:135], v[142:143]\n\t" \
  "v_cvt_pk_bf16_f32 v88, v176, v177\n\t" \
  "v_cvt_pk_bf16_f32 v89, v178, v179\n\t" \
  "v_cvt_pk_bf16_f32 v90, v180, v181\n\t" \
  "v_cvt_pk_bf16_f32 v91, v182, v183\n\t" \
  "buffer_load_dwordx4 v212, s[4:7], s96 offen lds\n\t" \
  "v_lshlrev_b32 v176, 16, v120\n\t" \
  "v_and_b32 v177, s98, v120\n\t" \
  "v_lshlrev_b32 v178, 16, v121\n\t" \
  "v_and_b32 v179, s98, v121\n\t" \
  "v_lshlrev_b32 v180, 16, v122\n\t" \
  "v_and_b32 v181, s98, v122\n\t" \
  "v_lshlrev_b32 v182, 16, v123\n\t" \
  "v_and_b32 v183, s98, v123\n\t" \
  "v_pk_fma_f32 v[176:177], v[176:177], v[172:173], v[174:175] op_sel_hi:[1,0,0]\n\t" \
  "v_pk_fma_f32 v[178:179], v[178:179], v[172:173], v[174:175] op_sel_hi:[1,0,0]\n\t" \
  "v_pk_fma_f32 v[180:181], v[180:181], v[172:173], v[174:175] op_sel_hi:[1,0,0]\n\t" \
  "v_pk_fma_f32 v[182:183], v[182:183], v[172:173], v[174:175] op_sel_hi:[1,0,0]\n\t" \
  "v_pk_fma_f32 v[176:177], v[176:177], v[128:129], v[136:137]\n\t" \
  "v_pk_fma_f32 v[178:179], v[178:179], v[130:131], v[138:139]\n\t" \
  "v_pk_fma_f32 v[180:181], v[180:181], v[132:133], v[140:141]\n\t" \
  "v_pk_fma_f32 v[182:183], v[182:183], v[134:135], v[142:143]\n\t" \
  "v_cvt_pk_bf16_f32 v120, v176, v177\n\t" \
  "v_cvt_pk_bf16_f32 v121, v178, v179\n\t" \
  "v_cvt_pk_bf16_f32 v122, v180, v181\n\t" \
  "v_cvt_pk_bf16_f32 v123, v182, v183\n\t" \
  "buffer_load_dwordx4 v213, s[4:7], s96 offen offset:1024 lds\n\t" \
  "ds_read_b128 v[128:131], v208 offset:1024\n\t" \
  "ds_read_b128 v[132:135], v208 offset:1040\n\t" \
  "ds_read_b128 v[136:139], v208 offset:1280\n\t" \
  "ds_read_b128 v[140:143], v208 offset:1296\n\t" \
  FSTP("v_writelane_b32 v255, s94, 30\n\t") \
  FSTP("v_writelane_b32 v255, s92, 31\n\t") \
  FSTP("s_memrealtime s[94:95]\n\t") \
  FSTP("s_memtime s[92:93]\n\t") \
  "s_add_u32 m0, s76, 98304\n\t" \
  "v_lshlrev_b32 v176, 16, v28\n\t" \
  "v_and_b32 v177, s98, v28\n\t" \
  "v_lshlrev_b32 v178, 16, v29\n\t" \
  "v_and_b32 v179, s98, v29\n\t" \
  "v_lshlrev_b32 v180, 16, v30\n\t" \
  "v_and_b32 v181, s98, v30\n\t" \
  "v_lshlrev_b32 v182, 16, v31\n\t" \
  "v_and_b32 v183, s98, v31\n\t" \
  "v_pk_fma_f32 v[176:177], v[176:177], v[160:161], v[162:163] op_sel_hi:[1,0,0]\n\t" \
  "v_pk_fma_f32 v[178:179], v[178:179], v[160:161], v[162:163] op_sel_hi:[1,0,0]\n\t" \
  "v_pk_fma_f32 v[180:181], v[180:181], v[160:161], v[162:163] op_sel_hi:[1,0,0]\n\t" \
  "v_pk_fma_f32 v[182:183], v[182:183], v[160:161], v[162:163] op_sel_hi:[1,0,0]\n\t" \
  "s_waitcnt lgkmcnt(4)\n\t" \
  "v_pk_fma_f32 v[176:177], v[176:177], v[144:145], v[152:153]\n\t" \
  "v_pk_fma_f32 v[178:179], v[178:179], v[146:147], v[154:155]\n\t" \
  "v_pk_fma_f32 v[180:181], v[180:181], v[148:149], v[156:157]\n\t" \
  "v_pk_fma_f32 v[182:183], v[182:183], v[150:151], v[158:159]\n\t" \
  "v_cvt_pk_bf16_f32 v28, v176, v177\n\t" \
  "v_cvt_pk_bf16_f32 v29, v178, v179\n\t" \
  "v_cvt_pk_bf16_f32 v30, v180, v181\n\t" \
  "v_cvt_pk_bf16_f32 v31, v182, v183\n\t" \
  "buffer_load_dwordx4 v200, s[20:23], s80 offen offset:0 lds\n\t" \
  "ds_read_b64_tr_b16 a[128:129], v184 offset:0\n\t" \
  "ds_read_b64_tr_b16 a[130:131], v184 offset:1024\n\t" \
  "ds_read_b64_tr_b16 a[132:133], v185 offset:0\n\t" \
  "ds_read_b64_tr_b16 a[134:135], v185 offset:1024\n\t" \
  "v_lshlrev_b32 v176, 16, v60\n\t" \
  "v_and_b32 v177, s98, v60\n\t" \
  "v_lshlrev_b32 v178, 16, v61\n\t" \
  "v_and_b32 v179, s98, v61\n\t" \
  "v_lshlrev_b32 v180, 16, v62\n\t" \
  "v_and_b32 v181, s98, v62\n\t" \
  "v_lshlrev_b32 v182, 16, v63\n\t" \
  "v_and_b32 v183, s98, v63\n\t" \
  "v_pk_fma_f32 v[176:177], v[176:177], v[164:165], v[166:167] op_sel_hi:[1,0,0]\n\t" \
  "v_pk_fma_f32 v[178:179], v[178:179], v[164:165], v[166:167] op_sel_hi:[1,0,0]\n\t" \
  "v_pk_fma_f32 v[180:181], v[180:181], v[164:165], v[166:167] op_sel_hi:[1,0,0]\n\t" \
  "v_pk_fma_f32 v[182:183], v[182:183], v[164:165], v[166:167] op_sel_hi:[1,0,0]\n\t" \
  "v_pk_fma_f32 v[176:177], v[176:177], v[144:145], v[152:153]\n\t" \
  "v_pk_fma_f32 v[178:179], v[178:179], v[146:147], v[154:155]\n\t" \
  "v_pk_fma_f32 v[180:181], v[180:181], v[148:149], v[156:157]\n\t" \
  "v_pk_fma_f32 v[182:183], v[182:183], v[150:151], v[158:159]\n\t" \
  "v_cvt_pk_bf16_f32 v60, v176, v177\n\t" \
  "v_cvt_pk_bf16_f32 v61, v178, v179\n\t" \
  "v_cvt_pk_bf16_f32 v62, v180, v181\n\t" \
  "v_cvt_pk_bf16_f32 v63, v182, v183\n\t" \
  "buffer_load_dwordx4 v201, s[20:23], s80 offen offset:1024 lds\n\t" \
  "ds_read_b64_tr_b16 a[136:137], v186 offset:0\n\t" \
  "ds_read_b64_tr_b16 a[138:139], v186 offset:1024\n\t" \
  "ds_read_b64_tr_b16 a[140:141], v187 offset:0\n\t" \
  "ds_read_b64_tr_b16 a[142:143], v187 offset:1024\n\t" \
  "v_lshlrev_b32 v176, 16, v92\n\t" \
  "v_and_b32 v177, s98, v92\n\t" \
  "v_lshlrev_b32 v178, 16, v93\n\t" \
  "v_and_b32 v179, s98, v93\n\t" \
  "v_lshlrev_b32 v180, 16, v94\n\t" \
  "v_and_b32 v181, s98, v94\n\t" \
  "v_lshlrev_b32 v182, 16, v95\n\t" \
  "v_and_b32 v183, s98, v95\n\t" \
  "v_pk_fma_f32 v[176:177], v[176:177], v[168:169], v[170:171] op_sel_hi:[1,0,0]\n\t" \
  "v_pk_fma_f32 v[178:179], v[178:179], v[168:169], v[170:171] op_sel_hi:[1,0,0]\n\t" \
  "v_pk_fma_f32 v[180:181], v[180:181], v[168:169], v[170:171] op_sel_hi:[1,0,0]\n\t" \
  "v_pk_fma_f32 v[182:183], v[182:183], v[168:169], v[170:171] op_sel_hi:[1,0,0]\n\t" \
  "v_pk_fma_f32 v[176:177], v[176:177], v[144:145], v[152:153]\n\t" \
  "v_pk_fma_f32 v[178:179], v[178:179], v[146:147], v[154:155]\n\t" \
  "v_pk_fma_f32 v[180:181], v[180:181], v[148:149], v[156:157]\n\t" \
  "v_pk_fma_f32 v[182:183], v[182:183], v[150:151], v[158:159]\n\t" \
  "v_cvt_pk_bf16_f32 v92, v176, v177\n\t" \
  "v_cvt_pk_bf16_f32 v93, v178, v179\n\t" \
  "v_cvt_pk_bf16_f32 v94, v180, v181\n\t" \
  "v_cvt_pk_bf16_f32 v95, v182, v183\n\t" \
  "buffer_load_dwordx4 v202, s[20:23], s80 offen offset:2048 lds\n\t" \
  "ds_read_b64_tr_b16 a[144:145], v188 offset:0\n\t" \
  "ds_read_b64_tr_b16 a[146:147], v188 offset:1024\n\t" \
  "ds_read_b64_tr_b16 a[148:149], v189 offset:0\n\t" \
  "ds_read_b64_tr_b16 a[150:151], v189 offset:1024\n\t" \
  "v_lshlrev_b32 v176, 16, v124\n\t" \
  "v_and_b32 v177, s98, v124\n\t" \
  "v_lshlrev_b32 v178, 16, v125\n\t" \
  "v_and_b32 v179, s98, v125\n\t" \
  "v_lshlrev_b32 v180, 16, v126\n\t" \
  "v_and_b32 v181, s98, v126\n\t" \
  "v_lshlrev_b32 v182, 16, v127\n\t" \
  "v_and_b32 v183, s98, v127\n\t" \
  "v_pk_fma_f32 v[176:177], v[176:177], v[172:173], v[174:175] op_sel_hi:[1,0,0]\n\t" \
  "v_pk_fma_f32 v[178:179], v[178:179], v[172:173], v[174:175] op_sel_hi:[1,0,0]\n\t" \
  "v_pk_fma_f32 v[180:181], v[180:181], v[172:173], v[174:175] op_sel_hi:[1,0,0]\n\t" \
  "v_pk_fma_f32 v[182:183], v[182:183], v[172:173], v[174:175] op_sel_hi:[1,0,0]\n\t" \
  "v_pk_fma_f32 v[176:177], v[176:177], v[144:145], v[152:153]\n\t" \
  "v_pk_fma_f32 v[178:179], v[178:179], v[146:147], v[154:155]\n\t" \
  "v_pk_fma_f32 v[180:181], v[180:181], v[148:149], v[156:157]\n\t" \
  "v_pk_fma_f32 v[182:183], v[182:183], v[150:151], v[158:159]\n\t" \
  "v_cvt_pk_bf16_f32 v124, v176, v177\n\t" \
  "v_cvt_pk_bf16_f32 v125, v178, v179\n\t" \
  "v_cvt_pk_bf16_f32 v126, v180, v181\n\t" \
  "v_cvt_pk_bf16_f32 v127, v182, v183\n\t" \
  "buffer_load_dwordx4 v203, s[20:23], s80 offen offset:3072 lds\n\t" \
  "ds_read_b64_tr_b16 a[152:153], v190 offset:0\n\t" \
  "ds_read_b64_tr_b16 a[154:155], v190 offset:1024\n\t" \
  "ds_read_b64_tr_b16 a[156:157], v191 offset:0\n\t" \
  "ds_read_b64_tr_b16 a[158:159], v191 offset:1024\n\t" \
  "ds_read_b128 v[144:147], v208 offset:1152\n\t" \
  "ds_read_b128 v[148:151], v208 offset:1168\n\t" \
  "ds_read_b128 v[152:155], v208 offset:1408\n\t" \
  "ds_read_b128 v[156:159], v208 offset:1424\n\t" \
  FSTP("v_writelane_b32 v255, s94, 32\n\t") \
  FSTP("v_writelane_b32 v255, s92, 33\n\t") \
  FSTP("s_memrealtime s[94:95]\n\t") \
  FSTP("s_memtime s[92:93]\n\t") \
  "s_waitcnt vmcnt(12)\n\t" \
  "s_mov_b64 exec, 1\n\t" \
  "buffer_store_dword v211, off, s[16:19], s85 sc1\n\t" \
  "s_mov_b64 exec, -1\n\t" \
  FSTP("v_writelane_b32 v255, s94, 34\n\t") \
  FSTP("v_writelane_b32 v255, s92, 35\n\t") \
  FSTP("s_memrealtime s[94:95]\n\t") \
  FSTP("s_memtime s[92:93]\n\t") \
  "s_waitcnt lgkmcnt(4)\n\t" \
  "v_mfma_f32_16x16x32_bf16 v[0:3], a[128:131], v[16:19], 0\n\t" \
  "s_add_u32 m0, s77, 0\n\t" \
  "s_xor_b32 s96, s87, 0\n\t" \
  "s_add_u32 s96, s96, s89\n\t" \
  "v_mfma_f32_16x16x32_bf16 v[32:35], a[128:131], v[48:51], 0\n\t" \
  "buffer_load_dwordx4 v212, s[4:7], s96 offen lds\n\t" \
  "v_mfma_f32_16x16x32_bf16 v[64:67], a[128:131], v[80:83], 0\n\t" \
  "ds_read_b64_tr_b16 a[160:161], v184 offset:8192\n\t" \
  "v_mfma_f32_16x16x32_bf16 v[96:99], a[128:131], v[112:115], 0\n\t" \
  "ds_read_b64_tr_b16 a[162:163], v184 offset:9216\n\t" \
  "v_mfma_f32_16x16x32_bf16 v[100:103], a[132:135], v[112:115], 0\n\t" \
  "buffer_load_dwordx4 v213, s[4:7], s96 offen offset:1024 lds\n\t" \
  "v_mfma_f32_16x16x32_bf16 v[68:71], a[132:135], v[80:83], 0\n\t" \
  "s_add_u32 m0, s77, 2048\n\t" \
  "ds_read_b64_tr_b16 a[164:165], v185 offset:8192\n\t" \
  "s_xor_b32 s96, s87, 131072\n\t" \
  "v_mfma_f32_16x16x32_bf16 v[36:39], a[132:135], v[48:51], 0\n\t" \
  "ds_read_b64_tr_b16 a[166:167], v185 offset:9216\n\t" \
  "s_add_u32 s96, s96, s89\n\t" \
  "v_mfma_f32_16x16x32_bf16 v[4:7], a[132:135], v[16:19], 0\n\t" \
  "ds_read_b64_tr_b16 a[168:169], v186 offset:8192\n\t" \
  "v_mfma_f32_16x16x32_bf16 v[8:11], a[136:139], v[16:19], 0\n\t" \
  "buffer_load_dwordx4 v212, s[4:7], s96 offen lds\n\t" \
  "v_mfma_f32_16x16x32_bf16 v[40:43], a[136:139], v[48:51], 0\n\t" \
  "ds_read_b64_tr_b16 a[170:171], v186 offset:9216\n\t" \
  "v_mfma_f32_16x16x32_bf16 v[72:75], a[136:139], v[80:83], 0\n\t" \
  "ds_read_b64_tr_b16 a[172:173], v187 offset:8192\n\t" \
  "v_mfma_f32_16x16x32_bf16 v[104:107], a[136:139], v[112:115], 0\n\t" \
  "buffer_load_dwordx4 v213, s[4:7], s96 offen offset:1024 lds\n\t" \
  "v_mfma_f32_16x16x32_bf16 v[108:111], a[140:143], v[112:115], 0\n\t" \
  "s_add_u32 m0, s77, 4096\n\t" \
  "ds_read_b64_tr_b16 a[174:175], v187 offset:9216\n\t" \
  "s_xor_b32 s96, s87, 262144\n\t" \
  "v_mfma_f32_16x16x32_bf16 v[76:79], a[140:143], v[80:83], 0\n\t" \
  "ds_read_b64_tr_b16 a[176:177], v188 offset:8192\n\t" \
  "s_add_u32 s96, s96, s89\n\t" \
  "v_mfma_f32_16x16x32_bf16 v[44:47], a[140:143], v[48:51], 0\n\t" \
  "buffer_load_dwordx4 v212, s[4:7], s96 offen lds\n\t" \
  "v_mfma_f32_16x16x32_bf16 v[12:15], a[140:143], v[16:19], 0\n\t" \
  "ds_read_b64_tr_b16 a[178:179], v188 offset:9216\n\t" \
  "v_mfma_f32_16x16x32_bf16 a[64:67], a[144:147], v[16:19], 0\n\t" \
  "ds_read_b64_tr_b16 a[180:181], v189 offset:8192\n\t" \
  "v_mfma_f32_16x16x32_bf16 a[68:71], a[144:147], v[48:51], 0\n\t" \
  "buffer_load_dwordx4 v213, s[4:7], s96 offen offset:1024 lds\n\t" \
  "v_mfma_f32_16x16x32_bf16 a[72:75], a[144:147], v[80:83], 0\n\t" \
  "s_add_u32 m0, s77, 6144\n\t" \
  "ds_read_b64_tr_b16 a[182:183], v189 offset:9216\n\t" \
  "s_xor_b32 s96, s87, 393216\n\t" \
  "v_mfma_f32_16x16x32_bf16 a[76:79], a[144:147], v[112:115], 0\n\t" \
  "ds_read_b64_tr_b16 a[184:185], v190 offset:8192\n\t" \
  "s_add_u32 s96, s96, s89\n\t" \
  "v_mfma_f32_16x16x32_bf16 a[92:95], a[148:151], v[112:115], 0\n\t" \
  "ds_read_b64_tr_b16 a[186:187], v190 offset:9216\n\t" \
  "v_mfma_f32_16x16x32_bf16 a[88:91], a[148:151], v[80:83], 0\n\t" \
  "buffer_load_dwordx4 v212, s[4:7], s96 offen lds\n\t" \
  "v_mfma_f32_16x16x32_bf16 a[84:87], a[148:151], v[48:51], 0\n\t" \
  "ds_read_b64_tr_b16 a[188:189], v191 offset:8192\n\t" \
  "v_mfma_f32_16x16x32_bf16 a[80:83], a[148:151], v[16:19], 0\n\t" \
  "ds_read_b64_tr_b16 a[190:191], v191 offset:9216\n\t" \
  "v_mfma_f32_16x16x32_bf16 a[96:99], a[152:155], v[16:19], 0\n\t" \
  "v_mfma_f32_16x16x32_bf16 a[100:103], a[152:155], v[48:51], 0\n\t" \
  "buffer_load_dwordx4 v213, s[4:7], s96 offen offset:1024 lds\n\t" \
  "v_mfma_f32_16x16x32_bf16 a[104:107], a[152:155], v[80:83], 0\n\t" \
  "v_mfma_f32_16x16x32_bf16 a[108:111], a[152:155], v[112:115], 0\n\t" \
  "v_mfma_f32_16x16x32_bf16 a[124:127], a[156:159], v[112:115], 0\n\t" \
  "v_mfma_f32_16x16x32_bf16 a[120:123], a[156:159], v[80:83], 0\n\t" \
  "s_add_u32 m0, s76, 0\n\t" \
  "v_mfma_f32_16x16x32_bf16 a[116:119], a[156:159], v[48:51], 0\n\t" \
  "s_waitcnt lgkmcnt(0)\n\t" \
  "v_mfma_f32_16x16x32_bf16 a[112:115], a[156:159], v[16:19], 0\n\t" \
  "s_waitcnt vmcnt(13)\n\t" \
  "ds_read_b128 v[16:19], v206 offset:0\n\t" \
  "ds_read_b128 v[48:51], v206 offset:2048\n\t" \
  "s_barrier\n\t" \
  FSTP("v_writelane_b32 v255, s94, 36\n\t") \
  FSTP("v_writelane_b32 v255, s92, 37\n\t") \
  FSTP("s_memrealtime s[94:95]\n\t") \
  FSTP("s_memtime s[92:93]\n\t") \
  "v_mfma_f32_16x16x32_bf16 v[0:3], a[160:163], v[20:23], v[0:3]\n\t" \
  "buffer_load_dwordx4 v200, s[20:23], s81 offen offset:0 lds\n\t" \
  "ds_read_b128 v[80:83], v206 offset:4096\n\t" \
  "ds_read_b128 v[112:115], v206 offset:6144\n\t" \
  "v_mfma_f32_16x16x32_bf16 v[32:35], a[160:163], v[52:55], v[32:35]\n\t" \
  "ds_read_b64_tr_b16 a[128:129], v184 offset:49152\n\t" \
  "v_mfma_f32_16x16x32_bf16 v[64:67], a[160:163], v[84:87], v[64:67]\n\t" \
  "ds_read_b64_tr_b16 a[130:131], v184 offset:50176\n\t" \
  "v_mfma_f32_16x16x32_bf16 v[96:99], a[160:163], v[116:119], v[96:99]\n\t" \
  "buffer_load_dwordx4 v201, s[20:23], s81 offen offset:1024 lds\n\t" \
  "v_mfma_f32_16x16x32_bf16 v[100:103], a[164:167], v[116:119], v[100:103]\n\t" \
  "ds_read_b64_tr_b16 a[132:133], v185 offset:49152\n\t" \
  "v_mfma_f32_16x16x32_bf16 v[68:71], a[164:167], v[84:87], v[68:71]\n\t" \
  "s_waitcnt lgkmcnt(3)\n\t" \
  "v_lshlrev_b32 v176, 16, v16\n\t" \
  "v_and_b32 v177, s98, v16\n\t" \
  "v_mfma_f32_16x16x32_bf16 v[36:39], a[164:167], v[52:55], v[36:39]\n\t" \
  "ds_read_b64_tr_b16 a[134:135], v185 offset:50176\n\t" \
  "v_lshlrev_b32 v178, 16, v17\n\t" \
  "v_mfma_f32_16x16x32_bf16 v[4:7], a[164:167], v[20:23], v[4:7]\n\t" \
  "v_and_b32 v179, s98, v17\n\t" \
  "v_lshlrev_b32 v180, 16, v18\n\t" \
  "v_mfma_f32_16x16x32_bf16 v[8:11], a[168:171], v[20:23], v[8:11]\n\t" \
  "buffer_load_dwordx4 v202, s[20:23], s81 offen offset:2048 lds\n\t" \
  "v_mfma_f32_16x16x32_bf16 v[40:43], a[168:171], v[52:55], v[40:43]\n\t" \
  "ds_read_b64_tr_b16 a[136:137], v186 offset:49152\n\t" \
  "v_and_b32 v181, s98, v18\n\t" \
  "v_mfma_f32_16x16x32_bf16 v[72:75], a[168:171], v[84:87], v[72:75]\n\t" \
  "ds_read_b64_tr_b16 a[138:139], v186 offset:50176\n\t" \
  "v_lshlrev_b32 v182, 16, v19\n\t" \
  "v_mfma_f32_16x16x32_bf16 v[104:107], a[168:171], v[116:119], v[104:107]\n\t" \
  "ds_read_b64_tr_b16 a[140:141], v187 offset:49152\n\t" \
  "v_and_b32 v183, s98, v19\n\t" \
  "v_mfma_f32_16x16x32_bf16 v[108:111], a[172:175], v[116:119], v[108:111]\n\t" \
  "buffer_load_dwordx4 v203, s[20:23], s81 offen offset:3072 lds\n\t" \
  "v_mfma_f32_16x16x32_bf16 v[76:79], a[172:175], v[84:87], v[76:79]\n\t" \
  "s_add_u32 s96, s78, 2097152\n\t" \
  "s_and_b32 s96, s96, 0x780000\n\t" \
  "s_add_u32 m0, s76, 16384\n\t" \
  "v_mfma_f32_16x16x32_bf16 v[44:47], a[172:175], v[52:55], v[44:47]\n\t" \
  "ds_read_b64_tr_b16 a[142:143], v187 offset:50176\n\t" \
  "v_lshlrev_b32 v220, 16, v48\n\t" \
  "v_and_b32 v221, s98, v48\n\t" \
  "v_mfma_f32_16x16x32_bf16 v[12:15], a[172:175], v[20:23], v[12:15]\n\t" \
  "buffer_load_dwordx4 v200, s[20:23], s96 offen offset:0 lds\n\t" \
  "v_mfma_f32_16x16x32_bf16 a[64:67], a[176:179], v[20:23], a[64:67]\n\t" \
  "ds_read_b64_tr_b16 a[144:145], v188 offset:49152\n\t" \
  "v_lshlrev_b32 v222, 16, v49\n\t" \
  "v_mfma_f32_16x16x32_bf16 a[68:71], a[176:179], v[52:55], a[68:71]\n\t" \
  "ds_read_b64_tr_b16 a[146:147], v188 offset:50176\n\t" \
  "v_and_b32 v223, s98, v49\n\t" \
  "v_mfma_f32_16x16x32_bf16 a[72:75], a[176:179], v[84:87], a[72:75]\n\t" \
  "ds_read_b64_tr_b16 a[148:149], v189 offset:49152\n\t" \
  "v_lshlrev_b32 v224, 16, v50\n\t" \
  "v_mfma_f32_16x16x32_bf16 a[76:79], a[176:179], v[116:119], a[76:79]\n\t" \
  "buffer_load_dwordx4 v201, s[20:23], s96 offen offset:1024 lds\n\t" \
  "v_mfma_f32_16x16x32_bf16 a[92:95], a[180:183], v[116:119], a[92:95]\n\t" \
  "ds_read_b64_tr_b16 a[150:151], v189 offset:50176\n\t" \
  "v_and_b32 v225, s98, v50\n\t" \
  "v_mfma_f32_16x16x32_bf16 a[88:91], a[180:183], v[84:87], a[88:91]\n\t" \
  "ds_read_b64_tr_b16 a[152:153], v190 offset:49152\n\t" \
  "v_lshlrev_b32 v226, 16, v51\n\t" \
  "v_mfma_f32_16x16x32_bf16 a[84:87], a[180:183], v[52:55], a[84:87]\n\t" \
  "ds_read_b64_tr_b16 a[154:155], v190 offset:50176\n\t" \
  "v_and_b32 v227, s98, v51\n\t" \
  "v_mfma_f32_16x16x32_bf16 a[80:83], a[180:183], v[20:23], a[80:83]\n\t" \
  "buffer_load_dwordx4 v202, s[20:23], s96 offen offset:2048 lds\n\t" \
  "ds_read_b64_tr_b16 a[156:157], v191 offset:49152\n\t" \
  "ds_read_b64_tr_b16 a[158:159], v191 offset:50176\n\t" \
  "v_lshlrev_b32 v228, 16, v80\n\t" \
  "v_pk_fma_f32 v[176:177], v[176:177], v[160:161], v[162:163] op_sel_hi:[1,0,0]\n\t" \
  "v_pk_fma_f32 v[178:179], v[178:179], v[160:161], v[162:163] op_sel_hi:[1,0,0]\n\t" \
  "v_pk_fma_f32 v[180:181], v[180:181], v[160:161], v[162:163] op_sel_hi:[1,0,0]\n\t" \
  "v_pk_fma_f32 v[182:183], v[182:183], v[160:161], v[162:163] op_sel_hi:[1,0,0]\n\t" \
  "v_pk_fma_f32 v[176:177], v[176:177], v[128:129], v[136:137]\n\t" \
  "v_pk_fma_f32 v[178:179], v[178:179], v[130:131], v[138:139]\n\t" \
  "v_pk_fma_f32 v[180:181], v[180:181], v[132:133], v[140:141]\n\t" \
  "v_pk_fma_f32 v[182:183], v[182:183], v[134:135], v[142:143]\n\t" \
  "v_pk_fma_f32 v[220:221], v[220:221], v[164:165], v[166:167] op_sel_hi:[1,0,0]\n\t" \
  "v_pk_fma_f32 v[222:223], v[222:223], v[164:165], v[166:167] op_sel_hi:[1,0,0]\n\t" \
  "v_pk_fma_f32 v[224:225], v[224:225], v[164:165], v[166:167] op_sel_hi:[1,0,0]\n\t" \
  "v_pk_fma_f32 v[226:227], v[226:227], v[164:165], v[166:167] op_sel_hi:[1,0,0]\n\t" \
  "v_pk_fma_f32 v[220:221], v[220:221], v[128:129], v[136:137]\n\t" \
  "v_pk_fma_f32 v[222:223], v[222:223], v[130:131], v[138:139]\n\t" \
  "v_pk_fma_f32 v[224:225], v[224:225], v[132:133], v[140:141]\n\t" \
  "v_pk_fma_f32 v[226:227], v[226:227], v[134:135], v[142:143]\n\t" \
  "v_cvt_pk_bf16_f32 v16, v176, v177\n\t" \
  "v_mfma_f32_16x16x32_bf16 a[96:99], a[184:187], v[20:23], a[96:99]\n\t" \
  "v_cvt_pk_bf16_f32 v17, v178, v179\n\t" \
  "v_cvt_pk_bf16_f32 v18, v180, v181\n\t" \
  "v_and_b32 v229, s98, v80\n\t" \
  "v_mfma_f32_16x16x32_bf16 a[100:103], a[184:187], v[52:55], a[100:103]\n\t" \
  "v_cvt_pk_bf16_f32 v19, v182, v183\n\t" \
  "v_cvt_pk_bf16_f32 v48, v220, v221\n\t" \
  "v_lshlrev_b32 v230, 16, v81\n\t" \
  "v_and_b32 v231, s98, v81\n\t" \
  "v_mfma_f32_16x16x32_bf16 a[104:107], a[184:187], v[84:87], a[104:107]\n\t" \
  "v_lshlrev_b32 v232, 16, v82\n\t" \
  "v_and_b32 v233, s98, v82\n\t" \
  "v_mfma_f32_16x16x32_bf16 a[108:111], a[184:187], v[116:119], a[108:111]\n\t" \
  "buffer_load_dwordx4 v203, s[20:23], s96 offen offset:3072 lds\n\t" \
  "v_mfma_f32_16x16x32_bf16 a[124:127], a[188:191], v[116:119], a[124:127]\n\t" \
  "v_lshlrev_b32 v234, 16, v83\n\t" \
  "v_and_b32 v235, s98, v83\n\t" \
  "v_mfma_f32_16x16x32_bf16 a[120:123], a[188:191], v[84:87], a[120:123]\n\t" \
  "s_add_u32 s96, s78, 2621440\n\t" \
  "v_lshlrev_b32 v236, 16, v112\n\t" \
  "v_and_b32 v237, s98, v112\n\t" \
  "v_mfma_f32_16x16x32_bf16 a[116:119], a[188:191], v[52:55], a[116:119]\n\t" \
  "s_and_b32 s96, s96, 0x780000\n\t" \
  "s_add_u32 m0, s76, 32768\n\t" \
  "v_lshlrev_b32 v238, 16, v113\n\t" \
  "v_mfma_f32_16x16x32_bf16 a[112:115], a[188:191], v[20:23], a[112:115]\n\t" \
  "s_waitcnt lgkmcnt(0)\n\t" \
  "ds_read_b128 v[20:23], v207 offset:0\n\t" \
  "ds_read_b128 v[52:55], v207 offset:2048\n\t" \
  "ds_read_b128 v[84:87], v207 offset:4096\n\t" \
  "v_and_b32 v239, s98, v113\n\t" \
  FSTP("v_writelane_b32 v255, s94, 38\n\t") \
  FSTP("v_writelane_b32 v255, s92, 39\n\t") \
  FSTP("s_memrealtime s[94:95]\n\t") \
  FSTP("s_memtime s[92:93]\n\t") \
  "v_mfma_f32_16x16x32_bf16 v[0:3], a[128:131], v[24:27], v[0:3]\n\t" \
  "buffer_load_dwordx4 v200, s[20:23], s96 offen offset:0 lds\n\t" \
  "ds_read_b128 v[116:119], v207 offset:6144\n\t" \
  "v_mfma_f32_16x16x32_bf16 v[32:35], a[128:131], v[56:59], v[32:35]\n\t" \
  "v_lshlrev_b32 v240, 16, v114\n\t" \
  "v_and_b32 v241, s98, v114\n\t" \
  "v_mfma_f32_16x16x32_bf16 v[64:67], a[128:131], v[88:91], v[64:67]\n\t" \
  "v_lshlrev_b32 v242, 16, v115\n\t" \
  "v_and_b32 v243, s98, v115\n\t" \
  "v_mfma_f32_16x16x32_bf16 v[96:99], a[128:131], v[120:123], v[96:99]\n\t" \
  "ds_read_b64_tr_b16 a[160:161], v184 offset:57344\n\t" \
  "ds_read_b64_tr_b16 a[162:163], v184 offset:58368\n\t" \
  "v_cvt_pk_bf16_f32 v49, v222, v223\n\t" \
  "v_cvt_pk_bf16_f32 v50, v224, v225\n\t" \
  "v_cvt_pk_bf16_f32 v51, v226, v227\n\t" \
  "v_pk_fma_f32 v[228:229], v[228:229], v[168:169], v[170:171] op_sel_hi:[1,0,0]\n\t" \
  "v_pk_fma_f32 v[230:231], v[230:231], v[168:169], v[170:171] op_sel_hi:[1,0,0]\n\t" \
  "v_pk_fma_f32 v[232:233], v[232:233], v[168:169], v[170:171] op_sel_hi:[1,0,0]\n\t" \
  "v_pk_fma_f32 v[234:235], v[234:235], v[168:169], v[170:171] op_sel_hi:[1,0,0]\n\t" \
  "v_pk_fma_f32 v[228:229], v[228:229], v[128:129], v[136:137]\n\t" \
  "v_pk_fma_f32 v[230:231], v[230:231], v[130:131], v[138:139]\n\t" \
  "v_pk_fma_f32 v[232:233], v[232:233], v[132:133], v[140:141]\n\t" \
  "v_pk_fma_f32 v[234:235], v[234:235], v[134:135], v[142:143]\n\t" \
  "v_pk_fma_f32 v[236:237], v[236:237], v[172:173], v[174:175] op_sel_hi:[1,0,0]\n\t" \
  "v_pk_fma_f32 v[238:239], v[238:239], v[172:173], v[174:175] op_sel_hi:[1,0,0]\n\t" \
  "v_pk_fma_f32 v[240:241], v[240:241], v[172:173], v[174:175] op_sel_hi:[1,0,0]\n\t" \
  "v_pk_fma_f32 v[242:243], v[242:243], v[172:173], v[174:175] op_sel_hi:[1,0,0]\n\t" \
  "v_pk_fma_f32 v[236:237], v[236:237], v[128:129], v[136:137]\n\t" \
  "v_pk_fma_f32 v[238:239], v[238:239], v[130:131], v[138:139]\n\t" \
  "v_pk_fma_f32 v[240:241], v[240:241], v[132:133], v[140:141]\n\t" \
  "v_pk_fma_f32 v[242:243], v[242:243], v[134:135], v[142:143]\n\t" \
  "v_cvt_pk_bf16_f32 v80, v228, v229\n\t" \
  "v_mfma_f32_16x16x32_bf16 v[100:103], a[132:135], v[120:123], v[100:103]\n\t" \
  "ds_read_b64_tr_b16 a[164:165], v185 offset:57344\n\t" \
  "v_cvt_pk_bf16_f32 v81, v230, v231\n\t" \
  "v_mfma_f32_16x16x32_bf16 v[68:71], a[132:135], v[88:91], v[68:71]\n\t" \
  "s_waitcnt lgkmcnt(3)\n\t" \
  "v_lshlrev_b32 v176, 16, v20\n\t" \
  "v_and_b32 v177, s98, v20\n\t" \
  "v_mfma_f32_16x16x32_bf16 v[36:39], a[132:135], v[56:59], v[36:39]\n\t" \
  "buffer_load_dwordx4 v201, s[20:23], s96 offen offset:1024 lds\n\t" \
  "v_mfma_f32_16x16x32_bf16 v[4:7], a[132:135], v[24:27], v[4:7]\n\t" \
  "ds_read_b64_tr_b16 a[166:167], v185 offset:58368\n\t" \
  "v_lshlrev_b32 v178, 16, v21\n\t" \
  "v_mfma_f32_16x16x32_bf16 v[8:11], a[136:139], v[24:27], v[8:11]\n\t" \
  "ds_read_b64_tr_b16 a[168:169], v186 offset:57344\n\t" \
  "v_and_b32 v179, s98, v21\n\t" \
  "v_mfma_f32_16x16x32_bf16 v[40:43], a[136:139], v[56:59], v[40:43]\n\t" \
  "v_lshlrev_b32 v180, 16, v22\n\t" \
  "v_and_b32 v181, s98, v22\n\t" \
  "v_mfma_f32_16x16x32_bf16 v[72:75], a[136:139], v[88:91], v[72:75]\n\t" \
  "ds_read_b64_tr_b16 a[170:171], v186 offset:58368\n\t" \
  "v_lshlrev_b32 v182, 16, v23\n\t" \
  "v_mfma_f32_16x16x32_bf16 v[104:107], a[136:139], v[120:123], v[104:107]\n\t" \
  "ds_read_b64_tr_b16 a[172:173], v187 offset:57344\n\t" \
  "v_and_b32 v183, s98, v23\n\t" \
  "v_mfma_f32_16x16x32_bf16 v[108:111], a[140:143], v[120:123], v[108:111]\n\t" \
  "buffer_load_dwordx4 v202, s[20:23], s96 offen offset:2048 lds\n\t" \
  "v_mfma_f32_16x16x32_bf16 v[76:79], a[140:143], v[88:91], v[76:79]\n\t" \
  "ds_read_b64_tr_b16 a[174:175], v187 offset:58368\n\t" \
  "v_lshlrev_b32 v220, 16, v52\n\t" \
  "v_mfma_f32_16x16x32_bf16 v[44:47], a[140:143], v[56:59], v[44:47]\n\t" \
  "v_and_b32 v221, s98, v52\n\t" \
  "v_lshlrev_b32 v222, 16, v53\n\t" \
  "v_mfma_f32_16x16x32_bf16 v[12:15], a[140:143], v[24:27], v[12:15]\n\t" \
  "ds_read_b64_tr_b16 a[176:177], v188 offset:57344\n\t" \
  "v_and_b32 v223, s98, v53\n\t" \
  "v_mfma_f32_16x16x32_bf16 a[64:67], a[144:147], v[24:27], a[64:67]\n\t" \
  "ds_read_b64_tr_b16 a[178:179], v188 offset:58368\n\t" \
  "v_lshlrev_b32 v224, 16, v54\n\t" \
  "v_mfma_f32_16x16x32_bf16 a[68:71], a[144:147], v[56:59], a[68:71]\n\t" \
  "ds_read_b64_tr_b16 a[180:181], v189 offset:57344\n\t" \
  "v_and_b32 v225, s98, v54\n\t" \
  "v_mfma_f32_16x16x32_bf16 a[72:75], a[144:147], v[88:91], a[72:75]\n\t" \
  "buffer_load_dwordx4 v203, s[20:23], s96 offen offset:3072 lds\n\t" \
  "v_mfma_f32_16x16x32_bf16 a[76:79], a[144:147], v[120:123], a[76:79]\n\t" \
  "ds_read_b64_tr_b16 a[182:183], v189 offset:58368\n\t" \
  "v_cvt_pk_bf16_f32 v82, v232, v233\n\t" \
  "v_mfma_f32_16x16x32_bf16 a[92:95], a[148:151], v[120:123], a[92:95]\n\t" \
  "v_lshlrev_b32 v226, 16, v55\n\t" \
  "v_and_b32 v227, s98, v55\n\t" \
  "v_mfma_f32_16x16x32_bf16 a[88:91], a[148:151], v[88:91], a[88:91]\n\t" \
  "ds_read_b64_tr_b16 a[184:185], v190 offset:57344\n\t" \
  "v_cvt_pk_bf16_f32 v83, v234, v235\n\t" \
  "v_cvt_pk_bf16_f32 v112, v236, v237\n\t" \
  "v_cvt_pk_bf16_f32 v113, v238, v239\n\t" \
  "v_pk_fma_f32 v[176:177], v[176:177], v[160:161], v[162:163] op_sel_hi:[1,0,0]\n\t" \
  "v_pk_fma_f32 v[178:179], v[178:179], v[160:161], v[162:163] op_sel_hi:[1,0,0]\n\t" \
  "v_pk_fma_f32 v[180:181], v[180:181], v[160:161], v[162:163] op_sel_hi:[1,0,0]\n\t" \
  "v_pk_fma_f32 v[182:183], v[182:183], v[160:161], v[162:163] op_sel_hi:[1,0,0]\n\t" \
  "v_pk_fma_f32 v[176:177], v[176:177], v[144:145], v[152:153]\n\t" \
  "v_pk_fma_f32 v[178:179], v[178:179], v[146:147], v[154:155]\n\t" \
  "v_pk_fma_f32 v[180:181], v[180:181], v[148:149], v[156:157]\n\t" \
  "v_pk_fma_f32 v[182:183], v[182:183], v[150:151], v[158:159]\n\t" \
  "v_pk_fma_f32 v[220:221], v[220:221], v[164:165], v[166:167] op_sel_hi:[1,0,0]\n\t" \
  "v_pk_fma_f32 v[222:223], v[222:223], v[164:165], v[166:167] op_sel_hi:[1,0,0]\n\t" \
  "v_pk_fma_f32 v[224:225], v[224:225], v[164:165], v[166:167] op_sel_hi:[1,0,0]\n\t" \
  "v_pk_fma_f32 v[226:227], v[226:227], v[164:165], v[166:167] op_sel_hi:[1,0,0]\n\t" \
  "v_pk_fma_f32 v[220:221], v[220:221], v[144:145], v[152:153]\n\t" \
  "v_pk_fma_f32 v[222:223], v[222:223], v[146:147], v[154:155]\n\t" \
  "v_pk_fma_f32 v[224:225], v[224:225], v[148:149], v[156:157]\n\t" \
  "v_pk_fma_f32 v[226:227], v[226:227], v[150:151], v[158:159]\n\t" \
  "v_cvt_pk_bf16_f32 v20, v176, v177\n\t" \
  "v_mfma_f32_16x16x32_bf16 a[84:87], a[148:151], v[56:59], a[84:87]\n\t" \
  "ds_read_b64_tr_b16 a[186:187], v190 offset:58368\n\t" \
  "v_lshlrev_b32 v228, 16, v84\n\t" \
  "v_mfma_f32_16x16x32_bf16 a[80:83], a[148:151], v[24:27], a[80:83]\n\t" \
  "ds_read_b64_tr_b16 a[188:189], v191 offset:57344\n\t" \
  "v_and_b32 v229, s98, v84\n\t" \
  "v_mfma_f32_16x16x32_bf16 a[96:99], a[152:155], v[24:27], a[96:99]\n\t" \
  "global_load_dword v246, v217, s[12:13]\n\t" \
  "v_mfma_f32_16x16x32_bf16 a[100:103], a[152:155], v[56:59], a[100:103]\n\t" \
  "ds_read_b64_tr_b16 a[190:191], v191 offset:58368\n\t" \
  "v_lshlrev_b32 v230, 16, v85\n\t" \
  "v_mfma_f32_16x16x32_bf16 a[104:107], a[152:155], v[88:91], a[104:107]\n\t" \
  "v_and_b32 v231, s98, v85\n\t" \
  "v_lshlrev_b32 v232, 16, v86\n\t" \
  "v_mfma_f32_16x16x32_bf16 a[108:111], a[152:155], v[120:123], a[108:111]\n\t" \
  ".Lhs1:\n\ts_cbranch_execz .Lhs1n\n\t" \
  "v_and_b32 v233, s98, v86\n\t" \
  "v_lshlrev_b32 v234, 16, v87\n\t" \
  "v_mfma_f32_16x16x32_bf16 a[124:127], a[156:159], v[120:123], a[124:127]\n\t" \
  "v_cvt_pk_bf16_f32 v114, v240, v241\n\t" \
  "v_cvt_pk_bf16_f32 v115, v242, v243\n\t" \
  "v_mfma_f32_16x16x32_bf16 a[120:123], a[156:159], v[88:91], a[120:123]\n\t" \
  "s_add_u32 s96, s78, 3145728\n\t" \
  "v_and_b32 v235, s98, v87\n\t" \
  "v_lshlrev_b32 v236, 16, v116\n\t" \
  "v_mfma_f32_16x16x32_bf16 a[116:119], a[156:159], v[56:59], a[116:119]\n\t" \
  "s_waitcnt vmcnt(22) lgkmcnt(0)\n\t" \
  "s_and_b32 s96, s96, 0x780000\n\t" \
  "s_add_u32 m0, s76, 49152\n\t" \
  "v_mfma_f32_16x16x32_bf16 a[112:115], a[156:159], v[24:27], a[112:115]\n\t" \
  "s_barrier\n\t" \
  FSTP("v_writelane_b32 v255, s94, 40\n\t") \
  FSTP("v_writelane_b32 v255, s92, 41\n\t") \
  FSTP("s_memrealtime s[94:95]\n\t") \
  FSTP("s_memtime s[92:93]\n\t") \
  "v_mfma_f32_16x16x32_bf16 v[0:3], a[160:163], v[28:31], v[0:3]\n\t" \
  "buffer_load_dwordx4 v200, s[20:23], s96 offen offset:0 lds\n\t" \
  "ds_read_b64_tr_b16 a[128:129], v192 offset:32768\n\t" \
  "v_mfma_f32_16x16x32_bf16 v[32:35], a[160:163], v[60:63], v[32:35]\n\t" \
  "s_waitcnt vmcnt(14)\n\t" \
  "ds_read_b128 v[24:27], v204 offset:0\n\t" \
  "ds_read_b64_tr_b16 a[130:131], v192 offset:33792\n\t" \
  "v_and_b32 v237, s98, v116\n\t" \
  "v_mfma_f32_16x16x32_bf16 v[64:67], a[160:163], v[92:95], v[64:67]\n\t" \
  "ds_read_b128 v[56:59], v204 offset:2048\n\t" \
  "ds_read_b64_tr_b16 a[132:133], v193 offset:32768\n\t" \
  "v_cvt_pk_bf16_f32 v21, v178, v179\n\t" \
  "v_mfma_f32_16x16x32_bf16 v[96:99], a[160:163], v[124:127], v[96:99]\n\t" \
  "ds_read_b128 v[88:91], v204 offset:4096\n\t" \
  "v_lshlrev_b32 v238, 16, v117\n\t" \
  "v_mfma_f32_16x16x32_bf16 v[100:103], a[164:167], v[124:127], v[100:103]\n\t" \
  "ds_read_b128 v[120:123], v204 offset:6144\n\t" \
  "v_cvt_pk_bf16_f32 v22, v180, v181\n\t" \
  "v_mfma_f32_16x16x32_bf16 v[68:71], a[164:167], v[92:95], v[68:71]\n\t" \
  "buffer_load_dwordx4 v201, s[20:23], s96 offen offset:1024 lds\n\t" \
  "v_mfma_f32_16x16x32_bf16 v[36:39], a[164:167], v[60:63], v[36:39]\n\t" \
  "ds_read_b64_tr_b16 a[134:135], v193 offset:33792\n\t" \
  "v_and_b32 v239, s98, v117\n\t" \
  "v_mfma_f32_16x16x32_bf16 v[4:7], a[164:167], v[28:31], v[4:7]\n\t" \
  "ds_read_b64_tr_b16 a[136:137], v194 offset:32768\n\t" \
  "v_cvt_pk_bf16_f32 v23, v182, v183\n\t" \
  "v_mfma_f32_16x16x32_bf16 v[8:11], a[168:171], v[28:31], v[8:11]\n\t" \
  "buffer_load_dwordx4 v202, s[20:23], s96 offen offset:2048 lds\n\t" \
  "v_mfma_f32_16x16x32_bf16 v[40:43], a[168:171], v[60:63], v[40:43]\n\t" \
  "ds_read_b64_tr_b16 a[138:139], v194 offset:33792\n\t" \
  "v_lshlrev_b32 v240, 16, v118\n\t" \
  "v_mfma_f32_16x16x32_bf16 v[72:75], a[168:171], v[92:95], v[72:75]\n\t" \
  "ds_read_b64_tr_b16 a[140:141], v195 offset:32768\n\t" \
  "v_and_b32 v241, s98, v118\n\t" \
  "v_mfma_f32_16x16x32_bf16 v[104:107], a[168:171], v[124:127], v[104:107]\n\t" \
  "v_lshlrev_b32 v242, 16, v119\n\t" \
  "v_and_b32 v243, s98, v119\n\t" \
  "v_mfma_f32_16x16x32_bf16 v[108:111], a[172:175], v[124:127], v[108:111]\n\t" \
  "buffer_load_dwordx4 v203, s[20:23], s96 offen offset:3072 lds\n\t" \
  "ds_read_b64_tr_b16 a[142:143], v195 offset:33792\n\t" \
  "ds_read_b128 v[128:131], v208 offset:1536\n\t" \
  "v_cvt_pk_bf16_f32 v52, v220, v221\n\t" \
  "v_cvt_pk_bf16_f32 v53, v222, v223\n\t" \
  "v_pk_fma_f32 v[228:229], v[228:229], v[168:169], v[170:171] op_sel_hi:[1,0,0]\n\t" \
  "v_pk_fma_f32 v[230:231], v[230:231], v[168:169], v[170:171] op_sel_hi:[1,0,0]\n\t" \
  "v_pk_fma_f32 v[232:233], v[232:233], v[168:169], v[170:171] op_sel_hi:[1,0,0]\n\t" \
  "v_pk_fma_f32 v[234:235], v[234:235], v[168:169], v[170:171] op_sel_hi:[1,0,0]\n\t" \
  "v_pk_fma_f32 v[228:229], v[228:229], v[144:145], v[152:153]\n\t" \
  "v_pk_fma_f32 v[230:231], v[230:231], v[146:147], v[154:155]\n\t" \
  "v_pk_fma_f32 v[232:233], v[232:233], v[148:149], v[156:157]\n\t" \
  "v_pk_fma_f32 v[234:235], v[234:235], v[150:151], v[158:159]\n\t" \
  "v_pk_fma_f32 v[236:237], v[236:237], v[172:173], v[174:175] op_sel_hi:[1,0,0]\n\t" \
  "v_pk_fma_f32 v[238:239], v[238:239], v[172:173], v[174:175] op_sel_hi:[1,0,0]\n\t" \
  "v_pk_fma_f32 v[240:241], v[240:241], v[172:173], v[174:175] op_sel_hi:[1,0,0]\n\t" \
  "v_pk_fma_f32 v[242:243], v[242:243], v[172:173], v[174:175] op_sel_hi:[1,0,0]\n\t" \
  "v_pk_fma_f32 v[236:237], v[236:237], v[144:145], v[152:153]\n\t" \
  "v_pk_fma_f32 v[238:239], v[238:239], v[146:147], v[154:155]\n\t" \
  "v_pk_fma_f32 v[240:241], v[240:241], v[148:149], v[156:157]\n\t" \
  "v_pk_fma_f32 v[242:243], v[242:243], v[150:151], v[158:159]\n\t" \
  "v_cvt_pk_bf16_f32 v84, v228, v229\n\t" \
  "v_mfma_f32_16x16x32_bf16 v[76:79], a[172:175], v[92:95], v[76:79]\n\t" \
  "s_add_u32 s96, s78, 3670016\n\t" \
  "s_and_b32 s96, s96, 0x780000\n\t" \
  "s_add_u32 m0, s76, 65536\n\t" \
  "v_mfma_f32_16x16x32_bf16 v[44:47], a[172:175], v[60:63], v[44:47]\n\t" \
  "ds_read_b64_tr_b16 a[144:145], v196 offset:32768\n\t" \
  "v_cvt_pk_bf16_f32 v54, v224, v225\n\t" \
  "v_mfma_f32_16x16x32_bf16 v[12:15], a[172:175], v[28:31], v[12:15]\n\t" \
  "buffer_load_dwordx4 v200, s[20:23], s96 offen offset:0 lds\n\t" \
  "v_mfma_f32_16x16x32_bf16 a[64:67], a[176:179], v[28:31], a[64:67]\n\t" \
  "s_waitcnt lgkmcnt(7)\n\t" \
  "v_lshlrev_b32 v176, 16, v24\n\t" \
  "v_and_b32 v177, s98, v24\n\t" \
  "v_mfma_f32_16x16x32_bf16 a[68:71], a[176:179], v[60:63], a[68:71]\n\t" \
  "ds_read_b64_tr_b16 a[146:147], v196 offset:33792\n\t" \
  "v_lshlrev_b32 v178, 16, v25\n\t" \
  "v_mfma_f32_16x16x32_bf16 a[72:75], a[176:179], v[92:95], a[72:75]\n\t" \
  "ds_read_b64_tr_b16 a[148:149], v197 offset:32768\n\t" \
  "v_and_b32 v179, s98, v25\n\t" \
  "v_mfma_f32_16x16x32_bf16 a[76:79], a[176:179], v[124:127], a[76:79]\n\t" \
  "buffer_load_dwordx4 v201, s[20:23], s96 offen offset:1024 lds\n\t" \
  "v_mfma_f32_16x16x32_bf16 a[92:95], a[180:183], v[124:127], a[92:95]\n\t" \
  "ds_read_b64_tr_b16 a[150:151], v197 offset:33792\n\t" \
  "v_lshlrev_b32 v180, 16, v26\n\t" \
  "v_mfma_f32_16x16x32_bf16 a[88:91], a[180:183], v[92:95], a[88:91]\n\t" \
  "ds_read_b64_tr_b16 a[152:153], v198 offset:32768\n\t" \
  "v_and_b32 v181, s98, v26\n\t" \
  "v_mfma_f32_16x16x32_bf16 a[84:87], a[180:183], v[60:63], a[84:87]\n\t" \
  "ds_read_b64_tr_b16 a[154:155], v198 offset:33792\n\t" \
  "v_lshlrev_b32 v182, 16, v27\n\t" \
  "v_mfma_f32_16x16x32_bf16 a[80:83], a[180:183], v[28:31], a[80:83]\n\t" \
  "buffer_load_dwordx4 v202, s[20:23], s96 offen offset:2048 lds\n\t" \
  "v_mfma_f32_16x16x32_bf16 a[96:99], a[184:187], v[28:31], a[96:99]\n\t" \
  "ds_read_b64_tr_b16 a[156:157], v199 offset:32768\n\t" \
  "v_cvt_pk_bf16_f32 v55, v226, v227\n\t" \
  "v_mfma_f32_16x16x32_bf16 a[100:103], a[184:187], v[60:63], a[100:103]\n\t" \
  "ds_read_b64_tr_b16 a[158:159], v199 offset:33792\n\t" \
  "v_and_b32 v183, s98, v27\n\t" \
  "v_mfma_f32_16x16x32_bf16 a[104:107], a[184:187], v[92:95], a[104:107]\n\t" \
  "v_lshlrev_b32 v220, 16, v56\n\t" \
  "v_and_b32 v221, s98, v56\n\t" \
  "v_mfma_f32_16x16x32_bf16 a[108:111], a[184:187], v[124:127], a[108:111]\n\t" \
  "buffer_load_dwordx4 v203, s[20:23], s96 offen offset:3072 lds\n\t" \
  "v_mfma_f32_16x16x32_bf16 a[124:127], a[188:191], v[124:127], a[124:127]\n\t" \
  "v_lshlrev_b32 v222, 16, v57\n\t" \
  "v_and_b32 v223, s98, v57\n\t" \
  "v_mfma_f32_16x16x32_bf16 a[120:123], a[188:191], v[92:95], a[120:123]\n\t" \
  "v_lshlrev_b32 v224, 16, v58\n\t" \
  "v_cvt_pk_bf16_f32 v85, v230, v231\n\t" \
  "v_mfma_f32_16x16x32_bf16 a[116:119], a[188:191], v[60:63], a[116:119]\n\t" \
  "v_and_b32 v225, s98, v58\n\t" \
  "v_lshlrev_b32 v226, 16, v59\n\t" \
  "v_mfma_f32_16x16x32_bf16 a[112:115], a[188:191], v[28:31], a[112:115]\n\t" \
  "s_waitcnt lgkmcnt(0)\n\t" \
  "v_cvt_pk_bf16_f32 v86, v232, v233\n\t" \
  "v_cvt_pk_bf16_f32 v87, v234, v235\n\t" \
  FSTP("v_writelane_b32 v255, s94, 42\n\t") \
  FSTP("v_writelane_b32 v255, s92, 43\n\t") \
  FSTP("s_memrealtime s[94:95]\n\t") \
  FSTP("s_memtime s[92:93]\n\t") \
  "v_mfma_f32_16x16x32_bf16 v[0:3], a[128:131], v[16:19], v[0:3]\n\t" \
  "buffer_load_dword v252, v210, s[16:19], s84 offen sc1\n\t" \
  "v_mfma_f32_16x16x32_bf16 v[32:35], a[128:131], v[48:51], v[32:35]\n\t" \
  "ds_read_b128 v[28:31], v205 offset:0\n\t" \
  "v_and_b32 v227, s98, v59\n\t" \
  "v_mfma_f32_16x16x32_bf16 v[64:67], a[128:131], v[80:83], v[64:67]\n\t" \
  "s_lshr_b32 s99, s87, 9\n\t" \
  "ds_read_b128 v[60:63], v205 offset:2048\n\t" \
  "ds_read_b64_tr_b16 a[160:161], v192 offset:40960\n\t" \
  "v_lshlrev_b32 v228, 16, v88\n\t" \
  "v_mfma_f32_16x16x32_bf16 v[96:99], a[128:131], v[112:115], v[96:99]\n\t" \
  "ds_read_b128 v[92:95], v205 offset:4096\n\t" \
  "ds_read_b64_tr_b16 a[162:163], v192 offset:41984\n\t" \
  "v_xor_b32 v248, s99, v214\n\t" \
  "v_mfma_f32_16x16x32_bf16 v[100:103], a[132:135], v[112:115], v[100:103]\n\t" \
  "v_xor_b32 v249, 256, v248\n\t" \
  "v_xor_b32 v250, 512, v248\n\t" \
  "v_mfma_f32_16x16x32_bf16 v[68:71], a[132:135], v[80:83], v[68:71]\n\t" \
  "v_xor_b32 v251, 768, v248\n\t" \
  "s_add_u32 s96, s78, 4194304\n\t" \
  "v_mfma_f32_16x16x32_bf16 v[36:39], a[132:135], v[48:51], v[36:39]\n\t" \
  "s_and_b32 s96, s96, 0x780000\n\t" \
  "ds_read_b128 v[124:127], v205 offset:6144\n\t" \
  "ds_read_b64_tr_b16 a[164:165], v193 offset:40960\n\t" \
  "s_add_u32 m0, s76, 81920\n\t" \
  "v_mfma_f32_16x16x32_bf16 v[4:7], a[132:135], v[16:19], v[4:7]\n\t" \
  "buffer_load_dwordx4 v200, s[20:23], s96 offen offset:0 lds\n\t" \
  "v_mfma_f32_16x16x32_bf16 v[8:11], a[136:139], v[16:19], v[8:11]\n\t" \
  "ds_read_b64_tr_b16 a[166:167], v193 offset:41984\n\t" \
  "v_and_b32 v229, s98, v88\n\t" \
  "v_mfma_f32_16x16x32_bf16 v[40:43], a[136:139], v[48:51], v[40:43]\n\t" \
  "ds_read_b64_tr_b16 a[168:169], v194 offset:40960\n\t" \
  "v_lshlrev_b32 v230, 16, v89\n\t" \
  "v_mfma_f32_16x16x32_bf16 v[72:75], a[136:139], v[80:83], v[72:75]\n\t" \
  "buffer_load_dwordx4 v201, s[20:23], s96 offen offset:1024 lds\n\t" \
  "v_mfma_f32_16x16x32_bf16 v[104:107], a[136:139], v[112:115], v[104:107]\n\t" \
  "ds_read_b64_tr_b16 a[170:171], v194 offset:41984\n\t" \
  "v_and_b32 v231, s98, v89\n\t" \
  "v_mfma_f32_16x16x32_bf16 v[108:111], a[140:143], v[112:115], v[108:111]\n\t" \
  "ds_read_b64_tr_b16 a[172:173], v195 offset:40960\n\t" \
  "v_cvt_pk_bf16_f32 v116, v236, v237\n\t" \
  "v_mfma_f32_16x16x32_bf16 v[76:79], a[140:143], v[80:83], v[76:79]\n\t" \
  "ds_read_b64_tr_b16 a[174:175], v195 offset:41984\n\t" \
  "v_lshlrev_b32 v232, 16, v90\n\t" \
  "v_mfma_f32_16x16x32_bf16 v[44:47], a[140:143], v[48:51], v[44:47]\n\t" \
  "ds_read_b64_tr_b16 a[176:177], v196 offset:40960\n\t" \
  "v_cvt_pk_bf16_f32 v117, v238, v239\n\t" \
  "v_mfma_f32_16x16x32_bf16 v[12:15], a[140:143], v[16:19], v[12:15]\n\t" \
  "buffer_load_dwordx4 v202, s[20:23], s96 offen offset:2048 lds\n\t" \
  "v_mfma_f32_16x16x32_bf16 a[64:67], a[144:147], v[16:19], a[64:67]\n\t" \
  "ds_read_b64_tr_b16 a[178:179], v196 offset:41984\n\t" \
  "v_and_b32 v233, s98, v90\n\t" \
  "v_mfma_f32_16x16x32_bf16 a[68:71], a[144:147], v[48:51], a[68:71]\n\t" \
  "ds_read_b64_tr_b16 a[180:181], v197 offset:40960\n\t" \
  "v_cvt_pk_bf16_f32 v118, v240, v241\n\t" \
  "v_mfma_f32_16x16x32_bf16 a[72:75], a[144:147], v[80:83], a[72:75]\n\t" \
  "ds_read_b64_tr_b16 a[182:183], v197 offset:41984\n\t" \
  "v_lshlrev_b32 v234, 16, v91\n\t" \
  "v_mfma_f32_16x16x32_bf16 a[76:79], a[144:147], v[112:115], a[76:79]\n\t" \
  "ds_read_b128 v[132:135], v208 offset:1552\n\t" \
  "v_cvt_pk_bf16_f32 v119, v242, v243\n\t" \
  "v_mfma_f32_16x16x32_bf16 a[92:95], a[148:151], v[112:115], a[92:95]\n\t" \
  "buffer_load_dwordx4 v203, s[20:23], s96 offen offset:3072 lds\n\t" \
  "v_mfma_f32_16x16x32_bf16 a[88:91], a[148:151], v[80:83], a[88:91]\n\t" \
  "ds_read_b64_tr_b16 a[184:185], v198 offset:40960\n\t" \
  "v_and_b32 v235, s98, v91\n\t" \
  "v_mfma_f32_16x16x32_bf16 a[84:87], a[148:151], v[48:51], a[84:87]\n\t" \
  "ds_read_b64_tr_b16 a[186:187], v198 offset:41984\n\t" \
  "v_lshlrev_b32 v236, 16, v120\n\t" \
  "v_mfma_f32_16x16x32_bf16 a[80:83], a[148:151], v[16:19], a[80:83]\n\t" \
  "ds_read_b64_tr_b16 a[188:189], v199 offset:40960\n\t" \
  "v_and_b32 v237, s98, v120\n\t" \
  "v_mfma_f32_16x16x32_bf16 a[96:99], a[152:155], v[16:19], a[96:99]\n\t" \
  "ds_read_b128 v[136:139], v208 offset:1792\n\t" \
  "v_lshlrev_b32 v238, 16, v121\n\t" \
  "v_mfma_f32_16x16x32_bf16 a[100:103], a[152:155], v[48:51], a[100:103]\n\t" \
  "global_load_dword v246, v216, s[20:21]\n\t" \
  "v_mfma_f32_16x16x32_bf16 a[104:107], a[152:155], v[80:83], a[104:107]\n\t" \
  "ds_read_b64_tr_b16 a[190:191], v199 offset:41984\n\t" \
  "v_and_b32 v239, s98, v121\n\t" \
  "v_mfma_f32_16x16x32_bf16 a[108:111], a[152:155], v[112:115], a[108:111]\n\t" \
  "ds_read_b128 v[140:143], v208 offset:1808\n\t" \
  "v_lshlrev_b32 v240, 16, v122\n\t" \
  "v_mfma_f32_16x16x32_bf16 a[124:127], a[156:159], v[112:115], a[124:127]\n\t" \
  "ds_read_b128 v[144:147], v208 offset:1664\n\t" \
  "v_and_b32 v241, s98, v122\n\t" \
  "v_lshlrev_b32 v242, 16, v123\n\t" \
  "v_and_b32 v243, s98, v123\n\t" \
  "s_waitcnt lgkmcnt(1)\n\t" \
  "v_pk_fma_f32 v[176:177], v[176:177], v[160:161], v[162:163] op_sel_hi:[1,0,0]\n\t" \
  "v_pk_fma_f32 v[178:179], v[178:179], v[160:161], v[162:163] op_sel_hi:[1,0,0]\n\t" \
  "v_pk_fma_f32 v[180:181], v[180:181], v[160:161], v[162:163] op_sel_hi:[1,0,0]\n\t" \
  "v_pk_fma_f32 v[182:183], v[182:183], v[160:161], v[162:163] op_sel_hi:[1,0,0]\n\t" \
  "v_pk_fma_f32 v[176:177], v[176:177], v[128:129], v[136:137]\n\t" \
  "v_pk_fma_f32 v[178:179], v[178:179], v[130:131], v[138:139]\n\t" \
  "v_pk_fma_f32 v[180:181], v[180:181], v[132:133], v[140:141]\n\t" \
  "v_pk_fma_f32 v[182:183], v[182:183], v[134:135], v[142:143]\n\t" \
  "v_pk_fma_f32 v[220:221], v[220:221], v[164:165], v[166:167] op_sel_hi:[1,0,0]\n\t" \
  "v_pk_fma_f32 v[222:223], v[222:223], v[164:165], v[166:167] op_sel_hi:[1,0,0]\n\t" \
  "v_pk_fma_f32 v[224:225], v[224:225], v[164:165], v[166:167] op_sel_hi:[1,0,0]\n\t" \
  "v_pk_fma_f32 v[226:227], v[226:227], v[164:165], v[166:167] op_sel_hi:[1,0,0]\n\t" \
  "v_pk_fma_f32 v[220:221], v[220:221], v[128:129], v[136:137]\n\t" \
  "v_pk_fma_f32 v[222:223], v[222:223], v[130:131], v[138:139]\n\t" \
  "v_pk_fma_f32 v[224:225], v[224:225], v[132:133], v[140:141]\n\t" \
  "v_pk_fma_f32 v[226:227], v[226:227], v[134:135], v[142:143]\n\t" \
  "v_cvt_pk_bf16_f32 v24, v176, v177\n\t" \
  "v_mfma_f32_16x16x32_bf16 a[120:123], a[156:159], v[80:83], a[120:123]\n\t" \
  "v_cvt_pk_bf16_f32 v25, v178, v179\n\t" \
  "v_cvt_pk_bf16_f32 v26, v180, v181\n\t" \
  "v_cvt_pk_bf16_f32 v27, v182, v183\n\t" \
  "v_cvt_pk_bf16_f32 v56, v220, v221\n\t" \
  "v_cvt_pk_bf16_f32 v57, v222, v223\n\t" \
  "v_pk_fma_f32 v[228:229], v[228:229], v[168:169], v[170:171] op_sel_hi:[1,0,0]\n\t" \
  "v_pk_fma_f32 v[230:231], v[230:231], v[168:169], v[170:171] op_sel_hi:[1,0,0]\n\t" \
  "v_pk_fma_f32 v[232:233], v[232:233], v[168:169], v[170:171] op_sel_hi:[1,0,0]\n\t" \
  "v_pk_fma_f32 v[234:235], v[234:235], v[168:169], v[170:171] op_sel_hi:[1,0,0]\n\t" \
  "v_pk_fma_f32 v[228:229], v[228:229], v[128:129], v[136:137]\n\t" \
  "v_pk_fma_f32 v[230:231], v[230:231], v[130:131], v[138:139]\n\t" \
  "v_pk_fma_f32 v[232:233], v[232:233], v[132:133], v[140:141]\n\t" \
  "v_pk_fma_f32 v[234:235], v[234:235], v[134:135], v[142:143]\n\t" \
  "v_pk_fma_f32 v[236:237], v[236:237], v[172:173], v[174:175] op_sel_hi:[1,0,0]\n\t" \
  "v_pk_fma_f32 v[238:239], v[238:239], v[172:173], v[174:175] op_sel_hi:[1,0,0]\n\t" \
  "v_pk_fma_f32 v[240:241], v[240:241], v[172:173], v[174:175] op_sel_hi:[1,0,0]\n\t" \
  "v_pk_fma_f32 v[242:243], v[242:243], v[172:173], v[174:175] op_sel_hi:[1,0,0]\n\t" \
  "v_pk_fma_f32 v[236:237], v[236:237], v[128:129], v[136:137]\n\t" \
  "v_pk_fma_f32 v[238:239], v[238:239], v[130:131], v[138:139]\n\t" \
  "v_pk_fma_f32 v[240:241], v[240:241], v[132:133], v[140:141]\n\t" \
  "v_pk_fma_f32 v[242:243], v[242:243], v[134:135], v[142:143]\n\t" \
  "v_cvt_pk_bf16_f32 v88, v228, v229\n\t" \
  "v_mfma_f32_16x16x32_bf16 a[116:119], a[156:159], v[48:51], a[116:119]\n\t" \
  "s_waitcnt vmcnt(23)\n\t" \
  "v_cvt_pk_bf16_f32 v58, v224, v225\n\t" \
  "v_cvt_pk_bf16_f32 v59, v226, v227\n\t" \
  "v_mfma_f32_16x16x32_bf16 a[112:115], a[156:159], v[16:19], a[112:115]\n\t" \
  "s_barrier\n\t" \
  FSTP("v_writelane_b32 v255, s94, 44\n\t") \
  FSTP("v_writelane_b32 v255, s92, 45\n\t") \
  FSTP("s_memrealtime s[94:95]\n\t") \
  FSTP("s_memtime s[92:93]\n\t") \
  "v_mfma_f32_16x16x32_bf16 v[0:3], a[160:163], v[20:23], v[0:3]\n\t" \
  "s_waitcnt vmcnt(5)\n\t" \
  "v_cmp_ne_u32 vcc, v211, v252\n\t" \
  "v_cvt_pk_bf16_f32 v89, v230, v231\n\t" \
  "v_mfma_f32_16x16x32_bf16 v[32:35], a[160:163], v[52:55], v[32:35]\n\t" \
  "ds_read_b64_tr_b16 a[128:129], v184 offset:0\n\t" \
  "v_cvt_pk_bf16_f32 v90, v232, v233\n\t" \
  "v_mfma_f32_16x16x32_bf16 v[64:67], a[160:163], v[84:87], v[64:67]\n\t" \
  "s_cbranch_vccnz .Lfq_s\n\t" \
  ".Lfq_k:\n\t" \
  "v_mfma_f32_16x16x32_bf16 v[96:99], a[160:163], v[116:119], v[96:99]\n\t" \
  "s_add_u32 s97, s78, 2097152\n\t" \
  "s_bfe_u32 s97, s97, 0x140003\n\t" \
  "s_add_u32 s97, s97, s82\n\t" \
  "v_mfma_f32_16x16x32_bf16 v[100:103], a[164:167], v[116:119], v[100:103]\n\t" \
  "buffer_load_dwordx4 a[0:3], v248, s[16:19], s97 offen sc1\n\t" \
  "v_mfma_f32_16x16x32_bf16 v[68:71], a[164:167], v[84:87], v[68:71]\n\t" \
  "buffer_load_dwordx4 a[4:7], v249, s[16:19], s97 offen sc1\n\t" \
  "v_mfma_f32_16x16x32_bf16 v[36:39], a[164:167], v[52:55], v[36:39]\n\t" \
  "ds_read_b64_tr_b16 a[130:131], v184 offset:1024\n\t" \
  "v_cvt_pk_bf16_f32 v91, v234, v235\n\t" \
  "v_mfma_f32_16x16x32_bf16 v[4:7], a[164:167], v[20:23], v[4:7]\n\t" \
  "ds_read_b64_tr_b16 a[132:133], v185 offset:0\n\t" \
  "v_cvt_pk_bf16_f32 v120, v236, v237\n\t" \
  "v_mfma_f32_16x16x32_bf16 v[8:11], a[168:171], v[20:23], v[8:11]\n\t" \
  "buffer_load_dwordx4 a[8:11], v250, s[16:19], s97 offen sc1\n\t" \
  "v_mfma_f32_16x16x32_bf16 v[40:43], a[168:171], v[52:55], v[40:43]\n\t" \
  "ds_read_b64_tr_b16 a[134:135], v185 offset:1024\n\t" \
  "v_cvt_pk_bf16_f32 v121, v238, v239\n\t" \
  "v_mfma_f32_16x16x32_bf16 v[72:75], a[168:171], v[84:87], v[72:75]\n\t" \
  "v_lshlrev_b32 v176, 16, v28\n\t" \
  "v_and_b32 v177, s98, v28\n\t" \
  "v_mfma_f32_16x16x32_bf16 v[104:107], a[168:171], v[116:119], v[104:107]\n\t" \
  "ds_read_b64_tr_b16 a[136:137], v186 offset:0\n\t" \
  "v_lshlrev_b32 v178, 16, v29\n\t" \
  "v_mfma_f32_16x16x32_bf16 v[108:111], a[172:175], v[116:119], v[108:111]\n\t" \
  "buffer_load_dwordx4 a[12:15], v251, s[16:19], s97 offen sc1\n\t" \
  "v_mfma_f32_16x16x32_bf16 v[76:79], a[172:175], v[84:87], v[76:79]\n\t" \
  "s_add_u32 s97, s97, s83\n\t" \
  "ds_read_b128 v[148:151], v208 offset:1680\n\t" \
  "ds_read_b64_tr_b16 a[138:139], v186 offset:1024\n\t" \
  "v_and_b32 v179, s98, v29\n\t" \
  "v_mfma_f32_16x16x32_bf16 v[44:47], a[172:175], v[52:55], v[44:47]\n\t" \
  "ds_read_b128 v[152:155], v208 offset:1920\n\t" \
  "ds_read_b64_tr_b16 a[140:141], v187 offset:0\n\t" \
  "v_lshlrev_b32 v180, 16, v30\n\t" \
  "v_mfma_f32_16x16x32_bf16 v[12:15], a[172:175], v[20:23], v[12:15]\n\t" \
  "buffer_load_dwordx4 a[16:19], v248, s[16:19], s97 offen sc1\n\t" \
  "v_mfma_f32_16x16x32_bf16 a[64:67], a[176:179], v[20:23], a[64:67]\n\t" \
  "ds_read_b64_tr_b16 a[142:143], v187 offset:1024\n\t" \
  "v_and_b32 v181, s98, v30\n\t" \
  "v_mfma_f32_16x16x32_bf16 a[68:71], a[176:179], v[52:55], a[68:71]\n\t" \
  "ds_read_b64_tr_b16 a[144:145], v188 offset:0\n\t" \
  "v_lshlrev_b32 v182, 16, v31\n\t" \
  "v_mfma_f32_16x16x32_bf16 a[72:75], a[176:179], v[84:87], a[72:75]\n\t" \
  "ds_read_b64_tr_b16 a[146:147], v188 offset:1024\n\t" \
  "v_and_b32 v183, s98, v31\n\t" \
  "v_mfma_f32_16x16x32_bf16 a[76:79], a[176:179], v[116:119], a[76:79]\n\t" \
  "buffer_load_dwordx4 a[20:23], v249, s[16:19], s97 offen sc1\n\t" \
  "v_mfma_f32_16x16x32_bf16 a[92:95], a[180:183], v[116:119], a[92:95]\n\t" \
  "ds_read_b64_tr_b16 a[148:149], v189 offset:0\n\t" \
  "v_lshlrev_b32 v220, 16, v60\n\t" \
  "v_mfma_f32_16x16x32_bf16 a[88:91], a[180:183], v[84:87], a[88:91]\n\t" \
  "ds_read_b64_tr_b16 a[150:151], v189 offset:1024\n\t" \
  "v_and_b32 v221, s98, v60\n\t" \
  "v_mfma_f32_16x16x32_bf16 a[84:87], a[180:183], v[52:55], a[84:87]\n\t" \
  "ds_read_b64_tr_b16 a[152:153], v190 offset:0\n\t" \
  "v_lshlrev_b32 v222, 16, v61\n\t" \
  "v_mfma_f32_16x16x32_bf16 a[80:83], a[180:183], v[20:23], a[80:83]\n\t" \
  "buffer_load_dwordx4 a[24:27], v250, s[16:19], s97 offen sc1\n\t" \
  "v_mfma_f32_16x16x32_bf16 a[96:99], a[184:187], v[20:23], a[96:99]\n\t" \
  "ds_read_b64_tr_b16 a[154:155], v190 offset:1024\n\t" \
  "v_cvt_pk_bf16_f32 v122, v240, v241\n\t" \
  "v_mfma_f32_16x16x32_bf16 a[100:103], a[184:187], v[52:55], a[100:103]\n\t" \
  "ds_read_b64_tr_b16 a[156:157], v191 offset:0\n\t" \
  "v_and_b32 v223, s98, v61\n\t" \
  "v_mfma_f32_16x16x32_bf16 a[104:107], a[184:187], v[84:87], a[104:107]\n\t" \
  "ds_read_b64_tr_b16 a[158:159], v191 offset:1024\n\t" \
  "v_cvt_pk_bf16_f32 v123, v242, v243\n\t" \
  "v_mfma_f32_16x16x32_bf16 a[108:111], a[184:187], v[116:119], a[108:111]\n\t" \
  "buffer_load_dwordx4 a[28:31], v251, s[16:19], s97 offen sc1\n\t" \
  "v_mfma_f32_16x16x32_bf16 a[124:127], a[188:191], v[116:119], a[124:127]\n\t" \
  "ds_read_b128 v[156:159], v208 offset:1936\n\t" \
  "v_lshlrev_b32 v224, 16, v62\n\t" \
  "v_mfma_f32_16x16x32_bf16 a[120:123], a[188:191], v[84:87], a[120:123]\n\t" \
  "s_add_u32 s97, s78, 2621440\n\t" \
  "v_and_b32 v225, s98, v62\n\t" \
  "v_lshlrev_b32 v226, 16, v63\n\t" \
  "v_mfma_f32_16x16x32_bf16 a[116:119], a[188:191], v[52:55], a[116:119]\n\t" \
  "s_bfe_u32 s97, s97, 0x140003\n\t" \
  "s_add_u32 s97, s97, s82\n\t" \
  "v_and_b32 v227, s98, v63\n\t" \
  "v_mfma_f32_16x16x32_bf16 a[112:115], a[188:191], v[20:23], a[112:115]\n\t" \
  "s_waitcnt lgkmcnt(1)\n\t" \
  "v_lshlrev_b32 v228, 16, v92\n\t" \
  "v_and_b32 v229, s98, v92\n\t" \
  "v_lshlrev_b32 v230, 16, v93\n\t" \
  "v_and_b32 v231, s98, v93\n\t" \
  "s_waitcnt lgkmcnt(0)\n\t" \
  "v_pk_fma_f32 v[176:177], v[176:177], v[160:161], v[162:163] op_sel_hi:[1,0,0]\n\t" \
  "v_pk_fma_f32 v[178:179], v[178:179], v[160:161], v[162:163] op_sel_hi:[1,0,0]\n\t" \
  "v_pk_fma_f32 v[180:181], v[180:181], v[160:161], v[162:163] op_sel_hi:[1,0,0]\n\t" \
  "v_pk_fma_f32 v[182:183], v[182:183], v[160:161], v[162:163] op_sel_hi:[1,0,0]\n\t" \
  "v_pk_fma_f32 v[176:177], v[176:177], v[144:145], v[152:153]\n\t" \
  "v_pk_fma_f32 v[178:179], v[178:179], v[146:147], v[154:155]\n\t" \
  "v_pk_fma_f32 v[180:181], v[180:181], v[148:149], v[156:157]\n\t" \
  "v_pk_fma_f32 v[182:183], v[182:183], v[150:151], v[158:159]\n\t" \
  "v_pk_fma_f32 v[220:221], v[220:221], v[164:165], v[166:167] op_sel_hi:[1,0,0]\n\t" \
  "v_pk_fma_f32 v[222:223], v[222:223], v[164:165], v[166:167] op_sel_hi:[1,0,0]\n\t" \
  "v_pk_fma_f32 v[224:225], v[224:225], v[164:165], v[166:167] op_sel_hi:[1,0,0]\n\t" \
  "v_pk_fma_f32 v[226:227], v[226:227], v[164:165], v[166:167] op_sel_hi:[1,0,0]\n\t" \
  "v_pk_fma_f32 v[220:221], v[220:221], v[144:145], v[152:153]\n\t" \
  "v_pk_fma_f32 v[222:223], v[222:223], v[146:147], v[154:155]\n\t" \
  "v_pk_fma_f32 v[224:225], v[224:225], v[148:149], v[156:157]\n\t" \
  "v_pk_fma_f32 v[226:227], v[226:227], v[150:151], v[158:159]\n\t" \
  "v_cvt_pk_bf16_f32 v28, v176, v177\n\t" \
  FSTP("v_writelane_b32 v255, s94, 46\n\t") \
  FSTP("v_writelane_b32 v255, s92, 47\n\t") \
  FSTP("s_memrealtime s[94:95]\n\t") \
  FSTP("s_memtime s[92:93]\n\t") \
  "v_mfma_f32_16x16x32_bf16 v[0:3], a[128:131], v[24:27], v[0:3]\n\t" \
  "buffer_load_dwordx4 a[32:35], v248, s[16:19], s97 offen sc1\n\t" \
  "v_mfma_f32_16x16x32_bf16 v[32:35], a[128:131], v[56:59], v[32:35]\n\t" \
  "ds_read_b64_tr_b16 a[160:161], v184 offset:8192\n\t" \
  "v_lshlrev_b32 v232, 16, v94\n\t" \
  "v_mfma_f32_16x16x32_bf16 v[64:67], a[128:131], v[88:91], v[64:67]\n\t" \
  "ds_read_b64_tr_b16 a[162:163], v184 offset:9216\n\t" \
  "v_and_b32 v233, s98, v94\n\t" \
  "v_mfma_f32_16x16x32_bf16 v[96:99], a[128:131], v[120:123], v[96:99]\n\t" \
  "v_lshlrev_b32 v234, 16, v95\n\t" \
  "v_and_b32 v235, s98, v95\n\t" \
  "v_mfma_f32_16x16x32_bf16 v[100:103], a[132:135], v[120:123], v[100:103]\n\t" \
  "buffer_load_dwordx4 a[36:39], v249, s[16:19], s97 offen sc1\n\t" \
  "v_mfma_f32_16x16x32_bf16 v[68:71], a[132:135], v[88:91], v[68:71]\n\t" \
  "ds_read_b64_tr_b16 a[164:165], v185 offset:8192\n\t" \
  "v_lshlrev_b32 v236, 16, v124\n\t" \
  "v_mfma_f32_16x16x32_bf16 v[36:39], a[132:135], v[56:59], v[36:39]\n\t" \
  "ds_read_b64_tr_b16 a[166:167], v185 offset:9216\n\t" \
  "v_and_b32 v237, s98, v124\n\t" \
  "v_mfma_f32_16x16x32_bf16 v[4:7], a[132:135], v[24:27], v[4:7]\n\t" \
  "ds_read_b64_tr_b16 a[168:169], v186 offset:8192\n\t" \
  "v_cvt_pk_bf16_f32 v29, v178, v179\n\t" \
  "v_mfma_f32_16x16x32_bf16 v[8:11], a[136:139], v[24:27], v[8:11]\n\t" \
  "buffer_load_dwordx4 a[40:43], v250, s[16:19], s97 offen sc1\n\t" \
  "v_mfma_f32_16x16x32_bf16 v[40:43], a[136:139], v[56:59], v[40:43]\n\t" \
  "ds_read_b64_tr_b16 a[170:171], v186 offset:9216\n\t" \
  "v_lshlrev_b32 v238, 16, v125\n\t" \
  "v_mfma_f32_16x16x32_bf16 v[72:75], a[136:139], v[88:91], v[72:75]\n\t" \
  "ds_read_b64_tr_b16 a[172:173], v187 offset:8192\n\t" \
  "v_and_b32 v239, s98, v125\n\t" \
  "v_mfma_f32_16x16x32_bf16 v[104:107], a[136:139], v[120:123], v[104:107]\n\t" \
  "v_cvt_pk_bf16_f32 v30, v180, v181\n\t" \
  "v_cvt_pk_bf16_f32 v31, v182, v183\n\t" \
  "v_mfma_f32_16x16x32_bf16 v[108:111], a[140:143], v[120:123], v[108:111]\n\t" \
  "buffer_load_dwordx4 a[44:47], v251, s[16:19], s97 offen sc1\n\t" \
  "v_mfma_f32_16x16x32_bf16 v[76:79], a[140:143], v[88:91], v[76:79]\n\t" \
  "s_add_u32 s97, s97, s83\n\t" \
  "ds_read_b64_tr_b16 a[174:175], v187 offset:9216\n\t" \
  "v_lshlrev_b32 v240, 16, v126\n\t" \
  "v_mfma_f32_16x16x32_bf16 v[44:47], a[140:143], v[56:59], v[44:47]\n\t" \
  "ds_read_b64_tr_b16 a[176:177], v188 offset:8192\n\t" \
  "v_and_b32 v241, s98, v126\n\t" \
  "v_mfma_f32_16x16x32_bf16 v[12:15], a[140:143], v[24:27], v[12:15]\n\t" \
  "buffer_load_dwordx4 a[48:51], v248, s[16:19], s97 offen sc1\n\t" \
  "v_mfma_f32_16x16x32_bf16 a[64:67], a[144:147], v[24:27], a[64:67]\n\t" \
  "ds_read_b64_tr_b16 a[178:179], v188 offset:9216\n\t" \
  "v_cvt_pk_bf16_f32 v60, v220, v221\n\t" \
  "v_mfma_f32_16x16x32_bf16 a[68:71], a[144:147], v[56:59], a[68:71]\n\t" \
  "ds_read_b64_tr_b16 a[180:181], v189 offset:8192\n\t" \
  "v_cvt_pk_bf16_f32 v61, v222, v223\n\t" \
  "v_mfma_f32_16x16x32_bf16 a[72:75], a[144:147], v[88:91], a[72:75]\n\t" \
  "ds_read_b64_tr_b16 a[182:183], v189 offset:9216\n\t" \
  "v_lshlrev_b32 v242, 16, v127\n\t" \
  "v_mfma_f32_16x16x32_bf16 a[76:79], a[144:147], v[120:123], a[76:79]\n\t" \
  "buffer_load_dwordx4 a[52:55], v249, s[16:19], s97 offen sc1\n\t" \
  "v_mfma_f32_16x16x32_bf16 a[92:95], a[148:151], v[120:123], a[92:95]\n\t" \
  "ds_read_b64_tr_b16 a[184:185], v190 offset:8192\n\t" \
  "v_and_b32 v243, s98, v127\n\t" \
  "v_mfma_f32_16x16x32_bf16 a[88:91], a[148:151], v[88:91], a[88:91]\n\t" \
  "buffer_load_dwordx4 a[56:59], v250, s[16:19], s97 offen sc1\n\t" \
  "ds_read_b64_tr_b16 a[186:187], v190 offset:9216\n\t" \
  "ds_read_b64_tr_b16 a[188:189], v191 offset:8192\n\t" \
  "v_cvt_pk_bf16_f32 v62, v224, v225\n\t" \
  "v_cvt_pk_bf16_f32 v63, v226, v227\n\t" \
  "v_pk_fma_f32 v[228:229], v[228:229], v[168:169], v[170:171] op_sel_hi:[1,0,0]\n\t" \
  "v_pk_fma_f32 v[230:231], v[230:231], v[168:169], v[170:171] op_sel_hi:[1,0,0]\n\t" \
  "v_pk_fma_f32 v[232:233], v[232:233], v[168:169], v[170:171] op_sel_hi:[1,0,0]\n\t" \
  "v_pk_fma_f32 v[234:235], v[234:235], v[168:169], v[170:171] op_sel_hi:[1,0,0]\n\t" \
  "v_pk_fma_f32 v[228:229], v[228:229], v[144:145], v[152:153]\n\t" \
  "v_pk_fma_f32 v[230:231], v[230:231], v[146:147], v[154:155]\n\t" \
  "v_pk_fma_f32 v[232:233], v[232:233], v[148:149], v[156:157]\n\t" \
  "v_pk_fma_f32 v[234:235], v[234:235], v[150:151], v[158:159]\n\t" \
  "v_pk_fma_f32 v[236:237], v[236:237], v[172:173], v[174:175] op_sel_hi:[1,0,0]\n\t" \
  "v_pk_fma_f32 v[238:239], v[238:239], v[172:173], v[174:175] op_sel_hi:[1,0,0]\n\t" \
  "v_pk_fma_f32 v[240:241], v[240:241], v[172:173], v[174:175] op_sel_hi:[1,0,0]\n\t" \
  "v_pk_fma_f32 v[242:243], v[242:243], v[172:173], v[174:175] op_sel_hi:[1,0,0]\n\t" \
  "v_pk_fma_f32 v[236:237], v[236:237], v[144:145], v[152:153]\n\t" \
  "v_pk_fma_f32 v[238:239], v[238:239], v[146:147], v[154:155]\n\t" \
  "v_pk_fma_f32 v[240:241], v[240:241], v[148:149], v[156:157]\n\t" \
  "v_pk_fma_f32 v[242:243], v[242:243], v[150:151], v[158:159]\n\t" \
  "v_cvt_pk_bf16_f32 v92, v228, v229\n\t" \
  "v_mfma_f32_16x16x32_bf16 a[84:87], a[148:151], v[56:59], a[84:87]\n\t" \
  "v_cvt_pk_bf16_f32 v93, v230, v231\n\t" \
  "v_cvt_pk_bf16_f32 v94, v232, v233\n\t" \
  "v_mfma_f32_16x16x32_bf16 a[80:83], a[148:151], v[24:27], a[80:83]\n\t" \
  "v_cvt_pk_bf16_f32 v95, v234, v235\n\t" \
  "v_cvt_pk_bf16_f32 v124, v236, v237\n\t" \
  "v_mfma_f32_16x16x32_bf16 a[96:99], a[152:155], v[24:27], a[96:99]\n\t" \
  "v_cvt_pk_bf16_f32 v125, v238, v239\n\t" \
  "v_cvt_pk_bf16_f32 v126, v240, v241\n\t" \
  "v_mfma_f32_16x16x32_bf16 a[100:103], a[152:155], v[56:59], a[100:103]\n\t" \
  "ds_read_b64_tr_b16 a[190:191], v191 offset:9216\n\t" \
  "v_cvt_pk_bf16_f32 v127, v242, v243\n\t" \
  "v_mfma_f32_16x16x32_bf16 a[104:107], a[152:155], v[88:91], a[104:107]\n\t" \
  "buffer_load_dwordx4 a[60:63], v251, s[16:19], s97 offen sc1\n\t" \
  "v_mfma_f32_16x16x32_bf16 a[108:111], a[152:155], v[120:123], a[108:111]\n\t" \
  "v_mfma_f32_16x16x32_bf16 a[124:127], a[156:159], v[120:123], a[124:127]\n\t" \
  "v_mfma_f32_16x16x32_bf16 a[120:123], a[156:159], v[88:91], a[120:123]\n\t" \
  "s_add_u32 s97, s78, 3145728\n\t" \
  "s_bfe_u32 s97, s97, 0x140003\n\t" \
  "s_add_u32 s97, s97, s82\n\t" \
  "v_mfma_f32_16x16x32_bf16 a[116:119], a[156:159], v[56:59], a[116:119]\n\t" \
  "s_waitcnt lgkmcnt(0)\n\t" \
  "v_mfma_f32_16x16x32_bf16 a[112:115], a[156:159], v[24:27], a[112:115]\n\t" \
  "s_barrier\n\t" \
  FSTP("v_writelane_b32 v255, s94, 48\n\t") \
  FSTP("v_writelane_b32 v255, s92, 49\n\t") \
  FSTP("s_memrealtime s[94:95]\n\t") \
  FSTP("s_memtime s[92:93]\n\t") \
  "v_mfma_f32_16x16x32_bf16 v[0:3], a[160:163], v[28:31], v[0:3]\n\t" \
  "buffer_load_dwordx4 a[192:195], v248, s[16:19], s97 offen sc1\n\t" \
  "v_mfma_f32_16x16x32_bf16 v[32:35], a[160:163], v[60:63], v[32:35]\n\t" \
  "ds_read_b64_tr_b16 a[128:129], v184 offset:16384\n\t" \
  "v_mfma_f32_16x16x32_bf16 v[64:67], a[160:163], v[92:95], v[64:67]\n\t" \
  "ds_read_b64_tr_b16 a[130:131], v184 offset:17408\n\t" \
  "v_mfma_f32_16x16x32_bf16 v[96:99], a[160:163], v[124:127], v[96:99]\n\t" \
  "ds_write_b32 v219, v218\n\t" \
  "v_mfma_f32_16x16x32_bf16 v[100:103], a[164:167], v[124:127], v[100:103]\n\t" \
  "buffer_load_dwordx4 a[196:199], v249, s[16:19], s97 offen sc1\n\t" \
  "v_mfma_f32_16x16x32_bf16 v[68:71], a[164:167], v[92:95], v[68:71]\n\t" \
  "ds_read_b64_tr_b16 a[132:133], v185 offset:16384\n\t" \
  "v_and_b32 v219, 0x7fffffe0, v219\n\t" \
  "v_mfma_f32_16x16x32_bf16 v[36:39], a[164:167], v[60:63], v[36:39]\n\t" \
  "ds_read_b64_tr_b16 a[134:135], v185 offset:17408\n\t" \
  "v_mfma_f32_16x16x32_bf16 v[4:7], a[164:167], v[28:31], v[4:7]\n\t" \
  "ds_read_b64_tr_b16 a[136:137], v186 offset:16384\n\t" \
  "v_mfma_f32_16x16x32_bf16 v[8:11], a[168:171], v[28:31], v[8:11]\n\t" \
  "buffer_load_dwordx4 a[200:203], v250, s[16:19], s97 offen sc1\n\t" \
  "v_mfma_f32_16x16x32_bf16 v[40:43], a[168:171], v[60:63], v[40:43]\n\t" \
  "ds_read_b64_tr_b16 a[138:139], v186 offset:17408\n\t" \
  "v_mfma_f32_16x16x32_bf16 v[72:75], a[168:171], v[92:95], v[72:75]\n\t" \
  "ds_read_b64_tr_b16 a[140:141], v187 offset:16384\n\t" \
  "v_mfma_f32_16x16x32_bf16 v[104:107], a[168:171], v[124:127], v[104:107]\n\t" \
  "ds_read_b128 v[168:171], v219 offset:0\n\t" \
  "v_mfma_f32_16x16x32_bf16 v[108:111], a[172:175], v[124:127], v[108:111]\n\t" \
  "buffer_load_dwordx4 a[204:207], v251, s[16:19], s97 offen sc1\n\t" \
  "v_mfma_f32_16x16x32_bf16 v[76:79], a[172:175], v[92:95], v[76:79]\n\t" \
  "ds_read_b64_tr_b16 a[142:143], v187 offset:17408\n\t" \
  "s_add_u32 s97, s97, s83\n\t" \
  "v_mfma_f32_16x16x32_bf16 v[44:47], a[172:175], v[60:63], v[44:47]\n\t" \
  "ds_read_b64_tr_b16 a[144:145], v188 offset:16384\n\t" \
  "v_mfma_f32_16x16x32_bf16 v[12:15], a[172:175], v[28:31], v[12:15]\n\t" \
  "buffer_load_dwordx4 a[208:211], v248, s[16:19], s97 offen sc1\n\t" \
  "v_mfma_f32_16x16x32_bf16 a[64:67], a[176:179], v[28:31], a[64:67]\n\t" \
  "ds_read_b64_tr_b16 a[146:147], v188 offset:17408\n\t" \
  "v_mfma_f32_16x16x32_bf16 a[68:71], a[176:179], v[60:63], a[68:71]\n\t" \
  "ds_read_b64_tr_b16 a[148:149], v189 offset:16384\n\t" \
  "v_mfma_f32_16x16x32_bf16 a[72:75], a[176:179], v[92:95], a[72:75]\n\t" \
  "ds_read_b64_tr_b16 a[150:151], v189 offset:17408\n\t" \
  "v_mfma_f32_16x16x32_bf16 a[76:79], a[176:179], v[124:127], a[76:79]\n\t" \
  "buffer_load_dwordx4 a[212:215], v249, s[16:19], s97 offen sc1\n\t" \
  "v_mfma_f32_16x16x32_bf16 a[92:95], a[180:183], v[124:127], a[92:95]\n\t" \
  "ds_read_b64_tr_b16 a[152:153], v190 offset:16384\n\t" \
  "v_mfma_f32_16x16x32_bf16 a[88:91], a[180:183], v[92:95], a[88:91]\n\t" \
  "ds_read_b128 v[172:175], v219 offset:16\n\t" \
  "v_mfma_f32_16x16x32_bf16 a[84:87], a[180:183], v[60:63], a[84:87]\n\t" \
  "ds_read_b64_tr_b16 a[154:155], v190 offset:17408\n\t" \
  "v_mfma_f32_16x16x32_bf16 a[80:83], a[180:183], v[28:31], a[80:83]\n\t" \
  "buffer_load_dwordx4 a[216:219], v250, s[16:19], s97 offen sc1\n\t" \
  "v_mfma_f32_16x16x32_bf16 a[96:99], a[184:187], v[28:31], a[96:99]\n\t" \
  "ds_read_b64_tr_b16 a[156:157], v191 offset:16384\n\t" \
  "v_mfma_f32_16x16x32_bf16 a[100:103], a[184:187], v[60:63], a[100:103]\n\t" \
  "ds_read_b64_tr_b16 a[158:159], v191 offset:17408\n\t" \
  "v_mfma_f32_16x16x32_bf16 a[104:107], a[184:187], v[92:95], a[104:107]\n\t" \
  "buffer_load_dwordx4 a[220:223], v251, s[16:19], s97 offen sc1\n\t" \
  "v_mfma_f32_16x16x32_bf16 a[108:111], a[184:187], v[124:127], a[108:111]\n\t" \
  "v_mfma_f32_16x16x32_bf16 a[124:127], a[188:191], v[124:127], a[124:127]\n\t" \
  "v_mfma_f32_16x16x32_bf16 a[120:123], a[188:191], v[92:95], a[120:123]\n\t" \
  "s_add_u32 s97, s78, 3670016\n\t" \
  "s_bfe_u32 s97, s97, 0x140003\n\t" \
  "s_add_u32 s97, s97, s82\n\t" \
  "v_mfma_f32_16x16x32_bf16 a[116:119], a[188:191], v[60:63], a[116:119]\n\t" \
  "v_mfma_f32_16x16x32_bf16 a[112:115], a[188:191], v[28:31], a[112:115]\n\t" \
  "s_waitcnt vmcnt(20) lgkmcnt(0)\n\t" \
  FSTP("v_writelane_b32 v255, s94, 50\n\t") \
  FSTP("v_writelane_b32 v255, s92, 51\n\t") \
  FSTP("s_memrealtime s[94:95]\n\t") \
  FSTP("s_memtime s[92:93]\n\t") \
  FSTS("s_memrealtime s[94:95]\n\t") \
  FSTS("s_waitcnt lgkmcnt(0)\n\t") \
  FSTS("v_writelane_b32 v255, s94, 14\n\t") \
  "v_mfma_f32_16x16x32_bf16 a[64:67], a[144:147], a[0:3], a[64:67]\n\t" \
  "buffer_load_dwordx4 a[224:227], v248, s[16:19], s97 offen sc1\n\t" \
  "v_mfma_f32_16x16x32_bf16 a[68:71], a[144:147], a[4:7], a[68:71]\n\t" \
  "s_bfe_u32 s39, s2, 0x30003\n\t" \
  "ds_read_b64_tr_b16 a[160:161], v184 offset:24576\n\t" \
  "s_lshl_b32 s40, s39, 18\n\t" \
  "v_mfma_f32_16x16x32_bf16 a[72:75], a[144:147], a[8:11], a[72:75]\n\t" \
  "s_lshr_b32 s98, s2, 3\n\t" \
  "ds_read_b64_tr_b16 a[162:163], v184 offset:25600\n\t" \
  "v_accvgpr_read_b32 v16, a64\n\t" \
  "v_mfma_f32_16x16x32_bf16 a[76:79], a[144:147], a[12:15], a[76:79]\n\t" \
  "s_lshl_b32 s41, s3, 5\n\t" \
  "v_accvgpr_read_b32 v17, a65\n\t" \
  "v_accvgpr_read_b32 v18, a66\n\t" \
  "v_mfma_f32_16x16x32_bf16 a[92:95], a[148:151], a[12:15], a[92:95]\n\t" \
  ".Lhs2:\n\ts_cbranch_execz .Lhs2n\n\t" \
  "buffer_load_dwordx4 a[228:231], v249, s[16:19], s97 offen sc1\n\t" \
  "v_mfma_f32_16x16x32_bf16 a[88:91], a[148:151], a[8:11], a[88:91]\n\t" \
  "s_add_u32 s41, s41, s98\n\t" \
  "ds_read_b64_tr_b16 a[164:165], v185 offset:24576\n\t" \
  "v_accvgpr_read_b32 v19, a67\n\t" \
  "v_mfma_f32_16x16x32_bf16 a[84:87], a[148:151], a[4:7], a[84:87]\n\t" \
  "s_lshl_b32 s41, s41, 7\n\t" \
  "ds_read_b64_tr_b16 a[166:167], v185 offset:25600\n\t" \
  "v_accvgpr_read_b32 v48, a68\n\t" \
  "v_mfma_f32_16x16x32_bf16 a[80:83], a[148:151], a[0:3], a[80:83]\n\t" \
  "s_and_b32 s12, s2, 3\n\t" \
  "ds_read_b64_tr_b16 a[168:169], v186 offset:24576\n\t" \
  "v_accvgpr_read_b32 v49, a69\n\t" \
  "v_mfma_f32_16x16x32_bf16 a[96:99], a[152:155], a[0:3], a[96:99]\n\t" \
  "buffer_load_dwordx4 a[232:235], v250, s[16:19], s97 offen sc1\n\t" \
  "v_mfma_f32_16x16x32_bf16 a[100:103], a[152:155], a[4:7], a[100:103]\n\t" \
  "s_bfe_u32 s13, s2, 0x10002\n\t" \
  "ds_read_b64_tr_b16 a[170:171], v186 offset:25600\n\t" \
  "v_accvgpr_read_b32 v50, a70\n\t" \
  "v_mfma_f32_16x16x32_bf16 a[104:107], a[152:155], a[8:11], a[104:107]\n\t" \
  "s_lshl1_add_u32 s12, s12, s13\n\t" \
  "ds_read_b64_tr_b16 a[172:173], v187 offset:24576\n\t" \
  "v_accvgpr_read_b32 v51, a71\n\t" \
  "v_mfma_f32_16x16x32_bf16 a[108:111], a[152:155], a[12:15], a[108:111]\n\t" \
  "s_bfe_u32 s59, s2, 0x20003\n\t" \
  "v_accvgpr_read_b32 v80, a72\n\t" \
  "v_accvgpr_read_b32 v81, a73\n\t" \
  "v_mfma_f32_16x16x32_bf16 a[124:127], a[156:159], a[12:15], a[124:127]\n\t" \
  "buffer_load_dwordx4 a[236:239], v251, s[16:19], s97 offen sc1\n\t" \
  "v_mfma_f32_16x16x32_bf16 a[120:123], a[156:159], a[8:11], a[120:123]\n\t" \
  "s_add_u32 s96, s78, 4718592\n\t" \
  "s_and_b32 s96, s96, 0x780000\n\t" \
  "s_add_u32 m0, s76, 98304\n\t" \
  "v_mfma_f32_16x16x32_bf16 a[116:119], a[156:159], a[4:7], a[116:119]\n\t" \
  "s_or_b32 s59, s59, 24\n\t" \
  "ds_read_b64_tr_b16 a[174:175], v187 offset:25600\n\t" \
  "v_accvgpr_read_b32 v82, a74\n\t" \
  "v_mfma_f32_16x16x32_bf16 a[112:115], a[156:159], a[0:3], a[112:115]\n\t" \
  "buffer_load_dwordx4 v200, s[20:23], s96 offen offset:0 lds\n\t" \
  "v_mfma_f32_16x16x32_bf16 v[0:3], a[128:131], a[0:3], v[0:3]\n\t" \
  "ds_read_b64_tr_b16 a[176:177], v188 offset:24576\n\t" \
  "v_accvgpr_read_b32 v83, a75\n\t" \
  "v_mfma_f32_16x16x32_bf16 v[32:35], a[128:131], a[4:7], v[32:35]\n\t" \
  "ds_read_b64_tr_b16 a[178:179], v188 offset:25600\n\t" \
  "v_accvgpr_read_b32 v112, a76\n\t" \
  "v_mfma_f32_16x16x32_bf16 v[64:67], a[128:131], a[8:11], v[64:67]\n\t" \
  "ds_read_b64_tr_b16 a[180:181], v189 offset:24576\n\t" \
  "v_accvgpr_read_b32 v113, a77\n\t" \
  "v_mfma_f32_16x16x32_bf16 v[96:99], a[128:131], a[12:15], v[96:99]\n\t" \
  "buffer_load_dwordx4 v201, s[20:23], s96 offen offset:1024 lds\n\t" \
  "v_mfma_f32_16x16x32_bf16 v[100:103], a[132:135], a[12:15], v[100:103]\n\t" \
  "ds_read_b64_tr_b16 a[182:183], v189 offset:25600\n\t" \
  "v_accvgpr_read_b32 v114, a78\n\t" \
  "v_mfma_f32_16x16x32_bf16 v[68:71], a[132:135], a[8:11], v[68:71]\n\t" \
  "ds_read_b64_tr_b16 a[184:185], v190 offset:24576\n\t" \
  "v_accvgpr_read_b32 v115, a79\n\t" \
  "v_mfma_f32_16x16x32_bf16 v[36:39], a[132:135], a[4:7], v[36:39]\n\t" \
  "ds_read_b64_tr_b16 a[186:187], v190 offset:25600\n\t" \
  "v_accvgpr_read_b32 v116, a92\n\t" \
  "v_mfma_f32_16x16x32_bf16 v[4:7], a[132:135], a[0:3], v[4:7]\n\t" \
  "buffer_load_dwordx4 v202, s[20:23], s96 offen offset:2048 lds\n\t" \
  "v_mfma_f32_16x16x32_bf16 v[8:11], a[136:139], a[0:3], v[8:11]\n\t" \
  "ds_read_b64_tr_b16 a[188:189], v191 offset:24576\n\t" \
  "v_accvgpr_read_b32 v117, a93\n\t" \
  "v_mfma_f32_16x16x32_bf16 v[40:43], a[136:139], a[4:7], v[40:43]\n\t" \
  "ds_read_b64_tr_b16 a[190:191], v191 offset:25600\n\t" \
  "v_accvgpr_read_b32 v118, a94\n\t" \
  "v_mfma_f32_16x16x32_bf16 v[72:75], a[136:139], a[8:11], v[72:75]\n\t" \
  "v_accvgpr_read_b32 v119, a95\n\t" \
  "v_accvgpr_read_b32 v84, a88\n\t" \
  "v_mfma_f32_16x16x32_bf16 v[104:107], a[136:139], a[12:15], v[104:107]\n\t" \
  "buffer_load_dwordx4 v203, s[20:23], s96 offen offset:3072 lds\n\t" \
  "v_mfma_f32_16x16x32_bf16 v[108:111], a[140:143], a[12:15], v[108:111]\n\t" \
  "v_accvgpr_read_b32 v85, a89\n\t" \
  "v_accvgpr_read_b32 v86, a90\n\t" \
  "v_mfma_f32_16x16x32_bf16 v[76:79], a[140:143], a[8:11], v[76:79]\n\t" \
  "s_add_u32 s97, s97, s83\n\t" \
  "v_accvgpr_read_b32 v87, a91\n\t" \
  "v_accvgpr_read_b32 v52, a84\n\t" \
  "v_mfma_f32_16x16x32_bf16 v[44:47], a[140:143], a[4:7], v[44:47]\n\t" \
  "s_waitcnt vmcnt(24) lgkmcnt(0)\n\t" \
  "v_accvgpr_read_b32 v53, a85\n\t" \
  "v_accvgpr_read_b32 v54, a86\n\t" \
  "v_mfma_f32_16x16x32_bf16 v[12:15], a[140:143], a[0:3], v[12:15]\n\t" \
  "s_barrier\n\t" \
  "v_mfma_f32_16x16x32_bf16 a[96:99], a[184:187], a[16:19], a[96:99]\n\t" \
  "buffer_load_dwordx4 a[240:243], v248, s[16:19], s97 offen sc1\n\t" \
  "v_mfma_f32_16x16x32_bf16 a[100:103], a[184:187], a[20:23], a[100:103]\n\t" \
  "ds_read_b64_tr_b16 a[128:129], v184 offset:32768\n\t" \
  "v_accvgpr_read_b32 v55, a87\n\t" \
  "v_mfma_f32_16x16x32_bf16 a[104:107], a[184:187], a[24:27], a[104:107]\n\t" \
  "ds_read_b64_tr_b16 a[130:131], v184 offset:33792\n\t" \
  "v_accvgpr_read_b32 v20, a80\n\t" \
  "v_mfma_f32_16x16x32_bf16 a[108:111], a[184:187], a[28:31], a[108:111]\n\t" \
  "v_accvgpr_read_b32 v21, a81\n\t" \
  "v_accvgpr_read_b32 v22, a82\n\t" \
  "v_mfma_f32_16x16x32_bf16 a[124:127], a[188:191], a[28:31], a[124:127]\n\t" \
  "buffer_load_dwordx4 a[244:247], v249, s[16:19], s97 offen sc1\n\t" \
  "v_mfma_f32_16x16x32_bf16 a[120:123], a[188:191], a[24:27], a[120:123]\n\t" \
  "ds_read_b64_tr_b16 a[132:133], v185 offset:32768\n\t" \
  "v_accvgpr_read_b32 v23, a83\n\t" \
  "v_mfma_f32_16x16x32_bf16 a[116:119], a[188:191], a[20:23], a[116:119]\n\t" \
  "ds_read_b64_tr_b16 a[134:135], v185 offset:33792\n\t" \
  "v_accvgpr_read_b32 v24, a96\n\t" \
  "v_mfma_f32_16x16x32_bf16 a[112:115], a[188:191], a[16:19], a[112:115]\n\t" \
  "ds_read_b64_tr_b16 a[136:137], v186 offset:32768\n\t" \
  "v_accvgpr_read_b32 v25, a97\n\t" \
  "v_mfma_f32_16x16x32_bf16 v[0:3], a[160:163], a[16:19], v[0:3]\n\t" \
  "buffer_load_dwordx4 a[248:251], v250, s[16:19], s97 offen sc1\n\t" \
  "v_mfma_f32_16x16x32_bf16 v[32:35], a[160:163], a[20:23], v[32:35]\n\t" \
  "ds_read_b64_tr_b16 a[138:139], v186 offset:33792\n\t" \
  "v_accvgpr_read_b32 v26, a98\n\t" \
  "v_mfma_f32_16x16x32_bf16 v[64:67], a[160:163], a[24:27], v[64:67]\n\t" \
  "ds_read_b64_tr_b16 a[140:141], v187 offset:32768\n\t" \
  "v_accvgpr_read_b32 v27, a99\n\t" \
  "v_mfma_f32_16x16x32_bf16 v[96:99], a[160:163], a[28:31], v[96:99]\n\t" \
  "v_accvgpr_read_b32 v56, a100\n\t" \
  "v_accvgpr_read_b32 v57, a101\n\t" \
  "v_mfma_f32_16x16x32_bf16 v[100:103], a[164:167], a[28:31], v[100:103]\n\t" \
  "buffer_load_dwordx4 a[252:255], v251, s[16:19], s97 offen sc1\n\t" \
  "v_mfma_f32_16x16x32_bf16 v[68:71], a[164:167], a[24:27], v[68:71]\n\t" \
  "s_add_u32 s97, s78, 4194304\n\t" \
  "s_bfe_u32 s97, s97, 0x140003\n\t" \
  "s_add_u32 s97, s97, s82\n\t" \
  "v_mfma_f32_16x16x32_bf16 v[36:39], a[164:167], a[20:23], v[36:39]\n\t" \
  "ds_read_b64_tr_b16 a[142:143], v187 offset:33792\n\t" \
  "v_accvgpr_read_b32 v58, a102\n\t" \
  "v_mfma_f32_16x16x32_bf16 v[4:7], a[164:167], a[16:19], v[4:7]\n\t" \
  "buffer_load_dwordx4 a[0:3], v248, s[16:19], s97 offen sc1\n\t" \
  "v_mfma_f32_16x16x32_bf16 v[8:11], a[168:171], a[16:19], v[8:11]\n\t" \
  "ds_read_b64_tr_b16 a[144:145], v188 offset:32768\n\t" \
  "v_accvgpr_read_b32 v59, a103\n\t" \
  "v_mfma_f32_16x16x32_bf16 v[40:43], a[168:171], a[20:23], v[40:43]\n\t" \
  "ds_read_b64_tr_b16 a[146:147], v188 offset:33792\n\t" \
  "v_accvgpr_read_b32 v88, a104\n\t" \
  "v_mfma_f32_16x16x32_bf16 v[72:75], a[168:171], a[24:27], v[72:75]\n\t" \
  "ds_read_b64_tr_b16 a[148:149], v189 offset:32768\n\t" \
  "v_accvgpr_read_b32 v89, a105\n\t" \
  "v_mfma_f32_16x16x32_bf16 v[104:107], a[168:171], a[28:31], v[104:107]\n\t" \
  "buffer_load_dwordx4 a[4:7], v249, s[16:19], s97 offen sc1\n\t" \
  "v_mfma_f32_16x16x32_bf16 v[108:111], a[172:175], a[28:31], v[108:111]\n\t" \
  "ds_read_b64_tr_b16 a[150:151], v189 offset:33792\n\t" \
  "v_accvgpr_read_b32 v90, a106\n\t" \
  "v_mfma_f32_16x16x32_bf16 v[76:79], a[172:175], a[24:27], v[76:79]\n\t" \
  "ds_read_b64_tr_b16 a[152:153], v190 offset:32768\n\t" \
  "v_accvgpr_read_b32 v91, a107\n\t" \
  "v_mfma_f32_16x16x32_bf16 v[44:47], a[172:175], a[20:23], v[44:47]\n\t" \
  "ds_read_b64_tr_b16 a[154:155], v190 offset:33792\n\t" \
  "v_accvgpr_read_b32 v120, a108\n\t" \
  "v_mfma_f32_16x16x32_bf16 v[12:15], a[172:175], a[16:19], v[12:15]\n\t" \
  "buffer_load_dwordx4 a[8:11], v250, s[16:19], s97 offen sc1\n\t" \
  "v_mfma_f32_16x16x32_bf16 v[16:19], a[176:179], a[16:19], v[16:19]\n\t" \
  "ds_read_b64_tr_b16 a[156:157], v191 offset:32768\n\t" \
  "v_accvgpr_read_b32 v121, a109\n\t" \
  "v_mfma_f32_16x16x32_bf16 v[48:51], a[176:179], a[20:23], v[48:51]\n\t" \
  "ds_read_b64_tr_b16 a[158:159], v191 offset:33792\n\t" \
  "v_accvgpr_read_b32 v122, a110\n\t" \
  "v_mfma_f32_16x16x32_bf16 v[80:83], a[176:179], a[24:27], v[80:83]\n\t" \
  "v_accvgpr_read_b32 v123, a111\n\t" \
  "v_accvgpr_read_b32 v124, a124\n\t" \
  "v_mfma_f32_16x16x32_bf16 v[112:115], a[176:179], a[28:31], v[112:115]\n\t" \
  "buffer_load_dwordx4 a[12:15], v251, s[16:19], s97 offen sc1\n\t" \
  "v_mfma_f32_16x16x32_bf16 v[116:119], a[180:183], a[28:31], v[116:119]\n\t" \
  "v_accvgpr_read_b32 v125, a125\n\t" \
  "v_accvgpr_read_b32 v126, a126\n\t" \
  "v_mfma_f32_16x16x32_bf16 v[84:87], a[180:183], a[24:27], v[84:87]\n\t" \
  "s_add_u32 s97, s97, s83\n\t" \
  "v_accvgpr_read_b32 v127, a127\n\t" \
  "v_accvgpr_read_b32 v92, a120\n\t" \
  "v_mfma_f32_16x16x32_bf16 v[52:55], a[180:183], a[20:23], v[52:55]\n\t" \
  "v_accvgpr_read_b32 v93, a121\n\t" \
  "v_accvgpr_read_b32 v94, a122\n\t" \
  "v_mfma_f32_16x16x32_bf16 v[20:23], a[180:183], a[16:19], v[20:23]\n\t" \
  "s_waitcnt vmcnt(28) lgkmcnt(0)\n\t" \
  "v_accvgpr_read_b32 v95, a123\n\t" \
  "v_accvgpr_read_b32 v60, a116\n\t" \
  "v_mfma_f32_16x16x32_bf16 v[0:3], a[128:131], a[32:35], v[0:3]\n\t" \
  "buffer_load_dwordx4 a[16:19], v248, s[16:19], s97 offen sc1\n\t" \
  "v_mfma_f32_16x16x32_bf16 v[32:35], a[128:131], a[36:39], v[32:35]\n\t" \
  "ds_read_b64_tr_b16 a[160:161], v184 offset:40960\n\t" \
  "v_accvgpr_read_b32 v61, a117\n\t" \
  "v_mfma_f32_16x16x32_bf16 v[64:67], a[128:131], a[40:43], v[64:67]\n\t" \
  "ds_read_b64_tr_b16 a[162:163], v184 offset:41984\n\t" \
  "v_accvgpr_read_b32 v62, a118\n\t" \
  "v_mfma_f32_16x16x32_bf16 v[96:99], a[128:131], a[44:47], v[96:99]\n\t" \
  "v_accvgpr_read_b32 v63, a119\n\t" \
  "v_accvgpr_read_b32 v28, a112\n\t" \
  "v_mfma_f32_16x16x32_bf16 v[100:103], a[132:135], a[44:47], v[100:103]\n\t" \
  "buffer_load_dwordx4 a[20:23], v249, s[16:19], s97 offen sc1\n\t" \
  "v_mfma_f32_16x16x32_bf16 v[68:71], a[132:135], a[40:43], v[68:71]\n\t" \
  "ds_read_b64_tr_b16 a[164:165], v185 offset:40960\n\t" \
  "v_accvgpr_read_b32 v29, a113\n\t" \
  "v_mfma_f32_16x16x32_bf16 v[36:39], a[132:135], a[36:39], v[36:39]\n\t" \
  "ds_read_b64_tr_b16 a[166:167], v185 offset:41984\n\t" \
  "v_accvgpr_read_b32 v30, a114\n\t" \
  "v_mfma_f32_16x16x32_bf16 v[4:7], a[132:135], a[32:35], v[4:7]\n\t" \
  "ds_read_b64_tr_b16 a[168:169], v186 offset:40960\n\t" \
  "v_accvgpr_read_b32 v31, a115\n\t" \
  "v_mfma_f32_16x16x32_bf16 v[8:11], a[136:139], a[32:35], v[8:11]\n\t" \
  "buffer_load_dwordx4 a[24:27], v250, s[16:19], s97 offen sc1\n\t" \
  "v_mfma_f32_16x16x32_bf16 v[40:43], a[136:139], a[36:39], v[40:43]\n\t" \
  "ds_read_b64_tr_b16 a[170:171], v186 offset:41984\n\t" \
  "v_mfma_f32_16x16x32_bf16 v[72:75], a[136:139], a[40:43], v[72:75]\n\t" \
  "ds_read_b64_tr_b16 a[172:173], v187 offset:40960\n\t" \
  "v_mfma_f32_16x16x32_bf16 v[104:107], a[136:139], a[44:47], v[104:107]\n\t" \
  "s_cmp_lt_u32 s98, 24\n\t" \
  "s_cselect_b32 s59, s98, s59\n\t" \
  "s_mul_i32 s53, s98, 57344\n\t" \
  "v_mfma_f32_16x16x32_bf16 v[108:111], a[140:143], a[44:47], v[108:111]\n\t" \
  "buffer_load_dwordx4 a[28:31], v251, s[16:19], s97 offen sc1\n\t" \
  "v_mfma_f32_16x16x32_bf16 v[76:79], a[140:143], a[40:43], v[76:79]\n\t" \
  "s_add_u32 s96, s78, 5242880\n\t" \
  "s_and_b32 s96, s96, 0x780000\n\t" \
  "s_add_u32 m0, s76, 0\n\t" \
  "v_mfma_f32_16x16x32_bf16 v[44:47], a[140:143], a[36:39], v[44:47]\n\t" \
  "s_lshl_b32 s14, s59, 5\n\t" \
  "ds_read_b64_tr_b16 a[174:175], v187 offset:41984\n\t" \
  "s_sub_u32 s53, s53, s14\n\t" \
  "v_mfma_f32_16x16x32_bf16 v[12:15], a[140:143], a[32:35], v[12:15]\n\t" \
  "buffer_load_dwordx4 v200, s[20:23], s96 offen offset:0 lds\n\t" \
  "v_mfma_f32_16x16x32_bf16 v[16:19], a[144:147], a[32:35], v[16:19]\n\t" \
  "s_lshl_b32 s52, s98, 16\n\t" \
  "ds_read_b64_tr_b16 a[176:177], v188 offset:40960\n\t" \
  "s_lshl_b32 s14, s3, 14\n\t" \
  "v_mfma_f32_16x16x32_bf16 v[48:51], a[144:147], a[36:39], v[48:51]\n\t" \
  "s_add_u32 s52, s52, s14\n\t" \
  "ds_read_b64_tr_b16 a[178:179], v188 offset:41984\n\t" \
  "s_lshl_b32 s14, s13, 13\n\t" \
  "v_mfma_f32_16x16x32_bf16 v[80:83], a[144:147], a[40:43], v[80:83]\n\t" \
  "s_add_u32 s52, s52, s14\n\t" \
  "ds_read_b64_tr_b16 a[180:181], v189 offset:40960\n\t" \
  "s_add_u32 s52, s52, s78\n\t" \
  "v_mfma_f32_16x16x32_bf16 v[112:115], a[144:147], a[44:47], v[112:115]\n\t" \
  "buffer_load_dwordx4 v201, s[20:23], s96 offen offset:1024 lds\n\t" \
  "v_mfma_f32_16x16x32_bf16 v[116:119], a[148:151], a[44:47], v[116:119]\n\t" \
  "s_lshl_b32 s51, s3, 15\n\t" \
  "ds_read_b64_tr_b16 a[182:183], v189 offset:41984\n\t" \
  "s_add_u32 s51, s51, s78\n\t" \
  "v_mfma_f32_16x16x32_bf16 v[84:87], a[148:151], a[40:43], v[84:87]\n\t" \
  "s_lshl_b32 s14, s39, 8\n\t" \
  "ds_read_b64_tr_b16 a[184:185], v190 offset:40960\n\t" \
  "s_add_u32 s51, s51, s14\n\t" \
  "v_mfma_f32_16x16x32_bf16 v[52:55], a[148:151], a[36:39], v[52:55]\n\t" \
  "s_lshr_b32 s98, s98, 2\n\t" \
  "ds_read_b64_tr_b16 a[186:187], v190 offset:41984\n\t" \
  "s_add_u32 s13, s13, 6\n\t" \
  "v_mfma_f32_16x16x32_bf16 v[20:23], a[148:151], a[32:35], v[20:23]\n\t" \
  "buffer_load_dwordx4 v202, s[20:23], s96 offen offset:2048 lds\n\t" \
  "v_mfma_f32_16x16x32_bf16 v[24:27], a[152:155], a[32:35], v[24:27]\n\t" \
  "ds_read_b64_tr_b16 a[188:189], v191 offset:40960\n\t" \
  "v_mfma_f32_16x16x32_bf16 v[56:59], a[152:155], a[36:39], v[56:59]\n\t" \
  "ds_read_b64_tr_b16 a[190:191], v191 offset:41984\n\t" \
  "v_mfma_f32_16x16x32_bf16 v[88:91], a[152:155], a[40:43], v[88:91]\n\t" \
  "s_cmp_eq_u32 s98, s13\n\t" \
  "s_cselect_b32 s57, 0x80000000, 0\n\t" \
  "s_cselect_b32 s30, 0, 0x8000\n\t" \
  "v_mfma_f32_16x16x32_bf16 v[120:123], a[152:155], a[44:47], v[120:123]\n\t" \
  "buffer_load_dwordx4 v203, s[20:23], s96 offen offset:3072 lds\n\t" \
  "v_mfma_f32_16x16x32_bf16 v[124:127], a[156:159], a[44:47], v[124:127]\n\t" \
  "s_mul_i32 s14, s12, 28\n\t" \
  "s_add_u32 s59, s59, s14\n\t" \
  "s_lshl_b32 s14, s59, 15\n\t" \
  "v_mfma_f32_16x16x32_bf16 v[92:95], a[156:159], a[40:43], v[92:95]\n\t" \
  "s_add_u32 s28, s28, s14\n\t" \
  "s_addc_u32 s29, s29, 0\n\t" \
  "s_mul_i32 s14, s3, 1835008\n\t" \
  "v_mfma_f32_16x16x32_bf16 v[60:63], a[156:159], a[36:39], v[60:63]\n\t" \
  "s_waitcnt vmcnt(32) lgkmcnt(0)\n\t" \
  "s_mov_b32 s99, 0\n\t" \
  "s_lshl_b32 s59, s59, 5\n\t" \
  "v_mfma_f32_16x16x32_bf16 v[28:31], a[156:159], a[32:35], v[28:31]\n\t" \
  "s_barrier\n\t" \
  "v_mfma_f32_16x16x32_bf16 v[0:3], a[160:163], a[48:51], v[0:3]\n\t" \
  "buffer_load_dwordx4 v[136:139], v209, s[8:11], s99 offen\n\t" \
  "v_mfma_f32_16x16x32_bf16 v[32:35], a[160:163], a[52:55], v[32:35]\n\t" \
  "s_mov_b32 s99, 12288\n\t" \
  "ds_read_b64_tr_b16 a[128:129], v184 offset:49152\n\t" \
  "s_add_u32 s14, s14, s59\n\t" \
  "v_mfma_f32_16x16x32_bf16 v[64:67], a[160:163], a[56:59], v[64:67]\n\t" \
  "ds_read_b64_tr_b16 a[130:131], v184 offset:50176\n\t" \
  "v_mfma_f32_16x16x32_bf16 v[96:99], a[160:163], a[60:63], v[96:99]\n\t" \
  "s_add_u32 s68, s68, s14\n\t" \
  "s_addc_u32 s69, s69, 0\n\t" \
  "v_mfma_f32_16x16x32_bf16 v[100:103], a[164:167], a[60:63], v[100:103]\n\t" \
  "ds_read_b64_tr_b16 a[132:133], v185 offset:49152\n\t" \
  "v_mfma_f32_16x16x32_bf16 v[68:71], a[164:167], a[56:59], v[68:71]\n\t" \
  "ds_read_b64_tr_b16 a[134:135], v185 offset:50176\n\t" \
  "v_mfma_f32_16x16x32_bf16 v[36:39], a[164:167], a[52:55], v[36:39]\n\t" \
  "s_add_u32 s72, s72, s14\n\t" \
  "s_addc_u32 s73, s73, 0\n\t" \
  "s_sub_u32 s70, s70, s14\n\t" \
  "v_mfma_f32_16x16x32_bf16 v[4:7], a[164:167], a[48:51], v[4:7]\n\t" \
  "s_sub_u32 s74, s74, s14\n\t" \
  "ds_read_b64_tr_b16 a[136:137], v186 offset:49152\n\t" \
  "s_lshr_b32 s14, s78, 2\n\t" \
  "v_mfma_f32_16x16x32_bf16 v[8:11], a[168:171], a[48:51], v[8:11]\n\t" \
  "buffer_load_dwordx4 v[140:143], v209, s[8:11], s99 offen\n\t" \
  "v_mfma_f32_16x16x32_bf16 v[40:43], a[168:171], a[52:55], v[40:43]\n\t" \
  "ds_read_b64_tr_b16 a[138:139], v186 offset:50176\n\t" \
  "s_mov_b32 s99, 24576\n\t" \
  "v_mfma_f32_16x16x32_bf16 v[72:75], a[168:171], a[56:59], v[72:75]\n\t" \
  "ds_read_b64_tr_b16 a[140:141], v187 offset:49152\n\t" \
  "v_mfma_f32_16x16x32_bf16 v[104:107], a[168:171], a[60:63], v[104:107]\n\t" \
  "s_add_u32 s60, s60, s14\n\t" \
  "s_addc_u32 s61, s61, 0\n\t" \
  "v_mfma_f32_16x16x32_bf16 v[108:111], a[172:175], a[60:63], v[108:111]\n\t" \
  "ds_read_b64_tr_b16 a[142:143], v187 offset:50176\n\t" \
  "v_mfma_f32_16x16x32_bf16 v[76:79], a[172:175], a[56:59], v[76:79]\n\t" \
  "ds_read_b64_tr_b16 a[144:145], v188 offset:49152\n\t" \
  "v_mfma_f32_16x16x32_bf16 v[44:47], a[172:175], a[52:55], v[44:47]\n\t" \
  "s_add_u32 s64, s64, s14\n\t" \
  "s_addc_u32 s65, s65, 0\n\t" \
  "s_lshl2_add_u32 s58, s2, s3\n\t" \
  "v_mfma_f32_16x16x32_bf16 v[12:15], a[172:175], a[48:51], v[12:15]\n\t" \
  "buffer_load_dwordx4 v[144:147], v209, s[8:11], s99 offen\n\t" \
  "v_mfma_f32_16x16x32_bf16 v[16:19], a[176:179], a[48:51], v[16:19]\n\t" \
  "s_lshl_b32 s58, s58, 11\n\t" \
  "ds_read_b64_tr_b16 a[146:147], v188 offset:50176\n\t" \
  "s_bfe_u32 s13, s2, 0x10004\n\t" \
  "v_mfma_f32_16x16x32_bf16 v[48:51], a[176:179], a[52:55], v[48:51]\n\t" \
  "s_lshl1_add_u32 s12, s12, s13\n\t" \
  "ds_read_b64_tr_b16 a[148:149], v189 offset:49152\n\t" \
  "s_lshl_b32 s13, s13, 13\n\t" \
  "v_mfma_f32_16x16x32_bf16 v[80:83], a[176:179], a[56:59], v[80:83]\n\t" \
  "s_lshl_b32 s14, s12, 7\n\t" \
  "ds_read_b64_tr_b16 a[150:151], v189 offset:50176\n\t" \
  "s_add_u32 s13, s13, s14\n\t" \
  "v_mfma_f32_16x16x32_bf16 v[112:115], a[176:179], a[60:63], v[112:115]\n\t" \
  "s_add_u32 s13, s13, 16384\n\t" \
  "ds_read_b64_tr_b16 a[152:153], v190 offset:49152\n\t" \
  "s_lshl_b32 s12, s12, 21\n\t" \
  "v_mfma_f32_16x16x32_bf16 v[116:119], a[180:183], a[60:63], v[116:119]\n\t" \
  "s_add_u32 s12, s12, 8388608\n\t" \
  "s_bitcmp1_b32 s2, 3\n\t" \
  "s_cselect_b32 s91, s12, s13\n\t" \
  "v_mfma_f32_16x16x32_bf16 v[84:87], a[180:183], a[56:59], v[84:87]\n\t" \
  "s_mov_b32 s99, 36864\n\t" \
  "ds_read_b64_tr_b16 a[154:155], v190 offset:50176\n\t" \
  "s_lshr_b32 s12, s57, s3\n\t" \
  "v_mfma_f32_16x16x32_bf16 v[52:55], a[180:183], a[52:55], v[52:55]\n\t" \
  "s_cmp_lt_i32 s12, 0\n\t" \
  "s_cselect_b32 s91, s91, 0x80000000\n\t" \
  "s_lshl_b32 s90, s3, 7\n\t" \
  "v_mfma_f32_16x16x32_bf16 v[20:23], a[180:183], a[48:51], v[20:23]\n\t" \
  "buffer_load_dwordx4 v[148:151], v209, s[8:11], s99 offen\n\t" \
  "v_mfma_f32_16x16x32_bf16 v[24:27], a[184:187], a[48:51], v[24:27]\n\t" \
  "s_add_u32 s90, s90, 0x220000\n\t" \
  "ds_read_b64_tr_b16 a[156:157], v191 offset:49152\n\t" \
  "v_mbcnt_lo_u32_b32 v217, -1, 0\n\t" \
  "v_mfma_f32_16x16x32_bf16 v[56:59], a[184:187], a[52:55], v[56:59]\n\t" \
  "s_and_b32 s98, s2, 0xc0\n\t" \
  "ds_read_b64_tr_b16 a[158:159], v191 offset:50176\n\t" \
  "v_mbcnt_hi_u32_b32 v217, -1, v217\n\t" \
  "v_mfma_f32_16x16x32_bf16 v[88:91], a[184:187], a[56:59], v[88:91]\n\t" \
  "s_lshl_b32 s98, s98, 9\n\t" \
  "v_cmp_gt_u32 vcc, 16, v217\n\t" \
  "v_lshlrev_b32 v217, 7, v217\n\t" \
  "v_mfma_f32_16x16x32_bf16 v[120:123], a[184:187], a[60:63], v[120:123]\n\t" \
  "s_add_u32 s90, s90, s98\n\t" \
  "v_bfrev_b32 v215, 1\n\t" \
  "v_cndmask_b32 v217, v215, v217, vcc\n\t" \
  "v_mfma_f32_16x16x32_bf16 v[124:127], a[188:191], a[60:63], v[124:127]\n\t" \
  "s_and_b32 s98, s2, 0x3\n\t" \
  "v_bfe_u32 v215, v217, 10, 1\n\t" \
  "s_mov_b32 s99, 11264\n\t" \
  "v_mfma_f32_16x16x32_bf16 v[92:95], a[188:191], a[56:59], v[92:95]\n\t" \
  "s_lshl_b32 s98, s98, 13\n\t" \
  "v_mad_u32_u24 v215, v215, s99, v217\n\t" \
  "s_add_u32 s90, s90, s98\n\t" \
  "v_mfma_f32_16x16x32_bf16 v[60:63], a[188:191], a[52:55], v[60:63]\n\t" \
  "s_and_b32 s98, s2, 0x4\n\t" \
  "s_lshl_b32 s98, s98, 10\n\t" \
  "s_add_u32 s90, s90, s98\n\t" \
  "v_mfma_f32_16x16x32_bf16 v[28:31], a[188:191], a[48:51], v[28:31]\n\t" \
  "s_waitcnt vmcnt(32) lgkmcnt(0)\n\t" \
  "s_and_b32 s98, s2, 0x38\n\t" \
  "s_lshl_b32 s98, s98, 6\n\t" \
  "v_mfma_f32_16x16x32_bf16 v[0:3], a[128:131], a[192:195], v[0:3]\n\t" \
  "s_add_u32 s90, s90, s98\n\t" \
  "ds_read_b64_tr_b16 a[160:161], v184 offset:57344\n\t" \
  "s_lshr_b32 s98, s2, 2\n\t" \
  "v_mfma_f32_16x16x32_bf16 v[32:35], a[128:131], a[196:199], v[32:35]\n\t" \
  "s_xor_b32 s98, s98, s3\n\t" \
  "ds_read_b64_tr_b16 a[162:163], v184 offset:58368\n\t" \
  "s_lshl_b32 s98, s98, 13\n\t" \
  "v_mfma_f32_16x16x32_bf16 v[64:67], a[128:131], a[200:203], v[64:67]\n\t" \
  "s_and_b32 s42, s98, 0x2000\n\t" \
  "ds_read_b64_tr_b16 a[164:165], v185 offset:57344\n\t" \
  "s_lshr_b32 s98, s2, 6\n\t" \
  "v_mfma_f32_16x16x32_bf16 v[96:99], a[128:131], a[204:207], v[96:99]\n\t" \
  "s_lshl_b32 s98, s98, 22\n\t" \
  "ds_read_b64_tr_b16 a[166:167], v185 offset:58368\n\t" \
  "s_add_u32 s42, s42, s98\n\t" \
  "v_mfma_f32_16x16x32_bf16 v[100:103], a[132:135], a[204:207], v[100:103]\n\t" \
  "s_lshl_b32 s47, s3, 15\n\t" \
  "ds_read_b64_tr_b16 a[168:169], v186 offset:57344\n\t" \
  "s_add_u32 s47, s47, 0x1800000\n\t" \
  "v_mfma_f32_16x16x32_bf16 v[68:71], a[132:135], a[200:203], v[68:71]\n\t" \
  "s_add_u32 s47, s47, s42\n\t" \
  "ds_read_b64_tr_b16 a[170:171], v186 offset:58368\n\t" \
  "s_and_b32 s98, s2, 0x38\n\t" \
  "v_mfma_f32_16x16x32_bf16 v[36:39], a[132:135], a[196:199], v[36:39]\n\t" \
  "s_lshl_b32 s98, s98, 16\n\t" \
  "ds_read_b64_tr_b16 a[172:173], v187 offset:57344\n\t" \
  "s_add_u32 s47, s47, s98\n\t" \
  "v_mfma_f32_16x16x32_bf16 v[4:7], a[132:135], a[192:195], v[4:7]\n\t" \
  "s_add_u32 s46, s42, 0x800000\n\t" \
  "ds_read_b64_tr_b16 a[174:175], v187 offset:58368\n\t" \
  "s_and_b32 s98, s2, 0x3\n\t" \
  "v_mfma_f32_16x16x32_bf16 v[8:11], a[136:139], a[192:195], v[8:11]\n\t" \
  "s_lshl_b32 s98, s98, 20\n\t" \
  "ds_read_b64_tr_b16 a[176:177], v188 offset:57344\n\t" \
  "s_add_u32 s46, s46, s98\n\t" \
  "v_mfma_f32_16x16x32_bf16 v[40:43], a[136:139], a[196:199], v[40:43]\n\t" \
  "s_and_b32 s98, s3, 2\n\t" \
  "ds_read_b64_tr_b16 a[178:179], v188 offset:58368\n\t" \
  "s_lshl_b32 s98, s98, 13\n\t" \
  "v_mfma_f32_16x16x32_bf16 v[72:75], a[136:139], a[200:203], v[72:75]\n\t" \
  "s_add_u32 s46, s46, s98\n\t" \
  "ds_read_b64_tr_b16 a[180:181], v189 offset:57344\n\t" \
  "s_and_b32 s98, s3, 1\n\t" \
  "v_mfma_f32_16x16x32_bf16 v[104:107], a[136:139], a[204:207], v[104:107]\n\t" \
  "s_lshl_b32 s98, s98, 12\n\t" \
  "ds_read_b64_tr_b16 a[182:183], v189 offset:58368\n\t" \
  "s_add_u32 s46, s46, s98\n\t" \
  "v_mfma_f32_16x16x32_bf16 v[108:111], a[140:143], a[204:207], v[108:111]\n\t" \
  "s_lshr_b32 s45, s87, 2\n\t" \
  "ds_read_b64_tr_b16 a[184:185], v190 offset:57344\n\t" \
  "s_add_u32 s45, s45, s46\n\t" \
  "v_mfma_f32_16x16x32_bf16 v[76:79], a[140:143], a[200:203], v[76:79]\n\t" \
  "s_and_b32 s98, s2, 0x38\n\t" \
  "ds_read_b64_tr_b16 a[186:187], v190 offset:58368\n\t" \
  "s_lshl_b32 s98, s98, 14\n\t" \
  "v_mfma_f32_16x16x32_bf16 v[44:47], a[140:143], a[196:199], v[44:47]\n\t" \
  "s_add_u32 s45, s45, s98\n\t" \
  "ds_read_b64_tr_b16 a[188:189], v191 offset:57344\n\t" \
  "s_lshr_b32 s98, s2, 1\n\t" \
  "v_mfma_f32_16x16x32_bf16 v[12:15], a[140:143], a[192:195], v[12:15]\n\t" \
  "s_xor_b32 s98, s98, s3\n\t" \
  "ds_read_b64_tr_b16 a[190:191], v191 offset:58368\n\t" \
  "s_lshl_b32 s98, s98, 13\n\t" \
  "v_mfma_f32_16x16x32_bf16 v[16:19], a[144:147], a[192:195], v[16:19]\n\t" \
  "s_and_b32 s42, s98, 0x2000\n\t" \
  "s_lshr_b32 s98, s2, 5\n\t" \
  "s_xor_b32 s98, s98, s3\n\t" \
  "v_mfma_f32_16x16x32_bf16 v[48:51], a[144:147], a[196:199], v[48:51]\n\t" \
  "s_lshl_b32 s98, s98, 13\n\t" \
  "s_and_b32 s43, s98, 0x2000\n\t" \
  "s_lshl_b32 s48, s3, 2\n\t" \
  "v_mfma_f32_16x16x32_bf16 v[80:83], a[144:147], a[200:203], v[80:83]\n\t" \
  "s_add_u32 s48, s48, 0x8000\n\t" \
  "s_and_b32 s98, s2, 0x80\n\t" \
  "s_lshl_b32 s98, s98, 7\n\t" \
  "v_mfma_f32_16x16x32_bf16 v[112:115], a[144:147], a[204:207], v[112:115]\n\t" \
  "s_add_u32 s48, s48, s98\n\t" \
  "s_and_b32 s98, s2, 0x4\n\t" \
  "s_lshl_b32 s98, s98, 11\n\t" \
  "v_mfma_f32_16x16x32_bf16 v[116:119], a[148:151], a[204:207], v[116:119]\n\t" \
  "s_add_u32 s48, s48, s98\n\t" \
  "s_and_b32 s98, s2, 0x78\n\t" \
  "s_lshl_b32 s98, s98, 6\n\t" \
  "v_mfma_f32_16x16x32_bf16 v[84:87], a[148:151], a[200:203], v[84:87]\n\t" \
  "s_add_u32 s48, s48, s98\n\t" \
  "s_lshl_b32 s49, s3, 7\n\t" \
  "s_add_u32 s49, s49, 0x20000\n\t" \
  "v_mfma_f32_16x16x32_bf16 v[52:55], a[148:151], a[196:199], v[52:55]\n\t" \
  "s_and_b32 s98, s2, 0xc0\n\t" \
  "s_lshl_b32 s98, s98, 9\n\t" \
  "s_add_u32 s49, s49, s98\n\t" \
  "v_mfma_f32_16x16x32_bf16 v[20:23], a[148:151], a[192:195], v[20:23]\n\t" \
  "s_and_b32 s98, s2, 0x2\n\t" \
  "s_lshl_b32 s98, s98, 13\n\t" \
  "s_add_u32 s49, s49, s98\n\t" \
  "v_mfma_f32_16x16x32_bf16 v[24:27], a[152:155], a[192:195], v[24:27]\n\t" \
  "s_and_b32 s98, s2, 0x4\n\t" \
  "s_lshl_b32 s98, s98, 11\n\t" \
  "s_add_u32 s49, s49, s98\n\t" \
  "v_mfma_f32_16x16x32_bf16 v[56:59], a[152:155], a[196:199], v[56:59]\n\t" \
  "s_and_b32 s98, s2, 0x1\n\t" \
  "s_lshl_b32 s98, s98, 12\n\t" \
  "s_add_u32 s49, s49, s98\n\t" \
  "v_mfma_f32_16x16x32_bf16 v[88:91], a[152:155], a[200:203], v[88:91]\n\t" \
  "s_lshl2_add_u32 s49, s39, s49\n\t" \
  "s_lshl_b32 s50, s3, 15\n\t" \
  "s_and_b32 s98, s2, 0x4\n\t" \
  "v_mfma_f32_16x16x32_bf16 v[120:123], a[152:155], a[204:207], v[120:123]\n\t" \
  "s_lshl_b32 s98, s98, 19\n\t" \
  "s_add_u32 s50, s50, s98\n\t" \
  "s_and_b32 s98, s2, 0xc0\n\t" \
  "v_mfma_f32_16x16x32_bf16 v[124:127], a[156:159], a[204:207], v[124:127]\n\t" \
  "s_lshl_b32 s98, s98, 13\n\t" \
  "s_add_u32 s50, s50, s98\n\t" \
  "s_and_b32 s98, s2, 0x3\n\t" \
  "v_mfma_f32_16x16x32_bf16 v[92:95], a[156:159], a[200:203], v[92:95]\n\t" \
  "s_lshl_b32 s98, s98, 17\n\t" \
  "s_add_u32 s50, s50, s98\n\t" \
  "s_and_b32 s98, s2, 0x38\n\t" \
  "v_mfma_f32_16x16x32_bf16 v[60:63], a[156:159], a[196:199], v[60:63]\n\t" \
  "s_waitcnt vmcnt(28) lgkmcnt(0)\n\t" \
  "s_add_u32 s97, s78, 4718592\n\t" \
  "s_bfe_u32 s97, s97, 0x140003\n\t" \
  "v_mfma_f32_16x16x32_bf16 v[28:31], a[156:159], a[192:195], v[28:31]\n\t" \
  "s_barrier\n\t" \
  "v_mfma_f32_16x16x32_bf16 v[0:3], a[160:163], a[208:211], v[0:3]\n\t" \
  "ds_read_b64_tr_b16 a[128:129], v192 offset:0\n\t" \
  "ds_read_b64_tr_b16 a[130:131], v192 offset:1024\n\t" \
  "ds_read_b64_tr_b16 a[132:133], v193 offset:0\n\t" \
  "s_add_u32 s97, s97, s82\n\t" \
  "v_mfma_f32_16x16x32_bf16 v[32:35], a[160:163], a[212:215], v[32:35]\n\t" \
  "buffer_load_dwordx4 a[32:35], v248, s[16:19], s97 offen sc1\n\t" \
  "ds_read_b64_tr_b16 a[134:135], v193 offset:1024\n\t" \
  "ds_read_b64_tr_b16 a[136:137], v194 offset:0\n\t" \
  "ds_read_b64_tr_b16 a[138:139], v194 offset:1024\n\t" \
  "v_mfma_f32_16x16x32_bf16 v[64:67], a[160:163], a[216:219], v[64:67]\n\t" \
  "ds_read_b64_tr_b16 a[140:141], v195 offset:0\n\t" \
  "ds_read_b64_tr_b16 a[142:143], v195 offset:1024\n\t" \
  "ds_read_b64_tr_b16 a[144:145], v196 offset:0\n\t" \
  "ds_read_b64_tr_b16 a[146:147], v196 offset:1024\n\t" \
  "v_mfma_f32_16x16x32_bf16 v[96:99], a[160:163], a[220:223], v[96:99]\n\t" \
  "ds_read_b64_tr_b16 a[148:149], v197 offset:0\n\t" \
  "ds_read_b64_tr_b16 a[150:151], v197 offset:1024\n\t" \
  "ds_read_b64_tr_b16 a[152:153], v198 offset:0\n\t" \
  "v_mbcnt_lo_u32_b32 v233, -1, 0\n\t" \
  "v_mfma_f32_16x16x32_bf16 v[100:103], a[164:167], a[220:223], v[100:103]\n\t" \
  "buffer_load_dwordx4 a[36:39], v249, s[16:19], s97 offen sc1\n\t" \
  "ds_read_b64_tr_b16 a[154:155], v198 offset:1024\n\t" \
  "s_lshl_b32 s98, s98, 5\n\t" \
  "v_mfma_f32_16x16x32_bf16 v[68:71], a[164:167], a[216:219], v[68:71]\n\t" \
  "s_add_u32 s50, s50, s98\n\t" \
  "ds_read_b64_tr_b16 a[156:157], v199 offset:0\n\t" \
  "v_mbcnt_hi_u32_b32 v233, -1, v233\n\t" \
  "v_mfma_f32_16x16x32_bf16 v[36:39], a[164:167], a[212:215], v[36:39]\n\t" \
  "s_lshr_b32 s98, s2, 2\n\t" \
  "ds_read_b64_tr_b16 a[158:159], v199 offset:1024\n\t" \
  "v_lshrrev_b32 v129, 4, v233\n\t" \
  "v_mfma_f32_16x16x32_bf16 v[4:7], a[164:167], a[208:211], v[4:7]\n\t" \
  "s_xor_b32 s98, s98, s2\n\t" \
  "ds_read_b64_tr_b16 a[64:65], v192 offset:8192\n\t" \
  "v_and_b32 v130, 15, v233\n\t" \
  "v_mfma_f32_16x16x32_bf16 v[8:11], a[168:171], a[208:211], v[8:11]\n\t" \
  "buffer_load_dwordx4 a[40:43], v250, s[16:19], s97 offen sc1\n\t" \
  "v_mfma_f32_16x16x32_bf16 v[40:43], a[168:171], a[212:215], v[40:43]\n\t" \
  "s_and_b32 s98, s98, 4\n\t" \
  "ds_read_b64_tr_b16 a[66:67], v192 offset:9216\n\t" \
  "v_bfe_u32 v131, v233, 2, 2\n\t" \
  "v_mfma_f32_16x16x32_bf16 v[72:75], a[168:171], a[216:219], v[72:75]\n\t" \
  "s_lshl_b32 s44, s98, 4\n\t" \
  "ds_read_b64_tr_b16 a[68:69], v193 offset:8192\n\t" \
  "v_and_b32 v132, 3, v233\n\t" \
  "v_mfma_f32_16x16x32_bf16 v[104:107], a[168:171], a[220:223], v[104:107]\n\t" \
  "s_and_b32 s98, s2, 0x8\n\t" \
  "ds_read_b64_tr_b16 a[70:71], v193 offset:9216\n\t" \
  "v_cmp_lt_u32 vcc, 6, v233\n\t" \
  "v_mfma_f32_16x16x32_bf16 v[108:111], a[172:175], a[220:223], v[108:111]\n\t" \
  "buffer_load_dwordx4 a[44:47], v251, s[16:19], s97 offen sc1\n\t" \
  "v_mfma_f32_16x16x32_bf16 v[76:79], a[172:175], a[216:219], v[76:79]\n\t" \
  "s_add_u32 s96, s78, 5767168\n\t" \
  "s_and_b32 s96, s96, 0x780000\n\t" \
  "ds_read_b64_tr_b16 a[72:73], v194 offset:8192\n\t" \
  "s_add_u32 m0, s76, 16384\n\t" \
  "v_mfma_f32_16x16x32_bf16 v[44:47], a[172:175], a[212:215], v[44:47]\n\t" \
  "s_lshl_b32 s98, s98, 2\n\t" \
  "ds_read_b64_tr_b16 a[74:75], v194 offset:9216\n\t" \
  "v_mov_b32 v133, 6272\n\t" \
  "v_mfma_f32_16x16x32_bf16 v[12:15], a[172:175], a[208:211], v[12:15]\n\t" \
  "buffer_load_dwordx4 v200, s[20:23], s96 offen offset:0 lds\n\t" \
  "v_mfma_f32_16x16x32_bf16 v[16:19], a[176:179], a[208:211], v[16:19]\n\t" \
  "s_add_u32 s44, s44, s98\n\t" \
  "ds_read_b64_tr_b16 a[76:77], v195 offset:8192\n\t" \
  "v_cndmask_b32 v133, 0, v133, vcc\n\t" \
  "v_mfma_f32_16x16x32_bf16 v[48:51], a[176:179], a[212:215], v[48:51]\n\t" \
  "s_and_b32 s98, s2, 0x3\n\t" \
  "ds_read_b64_tr_b16 a[78:79], v195 offset:9216\n\t" \
  "v_lshl_add_u32 v133, v233, 7, v133\n\t" \
  "v_mfma_f32_16x16x32_bf16 v[80:83], a[176:179], a[216:219], v[80:83]\n\t" \
  "s_lshl_b32 s98, s98, 3\n\t" \
  "ds_read_b64_tr_b16 a[80:81], v196 offset:8192\n\t" \
  "v_cmp_gt_u32 vcc, 14, v233\n\t" \
  "v_mfma_f32_16x16x32_bf16 v[112:115], a[176:179], a[220:223], v[112:115]\n\t" \
  "buffer_load_dwordx4 v201, s[20:23], s96 offen offset:1024 lds\n\t" \
  "v_mfma_f32_16x16x32_bf16 v[116:119], a[180:183], a[220:223], v[116:119]\n\t" \
  "s_add_u32 s44, s44, s98\n\t" \
  "ds_read_b64_tr_b16 a[82:83], v196 offset:9216\n\t" \
  "v_bfrev_b32 v247, 1\n\t" \
  "v_mfma_f32_16x16x32_bf16 v[84:87], a[180:183], a[216:219], v[84:87]\n\t" \
  "s_and_b32 s98, s2, 0x4\n\t" \
  "ds_read_b64_tr_b16 a[84:85], v197 offset:8192\n\t" \
  "v_cndmask_b32 v245, v247, v133, vcc\n\t" \
  "v_mfma_f32_16x16x32_bf16 v[52:55], a[180:183], a[212:215], v[52:55]\n\t" \
  "s_lshl_b32 s98, s98, 0\n\t" \
  "ds_read_b64_tr_b16 a[86:87], v197 offset:9216\n\t" \
  "v_writelane_b32 v247, s91, 0\n\t" \
  "v_mfma_f32_16x16x32_bf16 v[20:23], a[180:183], a[208:211], v[20:23]\n\t" \
  ".Lhs3:\n\ts_cbranch_execz .Lhs3n\n\t" \
  "buffer_load_dwordx4 v202, s[20:23], s96 offen offset:2048 lds\n\t" \
  "v_mfma_f32_16x16x32_bf16 v[24:27], a[184:187], a[208:211], v[24:27]\n\t" \
  "s_add_u32 s44, s44, s98\n\t" \
  "ds_read_b64_tr_b16 a[88:89], v198 offset:8192\n\t" \
  "v_lshrrev_b32 v133, 1, v129\n\t" \
  "v_mfma_f32_16x16x32_bf16 v[56:59], a[184:187], a[212:215], v[56:59]\n\t" \
  "s_add_u32 s44, s44, 0x2000\n\t" \
  "ds_read_b64_tr_b16 a[90:91], v198 offset:9216\n\t" \
  "v_and_b32 v134, 1, v129\n\t" \
  "v_mfma_f32_16x16x32_bf16 v[88:91], a[184:187], a[216:219], v[88:91]\n\t" \
  "ds_read_b64_tr_b16 a[92:93], v199 offset:8192\n\t" \
  "v_lshl_or_b32 v133, v134, 1, v133\n\t" \
  "v_mfma_f32_16x16x32_bf16 v[120:123], a[184:187], a[220:223], v[120:123]\n\t" \
  "buffer_load_dwordx4 v203, s[20:23], s96 offen offset:3072 lds\n\t" \
  "v_mfma_f32_16x16x32_bf16 v[124:127], a[188:191], a[220:223], v[124:127]\n\t" \
  "ds_read_b64_tr_b16 a[94:95], v199 offset:9216\n\t" \
  "v_xor_b32 v133, v133, v130\n\t" \
  "v_mfma_f32_16x16x32_bf16 v[92:95], a[188:191], a[216:219], v[92:95]\n\t" \
  "s_add_u32 s97, s97, s83\n\t" \
  "v_lshlrev_b32 v133, 4, v133\n\t" \
  "v_lshl_add_u32 v133, v130, 8, v133\n\t" \
  "v_mfma_f32_16x16x32_bf16 v[60:63], a[188:191], a[212:215], v[60:63]\n\t" \
  "s_waitcnt vmcnt(24) lgkmcnt(0)\n\t" \
  "v_add_u32 v220, 49152, v133\n\t" \
  "v_xor_b32 v221, 64, v220\n\t" \
  "v_mfma_f32_16x16x32_bf16 v[28:31], a[188:191], a[208:211], v[28:31]\n\t" \
  "s_barrier\n\t" \
  "v_mfma_f32_16x16x32_bf16 v[0:3], a[128:131], a[224:227], v[0:3]\n\t" \
  "buffer_load_dwordx4 a[48:51], v248, s[16:19], s97 offen sc1\n\t" \
  "v_mfma_f32_16x16x32_bf16 v[0:3], a[64:67], a[240:243], v[0:3]\n\t" \
  "ds_read_b64_tr_b16 a[160:161], v192 offset:16384\n\t" \
  "v_xor_b32 v222, 128, v220\n\t" \
  "v_mfma_f32_16x16x32_bf16 v[32:35], a[64:67], a[244:247], v[32:35]\n\t" \
  "ds_read_b64_tr_b16 a[162:163], v192 offset:17408\n\t" \
  "v_xor_b32 v223, 192, v220\n\t" \
  "v_mfma_f32_16x16x32_bf16 v[32:35], a[128:131], a[228:231], v[32:35]\n\t" \
  "v_xor_b32 v133, v130, v129\n\t" \
  "v_lshlrev_b32 v133, 4, v133\n\t" \
  "v_mfma_f32_16x16x32_bf16 v[64:67], a[128:131], a[232:235], v[64:67]\n\t" \
  "buffer_load_dwordx4 a[52:55], v249, s[16:19], s97 offen sc1\n\t" \
  "v_mfma_f32_16x16x32_bf16 v[64:67], a[64:67], a[248:251], v[64:67]\n\t" \
  "ds_read_b64_tr_b16 a[164:165], v193 offset:16384\n\t" \
  "v_lshl_add_u32 v224, v129, 8, v133\n\t" \
  "v_mfma_f32_16x16x32_bf16 v[96:99], a[64:67], a[252:255], v[96:99]\n\t" \
  "ds_read_b64_tr_b16 a[166:167], v193 offset:17408\n\t" \
  "v_xor_b32 v225, 64, v224\n\t" \
  "v_mfma_f32_16x16x32_bf16 v[96:99], a[128:131], a[236:239], v[96:99]\n\t" \
  "ds_read_b64_tr_b16 a[168:169], v194 offset:16384\n\t" \
  "v_xor_b32 v226, 128, v224\n\t" \
  "v_mfma_f32_16x16x32_bf16 v[4:7], a[132:135], a[224:227], v[4:7]\n\t" \
  "buffer_load_dwordx4 a[56:59], v250, s[16:19], s97 offen sc1\n\t" \
  "v_mfma_f32_16x16x32_bf16 v[4:7], a[68:71], a[240:243], v[4:7]\n\t" \
  "ds_read_b64_tr_b16 a[170:171], v194 offset:17408\n\t" \
  "v_xor_b32 v227, 192, v224\n\t" \
  "v_mfma_f32_16x16x32_bf16 v[36:39], a[68:71], a[244:247], v[36:39]\n\t" \
  "ds_read_b64_tr_b16 a[172:173], v195 offset:16384\n\t" \
  "v_lshlrev_b32 v133, 3, v132\n\t" \
  "v_mfma_f32_16x16x32_bf16 v[36:39], a[132:135], a[228:231], v[36:39]\n\t" \
  "v_lshl_add_u32 v133, v129, 10, v133\n\t" \
  "v_lshl_add_u32 v228, v131, 6, v133\n\t" \
  "v_mfma_f32_16x16x32_bf16 v[68:71], a[132:135], a[232:235], v[68:71]\n\t" \
  "buffer_load_dwordx4 a[60:63], v251, s[16:19], s97 offen sc1\n\t" \
  "v_mfma_f32_16x16x32_bf16 v[68:71], a[68:71], a[248:251], v[68:71]\n\t" \
  "s_add_u32 s97, s78, 5242880\n\t" \
  "s_bfe_u32 s97, s97, 0x140003\n\t" \
  "s_add_u32 s97, s97, s82\n\t" \
  "v_mfma_f32_16x16x32_bf16 v[100:103], a[68:71], a[252:255], v[100:103]\n\t" \
  "ds_read_b64_tr_b16 a[174:175], v195 offset:17408\n\t" \
  "v_lshl_add_u32 v228, v134, 5, v228\n\t" \
  "v_mfma_f32_16x16x32_bf16 v[100:103], a[132:135], a[236:239], v[100:103]\n\t" \
  "buffer_load_dwordx4 a[192:195], v248, s[16:19], s97 offen sc1\n\t" \
  "v_mfma_f32_16x16x32_bf16 v[8:11], a[136:139], a[224:227], v[8:11]\n\t" \
  "ds_read_b64_tr_b16 a[176:177], v196 offset:16384\n\t" \
  "v_add_u32 v228, 81920, v228\n\t" \
  "v_mfma_f32_16x16x32_bf16 v[8:11], a[72:75], a[240:243], v[8:11]\n\t" \
  "ds_read_b64_tr_b16 a[178:179], v196 offset:17408\n\t" \
  "v_xor_b32 v229, 32, v228\n\t" \
  "v_mfma_f32_16x16x32_bf16 v[40:43], a[72:75], a[244:247], v[40:43]\n\t" \
  "ds_read_b64_tr_b16 a[180:181], v197 offset:16384\n\t" \
  "v_lshl_add_u32 v244, v131, 8, v133\n\t" \
  "v_mfma_f32_16x16x32_bf16 v[40:43], a[136:139], a[228:231], v[40:43]\n\t" \
  "buffer_load_dwordx4 a[196:199], v249, s[16:19], s97 offen sc1\n\t" \
  "v_mfma_f32_16x16x32_bf16 v[72:75], a[136:139], a[232:235], v[72:75]\n\t" \
  "ds_read_b64_tr_b16 a[182:183], v197 offset:17408\n\t" \
  "v_lshl_add_u32 v244, v131, 5, v244\n\t" \
  "v_mfma_f32_16x16x32_bf16 v[72:75], a[72:75], a[248:251], v[72:75]\n\t" \
  "ds_read_b64_tr_b16 a[184:185], v198 offset:16384\n\t" \
  "v_lshl_add_u32 v244, v134, 7, v244\n\t" \
  "v_mfma_f32_16x16x32_bf16 v[104:107], a[72:75], a[252:255], v[104:107]\n\t" \
  "ds_read_b64_tr_b16 a[186:187], v198 offset:17408\n\t" \
  "v_lshlrev_b32 v133, 4, v132\n\t" \
  "v_mfma_f32_16x16x32_bf16 v[104:107], a[136:139], a[236:239], v[104:107]\n\t" \
  "buffer_load_dwordx4 a[200:203], v250, s[16:19], s97 offen sc1\n\t" \
  "v_mfma_f32_16x16x32_bf16 v[12:15], a[140:143], a[224:227], v[12:15]\n\t" \
  "ds_read_b64_tr_b16 a[188:189], v199 offset:16384\n\t" \
  "v_lshl_add_u32 v133, v129, 6, v133\n\t" \
  "v_mfma_f32_16x16x32_bf16 v[12:15], a[76:79], a[240:243], v[12:15]\n\t" \
  "ds_read_b64_tr_b16 a[190:191], v199 offset:17408\n\t" \
  "v_lshl_add_u32 v230, v131, 8, v133\n\t" \
  "v_mfma_f32_16x16x32_bf16 v[44:47], a[76:79], a[244:247], v[44:47]\n\t" \
  "ds_read_b64_tr_b16 a[96:97], v192 offset:24576\n\t" \
  "v_xor_b32 v231, 32, v230\n\t" \
  "v_mfma_f32_16x16x32_bf16 v[44:47], a[140:143], a[228:231], v[44:47]\n\t" \
  "buffer_load_dwordx4 a[204:207], v251, s[16:19], s97 offen sc1\n\t" \
  "v_mfma_f32_16x16x32_bf16 v[76:79], a[140:143], a[232:235], v[76:79]\n\t" \
  "ds_read_b64_tr_b16 a[98:99], v192 offset:25600\n\t" \
  "v_lshlrev_b32 v133, 1, v129\n\t" \
  "v_mfma_f32_16x16x32_bf16 v[76:79], a[76:79], a[248:251], v[76:79]\n\t" \
  "s_add_u32 s97, s97, s83\n\t" \
  "v_xor_b32 v133, v133, v130\n\t" \
  "v_lshlrev_b32 v133, 4, v133\n\t" \
  "v_mfma_f32_16x16x32_bf16 v[108:111], a[76:79], a[252:255], v[108:111]\n\t" \
  "ds_read_b64_tr_b16 a[100:101], v193 offset:24576\n\t" \
  "v_lshl_add_u32 v240, v129, 11, v133\n\t" \
  "v_mfma_f32_16x16x32_bf16 v[108:111], a[140:143], a[236:239], v[108:111]\n\t" \
  "ds_read_b64_tr_b16 a[102:103], v193 offset:25600\n\t" \
  "v_xor_b32 v241, 0x80, v240\n\t" \
  "v_mfma_f32_16x16x32_bf16 v[16:19], a[144:147], a[224:227], v[16:19]\n\t" \
  "buffer_load_dwordx4 a[208:211], v248, s[16:19], s97 offen sc1\n\t" \
  "v_mfma_f32_16x16x32_bf16 v[16:19], a[80:83], a[240:243], v[16:19]\n\t" \
  "ds_read_b64_tr_b16 a[104:105], v194 offset:24576\n\t" \
  "v_lshrrev_b32 v133, 1, v233\n\t" \
  "v_mfma_f32_16x16x32_bf16 v[48:51], a[80:83], a[244:247], v[48:51]\n\t" \
  "s_waitcnt vmcnt(20)\n\t" \
  "v_lshlrev_b32 v128, 16, v136\n\t" \
  "v_mul_u32_u24 v133, 57344, v133\n\t" \
  "v_mfma_f32_16x16x32_bf16 v[48:51], a[144:147], a[228:231], v[48:51]\n\t" \
  "ds_read_b64_tr_b16 a[106:107], v194 offset:25600\n\t" \
  "v_and_b32 v134, 1, v233\n\t" \
  "v_mfma_f32_16x16x32_bf16 v[80:83], a[144:147], a[232:235], v[80:83]\n\t" \
  "buffer_load_dwordx4 a[212:215], v249, s[16:19], s97 offen sc1\n\t" \
  "v_mfma_f32_16x16x32_bf16 v[80:83], a[80:83], a[248:251], v[80:83]\n\t" \
  "ds_read_b64_tr_b16 a[108:109], v195 offset:24576\n\t" \
  "v_lshl_add_u32 v133, v134, 4, v133\n\t" \
  "v_mfma_f32_16x16x32_bf16 v[112:115], a[80:83], a[252:255], v[112:115]\n\t" \
  "ds_read_b64_tr_b16 a[110:111], v195 offset:25600\n\t" \
  "v_or_b32 v232, s57, v133\n\t" \
  "v_mfma_f32_16x16x32_bf16 v[112:115], a[144:147], a[236:239], v[112:115]\n\t" \
  "ds_read_b64_tr_b16 a[112:113], v196 offset:24576\n\t" \
  "v_and_b32 v136, 0xffff0000, v136\n\t" \
  "v_mfma_f32_16x16x32_bf16 v[20:23], a[148:151], a[224:227], v[20:23]\n\t" \
  "buffer_load_dwordx4 a[216:219], v250, s[16:19], s97 offen sc1\n\t" \
  "v_mfma_f32_16x16x32_bf16 v[20:23], a[84:87], a[240:243], v[20:23]\n\t" \
  "ds_read_b64_tr_b16 a[114:115], v196 offset:25600\n\t" \
  "v_mul_f32 v176, v128, v168\n\t" \
  "v_mfma_f32_16x16x32_bf16 v[52:55], a[84:87], a[244:247], v[52:55]\n\t" \
  "v_mul_f32 v180, v136, v168\n\t" \
  "v_lshlrev_b32 v128, 16, v137\n\t" \
  "v_mfma_f32_16x16x32_bf16 v[52:55], a[148:151], a[228:231], v[52:55]\n\t" \
  "ds_read_b64_tr_b16 a[116:117], v197 offset:24576\n\t" \
  "v_and_b32 v137, 0xffff0000, v137\n\t" \
  "v_mfma_f32_16x16x32_bf16 v[84:87], a[148:151], a[232:235], v[84:87]\n\t" \
  "buffer_load_dwordx4 a[220:223], v251, s[16:19], s97 offen sc1\n\t" \
  "v_mfma_f32_16x16x32_bf16 v[84:87], a[84:87], a[248:251], v[84:87]\n\t" \
  "s_add_u32 s96, s78, 6291456\n\t" \
  "s_and_b32 s96, s96, 0x780000\n\t" \
  "s_add_u32 m0, s76, 32768\n\t" \
  "v_mfma_f32_16x16x32_bf16 v[116:119], a[84:87], a[252:255], v[116:119]\n\t" \
  "ds_read_b64_tr_b16 a[118:119], v197 offset:25600\n\t" \
  "v_mul_f32 v177, v128, v168\n\t" \
  "v_mfma_f32_16x16x32_bf16 v[116:119], a[148:151], a[236:239], v[116:119]\n\t" \
  "buffer_load_dwordx4 v200, s[20:23], s96 offen offset:0 lds\n\t" \
  "v_mfma_f32_16x16x32_bf16 v[24:27], a[152:155], a[224:227], v[24:27]\n\t" \
  "ds_read_b64_tr_b16 a[120:121], v198 offset:24576\n\t" \
  "v_mul_f32 v181, v137, v168\n\t" \
  "v_mfma_f32_16x16x32_bf16 v[24:27], a[88:91], a[240:243], v[24:27]\n\t" \
  "ds_read_b64_tr_b16 a[122:123], v198 offset:25600\n\t" \
  "v_lshlrev_b32 v128, 16, v138\n\t" \
  "v_mfma_f32_16x16x32_bf16 v[56:59], a[88:91], a[244:247], v[56:59]\n\t" \
  "ds_read_b64_tr_b16 a[124:125], v199 offset:24576\n\t" \
  "v_and_b32 v138, 0xffff0000, v138\n\t" \
  "v_mfma_f32_16x16x32_bf16 v[56:59], a[152:155], a[228:231], v[56:59]\n\t" \
  "buffer_load_dwordx4 v201, s[20:23], s96 offen offset:1024 lds\n\t" \
  "v_mfma_f32_16x16x32_bf16 v[88:91], a[152:155], a[232:235], v[88:91]\n\t" \
  "ds_read_b64_tr_b16 a[126:127], v199 offset:25600\n\t" \
  "v_mul_f32 v178, v128, v168\n\t" \
  "v_mfma_f32_16x16x32_bf16 v[88:91], a[88:91], a[248:251], v[88:91]\n\t" \
  "v_mul_f32 v182, v138, v168\n\t" \
  "v_lshlrev_b32 v128, 16, v139\n\t" \
  "v_mfma_f32_16x16x32_bf16 v[120:123], a[88:91], a[252:255], v[120:123]\n\t" \
  "v_and_b32 v139, 0xffff0000, v139\n\t" \
  "v_mul_f32 v179, v128, v168\n\t" \
  "v_mfma_f32_16x16x32_bf16 v[120:123], a[152:155], a[236:239], v[120:123]\n\t" \
  "buffer_load_dwordx4 v202, s[20:23], s96 offen offset:2048 lds\n\t" \
  "v_mfma_f32_16x16x32_bf16 v[28:31], a[156:159], a[224:227], v[28:31]\n\t" \
  "s_cmp_lt_i32 s91, 0\n\t" \
  "s_cselect_b32 s44, s91, s44\n\t" \
  "v_mul_f32 v183, v139, v168\n\t" \
  "v_mfma_f32_16x16x32_bf16 v[28:31], a[92:95], a[240:243], v[28:31]\n\t" \
  "s_waitcnt vmcnt(25)\n\t" \
  "v_lshlrev_b32 v128, 16, v140\n\t" \
  "v_and_b32 v140, 0xffff0000, v140\n\t" \
  "v_mfma_f32_16x16x32_bf16 v[60:63], a[92:95], a[244:247], v[60:63]\n\t" \
  "v_fma_f32 v176, v128, v169, v176\n\t" \
  "v_fma_f32 v180, v140, v169, v180\n\t" \
  "v_mfma_f32_16x16x32_bf16 v[60:63], a[156:159], a[228:231], v[60:63]\n\t" \
  "buffer_load_dwordx4 v203, s[20:23], s96 offen offset:3072 lds\n\t" \
  "v_mfma_f32_16x16x32_bf16 v[92:95], a[156:159], a[232:235], v[92:95]\n\t" \
  "v_lshlrev_b32 v128, 16, v141\n\t" \
  "v_and_b32 v141, 0xffff0000, v141\n\t" \
  "v_mfma_f32_16x16x32_bf16 v[92:95], a[92:95], a[248:251], v[92:95]\n\t" \
  "s_add_u32 s97, s78, 5767168\n\t" \
  "v_fma_f32 v177, v128, v169, v177\n\t" \
  "v_fma_f32 v181, v141, v169, v181\n\t" \
  "v_mfma_f32_16x16x32_bf16 v[124:127], a[92:95], a[252:255], v[124:127]\n\t" \
  "s_waitcnt lgkmcnt(0)\n\t" \
  "s_bfe_u32 s97, s97, 0x140003\n\t" \
  "s_add_u32 s97, s97, s82\n\t" \
  "v_mfma_f32_16x16x32_bf16 v[124:127], a[156:159], a[236:239], v[124:127]\n\t" \
  "s_barrier\n\t" \
  "v_mfma_f32_16x16x32_bf16 v[0:3], a[160:163], a[0:3], v[0:3]\n\t" \
  "buffer_load_dwordx4 a[224:227], v248, s[16:19], s97 offen sc1\n\t" \
  "v_mfma_f32_16x16x32_bf16 v[0:3], a[96:99], a[16:19], v[0:3]\n\t" \
  "ds_read_b64_tr_b16 a[128:129], v192 offset:32768\n\t" \
  "v_lshlrev_b32 v128, 16, v142\n\t" \
  "v_mfma_f32_16x16x32_bf16 v[32:35], a[96:99], a[20:23], v[32:35]\n\t" \
  "ds_read_b64_tr_b16 a[130:131], v192 offset:33792\n\t" \
  "v_and_b32 v142, 0xffff0000, v142\n\t" \
  "v_mfma_f32_16x16x32_bf16 v[32:35], a[160:163], a[4:7], v[32:35]\n\t" \
  "v_fma_f32 v178, v128, v169, v178\n\t" \
  "v_fma_f32 v182, v142, v169, v182\n\t" \
  "v_mfma_f32_16x16x32_bf16 v[64:67], a[160:163], a[8:11], v[64:67]\n\t" \
  "buffer_load_dwordx4 a[228:231], v249, s[16:19], s97 offen sc1\n\t" \
  "v_mfma_f32_16x16x32_bf16 v[64:67], a[96:99], a[24:27], v[64:67]\n\t" \
  "ds_read_b64_tr_b16 a[132:133], v193 offset:32768\n\t" \
  "v_lshlrev_b32 v128, 16, v143\n\t" \
  "v_mfma_f32_16x16x32_bf16 v[96:99], a[96:99], a[28:31], v[96:99]\n\t" \
  "ds_read_b64_tr_b16 a[134:135], v193 offset:33792\n\t" \
  "v_and_b32 v143, 0xffff0000, v143\n\t" \
  "v_mfma_f32_16x16x32_bf16 v[96:99], a[160:163], a[12:15], v[96:99]\n\t" \
  "ds_read_b64_tr_b16 a[136:137], v194 offset:32768\n\t" \
  "v_fma_f32 v179, v128, v169, v179\n\t" \
  "v_mfma_f32_16x16x32_bf16 v[4:7], a[164:167], a[0:3], v[4:7]\n\t" \
  "buffer_load_dwordx4 a[232:235], v250, s[16:19], s97 offen sc1\n\t" \
  "v_mfma_f32_16x16x32_bf16 v[4:7], a[100:103], a[16:19], v[4:7]\n\t" \
  "ds_read_b64_tr_b16 a[138:139], v194 offset:33792\n\t" \
  "v_fma_f32 v183, v143, v169, v183\n\t" \
  "v_mfma_f32_16x16x32_bf16 v[36:39], a[100:103], a[20:23], v[36:39]\n\t" \
  "s_waitcnt vmcnt(28)\n\t" \
  "v_lshlrev_b32 v128, 16, v144\n\t" \
  "v_and_b32 v144, 0xffff0000, v144\n\t" \
  "v_mfma_f32_16x16x32_bf16 v[36:39], a[164:167], a[4:7], v[36:39]\n\t" \
  "ds_read_b64_tr_b16 a[140:141], v195 offset:32768\n\t" \
  "v_fma_f32 v176, v128, v170, v176\n\t" \
  "v_mfma_f32_16x16x32_bf16 v[68:71], a[164:167], a[8:11], v[68:71]\n\t" \
  "buffer_load_dwordx4 a[236:239], v251, s[16:19], s97 offen sc1\n\t" \
  "v_mfma_f32_16x16x32_bf16 v[68:71], a[100:103], a[24:27], v[68:71]\n\t" \
  "s_add_u32 s96, s78, 6815744\n\t" \
  "s_and_b32 s96, s96, 0x780000\n\t" \
  "s_add_u32 m0, s76, 65536\n\t" \
  "v_mfma_f32_16x16x32_bf16 v[100:103], a[100:103], a[28:31], v[100:103]\n\t" \
  "ds_read_b64_tr_b16 a[142:143], v195 offset:33792\n\t" \
  "v_fma_f32 v180, v144, v170, v180\n\t" \
  "v_mfma_f32_16x16x32_bf16 v[100:103], a[164:167], a[12:15], v[100:103]\n\t" \
  "buffer_load_dwordx4 v200, s[20:23], s96 offen offset:0 lds\n\t" \
  "v_mfma_f32_16x16x32_bf16 v[8:11], a[168:171], a[0:3], v[8:11]\n\t" \
  "ds_read_b64_tr_b16 a[144:145], v196 offset:32768\n\t" \
  "v_lshlrev_b32 v128, 16, v145\n\t" \
  "v_mfma_f32_16x16x32_bf16 v[8:11], a[104:107], a[16:19], v[8:11]\n\t" \
  "ds_read_b64_tr_b16 a[146:147], v196 offset:33792\n\t" \
  "v_and_b32 v145, 0xffff0000, v145\n\t" \
  "v_mfma_f32_16x16x32_bf16 v[40:43], a[104:107], a[20:23], v[40:43]\n\t" \
  "ds_read_b64_tr_b16 a[148:149], v197 offset:32768\n\t" \
  "v_fma_f32 v177, v128, v170, v177\n\t" \
  "v_mfma_f32_16x16x32_bf16 v[40:43], a[168:171], a[4:7], v[40:43]\n\t" \
  "buffer_load_dwordx4 v201, s[20:23], s96 offen offset:1024 lds\n\t" \
  "v_mfma_f32_16x16x32_bf16 v[72:75], a[168:171], a[8:11], v[72:75]\n\t" \
  "ds_read_b64_tr_b16 a[150:151], v197 offset:33792\n\t" \
  "v_fma_f32 v181, v145, v170, v181\n\t" \
  "v_mfma_f32_16x16x32_bf16 v[72:75], a[104:107], a[24:27], v[72:75]\n\t" \
  "ds_read_b64_tr_b16 a[152:153], v198 offset:32768\n\t" \
  "v_lshlrev_b32 v128, 16, v146\n\t" \
  "v_mfma_f32_16x16x32_bf16 v[104:107], a[104:107], a[28:31], v[104:107]\n\t" \
  "ds_read_b64_tr_b16 a[154:155], v198 offset:33792\n\t" \
  "v_and_b32 v146, 0xffff0000, v146\n\t" \
  "v_mfma_f32_16x16x32_bf16 v[104:107], a[168:171], a[12:15], v[104:107]\n\t" \
  "buffer_load_dwordx4 v202, s[20:23], s96 offen offset:2048 lds\n\t" \
  "v_mfma_f32_16x16x32_bf16 v[12:15], a[172:175], a[0:3], v[12:15]\n\t" \
  "ds_read_b64_tr_b16 a[156:157], v199 offset:32768\n\t" \
  "v_fma_f32 v178, v128, v170, v178\n\t" \
  "v_mfma_f32_16x16x32_bf16 v[12:15], a[108:111], a[16:19], v[12:15]\n\t" \
  "ds_read_b64_tr_b16 a[158:159], v199 offset:33792\n\t" \
  "v_fma_f32 v182, v146, v170, v182\n\t" \
  "v_mfma_f32_16x16x32_bf16 v[44:47], a[108:111], a[20:23], v[44:47]\n\t" \
  "ds_read_b64_tr_b16 a[64:65], v192 offset:40960\n\t" \
  "v_lshlrev_b32 v128, 16, v147\n\t" \
  "v_mfma_f32_16x16x32_bf16 v[44:47], a[172:175], a[4:7], v[44:47]\n\t" \
  "buffer_load_dwordx4 v203, s[20:23], s96 offen offset:3072 lds\n\t" \
  "v_mfma_f32_16x16x32_bf16 v[76:79], a[172:175], a[8:11], v[76:79]\n\t" \
  "ds_read_b64_tr_b16 a[66:67], v192 offset:41984\n\t" \
  "v_and_b32 v147, 0xffff0000, v147\n\t" \
  "v_mfma_f32_16x16x32_bf16 v[76:79], a[108:111], a[24:27], v[76:79]\n\t" \
  "s_add_u32 s97, s97, s83\n\t" \
  "v_fma_f32 v179, v128, v170, v179\n\t" \
  "v_fma_f32 v183, v147, v170, v183\n\t" \
  "v_mfma_f32_16x16x32_bf16 v[108:111], a[108:111], a[28:31], v[108:111]\n\t" \
  "ds_read_b64_tr_b16 a[68:69], v193 offset:40960\n\t" \
  "v_mfma_f32_16x16x32_bf16 v[108:111], a[172:175], a[12:15], v[108:111]\n\t" \
  "s_waitcnt vmcnt(32)\n\t" \
  "v_lshlrev_b32 v128, 16, v148\n\t" \
  "v_and_b32 v148, 0xffff0000, v148\n\t" \
  "v_mfma_f32_16x16x32_bf16 v[16:19], a[176:179], a[0:3], v[16:19]\n\t" \
  "buffer_load_dwordx4 a[240:243], v248, s[16:19], s97 offen sc1\n\t" \
  "v_mfma_f32_16x16x32_bf16 v[16:19], a[112:115], a[16:19], v[16:19]\n\t" \
  "ds_read_b64_tr_b16 a[70:71], v193 offset:41984\n\t" \
  "v_fma_f32 v176, v128, v171, v176\n\t" \
  "v_mfma_f32_16x16x32_bf16 v[48:51], a[112:115], a[20:23], v[48:51]\n\t" \
  "ds_read_b64_tr_b16 a[72:73], v194 offset:40960\n\t" \
  "v_fma_f32 v180, v148, v171, v180\n\t" \
  "v_mfma_f32_16x16x32_bf16 v[48:51], a[176:179], a[4:7], v[48:51]\n\t" \
  "ds_read_b64_tr_b16 a[74:75], v194 offset:41984\n\t" \
  "v_lshlrev_b32 v128, 16, v149\n\t" \
  "v_mfma_f32_16x16x32_bf16 v[80:83], a[176:179], a[8:11], v[80:83]\n\t" \
  "ds_read_b64_tr_b16 a[76:77], v195 offset:40960\n\t" \
  "v_and_b32 v149, 0xffff0000, v149\n\t" \
  "v_mfma_f32_16x16x32_bf16 v[80:83], a[112:115], a[24:27], v[80:83]\n\t" \
  "v_fma_f32 v177, v128, v171, v177\n\t" \
  "v_fma_f32 v181, v149, v171, v181\n\t" \
  "v_mfma_f32_16x16x32_bf16 v[112:115], a[112:115], a[28:31], v[112:115]\n\t" \
  "ds_read_b64_tr_b16 a[78:79], v195 offset:41984\n\t" \
  "v_lshlrev_b32 v128, 16, v150\n\t" \
  "v_mfma_f32_16x16x32_bf16 v[112:115], a[176:179], a[12:15], v[112:115]\n\t" \
  "ds_read_b64_tr_b16 a[80:81], v196 offset:40960\n\t" \
  "v_and_b32 v150, 0xffff0000, v150\n\t" \
  "v_mfma_f32_16x16x32_bf16 v[20:23], a[180:183], a[0:3], v[20:23]\n\t" \
  "buffer_load_dwordx4 a[244:247], v249, s[16:19], s97 offen sc1\n\t" \
  "v_mfma_f32_16x16x32_bf16 v[20:23], a[116:119], a[16:19], v[20:23]\n\t" \
  "ds_read_b64_tr_b16 a[82:83], v196 offset:41984\n\t" \
  "v_fma_f32 v178, v128, v171, v178\n\t" \
  "v_mfma_f32_16x16x32_bf16 v[52:55], a[116:119], a[20:23], v[52:55]\n\t" \
  "v_fma_f32 v182, v150, v171, v182\n\t" \
  "v_lshlrev_b32 v128, 16, v151\n\t" \
  "v_mfma_f32_16x16x32_bf16 v[52:55], a[180:183], a[4:7], v[52:55]\n\t" \
  "ds_read_b64_tr_b16 a[84:85], v197 offset:40960\n\t" \
  "v_and_b32 v151, 0xffff0000, v151\n\t" \
  "v_mfma_f32_16x16x32_bf16 v[84:87], a[180:183], a[8:11], v[84:87]\n\t" \
  "ds_read_b64_tr_b16 a[86:87], v197 offset:41984\n\t" \
  "v_fma_f32 v179, v128, v171, v179\n\t" \
  "v_mfma_f32_16x16x32_bf16 v[84:87], a[116:119], a[24:27], v[84:87]\n\t" \
  "ds_read_b64_tr_b16 a[88:89], v198 offset:40960\n\t" \
  "v_fma_f32 v183, v151, v171, v183\n\t" \
  "v_mfma_f32_16x16x32_bf16 v[116:119], a[116:119], a[28:31], v[116:119]\n\t" \
  "buffer_load_dwordx4 a[248:251], v250, s[16:19], s97 offen sc1\n\t" \
  "v_mfma_f32_16x16x32_bf16 v[116:119], a[180:183], a[12:15], v[116:119]\n\t" \
  "ds_read_b64_tr_b16 a[90:91], v198 offset:41984\n\t" \
  "v_mfma_f32_16x16x32_bf16 v[24:27], a[184:187], a[0:3], v[24:27]\n\t" \
  "ds_read_b64_tr_b16 a[92:93], v199 offset:40960\n\t" \
  "v_mfma_f32_16x16x32_bf16 v[24:27], a[120:123], a[16:19], v[24:27]\n\t" \
  "ds_read_b64_tr_b16 a[94:95], v199 offset:41984\n\t" \
  "v_mfma_f32_16x16x32_bf16 v[56:59], a[120:123], a[20:23], v[56:59]\n\t" \
  "v_mfma_f32_16x16x32_bf16 v[56:59], a[184:187], a[4:7], v[56:59]\n\t" \
  "v_mfma_f32_16x16x32_bf16 v[88:91], a[184:187], a[8:11], v[88:91]\n\t" \
  "v_mfma_f32_16x16x32_bf16 v[88:91], a[120:123], a[24:27], v[88:91]\n\t" \
  "buffer_load_dwordx4 a[252:255], v251, s[16:19], s97 offen sc1\n\t" \
  "v_mfma_f32_16x16x32_bf16 v[120:123], a[120:123], a[28:31], v[120:123]\n\t" \
  "v_mfma_f32_16x16x32_bf16 v[120:123], a[184:187], a[12:15], v[120:123]\n\t" \
  "v_mfma_f32_16x16x32_bf16 v[28:31], a[188:191], a[0:3], v[28:31]\n\t" \
  "v_mfma_f32_16x16x32_bf16 v[28:31], a[124:127], a[16:19], v[28:31]\n\t" \
  "v_mfma_f32_16x16x32_bf16 v[60:63], a[124:127], a[20:23], v[60:63]\n\t" \
  "v_mfma_f32_16x16x32_bf16 v[60:63], a[188:191], a[4:7], v[60:63]\n\t" \
  "v_mfma_f32_16x16x32_bf16 v[92:95], a[188:191], a[8:11], v[92:95]\n\t" \
  "v_mfma_f32_16x16x32_bf16 v[92:95], a[124:127], a[24:27], v[92:95]\n\t" \
  "s_add_u32 s97, s78, 6291456\n\t" \
  "s_bfe_u32 s97, s97, 0x140003\n\t" \
  "s_add_u32 s97, s97, s82\n\t" \
  "v_mfma_f32_16x16x32_bf16 v[124:127], a[124:127], a[28:31], v[124:127]\n\t" \
  "s_waitcnt vmcnt(24) lgkmcnt(0)\n\t" \
  "v_mfma_f32_16x16x32_bf16 v[124:127], a[188:191], a[12:15], v[124:127]\n\t" \
  "s_barrier\n\t" \
  "v_mfma_f32_16x16x32_bf16 v[0:3], a[128:131], a[32:35], v[0:3]\n\t" \
  "buffer_load_dwordx4 a[0:3], v248, s[16:19], s97 offen sc1\n\t" \
  "v_mfma_f32_16x16x32_bf16 v[0:3], a[64:67], a[48:51], v[0:3]\n\t" \
  "ds_read_b64_tr_b16 a[160:161], v184 offset:0\n\t" \
  "v_mfma_f32_16x16x32_bf16 v[32:35], a[64:67], a[52:55], v[32:35]\n\t" \
  "ds_read_b64_tr_b16 a[162:163], v184 offset:1024\n\t" \
  "v_mfma_f32_16x16x32_bf16 v[32:35], a[128:131], a[36:39], v[32:35]\n\t" \
  "buffer_load_dwordx4 a[4:7], v249, s[16:19], s97 offen sc1\n\t" \
  "v_mfma_f32_16x16x32_bf16 v[64:67], a[128:131], a[40:43], v[64:67]\n\t" \
  "ds_read_b64_tr_b16 a[164:165], v185 offset:0\n\t" \
  "v_mfma_f32_16x16x32_bf16 v[64:67], a[64:67], a[56:59], v[64:67]\n\t" \
  "buffer_load_dwordx4 a[8:11], v250, s[16:19], s97 offen sc1\n\t" \
  "v_mfma_f32_16x16x32_bf16 v[96:99], a[64:67], a[60:63], v[96:99]\n\t" \
  "ds_read_b64_tr_b16 a[166:167], v185 offset:1024\n\t" \
  "v_mfma_f32_16x16x32_bf16 v[96:99], a[128:131], a[44:47], v[96:99]\n\t" \
  "ds_read_b64_tr_b16 a[168:169], v186 offset:0\n\t" \
  "v_mfma_f32_16x16x32_bf16 v[4:7], a[132:135], a[32:35], v[4:7]\n\t" \
  "buffer_load_dwordx4 a[12:15], v251, s[16:19], s97 offen sc1\n\t" \
  "v_mfma_f32_16x16x32_bf16 v[4:7], a[68:71], a[48:51], v[4:7]\n\t" \
  "s_add_u32 s96, s78, 7340032\n\t" \
  "s_and_b32 s96, s96, 0x780000\n\t" \
  "s_add_u32 m0, s76, 81920\n\t" \
  "v_mfma_f32_16x16x32_bf16 v[36:39], a[68:71], a[52:55], v[36:39]\n\t" \
  "buffer_load_dwordx4 v200, s[20:23], s96 offen offset:0 lds\n\t" \
  "v_mfma_f32_16x16x32_bf16 v[36:39], a[132:135], a[36:39], v[36:39]\n\t" \
  "ds_read_b64_tr_b16 a[170:171], v186 offset:1024\n\t" \
  "v_mfma_f32_16x16x32_bf16 v[68:71], a[132:135], a[40:43], v[68:71]\n\t" \
  "ds_read_b64_tr_b16 a[172:173], v187 offset:0\n\t" \
  "v_mfma_f32_16x16x32_bf16 v[68:71], a[68:71], a[56:59], v[68:71]\n\t" \
  "buffer_load_dwordx4 v201, s[20:23], s96 offen offset:1024 lds\n\t" \
  "v_mfma_f32_16x16x32_bf16 v[100:103], a[68:71], a[60:63], v[100:103]\n\t" \
  "ds_read_b64_tr_b16 a[174:175], v187 offset:1024\n\t" \
  "v_mfma_f32_16x16x32_bf16 v[100:103], a[132:135], a[44:47], v[100:103]\n\t" \
  "buffer_load_dwordx4 v202, s[20:23], s96 offen offset:2048 lds\n\t" \
  "v_mfma_f32_16x16x32_bf16 v[8:11], a[136:139], a[32:35], v[8:11]\n\t" \
  "ds_read_b64_tr_b16 a[176:177], v188 offset:0\n\t" \
  "v_mfma_f32_16x16x32_bf16 v[8:11], a[72:75], a[48:51], v[8:11]\n\t" \
  "ds_read_b64_tr_b16 a[178:179], v188 offset:1024\n\t" \
  "v_mfma_f32_16x16x32_bf16 v[40:43], a[72:75], a[52:55], v[40:43]\n\t" \
  "buffer_load_dwordx4 v203, s[20:23], s96 offen offset:3072 lds\n\t" \
  "v_mfma_f32_16x16x32_bf16 v[40:43], a[136:139], a[36:39], v[40:43]\n\t" \
  "ds_read_b64_tr_b16 a[180:181], v189 offset:0\n\t" \
  "s_add_u32 s97, s97, s83\n\t" \
  "v_mfma_f32_16x16x32_bf16 v[72:75], a[136:139], a[40:43], v[72:75]\n\t" \
  "buffer_load_dwordx4 a[16:19], v248, s[16:19], s97 offen sc1\n\t" \
  "v_mfma_f32_16x16x32_bf16 v[72:75], a[72:75], a[56:59], v[72:75]\n\t" \
  "ds_read_b64_tr_b16 a[182:183], v189 offset:1024\n\t" \
  "v_mfma_f32_16x16x32_bf16 v[104:107], a[72:75], a[60:63], v[104:107]\n\t" \
  "ds_read_b64_tr_b16 a[184:185], v190 offset:0\n\t" \
  "v_mfma_f32_16x16x32_bf16 v[104:107], a[136:139], a[44:47], v[104:107]\n\t" \
  "buffer_load_dwordx4 a[20:23], v249, s[16:19], s97 offen sc1\n\t" \
  "v_mfma_f32_16x16x32_bf16 v[12:15], a[140:143], a[32:35], v[12:15]\n\t" \
  "ds_read_b64_tr_b16 a[186:187], v190 offset:1024\n\t" \
  "v_mfma_f32_16x16x32_bf16 v[12:15], a[76:79], a[48:51], v[12:15]\n\t" \
  "buffer_load_dwordx4 a[24:27], v250, s[16:19], s97 offen sc1\n\t" \
  "v_mfma_f32_16x16x32_bf16 v[44:47], a[76:79], a[52:55], v[44:47]\n\t" \
  "ds_read_b64_tr_b16 a[188:189], v191 offset:0\n\t" \
  "v_mfma_f32_16x16x32_bf16 v[44:47], a[140:143], a[36:39], v[44:47]\n\t" \
  "ds_read_b64_tr_b16 a[190:191], v191 offset:1024\n\t" \
  "v_mfma_f32_16x16x32_bf16 v[76:79], a[140:143], a[40:43], v[76:79]\n\t" \
  "buffer_load_dwordx4 a[28:31], v251, s[16:19], s97 offen sc1\n\t" \
  "v_mfma_f32_16x16x32_bf16 v[76:79], a[76:79], a[56:59], v[76:79]\n\t" \
  "ds_read_b64_tr_b16 a[96:97], v184 offset:8192\n\t" \
  "s_mov_b32 s99, 49152\n\t" \
  "v_mfma_f32_16x16x32_bf16 v[108:111], a[76:79], a[60:63], v[108:111]\n\t" \
  "ds_read_b64_tr_b16 a[98:99], v184 offset:9216\n\t" \
  "v_mfma_f32_16x16x32_bf16 v[108:111], a[140:143], a[44:47], v[108:111]\n\t" \
  "ds_read_b64_tr_b16 a[100:101], v185 offset:8192\n\t" \
  "v_mfma_f32_16x16x32_bf16 v[16:19], a[144:147], a[32:35], v[16:19]\n\t" \
  "buffer_load_dwordx4 v[152:155], v209, s[8:11], s99 offen\n\t" \
  "v_mfma_f32_16x16x32_bf16 v[16:19], a[80:83], a[48:51], v[16:19]\n\t" \
  "ds_read_b64_tr_b16 a[102:103], v185 offset:9216\n\t" \
  "s_mov_b32 s99, 61440\n\t" \
  "v_mfma_f32_16x16x32_bf16 v[48:51], a[80:83], a[52:55], v[48:51]\n\t" \
  "ds_read_b64_tr_b16 a[104:105], v186 offset:8192\n\t" \
  "v_mfma_f32_16x16x32_bf16 v[48:51], a[144:147], a[36:39], v[48:51]\n\t" \
  "ds_read_b64_tr_b16 a[106:107], v186 offset:9216\n\t" \
  "v_mfma_f32_16x16x32_bf16 v[80:83], a[144:147], a[40:43], v[80:83]\n\t" \
  "ds_read_b64_tr_b16 a[108:109], v187 offset:8192\n\t" \
  "v_mfma_f32_16x16x32_bf16 v[80:83], a[80:83], a[56:59], v[80:83]\n\t" \
  "ds_read_b64_tr_b16 a[110:111], v187 offset:9216\n\t" \
  "v_mfma_f32_16x16x32_bf16 v[112:115], a[80:83], a[60:63], v[112:115]\n\t" \
  "buffer_load_dwordx4 v[156:159], v209, s[8:11], s99 offen\n\t" \
  "v_mfma_f32_16x16x32_bf16 v[112:115], a[144:147], a[44:47], v[112:115]\n\t" \
  "ds_read_b64_tr_b16 a[112:113], v188 offset:8192\n\t" \
  "s_mov_b32 s99, 73728\n\t" \
  "v_mfma_f32_16x16x32_bf16 v[20:23], a[148:151], a[32:35], v[20:23]\n\t" \
  "ds_read_b64_tr_b16 a[114:115], v188 offset:9216\n\t" \
  "v_mfma_f32_16x16x32_bf16 v[20:23], a[84:87], a[48:51], v[20:23]\n\t" \
  "ds_read_b64_tr_b16 a[116:117], v189 offset:8192\n\t" \
  "v_mfma_f32_16x16x32_bf16 v[52:55], a[84:87], a[52:55], v[52:55]\n\t" \
  "ds_read_b64_tr_b16 a[118:119], v189 offset:9216\n\t" \
  "v_mfma_f32_16x16x32_bf16 v[52:55], a[148:151], a[36:39], v[52:55]\n\t" \
  "ds_read_b64_tr_b16 a[120:121], v190 offset:8192\n\t" \
  "v_mfma_f32_16x16x32_bf16 v[84:87], a[148:151], a[40:43], v[84:87]\n\t" \
  "ds_read_b64_tr_b16 a[122:123], v190 offset:9216\n\t" \
  "v_mfma_f32_16x16x32_bf16 v[84:87], a[84:87], a[56:59], v[84:87]\n\t" \
  "ds_read_b64_tr_b16 a[124:125], v191 offset:8192\n\t" \
  "v_mfma_f32_16x16x32_bf16 v[116:119], a[84:87], a[60:63], v[116:119]\n\t" \
  "buffer_load_dwordx4 v[160:163], v209, s[8:11], s99 offen\n\t" \
  "v_mfma_f32_16x16x32_bf16 v[116:119], a[148:151], a[44:47], v[116:119]\n\t" \
  "ds_read_b64_tr_b16 a[126:127], v191 offset:9216\n\t" \
  "s_mov_b32 s99, 86016\n\t" \
  "v_mfma_f32_16x16x32_bf16 v[24:27], a[152:155], a[32:35], v[24:27]\n\t" \
  "v_mfma_f32_16x16x32_bf16 v[24:27], a[88:91], a[48:51], v[24:27]\n\t" \
  "v_mfma_f32_16x16x32_bf16 v[56:59], a[88:91], a[52:55], v[56:59]\n\t" \
  "v_mfma_f32_16x16x32_bf16 v[56:59], a[152:155], a[36:39], v[56:59]\n\t" \
  "v_mfma_f32_16x16x32_bf16 v[88:91], a[152:155], a[40:43], v[88:91]\n\t" \
  "v_mfma_f32_16x16x32_bf16 v[88:91], a[88:91], a[56:59], v[88:91]\n\t" \
  "buffer_load_dwordx4 v[164:167], v209, s[8:11], s99 offen\n\t" \
  "v_mfma_f32_16x16x32_bf16 v[120:123], a[88:91], a[60:63], v[120:123]\n\t" \
  "v_mfma_f32_16x16x32_bf16 v[120:123], a[152:155], a[44:47], v[120:123]\n\t" \
  "v_mfma_f32_16x16x32_bf16 v[28:31], a[156:159], a[32:35], v[28:31]\n\t" \
  "v_mfma_f32_16x16x32_bf16 v[28:31], a[92:95], a[48:51], v[28:31]\n\t" \
  "v_mfma_f32_16x16x32_bf16 v[60:63], a[92:95], a[52:55], v[60:63]\n\t" \
  "v_mfma_f32_16x16x32_bf16 v[60:63], a[156:159], a[36:39], v[60:63]\n\t" \
  "v_mfma_f32_16x16x32_bf16 v[92:95], a[156:159], a[40:43], v[92:95]\n\t" \
  "v_mfma_f32_16x16x32_bf16 v[92:95], a[92:95], a[56:59], v[92:95]\n\t" \
  "v_mfma_f32_16x16x32_bf16 v[124:127], a[92:95], a[60:63], v[124:127]\n\t" \
  "s_waitcnt vmcnt(32) lgkmcnt(0)\n\t" \
  "v_mfma_f32_16x16x32_bf16 v[124:127], a[156:159], a[44:47], v[124:127]\n\t" \
  "s_barrier\n\t" \
  "v_mfma_f32_16x16x32_bf16 v[0:3], a[160:163], a[192:195], v[0:3]\n\t" \
  "ds_read_b64_tr_b16 a[128:129], v184 offset:16384\n\t" \
  "v_mfma_f32_16x16x32_bf16 v[0:3], a[96:99], a[208:211], v[0:3]\n\t" \
  "ds_read_b64_tr_b16 a[130:131], v184 offset:17408\n\t" \
  "v_mfma_f32_16x16x32_bf16 v[32:35], a[96:99], a[212:215], v[32:35]\n\t" \
  "ds_read_b64_tr_b16 a[132:133], v185 offset:16384\n\t" \
  "v_mfma_f32_16x16x32_bf16 v[32:35], a[160:163], a[196:199], v[32:35]\n\t" \
  "ds_read_b64_tr_b16 a[134:135], v185 offset:17408\n\t" \
  "v_mfma_f32_16x16x32_bf16 v[64:67], a[160:163], a[200:203], v[64:67]\n\t" \
  "ds_read_b64_tr_b16 a[136:137], v186 offset:16384\n\t" \
  "v_mfma_f32_16x16x32_bf16 v[64:67], a[96:99], a[216:219], v[64:67]\n\t" \
  "ds_read_b64_tr_b16 a[138:139], v186 offset:17408\n\t" \
  "v_mfma_f32_16x16x32_bf16 v[96:99], a[96:99], a[220:223], v[96:99]\n\t" \
  "ds_read_b64_tr_b16 a[140:141], v187 offset:16384\n\t" \
  "v_mfma_f32_16x16x32_bf16 v[96:99], a[160:163], a[204:207], v[96:99]\n\t" \
  "ds_read_b64_tr_b16 a[142:143], v187 offset:17408\n\t" \
  "v_mfma_f32_16x16x32_bf16 v[4:7], a[164:167], a[192:195], v[4:7]\n\t" \
  "ds_read_b64_tr_b16 a[144:145], v188 offset:16384\n\t" \
  "v_mfma_f32_16x16x32_bf16 v[4:7], a[100:103], a[208:211], v[4:7]\n\t" \
  "ds_read_b64_tr_b16 a[146:147], v188 offset:17408\n\t" \
  "v_mfma_f32_16x16x32_bf16 v[36:39], a[100:103], a[212:215], v[36:39]\n\t" \
  "ds_read_b64_tr_b16 a[148:149], v189 offset:16384\n\t" \
  "v_mfma_f32_16x16x32_bf16 v[36:39], a[164:167], a[196:199], v[36:39]\n\t" \
  "ds_read_b64_tr_b16 a[150:151], v189 offset:17408\n\t" \
  "v_mfma_f32_16x16x32_bf16 v[68:71], a[164:167], a[200:203], v[68:71]\n\t" \
  "ds_read_b64_tr_b16 a[152:153], v190 offset:16384\n\t" \
  "v_mfma_f32_16x16x32_bf16 v[68:71], a[100:103], a[216:219], v[68:71]\n\t" \
  "ds_read_b64_tr_b16 a[154:155], v190 offset:17408\n\t" \
  "v_mfma_f32_16x16x32_bf16 v[100:103], a[100:103], a[220:223], v[100:103]\n\t" \
  "ds_read_b64_tr_b16 a[156:157], v191 offset:16384\n\t" \
  "v_mfma_f32_16x16x32_bf16 v[100:103], a[164:167], a[204:207], v[100:103]\n\t" \
  "ds_read_b64_tr_b16 a[158:159], v191 offset:17408\n\t" \
  "v_mfma_f32_16x16x32_bf16 v[8:11], a[168:171], a[192:195], v[8:11]\n\t" \
  "ds_read_b64_tr_b16 a[64:65], v184 offset:24576\n\t" \
  "v_mfma_f32_16x16x32_bf16 v[8:11], a[104:107], a[208:211], v[8:11]\n\t" \
  "v_mfma_f32_16x16x32_bf16 v[40:43], a[104:107], a[212:215], v[40:43]\n\t" \
  "ds_read_b64_tr_b16 a[66:67], v184 offset:25600\n\t" \
  "v_mfma_f32_16x16x32_bf16 v[40:43], a[168:171], a[196:199], v[40:43]\n\t" \
  "v_mfma_f32_16x16x32_bf16 v[72:75], a[168:171], a[200:203], v[72:75]\n\t" \
  "ds_read_b64_tr_b16 a[68:69], v185 offset:24576\n\t" \
  "v_mfma_f32_16x16x32_bf16 v[72:75], a[104:107], a[216:219], v[72:75]\n\t" \
  "ds_read_b64_tr_b16 a[70:71], v185 offset:25600\n\t" \
  "v_mfma_f32_16x16x32_bf16 v[104:107], a[104:107], a[220:223], v[104:107]\n\t" \
  "v_mfma_f32_16x16x32_bf16 v[104:107], a[168:171], a[204:207], v[104:107]\n\t" \
  "ds_read_b64_tr_b16 a[72:73], v186 offset:24576\n\t" \
  "v_mfma_f32_16x16x32_bf16 v[12:15], a[172:175], a[192:195], v[12:15]\n\t" \
  "v_mfma_f32_16x16x32_bf16 v[12:15], a[108:111], a[208:211], v[12:15]\n\t" \
  "ds_read_b64_tr_b16 a[74:75], v186 offset:25600\n\t" \
  "v_mfma_f32_16x16x32_bf16 v[44:47], a[108:111], a[212:215], v[44:47]\n\t" \
  "ds_read_b64_tr_b16 a[76:77], v187 offset:24576\n\t" \
  "v_mfma_f32_16x16x32_bf16 v[44:47], a[172:175], a[196:199], v[44:47]\n\t" \
  ".Lhs4:\n\ts_cbranch_execz .Lhs4n\n\t" \
  "v_mfma_f32_16x16x32_bf16 v[76:79], a[172:175], a[200:203], v[76:79]\n\t" \
  "ds_read_b64_tr_b16 a[78:79], v187 offset:25600\n\t" \
  "v_mfma_f32_16x16x32_bf16 v[76:79], a[108:111], a[216:219], v[76:79]\n\t" \
  "s_add_u32 s97, s78, 6815744\n\t" \
  "ds_read_b64_tr_b16 a[80:81], v188 offset:24576\n\t" \
  "s_bfe_u32 s97, s97, 0x140003\n\t" \
  "v_mfma_f32_16x16x32_bf16 v[108:111], a[108:111], a[220:223], v[108:111]\n\t" \
  "s_add_u32 s97, s97, s82\n\t" \
  "v_mfma_f32_16x16x32_bf16 v[108:111], a[172:175], a[204:207], v[108:111]\n\t" \
  "buffer_load_dwordx4 a[32:35], v248, s[16:19], s97 offen sc1\n\t" \
  "v_mfma_f32_16x16x32_bf16 v[16:19], a[176:179], a[192:195], v[16:19]\n\t" \
  "ds_read_b64_tr_b16 a[82:83], v188 offset:25600\n\t" \
  "v_mfma_f32_16x16x32_bf16 v[16:19], a[112:115], a[208:211], v[16:19]\n\t" \
  "ds_read_b64_tr_b16 a[84:85], v189 offset:24576\n\t" \
  "v_mfma_f32_16x16x32_bf16 v[48:51], a[112:115], a[212:215], v[48:51]\n\t" \
  "buffer_load_dwordx4 a[36:39], v249, s[16:19], s97 offen sc1\n\t" \
  "v_mfma_f32_16x16x32_bf16 v[48:51], a[176:179], a[196:199], v[48:51]\n\t" \
  "ds_read_b64_tr_b16 a[86:87], v189 offset:25600\n\t" \
  "v_mfma_f32_16x16x32_bf16 v[80:83], a[176:179], a[200:203], v[80:83]\n\t" \
  "ds_read_b64_tr_b16 a[88:89], v190 offset:24576\n\t" \
  "v_mfma_f32_16x16x32_bf16 v[80:83], a[112:115], a[216:219], v[80:83]\n\t" \
  "v_mfma_f32_16x16x32_bf16 v[112:115], a[112:115], a[220:223], v[112:115]\n\t" \
  "buffer_load_dwordx4 a[40:43], v250, s[16:19], s97 offen sc1\n\t" \
  "v_mfma_f32_16x16x32_bf16 v[112:115], a[176:179], a[204:207], v[112:115]\n\t" \
  "ds_read_b64_tr_b16 a[90:91], v190 offset:25600\n\t" \
  "v_mfma_f32_16x16x32_bf16 v[20:23], a[180:183], a[192:195], v[20:23]\n\t" \
  "ds_read_b64_tr_b16 a[92:93], v191 offset:24576\n\t" \
  "v_mfma_f32_16x16x32_bf16 v[20:23], a[116:119], a[208:211], v[20:23]\n\t" \
  "ds_read_b64_tr_b16 a[94:95], v191 offset:25600\n\t" \
  "v_mfma_f32_16x16x32_bf16 v[52:55], a[116:119], a[212:215], v[52:55]\n\t" \
  "buffer_load_dwordx4 a[44:47], v251, s[16:19], s97 offen sc1\n\t" \
  "v_mfma_f32_16x16x32_bf16 v[52:55], a[180:183], a[196:199], v[52:55]\n\t" \
  "s_add_u32 s96, s78, 7864320\n\t" \
  "s_and_b32 s96, s96, 0x780000\n\t" \
  "s_add_u32 m0, s76, 98304\n\t" \
  "v_mfma_f32_16x16x32_bf16 v[84:87], a[180:183], a[200:203], v[84:87]\n\t" \
  "v_mfma_f32_16x16x32_bf16 v[84:87], a[116:119], a[216:219], v[84:87]\n\t" \
  "v_mfma_f32_16x16x32_bf16 v[116:119], a[116:119], a[220:223], v[116:119]\n\t" \
  "buffer_load_dwordx4 v200, s[20:23], s96 offen offset:0 lds\n\t" \
  "v_mfma_f32_16x16x32_bf16 v[116:119], a[180:183], a[204:207], v[116:119]\n\t" \
  "v_mfma_f32_16x16x32_bf16 v[24:27], a[184:187], a[192:195], v[24:27]\n\t" \
  "v_mfma_f32_16x16x32_bf16 v[24:27], a[120:123], a[208:211], v[24:27]\n\t" \
  "buffer_load_dwordx4 v201, s[20:23], s96 offen offset:1024 lds\n\t" \
  "v_mfma_f32_16x16x32_bf16 v[56:59], a[120:123], a[212:215], v[56:59]\n\t" \
  "v_mfma_f32_16x16x32_bf16 v[56:59], a[184:187], a[196:199], v[56:59]\n\t" \
  "v_mfma_f32_16x16x32_bf16 v[88:91], a[184:187], a[200:203], v[88:91]\n\t" \
  "v_mfma_f32_16x16x32_bf16 v[88:91], a[120:123], a[216:219], v[88:91]\n\t" \
  "buffer_load_dwordx4 v202, s[20:23], s96 offen offset:2048 lds\n\t" \
  "v_mfma_f32_16x16x32_bf16 v[120:123], a[120:123], a[220:223], v[120:123]\n\t" \
  "v_mfma_f32_16x16x32_bf16 v[120:123], a[184:187], a[204:207], v[120:123]\n\t" \
  "v_mfma_f32_16x16x32_bf16 v[28:31], a[188:191], a[192:195], v[28:31]\n\t" \
  "v_mfma_f32_16x16x32_bf16 v[28:31], a[124:127], a[208:211], v[28:31]\n\t" \
  "buffer_load_dwordx4 v203, s[20:23], s96 offen offset:3072 lds\n\t" \
  "v_mfma_f32_16x16x32_bf16 v[60:63], a[124:127], a[212:215], v[60:63]\n\t" \
  "v_mfma_f32_16x16x32_bf16 v[60:63], a[188:191], a[196:199], v[60:63]\n\t" \
  "v_mfma_f32_16x16x32_bf16 v[92:95], a[188:191], a[200:203], v[92:95]\n\t" \
  "v_mfma_f32_16x16x32_bf16 v[92:95], a[124:127], a[216:219], v[92:95]\n\t" \
  "s_add_u32 s97, s97, s83\n\t" \
  "v_mfma_f32_16x16x32_bf16 v[124:127], a[124:127], a[220:223], v[124:127]\n\t" \
  "s_waitcnt vmcnt(24) lgkmcnt(0)\n\t" \
  "v_mfma_f32_16x16x32_bf16 v[124:127], a[188:191], a[204:207], v[124:127]\n\t" \
  "s_barrier\n\t" \
  "v_mfma_f32_16x16x32_bf16 v[0:3], a[128:131], a[224:227], v[0:3]\n\t" \
  "buffer_load_dwordx4 a[48:51], v248, s[16:19], s97 offen sc1\n\t" \
  "v_mfma_f32_16x16x32_bf16 v[0:3], a[64:67], a[240:243], v[0:3]\n\t" \
  "ds_read_b64_tr_b16 a[160:161], v184 offset:32768\n\t" \
  "v_mfma_f32_16x16x32_bf16 v[32:35], a[64:67], a[244:247], v[32:35]\n\t" \
  "ds_read_b64_tr_b16 a[162:163], v184 offset:33792\n\t" \
  "v_mfma_f32_16x16x32_bf16 v[32:35], a[128:131], a[228:231], v[32:35]\n\t" \
  "buffer_load_dwordx4 a[52:55], v249, s[16:19], s97 offen sc1\n\t" \
  "v_mfma_f32_16x16x32_bf16 v[64:67], a[128:131], a[232:235], v[64:67]\n\t" \
  "ds_read_b64_tr_b16 a[164:165], v185 offset:32768\n\t" \
  "v_mfma_f32_16x16x32_bf16 v[64:67], a[64:67], a[248:251], v[64:67]\n\t" \
  "ds_read_b64_tr_b16 a[166:167], v185 offset:33792\n\t" \
  "v_mfma_f32_16x16x32_bf16 v[96:99], a[64:67], a[252:255], v[96:99]\n\t" \
  "buffer_load_dwordx4 a[56:59], v250, s[16:19], s97 offen sc1\n\t" \
  "v_mfma_f32_16x16x32_bf16 v[96:99], a[128:131], a[236:239], v[96:99]\n\t" \
  "ds_read_b64_tr_b16 a[168:169], v186 offset:32768\n\t" \
  "v_mfma_f32_16x16x32_bf16 v[4:7], a[132:135], a[224:227], v[4:7]\n\t" \
  "ds_read_b64_tr_b16 a[170:171], v186 offset:33792\n\t" \
  "v_mfma_f32_16x16x32_bf16 v[4:7], a[68:71], a[240:243], v[4:7]\n\t" \
  "ds_read_b64_tr_b16 a[172:173], v187 offset:32768\n\t" \
  "v_mfma_f32_16x16x32_bf16 v[36:39], a[68:71], a[244:247], v[36:39]\n\t" \
  "buffer_load_dwordx4 a[60:63], v251, s[16:19], s97 offen sc1\n\t" \
  "v_mfma_f32_16x16x32_bf16 v[36:39], a[132:135], a[228:231], v[36:39]\n\t" \
  "s_add_u32 s97, s78, 7340032\n\t" \
  "ds_read_b64_tr_b16 a[174:175], v187 offset:33792\n\t" \
  "s_bfe_u32 s97, s97, 0x140003\n\t" \
  "v_mfma_f32_16x16x32_bf16 v[68:71], a[132:135], a[232:235], v[68:71]\n\t" \
  "ds_read_b64_tr_b16 a[176:177], v188 offset:32768\n\t" \
  "s_add_u32 s97, s97, s82\n\t" \
  "v_mfma_f32_16x16x32_bf16 v[68:71], a[68:71], a[248:251], v[68:71]\n\t" \
  "ds_read_b64_tr_b16 a[178:179], v188 offset:33792\n\t" \
  "v_mfma_f32_16x16x32_bf16 v[100:103], a[68:71], a[252:255], v[100:103]\n\t" \
  "buffer_load_dwordx4 a[192:195], v248, s[16:19], s97 offen sc1\n\t" \
  "v_mfma_f32_16x16x32_bf16 v[100:103], a[132:135], a[236:239], v[100:103]\n\t" \
  "ds_read_b64_tr_b16 a[180:181], v189 offset:32768\n\t" \
  "v_mfma_f32_16x16x32_bf16 v[8:11], a[136:139], a[224:227], v[8:11]\n\t" \
  "ds_read_b64_tr_b16 a[182:183], v189 offset:33792\n\t" \
  "v_mfma_f32_16x16x32_bf16 v[8:11], a[72:75], a[240:243], v[8:11]\n\t" \
  "buffer_load_dwordx4 a[196:199], v249, s[16:19], s97 offen sc1\n\t" \
  "v_mfma_f32_16x16x32_bf16 v[40:43], a[72:75], a[244:247], v[40:43]\n\t" \
  "ds_read_b64_tr_b16 a[184:185], v190 offset:32768\n\t" \
  "v_mfma_f32_16x16x32_bf16 v[40:43], a[136:139], a[228:231], v[40:43]\n\t" \
  "ds_read_b64_tr_b16 a[186:187], v190 offset:33792\n\t" \
  "v_mfma_f32_16x16x32_bf16 v[72:75], a[136:139], a[232:235], v[72:75]\n\t" \
  "ds_read_b64_tr_b16 a[188:189], v191 offset:32768\n\t" \
  "v_mfma_f32_16x16x32_bf16 v[72:75], a[72:75], a[248:251], v[72:75]\n\t" \
  "buffer_load_dwordx4 a[200:203], v250, s[16:19], s97 offen sc1\n\t" \
  "v_mfma_f32_16x16x32_bf16 v[104:107], a[72:75], a[252:255], v[104:107]\n\t" \
  "ds_read_b64_tr_b16 a[190:191], v191 offset:33792\n\t" \
  "v_mfma_f32_16x16x32_bf16 v[104:107], a[136:139], a[236:239], v[104:107]\n\t" \
  "ds_read_b64_tr_b16 a[96:97], v184 offset:40960\n\t" \
  "v_mfma_f32_16x16x32_bf16 v[12:15], a[140:143], a[224:227], v[12:15]\n\t" \
  "ds_read_b64_tr_b16 a[98:99], v184 offset:41984\n\t" \
  "v_mfma_f32_16x16x32_bf16 v[12:15], a[76:79], a[240:243], v[12:15]\n\t" \
  "buffer_load_dwordx4 a[204:207], v251, s[16:19], s97 offen sc1\n\t" \
  "v_mfma_f32_16x16x32_bf16 v[44:47], a[76:79], a[244:247], v[44:47]\n\t" \
  "ds_read_b64_tr_b16 a[100:101], v185 offset:40960\n\t" \
  "v_mfma_f32_16x16x32_bf16 v[44:47], a[140:143], a[228:231], v[44:47]\n\t" \
  "ds_read_b64_tr_b16 a[102:103], v185 offset:41984\n\t" \
  "v_mfma_f32_16x16x32_bf16 v[76:79], a[140:143], a[232:235], v[76:79]\n\t" \
  "ds_read_b64_tr_b16 a[104:105], v186 offset:40960\n\t" \
  "v_mfma_f32_16x16x32_bf16 v[76:79], a[76:79], a[248:251], v[76:79]\n\t" \
  "ds_read_b64_tr_b16 a[106:107], v186 offset:41984\n\t" \
  "s_add_u32 s97, s97, s83\n\t" \
  "v_mfma_f32_16x16x32_bf16 v[108:111], a[76:79], a[252:255], v[108:111]\n\t" \
  "ds_read_b64_tr_b16 a[108:109], v187 offset:40960\n\t" \
  "v_mfma_f32_16x16x32_bf16 v[108:111], a[140:143], a[236:239], v[108:111]\n\t" \
  "buffer_load_dwordx4 a[208:211], v248, s[16:19], s97 offen sc1\n\t" \
  "v_mfma_f32_16x16x32_bf16 v[16:19], a[144:147], a[224:227], v[16:19]\n\t" \
  "ds_read_b64_tr_b16 a[110:111], v187 offset:41984\n\t" \
  "v_mfma_f32_16x16x32_bf16 v[16:19], a[80:83], a[240:243], v[16:19]\n\t" \
  "ds_read_b64_tr_b16 a[112:113], v188 offset:40960\n\t" \
  "v_mfma_f32_16x16x32_bf16 v[48:51], a[80:83], a[244:247], v[48:51]\n\t" \
  "s_waitcnt vmcnt(20)\n\t" \
  "v_lshlrev_b32 v128, 16, v152\n\t" \
  "v_and_b32 v152, 0xffff0000, v152\n\t" \
  "v_mfma_f32_16x16x32_bf16 v[48:51], a[144:147], a[228:231], v[48:51]\n\t" \
  "v_fma_f32 v176, v128, v172, v176\n\t" \
  "v_fma_f32 v180, v152, v172, v180\n\t" \
  "v_mfma_f32_16x16x32_bf16 v[80:83], a[144:147], a[232:235], v[80:83]\n\t" \
  "v_lshlrev_b32 v128, 16, v153\n\t" \
  "v_and_b32 v153, 0xffff0000, v153\n\t" \
  "v_mfma_f32_16x16x32_bf16 v[80:83], a[80:83], a[248:251], v[80:83]\n\t" \
  "v_fma_f32 v177, v128, v172, v177\n\t" \
  "v_fma_f32 v181, v153, v172, v181\n\t" \
  "v_mfma_f32_16x16x32_bf16 v[112:115], a[80:83], a[252:255], v[112:115]\n\t" \
  "v_lshlrev_b32 v128, 16, v154\n\t" \
  "v_and_b32 v154, 0xffff0000, v154\n\t" \
  "v_mfma_f32_16x16x32_bf16 v[112:115], a[144:147], a[236:239], v[112:115]\n\t" \
  "v_fma_f32 v178, v128, v172, v178\n\t" \
  "v_fma_f32 v182, v154, v172, v182\n\t" \
  "v_mfma_f32_16x16x32_bf16 v[20:23], a[148:151], a[224:227], v[20:23]\n\t" \
  "buffer_load_dwordx4 a[212:215], v249, s[16:19], s97 offen sc1\n\t" \
  "v_mfma_f32_16x16x32_bf16 v[20:23], a[84:87], a[240:243], v[20:23]\n\t" \
  "ds_read_b64_tr_b16 a[114:115], v188 offset:41984\n\t" \
  "v_lshlrev_b32 v128, 16, v155\n\t" \
  "v_mfma_f32_16x16x32_bf16 v[52:55], a[84:87], a[244:247], v[52:55]\n\t" \
  "v_and_b32 v155, 0xffff0000, v155\n\t" \
  "v_fma_f32 v179, v128, v172, v179\n\t" \
  "v_mfma_f32_16x16x32_bf16 v[52:55], a[148:151], a[228:231], v[52:55]\n\t" \
  "ds_read_b64_tr_b16 a[116:117], v189 offset:40960\n\t" \
  "v_fma_f32 v183, v155, v172, v183\n\t" \
  "v_mfma_f32_16x16x32_bf16 v[84:87], a[148:151], a[232:235], v[84:87]\n\t" \
  "s_waitcnt vmcnt(20)\n\t" \
  "v_lshlrev_b32 v128, 16, v156\n\t" \
  "v_and_b32 v156, 0xffff0000, v156\n\t" \
  "v_mfma_f32_16x16x32_bf16 v[84:87], a[84:87], a[248:251], v[84:87]\n\t" \
  "ds_read_b64_tr_b16 a[118:119], v189 offset:41984\n\t" \
  "v_fma_f32 v176, v128, v173, v176\n\t" \
  "v_mfma_f32_16x16x32_bf16 v[116:119], a[84:87], a[252:255], v[116:119]\n\t" \
  "ds_read_b64_tr_b16 a[120:121], v190 offset:40960\n\t" \
  "v_fma_f32 v180, v156, v173, v180\n\t" \
  "v_mfma_f32_16x16x32_bf16 v[116:119], a[148:151], a[236:239], v[116:119]\n\t" \
  "buffer_load_dwordx4 a[216:219], v250, s[16:19], s97 offen sc1\n\t" \
  "v_mfma_f32_16x16x32_bf16 v[24:27], a[152:155], a[224:227], v[24:27]\n\t" \
  "ds_read_b64_tr_b16 a[122:123], v190 offset:41984\n\t" \
  "v_lshlrev_b32 v128, 16, v157\n\t" \
  "v_mfma_f32_16x16x32_bf16 v[24:27], a[88:91], a[240:243], v[24:27]\n\t" \
  "ds_read_b64_tr_b16 a[124:125], v191 offset:40960\n\t" \
  "v_and_b32 v157, 0xffff0000, v157\n\t" \
  "v_mfma_f32_16x16x32_bf16 v[56:59], a[88:91], a[244:247], v[56:59]\n\t" \
  "v_fma_f32 v177, v128, v173, v177\n\t" \
  "v_fma_f32 v181, v157, v173, v181\n\t" \
  "v_mfma_f32_16x16x32_bf16 v[56:59], a[152:155], a[228:231], v[56:59]\n\t" \
  "ds_read_b64_tr_b16 a[126:127], v191 offset:41984\n\t" \
  "v_lshlrev_b32 v128, 16, v158\n\t" \
  "v_mfma_f32_16x16x32_bf16 v[88:91], a[152:155], a[232:235], v[88:91]\n\t" \
  "v_and_b32 v158, 0xffff0000, v158\n\t" \
  "v_fma_f32 v178, v128, v173, v178\n\t" \
  "v_mfma_f32_16x16x32_bf16 v[88:91], a[88:91], a[248:251], v[88:91]\n\t" \
  "v_fma_f32 v182, v158, v173, v182\n\t" \
  "v_lshlrev_b32 v128, 16, v159\n\t" \
  "v_mfma_f32_16x16x32_bf16 v[120:123], a[88:91], a[252:255], v[120:123]\n\t" \
  "v_and_b32 v159, 0xffff0000, v159\n\t" \
  "v_fma_f32 v179, v128, v173, v179\n\t" \
  "v_mfma_f32_16x16x32_bf16 v[120:123], a[152:155], a[236:239], v[120:123]\n\t" \
  "buffer_load_dwordx4 a[220:223], v251, s[16:19], s97 offen sc1\n\t" \
  "v_mfma_f32_16x16x32_bf16 v[28:31], a[156:159], a[224:227], v[28:31]\n\t" \
  "v_fma_f32 v183, v159, v173, v183\n\t" \
  "v_mfma_f32_16x16x32_bf16 v[28:31], a[92:95], a[240:243], v[28:31]\n\t" \
  "s_waitcnt vmcnt(21)\n\t" \
  "v_lshlrev_b32 v128, 16, v160\n\t" \
  "v_and_b32 v160, 0xffff0000, v160\n\t" \
  "v_mfma_f32_16x16x32_bf16 v[60:63], a[92:95], a[244:247], v[60:63]\n\t" \
  "v_fma_f32 v176, v128, v174, v176\n\t" \
  "v_fma_f32 v180, v160, v174, v180\n\t" \
  "v_mfma_f32_16x16x32_bf16 v[60:63], a[156:159], a[228:231], v[60:63]\n\t" \
  "v_lshlrev_b32 v128, 16, v161\n\t" \
  "v_and_b32 v161, 0xffff0000, v161\n\t" \
  "v_mfma_f32_16x16x32_bf16 v[92:95], a[156:159], a[232:235], v[92:95]\n\t" \
  "v_fma_f32 v177, v128, v174, v177\n\t" \
  "v_fma_f32 v181, v161, v174, v181\n\t" \
  "v_mfma_f32_16x16x32_bf16 v[92:95], a[92:95], a[248:251], v[92:95]\n\t" \
  "s_add_u32 s97, s78, 7864320\n\t" \
  "v_lshlrev_b32 v128, 16, v162\n\t" \
  "v_and_b32 v162, 0xffff0000, v162\n\t" \
  "v_mfma_f32_16x16x32_bf16 v[124:127], a[92:95], a[252:255], v[124:127]\n\t" \
  "s_waitcnt lgkmcnt(0)\n\t" \
  "s_bfe_u32 s97, s97, 0x140003\n\t" \
  "s_add_u32 s97, s97, s82\n\t" \
  "v_mfma_f32_16x16x32_bf16 v[124:127], a[156:159], a[236:239], v[124:127]\n\t" \
  "s_barrier\n\t" \
  "v_mfma_f32_16x16x32_bf16 v[0:3], a[160:163], a[0:3], v[0:3]\n\t" \
  "buffer_load_dwordx4 a[224:227], v248, s[16:19], s97 offen sc1\n\t" \
  "v_mfma_f32_16x16x32_bf16 v[0:3], a[96:99], a[16:19], v[0:3]\n\t" \
  "ds_read_b64_tr_b16 a[128:129], v192 offset:0\n\t" \
  "v_fma_f32 v178, v128, v174, v178\n\t" \
  "v_mfma_f32_16x16x32_bf16 v[32:35], a[96:99], a[20:23], v[32:35]\n\t" \
  "ds_read_b64_tr_b16 a[130:131], v192 offset:1024\n\t" \
  "v_fma_f32 v182, v162, v174, v182\n\t" \
  "v_mfma_f32_16x16x32_bf16 v[32:35], a[160:163], a[4:7], v[32:35]\n\t" \
  "v_lshlrev_b32 v128, 16, v163\n\t" \
  "v_and_b32 v163, 0xffff0000, v163\n\t" \
  "v_mfma_f32_16x16x32_bf16 v[64:67], a[160:163], a[8:11], v[64:67]\n\t" \
  "ds_read_b64_tr_b16 a[132:133], v193 offset:0\n\t" \
  "v_fma_f32 v179, v128, v174, v179\n\t" \
  "v_mfma_f32_16x16x32_bf16 v[64:67], a[96:99], a[24:27], v[64:67]\n\t" \
  "ds_read_b64_tr_b16 a[134:135], v193 offset:1024\n\t" \
  "v_fma_f32 v183, v163, v174, v183\n\t" \
  "v_mfma_f32_16x16x32_bf16 v[96:99], a[96:99], a[28:31], v[96:99]\n\t" \
  "s_waitcnt vmcnt(21)\n\t" \
  "v_lshlrev_b32 v128, 16, v164\n\t" \
  "v_and_b32 v164, 0xffff0000, v164\n\t" \
  "v_mfma_f32_16x16x32_bf16 v[96:99], a[160:163], a[12:15], v[96:99]\n\t" \
  "ds_read_b64_tr_b16 a[136:137], v194 offset:0\n\t" \
  "v_fma_f32 v176, v128, v175, v176\n\t" \
  "v_mfma_f32_16x16x32_bf16 v[4:7], a[164:167], a[0:3], v[4:7]\n\t" \
  "buffer_load_dwordx4 a[228:231], v249, s[16:19], s97 offen sc1\n\t" \
  "v_mfma_f32_16x16x32_bf16 v[4:7], a[100:103], a[16:19], v[4:7]\n\t" \
  "ds_read_b64_tr_b16 a[138:139], v194 offset:1024\n\t" \
  "v_fma_f32 v180, v164, v175, v180\n\t" \
  "v_mfma_f32_16x16x32_bf16 v[36:39], a[100:103], a[20:23], v[36:39]\n\t" \
  "ds_read_b64_tr_b16 a[140:141], v195 offset:0\n\t" \
  "v_lshlrev_b32 v128, 16, v165\n\t" \
  "v_mfma_f32_16x16x32_bf16 v[36:39], a[164:167], a[4:7], v[36:39]\n\t" \
  "v_and_b32 v165, 0xffff0000, v165\n\t" \
  "v_fma_f32 v177, v128, v175, v177\n\t" \
  "v_mfma_f32_16x16x32_bf16 v[68:71], a[164:167], a[8:11], v[68:71]\n\t" \
  "ds_read_b64_tr_b16 a[142:143], v195 offset:1024\n\t" \
  "v_fma_f32 v181, v165, v175, v181\n\t" \
  "v_mfma_f32_16x16x32_bf16 v[68:71], a[100:103], a[24:27], v[68:71]\n\t" \
  "ds_read_b64_tr_b16 a[144:145], v196 offset:0\n\t" \
  "v_lshlrev_b32 v128, 16, v166\n\t" \
  "v_mfma_f32_16x16x32_bf16 v[100:103], a[100:103], a[28:31], v[100:103]\n\t" \
  "v_and_b32 v166, 0xffff0000, v166\n\t" \
  "v_fma_f32 v178, v128, v175, v178\n\t" \
  "v_mfma_f32_16x16x32_bf16 v[100:103], a[164:167], a[12:15], v[100:103]\n\t" \
  "buffer_load_dwordx4 a[232:235], v250, s[16:19], s97 offen sc1\n\t" \
  "v_mfma_f32_16x16x32_bf16 v[8:11], a[168:171], a[0:3], v[8:11]\n\t" \
  "ds_read_b64_tr_b16 a[146:147], v196 offset:1024\n\t" \
  "v_fma_f32 v182, v166, v175, v182\n\t" \
  "v_mfma_f32_16x16x32_bf16 v[8:11], a[104:107], a[16:19], v[8:11]\n\t" \
  "ds_read_b64_tr_b16 a[148:149], v197 offset:0\n\t" \
  "v_lshlrev_b32 v128, 16, v167\n\t" \
  "v_mfma_f32_16x16x32_bf16 v[40:43], a[104:107], a[20:23], v[40:43]\n\t" \
  "ds_read_b64_tr_b16 a[150:151], v197 offset:1024\n\t" \
  "v_and_b32 v167, 0xffff0000, v167\n\t" \
  "v_mfma_f32_16x16x32_bf16 v[40:43], a[168:171], a[4:7], v[40:43]\n\t" \
  "v_fma_f32 v179, v128, v175, v179\n\t" \
  "v_fma_f32 v183, v167, v175, v183\n\t" \
  "v_mfma_f32_16x16x32_bf16 v[72:75], a[168:171], a[8:11], v[72:75]\n\t" \
  "ds_read_b64_tr_b16 a[152:153], v198 offset:0\n\t" \
  "v_and_b32 v138, 7, v233\n\t" \
  "v_mfma_f32_16x16x32_bf16 v[72:75], a[104:107], a[24:27], v[72:75]\n\t" \
  "ds_read_b64_tr_b16 a[154:155], v198 offset:1024\n\t" \
  "v_lshlrev_b32 v138, 5, v138\n\t" \
  "v_mfma_f32_16x16x32_bf16 v[104:107], a[104:107], a[28:31], v[104:107]\n\t" \
  "v_permlane32_swap_b32 v176, v178\n\t" \
  "v_mfma_f32_16x16x32_bf16 v[104:107], a[168:171], a[12:15], v[104:107]\n\t" \
  "buffer_load_dwordx4 a[236:239], v251, s[16:19], s97 offen sc1\n\t" \
  "v_mfma_f32_16x16x32_bf16 v[12:15], a[172:175], a[0:3], v[12:15]\n\t" \
  "v_permlane32_swap_b32 v180, v182\n\t" \
  "v_mfma_f32_16x16x32_bf16 v[12:15], a[108:111], a[16:19], v[12:15]\n\t" \
  "ds_read_b64_tr_b16 a[156:157], v199 offset:0\n\t" \
  "v_mfma_f32_16x16x32_bf16 v[44:47], a[108:111], a[20:23], v[44:47]\n\t" \
  "ds_read_b64_tr_b16 a[158:159], v199 offset:1024\n\t" \
  "v_mfma_f32_16x16x32_bf16 v[44:47], a[172:175], a[4:7], v[44:47]\n\t" \
  "v_permlane32_swap_b32 v177, v179\n\t" \
  "v_mfma_f32_16x16x32_bf16 v[76:79], a[172:175], a[8:11], v[76:79]\n\t" \
  "ds_read_b64_tr_b16 a[64:65], v192 offset:8192\n\t" \
  "v_mfma_f32_16x16x32_bf16 v[76:79], a[108:111], a[24:27], v[76:79]\n\t" \
  "s_add_u32 s97, s97, s83\n\t" \
  "v_permlane32_swap_b32 v181, v183\n\t" \
  "v_mfma_f32_16x16x32_bf16 v[108:111], a[108:111], a[28:31], v[108:111]\n\t" \
  "ds_read_b64_tr_b16 a[66:67], v192 offset:9216\n\t" \
  "v_add_f32 v176, v176, v178\n\t" \
  "v_mfma_f32_16x16x32_bf16 v[108:111], a[172:175], a[12:15], v[108:111]\n\t" \
  "ds_read_b64_tr_b16 a[68:69], v193 offset:8192\n\t" \
  "v_add_f32 v177, v177, v179\n\t" \
  "v_mfma_f32_16x16x32_bf16 v[16:19], a[176:179], a[0:3], v[16:19]\n\t" \
  "buffer_load_dwordx4 a[240:243], v248, s[16:19], s97 offen sc1\n\t" \
  "v_mfma_f32_16x16x32_bf16 v[16:19], a[112:115], a[16:19], v[16:19]\n\t" \
  "ds_read_b64_tr_b16 a[70:71], v193 offset:9216\n\t" \
  "v_add_f32 v180, v180, v182\n\t" \
  "v_mfma_f32_16x16x32_bf16 v[48:51], a[112:115], a[20:23], v[48:51]\n\t" \
  "ds_read_b64_tr_b16 a[72:73], v194 offset:8192\n\t" \
  "v_add_f32 v181, v181, v183\n\t" \
  "v_mfma_f32_16x16x32_bf16 v[48:51], a[176:179], a[4:7], v[48:51]\n\t" \
  "ds_read_b64_tr_b16 a[74:75], v194 offset:9216\n\t" \
  "v_lshrrev_b32 v139, 4, v233\n\t" \
  "v_mfma_f32_16x16x32_bf16 v[80:83], a[176:179], a[8:11], v[80:83]\n\t" \
  "v_permlane16_swap_b32 v176, v177\n\t" \
  "v_mfma_f32_16x16x32_bf16 v[80:83], a[112:115], a[24:27], v[80:83]\n\t" \
  "buffer_load_dwordx4 a[244:247], v249, s[16:19], s97 offen sc1\n\t" \
  "v_mfma_f32_16x16x32_bf16 v[112:115], a[112:115], a[28:31], v[112:115]\n\t" \
  "ds_read_b64_tr_b16 a[76:77], v195 offset:8192\n\t" \
  "v_mfma_f32_16x16x32_bf16 v[112:115], a[176:179], a[12:15], v[112:115]\n\t" \
  "ds_read_b64_tr_b16 a[78:79], v195 offset:9216\n\t" \
  "v_mfma_f32_16x16x32_bf16 v[20:23], a[180:183], a[0:3], v[20:23]\n\t" \
  "ds_read_b64_tr_b16 a[80:81], v196 offset:8192\n\t" \
  "v_mfma_f32_16x16x32_bf16 v[20:23], a[116:119], a[16:19], v[20:23]\n\t" \
  "v_permlane16_swap_b32 v180, v181\n\t" \
  "v_mfma_f32_16x16x32_bf16 v[52:55], a[116:119], a[20:23], v[52:55]\n\t" \
  "buffer_load_dwordx4 a[248:251], v250, s[16:19], s97 offen sc1\n\t" \
  "v_mfma_f32_16x16x32_bf16 v[52:55], a[180:183], a[4:7], v[52:55]\n\t" \
  "ds_read_b64_tr_b16 a[82:83], v196 offset:9216\n\t" \
  "v_add_f32 v176, v176, v177\n\t" \
  "v_mfma_f32_16x16x32_bf16 v[84:87], a[180:183], a[8:11], v[84:87]\n\t" \
  "ds_read_b64_tr_b16 a[84:85], v197 offset:8192\n\t" \
  "v_add_f32 v180, v180, v181\n\t" \
  "v_mfma_f32_16x16x32_bf16 v[84:87], a[116:119], a[24:27], v[84:87]\n\t" \
  "ds_read_b64_tr_b16 a[86:87], v197 offset:9216\n\t" \
  "v_lshl_add_u32 v138, v139, 3, v138\n\t" \
  "v_mfma_f32_16x16x32_bf16 v[116:119], a[116:119], a[28:31], v[116:119]\n\t" \
  "ds_read_b64_tr_b16 a[88:89], v198 offset:8192\n\t" \
  "v_and_b32 v139, 0x7fffff00, v219\n\t" \
  "v_mfma_f32_16x16x32_bf16 v[116:119], a[180:183], a[12:15], v[116:119]\n\t" \
  "buffer_load_dwordx4 a[252:255], v251, s[16:19], s97 offen sc1\n\t" \
  "v_mfma_f32_16x16x32_bf16 v[24:27], a[184:187], a[0:3], v[24:27]\n\t" \
  "ds_read_b64_tr_b16 a[90:91], v198 offset:9216\n\t" \
  "v_add_f32_dpp v136, v176, v176 row_ror:8 row_mask:0xf bank_mask:0xf\n\t" \
  "v_mfma_f32_16x16x32_bf16 v[24:27], a[120:123], a[16:19], v[24:27]\n\t" \
  "ds_read_b64_tr_b16 a[92:93], v199 offset:8192\n\t" \
  "v_add_f32_dpp v137, v180, v180 row_ror:8 row_mask:0xf bank_mask:0xf\n\t" \
  "v_mfma_f32_16x16x32_bf16 v[56:59], a[120:123], a[20:23], v[56:59]\n\t" \
  "ds_read_b64_tr_b16 a[94:95], v199 offset:9216\n\t" \
  "v_add_u32 v138, v139, v138\n\t" \
  "v_mfma_f32_16x16x32_bf16 v[56:59], a[184:187], a[4:7], v[56:59]\n\t" \
  "ds_write_b64 v138, v[136:137] offset:1024\n\t" \
  "v_mfma_f32_16x16x32_bf16 v[88:91], a[184:187], a[8:11], v[88:91]\n\t" \
  "buffer_load_dword v246, v217, s[16:19], s58 offen sc1\n\t" \
  "v_mfma_f32_16x16x32_bf16 v[88:91], a[120:123], a[24:27], v[88:91]\n\t" \
  "s_add_u32 s99, s53, 0\n\t" \
  "v_and_b32 v140, 3, v233\n\t" \
  "v_bfe_u32 v141, v233, 4, 1\n\t" \
  "v_mfma_f32_16x16x32_bf16 v[120:123], a[120:123], a[28:31], v[120:123]\n\t" \
  "v_lshl_add_u32 v140, v141, 2, v140\n\t" \
  "v_bfe_u32 v141, v233, 2, 2\n\t" \
  "v_mfma_f32_16x16x32_bf16 v[120:123], a[184:187], a[12:15], v[120:123]\n\t" \
  "v_lshlrev_b32 v141, 8, v141\n\t" \
  "v_lshl_add_u32 v141, v140, 3, v141\n\t" \
  "v_mfma_f32_16x16x32_bf16 v[28:31], a[188:191], a[0:3], v[28:31]\n\t" \
  "v_lshl_add_u32 v141, s3, 6, v141\n\t" \
  "v_lshlrev_b32 v140, 4, v140\n\t" \
  "v_mfma_f32_16x16x32_bf16 v[28:31], a[124:127], a[16:19], v[28:31]\n\t" \
  "buffer_load_dword v246, v245, s[68:71], s99 offen\n\t" \
  "v_mfma_f32_16x16x32_bf16 v[60:63], a[124:127], a[20:23], v[60:63]\n\t" \
  "v_and_b32 v142, 0x2c, v233\n\t" \
  "v_lshl_or_b32 v140, v142, 26, v140\n\t" \
  "v_mfma_f32_16x16x32_bf16 v[60:63], a[188:191], a[4:7], v[60:63]\n\t" \
  "v_add_u32 v141, 151552, v141\n\t" \
  "v_mov_b32 v145, v211\n\t" \
  "v_mfma_f32_16x16x32_bf16 v[92:95], a[188:191], a[8:11], v[92:95]\n\t" \
  "v_mov_b32 v147, v211\n\t" \
  "v_mfma_f32_16x16x32_bf16 v[92:95], a[124:127], a[24:27], v[92:95]\n\t" \
  "s_add_u32 s99, s53, 14336\n\t" \
  "v_mfma_f32_16x16x32_bf16 v[124:127], a[124:127], a[28:31], v[124:127]\n\t" \
  "s_waitcnt vmcnt(18) lgkmcnt(1)\n\t" \
  "v_mfma_f32_16x16x32_bf16 v[124:127], a[188:191], a[12:15], v[124:127]\n\t" \
  "s_barrier\n\t" \
  "v_mfma_f32_16x16x32_bf16 v[0:3], a[128:131], a[32:35], v[0:3]\n\t" \
  "buffer_load_dword v246, v245, s[68:71], s99 offen\n\t" \
  "v_mfma_f32_16x16x32_bf16 v[0:3], a[64:67], a[48:51], v[0:3]\n\t" \
  "ds_read_b64_tr_b16 a[160:161], v192 offset:16384\n\t" \
  "v_mfma_f32_16x16x32_bf16 v[32:35], a[64:67], a[52:55], v[32:35]\n\t" \
  "ds_read_b64_tr_b16 a[162:163], v192 offset:17408\n\t" \
  "v_mfma_f32_16x16x32_bf16 v[32:35], a[128:131], a[36:39], v[32:35]\n\t" \
  "ds_read_b64_tr_b16 a[164:165], v193 offset:16384\n\t" \
  "v_mfma_f32_16x16x32_bf16 v[64:67], a[128:131], a[40:43], v[64:67]\n\t" \
  "ds_read_b64_tr_b16 a[166:167], v193 offset:17408\n\t" \
  "v_mfma_f32_16x16x32_bf16 v[64:67], a[64:67], a[56:59], v[64:67]\n\t" \
  "ds_read_b64_tr_b16 a[168:169], v194 offset:16384\n\t" \
  "v_mfma_f32_16x16x32_bf16 v[96:99], a[64:67], a[60:63], v[96:99]\n\t" \
  "ds_read_b64_tr_b16 a[170:171], v194 offset:17408\n\t" \
  "v_mfma_f32_16x16x32_bf16 v[96:99], a[128:131], a[44:47], v[96:99]\n\t" \
  "ds_read_b64_tr_b16 a[172:173], v195 offset:16384\n\t" \
  "v_mfma_f32_16x16x32_bf16 v[4:7], a[132:135], a[32:35], v[4:7]\n\t" \
  "ds_read_b64_tr_b16 a[174:175], v195 offset:17408\n\t" \
  "v_mfma_f32_16x16x32_bf16 v[4:7], a[68:71], a[48:51], v[4:7]\n\t" \
  "ds_read_b64_tr_b16 a[176:177], v196 offset:16384\n\t" \
  "v_mfma_f32_16x16x32_bf16 v[36:39], a[68:71], a[52:55], v[36:39]\n\t" \
  "ds_read_b64_tr_b16 a[178:179], v196 offset:17408\n\t" \
  "v_mfma_f32_16x16x32_bf16 v[36:39], a[132:135], a[36:39], v[36:39]\n\t" \
  "ds_read_b64_tr_b16 a[180:181], v197 offset:16384\n\t" \
  "v_mfma_f32_16x16x32_bf16 v[68:71], a[132:135], a[40:43], v[68:71]\n\t" \
  "ds_read_b64_tr_b16 a[182:183], v197 offset:17408\n\t" \
  "v_mfma_f32_16x16x32_bf16 v[68:71], a[68:71], a[56:59], v[68:71]\n\t" \
  "ds_read_b64_tr_b16 a[184:185], v198 offset:16384\n\t" \
  "v_mfma_f32_16x16x32_bf16 v[100:103], a[68:71], a[60:63], v[100:103]\n\t" \
  "ds_read_b64_tr_b16 a[186:187], v198 offset:17408\n\t" \
  "v_mfma_f32_16x16x32_bf16 v[100:103], a[132:135], a[44:47], v[100:103]\n\t" \
  "ds_read_b64_tr_b16 a[188:189], v199 offset:16384\n\t" \
  "v_mfma_f32_16x16x32_bf16 v[8:11], a[136:139], a[32:35], v[8:11]\n\t" \
  "ds_read_b64_tr_b16 a[190:191], v199 offset:17408\n\t" \
  "v_mfma_f32_16x16x32_bf16 v[8:11], a[72:75], a[48:51], v[8:11]\n\t" \
  "ds_read_b64_tr_b16 a[96:97], v192 offset:24576\n\t" \
  "v_mfma_f32_16x16x32_bf16 v[40:43], a[72:75], a[52:55], v[40:43]\n\t" \
  "ds_read_b64_tr_b16 a[98:99], v192 offset:25600\n\t" \
  "v_mfma_f32_16x16x32_bf16 v[40:43], a[136:139], a[36:39], v[40:43]\n\t" \
  "v_mfma_f32_16x16x32_bf16 v[72:75], a[136:139], a[40:43], v[72:75]\n\t" \
  "ds_read_b64_tr_b16 a[100:101], v193 offset:24576\n\t" \
  "v_mfma_f32_16x16x32_bf16 v[72:75], a[72:75], a[56:59], v[72:75]\n\t" \
  "ds_read_b64_tr_b16 a[102:103], v193 offset:25600\n\t" \
  "v_mfma_f32_16x16x32_bf16 v[104:107], a[72:75], a[60:63], v[104:107]\n\t" \
  "v_mfma_f32_16x16x32_bf16 v[104:107], a[136:139], a[44:47], v[104:107]\n\t" \
  "ds_read_b64_tr_b16 a[104:105], v194 offset:24576\n\t" \
  "v_mfma_f32_16x16x32_bf16 v[12:15], a[140:143], a[32:35], v[12:15]\n\t" \
  "v_mfma_f32_16x16x32_bf16 v[12:15], a[76:79], a[48:51], v[12:15]\n\t" \
  "ds_read_b64_tr_b16 a[106:107], v194 offset:25600\n\t" \
  "v_mfma_f32_16x16x32_bf16 v[44:47], a[76:79], a[52:55], v[44:47]\n\t" \
  "ds_read_b64_tr_b16 a[108:109], v195 offset:24576\n\t" \
  "v_mfma_f32_16x16x32_bf16 v[44:47], a[140:143], a[36:39], v[44:47]\n\t" \
  "v_mfma_f32_16x16x32_bf16 v[76:79], a[140:143], a[40:43], v[76:79]\n\t" \
  "ds_read_b64_tr_b16 a[110:111], v195 offset:25600\n\t" \
  "v_mfma_f32_16x16x32_bf16 v[76:79], a[76:79], a[56:59], v[76:79]\n\t" \
  "ds_read_b64_tr_b16 a[112:113], v196 offset:24576\n\t" \
  "s_add_u32 m0, s76, 16384\n\t" \
  "v_mfma_f32_16x16x32_bf16 v[108:111], a[76:79], a[60:63], v[108:111]\n\t" \
  "v_mfma_f32_16x16x32_bf16 v[108:111], a[140:143], a[44:47], v[108:111]\n\t" \
  "buffer_load_dwordx4 v224, s[60:63], s38 offen offset:0 lds\n\t" \
  "v_mfma_f32_16x16x32_bf16 v[16:19], a[144:147], a[32:35], v[16:19]\n\t" \
  "ds_read_b64_tr_b16 a[114:115], v196 offset:25600\n\t" \
  "v_mfma_f32_16x16x32_bf16 v[16:19], a[80:83], a[48:51], v[16:19]\n\t" \
  "ds_read_b64_tr_b16 a[116:117], v197 offset:24576\n\t" \
  "v_mfma_f32_16x16x32_bf16 v[48:51], a[80:83], a[52:55], v[48:51]\n\t" \
  "ds_read_b64_tr_b16 a[118:119], v197 offset:25600\n\t" \
  "v_mfma_f32_16x16x32_bf16 v[48:51], a[144:147], a[36:39], v[48:51]\n\t" \
  "v_mfma_f32_16x16x32_bf16 v[80:83], a[144:147], a[40:43], v[80:83]\n\t" \
  "buffer_load_dwordx4 v225, s[60:63], s38 offen offset:1024 lds\n\t" \
  "v_mfma_f32_16x16x32_bf16 v[80:83], a[80:83], a[56:59], v[80:83]\n\t" \
  "ds_read_b64_tr_b16 a[120:121], v198 offset:24576\n\t" \
  "v_mfma_f32_16x16x32_bf16 v[112:115], a[80:83], a[60:63], v[112:115]\n\t" \
  "ds_read_b64_tr_b16 a[122:123], v198 offset:25600\n\t" \
  "v_mfma_f32_16x16x32_bf16 v[112:115], a[144:147], a[44:47], v[112:115]\n\t" \
  "ds_read_b64_tr_b16 a[124:125], v199 offset:24576\n\t" \
  "v_mfma_f32_16x16x32_bf16 v[20:23], a[148:151], a[32:35], v[20:23]\n\t" \
  "v_mfma_f32_16x16x32_bf16 v[20:23], a[84:87], a[48:51], v[20:23]\n\t" \
  "buffer_load_dwordx4 v226, s[60:63], s38 offen offset:2048 lds\n\t" \
  "v_mfma_f32_16x16x32_bf16 v[52:55], a[84:87], a[52:55], v[52:55]\n\t" \
  "ds_read_b64_tr_b16 a[126:127], v199 offset:25600\n\t" \
  "v_mfma_f32_16x16x32_bf16 v[52:55], a[148:151], a[36:39], v[52:55]\n\t" \
  "v_mfma_f32_16x16x32_bf16 v[84:87], a[148:151], a[40:43], v[84:87]\n\t" \
  "v_mfma_f32_16x16x32_bf16 v[84:87], a[84:87], a[56:59], v[84:87]\n\t" \
  "v_mfma_f32_16x16x32_bf16 v[116:119], a[84:87], a[60:63], v[116:119]\n\t" \
  "buffer_load_dwordx4 v227, s[60:63], s38 offen offset:3072 lds\n\t" \
  "v_mfma_f32_16x16x32_bf16 v[116:119], a[148:151], a[44:47], v[116:119]\n\t" \
  "s_add_u32 m0, s76, 49152\n\t" \
  "v_mfma_f32_16x16x32_bf16 v[24:27], a[152:155], a[32:35], v[24:27]\n\t" \
  "v_mfma_f32_16x16x32_bf16 v[24:27], a[88:91], a[48:51], v[24:27]\n\t" \
  "v_mfma_f32_16x16x32_bf16 v[56:59], a[88:91], a[52:55], v[56:59]\n\t" \
  "v_mfma_f32_16x16x32_bf16 v[56:59], a[152:155], a[36:39], v[56:59]\n\t" \
  "buffer_load_dwordx4 v230, s[64:67], s38 offen offset:0 lds\n\t" \
  "v_mfma_f32_16x16x32_bf16 v[88:91], a[152:155], a[40:43], v[88:91]\n\t" \
  "v_mfma_f32_16x16x32_bf16 v[88:91], a[88:91], a[56:59], v[88:91]\n\t" \
  "v_mfma_f32_16x16x32_bf16 v[120:123], a[88:91], a[60:63], v[120:123]\n\t" \
  "v_mfma_f32_16x16x32_bf16 v[120:123], a[152:155], a[44:47], v[120:123]\n\t" \
  "v_mfma_f32_16x16x32_bf16 v[28:31], a[156:159], a[32:35], v[28:31]\n\t" \
  "buffer_load_dwordx4 v231, s[64:67], s38 offen offset:1024 lds\n\t" \
  "v_mfma_f32_16x16x32_bf16 v[28:31], a[92:95], a[48:51], v[28:31]\n\t" \
  "v_mfma_f32_16x16x32_bf16 v[60:63], a[92:95], a[52:55], v[60:63]\n\t" \
  "v_mfma_f32_16x16x32_bf16 v[60:63], a[156:159], a[36:39], v[60:63]\n\t" \
  "v_mfma_f32_16x16x32_bf16 v[92:95], a[156:159], a[40:43], v[92:95]\n\t" \
  "v_mfma_f32_16x16x32_bf16 v[92:95], a[92:95], a[56:59], v[92:95]\n\t" \
  "v_mfma_f32_16x16x32_bf16 v[124:127], a[92:95], a[60:63], v[124:127]\n\t" \
  "s_waitcnt vmcnt(17) lgkmcnt(0)\n\t" \
  "v_mfma_f32_16x16x32_bf16 v[124:127], a[156:159], a[44:47], v[124:127]\n\t" \
  "s_barrier\n\t" \
  "v_mfma_f32_16x16x32_bf16 v[0:3], a[160:163], a[192:195], v[0:3]\n\t" \
  "buffer_load_dwordx4 v230, s[64:67], s38 offen offset:2048 lds\n\t" \
  "v_mfma_f32_16x16x32_bf16 v[0:3], a[96:99], a[208:211], v[0:3]\n\t" \
  "ds_read_b64_tr_b16 a[128:129], v192 offset:32768\n\t" \
  "v_mfma_f32_16x16x32_bf16 v[32:35], a[96:99], a[212:215], v[32:35]\n\t" \
  "ds_read_b64_tr_b16 a[130:131], v192 offset:33792\n\t" \
  "v_mfma_f32_16x16x32_bf16 v[32:35], a[160:163], a[196:199], v[32:35]\n\t" \
  "ds_read_b64 v[142:143], v141 offset:1024\n\t" \
  "v_mfma_f32_16x16x32_bf16 v[64:67], a[160:163], a[200:203], v[64:67]\n\t" \
  "ds_read_b64_tr_b16 a[132:133], v193 offset:32768\n\t" \
  "v_mfma_f32_16x16x32_bf16 v[64:67], a[96:99], a[216:219], v[64:67]\n\t" \
  "buffer_load_dwordx4 v231, s[64:67], s38 offen offset:3072 lds\n\t" \
  "v_mfma_f32_16x16x32_bf16 v[96:99], a[96:99], a[220:223], v[96:99]\n\t" \
  "s_add_u32 m0, s76, 32768\n\t" \
  "ds_read_b64_tr_b16 a[134:135], v193 offset:33792\n\t" \
  "s_add_u32 s96, s38, 16384\n\t" \
  "v_mfma_f32_16x16x32_bf16 v[96:99], a[160:163], a[204:207], v[96:99]\n\t" \
  "ds_read_b64_tr_b16 a[136:137], v194 offset:32768\n\t" \
  "v_mfma_f32_16x16x32_bf16 v[4:7], a[164:167], a[192:195], v[4:7]\n\t" \
  "buffer_load_dwordx4 v224, s[60:63], s96 offen offset:0 lds\n\t" \
  "v_mfma_f32_16x16x32_bf16 v[4:7], a[100:103], a[208:211], v[4:7]\n\t" \
  "s_waitcnt lgkmcnt(3)\n\t" \
  "v_add_f32_dpp v142, v142, v142 row_ror:4 row_mask:0xf bank_mask:0xf\n\t" \
  "v_add_f32_dpp v143, v143, v143 row_ror:4 row_mask:0xf bank_mask:0xf\n\t" \
  "v_mfma_f32_16x16x32_bf16 v[36:39], a[100:103], a[212:215], v[36:39]\n\t" \
  "ds_read_b64_tr_b16 a[138:139], v194 offset:33792\n\t" \
  "v_add_f32_dpp v144, v142, v142 row_ror:8 row_mask:0xf bank_mask:0xf\n\t" \
  "v_mfma_f32_16x16x32_bf16 v[36:39], a[164:167], a[196:199], v[36:39]\n\t" \
  "ds_read_b64_tr_b16 a[140:141], v195 offset:32768\n\t" \
  "v_add_f32_dpp v146, v143, v143 row_ror:8 row_mask:0xf bank_mask:0xf\n\t" \
  "v_mfma_f32_16x16x32_bf16 v[68:71], a[164:167], a[200:203], v[68:71]\n\t" \
  "buffer_store_dwordx4 v[144:147], v140, s[16:19], s90 offen sc1\n\t" \
  "ds_read_b64_tr_b16 a[142:143], v195 offset:33792\n\t" \
  "ds_read_b64_tr_b16 a[144:145], v196 offset:32768\n\t" \
  "ds_read_b64_tr_b16 a[146:147], v196 offset:33792\n\t" \
  "v_mfma_f32_16x16x32_bf16 v[68:71], a[100:103], a[216:219], v[68:71]\n\t" \
  "buffer_load_dwordx4 v225, s[60:63], s96 offen offset:1024 lds\n\t" \
  "v_mfma_f32_16x16x32_bf16 v[100:103], a[100:103], a[220:223], v[100:103]\n\t" \
  "ds_read_b64_tr_b16 a[148:149], v197 offset:32768\n\t" \
  "v_mfma_f32_16x16x32_bf16 v[100:103], a[164:167], a[204:207], v[100:103]\n\t" \
  ".Lhs5:\n\ts_cbranch_execz .Lhs5n\n\t" \
  "ds_read_b64_tr_b16 a[150:151], v197 offset:33792\n\t" \
  "v_mfma_f32_16x16x32_bf16 v[8:11], a[168:171], a[192:195], v[8:11]\n\t" \
  "buffer_load_dwordx4 v226, s[60:63], s96 offen offset:2048 lds\n\t" \
  "v_mfma_f32_16x16x32_bf16 v[8:11], a[104:107], a[208:211], v[8:11]\n\t" \
  "ds_read_b64_tr_b16 a[152:153], v198 offset:32768\n\t" \
  "v_mfma_f32_16x16x32_bf16 v[40:43], a[104:107], a[212:215], v[40:43]\n\t" \
  "ds_read_b64_tr_b16 a[154:155], v198 offset:33792\n\t" \
  "v_mfma_f32_16x16x32_bf16 v[40:43], a[168:171], a[196:199], v[40:43]\n\t" \
  "ds_read_b64_tr_b16 a[156:157], v199 offset:32768\n\t" \
  "v_mfma_f32_16x16x32_bf16 v[72:75], a[168:171], a[200:203], v[72:75]\n\t" \
  "buffer_load_dwordx4 v227, s[60:63], s96 offen offset:3072 lds\n\t" \
  "v_mfma_f32_16x16x32_bf16 v[72:75], a[104:107], a[216:219], v[72:75]\n\t" \
  "ds_read_b64_tr_b16 a[158:159], v199 offset:33792\n\t" \
  "s_add_u32 s99, s53, 28672\n\t" \
  "v_mfma_f32_16x16x32_bf16 v[104:107], a[104:107], a[220:223], v[104:107]\n\t" \
  "ds_read_b64_tr_b16 a[64:65], v192 offset:40960\n\t" \
  "v_mfma_f32_16x16x32_bf16 v[104:107], a[168:171], a[204:207], v[104:107]\n\t" \
  "ds_read_b64_tr_b16 a[66:67], v192 offset:41984\n\t" \
  "v_mfma_f32_16x16x32_bf16 v[12:15], a[172:175], a[192:195], v[12:15]\n\t" \
  "buffer_load_dword v246, v245, s[68:71], s99 offen\n\t" \
  "v_mfma_f32_16x16x32_bf16 v[12:15], a[108:111], a[208:211], v[12:15]\n\t" \
  "ds_read_b64_tr_b16 a[68:69], v193 offset:40960\n\t" \
  "v_mfma_f32_16x16x32_bf16 v[44:47], a[108:111], a[212:215], v[44:47]\n\t" \
  "ds_read_b64_tr_b16 a[70:71], v193 offset:41984\n\t" \
  "v_mfma_f32_16x16x32_bf16 v[44:47], a[172:175], a[196:199], v[44:47]\n\t" \
  "ds_read_b64_tr_b16 a[72:73], v194 offset:40960\n\t" \
  "v_mfma_f32_16x16x32_bf16 v[76:79], a[172:175], a[200:203], v[76:79]\n\t" \
  "ds_read_b64_tr_b16 a[74:75], v194 offset:41984\n\t" \
  "v_mfma_f32_16x16x32_bf16 v[76:79], a[108:111], a[216:219], v[76:79]\n\t" \
  "ds_read_b64_tr_b16 a[76:77], v195 offset:40960\n\t" \
  "s_add_u32 s99, s53, 43008\n\t" \
  "v_mfma_f32_16x16x32_bf16 v[108:111], a[108:111], a[220:223], v[108:111]\n\t" \
  "ds_read_b64_tr_b16 a[78:79], v195 offset:41984\n\t" \
  "v_mfma_f32_16x16x32_bf16 v[108:111], a[172:175], a[204:207], v[108:111]\n\t" \
  "buffer_load_dword v246, v245, s[68:71], s99 offen\n\t" \
  "v_mfma_f32_16x16x32_bf16 v[16:19], a[176:179], a[192:195], v[16:19]\n\t" \
  "ds_read_b64_tr_b16 a[80:81], v196 offset:40960\n\t" \
  "v_mfma_f32_16x16x32_bf16 v[16:19], a[112:115], a[208:211], v[16:19]\n\t" \
  "ds_read_b64_tr_b16 a[82:83], v196 offset:41984\n\t" \
  "v_mfma_f32_16x16x32_bf16 v[48:51], a[112:115], a[212:215], v[48:51]\n\t" \
  "ds_read_b64_tr_b16 a[84:85], v197 offset:40960\n\t" \
  "v_mfma_f32_16x16x32_bf16 v[48:51], a[176:179], a[196:199], v[48:51]\n\t" \
  "ds_read_b64_tr_b16 a[86:87], v197 offset:41984\n\t" \
  "v_mfma_f32_16x16x32_bf16 v[80:83], a[176:179], a[200:203], v[80:83]\n\t" \
  "ds_read_b64_tr_b16 a[88:89], v198 offset:40960\n\t" \
  "v_mfma_f32_16x16x32_bf16 v[80:83], a[112:115], a[216:219], v[80:83]\n\t" \
  "v_mfma_f32_16x16x32_bf16 v[112:115], a[112:115], a[220:223], v[112:115]\n\t" \
  "ds_read_b64_tr_b16 a[90:91], v198 offset:41984\n\t" \
  "v_mfma_f32_16x16x32_bf16 v[112:115], a[176:179], a[204:207], v[112:115]\n\t" \
  "ds_read_b64_tr_b16 a[92:93], v199 offset:40960\n\t" \
  "v_mfma_f32_16x16x32_bf16 v[20:23], a[180:183], a[192:195], v[20:23]\n\t" \
  "v_mfma_f32_16x16x32_bf16 v[20:23], a[116:119], a[208:211], v[20:23]\n\t" \
  "ds_read_b64_tr_b16 a[94:95], v199 offset:41984\n\t" \
  "v_mfma_f32_16x16x32_bf16 v[52:55], a[116:119], a[212:215], v[52:55]\n\t" \
  "v_mfma_f32_16x16x32_bf16 v[52:55], a[180:183], a[196:199], v[52:55]\n\t" \
  "v_mfma_f32_16x16x32_bf16 v[84:87], a[180:183], a[200:203], v[84:87]\n\t" \
  "v_mfma_f32_16x16x32_bf16 v[84:87], a[116:119], a[216:219], v[84:87]\n\t" \
  "v_mfma_f32_16x16x32_bf16 v[116:119], a[116:119], a[220:223], v[116:119]\n\t" \
  "v_mfma_f32_16x16x32_bf16 v[116:119], a[180:183], a[204:207], v[116:119]\n\t" \
  "v_mfma_f32_16x16x32_bf16 v[24:27], a[184:187], a[192:195], v[24:27]\n\t" \
  "v_mfma_f32_16x16x32_bf16 v[24:27], a[120:123], a[208:211], v[24:27]\n\t" \
  "v_mfma_f32_16x16x32_bf16 v[56:59], a[120:123], a[212:215], v[56:59]\n\t" \
  "v_mfma_f32_16x16x32_bf16 v[56:59], a[184:187], a[196:199], v[56:59]\n\t" \
  "v_mfma_f32_16x16x32_bf16 v[88:91], a[184:187], a[200:203], v[88:91]\n\t" \
  "v_mfma_f32_16x16x32_bf16 v[88:91], a[120:123], a[216:219], v[88:91]\n\t" \
  "v_mfma_f32_16x16x32_bf16 v[120:123], a[120:123], a[220:223], v[120:123]\n\t" \
  "v_mfma_f32_16x16x32_bf16 v[120:123], a[184:187], a[204:207], v[120:123]\n\t" \
  "v_mfma_f32_16x16x32_bf16 v[28:31], a[188:191], a[192:195], v[28:31]\n\t" \
  "v_mfma_f32_16x16x32_bf16 v[28:31], a[124:127], a[208:211], v[28:31]\n\t" \
  "v_mfma_f32_16x16x32_bf16 v[60:63], a[124:127], a[212:215], v[60:63]\n\t" \
  "v_mfma_f32_16x16x32_bf16 v[60:63], a[188:191], a[196:199], v[60:63]\n\t" \
  "v_mfma_f32_16x16x32_bf16 v[92:95], a[188:191], a[200:203], v[92:95]\n\t" \
  "v_mfma_f32_16x16x32_bf16 v[92:95], a[124:127], a[216:219], v[92:95]\n\t" \
  "v_mfma_f32_16x16x32_bf16 v[124:127], a[124:127], a[220:223], v[124:127]\n\t" \
  "v_mfma_f32_16x16x32_bf16 v[124:127], a[188:191], a[204:207], v[124:127]\n\t" \
  "s_waitcnt vmcnt(18) lgkmcnt(0)\n\t" \
  FSTP("s_waitcnt lgkmcnt(0)\n\t") \
  FSTP("v_writelane_b32 v255, s94, 52\n\t") \
  FSTP("v_writelane_b32 v255, s92, 53\n\t") \
  FSTA("s_memrealtime s[94:95]\n\t") \
  FSTA("s_memtime s[92:93]\n\t") \
  FSTA("s_waitcnt lgkmcnt(0)\n\t") \
  FSTA("v_writelane_b32 v255, s94, 16\n\t") \
  FSTA("v_writelane_b32 v255, s92, 17\n\t") \
  ".subsection 1\n\t" \
  ".Lfr_s:\n\t" \
  "s_mov_b32 s54, 0\n\t" \
  ".Lfr_r:\n\t" \
  "s_sleep 1\n\t" \
  "buffer_load_dwordx4 v[128:131], v248, s[16:19], s53 offen sc1\n\t" \
  "buffer_load_dwordx4 v[132:135], v248, s[16:19], s58 offen sc1\n\t" \
  "buffer_load_dwordx4 v[136:139], v248, s[16:19], s90 offen sc1\n\t" \
  "buffer_load_dwordx4 v[140:143], v248, s[16:19], s91 offen sc1\n\t" \
  "buffer_load_dwordx4 v[160:163], v249, s[16:19], s96 offen sc1\n\t" \
  "buffer_load_dwordx4 v[164:167], v249, s[16:19], s97 offen sc1\n\t" \
  "buffer_load_dwordx4 v[168:171], v249, s[16:19], s99 offen sc1\n\t" \
  "buffer_load_dwordx4 v[172:175], v249, s[16:19], s57 offen sc1\n\t" \
  "s_waitcnt vmcnt(0)\n\t" \
  "v_xor_b32 v144, v211, v129\n\t" \
  "v_bitop3_b32 v144, v144, v131, v211 bitop3:0xf6\n\t" \
  "v_bitop3_b32 v144, v144, v133, v211 bitop3:0xf6\n\t" \
  "v_bitop3_b32 v144, v144, v135, v211 bitop3:0xf6\n\t" \
  "v_bitop3_b32 v144, v144, v137, v211 bitop3:0xf6\n\t" \
  "v_bitop3_b32 v144, v144, v139, v211 bitop3:0xf6\n\t" \
  "v_bitop3_b32 v144, v144, v141, v211 bitop3:0xf6\n\t" \
  "v_bitop3_b32 v144, v144, v143, v211 bitop3:0xf6\n\t" \
  "v_bitop3_b32 v144, v144, v161, v211 bitop3:0xf6\n\t" \
  "v_bitop3_b32 v144, v144, v163, v211 bitop3:0xf6\n\t" \
  "v_bitop3_b32 v144, v144, v165, v211 bitop3:0xf6\n\t" \
  "v_bitop3_b32 v144, v144, v167, v211 bitop3:0xf6\n\t" \
  "v_bitop3_b32 v144, v144, v169, v211 bitop3:0xf6\n\t" \
  "v_bitop3_b32 v144, v144, v171, v211 bitop3:0xf6\n\t" \
  "v_bitop3_b32 v144, v144, v173, v211 bitop3:0xf6\n\t" \
  "v_bitop3_b32 v144, v144, v175, v211 bitop3:0xf6\n\t" \
  "v_cmp_ne_u32 vcc, 0, v144\n\t" \
  "v_add_f32 v146, v128, v132\n\t" \
  "v_add_f32 v148, v136, v140\n\t" \
  "v_add_f32 v147, v130, v134\n\t" \
  "v_add_f32 v149, v138, v142\n\t" \
  "v_add_f32 v146, v146, v148\n\t" \
  "v_add_f32 v147, v147, v149\n\t" \
  "s_cbranch_vccz .Lfr_k\n\t" \
  "s_add_u32 s54, s54, 1\n\t" \
  "s_cmp_lt_u32 s54, " FQ_CAP "\n\t" \
  "s_cbranch_scc1 .Lfr_r\n\t" \
  "buffer_store_dword v211, off, s[16:19], 0 offset:256 sc1\n\t" \
  "s_branch .Lfr_k\n\t" \
  ".Lfq_s:\n\t" \
  "s_mov_b32 s97, 0\n\t" \
  ".Lfq_r:\n\t" \
  "buffer_load_dword v252, v210, s[16:19], s84 offen sc1\n\t" \
  "s_waitcnt vmcnt(0)\n\t" \
  "v_cmp_ne_u32 vcc, v211, v252\n\t" \
  "s_cbranch_vccz .Lfq_k\n\t" \
  "s_add_u32 s97, s97, 1\n\t" \
  "s_cmp_lt_u32 s97, " FQ_CAP "\n\t" \
  "s_cbranch_scc1 .Lfq_r\n\t" \
  "buffer_store_dword v211, off, s[16:19], 0 offset:256 sc1\n\t" \
  "s_branch .Lfq_k\n\t" \
  ".subsection 0\n\t"
static_assert(W_VOTE == 8192 && W_FLAGR == 32768 && W_FLAGO == 131072 && W_MODB == 2228224 && W_OT == 8388608 && W_XCH == 25165824 && W_EPOCH == 2490368 && W_FLAGH == 2494464 && W_ST1 == 2097152 && W_MODA == 2162688 && W_HT == 4194304 && W_PROBE == 16384);
#define STS(x)
#define AO_STV
#define STA(x)
#define STP(x)
#define AT_ASM \
  "s_cmp_eq_u32 s37, 0\n\t" \
  "s_cbranch_scc1 .Lsf_go\n\t" \
  "v_mfma_f32_16x16x32_bf16 v[28:31], a[156:159], a[224:227], v[28:31]\n\t" \
  "s_add_u32 s77, s51, 131072\n\t" \
  "v_add_u32 v210, 65536, v228\n\t" \
  "v_add_u32 v248, 14336, v240\n\t" \
  "v_mfma_f32_16x16x32_bf16 v[28:31], a[92:95], a[240:243], v[28:31]\n\t" \
  "v_add_u32 v211, 65536, v229\n\t" \
  "v_add_u32 v249, 21504, v241\n\t" \
  "v_mfma_f32_16x16x32_bf16 v[24:27], a[152:155], a[224:227], v[24:27]\n\t" \
  "v_mov_b32 v216, 0x3f803f80\n\t" \
  "v_mov_b32 v217, 0x3f803f80\n\t" \
  "v_mfma_f32_16x16x32_bf16 v[24:27], a[88:91], a[240:243], v[24:27]\n\t" \
  "v_mov_b32 v218, 0x3f803f80\n\t" \
  "v_add_u32 v241, 7168, v241\n\t" \
  "v_mfma_f32_16x16x32_bf16 v[20:23], a[148:151], a[224:227], v[20:23]\n\t" \
  "s_mov_b32 s89, 0\n\t" \
  "v_cvt_pk_bf16_f32 v31, v30, v31\n\t" \
  "v_cvt_pk_bf16_f32 v30, v28, v29\n\t" \
  "v_mfma_f32_16x16x32_bf16 v[20:23], a[84:87], a[240:243], v[20:23]\n\t" \
  "v_mov_b32 v219, 0x3f803f80\n\t" \
  "v_cvt_pk_bf16_f32 v28, v24, v25\n\t" \
  "v_mfma_f32_16x16x32_bf16 v[16:19], a[144:147], a[224:227], v[16:19]\n\t" \
  "v_cvt_pk_bf16_f32 v29, v26, v27\n\t" \
  "v_lshlrev_b32 v212, 7, v233\n\t" \
  "v_mfma_f32_16x16x32_bf16 v[16:19], a[80:83], a[240:243], v[16:19]\n\t" \
  "v_permlane16_swap_b32 v28, v30\n\t" \
  "v_mfma_f32_16x16x32_bf16 v[12:15], a[140:143], a[224:227], v[12:15]\n\t" \
  "v_cvt_pk_bf16_f32 v27, v22, v23\n\t" \
  "v_cvt_pk_bf16_f32 v26, v20, v21\n\t" \
  "v_mfma_f32_16x16x32_bf16 v[12:15], a[76:79], a[240:243], v[12:15]\n\t" \
  "v_permlane16_swap_b32 v29, v31\n\t" \
  "v_mfma_f32_16x16x32_bf16 v[8:11], a[136:139], a[224:227], v[8:11]\n\t" \
  "s_mov_b32 s54, 0\n\t" \
  "v_cvt_pk_bf16_f32 v24, v16, v17\n\t" \
  "v_cvt_pk_bf16_f32 v25, v18, v19\n\t" \
  "v_mfma_f32_16x16x32_bf16 v[8:11], a[72:75], a[240:243], v[8:11]\n\t" \
  "v_permlane16_swap_b32 v24, v26\n\t" \
  "v_mfma_f32_16x16x32_bf16 v[4:7], a[132:135], a[224:227], v[4:7]\n\t" \
  "v_cvt_pk_bf16_f32 v23, v14, v15\n\t" \
  "v_cvt_pk_bf16_f32 v22, v12, v13\n\t" \
  "v_mfma_f32_16x16x32_bf16 v[4:7], a[68:71], a[240:243], v[4:7]\n\t" \
  "v_permlane16_swap_b32 v25, v27\n\t" \
  "v_mfma_f32_16x16x32_bf16 v[0:3], a[128:131], a[224:227], v[0:3]\n\t" \
  "s_waitcnt vmcnt(2)\n\t" \
  "v_cvt_pk_bf16_f32 v20, v8, v9\n\t" \
  "v_cvt_pk_bf16_f32 v21, v10, v11\n\t" \
  "v_mfma_f32_16x16x32_bf16 v[0:3], a[64:67], a[240:243], v[0:3]\n\t" \
  "s_barrier\n\t" \
  "v_mfma_f32_16x16x32_bf16 v[60:63], a[156:159], a[228:231], v[60:63]\n\t" \
  "v_cvt_pk_bf16_f32 v19, v6, v7\n\t" \
  "v_cvt_pk_bf16_f32 v18, v4, v5\n\t" \
  "v_mfma_f32_16x16x32_bf16 v[60:63], a[92:95], a[244:247], v[60:63]\n\t" \
  "v_permlane16_swap_b32 v20, v22\n\t" \
  "v_mfma_f32_16x16x32_bf16 v[56:59], a[152:155], a[228:231], v[56:59]\n\t" \
  "ds_read_b128 a[0:3], v220 offset:0\n\t" \
  "v_cvt_pk_bf16_f32 v16, v0, v1\n\t" \
  "v_mfma_f32_16x16x32_bf16 v[56:59], a[88:91], a[244:247], v[56:59]\n\t" \
  "ds_read_b128 a[4:7], v221 offset:0\n\t" \
  "v_cvt_pk_bf16_f32 v17, v2, v3\n\t" \
  "v_mfma_f32_16x16x32_bf16 v[52:55], a[148:151], a[228:231], v[52:55]\n\t" \
  "v_cvt_pk_bf16_f32 v63, v62, v63\n\t" \
  "v_cvt_pk_bf16_f32 v62, v60, v61\n\t" \
  "v_mfma_f32_16x16x32_bf16 v[52:55], a[84:87], a[244:247], v[52:55]\n\t" \
  "v_permlane16_swap_b32 v21, v23\n\t" \
  "v_mfma_f32_16x16x32_bf16 v[48:51], a[144:147], a[228:231], v[48:51]\n\t" \
  "v_cvt_pk_bf16_f32 v60, v56, v57\n\t" \
  "v_cvt_pk_bf16_f32 v61, v58, v59\n\t" \
  "v_mfma_f32_16x16x32_bf16 v[48:51], a[80:83], a[244:247], v[48:51]\n\t" \
  "ds_read_b128 a[8:11], v222 offset:0\n\t" \
  "v_cmp_gt_u32 vcc, 16, v233\n\t" \
  "v_mfma_f32_16x16x32_bf16 v[44:47], a[140:143], a[228:231], v[44:47]\n\t" \
  "v_cvt_pk_bf16_f32 v59, v54, v55\n\t" \
  "v_cvt_pk_bf16_f32 v58, v52, v53\n\t" \
  "v_mfma_f32_16x16x32_bf16 v[44:47], a[76:79], a[244:247], v[44:47]\n\t" \
  "ds_read_b128 a[12:15], v223 offset:0\n\t" \
  "v_bfrev_b32 v214, 1\n\t" \
  "v_mfma_f32_16x16x32_bf16 v[40:43], a[136:139], a[228:231], v[40:43]\n\t" \
  "v_cvt_pk_bf16_f32 v56, v48, v49\n\t" \
  "v_cvt_pk_bf16_f32 v57, v50, v51\n\t" \
  "v_mfma_f32_16x16x32_bf16 v[40:43], a[72:75], a[244:247], v[40:43]\n\t" \
  "s_add_u32 s99, s53, 0\n\t" \
  "ds_read_b128 a[16:19], v220 offset:4096\n\t" \
  "v_cvt_pk_bf16_f32 v55, v46, v47\n\t" \
  "v_mfma_f32_16x16x32_bf16 v[36:39], a[132:135], a[228:231], v[36:39]\n\t" \
  "ds_read_b128 a[20:23], v221 offset:4096\n\t" \
  "v_cvt_pk_bf16_f32 v54, v44, v45\n\t" \
  "v_mfma_f32_16x16x32_bf16 v[36:39], a[68:71], a[244:247], v[36:39]\n\t" \
  "ds_read_b128 a[24:27], v222 offset:4096\n\t" \
  "v_cvt_pk_bf16_f32 v52, v40, v41\n\t" \
  "v_mfma_f32_16x16x32_bf16 v[32:35], a[128:131], a[228:231], v[32:35]\n\t" \
  "buffer_load_dword v246, v245, s[72:75], s99 offen\n\t" \
  "v_mfma_f32_16x16x32_bf16 v[32:35], a[64:67], a[244:247], v[32:35]\n\t" \
  "v_cvt_pk_bf16_f32 v53, v42, v43\n\t" \
  "v_permlane16_swap_b32 v16, v18\n\t" \
  "v_mfma_f32_16x16x32_bf16 v[92:95], a[156:159], a[232:235], v[92:95]\n\t" \
  "v_cvt_pk_bf16_f32 v51, v38, v39\n\t" \
  "v_cvt_pk_bf16_f32 v50, v36, v37\n\t" \
  "v_mfma_f32_16x16x32_bf16 v[92:95], a[92:95], a[248:251], v[92:95]\n\t" \
  "ds_read_b128 a[28:31], v223 offset:4096\n\t" \
  "v_permlane16_swap_b32 v17, v19\n\t" \
  "v_mfma_f32_16x16x32_bf16 v[88:91], a[152:155], a[232:235], v[88:91]\n\t" \
  "v_cvt_pk_bf16_f32 v48, v32, v33\n\t" \
  "v_permlane16_swap_b32 v60, v62\n\t" \
  "v_mfma_f32_16x16x32_bf16 v[88:91], a[88:91], a[248:251], v[88:91]\n\t" \
  "ds_read_b128 a[32:35], v220 offset:8192\n\t" \
  "v_cvt_pk_bf16_f32 v49, v34, v35\n\t" \
  "v_mfma_f32_16x16x32_bf16 v[84:87], a[148:151], a[232:235], v[84:87]\n\t" \
  "v_cvt_pk_bf16_f32 v95, v94, v95\n\t" \
  "v_cvt_pk_bf16_f32 v94, v92, v93\n\t" \
  "v_mfma_f32_16x16x32_bf16 v[84:87], a[84:87], a[248:251], v[84:87]\n\t" \
  "ds_read_b128 a[36:39], v221 offset:8192\n\t" \
  "v_permlane16_swap_b32 v61, v63\n\t" \
  "v_mfma_f32_16x16x32_bf16 v[80:83], a[144:147], a[232:235], v[80:83]\n\t" \
  "v_cvt_pk_bf16_f32 v92, v88, v89\n\t" \
  "v_cvt_pk_bf16_f32 v93, v90, v91\n\t" \
  "v_mfma_f32_16x16x32_bf16 v[80:83], a[80:83], a[248:251], v[80:83]\n\t" \
  "ds_read_b128 a[40:43], v222 offset:8192\n\t" \
  "v_permlane16_swap_b32 v56, v58\n\t" \
  "v_cvt_pk_bf16_f32 v91, v86, v87\n\t" \
  "v_mfma_f32_16x16x32_bf16 v[76:79], a[140:143], a[232:235], v[76:79]\n\t" \
  "buffer_load_dword v246, v247, s[16:19], 0 offen sc1\n\t" \
  "v_mfma_f32_16x16x32_bf16 v[76:79], a[76:79], a[248:251], v[76:79]\n\t" \
  "ds_read_b128 a[44:47], v223 offset:8192\n\t" \
  "v_permlane16_swap_b32 v57, v59\n\t" \
  "v_mfma_f32_16x16x32_bf16 v[72:75], a[136:139], a[232:235], v[72:75]\n\t" \
  "v_cvt_pk_bf16_f32 v90, v84, v85\n\t" \
  "v_cvt_pk_bf16_f32 v88, v80, v81\n\t" \
  "v_mfma_f32_16x16x32_bf16 v[72:75], a[72:75], a[248:251], v[72:75]\n\t" \
  "ds_read_b128 a[48:51], v220 offset:12288\n\t" \
  "v_permlane16_swap_b32 v52, v54\n\t" \
  "v_mfma_f32_16x16x32_bf16 v[68:71], a[132:135], a[232:235], v[68:71]\n\t" \
  "ds_read_b128 a[52:55], v221 offset:12288\n\t" \
  "v_cvt_pk_bf16_f32 v89, v82, v83\n\t" \
  "v_mfma_f32_16x16x32_bf16 v[68:71], a[68:71], a[248:251], v[68:71]\n\t" \
  "v_cvt_pk_bf16_f32 v87, v78, v79\n\t" \
  "v_permlane16_swap_b32 v53, v55\n\t" \
  "v_mfma_f32_16x16x32_bf16 v[64:67], a[128:131], a[232:235], v[64:67]\n\t" \
  "ds_read_b128 a[56:59], v222 offset:12288\n\t" \
  "v_cvt_pk_bf16_f32 v86, v76, v77\n\t" \
  "v_mfma_f32_16x16x32_bf16 v[64:67], a[64:67], a[248:251], v[64:67]\n\t" \
  "v_cvt_pk_bf16_f32 v84, v72, v73\n\t" \
  "v_permlane16_swap_b32 v48, v50\n\t" \
  "v_mfma_f32_16x16x32_bf16 v[124:127], a[156:159], a[236:239], v[124:127]\n\t" \
  "ds_read_b128 a[60:63], v223 offset:12288\n\t" \
  "v_cvt_pk_bf16_f32 v85, v74, v75\n\t" \
  "v_mfma_f32_16x16x32_bf16 v[124:127], a[92:95], a[252:255], v[124:127]\n\t" \
  "v_cvt_pk_bf16_f32 v83, v70, v71\n\t" \
  "v_permlane16_swap_b32 v49, v51\n\t" \
  "v_mfma_f32_16x16x32_bf16 v[120:123], a[152:155], a[236:239], v[120:123]\n\t" \
  "v_cvt_pk_bf16_f32 v82, v68, v69\n\t" \
  "v_cvt_pk_bf16_f32 v80, v64, v65\n\t" \
  "v_mfma_f32_16x16x32_bf16 v[120:123], a[88:91], a[252:255], v[120:123]\n\t" \
  "v_cvt_pk_bf16_f32 v81, v66, v67\n\t" \
  "v_permlane16_swap_b32 v92, v94\n\t" \
  "v_mfma_f32_16x16x32_bf16 v[116:119], a[148:151], a[236:239], v[116:119]\n\t" \
  "v_cvt_pk_bf16_f32 v127, v126, v127\n\t" \
  "v_cvt_pk_bf16_f32 v126, v124, v125\n\t" \
  "v_mfma_f32_16x16x32_bf16 v[116:119], a[84:87], a[252:255], v[116:119]\n\t" \
  "v_permlane16_swap_b32 v93, v95\n\t" \
  "v_mfma_f32_16x16x32_bf16 v[112:115], a[144:147], a[236:239], v[112:115]\n\t" \
  "v_cvt_pk_bf16_f32 v124, v120, v121\n\t" \
  "v_permlane16_swap_b32 v88, v90\n\t" \
  "v_mfma_f32_16x16x32_bf16 v[112:115], a[80:83], a[252:255], v[112:115]\n\t" \
  "v_permlane16_swap_b32 v89, v91\n\t" \
  "v_mfma_f32_16x16x32_bf16 v[108:111], a[140:143], a[236:239], v[108:111]\n\t" \
  "v_cvt_pk_bf16_f32 v125, v122, v123\n\t" \
  "v_cvt_pk_bf16_f32 v123, v118, v119\n\t" \
  "v_mfma_f32_16x16x32_bf16 v[108:111], a[76:79], a[252:255], v[108:111]\n\t" \
  "v_cvt_pk_bf16_f32 v122, v116, v117\n\t" \
  "v_permlane16_swap_b32 v84, v86\n\t" \
  "v_mfma_f32_16x16x32_bf16 v[104:107], a[136:139], a[236:239], v[104:107]\n\t" \
  "v_cvt_pk_bf16_f32 v120, v112, v113\n\t" \
  "v_cvt_pk_bf16_f32 v121, v114, v115\n\t" \
  "v_mfma_f32_16x16x32_bf16 v[104:107], a[72:75], a[252:255], v[104:107]\n\t" \
  "v_permlane16_swap_b32 v85, v87\n\t" \
  "v_mfma_f32_16x16x32_bf16 v[100:103], a[132:135], a[236:239], v[100:103]\n\t" \
  "s_add_u32 m0, s38, 49152\n\t" \
  "v_permlane16_swap_b32 v80, v82\n\t" \
  "v_mfma_f32_16x16x32_bf16 v[100:103], a[68:71], a[252:255], v[100:103]\n\t" \
  "s_add_u32 s96, s38, 32768\n\t" \
  "v_permlane16_swap_b32 v81, v83\n\t" \
  "v_mfma_f32_16x16x32_bf16 v[96:99], a[128:131], a[236:239], v[96:99]\n\t" \
  "s_waitcnt lgkmcnt(0)\n\t" \
  "v_cvt_pk_bf16_f32 v119, v110, v111\n\t" \
  "v_cvt_pk_bf16_f32 v118, v108, v109\n\t" \
  "v_mfma_f32_16x16x32_bf16 v[96:99], a[64:67], a[252:255], v[96:99]\n\t" \
  "s_barrier\n\t" \
  STS("s_memrealtime s[94:95]\n\t") \
  STS("s_memtime s[92:93]\n\t") \
  "v_mfma_f32_16x16x32_bf16 v[0:3], a[0:3], v[16:19], 0\n\t" \
  "buffer_load_dwordx4 v224, s[60:63], s96 offen offset:0 lds\n\t" \
  "v_mfma_f32_16x16x32_bf16 v[0:3], a[4:7], v[20:23], v[0:3]\n\t" \
  "v_permlane16_swap_b32 v124, v126\n\t" \
  "v_mfma_f32_16x16x32_bf16 v[0:3], a[8:11], v[24:27], v[0:3]\n\t" \
  "ds_read_b128 a[192:195], v220 offset:16384\n\t" \
  "v_cvt_pk_bf16_f32 v116, v104, v105\n\t" \
  "v_mfma_f32_16x16x32_bf16 v[0:3], a[12:15], v[28:31], v[0:3]\n\t" \
  "v_permlane16_swap_b32 v125, v127\n\t" \
  "v_mfma_f32_16x16x32_bf16 v[32:35], a[0:3], v[48:51], 0\n\t" \
  "ds_read_b128 a[196:199], v221 offset:16384\n\t" \
  "v_cvt_pk_bf16_f32 v117, v106, v107\n\t" \
  "v_mfma_f32_16x16x32_bf16 v[32:35], a[4:7], v[52:55], v[32:35]\n\t" \
  "v_cvt_pk_bf16_f32 v115, v102, v103\n\t" \
  "v_cvt_pk_bf16_f32 v114, v100, v101\n\t" \
  "s_nop 0\n\t" \
  "v_max3_f32 v194, v0, v1, v2\n\t" \
  "v_max_f32 v194, v194, v3\n\t" \
  "v_mfma_f32_16x16x32_bf16 v[32:35], a[8:11], v[56:59], v[32:35]\n\t" \
  "v_mov_b32 v195, v194\n\t" \
  "v_cvt_pk_bf16_f32 v112, v96, v97\n\t" \
  "s_nop 0\n\t" \
  "v_permlane32_swap_b32 v194, v195\n\t" \
  "v_mfma_f32_16x16x32_bf16 v[32:35], a[12:15], v[60:63], v[32:35]\n\t" \
  "v_permlane16_swap_b32 v120, v122\n\t" \
  "v_max_f32 v194, v194, v195\n\t" \
  "v_mov_b32 v195, v194\n\t" \
  "s_nop 1\n\t" \
  "v_permlane16_swap_b32 v194, v195\n\t" \
  "v_max_f32 v194, v194, v195\n\t" \
  "v_sub_f32 v176, 0xc2000000, v194\n\t" \
  "v_sub_f32 v177, 0xc2000000, v194\n\t" \
  "v_sub_f32 v178, 0xc2000000, v194\n\t" \
  "v_sub_f32 v179, 0xc2000000, v194\n\t" \
  "s_nop 1\n\t" \
  "v_mfma_f32_16x16x32_bf16 v[4:7], a[16:19], v[16:19], v[176:179]\n\t" \
  "v_add_f32 v0, v0, v176\n\t" \
  "v_add_f32 v1, v1, v176\n\t" \
  "v_add_f32 v2, v2, v176\n\t" \
  "v_add_f32 v3, v3, v176\n\t" \
  "v_mfma_f32_16x16x32_bf16 v[4:7], a[20:23], v[20:23], v[4:7]\n\t" \
  "v_max3_f32 v194, v32, v33, v34\n\t" \
  "v_max_f32 v194, v194, v35\n\t" \
  "v_mov_b32 v195, v194\n\t" \
  "v_exp_f32 v0, v0\n\t" \
  "v_mfma_f32_16x16x32_bf16 v[4:7], a[24:27], v[24:27], v[4:7]\n\t" \
  "buffer_load_dwordx4 v225, s[60:63], s96 offen offset:1024 lds\n\t" \
  "v_permlane32_swap_b32 v194, v195\n\t" \
  "v_max_f32 v194, v194, v195\n\t" \
  "v_mfma_f32_16x16x32_bf16 v[4:7], a[28:31], v[28:31], v[4:7]\n\t" \
  "v_mov_b32 v195, v194\n\t" \
  "v_exp_f32 v1, v1\n\t" \
  "v_exp_f32 v2, v2\n\t" \
  "v_mfma_f32_16x16x32_bf16 v[8:11], a[32:35], v[16:19], v[176:179]\n\t" \
  STS("v_writelane_b32 v255, s94, 18\n\t") \
  STS("v_writelane_b32 v255, s92, 19\n\t") \
  "v_permlane16_swap_b32 v194, v195\n\t" \
  "v_max_f32 v194, v194, v195\n\t" \
  "v_sub_f32 v180, 0xc2000000, v194\n\t" \
  "v_sub_f32 v181, 0xc2000000, v194\n\t" \
  "v_mfma_f32_16x16x32_bf16 v[8:11], a[36:39], v[20:23], v[8:11]\n\t" \
  "v_sub_f32 v182, 0xc2000000, v194\n\t" \
  "v_sub_f32 v183, 0xc2000000, v194\n\t" \
  "v_add_f32 v32, v32, v180\n\t" \
  "v_exp_f32 v3, v3\n\t" \
  "v_mfma_f32_16x16x32_bf16 v[8:11], a[40:43], v[24:27], v[8:11]\n\t" \
  "s_add_u32 s99, s53, 14336\n\t" \
  "v_add_f32 v33, v33, v180\n\t" \
  "v_exp_f32 v4, v4\n\t" \
  "v_exp_f32 v5, v5\n\t" \
  "v_mfma_f32_16x16x32_bf16 v[8:11], a[44:47], v[28:31], v[8:11]\n\t" \
  "v_exp_f32 v6, v6\n\t" \
  "v_exp_f32 v7, v7\n\t" \
  "v_add_f32 v34, v34, v180\n\t" \
  "v_mfma_f32_16x16x32_bf16 v[12:15], a[48:51], v[16:19], v[176:179]\n\t" \
  "buffer_load_dword v246, v245, s[72:75], s99 offen\n\t" \
  "v_mfma_f32_16x16x32_bf16 v[12:15], a[52:55], v[20:23], v[12:15]\n\t" \
  "ds_read_b128 a[200:203], v222 offset:16384\n\t" \
  "v_cvt_pk_bf16_f32 v113, v98, v99\n\t" \
  "v_add_f32 v35, v35, v180\n\t" \
  "v_exp_f32 v8, v8\n\t" \
  "v_mfma_f32_16x16x32_bf16 v[12:15], a[56:59], v[24:27], v[12:15]\n\t" \
  "ds_read_b128 a[204:207], v223 offset:16384\n\t" \
  "v_exp_f32 v9, v9\n\t" \
  "v_exp_f32 v10, v10\n\t" \
  "v_mfma_f32_16x16x32_bf16 v[12:15], a[60:63], v[28:31], v[12:15]\n\t" \
  "buffer_load_dwordx4 v226, s[60:63], s96 offen offset:2048 lds\n\t" \
  "v_exp_f32 v11, v11\n\t" \
  "v_mfma_f32_16x16x32_bf16 v[64:67], a[0:3], v[80:83], 0\n\t" \
  "ds_read_b128 a[208:211], v220 offset:20480\n\t" \
  "v_permlane16_swap_b32 v121, v123\n\t" \
  "v_mfma_f32_16x16x32_bf16 v[64:67], a[4:7], v[84:87], v[64:67]\n\t" \
  "ds_read_b128 a[212:215], v221 offset:20480\n\t" \
  "v_permlane16_swap_b32 v116, v118\n\t" \
  "v_exp_f32 v12, v12\n\t" \
  "v_mfma_f32_16x16x32_bf16 v[64:67], a[8:11], v[88:91], v[64:67]\n\t" \
  "ds_read_b128 a[216:219], v222 offset:20480\n\t" \
  "v_exp_f32 v13, v13\n\t" \
  "v_exp_f32 v14, v14\n\t" \
  "v_mfma_f32_16x16x32_bf16 v[64:67], a[12:15], v[92:95], v[64:67]\n\t" \
  "ds_read_b128 a[220:223], v223 offset:20480\n\t" \
  "v_exp_f32 v15, v15\n\t" \
  "v_exp_f32 v32, v32\n\t" \
  "v_mfma_f32_16x16x32_bf16 v[36:39], a[16:19], v[48:51], v[180:183]\n\t" \
  "v_exp_f32 v33, v33\n\t" \
  "v_exp_f32 v34, v34\n\t" \
  "v_mfma_f32_16x16x32_bf16 v[36:39], a[20:23], v[52:55], v[36:39]\n\t" \
  "s_nop 0\n\t" \
  "v_max3_f32 v194, v64, v65, v66\n\t" \
  "v_max_f32 v194, v194, v67\n\t" \
  "v_mov_b32 v195, v194\n\t" \
  "v_exp_f32 v35, v35\n\t" \
  "v_mfma_f32_16x16x32_bf16 v[36:39], a[24:27], v[56:59], v[36:39]\n\t" \
  "ds_read_b128 a[224:227], v220 offset:24576\n\t" \
  "v_permlane32_swap_b32 v194, v195\n\t" \
  "v_max_f32 v194, v194, v195\n\t" \
  "v_mov_b32 v195, v194\n\t" \
  "v_mfma_f32_16x16x32_bf16 v[36:39], a[28:31], v[60:63], v[36:39]\n\t" \
  "s_nop 0\n\t" \
  "v_permlane16_swap_b32 v194, v195\n\t" \
  "v_max_f32 v194, v194, v195\n\t" \
  "v_sub_f32 v184, 0xc2000000, v194\n\t" \
  "v_sub_f32 v185, 0xc2000000, v194\n\t" \
  "v_mfma_f32_16x16x32_bf16 v[40:43], a[32:35], v[48:51], v[180:183]\n\t" \
  "buffer_load_dwordx4 v227, s[60:63], s96 offen offset:3072 lds\n\t" \
  "v_sub_f32 v186, 0xc2000000, v194\n\t" \
  "v_sub_f32 v187, 0xc2000000, v194\n\t" \
  "v_mfma_f32_16x16x32_bf16 v[40:43], a[36:39], v[52:55], v[40:43]\n\t" \
  "v_add_f32 v64, v64, v184\n\t" \
  "v_permlane16_swap_b32 v117, v119\n\t" \
  "v_exp_f32 v36, v36\n\t" \
  "v_mfma_f32_16x16x32_bf16 v[40:43], a[40:43], v[56:59], v[40:43]\n\t" \
  "v_add_f32 v65, v65, v184\n\t" \
  "v_exp_f32 v37, v37\n\t" \
  "v_exp_f32 v38, v38\n\t" \
  "v_mfma_f32_16x16x32_bf16 v[40:43], a[44:47], v[60:63], v[40:43]\n\t" \
  "v_add_f32 v66, v66, v184\n\t" \
  "v_exp_f32 v39, v39\n\t" \
  "v_permlane16_swap_b32 v112, v114\n\t" \
  "v_mfma_f32_16x16x32_bf16 v[44:47], a[48:51], v[48:51], v[180:183]\n\t" \
  "s_add_u32 m0, s38, 147456\n\t" \
  "v_add_f32 v67, v67, v184\n\t" \
  "v_permlane16_swap_b32 v113, v115\n\t" \
  "v_mfma_f32_16x16x32_bf16 v[44:47], a[52:55], v[52:55], v[44:47]\n\t" \
  "s_add_u32 s96, s38, 16384\n\t" \
  "ds_read_b128 a[228:231], v221 offset:24576\n\t" \
  "v_exp_f32 v40, v40\n\t" \
  "v_mfma_f32_16x16x32_bf16 v[44:47], a[56:59], v[56:59], v[44:47]\n\t" \
  "ds_read_b128 a[232:235], v222 offset:24576\n\t" \
  "v_exp_f32 v41, v41\n\t" \
  "v_mfma_f32_16x16x32_bf16 v[44:47], a[60:63], v[60:63], v[44:47]\n\t" \
  "v_exp_f32 v42, v42\n\t" \
  "v_mfma_f32_16x16x32_bf16 v[96:99], a[0:3], v[112:115], 0\n\t" \
  "buffer_load_dwordx4 v230, s[64:67], s96 offen offset:0 lds\n\t" \
  "v_mfma_f32_16x16x32_bf16 v[96:99], a[4:7], v[116:119], v[96:99]\n\t" \
  "ds_read_b128 a[236:239], v223 offset:24576\n\t" \
  "v_exp_f32 v43, v43\n\t" \
  "v_mfma_f32_16x16x32_bf16 v[96:99], a[8:11], v[120:123], v[96:99]\n\t" \
  "v_cvt_pk_bf16_f32 v128, v0, v1\n\t" \
  "v_exp_f32 v44, v44\n\t" \
  "v_mfma_f32_16x16x32_bf16 v[96:99], a[12:15], v[124:127], v[96:99]\n\t" \
  "v_exp_f32 v45, v45\n\t" \
  "v_mfma_f32_16x16x32_bf16 v[68:71], a[16:19], v[80:83], v[184:187]\n\t" \
  "v_exp_f32 v46, v46\n\t" \
  "v_mfma_f32_16x16x32_bf16 v[68:71], a[20:23], v[84:87], v[68:71]\n\t" \
  "s_nop 3\n\t" \
  "v_max3_f32 v194, v96, v97, v98\n\t" \
  "v_mfma_f32_16x16x32_bf16 v[68:71], a[24:27], v[88:91], v[68:71]\n\t" \
  "ds_read_b128 a[240:243], v220 offset:28672\n\t" \
  "v_cvt_pk_bf16_f32 v129, v2, v3\n\t" \
  "v_max_f32 v194, v194, v99\n\t" \
  "v_mov_b32 v195, v194\n\t" \
  "v_mfma_f32_16x16x32_bf16 v[68:71], a[28:31], v[92:95], v[68:71]\n\t" \
  "v_exp_f32 v47, v47\n\t" \
  "v_permlane32_swap_b32 v194, v195\n\t" \
  "v_mfma_f32_16x16x32_bf16 v[72:75], a[32:35], v[80:83], v[184:187]\n\t" \
  "v_max_f32 v194, v194, v195\n\t" \
  "v_mov_b32 v195, v194\n\t" \
  "v_mfma_f32_16x16x32_bf16 v[72:75], a[36:39], v[84:87], v[72:75]\n\t" \
  "ds_read_b128 a[244:247], v221 offset:28672\n\t" \
  "v_permlane16_swap_b32 v194, v195\n\t" \
  "v_mfma_f32_16x16x32_bf16 v[72:75], a[40:43], v[88:91], v[72:75]\n\t" \
  "v_max_f32 v194, v194, v195\n\t" \
  "v_sub_f32 v188, 0xc2000000, v194\n\t" \
  "v_mfma_f32_16x16x32_bf16 v[72:75], a[44:47], v[92:95], v[72:75]\n\t" \
  "v_sub_f32 v189, 0xc2000000, v194\n\t" \
  "v_sub_f32 v190, 0xc2000000, v194\n\t" \
  "v_mfma_f32_16x16x32_bf16 v[76:79], a[48:51], v[80:83], v[184:187]\n\t" \
  "buffer_load_dwordx4 v231, s[64:67], s96 offen offset:1024 lds\n\t" \
  "v_mfma_f32_16x16x32_bf16 v[76:79], a[52:55], v[84:87], v[76:79]\n\t" \
  "v_sub_f32 v191, 0xc2000000, v194\n\t" \
  "v_add_f32 v96, v96, v188\n\t" \
  "v_mfma_f32_16x16x32_bf16 v[76:79], a[56:59], v[88:91], v[76:79]\n\t" \
  "v_add_f32 v97, v97, v188\n\t" \
  "v_exp_f32 v64, v64\n\t" \
  "v_mfma_f32_16x16x32_bf16 v[76:79], a[60:63], v[92:95], v[76:79]\n\t" \
  "ds_read_b128 a[248:251], v222 offset:28672\n\t" \
  "v_exp_f32 v65, v65\n\t" \
  "v_mfma_f32_16x16x32_bf16 v[100:103], a[16:19], v[112:115], v[188:191]\n\t" \
  "v_add_f32 v98, v98, v188\n\t" \
  "v_add_f32 v99, v99, v188\n\t" \
  "v_mfma_f32_16x16x32_bf16 v[100:103], a[20:23], v[116:119], v[100:103]\n\t" \
  "ds_read_b128 a[252:255], v223 offset:28672\n\t" \
  "v_exp_f32 v66, v66\n\t" \
  "v_mfma_f32_16x16x32_bf16 v[100:103], a[24:27], v[120:123], v[100:103]\n\t" \
  "buffer_load_dwordx4 v230, s[64:67], s96 offen offset:2048 lds\n\t" \
  "v_mfma_f32_16x16x32_bf16 v[100:103], a[28:31], v[124:127], v[100:103]\n\t" \
  "v_cvt_pk_bf16_f32 v130, v4, v5\n\t" \
  "v_exp_f32 v67, v67\n\t" \
  "v_mfma_f32_16x16x32_bf16 v[104:107], a[32:35], v[112:115], v[188:191]\n\t" \
  "v_cvt_pk_bf16_f32 v131, v6, v7\n\t" \
  "v_cndmask_b32 v212, v214, v212, vcc\n\t" \
  "v_mfma_f32_16x16x32_bf16 v[104:107], a[36:39], v[116:119], v[104:107]\n\t" \
  "v_cvt_pk_bf16_f32 v132, v8, v9\n\t" \
  "v_exp_f32 v68, v68\n\t" \
  "v_mfma_f32_16x16x32_bf16 v[104:107], a[40:43], v[120:123], v[104:107]\n\t" \
  "v_exp_f32 v69, v69\n\t" \
  "v_mfma_f32_16x16x32_bf16 v[104:107], a[44:47], v[124:127], v[104:107]\n\t" \
  "v_cvt_pk_bf16_f32 v133, v10, v11\n\t" \
  "v_cvt_pk_bf16_f32 v134, v12, v13\n\t" \
  "v_mfma_f32_16x16x32_bf16 v[108:111], a[48:51], v[112:115], v[188:191]\n\t" \
  "v_exp_f32 v70, v70\n\t" \
  "v_mfma_f32_16x16x32_bf16 v[108:111], a[52:55], v[116:119], v[108:111]\n\t" \
  "v_exp_f32 v71, v71\n\t" \
  "v_mfma_f32_16x16x32_bf16 v[108:111], a[56:59], v[120:123], v[108:111]\n\t" \
  "v_cvt_pk_bf16_f32 v135, v14, v15\n\t" \
  "v_cvt_pk_bf16_f32 v136, v32, v33\n\t" \
  "v_mfma_f32_16x16x32_bf16 v[108:111], a[60:63], v[124:127], v[108:111]\n\t" \
  "s_waitcnt lgkmcnt(0)\n\t" \
  "buffer_load_dwordx4 v231, s[64:67], s96 offen offset:3072 lds\n\t" \
  STA("s_memrealtime s[94:95]\n\t") \
  STA("s_memtime s[92:93]\n\t") \
  "v_mfma_f32_16x16x32_bf16 v[0:3], a[192:195], v[16:19], v[176:179]\n\t" \
  "s_add_u32 s99, s53, 28672\n\t" \
  "ds_read_b64_tr_b16 a[128:129], v228 offset:0\n\t" \
  "v_cvt_pk_bf16_f32 v137, v34, v35\n\t" \
  "v_mfma_f32_16x16x32_bf16 v[0:3], a[196:199], v[20:23], v[0:3]\n\t" \
  "v_exp_f32 v72, v72\n\t" \
  "v_mfma_f32_16x16x32_bf16 v[0:3], a[200:203], v[24:27], v[0:3]\n\t" \
  "buffer_load_dword v246, v245, s[72:75], s99 offen\n\t" \
  "v_mfma_f32_16x16x32_bf16 v[0:3], a[204:207], v[28:31], v[0:3]\n\t" \
  "ds_read_b64_tr_b16 a[130:131], v228 offset:4096\n\t" \
  "v_cvt_pk_bf16_f32 v138, v36, v37\n\t" \
  "v_mfma_f32_16x16x32_bf16 v[4:7], a[220:223], v[28:31], v[176:179]\n\t" \
  "ds_read_b64_tr_b16 a[132:133], v228 offset:8192\n\t" \
  "v_cvt_pk_bf16_f32 v139, v38, v39\n\t" \
  "v_mfma_f32_16x16x32_bf16 v[4:7], a[216:219], v[24:27], v[4:7]\n\t" \
  "v_exp_f32 v73, v73\n\t" \
  "v_mfma_f32_16x16x32_bf16 v[4:7], a[212:215], v[20:23], v[4:7]\n\t" \
  "ds_read_b64_tr_b16 a[134:135], v228 offset:12288\n\t" \
  "v_cvt_pk_bf16_f32 v140, v40, v41\n\t" \
  "v_mfma_f32_16x16x32_bf16 v[4:7], a[208:211], v[16:19], v[4:7]\n\t" \
  "ds_read_b64_tr_b16 a[136:137], v229 offset:0\n\t" \
  "v_cvt_pk_bf16_f32 v141, v42, v43\n\t" \
  "v_mfma_f32_16x16x32_bf16 v[8:11], a[224:227], v[16:19], v[176:179]\n\t" \
  "v_exp_f32 v74, v74\n\t" \
  "v_mfma_f32_16x16x32_bf16 v[8:11], a[228:231], v[20:23], v[8:11]\n\t" \
  "ds_read_b64_tr_b16 a[138:139], v229 offset:4096\n\t" \
  "v_cvt_pk_bf16_f32 v142, v44, v45\n\t" \
  "v_mfma_f32_16x16x32_bf16 v[8:11], a[232:235], v[24:27], v[8:11]\n\t" \
  "v_exp_f32 v75, v75\n\t" \
  "v_mfma_f32_16x16x32_bf16 v[8:11], a[236:239], v[28:31], v[8:11]\n\t" \
  "ds_read_b64_tr_b16 a[140:141], v229 offset:8192\n\t" \
  "v_lshl_add_u32 v214, v233, 14, s41\n\t" \
  "v_mfma_f32_16x16x32_bf16 v[12:15], a[252:255], v[28:31], v[176:179]\n\t" \
  STA("v_writelane_b32 v255, s94, 26\n\t") \
  STA("v_writelane_b32 v255, s92, 27\n\t") \
  "v_exp_f32 v76, v76\n\t" \
  "v_mfma_f32_16x16x32_bf16 v[12:15], a[248:251], v[24:27], v[12:15]\n\t" \
  "ds_read_b64_tr_b16 a[142:143], v229 offset:12288\n\t" \
  "v_min_u32 v214, .Lfz_z-.Lat_t0-4, v214\n\t" \
  "v_mfma_f32_16x16x32_bf16 v[12:15], a[244:247], v[20:23], v[12:15]\n\t" \
  "s_getpc_b64 s[84:85]\n\t" \
  ".Lat_t0:\n\t" \
  "ds_read_b64_tr_b16 a[144:145], v228 offset:256\n\t" \
  "v_cvt_pk_bf16_f32 v143, v46, v47\n\t" \
  "v_mfma_f32_16x16x32_bf16 v[12:15], a[240:243], v[16:19], v[12:15]\n\t" \
  "global_load_dword v246, v214, s[84:85]\n\t" \
  "v_mfma_f32_16x16x32_bf16 v[32:35], a[192:195], v[48:51], v[180:183]\n\t" \
  "v_exp_f32 v77, v77\n\t" \
  "v_mfma_f32_16x16x32_bf16 v[32:35], a[196:199], v[52:55], v[32:35]\n\t" \
  "ds_read_b64_tr_b16 a[146:147], v228 offset:4352\n\t" \
  "v_cvt_pk_bf16_f32 v144, v64, v65\n\t" \
  "v_mfma_f32_16x16x32_bf16 v[32:35], a[200:203], v[56:59], v[32:35]\n\t" \
  "ds_read_b64_tr_b16 a[148:149], v228 offset:8448\n\t" \
  "v_exp_f32 v78, v78\n\t" \
  "v_mfma_f32_16x16x32_bf16 v[32:35], a[204:207], v[60:63], v[32:35]\n\t" \
  "v_exp_f32 v79, v79\n\t" \
  "v_mfma_f32_16x16x32_bf16 v[36:39], a[220:223], v[60:63], v[180:183]\n\t" \
  "ds_read_b64_tr_b16 a[150:151], v228 offset:12544\n\t" \
  "v_cvt_pk_bf16_f32 v145, v66, v67\n\t" \
  "v_mfma_f32_16x16x32_bf16 v[36:39], a[216:219], v[56:59], v[36:39]\n\t" \
  "v_exp_f32 v96, v96\n\t" \
  "v_mfma_f32_16x16x32_bf16 v[36:39], a[212:215], v[52:55], v[36:39]\n\t" \
  "ds_read_b64_tr_b16 a[152:153], v229 offset:256\n\t" \
  "v_cvt_pk_bf16_f32 v146, v68, v69\n\t" \
  "v_mfma_f32_16x16x32_bf16 v[36:39], a[208:211], v[48:51], v[36:39]\n\t" \
  "ds_read_b64_tr_b16 a[154:155], v229 offset:4352\n\t" \
  "v_exp_f32 v97, v97\n\t" \
  "v_mfma_f32_16x16x32_bf16 v[40:43], a[224:227], v[48:51], v[180:183]\n\t" \
  "v_exp_f32 v98, v98\n\t" \
  "v_mfma_f32_16x16x32_bf16 v[40:43], a[228:231], v[52:55], v[40:43]\n\t" \
  "ds_read_b64_tr_b16 a[156:157], v229 offset:8448\n\t" \
  "v_cvt_pk_bf16_f32 v147, v70, v71\n\t" \
  "v_mfma_f32_16x16x32_bf16 v[40:43], a[232:235], v[56:59], v[40:43]\n\t" \
  "v_exp_f32 v99, v99\n\t" \
  "v_mfma_f32_16x16x32_bf16 v[40:43], a[236:239], v[60:63], v[40:43]\n\t" \
  "ds_read_b64_tr_b16 a[158:159], v229 offset:12544\n\t" \
  "v_cvt_pk_bf16_f32 v148, v72, v73\n\t" \
  "v_mfma_f32_16x16x32_bf16 v[44:47], a[252:255], v[60:63], v[180:183]\n\t" \
  "ds_read_b64_tr_b16 a[160:161], v228 offset:512\n\t" \
  "v_cvt_pk_bf16_f32 v149, v74, v75\n\t" \
  "v_mfma_f32_16x16x32_bf16 v[44:47], a[248:251], v[56:59], v[44:47]\n\t" \
  "v_exp_f32 v100, v100\n\t" \
  "v_mfma_f32_16x16x32_bf16 v[44:47], a[244:247], v[52:55], v[44:47]\n\t" \
  "v_exp_f32 v101, v101\n\t" \
  "v_mfma_f32_16x16x32_bf16 v[44:47], a[240:243], v[48:51], v[44:47]\n\t" \
  "ds_read_b64_tr_b16 a[162:163], v228 offset:4608\n\t" \
  "v_cvt_pk_bf16_f32 v150, v76, v77\n\t" \
  "v_mfma_f32_16x16x32_bf16 v[64:67], a[192:195], v[80:83], v[184:187]\n\t" \
  "ds_read_b64_tr_b16 a[164:165], v228 offset:8704\n\t" \
  "v_cvt_pk_bf16_f32 v151, v78, v79\n\t" \
  "v_mfma_f32_16x16x32_bf16 v[64:67], a[196:199], v[84:87], v[64:67]\n\t" \
  "v_exp_f32 v102, v102\n\t" \
  "v_mfma_f32_16x16x32_bf16 v[64:67], a[200:203], v[88:91], v[64:67]\n\t" \
  "ds_read_b64_tr_b16 a[166:167], v228 offset:12800\n\t" \
  "v_cvt_pk_bf16_f32 v152, v96, v97\n\t" \
  "v_mfma_f32_16x16x32_bf16 v[64:67], a[204:207], v[92:95], v[64:67]\n\t" \
  "ds_read_b64_tr_b16 a[168:169], v229 offset:512\n\t" \
  "v_exp_f32 v103, v103\n\t" \
  "v_mfma_f32_16x16x32_bf16 v[68:71], a[220:223], v[92:95], v[184:187]\n\t" \
  "v_exp_f32 v104, v104\n\t" \
  "v_mfma_f32_16x16x32_bf16 v[68:71], a[216:219], v[88:91], v[68:71]\n\t" \
  ".Lhs6:\n\ts_cbranch_execz .Lhs6n\n\t" \
  "ds_read_b64_tr_b16 a[170:171], v229 offset:4608\n\t" \
  "v_cvt_pk_bf16_f32 v153, v98, v99\n\t" \
  "v_mfma_f32_16x16x32_bf16 v[68:71], a[212:215], v[84:87], v[68:71]\n\t" \
  "v_exp_f32 v105, v105\n\t" \
  "v_mfma_f32_16x16x32_bf16 v[68:71], a[208:211], v[80:83], v[68:71]\n\t" \
  "ds_read_b64_tr_b16 a[172:173], v229 offset:8704\n\t" \
  "v_cvt_pk_bf16_f32 v154, v100, v101\n\t" \
  "v_mfma_f32_16x16x32_bf16 v[72:75], a[224:227], v[80:83], v[184:187]\n\t" \
  "v_exp_f32 v106, v106\n\t" \
  "v_mfma_f32_16x16x32_bf16 v[72:75], a[228:231], v[84:87], v[72:75]\n\t" \
  "ds_read_b64_tr_b16 a[174:175], v229 offset:12800\n\t" \
  "v_cvt_pk_bf16_f32 v155, v102, v103\n\t" \
  "v_mfma_f32_16x16x32_bf16 v[72:75], a[232:235], v[88:91], v[72:75]\n\t" \
  "v_exp_f32 v107, v107\n\t" \
  "v_mfma_f32_16x16x32_bf16 v[72:75], a[236:239], v[92:95], v[72:75]\n\t" \
  "ds_read_b64_tr_b16 a[176:177], v228 offset:768\n\t" \
  "v_cvt_pk_bf16_f32 v156, v104, v105\n\t" \
  "v_mfma_f32_16x16x32_bf16 v[76:79], a[252:255], v[92:95], v[184:187]\n\t" \
  "v_exp_f32 v108, v108\n\t" \
  "v_mfma_f32_16x16x32_bf16 v[76:79], a[248:251], v[88:91], v[76:79]\n\t" \
  "ds_read_b64_tr_b16 a[178:179], v228 offset:4864\n\t" \
  "v_cvt_pk_bf16_f32 v157, v106, v107\n\t" \
  "v_mfma_f32_16x16x32_bf16 v[76:79], a[244:247], v[84:87], v[76:79]\n\t" \
  "v_exp_f32 v109, v109\n\t" \
  "v_mfma_f32_16x16x32_bf16 v[76:79], a[240:243], v[80:83], v[76:79]\n\t" \
  "ds_read_b64_tr_b16 a[180:181], v228 offset:8960\n\t" \
  "v_cvt_pk_bf16_f32 v158, v108, v109\n\t" \
  "v_mfma_f32_16x16x32_bf16 v[96:99], a[192:195], v[112:115], v[188:191]\n\t" \
  "v_exp_f32 v110, v110\n\t" \
  "v_mfma_f32_16x16x32_bf16 v[96:99], a[196:199], v[116:119], v[96:99]\n\t" \
  "s_add_u32 s99, s53, 43008\n\t" \
  "v_exp_f32 v111, v111\n\t" \
  "v_mfma_f32_16x16x32_bf16 v[96:99], a[200:203], v[120:123], v[96:99]\n\t" \
  "ds_read_b64_tr_b16 a[182:183], v228 offset:13056\n\t" \
  "v_cvt_pk_bf16_f32 v159, v110, v111\n\t" \
  "v_mfma_f32_16x16x32_bf16 v[96:99], a[204:207], v[124:127], v[96:99]\n\t" \
  "v_exp_f32 v0, v0\n\t" \
  "v_mfma_f32_16x16x32_bf16 v[100:103], a[220:223], v[124:127], v[188:191]\n\t" \
  "buffer_load_dword v246, v245, s[72:75], s99 offen\n\t" \
  "v_mfma_f32_16x16x32_bf16 v[100:103], a[216:219], v[120:123], v[100:103]\n\t" \
  "ds_read_b64_tr_b16 a[184:185], v229 offset:768\n\t" \
  "v_mfma_f32_16x16x32_bf16 v[100:103], a[212:215], v[116:119], v[100:103]\n\t" \
  "ds_read_b64_tr_b16 a[186:187], v229 offset:4864\n\t" \
  "v_mfma_f32_16x16x32_bf16 v[100:103], a[208:211], v[112:115], v[100:103]\n\t" \
  "ds_read_b64_tr_b16 a[188:189], v229 offset:8960\n\t" \
  "v_mfma_f32_16x16x32_bf16 v[104:107], a[224:227], v[112:115], v[188:191]\n\t" \
  "v_exp_f32 v1, v1\n\t" \
  "v_mfma_f32_16x16x32_bf16 v[104:107], a[228:231], v[116:119], v[104:107]\n\t" \
  "ds_read_b64_tr_b16 a[190:191], v229 offset:13056\n\t" \
  "v_mfma_f32_16x16x32_bf16 v[104:107], a[232:235], v[120:123], v[104:107]\n\t" \
  "v_exp_f32 v2, v2\n\t" \
  "v_mfma_f32_16x16x32_bf16 v[104:107], a[236:239], v[124:127], v[104:107]\n\t" \
  "v_exp_f32 v3, v3\n\t" \
  "v_mfma_f32_16x16x32_bf16 v[108:111], a[252:255], v[124:127], v[188:191]\n\t" \
  "v_exp_f32 v4, v4\n\t" \
  "v_mfma_f32_16x16x32_bf16 v[108:111], a[248:251], v[120:123], v[108:111]\n\t" \
  "v_exp_f32 v5, v5\n\t" \
  "v_mfma_f32_16x16x32_bf16 v[108:111], a[244:247], v[116:119], v[108:111]\n\t" \
  "s_waitcnt vmcnt(3) lgkmcnt(0)\n\t" \
  "v_exp_f32 v6, v6\n\t" \
  "v_mfma_f32_16x16x32_bf16 v[108:111], a[240:243], v[112:115], v[108:111]\n\t" \
  "s_barrier\n\t" \
  STA("s_memrealtime s[94:95]\n\t") \
  STA("s_memtime s[92:93]\n\t") \
  "v_mfma_f32_16x16x32_bf16 a[0:3], a[128:131], v[128:131], 0\n\t" \
  "v_exp_f32 v7, v7\n\t" \
  "v_mfma_f32_16x16x32_bf16 a[0:3], a[132:135], v[132:135], a[0:3]\n\t" \
  "v_exp_f32 v8, v8\n\t" \
  "v_mfma_f32_16x16x32_bf16 a[4:7], a[136:139], v[128:131], 0\n\t" \
  "v_exp_f32 v9, v9\n\t" \
  "v_mfma_f32_16x16x32_bf16 a[4:7], a[140:143], v[132:135], a[4:7]\n\t" \
  "v_exp_f32 v10, v10\n\t" \
  "v_mfma_f32_16x16x32_bf16 a[8:11], a[144:147], v[128:131], 0\n\t" \
  "v_exp_f32 v11, v11\n\t" \
  "v_mfma_f32_16x16x32_bf16 a[8:11], a[148:151], v[132:135], a[8:11]\n\t" \
  "v_exp_f32 v12, v12\n\t" \
  "v_mfma_f32_16x16x32_bf16 a[12:15], a[152:155], v[128:131], 0\n\t" \
  "v_exp_f32 v13, v13\n\t" \
  "v_mfma_f32_16x16x32_bf16 a[12:15], a[156:159], v[132:135], a[12:15]\n\t" \
  "v_exp_f32 v14, v14\n\t" \
  "v_mfma_f32_16x16x32_bf16 a[16:19], a[160:163], v[128:131], 0\n\t" \
  "v_exp_f32 v15, v15\n\t" \
  "v_mfma_f32_16x16x32_bf16 a[16:19], a[164:167], v[132:135], a[16:19]\n\t" \
  "v_exp_f32 v32, v32\n\t" \
  "v_mfma_f32_16x16x32_bf16 a[20:23], a[168:171], v[128:131], 0\n\t" \
  "v_exp_f32 v33, v33\n\t" \
  "v_mfma_f32_16x16x32_bf16 a[20:23], a[172:175], v[132:135], a[20:23]\n\t" \
  "v_exp_f32 v34, v34\n\t" \
  "v_mfma_f32_16x16x32_bf16 a[24:27], a[176:179], v[128:131], 0\n\t" \
  STA("v_writelane_b32 v255, s94, 28\n\t") \
  STA("v_writelane_b32 v255, s92, 29\n\t") \
  "v_exp_f32 v35, v35\n\t" \
  "v_mfma_f32_16x16x32_bf16 a[24:27], a[180:183], v[132:135], a[24:27]\n\t" \
  "v_exp_f32 v36, v36\n\t" \
  "v_mfma_f32_16x16x32_bf16 a[28:31], a[184:187], v[128:131], 0\n\t" \
  "v_exp_f32 v37, v37\n\t" \
  "v_mfma_f32_16x16x32_bf16 a[28:31], a[188:191], v[132:135], a[28:31]\n\t" \
  "v_exp_f32 v38, v38\n\t" \
  "v_mfma_f32_16x16x32_bf16 v[160:163], v[216:219], v[128:131], 0\n\t" \
  "s_add_u32 m0, s38, 65536\n\t" \
  "v_exp_f32 v39, v39\n\t" \
  "v_mfma_f32_16x16x32_bf16 v[160:163], v[216:219], v[132:135], v[160:163]\n\t" \
  "s_add_u32 s96, s38, 49152\n\t" \
  "ds_read_b128 a[192:195], v220 offset:0\n\t" \
  "v_cvt_pk_bf16_f32 v128, v0, v1\n\t" \
  "v_mfma_f32_16x16x32_bf16 a[32:35], a[128:131], v[136:139], 0\n\t" \
  "ds_read_b128 a[196:199], v221 offset:0\n\t" \
  "v_cvt_pk_bf16_f32 v129, v2, v3\n\t" \
  "v_mfma_f32_16x16x32_bf16 a[32:35], a[132:135], v[140:143], a[32:35]\n\t" \
  "v_exp_f32 v40, v40\n\t" \
  "v_mfma_f32_16x16x32_bf16 a[36:39], a[136:139], v[136:139], 0\n\t" \
  "buffer_load_dwordx4 v224, s[60:63], s96 offen offset:0 lds\n\t" \
  "v_mfma_f32_16x16x32_bf16 a[36:39], a[140:143], v[140:143], a[36:39]\n\t" \
  "ds_read_b128 a[200:203], v222 offset:0\n\t" \
  "v_cvt_pk_bf16_f32 v130, v4, v5\n\t" \
  "v_mfma_f32_16x16x32_bf16 a[40:43], a[144:147], v[136:139], 0\n\t" \
  "v_exp_f32 v41, v41\n\t" \
  "v_mfma_f32_16x16x32_bf16 a[40:43], a[148:151], v[140:143], a[40:43]\n\t" \
  "ds_read_b128 a[204:207], v223 offset:0\n\t" \
  "v_cvt_pk_bf16_f32 v131, v6, v7\n\t" \
  "v_mfma_f32_16x16x32_bf16 a[44:47], a[152:155], v[136:139], 0\n\t" \
  "v_exp_f32 v42, v42\n\t" \
  "v_mfma_f32_16x16x32_bf16 a[44:47], a[156:159], v[140:143], a[44:47]\n\t" \
  "v_exp_f32 v43, v43\n\t" \
  "v_mfma_f32_16x16x32_bf16 a[48:51], a[160:163], v[136:139], 0\n\t" \
  "buffer_load_dwordx4 v225, s[60:63], s96 offen offset:1024 lds\n\t" \
  "v_mfma_f32_16x16x32_bf16 a[48:51], a[164:167], v[140:143], a[48:51]\n\t" \
  "ds_read_b128 a[208:211], v220 offset:4096\n\t" \
  "v_cvt_pk_bf16_f32 v132, v8, v9\n\t" \
  "v_mfma_f32_16x16x32_bf16 a[52:55], a[168:171], v[136:139], 0\n\t" \
  "v_exp_f32 v44, v44\n\t" \
  "v_mfma_f32_16x16x32_bf16 a[52:55], a[172:175], v[140:143], a[52:55]\n\t" \
  "ds_read_b128 a[212:215], v221 offset:4096\n\t" \
  "v_cvt_pk_bf16_f32 v133, v10, v11\n\t" \
  "v_mfma_f32_16x16x32_bf16 a[56:59], a[176:179], v[136:139], 0\n\t" \
  "v_exp_f32 v45, v45\n\t" \
  "v_mfma_f32_16x16x32_bf16 a[56:59], a[180:183], v[140:143], a[56:59]\n\t" \
  "v_exp_f32 v46, v46\n\t" \
  "v_mfma_f32_16x16x32_bf16 a[60:63], a[184:187], v[136:139], 0\n\t" \
  "buffer_load_dwordx4 v226, s[60:63], s96 offen offset:2048 lds\n\t" \
  "v_mfma_f32_16x16x32_bf16 a[60:63], a[188:191], v[140:143], a[60:63]\n\t" \
  "ds_read_b128 a[216:219], v222 offset:4096\n\t" \
  "v_cvt_pk_bf16_f32 v134, v12, v13\n\t" \
  "v_mfma_f32_16x16x32_bf16 v[164:167], v[216:219], v[136:139], 0\n\t" \
  "v_exp_f32 v47, v47\n\t" \
  "v_mfma_f32_16x16x32_bf16 v[164:167], v[216:219], v[140:143], v[164:167]\n\t" \
  "ds_read_b128 a[220:223], v223 offset:4096\n\t" \
  "v_cvt_pk_bf16_f32 v135, v14, v15\n\t" \
  "v_mfma_f32_16x16x32_bf16 a[64:67], a[128:131], v[144:147], 0\n\t" \
  "v_exp_f32 v64, v64\n\t" \
  "v_mfma_f32_16x16x32_bf16 a[64:67], a[132:135], v[148:151], a[64:67]\n\t" \
  "v_exp_f32 v65, v65\n\t" \
  "v_mfma_f32_16x16x32_bf16 a[68:71], a[136:139], v[144:147], 0\n\t" \
  "buffer_load_dwordx4 v227, s[60:63], s96 offen offset:3072 lds\n\t" \
  "v_mfma_f32_16x16x32_bf16 a[68:71], a[140:143], v[148:151], a[68:71]\n\t" \
  "ds_read_b128 a[224:227], v220 offset:8192\n\t" \
  "v_cvt_pk_bf16_f32 v136, v32, v33\n\t" \
  "v_mfma_f32_16x16x32_bf16 a[72:75], a[144:147], v[144:147], 0\n\t" \
  "s_add_u32 m0, s38, 81920\n\t" \
  "v_exp_f32 v66, v66\n\t" \
  "v_mfma_f32_16x16x32_bf16 a[72:75], a[148:151], v[148:151], a[72:75]\n\t" \
  "s_add_u32 s96, s38, 32768\n\t" \
  "ds_read_b128 a[228:231], v221 offset:8192\n\t" \
  "v_cvt_pk_bf16_f32 v137, v34, v35\n\t" \
  "v_mfma_f32_16x16x32_bf16 a[76:79], a[152:155], v[144:147], 0\n\t" \
  "v_exp_f32 v67, v67\n\t" \
  "v_mfma_f32_16x16x32_bf16 a[76:79], a[156:159], v[148:151], a[76:79]\n\t" \
  "v_exp_f32 v68, v68\n\t" \
  "v_mfma_f32_16x16x32_bf16 a[80:83], a[160:163], v[144:147], 0\n\t" \
  "buffer_load_dwordx4 v230, s[64:67], s96 offen offset:0 lds\n\t" \
  "v_mfma_f32_16x16x32_bf16 a[80:83], a[164:167], v[148:151], a[80:83]\n\t" \
  "ds_read_b128 a[232:235], v222 offset:8192\n\t" \
  "v_cvt_pk_bf16_f32 v138, v36, v37\n\t" \
  "v_mfma_f32_16x16x32_bf16 a[84:87], a[168:171], v[144:147], 0\n\t" \
  "v_exp_f32 v69, v69\n\t" \
  "v_mfma_f32_16x16x32_bf16 a[84:87], a[172:175], v[148:151], a[84:87]\n\t" \
  "ds_read_b128 a[236:239], v223 offset:8192\n\t" \
  "v_cvt_pk_bf16_f32 v139, v38, v39\n\t" \
  "v_mfma_f32_16x16x32_bf16 a[88:91], a[176:179], v[144:147], 0\n\t" \
  "v_exp_f32 v70, v70\n\t" \
  "v_mfma_f32_16x16x32_bf16 a[88:91], a[180:183], v[148:151], a[88:91]\n\t" \
  "v_exp_f32 v71, v71\n\t" \
  "v_mfma_f32_16x16x32_bf16 a[92:95], a[184:187], v[144:147], 0\n\t" \
  "buffer_load_dwordx4 v231, s[64:67], s96 offen offset:1024 lds\n\t" \
  "v_mfma_f32_16x16x32_bf16 a[92:95], a[188:191], v[148:151], a[92:95]\n\t" \
  "ds_read_b128 a[240:243], v220 offset:12288\n\t" \
  "v_cvt_pk_bf16_f32 v140, v40, v41\n\t" \
  "v_mfma_f32_16x16x32_bf16 v[168:171], v[216:219], v[144:147], 0\n\t" \
  "v_exp_f32 v72, v72\n\t" \
  "v_mfma_f32_16x16x32_bf16 v[168:171], v[216:219], v[148:151], v[168:171]\n\t" \
  "ds_read_b128 a[244:247], v221 offset:12288\n\t" \
  "v_cvt_pk_bf16_f32 v141, v42, v43\n\t" \
  "v_mfma_f32_16x16x32_bf16 a[96:99], a[128:131], v[152:155], 0\n\t" \
  "v_exp_f32 v73, v73\n\t" \
  "v_mfma_f32_16x16x32_bf16 a[96:99], a[132:135], v[156:159], a[96:99]\n\t" \
  "v_exp_f32 v74, v74\n\t" \
  "v_mfma_f32_16x16x32_bf16 a[100:103], a[136:139], v[152:155], 0\n\t" \
  "buffer_load_dwordx4 v230, s[64:67], s96 offen offset:2048 lds\n\t" \
  "v_mfma_f32_16x16x32_bf16 a[100:103], a[140:143], v[156:159], a[100:103]\n\t" \
  "ds_read_b128 a[248:251], v222 offset:12288\n\t" \
  "v_cvt_pk_bf16_f32 v142, v44, v45\n\t" \
  "v_mfma_f32_16x16x32_bf16 a[104:107], a[144:147], v[152:155], 0\n\t" \
  "v_exp_f32 v75, v75\n\t" \
  "v_mfma_f32_16x16x32_bf16 a[104:107], a[148:151], v[156:159], a[104:107]\n\t" \
  "ds_read_b128 a[252:255], v223 offset:12288\n\t" \
  "v_cvt_pk_bf16_f32 v143, v46, v47\n\t" \
  "v_mfma_f32_16x16x32_bf16 a[108:111], a[152:155], v[152:155], 0\n\t" \
  "v_exp_f32 v76, v76\n\t" \
  "v_mfma_f32_16x16x32_bf16 a[108:111], a[156:159], v[156:159], a[108:111]\n\t" \
  "v_exp_f32 v77, v77\n\t" \
  "v_mfma_f32_16x16x32_bf16 a[112:115], a[160:163], v[152:155], 0\n\t" \
  "buffer_load_dwordx4 v231, s[64:67], s96 offen offset:3072 lds\n\t" \
  "v_mfma_f32_16x16x32_bf16 a[112:115], a[164:167], v[156:159], a[112:115]\n\t" \
  "v_exp_f32 v78, v78\n\t" \
  "v_mfma_f32_16x16x32_bf16 a[116:119], a[168:171], v[152:155], 0\n\t" \
  "v_exp_f32 v79, v79\n\t" \
  "v_mfma_f32_16x16x32_bf16 a[116:119], a[172:175], v[156:159], a[116:119]\n\t" \
  "v_exp_f32 v96, v96\n\t" \
  "v_mfma_f32_16x16x32_bf16 a[120:123], a[176:179], v[152:155], 0\n\t" \
  "v_cvt_pk_bf16_f32 v144, v64, v65\n\t" \
  "v_cvt_pk_bf16_f32 v145, v66, v67\n\t" \
  "v_mfma_f32_16x16x32_bf16 a[120:123], a[180:183], v[156:159], a[120:123]\n\t" \
  "v_exp_f32 v97, v97\n\t" \
  "v_mfma_f32_16x16x32_bf16 a[124:127], a[184:187], v[152:155], 0\n\t" \
  "v_exp_f32 v98, v98\n\t" \
  "v_mfma_f32_16x16x32_bf16 a[124:127], a[188:191], v[156:159], a[124:127]\n\t" \
  "v_cvt_pk_bf16_f32 v146, v68, v69\n\t" \
  "v_cvt_pk_bf16_f32 v147, v70, v71\n\t" \
  "v_mfma_f32_16x16x32_bf16 v[172:175], v[216:219], v[152:155], 0\n\t" \
  "v_exp_f32 v99, v99\n\t" \
  "v_mfma_f32_16x16x32_bf16 v[172:175], v[216:219], v[156:159], v[172:175]\n\t" \
  "s_waitcnt lgkmcnt(12)\n\t" \
  "v_exp_f32 v100, v100\n\t" \
  STA("s_memrealtime s[94:95]\n\t") \
  STA("s_memtime s[92:93]\n\t") \
  "v_mfma_f32_16x16x32_bf16 v[0:3], a[192:195], v[16:19], v[176:179]\n\t" \
  "ds_read_b64_tr_b16 a[128:129], v210 offset:0\n\t" \
  "v_cvt_pk_bf16_f32 v148, v72, v73\n\t" \
  "v_mfma_f32_16x16x32_bf16 v[0:3], a[196:199], v[20:23], v[0:3]\n\t" \
  "v_exp_f32 v101, v101\n\t" \
  "v_mfma_f32_16x16x32_bf16 v[0:3], a[200:203], v[24:27], v[0:3]\n\t" \
  "ds_read_b64_tr_b16 a[130:131], v210 offset:4096\n\t" \
  "v_cvt_pk_bf16_f32 v149, v74, v75\n\t" \
  "v_mfma_f32_16x16x32_bf16 v[0:3], a[204:207], v[28:31], v[0:3]\n\t" \
  "s_waitcnt lgkmcnt(10)\n\t" \
  "v_exp_f32 v102, v102\n\t" \
  "v_mfma_f32_16x16x32_bf16 v[4:7], a[220:223], v[28:31], v[176:179]\n\t" \
  "ds_read_b64_tr_b16 a[132:133], v210 offset:8192\n\t" \
  "v_cvt_pk_bf16_f32 v150, v76, v77\n\t" \
  "v_mfma_f32_16x16x32_bf16 v[4:7], a[216:219], v[24:27], v[4:7]\n\t" \
  "v_exp_f32 v103, v103\n\t" \
  "v_mfma_f32_16x16x32_bf16 v[4:7], a[212:215], v[20:23], v[4:7]\n\t" \
  "ds_read_b64_tr_b16 a[134:135], v210 offset:12288\n\t" \
  "v_cvt_pk_bf16_f32 v151, v78, v79\n\t" \
  "v_mfma_f32_16x16x32_bf16 v[4:7], a[208:211], v[16:19], v[4:7]\n\t" \
  "s_waitcnt lgkmcnt(8)\n\t" \
  "ds_read_b64_tr_b16 a[136:137], v211 offset:0\n\t" \
  "v_cvt_pk_bf16_f32 v152, v96, v97\n\t" \
  "v_mfma_f32_16x16x32_bf16 v[8:11], a[224:227], v[16:19], v[176:179]\n\t" \
  "v_exp_f32 v104, v104\n\t" \
  "v_mfma_f32_16x16x32_bf16 v[8:11], a[228:231], v[20:23], v[8:11]\n\t" \
  "ds_read_b64_tr_b16 a[138:139], v211 offset:4096\n\t" \
  "v_cvt_pk_bf16_f32 v153, v98, v99\n\t" \
  "v_mfma_f32_16x16x32_bf16 v[8:11], a[232:235], v[24:27], v[8:11]\n\t" \
  "v_exp_f32 v105, v105\n\t" \
  "v_mfma_f32_16x16x32_bf16 v[8:11], a[236:239], v[28:31], v[8:11]\n\t" \
  "s_waitcnt lgkmcnt(6)\n\t" \
  "ds_read_b64_tr_b16 a[140:141], v211 offset:8192\n\t" \
  "v_cvt_pk_bf16_f32 v154, v100, v101\n\t" \
  "v_mfma_f32_16x16x32_bf16 v[12:15], a[252:255], v[28:31], v[176:179]\n\t" \
  STA("v_writelane_b32 v255, s94, 30\n\t") \
  STA("v_writelane_b32 v255, s92, 31\n\t") \
  "v_exp_f32 v106, v106\n\t" \
  "v_mfma_f32_16x16x32_bf16 v[12:15], a[248:251], v[24:27], v[12:15]\n\t" \
  "ds_read_b64_tr_b16 a[142:143], v211 offset:12288\n\t" \
  "v_cvt_pk_bf16_f32 v155, v102, v103\n\t" \
  "v_mfma_f32_16x16x32_bf16 v[12:15], a[244:247], v[20:23], v[12:15]\n\t" \
  "ds_read_b64_tr_b16 a[144:145], v210 offset:256\n\t" \
  "v_cvt_pk_bf16_f32 v156, v104, v105\n\t" \
  "v_mfma_f32_16x16x32_bf16 v[12:15], a[240:243], v[16:19], v[12:15]\n\t" \
  "v_exp_f32 v107, v107\n\t" \
  "v_mfma_f32_16x16x32_bf16 v[32:35], a[192:195], v[48:51], v[180:183]\n\t" \
  "s_add_u32 m0, s38, 16384\n\t" \
  "ds_read_b64_tr_b16 a[146:147], v210 offset:4352\n\t" \
  "v_cvt_pk_bf16_f32 v157, v106, v107\n\t" \
  "v_mfma_f32_16x16x32_bf16 v[32:35], a[196:199], v[52:55], v[32:35]\n\t" \
  "s_mov_b32 s96, 0\n\t" \
  "v_exp_f32 v108, v108\n\t" \
  "v_mfma_f32_16x16x32_bf16 v[32:35], a[200:203], v[56:59], v[32:35]\n\t" \
  "v_exp_f32 v109, v109\n\t" \
  "v_mfma_f32_16x16x32_bf16 v[32:35], a[204:207], v[60:63], v[32:35]\n\t" \
  "ds_read_b64_tr_b16 a[148:149], v210 offset:8448\n\t" \
  "v_cvt_pk_bf16_f32 v158, v108, v109\n\t" \
  "v_mfma_f32_16x16x32_bf16 v[36:39], a[220:223], v[60:63], v[180:183]\n\t" \
  "buffer_load_dwordx4 v232, s[68:71], s96 offen offset:0 lds\n\t" \
  "v_mfma_f32_16x16x32_bf16 v[36:39], a[216:219], v[56:59], v[36:39]\n\t" \
  "v_exp_f32 v110, v110\n\t" \
  "v_mfma_f32_16x16x32_bf16 v[36:39], a[212:215], v[52:55], v[36:39]\n\t" \
  "v_exp_f32 v111, v111\n\t" \
  "v_mfma_f32_16x16x32_bf16 v[36:39], a[208:211], v[48:51], v[36:39]\n\t" \
  "ds_read_b64_tr_b16 a[150:151], v210 offset:12544\n\t" \
  "v_cvt_pk_bf16_f32 v159, v110, v111\n\t" \
  "v_mfma_f32_16x16x32_bf16 v[40:43], a[224:227], v[48:51], v[180:183]\n\t" \
  "v_exp_f32 v0, v0\n\t" \
  "v_mfma_f32_16x16x32_bf16 v[40:43], a[228:231], v[52:55], v[40:43]\n\t" \
  "ds_read_b64_tr_b16 a[152:153], v211 offset:256\n\t" \
  "v_mfma_f32_16x16x32_bf16 v[40:43], a[232:235], v[56:59], v[40:43]\n\t" \
  "ds_read_b64_tr_b16 a[154:155], v211 offset:4352\n\t" \
  "s_mov_b32 s96, 6144\n\t" \
  "v_mfma_f32_16x16x32_bf16 v[40:43], a[236:239], v[60:63], v[40:43]\n\t" \
  "v_exp_f32 v1, v1\n\t" \
  "v_mfma_f32_16x16x32_bf16 v[44:47], a[252:255], v[60:63], v[180:183]\n\t" \
  "ds_read_b64_tr_b16 a[156:157], v211 offset:8448\n\t" \
  "v_mfma_f32_16x16x32_bf16 v[44:47], a[248:251], v[56:59], v[44:47]\n\t" \
  "v_exp_f32 v2, v2\n\t" \
  "v_mfma_f32_16x16x32_bf16 v[44:47], a[244:247], v[52:55], v[44:47]\n\t" \
  "buffer_load_dwordx4 v232, s[68:71], s96 offen offset:1024 lds\n\t" \
  "v_mfma_f32_16x16x32_bf16 v[44:47], a[240:243], v[48:51], v[44:47]\n\t" \
  "ds_read_b64_tr_b16 a[158:159], v211 offset:12544\n\t" \
  "v_mfma_f32_16x16x32_bf16 v[64:67], a[192:195], v[80:83], v[184:187]\n\t" \
  "ds_read_b64_tr_b16 a[160:161], v210 offset:512\n\t" \
  "v_mfma_f32_16x16x32_bf16 v[64:67], a[196:199], v[84:87], v[64:67]\n\t" \
  "ds_read_b64_tr_b16 a[162:163], v210 offset:4608\n\t" \
  "v_mfma_f32_16x16x32_bf16 v[64:67], a[200:203], v[88:91], v[64:67]\n\t" \
  "v_exp_f32 v3, v3\n\t" \
  "v_mfma_f32_16x16x32_bf16 v[64:67], a[204:207], v[92:95], v[64:67]\n\t" \
  "ds_read_b64_tr_b16 a[164:165], v210 offset:8704\n\t" \
  "v_mfma_f32_16x16x32_bf16 v[68:71], a[220:223], v[92:95], v[184:187]\n\t" \
  "ds_read_b64_tr_b16 a[166:167], v210 offset:12800\n\t" \
  "s_mov_b32 s96, 12288\n\t" \
  "v_mfma_f32_16x16x32_bf16 v[68:71], a[216:219], v[88:91], v[68:71]\n\t" \
  "v_exp_f32 v4, v4\n\t" \
  "v_mfma_f32_16x16x32_bf16 v[68:71], a[212:215], v[84:87], v[68:71]\n\t" \
  "ds_read_b64_tr_b16 a[168:169], v211 offset:512\n\t" \
  "v_mfma_f32_16x16x32_bf16 v[68:71], a[208:211], v[80:83], v[68:71]\n\t" \
  "v_exp_f32 v5, v5\n\t" \
  "v_mfma_f32_16x16x32_bf16 v[72:75], a[224:227], v[80:83], v[184:187]\n\t" \
  "buffer_load_dwordx4 v232, s[68:71], s96 offen offset:2048 lds\n\t" \
  "v_mfma_f32_16x16x32_bf16 v[72:75], a[228:231], v[84:87], v[72:75]\n\t" \
  "ds_read_b64_tr_b16 a[170:171], v211 offset:4608\n\t" \
  "v_mfma_f32_16x16x32_bf16 v[72:75], a[232:235], v[88:91], v[72:75]\n\t" \
  "ds_read_b64_tr_b16 a[172:173], v211 offset:8704\n\t" \
  "v_mfma_f32_16x16x32_bf16 v[72:75], a[236:239], v[92:95], v[72:75]\n\t" \
  "ds_read_b64_tr_b16 a[174:175], v211 offset:12800\n\t" \
  "v_mfma_f32_16x16x32_bf16 v[76:79], a[252:255], v[92:95], v[184:187]\n\t" \
  "v_exp_f32 v6, v6\n\t" \
  "v_mfma_f32_16x16x32_bf16 v[76:79], a[248:251], v[88:91], v[76:79]\n\t" \
  "ds_read_b64_tr_b16 a[176:177], v210 offset:768\n\t" \
  "v_mfma_f32_16x16x32_bf16 v[76:79], a[244:247], v[84:87], v[76:79]\n\t" \
  "s_mov_b32 s96, 18432\n\t" \
  "v_exp_f32 v7, v7\n\t" \
  "v_mfma_f32_16x16x32_bf16 v[76:79], a[240:243], v[80:83], v[76:79]\n\t" \
  "ds_read_b64_tr_b16 a[178:179], v210 offset:4864\n\t" \
  "v_mfma_f32_16x16x32_bf16 v[96:99], a[192:195], v[112:115], v[188:191]\n\t" \
  "ds_read_b64_tr_b16 a[180:181], v210 offset:8960\n\t" \
  "v_mfma_f32_16x16x32_bf16 v[96:99], a[196:199], v[116:119], v[96:99]\n\t" \
  "v_exp_f32 v8, v8\n\t" \
  "v_mfma_f32_16x16x32_bf16 v[96:99], a[200:203], v[120:123], v[96:99]\n\t" \
  "buffer_load_dwordx4 v232, s[68:71], s96 offen offset:3072 lds\n\t" \
  "v_mfma_f32_16x16x32_bf16 v[96:99], a[204:207], v[124:127], v[96:99]\n\t" \
  "ds_read_b64_tr_b16 a[182:183], v210 offset:13056\n\t" \
  "v_mfma_f32_16x16x32_bf16 v[100:103], a[220:223], v[124:127], v[188:191]\n\t" \
  "ds_read_b64_tr_b16 a[184:185], v211 offset:768\n\t" \
  "v_mfma_f32_16x16x32_bf16 v[100:103], a[216:219], v[120:123], v[100:103]\n\t" \
  "ds_read_b64_tr_b16 a[186:187], v211 offset:4864\n\t" \
  "v_mfma_f32_16x16x32_bf16 v[100:103], a[212:215], v[116:119], v[100:103]\n\t" \
  "ds_read_b64_tr_b16 a[188:189], v211 offset:8960\n\t" \
  "v_mfma_f32_16x16x32_bf16 v[100:103], a[208:211], v[112:115], v[100:103]\n\t" \
  "v_exp_f32 v9, v9\n\t" \
  "v_mfma_f32_16x16x32_bf16 v[104:107], a[224:227], v[112:115], v[188:191]\n\t" \
  "ds_read_b64_tr_b16 a[190:191], v211 offset:13056\n\t" \
  "v_mfma_f32_16x16x32_bf16 v[104:107], a[228:231], v[116:119], v[104:107]\n\t" \
  "v_exp_f32 v10, v10\n\t" \
  "v_mfma_f32_16x16x32_bf16 v[104:107], a[232:235], v[120:123], v[104:107]\n\t" \
  "v_exp_f32 v11, v11\n\t" \
  "v_mfma_f32_16x16x32_bf16 v[104:107], a[236:239], v[124:127], v[104:107]\n\t" \
  "v_exp_f32 v12, v12\n\t" \
  "v_mfma_f32_16x16x32_bf16 v[108:111], a[252:255], v[124:127], v[188:191]\n\t" \
  "v_exp_f32 v13, v13\n\t" \
  "v_mfma_f32_16x16x32_bf16 v[108:111], a[248:251], v[120:123], v[108:111]\n\t" \
  "v_exp_f32 v14, v14\n\t" \
  "v_mfma_f32_16x16x32_bf16 v[108:111], a[244:247], v[116:119], v[108:111]\n\t" \
  "s_waitcnt vmcnt(4) lgkmcnt(0)\n\t" \
  "v_exp_f32 v15, v15\n\t" \
  "v_mfma_f32_16x16x32_bf16 v[108:111], a[240:243], v[112:115], v[108:111]\n\t" \
  "s_barrier\n\t" \
  STA("s_memrealtime s[94:95]\n\t") \
  STA("s_memtime s[92:93]\n\t") \
  "v_mfma_f32_16x16x32_bf16 a[0:3], a[128:131], v[128:131], a[0:3]\n\t" \
  "v_exp_f32 v32, v32\n\t" \
  "v_mfma_f32_16x16x32_bf16 a[0:3], a[132:135], v[132:135], a[0:3]\n\t" \
  "v_exp_f32 v33, v33\n\t" \
  "v_mfma_f32_16x16x32_bf16 a[4:7], a[136:139], v[128:131], a[4:7]\n\t" \
  "v_exp_f32 v34, v34\n\t" \
  "v_mfma_f32_16x16x32_bf16 a[4:7], a[140:143], v[132:135], a[4:7]\n\t" \
  "v_exp_f32 v35, v35\n\t" \
  "v_mfma_f32_16x16x32_bf16 a[8:11], a[144:147], v[128:131], a[8:11]\n\t" \
  "v_exp_f32 v36, v36\n\t" \
  "v_mfma_f32_16x16x32_bf16 a[8:11], a[148:151], v[132:135], a[8:11]\n\t" \
  "v_exp_f32 v37, v37\n\t" \
  "v_mfma_f32_16x16x32_bf16 a[12:15], a[152:155], v[128:131], a[12:15]\n\t" \
  "v_exp_f32 v38, v38\n\t" \
  "v_mfma_f32_16x16x32_bf16 a[12:15], a[156:159], v[132:135], a[12:15]\n\t" \
  "v_exp_f32 v39, v39\n\t" \
  "v_mfma_f32_16x16x32_bf16 a[16:19], a[160:163], v[128:131], a[16:19]\n\t" \
  "v_exp_f32 v40, v40\n\t" \
  "v_mfma_f32_16x16x32_bf16 a[16:19], a[164:167], v[132:135], a[16:19]\n\t" \
  "v_exp_f32 v41, v41\n\t" \
  "v_mfma_f32_16x16x32_bf16 a[20:23], a[168:171], v[128:131], a[20:23]\n\t" \
  "v_exp_f32 v42, v42\n\t" \
  "v_mfma_f32_16x16x32_bf16 a[20:23], a[172:175], v[132:135], a[20:23]\n\t" \
  "v_exp_f32 v43, v43\n\t" \
  "v_mfma_f32_16x16x32_bf16 a[24:27], a[176:179], v[128:131], a[24:27]\n\t" \
  STA("v_writelane_b32 v255, s94, 32\n\t") \
  STA("v_writelane_b32 v255, s92, 33\n\t") \
  "buffer_store_dword v233, v247, s[16:19], 0 offen sc1\n\t" \
  "ds_read_b128 a[192:195], v220 offset:16384\n\t" \
  "v_mfma_f32_16x16x32_bf16 a[24:27], a[180:183], v[132:135], a[24:27]\n\t" \
  "v_exp_f32 v44, v44\n\t" \
  "v_mfma_f32_16x16x32_bf16 a[28:31], a[184:187], v[128:131], a[28:31]\n\t" \
  "v_exp_f32 v45, v45\n\t" \
  "v_mfma_f32_16x16x32_bf16 a[28:31], a[188:191], v[132:135], a[28:31]\n\t" \
  "v_exp_f32 v46, v46\n\t" \
  "v_mfma_f32_16x16x32_bf16 v[160:163], v[216:219], v[128:131], v[160:163]\n\t" \
  "s_add_u32 m0, s38, 49152\n\t" \
  "v_exp_f32 v47, v47\n\t" \
  "v_mfma_f32_16x16x32_bf16 v[160:163], v[216:219], v[132:135], v[160:163]\n\t" \
  "s_add_u32 s96, s38, 65536\n\t" \
  "ds_read_b128 a[196:199], v221 offset:16384\n\t" \
  "v_cvt_pk_bf16_f32 v128, v0, v1\n\t" \
  "v_mfma_f32_16x16x32_bf16 a[32:35], a[128:131], v[136:139], a[32:35]\n\t" \
  "v_exp_f32 v64, v64\n\t" \
  "v_mfma_f32_16x16x32_bf16 a[32:35], a[132:135], v[140:143], a[32:35]\n\t" \
  "v_exp_f32 v65, v65\n\t" \
  "v_mfma_f32_16x16x32_bf16 a[36:39], a[136:139], v[136:139], a[36:39]\n\t" \
  "buffer_load_dwordx4 v224, s[60:63], s96 offen offset:0 lds\n\t" \
  "v_mfma_f32_16x16x32_bf16 a[36:39], a[140:143], v[140:143], a[36:39]\n\t" \
  "ds_read_b128 a[200:203], v222 offset:16384\n\t" \
  "v_cvt_pk_bf16_f32 v129, v2, v3\n\t" \
  "v_mfma_f32_16x16x32_bf16 a[40:43], a[144:147], v[136:139], a[40:43]\n\t" \
  "v_exp_f32 v66, v66\n\t" \
  "v_mfma_f32_16x16x32_bf16 a[40:43], a[148:151], v[140:143], a[40:43]\n\t" \
  "ds_read_b128 a[204:207], v223 offset:16384\n\t" \
  "v_cvt_pk_bf16_f32 v130, v4, v5\n\t" \
  "v_mfma_f32_16x16x32_bf16 a[44:47], a[152:155], v[136:139], a[44:47]\n\t" \
  "buffer_load_dwordx4 v225, s[60:63], s96 offen offset:1024 lds\n\t" \
  "v_mfma_f32_16x16x32_bf16 a[44:47], a[156:159], v[140:143], a[44:47]\n\t" \
  "v_exp_f32 v67, v67\n\t" \
  "v_mfma_f32_16x16x32_bf16 a[48:51], a[160:163], v[136:139], a[48:51]\n\t" \
  "ds_read_b128 a[208:211], v220 offset:20480\n\t" \
  "v_cvt_pk_bf16_f32 v131, v6, v7\n\t" \
  "v_mfma_f32_16x16x32_bf16 a[48:51], a[164:167], v[140:143], a[48:51]\n\t" \
  "v_exp_f32 v68, v68\n\t" \
  "v_mfma_f32_16x16x32_bf16 a[52:55], a[168:171], v[136:139], a[52:55]\n\t" \
  "buffer_load_dwordx4 v226, s[60:63], s96 offen offset:2048 lds\n\t" \
  "v_mfma_f32_16x16x32_bf16 a[52:55], a[172:175], v[140:143], a[52:55]\n\t" \
  "ds_read_b128 a[212:215], v221 offset:20480\n\t" \
  "v_cvt_pk_bf16_f32 v132, v8, v9\n\t" \
  "v_mfma_f32_16x16x32_bf16 a[56:59], a[176:179], v[136:139], a[56:59]\n\t" \
  "v_exp_f32 v69, v69\n\t" \
  "v_mfma_f32_16x16x32_bf16 a[56:59], a[180:183], v[140:143], a[56:59]\n\t" \
  "v_cvt_pk_bf16_f32 v133, v10, v11\n\t" \
  "v_cvt_pk_bf16_f32 v134, v12, v13\n\t" \
  "v_mfma_f32_16x16x32_bf16 a[60:63], a[184:187], v[136:139], a[60:63]\n\t" \
  "buffer_load_dwordx4 v227, s[60:63], s96 offen offset:3072 lds\n\t" \
  "v_mfma_f32_16x16x32_bf16 a[60:63], a[188:191], v[140:143], a[60:63]\n\t" \
  "s_add_u32 m0, s38, 147456\n\t" \
  "ds_read_b128 a[216:219], v222 offset:20480\n\t" \
  "v_cvt_pk_bf16_f32 v135, v14, v15\n\t" \
  "v_mfma_f32_16x16x32_bf16 v[164:167], v[216:219], v[136:139], v[164:167]\n\t" \
  "s_add_u32 s96, s38, 49152\n\t" \
  "v_exp_f32 v70, v70\n\t" \
  "v_mfma_f32_16x16x32_bf16 v[164:167], v[216:219], v[140:143], v[164:167]\n\t" \
  "ds_read_b128 a[220:223], v223 offset:20480\n\t" \
  "v_cvt_pk_bf16_f32 v136, v32, v33\n\t" \
  "v_mfma_f32_16x16x32_bf16 a[64:67], a[128:131], v[144:147], a[64:67]\n\t" \
  "buffer_load_dwordx4 v230, s[64:67], s96 offen offset:0 lds\n\t" \
  "v_mfma_f32_16x16x32_bf16 a[64:67], a[132:135], v[148:151], a[64:67]\n\t" \
  "v_exp_f32 v71, v71\n\t" \
  "v_mfma_f32_16x16x32_bf16 a[68:71], a[136:139], v[144:147], a[68:71]\n\t" \
  "ds_read_b128 a[224:227], v220 offset:24576\n\t" \
  "v_cvt_pk_bf16_f32 v137, v34, v35\n\t" \
  "v_mfma_f32_16x16x32_bf16 a[68:71], a[140:143], v[148:151], a[68:71]\n\t" \
  "v_exp_f32 v72, v72\n\t" \
  "v_mfma_f32_16x16x32_bf16 a[72:75], a[144:147], v[144:147], a[72:75]\n\t" \
  "buffer_load_dwordx4 v231, s[64:67], s96 offen offset:1024 lds\n\t" \
  "v_mfma_f32_16x16x32_bf16 a[72:75], a[148:151], v[148:151], a[72:75]\n\t" \
  "ds_read_b128 a[228:231], v221 offset:24576\n\t" \
  "v_cvt_pk_bf16_f32 v138, v36, v37\n\t" \
  "v_mfma_f32_16x16x32_bf16 a[76:79], a[152:155], v[144:147], a[76:79]\n\t" \
  "v_exp_f32 v73, v73\n\t" \
  "v_mfma_f32_16x16x32_bf16 a[76:79], a[156:159], v[148:151], a[76:79]\n\t" \
  "v_exp_f32 v74, v74\n\t" \
  "v_mfma_f32_16x16x32_bf16 a[80:83], a[160:163], v[144:147], a[80:83]\n\t" \
  "buffer_load_dwordx4 v230, s[64:67], s96 offen offset:2048 lds\n\t" \
  "v_mfma_f32_16x16x32_bf16 a[80:83], a[164:167], v[148:151], a[80:83]\n\t" \
  "ds_read_b128 a[232:235], v222 offset:24576\n\t" \
  "v_cvt_pk_bf16_f32 v139, v38, v39\n\t" \
  "v_mfma_f32_16x16x32_bf16 a[84:87], a[168:171], v[144:147], a[84:87]\n\t" \
  "v_exp_f32 v75, v75\n\t" \
  "v_mfma_f32_16x16x32_bf16 a[84:87], a[172:175], v[148:151], a[84:87]\n\t" \
  "ds_read_b128 a[236:239], v223 offset:24576\n\t" \
  "v_cvt_pk_bf16_f32 v140, v40, v41\n\t" \
  "v_mfma_f32_16x16x32_bf16 a[88:91], a[176:179], v[144:147], a[88:91]\n\t" \
  "buffer_load_dwordx4 v231, s[64:67], s96 offen offset:3072 lds\n\t" \
  "v_mfma_f32_16x16x32_bf16 a[88:91], a[180:183], v[148:151], a[88:91]\n\t" \
  "s_add_u32 m0, s38, 32768\n\t" \
  "v_exp_f32 v76, v76\n\t" \
  "v_mfma_f32_16x16x32_bf16 a[92:95], a[184:187], v[144:147], a[92:95]\n\t" \
  "s_mov_b32 s96, 28672\n\t" \
  "ds_read_b128 a[240:243], v220 offset:28672\n\t" \
  "v_cvt_pk_bf16_f32 v141, v42, v43\n\t" \
  "v_mfma_f32_16x16x32_bf16 a[92:95], a[188:191], v[148:151], a[92:95]\n\t" \
  "v_exp_f32 v77, v77\n\t" \
  "v_mfma_f32_16x16x32_bf16 v[168:171], v[216:219], v[144:147], v[168:171]\n\t" \
  "buffer_load_dwordx4 v232, s[68:71], s96 offen offset:0 lds\n\t" \
  "v_mfma_f32_16x16x32_bf16 v[168:171], v[216:219], v[148:151], v[168:171]\n\t" \
  "s_mov_b32 s96, 34816\n\t" \
  "ds_read_b128 a[244:247], v221 offset:28672\n\t" \
  "v_cvt_pk_bf16_f32 v142, v44, v45\n\t" \
  "v_mfma_f32_16x16x32_bf16 a[96:99], a[128:131], v[152:155], a[96:99]\n\t" \
  "v_exp_f32 v78, v78\n\t" \
  "v_mfma_f32_16x16x32_bf16 a[96:99], a[132:135], v[156:159], a[96:99]\n\t" \
  "v_exp_f32 v79, v79\n\t" \
  "v_mfma_f32_16x16x32_bf16 a[100:103], a[136:139], v[152:155], a[100:103]\n\t" \
  "buffer_load_dwordx4 v232, s[68:71], s96 offen offset:1024 lds\n\t" \
  "v_mfma_f32_16x16x32_bf16 a[100:103], a[140:143], v[156:159], a[100:103]\n\t" \
  "s_mov_b32 s96, 40960\n\t" \
  "ds_read_b128 a[248:251], v222 offset:28672\n\t" \
  "v_cvt_pk_bf16_f32 v143, v46, v47\n\t" \
  "v_mfma_f32_16x16x32_bf16 a[104:107], a[144:147], v[152:155], a[104:107]\n\t" \
  "v_exp_f32 v96, v96\n\t" \
  "v_mfma_f32_16x16x32_bf16 a[104:107], a[148:151], v[156:159], a[104:107]\n\t" \
  "ds_read_b128 a[252:255], v223 offset:28672\n\t" \
  "v_cvt_pk_bf16_f32 v144, v64, v65\n\t" \
  "v_mfma_f32_16x16x32_bf16 a[108:111], a[152:155], v[152:155], a[108:111]\n\t" \
  "buffer_load_dwordx4 v232, s[68:71], s96 offen offset:2048 lds\n\t" \
  "v_mfma_f32_16x16x32_bf16 a[108:111], a[156:159], v[156:159], a[108:111]\n\t" \
  "s_mov_b32 s96, 47104\n\t" \
  "v_exp_f32 v97, v97\n\t" \
  "v_mfma_f32_16x16x32_bf16 a[112:115], a[160:163], v[152:155], a[112:115]\n\t" \
  "v_cvt_pk_bf16_f32 v145, v66, v67\n\t" \
  "v_cvt_pk_bf16_f32 v146, v68, v69\n\t" \
  "v_mfma_f32_16x16x32_bf16 a[112:115], a[164:167], v[156:159], a[112:115]\n\t" \
  "v_exp_f32 v98, v98\n\t" \
  "v_mfma_f32_16x16x32_bf16 a[116:119], a[168:171], v[152:155], a[116:119]\n\t" \
  "buffer_load_dwordx4 v232, s[68:71], s96 offen offset:3072 lds\n\t" \
  "v_mfma_f32_16x16x32_bf16 a[116:119], a[172:175], v[156:159], a[116:119]\n\t" \
  "v_exp_f32 v99, v99\n\t" \
  "v_mfma_f32_16x16x32_bf16 a[120:123], a[176:179], v[152:155], a[120:123]\n\t" \
  "v_cvt_pk_bf16_f32 v147, v70, v71\n\t" \
  "v_cvt_pk_bf16_f32 v148, v72, v73\n\t" \
  "v_mfma_f32_16x16x32_bf16 a[120:123], a[180:183], v[156:159], a[120:123]\n\t" \
  "v_exp_f32 v100, v100\n\t" \
  "v_mfma_f32_16x16x32_bf16 a[124:127], a[184:187], v[152:155], a[124:127]\n\t" \
  "v_exp_f32 v101, v101\n\t" \
  "v_mfma_f32_16x16x32_bf16 a[124:127], a[188:191], v[156:159], a[124:127]\n\t" \
  "v_cvt_pk_bf16_f32 v149, v74, v75\n\t" \
  "v_cvt_pk_bf16_f32 v150, v76, v77\n\t" \
  "v_mfma_f32_16x16x32_bf16 v[172:175], v[216:219], v[152:155], v[172:175]\n\t" \
  "v_exp_f32 v102, v102\n\t" \
  "v_mfma_f32_16x16x32_bf16 v[172:175], v[216:219], v[156:159], v[172:175]\n\t" \
  "s_waitcnt lgkmcnt(12)\n\t" \
  "v_exp_f32 v103, v103\n\t" \
  STA("s_memrealtime s[94:95]\n\t") \
  STA("s_memtime s[92:93]\n\t") \
  "v_mfma_f32_16x16x32_bf16 v[0:3], a[192:195], v[16:19], v[176:179]\n\t" \
  "ds_read_b64_tr_b16 a[128:129], v228 offset:0\n\t" \
  "v_cvt_pk_bf16_f32 v151, v78, v79\n\t" \
  "v_mfma_f32_16x16x32_bf16 v[0:3], a[196:199], v[20:23], v[0:3]\n\t" \
  "v_exp_f32 v104, v104\n\t" \
  "v_mfma_f32_16x16x32_bf16 v[0:3], a[200:203], v[24:27], v[0:3]\n\t" \
  "ds_read_b64_tr_b16 a[130:131], v228 offset:4096\n\t" \
  "v_cvt_pk_bf16_f32 v152, v96, v97\n\t" \
  "v_mfma_f32_16x16x32_bf16 v[0:3], a[204:207], v[28:31], v[0:3]\n\t" \
  "s_waitcnt lgkmcnt(10)\n\t" \
  "v_exp_f32 v105, v105\n\t" \
  "v_mfma_f32_16x16x32_bf16 v[4:7], a[220:223], v[28:31], v[176:179]\n\t" \
  "ds_read_b64_tr_b16 a[132:133], v228 offset:8192\n\t" \
  "v_cvt_pk_bf16_f32 v153, v98, v99\n\t" \
  "v_mfma_f32_16x16x32_bf16 v[4:7], a[216:219], v[24:27], v[4:7]\n\t" \
  "v_exp_f32 v106, v106\n\t" \
  "v_mfma_f32_16x16x32_bf16 v[4:7], a[212:215], v[20:23], v[4:7]\n\t" \
  "ds_read_b64_tr_b16 a[134:135], v228 offset:12288\n\t" \
  "v_cvt_pk_bf16_f32 v154, v100, v101\n\t" \
  "v_mfma_f32_16x16x32_bf16 v[4:7], a[208:211], v[16:19], v[4:7]\n\t" \
  "s_waitcnt lgkmcnt(8)\n\t" \
  "ds_read_b64_tr_b16 a[136:137], v229 offset:0\n\t" \
  "v_cvt_pk_bf16_f32 v155, v102, v103\n\t" \
  "v_mfma_f32_16x16x32_bf16 v[8:11], a[224:227], v[16:19], v[176:179]\n\t" \
  "v_exp_f32 v107, v107\n\t" \
  "v_mfma_f32_16x16x32_bf16 v[8:11], a[228:231], v[20:23], v[8:11]\n\t" \
  "ds_read_b64_tr_b16 a[138:139], v229 offset:4096\n\t" \
  "v_cvt_pk_bf16_f32 v156, v104, v105\n\t" \
  "v_mfma_f32_16x16x32_bf16 v[8:11], a[232:235], v[24:27], v[8:11]\n\t" \
  "v_exp_f32 v108, v108\n\t" \
  "v_mfma_f32_16x16x32_bf16 v[8:11], a[236:239], v[28:31], v[8:11]\n\t" \
  "s_waitcnt lgkmcnt(6)\n\t" \
  "ds_read_b64_tr_b16 a[140:141], v229 offset:8192\n\t" \
  "v_cvt_pk_bf16_f32 v157, v106, v107\n\t" \
  "v_mfma_f32_16x16x32_bf16 v[12:15], a[252:255], v[28:31], v[176:179]\n\t" \
  STA("v_writelane_b32 v255, s94, 34\n\t") \
  STA("v_writelane_b32 v255, s92, 35\n\t") \
  ".Lhs7:\n\ts_cbranch_execz .Lhs7n\n\t" \
  "v_exp_f32 v109, v109\n\t" \
  "v_mfma_f32_16x16x32_bf16 v[12:15], a[248:251], v[24:27], v[12:15]\n\t" \
  "ds_read_b64_tr_b16 a[142:143], v229 offset:12288\n\t" \
  "v_cvt_pk_bf16_f32 v158, v108, v109\n\t" \
  "v_mfma_f32_16x16x32_bf16 v[12:15], a[244:247], v[20:23], v[12:15]\n\t" \
  "v_exp_f32 v110, v110\n\t" \
  "v_mfma_f32_16x16x32_bf16 v[12:15], a[240:243], v[16:19], v[12:15]\n\t" \
  "v_exp_f32 v111, v111\n\t" \
  "v_mfma_f32_16x16x32_bf16 v[32:35], a[192:195], v[48:51], v[180:183]\n\t" \
  "s_add_u32 m0, s38, 114688\n\t" \
  "ds_read_b64_tr_b16 a[144:145], v228 offset:256\n\t" \
  "v_cvt_pk_bf16_f32 v159, v110, v111\n\t" \
  "v_mfma_f32_16x16x32_bf16 v[32:35], a[196:199], v[52:55], v[32:35]\n\t" \
  "s_mov_b32 s96, 0\n\t" \
  "v_exp_f32 v0, v0\n\t" \
  "v_mfma_f32_16x16x32_bf16 v[32:35], a[200:203], v[56:59], v[32:35]\n\t" \
  "v_exp_f32 v1, v1\n\t" \
  "v_mfma_f32_16x16x32_bf16 v[32:35], a[204:207], v[60:63], v[32:35]\n\t" \
  "ds_read_b64_tr_b16 a[146:147], v228 offset:4352\n\t" \
  "buffer_load_dword v246, v247, s[16:19], 0 offen sc1\n\t" \
  "v_mfma_f32_16x16x32_bf16 v[36:39], a[220:223], v[60:63], v[180:183]\n\t" \
  "buffer_load_dwordx4 v232, s[72:75], s96 offen offset:0 lds\n\t" \
  "v_mfma_f32_16x16x32_bf16 v[36:39], a[216:219], v[56:59], v[36:39]\n\t" \
  "ds_read_b64_tr_b16 a[148:149], v228 offset:8448\n\t" \
  "v_mfma_f32_16x16x32_bf16 v[36:39], a[212:215], v[52:55], v[36:39]\n\t" \
  "v_exp_f32 v2, v2\n\t" \
  "v_mfma_f32_16x16x32_bf16 v[36:39], a[208:211], v[48:51], v[36:39]\n\t" \
  "ds_read_b64_tr_b16 a[150:151], v228 offset:12544\n\t" \
  "v_mfma_f32_16x16x32_bf16 v[40:43], a[224:227], v[48:51], v[180:183]\n\t" \
  "ds_read_b64_tr_b16 a[152:153], v229 offset:256\n\t" \
  "v_mfma_f32_16x16x32_bf16 v[40:43], a[228:231], v[52:55], v[40:43]\n\t" \
  "v_exp_f32 v3, v3\n\t" \
  "v_mfma_f32_16x16x32_bf16 v[40:43], a[232:235], v[56:59], v[40:43]\n\t" \
  "ds_read_b64_tr_b16 a[154:155], v229 offset:4352\n\t" \
  "s_mov_b32 s96, 6144\n\t" \
  "v_mfma_f32_16x16x32_bf16 v[40:43], a[236:239], v[60:63], v[40:43]\n\t" \
  "v_exp_f32 v4, v4\n\t" \
  "v_mfma_f32_16x16x32_bf16 v[44:47], a[252:255], v[60:63], v[180:183]\n\t" \
  "ds_read_b64_tr_b16 a[156:157], v229 offset:8448\n\t" \
  "v_mfma_f32_16x16x32_bf16 v[44:47], a[248:251], v[56:59], v[44:47]\n\t" \
  "ds_read_b64_tr_b16 a[158:159], v229 offset:12544\n\t" \
  "v_mfma_f32_16x16x32_bf16 v[44:47], a[244:247], v[52:55], v[44:47]\n\t" \
  "buffer_load_dwordx4 v232, s[72:75], s96 offen offset:1024 lds\n\t" \
  "v_mfma_f32_16x16x32_bf16 v[44:47], a[240:243], v[48:51], v[44:47]\n\t" \
  "ds_read_b64_tr_b16 a[160:161], v228 offset:512\n\t" \
  "v_mfma_f32_16x16x32_bf16 v[64:67], a[192:195], v[80:83], v[184:187]\n\t" \
  "v_exp_f32 v5, v5\n\t" \
  "v_mfma_f32_16x16x32_bf16 v[64:67], a[196:199], v[84:87], v[64:67]\n\t" \
  "ds_read_b64_tr_b16 a[162:163], v228 offset:4608\n\t" \
  "v_mfma_f32_16x16x32_bf16 v[64:67], a[200:203], v[88:91], v[64:67]\n\t" \
  "ds_read_b64_tr_b16 a[164:165], v228 offset:8704\n\t" \
  "v_mfma_f32_16x16x32_bf16 v[64:67], a[204:207], v[92:95], v[64:67]\n\t" \
  "v_exp_f32 v6, v6\n\t" \
  "v_mfma_f32_16x16x32_bf16 v[68:71], a[220:223], v[92:95], v[184:187]\n\t" \
  "ds_read_b64_tr_b16 a[166:167], v228 offset:12800\n\t" \
  "s_mov_b32 s96, 12288\n\t" \
  "v_mfma_f32_16x16x32_bf16 v[68:71], a[216:219], v[88:91], v[68:71]\n\t" \
  "v_exp_f32 v7, v7\n\t" \
  "v_mfma_f32_16x16x32_bf16 v[68:71], a[212:215], v[84:87], v[68:71]\n\t" \
  "ds_read_b64_tr_b16 a[168:169], v229 offset:512\n\t" \
  "v_mfma_f32_16x16x32_bf16 v[68:71], a[208:211], v[80:83], v[68:71]\n\t" \
  "ds_read_b64_tr_b16 a[170:171], v229 offset:4608\n\t" \
  "v_mfma_f32_16x16x32_bf16 v[72:75], a[224:227], v[80:83], v[184:187]\n\t" \
  "buffer_load_dwordx4 v232, s[72:75], s96 offen offset:2048 lds\n\t" \
  "v_mfma_f32_16x16x32_bf16 v[72:75], a[228:231], v[84:87], v[72:75]\n\t" \
  "ds_read_b64_tr_b16 a[172:173], v229 offset:8704\n\t" \
  "v_mfma_f32_16x16x32_bf16 v[72:75], a[232:235], v[88:91], v[72:75]\n\t" \
  "v_exp_f32 v8, v8\n\t" \
  "v_mfma_f32_16x16x32_bf16 v[72:75], a[236:239], v[92:95], v[72:75]\n\t" \
  "ds_read_b64_tr_b16 a[174:175], v229 offset:12800\n\t" \
  "v_mfma_f32_16x16x32_bf16 v[76:79], a[252:255], v[92:95], v[184:187]\n\t" \
  "ds_read_b64_tr_b16 a[176:177], v228 offset:768\n\t" \
  "v_mfma_f32_16x16x32_bf16 v[76:79], a[248:251], v[88:91], v[76:79]\n\t" \
  "v_exp_f32 v9, v9\n\t" \
  "v_mfma_f32_16x16x32_bf16 v[76:79], a[244:247], v[84:87], v[76:79]\n\t" \
  "ds_read_b64_tr_b16 a[178:179], v228 offset:4864\n\t" \
  "s_mov_b32 s96, 18432\n\t" \
  "v_mfma_f32_16x16x32_bf16 v[76:79], a[240:243], v[80:83], v[76:79]\n\t" \
  "v_exp_f32 v10, v10\n\t" \
  "v_mfma_f32_16x16x32_bf16 v[96:99], a[192:195], v[112:115], v[188:191]\n\t" \
  "ds_read_b64_tr_b16 a[180:181], v228 offset:8960\n\t" \
  "v_mfma_f32_16x16x32_bf16 v[96:99], a[196:199], v[116:119], v[96:99]\n\t" \
  "ds_read_b64_tr_b16 a[182:183], v228 offset:13056\n\t" \
  "v_mfma_f32_16x16x32_bf16 v[96:99], a[200:203], v[120:123], v[96:99]\n\t" \
  "buffer_load_dwordx4 v232, s[72:75], s96 offen offset:3072 lds\n\t" \
  "v_mfma_f32_16x16x32_bf16 v[96:99], a[204:207], v[124:127], v[96:99]\n\t" \
  "ds_read_b64_tr_b16 a[184:185], v229 offset:768\n\t" \
  "v_mfma_f32_16x16x32_bf16 v[100:103], a[220:223], v[124:127], v[188:191]\n\t" \
  "ds_read_b64_tr_b16 a[186:187], v229 offset:4864\n\t" \
  "v_mfma_f32_16x16x32_bf16 v[100:103], a[216:219], v[120:123], v[100:103]\n\t" \
  "v_exp_f32 v11, v11\n\t" \
  "v_mfma_f32_16x16x32_bf16 v[100:103], a[212:215], v[116:119], v[100:103]\n\t" \
  "ds_read_b64_tr_b16 a[188:189], v229 offset:8960\n\t" \
  "v_mfma_f32_16x16x32_bf16 v[100:103], a[208:211], v[112:115], v[100:103]\n\t" \
  "ds_read_b64_tr_b16 a[190:191], v229 offset:13056\n\t" \
  "v_mfma_f32_16x16x32_bf16 v[104:107], a[224:227], v[112:115], v[188:191]\n\t" \
  "v_exp_f32 v12, v12\n\t" \
  "v_mfma_f32_16x16x32_bf16 v[104:107], a[228:231], v[116:119], v[104:107]\n\t" \
  "s_getreg_b32 s98, hwreg(HW_REG_IB_STS, 0, 4)\n\t" \
  "s_cmp_lg_u32 s98, 0\n\t" \
  "s_addc_u32 s54, s54, 0\n\t" \
  "v_mfma_f32_16x16x32_bf16 v[104:107], a[232:235], v[120:123], v[104:107]\n\t" \
  "v_exp_f32 v13, v13\n\t" \
  "v_mfma_f32_16x16x32_bf16 v[104:107], a[236:239], v[124:127], v[104:107]\n\t" \
  "s_getreg_b32 s98, hwreg(HW_REG_IB_STS, 0, 4)\n\t" \
  "s_cmp_lg_u32 s98, 0\n\t" \
  "s_addc_u32 s54, s54, 0\n\t" \
  "v_mfma_f32_16x16x32_bf16 v[108:111], a[252:255], v[124:127], v[188:191]\n\t" \
  "v_exp_f32 v14, v14\n\t" \
  "v_mfma_f32_16x16x32_bf16 v[108:111], a[248:251], v[120:123], v[108:111]\n\t" \
  "s_getreg_b32 s98, hwreg(HW_REG_IB_STS, 0, 4)\n\t" \
  "s_cmp_lg_u32 s98, 0\n\t" \
  "s_addc_u32 s54, s54, 0\n\t" \
  "v_mfma_f32_16x16x32_bf16 v[108:111], a[244:247], v[116:119], v[108:111]\n\t" \
  "s_waitcnt vmcnt(9) lgkmcnt(0)\n\t" \
  "s_getreg_b32 s98, hwreg(HW_REG_IB_STS, 0, 4)\n\t" \
  "s_cmp_lg_u32 s98, 0\n\t" \
  "s_addc_u32 s54, s54, 0\n\t" \
  "v_mfma_f32_16x16x32_bf16 v[108:111], a[240:243], v[112:115], v[108:111]\n\t" \
  "s_barrier\n\t" \
  STA("s_memrealtime s[94:95]\n\t") \
  STA("s_memtime s[92:93]\n\t") \
  "v_mfma_f32_16x16x32_bf16 a[0:3], a[128:131], v[128:131], a[0:3]\n\t" \
  "v_exp_f32 v15, v15\n\t" \
  "v_mfma_f32_16x16x32_bf16 a[0:3], a[132:135], v[132:135], a[0:3]\n\t" \
  "v_exp_f32 v32, v32\n\t" \
  "v_mfma_f32_16x16x32_bf16 a[4:7], a[136:139], v[128:131], a[4:7]\n\t" \
  "v_exp_f32 v33, v33\n\t" \
  "v_mfma_f32_16x16x32_bf16 a[4:7], a[140:143], v[132:135], a[4:7]\n\t" \
  "v_exp_f32 v34, v34\n\t" \
  "v_mfma_f32_16x16x32_bf16 a[8:11], a[144:147], v[128:131], a[8:11]\n\t" \
  STS("v_writelane_b32 v255, s54, 58\n\t") \
  "s_cmp_ge_u32 s54, 2\n\t" \
  "s_cselect_b32 s98, 0x4000, 0\n\t" \
  "s_or_b32 s98, s98, s36\n\t" \
  "v_mfma_f32_16x16x32_bf16 a[8:11], a[148:151], v[132:135], a[8:11]\n\t" \
  "s_cmp_ge_u32 s54, 3\n\t" \
  "s_cselect_b32 s54, 2.0, 0\n\t" \
  "s_or_b32 s98, s98, s54\n\t" \
  "v_mfma_f32_16x16x32_bf16 a[12:15], a[152:155], v[128:131], a[12:15]\n\t" \
  "ds_read_b128 a[192:195], v220 offset:0\n\t" \
  "v_mov_b32 v209, s98\n\t" \
  "v_mfma_f32_16x16x32_bf16 a[12:15], a[156:159], v[132:135], a[12:15]\n\t" \
  "ds_read_b128 a[196:199], v221 offset:0\n\t" \
  "v_bfrev_b32 v250, 1\n\t" \
  "v_mfma_f32_16x16x32_bf16 a[16:19], a[160:163], v[128:131], a[16:19]\n\t" \
  "v_writelane_b32 v250, s44, 0\n\t" \
  "v_mfma_f32_16x16x32_bf16 a[16:19], a[164:167], v[132:135], a[16:19]\n\t" \
  "v_exp_f32 v35, v35\n\t" \
  "v_mfma_f32_16x16x32_bf16 a[20:23], a[168:171], v[128:131], a[20:23]\n\t" \
  "buffer_store_dword v209, v250, s[16:19], 0 offen sc1\n\t" \
  "ds_read_b128 a[200:203], v222 offset:0\n\t" \
  "v_mfma_f32_16x16x32_bf16 a[20:23], a[172:175], v[132:135], a[20:23]\n\t" \
  "v_exp_f32 v36, v36\n\t" \
  "v_mfma_f32_16x16x32_bf16 a[24:27], a[176:179], v[128:131], a[24:27]\n\t" \
  STA("v_writelane_b32 v255, s94, 36\n\t") \
  STA("v_writelane_b32 v255, s92, 37\n\t") \
  "v_exp_f32 v37, v37\n\t" \
  "v_mfma_f32_16x16x32_bf16 a[24:27], a[180:183], v[132:135], a[24:27]\n\t" \
  "v_exp_f32 v38, v38\n\t" \
  "v_mfma_f32_16x16x32_bf16 a[28:31], a[184:187], v[128:131], a[28:31]\n\t" \
  "v_exp_f32 v39, v39\n\t" \
  "v_mfma_f32_16x16x32_bf16 a[28:31], a[188:191], v[132:135], a[28:31]\n\t" \
  "v_exp_f32 v40, v40\n\t" \
  "v_mfma_f32_16x16x32_bf16 v[160:163], v[216:219], v[128:131], v[160:163]\n\t" \
  "s_add_u32 m0, s38, 65536\n\t" \
  "v_exp_f32 v41, v41\n\t" \
  "v_mfma_f32_16x16x32_bf16 v[160:163], v[216:219], v[132:135], v[160:163]\n\t" \
  "s_add_u32 s96, s38, 81920\n\t" \
  "v_exp_f32 v42, v42\n\t" \
  "v_mfma_f32_16x16x32_bf16 a[32:35], a[128:131], v[136:139], a[32:35]\n\t" \
  "v_exp_f32 v43, v43\n\t" \
  "v_mfma_f32_16x16x32_bf16 a[32:35], a[132:135], v[140:143], a[32:35]\n\t" \
  "v_exp_f32 v44, v44\n\t" \
  "v_mfma_f32_16x16x32_bf16 a[36:39], a[136:139], v[136:139], a[36:39]\n\t" \
  "buffer_load_dwordx4 v224, s[60:63], s96 offen offset:0 lds\n\t" \
  "v_mfma_f32_16x16x32_bf16 a[36:39], a[140:143], v[140:143], a[36:39]\n\t" \
  "v_exp_f32 v45, v45\n\t" \
  "v_mfma_f32_16x16x32_bf16 a[40:43], a[144:147], v[136:139], a[40:43]\n\t" \
  "v_exp_f32 v46, v46\n\t" \
  "v_mfma_f32_16x16x32_bf16 a[40:43], a[148:151], v[140:143], a[40:43]\n\t" \
  "ds_read_b128 a[204:207], v223 offset:0\n\t" \
  "v_cvt_pk_bf16_f32 v128, v0, v1\n\t" \
  "v_mfma_f32_16x16x32_bf16 a[44:47], a[152:155], v[136:139], a[44:47]\n\t" \
  "buffer_load_dwordx4 v225, s[60:63], s96 offen offset:1024 lds\n\t" \
  "v_mfma_f32_16x16x32_bf16 a[44:47], a[156:159], v[140:143], a[44:47]\n\t" \
  "v_exp_f32 v47, v47\n\t" \
  "v_mfma_f32_16x16x32_bf16 a[48:51], a[160:163], v[136:139], a[48:51]\n\t" \
  "ds_read_b128 a[208:211], v220 offset:4096\n\t" \
  "v_cvt_pk_bf16_f32 v129, v2, v3\n\t" \
  "v_mfma_f32_16x16x32_bf16 a[48:51], a[164:167], v[140:143], a[48:51]\n\t" \
  "v_exp_f32 v64, v64\n\t" \
  "v_mfma_f32_16x16x32_bf16 a[52:55], a[168:171], v[136:139], a[52:55]\n\t" \
  "buffer_load_dwordx4 v226, s[60:63], s96 offen offset:2048 lds\n\t" \
  "v_mfma_f32_16x16x32_bf16 a[52:55], a[172:175], v[140:143], a[52:55]\n\t" \
  "ds_read_b128 a[212:215], v221 offset:4096\n\t" \
  "v_cvt_pk_bf16_f32 v130, v4, v5\n\t" \
  "v_mfma_f32_16x16x32_bf16 a[56:59], a[176:179], v[136:139], a[56:59]\n\t" \
  "v_exp_f32 v65, v65\n\t" \
  "v_mfma_f32_16x16x32_bf16 a[56:59], a[180:183], v[140:143], a[56:59]\n\t" \
  "v_exp_f32 v66, v66\n\t" \
  "v_mfma_f32_16x16x32_bf16 a[60:63], a[184:187], v[136:139], a[60:63]\n\t" \
  "buffer_load_dwordx4 v227, s[60:63], s96 offen offset:3072 lds\n\t" \
  "v_mfma_f32_16x16x32_bf16 a[60:63], a[188:191], v[140:143], a[60:63]\n\t" \
  "s_add_u32 m0, s38, 81920\n\t" \
  "ds_read_b128 a[216:219], v222 offset:4096\n\t" \
  "v_cvt_pk_bf16_f32 v131, v6, v7\n\t" \
  "v_mfma_f32_16x16x32_bf16 v[164:167], v[216:219], v[136:139], v[164:167]\n\t" \
  "s_add_u32 s96, s38, 65536\n\t" \
  "v_exp_f32 v67, v67\n\t" \
  "v_mfma_f32_16x16x32_bf16 v[164:167], v[216:219], v[140:143], v[164:167]\n\t" \
  "ds_read_b128 a[220:223], v223 offset:4096\n\t" \
  "v_cvt_pk_bf16_f32 v132, v8, v9\n\t" \
  "v_mfma_f32_16x16x32_bf16 a[64:67], a[128:131], v[144:147], a[64:67]\n\t" \
  "buffer_load_dwordx4 v230, s[64:67], s96 offen offset:0 lds\n\t" \
  "v_mfma_f32_16x16x32_bf16 a[64:67], a[132:135], v[148:151], a[64:67]\n\t" \
  "v_exp_f32 v68, v68\n\t" \
  "v_mfma_f32_16x16x32_bf16 a[68:71], a[136:139], v[144:147], a[68:71]\n\t" \
  "ds_read_b128 a[224:227], v220 offset:8192\n\t" \
  "v_cvt_pk_bf16_f32 v133, v10, v11\n\t" \
  "v_mfma_f32_16x16x32_bf16 a[68:71], a[140:143], v[148:151], a[68:71]\n\t" \
  "v_exp_f32 v69, v69\n\t" \
  "v_mfma_f32_16x16x32_bf16 a[72:75], a[144:147], v[144:147], a[72:75]\n\t" \
  "buffer_load_dwordx4 v231, s[64:67], s96 offen offset:1024 lds\n\t" \
  "v_mfma_f32_16x16x32_bf16 a[72:75], a[148:151], v[148:151], a[72:75]\n\t" \
  "ds_read_b128 a[228:231], v221 offset:8192\n\t" \
  "v_cvt_pk_bf16_f32 v134, v12, v13\n\t" \
  "v_mfma_f32_16x16x32_bf16 a[76:79], a[152:155], v[144:147], a[76:79]\n\t" \
  "v_exp_f32 v70, v70\n\t" \
  "v_mfma_f32_16x16x32_bf16 a[76:79], a[156:159], v[148:151], a[76:79]\n\t" \
  "v_exp_f32 v71, v71\n\t" \
  "v_mfma_f32_16x16x32_bf16 a[80:83], a[160:163], v[144:147], a[80:83]\n\t" \
  "buffer_load_dwordx4 v230, s[64:67], s96 offen offset:2048 lds\n\t" \
  "v_mfma_f32_16x16x32_bf16 a[80:83], a[164:167], v[148:151], a[80:83]\n\t" \
  "ds_read_b128 a[232:235], v222 offset:8192\n\t" \
  "v_cvt_pk_bf16_f32 v135, v14, v15\n\t" \
  "v_mfma_f32_16x16x32_bf16 a[84:87], a[168:171], v[144:147], a[84:87]\n\t" \
  "v_exp_f32 v72, v72\n\t" \
  "v_mfma_f32_16x16x32_bf16 a[84:87], a[172:175], v[148:151], a[84:87]\n\t" \
  "ds_read_b128 a[236:239], v223 offset:8192\n\t" \
  "v_cvt_pk_bf16_f32 v136, v32, v33\n\t" \
  "v_mfma_f32_16x16x32_bf16 a[88:91], a[176:179], v[144:147], a[88:91]\n\t" \
  "buffer_load_dwordx4 v231, s[64:67], s96 offen offset:3072 lds\n\t" \
  "v_mfma_f32_16x16x32_bf16 a[88:91], a[180:183], v[148:151], a[88:91]\n\t" \
  "s_add_u32 m0, s38, 131072\n\t" \
  "v_exp_f32 v73, v73\n\t" \
  "v_mfma_f32_16x16x32_bf16 a[92:95], a[184:187], v[144:147], a[92:95]\n\t" \
  "s_mov_b32 s96, 28672\n\t" \
  "ds_read_b128 a[240:243], v220 offset:12288\n\t" \
  "v_cvt_pk_bf16_f32 v137, v34, v35\n\t" \
  "v_mfma_f32_16x16x32_bf16 a[92:95], a[188:191], v[148:151], a[92:95]\n\t" \
  "v_exp_f32 v74, v74\n\t" \
  "v_mfma_f32_16x16x32_bf16 v[168:171], v[216:219], v[144:147], v[168:171]\n\t" \
  "buffer_load_dwordx4 v232, s[72:75], s96 offen offset:0 lds\n\t" \
  "v_mfma_f32_16x16x32_bf16 v[168:171], v[216:219], v[148:151], v[168:171]\n\t" \
  "s_mov_b32 s96, 34816\n\t" \
  "ds_read_b128 a[244:247], v221 offset:12288\n\t" \
  "v_cvt_pk_bf16_f32 v138, v36, v37\n\t" \
  "v_mfma_f32_16x16x32_bf16 a[96:99], a[128:131], v[152:155], a[96:99]\n\t" \
  "v_exp_f32 v75, v75\n\t" \
  "v_mfma_f32_16x16x32_bf16 a[96:99], a[132:135], v[156:159], a[96:99]\n\t" \
  "v_cvt_pk_bf16_f32 v139, v38, v39\n\t" \
  "v_cvt_pk_bf16_f32 v140, v40, v41\n\t" \
  "v_mfma_f32_16x16x32_bf16 a[100:103], a[136:139], v[152:155], a[100:103]\n\t" \
  "buffer_load_dwordx4 v232, s[72:75], s96 offen offset:1024 lds\n\t" \
  "v_mfma_f32_16x16x32_bf16 a[100:103], a[140:143], v[156:159], a[100:103]\n\t" \
  "s_mov_b32 s96, 40960\n\t" \
  "ds_read_b128 a[248:251], v222 offset:12288\n\t" \
  "v_cvt_pk_bf16_f32 v141, v42, v43\n\t" \
  "v_mfma_f32_16x16x32_bf16 a[104:107], a[144:147], v[152:155], a[104:107]\n\t" \
  "v_exp_f32 v76, v76\n\t" \
  "v_mfma_f32_16x16x32_bf16 a[104:107], a[148:151], v[156:159], a[104:107]\n\t" \
  "ds_read_b128 a[252:255], v223 offset:12288\n\t" \
  "v_cvt_pk_bf16_f32 v142, v44, v45\n\t" \
  "v_mfma_f32_16x16x32_bf16 a[108:111], a[152:155], v[152:155], a[108:111]\n\t" \
  "buffer_load_dwordx4 v232, s[72:75], s96 offen offset:2048 lds\n\t" \
  "v_mfma_f32_16x16x32_bf16 a[108:111], a[156:159], v[156:159], a[108:111]\n\t" \
  "s_mov_b32 s96, 47104\n\t" \
  "v_exp_f32 v77, v77\n\t" \
  "v_mfma_f32_16x16x32_bf16 a[112:115], a[160:163], v[152:155], a[112:115]\n\t" \
  "v_exp_f32 v78, v78\n\t" \
  "v_mfma_f32_16x16x32_bf16 a[112:115], a[164:167], v[156:159], a[112:115]\n\t" \
  "v_exp_f32 v79, v79\n\t" \
  "v_mfma_f32_16x16x32_bf16 a[116:119], a[168:171], v[152:155], a[116:119]\n\t" \
  "buffer_load_dwordx4 v232, s[72:75], s96 offen offset:3072 lds\n\t" \
  "v_mfma_f32_16x16x32_bf16 a[116:119], a[172:175], v[156:159], a[116:119]\n\t" \
  "v_cvt_pk_bf16_f32 v143, v46, v47\n\t" \
  "v_cvt_pk_bf16_f32 v144, v64, v65\n\t" \
  "v_mfma_f32_16x16x32_bf16 a[120:123], a[176:179], v[152:155], a[120:123]\n\t" \
  "v_exp_f32 v96, v96\n\t" \
  "v_mfma_f32_16x16x32_bf16 a[120:123], a[180:183], v[156:159], a[120:123]\n\t" \
  "s_add_u32 s99, s52, 0\n\t" \
  "v_exp_f32 v97, v97\n\t" \
  "v_mfma_f32_16x16x32_bf16 a[124:127], a[184:187], v[152:155], a[124:127]\n\t" \
  "v_cvt_pk_bf16_f32 v145, v66, v67\n\t" \
  "v_cvt_pk_bf16_f32 v146, v68, v69\n\t" \
  "v_mfma_f32_16x16x32_bf16 a[124:127], a[188:191], v[156:159], a[124:127]\n\t" \
  "v_exp_f32 v98, v98\n\t" \
  "v_mfma_f32_16x16x32_bf16 v[172:175], v[216:219], v[152:155], v[172:175]\n\t" \
  "buffer_load_dword v246, v212, s[24:27], s99 offen\n\t" \
  "v_mfma_f32_16x16x32_bf16 v[172:175], v[216:219], v[156:159], v[172:175]\n\t" \
  "s_waitcnt lgkmcnt(12)\n\t" \
  "v_exp_f32 v99, v99\n\t" \
  STA("s_memrealtime s[94:95]\n\t") \
  STA("s_memtime s[92:93]\n\t") \
  "v_mfma_f32_16x16x32_bf16 v[0:3], a[192:195], v[16:19], v[176:179]\n\t" \
  "s_lshr_b32 s78, s38, 6\n\t" \
  "ds_read_b64_tr_b16 a[128:129], v210 offset:0\n\t" \
  "v_cvt_pk_bf16_f32 v147, v70, v71\n\t" \
  "v_mfma_f32_16x16x32_bf16 v[0:3], a[196:199], v[20:23], v[0:3]\n\t" \
  "v_exp_f32 v100, v100\n\t" \
  "v_mfma_f32_16x16x32_bf16 v[0:3], a[200:203], v[24:27], v[0:3]\n\t" \
  "ds_read_b64_tr_b16 a[130:131], v210 offset:4096\n\t" \
  "v_cvt_pk_bf16_f32 v148, v72, v73\n\t" \
  "v_mfma_f32_16x16x32_bf16 v[0:3], a[204:207], v[28:31], v[0:3]\n\t" \
  "s_waitcnt lgkmcnt(10)\n\t" \
  "v_exp_f32 v101, v101\n\t" \
  "v_mfma_f32_16x16x32_bf16 v[4:7], a[220:223], v[28:31], v[176:179]\n\t" \
  "ds_read_b64_tr_b16 a[132:133], v210 offset:8192\n\t" \
  "v_cvt_pk_bf16_f32 v149, v74, v75\n\t" \
  "v_mfma_f32_16x16x32_bf16 v[4:7], a[216:219], v[24:27], v[4:7]\n\t" \
  "v_exp_f32 v102, v102\n\t" \
  "v_mfma_f32_16x16x32_bf16 v[4:7], a[212:215], v[20:23], v[4:7]\n\t" \
  "ds_read_b64_tr_b16 a[134:135], v210 offset:12288\n\t" \
  "v_cvt_pk_bf16_f32 v150, v76, v77\n\t" \
  "v_mfma_f32_16x16x32_bf16 v[4:7], a[208:211], v[16:19], v[4:7]\n\t" \
  "s_waitcnt lgkmcnt(8)\n\t" \
  "ds_read_b64_tr_b16 a[136:137], v211 offset:0\n\t" \
  "v_cvt_pk_bf16_f32 v151, v78, v79\n\t" \
  "v_mfma_f32_16x16x32_bf16 v[8:11], a[224:227], v[16:19], v[176:179]\n\t" \
  "v_exp_f32 v103, v103\n\t" \
  "v_mfma_f32_16x16x32_bf16 v[8:11], a[228:231], v[20:23], v[8:11]\n\t" \
  "s_add_u32 s99, s52, 2048\n\t" \
  "ds_read_b64_tr_b16 a[138:139], v211 offset:4096\n\t" \
  "v_cvt_pk_bf16_f32 v152, v96, v97\n\t" \
  "v_mfma_f32_16x16x32_bf16 v[8:11], a[232:235], v[24:27], v[8:11]\n\t" \
  "v_exp_f32 v104, v104\n\t" \
  "v_mfma_f32_16x16x32_bf16 v[8:11], a[236:239], v[28:31], v[8:11]\n\t" \
  "s_waitcnt lgkmcnt(6)\n\t" \
  "ds_read_b64_tr_b16 a[140:141], v211 offset:8192\n\t" \
  "v_cvt_pk_bf16_f32 v153, v98, v99\n\t" \
  "v_mfma_f32_16x16x32_bf16 v[12:15], a[252:255], v[28:31], v[176:179]\n\t" \
  STA("v_writelane_b32 v255, s94, 38\n\t") \
  STA("v_writelane_b32 v255, s92, 39\n\t") \
  "buffer_load_dword v246, v212, s[24:27], s99 offen\n\t" \
  "v_mfma_f32_16x16x32_bf16 v[12:15], a[248:251], v[24:27], v[12:15]\n\t" \
  "ds_read_b64_tr_b16 a[142:143], v211 offset:12288\n\t" \
  "v_cvt_pk_bf16_f32 v154, v100, v101\n\t" \
  "v_mfma_f32_16x16x32_bf16 v[12:15], a[244:247], v[20:23], v[12:15]\n\t" \
  "ds_read_b64_tr_b16 a[144:145], v210 offset:256\n\t" \
  "v_cvt_pk_bf16_f32 v155, v102, v103\n\t" \
  "v_mfma_f32_16x16x32_bf16 v[12:15], a[240:243], v[16:19], v[12:15]\n\t" \
  "v_exp_f32 v105, v105\n\t" \
  "v_mfma_f32_16x16x32_bf16 v[32:35], a[192:195], v[48:51], v[180:183]\n\t" \
  "ds_read_b64_tr_b16 a[146:147], v210 offset:4352\n\t" \
  "v_cvt_pk_bf16_f32 v156, v104, v105\n\t" \
  "v_mfma_f32_16x16x32_bf16 v[32:35], a[196:199], v[52:55], v[32:35]\n\t" \
  "v_exp_f32 v106, v106\n\t" \
  "v_mfma_f32_16x16x32_bf16 v[32:35], a[200:203], v[56:59], v[32:35]\n\t" \
  "ds_read_b64_tr_b16 a[148:149], v210 offset:8448\n\t" \
  "v_xor_b32 v234, s78, v244\n\t" \
  "v_mfma_f32_16x16x32_bf16 v[32:35], a[204:207], v[60:63], v[32:35]\n\t" \
  "v_exp_f32 v107, v107\n\t" \
  "v_mfma_f32_16x16x32_bf16 v[36:39], a[220:223], v[60:63], v[180:183]\n\t" \
  "ds_read_b64_tr_b16 a[150:151], v210 offset:12544\n\t" \
  "v_cvt_pk_bf16_f32 v157, v106, v107\n\t" \
  "v_mfma_f32_16x16x32_bf16 v[36:39], a[216:219], v[56:59], v[36:39]\n\t" \
  "ds_read_b64_tr_b16 a[152:153], v211 offset:256\n\t" \
  "v_xor_b32 v235, 32, v234\n\t" \
  "v_mfma_f32_16x16x32_bf16 v[36:39], a[212:215], v[52:55], v[36:39]\n\t" \
  "v_exp_f32 v108, v108\n\t" \
  "v_mfma_f32_16x16x32_bf16 v[36:39], a[208:211], v[48:51], v[36:39]\n\t" \
  "ds_read_b64_tr_b16 a[154:155], v211 offset:4352\n\t" \
  "v_add_u32 v236, 65536, v234\n\t" \
  "v_mfma_f32_16x16x32_bf16 v[40:43], a[224:227], v[48:51], v[180:183]\n\t" \
  "v_exp_f32 v109, v109\n\t" \
  "v_mfma_f32_16x16x32_bf16 v[40:43], a[228:231], v[52:55], v[40:43]\n\t" \
  "ds_read_b64_tr_b16 a[156:157], v211 offset:8448\n\t" \
  "v_cvt_pk_bf16_f32 v158, v108, v109\n\t" \
  "v_mfma_f32_16x16x32_bf16 v[40:43], a[232:235], v[56:59], v[40:43]\n\t" \
  "v_exp_f32 v110, v110\n\t" \
  "v_mfma_f32_16x16x32_bf16 v[40:43], a[236:239], v[60:63], v[40:43]\n\t" \
  "ds_read_b64_tr_b16 a[158:159], v211 offset:12544\n\t" \
  "v_add_u32 v237, 65536, v235\n\t" \
  "v_mfma_f32_16x16x32_bf16 v[44:47], a[252:255], v[60:63], v[180:183]\n\t" \
  "v_exp_f32 v111, v111\n\t" \
  "v_mfma_f32_16x16x32_bf16 v[44:47], a[248:251], v[56:59], v[44:47]\n\t" \
  "ds_read_b64_tr_b16 a[160:161], v210 offset:512\n\t" \
  "v_cvt_pk_bf16_f32 v159, v110, v111\n\t" \
  "v_mfma_f32_16x16x32_bf16 v[44:47], a[244:247], v[52:55], v[44:47]\n\t" \
  "ds_read_b64_tr_b16 a[162:163], v210 offset:4608\n\t" \
  "v_add_u32 v238, 131072, v234\n\t" \
  "v_mfma_f32_16x16x32_bf16 v[44:47], a[240:243], v[48:51], v[44:47]\n\t" \
  "v_exp_f32 v0, v0\n\t" \
  "v_mfma_f32_16x16x32_bf16 v[64:67], a[192:195], v[80:83], v[184:187]\n\t" \
  "ds_read_b64_tr_b16 a[164:165], v210 offset:8704\n\t" \
  "v_add_u32 v239, 131072, v235\n\t" \
  "v_mfma_f32_16x16x32_bf16 v[64:67], a[196:199], v[84:87], v[64:67]\n\t" \
  "v_exp_f32 v1, v1\n\t" \
  "v_mfma_f32_16x16x32_bf16 v[64:67], a[200:203], v[88:91], v[64:67]\n\t" \
  "ds_read_b64_tr_b16 a[166:167], v210 offset:12800\n\t" \
  "v_lshlrev_b32 v242, 4, v233\n\t" \
  "v_mfma_f32_16x16x32_bf16 v[64:67], a[204:207], v[92:95], v[64:67]\n\t" \
  "ds_read_b64_tr_b16 a[168:169], v211 offset:512\n\t" \
  "v_add_u32 v213, s38, v242\n\t" \
  "v_mfma_f32_16x16x32_bf16 v[68:71], a[220:223], v[92:95], v[184:187]\n\t" \
  "v_exp_f32 v2, v2\n\t" \
  "v_mfma_f32_16x16x32_bf16 v[68:71], a[216:219], v[88:91], v[68:71]\n\t" \
  "ds_read_b64_tr_b16 a[170:171], v211 offset:4608\n\t" \
  "v_add_u32 v215, 98304, v213\n\t" \
  "v_mfma_f32_16x16x32_bf16 v[68:71], a[212:215], v[84:87], v[68:71]\n\t" \
  "v_exp_f32 v3, v3\n\t" \
  "v_mfma_f32_16x16x32_bf16 v[68:71], a[208:211], v[80:83], v[68:71]\n\t" \
  "ds_read_b64_tr_b16 a[172:173], v211 offset:8704\n\t" \
  "v_mov_b32 v243, s36\n\t" \
  "v_mfma_f32_16x16x32_bf16 v[72:75], a[224:227], v[80:83], v[184:187]\n\t" \
  "v_exp_f32 v4, v4\n\t" \
  "v_mfma_f32_16x16x32_bf16 v[72:75], a[228:231], v[84:87], v[72:75]\n\t" \
  "ds_read_b64_tr_b16 a[174:175], v211 offset:12800\n\t" \
  "v_lshlrev_b32 v254, 9, v233\n\t" \
  "v_mfma_f32_16x16x32_bf16 v[72:75], a[232:235], v[88:91], v[72:75]\n\t" \
  "ds_read_b64_tr_b16 a[176:177], v210 offset:768\n\t" \
  "v_and_b32 v244, 0x38, v233\n\t" \
  "v_mfma_f32_16x16x32_bf16 v[72:75], a[236:239], v[92:95], v[72:75]\n\t" \
  "v_exp_f32 v5, v5\n\t" \
  "v_mfma_f32_16x16x32_bf16 v[76:79], a[252:255], v[92:95], v[184:187]\n\t" \
  "ds_read_b64_tr_b16 a[178:179], v210 offset:4864\n\t" \
  "v_lshl_or_b32 v254, v244, 26, v254\n\t" \
  "v_mfma_f32_16x16x32_bf16 v[76:79], a[248:251], v[88:91], v[76:79]\n\t" \
  "s_brev_b32 s78, 1\n\t" \
  "v_exp_f32 v6, v6\n\t" \
  "v_mfma_f32_16x16x32_bf16 v[76:79], a[244:247], v[84:87], v[76:79]\n\t" \
  "ds_read_b64_tr_b16 a[180:181], v210 offset:8960\n\t" \
  "v_mov_b32 v244, s78\n\t" \
  "v_mfma_f32_16x16x32_bf16 v[76:79], a[240:243], v[80:83], v[76:79]\n\t" \
  "v_exp_f32 v7, v7\n\t" \
  "v_mfma_f32_16x16x32_bf16 v[96:99], a[192:195], v[112:115], v[188:191]\n\t" \
  "ds_read_b64_tr_b16 a[182:183], v210 offset:13056\n\t" \
  "v_cmp_eq_u32 vcc, s39, v233\n\t" \
  "v_mfma_f32_16x16x32_bf16 v[96:99], a[196:199], v[116:119], v[96:99]\n\t" \
  "ds_read_b64_tr_b16 a[184:185], v211 offset:768\n\t" \
  "v_add_u32 v245, s39, v233\n\t" \
  "v_mfma_f32_16x16x32_bf16 v[96:99], a[200:203], v[120:123], v[96:99]\n\t" \
  "v_exp_f32 v8, v8\n\t" \
  "v_mfma_f32_16x16x32_bf16 v[96:99], a[204:207], v[124:127], v[96:99]\n\t" \
  "ds_read_b64_tr_b16 a[186:187], v211 offset:4864\n\t" \
  "v_bfi_b32 v245, 7, v245, v233\n\t" \
  "v_mfma_f32_16x16x32_bf16 v[100:103], a[220:223], v[124:127], v[188:191]\n\t" \
  "v_exp_f32 v9, v9\n\t" \
  "v_mfma_f32_16x16x32_bf16 v[100:103], a[216:219], v[120:123], v[100:103]\n\t" \
  "ds_read_b64_tr_b16 a[188:189], v211 offset:8960\n\t" \
  "v_cndmask_b32 v254, v254, v244, vcc\n\t" \
  "v_mfma_f32_16x16x32_bf16 v[100:103], a[212:215], v[116:119], v[100:103]\n\t" \
  "v_exp_f32 v10, v10\n\t" \
  "v_mfma_f32_16x16x32_bf16 v[100:103], a[208:211], v[112:115], v[100:103]\n\t" \
  "ds_read_b64_tr_b16 a[190:191], v211 offset:13056\n\t" \
  "v_and_b32 v245, 31, v245\n\t" \
  "v_mfma_f32_16x16x32_bf16 v[104:107], a[224:227], v[112:115], v[188:191]\n\t" \
  "v_exp_f32 v11, v11\n\t" \
  "v_mfma_f32_16x16x32_bf16 v[104:107], a[228:231], v[116:119], v[104:107]\n\t" \
  "v_exp_f32 v12, v12\n\t" \
  "v_mfma_f32_16x16x32_bf16 v[104:107], a[232:235], v[120:123], v[104:107]\n\t" \
  "v_exp_f32 v13, v13\n\t" \
  "v_mfma_f32_16x16x32_bf16 v[104:107], a[236:239], v[124:127], v[104:107]\n\t" \
  "v_exp_f32 v14, v14\n\t" \
  "v_mfma_f32_16x16x32_bf16 v[108:111], a[252:255], v[124:127], v[188:191]\n\t" \
  "v_exp_f32 v15, v15\n\t" \
  "v_mfma_f32_16x16x32_bf16 v[108:111], a[248:251], v[120:123], v[108:111]\n\t" \
  "v_exp_f32 v32, v32\n\t" \
  "v_mfma_f32_16x16x32_bf16 v[108:111], a[244:247], v[116:119], v[108:111]\n\t" \
  "s_waitcnt vmcnt(2) lgkmcnt(0)\n\t" \
  "v_exp_f32 v33, v33\n\t" \
  "v_mfma_f32_16x16x32_bf16 v[108:111], a[240:243], v[112:115], v[108:111]\n\t" \
  "s_barrier\n\t" \
  STA("s_memrealtime s[94:95]\n\t") \
  STA("s_memtime s[92:93]\n\t") \
  "v_mfma_f32_16x16x32_bf16 a[0:3], a[128:131], v[128:131], a[0:3]\n\t" \
  "v_exp_f32 v34, v34\n\t" \
  "v_mfma_f32_16x16x32_bf16 a[0:3], a[132:135], v[132:135], a[0:3]\n\t" \
  "v_exp_f32 v35, v35\n\t" \
  "v_mfma_f32_16x16x32_bf16 a[4:7], a[136:139], v[128:131], a[4:7]\n\t" \
  "v_exp_f32 v36, v36\n\t" \
  "v_mfma_f32_16x16x32_bf16 a[4:7], a[140:143], v[132:135], a[4:7]\n\t" \
  "v_exp_f32 v37, v37\n\t" \
  "v_mfma_f32_16x16x32_bf16 a[8:11], a[144:147], v[128:131], a[8:11]\n\t" \
  "v_exp_f32 v38, v38\n\t" \
  "v_mfma_f32_16x16x32_bf16 a[8:11], a[148:151], v[132:135], a[8:11]\n\t" \
  "v_exp_f32 v39, v39\n\t" \
  "v_mfma_f32_16x16x32_bf16 a[12:15], a[152:155], v[128:131], a[12:15]\n\t" \
  "v_exp_f32 v40, v40\n\t" \
  "v_mfma_f32_16x16x32_bf16 a[12:15], a[156:159], v[132:135], a[12:15]\n\t" \
  "v_exp_f32 v41, v41\n\t" \
  "v_mfma_f32_16x16x32_bf16 a[16:19], a[160:163], v[128:131], a[16:19]\n\t" \
  "v_exp_f32 v42, v42\n\t" \
  "v_mfma_f32_16x16x32_bf16 a[16:19], a[164:167], v[132:135], a[16:19]\n\t" \
  "v_exp_f32 v43, v43\n\t" \
  "v_mfma_f32_16x16x32_bf16 a[20:23], a[168:171], v[128:131], a[20:23]\n\t" \
  "v_exp_f32 v44, v44\n\t" \
  "v_mfma_f32_16x16x32_bf16 a[20:23], a[172:175], v[132:135], a[20:23]\n\t" \
  "v_exp_f32 v45, v45\n\t" \
  "v_mfma_f32_16x16x32_bf16 a[24:27], a[176:179], v[128:131], a[24:27]\n\t" \
  STA("v_writelane_b32 v255, s94, 40\n\t") \
  STA("v_writelane_b32 v255, s92, 41\n\t") \
  "v_exp_f32 v46, v46\n\t" \
  "v_mfma_f32_16x16x32_bf16 a[24:27], a[180:183], v[132:135], a[24:27]\n\t" \
  "v_exp_f32 v47, v47\n\t" \
  "v_mfma_f32_16x16x32_bf16 a[28:31], a[184:187], v[128:131], a[28:31]\n\t" \
  "ds_read_b128 a[192:195], v220 offset:16384\n\t" \
  "v_lshlrev_b32 v245, 2, v245\n\t" \
  "v_mfma_f32_16x16x32_bf16 a[28:31], a[188:191], v[132:135], a[28:31]\n\t" \
  "v_exp_f32 v64, v64\n\t" \
  "v_mfma_f32_16x16x32_bf16 v[160:163], v[216:219], v[128:131], v[160:163]\n\t" \
  "s_add_u32 m0, s38, 49152\n\t" \
  "v_exp_f32 v65, v65\n\t" \
  "v_mfma_f32_16x16x32_bf16 v[160:163], v[216:219], v[132:135], v[160:163]\n\t" \
  "s_add_u32 s96, s38, 98304\n\t" \
  "ds_read_b128 a[196:199], v221 offset:16384\n\t" \
  "v_cvt_pk_bf16_f32 v128, v0, v1\n\t" \
  "v_mfma_f32_16x16x32_bf16 a[32:35], a[128:131], v[136:139], a[32:35]\n\t" \
  "v_cvt_pk_bf16_f32 v129, v2, v3\n\t" \
  "v_cvt_pk_bf16_f32 v130, v4, v5\n\t" \
  "v_mfma_f32_16x16x32_bf16 a[32:35], a[132:135], v[140:143], a[32:35]\n\t" \
  "v_exp_f32 v66, v66\n\t" \
  "v_mfma_f32_16x16x32_bf16 a[36:39], a[136:139], v[136:139], a[36:39]\n\t" \
  "buffer_load_dwordx4 v224, s[60:63], s96 offen offset:0 lds\n\t" \
  "v_mfma_f32_16x16x32_bf16 a[36:39], a[140:143], v[140:143], a[36:39]\n\t" \
  "ds_read_b128 a[200:203], v222 offset:16384\n\t" \
  "v_cvt_pk_bf16_f32 v131, v6, v7\n\t" \
  "v_mfma_f32_16x16x32_bf16 a[40:43], a[144:147], v[136:139], a[40:43]\n\t" \
  "v_exp_f32 v67, v67\n\t" \
  "v_mfma_f32_16x16x32_bf16 a[40:43], a[148:151], v[140:143], a[40:43]\n\t" \
  "ds_read_b128 a[204:207], v223 offset:16384\n\t" \
  "v_cvt_pk_bf16_f32 v132, v8, v9\n\t" \
  "v_mfma_f32_16x16x32_bf16 a[44:47], a[152:155], v[136:139], a[44:47]\n\t" \
  "v_exp_f32 v68, v68\n\t" \
  "v_mfma_f32_16x16x32_bf16 a[44:47], a[156:159], v[140:143], a[44:47]\n\t" \
  "v_exp_f32 v69, v69\n\t" \
  "v_mfma_f32_16x16x32_bf16 a[48:51], a[160:163], v[136:139], a[48:51]\n\t" \
  "buffer_load_dwordx4 v225, s[60:63], s96 offen offset:1024 lds\n\t" \
  "v_mfma_f32_16x16x32_bf16 a[48:51], a[164:167], v[140:143], a[48:51]\n\t" \
  "ds_read_b128 a[208:211], v220 offset:20480\n\t" \
  "v_cvt_pk_bf16_f32 v133, v10, v11\n\t" \
  "v_mfma_f32_16x16x32_bf16 a[52:55], a[168:171], v[136:139], a[52:55]\n\t" \
  "v_exp_f32 v70, v70\n\t" \
  "v_mfma_f32_16x16x32_bf16 a[52:55], a[172:175], v[140:143], a[52:55]\n\t" \
  "ds_read_b128 a[212:215], v221 offset:20480\n\t" \
  "v_cvt_pk_bf16_f32 v134, v12, v13\n\t" \
  "v_mfma_f32_16x16x32_bf16 a[56:59], a[176:179], v[136:139], a[56:59]\n\t" \
  "v_exp_f32 v71, v71\n\t" \
  "v_mfma_f32_16x16x32_bf16 a[56:59], a[180:183], v[140:143], a[56:59]\n\t" \
  "v_exp_f32 v72, v72\n\t" \
  "v_mfma_f32_16x16x32_bf16 a[60:63], a[184:187], v[136:139], a[60:63]\n\t" \
  "buffer_load_dwordx4 v226, s[60:63], s96 offen offset:2048 lds\n\t" \
  "v_mfma_f32_16x16x32_bf16 a[60:63], a[188:191], v[140:143], a[60:63]\n\t" \
  "ds_read_b128 a[216:219], v222 offset:20480\n\t" \
  "v_cvt_pk_bf16_f32 v135, v14, v15\n\t" \
  "v_mfma_f32_16x16x32_bf16 v[164:167], v[216:219], v[136:139], v[164:167]\n\t" \
  "v_exp_f32 v73, v73\n\t" \
  "v_mfma_f32_16x16x32_bf16 v[164:167], v[216:219], v[140:143], v[164:167]\n\t" \
  "ds_read_b128 a[220:223], v223 offset:20480\n\t" \
  "v_cvt_pk_bf16_f32 v136, v32, v33\n\t" \
  "v_mfma_f32_16x16x32_bf16 a[64:67], a[128:131], v[144:147], a[64:67]\n\t" \
  "v_cvt_pk_bf16_f32 v137, v34, v35\n\t" \
  "v_cvt_pk_bf16_f32 v138, v36, v37\n\t" \
  "v_mfma_f32_16x16x32_bf16 a[64:67], a[132:135], v[148:151], a[64:67]\n\t" \
  "v_exp_f32 v74, v74\n\t" \
  "v_mfma_f32_16x16x32_bf16 a[68:71], a[136:139], v[144:147], a[68:71]\n\t" \
  "buffer_load_dwordx4 v227, s[60:63], s96 offen offset:3072 lds\n\t" \
  "v_mfma_f32_16x16x32_bf16 a[68:71], a[140:143], v[148:151], a[68:71]\n\t" \
  "ds_read_b128 a[224:227], v220 offset:24576\n\t" \
  "v_cvt_pk_bf16_f32 v139, v38, v39\n\t" \
  "v_mfma_f32_16x16x32_bf16 a[72:75], a[144:147], v[144:147], a[72:75]\n\t" \
  "s_add_u32 m0, s38, 147456\n\t" \
  "v_exp_f32 v75, v75\n\t" \
  "v_mfma_f32_16x16x32_bf16 a[72:75], a[148:151], v[148:151], a[72:75]\n\t" \
  "s_add_u32 s96, s38, 81920\n\t" \
  "ds_read_b128 a[228:231], v221 offset:24576\n\t" \
  "v_cvt_pk_bf16_f32 v140, v40, v41\n\t" \
  "v_mfma_f32_16x16x32_bf16 a[76:79], a[152:155], v[144:147], a[76:79]\n\t" \
  "v_exp_f32 v76, v76\n\t" \
  "v_mfma_f32_16x16x32_bf16 a[76:79], a[156:159], v[148:151], a[76:79]\n\t" \
  "v_exp_f32 v77, v77\n\t" \
  "v_mfma_f32_16x16x32_bf16 a[80:83], a[160:163], v[144:147], a[80:83]\n\t" \
  "buffer_load_dwordx4 v230, s[64:67], s96 offen offset:0 lds\n\t" \
  "v_mfma_f32_16x16x32_bf16 a[80:83], a[164:167], v[148:151], a[80:83]\n\t" \
  "ds_read_b128 a[232:235], v222 offset:24576\n\t" \
  "v_cvt_pk_bf16_f32 v141, v42, v43\n\t" \
  "v_mfma_f32_16x16x32_bf16 a[84:87], a[168:171], v[144:147], a[84:87]\n\t" \
  "v_exp_f32 v78, v78\n\t" \
  "v_mfma_f32_16x16x32_bf16 a[84:87], a[172:175], v[148:151], a[84:87]\n\t" \
  "ds_read_b128 a[236:239], v223 offset:24576\n\t" \
  "v_cvt_pk_bf16_f32 v142, v44, v45\n\t" \
  "v_mfma_f32_16x16x32_bf16 a[88:91], a[176:179], v[144:147], a[88:91]\n\t" \
  "v_exp_f32 v79, v79\n\t" \
  "v_mfma_f32_16x16x32_bf16 a[88:91], a[180:183], v[148:151], a[88:91]\n\t" \
  "v_exp_f32 v96, v96\n\t" \
  "v_mfma_f32_16x16x32_bf16 a[92:95], a[184:187], v[144:147], a[92:95]\n\t" \
  "buffer_load_dwordx4 v231, s[64:67], s96 offen offset:1024 lds\n\t" \
  "v_mfma_f32_16x16x32_bf16 a[92:95], a[188:191], v[148:151], a[92:95]\n\t" \
  "ds_read_b128 a[240:243], v220 offset:28672\n\t" \
  "v_cvt_pk_bf16_f32 v143, v46, v47\n\t" \
  "v_mfma_f32_16x16x32_bf16 v[168:171], v[216:219], v[144:147], v[168:171]\n\t" \
  ".Lhs8:\n\ts_cbranch_execz .Lhs8n\n\t" \
  "v_exp_f32 v97, v97\n\t" \
  "v_mfma_f32_16x16x32_bf16 v[168:171], v[216:219], v[148:151], v[168:171]\n\t" \
  "ds_read_b128 a[244:247], v221 offset:28672\n\t" \
  "v_cvt_pk_bf16_f32 v144, v64, v65\n\t" \
  "v_mfma_f32_16x16x32_bf16 a[96:99], a[128:131], v[152:155], a[96:99]\n\t" \
  "v_cvt_pk_bf16_f32 v145, v66, v67\n\t" \
  "v_cvt_pk_bf16_f32 v146, v68, v69\n\t" \
  "v_mfma_f32_16x16x32_bf16 a[96:99], a[132:135], v[156:159], a[96:99]\n\t" \
  "v_exp_f32 v98, v98\n\t" \
  "v_mfma_f32_16x16x32_bf16 a[100:103], a[136:139], v[152:155], a[100:103]\n\t" \
  "buffer_load_dwordx4 v230, s[64:67], s96 offen offset:2048 lds\n\t" \
  "v_mfma_f32_16x16x32_bf16 a[100:103], a[140:143], v[156:159], a[100:103]\n\t" \
  "ds_read_b128 a[248:251], v222 offset:28672\n\t" \
  "v_cvt_pk_bf16_f32 v147, v70, v71\n\t" \
  "v_mfma_f32_16x16x32_bf16 a[104:107], a[144:147], v[152:155], a[104:107]\n\t" \
  "v_exp_f32 v99, v99\n\t" \
  "v_mfma_f32_16x16x32_bf16 a[104:107], a[148:151], v[156:159], a[104:107]\n\t" \
  "ds_read_b128 a[252:255], v223 offset:28672\n\t" \
  "v_cvt_pk_bf16_f32 v148, v72, v73\n\t" \
  "v_mfma_f32_16x16x32_bf16 a[108:111], a[152:155], v[152:155], a[108:111]\n\t" \
  "v_exp_f32 v100, v100\n\t" \
  "v_mfma_f32_16x16x32_bf16 a[108:111], a[156:159], v[156:159], a[108:111]\n\t" \
  "v_exp_f32 v101, v101\n\t" \
  "v_mfma_f32_16x16x32_bf16 a[112:115], a[160:163], v[152:155], a[112:115]\n\t" \
  "buffer_load_dwordx4 v231, s[64:67], s96 offen offset:3072 lds\n\t" \
  "v_mfma_f32_16x16x32_bf16 a[112:115], a[164:167], v[156:159], a[112:115]\n\t" \
  "v_cvt_pk_bf16_f32 v149, v74, v75\n\t" \
  "v_cvt_pk_bf16_f32 v150, v76, v77\n\t" \
  "v_mfma_f32_16x16x32_bf16 a[116:119], a[168:171], v[152:155], a[116:119]\n\t" \
  "v_exp_f32 v102, v102\n\t" \
  "v_mfma_f32_16x16x32_bf16 a[116:119], a[172:175], v[156:159], a[116:119]\n\t" \
  "v_exp_f32 v103, v103\n\t" \
  "v_mfma_f32_16x16x32_bf16 a[120:123], a[176:179], v[152:155], a[120:123]\n\t" \
  "v_exp_f32 v104, v104\n\t" \
  "v_mfma_f32_16x16x32_bf16 a[120:123], a[180:183], v[156:159], a[120:123]\n\t" \
  "s_add_u32 s99, s52, 4096\n\t" \
  "v_exp_f32 v105, v105\n\t" \
  "v_mfma_f32_16x16x32_bf16 a[124:127], a[184:187], v[152:155], a[124:127]\n\t" \
  "v_exp_f32 v106, v106\n\t" \
  "v_mfma_f32_16x16x32_bf16 a[124:127], a[188:191], v[156:159], a[124:127]\n\t" \
  "v_exp_f32 v107, v107\n\t" \
  "v_mfma_f32_16x16x32_bf16 v[172:175], v[216:219], v[152:155], v[172:175]\n\t" \
  "buffer_load_dword v246, v212, s[24:27], s99 offen\n\t" \
  "v_mfma_f32_16x16x32_bf16 v[172:175], v[216:219], v[156:159], v[172:175]\n\t" \
  "s_waitcnt lgkmcnt(12)\n\t" \
  "v_cvt_pk_bf16_f32 v151, v78, v79\n\t" \
  "v_cvt_pk_bf16_f32 v152, v96, v97\n\t" \
  STA("s_memrealtime s[94:95]\n\t") \
  STA("s_memtime s[92:93]\n\t") \
  "v_mfma_f32_16x16x32_bf16 v[0:3], a[192:195], v[16:19], v[176:179]\n\t" \
  "ds_read_b64_tr_b16 a[128:129], v228 offset:0\n\t" \
  "v_cvt_pk_bf16_f32 v153, v98, v99\n\t" \
  "v_mfma_f32_16x16x32_bf16 v[0:3], a[196:199], v[20:23], v[0:3]\n\t" \
  "v_cvt_pk_bf16_f32 v154, v100, v101\n\t" \
  "v_cvt_pk_bf16_f32 v155, v102, v103\n\t" \
  "v_mfma_f32_16x16x32_bf16 v[0:3], a[200:203], v[24:27], v[0:3]\n\t" \
  "ds_read_b64_tr_b16 a[130:131], v228 offset:4096\n\t" \
  "v_cvt_pk_bf16_f32 v156, v104, v105\n\t" \
  "v_mfma_f32_16x16x32_bf16 v[0:3], a[204:207], v[28:31], v[0:3]\n\t" \
  "s_waitcnt lgkmcnt(10)\n\t" \
  "v_exp_f32 v108, v108\n\t" \
  "v_mfma_f32_16x16x32_bf16 v[4:7], a[220:223], v[28:31], v[176:179]\n\t" \
  "ds_read_b64_tr_b16 a[132:133], v228 offset:8192\n\t" \
  "v_cvt_pk_bf16_f32 v157, v106, v107\n\t" \
  "v_mfma_f32_16x16x32_bf16 v[4:7], a[216:219], v[24:27], v[4:7]\n\t" \
  "v_exp_f32 v109, v109\n\t" \
  "v_mfma_f32_16x16x32_bf16 v[4:7], a[212:215], v[20:23], v[4:7]\n\t" \
  "ds_read_b64_tr_b16 a[134:135], v228 offset:12288\n\t" \
  "v_cvt_pk_bf16_f32 v158, v108, v109\n\t" \
  "v_mfma_f32_16x16x32_bf16 v[4:7], a[208:211], v[16:19], v[4:7]\n\t" \
  "s_waitcnt lgkmcnt(8)\n\t" \
  "ds_read_b64_tr_b16 a[136:137], v229 offset:0\n\t" \
  "v_writelane_b32 v244, 0, 0\n\t" \
  "v_mfma_f32_16x16x32_bf16 v[8:11], a[224:227], v[16:19], v[176:179]\n\t" \
  "v_exp_f32 v110, v110\n\t" \
  "v_mfma_f32_16x16x32_bf16 v[8:11], a[228:231], v[20:23], v[8:11]\n\t" \
  "s_add_u32 s99, s52, 6144\n\t" \
  "v_exp_f32 v111, v111\n\t" \
  "v_mfma_f32_16x16x32_bf16 v[8:11], a[232:235], v[24:27], v[8:11]\n\t" \
  "ds_read_b64_tr_b16 a[138:139], v229 offset:4096\n\t" \
  "v_cvt_pk_bf16_f32 v159, v110, v111\n\t" \
  "v_mfma_f32_16x16x32_bf16 v[8:11], a[236:239], v[28:31], v[8:11]\n\t" \
  "s_waitcnt lgkmcnt(6)\n\t" \
  "v_exp_f32 v0, v0\n\t" \
  "v_mfma_f32_16x16x32_bf16 v[12:15], a[252:255], v[28:31], v[176:179]\n\t" \
  STA("v_writelane_b32 v255, s94, 42\n\t") \
  STA("v_writelane_b32 v255, s92, 43\n\t") \
  "buffer_load_dword v246, v212, s[24:27], s99 offen\n\t" \
  "v_mfma_f32_16x16x32_bf16 v[12:15], a[248:251], v[24:27], v[12:15]\n\t" \
  "v_exp_f32 v1, v1\n\t" \
  "v_mfma_f32_16x16x32_bf16 v[12:15], a[244:247], v[20:23], v[12:15]\n\t" \
  "v_exp_f32 v2, v2\n\t" \
  "v_mfma_f32_16x16x32_bf16 v[12:15], a[240:243], v[16:19], v[12:15]\n\t" \
  "ds_read_b64_tr_b16 a[140:141], v229 offset:8192\n\t" \
  "v_mfma_f32_16x16x32_bf16 v[32:35], a[192:195], v[48:51], v[180:183]\n\t" \
  "ds_read_b64_tr_b16 a[142:143], v229 offset:12288\n\t" \
  "v_mfma_f32_16x16x32_bf16 v[32:35], a[196:199], v[52:55], v[32:35]\n\t" \
  "v_exp_f32 v3, v3\n\t" \
  "v_mfma_f32_16x16x32_bf16 v[32:35], a[200:203], v[56:59], v[32:35]\n\t" \
  "ds_read_b64_tr_b16 a[144:145], v228 offset:256\n\t" \
  "v_mfma_f32_16x16x32_bf16 v[32:35], a[204:207], v[60:63], v[32:35]\n\t" \
  "v_exp_f32 v4, v4\n\t" \
  "v_mfma_f32_16x16x32_bf16 v[36:39], a[220:223], v[60:63], v[180:183]\n\t" \
  "ds_read_b64_tr_b16 a[146:147], v228 offset:4352\n\t" \
  "v_mfma_f32_16x16x32_bf16 v[36:39], a[216:219], v[56:59], v[36:39]\n\t" \
  "ds_read_b64_tr_b16 a[148:149], v228 offset:8448\n\t" \
  "v_mfma_f32_16x16x32_bf16 v[36:39], a[212:215], v[52:55], v[36:39]\n\t" \
  "v_exp_f32 v5, v5\n\t" \
  "v_mfma_f32_16x16x32_bf16 v[36:39], a[208:211], v[48:51], v[36:39]\n\t" \
  "ds_read_b64_tr_b16 a[150:151], v228 offset:12544\n\t" \
  "v_mfma_f32_16x16x32_bf16 v[40:43], a[224:227], v[48:51], v[180:183]\n\t" \
  "v_exp_f32 v6, v6\n\t" \
  "v_mfma_f32_16x16x32_bf16 v[40:43], a[228:231], v[52:55], v[40:43]\n\t" \
  "ds_read_b64_tr_b16 a[152:153], v229 offset:256\n\t" \
  "v_mfma_f32_16x16x32_bf16 v[40:43], a[232:235], v[56:59], v[40:43]\n\t" \
  "v_exp_f32 v7, v7\n\t" \
  "v_mfma_f32_16x16x32_bf16 v[40:43], a[236:239], v[60:63], v[40:43]\n\t" \
  "ds_read_b64_tr_b16 a[154:155], v229 offset:4352\n\t" \
  "v_mfma_f32_16x16x32_bf16 v[44:47], a[252:255], v[60:63], v[180:183]\n\t" \
  "ds_read_b64_tr_b16 a[156:157], v229 offset:8448\n\t" \
  "v_mfma_f32_16x16x32_bf16 v[44:47], a[248:251], v[56:59], v[44:47]\n\t" \
  "v_exp_f32 v8, v8\n\t" \
  "v_mfma_f32_16x16x32_bf16 v[44:47], a[244:247], v[52:55], v[44:47]\n\t" \
  "ds_read_b64_tr_b16 a[158:159], v229 offset:12544\n\t" \
  "v_mfma_f32_16x16x32_bf16 v[44:47], a[240:243], v[48:51], v[44:47]\n\t" \
  "v_exp_f32 v9, v9\n\t" \
  "v_mfma_f32_16x16x32_bf16 v[64:67], a[192:195], v[80:83], v[184:187]\n\t" \
  "ds_read_b64_tr_b16 a[160:161], v228 offset:512\n\t" \
  "v_mfma_f32_16x16x32_bf16 v[64:67], a[196:199], v[84:87], v[64:67]\n\t" \
  "ds_read_b64_tr_b16 a[162:163], v228 offset:4608\n\t" \
  "v_mfma_f32_16x16x32_bf16 v[64:67], a[200:203], v[88:91], v[64:67]\n\t" \
  "v_exp_f32 v10, v10\n\t" \
  "v_mfma_f32_16x16x32_bf16 v[64:67], a[204:207], v[92:95], v[64:67]\n\t" \
  "ds_read_b64_tr_b16 a[164:165], v228 offset:8704\n\t" \
  "v_mfma_f32_16x16x32_bf16 v[68:71], a[220:223], v[92:95], v[184:187]\n\t" \
  "v_exp_f32 v11, v11\n\t" \
  "v_mfma_f32_16x16x32_bf16 v[68:71], a[216:219], v[88:91], v[68:71]\n\t" \
  "ds_read_b64_tr_b16 a[166:167], v228 offset:12800\n\t" \
  "v_mfma_f32_16x16x32_bf16 v[68:71], a[212:215], v[84:87], v[68:71]\n\t" \
  "ds_read_b64_tr_b16 a[168:169], v229 offset:512\n\t" \
  "v_mfma_f32_16x16x32_bf16 v[68:71], a[208:211], v[80:83], v[68:71]\n\t" \
  "v_exp_f32 v12, v12\n\t" \
  "v_mfma_f32_16x16x32_bf16 v[72:75], a[224:227], v[80:83], v[184:187]\n\t" \
  "ds_read_b64_tr_b16 a[170:171], v229 offset:4608\n\t" \
  "v_mfma_f32_16x16x32_bf16 v[72:75], a[228:231], v[84:87], v[72:75]\n\t" \
  "ds_read_b64_tr_b16 a[172:173], v229 offset:8704\n\t" \
  "v_mfma_f32_16x16x32_bf16 v[72:75], a[232:235], v[88:91], v[72:75]\n\t" \
  "v_exp_f32 v13, v13\n\t" \
  "v_mfma_f32_16x16x32_bf16 v[72:75], a[236:239], v[92:95], v[72:75]\n\t" \
  "ds_read_b64_tr_b16 a[174:175], v229 offset:12800\n\t" \
  "v_mfma_f32_16x16x32_bf16 v[76:79], a[252:255], v[92:95], v[184:187]\n\t" \
  "v_exp_f32 v14, v14\n\t" \
  "v_mfma_f32_16x16x32_bf16 v[76:79], a[248:251], v[88:91], v[76:79]\n\t" \
  "ds_read_b64_tr_b16 a[176:177], v228 offset:768\n\t" \
  "v_mfma_f32_16x16x32_bf16 v[76:79], a[244:247], v[84:87], v[76:79]\n\t" \
  "ds_read_b64_tr_b16 a[178:179], v228 offset:4864\n\t" \
  "v_mfma_f32_16x16x32_bf16 v[76:79], a[240:243], v[80:83], v[76:79]\n\t" \
  "v_exp_f32 v15, v15\n\t" \
  "v_mfma_f32_16x16x32_bf16 v[96:99], a[192:195], v[112:115], v[188:191]\n\t" \
  "ds_read_b64_tr_b16 a[180:181], v228 offset:8960\n\t" \
  "v_mfma_f32_16x16x32_bf16 v[96:99], a[196:199], v[116:119], v[96:99]\n\t" \
  "ds_read_b64_tr_b16 a[182:183], v228 offset:13056\n\t" \
  "v_mfma_f32_16x16x32_bf16 v[96:99], a[200:203], v[120:123], v[96:99]\n\t" \
  "v_exp_f32 v32, v32\n\t" \
  "v_mfma_f32_16x16x32_bf16 v[96:99], a[204:207], v[124:127], v[96:99]\n\t" \
  "ds_read_b64_tr_b16 a[184:185], v229 offset:768\n\t" \
  "v_mfma_f32_16x16x32_bf16 v[100:103], a[220:223], v[124:127], v[188:191]\n\t" \
  "v_exp_f32 v33, v33\n\t" \
  "v_mfma_f32_16x16x32_bf16 v[100:103], a[216:219], v[120:123], v[100:103]\n\t" \
  "ds_read_b64_tr_b16 a[186:187], v229 offset:4864\n\t" \
  "v_mfma_f32_16x16x32_bf16 v[100:103], a[212:215], v[116:119], v[100:103]\n\t" \
  "ds_read_b64_tr_b16 a[188:189], v229 offset:8960\n\t" \
  "v_mfma_f32_16x16x32_bf16 v[100:103], a[208:211], v[112:115], v[100:103]\n\t" \
  "v_exp_f32 v34, v34\n\t" \
  "v_mfma_f32_16x16x32_bf16 v[104:107], a[224:227], v[112:115], v[188:191]\n\t" \
  "ds_read_b64_tr_b16 a[190:191], v229 offset:13056\n\t" \
  "v_mfma_f32_16x16x32_bf16 v[104:107], a[228:231], v[116:119], v[104:107]\n\t" \
  "v_exp_f32 v35, v35\n\t" \
  "v_mfma_f32_16x16x32_bf16 v[104:107], a[232:235], v[120:123], v[104:107]\n\t" \
  "v_exp_f32 v36, v36\n\t" \
  "v_mfma_f32_16x16x32_bf16 v[104:107], a[236:239], v[124:127], v[104:107]\n\t" \
  "v_exp_f32 v37, v37\n\t" \
  "v_mfma_f32_16x16x32_bf16 v[108:111], a[252:255], v[124:127], v[188:191]\n\t" \
  "v_exp_f32 v38, v38\n\t" \
  "v_mfma_f32_16x16x32_bf16 v[108:111], a[248:251], v[120:123], v[108:111]\n\t" \
  "v_exp_f32 v39, v39\n\t" \
  "v_mfma_f32_16x16x32_bf16 v[108:111], a[244:247], v[116:119], v[108:111]\n\t" \
  "s_waitcnt vmcnt(2) lgkmcnt(0)\n\t" \
  "v_exp_f32 v40, v40\n\t" \
  "v_mfma_f32_16x16x32_bf16 v[108:111], a[240:243], v[112:115], v[108:111]\n\t" \
  "s_barrier\n\t" \
  STA("s_memrealtime s[94:95]\n\t") \
  STA("s_memtime s[92:93]\n\t") \
  "v_mfma_f32_16x16x32_bf16 a[0:3], a[128:131], v[128:131], a[0:3]\n\t" \
  "v_exp_f32 v41, v41\n\t" \
  "v_mfma_f32_16x16x32_bf16 a[0:3], a[132:135], v[132:135], a[0:3]\n\t" \
  "v_exp_f32 v42, v42\n\t" \
  "v_mfma_f32_16x16x32_bf16 a[4:7], a[136:139], v[128:131], a[4:7]\n\t" \
  "v_exp_f32 v43, v43\n\t" \
  "v_mfma_f32_16x16x32_bf16 a[4:7], a[140:143], v[132:135], a[4:7]\n\t" \
  "v_exp_f32 v44, v44\n\t" \
  "v_mfma_f32_16x16x32_bf16 a[8:11], a[144:147], v[128:131], a[8:11]\n\t" \
  "v_exp_f32 v45, v45\n\t" \
  "v_mfma_f32_16x16x32_bf16 a[8:11], a[148:151], v[132:135], a[8:11]\n\t" \
  "v_exp_f32 v46, v46\n\t" \
  "v_mfma_f32_16x16x32_bf16 a[12:15], a[152:155], v[128:131], a[12:15]\n\t" \
  "v_exp_f32 v47, v47\n\t" \
  "v_mfma_f32_16x16x32_bf16 a[12:15], a[156:159], v[132:135], a[12:15]\n\t" \
  "v_exp_f32 v64, v64\n\t" \
  "v_mfma_f32_16x16x32_bf16 a[16:19], a[160:163], v[128:131], a[16:19]\n\t" \
  "v_exp_f32 v65, v65\n\t" \
  "v_mfma_f32_16x16x32_bf16 a[16:19], a[164:167], v[132:135], a[16:19]\n\t" \
  "v_exp_f32 v66, v66\n\t" \
  "v_mfma_f32_16x16x32_bf16 a[20:23], a[168:171], v[128:131], a[20:23]\n\t" \
  "v_exp_f32 v67, v67\n\t" \
  "v_mfma_f32_16x16x32_bf16 a[20:23], a[172:175], v[132:135], a[20:23]\n\t" \
  "v_exp_f32 v68, v68\n\t" \
  "v_mfma_f32_16x16x32_bf16 a[24:27], a[176:179], v[128:131], a[24:27]\n\t" \
  STA("v_writelane_b32 v255, s94, 44\n\t") \
  STA("v_writelane_b32 v255, s92, 45\n\t") \
  "v_exp_f32 v69, v69\n\t" \
  "v_mfma_f32_16x16x32_bf16 a[24:27], a[180:183], v[132:135], a[24:27]\n\t" \
  "v_exp_f32 v70, v70\n\t" \
  "v_mfma_f32_16x16x32_bf16 a[28:31], a[184:187], v[128:131], a[28:31]\n\t" \
  "v_exp_f32 v71, v71\n\t" \
  "v_mfma_f32_16x16x32_bf16 a[28:31], a[188:191], v[132:135], a[28:31]\n\t" \
  "v_exp_f32 v72, v72\n\t" \
  "v_mfma_f32_16x16x32_bf16 v[160:163], v[216:219], v[128:131], v[160:163]\n\t" \
  "s_add_u32 m0, s38, 65536\n\t" \
  "v_exp_f32 v73, v73\n\t" \
  "v_mfma_f32_16x16x32_bf16 v[160:163], v[216:219], v[132:135], v[160:163]\n\t" \
  "s_add_u32 s96, s38, 114688\n\t" \
  "ds_read_b128 a[192:195], v220 offset:0\n\t" \
  "v_cvt_pk_bf16_f32 v128, v0, v1\n\t" \
  "v_mfma_f32_16x16x32_bf16 a[32:35], a[128:131], v[136:139], a[32:35]\n\t" \
  "ds_read_b128 a[196:199], v221 offset:0\n\t" \
  "v_cvt_pk_bf16_f32 v129, v2, v3\n\t" \
  "v_mfma_f32_16x16x32_bf16 a[32:35], a[132:135], v[140:143], a[32:35]\n\t" \
  "ds_read_b128 v[192:195], v215 offset:32768\n\t" \
  "v_cvt_pk_bf16_f32 v130, v4, v5\n\t" \
  "v_mfma_f32_16x16x32_bf16 a[36:39], a[136:139], v[136:139], a[36:39]\n\t" \
  "buffer_load_dwordx4 v224, s[60:63], s96 offen offset:0 lds\n\t" \
  "v_mfma_f32_16x16x32_bf16 a[36:39], a[140:143], v[140:143], a[36:39]\n\t" \
  "ds_read_b128 a[200:203], v222 offset:0\n\t" \
  "v_cvt_pk_bf16_f32 v131, v6, v7\n\t" \
  "v_mfma_f32_16x16x32_bf16 a[40:43], a[144:147], v[136:139], a[40:43]\n\t" \
  "ds_read_b128 v[196:199], v215 offset:33792\n\t" \
  "v_cvt_pk_bf16_f32 v132, v8, v9\n\t" \
  "v_mfma_f32_16x16x32_bf16 a[40:43], a[148:151], v[140:143], a[40:43]\n\t" \
  "ds_read_b128 v[200:203], v215 offset:34816\n\t" \
  "v_cvt_pk_bf16_f32 v133, v10, v11\n\t" \
  "v_mfma_f32_16x16x32_bf16 a[44:47], a[152:155], v[136:139], a[44:47]\n\t" \
  "buffer_load_dwordx4 v225, s[60:63], s96 offen offset:1024 lds\n\t" \
  "v_mfma_f32_16x16x32_bf16 a[44:47], a[156:159], v[140:143], a[44:47]\n\t" \
  "ds_read_b128 a[204:207], v223 offset:0\n\t" \
  "v_cvt_pk_bf16_f32 v134, v12, v13\n\t" \
  "v_mfma_f32_16x16x32_bf16 a[48:51], a[160:163], v[136:139], a[48:51]\n\t" \
  "ds_read_b128 v[204:207], v215 offset:35840\n\t" \
  "v_cvt_pk_bf16_f32 v135, v14, v15\n\t" \
  "v_mfma_f32_16x16x32_bf16 a[48:51], a[164:167], v[140:143], a[48:51]\n\t" \
  "v_exp_f32 v74, v74\n\t" \
  "v_mfma_f32_16x16x32_bf16 a[52:55], a[168:171], v[136:139], a[52:55]\n\t" \
  "buffer_load_dwordx4 v226, s[60:63], s96 offen offset:2048 lds\n\t" \
  "v_mfma_f32_16x16x32_bf16 a[52:55], a[172:175], v[140:143], a[52:55]\n\t" \
  "v_exp_f32 v75, v75\n\t" \
  "v_mfma_f32_16x16x32_bf16 a[56:59], a[176:179], v[136:139], a[56:59]\n\t" \
  "v_exp_f32 v76, v76\n\t" \
  "v_mfma_f32_16x16x32_bf16 a[56:59], a[180:183], v[140:143], a[56:59]\n\t" \
  "ds_read_b128 a[208:211], v220 offset:4096\n\t" \
  "v_mfma_f32_16x16x32_bf16 a[60:63], a[184:187], v[136:139], a[60:63]\n\t" \
  "buffer_load_dwordx4 v227, s[60:63], s96 offen offset:3072 lds\n\t" \
  "v_mfma_f32_16x16x32_bf16 a[60:63], a[188:191], v[140:143], a[60:63]\n\t" \
  "s_add_u32 m0, s38, 81920\n\t" \
  "ds_read_b128 a[212:215], v221 offset:4096\n\t" \
  "s_add_u32 s96, s38, 98304\n\t" \
  "v_mfma_f32_16x16x32_bf16 v[164:167], v[216:219], v[136:139], v[164:167]\n\t" \
  "v_exp_f32 v77, v77\n\t" \
  "v_mfma_f32_16x16x32_bf16 v[164:167], v[216:219], v[140:143], v[164:167]\n\t" \
  "ds_read_b128 a[216:219], v222 offset:4096\n\t" \
  "v_cvt_pk_bf16_f32 v136, v32, v33\n\t" \
  "v_mfma_f32_16x16x32_bf16 a[64:67], a[128:131], v[144:147], a[64:67]\n\t" \
  "buffer_load_dwordx4 v230, s[64:67], s96 offen offset:0 lds\n\t" \
  "v_mfma_f32_16x16x32_bf16 a[64:67], a[132:135], v[148:151], a[64:67]\n\t" \
  "ds_read_b128 a[220:223], v223 offset:4096\n\t" \
  "v_cvt_pk_bf16_f32 v137, v34, v35\n\t" \
  "v_mfma_f32_16x16x32_bf16 a[68:71], a[136:139], v[144:147], a[68:71]\n\t" \
  "ds_read_b128 a[224:227], v220 offset:8192\n\t" \
  "v_cvt_pk_bf16_f32 v138, v36, v37\n\t" \
  "v_mfma_f32_16x16x32_bf16 a[68:71], a[140:143], v[148:151], a[68:71]\n\t" \
  "v_cvt_pk_bf16_f32 v139, v38, v39\n\t" \
  "v_cvt_pk_bf16_f32 v140, v40, v41\n\t" \
  "v_mfma_f32_16x16x32_bf16 a[72:75], a[144:147], v[144:147], a[72:75]\n\t" \
  "buffer_load_dwordx4 v231, s[64:67], s96 offen offset:1024 lds\n\t" \
  "v_mfma_f32_16x16x32_bf16 a[72:75], a[148:151], v[148:151], a[72:75]\n\t" \
  "ds_read_b128 a[228:231], v221 offset:8192\n\t" \
  "v_cvt_pk_bf16_f32 v141, v42, v43\n\t" \
  "v_mfma_f32_16x16x32_bf16 a[76:79], a[152:155], v[144:147], a[76:79]\n\t" \
  "v_exp_f32 v78, v78\n\t" \
  "v_mfma_f32_16x16x32_bf16 a[76:79], a[156:159], v[148:151], a[76:79]\n\t" \
  "v_cvt_pk_bf16_f32 v142, v44, v45\n\t" \
  "v_cvt_pk_bf16_f32 v143, v46, v47\n\t" \
  "v_mfma_f32_16x16x32_bf16 a[80:83], a[160:163], v[144:147], a[80:83]\n\t" \
  "buffer_load_dwordx4 v230, s[64:67], s96 offen offset:2048 lds\n\t" \
  "v_mfma_f32_16x16x32_bf16 a[80:83], a[164:167], v[148:151], a[80:83]\n\t" \
  "v_exp_f32 v79, v79\n\t" \
  "v_mfma_f32_16x16x32_bf16 a[84:87], a[168:171], v[144:147], a[84:87]\n\t" \
  "v_exp_f32 v96, v96\n\t" \
  "v_mfma_f32_16x16x32_bf16 a[84:87], a[172:175], v[148:151], a[84:87]\n\t" \
  "v_exp_f32 v97, v97\n\t" \
  "v_mfma_f32_16x16x32_bf16 a[88:91], a[176:179], v[144:147], a[88:91]\n\t" \
  "buffer_load_dwordx4 v231, s[64:67], s96 offen offset:3072 lds\n\t" \
  "v_mfma_f32_16x16x32_bf16 a[88:91], a[180:183], v[148:151], a[88:91]\n\t" \
  "s_add_u32 s96, s40, 524288\n\t" \
  "ds_read_b128 a[232:235], v222 offset:8192\n\t" \
  "s_and_b32 s96, s96, 0x1c0000\n\t" \
  "v_mfma_f32_16x16x32_bf16 a[92:95], a[184:187], v[144:147], a[92:95]\n\t" \
  "s_add_u32 s96, s96, s51\n\t" \
  "ds_read_b128 a[236:239], v223 offset:8192\n\t" \
  "s_add_u32 m0, s38, 131072\n\t" \
  "v_mfma_f32_16x16x32_bf16 a[92:95], a[188:191], v[148:151], a[92:95]\n\t" \
  "v_exp_f32 v98, v98\n\t" \
  "v_mfma_f32_16x16x32_bf16 v[168:171], v[216:219], v[144:147], v[168:171]\n\t" \
  "s_waitcnt lgkmcnt(8)\n\t" \
  "buffer_load_dwordx4 v240, s[24:27], s96 offen offset:0 lds\n\t" \
  "v_mfma_f32_16x16x32_bf16 v[168:171], v[216:219], v[148:151], v[168:171]\n\t" \
  "ds_read_b128 a[240:243], v220 offset:12288\n\t" \
  "v_cvt_pk_bf16_f32 v144, v64, v65\n\t" \
  "v_mfma_f32_16x16x32_bf16 a[96:99], a[128:131], v[152:155], a[96:99]\n\t" \
  "ds_read_b128 a[244:247], v221 offset:12288\n\t" \
  "v_cvt_pk_bf16_f32 v145, v66, v67\n\t" \
  "v_mfma_f32_16x16x32_bf16 a[96:99], a[132:135], v[156:159], a[96:99]\n\t" \
  "v_cvt_pk_bf16_f32 v146, v68, v69\n\t" \
  "v_cvt_pk_bf16_f32 v147, v70, v71\n\t" \
  "v_mfma_f32_16x16x32_bf16 a[100:103], a[136:139], v[152:155], a[100:103]\n\t" \
  "buffer_load_dwordx4 v241, s[24:27], s96 offen offset:1024 lds\n\t" \
  "v_mfma_f32_16x16x32_bf16 a[100:103], a[140:143], v[156:159], a[100:103]\n\t" \
  "ds_read_b128 a[248:251], v222 offset:12288\n\t" \
  "v_cvt_pk_bf16_f32 v148, v72, v73\n\t" \
  "v_mfma_f32_16x16x32_bf16 a[104:107], a[144:147], v[152:155], a[104:107]\n\t" \
  "v_exp_f32 v99, v99\n\t" \
  "v_mfma_f32_16x16x32_bf16 a[104:107], a[148:151], v[156:159], a[104:107]\n\t" \
  "ds_read_b128 a[252:255], v223 offset:12288\n\t" \
  "v_cvt_pk_bf16_f32 v149, v74, v75\n\t" \
  "v_mfma_f32_16x16x32_bf16 a[108:111], a[152:155], v[152:155], a[108:111]\n\t" \
  "buffer_load_dwordx4 v248, s[24:27], s96 offen offset:2048 lds\n\t" \
  "v_mfma_f32_16x16x32_bf16 a[108:111], a[156:159], v[156:159], a[108:111]\n\t" \
  "v_exp_f32 v100, v100\n\t" \
  "v_mfma_f32_16x16x32_bf16 a[112:115], a[160:163], v[152:155], a[112:115]\n\t" \
  "v_exp_f32 v101, v101\n\t" \
  "v_mfma_f32_16x16x32_bf16 a[112:115], a[164:167], v[156:159], a[112:115]\n\t" \
  "v_exp_f32 v102, v102\n\t" \
  "v_mfma_f32_16x16x32_bf16 a[116:119], a[168:171], v[152:155], a[116:119]\n\t" \
  "buffer_load_dwordx4 v249, s[24:27], s96 offen offset:3072 lds\n\t" \
  "v_mfma_f32_16x16x32_bf16 a[116:119], a[172:175], v[156:159], a[116:119]\n\t" \
  "v_cvt_pk_bf16_f32 v150, v76, v77\n\t" \
  "v_cvt_pk_bf16_f32 v151, v78, v79\n\t" \
  "v_mfma_f32_16x16x32_bf16 a[120:123], a[176:179], v[152:155], a[120:123]\n\t" \
  "v_exp_f32 v103, v103\n\t" \
  "v_mfma_f32_16x16x32_bf16 a[120:123], a[180:183], v[156:159], a[120:123]\n\t" \
  "s_lshl1_add_u32 s99, s38, 0\n\t" \
  "v_exp_f32 v104, v104\n\t" \
  "v_mfma_f32_16x16x32_bf16 a[124:127], a[184:187], v[152:155], a[124:127]\n\t" \
  "v_exp_f32 v105, v105\n\t" \
  "v_mfma_f32_16x16x32_bf16 a[124:127], a[188:191], v[156:159], a[124:127]\n\t" \
  "v_exp_f32 v106, v106\n\t" \
  "v_mfma_f32_16x16x32_bf16 v[172:175], v[216:219], v[152:155], v[172:175]\n\t" \
  "buffer_load_dword v246, v212, s[28:31], s99 offen\n\t" \
  "v_mfma_f32_16x16x32_bf16 v[172:175], v[216:219], v[156:159], v[172:175]\n\t" \
  "v_cvt_pk_bf16_f32 v152, v96, v97\n\t" \
  "v_cvt_pk_bf16_f32 v153, v98, v99\n\t" \
  STA("s_memrealtime s[94:95]\n\t") \
  STA("s_memtime s[92:93]\n\t") \
  "v_mfma_f32_16x16x32_bf16 v[0:3], a[192:195], v[16:19], v[176:179]\n\t" \
  "ds_read_b64_tr_b16 a[128:129], v210 offset:0\n\t" \
  "v_cvt_pk_bf16_f32 v154, v100, v101\n\t" \
  "v_mfma_f32_16x16x32_bf16 v[0:3], a[196:199], v[20:23], v[0:3]\n\t" \
  "v_exp_f32 v107, v107\n\t" \
  "v_mfma_f32_16x16x32_bf16 v[0:3], a[200:203], v[24:27], v[0:3]\n\t" \
  "ds_read_b64_tr_b16 a[130:131], v210 offset:4096\n\t" \
  "v_cvt_pk_bf16_f32 v155, v102, v103\n\t" \
  "v_mfma_f32_16x16x32_bf16 v[0:3], a[204:207], v[28:31], v[0:3]\n\t" \
  "s_waitcnt lgkmcnt(10)\n\t" \
  "v_exp_f32 v108, v108\n\t" \
  "v_mfma_f32_16x16x32_bf16 v[4:7], a[220:223], v[28:31], v[176:179]\n\t" \
  "ds_read_b64_tr_b16 a[132:133], v210 offset:8192\n\t" \
  "v_lshrrev_b32 v250, 1, v233\n\t" \
  "v_mfma_f32_16x16x32_bf16 v[4:7], a[216:219], v[24:27], v[4:7]\n\t" \
  "v_lshl_add_u32 v214, v233, 14, s41\n\t" \
  "v_lshlrev_b32 v250, 2, v250\n\t" \
  "v_mfma_f32_16x16x32_bf16 v[4:7], a[212:215], v[20:23], v[4:7]\n\t" \
  "v_min_u32 v214, .Lfz_z-.Lat_t1-4, v214\n\t" \
  "v_add_u32 v250, 8192, v250\n\t" \
  "v_mfma_f32_16x16x32_bf16 v[4:7], a[208:211], v[16:19], v[4:7]\n\t" \
  "s_waitcnt lgkmcnt(7)\n\t" \
  "ds_read_b64_tr_b16 a[134:135], v210 offset:12288\n\t" \
  "v_cvt_pk_bf16_f32 v156, v104, v105\n\t" \
  "v_mfma_f32_16x16x32_bf16 v[8:11], a[224:227], v[16:19], v[176:179]\n\t" \
  "buffer_load_dword v209, v250, s[16:19], 0 offen sc1\n\t" \
  "v_mfma_f32_16x16x32_bf16 v[8:11], a[228:231], v[20:23], v[8:11]\n\t" \
  "s_getpc_b64 s[84:85]\n\t" \
  ".Lat_t1:\n\t" \
  "ds_read_b64_tr_b16 a[136:137], v211 offset:0\n\t" \
  "v_cvt_pk_bf16_f32 v157, v106, v107\n\t" \
  "v_mfma_f32_16x16x32_bf16 v[8:11], a[232:235], v[24:27], v[8:11]\n\t" \
  "global_load_dword v246, v214, s[84:85]\n\t" \
  "v_mfma_f32_16x16x32_bf16 v[8:11], a[236:239], v[28:31], v[8:11]\n\t" \
  "s_waitcnt lgkmcnt(5)\n\t" \
  "ds_read_b64_tr_b16 a[138:139], v211 offset:4096\n\t" \
  "s_lshl1_add_u32 s99, s38, 2048\n\t" \
  "v_mfma_f32_16x16x32_bf16 v[12:15], a[252:255], v[28:31], v[176:179]\n\t" \
  STA("v_writelane_b32 v255, s94, 46\n\t") \
  STA("v_writelane_b32 v255, s92, 47\n\t") \
  "buffer_load_dword v246, v212, s[28:31], s99 offen\n\t" \
  "v_mfma_f32_16x16x32_bf16 v[12:15], a[248:251], v[24:27], v[12:15]\n\t" \
  "v_exp_f32 v109, v109\n\t" \
  "v_mfma_f32_16x16x32_bf16 v[12:15], a[244:247], v[20:23], v[12:15]\n\t" \
  "ds_read_b64_tr_b16 a[140:141], v211 offset:8192\n\t" \
  "v_cvt_pk_bf16_f32 v158, v108, v109\n\t" \
  "v_mfma_f32_16x16x32_bf16 v[12:15], a[240:243], v[16:19], v[12:15]\n\t" \
  "v_exp_f32 v110, v110\n\t" \
  "v_mfma_f32_16x16x32_bf16 v[32:35], a[192:195], v[48:51], v[180:183]\n\t" \
  "s_add_u32 s96, s40, 0\n\t" \
  "v_exp_f32 v111, v111\n\t" \
  "v_mfma_f32_16x16x32_bf16 v[32:35], a[196:199], v[52:55], v[32:35]\n\t" \
  "s_and_b32 s96, s96, 0x1c0000\n\t" \
  "ds_read_b64_tr_b16 a[142:143], v211 offset:12288\n\t" \
  "v_cvt_pk_bf16_f32 v159, v110, v111\n\t" \
  "v_mfma_f32_16x16x32_bf16 v[32:35], a[200:203], v[56:59], v[32:35]\n\t" \
  "s_add_u32 s96, s96, s51\n\t" \
  "ds_read_b64_tr_b16 a[144:145], v210 offset:256\n\t" \
  "s_add_u32 m0, s38, 0\n\t" \
  "v_mfma_f32_16x16x32_bf16 v[32:35], a[204:207], v[60:63], v[32:35]\n\t" \
  "v_exp_f32 v0, v0\n\t" \
  "v_mfma_f32_16x16x32_bf16 v[36:39], a[220:223], v[60:63], v[180:183]\n\t" \
  "buffer_load_dwordx4 v240, s[24:27], s96 offen offset:0 lds\n\t" \
  "v_mfma_f32_16x16x32_bf16 v[36:39], a[216:219], v[56:59], v[36:39]\n\t" \
  "ds_read_b64_tr_b16 a[146:147], v210 offset:4352\n\t" \
  "v_mfma_f32_16x16x32_bf16 v[36:39], a[212:215], v[52:55], v[36:39]\n\t" \
  "ds_read_b64_tr_b16 a[148:149], v210 offset:8448\n\t" \
  "v_mfma_f32_16x16x32_bf16 v[36:39], a[208:211], v[48:51], v[36:39]\n\t" \
  "ds_read_b64_tr_b16 a[150:151], v210 offset:12544\n\t" \
  "v_mfma_f32_16x16x32_bf16 v[40:43], a[224:227], v[48:51], v[180:183]\n\t" \
  "v_exp_f32 v1, v1\n\t" \
  "v_mfma_f32_16x16x32_bf16 v[40:43], a[228:231], v[52:55], v[40:43]\n\t" \
  "ds_read_b64_tr_b16 a[152:153], v211 offset:256\n\t" \
  "v_mfma_f32_16x16x32_bf16 v[40:43], a[232:235], v[56:59], v[40:43]\n\t" \
  "v_exp_f32 v2, v2\n\t" \
  "v_mfma_f32_16x16x32_bf16 v[40:43], a[236:239], v[60:63], v[40:43]\n\t" \
  "ds_read_b64_tr_b16 a[154:155], v211 offset:4352\n\t" \
  "v_mfma_f32_16x16x32_bf16 v[44:47], a[252:255], v[60:63], v[180:183]\n\t" \
  "v_exp_f32 v3, v3\n\t" \
  "v_mfma_f32_16x16x32_bf16 v[44:47], a[248:251], v[56:59], v[44:47]\n\t" \
  "ds_read_b64_tr_b16 a[156:157], v211 offset:8448\n\t" \
  "v_mfma_f32_16x16x32_bf16 v[44:47], a[244:247], v[52:55], v[44:47]\n\t" \
  "buffer_load_dwordx4 v241, s[24:27], s96 offen offset:1024 lds\n\t" \
  "v_mfma_f32_16x16x32_bf16 v[44:47], a[240:243], v[48:51], v[44:47]\n\t" \
  "ds_read_b64_tr_b16 a[158:159], v211 offset:12544\n\t" \
  "v_mfma_f32_16x16x32_bf16 v[64:67], a[192:195], v[80:83], v[184:187]\n\t" \
  "ds_read_b64_tr_b16 a[160:161], v210 offset:512\n\t" \
  "v_mfma_f32_16x16x32_bf16 v[64:67], a[196:199], v[84:87], v[64:67]\n\t" \
  "v_exp_f32 v4, v4\n\t" \
  "v_mfma_f32_16x16x32_bf16 v[64:67], a[200:203], v[88:91], v[64:67]\n\t" \
  "ds_read_b64_tr_b16 a[162:163], v210 offset:4608\n\t" \
  "v_mfma_f32_16x16x32_bf16 v[64:67], a[204:207], v[92:95], v[64:67]\n\t" \
  "ds_read_b64_tr_b16 a[164:165], v210 offset:8704\n\t" \
  "v_mfma_f32_16x16x32_bf16 v[68:71], a[220:223], v[92:95], v[184:187]\n\t" \
  "v_exp_f32 v5, v5\n\t" \
  "v_mfma_f32_16x16x32_bf16 v[68:71], a[216:219], v[88:91], v[68:71]\n\t" \
  "ds_read_b64_tr_b16 a[166:167], v210 offset:12800\n\t" \
  "v_mfma_f32_16x16x32_bf16 v[68:71], a[212:215], v[84:87], v[68:71]\n\t" \
  "v_exp_f32 v6, v6\n\t" \
  "v_mfma_f32_16x16x32_bf16 v[68:71], a[208:211], v[80:83], v[68:71]\n\t" \
  "ds_read_b64_tr_b16 a[168:169], v211 offset:512\n\t" \
  "v_mfma_f32_16x16x32_bf16 v[72:75], a[224:227], v[80:83], v[184:187]\n\t" \
  "buffer_load_dwordx4 v248, s[24:27], s96 offen offset:2048 lds\n\t" \
  "v_mfma_f32_16x16x32_bf16 v[72:75], a[228:231], v[84:87], v[72:75]\n\t" \
  "ds_read_b64_tr_b16 a[170:171], v211 offset:4608\n\t" \
  "v_mfma_f32_16x16x32_bf16 v[72:75], a[232:235], v[88:91], v[72:75]\n\t" \
  "ds_read_b64_tr_b16 a[172:173], v211 offset:8704\n\t" \
  "v_mfma_f32_16x16x32_bf16 v[72:75], a[236:239], v[92:95], v[72:75]\n\t" \
  "v_exp_f32 v7, v7\n\t" \
  "v_mfma_f32_16x16x32_bf16 v[76:79], a[252:255], v[92:95], v[184:187]\n\t" \
  "ds_read_b64_tr_b16 a[174:175], v211 offset:12800\n\t" \
  "v_mfma_f32_16x16x32_bf16 v[76:79], a[248:251], v[88:91], v[76:79]\n\t" \
  "v_exp_f32 v8, v8\n\t" \
  "v_mfma_f32_16x16x32_bf16 v[76:79], a[244:247], v[84:87], v[76:79]\n\t" \
  "ds_read_b64_tr_b16 a[176:177], v210 offset:768\n\t" \
  "v_mfma_f32_16x16x32_bf16 v[76:79], a[240:243], v[80:83], v[76:79]\n\t" \
  "ds_read_b64_tr_b16 a[178:179], v210 offset:4864\n\t" \
  "v_mfma_f32_16x16x32_bf16 v[96:99], a[192:195], v[112:115], v[188:191]\n\t" \
  "v_exp_f32 v9, v9\n\t" \
  "v_mfma_f32_16x16x32_bf16 v[96:99], a[196:199], v[116:119], v[96:99]\n\t" \
  "ds_read_b64_tr_b16 a[180:181], v210 offset:8960\n\t" \
  "v_mfma_f32_16x16x32_bf16 v[96:99], a[200:203], v[120:123], v[96:99]\n\t" \
  "buffer_load_dwordx4 v249, s[24:27], s96 offen offset:3072 lds\n\t" \
  "v_mfma_f32_16x16x32_bf16 v[96:99], a[204:207], v[124:127], v[96:99]\n\t" \
  "ds_read_b64_tr_b16 a[182:183], v210 offset:13056\n\t" \
  "v_mfma_f32_16x16x32_bf16 v[100:103], a[220:223], v[124:127], v[188:191]\n\t" \
  "v_exp_f32 v10, v10\n\t" \
  "v_mfma_f32_16x16x32_bf16 v[100:103], a[216:219], v[120:123], v[100:103]\n\t" \
  "ds_read_b64_tr_b16 a[184:185], v211 offset:768\n\t" \
  "v_mfma_f32_16x16x32_bf16 v[100:103], a[212:215], v[116:119], v[100:103]\n\t" \
  "ds_read_b64_tr_b16 a[186:187], v211 offset:4864\n\t" \
  "v_mfma_f32_16x16x32_bf16 v[100:103], a[208:211], v[112:115], v[100:103]\n\t" \
  "v_exp_f32 v11, v11\n\t" \
  "v_mfma_f32_16x16x32_bf16 v[104:107], a[224:227], v[112:115], v[188:191]\n\t" \
  "ds_read_b64_tr_b16 a[188:189], v211 offset:8960\n\t" \
  "v_mfma_f32_16x16x32_bf16 v[104:107], a[228:231], v[116:119], v[104:107]\n\t" \
  "v_exp_f32 v12, v12\n\t" \
  "v_mfma_f32_16x16x32_bf16 v[104:107], a[232:235], v[120:123], v[104:107]\n\t" \
  "ds_read_b64_tr_b16 a[190:191], v211 offset:13056\n\t" \
  "v_mfma_f32_16x16x32_bf16 v[104:107], a[236:239], v[124:127], v[104:107]\n\t" \
  "v_exp_f32 v13, v13\n\t" \
  "v_mfma_f32_16x16x32_bf16 v[108:111], a[252:255], v[124:127], v[188:191]\n\t" \
  "v_exp_f32 v14, v14\n\t" \
  "v_mfma_f32_16x16x32_bf16 v[108:111], a[248:251], v[120:123], v[108:111]\n\t" \
  "v_exp_f32 v15, v15\n\t" \
  "v_mfma_f32_16x16x32_bf16 v[108:111], a[244:247], v[116:119], v[108:111]\n\t" \
  "s_waitcnt vmcnt(12) lgkmcnt(0)\n\t" \
  "v_exp_f32 v32, v32\n\t" \
  "v_mfma_f32_16x16x32_bf16 v[108:111], a[240:243], v[112:115], v[108:111]\n\t" \
  "s_barrier\n\t" \
  STA("s_memrealtime s[94:95]\n\t") \
  STA("s_memtime s[92:93]\n\t") \
  "v_mfma_f32_16x16x32_bf16 a[0:3], a[128:131], v[128:131], a[0:3]\n\t" \
  "v_exp_f32 v33, v33\n\t" \
  "v_mfma_f32_16x16x32_bf16 a[0:3], a[132:135], v[132:135], a[0:3]\n\t" \
  "v_exp_f32 v34, v34\n\t" \
  "v_mfma_f32_16x16x32_bf16 a[4:7], a[136:139], v[128:131], a[4:7]\n\t" \
  "v_exp_f32 v35, v35\n\t" \
  "v_mfma_f32_16x16x32_bf16 a[4:7], a[140:143], v[132:135], a[4:7]\n\t" \
  "v_exp_f32 v36, v36\n\t" \
  "v_mfma_f32_16x16x32_bf16 a[8:11], a[144:147], v[128:131], a[8:11]\n\t" \
  "v_exp_f32 v37, v37\n\t" \
  "v_mfma_f32_16x16x32_bf16 a[8:11], a[148:151], v[132:135], a[8:11]\n\t" \
  "v_exp_f32 v38, v38\n\t" \
  "v_mfma_f32_16x16x32_bf16 a[12:15], a[152:155], v[128:131], a[12:15]\n\t" \
  "v_exp_f32 v39, v39\n\t" \
  "v_mfma_f32_16x16x32_bf16 a[12:15], a[156:159], v[132:135], a[12:15]\n\t" \
  "v_exp_f32 v40, v40\n\t" \
  "v_mfma_f32_16x16x32_bf16 a[16:19], a[160:163], v[128:131], a[16:19]\n\t" \
  "v_exp_f32 v41, v41\n\t" \
  "v_mfma_f32_16x16x32_bf16 a[16:19], a[164:167], v[132:135], a[16:19]\n\t" \
  "v_exp_f32 v42, v42\n\t" \
  "v_mfma_f32_16x16x32_bf16 a[20:23], a[168:171], v[128:131], a[20:23]\n\t" \
  "v_exp_f32 v43, v43\n\t" \
  "v_mfma_f32_16x16x32_bf16 a[20:23], a[172:175], v[132:135], a[20:23]\n\t" \
  "v_exp_f32 v44, v44\n\t" \
  "v_mfma_f32_16x16x32_bf16 a[24:27], a[176:179], v[128:131], a[24:27]\n\t" \
  STA("v_writelane_b32 v255, s94, 48\n\t") \
  STA("v_writelane_b32 v255, s92, 49\n\t") \
  "v_exp_f32 v45, v45\n\t" \
  "v_mfma_f32_16x16x32_bf16 a[24:27], a[180:183], v[132:135], a[24:27]\n\t" \
  "v_exp_f32 v46, v46\n\t" \
  "v_mfma_f32_16x16x32_bf16 a[28:31], a[184:187], v[128:131], a[28:31]\n\t" \
  "v_exp_f32 v47, v47\n\t" \
  "v_mfma_f32_16x16x32_bf16 a[28:31], a[188:191], v[132:135], a[28:31]\n\t" \
  "v_exp_f32 v64, v64\n\t" \
  "v_mfma_f32_16x16x32_bf16 v[160:163], v[216:219], v[128:131], v[160:163]\n\t" \
  "s_add_u32 m0, s38, 147456\n\t" \
  "v_exp_f32 v65, v65\n\t" \
  "v_mfma_f32_16x16x32_bf16 v[160:163], v[216:219], v[132:135], v[160:163]\n\t" \
  "s_add_u32 s96, s38, 114688\n\t" \
  "ds_read_b128 a[192:195], v220 offset:16384\n\t" \
  "v_cvt_pk_bf16_f32 v128, v0, v1\n\t" \
  "v_mfma_f32_16x16x32_bf16 a[32:35], a[128:131], v[136:139], a[32:35]\n\t" \
  "v_exp_f32 v66, v66\n\t" \
  "v_mfma_f32_16x16x32_bf16 a[32:35], a[132:135], v[140:143], a[32:35]\n\t" \
  "ds_read_b128 a[196:199], v221 offset:16384\n\t" \
  "v_cvt_pk_bf16_f32 v129, v2, v3\n\t" \
  "v_mfma_f32_16x16x32_bf16 a[36:39], a[136:139], v[136:139], a[36:39]\n\t" \
  "buffer_load_dwordx4 v230, s[64:67], s96 offen offset:0 lds\n\t" \
  "v_mfma_f32_16x16x32_bf16 a[36:39], a[140:143], v[140:143], a[36:39]\n\t" \
  "v_exp_f32 v67, v67\n\t" \
  "v_mfma_f32_16x16x32_bf16 a[40:43], a[144:147], v[136:139], a[40:43]\n\t" \
  "ds_read_b128 a[200:203], v222 offset:16384\n\t" \
  "v_cvt_pk_bf16_f32 v130, v4, v5\n\t" \
  "v_mfma_f32_16x16x32_bf16 a[40:43], a[148:151], v[140:143], a[40:43]\n\t" \
  "v_exp_f32 v68, v68\n\t" \
  "v_mfma_f32_16x16x32_bf16 a[44:47], a[152:155], v[136:139], a[44:47]\n\t" \
  "buffer_load_dwordx4 v231, s[64:67], s96 offen offset:1024 lds\n\t" \
  "v_mfma_f32_16x16x32_bf16 a[44:47], a[156:159], v[140:143], a[44:47]\n\t" \
  "ds_read_b128 a[204:207], v223 offset:16384\n\t" \
  "v_cvt_pk_bf16_f32 v131, v6, v7\n\t" \
  "v_mfma_f32_16x16x32_bf16 a[48:51], a[160:163], v[136:139], a[48:51]\n\t" \
  "v_exp_f32 v69, v69\n\t" \
  "v_mfma_f32_16x16x32_bf16 a[48:51], a[164:167], v[140:143], a[48:51]\n\t" \
  "ds_read_b128 a[208:211], v220 offset:20480\n\t" \
  "v_cvt_pk_bf16_f32 v132, v8, v9\n\t" \
  "v_mfma_f32_16x16x32_bf16 a[52:55], a[168:171], v[136:139], a[52:55]\n\t" \
  "buffer_load_dwordx4 v230, s[64:67], s96 offen offset:2048 lds\n\t" \
  "v_mfma_f32_16x16x32_bf16 a[52:55], a[172:175], v[140:143], a[52:55]\n\t" \
  "v_exp_f32 v70, v70\n\t" \
  "v_mfma_f32_16x16x32_bf16 a[56:59], a[176:179], v[136:139], a[56:59]\n\t" \
  "ds_read_b128 a[212:215], v221 offset:20480\n\t" \
  "v_cvt_pk_bf16_f32 v133, v10, v11\n\t" \
  "v_mfma_f32_16x16x32_bf16 a[56:59], a[180:183], v[140:143], a[56:59]\n\t" \
  ".Lhs9:\n\ts_cbranch_execz .Lhs9n\n\t" \
  "v_exp_f32 v71, v71\n\t" \
  "v_mfma_f32_16x16x32_bf16 a[60:63], a[184:187], v[136:139], a[60:63]\n\t" \
  "buffer_load_dwordx4 v231, s[64:67], s96 offen offset:3072 lds\n\t" \
  "v_mfma_f32_16x16x32_bf16 a[60:63], a[188:191], v[140:143], a[60:63]\n\t" \
  "s_add_u32 s96, s40, 0\n\t" \
  "v_exp_f32 v72, v72\n\t" \
  "v_mfma_f32_16x16x32_bf16 v[164:167], v[216:219], v[136:139], v[164:167]\n\t" \
  "s_and_b32 s96, s96, 0x1c0000\n\t" \
  "s_add_u32 s96, s96, s77\n\t" \
  "s_add_u32 m0, s38, 98304\n\t" \
  "v_mfma_f32_16x16x32_bf16 v[164:167], v[216:219], v[140:143], v[164:167]\n\t" \
  "ds_read_b128 a[216:219], v222 offset:20480\n\t" \
  "v_cvt_pk_bf16_f32 v134, v12, v13\n\t" \
  "v_mfma_f32_16x16x32_bf16 a[64:67], a[128:131], v[144:147], a[64:67]\n\t" \
  "buffer_load_dwordx4 v240, s[24:27], s96 offen offset:0 lds\n\t" \
  "v_mfma_f32_16x16x32_bf16 a[64:67], a[132:135], v[148:151], a[64:67]\n\t" \
  "ds_read_b128 a[220:223], v223 offset:20480\n\t" \
  "v_cvt_pk_bf16_f32 v135, v14, v15\n\t" \
  "v_mfma_f32_16x16x32_bf16 a[68:71], a[136:139], v[144:147], a[68:71]\n\t" \
  "v_exp_f32 v73, v73\n\t" \
  "v_mfma_f32_16x16x32_bf16 a[68:71], a[140:143], v[148:151], a[68:71]\n\t" \
  "v_exp_f32 v74, v74\n\t" \
  "v_mfma_f32_16x16x32_bf16 a[72:75], a[144:147], v[144:147], a[72:75]\n\t" \
  "buffer_load_dwordx4 v241, s[24:27], s96 offen offset:1024 lds\n\t" \
  "v_mfma_f32_16x16x32_bf16 a[72:75], a[148:151], v[148:151], a[72:75]\n\t" \
  "s_waitcnt vmcnt(12)\n\t" \
  "v_and_b32 v251, 0xbfffbfff, v209\n\t" \
  "v_cmp_ne_u32_e64 s[84:85], s36, v251\n\t" \
  "v_mfma_f32_16x16x32_bf16 a[76:79], a[152:155], v[144:147], a[76:79]\n\t" \
  "s_cmp_lg_u64 s[84:85], 0\n\t" \
  "s_cbranch_scc1 .Lat_v\n\t" \
  ".Lat_w:\n\t" \
  "v_mfma_f32_16x16x32_bf16 a[76:79], a[156:159], v[148:151], a[76:79]\n\t" \
  "v_and_b32 v251, 1, v233\n\t" \
  "v_lshlrev_b32 v251, 4, v251\n\t" \
  "v_mfma_f32_16x16x32_bf16 a[80:83], a[160:163], v[144:147], a[80:83]\n\t" \
  "buffer_load_dwordx4 v248, s[24:27], s96 offen offset:2048 lds\n\t" \
  "v_mfma_f32_16x16x32_bf16 a[80:83], a[164:167], v[148:151], a[80:83]\n\t" \
  "v_lshrrev_b32 v251, v251, v209\n\t" \
  "v_and_b32 v251, 0x4000, v251\n\t" \
  "v_mfma_f32_16x16x32_bf16 a[84:87], a[168:171], v[144:147], a[84:87]\n\t" \
  "ds_read_b128 a[224:227], v220 offset:24576\n\t" \
  "v_cmp_ne_u32_e64 s[84:85], 0, v251\n\t" \
  STS("s_nop 4\n\t") \
  STS("v_writelane_b32 v255, s84, 60\n\t") \
  STS("v_writelane_b32 v255, s85, 61\n\t") \
  "v_mfma_f32_16x16x32_bf16 a[84:87], a[172:175], v[148:151], a[84:87]\n\t" \
  "s_bcnt1_i32_b32 s98, s84\n\t" \
  "ds_read_b128 a[228:231], v221 offset:24576\n\t" \
  "s_bcnt1_i32_b32 s54, s85\n\t" \
  "v_mfma_f32_16x16x32_bf16 a[88:91], a[176:179], v[144:147], a[88:91]\n\t" \
  "buffer_load_dwordx4 v249, s[24:27], s96 offen offset:3072 lds\n\t" \
  "v_mfma_f32_16x16x32_bf16 a[88:91], a[180:183], v[148:151], a[88:91]\n\t" \
  "s_cmp_gt_u32 s98, s54\n\t" \
  "s_cselect_b32 s55, 0x2000, 0\n\t" \
  "s_cselect_b32 s98, s84, s85\n\t" \
  "v_mfma_f32_16x16x32_bf16 a[92:95], a[184:187], v[144:147], a[92:95]\n\t" \
  "s_add_u32 s96, s40, 262144\n\t" \
  "s_and_b32 s96, s96, 0x1c0000\n\t" \
  "s_add_u32 s96, s96, s51\n\t" \
  "v_mfma_f32_16x16x32_bf16 a[92:95], a[188:191], v[148:151], a[92:95]\n\t" \
  "s_add_u32 m0, s38, 49152\n\t" \
  "s_cmp_lg_u32 s55, 0\n\t" \
  "s_cselect_b32 s84, s85, s84\n\t" \
  "v_mfma_f32_16x16x32_bf16 v[168:171], v[216:219], v[144:147], v[168:171]\n\t" \
  "s_lshr_b32 s85, s98, 16\n\t" \
  "s_bcnt1_i32_b32 s85, s85\n\t" \
  "s_lshr_b32 s84, s84, 16\n\t" \
  "v_mfma_f32_16x16x32_bf16 v[168:171], v[216:219], v[148:151], v[168:171]\n\t" \
  "buffer_load_dwordx4 v240, s[24:27], s96 offen offset:0 lds\n\t" \
  "v_mfma_f32_16x16x32_bf16 a[96:99], a[128:131], v[152:155], a[96:99]\n\t" \
  "s_bcnt1_i32_b32 s84, s84\n\t" \
  "ds_read_b128 a[232:235], v222 offset:24576\n\t" \
  "s_add_u32 s84, s84, 7\n\t" \
  "v_mfma_f32_16x16x32_bf16 a[96:99], a[132:135], v[156:159], a[96:99]\n\t" \
  "s_cmp_ge_u32 s85, s84\n\t" \
  "s_cselect_b32 s54, -1, 0\n\t" \
  "s_xor_b32 s98, s42, s55\n\t" \
  "v_mfma_f32_16x16x32_bf16 a[100:103], a[136:139], v[152:155], a[100:103]\n\t" \
  "s_and_b32 s98, s98, s54\n\t" \
  "s_xor_b32 s45, s45, s98\n\t" \
  "s_xor_b32 s46, s46, s98\n\t" \
  STS("v_writelane_b32 v255, s54, 57\n\t") \
  STS("v_writelane_b32 v255, s55, 59\n\t") \
  "v_mfma_f32_16x16x32_bf16 a[100:103], a[140:143], v[156:159], a[100:103]\n\t" \
  "buffer_load_dwordx4 v241, s[24:27], s96 offen offset:1024 lds\n\t" \
  "v_mfma_f32_16x16x32_bf16 a[104:107], a[144:147], v[152:155], a[104:107]\n\t" \
  "s_xor_b32 s98, s43, s55\n\t" \
  "s_and_b32 s98, s98, s54\n\t" \
  "s_xor_b32 s47, s47, s98\n\t" \
  "v_mfma_f32_16x16x32_bf16 a[104:107], a[148:151], v[156:159], a[104:107]\n\t" \
  "s_xor_b32 s48, s48, s55\n\t" \
  "ds_read_b128 a[236:239], v223 offset:24576\n\t" \
  "s_xor_b32 s49, s49, s55\n\t" \
  "v_mfma_f32_16x16x32_bf16 a[108:111], a[152:155], v[152:155], a[108:111]\n\t" \
  "buffer_load_dwordx4 v248, s[24:27], s96 offen offset:2048 lds\n\t" \
  "v_mfma_f32_16x16x32_bf16 a[108:111], a[156:159], v[156:159], a[108:111]\n\t" \
  "s_xor_b32 s98, s45, 0\n\t" \
  "ds_read_b128 a[240:243], v220 offset:28672\n\t" \
  "v_exp_f32 v75, v75\n\t" \
  "v_mfma_f32_16x16x32_bf16 a[112:115], a[160:163], v[152:155], a[112:115]\n\t" \
  "s_lshl_b32 s64, s39, 17\n\t" \
  "ds_read_b128 a[244:247], v221 offset:28672\n\t" \
  "v_exp_f32 v76, v76\n\t" \
  "v_mfma_f32_16x16x32_bf16 a[112:115], a[164:167], v[156:159], a[112:115]\n\t" \
  "s_bfe_u32 s70, s46, 0x20014\n\t" \
  "ds_read_b128 a[248:251], v222 offset:28672\n\t" \
  "v_exp_f32 v77, v77\n\t" \
  "v_mfma_f32_16x16x32_bf16 a[116:119], a[168:171], v[152:155], a[116:119]\n\t" \
  "buffer_load_dwordx4 v249, s[24:27], s96 offen offset:3072 lds\n\t" \
  "v_mfma_f32_16x16x32_bf16 a[116:119], a[172:175], v[156:159], a[116:119]\n\t" \
  "s_lshl_b32 s70, s70, 14\n\t" \
  "ds_read_b128 a[252:255], v223 offset:28672\n\t" \
  "v_exp_f32 v78, v78\n\t" \
  "v_mfma_f32_16x16x32_bf16 a[120:123], a[176:179], v[152:155], a[120:123]\n\t" \
  "s_xor_b32 s67, s70, 49152\n\t" \
  "v_cvt_pk_bf16_f32 v136, v32, v33\n\t" \
  "v_exp_f32 v79, v79\n\t" \
  "v_mfma_f32_16x16x32_bf16 a[120:123], a[180:183], v[156:159], a[120:123]\n\t" \
  "s_lshl1_add_u32 s60, s67, s46\n\t" \
  "v_exp_f32 v96, v96\n\t" \
  "v_mfma_f32_16x16x32_bf16 a[124:127], a[184:187], v[152:155], a[124:127]\n\t" \
  "s_lshl1_add_u32 s99, s38, 4096\n\t" \
  "v_exp_f32 v97, v97\n\t" \
  "v_mfma_f32_16x16x32_bf16 a[124:127], a[188:191], v[156:159], a[124:127]\n\t" \
  "s_xor_b32 s68, s70, 32768\n\t" \
  "v_exp_f32 v98, v98\n\t" \
  "v_mfma_f32_16x16x32_bf16 v[172:175], v[216:219], v[152:155], v[172:175]\n\t" \
  "buffer_load_dword v246, v212, s[28:31], s99 offen\n\t" \
  "v_mfma_f32_16x16x32_bf16 v[172:175], v[216:219], v[156:159], v[172:175]\n\t" \
  "s_waitcnt lgkmcnt(12)\n\t" \
  "v_exp_f32 v99, v99\n\t" \
  STA("s_memrealtime s[94:95]\n\t") \
  STA("s_memtime s[92:93]\n\t") \
  "v_mfma_f32_16x16x32_bf16 v[0:3], a[192:195], v[16:19], v[176:179]\n\t" \
  "s_lshl1_add_u32 s61, s68, s46\n\t" \
  "ds_read_b64_tr_b16 a[128:129], v228 offset:0\n\t" \
  "v_cvt_pk_bf16_f32 v137, v34, v35\n\t" \
  "v_mfma_f32_16x16x32_bf16 v[0:3], a[196:199], v[20:23], v[0:3]\n\t" \
  "s_xor_b32 s69, s70, 16384\n\t" \
  "ds_read_b64_tr_b16 a[130:131], v228 offset:4096\n\t" \
  "v_cvt_pk_bf16_f32 v138, v36, v37\n\t" \
  "v_mfma_f32_16x16x32_bf16 v[0:3], a[200:203], v[24:27], v[0:3]\n\t" \
  "s_lshl1_add_u32 s62, s69, s46\n\t" \
  "ds_read_b64_tr_b16 a[132:133], v228 offset:8192\n\t" \
  "v_exp_f32 v100, v100\n\t" \
  "v_mfma_f32_16x16x32_bf16 v[0:3], a[204:207], v[28:31], v[0:3]\n\t" \
  "s_waitcnt lgkmcnt(11)\n\t" \
  "ds_read_b64_tr_b16 a[134:135], v228 offset:12288\n\t" \
  "v_cvt_pk_bf16_f32 v139, v38, v39\n\t" \
  "v_mfma_f32_16x16x32_bf16 v[4:7], a[220:223], v[28:31], v[176:179]\n\t" \
  "s_lshl1_add_u32 s63, s70, s46\n\t" \
  "ds_read_b64_tr_b16 a[136:137], v229 offset:0\n\t" \
  "v_cvt_pk_bf16_f32 v140, v40, v41\n\t" \
  "v_mfma_f32_16x16x32_bf16 v[4:7], a[216:219], v[24:27], v[4:7]\n\t" \
  "s_mul_i32 s65, s39, 508\n\t" \
  "ds_read_b64_tr_b16 a[138:139], v229 offset:4096\n\t" \
  "v_exp_f32 v101, v101\n\t" \
  "v_mfma_f32_16x16x32_bf16 v[4:7], a[212:215], v[20:23], v[4:7]\n\t" \
  "s_add_u32 s65, s65, s49\n\t" \
  "ds_read_b64_tr_b16 a[140:141], v229 offset:8192\n\t" \
  "v_cvt_pk_bf16_f32 v141, v42, v43\n\t" \
  "v_mfma_f32_16x16x32_bf16 v[4:7], a[208:211], v[16:19], v[4:7]\n\t" \
  "s_waitcnt lgkmcnt(11)\n\t" \
  "ds_read_b64_tr_b16 a[142:143], v229 offset:12288\n\t" \
  "v_cvt_pk_bf16_f32 v142, v44, v45\n\t" \
  "v_mfma_f32_16x16x32_bf16 v[8:11], a[224:227], v[16:19], v[176:179]\n\t" \
  "s_lshl_b32 s80, s39, 2\n\t" \
  "v_exp_f32 v102, v102\n\t" \
  "v_mfma_f32_16x16x32_bf16 v[8:11], a[228:231], v[20:23], v[8:11]\n\t" \
  "s_add_u32 s80, s80, s3\n\t" \
  "v_exp_f32 v103, v103\n\t" \
  "v_mfma_f32_16x16x32_bf16 v[8:11], a[232:235], v[24:27], v[8:11]\n\t" \
  "s_lshl1_add_u32 s99, s38, 6144\n\t" \
  "v_exp_f32 v104, v104\n\t" \
  "v_mfma_f32_16x16x32_bf16 v[8:11], a[236:239], v[28:31], v[8:11]\n\t" \
  "s_waitcnt lgkmcnt(8)\n\t" \
  "v_exp_f32 v105, v105\n\t" \
  "v_mfma_f32_16x16x32_bf16 v[12:15], a[252:255], v[28:31], v[176:179]\n\t" \
  STA("v_writelane_b32 v255, s94, 50\n\t") \
  STA("v_writelane_b32 v255, s92, 51\n\t") \
  "buffer_load_dword v246, v212, s[28:31], s99 offen\n\t" \
  "v_mfma_f32_16x16x32_bf16 v[12:15], a[248:251], v[24:27], v[12:15]\n\t" \
  "s_barrier\n\t" \
  "v_mfma_f32_16x16x32_bf16 v[12:15], a[244:247], v[20:23], v[12:15]\n\t" \
  "s_lshl_b32 s80, s80, 6\n\t" \
  "ds_read_b64_tr_b16 a[144:145], v228 offset:256\n\t" \
  "v_cvt_pk_bf16_f32 v143, v46, v47\n\t" \
  "v_mfma_f32_16x16x32_bf16 v[12:15], a[240:243], v[16:19], v[12:15]\n\t" \
  "s_add_u32 s76, s38, 147456\n\t" \
  "ds_read_b64_tr_b16 a[146:147], v228 offset:4352\n\t" \
  "v_cvt_pk_bf16_f32 v144, v64, v65\n\t" \
  "v_mfma_f32_16x16x32_bf16 v[32:35], a[192:195], v[48:51], v[180:183]\n\t" \
  "s_lshl_b32 s75, s80, 2\n\t" \
  "v_exp_f32 v106, v106\n\t" \
  "v_mfma_f32_16x16x32_bf16 v[32:35], a[196:199], v[52:55], v[32:35]\n\t" \
  "s_add_u32 s75, s75, 2228224\n\t" \
  "ds_read_b64_tr_b16 a[148:149], v228 offset:8448\n\t" \
  "v_cvt_pk_bf16_f32 v145, v66, v67\n\t" \
  "v_mfma_f32_16x16x32_bf16 v[32:35], a[200:203], v[56:59], v[32:35]\n\t" \
  "s_add_u32 s96, s40, 262144\n\t" \
  "s_and_b32 s96, s96, 0x1c0000\n\t" \
  "s_add_u32 s96, s96, s77\n\t" \
  "v_mfma_f32_16x16x32_bf16 v[32:35], a[204:207], v[60:63], v[32:35]\n\t" \
  "s_add_u32 m0, s38, 65536\n\t" \
  "ds_read_b64_tr_b16 a[150:151], v228 offset:12544\n\t" \
  "v_cvt_pk_bf16_f32 v146, v68, v69\n\t" \
  "v_mfma_f32_16x16x32_bf16 v[36:39], a[220:223], v[60:63], v[180:183]\n\t" \
  "buffer_load_dwordx4 v240, s[24:27], s96 offen offset:0 lds\n\t" \
  "v_mfma_f32_16x16x32_bf16 v[36:39], a[216:219], v[56:59], v[36:39]\n\t" \
  "s_lshr_b32 s71, s67, 7\n\t" \
  "ds_read_b64_tr_b16 a[152:153], v229 offset:256\n\t" \
  "v_exp_f32 v107, v107\n\t" \
  "v_mfma_f32_16x16x32_bf16 v[36:39], a[212:215], v[52:55], v[36:39]\n\t" \
  "s_add_u32 s71, s71, s48\n\t" \
  "ds_read_b64_tr_b16 a[154:155], v229 offset:4352\n\t" \
  "v_exp_f32 v108, v108\n\t" \
  "v_mfma_f32_16x16x32_bf16 v[36:39], a[208:211], v[48:51], v[36:39]\n\t" \
  "s_lshr_b32 s72, s68, 7\n\t" \
  "ds_read_b64_tr_b16 a[156:157], v229 offset:8448\n\t" \
  "v_cvt_pk_bf16_f32 v147, v70, v71\n\t" \
  "v_mfma_f32_16x16x32_bf16 v[40:43], a[224:227], v[48:51], v[180:183]\n\t" \
  "s_add_u32 s72, s72, s48\n\t" \
  "v_cvt_pk_bf16_f32 v148, v72, v73\n\t" \
  "v_exp_f32 v109, v109\n\t" \
  "v_mfma_f32_16x16x32_bf16 v[40:43], a[228:231], v[52:55], v[40:43]\n\t" \
  "s_lshr_b32 s66, s69, 7\n\t" \
  "v_exp_f32 v110, v110\n\t" \
  "v_mfma_f32_16x16x32_bf16 v[40:43], a[232:235], v[56:59], v[40:43]\n\t" \
  "s_add_u32 s66, s66, s48\n\t" \
  "ds_read_b64_tr_b16 a[158:159], v229 offset:12544\n\t" \
  "v_cvt_pk_bf16_f32 v149, v74, v75\n\t" \
  "v_mfma_f32_16x16x32_bf16 v[40:43], a[236:239], v[60:63], v[40:43]\n\t" \
  "s_lshr_b32 s73, s70, 7\n\t" \
  "ds_read_b64_tr_b16 a[160:161], v228 offset:512\n\t" \
  "v_cvt_pk_bf16_f32 v150, v76, v77\n\t" \
  "v_mfma_f32_16x16x32_bf16 v[44:47], a[252:255], v[60:63], v[180:183]\n\t" \
  "s_add_u32 s73, s73, s48\n\t" \
  "v_exp_f32 v111, v111\n\t" \
  "v_mfma_f32_16x16x32_bf16 v[44:47], a[248:251], v[56:59], v[44:47]\n\t" \
  "s_lshl3_add_u32 s67, s67, s47\n\t" \
  "v_exp_f32 v0, v0\n\t" \
  "v_mfma_f32_16x16x32_bf16 v[44:47], a[244:247], v[52:55], v[44:47]\n\t" \
  "buffer_load_dwordx4 v241, s[24:27], s96 offen offset:1024 lds\n\t" \
  "v_mfma_f32_16x16x32_bf16 v[44:47], a[240:243], v[48:51], v[44:47]\n\t" \
  "s_lshl3_add_u32 s68, s68, s47\n\t" \
  "ds_read_b64_tr_b16 a[162:163], v228 offset:4608\n\t" \
  "v_cvt_pk_bf16_f32 v151, v78, v79\n\t" \
  "v_mfma_f32_16x16x32_bf16 v[64:67], a[192:195], v[80:83], v[184:187]\n\t" \
  "s_add_u32 s68, s68, 4096\n\t" \
  "ds_read_b64_tr_b16 a[164:165], v228 offset:8704\n\t" \
  "v_cvt_pk_bf16_f32 v152, v96, v97\n\t" \
  "v_mfma_f32_16x16x32_bf16 v[64:67], a[196:199], v[84:87], v[64:67]\n\t" \
  "s_lshl3_add_u32 s69, s69, s47\n\t" \
  "v_cvt_pk_bf16_f32 v153, v98, v99\n\t" \
  "v_exp_f32 v1, v1\n\t" \
  "v_mfma_f32_16x16x32_bf16 v[64:67], a[200:203], v[88:91], v[64:67]\n\t" \
  "s_add_u32 s69, s69, 16384\n\t" \
  "ds_read_b64_tr_b16 a[166:167], v228 offset:12800\n\t" \
  "v_cvt_pk_bf16_f32 v154, v100, v101\n\t" \
  "v_mfma_f32_16x16x32_bf16 v[64:67], a[204:207], v[92:95], v[64:67]\n\t" \
  "s_lshl3_add_u32 s70, s70, s47\n\t" \
  "ds_read_b64_tr_b16 a[168:169], v229 offset:512\n\t" \
  "v_cvt_pk_bf16_f32 v155, v102, v103\n\t" \
  "v_mfma_f32_16x16x32_bf16 v[68:71], a[220:223], v[92:95], v[184:187]\n\t" \
  "v_exp_f32 v2, v2\n\t" \
  "v_mfma_f32_16x16x32_bf16 v[68:71], a[216:219], v[88:91], v[68:71]\n\t" \
  "v_exp_f32 v3, v3\n\t" \
  "v_mfma_f32_16x16x32_bf16 v[68:71], a[212:215], v[84:87], v[68:71]\n\t" \
  "ds_read_b64_tr_b16 a[170:171], v229 offset:4608\n\t" \
  "v_cvt_pk_bf16_f32 v156, v104, v105\n\t" \
  "v_mfma_f32_16x16x32_bf16 v[68:71], a[208:211], v[80:83], v[68:71]\n\t" \
  "v_exp_f32 v4, v4\n\t" \
  "v_mfma_f32_16x16x32_bf16 v[72:75], a[224:227], v[80:83], v[184:187]\n\t" \
  "buffer_load_dwordx4 v248, s[24:27], s96 offen offset:2048 lds\n\t" \
  "v_mfma_f32_16x16x32_bf16 v[72:75], a[228:231], v[84:87], v[72:75]\n\t" \
  "ds_read_b64_tr_b16 a[172:173], v229 offset:8704\n\t" \
  "v_cvt_pk_bf16_f32 v157, v106, v107\n\t" \
  "v_mfma_f32_16x16x32_bf16 v[72:75], a[232:235], v[88:91], v[72:75]\n\t" \
  "ds_read_b64_tr_b16 a[174:175], v229 offset:12800\n\t" \
  "v_cvt_pk_bf16_f32 v158, v108, v109\n\t" \
  "v_mfma_f32_16x16x32_bf16 v[72:75], a[236:239], v[92:95], v[72:75]\n\t" \
  "ds_read_b64_tr_b16 a[176:177], v228 offset:768\n\t" \
  "v_exp_f32 v5, v5\n\t" \
  "v_mfma_f32_16x16x32_bf16 v[76:79], a[252:255], v[92:95], v[184:187]\n\t" \
  "v_exp_f32 v6, v6\n\t" \
  "v_mfma_f32_16x16x32_bf16 v[76:79], a[248:251], v[88:91], v[76:79]\n\t" \
  "ds_read_b64_tr_b16 a[178:179], v228 offset:4864\n\t" \
  "v_cvt_pk_bf16_f32 v159, v110, v111\n\t" \
  "v_mfma_f32_16x16x32_bf16 v[76:79], a[244:247], v[84:87], v[76:79]\n\t" \
  "v_exp_f32 v7, v7\n\t" \
  "v_mfma_f32_16x16x32_bf16 v[76:79], a[240:243], v[80:83], v[76:79]\n\t" \
  "v_exp_f32 v8, v8\n\t" \
  "v_mfma_f32_16x16x32_bf16 v[96:99], a[192:195], v[112:115], v[188:191]\n\t" \
  "v_exp_f32 v9, v9\n\t" \
  "v_mfma_f32_16x16x32_bf16 v[96:99], a[196:199], v[116:119], v[96:99]\n\t" \
  "ds_read_b64_tr_b16 a[180:181], v228 offset:8960\n\t" \
  "v_cvt_pk_bf16_f32 v0, v0, v1\n\t" \
  "v_mfma_f32_16x16x32_bf16 v[96:99], a[200:203], v[120:123], v[96:99]\n\t" \
  "buffer_load_dwordx4 v249, s[24:27], s96 offen offset:3072 lds\n\t" \
  "v_mfma_f32_16x16x32_bf16 v[96:99], a[204:207], v[124:127], v[96:99]\n\t" \
  "ds_read_b64_tr_b16 a[182:183], v228 offset:13056\n\t" \
  "v_cvt_pk_bf16_f32 v1, v2, v3\n\t" \
  "v_mfma_f32_16x16x32_bf16 v[100:103], a[220:223], v[124:127], v[188:191]\n\t" \
  "v_exp_f32 v10, v10\n\t" \
  "v_mfma_f32_16x16x32_bf16 v[100:103], a[216:219], v[120:123], v[100:103]\n\t" \
  "ds_read_b64_tr_b16 a[184:185], v229 offset:768\n\t" \
  "v_cvt_pk_bf16_f32 v2, v4, v5\n\t" \
  "v_mfma_f32_16x16x32_bf16 v[100:103], a[212:215], v[116:119], v[100:103]\n\t" \
  "v_exp_f32 v11, v11\n\t" \
  "v_mfma_f32_16x16x32_bf16 v[100:103], a[208:211], v[112:115], v[100:103]\n\t" \
  "ds_read_b64_tr_b16 a[186:187], v229 offset:4864\n\t" \
  "v_cvt_pk_bf16_f32 v3, v6, v7\n\t" \
  "v_mfma_f32_16x16x32_bf16 v[104:107], a[224:227], v[112:115], v[188:191]\n\t" \
  "ds_read_b64_tr_b16 a[188:189], v229 offset:8960\n\t" \
  "v_cvt_pk_bf16_f32 v8, v8, v9\n\t" \
  "v_mfma_f32_16x16x32_bf16 v[104:107], a[228:231], v[116:119], v[104:107]\n\t" \
  "ds_read_b64_tr_b16 a[190:191], v229 offset:13056\n\t" \
  "v_exp_f32 v12, v12\n\t" \
  "v_mfma_f32_16x16x32_bf16 v[104:107], a[232:235], v[120:123], v[104:107]\n\t" \
  "s_add_u32 s96, s40, 524288\n\t" \
  "v_exp_f32 v13, v13\n\t" \
  "v_mfma_f32_16x16x32_bf16 v[104:107], a[236:239], v[124:127], v[104:107]\n\t" \
  "s_and_b32 s96, s96, 0x1c0000\n\t" \
  "v_exp_f32 v14, v14\n\t" \
  "v_mfma_f32_16x16x32_bf16 v[108:111], a[252:255], v[124:127], v[188:191]\n\t" \
  "s_add_u32 s96, s96, s77\n\t" \
  "v_exp_f32 v15, v15\n\t" \
  "v_mfma_f32_16x16x32_bf16 v[108:111], a[248:251], v[120:123], v[108:111]\n\t" \
  "s_add_u32 m0, s38, 81920\n\t" \
  "v_exp_f32 v32, v32\n\t" \
  "v_mfma_f32_16x16x32_bf16 v[108:111], a[244:247], v[116:119], v[108:111]\n\t" \
  "s_waitcnt vmcnt(6) lgkmcnt(0)\n\t" \
  "v_exp_f32 v33, v33\n\t" \
  "v_mfma_f32_16x16x32_bf16 v[108:111], a[240:243], v[112:115], v[108:111]\n\t" \
  "s_barrier\n\t" \
  STS("s_memrealtime s[94:95]\n\t") \
  STS("s_memtime s[92:93]\n\t") \
  "v_mfma_f32_16x16x32_bf16 a[0:3], a[128:131], v[128:131], a[0:3]\n\t" \
  "buffer_load_dwordx4 v240, s[24:27], s96 offen offset:0 lds\n\t" \
  "ds_read_b64_tr_b16 v[16:17], v210 offset:0\n\t" \
  "ds_read_b128 v[176:179], v215 offset:16384\n\t" \
  "ds_read_b128 v[180:183], v215 offset:17408\n\t" \
  "v_mfma_f32_16x16x32_bf16 a[0:3], a[132:135], v[132:135], a[0:3]\n\t" \
  "ds_read_b64_tr_b16 v[18:19], v210 offset:4096\n\t" \
  "ds_read_b64_tr_b16 v[20:21], v210 offset:8192\n\t" \
  "ds_read_b64_tr_b16 v[22:23], v210 offset:12288\n\t" \
  "ds_read_b64_tr_b16 v[24:25], v211 offset:0\n\t" \
  "v_mfma_f32_16x16x32_bf16 a[4:7], a[136:139], v[128:131], a[4:7]\n\t" \
  "ds_read_b64_tr_b16 v[26:27], v211 offset:4096\n\t" \
  "ds_read_b64_tr_b16 v[28:29], v211 offset:8192\n\t" \
  "ds_read_b64_tr_b16 v[30:31], v211 offset:12288\n\t" \
  "ds_read_b128 v[184:187], v215 offset:18432\n\t" \
  "v_mfma_f32_16x16x32_bf16 a[4:7], a[140:143], v[132:135], a[4:7]\n\t" \
  "buffer_load_dwordx4 v241, s[24:27], s96 offen offset:1024 lds\n\t" \
  "v_mfma_f32_16x16x32_bf16 a[8:11], a[144:147], v[128:131], a[8:11]\n\t" \
  "ds_read_b128 v[188:191], v215 offset:19456\n\t" \
  "ds_read_b64_tr_b16 v[48:49], v210 offset:256\n\t" \
  "v_cvt_pk_bf16_f32 v9, v10, v11\n\t" \
  "v_mfma_f32_16x16x32_bf16 a[8:11], a[148:151], v[132:135], a[8:11]\n\t" \
  "ds_read_b64_tr_b16 v[50:51], v210 offset:4352\n\t" \
  "v_cvt_pk_bf16_f32 v10, v12, v13\n\t" \
  "v_mfma_f32_16x16x32_bf16 a[12:15], a[152:155], v[128:131], a[12:15]\n\t" \
  "buffer_load_dwordx4 v248, s[24:27], s96 offen offset:2048 lds\n\t" \
  "v_mfma_f32_16x16x32_bf16 a[12:15], a[156:159], v[132:135], a[12:15]\n\t" \
  "v_exp_f32 v34, v34\n\t" \
  "v_mfma_f32_16x16x32_bf16 a[16:19], a[160:163], v[128:131], a[16:19]\n\t" \
  "ds_read_b64_tr_b16 v[52:53], v210 offset:8448\n\t" \
  "v_cvt_pk_bf16_f32 v11, v14, v15\n\t" \
  "v_mfma_f32_16x16x32_bf16 a[16:19], a[164:167], v[132:135], a[16:19]\n\t" \
  "buffer_load_dwordx4 v249, s[24:27], s96 offen offset:3072 lds\n\t" \
  "v_mfma_f32_16x16x32_bf16 a[20:23], a[168:171], v[128:131], a[20:23]\n\t" \
  "s_add_u32 s96, s40, 786432\n\t" \
  "ds_read_b64_tr_b16 v[54:55], v210 offset:12544\n\t" \
  "v_cvt_pk_bf16_f32 v32, v32, v33\n\t" \
  "v_mfma_f32_16x16x32_bf16 a[20:23], a[172:175], v[132:135], a[20:23]\n\t" \
  "s_and_b32 s96, s96, 0x1c0000\n\t" \
  "v_exp_f32 v35, v35\n\t" \
  "v_mfma_f32_16x16x32_bf16 a[24:27], a[176:179], v[128:131], a[24:27]\n\t" \
  STS("v_writelane_b32 v255, s94, 20\n\t") \
  STS("v_writelane_b32 v255, s92, 21\n\t") \
  "s_add_u32 s96, s96, s51\n\t" \
  "v_cvt_pk_bf16_f32 v33, v34, v35\n\t" \
  "s_add_u32 m0, s38, 114688\n\t" \
  "v_mfma_f32_16x16x32_bf16 a[24:27], a[180:183], v[132:135], a[24:27]\n\t" \
  "v_exp_f32 v36, v36\n\t" \
  "v_mfma_f32_16x16x32_bf16 a[28:31], a[184:187], v[128:131], a[28:31]\n\t" \
  "s_waitcnt lgkmcnt(4)\n\t" \
  "buffer_load_dwordx4 v240, s[24:27], s96 offen offset:0 lds\n\t" \
  "v_mfma_f32_16x16x32_bf16 a[28:31], a[188:191], v[132:135], a[28:31]\n\t" \
  "ds_read_b64_tr_b16 v[56:57], v211 offset:256\n\t" \
  "v_add_u32 v230, 0x10000, v242\n\t" \
  "v_mfma_f32_16x16x32_bf16 v[160:163], v[216:219], v[128:131], v[160:163]\n\t" \
  "ds_read_b64_tr_b16 v[58:59], v211 offset:4352\n\t" \
  "v_add_u32 v232, 0x20000, v242\n\t" \
  "v_mfma_f32_16x16x32_bf16 v[160:163], v[216:219], v[132:135], v[160:163]\n\t" \
  "buffer_load_dwordx4 v241, s[24:27], s96 offen offset:1024 lds\n\t" \
  "v_mfma_f32_16x16x32_bf16 a[32:35], a[128:131], v[136:139], a[32:35]\n\t" \
  "v_exp_f32 v37, v37\n\t" \
  "v_mfma_f32_16x16x32_bf16 a[32:35], a[132:135], v[140:143], a[32:35]\n\t" \
  "ds_read_b64_tr_b16 v[60:61], v211 offset:8448\n\t" \
  "v_cvt_pk_bf16_f32 v34, v36, v37\n\t" \
  "v_mfma_f32_16x16x32_bf16 a[36:39], a[136:139], v[136:139], a[36:39]\n\t" \
  "buffer_load_dwordx4 v248, s[24:27], s96 offen offset:2048 lds\n\t" \
  "v_mfma_f32_16x16x32_bf16 a[36:39], a[140:143], v[140:143], a[36:39]\n\t" \
  "v_exp_f32 v38, v38\n\t" \
  "v_mfma_f32_16x16x32_bf16 a[40:43], a[144:147], v[136:139], a[40:43]\n\t" \
  "ds_read_b64_tr_b16 v[62:63], v211 offset:12544\n\t" \
  "v_mfma_f32_16x16x32_bf16 a[40:43], a[148:151], v[140:143], a[40:43]\n\t" \
  "buffer_load_dwordx4 v249, s[24:27], s96 offen offset:3072 lds\n\t" \
  "v_mfma_f32_16x16x32_bf16 a[44:47], a[152:155], v[136:139], a[44:47]\n\t" \
  "ds_read_b64_tr_b16 v[80:81], v210 offset:512\n\t" \
  "v_mfma_f32_16x16x32_bf16 a[44:47], a[156:159], v[140:143], a[44:47]\n\t" \
  "ds_read_b64_tr_b16 a[192:193], v234 offset:0\n\t" \
  "ds_read_b64_tr_b16 v[82:83], v210 offset:4608\n\t" \
  "v_exp_f32 v39, v39\n\t" \
  "v_mfma_f32_16x16x32_bf16 a[48:51], a[160:163], v[136:139], a[48:51]\n\t" \
  "ds_read_b64_tr_b16 v[84:85], v210 offset:8704\n\t" \
  "v_exp_f32 v40, v40\n\t" \
  "v_mfma_f32_16x16x32_bf16 a[48:51], a[164:167], v[140:143], a[48:51]\n\t" \
  "ds_read_b64_tr_b16 a[194:195], v234 offset:4096\n\t" \
  "ds_read_b64_tr_b16 v[86:87], v210 offset:12800\n\t" \
  "v_cvt_pk_bf16_f32 v35, v38, v39\n\t" \
  "v_mfma_f32_16x16x32_bf16 a[52:55], a[168:171], v[136:139], a[52:55]\n\t" \
  "v_exp_f32 v41, v41\n\t" \
  "v_mfma_f32_16x16x32_bf16 a[52:55], a[172:175], v[140:143], a[52:55]\n\t" \
  "ds_read_b64_tr_b16 v[88:89], v211 offset:512\n\t" \
  "v_cvt_pk_bf16_f32 v40, v40, v41\n\t" \
  "v_mfma_f32_16x16x32_bf16 a[56:59], a[176:179], v[136:139], a[56:59]\n\t" \
  "v_exp_f32 v42, v42\n\t" \
  "v_mfma_f32_16x16x32_bf16 a[56:59], a[180:183], v[140:143], a[56:59]\n\t" \
  "ds_read_b64_tr_b16 v[90:91], v211 offset:4608\n\t" \
  "v_mfma_f32_16x16x32_bf16 a[60:63], a[184:187], v[136:139], a[60:63]\n\t" \
  "ds_read_b64_tr_b16 v[92:93], v211 offset:8704\n\t" \
  "v_mfma_f32_16x16x32_bf16 a[60:63], a[188:191], v[140:143], a[60:63]\n\t" \
  "v_exp_f32 v43, v43\n\t" \
  "v_mfma_f32_16x16x32_bf16 v[164:167], v[216:219], v[136:139], v[164:167]\n\t" \
  "ds_read_b64_tr_b16 a[196:197], v235 offset:0\n\t" \
  "ds_read_b64_tr_b16 v[94:95], v211 offset:12800\n\t" \
  "v_cvt_pk_bf16_f32 v41, v42, v43\n\t" \
  "v_mfma_f32_16x16x32_bf16 v[164:167], v[216:219], v[140:143], v[164:167]\n\t" \
  "ds_read_b64_tr_b16 a[198:199], v235 offset:4096\n\t" \
  "ds_read_b64_tr_b16 v[112:113], v210 offset:768\n\t" \
  "v_exp_f32 v44, v44\n\t" \
  "v_mfma_f32_16x16x32_bf16 a[0:3], v[16:19], v[0:3], a[0:3]\n\t" \
  "ds_read_b64_tr_b16 v[114:115], v210 offset:4864\n\t" \
  "v_mfma_f32_16x16x32_bf16 a[0:3], v[20:23], v[8:11], a[0:3]\n\t" \
  "v_exp_f32 v45, v45\n\t" \
  "v_mfma_f32_16x16x32_bf16 v[160:163], v[216:219], v[0:3], v[160:163]\n\t" \
  "ds_read_b64_tr_b16 v[116:117], v210 offset:8960\n\t" \
  "v_cvt_pk_bf16_f32 v42, v44, v45\n\t" \
  "v_mfma_f32_16x16x32_bf16 v[160:163], v[216:219], v[8:11], v[160:163]\n\t" \
  "ds_read_b64_tr_b16 v[118:119], v210 offset:13056\n\t" \
  "v_mfma_f32_16x16x32_bf16 a[4:7], v[24:27], v[0:3], a[4:7]\n\t" \
  "v_exp_f32 v46, v46\n\t" \
  "v_mfma_f32_16x16x32_bf16 a[4:7], v[28:31], v[8:11], a[4:7]\n\t" \
  "s_waitcnt lgkmcnt(15)\n\t" \
  "ds_read_b64_tr_b16 v[120:121], v211 offset:768\n\t" \
  "v_mfma_f32_16x16x32_bf16 a[8:11], v[48:51], v[0:3], a[8:11]\n\t" \
  "ds_read_b64_tr_b16 v[122:123], v211 offset:4864\n\t" \
  "v_exp_f32 v47, v47\n\t" \
  "v_mfma_f32_16x16x32_bf16 a[8:11], v[52:55], v[8:11], a[8:11]\n\t" \
  "v_rcp_f32 v224, v160\n\t" \
  "v_mfma_f32_16x16x32_bf16 a[12:15], v[56:59], v[0:3], a[12:15]\n\t" \
  "ds_read_b64_tr_b16 v[124:125], v211 offset:8960\n\t" \
  "v_accvgpr_read_b32 v128, a0\n\t" \
  "v_accvgpr_read_b32 v129, a1\n\t" \
  "v_accvgpr_read_b32 v130, a2\n\t" \
  "v_accvgpr_read_b32 v131, a3\n\t" \
  "v_accvgpr_read_b32 v132, a4\n\t" \
  "v_accvgpr_read_b32 v133, a5\n\t" \
  "v_accvgpr_read_b32 v134, a6\n\t" \
  "v_accvgpr_read_b32 v135, a7\n\t" \
  "v_pk_mul_f32 v[128:129], v[128:129], v[224:225] op_sel_hi:[1,0]\n\t" \
  "v_pk_mul_f32 v[130:131], v[130:131], v[224:225] op_sel_hi:[1,0]\n\t" \
  "v_pk_mul_f32 v[132:133], v[132:133], v[224:225] op_sel_hi:[1,0]\n\t" \
  "v_pk_mul_f32 v[134:135], v[134:135], v[224:225] op_sel_hi:[1,0]\n\t" \
  "v_cvt_pk_bf16_f32 v128, v128, v129\n\t" \
  "v_cvt_pk_bf16_f32 v129, v130, v131\n\t" \
  "v_cvt_pk_bf16_f32 v130, v132, v133\n\t" \
  "v_cvt_pk_bf16_f32 v131, v134, v135\n\t" \
  "buffer_store_dwordx4 v[128:131], v242, s[16:19], s98 offen offset:0 sc1\n\t" \
  "v_mfma_f32_16x16x32_bf16 a[12:15], v[60:63], v[8:11], a[12:15]\n\t" \
  "s_waitcnt lgkmcnt(8)\n\t" \
  "ds_read_b64_tr_b16 a[200:201], v234 offset:8192\n\t" \
  "ds_read_b64_tr_b16 v[126:127], v211 offset:13056\n\t" \
  "v_cmp_lt_f32 vcc, 0x5f800000, v160\n\t" \
  "v_mfma_f32_16x16x32_bf16 a[16:19], v[80:83], v[0:3], a[16:19]\n\t" \
  "s_cmp_lg_u64 vcc, 0\n\t" \
  "s_cselect_b32 s89, s78, s89\n\t" \
  "v_or_b32 v254, s89, v254\n\t" \
  "v_mfma_f32_16x16x32_bf16 a[16:19], v[84:87], v[8:11], a[16:19]\n\t" \
  "ds_read_b64_tr_b16 a[202:203], v234 offset:12288\n\t" \
  "v_exp_f32 v64, v64\n\t" \
  "v_mfma_f32_16x16x32_bf16 a[20:23], v[88:91], v[0:3], a[20:23]\n\t" \
  "v_accvgpr_read_b32 v136, a8\n\t" \
  "v_accvgpr_read_b32 v137, a9\n\t" \
  "v_accvgpr_read_b32 v138, a10\n\t" \
  "v_accvgpr_read_b32 v139, a11\n\t" \
  "v_accvgpr_read_b32 v140, a12\n\t" \
  "v_accvgpr_read_b32 v141, a13\n\t" \
  "v_accvgpr_read_b32 v142, a14\n\t" \
  "v_accvgpr_read_b32 v143, a15\n\t" \
  "v_pk_mul_f32 v[136:137], v[136:137], v[224:225] op_sel_hi:[1,0]\n\t" \
  "v_pk_mul_f32 v[138:139], v[138:139], v[224:225] op_sel_hi:[1,0]\n\t" \
  "v_pk_mul_f32 v[140:141], v[140:141], v[224:225] op_sel_hi:[1,0]\n\t" \
  "v_pk_mul_f32 v[142:143], v[142:143], v[224:225] op_sel_hi:[1,0]\n\t" \
  "v_cvt_pk_bf16_f32 v136, v136, v137\n\t" \
  "v_cvt_pk_bf16_f32 v137, v138, v139\n\t" \
  "v_cvt_pk_bf16_f32 v138, v140, v141\n\t" \
  "v_cvt_pk_bf16_f32 v139, v142, v143\n\t" \
  "buffer_store_dwordx4 v[136:139], v242, s[16:19], s98 offen offset:1024 sc1\n\t" \
  "v_mfma_f32_16x16x32_bf16 a[20:23], v[92:95], v[8:11], a[20:23]\n\t" \
  "s_waitcnt lgkmcnt(1)\n\t" \
  "ds_read_b64_tr_b16 a[204:205], v235 offset:8192\n\t" \
  "v_exp_f32 v65, v65\n\t" \
  "v_mfma_f32_16x16x32_bf16 a[24:27], v[112:115], v[0:3], a[24:27]\n\t" \
  "ds_read_b128 a[224:227], v213 offset:16384\n\t" \
  "v_exp_f32 v66, v66\n\t" \
  "v_mfma_f32_16x16x32_bf16 a[24:27], v[116:119], v[8:11], a[24:27]\n\t" \
  "ds_read_b64_tr_b16 a[206:207], v235 offset:12288\n\t" \
  "v_exp_f32 v67, v67\n\t" \
  "v_mfma_f32_16x16x32_bf16 a[28:31], v[120:123], v[0:3], a[28:31]\n\t" \
  "ds_read_b128 a[228:231], v213 offset:17408\n\t" \
  "v_exp_f32 v68, v68\n\t" \
  "v_mfma_f32_16x16x32_bf16 a[28:31], v[124:127], v[8:11], a[28:31]\n\t" \
  "ds_read_b128 a[232:235], v213 offset:18432\n\t" \
  "v_exp_f32 v69, v69\n\t" \
  "v_mfma_f32_16x16x32_bf16 a[64:67], a[128:131], v[144:147], a[64:67]\n\t" \
  "ds_read_b128 a[236:239], v213 offset:19456\n\t" \
  "v_exp_f32 v70, v70\n\t" \
  "v_mfma_f32_16x16x32_bf16 a[64:67], a[132:135], v[148:151], a[64:67]\n\t" \
  "ds_write_b128 v213, v[128:131] offset:16384\n\t" \
  "s_barrier\n\t" \
  "v_mfma_f32_16x16x32_bf16 a[68:71], a[136:139], v[144:147], a[68:71]\n\t" \
  "ds_write_b128 v213, v[136:139] offset:17408\n\t" \
  "v_accvgpr_read_b32 v0, a16\n\t" \
  "v_accvgpr_read_b32 v1, a17\n\t" \
  "v_accvgpr_read_b32 v2, a18\n\t" \
  "v_accvgpr_read_b32 v3, a19\n\t" \
  "v_accvgpr_read_b32 v4, a20\n\t" \
  "v_accvgpr_read_b32 v5, a21\n\t" \
  "v_accvgpr_read_b32 v6, a22\n\t" \
  "v_accvgpr_read_b32 v7, a23\n\t" \
  "v_pk_mul_f32 v[0:1], v[0:1], v[224:225] op_sel_hi:[1,0]\n\t" \
  "v_pk_mul_f32 v[2:3], v[2:3], v[224:225] op_sel_hi:[1,0]\n\t" \
  "v_pk_mul_f32 v[4:5], v[4:5], v[224:225] op_sel_hi:[1,0]\n\t" \
  "v_pk_mul_f32 v[6:7], v[6:7], v[224:225] op_sel_hi:[1,0]\n\t" \
  "v_cvt_pk_bf16_f32 v0, v0, v1\n\t" \
  "v_cvt_pk_bf16_f32 v1, v2, v3\n\t" \
  "v_cvt_pk_bf16_f32 v2, v4, v5\n\t" \
  "v_cvt_pk_bf16_f32 v3, v6, v7\n\t" \
  "buffer_store_dwordx4 v[0:3], v242, s[16:19], s98 offen offset:2048 sc1\n\t" \
  "v_mfma_f32_16x16x32_bf16 a[68:71], a[140:143], v[148:151], a[68:71]\n\t" \
  "ds_write_b128 v213, v[0:3] offset:18432\n\t" \
  "v_accvgpr_read_b32 v8, a24\n\t" \
  "v_accvgpr_read_b32 v9, a25\n\t" \
  "v_accvgpr_read_b32 v10, a26\n\t" \
  "v_accvgpr_read_b32 v11, a27\n\t" \
  "v_accvgpr_read_b32 v12, a28\n\t" \
  "v_accvgpr_read_b32 v13, a29\n\t" \
  "v_accvgpr_read_b32 v14, a30\n\t" \
  "v_accvgpr_read_b32 v15, a31\n\t" \
  "v_pk_mul_f32 v[8:9], v[8:9], v[224:225] op_sel_hi:[1,0]\n\t" \
  "v_pk_mul_f32 v[10:11], v[10:11], v[224:225] op_sel_hi:[1,0]\n\t" \
  "v_pk_mul_f32 v[12:13], v[12:13], v[224:225] op_sel_hi:[1,0]\n\t" \
  "v_pk_mul_f32 v[14:15], v[14:15], v[224:225] op_sel_hi:[1,0]\n\t" \
  "v_cvt_pk_bf16_f32 v8, v8, v9\n\t" \
  "v_cvt_pk_bf16_f32 v9, v10, v11\n\t" \
  "v_cvt_pk_bf16_f32 v10, v12, v13\n\t" \
  "v_cvt_pk_bf16_f32 v11, v14, v15\n\t" \
  "buffer_store_dwordx4 v[8:11], v242, s[16:19], s98 offen offset:3072 sc1\n\t" \
  "v_mfma_f32_16x16x32_bf16 a[72:75], a[144:147], v[144:147], a[72:75]\n\t" \
  "ds_write_b128 v213, v[8:11] offset:19456\n\t" \
  "v_mfma_f32_16x16x32_bf16 a[72:75], a[148:151], v[148:151], a[72:75]\n\t" \
  "s_xor_b32 s98, s45, 32768\n\t" \
  "ds_read_b64_tr_b16 a[208:209], v236 offset:32768\n\t" \
  "v_exp_f32 v71, v71\n\t" \
  "v_mfma_f32_16x16x32_bf16 a[76:79], a[152:155], v[144:147], a[76:79]\n\t" \
  "v_exp_f32 v72, v72\n\t" \
  "v_mfma_f32_16x16x32_bf16 a[76:79], a[156:159], v[148:151], a[76:79]\n\t" \
  "v_exp_f32 v73, v73\n\t" \
  "v_mfma_f32_16x16x32_bf16 a[80:83], a[160:163], v[144:147], a[80:83]\n\t" \
  "ds_read_b64_tr_b16 a[210:211], v236 offset:36864\n\t" \
  "v_cvt_pk_bf16_f32 v43, v46, v47\n\t" \
  "v_mfma_f32_16x16x32_bf16 a[80:83], a[164:167], v[148:151], a[80:83]\n\t" \
  "v_exp_f32 v74, v74\n\t" \
  "v_mfma_f32_16x16x32_bf16 a[84:87], a[168:171], v[144:147], a[84:87]\n\t" \
  "v_exp_f32 v75, v75\n\t" \
  "v_mfma_f32_16x16x32_bf16 a[84:87], a[172:175], v[148:151], a[84:87]\n\t" \
  "v_exp_f32 v76, v76\n\t" \
  "v_mfma_f32_16x16x32_bf16 a[88:91], a[176:179], v[144:147], a[88:91]\n\t" \
  "v_exp_f32 v77, v77\n\t" \
  "v_mfma_f32_16x16x32_bf16 a[88:91], a[180:183], v[148:151], a[88:91]\n\t" \
  "ds_read_b64_tr_b16 a[212:213], v237 offset:32768\n\t" \
  "v_cvt_pk_bf16_f32 v64, v64, v65\n\t" \
  "v_mfma_f32_16x16x32_bf16 a[92:95], a[184:187], v[144:147], a[92:95]\n\t" \
  ".Lhs10:\n\ts_cbranch_execz .Lhs10n\n\t" \
  "v_exp_f32 v78, v78\n\t" \
  "v_mfma_f32_16x16x32_bf16 a[92:95], a[188:191], v[148:151], a[92:95]\n\t" \
  "ds_read_b64_tr_b16 a[214:215], v237 offset:36864\n\t" \
  "v_cvt_pk_bf16_f32 v65, v66, v67\n\t" \
  "v_mfma_f32_16x16x32_bf16 v[168:171], v[216:219], v[144:147], v[168:171]\n\t" \
  "v_exp_f32 v79, v79\n\t" \
  "v_mfma_f32_16x16x32_bf16 v[168:171], v[216:219], v[148:151], v[168:171]\n\t" \
  "s_add_u32 s96, s40, 786432\n\t" \
  "v_exp_f32 v96, v96\n\t" \
  "v_mfma_f32_16x16x32_bf16 a[32:35], v[16:19], v[32:35], a[32:35]\n\t" \
  "s_and_b32 s96, s96, 0x1c0000\n\t" \
  "ds_read_b64_tr_b16 a[216:217], v236 offset:40960\n\t" \
  "v_cvt_pk_bf16_f32 v66, v68, v69\n\t" \
  "v_mfma_f32_16x16x32_bf16 a[32:35], v[20:23], v[40:43], a[32:35]\n\t" \
  "s_add_u32 s96, s96, s77\n\t" \
  "v_exp_f32 v97, v97\n\t" \
  "v_mfma_f32_16x16x32_bf16 v[164:167], v[216:219], v[32:35], v[164:167]\n\t" \
  "s_add_u32 m0, s38, 147456\n\t" \
  "v_exp_f32 v98, v98\n\t" \
  "v_mfma_f32_16x16x32_bf16 v[164:167], v[216:219], v[40:43], v[164:167]\n\t" \
  "ds_read_b64_tr_b16 a[218:219], v236 offset:45056\n\t" \
  "v_cvt_pk_bf16_f32 v67, v70, v71\n\t" \
  "v_mfma_f32_16x16x32_bf16 a[36:39], v[24:27], v[32:35], a[36:39]\n\t" \
  "v_exp_f32 v99, v99\n\t" \
  "v_mfma_f32_16x16x32_bf16 a[36:39], v[28:31], v[40:43], a[36:39]\n\t" \
  "v_cvt_pk_bf16_f32 v72, v72, v73\n\t" \
  "v_cvt_pk_bf16_f32 v73, v74, v75\n\t" \
  "s_nop 0\n\t" \
  "v_rcp_f32 v226, v164\n\t" \
  "v_mfma_f32_16x16x32_bf16 a[40:43], v[48:51], v[32:35], a[40:43]\n\t" \
  "buffer_load_dwordx4 v240, s[24:27], s96 offen offset:0 lds\n\t" \
  "v_accvgpr_read_b32 v0, a32\n\t" \
  "v_accvgpr_read_b32 v1, a33\n\t" \
  "v_mfma_f32_16x16x32_bf16 a[40:43], v[52:55], v[40:43], a[40:43]\n\t" \
  "v_cmp_lt_f32 vcc, 0x5f800000, v164\n\t" \
  "v_accvgpr_read_b32 v2, a34\n\t" \
  "v_accvgpr_read_b32 v3, a35\n\t" \
  "v_mfma_f32_16x16x32_bf16 a[44:47], v[56:59], v[32:35], a[44:47]\n\t" \
  "s_cmp_lg_u64 vcc, 0\n\t" \
  "s_cselect_b32 s89, s78, s89\n\t" \
  "v_or_b32 v254, s89, v254\n\t" \
  "v_mfma_f32_16x16x32_bf16 a[44:47], v[60:63], v[40:43], a[44:47]\n\t" \
  "buffer_load_dwordx4 v241, s[24:27], s96 offen offset:1024 lds\n\t" \
  "v_accvgpr_read_b32 v4, a36\n\t" \
  "v_accvgpr_read_b32 v5, a37\n\t" \
  "v_mfma_f32_16x16x32_bf16 a[48:51], v[80:83], v[32:35], a[48:51]\n\t" \
  "ds_read_b64_tr_b16 a[220:221], v237 offset:40960\n\t" \
  "v_accvgpr_read_b32 v6, a38\n\t" \
  "v_accvgpr_read_b32 v7, a39\n\t" \
  "v_mfma_f32_16x16x32_bf16 a[48:51], v[84:87], v[40:43], a[48:51]\n\t" \
  "buffer_load_dwordx4 v248, s[24:27], s96 offen offset:2048 lds\n\t" \
  "v_mfma_f32_16x16x32_bf16 a[52:55], v[88:91], v[32:35], a[52:55]\n\t" \
  "ds_read_b64_tr_b16 a[222:223], v237 offset:45056\n\t" \
  "v_mul_f32 v0, v0, v226\n\t" \
  "v_mul_f32 v1, v1, v226\n\t" \
  "v_mfma_f32_16x16x32_bf16 a[52:55], v[92:95], v[40:43], a[52:55]\n\t" \
  "v_exp_f32 v100, v100\n\t" \
  "v_mul_f32 v2, v2, v226\n\t" \
  "v_mul_f32 v3, v3, v226\n\t" \
  "v_mfma_f32_16x16x32_bf16 a[56:59], v[112:115], v[32:35], a[56:59]\n\t" \
  "buffer_load_dwordx4 v249, s[24:27], s96 offen offset:3072 lds\n\t" \
  "v_mfma_f32_16x16x32_bf16 a[56:59], v[116:119], v[40:43], a[56:59]\n\t" \
  "v_exp_f32 v101, v101\n\t" \
  "v_mul_f32 v4, v4, v226\n\t" \
  "v_mul_f32 v5, v5, v226\n\t" \
  "v_mfma_f32_16x16x32_bf16 a[60:63], v[120:123], v[32:35], a[60:63]\n\t" \
  "v_exp_f32 v102, v102\n\t" \
  "v_mul_f32 v6, v6, v226\n\t" \
  "v_mul_f32 v7, v7, v226\n\t" \
  "v_mfma_f32_16x16x32_bf16 a[60:63], v[124:127], v[40:43], a[60:63]\n\t" \
  "v_cvt_pk_bf16_f32 v32, v0, v1\n\t" \
  "v_cvt_pk_bf16_f32 v33, v2, v3\n\t" \
  "v_cvt_pk_bf16_f32 v34, v4, v5\n\t" \
  "v_cvt_pk_bf16_f32 v35, v6, v7\n\t" \
  "v_mfma_f32_16x16x32_bf16 a[96:99], a[128:131], v[152:155], a[96:99]\n\t" \
  "v_accvgpr_read_b32 v8, a40\n\t" \
  "v_accvgpr_read_b32 v9, a41\n\t" \
  "v_accvgpr_read_b32 v10, a42\n\t" \
  "v_accvgpr_read_b32 v11, a43\n\t" \
  "v_mfma_f32_16x16x32_bf16 a[96:99], a[132:135], v[156:159], a[96:99]\n\t" \
  "v_accvgpr_read_b32 v12, a44\n\t" \
  "v_accvgpr_read_b32 v13, a45\n\t" \
  "v_accvgpr_read_b32 v14, a46\n\t" \
  "v_accvgpr_read_b32 v15, a47\n\t" \
  "v_mfma_f32_16x16x32_bf16 a[100:103], a[136:139], v[152:155], a[100:103]\n\t" \
  "v_mul_f32 v8, v8, v226\n\t" \
  "v_mul_f32 v9, v9, v226\n\t" \
  "v_mul_f32 v10, v10, v226\n\t" \
  "v_mul_f32 v11, v11, v226\n\t" \
  "v_mfma_f32_16x16x32_bf16 a[100:103], a[140:143], v[156:159], a[100:103]\n\t" \
  "v_mul_f32 v12, v12, v226\n\t" \
  "v_mul_f32 v13, v13, v226\n\t" \
  "v_mul_f32 v14, v14, v226\n\t" \
  "v_mul_f32 v15, v15, v226\n\t" \
  "v_mfma_f32_16x16x32_bf16 a[104:107], a[144:147], v[152:155], a[104:107]\n\t" \
  "s_waitcnt vmcnt(8) lgkmcnt(0)\n\t" \
  "v_cvt_pk_bf16_f32 v36, v8, v9\n\t" \
  "v_cvt_pk_bf16_f32 v37, v10, v11\n\t" \
  "v_cvt_pk_bf16_f32 v38, v12, v13\n\t" \
  "v_cvt_pk_bf16_f32 v39, v14, v15\n\t" \
  "v_mfma_f32_16x16x32_bf16 a[104:107], a[148:151], v[156:159], a[104:107]\n\t" \
  "v_accvgpr_read_b32 v0, a48\n\t" \
  "v_accvgpr_read_b32 v1, a49\n\t" \
  "s_barrier\n\t" \
  "v_mfma_f32_16x16x32_bf16 a[108:111], a[152:155], v[152:155], a[108:111]\n\t" \
  "buffer_store_dwordx4 v[32:35], v242, s[16:19], s98 offen offset:0 sc1\n\t" \
  "v_accvgpr_read_b32 v2, a50\n\t" \
  "v_accvgpr_read_b32 v3, a51\n\t" \
  "v_accvgpr_read_b32 v4, a52\n\t" \
  "v_mfma_f32_16x16x32_bf16 a[108:111], a[156:159], v[156:159], a[108:111]\n\t" \
  "s_add_u32 s96, s40, 1048576\n\t" \
  "s_and_b32 s96, s96, 0x1c0000\n\t" \
  "s_add_u32 s96, s96, s51\n\t" \
  "v_accvgpr_read_b32 v5, a53\n\t" \
  "v_mfma_f32_16x16x32_bf16 a[112:115], a[160:163], v[152:155], a[112:115]\n\t" \
  "s_add_u32 m0, s38, 0\n\t" \
  "v_accvgpr_read_b32 v6, a54\n\t" \
  "v_accvgpr_read_b32 v7, a55\n\t" \
  "v_mul_f32 v0, v0, v226\n\t" \
  "v_mfma_f32_16x16x32_bf16 a[112:115], a[164:167], v[156:159], a[112:115]\n\t" \
  "buffer_load_dwordx4 v240, s[24:27], s96 offen offset:0 lds\n\t" \
  "v_mul_f32 v1, v1, v226\n\t" \
  "v_mul_f32 v2, v2, v226\n\t" \
  "v_mul_f32 v3, v3, v226\n\t" \
  "v_mfma_f32_16x16x32_bf16 a[116:119], a[168:171], v[152:155], a[116:119]\n\t" \
  "v_mul_f32 v4, v4, v226\n\t" \
  "v_mul_f32 v5, v5, v226\n\t" \
  "v_mul_f32 v6, v6, v226\n\t" \
  "v_mul_f32 v7, v7, v226\n\t" \
  "v_mfma_f32_16x16x32_bf16 a[116:119], a[172:175], v[156:159], a[116:119]\n\t" \
  "buffer_load_dwordx4 v241, s[24:27], s96 offen offset:1024 lds\n\t" \
  "v_cvt_pk_bf16_f32 v40, v0, v1\n\t" \
  "v_cvt_pk_bf16_f32 v41, v2, v3\n\t" \
  "v_cvt_pk_bf16_f32 v42, v4, v5\n\t" \
  "v_mfma_f32_16x16x32_bf16 a[120:123], a[176:179], v[152:155], a[120:123]\n\t" \
  "v_cvt_pk_bf16_f32 v74, v76, v77\n\t" \
  "v_cvt_pk_bf16_f32 v43, v6, v7\n\t" \
  "v_accvgpr_read_b32 v8, a56\n\t" \
  "v_accvgpr_read_b32 v9, a57\n\t" \
  "v_mfma_f32_16x16x32_bf16 a[120:123], a[180:183], v[156:159], a[120:123]\n\t" \
  "v_accvgpr_read_b32 v10, a58\n\t" \
  "v_accvgpr_read_b32 v11, a59\n\t" \
  "v_accvgpr_read_b32 v12, a60\n\t" \
  "v_exp_f32 v103, v103\n\t" \
  "v_mfma_f32_16x16x32_bf16 a[124:127], a[184:187], v[152:155], a[124:127]\n\t" \
  "buffer_load_dwordx4 v248, s[24:27], s96 offen offset:2048 lds\n\t" \
  "v_accvgpr_read_b32 v13, a61\n\t" \
  "v_accvgpr_read_b32 v14, a62\n\t" \
  "v_accvgpr_read_b32 v15, a63\n\t" \
  "v_mfma_f32_16x16x32_bf16 a[124:127], a[188:191], v[156:159], a[124:127]\n\t" \
  "v_cvt_pk_bf16_f32 v75, v78, v79\n\t" \
  "v_mul_f32 v8, v8, v226\n\t" \
  "v_mul_f32 v9, v9, v226\n\t" \
  "v_mul_f32 v10, v10, v226\n\t" \
  "v_mfma_f32_16x16x32_bf16 v[172:175], v[216:219], v[152:155], v[172:175]\n\t" \
  "v_cvt_pk_bf16_f32 v96, v96, v97\n\t" \
  "v_exp_f32 v104, v104\n\t" \
  "v_mul_f32 v11, v11, v226\n\t" \
  "v_mul_f32 v12, v12, v226\n\t" \
  "v_mfma_f32_16x16x32_bf16 v[172:175], v[216:219], v[156:159], v[172:175]\n\t" \
  "buffer_store_dwordx4 v[36:39], v242, s[16:19], s98 offen offset:1024 sc1\n\t" \
  "v_exp_f32 v105, v105\n\t" \
  "v_mul_f32 v13, v13, v226\n\t" \
  "v_exp_f32 v106, v106\n\t" \
  "v_mfma_f32_16x16x32_bf16 a[64:67], v[16:19], v[64:67], a[64:67]\n\t" \
  "v_cvt_pk_bf16_f32 v97, v98, v99\n\t" \
  "v_exp_f32 v107, v107\n\t" \
  "v_mfma_f32_16x16x32_bf16 a[64:67], v[20:23], v[72:75], a[64:67]\n\t" \
  "v_cvt_pk_bf16_f32 v98, v100, v101\n\t" \
  "v_mul_f32 v14, v14, v226\n\t" \
  "v_mfma_f32_16x16x32_bf16 v[168:171], v[216:219], v[64:67], v[168:171]\n\t" \
  "v_mul_f32 v15, v15, v226\n\t" \
  "v_exp_f32 v108, v108\n\t" \
  "v_mfma_f32_16x16x32_bf16 v[168:171], v[216:219], v[72:75], v[168:171]\n\t" \
  "v_cvt_pk_bf16_f32 v99, v102, v103\n\t" \
  "v_cvt_pk_bf16_f32 v44, v8, v9\n\t" \
  "v_mfma_f32_16x16x32_bf16 a[68:71], v[24:27], v[64:67], a[68:71]\n\t" \
  "v_cvt_pk_bf16_f32 v104, v104, v105\n\t" \
  "v_cvt_pk_bf16_f32 v45, v10, v11\n\t" \
  "v_mfma_f32_16x16x32_bf16 a[68:71], v[28:31], v[72:75], a[68:71]\n\t" \
  "v_cvt_pk_bf16_f32 v105, v106, v107\n\t" \
  "v_exp_f32 v109, v109\n\t" \
  "v_mfma_f32_16x16x32_bf16 a[72:75], v[48:51], v[64:67], a[72:75]\n\t" \
  "v_rcp_f32 v220, v168\n\t" \
  "v_mfma_f32_16x16x32_bf16 a[72:75], v[52:55], v[72:75], a[72:75]\n\t" \
  "v_cmp_lt_f32 vcc, 0x5f800000, v168\n\t" \
  "v_exp_f32 v110, v110\n\t" \
  "v_mfma_f32_16x16x32_bf16 a[76:79], v[56:59], v[64:67], a[76:79]\n\t" \
  "s_cmp_lg_u64 vcc, 0\n\t" \
  "s_cselect_b32 s89, s78, s89\n\t" \
  "v_or_b32 v254, s89, v254\n\t" \
  "v_mfma_f32_16x16x32_bf16 a[76:79], v[60:63], v[72:75], a[76:79]\n\t" \
  "buffer_load_dwordx4 v249, s[24:27], s96 offen offset:3072 lds\n\t" \
  "v_mfma_f32_16x16x32_bf16 a[80:83], v[80:83], v[64:67], a[80:83]\n\t" \
  "ds_read_b64_tr_b16 a[128:129], v234 offset:49152\n\t" \
  "v_cvt_pk_bf16_f32 v46, v12, v13\n\t" \
  "v_mfma_f32_16x16x32_bf16 a[80:83], v[84:87], v[72:75], a[80:83]\n\t" \
  "v_cvt_pk_bf16_f32 v106, v108, v109\n\t" \
  "v_exp_f32 v111, v111\n\t" \
  "v_mfma_f32_16x16x32_bf16 a[84:87], v[88:91], v[64:67], a[84:87]\n\t" \
  "ds_read_b64_tr_b16 a[130:131], v234 offset:53248\n\t" \
  "v_cvt_pk_bf16_f32 v47, v14, v15\n\t" \
  "v_mfma_f32_16x16x32_bf16 a[84:87], v[92:95], v[72:75], a[84:87]\n\t" \
  "ds_read_b64_tr_b16 a[132:133], v235 offset:49152\n\t" \
  "v_cvt_pk_bf16_f32 v107, v110, v111\n\t" \
  "v_mfma_f32_16x16x32_bf16 a[88:91], v[112:115], v[64:67], a[88:91]\n\t" \
  "ds_read_b128 a[240:243], v213 offset:32768\n\t" \
  "v_mfma_f32_16x16x32_bf16 a[88:91], v[116:119], v[72:75], a[88:91]\n\t" \
  "s_waitcnt vmcnt(10)\n\t" \
  "buffer_store_dword v243, v254, s[16:19], s49 offen offset:0 sc1\n\t" \
  "ds_read_b64_tr_b16 a[134:135], v235 offset:53248\n\t" \
  "v_mfma_f32_16x16x32_bf16 a[92:95], v[120:123], v[64:67], a[92:95]\n\t" \
  "ds_read_b128 a[244:247], v213 offset:33792\n\t" \
  "v_mfma_f32_16x16x32_bf16 a[92:95], v[124:127], v[72:75], a[92:95]\n\t" \
  "buffer_store_dwordx4 v[40:43], v242, s[16:19], s98 offen offset:2048 sc1\n\t" \
  "v_accvgpr_read_b32 v64, a64\n\t" \
  "v_accvgpr_read_b32 v65, a65\n\t" \
  "v_accvgpr_read_b32 v66, a66\n\t" \
  "v_mfma_f32_16x16x32_bf16 a[96:99], v[16:19], v[96:99], a[96:99]\n\t" \
  "v_accvgpr_read_b32 v67, a67\n\t" \
  "v_accvgpr_read_b32 v68, a68\n\t" \
  "v_mfma_f32_16x16x32_bf16 a[96:99], v[20:23], v[104:107], a[96:99]\n\t" \
  "s_barrier\n\t" \
  "v_mfma_f32_16x16x32_bf16 v[172:175], v[216:219], v[96:99], v[172:175]\n\t" \
  "ds_read_b128 v[128:131], v242 offset:16384\n\t" \
  "v_accvgpr_read_b32 v69, a69\n\t" \
  "v_mfma_f32_16x16x32_bf16 v[172:175], v[216:219], v[104:107], v[172:175]\n\t" \
  "v_accvgpr_read_b32 v70, a70\n\t" \
  "v_accvgpr_read_b32 v71, a71\n\t" \
  "v_mfma_f32_16x16x32_bf16 a[100:103], v[24:27], v[96:99], a[100:103]\n\t" \
  "buffer_store_dwordx4 v[44:47], v242, s[16:19], s98 offen offset:3072 sc1\n\t" \
  "v_mul_f32 v64, v64, v220\n\t" \
  "ds_read_b128 v[132:135], v242 offset:17408\n\t" \
  "v_mul_f32 v65, v65, v220\n\t" \
  "v_mfma_f32_16x16x32_bf16 a[100:103], v[28:31], v[104:107], a[100:103]\n\t" \
  "s_xor_b32 s98, s45, 65536\n\t" \
  "v_rcp_f32 v222, v172\n\t" \
  "v_mfma_f32_16x16x32_bf16 a[104:107], v[48:51], v[96:99], a[104:107]\n\t" \
  "ds_read_b128 v[136:139], v242 offset:18432\n\t" \
  "v_cmp_lt_f32 vcc, 0x5f800000, v172\n\t" \
  "v_mfma_f32_16x16x32_bf16 a[104:107], v[52:55], v[104:107], a[104:107]\n\t" \
  "s_cmp_lg_u64 vcc, 0\n\t" \
  "s_cselect_b32 s89, s78, s89\n\t" \
  "v_or_b32 v254, s89, v254\n\t" \
  "v_mfma_f32_16x16x32_bf16 a[108:111], v[56:59], v[96:99], a[108:111]\n\t" \
  "s_cmp_lg_u32 s89, 0\n\t" \
  "s_cbranch_scc1 .Lat_x\n\t" \
  ".Lat_y:\n\t" \
  "v_mfma_f32_16x16x32_bf16 a[108:111], v[60:63], v[104:107], a[108:111]\n\t" \
  "ds_read_b128 v[140:143], v242 offset:19456\n\t" \
  "v_mul_f32 v66, v66, v220\n\t" \
  "v_mfma_f32_16x16x32_bf16 a[112:115], v[80:83], v[96:99], a[112:115]\n\t" \
  "ds_read_b128 v[144:147], v242 offset:20480\n\t" \
  "v_mul_f32 v67, v67, v220\n\t" \
  "v_mfma_f32_16x16x32_bf16 a[112:115], v[84:87], v[104:107], a[112:115]\n\t" \
  "ds_read_b64_tr_b16 a[136:137], v234 offset:57344\n\t" \
  "v_mul_f32 v68, v68, v220\n\t" \
  "v_mfma_f32_16x16x32_bf16 a[116:119], v[88:91], v[96:99], a[116:119]\n\t" \
  "ds_read_b128 v[148:151], v242 offset:21504\n\t" \
  "v_mul_f32 v69, v69, v220\n\t" \
  "v_mfma_f32_16x16x32_bf16 a[116:119], v[92:95], v[104:107], a[116:119]\n\t" \
  "ds_read_b64_tr_b16 a[138:139], v234 offset:61440\n\t" \
  "v_mul_f32 v70, v70, v220\n\t" \
  "v_mfma_f32_16x16x32_bf16 a[120:123], v[112:115], v[96:99], a[120:123]\n\t" \
  "ds_read_b128 v[152:155], v242 offset:22528\n\t" \
  "v_mul_f32 v71, v71, v220\n\t" \
  "v_mfma_f32_16x16x32_bf16 a[120:123], v[116:119], v[104:107], a[120:123]\n\t" \
  "ds_read_b128 v[156:159], v242 offset:23552\n\t" \
  "v_mfma_f32_16x16x32_bf16 a[124:127], v[120:123], v[96:99], a[124:127]\n\t" \
  "ds_read_b64_tr_b16 a[140:141], v235 offset:57344\n\t" \
  "v_mfma_f32_16x16x32_bf16 a[124:127], v[124:127], v[104:107], a[124:127]\n\t" \
  "s_waitcnt lgkmcnt(7)\n\t" \
  "ds_read_b64_tr_b16 a[142:143], v235 offset:61440\n\t" \
  "ds_read_b128 a[248:251], v213 offset:34816\n\t" \
  ".Lat_join:\n\t" \
  STS("s_memrealtime s[94:95]\n\t") \
  STS("s_memtime s[92:93]\n\t") \
  "v_mfma_f32_16x16x32_bf16 v[0:3], a[192:195], v[128:131], 0\n\t" \
  "ds_read_b128 v[160:163], v242 offset:24576\n\t" \
  "v_cvt_pk_bf16_f32 v112, v64, v65\n\t" \
  "v_cvt_pk_bf16_f32 v113, v66, v67\n\t" \
  "v_cvt_pk_bf16_f32 v114, v68, v69\n\t" \
  "v_mfma_f32_16x16x32_bf16 v[0:3], a[200:203], v[132:135], v[0:3]\n\t" \
  "ds_read_b128 a[252:255], v213 offset:35840\n\t" \
  "v_cvt_pk_bf16_f32 v115, v70, v71\n\t" \
  "v_accvgpr_read_b32 v72, a72\n\t" \
  "v_accvgpr_read_b32 v73, a73\n\t" \
  "v_mfma_f32_16x16x32_bf16 v[0:3], a[208:211], v[136:139], v[0:3]\n\t" \
  "ds_read_b128 v[164:167], v242 offset:25600\n\t" \
  "v_accvgpr_read_b32 v74, a74\n\t" \
  "v_accvgpr_read_b32 v75, a75\n\t" \
  "v_accvgpr_read_b32 v76, a76\n\t" \
  "v_mfma_f32_16x16x32_bf16 v[0:3], a[216:219], v[140:143], v[0:3]\n\t" \
  "v_accvgpr_read_b32 v77, a77\n\t" \
  "v_accvgpr_read_b32 v78, a78\n\t" \
  "v_accvgpr_read_b32 v79, a79\n\t" \
  "v_mul_f32 v72, v72, v220\n\t" \
  "v_mfma_f32_16x16x32_bf16 v[16:19], a[220:223], v[140:143], 0\n\t" \
  "v_mul_f32 v73, v73, v220\n\t" \
  "ds_read_b128 v[168:171], v242 offset:26624\n\t" \
  "ds_write_b128 v213, v[32:35] offset:32768\n\t" \
  "v_mfma_f32_16x16x32_bf16 v[16:19], a[212:215], v[136:139], v[16:19]\n\t" \
  "v_mul_f32 v74, v74, v220\n\t" \
  "v_mul_f32 v75, v75, v220\n\t" \
  "v_mul_f32 v76, v76, v220\n\t" \
  "v_mul_f32 v77, v77, v220\n\t" \
  "v_mfma_f32_16x16x32_bf16 v[16:19], a[204:207], v[132:135], v[16:19]\n\t" \
  "v_mul_f32 v78, v78, v220\n\t" \
  "ds_read_b128 v[172:175], v242 offset:27648\n\t" \
  "ds_write_b128 v213, v[36:39] offset:33792\n\t" \
  "v_mfma_f32_16x16x32_bf16 v[16:19], a[196:199], v[128:131], v[16:19]\n\t" \
  "s_waitcnt lgkmcnt(10)\n\t" \
  "v_mul_f32 v79, v79, v220\n\t" \
  "v_cvt_pk_bf16_f32 v116, v72, v73\n\t" \
  "v_cvt_pk_bf16_f32 v117, v74, v75\n\t" \
  "v_cvt_pk_bf16_f32 v118, v76, v77\n\t" \
  "v_mfma_f32_16x16x32_bf16 v[4:7], a[192:195], v[144:147], 0\n\t" \
  "v_cvt_pk_bf16_f32 v119, v78, v79\n\t" \
  "ds_read_b128 v[128:131], v242 offset:28672\n\t" \
  "ds_read_b128 v[132:135], v242 offset:29696\n\t" \
  "ds_write_b128 v213, v[40:43] offset:34816\n\t" \
  "v_mfma_f32_16x16x32_bf16 v[4:7], a[200:203], v[148:151], v[4:7]\n\t" \
  "v_accvgpr_read_b32 v64, a80\n\t" \
  "ds_read_b128 v[136:139], v242 offset:30720\n\t" \
  "ds_write_b128 v213, v[44:47] offset:35840\n\t" \
  "v_mfma_f32_16x16x32_bf16 v[4:7], a[208:211], v[152:155], v[4:7]\n\t" \
  "ds_read_b64_tr_b16 a[144:145], v236 offset:0\n\t" \
  "v_accvgpr_read_b32 v65, a81\n\t" \
  "v_accvgpr_read_b32 v66, a82\n\t" \
  "v_accvgpr_read_b32 v67, a83\n\t" \
  "v_mfma_f32_16x16x32_bf16 v[4:7], a[216:219], v[156:159], v[4:7]\n\t" \
  "v_accvgpr_read_b32 v68, a84\n\t" \
  "v_accvgpr_read_b32 v69, a85\n\t" \
  "v_accvgpr_read_b32 v70, a86\n\t" \
  "v_accvgpr_read_b32 v71, a87\n\t" \
  "v_mfma_f32_16x16x32_bf16 v[20:23], a[220:223], v[156:159], 0\n\t" \
  STS("v_writelane_b32 v255, s94, 22\n\t") \
  STS("v_writelane_b32 v255, s92, 23\n\t") \
  "buffer_load_dword v246, v245, s[16:19], s65 offen sc1\n\t" \
  "v_mul_f32 v64, v64, v220\n\t" \
  "v_mul_f32 v65, v65, v220\n\t" \
  "v_mul_f32 v66, v66, v220\n\t" \
  "v_mfma_f32_16x16x32_bf16 v[20:23], a[212:215], v[152:155], v[20:23]\n\t" \
  "ds_read_b64_tr_b16 a[146:147], v236 offset:4096\n\t" \
  "v_mul_f32 v67, v67, v220\n\t" \
  "v_mul_f32 v68, v68, v220\n\t" \
  "v_mul_f32 v69, v69, v220\n\t" \
  "v_mfma_f32_16x16x32_bf16 v[20:23], a[204:207], v[148:151], v[20:23]\n\t" \
  "s_waitcnt vmcnt(10) lgkmcnt(2)\n\t" \
  "buffer_store_dwordx4 v[112:115], v242, s[16:19], s98 offen offset:0 sc1\n\t" \
  "v_mul_f32 v70, v70, v220\n\t" \
  "ds_read_b128 v[140:143], v242 offset:31744\n\t" \
  "v_mul_f32 v71, v71, v220\n\t" \
  "v_mfma_f32_16x16x32_bf16 v[20:23], a[196:199], v[144:147], v[20:23]\n\t" \
  "v_cvt_pk_bf16_f32 v120, v64, v65\n\t" \
  "v_cvt_pk_bf16_f32 v121, v66, v67\n\t" \
  "s_barrier\n\t" \
  "v_mfma_f32_16x16x32_bf16 v[8:11], a[192:195], v[160:163], 0\n\t" \
  "ds_read_b128 v[144:147], v242 offset:32768\n\t" \
  "v_cvt_pk_bf16_f32 v122, v68, v69\n\t" \
  "v_cvt_pk_bf16_f32 v123, v70, v71\n\t" \
  "v_accvgpr_read_b32 v72, a88\n\t" \
  "v_mfma_f32_16x16x32_bf16 v[8:11], a[200:203], v[164:167], v[8:11]\n\t" \
  "ds_read_b64_tr_b16 a[148:149], v237 offset:0\n\t" \
  "v_accvgpr_read_b32 v73, a89\n\t" \
  "v_accvgpr_read_b32 v74, a90\n\t" \
  "v_accvgpr_read_b32 v75, a91\n\t" \
  "v_mfma_f32_16x16x32_bf16 v[8:11], a[208:211], v[168:171], v[8:11]\n\t" \
  "buffer_store_dwordx4 v[116:119], v242, s[16:19], s98 offen offset:1024 sc1\n\t" \
  "ds_read_b32 v209, v215 offset:0\n\t" \
  "ds_read_b128 v[148:151], v242 offset:33792\n\t" \
  "v_accvgpr_read_b32 v76, a92\n\t" \
  "v_mfma_f32_16x16x32_bf16 v[8:11], a[216:219], v[172:175], v[8:11]\n\t" \
  "ds_read_b64_tr_b16 a[150:151], v237 offset:4096\n\t" \
  "v_accvgpr_read_b32 v77, a93\n\t" \
  "v_mfma_f32_16x16x32_bf16 v[24:27], a[220:223], v[172:175], 0\n\t" \
  "ds_read_b64_tr_b16 a[152:153], v236 offset:8192\n\t" \
  "ds_read_b128 v[152:155], v242 offset:34816\n\t" \
  "v_accvgpr_read_b32 v78, a94\n\t" \
  "v_mfma_f32_16x16x32_bf16 v[24:27], a[212:215], v[168:171], v[24:27]\n\t" \
  "ds_read_b64_tr_b16 a[154:155], v236 offset:12288\n\t" \
  "v_accvgpr_read_b32 v79, a95\n\t" \
  "v_mfma_f32_16x16x32_bf16 v[24:27], a[204:207], v[164:167], v[24:27]\n\t" \
  "ds_read_b64_tr_b16 a[156:157], v237 offset:8192\n\t" \
  "ds_read_b128 v[156:159], v242 offset:35840\n\t" \
  "v_mul_f32 v72, v72, v220\n\t" \
  "v_mfma_f32_16x16x32_bf16 v[24:27], a[196:199], v[160:163], v[24:27]\n\t" \
  "s_waitcnt lgkmcnt(10)\n\t" \
  "s_waitcnt lgkmcnt(7)\n\t" \
  "v_cmp_eq_u32 vcc, s36, v209\n\t" \
  "v_mfma_f32_16x16x32_bf16 v[12:15], a[192:195], v[128:131], 0\n\t" \
  "s_and_b32 vcc_lo, vcc_lo, s37\n\t" \
  "s_cbranch_scc1 .Lat_rd\n\t" \
  "v_mfma_f32_16x16x32_bf16 v[12:15], a[200:203], v[132:135], v[12:15]\n\t" \
  "ds_read_b128 v[160:163], v242 offset:36864\n\t" \
  "v_mul_f32 v73, v73, v220\n\t" \
  "v_mul_f32 v74, v74, v220\n\t" \
  "v_mfma_f32_16x16x32_bf16 v[12:15], a[208:211], v[136:139], v[12:15]\n\t" \
  "ds_write_b128 v215, v[112:115] offset:0\n\t" \
  "v_mfma_f32_16x16x32_bf16 v[12:15], a[216:219], v[140:143], v[12:15]\n\t" \
  "ds_read_b64_tr_b16 a[158:159], v237 offset:12288\n\t" \
  "ds_read_b128 v[164:167], v242 offset:37888\n\t" \
  "v_mul_f32 v75, v75, v220\n\t" \
  "v_mul_f32 v76, v76, v220\n\t" \
  "v_mfma_f32_16x16x32_bf16 v[28:31], a[220:223], v[140:143], 0\n\t" \
  "ds_write_b128 v215, v[116:119] offset:1024\n\t" \
  "v_mfma_f32_16x16x32_bf16 v[28:31], a[212:215], v[136:139], v[28:31]\n\t" \
  "ds_read_b64_tr_b16 a[160:161], v238 offset:0\n\t" \
  "ds_read_b128 v[168:171], v242 offset:38912\n\t" \
  "v_mul_f32 v77, v77, v220\n\t" \
  "v_mfma_f32_16x16x32_bf16 v[28:31], a[204:207], v[132:135], v[28:31]\n\t" \
  "ds_read_b64_tr_b16 a[162:163], v238 offset:4096\n\t" \
  "ds_read_b128 v[172:175], v242 offset:39936\n\t" \
  "v_mul_f32 v78, v78, v220\n\t" \
  "v_mfma_f32_16x16x32_bf16 v[28:31], a[196:199], v[128:131], v[28:31]\n\t" \
  "s_waitcnt lgkmcnt(9)\n\t" \
  "ds_write_b128 v215, v[120:123] offset:2048\n\t" \
  STP("s_memrealtime s[94:95]\n\t") \
  STP("s_memtime s[92:93]\n\t") \
  "v_mfma_f32_16x16x32_bf16 v[32:35], a[192:195], v[144:147], 0\n\t" \
  "ds_read_b128 v[128:131], v242 offset:40960\n\t" \
  "v_mul_f32 v79, v79, v220\n\t" \
  "v_mfma_f32_16x16x32_bf16 v[32:35], a[200:203], v[148:151], v[32:35]\n\t" \
  "ds_read_b64_tr_b16 a[164:165], v239 offset:0\n\t" \
  "v_cvt_pk_bf16_f32 v124, v72, v73\n\t" \
  "v_mfma_f32_16x16x32_bf16 v[32:35], a[208:211], v[152:155], v[32:35]\n\t" \
  "ds_read_b64_tr_b16 a[166:167], v239 offset:4096\n\t" \
  "ds_read_b128 v[132:135], v242 offset:41984\n\t" \
  "v_cvt_pk_bf16_f32 v125, v74, v75\n\t" \
  "v_mfma_f32_16x16x32_bf16 v[32:35], a[216:219], v[156:159], v[32:35]\n\t" \
  "ds_read_b64_tr_b16 a[168:169], v238 offset:8192\n\t" \
  "ds_read_b128 v[136:139], v242 offset:43008\n\t" \
  "v_cvt_pk_bf16_f32 v126, v76, v77\n\t" \
  "v_mfma_f32_16x16x32_bf16 v[48:51], a[220:223], v[156:159], 0\n\t" \
  "ds_read_b64_tr_b16 a[170:171], v238 offset:12288\n\t" \
  "v_cvt_pk_bf16_f32 v127, v78, v79\n\t" \
  "v_mfma_f32_16x16x32_bf16 v[48:51], a[212:215], v[152:155], v[48:51]\n\t" \
  "ds_write_b128 v215, v[124:127] offset:3072\n\t" \
  "v_mfma_f32_16x16x32_bf16 v[48:51], a[204:207], v[148:151], v[48:51]\n\t" \
  "s_waitcnt vmcnt(3)\n\t" \
  "buffer_store_dword v243, v254, s[16:19], s49 offen offset:32 sc1\n\t" \
  "ds_read_b128 v[140:143], v242 offset:44032\n\t" \
  "v_mfma_f32_16x16x32_bf16 v[48:51], a[196:199], v[144:147], v[48:51]\n\t" \
  "s_waitcnt lgkmcnt(10)\n\t" \
  "ds_read_b64_tr_b16 a[172:173], v239 offset:8192\n\t" \
  "s_add_u32 s97, s64, 131072\n\t" \
  "v_mfma_f32_16x16x32_bf16 v[36:39], a[192:195], v[160:163], 0\n\t" \
  "s_and_b32 s97, s97, 0xe0000\n\t" \
  "ds_read_b128 v[144:147], v242 offset:45056\n\t" \
  "s_add_u32 s97, s97, s60\n\t" \
  "v_mfma_f32_16x16x32_bf16 v[36:39], a[200:203], v[164:167], v[36:39]\n\t" \
  "s_add_u32 m0, s38, 49152\n\t" \
  "s_waitcnt vmcnt(3)\n\t" \
  "v_cmp_ne_u32 vcc, s36, v246\n\t" \
  "v_mfma_f32_16x16x32_bf16 v[36:39], a[208:211], v[168:171], v[36:39]\n\t" \
  "s_and_b32 vcc_lo, vcc_lo, 0xe\n\t" \
  "s_cbranch_scc1 .Lat_s0\n\t" \
  ".Lat_k0:\n\t" \
  "v_mfma_f32_16x16x32_bf16 v[36:39], a[216:219], v[172:175], v[36:39]\n\t" \
  "ds_read_b64_tr_b16 a[174:175], v239 offset:12288\n\t" \
  "ds_read_b128 v[148:151], v242 offset:46080\n\t" \
  "v_mfma_f32_16x16x32_bf16 v[52:55], a[220:223], v[172:175], 0\n\t" \
  STP("v_writelane_b32 v255, s94, 24\n\t") \
  STP("v_writelane_b32 v255, s92, 25\n\t") \
  "buffer_load_dwordx4 v242, s[16:19], s97 offen offset:0 sc1 lds\n\t" \
  "v_mfma_f32_16x16x32_bf16 v[52:55], a[212:215], v[168:171], v[52:55]\n\t" \
  "ds_read_b64_tr_b16 a[176:177], v236 offset:16384\n\t" \
  "ds_read_b128 v[152:155], v242 offset:47104\n\t" \
  "v_mfma_f32_16x16x32_bf16 v[52:55], a[204:207], v[164:167], v[52:55]\n\t" \
  "s_waitcnt lgkmcnt(6)\n\t" \
  "ds_read_b64_tr_b16 a[178:179], v236 offset:20480\n\t" \
  "ds_read_b128 v[156:159], v242 offset:48128\n\t" \
  "v_mfma_f32_16x16x32_bf16 v[52:55], a[196:199], v[160:163], v[52:55]\n\t" \
  "s_barrier\n\t" \
  "v_mfma_f32_16x16x32_bf16 v[40:43], a[192:195], v[128:131], 0\n\t" \
  "buffer_load_dwordx4 v242, s[16:19], s97 offen offset:1024 sc1 lds\n\t" \
  "v_mfma_f32_16x16x32_bf16 v[40:43], a[200:203], v[132:135], v[40:43]\n\t" \
  "ds_read_b128 v[160:163], v230 offset:32768\n\t" \
  "v_mfma_f32_16x16x32_bf16 v[40:43], a[208:211], v[136:139], v[40:43]\n\t" \
  "ds_read_b128 v[164:167], v230 offset:33792\n\t" \
  "v_mfma_f32_16x16x32_bf16 v[40:43], a[216:219], v[140:143], v[40:43]\n\t" \
  "buffer_load_dwordx4 v242, s[16:19], s97 offen offset:2048 sc1 lds\n\t" \
  "v_mfma_f32_16x16x32_bf16 v[56:59], a[220:223], v[140:143], 0\n\t" \
  "ds_read_b128 v[168:171], v230 offset:34816\n\t" \
  "v_mfma_f32_16x16x32_bf16 v[56:59], a[212:215], v[136:139], v[56:59]\n\t" \
  "ds_read_b64_tr_b16 a[180:181], v237 offset:16384\n\t" \
  "v_mfma_f32_16x16x32_bf16 v[56:59], a[204:207], v[132:135], v[56:59]\n\t" \
  "buffer_load_dwordx4 v242, s[16:19], s97 offen offset:3072 sc1 lds\n\t" \
  "v_mfma_f32_16x16x32_bf16 v[56:59], a[196:199], v[128:131], v[56:59]\n\t" \
  "s_waitcnt lgkmcnt(4)\n\t" \
  "s_add_u32 s97, s64, 262144\n\t" \
  "s_and_b32 s97, s97, 0xe0000\n\t" \
  "ds_read_b128 v[172:175], v230 offset:35840\n\t" \
  "s_add_u32 s97, s97, s60\n\t" \
  "v_mfma_f32_16x16x32_bf16 v[44:47], a[192:195], v[144:147], 0\n\t" \
  "s_add_u32 m0, s38, 65536\n\t" \
  "ds_read_b64_tr_b16 a[182:183], v237 offset:20480\n\t" \
  "ds_read_b128 v[128:131], v230 offset:36864\n\t" \
  "ds_read_b128 v[132:135], v230 offset:37888\n\t" \
  "v_mfma_f32_16x16x32_bf16 v[44:47], a[200:203], v[148:151], v[44:47]\n\t" \
  "buffer_load_dwordx4 v242, s[16:19], s97 offen offset:0 sc1 lds\n\t" \
  "v_mfma_f32_16x16x32_bf16 v[44:47], a[208:211], v[152:155], v[44:47]\n\t" \
  "ds_read_b64_tr_b16 a[184:185], v236 offset:24576\n\t" \
  "ds_read_b128 v[136:139], v230 offset:38912\n\t" \
  "v_mfma_f32_16x16x32_bf16 v[44:47], a[216:219], v[156:159], v[44:47]\n\t" \
  "ds_read_b64_tr_b16 a[186:187], v236 offset:28672\n\t" \
  "v_mfma_f32_16x16x32_bf16 v[60:63], a[220:223], v[156:159], 0\n\t" \
  "buffer_load_dwordx4 v242, s[16:19], s97 offen offset:1024 sc1 lds\n\t" \
  "v_mfma_f32_16x16x32_bf16 v[60:63], a[212:215], v[152:155], v[60:63]\n\t" \
  "ds_read_b128 v[140:143], v230 offset:39936\n\t" \
  "v_mfma_f32_16x16x32_bf16 v[60:63], a[204:207], v[148:151], v[60:63]\n\t" \
  "ds_read_b64_tr_b16 a[188:189], v237 offset:24576\n\t" \
  "v_mfma_f32_16x16x32_bf16 v[60:63], a[196:199], v[144:147], v[60:63]\n\t" \
  "s_waitcnt lgkmcnt(8)\n\t" \
  "buffer_load_dwordx4 v242, s[16:19], s97 offen offset:2048 sc1 lds\n\t" \
  STP("s_memrealtime s[94:95]\n\t") \
  STP("s_memtime s[92:93]\n\t") \
  "v_mfma_f32_16x16x32_bf16 v[64:67], a[192:195], v[160:163], 0\n\t" \
  "ds_read_b128 v[144:147], v230 offset:40960\n\t" \
  "v_mfma_f32_16x16x32_bf16 v[64:67], a[200:203], v[164:167], v[64:67]\n\t" \
  "ds_read_b64_tr_b16 a[190:191], v237 offset:28672\n\t" \
  "v_mfma_f32_16x16x32_bf16 v[64:67], a[208:211], v[168:171], v[64:67]\n\t" \
  "buffer_load_dwordx4 v242, s[16:19], s97 offen offset:3072 sc1 lds\n\t" \
  "v_mfma_f32_16x16x32_bf16 v[64:67], a[216:219], v[172:175], v[64:67]\n\t" \
  "s_add_u32 s97, s64, 393216\n\t" \
  "s_and_b32 s97, s97, 0xe0000\n\t" \
  "ds_read_b128 v[148:151], v230 offset:41984\n\t" \
  "s_add_u32 s97, s97, s60\n\t" \
  "v_mfma_f32_16x16x32_bf16 v[80:83], a[220:223], v[172:175], 0\n\t" \
  "s_add_u32 m0, s38, 16384\n\t" \
  "ds_read_b128 v[152:155], v230 offset:43008\n\t" \
  "v_mfma_f32_16x16x32_bf16 v[80:83], a[212:215], v[168:171], v[80:83]\n\t" \
  "buffer_load_dwordx4 v242, s[16:19], s97 offen offset:0 sc1 lds\n\t" \
  "v_mfma_f32_16x16x32_bf16 v[80:83], a[204:207], v[164:167], v[80:83]\n\t" \
  "ds_read_b128 v[156:159], v230 offset:44032\n\t" \
  "v_mfma_f32_16x16x32_bf16 v[80:83], a[196:199], v[160:163], v[80:83]\n\t" \
  "s_waitcnt lgkmcnt(6)\n\t" \
  "ds_read_b64_tr_b16 a[0:1], v236 offset:49152\n\t" \
  "v_mfma_f32_16x16x32_bf16 v[68:71], a[192:195], v[128:131], 0\n\t" \
  "s_mov_b64 exec, 0\n\t" \
  "s_cmp_eq_u32 s3, 0\n\t" \
  "s_cbranch_scc1 .Lhc10\n\t" \
  "s_cmp_eq_u32 s3, 1\n\t" \
  "s_cbranch_scc1 .Lhc11\n\t" \
  "s_cmp_eq_u32 s3, 2\n\t" \
  "s_cbranch_scc1 .Lhc12\n\t" \
  "s_branch .Lhc13\n\t" \
  ".Lhr1:\n\t" \
  "s_mov_b64 exec, -1\n\t" \
  "v_mfma_f32_16x16x32_bf16 v[68:71], a[200:203], v[132:135], v[68:71]\n\t" \
  "buffer_load_dwordx4 v242, s[16:19], s97 offen offset:1024 sc1 lds\n\t" \
  "v_mfma_f32_16x16x32_bf16 v[68:71], a[208:211], v[136:139], v[68:71]\n\t" \
  "ds_read_b64_tr_b16 a[2:3], v236 offset:53248\n\t" \
  "ds_read_b128 v[160:163], v230 offset:45056\n\t" \
  "v_mfma_f32_16x16x32_bf16 v[68:71], a[216:219], v[140:143], v[68:71]\n\t" \
  "buffer_load_dwordx4 v242, s[16:19], s97 offen offset:2048 sc1 lds\n\t" \
  "v_mfma_f32_16x16x32_bf16 v[84:87], a[220:223], v[140:143], 0\n\t" \
  STP("v_writelane_b32 v255, s94, 26\n\t") \
  STP("v_writelane_b32 v255, s92, 27\n\t") \
  "ds_read_b128 v[164:167], v230 offset:46080\n\t" \
  "v_mfma_f32_16x16x32_bf16 v[84:87], a[212:215], v[136:139], v[84:87]\n\t" \
  "ds_read_b128 v[168:171], v230 offset:47104\n\t" \
  "v_mfma_f32_16x16x32_bf16 v[84:87], a[204:207], v[132:135], v[84:87]\n\t" \
  "s_waitcnt vmcnt(7) lgkmcnt(5)\n\t" \
  "buffer_load_dwordx4 v242, s[16:19], s97 offen offset:3072 sc1 lds\n\t" \
  "v_mfma_f32_16x16x32_bf16 v[84:87], a[196:199], v[128:131], v[84:87]\n\t" \
  "s_barrier\n\t" \
  "v_mfma_f32_16x16x32_bf16 v[72:75], a[192:195], v[144:147], 0\n\t" \
  "ds_read_b128 v[172:175], v230 offset:48128\n\t" \
  "v_mfma_f32_16x16x32_bf16 v[72:75], a[200:203], v[148:151], v[72:75]\n\t" \
  "ds_read_b128 v[128:131], v242 offset:49152\n\t" \
  "v_mfma_f32_16x16x32_bf16 v[72:75], a[208:211], v[152:155], v[72:75]\n\t" \
  "ds_read_b64_tr_b16 a[4:5], v237 offset:49152\n\t" \
  "v_mfma_f32_16x16x32_bf16 v[72:75], a[216:219], v[156:159], v[72:75]\n\t" \
  "ds_read_b64_tr_b16 a[6:7], v237 offset:53248\n\t" \
  "ds_read_b128 v[132:135], v242 offset:50176\n\t" \
  "v_mfma_f32_16x16x32_bf16 v[88:91], a[220:223], v[156:159], 0\n\t" \
  "s_add_u32 s96, s40, 1048576\n\t" \
  "ds_read_b64_tr_b16 a[8:9], v236 offset:57344\n\t" \
  "s_and_b32 s96, s96, 0x1c0000\n\t" \
  "v_mfma_f32_16x16x32_bf16 v[88:91], a[212:215], v[152:155], v[88:91]\n\t" \
  "s_add_u32 s96, s96, s77\n\t" \
  "ds_read_b128 v[136:139], v242 offset:51200\n\t" \
  "s_add_u32 m0, s38, 81920\n\t" \
  "v_mfma_f32_16x16x32_bf16 v[88:91], a[204:207], v[148:151], v[88:91]\n\t" \
  "ds_read_b128 v[140:143], v242 offset:52224\n\t" \
  "v_mfma_f32_16x16x32_bf16 v[88:91], a[196:199], v[144:147], v[88:91]\n\t" \
  "s_waitcnt lgkmcnt(7)\n\t" \
  "ds_read_b64_tr_b16 a[10:11], v236 offset:61440\n\t" \
  "v_mfma_f32_16x16x32_bf16 v[76:79], a[192:195], v[160:163], 0\n\t" \
  "buffer_load_dword v246, v245, s[16:19], s65 offen sc1\n\t" \
  "v_mfma_f32_16x16x32_bf16 v[76:79], a[200:203], v[164:167], v[76:79]\n\t" \
  "ds_read_b64_tr_b16 a[12:13], v237 offset:57344\n\t" \
  "ds_read_b128 v[144:147], v242 offset:53248\n\t" \
  "v_mfma_f32_16x16x32_bf16 v[76:79], a[208:211], v[168:171], v[76:79]\n\t" \
  "buffer_load_dwordx4 v240, s[24:27], s96 offen offset:0 lds\n\t" \
  "v_mfma_f32_16x16x32_bf16 v[76:79], a[216:219], v[172:175], v[76:79]\n\t" \
  "ds_read_b64_tr_b16 a[14:15], v237 offset:61440\n\t" \
  "ds_read_b128 v[148:151], v242 offset:54272\n\t" \
  ".Lhs11:\n\ts_cbranch_execz .Lhs11n\n\t" \
  "v_mfma_f32_16x16x32_bf16 v[92:95], a[220:223], v[172:175], 0\n\t" \
  "ds_read_b128 v[152:155], v242 offset:55296\n\t" \
  "v_mfma_f32_16x16x32_bf16 v[92:95], a[212:215], v[168:171], v[92:95]\n\t" \
  "buffer_load_dwordx4 v241, s[24:27], s96 offen offset:1024 lds\n\t" \
  "v_mfma_f32_16x16x32_bf16 v[92:95], a[204:207], v[164:167], v[92:95]\n\t" \
  "ds_read_b128 v[156:159], v242 offset:56320\n\t" \
  "v_mfma_f32_16x16x32_bf16 v[92:95], a[196:199], v[160:163], v[92:95]\n\t" \
  "s_waitcnt lgkmcnt(7)\n\t" \
  "ds_read_b64_tr_b16 a[16:17], v238 offset:16384\n\t" \
  STP("s_memrealtime s[94:95]\n\t") \
  STP("s_memtime s[92:93]\n\t") \
  "v_mfma_f32_16x16x32_bf16 v[0:3], a[128:131], v[128:131], v[0:3]\n\t" \
  "buffer_load_dwordx4 v248, s[24:27], s96 offen offset:2048 lds\n\t" \
  "v_mfma_f32_16x16x32_bf16 v[0:3], a[136:139], v[132:135], v[0:3]\n\t" \
  "ds_read_b64_tr_b16 a[18:19], v238 offset:20480\n\t" \
  "ds_read_b128 v[160:163], v242 offset:57344\n\t" \
  "v_accvgpr_read_b32 v96, a96\n\t" \
  "v_mfma_f32_16x16x32_bf16 v[0:3], a[144:147], v[136:139], v[0:3]\n\t" \
  "ds_read_b64_tr_b16 a[20:21], v239 offset:16384\n\t" \
  "ds_read_b128 v[164:167], v242 offset:58368\n\t" \
  "v_accvgpr_read_b32 v97, a97\n\t" \
  "v_mfma_f32_16x16x32_bf16 v[0:3], a[152:155], v[140:143], v[0:3]\n\t" \
  "buffer_load_dwordx4 v249, s[24:27], s96 offen offset:3072 lds\n\t" \
  "v_mfma_f32_16x16x32_bf16 v[16:19], a[156:159], v[140:143], v[16:19]\n\t" \
  "s_add_u32 s96, s40, 1310720\n\t" \
  "ds_read_b128 v[168:171], v242 offset:59392\n\t" \
  "v_accvgpr_read_b32 v98, a98\n\t" \
  "v_mfma_f32_16x16x32_bf16 v[16:19], a[148:151], v[136:139], v[16:19]\n\t" \
  "s_and_b32 s96, s96, 0x1c0000\n\t" \
  "ds_read_b64_tr_b16 a[22:23], v239 offset:20480\n\t" \
  "v_accvgpr_read_b32 v99, a99\n\t" \
  "v_mfma_f32_16x16x32_bf16 v[16:19], a[140:143], v[132:135], v[16:19]\n\t" \
  "buffer_store_dwordx4 v[120:123], v242, s[16:19], s98 offen offset:2048 sc1\n\t" \
  "ds_read_b64_tr_b16 a[24:25], v238 offset:24576\n\t" \
  "ds_read_b64_tr_b16 a[26:27], v238 offset:28672\n\t" \
  "ds_read_b128 v[172:175], v242 offset:60416\n\t" \
  "v_mfma_f32_16x16x32_bf16 v[16:19], a[132:135], v[128:131], v[16:19]\n\t" \
  "s_waitcnt lgkmcnt(10)\n\t" \
  "ds_read_b64_tr_b16 a[28:29], v239 offset:24576\n\t" \
  "v_accvgpr_read_b32 v100, a100\n\t" \
  "v_mfma_f32_16x16x32_bf16 v[4:7], a[128:131], v[144:147], v[4:7]\n\t" \
  "s_add_u32 s96, s96, s51\n\t" \
  "ds_read_b128 v[128:131], v242 offset:61440\n\t" \
  "s_add_u32 m0, s38, 131072\n\t" \
  "v_mfma_f32_16x16x32_bf16 v[4:7], a[136:139], v[148:151], v[4:7]\n\t" \
  "ds_read_b64_tr_b16 a[30:31], v239 offset:28672\n\t" \
  "v_accvgpr_read_b32 v101, a101\n\t" \
  "v_mfma_f32_16x16x32_bf16 v[4:7], a[144:147], v[152:155], v[4:7]\n\t" \
  "buffer_load_dwordx4 v240, s[24:27], s96 offen offset:0 lds\n\t" \
  "v_mfma_f32_16x16x32_bf16 v[4:7], a[152:155], v[156:159], v[4:7]\n\t" \
  "ds_read_b128 v[132:135], v242 offset:62464\n\t" \
  "v_accvgpr_read_b32 v102, a102\n\t" \
  "v_mfma_f32_16x16x32_bf16 v[20:23], a[156:159], v[156:159], v[20:23]\n\t" \
  STP("v_writelane_b32 v255, s94, 28\n\t") \
  STP("v_writelane_b32 v255, s92, 29\n\t") \
  "ds_read_b128 v[136:139], v242 offset:63488\n\t" \
  "v_accvgpr_read_b32 v103, a103\n\t" \
  "v_mfma_f32_16x16x32_bf16 v[20:23], a[148:151], v[152:155], v[20:23]\n\t" \
  "buffer_load_dwordx4 v241, s[24:27], s96 offen offset:1024 lds\n\t" \
  "v_mfma_f32_16x16x32_bf16 v[20:23], a[140:143], v[148:151], v[20:23]\n\t" \
  "s_waitcnt vmcnt(12) lgkmcnt(5)\n\t" \
  "ds_read_b128 v[140:143], v242 offset:64512\n\t" \
  "v_mul_f32 v96, v96, v222\n\t" \
  "v_mfma_f32_16x16x32_bf16 v[20:23], a[132:135], v[144:147], v[20:23]\n\t" \
  "s_barrier\n\t" \
  "v_mfma_f32_16x16x32_bf16 v[8:11], a[128:131], v[160:163], v[8:11]\n\t" \
  "buffer_load_dwordx4 v248, s[24:27], s96 offen offset:2048 lds\n\t" \
  "v_mfma_f32_16x16x32_bf16 v[8:11], a[136:139], v[164:167], v[8:11]\n\t" \
  "ds_read_b128 v[144:147], v230 offset:0\n\t" \
  "v_mul_f32 v97, v97, v222\n\t" \
  "v_mfma_f32_16x16x32_bf16 v[8:11], a[144:147], v[168:171], v[8:11]\n\t" \
  "ds_read_b64_tr_b16 a[32:33], v234 offset:0\n\t" \
  "v_mul_f32 v98, v98, v222\n\t" \
  "v_mfma_f32_16x16x32_bf16 v[8:11], a[152:155], v[172:175], v[8:11]\n\t" \
  "ds_read_b128 v[148:151], v230 offset:1024\n\t" \
  "v_mul_f32 v99, v99, v222\n\t" \
  "v_mfma_f32_16x16x32_bf16 v[24:27], a[156:159], v[172:175], v[24:27]\n\t" \
  "buffer_load_dwordx4 v249, s[24:27], s96 offen offset:3072 lds\n\t" \
  "v_mfma_f32_16x16x32_bf16 v[24:27], a[148:151], v[168:171], v[24:27]\n\t" \
  "ds_read_b128 v[152:155], v230 offset:2048\n\t" \
  "v_mul_f32 v100, v100, v222\n\t" \
  "v_mfma_f32_16x16x32_bf16 v[24:27], a[140:143], v[164:167], v[24:27]\n\t" \
  "s_add_u32 s97, s64, 131072\n\t" \
  "ds_read_b128 v[156:159], v230 offset:3072\n\t" \
  "v_mul_f32 v101, v101, v222\n\t" \
  "v_mfma_f32_16x16x32_bf16 v[24:27], a[132:135], v[160:163], v[24:27]\n\t" \
  "s_waitcnt lgkmcnt(5)\n\t" \
  "ds_read_b64_tr_b16 a[34:35], v234 offset:4096\n\t" \
  "v_mul_f32 v102, v102, v222\n\t" \
  "v_mfma_f32_16x16x32_bf16 v[12:15], a[128:131], v[128:131], v[12:15]\n\t" \
  "s_and_b32 s97, s97, 0xe0000\n\t" \
  "ds_read_b128 v[160:163], v230 offset:4096\n\t" \
  "v_mul_f32 v103, v103, v222\n\t" \
  "v_mfma_f32_16x16x32_bf16 v[12:15], a[136:139], v[132:135], v[12:15]\n\t" \
  "s_waitcnt vmcnt(9)\n\t" \
  "v_cmp_ne_u32 vcc, s36, v246\n\t" \
  "v_cvt_pk_bf16_f32 v96, v96, v97\n\t" \
  "v_mfma_f32_16x16x32_bf16 v[12:15], a[144:147], v[136:139], v[12:15]\n\t" \
  "s_add_u32 s97, s97, s61\n\t" \
  "ds_read_b64_tr_b16 a[36:37], v235 offset:0\n\t" \
  "s_add_u32 m0, s38, 98304\n\t" \
  "v_mfma_f32_16x16x32_bf16 v[12:15], a[152:155], v[140:143], v[12:15]\n\t" \
  "s_and_b32 vcc_lo, vcc_lo, 0xfefe\n\t" \
  "s_cbranch_scc1 .Lat_s1\n\t" \
  ".Lat_k1:\n\t" \
  "ds_read_b128 v[164:167], v230 offset:5120\n\t" \
  "v_cvt_pk_bf16_f32 v97, v98, v99\n\t" \
  "v_mfma_f32_16x16x32_bf16 v[28:31], a[156:159], v[140:143], v[28:31]\n\t" \
  "buffer_load_dwordx4 v242, s[16:19], s97 offen offset:0 sc1 lds\n\t" \
  "v_mfma_f32_16x16x32_bf16 v[28:31], a[148:151], v[136:139], v[28:31]\n\t" \
  "ds_read_b128 v[168:171], v230 offset:6144\n\t" \
  "v_cvt_pk_bf16_f32 v98, v100, v101\n\t" \
  "v_mfma_f32_16x16x32_bf16 v[28:31], a[140:143], v[132:135], v[28:31]\n\t" \
  "ds_read_b64_tr_b16 a[38:39], v235 offset:4096\n\t" \
  "v_cvt_pk_bf16_f32 v99, v102, v103\n\t" \
  "v_mfma_f32_16x16x32_bf16 v[28:31], a[132:135], v[128:131], v[28:31]\n\t" \
  "s_waitcnt lgkmcnt(6)\n\t" \
  "buffer_load_dwordx4 v242, s[16:19], s97 offen offset:1024 sc1 lds\n\t" \
  STP("s_memrealtime s[94:95]\n\t") \
  STP("s_memtime s[92:93]\n\t") \
  "v_mfma_f32_16x16x32_bf16 v[0:3], a[160:163], v[144:147], v[0:3]\n\t" \
  "ds_write_b128 v213, v[96:99] offset:32768\n\t" \
  "v_mfma_f32_16x16x32_bf16 v[0:3], a[168:171], v[148:151], v[0:3]\n\t" \
  "ds_read_b64_tr_b16 a[40:41], v234 offset:8192\n\t" \
  "ds_read_b128 v[172:175], v230 offset:7168\n\t" \
  "v_accvgpr_read_b32 v104, a104\n\t" \
  "v_mfma_f32_16x16x32_bf16 v[0:3], a[176:179], v[152:155], v[0:3]\n\t" \
  "ds_read_b64_tr_b16 a[42:43], v234 offset:12288\n\t" \
  "ds_read_b128 v[128:131], v230 offset:8192\n\t" \
  "v_accvgpr_read_b32 v105, a105\n\t" \
  "v_mfma_f32_16x16x32_bf16 v[0:3], a[184:187], v[156:159], v[0:3]\n\t" \
  "ds_read_b128 v[132:135], v230 offset:9216\n\t" \
  "v_accvgpr_read_b32 v106, a106\n\t" \
  "v_mfma_f32_16x16x32_bf16 v[16:19], a[188:191], v[156:159], v[16:19]\n\t" \
  "ds_read_b64_tr_b16 a[44:45], v235 offset:8192\n\t" \
  "v_accvgpr_read_b32 v107, a107\n\t" \
  "v_mfma_f32_16x16x32_bf16 v[16:19], a[180:183], v[152:155], v[16:19]\n\t" \
  "ds_read_b128 v[136:139], v230 offset:10240\n\t" \
  "v_accvgpr_read_b32 v108, a108\n\t" \
  "v_mfma_f32_16x16x32_bf16 v[16:19], a[172:175], v[148:151], v[16:19]\n\t" \
  "ds_read_b64_tr_b16 a[46:47], v235 offset:12288\n\t" \
  "v_accvgpr_read_b32 v109, a109\n\t" \
  "v_mfma_f32_16x16x32_bf16 v[16:19], a[164:167], v[144:147], v[16:19]\n\t" \
  "s_waitcnt lgkmcnt(6)\n\t" \
  "ds_read_b128 v[140:143], v230 offset:11264\n\t" \
  "v_accvgpr_read_b32 v110, a110\n\t" \
  "v_mfma_f32_16x16x32_bf16 v[4:7], a[160:163], v[160:163], v[4:7]\n\t" \
  "buffer_load_dwordx4 v242, s[16:19], s97 offen offset:2048 sc1 lds\n\t" \
  "v_mfma_f32_16x16x32_bf16 v[4:7], a[168:171], v[164:167], v[4:7]\n\t" \
  "ds_read_b128 v[144:147], v230 offset:12288\n\t" \
  "v_accvgpr_read_b32 v111, a111\n\t" \
  "v_mfma_f32_16x16x32_bf16 v[4:7], a[176:179], v[168:171], v[4:7]\n\t" \
  "ds_read_b128 v[148:151], v230 offset:13312\n\t" \
  "v_mul_f32 v104, v104, v222\n\t" \
  "v_mfma_f32_16x16x32_bf16 v[4:7], a[184:187], v[172:175], v[4:7]\n\t" \
  "buffer_load_dwordx4 v242, s[16:19], s97 offen offset:3072 sc1 lds\n\t" \
  "v_mfma_f32_16x16x32_bf16 v[20:23], a[188:191], v[172:175], v[20:23]\n\t" \
  STP("v_writelane_b32 v255, s94, 30\n\t") \
  STP("v_writelane_b32 v255, s92, 31\n\t") \
  "ds_read_b128 v[152:155], v230 offset:14336\n\t" \
  "v_mul_f32 v105, v105, v222\n\t" \
  "v_mfma_f32_16x16x32_bf16 v[20:23], a[180:183], v[168:171], v[20:23]\n\t" \
  "v_mul_f32 v106, v106, v222\n\t" \
  "v_mul_f32 v107, v107, v222\n\t" \
  "v_mfma_f32_16x16x32_bf16 v[20:23], a[172:175], v[164:167], v[20:23]\n\t" \
  "s_waitcnt lgkmcnt(3)\n\t" \
  "ds_read_b128 v[156:159], v230 offset:15360\n\t" \
  "v_mul_f32 v108, v108, v222\n\t" \
  "v_mfma_f32_16x16x32_bf16 v[20:23], a[164:167], v[160:163], v[20:23]\n\t" \
  "s_barrier\n\t" \
  "v_mfma_f32_16x16x32_bf16 v[8:11], a[160:163], v[128:131], v[8:11]\n\t" \
  "buffer_store_dwordx4 v[124:127], v242, s[16:19], s98 offen offset:3072 sc1\n\t" \
  "v_mul_f32 v109, v109, v222\n\t" \
  "ds_read_b128 v[160:163], v242 offset:16384\n\t" \
  "v_mul_f32 v110, v110, v222\n\t" \
  "v_mfma_f32_16x16x32_bf16 v[8:11], a[168:171], v[132:135], v[8:11]\n\t" \
  "s_xor_b32 s98, s45, 98304\n\t" \
  "v_mul_f32 v111, v111, v222\n\t" \
  "v_cvt_pk_bf16_f32 v104, v104, v105\n\t" \
  "v_mfma_f32_16x16x32_bf16 v[8:11], a[176:179], v[136:139], v[8:11]\n\t" \
  "s_add_u32 s97, s64, 524288\n\t" \
  "ds_read_b128 v[164:167], v242 offset:17408\n\t" \
  "v_cvt_pk_bf16_f32 v105, v106, v107\n\t" \
  "v_mfma_f32_16x16x32_bf16 v[8:11], a[184:187], v[140:143], v[8:11]\n\t" \
  "s_and_b32 s97, s97, 0xe0000\n\t" \
  "v_cvt_pk_bf16_f32 v106, v108, v109\n\t" \
  "v_cvt_pk_bf16_f32 v107, v110, v111\n\t" \
  "v_mfma_f32_16x16x32_bf16 v[24:27], a[188:191], v[140:143], v[24:27]\n\t" \
  "s_add_u32 s97, s97, s60\n\t" \
  "ds_read_b128 v[168:171], v242 offset:18432\n\t" \
  "s_add_u32 m0, s38, 0\n\t" \
  "v_mfma_f32_16x16x32_bf16 v[24:27], a[180:183], v[136:139], v[24:27]\n\t" \
  "ds_write_b128 v213, v[104:107] offset:33792\n\t" \
  "v_mfma_f32_16x16x32_bf16 v[24:27], a[172:175], v[132:135], v[24:27]\n\t" \
  "buffer_load_dwordx4 v242, s[16:19], s97 offen offset:0 sc1 lds\n\t" \
  "v_mfma_f32_16x16x32_bf16 v[24:27], a[164:167], v[128:131], v[24:27]\n\t" \
  "s_waitcnt lgkmcnt(4)\n\t" \
  "ds_read_b128 v[172:175], v242 offset:19456\n\t" \
  "v_mfma_f32_16x16x32_bf16 v[12:15], a[160:163], v[144:147], v[12:15]\n\t" \
  "ds_read_b128 v[128:131], v242 offset:20480\n\t" \
  "v_mfma_f32_16x16x32_bf16 v[12:15], a[168:171], v[148:151], v[12:15]\n\t" \
  "buffer_load_dwordx4 v242, s[16:19], s97 offen offset:1024 sc1 lds\n\t" \
  "v_mfma_f32_16x16x32_bf16 v[12:15], a[176:179], v[152:155], v[12:15]\n\t" \
  "ds_read_b128 v[132:135], v242 offset:21504\n\t" \
  "v_mfma_f32_16x16x32_bf16 v[12:15], a[184:187], v[156:159], v[12:15]\n\t" \
  "ds_read_b128 v[136:139], v242 offset:22528\n\t" \
  "v_mfma_f32_16x16x32_bf16 v[28:31], a[188:191], v[156:159], v[28:31]\n\t" \
  "ds_read_b128 v[140:143], v242 offset:23552\n\t" \
  "v_mfma_f32_16x16x32_bf16 v[28:31], a[180:183], v[152:155], v[28:31]\n\t" \
  "v_mfma_f32_16x16x32_bf16 v[28:31], a[172:175], v[148:151], v[28:31]\n\t" \
  "v_mfma_f32_16x16x32_bf16 v[28:31], a[164:167], v[144:147], v[28:31]\n\t" \
  "s_waitcnt lgkmcnt(4)\n\t" \
  "v_mfma_f32_16x16x32_bf16 v[0:3], a[0:3], v[160:163], v[0:3]\n\t" \
  "ds_read_b128 v[144:147], v242 offset:24576\n\t" \
  "v_mfma_f32_16x16x32_bf16 v[0:3], a[8:11], v[164:167], v[0:3]\n\t" \
  "ds_read_b128 v[148:151], v242 offset:25600\n\t" \
  "v_mfma_f32_16x16x32_bf16 v[0:3], a[16:19], v[168:171], v[0:3]\n\t" \
  "buffer_load_dwordx4 v242, s[16:19], s97 offen offset:2048 sc1 lds\n\t" \
  "v_mfma_f32_16x16x32_bf16 v[0:3], a[24:27], v[172:175], v[0:3]\n\t" \
  "ds_read_b128 v[152:155], v242 offset:26624\n\t" \
  "v_mfma_f32_16x16x32_bf16 v[16:19], a[28:31], v[172:175], v[16:19]\n\t" \
  "ds_read_b128 v[156:159], v242 offset:27648\n\t" \
  "v_mfma_f32_16x16x32_bf16 v[16:19], a[20:23], v[168:171], v[16:19]\n\t" \
  "buffer_load_dwordx4 v242, s[16:19], s97 offen offset:3072 sc1 lds\n\t" \
  "v_mfma_f32_16x16x32_bf16 v[16:19], a[12:15], v[164:167], v[16:19]\n\t" \
  "v_mfma_f32_16x16x32_bf16 v[16:19], a[4:7], v[160:163], v[16:19]\n\t" \
  "s_waitcnt lgkmcnt(4)\n\t" \
  "v_mfma_f32_16x16x32_bf16 v[4:7], a[0:3], v[128:131], v[4:7]\n\t" \
  "buffer_store_dwordx4 v[96:99], v242, s[16:19], s98 offen offset:0 sc1\n\t" \
  "ds_read_b128 v[160:163], v242 offset:28672\n\t" \
  "ds_read_b128 v[164:167], v242 offset:29696\n\t" \
  "ds_read_b128 v[168:171], v242 offset:30720\n\t" \
  "v_mfma_f32_16x16x32_bf16 v[4:7], a[8:11], v[132:135], v[4:7]\n\t" \
  "s_add_u32 s97, s64, 262144\n\t" \
  "v_accvgpr_read_b32 v96, a112\n\t" \
  "v_accvgpr_read_b32 v97, a113\n\t" \
  "v_mfma_f32_16x16x32_bf16 v[4:7], a[16:19], v[136:139], v[4:7]\n\t" \
  "s_and_b32 s97, s97, 0xe0000\n\t" \
  "v_accvgpr_read_b32 v98, a114\n\t" \
  "v_accvgpr_read_b32 v99, a115\n\t" \
  "v_mfma_f32_16x16x32_bf16 v[4:7], a[24:27], v[140:143], v[4:7]\n\t" \
  "s_add_u32 s97, s97, s61\n\t" \
  "ds_read_b128 v[172:175], v242 offset:31744\n\t" \
  "v_accvgpr_read_b32 v100, a116\n\t" \
  "v_mfma_f32_16x16x32_bf16 v[20:23], a[28:31], v[140:143], v[20:23]\n\t" \
  "s_add_u32 m0, s38, 147456\n\t" \
  "v_accvgpr_read_b32 v101, a117\n\t" \
  "v_accvgpr_read_b32 v102, a118\n\t" \
  "v_mfma_f32_16x16x32_bf16 v[20:23], a[20:23], v[136:139], v[20:23]\n\t" \
  "v_accvgpr_read_b32 v103, a119\n\t" \
  "v_mul_f32 v96, v96, v222\n\t" \
  "v_mfma_f32_16x16x32_bf16 v[20:23], a[12:15], v[132:135], v[20:23]\n\t" \
  "s_waitcnt vmcnt(6) lgkmcnt(4)\n\t" \
  "buffer_load_dwordx4 v242, s[16:19], s97 offen offset:0 sc1 lds\n\t" \
  "v_mfma_f32_16x16x32_bf16 v[20:23], a[4:7], v[128:131], v[20:23]\n\t" \
  "s_barrier\n\t" \
  "v_mfma_f32_16x16x32_bf16 v[8:11], a[0:3], v[144:147], v[8:11]\n\t" \
  "ds_read_b64_tr_b16 a[48:49], v236 offset:16384\n\t" \
  "ds_read_b128 v[128:131], v230 offset:32768\n\t" \
  "v_mul_f32 v97, v97, v222\n\t" \
  "v_mfma_f32_16x16x32_bf16 v[8:11], a[8:11], v[148:151], v[8:11]\n\t" \
  "buffer_load_dwordx4 v242, s[16:19], s97 offen offset:1024 sc1 lds\n\t" \
  "v_mfma_f32_16x16x32_bf16 v[8:11], a[16:19], v[152:155], v[8:11]\n\t" \
  "ds_read_b128 v[132:135], v230 offset:33792\n\t" \
  "v_mul_f32 v98, v98, v222\n\t" \
  "v_mfma_f32_16x16x32_bf16 v[8:11], a[24:27], v[156:159], v[8:11]\n\t" \
  "ds_read_b64_tr_b16 a[50:51], v236 offset:20480\n\t" \
  "ds_read_b128 v[136:139], v230 offset:34816\n\t" \
  "v_mul_f32 v99, v99, v222\n\t" \
  "v_mfma_f32_16x16x32_bf16 v[24:27], a[28:31], v[156:159], v[24:27]\n\t" \
  "buffer_load_dwordx4 v242, s[16:19], s97 offen offset:2048 sc1 lds\n\t" \
  "v_mfma_f32_16x16x32_bf16 v[24:27], a[20:23], v[152:155], v[24:27]\n\t" \
  "ds_read_b128 v[140:143], v230 offset:35840\n\t" \
  "v_mul_f32 v100, v100, v222\n\t" \
  "v_mfma_f32_16x16x32_bf16 v[24:27], a[12:15], v[148:151], v[24:27]\n\t" \
  "ds_read_b64_tr_b16 a[52:53], v237 offset:16384\n\t" \
  "v_mul_f32 v101, v101, v222\n\t" \
  "v_mfma_f32_16x16x32_bf16 v[24:27], a[4:7], v[144:147], v[24:27]\n\t" \
  "s_waitcnt lgkmcnt(7)\n\t" \
  "buffer_load_dwordx4 v242, s[16:19], s97 offen offset:3072 sc1 lds\n\t" \
  "v_mfma_f32_16x16x32_bf16 v[12:15], a[0:3], v[160:163], v[12:15]\n\t" \
  "s_add_u32 s96, s40, 1310720\n\t" \
  "ds_read_b128 v[144:147], v230 offset:36864\n\t" \
  "v_mul_f32 v102, v102, v222\n\t" \
  "v_mfma_f32_16x16x32_bf16 v[12:15], a[8:11], v[164:167], v[12:15]\n\t" \
  "s_and_b32 s96, s96, 0x1c0000\n\t" \
  "ds_read_b128 v[148:151], v230 offset:37888\n\t" \
  "v_mul_f32 v103, v103, v222\n\t" \
  "v_mfma_f32_16x16x32_bf16 v[12:15], a[16:19], v[168:171], v[12:15]\n\t" \
  "buffer_store_dwordx4 v[104:107], v242, s[16:19], s98 offen offset:1024 sc1\n\t" \
  "ds_read_b64_tr_b16 a[54:55], v237 offset:20480\n\t" \
  "ds_read_b128 v[152:155], v230 offset:38912\n\t" \
  "v_cvt_pk_bf16_f32 v96, v96, v97\n\t" \
  "v_mfma_f32_16x16x32_bf16 v[12:15], a[24:27], v[172:175], v[12:15]\n\t" \
  "s_add_u32 s96, s96, s77\n\t" \
  "ds_read_b128 v[156:159], v230 offset:39936\n\t" \
  "v_cvt_pk_bf16_f32 v97, v98, v99\n\t" \
  "v_mfma_f32_16x16x32_bf16 v[28:31], a[28:31], v[172:175], v[28:31]\n\t" \
  "s_add_u32 m0, s38, 65536\n\t" \
  "ds_read_b64_tr_b16 a[56:57], v236 offset:24576\n\t" \
  "v_cvt_pk_bf16_f32 v98, v100, v101\n\t" \
  "v_mfma_f32_16x16x32_bf16 v[28:31], a[20:23], v[168:171], v[28:31]\n\t" \
  "v_cvt_pk_bf16_f32 v99, v102, v103\n\t" \
  "v_accvgpr_read_b32 v104, a120\n\t" \
  "v_mfma_f32_16x16x32_bf16 v[28:31], a[12:15], v[164:167], v[28:31]\n\t" \
  "buffer_load_dwordx4 v240, s[24:27], s96 offen offset:0 lds\n\t" \
  "v_mfma_f32_16x16x32_bf16 v[28:31], a[4:7], v[160:163], v[28:31]\n\t" \
  "s_waitcnt lgkmcnt(7)\n\t" \
  "ds_write_b128 v213, v[96:99] offset:34816\n\t" \
  "v_mfma_f32_16x16x32_bf16 v[32:35], a[128:131], v[128:131], v[32:35]\n\t" \
  "ds_read_b128 v[160:163], v230 offset:40960\n\t" \
  "v_accvgpr_read_b32 v105, a121\n\t" \
  "v_mfma_f32_16x16x32_bf16 v[32:35], a[136:139], v[132:135], v[32:35]\n\t" \
  "buffer_load_dwordx4 v241, s[24:27], s96 offen offset:1024 lds\n\t" \
  "v_mfma_f32_16x16x32_bf16 v[32:35], a[144:147], v[136:139], v[32:35]\n\t" \
  "ds_read_b128 v[164:167], v230 offset:41984\n\t" \
  "v_accvgpr_read_b32 v106, a122\n\t" \
  "v_mfma_f32_16x16x32_bf16 v[32:35], a[152:155], v[140:143], v[32:35]\n\t" \
  "ds_read_b64_tr_b16 a[58:59], v236 offset:28672\n\t" \
  "v_accvgpr_read_b32 v107, a123\n\t" \
  "v_mfma_f32_16x16x32_bf16 v[48:51], a[156:159], v[140:143], v[48:51]\n\t" \
  "buffer_load_dwordx4 v248, s[24:27], s96 offen offset:2048 lds\n\t" \
  "v_mfma_f32_16x16x32_bf16 v[48:51], a[148:151], v[136:139], v[48:51]\n\t" \
  "ds_read_b128 v[168:171], v230 offset:43008\n\t" \
  "v_accvgpr_read_b32 v108, a124\n\t" \
  "v_mfma_f32_16x16x32_bf16 v[48:51], a[140:143], v[132:135], v[48:51]\n\t" \
  "ds_read_b64_tr_b16 a[60:61], v237 offset:24576\n\t" \
  "v_accvgpr_read_b32 v109, a125\n\t" \
  "v_mfma_f32_16x16x32_bf16 v[48:51], a[132:135], v[128:131], v[48:51]\n\t" \
  "s_waitcnt lgkmcnt(7)\n\t" \
  "ds_read_b128 v[172:175], v230 offset:44032\n\t" \
  "v_accvgpr_read_b32 v110, a126\n\t" \
  "v_mfma_f32_16x16x32_bf16 v[36:39], a[128:131], v[144:147], v[36:39]\n\t" \
  "buffer_load_dwordx4 v249, s[24:27], s96 offen offset:3072 lds\n\t" \
  "v_mfma_f32_16x16x32_bf16 v[36:39], a[136:139], v[148:151], v[36:39]\n\t" \
  "ds_read_b64_tr_b16 a[62:63], v237 offset:28672\n\t" \
  "v_accvgpr_read_b32 v111, a127\n\t" \
  "v_mfma_f32_16x16x32_bf16 v[36:39], a[144:147], v[152:155], v[36:39]\n\t" \
  "ds_read_b128 v[128:131], v230 offset:45056\n\t" \
  "v_mul_f32 v104, v104, v222\n\t" \
  "v_mfma_f32_16x16x32_bf16 v[36:39], a[152:155], v[156:159], v[36:39]\n\t" \
  "buffer_store_dwordx4 v[96:99], v242, s[16:19], s98 offen offset:2048 sc1\n\t" \
  "ds_read_b64_tr_b16 a[64:65], v238 offset:0\n\t" \
  "ds_read_b64_tr_b16 a[66:67], v238 offset:4096\n\t" \
  "ds_read_b128 v[132:135], v230 offset:46080\n\t" \
  "v_mfma_f32_16x16x32_bf16 v[52:55], a[156:159], v[156:159], v[52:55]\n\t" \
  "ds_read_b128 v[136:139], v230 offset:47104\n\t" \
  "v_mul_f32 v105, v105, v222\n\t" \
  "v_mfma_f32_16x16x32_bf16 v[52:55], a[148:151], v[152:155], v[52:55]\n\t" \
  "ds_read_b64_tr_b16 a[68:69], v239 offset:0\n\t" \
  "v_mul_f32 v106, v106, v222\n\t" \
  "v_mfma_f32_16x16x32_bf16 v[52:55], a[140:143], v[148:151], v[52:55]\n\t" \
  "s_waitcnt vmcnt(11) lgkmcnt(6)\n\t" \
  "ds_read_b128 v[140:143], v230 offset:48128\n\t" \
  "v_mul_f32 v107, v107, v222\n\t" \
  "v_mfma_f32_16x16x32_bf16 v[52:55], a[132:135], v[144:147], v[52:55]\n\t" \
  "s_barrier\n\t" \
  "v_mfma_f32_16x16x32_bf16 v[40:43], a[128:131], v[160:163], v[40:43]\n\t" \
  "ds_read_b128 v[144:147], v242 offset:0\n\t" \
  "v_mul_f32 v108, v108, v222\n\t" \
  "v_mfma_f32_16x16x32_bf16 v[40:43], a[136:139], v[164:167], v[40:43]\n\t" \
  "s_add_u32 s96, s40, 1572864\n\t" \
  "ds_read_b64_tr_b16 a[70:71], v239 offset:4096\n\t" \
  "v_mul_f32 v109, v109, v222\n\t" \
  "v_mfma_f32_16x16x32_bf16 v[40:43], a[144:147], v[168:171], v[40:43]\n\t" \
  "s_and_b32 s96, s96, 0x1c0000\n\t" \
  "ds_read_b128 v[148:151], v242 offset:1024\n\t" \
  "v_mul_f32 v110, v110, v222\n\t" \
  "v_mfma_f32_16x16x32_bf16 v[40:43], a[152:155], v[172:175], v[40:43]\n\t" \
  "s_add_u32 s96, s96, s51\n\t" \
  "ds_read_b64_tr_b16 a[72:73], v238 offset:8192\n\t" \
  "v_mul_f32 v111, v111, v222\n\t" \
  "v_mfma_f32_16x16x32_bf16 v[56:59], a[156:159], v[172:175], v[56:59]\n\t" \
  "s_add_u32 m0, s38, 81920\n\t" \
  "ds_read_b128 v[152:155], v242 offset:2048\n\t" \
  "v_cvt_pk_bf16_f32 v104, v104, v105\n\t" \
  "v_mfma_f32_16x16x32_bf16 v[56:59], a[148:151], v[168:171], v[56:59]\n\t" \
  "ds_read_b64_tr_b16 a[74:75], v238 offset:12288\n\t" \
  "v_cvt_pk_bf16_f32 v105, v106, v107\n\t" \
  "v_mfma_f32_16x16x32_bf16 v[56:59], a[140:143], v[164:167], v[56:59]\n\t" \
  "ds_read_b128 v[156:159], v242 offset:3072\n\t" \
  "v_cvt_pk_bf16_f32 v106, v108, v109\n\t" \
  "v_mfma_f32_16x16x32_bf16 v[56:59], a[132:135], v[160:163], v[56:59]\n\t" \
  "s_waitcnt lgkmcnt(7)\n\t" \
  "buffer_load_dwordx4 v240, s[24:27], s96 offen offset:0 lds\n\t" \
  "v_mfma_f32_16x16x32_bf16 v[44:47], a[128:131], v[128:131], v[44:47]\n\t" \
  "ds_read_b128 v[160:163], v242 offset:4096\n\t" \
  "v_cvt_pk_bf16_f32 v107, v110, v111\n\t" \
  "v_mfma_f32_16x16x32_bf16 v[44:47], a[136:139], v[132:135], v[44:47]\n\t" \
  "ds_write_b128 v213, v[104:107] offset:35840\n\t" \
  "v_mfma_f32_16x16x32_bf16 v[44:47], a[144:147], v[136:139], v[44:47]\n\t" \
  "buffer_load_dwordx4 v241, s[24:27], s96 offen offset:1024 lds\n\t" \
  "v_mfma_f32_16x16x32_bf16 v[44:47], a[152:155], v[140:143], v[44:47]\n\t" \
  "ds_read_b128 v[164:167], v242 offset:5120\n\t" \
  "v_mfma_f32_16x16x32_bf16 v[60:63], a[156:159], v[140:143], v[60:63]\n\t" \
  "ds_read_b128 v[168:171], v242 offset:6144\n\t" \
  "v_mfma_f32_16x16x32_bf16 v[60:63], a[148:151], v[136:139], v[60:63]\n\t" \
  "ds_read_b64_tr_b16 a[76:77], v239 offset:8192\n\t" \
  "v_mfma_f32_16x16x32_bf16 v[60:63], a[140:143], v[132:135], v[60:63]\n\t" \
  "buffer_load_dwordx4 v248, s[24:27], s96 offen offset:2048 lds\n\t" \
  "v_mfma_f32_16x16x32_bf16 v[60:63], a[132:135], v[128:131], v[60:63]\n\t" \
  "s_waitcnt lgkmcnt(5)\n\t" \
  "ds_read_b128 v[172:175], v242 offset:7168\n\t" \
  "v_mfma_f32_16x16x32_bf16 v[0:3], a[32:35], v[144:147], v[0:3]\n\t" \
  "ds_read_b64_tr_b16 a[78:79], v239 offset:12288\n\t" \
  "v_mfma_f32_16x16x32_bf16 v[0:3], a[40:43], v[148:151], v[0:3]\n\t" \
  "buffer_load_dwordx4 v249, s[24:27], s96 offen offset:3072 lds\n\t" \
  "v_mfma_f32_16x16x32_bf16 v[0:3], a[48:51], v[152:155], v[0:3]\n\t" \
  "ds_read_b128 v[128:131], v242 offset:8192\n\t" \
  "v_mfma_f32_16x16x32_bf16 v[0:3], a[56:59], v[156:159], v[0:3]\n\t" \
  "ds_read_b128 v[132:135], v242 offset:9216\n\t" \
  "v_mfma_f32_16x16x32_bf16 v[16:19], a[60:63], v[156:159], v[16:19]\n\t" \
  "ds_read_b128 v[136:139], v242 offset:10240\n\t" \
  "v_mfma_f32_16x16x32_bf16 v[16:19], a[52:55], v[152:155], v[16:19]\n\t" \
  "buffer_store_dwordx4 v[104:107], v242, s[16:19], s98 offen offset:3072 sc1\n\t" \
  "ds_read_b128 v[140:143], v242 offset:11264\n\t" \
  "v_mfma_f32_16x16x32_bf16 v[16:19], a[44:47], v[148:151], v[16:19]\n\t" \
  "v_mfma_f32_16x16x32_bf16 v[16:19], a[36:39], v[144:147], v[16:19]\n\t" \
  "s_waitcnt lgkmcnt(5)\n\t" \
  "v_mfma_f32_16x16x32_bf16 v[4:7], a[32:35], v[160:163], v[4:7]\n\t" \
  "ds_read_b128 v[144:147], v242 offset:12288\n\t" \
  "v_mfma_f32_16x16x32_bf16 v[4:7], a[40:43], v[164:167], v[4:7]\n\t" \
  "ds_read_b128 v[148:151], v242 offset:13312\n\t" \
  "v_mfma_f32_16x16x32_bf16 v[4:7], a[48:51], v[168:171], v[4:7]\n\t" \
  "ds_read_b128 v[152:155], v242 offset:14336\n\t" \
  "v_mfma_f32_16x16x32_bf16 v[4:7], a[56:59], v[172:175], v[4:7]\n\t" \
  "s_add_u32 s97, s64, 655360\n\t" \
  "ds_read_b128 v[156:159], v242 offset:15360\n\t" \
  "s_and_b32 s97, s97, 0xe0000\n\t" \
  "v_mfma_f32_16x16x32_bf16 v[20:23], a[60:63], v[172:175], v[20:23]\n\t" \
  "s_add_u32 s97, s97, s60\n\t" \
  "s_add_u32 m0, s38, 131072\n\t" \
  "v_mfma_f32_16x16x32_bf16 v[20:23], a[52:55], v[168:171], v[20:23]\n\t" \
  "v_mfma_f32_16x16x32_bf16 v[20:23], a[44:47], v[164:167], v[20:23]\n\t" \
  "s_waitcnt vmcnt(11) lgkmcnt(4)\n\t" \
  "v_mfma_f32_16x16x32_bf16 v[20:23], a[36:39], v[160:163], v[20:23]\n\t" \
  "s_barrier\n\t" \
  "v_mfma_f32_16x16x32_bf16 v[8:11], a[32:35], v[128:131], v[8:11]\n\t" \
  "ds_read_b128 v[160:163], v232 offset:16384\n\t" \
  "v_mfma_f32_16x16x32_bf16 v[8:11], a[40:43], v[132:135], v[8:11]\n\t" \
  "buffer_load_dwordx4 v242, s[16:19], s97 offen offset:0 sc1 lds\n\t" \
  "v_mfma_f32_16x16x32_bf16 v[8:11], a[48:51], v[136:139], v[8:11]\n\t" \
  "ds_read_b128 v[164:167], v232 offset:17408\n\t" \
  "v_mfma_f32_16x16x32_bf16 v[8:11], a[56:59], v[140:143], v[8:11]\n\t" \
  "ds_read_b128 v[168:171], v232 offset:18432\n\t" \
  "v_mfma_f32_16x16x32_bf16 v[24:27], a[60:63], v[140:143], v[24:27]\n\t" \
  "ds_read_b128 v[172:175], v232 offset:19456\n\t" \
  "v_mfma_f32_16x16x32_bf16 v[24:27], a[52:55], v[136:139], v[24:27]\n\t" \
  "v_mfma_f32_16x16x32_bf16 v[24:27], a[44:47], v[132:135], v[24:27]\n\t" \
  "buffer_load_dwordx4 v242, s[16:19], s97 offen offset:1024 sc1 lds\n\t" \
  "v_mfma_f32_16x16x32_bf16 v[24:27], a[36:39], v[128:131], v[24:27]\n\t" \
  "s_waitcnt lgkmcnt(4)\n\t" \
  "v_mfma_f32_16x16x32_bf16 v[12:15], a[32:35], v[144:147], v[12:15]\n\t" \
  "ds_read_b128 v[128:131], v232 offset:20480\n\t" \
  "v_mfma_f32_16x16x32_bf16 v[12:15], a[40:43], v[148:151], v[12:15]\n\t" \
  "buffer_load_dwordx4 v242, s[16:19], s97 offen offset:2048 sc1 lds\n\t" \
  "v_mfma_f32_16x16x32_bf16 v[12:15], a[48:51], v[152:155], v[12:15]\n\t" \
  "ds_read_b128 v[132:135], v232 offset:21504\n\t" \
  "v_mfma_f32_16x16x32_bf16 v[12:15], a[56:59], v[156:159], v[12:15]\n\t" \
  "ds_read_b128 v[136:139], v232 offset:22528\n\t" \
  "v_mfma_f32_16x16x32_bf16 v[28:31], a[60:63], v[156:159], v[28:31]\n\t" \
  "buffer_load_dwordx4 v242, s[16:19], s97 offen offset:3072 sc1 lds\n\t" \
  "v_mfma_f32_16x16x32_bf16 v[28:31], a[52:55], v[152:155], v[28:31]\n\t" \
  "s_add_u32 s96, s40, 1572864\n\t" \
  "ds_read_b128 v[140:143], v232 offset:23552\n\t" \
  "s_and_b32 s96, s96, 0x1c0000\n\t" \
  "v_mfma_f32_16x16x32_bf16 v[28:31], a[44:47], v[148:151], v[28:31]\n\t" \
  "s_add_u32 s96, s96, s77\n\t" \
  "s_add_u32 m0, s38, 16384\n\t" \
  "v_mfma_f32_16x16x32_bf16 v[28:31], a[36:39], v[144:147], v[28:31]\n\t" \
  "s_waitcnt lgkmcnt(4)\n\t" \
  "v_mfma_f32_16x16x32_bf16 v[32:35], a[160:163], v[160:163], v[32:35]\n\t" \
  "buffer_load_dwordx4 v240, s[24:27], s96 offen offset:0 lds\n\t" \
  "v_mfma_f32_16x16x32_bf16 v[32:35], a[168:171], v[164:167], v[32:35]\n\t" \
  "ds_read_b128 v[144:147], v232 offset:24576\n\t" \
  "v_mfma_f32_16x16x32_bf16 v[32:35], a[176:179], v[168:171], v[32:35]\n\t" \
  "ds_read_b128 v[148:151], v232 offset:25600\n\t" \
  "v_mfma_f32_16x16x32_bf16 v[32:35], a[184:187], v[172:175], v[32:35]\n\t" \
  "ds_read_b128 v[152:155], v232 offset:26624\n\t" \
  "v_mfma_f32_16x16x32_bf16 v[48:51], a[188:191], v[172:175], v[48:51]\n\t" \
  "buffer_load_dwordx4 v241, s[24:27], s96 offen offset:1024 lds\n\t" \
  "v_mfma_f32_16x16x32_bf16 v[48:51], a[180:183], v[168:171], v[48:51]\n\t" \
  "ds_read_b128 v[156:159], v232 offset:27648\n\t" \
  "v_mfma_f32_16x16x32_bf16 v[48:51], a[172:175], v[164:167], v[48:51]\n\t" \
  "v_mfma_f32_16x16x32_bf16 v[48:51], a[164:167], v[160:163], v[48:51]\n\t" \
  "s_waitcnt lgkmcnt(4)\n\t" \
  "v_mfma_f32_16x16x32_bf16 v[36:39], a[160:163], v[128:131], v[36:39]\n\t" \
  "ds_read_b128 v[160:163], v232 offset:28672\n\t" \
  "v_mfma_f32_16x16x32_bf16 v[36:39], a[168:171], v[132:135], v[36:39]\n\t" \
  "ds_read_b128 v[164:167], v232 offset:29696\n\t" \
  "v_mfma_f32_16x16x32_bf16 v[36:39], a[176:179], v[136:139], v[36:39]\n\t" \
  "ds_read_b128 v[168:171], v232 offset:30720\n\t" \
  "v_mfma_f32_16x16x32_bf16 v[36:39], a[184:187], v[140:143], v[36:39]\n\t" \
  "ds_read_b128 v[172:175], v232 offset:31744\n\t" \
  "v_mfma_f32_16x16x32_bf16 v[52:55], a[188:191], v[140:143], v[52:55]\n\t" \
  "v_mfma_f32_16x16x32_bf16 v[52:55], a[180:183], v[136:139], v[52:55]\n\t" \
  "v_mfma_f32_16x16x32_bf16 v[52:55], a[172:175], v[132:135], v[52:55]\n\t" \
  "s_waitcnt vmcnt(12) lgkmcnt(4)\n\t" \
  "v_mfma_f32_16x16x32_bf16 v[52:55], a[164:167], v[128:131], v[52:55]\n\t" \
  "s_barrier\n\t" \
  "v_mfma_f32_16x16x32_bf16 v[40:43], a[160:163], v[144:147], v[40:43]\n\t" \
  "buffer_load_dwordx4 v248, s[24:27], s96 offen offset:2048 lds\n\t" \
  "v_mfma_f32_16x16x32_bf16 v[40:43], a[168:171], v[148:151], v[40:43]\n\t" \
  "ds_read_b128 v[128:131], v242 offset:32768\n\t" \
  "v_mfma_f32_16x16x32_bf16 v[40:43], a[176:179], v[152:155], v[40:43]\n\t" \
  "ds_read_b128 v[132:135], v242 offset:33792\n\t" \
  "v_mfma_f32_16x16x32_bf16 v[40:43], a[184:187], v[156:159], v[40:43]\n\t" \
  "ds_read_b64_tr_b16 a[80:81], v236 offset:0\n\t" \
  "v_mfma_f32_16x16x32_bf16 v[56:59], a[188:191], v[156:159], v[56:59]\n\t" \
  "buffer_load_dwordx4 v249, s[24:27], s96 offen offset:3072 lds\n\t" \
  "v_mfma_f32_16x16x32_bf16 v[56:59], a[180:183], v[152:155], v[56:59]\n\t" \
  "s_add_u32 s97, s64, 393216\n\t" \
  "ds_read_b128 v[136:139], v242 offset:34816\n\t" \
  "s_and_b32 s97, s97, 0xe0000\n\t" \
  "v_mfma_f32_16x16x32_bf16 v[56:59], a[172:175], v[148:151], v[56:59]\n\t" \
  "s_add_u32 s97, s97, s61\n\t" \
  "ds_read_b128 v[140:143], v242 offset:35840\n\t" \
  "s_add_u32 m0, s38, 0\n\t" \
  "v_mfma_f32_16x16x32_bf16 v[56:59], a[164:167], v[144:147], v[56:59]\n\t" \
  "s_waitcnt lgkmcnt(5)\n\t" \
  "ds_read_b64_tr_b16 a[82:83], v236 offset:4096\n\t" \
  "v_mfma_f32_16x16x32_bf16 v[44:47], a[160:163], v[160:163], v[44:47]\n\t" \
  "buffer_load_dwordx4 v242, s[16:19], s97 offen offset:0 sc1 lds\n\t" \
  "v_mfma_f32_16x16x32_bf16 v[44:47], a[168:171], v[164:167], v[44:47]\n\t" \
  "ds_read_b64_tr_b16 a[84:85], v237 offset:0\n\t" \
  "ds_read_b128 v[144:147], v242 offset:36864\n\t" \
  "v_mfma_f32_16x16x32_bf16 v[44:47], a[176:179], v[168:171], v[44:47]\n\t" \
  "ds_read_b64_tr_b16 a[86:87], v237 offset:4096\n\t" \
  "ds_read_b128 v[148:151], v242 offset:37888\n\t" \
  "v_mfma_f32_16x16x32_bf16 v[44:47], a[184:187], v[172:175], v[44:47]\n\t" \
  "buffer_load_dwordx4 v242, s[16:19], s97 offen offset:1024 sc1 lds\n\t" \
  "v_mfma_f32_16x16x32_bf16 v[60:63], a[188:191], v[172:175], v[60:63]\n\t" \
  "ds_read_b128 v[152:155], v242 offset:38912\n\t" \
  "v_mfma_f32_16x16x32_bf16 v[60:63], a[180:183], v[168:171], v[60:63]\n\t" \
  "ds_read_b64_tr_b16 a[88:89], v236 offset:8192\n\t" \
  "v_mfma_f32_16x16x32_bf16 v[60:63], a[172:175], v[164:167], v[60:63]\n\t" \
  "ds_read_b128 v[156:159], v242 offset:39936\n\t" \
  "v_mfma_f32_16x16x32_bf16 v[60:63], a[164:167], v[160:163], v[60:63]\n\t" \
  "s_waitcnt lgkmcnt(8)\n\t" \
  "ds_read_b64_tr_b16 a[90:91], v236 offset:12288\n\t" \
  "v_mfma_f32_16x16x32_bf16 v[96:99], a[192:195], v[128:131], 0\n\t" \
  "buffer_load_dwordx4 v242, s[16:19], s97 offen offset:2048 sc1 lds\n\t" \
  "v_mfma_f32_16x16x32_bf16 v[96:99], a[200:203], v[132:135], v[96:99]\n\t" \
  "ds_read_b128 v[160:163], v242 offset:40960\n\t" \
  "v_mfma_f32_16x16x32_bf16 v[96:99], a[208:211], v[136:139], v[96:99]\n\t" \
  "ds_read_b128 v[164:167], v242 offset:41984\n\t" \
  "v_mfma_f32_16x16x32_bf16 v[96:99], a[216:219], v[140:143], v[96:99]\n\t" \
  "buffer_load_dwordx4 v242, s[16:19], s97 offen offset:3072 sc1 lds\n\t" \
  "v_mfma_f32_16x16x32_bf16 v[112:115], a[220:223], v[140:143], 0\n\t" \
  "s_add_u32 s96, s40, 1835008\n\t" \
  "ds_read_b128 v[168:171], v242 offset:43008\n\t" \
  "s_and_b32 s96, s96, 0x1c0000\n\t" \
  "v_mfma_f32_16x16x32_bf16 v[112:115], a[212:215], v[136:139], v[112:115]\n\t" \
  "s_add_u32 s96, s96, s51\n\t" \
  "ds_read_b64_tr_b16 a[92:93], v237 offset:8192\n\t" \
  "s_add_u32 m0, s38, 49152\n\t" \
  "v_mfma_f32_16x16x32_bf16 v[112:115], a[204:207], v[132:135], v[112:115]\n\t" \
  "s_waitcnt vmcnt(12)\n\t" \
  "buffer_store_dword v243, v254, s[16:19], s49 offen offset:64 sc1\n\t" \
  "ds_read_b128 v[172:175], v242 offset:44032\n\t" \
  "v_mfma_f32_16x16x32_bf16 v[112:115], a[196:199], v[128:131], v[112:115]\n\t" \
  "s_waitcnt lgkmcnt(6)\n\t" \
  "ds_read_b64_tr_b16 a[94:95], v237 offset:12288\n\t" \
  ".Lhs12:\n\ts_cbranch_execz .Lhs12n\n\t" \
  "v_mfma_f32_16x16x32_bf16 v[100:103], a[192:195], v[144:147], 0\n\t" \
  "buffer_load_dwordx4 v240, s[24:27], s96 offen offset:0 lds\n\t" \
  "v_mfma_f32_16x16x32_bf16 v[100:103], a[200:203], v[148:151], v[100:103]\n\t" \
  "ds_read_b128 v[128:131], v242 offset:45056\n\t" \
  "v_mfma_f32_16x16x32_bf16 v[100:103], a[208:211], v[152:155], v[100:103]\n\t" \
  "ds_read_b128 v[132:135], v242 offset:46080\n\t" \
  "v_mfma_f32_16x16x32_bf16 v[100:103], a[216:219], v[156:159], v[100:103]\n\t" \
  "buffer_load_dwordx4 v241, s[24:27], s96 offen offset:1024 lds\n\t" \
  "v_mfma_f32_16x16x32_bf16 v[116:119], a[220:223], v[156:159], 0\n\t" \
  "ds_read_b128 v[136:139], v242 offset:47104\n\t" \
  "v_mfma_f32_16x16x32_bf16 v[116:119], a[212:215], v[152:155], v[116:119]\n\t" \
  "ds_read_b128 v[140:143], v242 offset:48128\n\t" \
  "v_mfma_f32_16x16x32_bf16 v[116:119], a[204:207], v[148:151], v[116:119]\n\t" \
  "s_waitcnt vmcnt(11) lgkmcnt(4)\n\t" \
  "v_mfma_f32_16x16x32_bf16 v[116:119], a[196:199], v[144:147], v[116:119]\n\t" \
  "s_barrier\n\t" \
  "v_mfma_f32_16x16x32_bf16 v[104:107], a[192:195], v[160:163], 0\n\t" \
  "ds_read_b128 v[144:147], v232 offset:0\n\t" \
  "v_mfma_f32_16x16x32_bf16 v[104:107], a[200:203], v[164:167], v[104:107]\n\t" \
  "ds_read_b64_tr_b16 a[96:97], v236 offset:16384\n\t" \
  "v_mfma_f32_16x16x32_bf16 v[104:107], a[208:211], v[168:171], v[104:107]\n\t" \
  "ds_read_b128 v[148:151], v232 offset:1024\n\t" \
  "v_mfma_f32_16x16x32_bf16 v[104:107], a[216:219], v[172:175], v[104:107]\n\t" \
  "ds_read_b64_tr_b16 a[98:99], v236 offset:20480\n\t" \
  "v_mfma_f32_16x16x32_bf16 v[120:123], a[220:223], v[172:175], 0\n\t" \
  "ds_read_b128 v[152:155], v232 offset:2048\n\t" \
  "v_mfma_f32_16x16x32_bf16 v[120:123], a[212:215], v[168:171], v[120:123]\n\t" \
  "ds_read_b128 v[156:159], v232 offset:3072\n\t" \
  "v_mfma_f32_16x16x32_bf16 v[120:123], a[204:207], v[164:167], v[120:123]\n\t" \
  "ds_read_b64_tr_b16 a[100:101], v237 offset:16384\n\t" \
  "v_mfma_f32_16x16x32_bf16 v[120:123], a[196:199], v[160:163], v[120:123]\n\t" \
  "s_waitcnt lgkmcnt(7)\n\t" \
  "ds_read_b64_tr_b16 a[102:103], v237 offset:20480\n\t" \
  "v_mfma_f32_16x16x32_bf16 v[108:111], a[192:195], v[128:131], 0\n\t" \
  "ds_read_b128 v[160:163], v232 offset:4096\n\t" \
  "v_mfma_f32_16x16x32_bf16 v[108:111], a[200:203], v[132:135], v[108:111]\n\t" \
  "ds_read_b128 v[164:167], v232 offset:5120\n\t" \
  "v_mfma_f32_16x16x32_bf16 v[108:111], a[208:211], v[136:139], v[108:111]\n\t" \
  "ds_read_b128 v[168:171], v232 offset:6144\n\t" \
  "v_mfma_f32_16x16x32_bf16 v[108:111], a[216:219], v[140:143], v[108:111]\n\t" \
  "buffer_load_dwordx4 v248, s[24:27], s96 offen offset:2048 lds\n\t" \
  "v_mfma_f32_16x16x32_bf16 v[124:127], a[220:223], v[140:143], 0\n\t" \
  "ds_read_b64_tr_b16 a[104:105], v236 offset:24576\n\t" \
  "v_mfma_f32_16x16x32_bf16 v[124:127], a[212:215], v[136:139], v[124:127]\n\t" \
  "ds_read_b128 v[172:175], v232 offset:7168\n\t" \
  "v_mfma_f32_16x16x32_bf16 v[124:127], a[204:207], v[132:135], v[124:127]\n\t" \
  "buffer_load_dwordx4 v249, s[24:27], s96 offen offset:3072 lds\n\t" \
  "v_mfma_f32_16x16x32_bf16 v[124:127], a[196:199], v[128:131], v[124:127]\n\t" \
  "s_waitcnt lgkmcnt(7)\n\t" \
  "ds_read_b64_tr_b16 a[106:107], v236 offset:28672\n\t" \
  "s_add_u32 s97, s64, 786432\n\t" \
  "v_mfma_f32_16x16x32_bf16 v[0:3], a[64:67], v[144:147], v[0:3]\n\t" \
  "s_and_b32 s97, s97, 0xe0000\n\t" \
  "s_add_u32 s97, s97, s60\n\t" \
  "s_add_u32 m0, s38, 65536\n\t" \
  "v_mfma_f32_16x16x32_bf16 v[0:3], a[72:75], v[148:151], v[0:3]\n\t" \
  "ds_read_b128 v[128:131], v232 offset:8192\n\t" \
  "v_lshrrev_b32 v252, 5, v233\n\t" \
  "v_mfma_f32_16x16x32_bf16 v[0:3], a[80:83], v[152:155], v[0:3]\n\t" \
  "buffer_load_dwordx4 v242, s[16:19], s97 offen offset:0 sc1 lds\n\t" \
  "v_mfma_f32_16x16x32_bf16 v[0:3], a[88:91], v[156:159], v[0:3]\n\t" \
  "ds_read_b128 v[132:135], v232 offset:9216\n\t" \
  "v_mul_u32_u24 v252, 1536, v252\n\t" \
  "v_mfma_f32_16x16x32_bf16 v[16:19], a[92:95], v[156:159], v[16:19]\n\t" \
  "ds_read_b128 v[136:139], v232 offset:10240\n\t" \
  "v_lshl_add_u32 v252, s3, 9, v252\n\t" \
  "v_mfma_f32_16x16x32_bf16 v[16:19], a[84:87], v[152:155], v[16:19]\n\t" \
  "ds_read_b64_tr_b16 a[108:109], v237 offset:24576\n\t" \
  "v_add_u32 v252, v252, v242\n\t" \
  "v_mfma_f32_16x16x32_bf16 v[16:19], a[76:79], v[148:151], v[16:19]\n\t" \
  "buffer_load_dwordx4 v242, s[16:19], s97 offen offset:1024 sc1 lds\n\t" \
  "v_mfma_f32_16x16x32_bf16 v[16:19], a[68:71], v[144:147], v[16:19]\n\t" \
  "s_waitcnt lgkmcnt(5)\n\t" \
  "ds_read_b128 v[140:143], v232 offset:11264\n\t" \
  "v_lshrrev_b32 v247, 1, v242\n\t" \
  "v_mfma_f32_16x16x32_bf16 v[4:7], a[64:67], v[160:163], v[4:7]\n\t" \
  "ds_read_b64_tr_b16 a[110:111], v237 offset:28672\n\t" \
  "v_add_u32 v247, s38, v247\n\t" \
  "v_mfma_f32_16x16x32_bf16 v[4:7], a[72:75], v[164:167], v[4:7]\n\t" \
  "buffer_load_dwordx4 v242, s[16:19], s97 offen offset:2048 sc1 lds\n\t" \
  "v_mfma_f32_16x16x32_bf16 v[4:7], a[80:83], v[168:171], v[4:7]\n\t" \
  "ds_read_b128 v[144:147], v232 offset:12288\n\t" \
  "v_mfma_f32_16x16x32_bf16 v[4:7], a[88:91], v[172:175], v[4:7]\n\t" \
  "ds_read_b128 v[148:151], v232 offset:13312\n\t" \
  "v_mfma_f32_16x16x32_bf16 v[20:23], a[92:95], v[172:175], v[20:23]\n\t" \
  "buffer_load_dwordx4 v242, s[16:19], s97 offen offset:3072 sc1 lds\n\t" \
  "v_mfma_f32_16x16x32_bf16 v[20:23], a[84:87], v[168:171], v[20:23]\n\t" \
  "s_add_u32 s96, s40, 1835008\n\t" \
  "ds_read_b128 v[152:155], v232 offset:14336\n\t" \
  "s_and_b32 s96, s96, 0x1c0000\n\t" \
  "v_mfma_f32_16x16x32_bf16 v[20:23], a[76:79], v[164:167], v[20:23]\n\t" \
  "s_waitcnt vmcnt(9) lgkmcnt(3)\n\t" \
  "s_add_u32 s96, s96, s77\n\t" \
  "s_add_u32 m0, s38, 81920\n\t" \
  "v_mfma_f32_16x16x32_bf16 v[20:23], a[68:71], v[160:163], v[20:23]\n\t" \
  "s_barrier\n\t" \
  "v_mfma_f32_16x16x32_bf16 v[8:11], a[64:67], v[128:131], v[8:11]\n\t" \
  "buffer_load_dwordx4 v240, s[24:27], s96 offen offset:0 lds\n\t" \
  "v_mfma_f32_16x16x32_bf16 v[8:11], a[72:75], v[132:135], v[8:11]\n\t" \
  "ds_read_b128 v[156:159], v232 offset:15360\n\t" \
  "v_mfma_f32_16x16x32_bf16 v[8:11], a[80:83], v[136:139], v[8:11]\n\t" \
  "ds_read_b128 v[160:163], v242 offset:0\n\t" \
  "v_mfma_f32_16x16x32_bf16 v[8:11], a[88:91], v[140:143], v[8:11]\n\t" \
  "buffer_load_dwordx4 v241, s[24:27], s96 offen offset:1024 lds\n\t" \
  "v_mfma_f32_16x16x32_bf16 v[24:27], a[92:95], v[140:143], v[24:27]\n\t" \
  "ds_read_b128 v[164:167], v242 offset:1024\n\t" \
  "v_mfma_f32_16x16x32_bf16 v[24:27], a[84:87], v[136:139], v[24:27]\n\t" \
  "ds_read_b128 v[168:171], v242 offset:2048\n\t" \
  "v_mfma_f32_16x16x32_bf16 v[24:27], a[76:79], v[132:135], v[24:27]\n\t" \
  "buffer_load_dwordx4 v248, s[24:27], s96 offen offset:2048 lds\n\t" \
  "v_mfma_f32_16x16x32_bf16 v[24:27], a[68:71], v[128:131], v[24:27]\n\t" \
  "s_waitcnt lgkmcnt(3)\n\t" \
  "ds_read_b128 v[172:175], v242 offset:3072\n\t" \
  "v_mfma_f32_16x16x32_bf16 v[12:15], a[64:67], v[144:147], v[12:15]\n\t" \
  "ds_read_b64_tr_b16 a[112:113], v234 offset:16384\n\t" \
  "v_mfma_f32_16x16x32_bf16 v[12:15], a[72:75], v[148:151], v[12:15]\n\t" \
  "ds_read_b128 v[128:131], v242 offset:4096\n\t" \
  "v_mfma_f32_16x16x32_bf16 v[12:15], a[80:83], v[152:155], v[12:15]\n\t" \
  "ds_read_b128 v[132:135], v242 offset:5120\n\t" \
  "v_mfma_f32_16x16x32_bf16 v[12:15], a[88:91], v[156:159], v[12:15]\n\t" \
  "buffer_load_dwordx4 v249, s[24:27], s96 offen offset:3072 lds\n\t" \
  "v_mfma_f32_16x16x32_bf16 v[28:31], a[92:95], v[156:159], v[28:31]\n\t" \
  "s_add_u32 s97, s64, 524288\n\t" \
  "s_and_b32 s97, s97, 0xe0000\n\t" \
  "s_add_u32 s97, s97, s61\n\t" \
  "v_mfma_f32_16x16x32_bf16 v[28:31], a[84:87], v[152:155], v[28:31]\n\t" \
  "s_add_u32 m0, s38, 147456\n\t" \
  "ds_read_b128 v[136:139], v242 offset:6144\n\t" \
  "v_mfma_f32_16x16x32_bf16 v[28:31], a[76:79], v[148:151], v[28:31]\n\t" \
  "buffer_load_dwordx4 v242, s[16:19], s97 offen offset:0 sc1 lds\n\t" \
  "v_mfma_f32_16x16x32_bf16 v[28:31], a[68:71], v[144:147], v[28:31]\n\t" \
  "s_waitcnt lgkmcnt(4)\n\t" \
  "ds_read_b64_tr_b16 a[114:115], v234 offset:20480\n\t" \
  STP("s_memrealtime s[94:95]\n\t") \
  STP("s_memtime s[92:93]\n\t") \
  "v_mfma_f32_16x16x32_bf16 v[32:35], a[0:3], v[160:163], v[32:35]\n\t" \
  "ds_read_b128 v[140:143], v242 offset:7168\n\t" \
  "v_mfma_f32_16x16x32_bf16 v[32:35], a[8:11], v[164:167], v[32:35]\n\t" \
  "ds_read_b64_tr_b16 a[116:117], v235 offset:16384\n\t" \
  "v_mfma_f32_16x16x32_bf16 v[32:35], a[16:19], v[168:171], v[32:35]\n\t" \
  "ds_read_b128 v[144:147], v242 offset:8192\n\t" \
  "v_mfma_f32_16x16x32_bf16 v[32:35], a[24:27], v[172:175], v[32:35]\n\t" \
  "ds_read_b64_tr_b16 a[118:119], v235 offset:20480\n\t" \
  "v_mfma_f32_16x16x32_bf16 v[48:51], a[28:31], v[172:175], v[48:51]\n\t" \
  "ds_read_b128 v[148:151], v242 offset:9216\n\t" \
  "v_mfma_f32_16x16x32_bf16 v[48:51], a[20:23], v[168:171], v[48:51]\n\t" \
  "ds_read_b64_tr_b16 a[120:121], v234 offset:24576\n\t" \
  "v_mfma_f32_16x16x32_bf16 v[48:51], a[12:15], v[164:167], v[48:51]\n\t" \
  "ds_read_b128 v[152:155], v242 offset:10240\n\t" \
  "v_mfma_f32_16x16x32_bf16 v[48:51], a[4:7], v[160:163], v[48:51]\n\t" \
  "s_waitcnt lgkmcnt(6)\n\t" \
  "ds_read_b64_tr_b16 a[122:123], v234 offset:28672\n\t" \
  "v_mfma_f32_16x16x32_bf16 v[36:39], a[0:3], v[128:131], v[36:39]\n\t" \
  "ds_read_b128 v[156:159], v242 offset:11264\n\t" \
  "v_mfma_f32_16x16x32_bf16 v[36:39], a[8:11], v[132:135], v[36:39]\n\t" \
  "ds_read_b64_tr_b16 a[124:125], v235 offset:24576\n\t" \
  "v_mfma_f32_16x16x32_bf16 v[36:39], a[16:19], v[136:139], v[36:39]\n\t" \
  "ds_read_b128 v[160:163], v242 offset:12288\n\t" \
  "v_mfma_f32_16x16x32_bf16 v[36:39], a[24:27], v[140:143], v[36:39]\n\t" \
  "ds_read_b128 v[164:167], v242 offset:13312\n\t" \
  "ds_read_b64_tr_b16 a[126:127], v235 offset:28672\n\t" \
  "v_mfma_f32_16x16x32_bf16 v[52:55], a[28:31], v[140:143], v[52:55]\n\t" \
  STP("v_writelane_b32 v255, s94, 32\n\t") \
  STP("v_writelane_b32 v255, s92, 33\n\t") \
  "ds_read_b128 v[168:171], v242 offset:14336\n\t" \
  "v_mfma_f32_16x16x32_bf16 v[52:55], a[20:23], v[136:139], v[52:55]\n\t" \
  "buffer_load_dwordx4 v242, s[16:19], s97 offen offset:1024 sc1 lds\n\t" \
  "v_mfma_f32_16x16x32_bf16 v[52:55], a[12:15], v[132:135], v[52:55]\n\t" \
  "s_waitcnt vmcnt(6) lgkmcnt(1)\n\t" \
  "ds_read_b128 v[172:175], v242 offset:15360\n\t" \
  "v_mfma_f32_16x16x32_bf16 v[52:55], a[4:7], v[128:131], v[52:55]\n\t" \
  "s_barrier\n\t" \
  "v_mfma_f32_16x16x32_bf16 v[40:43], a[0:3], v[144:147], v[40:43]\n\t" \
  "buffer_load_dwordx4 v242, s[16:19], s97 offen offset:2048 sc1 lds\n\t" \
  "v_mfma_f32_16x16x32_bf16 v[40:43], a[8:11], v[148:151], v[40:43]\n\t" \
  "ds_read_b128 v[128:131], v230 offset:0\n\t" \
  "v_mfma_f32_16x16x32_bf16 v[40:43], a[16:19], v[152:155], v[40:43]\n\t" \
  "ds_read_b128 v[132:135], v230 offset:1024\n\t" \
  "v_mfma_f32_16x16x32_bf16 v[40:43], a[24:27], v[156:159], v[40:43]\n\t" \
  "buffer_load_dwordx4 v242, s[16:19], s97 offen offset:3072 sc1 lds\n\t" \
  "v_mfma_f32_16x16x32_bf16 v[56:59], a[28:31], v[156:159], v[56:59]\n\t" \
  "s_add_u32 s97, s64, 917504\n\t" \
  "s_and_b32 s97, s97, 0xe0000\n\t" \
  "s_add_u32 s97, s97, s60\n\t" \
  "v_mfma_f32_16x16x32_bf16 v[56:59], a[20:23], v[152:155], v[56:59]\n\t" \
  "s_add_u32 m0, s38, 131072\n\t" \
  "ds_read_b128 v[136:139], v230 offset:2048\n\t" \
  "v_mfma_f32_16x16x32_bf16 v[56:59], a[12:15], v[148:151], v[56:59]\n\t" \
  "buffer_load_dwordx4 v242, s[16:19], s97 offen offset:0 sc1 lds\n\t" \
  "v_mfma_f32_16x16x32_bf16 v[56:59], a[4:7], v[144:147], v[56:59]\n\t" \
  "s_waitcnt lgkmcnt(3)\n\t" \
  "ds_read_b128 v[140:143], v230 offset:3072\n\t" \
  "v_mfma_f32_16x16x32_bf16 v[44:47], a[0:3], v[160:163], v[44:47]\n\t" \
  "ds_read_b128 v[144:147], v230 offset:4096\n\t" \
  "v_mfma_f32_16x16x32_bf16 v[44:47], a[8:11], v[164:167], v[44:47]\n\t" \
  "buffer_load_dwordx4 v242, s[16:19], s97 offen offset:1024 sc1 lds\n\t" \
  "v_mfma_f32_16x16x32_bf16 v[44:47], a[16:19], v[168:171], v[44:47]\n\t" \
  "ds_read_b128 v[148:151], v230 offset:5120\n\t" \
  "v_mfma_f32_16x16x32_bf16 v[44:47], a[24:27], v[172:175], v[44:47]\n\t" \
  "ds_read_b128 v[152:155], v230 offset:6144\n\t" \
  "v_mfma_f32_16x16x32_bf16 v[60:63], a[28:31], v[172:175], v[60:63]\n\t" \
  "buffer_load_dwordx4 v242, s[16:19], s97 offen offset:2048 sc1 lds\n\t" \
  "v_mfma_f32_16x16x32_bf16 v[60:63], a[20:23], v[168:171], v[60:63]\n\t" \
  "ds_read_b128 v[156:159], v230 offset:7168\n\t" \
  "v_mfma_f32_16x16x32_bf16 v[60:63], a[12:15], v[164:167], v[60:63]\n\t" \
  "v_mfma_f32_16x16x32_bf16 v[60:63], a[4:7], v[160:163], v[60:63]\n\t" \
  "s_waitcnt lgkmcnt(4)\n\t" \
  "v_mfma_f32_16x16x32_bf16 v[0:3], a[96:99], v[128:131], v[0:3]\n\t" \
  "buffer_load_dwordx4 v242, s[16:19], s97 offen offset:3072 sc1 lds\n\t" \
  "v_mfma_f32_16x16x32_bf16 v[0:3], a[104:107], v[132:135], v[0:3]\n\t" \
  "s_add_u32 s97, s64, 655360\n\t" \
  "ds_read_b128 v[160:163], v230 offset:8192\n\t" \
  "s_and_b32 s97, s97, 0xe0000\n\t" \
  "v_mfma_f32_16x16x32_bf16 v[0:3], a[112:115], v[136:139], v[0:3]\n\t" \
  "s_add_u32 s97, s97, s61\n\t" \
  "ds_read_b128 v[164:167], v230 offset:9216\n\t" \
  "s_add_u32 m0, s38, 16384\n\t" \
  "v_mfma_f32_16x16x32_bf16 v[0:3], a[120:123], v[140:143], v[0:3]\n\t" \
  "ds_read_b128 v[168:171], v230 offset:10240\n\t" \
  "v_mfma_f32_16x16x32_bf16 v[16:19], a[124:127], v[140:143], v[16:19]\n\t" \
  "buffer_load_dwordx4 v242, s[16:19], s97 offen offset:0 sc1 lds\n\t" \
  "v_mfma_f32_16x16x32_bf16 v[16:19], a[116:119], v[136:139], v[16:19]\n\t" \
  "ds_read_b128 v[172:175], v230 offset:11264\n\t" \
  "v_mfma_f32_16x16x32_bf16 v[16:19], a[108:111], v[132:135], v[16:19]\n\t" \
  "v_mfma_f32_16x16x32_bf16 v[16:19], a[100:103], v[128:131], v[16:19]\n\t" \
  "s_waitcnt lgkmcnt(4)\n\t" \
  "buffer_load_dword v246, v245, s[16:19], s65 offen sc1\n\t" \
  "v_mfma_f32_16x16x32_bf16 v[4:7], a[96:99], v[144:147], v[4:7]\n\t" \
  "ds_read_b128 v[128:131], v230 offset:12288\n\t" \
  "v_mfma_f32_16x16x32_bf16 v[4:7], a[104:107], v[148:151], v[4:7]\n\t" \
  "ds_read_b128 v[132:135], v230 offset:13312\n\t" \
  "v_mfma_f32_16x16x32_bf16 v[4:7], a[112:115], v[152:155], v[4:7]\n\t" \
  "ds_read_b128 v[136:139], v230 offset:14336\n\t" \
  "v_mfma_f32_16x16x32_bf16 v[4:7], a[120:123], v[156:159], v[4:7]\n\t" \
  "buffer_load_dwordx4 v242, s[16:19], s97 offen offset:1024 sc1 lds\n\t" \
  "v_mfma_f32_16x16x32_bf16 v[20:23], a[124:127], v[156:159], v[20:23]\n\t" \
  "ds_read_b128 v[140:143], v230 offset:15360\n\t" \
  "v_mfma_f32_16x16x32_bf16 v[20:23], a[116:119], v[152:155], v[20:23]\n\t" \
  "v_mfma_f32_16x16x32_bf16 v[20:23], a[108:111], v[148:151], v[20:23]\n\t" \
  "s_waitcnt vmcnt(7) lgkmcnt(4)\n\t" \
  "buffer_load_dwordx4 v242, s[16:19], s97 offen offset:2048 sc1 lds\n\t" \
  "v_mfma_f32_16x16x32_bf16 v[20:23], a[100:103], v[144:147], v[20:23]\n\t" \
  "s_barrier\n\t" \
  "v_mfma_f32_16x16x32_bf16 v[8:11], a[96:99], v[160:163], v[8:11]\n\t" \
  "ds_read_b64_tr_b16 a[192:193], v234 offset:49152\n\t" \
  "ds_read_b128 v[144:147], v232 offset:16384\n\t" \
  "ds_read_b128 v[148:151], v232 offset:17408\n\t" \
  "v_mfma_f32_16x16x32_bf16 v[8:11], a[104:107], v[164:167], v[8:11]\n\t" \
  "buffer_load_dwordx4 v242, s[16:19], s97 offen offset:3072 sc1 lds\n\t" \
  "v_mfma_f32_16x16x32_bf16 v[8:11], a[112:115], v[168:171], v[8:11]\n\t" \
  "s_add_u32 s97, s64, 786432\n\t" \
  "s_and_b32 s97, s97, 0xe0000\n\t" \
  "ds_read_b128 v[152:155], v232 offset:18432\n\t" \
  "s_add_u32 s97, s97, s61\n\t" \
  "v_mfma_f32_16x16x32_bf16 v[8:11], a[120:123], v[172:175], v[8:11]\n\t" \
  "s_add_u32 m0, s38, 0\n\t" \
  "ds_read_b128 v[156:159], v232 offset:19456\n\t" \
  "v_mfma_f32_16x16x32_bf16 v[24:27], a[124:127], v[172:175], v[24:27]\n\t" \
  "buffer_load_dwordx4 v242, s[16:19], s97 offen offset:0 sc1 lds\n\t" \
  "v_mfma_f32_16x16x32_bf16 v[24:27], a[116:119], v[168:171], v[24:27]\n\t" \
  "ds_read_b64_tr_b16 a[194:195], v234 offset:53248\n\t" \
  "v_mfma_f32_16x16x32_bf16 v[24:27], a[108:111], v[164:167], v[24:27]\n\t" \
  "ds_read_b64_tr_b16 a[196:197], v235 offset:49152\n\t" \
  "v_mfma_f32_16x16x32_bf16 v[24:27], a[100:103], v[160:163], v[24:27]\n\t" \
  "s_waitcnt lgkmcnt(7)\n\t" \
  "buffer_load_dwordx4 v242, s[16:19], s97 offen offset:1024 sc1 lds\n\t" \
  "v_mfma_f32_16x16x32_bf16 v[12:15], a[96:99], v[128:131], v[12:15]\n\t" \
  "ds_read_b64_tr_b16 a[198:199], v235 offset:53248\n\t" \
  "ds_read_b128 v[160:163], v232 offset:20480\n\t" \
  "v_mfma_f32_16x16x32_bf16 v[12:15], a[104:107], v[132:135], v[12:15]\n\t" \
  "ds_read_b128 v[164:167], v232 offset:21504\n\t" \
  "v_mfma_f32_16x16x32_bf16 v[12:15], a[112:115], v[136:139], v[12:15]\n\t" \
  "buffer_load_dwordx4 v242, s[16:19], s97 offen offset:2048 sc1 lds\n\t" \
  "v_mfma_f32_16x16x32_bf16 v[12:15], a[120:123], v[140:143], v[12:15]\n\t" \
  "ds_read_b64_tr_b16 a[200:201], v234 offset:57344\n\t" \
  "ds_read_b128 v[168:171], v232 offset:22528\n\t" \
  "v_mfma_f32_16x16x32_bf16 v[28:31], a[124:127], v[140:143], v[28:31]\n\t" \
  "ds_read_b128 v[172:175], v232 offset:23552\n\t" \
  "v_mfma_f32_16x16x32_bf16 v[28:31], a[116:119], v[136:139], v[28:31]\n\t" \
  "buffer_load_dwordx4 v242, s[16:19], s97 offen offset:3072 sc1 lds\n\t" \
  "v_mfma_f32_16x16x32_bf16 v[28:31], a[108:111], v[132:135], v[28:31]\n\t" \
  "s_add_u32 s97, s64, 917504\n\t" \
  "s_and_b32 s97, s97, 0xe0000\n\t" \
  "s_add_u32 s97, s97, s61\n\t" \
  "v_mfma_f32_16x16x32_bf16 v[28:31], a[100:103], v[128:131], v[28:31]\n\t" \
  "s_waitcnt lgkmcnt(8)\n\t" \
  "ds_read_b64_tr_b16 a[202:203], v234 offset:61440\n\t" \
  "s_add_u32 m0, s38, 32768\n\t" \
  "v_mfma_f32_16x16x32_bf16 v[32:35], a[32:35], v[144:147], v[32:35]\n\t" \
  "buffer_load_dwordx4 v242, s[16:19], s97 offen offset:0 sc1 lds\n\t" \
  "ds_read_b128 v[128:131], v232 offset:24576\n\t" \
  "v_mfma_f32_16x16x32_bf16 v[32:35], a[40:43], v[148:151], v[32:35]\n\t" \
  "ds_read_b64_tr_b16 a[204:205], v235 offset:57344\n\t" \
  "ds_read_b128 v[132:135], v232 offset:25600\n\t" \
  "v_mfma_f32_16x16x32_bf16 v[32:35], a[48:51], v[152:155], v[32:35]\n\t" \
  "ds_read_b128 v[136:139], v232 offset:26624\n\t" \
  "v_mfma_f32_16x16x32_bf16 v[32:35], a[56:59], v[156:159], v[32:35]\n\t" \
  "buffer_load_dwordx4 v242, s[16:19], s97 offen offset:1024 sc1 lds\n\t" \
  "v_mfma_f32_16x16x32_bf16 v[48:51], a[60:63], v[156:159], v[48:51]\n\t" \
  "ds_read_b128 v[140:143], v232 offset:27648\n\t" \
  "v_mfma_f32_16x16x32_bf16 v[48:51], a[52:55], v[152:155], v[48:51]\n\t" \
  "ds_read_b64_tr_b16 a[206:207], v235 offset:61440\n\t" \
  "v_mfma_f32_16x16x32_bf16 v[48:51], a[44:47], v[148:151], v[48:51]\n\t" \
  "buffer_load_dwordx4 v242, s[16:19], s97 offen offset:2048 sc1 lds\n\t" \
  "v_mfma_f32_16x16x32_bf16 v[48:51], a[36:39], v[144:147], v[48:51]\n\t" \
  "s_waitcnt lgkmcnt(7)\n\t" \
  "ds_read_b64_tr_b16 a[208:209], v236 offset:16384\n\t" \
  "v_mfma_f32_16x16x32_bf16 v[36:39], a[32:35], v[160:163], v[36:39]\n\t" \
  "ds_read_b64_tr_b16 a[210:211], v236 offset:20480\n\t" \
  "ds_read_b128 v[144:147], v232 offset:28672\n\t" \
  "ds_read_b128 v[148:151], v232 offset:29696\n\t" \
  "v_mfma_f32_16x16x32_bf16 v[36:39], a[40:43], v[164:167], v[36:39]\n\t" \
  "buffer_load_dwordx4 v242, s[16:19], s97 offen offset:3072 sc1 lds\n\t" \
  "v_mfma_f32_16x16x32_bf16 v[36:39], a[48:51], v[168:171], v[36:39]\n\t" \
  "s_add_u32 s97, s64, 131072\n\t" \
  "ds_read_b128 v[152:155], v232 offset:30720\n\t" \
  "s_and_b32 s97, s97, 0xe0000\n\t" \
  "v_mfma_f32_16x16x32_bf16 v[36:39], a[56:59], v[172:175], v[36:39]\n\t" \
  "s_add_u32 s97, s97, s62\n\t" \
  "ds_read_b128 v[156:159], v232 offset:31744\n\t" \
  "s_add_u32 m0, s38, 65536\n\t" \
  "v_mfma_f32_16x16x32_bf16 v[52:55], a[60:63], v[172:175], v[52:55]\n\t" \
  "ds_read_b64_tr_b16 a[212:213], v237 offset:16384\n\t" \
  "v_mfma_f32_16x16x32_bf16 v[52:55], a[52:55], v[168:171], v[52:55]\n\t" \
  "s_waitcnt vmcnt(11)\n\t" \
  "v_cmp_ne_u32 vcc, s36, v246\n\t" \
  "v_mfma_f32_16x16x32_bf16 v[52:55], a[44:47], v[164:167], v[52:55]\n\t" \
  "s_waitcnt lgkmcnt(7)\n\t" \
  "s_and_b32 vcc_lo, vcc_lo, 0xfe0000\n\t" \
  "s_cbranch_scc1 .Lat_s2\n\t" \
  ".Lat_k2:\n\t" \
  "v_mfma_f32_16x16x32_bf16 v[52:55], a[36:39], v[160:163], v[52:55]\n\t" \
  "s_barrier\n\t" \
  "v_mfma_f32_16x16x32_bf16 v[40:43], a[32:35], v[128:131], v[40:43]\n\t" \
  "buffer_load_dwordx4 v242, s[16:19], s97 offen offset:0 sc1 lds\n\t" \
  "ds_read_b128 v[160:163], v232 offset:0\n\t" \
  "v_mfma_f32_16x16x32_bf16 v[40:43], a[40:43], v[132:135], v[40:43]\n\t" \
  "ds_read_b128 v[164:167], v232 offset:1024\n\t" \
  "ds_read_b64_tr_b16 a[214:215], v237 offset:20480\n\t" \
  "v_mfma_f32_16x16x32_bf16 v[40:43], a[48:51], v[136:139], v[40:43]\n\t" \
  "ds_read_b64_tr_b16 a[216:217], v236 offset:24576\n\t" \
  "v_mfma_f32_16x16x32_bf16 v[40:43], a[56:59], v[140:143], v[40:43]\n\t" \
  "buffer_load_dwordx4 v242, s[16:19], s97 offen offset:1024 sc1 lds\n\t" \
  "v_mfma_f32_16x16x32_bf16 v[56:59], a[60:63], v[140:143], v[56:59]\n\t" \
  "ds_read_b128 v[168:171], v232 offset:2048\n\t" \
  "v_mfma_f32_16x16x32_bf16 v[56:59], a[52:55], v[136:139], v[56:59]\n\t" \
  "ds_read_b64_tr_b16 a[218:219], v236 offset:28672\n\t" \
  "v_mfma_f32_16x16x32_bf16 v[56:59], a[44:47], v[132:135], v[56:59]\n\t" \
  "buffer_load_dwordx4 v242, s[16:19], s97 offen offset:2048 sc1 lds\n\t" \
  "v_mfma_f32_16x16x32_bf16 v[56:59], a[36:39], v[128:131], v[56:59]\n\t" \
  "s_waitcnt lgkmcnt(7)\n\t" \
  "ds_read_b128 v[172:175], v232 offset:3072\n\t" \
  "v_mfma_f32_16x16x32_bf16 v[44:47], a[32:35], v[144:147], v[44:47]\n\t" \
  "ds_read_b64_tr_b16 a[220:221], v237 offset:24576\n\t" \
  "v_mfma_f32_16x16x32_bf16 v[44:47], a[40:43], v[148:151], v[44:47]\n\t" \
  "buffer_load_dwordx4 v242, s[16:19], s97 offen offset:3072 sc1 lds\n\t" \
  "v_mfma_f32_16x16x32_bf16 v[44:47], a[48:51], v[152:155], v[44:47]\n\t" \
  "s_add_u32 s97, s64, 262144\n\t" \
  "ds_read_b128 v[128:131], v232 offset:4096\n\t" \
  "s_and_b32 s97, s97, 0xe0000\n\t" \
  "v_mfma_f32_16x16x32_bf16 v[44:47], a[56:59], v[156:159], v[44:47]\n\t" \
  "s_add_u32 s97, s97, s62\n\t" \
  "ds_read_b128 v[132:135], v232 offset:5120\n\t" \
  "ds_read_b64_tr_b16 a[222:223], v237 offset:28672\n\t" \
  "s_add_u32 m0, s38, 49152\n\t" \
  "v_mfma_f32_16x16x32_bf16 v[60:63], a[60:63], v[156:159], v[60:63]\n\t" \
  "ds_read_b128 v[136:139], v232 offset:6144\n\t" \
  "v_mfma_f32_16x16x32_bf16 v[60:63], a[52:55], v[152:155], v[60:63]\n\t" \
  "buffer_load_dwordx4 v242, s[16:19], s97 offen offset:0 sc1 lds\n\t" \
  "v_mfma_f32_16x16x32_bf16 v[60:63], a[44:47], v[148:151], v[60:63]\n\t" \
  "ds_read_b128 v[140:143], v232 offset:7168\n\t" \
  "v_mfma_f32_16x16x32_bf16 v[60:63], a[36:39], v[144:147], v[60:63]\n\t" \
  "s_waitcnt lgkmcnt(2)\n\t" \
  "v_mfma_f32_16x16x32_bf16 v[0:3], a[192:195], v[160:163], v[0:3]\n\t" \
  "buffer_load_dwordx4 v242, s[16:19], s97 offen offset:1024 sc1 lds\n\t" \
  "v_mfma_f32_16x16x32_bf16 v[0:3], a[200:203], v[164:167], v[0:3]\n\t" \
  "ds_read_b128 v[144:147], v232 offset:8192\n\t" \
  "v_mfma_f32_16x16x32_bf16 v[0:3], a[208:211], v[168:171], v[0:3]\n\t" \
  "ds_read_b128 v[148:151], v232 offset:9216\n\t" \
  "v_mfma_f32_16x16x32_bf16 v[0:3], a[216:219], v[172:175], v[0:3]\n\t" \
  "ds_read_b128 v[152:155], v232 offset:10240\n\t" \
  "v_mfma_f32_16x16x32_bf16 v[16:19], a[220:223], v[172:175], v[16:19]\n\t" \
  "buffer_load_dwordx4 v242, s[16:19], s97 offen offset:2048 sc1 lds\n\t" \
  "v_mfma_f32_16x16x32_bf16 v[16:19], a[212:215], v[168:171], v[16:19]\n\t" \
  "ds_read_b128 v[156:159], v232 offset:11264\n\t" \
  "v_mfma_f32_16x16x32_bf16 v[16:19], a[204:207], v[164:167], v[16:19]\n\t" \
  "v_mfma_f32_16x16x32_bf16 v[16:19], a[196:199], v[160:163], v[16:19]\n\t" \
  "s_waitcnt lgkmcnt(4)\n\t" \
  "v_cvt_pk_bf16_f32 v208, v0, v1\n\t" \
  "v_cvt_pk_bf16_f32 v209, v2, v3\n\t" \
  "v_mfma_f32_16x16x32_bf16 v[4:7], a[192:195], v[128:131], v[4:7]\n\t" \
  "buffer_load_dwordx4 v242, s[16:19], s97 offen offset:3072 sc1 lds\n\t" \
  "v_mfma_f32_16x16x32_bf16 v[4:7], a[200:203], v[132:135], v[4:7]\n\t" \
  "s_add_u32 s97, s64, 393216\n\t" \
  "s_and_b32 s97, s97, 0xe0000\n\t" \
  "s_add_u32 s97, s97, s62\n\t" \
  "v_mfma_f32_16x16x32_bf16 v[4:7], a[208:211], v[136:139], v[4:7]\n\t" \
  "s_add_u32 m0, s38, 98304\n\t" \
  "ds_read_b128 v[160:163], v232 offset:12288\n\t" \
  "v_cvt_pk_bf16_f32 v210, v16, v17\n\t" \
  "v_mfma_f32_16x16x32_bf16 v[4:7], a[216:219], v[140:143], v[4:7]\n\t" \
  "buffer_load_dwordx4 v242, s[16:19], s97 offen offset:0 sc1 lds\n\t" \
  "v_mfma_f32_16x16x32_bf16 v[20:23], a[220:223], v[140:143], v[20:23]\n\t" \
  "ds_read_b128 v[164:167], v232 offset:13312\n\t" \
  "v_cvt_pk_bf16_f32 v211, v18, v19\n\t" \
  "v_mfma_f32_16x16x32_bf16 v[20:23], a[212:215], v[136:139], v[20:23]\n\t" \
  "ds_read_b128 v[168:171], v232 offset:14336\n\t" \
  "v_mfma_f32_16x16x32_bf16 v[20:23], a[204:207], v[132:135], v[20:23]\n\t" \
  "s_waitcnt vmcnt(17) lgkmcnt(3)\n\t" \
  "ds_read_b128 v[172:175], v232 offset:15360\n\t" \
  "v_mfma_f32_16x16x32_bf16 v[20:23], a[196:199], v[128:131], v[20:23]\n\t" \
  "s_barrier\n\t" \
  "v_mfma_f32_16x16x32_bf16 v[8:11], a[192:195], v[144:147], v[8:11]\n\t" \
  "buffer_store_dwordx4 v[208:211], v242, s[16:19], s67 offen offset:0 sc1\n\t" \
  "ds_read_b128 v[128:131], v242 offset:16384\n\t" \
  "v_cvt_pk_bf16_f32 v212, v4, v5\n\t" \
  "v_cvt_pk_bf16_f32 v213, v6, v7\n\t" \
  "v_mfma_f32_16x16x32_bf16 v[8:11], a[200:203], v[148:151], v[8:11]\n\t" \
  "ds_read_b128 v[132:135], v242 offset:17408\n\t" \
  "v_cvt_pk_bf16_f32 v214, v20, v21\n\t" \
  "v_mfma_f32_16x16x32_bf16 v[8:11], a[208:211], v[152:155], v[8:11]\n\t" \
  "ds_read_b128 v[136:139], v242 offset:18432\n\t" \
  "v_cvt_pk_bf16_f32 v215, v22, v23\n\t" \
  "v_mfma_f32_16x16x32_bf16 v[8:11], a[216:219], v[156:159], v[8:11]\n\t" \
  "ds_read_b128 v[140:143], v242 offset:19456\n\t" \
  "v_mfma_f32_16x16x32_bf16 v[24:27], a[220:223], v[156:159], v[24:27]\n\t" \
  "v_mfma_f32_16x16x32_bf16 v[24:27], a[212:215], v[152:155], v[24:27]\n\t" \
  "v_mfma_f32_16x16x32_bf16 v[24:27], a[204:207], v[148:151], v[24:27]\n\t" \
  "v_mfma_f32_16x16x32_bf16 v[24:27], a[196:199], v[144:147], v[24:27]\n\t" \
  "s_waitcnt lgkmcnt(4)\n\t" \
  "v_mfma_f32_16x16x32_bf16 v[12:15], a[192:195], v[160:163], v[12:15]\n\t" \
  "ds_read_b128 v[144:147], v242 offset:20480\n\t" \
  "v_cvt_pk_bf16_f32 v216, v8, v9\n\t" \
  "v_mfma_f32_16x16x32_bf16 v[12:15], a[200:203], v[164:167], v[12:15]\n\t" \
  "ds_read_b128 v[148:151], v242 offset:21504\n\t" \
  "v_cvt_pk_bf16_f32 v217, v10, v11\n\t" \
  "v_mfma_f32_16x16x32_bf16 v[12:15], a[208:211], v[168:171], v[12:15]\n\t" \
  "v_cvt_pk_bf16_f32 v218, v24, v25\n\t" \
  "v_cvt_pk_bf16_f32 v219, v26, v27\n\t" \
  "v_mfma_f32_16x16x32_bf16 v[12:15], a[216:219], v[172:175], v[12:15]\n\t" \
  "ds_read_b128 v[152:155], v242 offset:22528\n\t" \
  "v_mfma_f32_16x16x32_bf16 v[28:31], a[220:223], v[172:175], v[28:31]\n\t" \
  "buffer_store_dwordx4 v[212:215], v242, s[16:19], s67 offen offset:1024 sc1\n\t" \
  "ds_read_b128 v[156:159], v242 offset:23552\n\t" \
  "v_mfma_f32_16x16x32_bf16 v[28:31], a[212:215], v[168:171], v[28:31]\n\t" \
  "v_mfma_f32_16x16x32_bf16 v[28:31], a[204:207], v[164:167], v[28:31]\n\t" \
  "v_mfma_f32_16x16x32_bf16 v[28:31], a[196:199], v[160:163], v[28:31]\n\t" \
  "s_waitcnt lgkmcnt(4)\n\t" \
  "v_cvt_pk_bf16_f32 v220, v12, v13\n\t" \
  "v_cvt_pk_bf16_f32 v221, v14, v15\n\t" \
  "v_mfma_f32_16x16x32_bf16 v[32:35], a[64:67], v[128:131], v[32:35]\n\t" \
  "ds_read_b128 v[160:163], v242 offset:24576\n\t" \
  "v_mfma_f32_16x16x32_bf16 v[32:35], a[72:75], v[132:135], v[32:35]\n\t" \
  "ds_read_b128 v[164:167], v242 offset:25600\n\t" \
  "v_mfma_f32_16x16x32_bf16 v[32:35], a[80:83], v[136:139], v[32:35]\n\t" \
  "v_cvt_pk_bf16_f32 v222, v28, v29\n\t" \
  "v_cvt_pk_bf16_f32 v223, v30, v31\n\t" \
  "v_mfma_f32_16x16x32_bf16 v[32:35], a[88:91], v[140:143], v[32:35]\n\t" \
  "ds_read_b128 v[168:171], v242 offset:26624\n\t" \
  "v_mfma_f32_16x16x32_bf16 v[48:51], a[92:95], v[140:143], v[48:51]\n\t" \
  "ds_read_b128 v[172:175], v242 offset:27648\n\t" \
  "v_mfma_f32_16x16x32_bf16 v[48:51], a[84:87], v[136:139], v[48:51]\n\t" \
  "v_mfma_f32_16x16x32_bf16 v[48:51], a[76:79], v[132:135], v[48:51]\n\t" \
  "v_mfma_f32_16x16x32_bf16 v[48:51], a[68:71], v[128:131], v[48:51]\n\t" \
  "s_waitcnt lgkmcnt(4)\n\t" \
  "v_mfma_f32_16x16x32_bf16 v[36:39], a[64:67], v[144:147], v[36:39]\n\t" \
  "ds_read_b128 v[128:131], v242 offset:28672\n\t" \
  "v_mfma_f32_16x16x32_bf16 v[36:39], a[72:75], v[148:151], v[36:39]\n\t" \
  "buffer_store_dwordx4 v[216:219], v242, s[16:19], s67 offen offset:2048 sc1\n\t" \
  "ds_read_b128 v[132:135], v242 offset:29696\n\t" \
  "ds_read_b128 v[136:139], v242 offset:30720\n\t" \
  "ds_read_b128 v[140:143], v242 offset:31744\n\t" \
  "v_mfma_f32_16x16x32_bf16 v[36:39], a[80:83], v[152:155], v[36:39]\n\t" \
  "v_mfma_f32_16x16x32_bf16 v[36:39], a[88:91], v[156:159], v[36:39]\n\t" \
  "v_mfma_f32_16x16x32_bf16 v[52:55], a[92:95], v[156:159], v[52:55]\n\t" \
  "v_mfma_f32_16x16x32_bf16 v[52:55], a[84:87], v[152:155], v[52:55]\n\t" \
  "v_mfma_f32_16x16x32_bf16 v[52:55], a[76:79], v[148:151], v[52:55]\n\t" \
  "s_waitcnt vmcnt(16) lgkmcnt(4)\n\t" \
  "v_mfma_f32_16x16x32_bf16 v[52:55], a[68:71], v[144:147], v[52:55]\n\t" \
  "s_barrier\n\t" \
  "v_mfma_f32_16x16x32_bf16 v[40:43], a[64:67], v[160:163], v[40:43]\n\t" \
  "ds_read_b128 v[144:147], v242 offset:0\n\t" \
  "v_mfma_f32_16x16x32_bf16 v[40:43], a[72:75], v[164:167], v[40:43]\n\t" \
  "ds_read_b128 v[148:151], v242 offset:1024\n\t" \
  "v_mfma_f32_16x16x32_bf16 v[40:43], a[80:83], v[168:171], v[40:43]\n\t" \
  "ds_read_b128 v[152:155], v242 offset:2048\n\t" \
  "v_mfma_f32_16x16x32_bf16 v[40:43], a[88:91], v[172:175], v[40:43]\n\t" \
  "ds_read_b128 v[156:159], v242 offset:3072\n\t" \
  "v_mfma_f32_16x16x32_bf16 v[56:59], a[92:95], v[172:175], v[56:59]\n\t" \
  "v_mfma_f32_16x16x32_bf16 v[56:59], a[84:87], v[168:171], v[56:59]\n\t" \
  "v_mfma_f32_16x16x32_bf16 v[56:59], a[76:79], v[164:167], v[56:59]\n\t" \
  "buffer_store_dwordx4 v[220:223], v242, s[16:19], s67 offen offset:3072 sc1\n\t" \
  "v_mfma_f32_16x16x32_bf16 v[56:59], a[68:71], v[160:163], v[56:59]\n\t" \
  "s_waitcnt lgkmcnt(4)\n\t" \
  "v_mfma_f32_16x16x32_bf16 v[44:47], a[64:67], v[128:131], v[44:47]\n\t" \
  "ds_read_b128 v[160:163], v242 offset:4096\n\t" \
  "v_mfma_f32_16x16x32_bf16 v[44:47], a[72:75], v[132:135], v[44:47]\n\t" \
  "ds_read_b128 v[164:167], v242 offset:5120\n\t" \
  "v_mfma_f32_16x16x32_bf16 v[44:47], a[80:83], v[136:139], v[44:47]\n\t" \
  "ds_read_b128 v[168:171], v242 offset:6144\n\t" \
  "v_mfma_f32_16x16x32_bf16 v[44:47], a[88:91], v[140:143], v[44:47]\n\t" \
  "ds_read_b128 v[172:175], v242 offset:7168\n\t" \
  "v_mfma_f32_16x16x32_bf16 v[60:63], a[92:95], v[140:143], v[60:63]\n\t" \
  "v_mfma_f32_16x16x32_bf16 v[60:63], a[84:87], v[136:139], v[60:63]\n\t" \
  "v_mfma_f32_16x16x32_bf16 v[60:63], a[76:79], v[132:135], v[60:63]\n\t" \
  "v_mfma_f32_16x16x32_bf16 v[60:63], a[68:71], v[128:131], v[60:63]\n\t" \
  "s_waitcnt lgkmcnt(4)\n\t" \
  "v_mfma_f32_16x16x32_bf16 v[32:35], a[96:99], v[144:147], v[32:35]\n\t" \
  "ds_read_b128 v[128:131], v242 offset:8192\n\t" \
  "v_mfma_f32_16x16x32_bf16 v[32:35], a[104:107], v[148:151], v[32:35]\n\t" \
  "ds_read_b128 v[132:135], v242 offset:9216\n\t" \
  "v_mfma_f32_16x16x32_bf16 v[32:35], a[112:115], v[152:155], v[32:35]\n\t" \
  "ds_read_b128 v[136:139], v242 offset:10240\n\t" \
  "v_mfma_f32_16x16x32_bf16 v[32:35], a[120:123], v[156:159], v[32:35]\n\t" \
  "buffer_load_dwordx4 v242, s[16:19], s97 offen offset:1024 sc1 lds\n\t" \
  "v_mfma_f32_16x16x32_bf16 v[48:51], a[124:127], v[156:159], v[48:51]\n\t" \
  "ds_read_b128 v[140:143], v242 offset:11264\n\t" \
  "v_mfma_f32_16x16x32_bf16 v[48:51], a[116:119], v[152:155], v[48:51]\n\t" \
  "v_mfma_f32_16x16x32_bf16 v[48:51], a[108:111], v[148:151], v[48:51]\n\t" \
  "v_mfma_f32_16x16x32_bf16 v[48:51], a[100:103], v[144:147], v[48:51]\n\t" \
  "s_waitcnt lgkmcnt(4)\n\t" \
  "buffer_load_dwordx4 v242, s[16:19], s97 offen offset:2048 sc1 lds\n\t" \
  "v_mfma_f32_16x16x32_bf16 v[36:39], a[96:99], v[160:163], v[36:39]\n\t" \
  "ds_read_b128 v[144:147], v242 offset:12288\n\t" \
  "v_mfma_f32_16x16x32_bf16 v[36:39], a[104:107], v[164:167], v[36:39]\n\t" \
  "ds_read_b128 v[148:151], v242 offset:13312\n\t" \
  "v_mfma_f32_16x16x32_bf16 v[36:39], a[112:115], v[168:171], v[36:39]\n\t" \
  "buffer_load_dwordx4 v242, s[16:19], s97 offen offset:3072 sc1 lds\n\t" \
  "v_mfma_f32_16x16x32_bf16 v[36:39], a[120:123], v[172:175], v[36:39]\n\t" \
  "s_add_u32 s97, s64, 524288\n\t" \
  "ds_read_b128 v[152:155], v242 offset:14336\n\t" \
  "s_and_b32 s97, s97, 0xe0000\n\t" \
  "v_mfma_f32_16x16x32_bf16 v[52:55], a[124:127], v[172:175], v[52:55]\n\t" \
  "s_add_u32 s97, s97, s62\n\t" \
  "ds_read_b128 v[156:159], v242 offset:15360\n\t" \
  "s_add_u32 m0, s38, 81920\n\t" \
  "v_mfma_f32_16x16x32_bf16 v[52:55], a[116:119], v[168:171], v[52:55]\n\t" \
  "v_mfma_f32_16x16x32_bf16 v[52:55], a[108:111], v[164:167], v[52:55]\n\t" \
  "s_waitcnt vmcnt(16) lgkmcnt(4)\n\t" \
  "buffer_load_dwordx4 v242, s[16:19], s97 offen offset:0 sc1 lds\n\t" \
  "v_mfma_f32_16x16x32_bf16 v[52:55], a[100:103], v[160:163], v[52:55]\n\t" \
  "s_barrier\n\t" \
  ".Lhs13:\n\ts_cbranch_execz .Lhs13n\n\t" \
  "v_mfma_f32_16x16x32_bf16 v[40:43], a[96:99], v[128:131], v[40:43]\n\t" \
  "ds_read_b128 v[160:163], v242 offset:32768\n\t" \
  "v_mfma_f32_16x16x32_bf16 v[40:43], a[104:107], v[132:135], v[40:43]\n\t" \
  "ds_read_b128 v[164:167], v242 offset:33792\n\t" \
  "v_mfma_f32_16x16x32_bf16 v[40:43], a[112:115], v[136:139], v[40:43]\n\t" \
  "buffer_load_dwordx4 v242, s[16:19], s97 offen offset:1024 sc1 lds\n\t" \
  "v_mfma_f32_16x16x32_bf16 v[40:43], a[120:123], v[140:143], v[40:43]\n\t" \
  "ds_read_b128 v[168:171], v242 offset:34816\n\t" \
  "v_mfma_f32_16x16x32_bf16 v[56:59], a[124:127], v[140:143], v[56:59]\n\t" \
  "ds_read_b128 v[172:175], v242 offset:35840\n\t" \
  "v_mfma_f32_16x16x32_bf16 v[56:59], a[116:119], v[136:139], v[56:59]\n\t" \
  "buffer_load_dwordx4 v242, s[16:19], s97 offen offset:2048 sc1 lds\n\t" \
  "v_mfma_f32_16x16x32_bf16 v[56:59], a[108:111], v[132:135], v[56:59]\n\t" \
  "v_mfma_f32_16x16x32_bf16 v[56:59], a[100:103], v[128:131], v[56:59]\n\t" \
  "s_waitcnt lgkmcnt(4)\n\t" \
  "v_mfma_f32_16x16x32_bf16 v[44:47], a[96:99], v[144:147], v[44:47]\n\t" \
  "ds_read_b128 v[128:131], v242 offset:36864\n\t" \
  "v_mfma_f32_16x16x32_bf16 v[44:47], a[104:107], v[148:151], v[44:47]\n\t" \
  "buffer_load_dwordx4 v242, s[16:19], s97 offen offset:3072 sc1 lds\n\t" \
  "v_mfma_f32_16x16x32_bf16 v[44:47], a[112:115], v[152:155], v[44:47]\n\t" \
  "s_add_u32 s97, s64, 655360\n\t" \
  "ds_read_b128 v[132:135], v242 offset:37888\n\t" \
  "s_and_b32 s97, s97, 0xe0000\n\t" \
  "v_mfma_f32_16x16x32_bf16 v[44:47], a[120:123], v[156:159], v[44:47]\n\t" \
  "s_add_u32 s97, s97, s62\n\t" \
  "ds_read_b128 v[136:139], v242 offset:38912\n\t" \
  "s_add_u32 m0, s38, 131072\n\t" \
  "v_mfma_f32_16x16x32_bf16 v[60:63], a[124:127], v[156:159], v[60:63]\n\t" \
  "ds_read_b128 v[140:143], v242 offset:39936\n\t" \
  "v_mfma_f32_16x16x32_bf16 v[60:63], a[116:119], v[152:155], v[60:63]\n\t" \
  "buffer_load_dwordx4 v242, s[16:19], s97 offen offset:0 sc1 lds\n\t" \
  "v_mfma_f32_16x16x32_bf16 v[60:63], a[108:111], v[148:151], v[60:63]\n\t" \
  "v_mfma_f32_16x16x32_bf16 v[60:63], a[100:103], v[144:147], v[60:63]\n\t" \
  "s_waitcnt lgkmcnt(4)\n\t" \
  "v_mfma_f32_16x16x32_bf16 v[32:35], a[192:195], v[160:163], v[32:35]\n\t" \
  "buffer_load_dwordx4 v242, s[16:19], s97 offen offset:1024 sc1 lds\n\t" \
  "v_mfma_f32_16x16x32_bf16 v[32:35], a[200:203], v[164:167], v[32:35]\n\t" \
  "ds_read_b128 v[144:147], v242 offset:40960\n\t" \
  "v_mfma_f32_16x16x32_bf16 v[32:35], a[208:211], v[168:171], v[32:35]\n\t" \
  "ds_read_b128 v[148:151], v242 offset:41984\n\t" \
  "v_mfma_f32_16x16x32_bf16 v[32:35], a[216:219], v[172:175], v[32:35]\n\t" \
  "ds_read_b128 v[152:155], v242 offset:43008\n\t" \
  "v_mfma_f32_16x16x32_bf16 v[48:51], a[220:223], v[172:175], v[48:51]\n\t" \
  "ds_read_b128 v[156:159], v242 offset:44032\n\t" \
  "v_mfma_f32_16x16x32_bf16 v[48:51], a[212:215], v[168:171], v[48:51]\n\t" \
  "v_mfma_f32_16x16x32_bf16 v[48:51], a[204:207], v[164:167], v[48:51]\n\t" \
  "buffer_load_dwordx4 v242, s[16:19], s97 offen offset:2048 sc1 lds\n\t" \
  "v_mfma_f32_16x16x32_bf16 v[48:51], a[196:199], v[160:163], v[48:51]\n\t" \
  "s_waitcnt lgkmcnt(4)\n\t" \
  "v_cvt_pk_bf16_f32 v0, v32, v33\n\t" \
  "v_cvt_pk_bf16_f32 v1, v34, v35\n\t" \
  "v_mfma_f32_16x16x32_bf16 v[36:39], a[192:195], v[128:131], v[36:39]\n\t" \
  "ds_read_b128 v[160:163], v242 offset:45056\n\t" \
  "v_mfma_f32_16x16x32_bf16 v[36:39], a[200:203], v[132:135], v[36:39]\n\t" \
  "ds_read_b128 v[164:167], v242 offset:46080\n\t" \
  "v_mfma_f32_16x16x32_bf16 v[36:39], a[208:211], v[136:139], v[36:39]\n\t" \
  "v_cvt_pk_bf16_f32 v2, v48, v49\n\t" \
  "v_cvt_pk_bf16_f32 v3, v50, v51\n\t" \
  "v_mfma_f32_16x16x32_bf16 v[36:39], a[216:219], v[140:143], v[36:39]\n\t" \
  "ds_read_b128 v[168:171], v242 offset:47104\n\t" \
  "v_mfma_f32_16x16x32_bf16 v[52:55], a[220:223], v[140:143], v[52:55]\n\t" \
  "ds_read_b128 v[172:175], v242 offset:48128\n\t" \
  "v_mfma_f32_16x16x32_bf16 v[52:55], a[212:215], v[136:139], v[52:55]\n\t" \
  "v_mfma_f32_16x16x32_bf16 v[52:55], a[204:207], v[132:135], v[52:55]\n\t" \
  "s_waitcnt vmcnt(15) lgkmcnt(4)\n\t" \
  "v_mfma_f32_16x16x32_bf16 v[52:55], a[196:199], v[128:131], v[52:55]\n\t" \
  "s_barrier\n\t" \
  "v_mfma_f32_16x16x32_bf16 v[40:43], a[192:195], v[144:147], v[40:43]\n\t" \
  "ds_read_b128 v[128:131], v230 offset:0\n\t" \
  "v_cvt_pk_bf16_f32 v4, v36, v37\n\t" \
  "v_mfma_f32_16x16x32_bf16 v[40:43], a[200:203], v[148:151], v[40:43]\n\t" \
  "v_cvt_pk_bf16_f32 v5, v38, v39\n\t" \
  "v_and_b32 v235, 15, v233\n\t" \
  "v_mfma_f32_16x16x32_bf16 v[40:43], a[208:211], v[152:155], v[40:43]\n\t" \
  "v_cvt_pk_bf16_f32 v6, v52, v53\n\t" \
  "v_cvt_pk_bf16_f32 v7, v54, v55\n\t" \
  "v_mfma_f32_16x16x32_bf16 v[40:43], a[216:219], v[156:159], v[40:43]\n\t" \
  "ds_read_b128 v[132:135], v230 offset:1024\n\t" \
  "v_lshrrev_b32 v236, 4, v233\n\t" \
  "v_mfma_f32_16x16x32_bf16 v[56:59], a[220:223], v[156:159], v[56:59]\n\t" \
  "buffer_store_dwordx4 v[0:3], v242, s[16:19], s68 offen offset:0 sc1\n\t" \
  "ds_read_b128 v[136:139], v230 offset:2048\n\t" \
  "v_lshlrev_b32 v234, 13, v236\n\t" \
  "v_lshl_add_u32 v234, v235, 4, v234\n\t" \
  "v_mfma_f32_16x16x32_bf16 v[56:59], a[212:215], v[152:155], v[56:59]\n\t" \
  "v_lshlrev_b32 v240, 7, v236\n\t" \
  "v_lshl_add_u32 v240, v235, 3, v240\n\t" \
  "v_mfma_f32_16x16x32_bf16 v[56:59], a[204:207], v[148:151], v[56:59]\n\t" \
  "buffer_load_dwordx4 v242, s[16:19], s97 offen offset:3072 sc1 lds\n\t" \
  "v_mfma_f32_16x16x32_bf16 v[56:59], a[196:199], v[144:147], v[56:59]\n\t" \
  "s_waitcnt lgkmcnt(3)\n\t" \
  "ds_read_b128 v[140:143], v230 offset:3072\n\t" \
  "v_cvt_pk_bf16_f32 v8, v40, v41\n\t" \
  "v_mfma_f32_16x16x32_bf16 v[44:47], a[192:195], v[160:163], v[44:47]\n\t" \
  "ds_read_b128 v[144:147], v230 offset:4096\n\t" \
  "v_cvt_pk_bf16_f32 v9, v42, v43\n\t" \
  "v_mfma_f32_16x16x32_bf16 v[44:47], a[200:203], v[164:167], v[44:47]\n\t" \
  "s_waitcnt vmcnt(12)\n\t" \
  "buffer_store_dword v243, v244, s[16:19], s71 offen offset:0 sc1\n\t" \
  "ds_read_b128 v[148:151], v230 offset:5120\n\t" \
  "v_mfma_f32_16x16x32_bf16 v[44:47], a[208:211], v[168:171], v[44:47]\n\t" \
  "v_cvt_pk_bf16_f32 v10, v56, v57\n\t" \
  "v_cvt_pk_bf16_f32 v11, v58, v59\n\t" \
  "v_mfma_f32_16x16x32_bf16 v[44:47], a[216:219], v[172:175], v[44:47]\n\t" \
  "buffer_store_dwordx4 v[4:7], v242, s[16:19], s68 offen offset:1024 sc1\n\t" \
  "ds_read_b128 v[152:155], v230 offset:6144\n\t" \
  "v_add_u32 v240, s76, v240\n\t" \
  "v_lshlrev_b32 v235, 2, v235\n\t" \
  "v_mfma_f32_16x16x32_bf16 v[60:63], a[220:223], v[172:175], v[60:63]\n\t" \
  "ds_read_b128 v[156:159], v230 offset:7168\n\t" \
  "v_mfma_f32_16x16x32_bf16 v[60:63], a[212:215], v[168:171], v[60:63]\n\t" \
  "v_mfma_f32_16x16x32_bf16 v[60:63], a[204:207], v[164:167], v[60:63]\n\t" \
  "v_mfma_f32_16x16x32_bf16 v[60:63], a[196:199], v[160:163], v[60:63]\n\t" \
  "s_waitcnt lgkmcnt(4)\n\t" \
  "v_cvt_pk_bf16_f32 v12, v44, v45\n\t" \
  "v_cvt_pk_bf16_f32 v13, v46, v47\n\t" \
  STP("s_memrealtime s[94:95]\n\t") \
  STP("s_memtime s[92:93]\n\t") \
  "v_mfma_f32_16x16x32_bf16 v[64:67], a[128:131], v[128:131], v[64:67]\n\t" \
  "ds_read_b128 v[160:163], v230 offset:8192\n\t" \
  "v_mfma_f32_16x16x32_bf16 v[64:67], a[136:139], v[132:135], v[64:67]\n\t" \
  "ds_read_b128 v[164:167], v230 offset:9216\n\t" \
  "v_mfma_f32_16x16x32_bf16 v[64:67], a[144:147], v[136:139], v[64:67]\n\t" \
  "v_cvt_pk_bf16_f32 v14, v60, v61\n\t" \
  "v_cvt_pk_bf16_f32 v15, v62, v63\n\t" \
  "v_mfma_f32_16x16x32_bf16 v[64:67], a[152:155], v[140:143], v[64:67]\n\t" \
  "ds_read_b128 v[168:171], v230 offset:10240\n\t" \
  "v_mfma_f32_16x16x32_bf16 v[80:83], a[156:159], v[140:143], v[80:83]\n\t" \
  "ds_read_b128 v[172:175], v230 offset:11264\n\t" \
  "s_add_u32 s78, s75, 0\n\t" \
  "v_mfma_f32_16x16x32_bf16 v[80:83], a[148:151], v[136:139], v[80:83]\n\t" \
  "v_mfma_f32_16x16x32_bf16 v[80:83], a[140:143], v[132:135], v[80:83]\n\t" \
  "v_mfma_f32_16x16x32_bf16 v[80:83], a[132:135], v[128:131], v[80:83]\n\t" \
  "s_waitcnt lgkmcnt(4)\n\t" \
  "v_mfma_f32_16x16x32_bf16 v[68:71], a[128:131], v[144:147], v[68:71]\n\t" \
  "buffer_load_dwordx4 v[208:211], v234, s[16:19], s78 offen sc1\n\t" \
  "v_mfma_f32_16x16x32_bf16 v[68:71], a[136:139], v[148:151], v[68:71]\n\t" \
  "ds_read_b128 v[128:131], v230 offset:12288\n\t" \
  "v_mfma_f32_16x16x32_bf16 v[68:71], a[144:147], v[152:155], v[68:71]\n\t" \
  "ds_read_b128 v[132:135], v230 offset:13312\n\t" \
  "s_add_u32 s78, s75, 32768\n\t" \
  "v_mfma_f32_16x16x32_bf16 v[68:71], a[152:155], v[156:159], v[68:71]\n\t" \
  "ds_read_b128 v[136:139], v230 offset:14336\n\t" \
  "v_mfma_f32_16x16x32_bf16 v[84:87], a[156:159], v[156:159], v[84:87]\n\t" \
  STP("v_writelane_b32 v255, s94, 34\n\t") \
  STP("v_writelane_b32 v255, s92, 35\n\t") \
  "ds_read_b128 v[140:143], v230 offset:15360\n\t" \
  "v_mfma_f32_16x16x32_bf16 v[84:87], a[148:151], v[152:155], v[84:87]\n\t" \
  "v_mfma_f32_16x16x32_bf16 v[84:87], a[140:143], v[148:151], v[84:87]\n\t" \
  "buffer_load_dwordx4 v[212:215], v234, s[16:19], s78 offen sc1\n\t" \
  "v_mfma_f32_16x16x32_bf16 v[84:87], a[132:135], v[144:147], v[84:87]\n\t" \
  "s_waitcnt lgkmcnt(4)\n\t" \
  "s_add_u32 s78, s75, 65536\n\t" \
  "v_mfma_f32_16x16x32_bf16 v[72:75], a[128:131], v[160:163], v[72:75]\n\t" \
  "ds_read_b128 v[144:147], v242 offset:49152\n\t" \
  "v_mfma_f32_16x16x32_bf16 v[72:75], a[136:139], v[164:167], v[72:75]\n\t" \
  "ds_read_b128 v[148:151], v242 offset:50176\n\t" \
  "v_mfma_f32_16x16x32_bf16 v[72:75], a[144:147], v[168:171], v[72:75]\n\t" \
  "ds_read_b128 v[152:155], v242 offset:51200\n\t" \
  "v_mfma_f32_16x16x32_bf16 v[72:75], a[152:155], v[172:175], v[72:75]\n\t" \
  "buffer_load_dwordx4 v[216:219], v234, s[16:19], s78 offen sc1\n\t" \
  "v_mfma_f32_16x16x32_bf16 v[88:91], a[156:159], v[172:175], v[88:91]\n\t" \
  "ds_read_b128 v[156:159], v242 offset:52224\n\t" \
  "s_add_u32 s78, s75, 98304\n\t" \
  "v_mfma_f32_16x16x32_bf16 v[88:91], a[148:151], v[168:171], v[88:91]\n\t" \
  "v_mfma_f32_16x16x32_bf16 v[88:91], a[140:143], v[164:167], v[88:91]\n\t" \
  "v_mfma_f32_16x16x32_bf16 v[88:91], a[132:135], v[160:163], v[88:91]\n\t" \
  "s_waitcnt lgkmcnt(4)\n\t" \
  "v_mfma_f32_16x16x32_bf16 v[76:79], a[128:131], v[128:131], v[76:79]\n\t" \
  "buffer_load_dwordx4 v[220:223], v234, s[16:19], s78 offen sc1\n\t" \
  "v_mfma_f32_16x16x32_bf16 v[76:79], a[136:139], v[132:135], v[76:79]\n\t" \
  "ds_read_b128 v[160:163], v242 offset:53248\n\t" \
  "v_mfma_f32_16x16x32_bf16 v[76:79], a[144:147], v[136:139], v[76:79]\n\t" \
  "ds_read_b128 v[164:167], v242 offset:54272\n\t" \
  "v_mfma_f32_16x16x32_bf16 v[76:79], a[152:155], v[140:143], v[76:79]\n\t" \
  "ds_read_b128 v[168:171], v242 offset:55296\n\t" \
  "v_mfma_f32_16x16x32_bf16 v[92:95], a[156:159], v[140:143], v[92:95]\n\t" \
  "ds_read_b128 v[172:175], v242 offset:56320\n\t" \
  "v_mfma_f32_16x16x32_bf16 v[92:95], a[148:151], v[136:139], v[92:95]\n\t" \
  "buffer_load_dword v224, v235, s[32:35], s80 offen\n\t" \
  "v_mfma_f32_16x16x32_bf16 v[92:95], a[140:143], v[132:135], v[92:95]\n\t" \
  "v_lshrrev_b32 v250, 4, v233\n\t" \
  "v_and_b32 v238, 15, v233\n\t" \
  "v_mfma_f32_16x16x32_bf16 v[92:95], a[132:135], v[128:131], v[92:95]\n\t" \
  "s_waitcnt lgkmcnt(4)\n\t" \
  "v_xor_b32 v26, v238, v250\n\t" \
  "v_lshlrev_b32 v26, 4, v26\n\t" \
  "v_mfma_f32_16x16x32_bf16 v[64:67], a[160:163], v[144:147], v[64:67]\n\t" \
  "ds_read_b128 v[128:131], v242 offset:57344\n\t" \
  "v_lshl_add_u32 v26, v250, 11, v26\n\t" \
  "v_mfma_f32_16x16x32_bf16 v[64:67], a[168:171], v[148:151], v[64:67]\n\t" \
  "v_xor_b32 v27, 64, v26\n\t" \
  "v_xor_b32 v28, 0x80, v26\n\t" \
  "v_mfma_f32_16x16x32_bf16 v[64:67], a[176:179], v[152:155], v[64:67]\n\t" \
  "buffer_store_dwordx4 v[8:11], v242, s[16:19], s68 offen offset:2048 sc1\n\t" \
  "ds_read_b128 v[132:135], v242 offset:58368\n\t" \
  "v_xor_b32 v29, 0xc0, v26\n\t" \
  "v_lshrrev_b32 v30, 5, v233\n\t" \
  "v_mfma_f32_16x16x32_bf16 v[64:67], a[184:187], v[156:159], v[64:67]\n\t" \
  "v_lshl_add_u32 v30, s3, 2, v30\n\t" \
  "v_xor_b32 v30, v30, v238\n\t" \
  "v_mfma_f32_16x16x32_bf16 v[80:83], a[188:191], v[156:159], v[80:83]\n\t" \
  "ds_read_b128 v[136:139], v242 offset:59392\n\t" \
  "v_lshlrev_b32 v30, 4, v30\n\t" \
  "v_mfma_f32_16x16x32_bf16 v[80:83], a[180:183], v[152:155], v[80:83]\n\t" \
  "v_lshl_add_u32 v30, v238, 8, v30\n\t" \
  "v_and_b32 v250, 1, v250\n\t" \
  "v_mfma_f32_16x16x32_bf16 v[80:83], a[172:175], v[148:151], v[80:83]\n\t" \
  "ds_read_b128 v[140:143], v242 offset:60416\n\t" \
  "v_lshl_add_u32 v30, v250, 3, v30\n\t" \
  "v_mfma_f32_16x16x32_bf16 v[80:83], a[164:167], v[144:147], v[80:83]\n\t" \
  "s_waitcnt lgkmcnt(4)\n\t" \
  "v_add_u32 v238, 0, v30\n\t" \
  "v_xor_b32 v239, 32, v238\n\t" \
  "v_mfma_f32_16x16x32_bf16 v[68:71], a[160:163], v[160:163], v[68:71]\n\t" \
  "ds_read_b128 v[144:147], v242 offset:61440\n\t" \
  "v_bfe_u32 v237, v233, 1, 2\n\t" \
  "v_mfma_f32_16x16x32_bf16 v[68:71], a[168:171], v[164:167], v[68:71]\n\t" \
  "v_bfe_u32 v16, v233, 3, 1\n\t" \
  "v_lshlrev_b32 v237, 4, v237\n\t" \
  "v_mfma_f32_16x16x32_bf16 v[68:71], a[176:179], v[168:171], v[68:71]\n\t" \
  "s_add_u32 s97, s64, 786432\n\t" \
  "ds_read_b128 v[148:151], v242 offset:62464\n\t" \
  "v_lshl_add_u32 v237, v16, 3, v237\n\t" \
  "v_mfma_f32_16x16x32_bf16 v[68:71], a[184:187], v[172:175], v[68:71]\n\t" \
  "s_and_b32 s97, s97, 0xe0000\n\t" \
  "v_and_b32 v16, 1, v233\n\t" \
  "v_lshl_add_u32 v237, v16, 2, v237\n\t" \
  "v_mfma_f32_16x16x32_bf16 v[84:87], a[188:191], v[172:175], v[84:87]\n\t" \
  "s_add_u32 s97, s97, s62\n\t" \
  "ds_read_b128 v[152:155], v242 offset:63488\n\t" \
  "v_add_u32 v237, s76, v237\n\t" \
  "v_mfma_f32_16x16x32_bf16 v[84:87], a[180:183], v[168:171], v[84:87]\n\t" \
  "s_add_u32 m0, s38, 65536\n\t" \
  "v_cmp_eq_u32 vcc, 2, v236\n\t" \
  "v_mov_b32 v16, 0\n\t" \
  "v_mfma_f32_16x16x32_bf16 v[84:87], a[172:175], v[164:167], v[84:87]\n\t" \
  "s_waitcnt vmcnt(13) lgkmcnt(3)\n\t" \
  "ds_read_b128 v[156:159], v242 offset:64512\n\t" \
  "v_mov_b32 v17, 0\n\t" \
  "v_mfma_f32_16x16x32_bf16 v[84:87], a[164:167], v[160:163], v[84:87]\n\t" \
  "s_barrier\n\t" \
  "v_mfma_f32_16x16x32_bf16 v[72:75], a[160:163], v[128:131], v[72:75]\n\t" \
  "buffer_load_dwordx4 v242, s[16:19], s97 offen offset:0 sc1 lds\n\t" \
  "v_mfma_f32_16x16x32_bf16 v[72:75], a[168:171], v[132:135], v[72:75]\n\t" \
  "ds_read_b128 v[160:163], v230 offset:32768\n\t" \
  "v_mov_b32 v18, 0\n\t" \
  "v_mfma_f32_16x16x32_bf16 v[72:75], a[176:179], v[136:139], v[72:75]\n\t" \
  "ds_read_b128 v[164:167], v230 offset:33792\n\t" \
  "v_mov_b32 v19, 0\n\t" \
  "v_mfma_f32_16x16x32_bf16 v[72:75], a[184:187], v[140:143], v[72:75]\n\t" \
  "buffer_load_dwordx4 v242, s[16:19], s97 offen offset:1024 sc1 lds\n\t" \
  "v_mfma_f32_16x16x32_bf16 v[88:91], a[188:191], v[140:143], v[88:91]\n\t" \
  "ds_read_b128 v[168:171], v230 offset:34816\n\t" \
  "v_cndmask_b32 v236, 0, 1.0, vcc\n\t" \
  "v_mfma_f32_16x16x32_bf16 v[88:91], a[180:183], v[136:139], v[88:91]\n\t" \
  "v_mov_b32 v20, 0x3f803f80\n\t" \
  "v_add_u32 v21, s76, v242\n\t" \
  "v_mfma_f32_16x16x32_bf16 v[88:91], a[172:175], v[132:135], v[88:91]\n\t" \
  "ds_write_b128 v21, v[16:19] offset:0\n\t" \
  "v_mfma_f32_16x16x32_bf16 v[88:91], a[164:167], v[128:131], v[88:91]\n\t" \
  "s_waitcnt lgkmcnt(4)\n\t" \
  "buffer_load_dwordx4 v242, s[16:19], s97 offen offset:2048 sc1 lds\n\t" \
  "v_mfma_f32_16x16x32_bf16 v[76:79], a[160:163], v[144:147], v[76:79]\n\t" \
  "ds_read_b128 v[172:175], v230 offset:35840\n\t" \
  "v_mfma_f32_16x16x32_bf16 v[76:79], a[168:171], v[148:151], v[76:79]\n\t" \
  "ds_write_b128 v21, v[16:19] offset:1024\n\t" \
  "v_mfma_f32_16x16x32_bf16 v[76:79], a[176:179], v[152:155], v[76:79]\n\t" \
  "buffer_load_dwordx4 v242, s[16:19], s97 offen offset:3072 sc1 lds\n\t" \
  "v_mfma_f32_16x16x32_bf16 v[76:79], a[184:187], v[156:159], v[76:79]\n\t" \
  "ds_read_b128 v[128:131], v230 offset:36864\n\t" \
  "v_mfma_f32_16x16x32_bf16 v[92:95], a[188:191], v[156:159], v[92:95]\n\t" \
  "ds_read_b128 v[132:135], v230 offset:37888\n\t" \
  "v_mfma_f32_16x16x32_bf16 v[92:95], a[180:183], v[152:155], v[92:95]\n\t" \
  "ds_write_b128 v21, v[16:19] offset:1536\n\t" \
  "v_mfma_f32_16x16x32_bf16 v[92:95], a[172:175], v[148:151], v[92:95]\n\t" \
  "buffer_store_dwordx4 v[12:15], v242, s[16:19], s68 offen offset:3072 sc1\n\t" \
  "ds_read_b128 v[136:139], v230 offset:38912\n\t" \
  "ds_read_b128 v[140:143], v230 offset:39936\n\t" \
  "v_mfma_f32_16x16x32_bf16 v[92:95], a[164:167], v[144:147], v[92:95]\n\t" \
  "s_waitcnt lgkmcnt(6)\n\t" \
  "s_mov_b64 exec, 0xffff\n\t" \
  "ds_write_b32 v237, v20 offset:960\n\t" \
  "s_mov_b64 exec, -1\n\t" \
  "v_mfma_f32_16x16x32_bf16 v[64:67], a[0:3], v[160:163], v[64:67]\n\t" \
  "ds_read_b128 v[144:147], v230 offset:40960\n\t" \
  "v_mfma_f32_16x16x32_bf16 v[64:67], a[8:11], v[164:167], v[64:67]\n\t" \
  "ds_read_b128 v[148:151], v230 offset:41984\n\t" \
  "v_mfma_f32_16x16x32_bf16 v[64:67], a[16:19], v[168:171], v[64:67]\n\t" \
  "ds_read_b128 v[152:155], v230 offset:43008\n\t" \
  "v_mfma_f32_16x16x32_bf16 v[64:67], a[24:27], v[172:175], v[64:67]\n\t" \
  "ds_read_b128 v[156:159], v230 offset:44032\n\t" \
  "v_mfma_f32_16x16x32_bf16 v[80:83], a[28:31], v[172:175], v[80:83]\n\t" \
  "v_mfma_f32_16x16x32_bf16 v[80:83], a[20:23], v[168:171], v[80:83]\n\t" \
  "s_add_u32 m0, s38, 0\n\t" \
  "s_add_u32 s96, s50, 0\n\t" \
  "v_mfma_f32_16x16x32_bf16 v[80:83], a[12:15], v[164:167], v[80:83]\n\t" \
  "v_mfma_f32_16x16x32_bf16 v[80:83], a[4:7], v[160:163], v[80:83]\n\t" \
  "s_waitcnt lgkmcnt(5)\n\t" \
  "v_mfma_f32_16x16x32_bf16 v[68:71], a[0:3], v[128:131], v[68:71]\n\t" \
  "ds_read_b128 v[160:163], v230 offset:45056\n\t" \
  "v_mfma_f32_16x16x32_bf16 v[68:71], a[8:11], v[132:135], v[68:71]\n\t" \
  "ds_read_b128 v[164:167], v230 offset:46080\n\t" \
  "v_mfma_f32_16x16x32_bf16 v[68:71], a[16:19], v[136:139], v[68:71]\n\t" \
  "ds_read_b128 v[168:171], v230 offset:47104\n\t" \
  "v_mfma_f32_16x16x32_bf16 v[68:71], a[24:27], v[140:143], v[68:71]\n\t" \
  "buffer_load_dwordx4 v26, s[4:7], s96 offen offset:0 lds\n\t" \
  "v_mfma_f32_16x16x32_bf16 v[84:87], a[28:31], v[140:143], v[84:87]\n\t" \
  "s_add_u32 s96, s50, 7168\n\t" \
  "ds_read_b128 v[172:175], v230 offset:48128\n\t" \
  "v_mfma_f32_16x16x32_bf16 v[84:87], a[20:23], v[136:139], v[84:87]\n\t" \
  "v_mfma_f32_16x16x32_bf16 v[84:87], a[12:15], v[132:135], v[84:87]\n\t" \
  "v_mfma_f32_16x16x32_bf16 v[84:87], a[4:7], v[128:131], v[84:87]\n\t" \
  "s_waitcnt lgkmcnt(4)\n\t" \
  "v_mfma_f32_16x16x32_bf16 v[72:75], a[0:3], v[144:147], v[72:75]\n\t" \
  "buffer_load_dwordx4 v27, s[4:7], s96 offen offset:1024 lds\n\t" \
  "v_mfma_f32_16x16x32_bf16 v[72:75], a[8:11], v[148:151], v[72:75]\n\t" \
  "s_add_u32 s96, s50, 14336\n\t" \
  "ds_read_b128 v[128:131], v230 offset:16384\n\t" \
  "v_mfma_f32_16x16x32_bf16 v[72:75], a[16:19], v[152:155], v[72:75]\n\t" \
  "ds_read_b128 v[132:135], v230 offset:17408\n\t" \
  "v_mfma_f32_16x16x32_bf16 v[72:75], a[24:27], v[156:159], v[72:75]\n\t" \
  "ds_read_b128 v[136:139], v230 offset:18432\n\t" \
  "v_mfma_f32_16x16x32_bf16 v[88:91], a[28:31], v[156:159], v[88:91]\n\t" \
  "ds_read_b128 v[140:143], v230 offset:19456\n\t" \
  "v_mfma_f32_16x16x32_bf16 v[88:91], a[20:23], v[152:155], v[88:91]\n\t" \
  "buffer_load_dwordx4 v28, s[4:7], s96 offen offset:2048 lds\n\t" \
  "v_mfma_f32_16x16x32_bf16 v[88:91], a[12:15], v[148:151], v[88:91]\n\t" \
  "s_add_u32 s96, s50, 21504\n\t" \
  "v_mfma_f32_16x16x32_bf16 v[88:91], a[4:7], v[144:147], v[88:91]\n\t" \
  "s_waitcnt lgkmcnt(4)\n\t" \
  "v_mfma_f32_16x16x32_bf16 v[76:79], a[0:3], v[160:163], v[76:79]\n\t" \
  "ds_read_b128 v[144:147], v230 offset:20480\n\t" \
  "v_mfma_f32_16x16x32_bf16 v[76:79], a[8:11], v[164:167], v[76:79]\n\t" \
  "ds_read_b128 v[148:151], v230 offset:21504\n\t" \
  "v_mfma_f32_16x16x32_bf16 v[76:79], a[16:19], v[168:171], v[76:79]\n\t" \
  "buffer_load_dwordx4 v29, s[4:7], s96 offen offset:3072 lds\n\t" \
  "v_mfma_f32_16x16x32_bf16 v[76:79], a[24:27], v[172:175], v[76:79]\n\t" \
  "s_add_u32 s97, s64, 917504\n\t" \
  "ds_read_b128 v[152:155], v230 offset:22528\n\t" \
  "s_and_b32 s97, s97, 0xe0000\n\t" \
  "v_mfma_f32_16x16x32_bf16 v[92:95], a[28:31], v[172:175], v[92:95]\n\t" \
  "s_add_u32 s97, s97, s62\n\t" \
  "ds_read_b128 v[156:159], v230 offset:23552\n\t" \
  "s_add_u32 m0, s38, 16384\n\t" \
  "v_mfma_f32_16x16x32_bf16 v[92:95], a[20:23], v[168:171], v[92:95]\n\t" \
  "v_mfma_f32_16x16x32_bf16 v[92:95], a[12:15], v[164:167], v[92:95]\n\t" \
  "s_waitcnt vmcnt(10)\n\t" \
  "v_xor_b32 v246, v243, v209\n\t" \
  "v_bitop3_b32 v246, v246, v211, v243 bitop3:0xf6\n\t" \
  "v_mfma_f32_16x16x32_bf16 v[92:95], a[4:7], v[160:163], v[92:95]\n\t" \
  "s_waitcnt lgkmcnt(4)\n\t" \
  "v_bitop3_b32 v246, v246, v213, v243 bitop3:0xf6\n\t" \
  "v_bitop3_b32 v246, v246, v215, v243 bitop3:0xf6\n\t" \
  "v_mfma_f32_16x16x32_bf16 v[64:67], a[32:35], v[128:131], v[64:67]\n\t" \
  "v_bitop3_b32 v246, v246, v217, v243 bitop3:0xf6\n\t" \
  "v_bitop3_b32 v246, v246, v219, v243 bitop3:0xf6\n\t" \
  "v_mfma_f32_16x16x32_bf16 v[64:67], a[40:43], v[132:135], v[64:67]\n\t" \
  "v_bitop3_b32 v246, v246, v221, v243 bitop3:0xf6\n\t" \
  "v_bitop3_b32 v246, v246, v223, v243 bitop3:0xf6\n\t" \
  "v_mfma_f32_16x16x32_bf16 v[64:67], a[48:51], v[136:139], v[64:67]\n\t" \
  "buffer_load_dwordx4 v242, s[16:19], s97 offen offset:0 sc1 lds\n\t" \
  "v_mfma_f32_16x16x32_bf16 v[64:67], a[56:59], v[140:143], v[64:67]\n\t" \
  "ds_read_b128 v[160:163], v230 offset:24576\n\t" \
  "v_cmp_ne_u32 vcc, 0, v246\n\t" \
  "v_mfma_f32_16x16x32_bf16 v[80:83], a[60:63], v[140:143], v[80:83]\n\t" \
  "buffer_load_dwordx4 v242, s[16:19], s97 offen offset:1024 sc1 lds\n\t" \
  "v_mfma_f32_16x16x32_bf16 v[80:83], a[52:55], v[136:139], v[80:83]\n\t" \
  "s_cbranch_vccnz .Lat_s3\n\t" \
  ".Lat_k3:\n\t" \
  "v_mfma_f32_16x16x32_bf16 v[80:83], a[44:47], v[132:135], v[80:83]\n\t" \
  "ds_read_b128 v[164:167], v230 offset:25600\n\t" \
  "v_add_f32 v208, v208, v212\n\t" \
  "v_mfma_f32_16x16x32_bf16 v[80:83], a[36:39], v[128:131], v[80:83]\n\t" \
  "s_waitcnt lgkmcnt(2)\n\t" \
  "buffer_load_dwordx4 v242, s[16:19], s97 offen offset:2048 sc1 lds\n\t" \
  "v_mfma_f32_16x16x32_bf16 v[68:71], a[32:35], v[144:147], v[68:71]\n\t" \
  "ds_read_b128 v[168:171], v230 offset:26624\n\t" \
  "v_add_f32 v216, v216, v220\n\t" \
  "v_mfma_f32_16x16x32_bf16 v[68:71], a[40:43], v[148:151], v[68:71]\n\t" \
  "ds_read_b128 v[172:175], v230 offset:27648\n\t" \
  "v_add_f32 v210, v210, v214\n\t" \
  "v_mfma_f32_16x16x32_bf16 v[68:71], a[48:51], v[152:155], v[68:71]\n\t" \
  "ds_read_b128 v[128:131], v230 offset:28672\n\t" \
  "v_add_f32 v218, v218, v222\n\t" \
  "v_mfma_f32_16x16x32_bf16 v[68:71], a[56:59], v[156:159], v[68:71]\n\t" \
  "buffer_load_dwordx4 v242, s[16:19], s97 offen offset:3072 sc1 lds\n\t" \
  "v_mfma_f32_16x16x32_bf16 v[84:87], a[60:63], v[156:159], v[84:87]\n\t" \
  "s_add_u32 s97, s64, 131072\n\t" \
  "s_and_b32 s97, s97, 0xe0000\n\t" \
  "s_add_u32 s97, s97, s63\n\t" \
  "v_mfma_f32_16x16x32_bf16 v[84:87], a[52:55], v[152:155], v[84:87]\n\t" \
  "s_add_u32 m0, s38, 32768\n\t" \
  "ds_read_b128 v[132:135], v230 offset:29696\n\t" \
  "v_add_f32 v208, v208, v216\n\t" \
  "v_mfma_f32_16x16x32_bf16 v[84:87], a[44:47], v[148:151], v[84:87]\n\t" \
  "s_waitcnt vmcnt(9) lgkmcnt(2)\n\t" \
  "buffer_load_dwordx4 v242, s[16:19], s97 offen offset:0 sc1 lds\n\t" \
  "v_mfma_f32_16x16x32_bf16 v[84:87], a[36:39], v[144:147], v[84:87]\n\t" \
  "s_barrier\n\t" \
  "v_mfma_f32_16x16x32_bf16 v[72:75], a[32:35], v[160:163], v[72:75]\n\t" \
  "ds_read_b128 v[136:139], v230 offset:30720\n\t" \
  "v_add_f32 v210, v210, v218\n\t" \
  "v_mfma_f32_16x16x32_bf16 v[72:75], a[40:43], v[164:167], v[72:75]\n\t" \
  "ds_read_b128 v[140:143], v230 offset:31744\n\t" \
  "v_lshlrev_b32 v19, 16, v224\n\t" \
  "v_mfma_f32_16x16x32_bf16 v[72:75], a[48:51], v[168:171], v[72:75]\n\t" \
  "ds_read_b128 v[144:147], v232 offset:0\n\t" \
  "v_and_b32 v20, 0xffff0000, v224\n\t" \
  "v_mfma_f32_16x16x32_bf16 v[72:75], a[56:59], v[172:175], v[72:75]\n\t" \
  "buffer_load_dwordx4 v242, s[16:19], s97 offen offset:1024 sc1 lds\n\t" \
  "v_mfma_f32_16x16x32_bf16 v[88:91], a[60:63], v[172:175], v[88:91]\n\t" \
  "ds_read_b128 v[148:151], v232 offset:1024\n\t" \
  "v_cvt_pk_bf16_f32 v18, v208, v210\n\t" \
  "v_mfma_f32_16x16x32_bf16 v[88:91], a[52:55], v[168:171], v[88:91]\n\t" \
  "ds_read_b128 v[152:155], v232 offset:2048\n\t" \
  "v_lshlrev_b32 v16, 16, v18\n\t" \
  "v_mfma_f32_16x16x32_bf16 v[88:91], a[44:47], v[164:167], v[88:91]\n\t" \
  "ds_read_b128 v[156:159], v232 offset:3072\n\t" \
  "v_and_b32 v17, 0xffff0000, v18\n\t" \
  "v_mfma_f32_16x16x32_bf16 v[88:91], a[36:39], v[160:163], v[88:91]\n\t" \
  "s_waitcnt lgkmcnt(4)\n\t" \
  "v_add_f32 v16, v16, v236\n\t" \
  "v_add_f32 v17, v17, v236\n\t" \
  "v_mfma_f32_16x16x32_bf16 v[76:79], a[32:35], v[128:131], v[76:79]\n\t" \
  "ds_read_b128 v[160:163], v232 offset:4096\n\t" \
  "v_cvt_pk_bf16_f32 v18, v16, v17\n\t" \
  "v_mfma_f32_16x16x32_bf16 v[76:79], a[40:43], v[132:135], v[76:79]\n\t" \
  "v_lshlrev_b32 v16, 16, v18\n\t" \
  "v_and_b32 v17, 0xffff0000, v18\n\t" \
  "v_mfma_f32_16x16x32_bf16 v[76:79], a[48:51], v[136:139], v[76:79]\n\t" \
  "ds_read_b128 v[164:167], v232 offset:5120\n\t" \
  "v_mfma_f32_16x16x32_bf16 v[76:79], a[56:59], v[140:143], v[76:79]\n\t" \
  "ds_write_b64 v240, v[16:17] offset:2560\n\t" \
  "v_mfma_f32_16x16x32_bf16 v[92:95], a[60:63], v[140:143], v[92:95]\n\t" \
  "buffer_load_dwordx4 v242, s[16:19], s97 offen offset:2048 sc1 lds\n\t" \
  "v_mfma_f32_16x16x32_bf16 v[92:95], a[52:55], v[136:139], v[92:95]\n\t" \
  "ds_read_b128 v[168:171], v232 offset:6144\n\t" \
  "v_mul_f32 v22, v16, v19\n\t" \
  "v_mfma_f32_16x16x32_bf16 v[92:95], a[44:47], v[132:135], v[92:95]\n\t" \
  "ds_read_b128 v[172:175], v232 offset:7168\n\t" \
  "v_mul_f32 v23, v17, v20\n\t" \
  "v_mfma_f32_16x16x32_bf16 v[92:95], a[36:39], v[128:131], v[92:95]\n\t" \
  "s_waitcnt lgkmcnt(5)\n\t" \
  "v_cvt_pk_bf16_f32 v24, v22, v23\n\t" \
  "v_add_f32 v18, v22, v23\n\t" \
  "v_mfma_f32_16x16x32_bf16 v[64:67], a[64:67], v[144:147], v[64:67]\n\t" \
  "ds_read_b128 v[128:131], v232 offset:8192\n\t" \
  "v_lshlrev_b32 v19, 16, v24\n\t" \
  "v_mfma_f32_16x16x32_bf16 v[64:67], a[72:75], v[148:151], v[64:67]\n\t" \
  "s_waitcnt vmcnt(11)\n\t" \
  "buffer_store_dword v243, v244, s[16:19], s72 offen offset:16 sc1\n\t" \
  "ds_read_b128 v[132:135], v232 offset:9216\n\t" \
  "v_mfma_f32_16x16x32_bf16 v[64:67], a[80:83], v[152:155], v[64:67]\n\t" \
  "v_and_b32 v20, 0xffff0000, v24\n\t" \
  "v_add_f32_dpp v18, v18, v18 row_ror:8 row_mask:0xf bank_mask:0xf\n\t" \
  "v_mfma_f32_16x16x32_bf16 v[64:67], a[88:91], v[156:159], v[64:67]\n\t" \
  "v_sub_f32 v22, v22, v19\n\t" \
  "v_sub_f32 v23, v23, v20\n\t" \
  "v_mfma_f32_16x16x32_bf16 v[80:83], a[92:95], v[156:159], v[80:83]\n\t" \
  "ds_read_b128 v[136:139], v232 offset:10240\n\t" \
  "v_add_f32_dpp v18, v18, v18 row_ror:4 row_mask:0xf bank_mask:0xf\n\t" \
  "v_mfma_f32_16x16x32_bf16 v[80:83], a[84:87], v[152:155], v[80:83]\n\t" \
  "ds_read_b128 v[140:143], v232 offset:11264\n\t" \
  "v_cvt_pk_bf16_f32 v25, v22, v23\n\t" \
  "v_mfma_f32_16x16x32_bf16 v[80:83], a[76:79], v[148:151], v[80:83]\n\t" \
  "s_mov_b32 s84, 0\n\t" \
  "v_add_f32_dpp v18, v18, v18 row_ror:2 row_mask:0xf bank_mask:0xf\n\t" \
  "s_mov_b32 s85, 0xffff\n\t" \
  "v_mfma_f32_16x16x32_bf16 v[80:83], a[68:71], v[144:147], v[80:83]\n\t" \
  "s_waitcnt lgkmcnt(4)\n\t" \
  "v_add_f32_dpp v18, v18, v18 row_ror:1 row_mask:0xf bank_mask:0xf\n\t" \
  "v_mfma_f32_16x16x32_bf16 v[68:71], a[64:67], v[160:163], v[68:71]\n\t" \
  "s_mov_b64 exec, s[84:85]\n\t" \
  "ds_write_b32 v237, v24 offset:1040\n\t" \
  "ds_write_b32 v237, v25 offset:1120\n\t" \
  "s_mov_b64 exec, -1\n\t" \
  "v_mfma_f32_16x16x32_bf16 v[68:71], a[72:75], v[164:167], v[68:71]\n\t" \
  "ds_read_b128 v[144:147], v232 offset:12288\n\t" \
  "v_readlane_b32 s82, v18, 32\n\t" \
  "v_mfma_f32_16x16x32_bf16 v[68:71], a[80:83], v[168:171], v[68:71]\n\t" \
  "ds_read_b128 v[148:151], v232 offset:13312\n\t" \
  "v_readlane_b32 s83, v18, 16\n\t" \
  "v_mfma_f32_16x16x32_bf16 v[68:71], a[88:91], v[172:175], v[68:71]\n\t" \
  "ds_read_b128 v[152:155], v232 offset:14336\n\t" \
  "v_and_b32 v235, 16, v242\n\t" \
  "v_mfma_f32_16x16x32_bf16 v[84:87], a[92:95], v[172:175], v[84:87]\n\t" \
  "buffer_load_dwordx4 v242, s[16:19], s97 offen offset:3072 sc1 lds\n\t" \
  "v_mfma_f32_16x16x32_bf16 v[84:87], a[84:87], v[168:171], v[84:87]\n\t" \
  "s_add_u32 s97, s64, 262144\n\t" \
  "ds_read_b128 v[156:159], v232 offset:15360\n\t" \
  "s_and_b32 s97, s97, 0xe0000\n\t" \
  "v_mfma_f32_16x16x32_bf16 v[84:87], a[76:79], v[164:167], v[84:87]\n\t" \
  "s_add_u32 s97, s97, s63\n\t" \
  "s_add_u32 m0, s38, 49152\n\t" \
  "v_mfma_f32_16x16x32_bf16 v[84:87], a[68:71], v[160:163], v[84:87]\n\t" \
  "s_waitcnt lgkmcnt(6)\n\t" \
  "v_mfma_f32_16x16x32_bf16 v[72:75], a[64:67], v[128:131], v[72:75]\n\t" \
  "ds_read_b128 v[160:163], v230 offset:0\n\t" \
  "v_mfma_f32_16x16x32_bf16 v[72:75], a[72:75], v[132:135], v[72:75]\n\t" \
  "buffer_load_dwordx4 v242, s[16:19], s97 offen offset:0 sc1 lds\n\t" \
  "v_mfma_f32_16x16x32_bf16 v[72:75], a[80:83], v[136:139], v[72:75]\n\t" \
  "ds_read_b128 v[164:167], v230 offset:1024\n\t" \
  "v_mfma_f32_16x16x32_bf16 v[72:75], a[88:91], v[140:143], v[72:75]\n\t" \
  "ds_read_b128 v[168:171], v230 offset:2048\n\t" \
  "v_mfma_f32_16x16x32_bf16 v[88:91], a[92:95], v[140:143], v[88:91]\n\t" \
  "ds_read_b128 v[172:175], v230 offset:3072\n\t" \
  "v_mfma_f32_16x16x32_bf16 v[88:91], a[84:87], v[136:139], v[88:91]\n\t" \
  "v_mfma_f32_16x16x32_bf16 v[88:91], a[76:79], v[132:135], v[88:91]\n\t" \
  "buffer_load_dwordx4 v242, s[16:19], s97 offen offset:1024 sc1 lds\n\t" \
  "v_mfma_f32_16x16x32_bf16 v[88:91], a[68:71], v[128:131], v[88:91]\n\t" \
  "s_waitcnt lgkmcnt(4)\n\t" \
  "v_mfma_f32_16x16x32_bf16 v[76:79], a[64:67], v[144:147], v[76:79]\n\t" \
  "ds_read_b128 v[128:131], v230 offset:4096\n\t" \
  "v_mfma_f32_16x16x32_bf16 v[76:79], a[72:75], v[148:151], v[76:79]\n\t" \
  "buffer_load_dwordx4 v242, s[16:19], s97 offen offset:2048 sc1 lds\n\t" \
  "v_mfma_f32_16x16x32_bf16 v[76:79], a[80:83], v[152:155], v[76:79]\n\t" \
  "ds_read_b128 v[132:135], v230 offset:5120\n\t" \
  "v_mfma_f32_16x16x32_bf16 v[76:79], a[88:91], v[156:159], v[76:79]\n\t" \
  "ds_read_b128 v[136:139], v230 offset:6144\n\t" \
  "v_mfma_f32_16x16x32_bf16 v[92:95], a[92:95], v[156:159], v[92:95]\n\t" \
  "buffer_load_dwordx4 v242, s[16:19], s97 offen offset:3072 sc1 lds\n\t" \
  "v_mfma_f32_16x16x32_bf16 v[92:95], a[84:87], v[152:155], v[92:95]\n\t" \
  "s_add_u32 s97, s64, 393216\n\t" \
  "s_and_b32 s97, s97, 0xe0000\n\t" \
  "s_add_u32 s97, s97, s63\n\t" \
  "v_mfma_f32_16x16x32_bf16 v[92:95], a[76:79], v[148:151], v[92:95]\n\t" \
  "s_add_u32 m0, s38, 98304\n\t" \
  "ds_read_b128 v[140:143], v230 offset:7168\n\t" \
  "v_mfma_f32_16x16x32_bf16 v[92:95], a[68:71], v[144:147], v[92:95]\n\t" \
  "s_waitcnt lgkmcnt(4)\n\t" \
  "buffer_load_dwordx4 v242, s[16:19], s97 offen offset:0 sc1 lds\n\t" \
  "v_mfma_f32_16x16x32_bf16 v[64:67], a[96:99], v[160:163], v[64:67]\n\t" \
  "ds_read_b128 v[144:147], v230 offset:8192\n\t" \
  "v_mfma_f32_16x16x32_bf16 v[64:67], a[104:107], v[164:167], v[64:67]\n\t" \
  "ds_read_b128 v[148:151], v230 offset:9216\n\t" \
  "v_mfma_f32_16x16x32_bf16 v[64:67], a[112:115], v[168:171], v[64:67]\n\t" \
  "buffer_load_dwordx4 v242, s[16:19], s97 offen offset:1024 sc1 lds\n\t" \
  "v_mfma_f32_16x16x32_bf16 v[64:67], a[120:123], v[172:175], v[64:67]\n\t" \
  "ds_read_b128 v[152:155], v230 offset:10240\n\t" \
  "v_mfma_f32_16x16x32_bf16 v[80:83], a[124:127], v[172:175], v[80:83]\n\t" \
  "ds_read_b128 v[156:159], v230 offset:11264\n\t" \
  "v_mfma_f32_16x16x32_bf16 v[80:83], a[116:119], v[168:171], v[80:83]\n\t" \
  "v_mfma_f32_16x16x32_bf16 v[80:83], a[108:111], v[164:167], v[80:83]\n\t" \
  "v_mfma_f32_16x16x32_bf16 v[80:83], a[100:103], v[160:163], v[80:83]\n\t" \
  "s_waitcnt lgkmcnt(4)\n\t" \
  "v_mfma_f32_16x16x32_bf16 v[68:71], a[96:99], v[128:131], v[68:71]\n\t" \
  "ds_read_b128 v[160:163], v230 offset:12288\n\t" \
  "v_mfma_f32_16x16x32_bf16 v[68:71], a[104:107], v[132:135], v[68:71]\n\t" \
  "ds_read_b128 v[164:167], v230 offset:13312\n\t" \
  "v_mfma_f32_16x16x32_bf16 v[68:71], a[112:115], v[136:139], v[68:71]\n\t" \
  "buffer_load_dwordx4 v242, s[16:19], s97 offen offset:2048 sc1 lds\n\t" \
  "v_mfma_f32_16x16x32_bf16 v[68:71], a[120:123], v[140:143], v[68:71]\n\t" \
  "ds_read_b128 v[168:171], v230 offset:14336\n\t" \
  "v_mfma_f32_16x16x32_bf16 v[84:87], a[124:127], v[140:143], v[84:87]\n\t" \
  "ds_read_b128 v[172:175], v230 offset:15360\n\t" \
  "v_mfma_f32_16x16x32_bf16 v[84:87], a[116:119], v[136:139], v[84:87]\n\t" \
  "v_mfma_f32_16x16x32_bf16 v[84:87], a[108:111], v[132:135], v[84:87]\n\t" \
  "s_waitcnt vmcnt(7) lgkmcnt(4)\n\t" \
  "buffer_load_dwordx4 v242, s[16:19], s97 offen offset:3072 sc1 lds\n\t" \
  "v_mfma_f32_16x16x32_bf16 v[84:87], a[100:103], v[128:131], v[84:87]\n\t" \
  "s_barrier\n\t" \
  "v_mfma_f32_16x16x32_bf16 v[72:75], a[96:99], v[144:147], v[72:75]\n\t" \
  "s_add_u32 s97, s64, 524288\n\t" \
  "s_and_b32 s97, s97, 0xe0000\n\t" \
  "s_add_u32 s97, s97, s63\n\t" \
  "v_mfma_f32_16x16x32_bf16 v[72:75], a[104:107], v[148:151], v[72:75]\n\t" \
  "s_add_u32 m0, s38, 81920\n\t" \
  "ds_read_b128 v[128:131], v242 offset:16384\n\t" \
  ".Lhs14:\n\ts_cbranch_execz .Lhs14n\n\t" \
  "v_mfma_f32_16x16x32_bf16 v[72:75], a[112:115], v[152:155], v[72:75]\n\t" \
  "buffer_load_dwordx4 v242, s[16:19], s97 offen offset:0 sc1 lds\n\t" \
  "v_mfma_f32_16x16x32_bf16 v[72:75], a[120:123], v[156:159], v[72:75]\n\t" \
  "ds_read_b128 v[132:135], v242 offset:17408\n\t" \
  "v_mfma_f32_16x16x32_bf16 v[88:91], a[124:127], v[156:159], v[88:91]\n\t" \
  "buffer_load_dwordx4 v242, s[16:19], s97 offen offset:1024 sc1 lds\n\t" \
  "v_mfma_f32_16x16x32_bf16 v[88:91], a[116:119], v[152:155], v[88:91]\n\t" \
  "ds_read_b128 v[136:139], v242 offset:18432\n\t" \
  "v_mfma_f32_16x16x32_bf16 v[88:91], a[108:111], v[148:151], v[88:91]\n\t" \
  "ds_read_b128 v[140:143], v242 offset:19456\n\t" \
  "v_mfma_f32_16x16x32_bf16 v[88:91], a[100:103], v[144:147], v[88:91]\n\t" \
  "s_waitcnt lgkmcnt(4)\n\t" \
  "buffer_load_dwordx4 v242, s[16:19], s97 offen offset:2048 sc1 lds\n\t" \
  "v_mfma_f32_16x16x32_bf16 v[76:79], a[96:99], v[160:163], v[76:79]\n\t" \
  "ds_read_b128 v[144:147], v242 offset:20480\n\t" \
  "v_mfma_f32_16x16x32_bf16 v[76:79], a[104:107], v[164:167], v[76:79]\n\t" \
  "ds_read_b128 v[148:151], v242 offset:21504\n\t" \
  "v_mfma_f32_16x16x32_bf16 v[76:79], a[112:115], v[168:171], v[76:79]\n\t" \
  "buffer_load_dwordx4 v242, s[16:19], s97 offen offset:3072 sc1 lds\n\t" \
  "v_mfma_f32_16x16x32_bf16 v[76:79], a[120:123], v[172:175], v[76:79]\n\t" \
  "s_add_u32 s97, s64, 655360\n\t" \
  "s_and_b32 s97, s97, 0xe0000\n\t" \
  "s_add_u32 s97, s97, s63\n\t" \
  "v_mfma_f32_16x16x32_bf16 v[92:95], a[124:127], v[172:175], v[92:95]\n\t" \
  "s_add_u32 m0, s38, 131072\n\t" \
  "ds_read_b128 v[152:155], v242 offset:22528\n\t" \
  "v_mfma_f32_16x16x32_bf16 v[92:95], a[116:119], v[168:171], v[92:95]\n\t" \
  "buffer_load_dwordx4 v242, s[16:19], s97 offen offset:0 sc1 lds\n\t" \
  "v_mfma_f32_16x16x32_bf16 v[92:95], a[108:111], v[164:167], v[92:95]\n\t" \
  "ds_read_b128 v[156:159], v242 offset:23552\n\t" \
  "v_mfma_f32_16x16x32_bf16 v[92:95], a[100:103], v[160:163], v[92:95]\n\t" \
  "s_waitcnt lgkmcnt(4)\n\t" \
  "v_mfma_f32_16x16x32_bf16 v[64:67], a[192:195], v[128:131], v[64:67]\n\t" \
  "buffer_load_dwordx4 v242, s[16:19], s97 offen offset:1024 sc1 lds\n\t" \
  "v_mfma_f32_16x16x32_bf16 v[64:67], a[200:203], v[132:135], v[64:67]\n\t" \
  "ds_read_b128 v[160:163], v242 offset:24576\n\t" \
  "v_mfma_f32_16x16x32_bf16 v[64:67], a[208:211], v[136:139], v[64:67]\n\t" \
  "ds_read_b128 v[164:167], v242 offset:25600\n\t" \
  "v_mfma_f32_16x16x32_bf16 v[64:67], a[216:219], v[140:143], v[64:67]\n\t" \
  "buffer_load_dwordx4 v242, s[16:19], s97 offen offset:2048 sc1 lds\n\t" \
  "v_mfma_f32_16x16x32_bf16 v[80:83], a[220:223], v[140:143], v[80:83]\n\t" \
  "ds_read_b128 v[168:171], v242 offset:26624\n\t" \
  "v_mfma_f32_16x16x32_bf16 v[80:83], a[212:215], v[136:139], v[80:83]\n\t" \
  "ds_read_b128 v[172:175], v242 offset:27648\n\t" \
  "v_mfma_f32_16x16x32_bf16 v[80:83], a[204:207], v[132:135], v[80:83]\n\t" \
  "buffer_load_dwordx4 v242, s[16:19], s97 offen offset:3072 sc1 lds\n\t" \
  "v_mfma_f32_16x16x32_bf16 v[80:83], a[196:199], v[128:131], v[80:83]\n\t" \
  "s_waitcnt lgkmcnt(4)\n\t" \
  "v_cvt_pk_bf16_f32 v0, v64, v65\n\t" \
  "v_cvt_pk_bf16_f32 v1, v66, v67\n\t" \
  "v_mfma_f32_16x16x32_bf16 v[68:71], a[192:195], v[144:147], v[68:71]\n\t" \
  "buffer_load_dword v246, v235, s[16:19], s73 offen sc1\n\t" \
  "v_mfma_f32_16x16x32_bf16 v[68:71], a[200:203], v[148:151], v[68:71]\n\t" \
  "ds_read_b128 v[128:131], v242 offset:28672\n\t" \
  "v_mov_b32 v250, 0x3f80\n\t" \
  "v_mfma_f32_16x16x32_bf16 v[68:71], a[208:211], v[152:155], v[68:71]\n\t" \
  "v_cvt_pk_bf16_f32 v2, v80, v81\n\t" \
  "v_cvt_pk_bf16_f32 v3, v82, v83\n\t" \
  "v_mfma_f32_16x16x32_bf16 v[68:71], a[216:219], v[156:159], v[68:71]\n\t" \
  "ds_read_b128 v[132:135], v242 offset:29696\n\t" \
  "v_lshlrev_b32 v250, v235, v250\n\t" \
  "v_mfma_f32_16x16x32_bf16 v[84:87], a[220:223], v[156:159], v[84:87]\n\t" \
  "s_mov_b32 s84, 0x300003\n\t" \
  "ds_read_b128 v[136:139], v242 offset:30720\n\t" \
  "s_mov_b32 s85, 0x30000300\n\t" \
  "v_mfma_f32_16x16x32_bf16 v[84:87], a[212:215], v[152:155], v[84:87]\n\t" \
  "s_mov_b32 s86, 0xc0000c\n\t" \
  "s_mov_b32 s87, 0xc0000c00\n\t" \
  "v_cndmask_b32 v248, 0, v250, s[84:85]\n\t" \
  "v_mfma_f32_16x16x32_bf16 v[84:87], a[204:207], v[148:151], v[84:87]\n\t" \
  "ds_read_b128 v[140:143], v242 offset:31744\n\t" \
  "v_cndmask_b32 v249, 0, v250, s[86:87]\n\t" \
  "v_mfma_f32_16x16x32_bf16 v[84:87], a[196:199], v[144:147], v[84:87]\n\t" \
  "s_waitcnt lgkmcnt(4)\n\t" \
  "v_cvt_pk_bf16_f32 v4, v68, v69\n\t" \
  "v_cvt_pk_bf16_f32 v5, v70, v71\n\t" \
  "v_mfma_f32_16x16x32_bf16 v[72:75], a[192:195], v[160:163], v[72:75]\n\t" \
  "buffer_store_dwordx4 v[0:3], v242, s[16:19], s69 offen offset:0 sc1\n\t" \
  "ds_read_b128 v[144:147], v242 offset:32768\n\t" \
  "v_and_b32 v236, 15, v233\n\t" \
  "v_lshrrev_b32 v250, 4, v233\n\t" \
  "v_mfma_f32_16x16x32_bf16 v[72:75], a[200:203], v[164:167], v[72:75]\n\t" \
  "v_cvt_pk_bf16_f32 v6, v84, v85\n\t" \
  "v_cvt_pk_bf16_f32 v7, v86, v87\n\t" \
  "v_mfma_f32_16x16x32_bf16 v[72:75], a[208:211], v[168:171], v[72:75]\n\t" \
  "ds_read_b128 v[148:151], v242 offset:33792\n\t" \
  "v_mul_u32_u24 v236, 80, v236\n\t" \
  "v_mfma_f32_16x16x32_bf16 v[72:75], a[216:219], v[172:175], v[72:75]\n\t" \
  "v_lshlrev_b32 v237, 4, v250\n\t" \
  "v_add_u32 v237, s76, v237\n\t" \
  "v_mfma_f32_16x16x32_bf16 v[88:91], a[220:223], v[172:175], v[88:91]\n\t" \
  "ds_read_b128 v[152:155], v242 offset:34816\n\t" \
  "v_add_u32 v236, v237, v236\n\t" \
  "v_mfma_f32_16x16x32_bf16 v[88:91], a[212:215], v[168:171], v[88:91]\n\t" \
  "s_add_u32 s78, s70, 4096\n\t" \
  "ds_read_b128 v[156:159], v242 offset:35840\n\t" \
  "s_add_u32 s79, s70, 16384\n\t" \
  "v_mfma_f32_16x16x32_bf16 v[88:91], a[204:207], v[164:167], v[88:91]\n\t" \
  "v_mfma_f32_16x16x32_bf16 v[88:91], a[196:199], v[160:163], v[88:91]\n\t" \
  "s_waitcnt lgkmcnt(4)\n\t" \
  "v_cvt_pk_bf16_f32 v8, v72, v73\n\t" \
  "v_cvt_pk_bf16_f32 v9, v74, v75\n\t" \
  "v_mfma_f32_16x16x32_bf16 v[76:79], a[192:195], v[128:131], v[76:79]\n\t" \
  "buffer_store_dwordx4 v[4:7], v242, s[16:19], s69 offen offset:1024 sc1\n\t" \
  "ds_read_b128 v[160:163], v242 offset:36864\n\t" \
  "ds_read_b128 v[164:167], v242 offset:37888\n\t" \
  "ds_read_b128 v[168:171], v242 offset:38912\n\t" \
  "v_mfma_f32_16x16x32_bf16 v[76:79], a[200:203], v[132:135], v[76:79]\n\t" \
  "v_cvt_pk_bf16_f32 v10, v88, v89\n\t" \
  "v_cvt_pk_bf16_f32 v11, v90, v91\n\t" \
  "v_mfma_f32_16x16x32_bf16 v[76:79], a[208:211], v[136:139], v[76:79]\n\t" \
  "ds_read_b128 v[172:175], v242 offset:39936\n\t" \
  "v_mfma_f32_16x16x32_bf16 v[76:79], a[216:219], v[140:143], v[76:79]\n\t" \
  "v_mfma_f32_16x16x32_bf16 v[92:95], a[220:223], v[140:143], v[92:95]\n\t" \
  "v_mfma_f32_16x16x32_bf16 v[92:95], a[212:215], v[136:139], v[92:95]\n\t" \
  "v_mfma_f32_16x16x32_bf16 v[92:95], a[204:207], v[132:135], v[92:95]\n\t" \
  "v_mfma_f32_16x16x32_bf16 v[92:95], a[196:199], v[128:131], v[92:95]\n\t" \
  "s_waitcnt lgkmcnt(4)\n\t" \
  STP("s_memrealtime s[94:95]\n\t") \
  STP("s_memtime s[92:93]\n\t") \
  "v_mfma_f32_16x16x32_bf16 v[96:99], a[128:131], v[144:147], v[96:99]\n\t" \
  "buffer_store_dwordx4 v[8:11], v242, s[16:19], s69 offen offset:2048 sc1\n\t" \
  "ds_read_b128 v[128:131], v242 offset:40960\n\t" \
  "v_cvt_pk_bf16_f32 v12, v76, v77\n\t" \
  "v_cvt_pk_bf16_f32 v13, v78, v79\n\t" \
  "v_mfma_f32_16x16x32_bf16 v[96:99], a[136:139], v[148:151], v[96:99]\n\t" \
  "ds_read_b64 v[208:209], v238 offset:0\n\t" \
  "v_cvt_pk_bf16_f32 v14, v92, v93\n\t" \
  "v_mfma_f32_16x16x32_bf16 v[96:99], a[144:147], v[152:155], v[96:99]\n\t" \
  "ds_read_b128 v[132:135], v242 offset:41984\n\t" \
  "v_cvt_pk_bf16_f32 v15, v94, v95\n\t" \
  "v_mfma_f32_16x16x32_bf16 v[96:99], a[152:155], v[156:159], v[96:99]\n\t" \
  "ds_read_b64 v[210:211], v238 offset:4096\n\t" \
  "v_mfma_f32_16x16x32_bf16 v[112:115], a[156:159], v[156:159], v[112:115]\n\t" \
  "ds_read_b128 v[136:139], v242 offset:43008\n\t" \
  "v_mfma_f32_16x16x32_bf16 v[112:115], a[148:151], v[152:155], v[112:115]\n\t" \
  "ds_read_b64 v[212:213], v238 offset:8192\n\t" \
  "v_mfma_f32_16x16x32_bf16 v[112:115], a[140:143], v[148:151], v[112:115]\n\t" \
  "ds_read_b128 v[140:143], v242 offset:44032\n\t" \
  "v_mfma_f32_16x16x32_bf16 v[112:115], a[132:135], v[144:147], v[112:115]\n\t" \
  "s_waitcnt lgkmcnt(7)\n\t" \
  "ds_read_b64 v[214:215], v238 offset:12288\n\t" \
  "v_mfma_f32_16x16x32_bf16 v[100:103], a[128:131], v[160:163], v[100:103]\n\t" \
  "buffer_store_dwordx4 v[12:15], v242, s[16:19], s69 offen offset:3072 sc1\n\t" \
  "ds_read_b128 v[144:147], v242 offset:45056\n\t" \
  "ds_read_b64 v[216:217], v239 offset:0\n\t" \
  "ds_read_b128 v[148:151], v242 offset:46080\n\t" \
  "v_mfma_f32_16x16x32_bf16 v[100:103], a[136:139], v[164:167], v[100:103]\n\t" \
  "ds_read_b128 v[152:155], v242 offset:47104\n\t" \
  "v_mfma_f32_16x16x32_bf16 v[100:103], a[144:147], v[168:171], v[100:103]\n\t" \
  "ds_read_b64 v[218:219], v239 offset:4096\n\t" \
  "v_mfma_f32_16x16x32_bf16 v[100:103], a[152:155], v[172:175], v[100:103]\n\t" \
  "ds_read_b128 v[156:159], v242 offset:48128\n\t" \
  "v_mfma_f32_16x16x32_bf16 v[116:119], a[156:159], v[172:175], v[116:119]\n\t" \
  STP("v_writelane_b32 v255, s94, 36\n\t") \
  STP("v_writelane_b32 v255, s92, 37\n\t") \
  "ds_read_b64 v[220:221], v239 offset:8192\n\t" \
  "v_mfma_f32_16x16x32_bf16 v[116:119], a[148:151], v[168:171], v[116:119]\n\t" \
  "ds_read_b64 v[222:223], v239 offset:12288\n\t" \
  "v_mfma_f32_16x16x32_bf16 v[116:119], a[140:143], v[164:167], v[116:119]\n\t" \
  "s_waitcnt vmcnt(13) lgkmcnt(9)\n\t" \
  "s_waitcnt lgkmcnt(0)\n\t" \
  "v_lshlrev_b32 v0, 16, v208\n\t" \
  "v_mfma_f32_16x16x32_bf16 v[116:119], a[132:135], v[160:163], v[116:119]\n\t" \
  "s_barrier\n\t" \
  "v_mfma_f32_16x16x32_bf16 v[104:107], a[128:131], v[128:131], v[104:107]\n\t" \
  "ds_read_b128 v[160:163], v242 offset:49152\n\t" \
  "v_and_b32 v1, 0xffff0000, v208\n\t" \
  "v_mfma_f32_16x16x32_bf16 v[104:107], a[136:139], v[132:135], v[104:107]\n\t" \
  "v_lshlrev_b32 v2, 16, v209\n\t" \
  "v_and_b32 v3, 0xffff0000, v209\n\t" \
  "v_mfma_f32_16x16x32_bf16 v[104:107], a[144:147], v[136:139], v[104:107]\n\t" \
  "ds_read_b128 v[164:167], v242 offset:50176\n\t" \
  "v_lshlrev_b32 v4, 16, v210\n\t" \
  "v_mfma_f32_16x16x32_bf16 v[104:107], a[152:155], v[140:143], v[104:107]\n\t" \
  "v_and_b32 v5, 0xffff0000, v210\n\t" \
  "v_lshlrev_b32 v6, 16, v211\n\t" \
  "v_mfma_f32_16x16x32_bf16 v[120:123], a[156:159], v[140:143], v[120:123]\n\t" \
  "s_add_u32 s97, s64, 786432\n\t" \
  "ds_read_b128 v[168:171], v242 offset:51200\n\t" \
  "v_and_b32 v7, 0xffff0000, v211\n\t" \
  "v_mfma_f32_16x16x32_bf16 v[120:123], a[148:151], v[136:139], v[120:123]\n\t" \
  "s_and_b32 s97, s97, 0xe0000\n\t" \
  "v_lshlrev_b32 v8, 16, v212\n\t" \
  "v_and_b32 v9, 0xffff0000, v212\n\t" \
  "v_mfma_f32_16x16x32_bf16 v[120:123], a[140:143], v[132:135], v[120:123]\n\t" \
  "s_add_u32 s97, s97, s63\n\t" \
  "ds_read_b128 v[172:175], v242 offset:52224\n\t" \
  "v_lshlrev_b32 v10, 16, v213\n\t" \
  "v_mfma_f32_16x16x32_bf16 v[120:123], a[132:135], v[128:131], v[120:123]\n\t" \
  "s_add_u32 m0, s38, 65536\n\t" \
  "v_and_b32 v11, 0xffff0000, v213\n\t" \
  "v_lshlrev_b32 v12, 16, v214\n\t" \
  "v_mfma_f32_16x16x32_bf16 v[108:111], a[128:131], v[144:147], v[108:111]\n\t" \
  "ds_read_b128 v[128:131], v242 offset:53248\n\t" \
  "v_and_b32 v13, 0xffff0000, v214\n\t" \
  "v_mfma_f32_16x16x32_bf16 v[108:111], a[136:139], v[148:151], v[108:111]\n\t" \
  "v_lshlrev_b32 v14, 16, v215\n\t" \
  "v_and_b32 v15, 0xffff0000, v215\n\t" \
  "v_mfma_f32_16x16x32_bf16 v[108:111], a[144:147], v[152:155], v[108:111]\n\t" \
  "buffer_load_dwordx4 v242, s[16:19], s97 offen offset:0 sc1 lds\n\t" \
  "v_mfma_f32_16x16x32_bf16 v[108:111], a[152:155], v[156:159], v[108:111]\n\t" \
  "ds_read_b128 v[132:135], v242 offset:54272\n\t" \
  "v_lshlrev_b32 v16, 16, v216\n\t" \
  "v_mfma_f32_16x16x32_bf16 v[124:127], a[156:159], v[156:159], v[124:127]\n\t" \
  "ds_read_b128 v[136:139], v242 offset:55296\n\t" \
  "v_and_b32 v17, 0xffff0000, v216\n\t" \
  "v_mfma_f32_16x16x32_bf16 v[124:127], a[148:151], v[152:155], v[124:127]\n\t" \
  "buffer_load_dwordx4 v242, s[16:19], s97 offen offset:1024 sc1 lds\n\t" \
  "v_mfma_f32_16x16x32_bf16 v[124:127], a[140:143], v[148:151], v[124:127]\n\t" \
  "ds_read_b128 v[140:143], v242 offset:56320\n\t" \
  "v_lshlrev_b32 v18, 16, v217\n\t" \
  "v_mfma_f32_16x16x32_bf16 v[124:127], a[132:135], v[144:147], v[124:127]\n\t" \
  "s_waitcnt lgkmcnt(4)\n\t" \
  "v_and_b32 v19, 0xffff0000, v217\n\t" \
  "v_lshlrev_b32 v20, 16, v218\n\t" \
  "v_mfma_f32_16x16x32_bf16 v[96:99], a[160:163], v[160:163], v[96:99]\n\t" \
  "ds_read_b128 v[144:147], v242 offset:57344\n\t" \
  "v_and_b32 v21, 0xffff0000, v218\n\t" \
  "v_mfma_f32_16x16x32_bf16 v[96:99], a[168:171], v[164:167], v[96:99]\n\t" \
  "buffer_load_dwordx4 v242, s[16:19], s97 offen offset:2048 sc1 lds\n\t" \
  "v_mfma_f32_16x16x32_bf16 v[96:99], a[176:179], v[168:171], v[96:99]\n\t" \
  "ds_read_b128 v[148:151], v242 offset:58368\n\t" \
  "v_lshlrev_b32 v22, 16, v219\n\t" \
  "v_mfma_f32_16x16x32_bf16 v[96:99], a[184:187], v[172:175], v[96:99]\n\t" \
  "v_and_b32 v23, 0xffff0000, v219\n\t" \
  "v_lshlrev_b32 v24, 16, v220\n\t" \
  "v_mfma_f32_16x16x32_bf16 v[112:115], a[188:191], v[172:175], v[112:115]\n\t" \
  "ds_read_b128 v[152:155], v242 offset:59392\n\t" \
  "v_and_b32 v25, 0xffff0000, v220\n\t" \
  "v_mfma_f32_16x16x32_bf16 v[112:115], a[180:183], v[168:171], v[112:115]\n\t" \
  "buffer_load_dwordx4 v242, s[16:19], s97 offen offset:3072 sc1 lds\n\t" \
  "v_mfma_f32_16x16x32_bf16 v[112:115], a[172:175], v[164:167], v[112:115]\n\t" \
  "s_add_u32 s97, s64, 917504\n\t" \
  "s_and_b32 s97, s97, 0xe0000\n\t" \
  "s_add_u32 s97, s97, s63\n\t" \
  "v_mfma_f32_16x16x32_bf16 v[112:115], a[164:167], v[160:163], v[112:115]\n\t" \
  "s_waitcnt lgkmcnt(3)\n\t" \
  "ds_read_b128 v[156:159], v242 offset:60416\n\t" \
  "s_add_u32 m0, s38, 16384\n\t" \
  "v_mfma_f32_16x16x32_bf16 v[100:103], a[160:163], v[128:131], v[100:103]\n\t" \
  "buffer_load_dwordx4 v242, s[16:19], s97 offen offset:0 sc1 lds\n\t" \
  "v_mfma_f32_16x16x32_bf16 v[100:103], a[168:171], v[132:135], v[100:103]\n\t" \
  "ds_read_b128 v[160:163], v242 offset:61440\n\t" \
  "v_lshlrev_b32 v26, 16, v221\n\t" \
  "v_mfma_f32_16x16x32_bf16 v[100:103], a[176:179], v[136:139], v[100:103]\n\t" \
  "ds_read_b128 v[164:167], v242 offset:62464\n\t" \
  "v_and_b32 v27, 0xffff0000, v221\n\t" \
  "v_mfma_f32_16x16x32_bf16 v[100:103], a[184:187], v[140:143], v[100:103]\n\t" \
  "v_lshlrev_b32 v28, 16, v222\n\t" \
  "v_and_b32 v29, 0xffff0000, v222\n\t" \
  "v_mfma_f32_16x16x32_bf16 v[116:119], a[188:191], v[140:143], v[116:119]\n\t" \
  "buffer_load_dwordx4 v242, s[16:19], s97 offen offset:1024 sc1 lds\n\t" \
  "v_mfma_f32_16x16x32_bf16 v[116:119], a[180:183], v[136:139], v[116:119]\n\t" \
  "ds_read_b128 v[168:171], v242 offset:63488\n\t" \
  "v_lshlrev_b32 v30, 16, v223\n\t" \
  "v_mfma_f32_16x16x32_bf16 v[116:119], a[172:175], v[132:135], v[116:119]\n\t" \
  "ds_read_b128 v[172:175], v242 offset:64512\n\t" \
  "v_and_b32 v31, 0xffff0000, v223\n\t" \
  "v_mfma_f32_16x16x32_bf16 v[116:119], a[164:167], v[128:131], v[116:119]\n\t" \
  "s_waitcnt lgkmcnt(4)\n\t" \
  "v_mfma_f32_16x16x32_bf16 v[104:107], a[160:163], v[144:147], v[104:107]\n\t" \
  "buffer_load_dwordx4 v242, s[16:19], s97 offen offset:2048 sc1 lds\n\t" \
  "v_mfma_f32_16x16x32_bf16 v[104:107], a[168:171], v[148:151], v[104:107]\n\t" \
  "s_waitcnt vmcnt(11)\n\t" \
  "v_cmp_ne_u32 vcc, s36, v246\n\t" \
  "v_mfma_f32_16x16x32_bf16 v[104:107], a[176:179], v[152:155], v[104:107]\n\t" \
  "ds_read_b128 v[128:131], v230 offset:32768\n\t" \
  "v_mfma_f32_16x16x32_bf16 v[104:107], a[184:187], v[156:159], v[104:107]\n\t" \
  "s_cbranch_vccnz .Lat_s4\n\t" \
  ".Lat_k4:\n\t" \
  "v_mfma_f32_16x16x32_bf16 v[120:123], a[188:191], v[156:159], v[120:123]\n\t" \
  "buffer_load_dwordx4 v[32:35], v242, s[16:19], s70 offen offset:0 sc1\n\t" \
  "v_mfma_f32_16x16x32_bf16 v[120:123], a[180:183], v[152:155], v[120:123]\n\t" \
  "ds_read_b128 v[132:135], v230 offset:33792\n\t" \
  "v_mfma_f32_16x16x32_bf16 v[120:123], a[172:175], v[148:151], v[120:123]\n\t" \
  "ds_read_b128 v[136:139], v230 offset:34816\n\t" \
  "v_mfma_f32_16x16x32_bf16 v[120:123], a[164:167], v[144:147], v[120:123]\n\t" \
  "s_waitcnt lgkmcnt(3)\n\t" \
  "ds_read_b128 v[140:143], v230 offset:35840\n\t" \
  "v_mfma_f32_16x16x32_bf16 v[108:111], a[160:163], v[160:163], v[108:111]\n\t" \
  "buffer_load_dwordx4 v242, s[16:19], s97 offen offset:3072 sc1 lds\n\t" \
  "v_mfma_f32_16x16x32_bf16 v[108:111], a[168:171], v[164:167], v[108:111]\n\t" \
  "ds_read_b128 v[144:147], v230 offset:36864\n\t" \
  "v_mfma_f32_16x16x32_bf16 v[108:111], a[176:179], v[168:171], v[108:111]\n\t" \
  "ds_read_b128 v[148:151], v230 offset:37888\n\t" \
  "v_mfma_f32_16x16x32_bf16 v[108:111], a[184:187], v[172:175], v[108:111]\n\t" \
  "ds_read_b128 v[152:155], v230 offset:38912\n\t" \
  "v_mfma_f32_16x16x32_bf16 v[124:127], a[188:191], v[172:175], v[124:127]\n\t" \
  "ds_read_b128 v[156:159], v230 offset:39936\n\t" \
  "v_mfma_f32_16x16x32_bf16 v[124:127], a[180:183], v[168:171], v[124:127]\n\t" \
  "buffer_load_dwordx4 v[36:39], v242, s[16:19], s70 offen offset:1024 sc1\n\t" \
  "v_mfma_f32_16x16x32_bf16 v[124:127], a[172:175], v[164:167], v[124:127]\n\t" \
  "v_mfma_f32_16x16x32_bf16 v[124:127], a[164:167], v[160:163], v[124:127]\n\t" \
  "s_waitcnt lgkmcnt(4)\n\t" \
  "v_mfma_f32_16x16x32_bf16 v[96:99], a[0:3], v[128:131], v[96:99]\n\t" \
  "ds_read_b128 v[160:163], v230 offset:40960\n\t" \
  "v_mfma_f32_16x16x32_bf16 v[96:99], a[8:11], v[132:135], v[96:99]\n\t" \
  "ds_read_b128 v[164:167], v230 offset:41984\n\t" \
  "v_mfma_f32_16x16x32_bf16 v[96:99], a[16:19], v[136:139], v[96:99]\n\t" \
  "ds_read_b128 v[168:171], v230 offset:43008\n\t" \
  "v_mfma_f32_16x16x32_bf16 v[96:99], a[24:27], v[140:143], v[96:99]\n\t" \
  "ds_read_b128 v[172:175], v230 offset:44032\n\t" \
  "v_mfma_f32_16x16x32_bf16 v[112:115], a[28:31], v[140:143], v[112:115]\n\t" \
  "v_mfma_f32_16x16x32_bf16 v[112:115], a[20:23], v[136:139], v[112:115]\n\t" \
  "v_mfma_f32_16x16x32_bf16 v[112:115], a[12:15], v[132:135], v[112:115]\n\t" \
  "v_mfma_f32_16x16x32_bf16 v[112:115], a[4:7], v[128:131], v[112:115]\n\t" \
  "s_waitcnt lgkmcnt(4)\n\t" \
  "v_mfma_f32_16x16x32_bf16 v[100:103], a[0:3], v[144:147], v[100:103]\n\t" \
  "ds_read_b128 v[128:131], v230 offset:45056\n\t" \
  "v_mfma_f32_16x16x32_bf16 v[100:103], a[8:11], v[148:151], v[100:103]\n\t" \
  "ds_read_b128 v[132:135], v230 offset:46080\n\t" \
  "v_mfma_f32_16x16x32_bf16 v[100:103], a[16:19], v[152:155], v[100:103]\n\t" \
  "buffer_load_dwordx4 v[40:43], v242, s[16:19], s70 offen offset:2048 sc1\n\t" \
  "v_mfma_f32_16x16x32_bf16 v[100:103], a[24:27], v[156:159], v[100:103]\n\t" \
  "ds_read_b128 v[136:139], v230 offset:47104\n\t" \
  "v_mfma_f32_16x16x32_bf16 v[116:119], a[28:31], v[156:159], v[116:119]\n\t" \
  "ds_read_b128 v[140:143], v230 offset:48128\n\t" \
  "v_mfma_f32_16x16x32_bf16 v[116:119], a[20:23], v[152:155], v[116:119]\n\t" \
  "buffer_load_dwordx4 v[44:47], v242, s[16:19], s70 offen offset:3072 sc1\n\t" \
  "v_mfma_f32_16x16x32_bf16 v[116:119], a[12:15], v[148:151], v[116:119]\n\t" \
  "s_waitcnt lgkmcnt(4)\n\t" \
  "v_mfma_f32_16x16x32_bf16 v[116:119], a[4:7], v[144:147], v[116:119]\n\t" \
  "s_barrier\n\t" \
  "v_mfma_f32_16x16x32_bf16 v[104:107], a[0:3], v[160:163], v[104:107]\n\t" \
  "s_waitcnt vmcnt(12)\n\t" \
  "buffer_store_dword v243, v244, s[16:19], s66 offen offset:32 sc1\n\t" \
  "ds_read_b128 v[144:147], v230 offset:16384\n\t" \
  "v_mfma_f32_16x16x32_bf16 v[104:107], a[8:11], v[164:167], v[104:107]\n\t" \
  "ds_read_b128 v[148:151], v230 offset:17408\n\t" \
  "v_mfma_f32_16x16x32_bf16 v[104:107], a[16:19], v[168:171], v[104:107]\n\t" \
  "buffer_load_dwordx4 v[48:51], v242, s[16:19], s78 offen offset:0 sc1\n\t" \
  "v_mfma_f32_16x16x32_bf16 v[104:107], a[24:27], v[172:175], v[104:107]\n\t" \
  "ds_read_b128 v[152:155], v230 offset:18432\n\t" \
  "v_mfma_f32_16x16x32_bf16 v[120:123], a[28:31], v[172:175], v[120:123]\n\t" \
  "ds_read_b128 v[156:159], v230 offset:19456\n\t" \
  "v_mfma_f32_16x16x32_bf16 v[120:123], a[20:23], v[168:171], v[120:123]\n\t" \
  "buffer_load_dwordx4 v[52:55], v242, s[16:19], s78 offen offset:1024 sc1\n\t" \
  "v_mfma_f32_16x16x32_bf16 v[120:123], a[12:15], v[164:167], v[120:123]\n\t" \
  "v_mfma_f32_16x16x32_bf16 v[120:123], a[4:7], v[160:163], v[120:123]\n\t" \
  "s_waitcnt lgkmcnt(4)\n\t" \
  "v_mfma_f32_16x16x32_bf16 v[108:111], a[0:3], v[128:131], v[108:111]\n\t" \
  "buffer_load_dwordx4 v[56:59], v242, s[16:19], s78 offen offset:2048 sc1\n\t" \
  "v_mfma_f32_16x16x32_bf16 v[108:111], a[8:11], v[132:135], v[108:111]\n\t" \
  "ds_read_b128 v[160:163], v230 offset:20480\n\t" \
  "v_mfma_f32_16x16x32_bf16 v[108:111], a[16:19], v[136:139], v[108:111]\n\t" \
  "ds_read_b128 v[164:167], v230 offset:21504\n\t" \
  "v_mfma_f32_16x16x32_bf16 v[108:111], a[24:27], v[140:143], v[108:111]\n\t" \
  "ds_read_b128 v[168:171], v230 offset:22528\n\t" \
  "v_mfma_f32_16x16x32_bf16 v[124:127], a[28:31], v[140:143], v[124:127]\n\t" \
  "ds_read_b128 v[172:175], v230 offset:23552\n\t" \
  "v_mfma_f32_16x16x32_bf16 v[124:127], a[20:23], v[136:139], v[124:127]\n\t" \
  "buffer_load_dwordx4 v[60:63], v242, s[16:19], s78 offen offset:3072 sc1\n\t" \
  "v_mfma_f32_16x16x32_bf16 v[124:127], a[12:15], v[132:135], v[124:127]\n\t" \
  "v_accvgpr_write_b32 a128, 0\n\t" \
  "v_accvgpr_write_b32 a129, 0\n\t" \
  "v_mfma_f32_16x16x32_bf16 v[124:127], a[4:7], v[128:131], v[124:127]\n\t" \
  "s_waitcnt lgkmcnt(4)\n\t" \
  "v_accvgpr_write_b32 a130, v248\n\t" \
  "v_accvgpr_write_b32 a131, v249\n\t" \
  "v_mfma_f32_16x16x32_bf16 v[96:99], a[32:35], v[144:147], v[96:99]\n\t" \
  "ds_read_b128 v[128:131], v230 offset:24576\n\t" \
  "v_accvgpr_write_b32 a132, 0\n\t" \
  "v_mfma_f32_16x16x32_bf16 v[96:99], a[40:43], v[148:151], v[96:99]\n\t" \
  "ds_read_b128 v[132:135], v230 offset:25600\n\t" \
  "v_accvgpr_write_b32 a133, 0\n\t" \
  "v_mfma_f32_16x16x32_bf16 v[96:99], a[48:51], v[152:155], v[96:99]\n\t" \
  "ds_read_b128 a[176:179], v236 offset:960\n\t" \
  "v_mfma_f32_16x16x32_bf16 v[96:99], a[56:59], v[156:159], v[96:99]\n\t" \
  "ds_read_b128 a[180:183], v236 offset:640\n\t" \
  "v_mfma_f32_16x16x32_bf16 v[112:115], a[60:63], v[156:159], v[112:115]\n\t" \
  "ds_read_b128 v[136:139], v230 offset:26624\n\t" \
  "v_mfma_f32_16x16x32_bf16 v[112:115], a[52:55], v[152:155], v[112:115]\n\t" \
  "ds_read_b128 a[184:187], v236 offset:320\n\t" \
  "v_mfma_f32_16x16x32_bf16 v[112:115], a[44:47], v[148:151], v[112:115]\n\t" \
  "ds_read_b128 v[140:143], v230 offset:27648\n\t" \
  "v_mfma_f32_16x16x32_bf16 v[112:115], a[36:39], v[144:147], v[112:115]\n\t" \
  "s_waitcnt lgkmcnt(7)\n\t" \
  "ds_read_b128 a[188:191], v236 offset:0\n\t" \
  "v_mfma_f32_16x16x32_bf16 v[100:103], a[32:35], v[160:163], v[100:103]\n\t" \
  "ds_read_b128 v[144:147], v230 offset:28672\n\t" \
  "v_mfma_f32_16x16x32_bf16 v[100:103], a[40:43], v[164:167], v[100:103]\n\t" \
  "ds_read_b128 v[148:151], v230 offset:29696\n\t" \
  "v_mfma_f32_16x16x32_bf16 v[100:103], a[48:51], v[168:171], v[100:103]\n\t" \
  "ds_read_b128 v[152:155], v230 offset:30720\n\t" \
  "v_mfma_f32_16x16x32_bf16 v[100:103], a[56:59], v[172:175], v[100:103]\n\t" \
  "ds_read_b128 v[156:159], v230 offset:31744\n\t" \
  "v_mfma_f32_16x16x32_bf16 v[116:119], a[60:63], v[172:175], v[116:119]\n\t" \
  "v_mfma_f32_16x16x32_bf16 v[116:119], a[52:55], v[168:171], v[116:119]\n\t" \
  "v_mfma_f32_16x16x32_bf16 v[116:119], a[44:47], v[164:167], v[116:119]\n\t" \
  "v_mfma_f32_16x16x32_bf16 v[116:119], a[36:39], v[160:163], v[116:119]\n\t" \
  "s_waitcnt lgkmcnt(5)\n\t" \
  "v_mfma_f32_16x16x32_bf16 v[104:107], a[32:35], v[128:131], v[104:107]\n\t" \
  "ds_read_b128 v[160:163], v232 offset:0\n\t" \
  "v_mfma_f32_16x16x32_bf16 v[104:107], a[40:43], v[132:135], v[104:107]\n\t" \
  "ds_read_b128 v[164:167], v232 offset:1024\n\t" \
  "v_mfma_f32_16x16x32_bf16 v[104:107], a[48:51], v[136:139], v[104:107]\n\t" \
  "ds_read_b128 v[168:171], v232 offset:2048\n\t" \
  "v_mfma_f32_16x16x32_bf16 v[104:107], a[56:59], v[140:143], v[104:107]\n\t" \
  "ds_read_b128 v[172:175], v232 offset:3072\n\t" \
  "v_mfma_f32_16x16x32_bf16 v[120:123], a[60:63], v[140:143], v[120:123]\n\t" \
  "v_mfma_f32_16x16x32_bf16 v[120:123], a[52:55], v[136:139], v[120:123]\n\t" \
  "v_mfma_f32_16x16x32_bf16 v[120:123], a[44:47], v[132:135], v[120:123]\n\t" \
  "v_mfma_f32_16x16x32_bf16 v[120:123], a[36:39], v[128:131], v[120:123]\n\t" \
  "s_waitcnt lgkmcnt(4)\n\t" \
  "v_mfma_f32_16x16x32_bf16 v[108:111], a[32:35], v[144:147], v[108:111]\n\t" \
  "ds_read_b128 v[128:131], v232 offset:4096\n\t" \
  "v_mfma_f32_16x16x32_bf16 v[108:111], a[40:43], v[148:151], v[108:111]\n\t" \
  "ds_read_b128 v[132:135], v232 offset:5120\n\t" \
  "v_mfma_f32_16x16x32_bf16 v[108:111], a[48:51], v[152:155], v[108:111]\n\t" \
  "ds_read_b128 v[136:139], v232 offset:6144\n\t" \
  "v_mfma_f32_16x16x32_bf16 v[108:111], a[56:59], v[156:159], v[108:111]\n\t" \
  "ds_read_b128 v[140:143], v232 offset:7168\n\t" \
  "v_mfma_f32_16x16x32_bf16 v[124:127], a[60:63], v[156:159], v[124:127]\n\t" \
  "buffer_load_dword v246, off, s[16:19], s73 offset:32 sc1\n\t" \
  "v_mfma_f32_16x16x32_bf16 v[124:127], a[52:55], v[152:155], v[124:127]\n\t" \
  "v_mfma_f32_16x16x32_bf16 v[124:127], a[44:47], v[148:151], v[124:127]\n\t" \
  "v_mfma_f32_16x16x32_bf16 v[124:127], a[36:39], v[144:147], v[124:127]\n\t" \
  "s_waitcnt lgkmcnt(4)\n\t" \
  "v_mfma_f32_16x16x32_bf16 v[96:99], a[64:67], v[160:163], v[96:99]\n\t" \
  "ds_read_b128 v[144:147], v232 offset:8192\n\t" \
  "v_mfma_f32_16x16x32_bf16 v[96:99], a[72:75], v[164:167], v[96:99]\n\t" \
  "ds_read_b128 v[148:151], v232 offset:9216\n\t" \
  "v_mfma_f32_16x16x32_bf16 v[96:99], a[80:83], v[168:171], v[96:99]\n\t" \
  "ds_read_b128 v[152:155], v232 offset:10240\n\t" \
  "v_mfma_f32_16x16x32_bf16 v[96:99], a[88:91], v[172:175], v[96:99]\n\t" \
  "ds_read_b128 v[156:159], v232 offset:11264\n\t" \
  "v_mfma_f32_16x16x32_bf16 v[112:115], a[92:95], v[172:175], v[112:115]\n\t" \
  "v_mfma_f32_16x16x32_bf16 v[112:115], a[84:87], v[168:171], v[112:115]\n\t" \
  "v_mfma_f32_16x16x32_bf16 v[112:115], a[76:79], v[164:167], v[112:115]\n\t" \
  "v_mfma_f32_16x16x32_bf16 v[112:115], a[68:71], v[160:163], v[112:115]\n\t" \
  "s_waitcnt lgkmcnt(4)\n\t" \
  "v_mfma_f32_16x16x32_bf16 v[100:103], a[64:67], v[128:131], v[100:103]\n\t" \
  "ds_read_b128 v[160:163], v232 offset:12288\n\t" \
  "v_mfma_f32_16x16x32_bf16 v[100:103], a[72:75], v[132:135], v[100:103]\n\t" \
  "ds_read_b128 v[164:167], v232 offset:13312\n\t" \
  "v_mfma_f32_16x16x32_bf16 v[100:103], a[80:83], v[136:139], v[100:103]\n\t" \
  "ds_read_b128 v[168:171], v232 offset:14336\n\t" \
  "v_mfma_f32_16x16x32_bf16 v[100:103], a[88:91], v[140:143], v[100:103]\n\t" \
  "ds_read_b128 v[172:175], v232 offset:15360\n\t" \
  "v_mfma_f32_16x16x32_bf16 v[116:119], a[92:95], v[140:143], v[116:119]\n\t" \
  "v_mfma_f32_16x16x32_bf16 v[116:119], a[84:87], v[136:139], v[116:119]\n\t" \
  "v_mfma_f32_16x16x32_bf16 v[116:119], a[76:79], v[132:135], v[116:119]\n\t" \
  "s_waitcnt vmcnt(9) lgkmcnt(4)\n\t" \
  "v_mfma_f32_16x16x32_bf16 v[116:119], a[68:71], v[128:131], v[116:119]\n\t" \
  "s_barrier\n\t" \
  "v_mfma_f32_16x16x32_bf16 v[104:107], a[64:67], v[144:147], v[104:107]\n\t" \
  "ds_read_b128 v[128:131], v230 offset:0\n\t" \
  "v_mfma_f32_16x16x32_bf16 v[104:107], a[72:75], v[148:151], v[104:107]\n\t" \
  "ds_read_b128 v[132:135], v230 offset:1024\n\t" \
  "v_mfma_f32_16x16x32_bf16 v[104:107], a[80:83], v[152:155], v[104:107]\n\t" \
  "ds_read_b128 v[136:139], v230 offset:2048\n\t" \
  "v_mfma_f32_16x16x32_bf16 v[104:107], a[88:91], v[156:159], v[104:107]\n\t" \
  "ds_read_b128 v[140:143], v230 offset:3072\n\t" \
  "v_mfma_f32_16x16x32_bf16 v[120:123], a[92:95], v[156:159], v[120:123]\n\t" \
  "v_mfma_f32_16x16x32_bf16 v[120:123], a[84:87], v[152:155], v[120:123]\n\t" \
  "v_mfma_f32_16x16x32_bf16 v[120:123], a[76:79], v[148:151], v[120:123]\n\t" \
  "v_mfma_f32_16x16x32_bf16 v[120:123], a[68:71], v[144:147], v[120:123]\n\t" \
  "s_waitcnt lgkmcnt(4)\n\t" \
  "v_mfma_f32_16x16x32_bf16 v[108:111], a[64:67], v[160:163], v[108:111]\n\t" \
  "ds_read_b128 v[144:147], v230 offset:4096\n\t" \
  "v_mfma_f32_16x16x32_bf16 v[108:111], a[72:75], v[164:167], v[108:111]\n\t" \
  "ds_read_b128 v[148:151], v230 offset:5120\n\t" \
  "v_mfma_f32_16x16x32_bf16 v[108:111], a[80:83], v[168:171], v[108:111]\n\t" \
  "ds_read_b128 v[152:155], v230 offset:6144\n\t" \
  "v_mfma_f32_16x16x32_bf16 v[108:111], a[88:91], v[172:175], v[108:111]\n\t" \
  "buffer_load_dword v251, off, s[16:19], s73 offset:32 sc1\n\t" \
  "v_mfma_f32_16x16x32_bf16 v[124:127], a[92:95], v[172:175], v[124:127]\n\t" \
  "ds_read_b128 v[156:159], v230 offset:7168\n\t" \
  "v_mfma_f32_16x16x32_bf16 v[124:127], a[84:87], v[168:171], v[124:127]\n\t" \
  "v_mfma_f32_16x16x32_bf16 v[124:127], a[76:79], v[164:167], v[124:127]\n\t" \
  "v_mfma_f32_16x16x32_bf16 v[124:127], a[68:71], v[160:163], v[124:127]\n\t" \
  "s_waitcnt lgkmcnt(4)\n\t" \
  "v_mfma_f32_16x16x32_bf16 v[96:99], a[96:99], v[128:131], v[96:99]\n\t" \
  "s_waitcnt vmcnt(1)\n\t" \
  "v_cmp_ne_u32 vcc, s36, v246\n\t" \
  "v_mfma_f32_16x16x32_bf16 v[96:99], a[104:107], v[132:135], v[96:99]\n\t" \
  "ds_read_b128 v[160:163], v230 offset:8192\n\t" \
  "v_mfma_f32_16x16x32_bf16 v[96:99], a[112:115], v[136:139], v[96:99]\n\t" \
  "ds_read_b128 v[164:167], v230 offset:9216\n\t" \
  "s_mov_b32 s88, vcc_lo\n\t" \
  "s_cbranch_vccnz .Lat_n2\n\t" \
  "buffer_load_dwordx4 v[64:67], v242, s[16:19], s79 offen offset:0 sc1\n\t" \
  "buffer_load_dwordx4 v[68:71], v242, s[16:19], s79 offen offset:1024 sc1\n\t" \
  "buffer_load_dwordx4 v[72:75], v242, s[16:19], s79 offen offset:2048 sc1\n\t" \
  "buffer_load_dwordx4 v[76:79], v242, s[16:19], s79 offen offset:3072 sc1\n\t" \
  ".Lat_n2:\n\t" \
  "v_mfma_f32_16x16x32_bf16 v[96:99], a[120:123], v[140:143], v[96:99]\n\t" \
  "ds_read_b128 v[168:171], v230 offset:10240\n\t" \
  "v_mfma_f32_16x16x32_bf16 v[112:115], a[124:127], v[140:143], v[112:115]\n\t" \
  "ds_read_b128 v[172:175], v230 offset:11264\n\t" \
  "v_mfma_f32_16x16x32_bf16 v[112:115], a[116:119], v[136:139], v[112:115]\n\t" \
  "v_mfma_f32_16x16x32_bf16 v[112:115], a[108:111], v[132:135], v[112:115]\n\t" \
  "v_mfma_f32_16x16x32_bf16 v[112:115], a[100:103], v[128:131], v[112:115]\n\t" \
  "s_waitcnt lgkmcnt(4)\n\t" \
  "v_mfma_f32_16x16x32_bf16 v[100:103], a[96:99], v[144:147], v[100:103]\n\t" \
  "ds_read_b128 v[128:131], v230 offset:12288\n\t" \
  "v_mfma_f32_16x16x32_bf16 v[100:103], a[104:107], v[148:151], v[100:103]\n\t" \
  "ds_read_b128 v[132:135], v230 offset:13312\n\t" \
  "v_mfma_f32_16x16x32_bf16 v[100:103], a[112:115], v[152:155], v[100:103]\n\t" \
  "ds_read_b128 v[136:139], v230 offset:14336\n\t" \
  "v_mfma_f32_16x16x32_bf16 v[100:103], a[120:123], v[156:159], v[100:103]\n\t" \
  "ds_read_b128 v[140:143], v230 offset:15360\n\t" \
  "v_mfma_f32_16x16x32_bf16 v[116:119], a[124:127], v[156:159], v[116:119]\n\t" \
  "v_mfma_f32_16x16x32_bf16 v[116:119], a[116:119], v[152:155], v[116:119]\n\t" \
  "v_mfma_f32_16x16x32_bf16 v[116:119], a[108:111], v[148:151], v[116:119]\n\t" \
  "v_mfma_f32_16x16x32_bf16 v[116:119], a[100:103], v[144:147], v[116:119]\n\t" \
  "s_waitcnt lgkmcnt(4)\n\t" \
  "v_mfma_f32_16x16x32_bf16 v[104:107], a[96:99], v[160:163], v[104:107]\n\t" \
  "ds_read_b128 v[144:147], v242 offset:16384\n\t" \
  "v_mfma_f32_16x16x32_bf16 v[104:107], a[104:107], v[164:167], v[104:107]\n\t" \
  "ds_read_b128 v[148:151], v242 offset:17408\n\t" \
  "v_mfma_f32_16x16x32_bf16 v[104:107], a[112:115], v[168:171], v[104:107]\n\t" \
  "ds_read_b128 v[152:155], v242 offset:18432\n\t" \
  "v_mfma_f32_16x16x32_bf16 v[104:107], a[120:123], v[172:175], v[104:107]\n\t" \
  "ds_read_b128 v[156:159], v242 offset:19456\n\t" \
  "v_mfma_f32_16x16x32_bf16 v[120:123], a[124:127], v[172:175], v[120:123]\n\t" \
  "v_mfma_f32_16x16x32_bf16 v[120:123], a[116:119], v[168:171], v[120:123]\n\t" \
  "v_mfma_f32_16x16x32_bf16 v[120:123], a[108:111], v[164:167], v[120:123]\n\t" \
  "v_mfma_f32_16x16x32_bf16 v[120:123], a[100:103], v[160:163], v[120:123]\n\t" \
  "s_waitcnt lgkmcnt(4)\n\t" \
  "v_mfma_f32_16x16x32_bf16 v[108:111], a[96:99], v[128:131], v[108:111]\n\t" \
  "ds_read_b128 v[160:163], v242 offset:20480\n\t" \
  "v_mfma_f32_16x16x32_bf16 v[108:111], a[104:107], v[132:135], v[108:111]\n\t" \
  "ds_read_b128 v[164:167], v242 offset:21504\n\t" \
  "v_mfma_f32_16x16x32_bf16 v[108:111], a[112:115], v[136:139], v[108:111]\n\t" \
  "ds_read_b128 v[168:171], v242 offset:22528\n\t" \
  "v_mfma_f32_16x16x32_bf16 v[108:111], a[120:123], v[140:143], v[108:111]\n\t" \
  "ds_read_b128 v[172:175], v242 offset:23552\n\t" \
  "v_mfma_f32_16x16x32_bf16 v[124:127], a[124:127], v[140:143], v[124:127]\n\t" \
  "v_mfma_f32_16x16x32_bf16 v[124:127], a[116:119], v[136:139], v[124:127]\n\t" \
  "v_mfma_f32_16x16x32_bf16 v[124:127], a[108:111], v[132:135], v[124:127]\n\t" \
  "v_mfma_f32_16x16x32_bf16 v[124:127], a[100:103], v[128:131], v[124:127]\n\t" \
  "v_mfma_f32_16x16x32_bf16 v[96:99], a[130:133], v[32:35], v[96:99]\n\t" \
  "v_mfma_f32_16x16x32_bf16 v[100:103], a[130:133], v[36:39], v[100:103]\n\t" \
  "v_mfma_f32_16x16x32_bf16 v[104:107], a[130:133], v[40:43], v[104:107]\n\t" \
  "v_mfma_f32_16x16x32_bf16 v[108:111], a[130:133], v[44:47], v[108:111]\n\t" \
  "v_mfma_f32_16x16x32_bf16 v[112:115], a[128:131], v[32:35], v[112:115]\n\t" \
  "v_mfma_f32_16x16x32_bf16 v[116:119], a[128:131], v[36:39], v[116:119]\n\t" \
  "v_mfma_f32_16x16x32_bf16 v[120:123], a[128:131], v[40:43], v[120:123]\n\t" \
  "v_mfma_f32_16x16x32_bf16 v[124:127], a[128:131], v[44:47], v[124:127]\n\t" \
  "s_waitcnt lgkmcnt(4)\n\t" \
  "v_mfma_f32_16x16x32_bf16 v[96:99], a[192:195], v[144:147], v[96:99]\n\t" \
  "s_cmp_lg_u32 s88, 0\n\t" \
  "s_cbranch_scc1 .Lat_m3\n\t" \
  ".Lat_n3:\n\t" \
  "v_mfma_f32_16x16x32_bf16 v[96:99], a[200:203], v[148:151], v[96:99]\n\t" \
  "s_add_u32 m0, s38, 32768\n\t" \
  "ds_read_b128 v[128:131], v242 offset:24576\n\t" \
  "s_mov_b32 s96, 0\n\t" \
  "v_mfma_f32_16x16x32_bf16 v[96:99], a[208:211], v[152:155], v[96:99]\n\t" \
  ".Lhs15:\n\ts_cbranch_execz .Lhs15n\n\t" \
  "buffer_load_dwordx4 v252, s[28:31], s96 offen offset:0 lds\n\t" \
  "v_mfma_f32_16x16x32_bf16 v[96:99], a[216:219], v[156:159], v[96:99]\n\t" \
  "ds_read_b128 v[132:135], v242 offset:25600\n\t" \
  "s_mov_b32 s96, 3072\n\t" \
  "v_mfma_f32_16x16x32_bf16 v[112:115], a[220:223], v[156:159], v[112:115]\n\t" \
  "buffer_load_dwordx4 v252, s[28:31], s96 offen offset:1024 lds\n\t" \
  "v_mfma_f32_16x16x32_bf16 v[112:115], a[212:215], v[152:155], v[112:115]\n\t" \
  "ds_read_b128 v[136:139], v242 offset:26624\n\t" \
  "s_mov_b32 s96, 6144\n\t" \
  "v_mfma_f32_16x16x32_bf16 v[112:115], a[204:207], v[148:151], v[112:115]\n\t" \
  "buffer_load_dwordx4 v252, s[28:31], s96 offen offset:2048 lds\n\t" \
  "v_mfma_f32_16x16x32_bf16 v[112:115], a[196:199], v[144:147], v[112:115]\n\t" \
  "ds_read_b128 v[140:143], v242 offset:27648\n\t" \
  "s_mov_b32 s96, 9216\n\t" \
  "v_mfma_f32_16x16x32_bf16 v[96:99], a[130:133], v[48:51], v[96:99]\n\t" \
  "buffer_load_dwordx4 v252, s[28:31], s96 offen offset:3072 lds\n\t" \
  "v_mfma_f32_16x16x32_bf16 v[100:103], a[130:133], v[52:55], v[100:103]\n\t" \
  "s_add_u32 m0, s38, 49152\n\t" \
  "s_mov_b32 s96, 16384\n\t" \
  "v_mfma_f32_16x16x32_bf16 v[104:107], a[130:133], v[56:59], v[104:107]\n\t" \
  "buffer_load_dwordx4 v252, s[28:31], s96 offen offset:0 lds\n\t" \
  "v_mfma_f32_16x16x32_bf16 v[108:111], a[130:133], v[60:63], v[108:111]\n\t" \
  "s_mov_b32 s96, 19456\n\t" \
  "v_mfma_f32_16x16x32_bf16 v[112:115], a[128:131], v[48:51], v[112:115]\n\t" \
  "buffer_load_dwordx4 v252, s[28:31], s96 offen offset:1024 lds\n\t" \
  "v_mfma_f32_16x16x32_bf16 v[116:119], a[128:131], v[52:55], v[116:119]\n\t" \
  "s_mov_b32 s96, 22528\n\t" \
  "v_mfma_f32_16x16x32_bf16 v[120:123], a[128:131], v[56:59], v[120:123]\n\t" \
  "buffer_load_dwordx4 v252, s[28:31], s96 offen offset:2048 lds\n\t" \
  "v_mfma_f32_16x16x32_bf16 v[124:127], a[128:131], v[60:63], v[124:127]\n\t" \
  "s_waitcnt lgkmcnt(4)\n\t" \
  "s_mov_b32 s96, 25600\n\t" \
  "v_mfma_f32_16x16x32_bf16 v[100:103], a[192:195], v[160:163], v[100:103]\n\t" \
  "buffer_load_dwordx4 v252, s[28:31], s96 offen offset:3072 lds\n\t" \
  "v_mfma_f32_16x16x32_bf16 v[100:103], a[200:203], v[164:167], v[100:103]\n\t" \
  "ds_read_b128 v[144:147], v242 offset:28672\n\t" \
  "v_mfma_f32_16x16x32_bf16 v[100:103], a[208:211], v[168:171], v[100:103]\n\t" \
  "ds_read_b128 v[148:151], v242 offset:29696\n\t" \
  "v_mfma_f32_16x16x32_bf16 v[100:103], a[216:219], v[172:175], v[100:103]\n\t" \
  "ds_read_b128 v[152:155], v242 offset:30720\n\t" \
  "v_mfma_f32_16x16x32_bf16 v[116:119], a[220:223], v[172:175], v[116:119]\n\t" \
  "ds_read_b128 v[156:159], v242 offset:31744\n\t" \
  "v_mfma_f32_16x16x32_bf16 v[116:119], a[212:215], v[168:171], v[116:119]\n\t" \
  "v_mfma_f32_16x16x32_bf16 v[116:119], a[204:207], v[164:167], v[116:119]\n\t" \
  "v_mfma_f32_16x16x32_bf16 v[116:119], a[196:199], v[160:163], v[116:119]\n\t" \
  "s_waitcnt lgkmcnt(4)\n\t" \
  "v_mfma_f32_16x16x32_bf16 v[104:107], a[192:195], v[128:131], v[104:107]\n\t" \
  "ds_read_b128 v[160:163], v237 offset:2560\n\t" \
  "v_mfma_f32_16x16x32_bf16 v[104:107], a[200:203], v[132:135], v[104:107]\n\t" \
  "ds_read_b128 v[164:167], v237 offset:2624\n\t" \
  "v_mfma_f32_16x16x32_bf16 v[104:107], a[208:211], v[136:139], v[104:107]\n\t" \
  "v_mfma_f32_16x16x32_bf16 v[104:107], a[216:219], v[140:143], v[104:107]\n\t" \
  "v_mfma_f32_16x16x32_bf16 v[120:123], a[220:223], v[140:143], v[120:123]\n\t" \
  "v_mfma_f32_16x16x32_bf16 v[120:123], a[212:215], v[136:139], v[120:123]\n\t" \
  "v_mfma_f32_16x16x32_bf16 v[120:123], a[204:207], v[132:135], v[120:123]\n\t" \
  "v_mfma_f32_16x16x32_bf16 v[120:123], a[196:199], v[128:131], v[120:123]\n\t" \
  "s_waitcnt lgkmcnt(2)\n\t" \
  "v_mfma_f32_16x16x32_bf16 v[108:111], a[192:195], v[144:147], v[108:111]\n\t" \
  "v_mfma_f32_16x16x32_bf16 v[108:111], a[200:203], v[148:151], v[108:111]\n\t" \
  "v_mfma_f32_16x16x32_bf16 v[108:111], a[208:211], v[152:155], v[108:111]\n\t" \
  "v_mfma_f32_16x16x32_bf16 v[108:111], a[216:219], v[156:159], v[108:111]\n\t" \
  "v_mfma_f32_16x16x32_bf16 v[124:127], a[220:223], v[156:159], v[124:127]\n\t" \
  "v_mfma_f32_16x16x32_bf16 v[124:127], a[212:215], v[152:155], v[124:127]\n\t" \
  "v_mfma_f32_16x16x32_bf16 v[124:127], a[204:207], v[148:151], v[124:127]\n\t" \
  "v_mfma_f32_16x16x32_bf16 v[124:127], a[196:199], v[144:147], v[124:127]\n\t" \
  STP("s_memrealtime s[94:95]\n\t") \
  STP("s_memtime s[92:93]\n\t") \
  "s_cmp_lg_u32 s88, 0\n\t" \
  "s_cbranch_scc1 .Lat_s99\n\t" \
  ".Lat_d2:\n\t" \
  "s_waitcnt vmcnt(8)\n\t" \
  "v_mfma_f32_16x16x32_bf16 v[96:99], a[130:133], v[64:67], v[96:99]\n\t" \
  "v_mfma_f32_16x16x32_bf16 v[100:103], a[130:133], v[68:71], v[100:103]\n\t" \
  "v_mfma_f32_16x16x32_bf16 v[104:107], a[130:133], v[72:75], v[104:107]\n\t" \
  "v_mfma_f32_16x16x32_bf16 v[108:111], a[130:133], v[76:79], v[108:111]\n\t" \
  "v_mfma_f32_16x16x32_bf16 v[112:115], a[128:131], v[64:67], v[112:115]\n\t" \
  "v_mfma_f32_16x16x32_bf16 v[116:119], a[128:131], v[68:71], v[116:119]\n\t" \
  "v_mfma_f32_16x16x32_bf16 v[120:123], a[128:131], v[72:75], v[120:123]\n\t" \
  "v_mfma_f32_16x16x32_bf16 v[124:127], a[128:131], v[76:79], v[124:127]\n\t" \
  "s_waitcnt lgkmcnt(0)\n\t" \
  STP("v_writelane_b32 v255, s94, 38\n\t") \
  STP("v_writelane_b32 v255, s92, 39\n\t") \
  "s_nop 7\n\t" \
  ".subsection 1\n\t" \
  ".Lat_x:\n\t" \
  "v_lshlrev_b32 v209, 12, v233\n\t" \
  "v_add_u32 v209, 98304, v209\n\t" \
  "s_mov_b64 exec, 15\n\t" \
  "ds_write_b32 v209, v243\n\t" \
  "s_mov_b64 exec, -1\n\t" \
  "s_waitcnt lgkmcnt(0)\n\t" \
  "s_branch .Lat_y\n\t" \
  ".Lat_rd:\n\t" \
  "s_waitcnt vmcnt(0) lgkmcnt(0)\n\t" \
  "s_barrier\n\t" \
  "s_nop 7\n\t" \
  "v_lshl_add_u32 v0, s3, 6, v233\n\t" \
  "s_mov_b32 s37, 0\n\t" \
  "s_branch .Lfr_again\n\t" \
  ".Lat_s0:\n\t" \
  "s_mov_b32 s81, 0\n\t" \
  ".Lat_r0:\n\t" \
  "s_sleep 4\n\t" \
  "buffer_load_dword v246, v245, s[16:19], s65 offen sc1\n\t" \
  "s_waitcnt vmcnt(0)\n\t" \
  "v_cmp_ne_u32 vcc, s36, v246\n\t" \
  "s_and_b32 vcc_lo, vcc_lo, 0xe\n\t" \
  "s_cbranch_scc0 .Lat_k0\n\t" \
  "s_add_u32 s81, s81, 1\n\t" \
  "s_cmp_lt_u32 s81, " FQ_CAP "\n\t" \
  "s_cbranch_scc1 .Lat_r0\n\t" \
  "buffer_store_dword v243, off, s[16:19], 0 offset:256 sc1\n\t" \
  "s_branch .Lat_k0\n\t" \
  ".Lat_s1:\n\t" \
  "s_mov_b32 s81, 0\n\t" \
  ".Lat_r1:\n\t" \
  "s_sleep 4\n\t" \
  "buffer_load_dword v246, v245, s[16:19], s65 offen sc1\n\t" \
  "s_waitcnt vmcnt(0)\n\t" \
  "v_cmp_ne_u32 vcc, s36, v246\n\t" \
  "s_and_b32 vcc_lo, vcc_lo, 0xfefe\n\t" \
  "s_cbranch_scc0 .Lat_k1\n\t" \
  "s_add_u32 s81, s81, 1\n\t" \
  "s_cmp_lt_u32 s81, " FQ_CAP "\n\t" \
  "s_cbranch_scc1 .Lat_r1\n\t" \
  "buffer_store_dword v243, off, s[16:19], 0 offset:256 sc1\n\t" \
  "s_branch .Lat_k1\n\t" \
  ".Lat_s2:\n\t" \
  "s_mov_b32 s81, 0\n\t" \
  ".Lat_r2:\n\t" \
  "s_sleep 4\n\t" \
  "buffer_load_dword v246, v245, s[16:19], s65 offen sc1\n\t" \
  "s_waitcnt vmcnt(0)\n\t" \
  "v_cmp_ne_u32 vcc, s36, v246\n\t" \
  "s_and_b32 vcc_lo, vcc_lo, 0xfe0000\n\t" \
  "s_cbranch_scc0 .Lat_k2\n\t" \
  "s_add_u32 s81, s81, 1\n\t" \
  "s_cmp_lt_u32 s81, " FQ_CAP "\n\t" \
  "s_cbranch_scc1 .Lat_r2\n\t" \
  "buffer_store_dword v243, off, s[16:19], 0 offset:256 sc1\n\t" \
  "s_branch .Lat_k2\n\t" \
  ".Lat_s3:\n\t" \
  "s_mov_b32 s81, 0\n\t" \
  ".Lat_r3:\n\t" \
  "s_sleep 4\n\t" \
  "s_add_u32 s78, s75, 0\n\t" \
  "buffer_load_dwordx4 v[208:211], v234, s[16:19], s78 offen sc1\n\t" \
  "s_add_u32 s78, s75, 32768\n\t" \
  "buffer_load_dwordx4 v[212:215], v234, s[16:19], s78 offen sc1\n\t" \
  "s_add_u32 s78, s75, 65536\n\t" \
  "buffer_load_dwordx4 v[216:219], v234, s[16:19], s78 offen sc1\n\t" \
  "s_add_u32 s78, s75, 98304\n\t" \
  "buffer_load_dwordx4 v[220:223], v234, s[16:19], s78 offen sc1\n\t" \
  "s_waitcnt vmcnt(0)\n\t" \
  "v_xor_b32 v246, v243, v209\n\t" \
  "v_bitop3_b32 v246, v246, v211, v243 bitop3:0xf6\n\t" \
  "v_bitop3_b32 v246, v246, v213, v243 bitop3:0xf6\n\t" \
  "v_bitop3_b32 v246, v246, v215, v243 bitop3:0xf6\n\t" \
  "v_bitop3_b32 v246, v246, v217, v243 bitop3:0xf6\n\t" \
  "v_bitop3_b32 v246, v246, v219, v243 bitop3:0xf6\n\t" \
  "v_bitop3_b32 v246, v246, v221, v243 bitop3:0xf6\n\t" \
  "v_bitop3_b32 v246, v246, v223, v243 bitop3:0xf6\n\t" \
  "v_cmp_ne_u32 vcc, 0, v246\n\t" \
  "s_cbranch_vccz .Lat_k3\n\t" \
  "s_add_u32 s81, s81, 1\n\t" \
  "s_cmp_lt_u32 s81, " FQ_CAP "\n\t" \
  "s_cbranch_scc1 .Lat_r3\n\t" \
  "buffer_store_dword v243, off, s[16:19], 0 offset:256 sc1\n\t" \
  "s_branch .Lat_k3\n\t" \
  ".Lat_s4:\n\t" \
  "s_mov_b32 s81, 0\n\t" \
  ".Lat_r4:\n\t" \
  "s_sleep 4\n\t" \
  "buffer_load_dword v246, v235, s[16:19], s73 offen sc1\n\t" \
  "s_waitcnt vmcnt(0)\n\t" \
  "v_cmp_ne_u32 vcc, s36, v246\n\t" \
  "s_cbranch_vccz .Lat_k4\n\t" \
  "s_add_u32 s81, s81, 1\n\t" \
  "s_cmp_lt_u32 s81, " FQ_CAP "\n\t" \
  "s_cbranch_scc1 .Lat_r4\n\t" \
  "buffer_store_dword v243, off, s[16:19], 0 offset:256 sc1\n\t" \
  "s_branch .Lat_k4\n\t" \
  ".Lat_m3:\n\t" \
  "s_waitcnt vmcnt(0)\n\t" \
  "v_cmp_ne_u32 vcc, s36, v251\n\t" \
  "s_mov_b32 s88, vcc_lo\n\t" \
  "s_cbranch_vccnz .Lat_n3\n\t" \
  "buffer_load_dwordx4 v[64:67], v242, s[16:19], s79 offen offset:0 sc1\n\t" \
  "buffer_load_dwordx4 v[68:71], v242, s[16:19], s79 offen offset:1024 sc1\n\t" \
  "buffer_load_dwordx4 v[72:75], v242, s[16:19], s79 offen offset:2048 sc1\n\t" \
  "buffer_load_dwordx4 v[76:79], v242, s[16:19], s79 offen offset:3072 sc1\n\t" \
  "s_branch .Lat_n3\n\t" \
  ".Lat_v:\n\t" \
  "s_mov_b32 s98, 0\n\t" \
  ".Lat_v1:\n\t" \
  "s_sleep 4\n\t" \
  "buffer_load_dword v209, v250, s[16:19], 0 offen sc1\n\t" \
  "s_waitcnt vmcnt(0)\n\t" \
  "v_and_b32 v251, 0xbfffbfff, v209\n\t" \
  "v_cmp_ne_u32_e64 s[84:85], s36, v251\n\t" \
  "s_cmp_lg_u64 s[84:85], 0\n\t" \
  "s_cbranch_scc0 .Lat_w\n\t" \
  "s_add_u32 s98, s98, 1\n\t" \
  "s_cmp_lt_u32 s98, " FQ_CAP "\n\t" \
  "s_cbranch_scc1 .Lat_v1\n\t" \
  "v_mov_b32 v251, s36\n\t" \
  "buffer_store_dword v251, off, s[16:19], 0 offset:256 sc1\n\t" \
  "s_branch .Lat_w\n\t" \
  ".Lat_s99:\n\t" \
  "s_mov_b32 s81, 0\n\t" \
  ".Lat_r99:\n\t" \
  "buffer_load_dword v246, off, s[16:19], s73 offset:32 sc1\n\t" \
  "s_waitcnt vmcnt(0)\n\t" \
  "v_cmp_ne_u32 vcc, s36, v246\n\t" \
  "s_cbranch_vccz .Lat_k99\n\t" \
  "s_add_u32 s81, s81, 1\n\t" \
  "s_cmp_lt_u32 s81, " FQ_CAP "\n\t" \
  "s_cbranch_scc1 .Lat_r99\n\t" \
  "buffer_store_dword v243, off, s[16:19], 0 offset:256 sc1\n\t" \
  ".Lat_k99:\n\t" \
  "buffer_load_dwordx4 v[64:67], v242, s[16:19], s79 offen offset:0 sc1\n\t" \
  "buffer_load_dwordx4 v[68:71], v242, s[16:19], s79 offen offset:1024 sc1\n\t" \
  "buffer_load_dwordx4 v[72:75], v242, s[16:19], s79 offen offset:2048 sc1\n\t" \
  "buffer_load_dwordx4 v[76:79], v242, s[16:19], s79 offen offset:3072 sc1\n\t" \
  "s_waitcnt vmcnt(0)\n\t" \
  "s_branch .Lat_d2\n\t" \
  "s_endpgm\n\t" \
  ".Lfz_z:\n\t" \
  ".fill 128, 4, 0xBF800000\n\t" \
  ".subsection 2\n\t" \
  ".Lsf_go:\n\t" \
  "v_mfma_f32_16x16x32_bf16 v[28:31], a[156:159], a[224:227], v[28:31]\n\t" \
  "v_mov_b32 v196, 1.0\n\t" \
  "v_mov_b32 v197, 1.0\n\t" \
  "v_mfma_f32_16x16x32_bf16 v[28:31], a[92:95], a[240:243], v[28:31]\n\t" \
  "v_mov_b32 v198, 1.0\n\t" \
  "v_mov_b32 v199, 1.0\n\t" \
  "v_mfma_f32_16x16x32_bf16 v[24:27], a[152:155], a[224:227], v[24:27]\n\t" \
  "s_mov_b32 s99, 0\n\t" \
  "v_add_u32 v210, 65536, v228\n\t" \
  "v_add_u32 v211, 65536, v229\n\t" \
  "v_mfma_f32_16x16x32_bf16 v[24:27], a[88:91], a[240:243], v[24:27]\n\t" \
  "v_mov_b32 v216, 0x3f803f80\n\t" \
  "v_cvt_pk_bf16_f32 v31, v30, v31\n\t" \
  "v_mfma_f32_16x16x32_bf16 v[20:23], a[148:151], a[224:227], v[20:23]\n\t" \
  "v_mov_b32 v217, 0x3f803f80\n\t" \
  "v_cvt_pk_bf16_f32 v30, v28, v29\n\t" \
  "v_mfma_f32_16x16x32_bf16 v[20:23], a[84:87], a[240:243], v[20:23]\n\t" \
  "v_mov_b32 v218, 0x3f803f80\n\t" \
  "v_mov_b32 v219, 0x3f803f80\n\t" \
  "v_mfma_f32_16x16x32_bf16 v[16:19], a[144:147], a[224:227], v[16:19]\n\t" \
  "v_cvt_pk_bf16_f32 v28, v24, v25\n\t" \
  "v_cvt_pk_bf16_f32 v29, v26, v27\n\t" \
  "v_mfma_f32_16x16x32_bf16 v[16:19], a[80:83], a[240:243], v[16:19]\n\t" \
  "v_permlane16_swap_b32 v28, v30\n\t" \
  "v_mfma_f32_16x16x32_bf16 v[12:15], a[140:143], a[224:227], v[12:15]\n\t" \
  "v_cvt_pk_bf16_f32 v27, v22, v23\n\t" \
  "v_cvt_pk_bf16_f32 v26, v20, v21\n\t" \
  "v_mfma_f32_16x16x32_bf16 v[12:15], a[76:79], a[240:243], v[12:15]\n\t" \
  "v_permlane16_swap_b32 v29, v31\n\t" \
  "v_mfma_f32_16x16x32_bf16 v[8:11], a[136:139], a[224:227], v[8:11]\n\t" \
  "v_mfma_f32_16x16x32_bf16 v[8:11], a[72:75], a[240:243], v[8:11]\n\t" \
  "v_cvt_pk_bf16_f32 v24, v16, v17\n\t" \
  "v_cvt_pk_bf16_f32 v25, v18, v19\n\t" \
  "v_mfma_f32_16x16x32_bf16 v[4:7], a[132:135], a[224:227], v[4:7]\n\t" \
  "v_permlane16_swap_b32 v24, v26\n\t" \
  "v_mfma_f32_16x16x32_bf16 v[4:7], a[68:71], a[240:243], v[4:7]\n\t" \
  "v_cvt_pk_bf16_f32 v23, v14, v15\n\t" \
  "v_cvt_pk_bf16_f32 v22, v12, v13\n\t" \
  "v_mfma_f32_16x16x32_bf16 v[0:3], a[128:131], a[224:227], v[0:3]\n\t" \
  "s_waitcnt vmcnt(2)\n\t" \
  "v_cvt_pk_bf16_f32 v20, v8, v9\n\t" \
  "v_cvt_pk_bf16_f32 v21, v10, v11\n\t" \
  "v_mfma_f32_16x16x32_bf16 v[0:3], a[64:67], a[240:243], v[0:3]\n\t" \
  "s_barrier\n\t" \
  "v_mfma_f32_16x16x32_bf16 v[60:63], a[156:159], a[228:231], v[60:63]\n\t" \
  "v_cvt_pk_bf16_f32 v19, v6, v7\n\t" \
  "v_cvt_pk_bf16_f32 v18, v4, v5\n\t" \
  "v_mfma_f32_16x16x32_bf16 v[60:63], a[92:95], a[244:247], v[60:63]\n\t" \
  "v_permlane16_swap_b32 v25, v27\n\t" \
  "v_mfma_f32_16x16x32_bf16 v[56:59], a[152:155], a[228:231], v[56:59]\n\t" \
  "ds_read_b128 a[0:3], v220 offset:0\n\t" \
  "v_cvt_pk_bf16_f32 v16, v0, v1\n\t" \
  "v_mfma_f32_16x16x32_bf16 v[56:59], a[88:91], a[244:247], v[56:59]\n\t" \
  "ds_read_b128 a[4:7], v221 offset:0\n\t" \
  "v_cvt_pk_bf16_f32 v17, v2, v3\n\t" \
  "v_mfma_f32_16x16x32_bf16 v[52:55], a[148:151], a[228:231], v[52:55]\n\t" \
  "v_cvt_pk_bf16_f32 v63, v62, v63\n\t" \
  "v_cvt_pk_bf16_f32 v62, v60, v61\n\t" \
  "v_mfma_f32_16x16x32_bf16 v[52:55], a[84:87], a[244:247], v[52:55]\n\t" \
  "v_permlane16_swap_b32 v20, v22\n\t" \
  "v_mfma_f32_16x16x32_bf16 v[48:51], a[144:147], a[228:231], v[48:51]\n\t" \
  "ds_read_b128 a[8:11], v222 offset:0\n\t" \
  "v_cvt_pk_bf16_f32 v60, v56, v57\n\t" \
  "v_mfma_f32_16x16x32_bf16 v[48:51], a[80:83], a[244:247], v[48:51]\n\t" \
  "ds_read_b128 a[12:15], v223 offset:0\n\t" \
  "v_cvt_pk_bf16_f32 v61, v58, v59\n\t" \
  "v_mfma_f32_16x16x32_bf16 v[44:47], a[140:143], a[228:231], v[44:47]\n\t" \
  "v_cvt_pk_bf16_f32 v59, v54, v55\n\t" \
  "v_cvt_pk_bf16_f32 v58, v52, v53\n\t" \
  "v_mfma_f32_16x16x32_bf16 v[44:47], a[76:79], a[244:247], v[44:47]\n\t" \
  "v_permlane16_swap_b32 v21, v23\n\t" \
  "v_mfma_f32_16x16x32_bf16 v[40:43], a[136:139], a[228:231], v[40:43]\n\t" \
  "ds_read_b128 a[16:19], v220 offset:4096\n\t" \
  "v_cvt_pk_bf16_f32 v56, v48, v49\n\t" \
  "v_mfma_f32_16x16x32_bf16 v[40:43], a[72:75], a[244:247], v[40:43]\n\t" \
  "ds_read_b128 a[20:23], v221 offset:4096\n\t" \
  "v_cvt_pk_bf16_f32 v57, v50, v51\n\t" \
  "v_mfma_f32_16x16x32_bf16 v[36:39], a[132:135], a[228:231], v[36:39]\n\t" \
  "v_cvt_pk_bf16_f32 v55, v46, v47\n\t" \
  "v_cvt_pk_bf16_f32 v54, v44, v45\n\t" \
  "v_mfma_f32_16x16x32_bf16 v[36:39], a[68:71], a[244:247], v[36:39]\n\t" \
  "v_permlane16_swap_b32 v16, v18\n\t" \
  "v_mfma_f32_16x16x32_bf16 v[32:35], a[128:131], a[228:231], v[32:35]\n\t" \
  "ds_read_b128 a[24:27], v222 offset:4096\n\t" \
  "v_cvt_pk_bf16_f32 v52, v40, v41\n\t" \
  "v_mfma_f32_16x16x32_bf16 v[32:35], a[64:67], a[244:247], v[32:35]\n\t" \
  "ds_read_b128 a[28:31], v223 offset:4096\n\t" \
  "v_cvt_pk_bf16_f32 v53, v42, v43\n\t" \
  "v_mfma_f32_16x16x32_bf16 v[92:95], a[156:159], a[232:235], v[92:95]\n\t" \
  "v_cvt_pk_bf16_f32 v51, v38, v39\n\t" \
  "v_cvt_pk_bf16_f32 v50, v36, v37\n\t" \
  "v_mfma_f32_16x16x32_bf16 v[92:95], a[92:95], a[248:251], v[92:95]\n\t" \
  "v_permlane16_swap_b32 v17, v19\n\t" \
  "v_mfma_f32_16x16x32_bf16 v[88:91], a[152:155], a[232:235], v[88:91]\n\t" \
  "v_cvt_pk_bf16_f32 v48, v32, v33\n\t" \
  "v_cvt_pk_bf16_f32 v49, v34, v35\n\t" \
  "v_mfma_f32_16x16x32_bf16 v[88:91], a[88:91], a[248:251], v[88:91]\n\t" \
  "v_permlane16_swap_b32 v60, v62\n\t" \
  "v_mfma_f32_16x16x32_bf16 v[84:87], a[148:151], a[232:235], v[84:87]\n\t" \
  "ds_read_b128 a[32:35], v220 offset:8192\n\t" \
  "v_cvt_pk_bf16_f32 v95, v94, v95\n\t" \
  "v_mfma_f32_16x16x32_bf16 v[84:87], a[84:87], a[248:251], v[84:87]\n\t" \
  "ds_read_b128 a[36:39], v221 offset:8192\n\t" \
  "v_cvt_pk_bf16_f32 v94, v92, v93\n\t" \
  "v_mfma_f32_16x16x32_bf16 v[80:83], a[144:147], a[232:235], v[80:83]\n\t" \
  "v_cvt_pk_bf16_f32 v92, v88, v89\n\t" \
  "v_permlane16_swap_b32 v61, v63\n\t" \
  "v_mfma_f32_16x16x32_bf16 v[80:83], a[80:83], a[248:251], v[80:83]\n\t" \
  "ds_read_b128 a[40:43], v222 offset:8192\n\t" \
  "v_cvt_pk_bf16_f32 v93, v90, v91\n\t" \
  "v_mfma_f32_16x16x32_bf16 v[76:79], a[140:143], a[232:235], v[76:79]\n\t" \
  "v_cvt_pk_bf16_f32 v91, v86, v87\n\t" \
  "v_cvt_pk_bf16_f32 v90, v84, v85\n\t" \
  "v_mfma_f32_16x16x32_bf16 v[76:79], a[76:79], a[248:251], v[76:79]\n\t" \
  "ds_read_b128 a[44:47], v223 offset:8192\n\t" \
  "v_permlane16_swap_b32 v56, v58\n\t" \
  "v_mfma_f32_16x16x32_bf16 v[72:75], a[136:139], a[232:235], v[72:75]\n\t" \
  "v_cvt_pk_bf16_f32 v88, v80, v81\n\t" \
  "v_permlane16_swap_b32 v57, v59\n\t" \
  "v_mfma_f32_16x16x32_bf16 v[72:75], a[72:75], a[248:251], v[72:75]\n\t" \
  "ds_read_b128 a[48:51], v220 offset:12288\n\t" \
  "v_cvt_pk_bf16_f32 v89, v82, v83\n\t" \
  "v_mfma_f32_16x16x32_bf16 v[68:71], a[132:135], a[232:235], v[68:71]\n\t" \
  "ds_read_b128 a[52:55], v221 offset:12288\n\t" \
  "v_cvt_pk_bf16_f32 v87, v78, v79\n\t" \
  "v_mfma_f32_16x16x32_bf16 v[68:71], a[68:71], a[248:251], v[68:71]\n\t" \
  "v_cvt_pk_bf16_f32 v86, v76, v77\n\t" \
  "v_permlane16_swap_b32 v52, v54\n\t" \
  "v_mfma_f32_16x16x32_bf16 v[64:67], a[128:131], a[232:235], v[64:67]\n\t" \
  "ds_read_b128 a[56:59], v222 offset:12288\n\t" \
  "v_permlane16_swap_b32 v53, v55\n\t" \
  "v_mfma_f32_16x16x32_bf16 v[64:67], a[64:67], a[248:251], v[64:67]\n\t" \
  "v_cvt_pk_bf16_f32 v84, v72, v73\n\t" \
  "v_cvt_pk_bf16_f32 v85, v74, v75\n\t" \
  "v_mfma_f32_16x16x32_bf16 v[124:127], a[156:159], a[236:239], v[124:127]\n\t" \
  "ds_read_b128 a[60:63], v223 offset:12288\n\t" \
  "v_cvt_pk_bf16_f32 v83, v70, v71\n\t" \
  "v_mfma_f32_16x16x32_bf16 v[124:127], a[92:95], a[252:255], v[124:127]\n\t" \
  "v_cvt_pk_bf16_f32 v82, v68, v69\n\t" \
  "v_permlane16_swap_b32 v48, v50\n\t" \
  "v_mfma_f32_16x16x32_bf16 v[120:123], a[152:155], a[236:239], v[120:123]\n\t" \
  "v_cvt_pk_bf16_f32 v80, v64, v65\n\t" \
  "v_permlane16_swap_b32 v49, v51\n\t" \
  "v_mfma_f32_16x16x32_bf16 v[120:123], a[88:91], a[252:255], v[120:123]\n\t" \
  "v_permlane16_swap_b32 v92, v94\n\t" \
  "v_mfma_f32_16x16x32_bf16 v[116:119], a[148:151], a[236:239], v[116:119]\n\t" \
  "v_cvt_pk_bf16_f32 v81, v66, v67\n\t" \
  "v_cvt_pk_bf16_f32 v127, v126, v127\n\t" \
  "v_mfma_f32_16x16x32_bf16 v[116:119], a[84:87], a[252:255], v[116:119]\n\t" \
  "v_permlane16_swap_b32 v93, v95\n\t" \
  "v_mfma_f32_16x16x32_bf16 v[112:115], a[144:147], a[236:239], v[112:115]\n\t" \
  "v_cvt_pk_bf16_f32 v126, v124, v125\n\t" \
  "v_cvt_pk_bf16_f32 v124, v120, v121\n\t" \
  "v_mfma_f32_16x16x32_bf16 v[112:115], a[80:83], a[252:255], v[112:115]\n\t" \
  "v_cvt_pk_bf16_f32 v125, v122, v123\n\t" \
  "v_permlane16_swap_b32 v88, v90\n\t" \
  "v_mfma_f32_16x16x32_bf16 v[108:111], a[140:143], a[236:239], v[108:111]\n\t" \
  "v_permlane16_swap_b32 v89, v91\n\t" \
  "v_mfma_f32_16x16x32_bf16 v[108:111], a[76:79], a[252:255], v[108:111]\n\t" \
  "v_cvt_pk_bf16_f32 v123, v118, v119\n\t" \
  "v_cvt_pk_bf16_f32 v122, v116, v117\n\t" \
  "v_mfma_f32_16x16x32_bf16 v[104:107], a[136:139], a[236:239], v[104:107]\n\t" \
  "v_permlane16_swap_b32 v84, v86\n\t" \
  "v_mfma_f32_16x16x32_bf16 v[104:107], a[72:75], a[252:255], v[104:107]\n\t" \
  "v_cvt_pk_bf16_f32 v120, v112, v113\n\t" \
  "v_cvt_pk_bf16_f32 v121, v114, v115\n\t" \
  "v_mfma_f32_16x16x32_bf16 v[100:103], a[132:135], a[236:239], v[100:103]\n\t" \
  "s_add_u32 m0, s38, 49152\n\t" \
  "v_permlane16_swap_b32 v85, v87\n\t" \
  "v_mfma_f32_16x16x32_bf16 v[100:103], a[68:71], a[252:255], v[100:103]\n\t" \
  "s_add_u32 s96, s38, 32768\n\t" \
  "v_permlane16_swap_b32 v80, v82\n\t" \
  "v_mfma_f32_16x16x32_bf16 v[96:99], a[128:131], a[236:239], v[96:99]\n\t" \
  "s_waitcnt lgkmcnt(0)\n\t" \
  "v_cvt_pk_bf16_f32 v119, v110, v111\n\t" \
  "v_cvt_pk_bf16_f32 v118, v108, v109\n\t" \
  "v_mfma_f32_16x16x32_bf16 v[96:99], a[64:67], a[252:255], v[96:99]\n\t" \
  "s_barrier\n\t" \
  "v_mfma_f32_16x16x32_bf16 v[0:3], a[0:3], v[16:19], 0\n\t" \
  "buffer_load_dwordx4 v224, s[60:63], s96 offen offset:0 lds\n\t" \
  "v_mfma_f32_16x16x32_bf16 v[0:3], a[4:7], v[20:23], v[0:3]\n\t" \
  "v_permlane16_swap_b32 v81, v83\n\t" \
  "v_mfma_f32_16x16x32_bf16 v[0:3], a[8:11], v[24:27], v[0:3]\n\t" \
  "ds_read_b128 a[192:195], v220 offset:16384\n\t" \
  "v_cvt_pk_bf16_f32 v116, v104, v105\n\t" \
  "v_mfma_f32_16x16x32_bf16 v[0:3], a[12:15], v[28:31], v[0:3]\n\t" \
  "v_permlane16_swap_b32 v124, v126\n\t" \
  "v_mfma_f32_16x16x32_bf16 v[32:35], a[0:3], v[48:51], 0\n\t" \
  "v_permlane16_swap_b32 v125, v127\n\t" \
  "v_mfma_f32_16x16x32_bf16 v[32:35], a[4:7], v[52:55], v[32:35]\n\t" \
  "s_nop 3\n\t" \
  "v_max3_f32 v194, v0, v1, v2\n\t" \
  "v_mfma_f32_16x16x32_bf16 v[32:35], a[8:11], v[56:59], v[32:35]\n\t" \
  "ds_read_b128 a[196:199], v221 offset:16384\n\t" \
  "v_cvt_pk_bf16_f32 v117, v106, v107\n\t" \
  "v_max_f32 v194, v194, v3\n\t" \
  "v_mov_b32 v195, v194\n\t" \
  "v_mfma_f32_16x16x32_bf16 v[32:35], a[12:15], v[60:63], v[32:35]\n\t" \
  "v_cvt_pk_bf16_f32 v115, v102, v103\n\t" \
  "v_permlane32_swap_b32 v194, v195\n\t" \
  "v_max_f32 v194, v194, v195\n\t" \
  "v_mov_b32 v195, v194\n\t" \
  "s_nop 1\n\t" \
  "v_permlane16_swap_b32 v194, v195\n\t" \
  "v_max_f32 v194, v194, v195\n\t" \
  "v_sub_f32 v176, 0xc2000000, v194\n\t" \
  "v_sub_f32 v177, 0xc2000000, v194\n\t" \
  "v_sub_f32 v178, 0xc2000000, v194\n\t" \
  "v_sub_f32 v179, 0xc2000000, v194\n\t" \
  "s_nop 1\n\t" \
  "v_mfma_f32_16x16x32_bf16 v[4:7], a[16:19], v[16:19], v[176:179]\n\t" \
  "v_add_f32 v0, v0, v176\n\t" \
  "v_add_f32 v1, v1, v176\n\t" \
  "v_add_f32 v2, v2, v176\n\t" \
  "v_add_f32 v3, v3, v176\n\t" \
  "v_mfma_f32_16x16x32_bf16 v[4:7], a[20:23], v[20:23], v[4:7]\n\t" \
  "buffer_load_dwordx4 v225, s[60:63], s96 offen offset:1024 lds\n\t" \
  "v_max3_f32 v194, v32, v33, v34\n\t" \
  "v_max_f32 v194, v194, v35\n\t" \
  "v_mov_b32 v195, v194\n\t" \
  "v_mfma_f32_16x16x32_bf16 v[4:7], a[24:27], v[24:27], v[4:7]\n\t" \
  "ds_read_b128 a[200:203], v222 offset:16384\n\t" \
  "v_permlane32_swap_b32 v194, v195\n\t" \
  "v_max_f32 v194, v194, v195\n\t" \
  "v_mov_b32 v195, v194\n\t" \
  "v_mfma_f32_16x16x32_bf16 v[4:7], a[28:31], v[28:31], v[4:7]\n\t" \
  "v_permlane16_swap_b32 v120, v122\n\t" \
  "v_permlane16_swap_b32 v194, v195\n\t" \
  "v_max_f32 v194, v194, v195\n\t" \
  "v_mfma_f32_16x16x32_bf16 v[8:11], a[32:35], v[16:19], v[176:179]\n\t" \
  "v_sub_f32 v180, 0xc2000000, v194\n\t" \
  "v_sub_f32 v181, 0xc2000000, v194\n\t" \
  "v_mfma_f32_16x16x32_bf16 v[8:11], a[36:39], v[20:23], v[8:11]\n\t" \
  "v_sub_f32 v182, 0xc2000000, v194\n\t" \
  "v_sub_f32 v183, 0xc2000000, v194\n\t" \
  "v_mfma_f32_16x16x32_bf16 v[8:11], a[40:43], v[24:27], v[8:11]\n\t" \
  "v_add_f32 v32, v32, v180\n\t" \
  "v_add_f32 v33, v33, v180\n\t" \
  "v_max3_f32 v192, v4, v5, v6\n\t" \
  "v_mfma_f32_16x16x32_bf16 v[8:11], a[44:47], v[28:31], v[8:11]\n\t" \
  "v_add_f32 v34, v34, v180\n\t" \
  "v_permlane16_swap_b32 v121, v123\n\t" \
  "v_mfma_f32_16x16x32_bf16 v[12:15], a[48:51], v[16:19], v[176:179]\n\t" \
  "v_add_f32 v35, v35, v180\n\t" \
  "v_permlane16_swap_b32 v116, v118\n\t" \
  "v_mfma_f32_16x16x32_bf16 v[12:15], a[52:55], v[20:23], v[12:15]\n\t" \
  "ds_read_b128 a[204:207], v223 offset:16384\n\t" \
  "v_cvt_pk_bf16_f32 v114, v100, v101\n\t" \
  "v_max3_f32 v203, v7, v8, v9\n\t" \
  "v_max3_f32 v192, v192, v10, v11\n\t" \
  "v_mfma_f32_16x16x32_bf16 v[12:15], a[56:59], v[24:27], v[12:15]\n\t" \
  "buffer_load_dwordx4 v226, s[60:63], s96 offen offset:2048 lds\n\t" \
  "v_mfma_f32_16x16x32_bf16 v[12:15], a[60:63], v[28:31], v[12:15]\n\t" \
  "ds_read_b128 a[208:211], v220 offset:20480\n\t" \
  "v_permlane16_swap_b32 v117, v119\n\t" \
  "v_mfma_f32_16x16x32_bf16 v[64:67], a[0:3], v[80:83], 0\n\t" \
  "ds_read_b128 a[212:215], v221 offset:20480\n\t" \
  "v_cvt_pk_bf16_f32 v112, v96, v97\n\t" \
  "v_mfma_f32_16x16x32_bf16 v[64:67], a[4:7], v[84:87], v[64:67]\n\t" \
  "v_cvt_pk_bf16_f32 v113, v98, v99\n\t" \
  "s_nop 0\n\t" \
  "v_max3_f32 v203, v203, v12, v13\n\t" \
  "v_max3_f32 v192, v192, v14, v15\n\t" \
  "v_max_f32 v192, v192, v203\n\t" \
  "v_mfma_f32_16x16x32_bf16 v[64:67], a[8:11], v[88:91], v[64:67]\n\t" \
  "v_permlane16_swap_b32 v112, v114\n\t" \
  "v_cmp_lt_f32 vcc, 0x42800000, v192\n\t" \
  "v_mfma_f32_16x16x32_bf16 v[64:67], a[12:15], v[92:95], v[64:67]\n\t" \
  "v_permlane16_swap_b32 v113, v115\n\t" \
  "v_mfma_f32_16x16x32_bf16 v[36:39], a[16:19], v[48:51], v[180:183]\n\t" \
  "s_cbranch_vccnz .Lsf_c00\n\t" \
  ".Lsf_r00:\n\t" \
  "v_mfma_f32_16x16x32_bf16 v[36:39], a[20:23], v[52:55], v[36:39]\n\t" \
  "ds_read_b128 a[216:219], v222 offset:20480\n\t" \
  "v_exp_f32 v0, v0\n\t" \
  "v_exp_f32 v1, v1\n\t" \
  "v_mfma_f32_16x16x32_bf16 v[36:39], a[24:27], v[56:59], v[36:39]\n\t" \
  "v_max3_f32 v194, v64, v65, v66\n\t" \
  "v_max_f32 v194, v194, v67\n\t" \
  "v_mov_b32 v195, v194\n\t" \
  "v_exp_f32 v2, v2\n\t" \
  "v_mfma_f32_16x16x32_bf16 v[36:39], a[28:31], v[60:63], v[36:39]\n\t" \
  "ds_read_b128 a[220:223], v223 offset:20480\n\t" \
  "v_permlane32_swap_b32 v194, v195\n\t" \
  "v_max_f32 v194, v194, v195\n\t" \
  "v_mov_b32 v195, v194\n\t" \
  "v_mfma_f32_16x16x32_bf16 v[40:43], a[32:35], v[48:51], v[180:183]\n\t" \
  "s_nop 0\n\t" \
  "v_permlane16_swap_b32 v194, v195\n\t" \
  "v_max_f32 v194, v194, v195\n\t" \
  "v_sub_f32 v184, 0xc2000000, v194\n\t" \
  "v_sub_f32 v185, 0xc2000000, v194\n\t" \
  "v_mfma_f32_16x16x32_bf16 v[40:43], a[36:39], v[52:55], v[40:43]\n\t" \
  "buffer_load_dwordx4 v227, s[60:63], s96 offen offset:3072 lds\n\t" \
  "v_exp_f32 v3, v3\n\t" \
  "v_sub_f32 v186, 0xc2000000, v194\n\t" \
  "v_mfma_f32_16x16x32_bf16 v[40:43], a[40:43], v[56:59], v[40:43]\n\t" \
  "v_sub_f32 v187, 0xc2000000, v194\n\t" \
  "v_add_f32 v64, v64, v184\n\t" \
  "v_max3_f32 v192, v36, v37, v38\n\t" \
  "v_exp_f32 v4, v4\n\t" \
  "v_mfma_f32_16x16x32_bf16 v[40:43], a[44:47], v[60:63], v[40:43]\n\t" \
  "v_add_f32 v65, v65, v184\n\t" \
  "v_exp_f32 v5, v5\n\t" \
  "v_exp_f32 v6, v6\n\t" \
  "v_mfma_f32_16x16x32_bf16 v[44:47], a[48:51], v[48:51], v[180:183]\n\t" \
  "s_add_u32 m0, s38, 147456\n\t" \
  "v_add_f32 v66, v66, v184\n\t" \
  "v_exp_f32 v7, v7\n\t" \
  "v_exp_f32 v8, v8\n\t" \
  "v_mfma_f32_16x16x32_bf16 v[44:47], a[52:55], v[52:55], v[44:47]\n\t" \
  "s_add_u32 s96, s38, 16384\n\t" \
  "v_max3_f32 v203, v39, v40, v41\n\t" \
  "v_exp_f32 v9, v9\n\t" \
  "v_exp_f32 v10, v10\n\t" \
  "v_mfma_f32_16x16x32_bf16 v[44:47], a[56:59], v[56:59], v[44:47]\n\t" \
  "v_max3_f32 v192, v192, v42, v43\n\t" \
  "v_exp_f32 v11, v11\n\t" \
  "v_exp_f32 v12, v12\n\t" \
  "v_mfma_f32_16x16x32_bf16 v[44:47], a[60:63], v[60:63], v[44:47]\n\t" \
  "v_add_f32 v67, v67, v184\n\t" \
  "v_exp_f32 v13, v13\n\t" \
  "v_exp_f32 v14, v14\n\t" \
  "v_mfma_f32_16x16x32_bf16 v[96:99], a[0:3], v[112:115], 0\n\t" \
  "buffer_load_dwordx4 v230, s[64:67], s96 offen offset:0 lds\n\t" \
  "v_exp_f32 v15, v15\n\t" \
  "v_mfma_f32_16x16x32_bf16 v[96:99], a[4:7], v[116:119], v[96:99]\n\t" \
  "s_nop 0\n\t" \
  "v_max3_f32 v203, v203, v44, v45\n\t" \
  "v_max3_f32 v192, v192, v46, v47\n\t" \
  "v_max_f32 v192, v192, v203\n\t" \
  "v_cmp_lt_f32 vcc, 0x42800000, v192\n\t" \
  "v_mfma_f32_16x16x32_bf16 v[96:99], a[8:11], v[120:123], v[96:99]\n\t" \
  "ds_read_b128 a[224:227], v220 offset:24576\n\t" \
  "v_cvt_pk_bf16_f32 v128, v0, v1\n\t" \
  "v_mfma_f32_16x16x32_bf16 v[96:99], a[12:15], v[124:127], v[96:99]\n\t" \
  "ds_read_b128 a[228:231], v221 offset:24576\n\t" \
  "s_cbranch_vccnz .Lsf_c01\n\t" \
  ".Lsf_r01:\n\t" \
  "v_mfma_f32_16x16x32_bf16 v[68:71], a[16:19], v[80:83], v[184:187]\n\t" \
  "ds_read_b128 a[232:235], v222 offset:24576\n\t" \
  "v_exp_f32 v32, v32\n\t" \
  "v_exp_f32 v33, v33\n\t" \
  "v_mfma_f32_16x16x32_bf16 v[68:71], a[20:23], v[84:87], v[68:71]\n\t" \
  "s_nop 0\n\t" \
  "v_max3_f32 v194, v96, v97, v98\n\t" \
  "v_max_f32 v194, v194, v99\n\t" \
  "v_mov_b32 v195, v194\n\t" \
  "v_exp_f32 v34, v34\n\t" \
  "v_mfma_f32_16x16x32_bf16 v[68:71], a[24:27], v[88:91], v[68:71]\n\t" \
  "ds_read_b128 a[236:239], v223 offset:24576\n\t" \
  "v_permlane32_swap_b32 v194, v195\n\t" \
  "v_max_f32 v194, v194, v195\n\t" \
  "v_mov_b32 v195, v194\n\t" \
  "v_mfma_f32_16x16x32_bf16 v[68:71], a[28:31], v[92:95], v[68:71]\n\t" \
  "ds_read_b128 a[240:243], v220 offset:28672\n\t" \
  "v_permlane16_swap_b32 v194, v195\n\t" \
  "v_max_f32 v194, v194, v195\n\t" \
  "v_sub_f32 v188, 0xc2000000, v194\n\t" \
  "v_mfma_f32_16x16x32_bf16 v[72:75], a[32:35], v[80:83], v[184:187]\n\t" \
  "v_sub_f32 v189, 0xc2000000, v194\n\t" \
  "v_sub_f32 v190, 0xc2000000, v194\n\t" \
  "v_exp_f32 v35, v35\n\t" \
  "v_max3_f32 v192, v68, v69, v70\n\t" \
  "v_mfma_f32_16x16x32_bf16 v[72:75], a[36:39], v[84:87], v[72:75]\n\t" \
  "v_sub_f32 v191, 0xc2000000, v194\n\t" \
  "v_exp_f32 v36, v36\n\t" \
  "v_exp_f32 v37, v37\n\t" \
  "v_mfma_f32_16x16x32_bf16 v[72:75], a[40:43], v[88:91], v[72:75]\n\t" \
  "buffer_load_dwordx4 v231, s[64:67], s96 offen offset:1024 lds\n\t" \
  "v_exp_f32 v38, v38\n\t" \
  "v_mfma_f32_16x16x32_bf16 v[72:75], a[44:47], v[92:95], v[72:75]\n\t" \
  "v_add_f32 v96, v96, v188\n\t" \
  "v_exp_f32 v39, v39\n\t" \
  "v_exp_f32 v40, v40\n\t" \
  "v_mfma_f32_16x16x32_bf16 v[76:79], a[48:51], v[80:83], v[184:187]\n\t" \
  "ds_read_b128 a[244:247], v221 offset:28672\n\t" \
  "v_exp_f32 v41, v41\n\t" \
  "v_exp_f32 v42, v42\n\t" \
  "v_mfma_f32_16x16x32_bf16 v[76:79], a[52:55], v[84:87], v[76:79]\n\t" \
  "v_add_f32 v97, v97, v188\n\t" \
  "v_max3_f32 v203, v71, v72, v73\n\t" \
  "v_exp_f32 v43, v43\n\t" \
  "v_max3_f32 v192, v192, v74, v75\n\t" \
  "v_mfma_f32_16x16x32_bf16 v[76:79], a[56:59], v[88:91], v[76:79]\n\t" \
  "v_add_f32 v98, v98, v188\n\t" \
  "v_exp_f32 v44, v44\n\t" \
  "v_exp_f32 v45, v45\n\t" \
  "v_mfma_f32_16x16x32_bf16 v[76:79], a[60:63], v[92:95], v[76:79]\n\t" \
  "ds_read_b128 a[248:251], v222 offset:28672\n\t" \
  "v_exp_f32 v46, v46\n\t" \
  "v_exp_f32 v47, v47\n\t" \
  "v_mfma_f32_16x16x32_bf16 v[100:103], a[16:19], v[112:115], v[188:191]\n\t" \
  "s_nop 3\n\t" \
  "v_max3_f32 v203, v203, v76, v77\n\t" \
  "v_mfma_f32_16x16x32_bf16 v[100:103], a[20:23], v[116:119], v[100:103]\n\t" \
  "ds_read_b128 a[252:255], v223 offset:28672\n\t" \
  "v_max3_f32 v192, v192, v78, v79\n\t" \
  "v_max_f32 v192, v192, v203\n\t" \
  "v_cmp_lt_f32 vcc, 0x42800000, v192\n\t" \
  "v_mfma_f32_16x16x32_bf16 v[100:103], a[24:27], v[120:123], v[100:103]\n\t" \
  "buffer_load_dwordx4 v230, s[64:67], s96 offen offset:2048 lds\n\t" \
  "v_mfma_f32_16x16x32_bf16 v[100:103], a[28:31], v[124:127], v[100:103]\n\t" \
  "v_add_f32 v99, v99, v188\n\t" \
  "v_cvt_pk_bf16_f32 v129, v2, v3\n\t" \
  "s_cbranch_vccnz .Lsf_c02\n\t" \
  ".Lsf_r02:\n\t" \
  "v_mfma_f32_16x16x32_bf16 v[104:107], a[32:35], v[112:115], v[188:191]\n\t" \
  "v_cvt_pk_bf16_f32 v130, v4, v5\n\t" \
  "v_cvt_pk_bf16_f32 v131, v6, v7\n\t" \
  "v_exp_f32 v64, v64\n\t" \
  "v_mfma_f32_16x16x32_bf16 v[104:107], a[36:39], v[116:119], v[104:107]\n\t" \
  "v_cvt_pk_bf16_f32 v132, v8, v9\n\t" \
  "v_exp_f32 v65, v65\n\t" \
  "v_max3_f32 v192, v100, v101, v102\n\t" \
  "v_mfma_f32_16x16x32_bf16 v[104:107], a[40:43], v[120:123], v[104:107]\n\t" \
  "v_cvt_pk_bf16_f32 v133, v10, v11\n\t" \
  "v_exp_f32 v66, v66\n\t" \
  "v_mfma_f32_16x16x32_bf16 v[104:107], a[44:47], v[124:127], v[104:107]\n\t" \
  "v_cvt_pk_bf16_f32 v134, v12, v13\n\t" \
  "v_exp_f32 v67, v67\n\t" \
  "v_mfma_f32_16x16x32_bf16 v[108:111], a[48:51], v[112:115], v[188:191]\n\t" \
  "v_cvt_pk_bf16_f32 v135, v14, v15\n\t" \
  "v_exp_f32 v68, v68\n\t" \
  "v_mfma_f32_16x16x32_bf16 v[108:111], a[52:55], v[116:119], v[108:111]\n\t" \
  "v_exp_f32 v69, v69\n\t" \
  "v_mfma_f32_16x16x32_bf16 v[108:111], a[56:59], v[120:123], v[108:111]\n\t" \
  "v_max3_f32 v203, v103, v104, v105\n\t" \
  "v_max3_f32 v192, v192, v106, v107\n\t" \
  "v_mfma_f32_16x16x32_bf16 v[108:111], a[60:63], v[124:127], v[108:111]\n\t" \
  "s_waitcnt lgkmcnt(0)\n\t" \
  "buffer_load_dwordx4 v231, s[64:67], s96 offen offset:3072 lds\n\t" \
  "v_mfma_f32_16x16x32_bf16 v[0:3], a[192:195], v[16:19], v[176:179]\n\t" \
  "ds_read_b64_tr_b16 a[128:129], v228 offset:0\n\t" \
  "v_cvt_pk_bf16_f32 v136, v32, v33\n\t" \
  "v_mfma_f32_16x16x32_bf16 v[0:3], a[196:199], v[20:23], v[0:3]\n\t" \
  "v_cvt_pk_bf16_f32 v137, v34, v35\n\t" \
  "v_exp_f32 v70, v70\n\t" \
  "v_mfma_f32_16x16x32_bf16 v[0:3], a[200:203], v[24:27], v[0:3]\n\t" \
  "v_max3_f32 v203, v203, v108, v109\n\t" \
  "v_max3_f32 v192, v192, v110, v111\n\t" \
  "v_mfma_f32_16x16x32_bf16 v[0:3], a[204:207], v[28:31], v[0:3]\n\t" \
  "v_max_f32 v192, v192, v203\n\t" \
  "v_cmp_lt_f32 vcc, 0x42800000, v192\n\t" \
  "v_mfma_f32_16x16x32_bf16 v[4:7], a[220:223], v[28:31], v[176:179]\n\t" \
  "ds_read_b64_tr_b16 a[130:131], v228 offset:4096\n\t" \
  "v_exp_f32 v71, v71\n\t" \
  "v_mfma_f32_16x16x32_bf16 v[4:7], a[216:219], v[24:27], v[4:7]\n\t" \
  "s_cbranch_vccnz .Lsf_c03\n\t" \
  ".Lsf_r03:\n\t" \
  "v_mfma_f32_16x16x32_bf16 v[4:7], a[212:215], v[20:23], v[4:7]\n\t" \
  "ds_read_b64_tr_b16 a[132:133], v228 offset:8192\n\t" \
  "v_exp_f32 v72, v72\n\t" \
  "v_mfma_f32_16x16x32_bf16 v[4:7], a[208:211], v[16:19], v[4:7]\n\t" \
  "s_cmp_lg_u32 s99, 0\n\t" \
  "s_cbranch_scc1 .Lsf_p0\n\t" \
  ".Lsf_q0:\n\t" \
  "v_mfma_f32_16x16x32_bf16 v[8:11], a[224:227], v[16:19], v[176:179]\n\t" \
  "ds_read_b64_tr_b16 a[134:135], v228 offset:12288\n\t" \
  "v_max3_f32 v192, v0, v1, v2\n\t" \
  "v_mfma_f32_16x16x32_bf16 v[8:11], a[228:231], v[20:23], v[8:11]\n\t" \
  "ds_read_b64_tr_b16 a[136:137], v229 offset:0\n\t" \
  "v_exp_f32 v73, v73\n\t" \
  "v_mfma_f32_16x16x32_bf16 v[8:11], a[232:235], v[24:27], v[8:11]\n\t" \
  "v_max3_f32 v203, v3, v4, v5\n\t" \
  "v_max3_f32 v192, v192, v6, v7\n\t" \
  "v_mfma_f32_16x16x32_bf16 v[8:11], a[236:239], v[28:31], v[8:11]\n\t" \
  "ds_read_b64_tr_b16 a[138:139], v229 offset:4096\n\t" \
  "v_exp_f32 v74, v74\n\t" \
  "v_mfma_f32_16x16x32_bf16 v[12:15], a[252:255], v[28:31], v[176:179]\n\t" \
  "ds_read_b64_tr_b16 a[140:141], v229 offset:8192\n\t" \
  "v_exp_f32 v75, v75\n\t" \
  "v_mfma_f32_16x16x32_bf16 v[12:15], a[248:251], v[24:27], v[12:15]\n\t" \
  "ds_read_b64_tr_b16 a[142:143], v229 offset:12288\n\t" \
  "v_cvt_pk_bf16_f32 v138, v36, v37\n\t" \
  "v_mfma_f32_16x16x32_bf16 v[12:15], a[244:247], v[20:23], v[12:15]\n\t" \
  "v_max3_f32 v203, v203, v8, v9\n\t" \
  "v_max3_f32 v192, v192, v10, v11\n\t" \
  "v_mfma_f32_16x16x32_bf16 v[12:15], a[240:243], v[16:19], v[12:15]\n\t" \
  "ds_read_b64_tr_b16 a[144:145], v228 offset:256\n\t" \
  "v_exp_f32 v76, v76\n\t" \
  "v_mfma_f32_16x16x32_bf16 v[32:35], a[192:195], v[48:51], v[180:183]\n\t" \
  "v_cvt_pk_bf16_f32 v139, v38, v39\n\t" \
  "v_exp_f32 v77, v77\n\t" \
  "v_mfma_f32_16x16x32_bf16 v[32:35], a[196:199], v[52:55], v[32:35]\n\t" \
  "ds_read_b64_tr_b16 a[146:147], v228 offset:4352\n\t" \
  "v_cvt_pk_bf16_f32 v140, v40, v41\n\t" \
  "v_mfma_f32_16x16x32_bf16 v[32:35], a[200:203], v[56:59], v[32:35]\n\t" \
  "v_max3_f32 v203, v203, v12, v13\n\t" \
  "v_max3_f32 v192, v192, v14, v15\n\t" \
  "v_mfma_f32_16x16x32_bf16 v[32:35], a[204:207], v[60:63], v[32:35]\n\t" \
  "v_max_f32 v192, v192, v203\n\t" \
  "v_cmp_lt_f32 vcc, 0x42800000, v192\n\t" \
  "v_mfma_f32_16x16x32_bf16 v[36:39], a[220:223], v[60:63], v[180:183]\n\t" \
  "ds_read_b64_tr_b16 a[148:149], v228 offset:8448\n\t" \
  "v_exp_f32 v78, v78\n\t" \
  "v_mfma_f32_16x16x32_bf16 v[36:39], a[216:219], v[56:59], v[36:39]\n\t" \
  "s_cbranch_vccnz .Lsf_c10\n\t" \
  ".Lsf_r10:\n\t" \
  "v_mfma_f32_16x16x32_bf16 v[36:39], a[212:215], v[52:55], v[36:39]\n\t" \
  "v_max3_f32 v192, v32, v33, v34\n\t" \
  "v_exp_f32 v79, v79\n\t" \
  "v_cvt_pk_bf16_f32 v141, v42, v43\n\t" \
  "v_mfma_f32_16x16x32_bf16 v[36:39], a[208:211], v[48:51], v[36:39]\n\t" \
  "ds_read_b64_tr_b16 a[150:151], v228 offset:12544\n\t" \
  "v_exp_f32 v96, v96\n\t" \
  "v_mfma_f32_16x16x32_bf16 v[40:43], a[224:227], v[48:51], v[180:183]\n\t" \
  "ds_read_b64_tr_b16 a[152:153], v229 offset:256\n\t" \
  "v_exp_f32 v97, v97\n\t" \
  "v_mfma_f32_16x16x32_bf16 v[40:43], a[228:231], v[52:55], v[40:43]\n\t" \
  "ds_read_b64_tr_b16 a[154:155], v229 offset:4352\n\t" \
  "v_cvt_pk_bf16_f32 v142, v44, v45\n\t" \
  "v_mfma_f32_16x16x32_bf16 v[40:43], a[232:235], v[56:59], v[40:43]\n\t" \
  "v_max3_f32 v203, v35, v36, v37\n\t" \
  "v_max3_f32 v192, v192, v38, v39\n\t" \
  "v_mfma_f32_16x16x32_bf16 v[40:43], a[236:239], v[60:63], v[40:43]\n\t" \
  "ds_read_b64_tr_b16 a[156:157], v229 offset:8448\n\t" \
  "v_exp_f32 v98, v98\n\t" \
  "v_cvt_pk_bf16_f32 v143, v46, v47\n\t" \
  "v_mfma_f32_16x16x32_bf16 v[44:47], a[252:255], v[60:63], v[180:183]\n\t" \
  "ds_read_b64_tr_b16 a[158:159], v229 offset:12544\n\t" \
  "v_exp_f32 v99, v99\n\t" \
  "v_mfma_f32_16x16x32_bf16 v[44:47], a[248:251], v[56:59], v[44:47]\n\t" \
  "v_cvt_pk_bf16_f32 v144, v64, v65\n\t" \
  "v_max3_f32 v203, v203, v40, v41\n\t" \
  "v_mfma_f32_16x16x32_bf16 v[44:47], a[244:247], v[52:55], v[44:47]\n\t" \
  "v_max3_f32 v192, v192, v42, v43\n\t" \
  "v_exp_f32 v100, v100\n\t" \
  "v_mfma_f32_16x16x32_bf16 v[44:47], a[240:243], v[48:51], v[44:47]\n\t" \
  "ds_read_b64_tr_b16 a[160:161], v228 offset:512\n\t" \
  "v_cvt_pk_bf16_f32 v145, v66, v67\n\t" \
  "v_mfma_f32_16x16x32_bf16 v[64:67], a[192:195], v[80:83], v[184:187]\n\t" \
  "ds_read_b64_tr_b16 a[162:163], v228 offset:4608\n\t" \
  "v_exp_f32 v101, v101\n\t" \
  "v_mfma_f32_16x16x32_bf16 v[64:67], a[196:199], v[84:87], v[64:67]\n\t" \
  "v_cvt_pk_bf16_f32 v146, v68, v69\n\t" \
  "v_cvt_pk_bf16_f32 v147, v70, v71\n\t" \
  "v_mfma_f32_16x16x32_bf16 v[64:67], a[200:203], v[88:91], v[64:67]\n\t" \
  "v_max3_f32 v203, v203, v44, v45\n\t" \
  "v_max3_f32 v192, v192, v46, v47\n\t" \
  "v_mfma_f32_16x16x32_bf16 v[64:67], a[204:207], v[92:95], v[64:67]\n\t" \
  "v_max_f32 v192, v192, v203\n\t" \
  "v_cmp_lt_f32 vcc, 0x42800000, v192\n\t" \
  "v_mfma_f32_16x16x32_bf16 v[68:71], a[220:223], v[92:95], v[184:187]\n\t" \
  "ds_read_b64_tr_b16 a[164:165], v228 offset:8704\n\t" \
  "v_exp_f32 v102, v102\n\t" \
  "v_mfma_f32_16x16x32_bf16 v[68:71], a[216:219], v[88:91], v[68:71]\n\t" \
  "s_cbranch_vccnz .Lsf_c11\n\t" \
  ".Lsf_r11:\n\t" \
  "v_mfma_f32_16x16x32_bf16 v[68:71], a[212:215], v[84:87], v[68:71]\n\t" \
  "v_max3_f32 v192, v64, v65, v66\n\t" \
  "v_exp_f32 v103, v103\n\t" \
  "v_cvt_pk_bf16_f32 v148, v72, v73\n\t" \
  "v_cvt_pk_bf16_f32 v149, v74, v75\n\t" \
  "v_mfma_f32_16x16x32_bf16 v[68:71], a[208:211], v[80:83], v[68:71]\n\t" \
  "ds_read_b64_tr_b16 a[166:167], v228 offset:12800\n\t" \
  "v_exp_f32 v104, v104\n\t" \
  "v_mfma_f32_16x16x32_bf16 v[72:75], a[224:227], v[80:83], v[184:187]\n\t" \
  "ds_read_b64_tr_b16 a[168:169], v229 offset:512\n\t" \
  "v_exp_f32 v105, v105\n\t" \
  "v_mfma_f32_16x16x32_bf16 v[72:75], a[228:231], v[84:87], v[72:75]\n\t" \
  "ds_read_b64_tr_b16 a[170:171], v229 offset:4608\n\t" \
  "v_cvt_pk_bf16_f32 v150, v76, v77\n\t" \
  "v_mfma_f32_16x16x32_bf16 v[72:75], a[232:235], v[88:91], v[72:75]\n\t" \
  "v_max3_f32 v203, v67, v68, v69\n\t" \
  "v_max3_f32 v192, v192, v70, v71\n\t" \
  "v_mfma_f32_16x16x32_bf16 v[72:75], a[236:239], v[92:95], v[72:75]\n\t" \
  "ds_read_b64_tr_b16 a[172:173], v229 offset:8704\n\t" \
  "v_exp_f32 v106, v106\n\t" \
  "v_cvt_pk_bf16_f32 v151, v78, v79\n\t" \
  "v_mfma_f32_16x16x32_bf16 v[76:79], a[252:255], v[92:95], v[184:187]\n\t" \
  "ds_read_b64_tr_b16 a[174:175], v229 offset:12800\n\t" \
  "v_exp_f32 v107, v107\n\t" \
  "v_mfma_f32_16x16x32_bf16 v[76:79], a[248:251], v[88:91], v[76:79]\n\t" \
  "ds_read_b64_tr_b16 a[176:177], v228 offset:768\n\t" \
  "v_max3_f32 v203, v203, v72, v73\n\t" \
  "v_mfma_f32_16x16x32_bf16 v[76:79], a[244:247], v[84:87], v[76:79]\n\t" \
  "v_max3_f32 v192, v192, v74, v75\n\t" \
  "v_exp_f32 v108, v108\n\t" \
  "v_mfma_f32_16x16x32_bf16 v[76:79], a[240:243], v[80:83], v[76:79]\n\t" \
  "ds_read_b64_tr_b16 a[178:179], v228 offset:4864\n\t" \
  "v_cvt_pk_bf16_f32 v152, v96, v97\n\t" \
  "v_cvt_pk_bf16_f32 v153, v98, v99\n\t" \
  "v_mfma_f32_16x16x32_bf16 v[96:99], a[192:195], v[112:115], v[188:191]\n\t" \
  "ds_read_b64_tr_b16 a[180:181], v228 offset:8960\n\t" \
  "v_exp_f32 v109, v109\n\t" \
  "v_mfma_f32_16x16x32_bf16 v[96:99], a[196:199], v[116:119], v[96:99]\n\t" \
  "v_cvt_pk_bf16_f32 v154, v100, v101\n\t" \
  "v_max3_f32 v203, v203, v76, v77\n\t" \
  "v_mfma_f32_16x16x32_bf16 v[96:99], a[200:203], v[120:123], v[96:99]\n\t" \
  "v_max3_f32 v192, v192, v78, v79\n\t" \
  "v_max_f32 v192, v192, v203\n\t" \
  "v_mfma_f32_16x16x32_bf16 v[96:99], a[204:207], v[124:127], v[96:99]\n\t" \
  "v_cmp_lt_f32 vcc, 0x42800000, v192\n\t" \
  "v_exp_f32 v110, v110\n\t" \
  "v_cvt_pk_bf16_f32 v155, v102, v103\n\t" \
  "v_mfma_f32_16x16x32_bf16 v[100:103], a[220:223], v[124:127], v[188:191]\n\t" \
  "ds_read_b64_tr_b16 a[182:183], v228 offset:13056\n\t" \
  "v_exp_f32 v111, v111\n\t" \
  "v_mfma_f32_16x16x32_bf16 v[100:103], a[216:219], v[120:123], v[100:103]\n\t" \
  "s_cbranch_vccnz .Lsf_c12\n\t" \
  ".Lsf_r12:\n\t" \
  "v_mfma_f32_16x16x32_bf16 v[100:103], a[212:215], v[116:119], v[100:103]\n\t" \
  "ds_read_b64_tr_b16 a[184:185], v229 offset:768\n\t" \
  "v_max3_f32 v192, v96, v97, v98\n\t" \
  "v_mfma_f32_16x16x32_bf16 v[100:103], a[208:211], v[112:115], v[100:103]\n\t" \
  "ds_read_b64_tr_b16 a[186:187], v229 offset:4864\n\t" \
  "v_cvt_pk_bf16_f32 v156, v104, v105\n\t" \
  "v_cvt_pk_bf16_f32 v157, v106, v107\n\t" \
  "v_mfma_f32_16x16x32_bf16 v[104:107], a[224:227], v[112:115], v[188:191]\n\t" \
  "ds_read_b64_tr_b16 a[188:189], v229 offset:8960\n\t" \
  "v_cvt_pk_bf16_f32 v158, v108, v109\n\t" \
  "v_mfma_f32_16x16x32_bf16 v[104:107], a[228:231], v[116:119], v[104:107]\n\t" \
  "ds_read_b64_tr_b16 a[190:191], v229 offset:13056\n\t" \
  "v_cvt_pk_bf16_f32 v159, v110, v111\n\t" \
  "v_mfma_f32_16x16x32_bf16 v[104:107], a[232:235], v[120:123], v[104:107]\n\t" \
  "v_max3_f32 v203, v99, v100, v101\n\t" \
  "v_max3_f32 v192, v192, v102, v103\n\t" \
  "v_mfma_f32_16x16x32_bf16 v[104:107], a[236:239], v[124:127], v[104:107]\n\t" \
  "v_exp_f32 v0, v0\n\t" \
  "v_mfma_f32_16x16x32_bf16 v[108:111], a[252:255], v[124:127], v[188:191]\n\t" \
  "v_exp_f32 v1, v1\n\t" \
  "v_mfma_f32_16x16x32_bf16 v[108:111], a[248:251], v[120:123], v[108:111]\n\t" \
  "v_exp_f32 v2, v2\n\t" \
  "v_mfma_f32_16x16x32_bf16 v[108:111], a[244:247], v[116:119], v[108:111]\n\t" \
  "s_waitcnt vmcnt(0) lgkmcnt(0)\n\t" \
  "v_exp_f32 v3, v3\n\t" \
  "v_mfma_f32_16x16x32_bf16 v[108:111], a[240:243], v[112:115], v[108:111]\n\t" \
  "s_barrier\n\t" \
  "v_mfma_f32_16x16x32_bf16 a[0:3], a[128:131], v[128:131], 0\n\t" \
  "v_max3_f32 v203, v203, v104, v105\n\t" \
  "v_max3_f32 v192, v192, v106, v107\n\t" \
  "v_mfma_f32_16x16x32_bf16 a[0:3], a[132:135], v[132:135], a[0:3]\n\t" \
  "v_exp_f32 v4, v4\n\t" \
  "v_mfma_f32_16x16x32_bf16 a[4:7], a[136:139], v[128:131], 0\n\t" \
  "v_exp_f32 v5, v5\n\t" \
  "v_mfma_f32_16x16x32_bf16 a[4:7], a[140:143], v[132:135], a[4:7]\n\t" \
  "v_max3_f32 v203, v203, v108, v109\n\t" \
  "v_max3_f32 v192, v192, v110, v111\n\t" \
  "v_mfma_f32_16x16x32_bf16 a[8:11], a[144:147], v[128:131], 0\n\t" \
  "v_max_f32 v192, v192, v203\n\t" \
  "v_cmp_lt_f32 vcc, 0x42800000, v192\n\t" \
  "v_mfma_f32_16x16x32_bf16 a[8:11], a[148:151], v[132:135], a[8:11]\n\t" \
  "v_exp_f32 v6, v6\n\t" \
  "v_mfma_f32_16x16x32_bf16 a[12:15], a[152:155], v[128:131], 0\n\t" \
  "s_cbranch_vccnz .Lsf_c13\n\t" \
  ".Lsf_r13:\n\t" \
  "v_mfma_f32_16x16x32_bf16 a[12:15], a[156:159], v[132:135], a[12:15]\n\t" \
  "s_add_u32 m0, s38, 65536\n\t" \
  "v_exp_f32 v7, v7\n\t" \
  "v_mfma_f32_16x16x32_bf16 a[16:19], a[160:163], v[128:131], 0\n\t" \
  "s_add_u32 s96, s38, 49152\n\t" \
  "v_exp_f32 v8, v8\n\t" \
  "v_mfma_f32_16x16x32_bf16 a[16:19], a[164:167], v[132:135], a[16:19]\n\t" \
  "v_exp_f32 v9, v9\n\t" \
  "v_mfma_f32_16x16x32_bf16 a[20:23], a[168:171], v[128:131], 0\n\t" \
  "buffer_load_dwordx4 v224, s[60:63], s96 offen offset:0 lds\n\t" \
  "v_mfma_f32_16x16x32_bf16 a[20:23], a[172:175], v[132:135], a[20:23]\n\t" \
  "v_exp_f32 v10, v10\n\t" \
  "v_mfma_f32_16x16x32_bf16 a[24:27], a[176:179], v[128:131], 0\n\t" \
  "v_exp_f32 v11, v11\n\t" \
  "v_mfma_f32_16x16x32_bf16 a[24:27], a[180:183], v[132:135], a[24:27]\n\t" \
  "v_exp_f32 v12, v12\n\t" \
  "v_mfma_f32_16x16x32_bf16 a[28:31], a[184:187], v[128:131], 0\n\t" \
  "v_exp_f32 v13, v13\n\t" \
  "v_mfma_f32_16x16x32_bf16 a[28:31], a[188:191], v[132:135], a[28:31]\n\t" \
  "v_exp_f32 v14, v14\n\t" \
  "v_mfma_f32_16x16x32_bf16 v[160:163], v[216:219], v[128:131], 0\n\t" \
  "v_exp_f32 v15, v15\n\t" \
  "v_mfma_f32_16x16x32_bf16 v[160:163], v[216:219], v[132:135], v[160:163]\n\t" \
  "buffer_load_dwordx4 v225, s[60:63], s96 offen offset:1024 lds\n\t" \
  "v_mfma_f32_16x16x32_bf16 a[32:35], a[128:131], v[136:139], 0\n\t" \
  "ds_read_b128 a[192:195], v220 offset:0\n\t" \
  "v_cvt_pk_bf16_f32 v128, v0, v1\n\t" \
  "v_mfma_f32_16x16x32_bf16 a[32:35], a[132:135], v[140:143], a[32:35]\n\t" \
  "ds_read_b128 a[196:199], v221 offset:0\n\t" \
  "v_cvt_pk_bf16_f32 v129, v2, v3\n\t" \
  "v_mfma_f32_16x16x32_bf16 a[36:39], a[136:139], v[136:139], 0\n\t" \
  "v_exp_f32 v32, v32\n\t" \
  "v_mfma_f32_16x16x32_bf16 a[36:39], a[140:143], v[140:143], a[36:39]\n\t" \
  "ds_read_b128 a[200:203], v222 offset:0\n\t" \
  "v_cvt_pk_bf16_f32 v130, v4, v5\n\t" \
  "v_mfma_f32_16x16x32_bf16 a[40:43], a[144:147], v[136:139], 0\n\t" \
  "v_exp_f32 v33, v33\n\t" \
  "v_mfma_f32_16x16x32_bf16 a[40:43], a[148:151], v[140:143], a[40:43]\n\t" \
  "buffer_load_dwordx4 v226, s[60:63], s96 offen offset:2048 lds\n\t" \
  "v_mfma_f32_16x16x32_bf16 a[44:47], a[152:155], v[136:139], 0\n\t" \
  "ds_read_b128 a[204:207], v223 offset:0\n\t" \
  "v_cvt_pk_bf16_f32 v131, v6, v7\n\t" \
  "v_mfma_f32_16x16x32_bf16 a[44:47], a[156:159], v[140:143], a[44:47]\n\t" \
  "v_exp_f32 v34, v34\n\t" \
  "v_mfma_f32_16x16x32_bf16 a[48:51], a[160:163], v[136:139], 0\n\t" \
  "v_exp_f32 v35, v35\n\t" \
  "v_mfma_f32_16x16x32_bf16 a[48:51], a[164:167], v[140:143], a[48:51]\n\t" \
  "ds_read_b128 a[208:211], v220 offset:4096\n\t" \
  "v_cvt_pk_bf16_f32 v132, v8, v9\n\t" \
  "v_mfma_f32_16x16x32_bf16 a[52:55], a[168:171], v[136:139], 0\n\t" \
  "v_exp_f32 v36, v36\n\t" \
  "v_mfma_f32_16x16x32_bf16 a[52:55], a[172:175], v[140:143], a[52:55]\n\t" \
  "buffer_load_dwordx4 v227, s[60:63], s96 offen offset:3072 lds\n\t" \
  "v_mfma_f32_16x16x32_bf16 a[56:59], a[176:179], v[136:139], 0\n\t" \
  "ds_read_b128 a[212:215], v221 offset:4096\n\t" \
  "v_cvt_pk_bf16_f32 v133, v10, v11\n\t" \
  "v_mfma_f32_16x16x32_bf16 a[56:59], a[180:183], v[140:143], a[56:59]\n\t" \
  "s_add_u32 m0, s38, 81920\n\t" \
  "v_exp_f32 v37, v37\n\t" \
  "v_mfma_f32_16x16x32_bf16 a[60:63], a[184:187], v[136:139], 0\n\t" \
  "s_add_u32 s96, s38, 32768\n\t" \
  "v_exp_f32 v38, v38\n\t" \
  "v_mfma_f32_16x16x32_bf16 a[60:63], a[188:191], v[140:143], a[60:63]\n\t" \
  "ds_read_b128 a[216:219], v222 offset:4096\n\t" \
  "v_cvt_pk_bf16_f32 v134, v12, v13\n\t" \
  "v_mfma_f32_16x16x32_bf16 v[164:167], v[216:219], v[136:139], 0\n\t" \
  "v_exp_f32 v39, v39\n\t" \
  "v_mfma_f32_16x16x32_bf16 v[164:167], v[216:219], v[140:143], v[164:167]\n\t" \
  "buffer_load_dwordx4 v230, s[64:67], s96 offen offset:0 lds\n\t" \
  "v_mfma_f32_16x16x32_bf16 a[64:67], a[128:131], v[144:147], 0\n\t" \
  "ds_read_b128 a[220:223], v223 offset:4096\n\t" \
  "v_cvt_pk_bf16_f32 v135, v14, v15\n\t" \
  "v_mfma_f32_16x16x32_bf16 a[64:67], a[132:135], v[148:151], a[64:67]\n\t" \
  "v_exp_f32 v40, v40\n\t" \
  "v_mfma_f32_16x16x32_bf16 a[68:71], a[136:139], v[144:147], 0\n\t" \
  "ds_read_b128 a[224:227], v220 offset:8192\n\t" \
  "v_cvt_pk_bf16_f32 v136, v32, v33\n\t" \
  "v_mfma_f32_16x16x32_bf16 a[68:71], a[140:143], v[148:151], a[68:71]\n\t" \
  "v_exp_f32 v41, v41\n\t" \
  "v_mfma_f32_16x16x32_bf16 a[72:75], a[144:147], v[144:147], 0\n\t" \
  "v_exp_f32 v42, v42\n\t" \
  "v_mfma_f32_16x16x32_bf16 a[72:75], a[148:151], v[148:151], a[72:75]\n\t" \
  "ds_read_b128 a[228:231], v221 offset:8192\n\t" \
  "v_cvt_pk_bf16_f32 v137, v34, v35\n\t" \
  "v_mfma_f32_16x16x32_bf16 a[76:79], a[152:155], v[144:147], 0\n\t" \
  "buffer_load_dwordx4 v231, s[64:67], s96 offen offset:1024 lds\n\t" \
  "v_mfma_f32_16x16x32_bf16 a[76:79], a[156:159], v[148:151], a[76:79]\n\t" \
  "v_exp_f32 v43, v43\n\t" \
  "v_mfma_f32_16x16x32_bf16 a[80:83], a[160:163], v[144:147], 0\n\t" \
  "ds_read_b128 a[232:235], v222 offset:8192\n\t" \
  "v_cvt_pk_bf16_f32 v138, v36, v37\n\t" \
  "v_mfma_f32_16x16x32_bf16 a[80:83], a[164:167], v[148:151], a[80:83]\n\t" \
  "v_exp_f32 v44, v44\n\t" \
  "v_mfma_f32_16x16x32_bf16 a[84:87], a[168:171], v[144:147], 0\n\t" \
  "v_exp_f32 v45, v45\n\t" \
  "v_mfma_f32_16x16x32_bf16 a[84:87], a[172:175], v[148:151], a[84:87]\n\t" \
  "ds_read_b128 a[236:239], v223 offset:8192\n\t" \
  "v_cvt_pk_bf16_f32 v139, v38, v39\n\t" \
  "v_mfma_f32_16x16x32_bf16 a[88:91], a[176:179], v[144:147], 0\n\t" \
  "buffer_load_dwordx4 v230, s[64:67], s96 offen offset:2048 lds\n\t" \
  "v_mfma_f32_16x16x32_bf16 a[88:91], a[180:183], v[148:151], a[88:91]\n\t" \
  "v_exp_f32 v46, v46\n\t" \
  "v_mfma_f32_16x16x32_bf16 a[92:95], a[184:187], v[144:147], 0\n\t" \
  "ds_read_b128 a[240:243], v220 offset:12288\n\t" \
  "v_cvt_pk_bf16_f32 v140, v40, v41\n\t" \
  "v_mfma_f32_16x16x32_bf16 a[92:95], a[188:191], v[148:151], a[92:95]\n\t" \
  "v_exp_f32 v47, v47\n\t" \
  "v_mfma_f32_16x16x32_bf16 v[168:171], v[216:219], v[144:147], 0\n\t" \
  "v_exp_f32 v64, v64\n\t" \
  "v_mfma_f32_16x16x32_bf16 v[168:171], v[216:219], v[148:151], v[168:171]\n\t" \
  "ds_read_b128 a[244:247], v221 offset:12288\n\t" \
  "v_cvt_pk_bf16_f32 v141, v42, v43\n\t" \
  "v_mfma_f32_16x16x32_bf16 a[96:99], a[128:131], v[152:155], 0\n\t" \
  "buffer_load_dwordx4 v231, s[64:67], s96 offen offset:3072 lds\n\t" \
  "v_mfma_f32_16x16x32_bf16 a[96:99], a[132:135], v[156:159], a[96:99]\n\t" \
  "v_exp_f32 v65, v65\n\t" \
  "v_mfma_f32_16x16x32_bf16 a[100:103], a[136:139], v[152:155], 0\n\t" \
  "ds_read_b128 a[248:251], v222 offset:12288\n\t" \
  "v_cvt_pk_bf16_f32 v142, v44, v45\n\t" \
  "v_mfma_f32_16x16x32_bf16 a[100:103], a[140:143], v[156:159], a[100:103]\n\t" \
  "v_exp_f32 v66, v66\n\t" \
  "v_mfma_f32_16x16x32_bf16 a[104:107], a[144:147], v[152:155], 0\n\t" \
  "v_exp_f32 v67, v67\n\t" \
  "v_mfma_f32_16x16x32_bf16 a[104:107], a[148:151], v[156:159], a[104:107]\n\t" \
  "ds_read_b128 a[252:255], v223 offset:12288\n\t" \
  "v_cvt_pk_bf16_f32 v143, v46, v47\n\t" \
  "v_mfma_f32_16x16x32_bf16 a[108:111], a[152:155], v[152:155], 0\n\t" \
  "v_exp_f32 v68, v68\n\t" \
  "v_mfma_f32_16x16x32_bf16 a[108:111], a[156:159], v[156:159], a[108:111]\n\t" \
  "v_exp_f32 v69, v69\n\t" \
  "v_mfma_f32_16x16x32_bf16 a[112:115], a[160:163], v[152:155], 0\n\t" \
  "v_exp_f32 v70, v70\n\t" \
  "v_mfma_f32_16x16x32_bf16 a[112:115], a[164:167], v[156:159], a[112:115]\n\t" \
  "v_exp_f32 v71, v71\n\t" \
  "v_mfma_f32_16x16x32_bf16 a[116:119], a[168:171], v[152:155], 0\n\t" \
  "v_exp_f32 v72, v72\n\t" \
  "v_mfma_f32_16x16x32_bf16 a[116:119], a[172:175], v[156:159], a[116:119]\n\t" \
  "v_exp_f32 v73, v73\n\t" \
  "v_mfma_f32_16x16x32_bf16 a[120:123], a[176:179], v[152:155], 0\n\t" \
  "v_exp_f32 v74, v74\n\t" \
  "v_mfma_f32_16x16x32_bf16 a[120:123], a[180:183], v[156:159], a[120:123]\n\t" \
  "v_exp_f32 v75, v75\n\t" \
  "v_mfma_f32_16x16x32_bf16 a[124:127], a[184:187], v[152:155], 0\n\t" \
  "v_exp_f32 v76, v76\n\t" \
  "v_mfma_f32_16x16x32_bf16 a[124:127], a[188:191], v[156:159], a[124:127]\n\t" \
  "v_exp_f32 v77, v77\n\t" \
  "v_mfma_f32_16x16x32_bf16 v[172:175], v[216:219], v[152:155], 0\n\t" \
  "v_exp_f32 v78, v78\n\t" \
  "v_mfma_f32_16x16x32_bf16 v[172:175], v[216:219], v[156:159], v[172:175]\n\t" \
  "s_waitcnt lgkmcnt(12)\n\t" \
  "s_cmp_lg_u32 s99, 0\n\t" \
  "s_cbranch_scc1 .Lsf_p1\n\t" \
  ".Lsf_q1:\n\t" \
  "v_mfma_f32_16x16x32_bf16 v[0:3], a[192:195], v[16:19], v[176:179]\n\t" \
  "ds_read_b64_tr_b16 a[128:129], v210 offset:0\n\t" \
  "v_cvt_pk_bf16_f32 v144, v64, v65\n\t" \
  "v_mfma_f32_16x16x32_bf16 v[0:3], a[196:199], v[20:23], v[0:3]\n\t" \
  "v_exp_f32 v79, v79\n\t" \
  "v_mfma_f32_16x16x32_bf16 v[0:3], a[200:203], v[24:27], v[0:3]\n\t" \
  "ds_read_b64_tr_b16 a[130:131], v210 offset:4096\n\t" \
  "v_cvt_pk_bf16_f32 v145, v66, v67\n\t" \
  "v_mfma_f32_16x16x32_bf16 v[0:3], a[204:207], v[28:31], v[0:3]\n\t" \
  "s_waitcnt lgkmcnt(10)\n\t" \
  "v_exp_f32 v96, v96\n\t" \
  "v_mfma_f32_16x16x32_bf16 v[4:7], a[220:223], v[28:31], v[176:179]\n\t" \
  "ds_read_b64_tr_b16 a[132:133], v210 offset:8192\n\t" \
  "v_cvt_pk_bf16_f32 v146, v68, v69\n\t" \
  "v_mfma_f32_16x16x32_bf16 v[4:7], a[216:219], v[24:27], v[4:7]\n\t" \
  "v_exp_f32 v97, v97\n\t" \
  "v_mfma_f32_16x16x32_bf16 v[4:7], a[212:215], v[20:23], v[4:7]\n\t" \
  "ds_read_b64_tr_b16 a[134:135], v210 offset:12288\n\t" \
  "v_max3_f32 v192, v0, v1, v2\n\t" \
  "v_mfma_f32_16x16x32_bf16 v[4:7], a[208:211], v[16:19], v[4:7]\n\t" \
  "s_waitcnt lgkmcnt(8)\n\t" \
  "ds_read_b64_tr_b16 a[136:137], v211 offset:0\n\t" \
  "v_cvt_pk_bf16_f32 v147, v70, v71\n\t" \
  "v_mfma_f32_16x16x32_bf16 v[8:11], a[224:227], v[16:19], v[176:179]\n\t" \
  "v_exp_f32 v98, v98\n\t" \
  "v_mfma_f32_16x16x32_bf16 v[8:11], a[228:231], v[20:23], v[8:11]\n\t" \
  "ds_read_b64_tr_b16 a[138:139], v211 offset:4096\n\t" \
  "v_cvt_pk_bf16_f32 v148, v72, v73\n\t" \
  "v_mfma_f32_16x16x32_bf16 v[8:11], a[232:235], v[24:27], v[8:11]\n\t" \
  "v_max3_f32 v203, v3, v4, v5\n\t" \
  "v_max3_f32 v192, v192, v6, v7\n\t" \
  "v_mfma_f32_16x16x32_bf16 v[8:11], a[236:239], v[28:31], v[8:11]\n\t" \
  "s_waitcnt lgkmcnt(6)\n\t" \
  "ds_read_b64_tr_b16 a[140:141], v211 offset:8192\n\t" \
  "v_cvt_pk_bf16_f32 v149, v74, v75\n\t" \
  "v_mfma_f32_16x16x32_bf16 v[12:15], a[252:255], v[28:31], v[176:179]\n\t" \
  "v_exp_f32 v99, v99\n\t" \
  "v_mfma_f32_16x16x32_bf16 v[12:15], a[248:251], v[24:27], v[12:15]\n\t" \
  "ds_read_b64_tr_b16 a[142:143], v211 offset:12288\n\t" \
  "v_cvt_pk_bf16_f32 v150, v76, v77\n\t" \
  "v_mfma_f32_16x16x32_bf16 v[12:15], a[244:247], v[20:23], v[12:15]\n\t" \
  "v_max3_f32 v203, v203, v8, v9\n\t" \
  "v_max3_f32 v192, v192, v10, v11\n\t" \
  "v_mfma_f32_16x16x32_bf16 v[12:15], a[240:243], v[16:19], v[12:15]\n\t" \
  "ds_read_b64_tr_b16 a[144:145], v210 offset:256\n\t" \
  "v_cvt_pk_bf16_f32 v151, v78, v79\n\t" \
  "v_mfma_f32_16x16x32_bf16 v[32:35], a[192:195], v[48:51], v[180:183]\n\t" \
  "ds_read_b64_tr_b16 a[146:147], v210 offset:4352\n\t" \
  "v_cvt_pk_bf16_f32 v152, v96, v97\n\t" \
  "v_mfma_f32_16x16x32_bf16 v[32:35], a[196:199], v[52:55], v[32:35]\n\t" \
  "v_exp_f32 v100, v100\n\t" \
  "v_mfma_f32_16x16x32_bf16 v[32:35], a[200:203], v[56:59], v[32:35]\n\t" \
  "v_max3_f32 v203, v203, v12, v13\n\t" \
  "v_max3_f32 v192, v192, v14, v15\n\t" \
  "v_mfma_f32_16x16x32_bf16 v[32:35], a[204:207], v[60:63], v[32:35]\n\t" \
  "v_max_f32 v192, v192, v203\n\t" \
  "v_cmp_lt_f32 vcc, 0x42800000, v192\n\t" \
  "v_mfma_f32_16x16x32_bf16 v[36:39], a[220:223], v[60:63], v[180:183]\n\t" \
  "ds_read_b64_tr_b16 a[148:149], v210 offset:8448\n\t" \
  "v_cvt_pk_bf16_f32 v153, v98, v99\n\t" \
  "v_mfma_f32_16x16x32_bf16 v[36:39], a[216:219], v[56:59], v[36:39]\n\t" \
  "s_cbranch_vccnz .Lsf_c20\n\t" \
  ".Lsf_r20:\n\t" \
  "v_mfma_f32_16x16x32_bf16 v[36:39], a[212:215], v[52:55], v[36:39]\n\t" \
  "ds_read_b64_tr_b16 a[150:151], v210 offset:12544\n\t" \
  "v_max3_f32 v192, v32, v33, v34\n\t" \
  "v_mfma_f32_16x16x32_bf16 v[36:39], a[208:211], v[48:51], v[36:39]\n\t" \
  "v_exp_f32 v101, v101\n\t" \
  "v_mfma_f32_16x16x32_bf16 v[40:43], a[224:227], v[48:51], v[180:183]\n\t" \
  "ds_read_b64_tr_b16 a[152:153], v211 offset:256\n\t" \
  "v_cvt_pk_bf16_f32 v154, v100, v101\n\t" \
  "v_mfma_f32_16x16x32_bf16 v[40:43], a[228:231], v[52:55], v[40:43]\n\t" \
  "v_exp_f32 v102, v102\n\t" \
  "v_mfma_f32_16x16x32_bf16 v[40:43], a[232:235], v[56:59], v[40:43]\n\t" \
  "ds_read_b64_tr_b16 a[154:155], v211 offset:4352\n\t" \
  "v_max3_f32 v203, v35, v36, v37\n\t" \
  "v_mfma_f32_16x16x32_bf16 v[40:43], a[236:239], v[60:63], v[40:43]\n\t" \
  "ds_read_b64_tr_b16 a[156:157], v211 offset:8448\n\t" \
  "v_max3_f32 v192, v192, v38, v39\n\t" \
  "v_mfma_f32_16x16x32_bf16 v[44:47], a[252:255], v[60:63], v[180:183]\n\t" \
  "v_exp_f32 v103, v103\n\t" \
  "v_mfma_f32_16x16x32_bf16 v[44:47], a[248:251], v[56:59], v[44:47]\n\t" \
  "ds_read_b64_tr_b16 a[158:159], v211 offset:12544\n\t" \
  "v_cvt_pk_bf16_f32 v155, v102, v103\n\t" \
  "v_mfma_f32_16x16x32_bf16 v[44:47], a[244:247], v[52:55], v[44:47]\n\t" \
  "v_max3_f32 v203, v203, v40, v41\n\t" \
  "v_max3_f32 v192, v192, v42, v43\n\t" \
  "v_mfma_f32_16x16x32_bf16 v[44:47], a[240:243], v[48:51], v[44:47]\n\t" \
  "v_exp_f32 v104, v104\n\t" \
  "v_mfma_f32_16x16x32_bf16 v[64:67], a[192:195], v[80:83], v[184:187]\n\t" \
  "v_exp_f32 v105, v105\n\t" \
  "v_mfma_f32_16x16x32_bf16 v[64:67], a[196:199], v[84:87], v[64:67]\n\t" \
  "ds_read_b64_tr_b16 a[160:161], v210 offset:512\n\t" \
  "v_cvt_pk_bf16_f32 v156, v104, v105\n\t" \
  "v_mfma_f32_16x16x32_bf16 v[64:67], a[200:203], v[88:91], v[64:67]\n\t" \
  "ds_read_b64_tr_b16 a[162:163], v210 offset:4608\n\t" \
  "v_max3_f32 v203, v203, v44, v45\n\t" \
  "v_mfma_f32_16x16x32_bf16 v[64:67], a[204:207], v[92:95], v[64:67]\n\t" \
  "v_max3_f32 v192, v192, v46, v47\n\t" \
  "v_max_f32 v192, v192, v203\n\t" \
  "v_mfma_f32_16x16x32_bf16 v[68:71], a[220:223], v[92:95], v[184:187]\n\t" \
  "ds_read_b64_tr_b16 a[164:165], v210 offset:8704\n\t" \
  "v_cmp_lt_f32 vcc, 0x42800000, v192\n\t" \
  "v_mfma_f32_16x16x32_bf16 v[68:71], a[216:219], v[88:91], v[68:71]\n\t" \
  "v_exp_f32 v106, v106\n\t" \
  "v_mfma_f32_16x16x32_bf16 v[68:71], a[212:215], v[84:87], v[68:71]\n\t" \
  "s_cbranch_vccnz .Lsf_c21\n\t" \
  ".Lsf_r21:\n\t" \
  "v_mfma_f32_16x16x32_bf16 v[68:71], a[208:211], v[80:83], v[68:71]\n\t" \
  "ds_read_b64_tr_b16 a[166:167], v210 offset:12800\n\t" \
  "v_max3_f32 v192, v64, v65, v66\n\t" \
  "v_mfma_f32_16x16x32_bf16 v[72:75], a[224:227], v[80:83], v[184:187]\n\t" \
  "v_exp_f32 v107, v107\n\t" \
  "v_mfma_f32_16x16x32_bf16 v[72:75], a[228:231], v[84:87], v[72:75]\n\t" \
  "ds_read_b64_tr_b16 a[168:169], v211 offset:512\n\t" \
  "v_cvt_pk_bf16_f32 v157, v106, v107\n\t" \
  "v_mfma_f32_16x16x32_bf16 v[72:75], a[232:235], v[88:91], v[72:75]\n\t" \
  "v_max3_f32 v203, v67, v68, v69\n\t" \
  "v_max3_f32 v192, v192, v70, v71\n\t" \
  "v_mfma_f32_16x16x32_bf16 v[72:75], a[236:239], v[92:95], v[72:75]\n\t" \
  "v_exp_f32 v108, v108\n\t" \
  "v_mfma_f32_16x16x32_bf16 v[76:79], a[252:255], v[92:95], v[184:187]\n\t" \
  "v_exp_f32 v109, v109\n\t" \
  "v_mfma_f32_16x16x32_bf16 v[76:79], a[248:251], v[88:91], v[76:79]\n\t" \
  "ds_read_b64_tr_b16 a[170:171], v211 offset:4608\n\t" \
  "v_cvt_pk_bf16_f32 v158, v108, v109\n\t" \
  "v_mfma_f32_16x16x32_bf16 v[76:79], a[244:247], v[84:87], v[76:79]\n\t" \
  "ds_read_b64_tr_b16 a[172:173], v211 offset:8704\n\t" \
  "v_max3_f32 v203, v203, v72, v73\n\t" \
  "v_mfma_f32_16x16x32_bf16 v[76:79], a[240:243], v[80:83], v[76:79]\n\t" \
  "ds_read_b64_tr_b16 a[174:175], v211 offset:12800\n\t" \
  "v_max3_f32 v192, v192, v74, v75\n\t" \
  "v_mfma_f32_16x16x32_bf16 v[96:99], a[192:195], v[112:115], v[188:191]\n\t" \
  "v_exp_f32 v110, v110\n\t" \
  "v_mfma_f32_16x16x32_bf16 v[96:99], a[196:199], v[116:119], v[96:99]\n\t" \
  "v_exp_f32 v111, v111\n\t" \
  "v_mfma_f32_16x16x32_bf16 v[96:99], a[200:203], v[120:123], v[96:99]\n\t" \
  "ds_read_b64_tr_b16 a[176:177], v210 offset:768\n\t" \
  "v_max3_f32 v203, v203, v76, v77\n\t" \
  "v_mfma_f32_16x16x32_bf16 v[96:99], a[204:207], v[124:127], v[96:99]\n\t" \
  "v_max3_f32 v192, v192, v78, v79\n\t" \
  "v_max_f32 v192, v192, v203\n\t" \
  "v_mfma_f32_16x16x32_bf16 v[100:103], a[220:223], v[124:127], v[188:191]\n\t" \
  "ds_read_b64_tr_b16 a[178:179], v210 offset:4864\n\t" \
  "v_cmp_lt_f32 vcc, 0x42800000, v192\n\t" \
  "v_mfma_f32_16x16x32_bf16 v[100:103], a[216:219], v[120:123], v[100:103]\n\t" \
  "ds_read_b64_tr_b16 a[180:181], v210 offset:8960\n\t" \
  "v_cvt_pk_bf16_f32 v159, v110, v111\n\t" \
  "v_mfma_f32_16x16x32_bf16 v[100:103], a[212:215], v[116:119], v[100:103]\n\t" \
  "s_cbranch_vccnz .Lsf_c22\n\t" \
  ".Lsf_r22:\n\t" \
  "v_mfma_f32_16x16x32_bf16 v[100:103], a[208:211], v[112:115], v[100:103]\n\t" \
  "ds_read_b64_tr_b16 a[182:183], v210 offset:13056\n\t" \
  "v_max3_f32 v192, v96, v97, v98\n\t" \
  "v_mfma_f32_16x16x32_bf16 v[104:107], a[224:227], v[112:115], v[188:191]\n\t" \
  "ds_read_b64_tr_b16 a[184:185], v211 offset:768\n\t" \
  "v_mfma_f32_16x16x32_bf16 v[104:107], a[228:231], v[116:119], v[104:107]\n\t" \
  "ds_read_b64_tr_b16 a[186:187], v211 offset:4864\n\t" \
  "v_mfma_f32_16x16x32_bf16 v[104:107], a[232:235], v[120:123], v[104:107]\n\t" \
  "ds_read_b64_tr_b16 a[188:189], v211 offset:8960\n\t" \
  "v_max3_f32 v203, v99, v100, v101\n\t" \
  "v_mfma_f32_16x16x32_bf16 v[104:107], a[236:239], v[124:127], v[104:107]\n\t" \
  "ds_read_b64_tr_b16 a[190:191], v211 offset:13056\n\t" \
  "v_max3_f32 v192, v192, v102, v103\n\t" \
  "v_mfma_f32_16x16x32_bf16 v[108:111], a[252:255], v[124:127], v[188:191]\n\t" \
  "v_exp_f32 v0, v0\n\t" \
  "v_mfma_f32_16x16x32_bf16 v[108:111], a[248:251], v[120:123], v[108:111]\n\t" \
  "v_exp_f32 v1, v1\n\t" \
  "v_mfma_f32_16x16x32_bf16 v[108:111], a[244:247], v[116:119], v[108:111]\n\t" \
  "s_waitcnt vmcnt(0) lgkmcnt(0)\n\t" \
  "v_max3_f32 v203, v203, v104, v105\n\t" \
  "v_max3_f32 v192, v192, v106, v107\n\t" \
  "v_mfma_f32_16x16x32_bf16 v[108:111], a[240:243], v[112:115], v[108:111]\n\t" \
  "s_barrier\n\t" \
  "v_mfma_f32_16x16x32_bf16 a[0:3], a[128:131], v[128:131], a[0:3]\n\t" \
  "v_exp_f32 v2, v2\n\t" \
  "v_mfma_f32_16x16x32_bf16 a[0:3], a[132:135], v[132:135], a[0:3]\n\t" \
  "v_exp_f32 v3, v3\n\t" \
  "v_mfma_f32_16x16x32_bf16 a[4:7], a[136:139], v[128:131], a[4:7]\n\t" \
  "v_exp_f32 v4, v4\n\t" \
  "v_mfma_f32_16x16x32_bf16 a[4:7], a[140:143], v[132:135], a[4:7]\n\t" \
  "v_max3_f32 v203, v203, v108, v109\n\t" \
  "v_max3_f32 v192, v192, v110, v111\n\t" \
  "v_mfma_f32_16x16x32_bf16 a[8:11], a[144:147], v[128:131], a[8:11]\n\t" \
  "v_max_f32 v192, v192, v203\n\t" \
  "v_cmp_lt_f32 vcc, 0x42800000, v192\n\t" \
  "v_mfma_f32_16x16x32_bf16 a[8:11], a[148:151], v[132:135], a[8:11]\n\t" \
  "v_exp_f32 v5, v5\n\t" \
  "v_mfma_f32_16x16x32_bf16 a[12:15], a[152:155], v[128:131], a[12:15]\n\t" \
  "s_cbranch_vccnz .Lsf_c23\n\t" \
  ".Lsf_r23:\n\t" \
  "v_mfma_f32_16x16x32_bf16 a[12:15], a[156:159], v[132:135], a[12:15]\n\t" \
  "s_add_u32 m0, s38, 49152\n\t" \
  "v_exp_f32 v6, v6\n\t" \
  "v_mfma_f32_16x16x32_bf16 a[16:19], a[160:163], v[128:131], a[16:19]\n\t" \
  "s_add_u32 s96, s38, 65536\n\t" \
  "v_exp_f32 v7, v7\n\t" \
  "v_mfma_f32_16x16x32_bf16 a[16:19], a[164:167], v[132:135], a[16:19]\n\t" \
  "v_exp_f32 v8, v8\n\t" \
  "v_mfma_f32_16x16x32_bf16 a[20:23], a[168:171], v[128:131], a[20:23]\n\t" \
  "buffer_load_dwordx4 v224, s[60:63], s96 offen offset:0 lds\n\t" \
  "v_mfma_f32_16x16x32_bf16 a[20:23], a[172:175], v[132:135], a[20:23]\n\t" \
  "v_exp_f32 v9, v9\n\t" \
  "v_mfma_f32_16x16x32_bf16 a[24:27], a[176:179], v[128:131], a[24:27]\n\t" \
  "v_exp_f32 v10, v10\n\t" \
  "v_mfma_f32_16x16x32_bf16 a[24:27], a[180:183], v[132:135], a[24:27]\n\t" \
  "v_exp_f32 v11, v11\n\t" \
  "v_mfma_f32_16x16x32_bf16 a[28:31], a[184:187], v[128:131], a[28:31]\n\t" \
  "v_exp_f32 v12, v12\n\t" \
  "v_mfma_f32_16x16x32_bf16 a[28:31], a[188:191], v[132:135], a[28:31]\n\t" \
  "v_exp_f32 v13, v13\n\t" \
  "v_mfma_f32_16x16x32_bf16 v[160:163], v[216:219], v[128:131], v[160:163]\n\t" \
  "v_exp_f32 v14, v14\n\t" \
  "v_mfma_f32_16x16x32_bf16 v[160:163], v[216:219], v[132:135], v[160:163]\n\t" \
  "buffer_load_dwordx4 v225, s[60:63], s96 offen offset:1024 lds\n\t" \
  "v_mfma_f32_16x16x32_bf16 a[32:35], a[128:131], v[136:139], a[32:35]\n\t" \
  "ds_read_b128 a[192:195], v220 offset:16384\n\t" \
  "v_cvt_pk_bf16_f32 v128, v0, v1\n\t" \
  "v_mfma_f32_16x16x32_bf16 a[32:35], a[132:135], v[140:143], a[32:35]\n\t" \
  "ds_read_b128 a[196:199], v221 offset:16384\n\t" \
  "v_cvt_pk_bf16_f32 v129, v2, v3\n\t" \
  "v_mfma_f32_16x16x32_bf16 a[36:39], a[136:139], v[136:139], a[36:39]\n\t" \
  "v_exp_f32 v15, v15\n\t" \
  "v_mfma_f32_16x16x32_bf16 a[36:39], a[140:143], v[140:143], a[36:39]\n\t" \
  "v_exp_f32 v32, v32\n\t" \
  "v_mfma_f32_16x16x32_bf16 a[40:43], a[144:147], v[136:139], a[40:43]\n\t" \
  "ds_read_b128 a[200:203], v222 offset:16384\n\t" \
  "v_cvt_pk_bf16_f32 v130, v4, v5\n\t" \
  "v_mfma_f32_16x16x32_bf16 a[40:43], a[148:151], v[140:143], a[40:43]\n\t" \
  "buffer_load_dwordx4 v226, s[60:63], s96 offen offset:2048 lds\n\t" \
  "v_mfma_f32_16x16x32_bf16 a[44:47], a[152:155], v[136:139], a[44:47]\n\t" \
  "ds_read_b128 a[204:207], v223 offset:16384\n\t" \
  "v_cvt_pk_bf16_f32 v131, v6, v7\n\t" \
  "v_mfma_f32_16x16x32_bf16 a[44:47], a[156:159], v[140:143], a[44:47]\n\t" \
  "v_exp_f32 v33, v33\n\t" \
  "v_mfma_f32_16x16x32_bf16 a[48:51], a[160:163], v[136:139], a[48:51]\n\t" \
  "v_exp_f32 v34, v34\n\t" \
  "v_mfma_f32_16x16x32_bf16 a[48:51], a[164:167], v[140:143], a[48:51]\n\t" \
  "ds_read_b128 a[208:211], v220 offset:20480\n\t" \
  "v_cvt_pk_bf16_f32 v132, v8, v9\n\t" \
  "v_mfma_f32_16x16x32_bf16 a[52:55], a[168:171], v[136:139], a[52:55]\n\t" \
  "v_exp_f32 v35, v35\n\t" \
  "v_mfma_f32_16x16x32_bf16 a[52:55], a[172:175], v[140:143], a[52:55]\n\t" \
  "buffer_load_dwordx4 v227, s[60:63], s96 offen offset:3072 lds\n\t" \
  "v_mfma_f32_16x16x32_bf16 a[56:59], a[176:179], v[136:139], a[56:59]\n\t" \
  "ds_read_b128 a[212:215], v221 offset:20480\n\t" \
  "v_cvt_pk_bf16_f32 v133, v10, v11\n\t" \
  "v_mfma_f32_16x16x32_bf16 a[56:59], a[180:183], v[140:143], a[56:59]\n\t" \
  "s_add_u32 m0, s38, 147456\n\t" \
  "v_exp_f32 v36, v36\n\t" \
  "v_mfma_f32_16x16x32_bf16 a[60:63], a[184:187], v[136:139], a[60:63]\n\t" \
  "s_add_u32 s96, s38, 49152\n\t" \
  "v_exp_f32 v37, v37\n\t" \
  "v_mfma_f32_16x16x32_bf16 a[60:63], a[188:191], v[140:143], a[60:63]\n\t" \
  "ds_read_b128 a[216:219], v222 offset:20480\n\t" \
  "v_cvt_pk_bf16_f32 v134, v12, v13\n\t" \
  "v_mfma_f32_16x16x32_bf16 v[164:167], v[216:219], v[136:139], v[164:167]\n\t" \
  "v_exp_f32 v38, v38\n\t" \
  "v_mfma_f32_16x16x32_bf16 v[164:167], v[216:219], v[140:143], v[164:167]\n\t" \
  "buffer_load_dwordx4 v230, s[64:67], s96 offen offset:0 lds\n\t" \
  "v_mfma_f32_16x16x32_bf16 a[64:67], a[128:131], v[144:147], a[64:67]\n\t" \
  "ds_read_b128 a[220:223], v223 offset:20480\n\t" \
  "v_cvt_pk_bf16_f32 v135, v14, v15\n\t" \
  "v_mfma_f32_16x16x32_bf16 a[64:67], a[132:135], v[148:151], a[64:67]\n\t" \
  "v_exp_f32 v39, v39\n\t" \
  "v_mfma_f32_16x16x32_bf16 a[68:71], a[136:139], v[144:147], a[68:71]\n\t" \
  "v_exp_f32 v40, v40\n\t" \
  "v_mfma_f32_16x16x32_bf16 a[68:71], a[140:143], v[148:151], a[68:71]\n\t" \
  "ds_read_b128 a[224:227], v220 offset:24576\n\t" \
  "v_cvt_pk_bf16_f32 v136, v32, v33\n\t" \
  "v_mfma_f32_16x16x32_bf16 a[72:75], a[144:147], v[144:147], a[72:75]\n\t" \
  "v_exp_f32 v41, v41\n\t" \
  "v_mfma_f32_16x16x32_bf16 a[72:75], a[148:151], v[148:151], a[72:75]\n\t" \
  "ds_read_b128 a[228:231], v221 offset:24576\n\t" \
  "v_cvt_pk_bf16_f32 v137, v34, v35\n\t" \
  "v_mfma_f32_16x16x32_bf16 a[76:79], a[152:155], v[144:147], a[76:79]\n\t" \
  "buffer_load_dwordx4 v231, s[64:67], s96 offen offset:1024 lds\n\t" \
  "v_mfma_f32_16x16x32_bf16 a[76:79], a[156:159], v[148:151], a[76:79]\n\t" \
  "v_exp_f32 v42, v42\n\t" \
  "v_mfma_f32_16x16x32_bf16 a[80:83], a[160:163], v[144:147], a[80:83]\n\t" \
  "ds_read_b128 a[232:235], v222 offset:24576\n\t" \
  "v_cvt_pk_bf16_f32 v138, v36, v37\n\t" \
  "v_mfma_f32_16x16x32_bf16 a[80:83], a[164:167], v[148:151], a[80:83]\n\t" \
  "v_exp_f32 v43, v43\n\t" \
  "v_mfma_f32_16x16x32_bf16 a[84:87], a[168:171], v[144:147], a[84:87]\n\t" \
  "v_exp_f32 v44, v44\n\t" \
  "v_mfma_f32_16x16x32_bf16 a[84:87], a[172:175], v[148:151], a[84:87]\n\t" \
  "ds_read_b128 a[236:239], v223 offset:24576\n\t" \
  "v_cvt_pk_bf16_f32 v139, v38, v39\n\t" \
  "v_mfma_f32_16x16x32_bf16 a[88:91], a[176:179], v[144:147], a[88:91]\n\t" \
  "buffer_load_dwordx4 v230, s[64:67], s96 offen offset:2048 lds\n\t" \
  "v_mfma_f32_16x16x32_bf16 a[88:91], a[180:183], v[148:151], a[88:91]\n\t" \
  "v_exp_f32 v45, v45\n\t" \
  "v_mfma_f32_16x16x32_bf16 a[92:95], a[184:187], v[144:147], a[92:95]\n\t" \
  "ds_read_b128 a[240:243], v220 offset:28672\n\t" \
  "v_cvt_pk_bf16_f32 v140, v40, v41\n\t" \
  "v_mfma_f32_16x16x32_bf16 a[92:95], a[188:191], v[148:151], a[92:95]\n\t" \
  "v_exp_f32 v46, v46\n\t" \
  "v_mfma_f32_16x16x32_bf16 v[168:171], v[216:219], v[144:147], v[168:171]\n\t" \
  "v_exp_f32 v47, v47\n\t" \
  "v_mfma_f32_16x16x32_bf16 v[168:171], v[216:219], v[148:151], v[168:171]\n\t" \
  "ds_read_b128 a[244:247], v221 offset:28672\n\t" \
  "v_cvt_pk_bf16_f32 v141, v42, v43\n\t" \
  "v_mfma_f32_16x16x32_bf16 a[96:99], a[128:131], v[152:155], a[96:99]\n\t" \
  "buffer_load_dwordx4 v231, s[64:67], s96 offen offset:3072 lds\n\t" \
  "v_mfma_f32_16x16x32_bf16 a[96:99], a[132:135], v[156:159], a[96:99]\n\t" \
  "v_exp_f32 v64, v64\n\t" \
  "v_mfma_f32_16x16x32_bf16 a[100:103], a[136:139], v[152:155], a[100:103]\n\t" \
  "ds_read_b128 a[248:251], v222 offset:28672\n\t" \
  "v_cvt_pk_bf16_f32 v142, v44, v45\n\t" \
  "v_mfma_f32_16x16x32_bf16 a[100:103], a[140:143], v[156:159], a[100:103]\n\t" \
  "v_exp_f32 v65, v65\n\t" \
  "v_mfma_f32_16x16x32_bf16 a[104:107], a[144:147], v[152:155], a[104:107]\n\t" \
  "v_exp_f32 v66, v66\n\t" \
  "v_mfma_f32_16x16x32_bf16 a[104:107], a[148:151], v[156:159], a[104:107]\n\t" \
  "ds_read_b128 a[252:255], v223 offset:28672\n\t" \
  "v_cvt_pk_bf16_f32 v143, v46, v47\n\t" \
  "v_mfma_f32_16x16x32_bf16 a[108:111], a[152:155], v[152:155], a[108:111]\n\t" \
  "v_exp_f32 v67, v67\n\t" \
  "v_mfma_f32_16x16x32_bf16 a[108:111], a[156:159], v[156:159], a[108:111]\n\t" \
  "v_exp_f32 v68, v68\n\t" \
  "v_mfma_f32_16x16x32_bf16 a[112:115], a[160:163], v[152:155], a[112:115]\n\t" \
  "v_exp_f32 v69, v69\n\t" \
  "v_mfma_f32_16x16x32_bf16 a[112:115], a[164:167], v[156:159], a[112:115]\n\t" \
  "v_exp_f32 v70, v70\n\t" \
  "v_mfma_f32_16x16x32_bf16 a[116:119], a[168:171], v[152:155], a[116:119]\n\t" \
  "v_exp_f32 v71, v71\n\t" \
  "v_mfma_f32_16x16x32_bf16 a[116:119], a[172:175], v[156:159], a[116:119]\n\t" \
  "v_exp_f32 v72, v72\n\t" \
  "v_mfma_f32_16x16x32_bf16 a[120:123], a[176:179], v[152:155], a[120:123]\n\t" \
  "v_exp_f32 v73, v73\n\t" \
  "v_mfma_f32_16x16x32_bf16 a[120:123], a[180:183], v[156:159], a[120:123]\n\t" \
  "v_exp_f32 v74, v74\n\t" \
  "v_mfma_f32_16x16x32_bf16 a[124:127], a[184:187], v[152:155], a[124:127]\n\t" \
  "v_exp_f32 v75, v75\n\t" \
  "v_mfma_f32_16x16x32_bf16 a[124:127], a[188:191], v[156:159], a[124:127]\n\t" \
  "v_exp_f32 v76, v76\n\t" \
  "v_mfma_f32_16x16x32_bf16 v[172:175], v[216:219], v[152:155], v[172:175]\n\t" \
  "v_exp_f32 v77, v77\n\t" \
  "v_mfma_f32_16x16x32_bf16 v[172:175], v[216:219], v[156:159], v[172:175]\n\t" \
  "s_waitcnt lgkmcnt(12)\n\t" \
  "s_cmp_lg_u32 s99, 0\n\t" \
  "s_cbranch_scc1 .Lsf_p2\n\t" \
  ".Lsf_q2:\n\t" \
  "v_mfma_f32_16x16x32_bf16 v[0:3], a[192:195], v[16:19], v[176:179]\n\t" \
  "ds_read_b64_tr_b16 a[128:129], v228 offset:0\n\t" \
  "v_cvt_pk_bf16_f32 v144, v64, v65\n\t" \
  "v_mfma_f32_16x16x32_bf16 v[0:3], a[196:199], v[20:23], v[0:3]\n\t" \
  "v_exp_f32 v78, v78\n\t" \
  "v_mfma_f32_16x16x32_bf16 v[0:3], a[200:203], v[24:27], v[0:3]\n\t" \
  "ds_read_b64_tr_b16 a[130:131], v228 offset:4096\n\t" \
  "v_cvt_pk_bf16_f32 v145, v66, v67\n\t" \
  "v_mfma_f32_16x16x32_bf16 v[0:3], a[204:207], v[28:31], v[0:3]\n\t" \
  "s_waitcnt lgkmcnt(10)\n\t" \
  "v_exp_f32 v79, v79\n\t" \
  "v_mfma_f32_16x16x32_bf16 v[4:7], a[220:223], v[28:31], v[176:179]\n\t" \
  "ds_read_b64_tr_b16 a[132:133], v228 offset:8192\n\t" \
  "v_cvt_pk_bf16_f32 v146, v68, v69\n\t" \
  "v_mfma_f32_16x16x32_bf16 v[4:7], a[216:219], v[24:27], v[4:7]\n\t" \
  "v_exp_f32 v96, v96\n\t" \
  "v_mfma_f32_16x16x32_bf16 v[4:7], a[212:215], v[20:23], v[4:7]\n\t" \
  "ds_read_b64_tr_b16 a[134:135], v228 offset:12288\n\t" \
  "v_max3_f32 v192, v0, v1, v2\n\t" \
  "v_mfma_f32_16x16x32_bf16 v[4:7], a[208:211], v[16:19], v[4:7]\n\t" \
  "s_waitcnt lgkmcnt(8)\n\t" \
  "ds_read_b64_tr_b16 a[136:137], v229 offset:0\n\t" \
  "v_cvt_pk_bf16_f32 v147, v70, v71\n\t" \
  "v_mfma_f32_16x16x32_bf16 v[8:11], a[224:227], v[16:19], v[176:179]\n\t" \
  "v_exp_f32 v97, v97\n\t" \
  "v_mfma_f32_16x16x32_bf16 v[8:11], a[228:231], v[20:23], v[8:11]\n\t" \
  "ds_read_b64_tr_b16 a[138:139], v229 offset:4096\n\t" \
  "v_cvt_pk_bf16_f32 v148, v72, v73\n\t" \
  "v_mfma_f32_16x16x32_bf16 v[8:11], a[232:235], v[24:27], v[8:11]\n\t" \
  "v_max3_f32 v203, v3, v4, v5\n\t" \
  "v_max3_f32 v192, v192, v6, v7\n\t" \
  "v_mfma_f32_16x16x32_bf16 v[8:11], a[236:239], v[28:31], v[8:11]\n\t" \
  "s_waitcnt lgkmcnt(6)\n\t" \
  "ds_read_b64_tr_b16 a[140:141], v229 offset:8192\n\t" \
  "v_cvt_pk_bf16_f32 v149, v74, v75\n\t" \
  "v_mfma_f32_16x16x32_bf16 v[12:15], a[252:255], v[28:31], v[176:179]\n\t" \
  "v_exp_f32 v98, v98\n\t" \
  "v_mfma_f32_16x16x32_bf16 v[12:15], a[248:251], v[24:27], v[12:15]\n\t" \
  "ds_read_b64_tr_b16 a[142:143], v229 offset:12288\n\t" \
  "v_cvt_pk_bf16_f32 v150, v76, v77\n\t" \
  "v_mfma_f32_16x16x32_bf16 v[12:15], a[244:247], v[20:23], v[12:15]\n\t" \
  "v_max3_f32 v203, v203, v8, v9\n\t" \
  "v_max3_f32 v192, v192, v10, v11\n\t" \
  "v_mfma_f32_16x16x32_bf16 v[12:15], a[240:243], v[16:19], v[12:15]\n\t" \
  "ds_read_b64_tr_b16 a[144:145], v228 offset:256\n\t" \
  "v_cvt_pk_bf16_f32 v151, v78, v79\n\t" \
  "v_mfma_f32_16x16x32_bf16 v[32:35], a[192:195], v[48:51], v[180:183]\n\t" \
  "ds_read_b64_tr_b16 a[146:147], v228 offset:4352\n\t" \
  "v_cvt_pk_bf16_f32 v152, v96, v97\n\t" \
  "v_mfma_f32_16x16x32_bf16 v[32:35], a[196:199], v[52:55], v[32:35]\n\t" \
  "v_exp_f32 v99, v99\n\t" \
  "v_mfma_f32_16x16x32_bf16 v[32:35], a[200:203], v[56:59], v[32:35]\n\t" \
  "v_max3_f32 v203, v203, v12, v13\n\t" \
  "v_max3_f32 v192, v192, v14, v15\n\t" \
  "v_mfma_f32_16x16x32_bf16 v[32:35], a[204:207], v[60:63], v[32:35]\n\t" \
  "v_max_f32 v192, v192, v203\n\t" \
  "v_cmp_lt_f32 vcc, 0x42800000, v192\n\t" \
  "v_mfma_f32_16x16x32_bf16 v[36:39], a[220:223], v[60:63], v[180:183]\n\t" \
  "ds_read_b64_tr_b16 a[148:149], v228 offset:8448\n\t" \
  "v_cvt_pk_bf16_f32 v153, v98, v99\n\t" \
  "v_mfma_f32_16x16x32_bf16 v[36:39], a[216:219], v[56:59], v[36:39]\n\t" \
  "s_cbranch_vccnz .Lsf_c30\n\t" \
  ".Lsf_r30:\n\t" \
  "v_mfma_f32_16x16x32_bf16 v[36:39], a[212:215], v[52:55], v[36:39]\n\t" \
  "ds_read_b64_tr_b16 a[150:151], v228 offset:12544\n\t" \
  "v_max3_f32 v192, v32, v33, v34\n\t" \
  "v_mfma_f32_16x16x32_bf16 v[36:39], a[208:211], v[48:51], v[36:39]\n\t" \
  "v_exp_f32 v100, v100\n\t" \
  "v_mfma_f32_16x16x32_bf16 v[40:43], a[224:227], v[48:51], v[180:183]\n\t" \
  "v_exp_f32 v101, v101\n\t" \
  "v_mfma_f32_16x16x32_bf16 v[40:43], a[228:231], v[52:55], v[40:43]\n\t" \
  "ds_read_b64_tr_b16 a[152:153], v229 offset:256\n\t" \
  "v_cvt_pk_bf16_f32 v154, v100, v101\n\t" \
  "v_mfma_f32_16x16x32_bf16 v[40:43], a[232:235], v[56:59], v[40:43]\n\t" \
  "ds_read_b64_tr_b16 a[154:155], v229 offset:4352\n\t" \
  "v_max3_f32 v203, v35, v36, v37\n\t" \
  "v_mfma_f32_16x16x32_bf16 v[40:43], a[236:239], v[60:63], v[40:43]\n\t" \
  "ds_read_b64_tr_b16 a[156:157], v229 offset:8448\n\t" \
  "v_max3_f32 v192, v192, v38, v39\n\t" \
  "v_mfma_f32_16x16x32_bf16 v[44:47], a[252:255], v[60:63], v[180:183]\n\t" \
  "v_exp_f32 v102, v102\n\t" \
  "v_mfma_f32_16x16x32_bf16 v[44:47], a[248:251], v[56:59], v[44:47]\n\t" \
  "v_exp_f32 v103, v103\n\t" \
  "v_mfma_f32_16x16x32_bf16 v[44:47], a[244:247], v[52:55], v[44:47]\n\t" \
  "ds_read_b64_tr_b16 a[158:159], v229 offset:12544\n\t" \
  "v_max3_f32 v203, v203, v40, v41\n\t" \
  "v_mfma_f32_16x16x32_bf16 v[44:47], a[240:243], v[48:51], v[44:47]\n\t" \
  "ds_read_b64_tr_b16 a[160:161], v228 offset:512\n\t" \
  "v_max3_f32 v192, v192, v42, v43\n\t" \
  "v_mfma_f32_16x16x32_bf16 v[64:67], a[192:195], v[80:83], v[184:187]\n\t" \
  "ds_read_b64_tr_b16 a[162:163], v228 offset:4608\n\t" \
  "v_cvt_pk_bf16_f32 v155, v102, v103\n\t" \
  "v_mfma_f32_16x16x32_bf16 v[64:67], a[196:199], v[84:87], v[64:67]\n\t" \
  "v_exp_f32 v104, v104\n\t" \
  "v_mfma_f32_16x16x32_bf16 v[64:67], a[200:203], v[88:91], v[64:67]\n\t" \
  "v_max3_f32 v203, v203, v44, v45\n\t" \
  "v_max3_f32 v192, v192, v46, v47\n\t" \
  "v_mfma_f32_16x16x32_bf16 v[64:67], a[204:207], v[92:95], v[64:67]\n\t" \
  "v_max_f32 v192, v192, v203\n\t" \
  "v_cmp_lt_f32 vcc, 0x42800000, v192\n\t" \
  "v_mfma_f32_16x16x32_bf16 v[68:71], a[220:223], v[92:95], v[184:187]\n\t" \
  "v_exp_f32 v105, v105\n\t" \
  "v_mfma_f32_16x16x32_bf16 v[68:71], a[216:219], v[88:91], v[68:71]\n\t" \
  "s_cbranch_vccnz .Lsf_c31\n\t" \
  ".Lsf_r31:\n\t" \
  "v_mfma_f32_16x16x32_bf16 v[68:71], a[212:215], v[84:87], v[68:71]\n\t" \
  "ds_read_b64_tr_b16 a[164:165], v228 offset:8704\n\t" \
  "v_max3_f32 v192, v64, v65, v66\n\t" \
  "v_mfma_f32_16x16x32_bf16 v[68:71], a[208:211], v[80:83], v[68:71]\n\t" \
  "ds_read_b64_tr_b16 a[166:167], v228 offset:12800\n\t" \
  "v_cvt_pk_bf16_f32 v156, v104, v105\n\t" \
  "v_mfma_f32_16x16x32_bf16 v[72:75], a[224:227], v[80:83], v[184:187]\n\t" \
  "v_exp_f32 v106, v106\n\t" \
  "v_mfma_f32_16x16x32_bf16 v[72:75], a[228:231], v[84:87], v[72:75]\n\t" \
  "v_exp_f32 v107, v107\n\t" \
  "v_mfma_f32_16x16x32_bf16 v[72:75], a[232:235], v[88:91], v[72:75]\n\t" \
  "ds_read_b64_tr_b16 a[168:169], v229 offset:512\n\t" \
  "v_max3_f32 v203, v67, v68, v69\n\t" \
  "v_mfma_f32_16x16x32_bf16 v[72:75], a[236:239], v[92:95], v[72:75]\n\t" \
  "ds_read_b64_tr_b16 a[170:171], v229 offset:4608\n\t" \
  "v_max3_f32 v192, v192, v70, v71\n\t" \
  "v_mfma_f32_16x16x32_bf16 v[76:79], a[252:255], v[92:95], v[184:187]\n\t" \
  "ds_read_b64_tr_b16 a[172:173], v229 offset:8704\n\t" \
  "v_cvt_pk_bf16_f32 v157, v106, v107\n\t" \
  "v_mfma_f32_16x16x32_bf16 v[76:79], a[248:251], v[88:91], v[76:79]\n\t" \
  "v_exp_f32 v108, v108\n\t" \
  "v_mfma_f32_16x16x32_bf16 v[76:79], a[244:247], v[84:87], v[76:79]\n\t" \
  "v_max3_f32 v203, v203, v72, v73\n\t" \
  "v_max3_f32 v192, v192, v74, v75\n\t" \
  "v_mfma_f32_16x16x32_bf16 v[76:79], a[240:243], v[80:83], v[76:79]\n\t" \
  "v_exp_f32 v109, v109\n\t" \
  "v_mfma_f32_16x16x32_bf16 v[96:99], a[192:195], v[112:115], v[188:191]\n\t" \
  "ds_read_b64_tr_b16 a[174:175], v229 offset:12800\n\t" \
  "v_cvt_pk_bf16_f32 v158, v108, v109\n\t" \
  "v_mfma_f32_16x16x32_bf16 v[96:99], a[196:199], v[116:119], v[96:99]\n\t" \
  "v_exp_f32 v110, v110\n\t" \
  "v_mfma_f32_16x16x32_bf16 v[96:99], a[200:203], v[120:123], v[96:99]\n\t" \
  "ds_read_b64_tr_b16 a[176:177], v228 offset:768\n\t" \
  "v_max3_f32 v203, v203, v76, v77\n\t" \
  "v_mfma_f32_16x16x32_bf16 v[96:99], a[204:207], v[124:127], v[96:99]\n\t" \
  "v_max3_f32 v192, v192, v78, v79\n\t" \
  "v_max_f32 v192, v192, v203\n\t" \
  "v_mfma_f32_16x16x32_bf16 v[100:103], a[220:223], v[124:127], v[188:191]\n\t" \
  "ds_read_b64_tr_b16 a[178:179], v228 offset:4864\n\t" \
  "v_cmp_lt_f32 vcc, 0x42800000, v192\n\t" \
  "v_mfma_f32_16x16x32_bf16 v[100:103], a[216:219], v[120:123], v[100:103]\n\t" \
  "ds_read_b64_tr_b16 a[180:181], v228 offset:8960\n\t" \
  "v_exp_f32 v111, v111\n\t" \
  "v_mfma_f32_16x16x32_bf16 v[100:103], a[212:215], v[116:119], v[100:103]\n\t" \
  "s_cbranch_vccnz .Lsf_c32\n\t" \
  ".Lsf_r32:\n\t" \
  "v_mfma_f32_16x16x32_bf16 v[100:103], a[208:211], v[112:115], v[100:103]\n\t" \
  "ds_read_b64_tr_b16 a[182:183], v228 offset:13056\n\t" \
  "v_max3_f32 v192, v96, v97, v98\n\t" \
  "v_mfma_f32_16x16x32_bf16 v[104:107], a[224:227], v[112:115], v[188:191]\n\t" \
  "ds_read_b64_tr_b16 a[184:185], v229 offset:768\n\t" \
  "v_cvt_pk_bf16_f32 v159, v110, v111\n\t" \
  "v_mfma_f32_16x16x32_bf16 v[104:107], a[228:231], v[116:119], v[104:107]\n\t" \
  "ds_read_b64_tr_b16 a[186:187], v229 offset:4864\n\t" \
  "v_mfma_f32_16x16x32_bf16 v[104:107], a[232:235], v[120:123], v[104:107]\n\t" \
  "ds_read_b64_tr_b16 a[188:189], v229 offset:8960\n\t" \
  "v_max3_f32 v203, v99, v100, v101\n\t" \
  "v_mfma_f32_16x16x32_bf16 v[104:107], a[236:239], v[124:127], v[104:107]\n\t" \
  "ds_read_b64_tr_b16 a[190:191], v229 offset:13056\n\t" \
  "v_max3_f32 v192, v192, v102, v103\n\t" \
  "v_mfma_f32_16x16x32_bf16 v[108:111], a[252:255], v[124:127], v[188:191]\n\t" \
  "v_exp_f32 v0, v0\n\t" \
  "v_mfma_f32_16x16x32_bf16 v[108:111], a[248:251], v[120:123], v[108:111]\n\t" \
  "v_exp_f32 v1, v1\n\t" \
  "v_mfma_f32_16x16x32_bf16 v[108:111], a[244:247], v[116:119], v[108:111]\n\t" \
  "s_waitcnt vmcnt(0) lgkmcnt(0)\n\t" \
  "v_max3_f32 v203, v203, v104, v105\n\t" \
  "v_max3_f32 v192, v192, v106, v107\n\t" \
  "v_mfma_f32_16x16x32_bf16 v[108:111], a[240:243], v[112:115], v[108:111]\n\t" \
  "s_barrier\n\t" \
  "v_mfma_f32_16x16x32_bf16 a[0:3], a[128:131], v[128:131], a[0:3]\n\t" \
  "v_exp_f32 v2, v2\n\t" \
  "v_mfma_f32_16x16x32_bf16 a[0:3], a[132:135], v[132:135], a[0:3]\n\t" \
  "v_exp_f32 v3, v3\n\t" \
  "v_mfma_f32_16x16x32_bf16 a[4:7], a[136:139], v[128:131], a[4:7]\n\t" \
  "v_exp_f32 v4, v4\n\t" \
  "v_mfma_f32_16x16x32_bf16 a[4:7], a[140:143], v[132:135], a[4:7]\n\t" \
  "v_max3_f32 v203, v203, v108, v109\n\t" \
  "v_max3_f32 v192, v192, v110, v111\n\t" \
  "v_mfma_f32_16x16x32_bf16 a[8:11], a[144:147], v[128:131], a[8:11]\n\t" \
  "v_max_f32 v192, v192, v203\n\t" \
  "v_cmp_lt_f32 vcc, 0x42800000, v192\n\t" \
  "v_mfma_f32_16x16x32_bf16 a[8:11], a[148:151], v[132:135], a[8:11]\n\t" \
  "v_exp_f32 v5, v5\n\t" \
  "v_mfma_f32_16x16x32_bf16 a[12:15], a[152:155], v[128:131], a[12:15]\n\t" \
  "s_cbranch_vccnz .Lsf_c33\n\t" \
  ".Lsf_r33:\n\t" \
  "v_mfma_f32_16x16x32_bf16 a[12:15], a[156:159], v[132:135], a[12:15]\n\t" \
  "s_add_u32 m0, s38, 65536\n\t" \
  "v_exp_f32 v6, v6\n\t" \
  "v_mfma_f32_16x16x32_bf16 a[16:19], a[160:163], v[128:131], a[16:19]\n\t" \
  "s_add_u32 s96, s38, 81920\n\t" \
  "v_exp_f32 v7, v7\n\t" \
  "v_mfma_f32_16x16x32_bf16 a[16:19], a[164:167], v[132:135], a[16:19]\n\t" \
  "v_exp_f32 v8, v8\n\t" \
  "v_mfma_f32_16x16x32_bf16 a[20:23], a[168:171], v[128:131], a[20:23]\n\t" \
  "buffer_load_dwordx4 v224, s[60:63], s96 offen offset:0 lds\n\t" \
  "v_mfma_f32_16x16x32_bf16 a[20:23], a[172:175], v[132:135], a[20:23]\n\t" \
  "v_exp_f32 v9, v9\n\t" \
  "v_mfma_f32_16x16x32_bf16 a[24:27], a[176:179], v[128:131], a[24:27]\n\t" \
  "v_exp_f32 v10, v10\n\t" \
  "v_mfma_f32_16x16x32_bf16 a[24:27], a[180:183], v[132:135], a[24:27]\n\t" \
  "v_exp_f32 v11, v11\n\t" \
  "v_mfma_f32_16x16x32_bf16 a[28:31], a[184:187], v[128:131], a[28:31]\n\t" \
  "v_exp_f32 v12, v12\n\t" \
  "v_mfma_f32_16x16x32_bf16 a[28:31], a[188:191], v[132:135], a[28:31]\n\t" \
  "v_exp_f32 v13, v13\n\t" \
  "v_mfma_f32_16x16x32_bf16 v[160:163], v[216:219], v[128:131], v[160:163]\n\t" \
  "v_exp_f32 v14, v14\n\t" \
  "v_mfma_f32_16x16x32_bf16 v[160:163], v[216:219], v[132:135], v[160:163]\n\t" \
  "buffer_load_dwordx4 v225, s[60:63], s96 offen offset:1024 lds\n\t" \
  "v_mfma_f32_16x16x32_bf16 a[32:35], a[128:131], v[136:139], a[32:35]\n\t" \
  "ds_read_b128 a[192:195], v220 offset:0\n\t" \
  "v_cvt_pk_bf16_f32 v128, v0, v1\n\t" \
  "v_mfma_f32_16x16x32_bf16 a[32:35], a[132:135], v[140:143], a[32:35]\n\t" \
  "ds_read_b128 a[196:199], v221 offset:0\n\t" \
  "v_cvt_pk_bf16_f32 v129, v2, v3\n\t" \
  "v_mfma_f32_16x16x32_bf16 a[36:39], a[136:139], v[136:139], a[36:39]\n\t" \
  "v_exp_f32 v15, v15\n\t" \
  "v_mfma_f32_16x16x32_bf16 a[36:39], a[140:143], v[140:143], a[36:39]\n\t" \
  "v_exp_f32 v32, v32\n\t" \
  "v_mfma_f32_16x16x32_bf16 a[40:43], a[144:147], v[136:139], a[40:43]\n\t" \
  "ds_read_b128 a[200:203], v222 offset:0\n\t" \
  "v_cvt_pk_bf16_f32 v130, v4, v5\n\t" \
  "v_mfma_f32_16x16x32_bf16 a[40:43], a[148:151], v[140:143], a[40:43]\n\t" \
  "buffer_load_dwordx4 v226, s[60:63], s96 offen offset:2048 lds\n\t" \
  "v_mfma_f32_16x16x32_bf16 a[44:47], a[152:155], v[136:139], a[44:47]\n\t" \
  "ds_read_b128 a[204:207], v223 offset:0\n\t" \
  "v_cvt_pk_bf16_f32 v131, v6, v7\n\t" \
  "v_mfma_f32_16x16x32_bf16 a[44:47], a[156:159], v[140:143], a[44:47]\n\t" \
  "v_exp_f32 v33, v33\n\t" \
  "v_mfma_f32_16x16x32_bf16 a[48:51], a[160:163], v[136:139], a[48:51]\n\t" \
  "v_exp_f32 v34, v34\n\t" \
  "v_mfma_f32_16x16x32_bf16 a[48:51], a[164:167], v[140:143], a[48:51]\n\t" \
  "ds_read_b128 a[208:211], v220 offset:4096\n\t" \
  "v_cvt_pk_bf16_f32 v132, v8, v9\n\t" \
  "v_mfma_f32_16x16x32_bf16 a[52:55], a[168:171], v[136:139], a[52:55]\n\t" \
  "v_exp_f32 v35, v35\n\t" \
  "v_mfma_f32_16x16x32_bf16 a[52:55], a[172:175], v[140:143], a[52:55]\n\t" \
  "buffer_load_dwordx4 v227, s[60:63], s96 offen offset:3072 lds\n\t" \
  "v_mfma_f32_16x16x32_bf16 a[56:59], a[176:179], v[136:139], a[56:59]\n\t" \
  "ds_read_b128 a[212:215], v221 offset:4096\n\t" \
  "v_cvt_pk_bf16_f32 v133, v10, v11\n\t" \
  "v_mfma_f32_16x16x32_bf16 a[56:59], a[180:183], v[140:143], a[56:59]\n\t" \
  "s_add_u32 m0, s38, 81920\n\t" \
  "v_exp_f32 v36, v36\n\t" \
  "v_mfma_f32_16x16x32_bf16 a[60:63], a[184:187], v[136:139], a[60:63]\n\t" \
  "s_add_u32 s96, s38, 65536\n\t" \
  "v_exp_f32 v37, v37\n\t" \
  "v_mfma_f32_16x16x32_bf16 a[60:63], a[188:191], v[140:143], a[60:63]\n\t" \
  "ds_read_b128 a[216:219], v222 offset:4096\n\t" \
  "v_cvt_pk_bf16_f32 v134, v12, v13\n\t" \
  "v_mfma_f32_16x16x32_bf16 v[164:167], v[216:219], v[136:139], v[164:167]\n\t" \
  "v_exp_f32 v38, v38\n\t" \
  "v_mfma_f32_16x16x32_bf16 v[164:167], v[216:219], v[140:143], v[164:167]\n\t" \
  "buffer_load_dwordx4 v230, s[64:67], s96 offen offset:0 lds\n\t" \
  "v_mfma_f32_16x16x32_bf16 a[64:67], a[128:131], v[144:147], a[64:67]\n\t" \
  "ds_read_b128 a[220:223], v223 offset:4096\n\t" \
  "v_cvt_pk_bf16_f32 v135, v14, v15\n\t" \
  "v_mfma_f32_16x16x32_bf16 a[64:67], a[132:135], v[148:151], a[64:67]\n\t" \
  "v_exp_f32 v39, v39\n\t" \
  "v_mfma_f32_16x16x32_bf16 a[68:71], a[136:139], v[144:147], a[68:71]\n\t" \
  "v_exp_f32 v40, v40\n\t" \
  "v_mfma_f32_16x16x32_bf16 a[68:71], a[140:143], v[148:151], a[68:71]\n\t" \
  "ds_read_b128 a[224:227], v220 offset:8192\n\t" \
  "v_cvt_pk_bf16_f32 v136, v32, v33\n\t" \
  "v_mfma_f32_16x16x32_bf16 a[72:75], a[144:147], v[144:147], a[72:75]\n\t" \
  "v_exp_f32 v41, v41\n\t" \
  "v_mfma_f32_16x16x32_bf16 a[72:75], a[148:151], v[148:151], a[72:75]\n\t" \
  "ds_read_b128 a[228:231], v221 offset:8192\n\t" \
  "v_cvt_pk_bf16_f32 v137, v34, v35\n\t" \
  "v_mfma_f32_16x16x32_bf16 a[76:79], a[152:155], v[144:147], a[76:79]\n\t" \
  "buffer_load_dwordx4 v231, s[64:67], s96 offen offset:1024 lds\n\t" \
  "v_mfma_f32_16x16x32_bf16 a[76:79], a[156:159], v[148:151], a[76:79]\n\t" \
  "v_exp_f32 v42, v42\n\t" \
  "v_mfma_f32_16x16x32_bf16 a[80:83], a[160:163], v[144:147], a[80:83]\n\t" \
  "ds_read_b128 a[232:235], v222 offset:8192\n\t" \
  "v_cvt_pk_bf16_f32 v138, v36, v37\n\t" \
  "v_mfma_f32_16x16x32_bf16 a[80:83], a[164:167], v[148:151], a[80:83]\n\t" \
  "v_exp_f32 v43, v43\n\t" \
  "v_mfma_f32_16x16x32_bf16 a[84:87], a[168:171], v[144:147], a[84:87]\n\t" \
  "v_exp_f32 v44, v44\n\t" \
  "v_mfma_f32_16x16x32_bf16 a[84:87], a[172:175], v[148:151], a[84:87]\n\t" \
  "ds_read_b128 a[236:239], v223 offset:8192\n\t" \
  "v_cvt_pk_bf16_f32 v139, v38, v39\n\t" \
  "v_mfma_f32_16x16x32_bf16 a[88:91], a[176:179], v[144:147], a[88:91]\n\t" \
  "buffer_load_dwordx4 v230, s[64:67], s96 offen offset:2048 lds\n\t" \
  "v_mfma_f32_16x16x32_bf16 a[88:91], a[180:183], v[148:151], a[88:91]\n\t" \
  "v_exp_f32 v45, v45\n\t" \
  "v_mfma_f32_16x16x32_bf16 a[92:95], a[184:187], v[144:147], a[92:95]\n\t" \
  "ds_read_b128 a[240:243], v220 offset:12288\n\t" \
  "v_cvt_pk_bf16_f32 v140, v40, v41\n\t" \
  "v_mfma_f32_16x16x32_bf16 a[92:95], a[188:191], v[148:151], a[92:95]\n\t" \
  "v_exp_f32 v46, v46\n\t" \
  "v_mfma_f32_16x16x32_bf16 v[168:171], v[216:219], v[144:147], v[168:171]\n\t" \
  "v_exp_f32 v47, v47\n\t" \
  "v_mfma_f32_16x16x32_bf16 v[168:171], v[216:219], v[148:151], v[168:171]\n\t" \
  "ds_read_b128 a[244:247], v221 offset:12288\n\t" \
  "v_cvt_pk_bf16_f32 v141, v42, v43\n\t" \
  "v_mfma_f32_16x16x32_bf16 a[96:99], a[128:131], v[152:155], a[96:99]\n\t" \
  "buffer_load_dwordx4 v231, s[64:67], s96 offen offset:3072 lds\n\t" \
  "v_mfma_f32_16x16x32_bf16 a[96:99], a[132:135], v[156:159], a[96:99]\n\t" \
  "v_exp_f32 v64, v64\n\t" \
  "v_mfma_f32_16x16x32_bf16 a[100:103], a[136:139], v[152:155], a[100:103]\n\t" \
  "ds_read_b128 a[248:251], v222 offset:12288\n\t" \
  "v_cvt_pk_bf16_f32 v142, v44, v45\n\t" \
  "v_mfma_f32_16x16x32_bf16 a[100:103], a[140:143], v[156:159], a[100:103]\n\t" \
  "v_exp_f32 v65, v65\n\t" \
  "v_mfma_f32_16x16x32_bf16 a[104:107], a[144:147], v[152:155], a[104:107]\n\t" \
  "v_exp_f32 v66, v66\n\t" \
  "v_mfma_f32_16x16x32_bf16 a[104:107], a[148:151], v[156:159], a[104:107]\n\t" \
  "ds_read_b128 a[252:255], v223 offset:12288\n\t" \
  "v_cvt_pk_bf16_f32 v143, v46, v47\n\t" \
  "v_mfma_f32_16x16x32_bf16 a[108:111], a[152:155], v[152:155], a[108:111]\n\t" \
  "v_exp_f32 v67, v67\n\t" \
  "v_mfma_f32_16x16x32_bf16 a[108:111], a[156:159], v[156:159], a[108:111]\n\t" \
  "v_exp_f32 v68, v68\n\t" \
  "v_mfma_f32_16x16x32_bf16 a[112:115], a[160:163], v[152:155], a[112:115]\n\t" \
  "v_exp_f32 v69, v69\n\t" \
  "v_mfma_f32_16x16x32_bf16 a[112:115], a[164:167], v[156:159], a[112:115]\n\t" \
  "v_exp_f32 v70, v70\n\t" \
  "v_mfma_f32_16x16x32_bf16 a[116:119], a[168:171], v[152:155], a[116:119]\n\t" \
  "v_exp_f32 v71, v71\n\t" \
  "v_mfma_f32_16x16x32_bf16 a[116:119], a[172:175], v[156:159], a[116:119]\n\t" \
  "v_exp_f32 v72, v72\n\t" \
  "v_mfma_f32_16x16x32_bf16 a[120:123], a[176:179], v[152:155], a[120:123]\n\t" \
  "v_exp_f32 v73, v73\n\t" \
  "v_mfma_f32_16x16x32_bf16 a[120:123], a[180:183], v[156:159], a[120:123]\n\t" \
  "v_exp_f32 v74, v74\n\t" \
  "v_mfma_f32_16x16x32_bf16 a[124:127], a[184:187], v[152:155], a[124:127]\n\t" \
  "v_exp_f32 v75, v75\n\t" \
  "v_mfma_f32_16x16x32_bf16 a[124:127], a[188:191], v[156:159], a[124:127]\n\t" \
  "v_exp_f32 v76, v76\n\t" \
  "v_mfma_f32_16x16x32_bf16 v[172:175], v[216:219], v[152:155], v[172:175]\n\t" \
  "v_exp_f32 v77, v77\n\t" \
  "v_mfma_f32_16x16x32_bf16 v[172:175], v[216:219], v[156:159], v[172:175]\n\t" \
  "s_waitcnt lgkmcnt(12)\n\t" \
  "s_cmp_lg_u32 s99, 0\n\t" \
  "s_cbranch_scc1 .Lsf_p3\n\t" \
  ".Lsf_q3:\n\t" \
  "v_mfma_f32_16x16x32_bf16 v[0:3], a[192:195], v[16:19], v[176:179]\n\t" \
  "ds_read_b64_tr_b16 a[128:129], v210 offset:0\n\t" \
  "v_cvt_pk_bf16_f32 v144, v64, v65\n\t" \
  "v_mfma_f32_16x16x32_bf16 v[0:3], a[196:199], v[20:23], v[0:3]\n\t" \
  "v_exp_f32 v78, v78\n\t" \
  "v_mfma_f32_16x16x32_bf16 v[0:3], a[200:203], v[24:27], v[0:3]\n\t" \
  "ds_read_b64_tr_b16 a[130:131], v210 offset:4096\n\t" \
  "v_cvt_pk_bf16_f32 v145, v66, v67\n\t" \
  "v_mfma_f32_16x16x32_bf16 v[0:3], a[204:207], v[28:31], v[0:3]\n\t" \
  "s_waitcnt lgkmcnt(10)\n\t" \
  "v_exp_f32 v79, v79\n\t" \
  "v_mfma_f32_16x16x32_bf16 v[4:7], a[220:223], v[28:31], v[176:179]\n\t" \
  "ds_read_b64_tr_b16 a[132:133], v210 offset:8192\n\t" \
  "v_cvt_pk_bf16_f32 v146, v68, v69\n\t" \
  "v_mfma_f32_16x16x32_bf16 v[4:7], a[216:219], v[24:27], v[4:7]\n\t" \
  "v_exp_f32 v96, v96\n\t" \
  "v_mfma_f32_16x16x32_bf16 v[4:7], a[212:215], v[20:23], v[4:7]\n\t" \
  "ds_read_b64_tr_b16 a[134:135], v210 offset:12288\n\t" \
  "v_max3_f32 v192, v0, v1, v2\n\t" \
  "v_mfma_f32_16x16x32_bf16 v[4:7], a[208:211], v[16:19], v[4:7]\n\t" \
  "s_waitcnt lgkmcnt(8)\n\t" \
  "ds_read_b64_tr_b16 a[136:137], v211 offset:0\n\t" \
  "v_cvt_pk_bf16_f32 v147, v70, v71\n\t" \
  "v_mfma_f32_16x16x32_bf16 v[8:11], a[224:227], v[16:19], v[176:179]\n\t" \
  "v_exp_f32 v97, v97\n\t" \
  "v_mfma_f32_16x16x32_bf16 v[8:11], a[228:231], v[20:23], v[8:11]\n\t" \
  "ds_read_b64_tr_b16 a[138:139], v211 offset:4096\n\t" \
  "v_cvt_pk_bf16_f32 v148, v72, v73\n\t" \
  "v_mfma_f32_16x16x32_bf16 v[8:11], a[232:235], v[24:27], v[8:11]\n\t" \
  "v_max3_f32 v203, v3, v4, v5\n\t" \
  "v_max3_f32 v192, v192, v6, v7\n\t" \
  "v_mfma_f32_16x16x32_bf16 v[8:11], a[236:239], v[28:31], v[8:11]\n\t" \
  "s_waitcnt lgkmcnt(6)\n\t" \
  "ds_read_b64_tr_b16 a[140:141], v211 offset:8192\n\t" \
  "v_cvt_pk_bf16_f32 v149, v74, v75\n\t" \
  "v_mfma_f32_16x16x32_bf16 v[12:15], a[252:255], v[28:31], v[176:179]\n\t" \
  "v_exp_f32 v98, v98\n\t" \
  "v_mfma_f32_16x16x32_bf16 v[12:15], a[248:251], v[24:27], v[12:15]\n\t" \
  "ds_read_b64_tr_b16 a[142:143], v211 offset:12288\n\t" \
  "v_cvt_pk_bf16_f32 v150, v76, v77\n\t" \
  "v_mfma_f32_16x16x32_bf16 v[12:15], a[244:247], v[20:23], v[12:15]\n\t" \
  "v_max3_f32 v203, v203, v8, v9\n\t" \
  "v_max3_f32 v192, v192, v10, v11\n\t" \
  "v_mfma_f32_16x16x32_bf16 v[12:15], a[240:243], v[16:19], v[12:15]\n\t" \
  "ds_read_b64_tr_b16 a[144:145], v210 offset:256\n\t" \
  "v_cvt_pk_bf16_f32 v151, v78, v79\n\t" \
  "v_mfma_f32_16x16x32_bf16 v[32:35], a[192:195], v[48:51], v[180:183]\n\t" \
  "ds_read_b64_tr_b16 a[146:147], v210 offset:4352\n\t" \
  "v_cvt_pk_bf16_f32 v152, v96, v97\n\t" \
  "v_mfma_f32_16x16x32_bf16 v[32:35], a[196:199], v[52:55], v[32:35]\n\t" \
  "v_exp_f32 v99, v99\n\t" \
  "v_mfma_f32_16x16x32_bf16 v[32:35], a[200:203], v[56:59], v[32:35]\n\t" \
  "v_max3_f32 v203, v203, v12, v13\n\t" \
  "v_max3_f32 v192, v192, v14, v15\n\t" \
  "v_mfma_f32_16x16x32_bf16 v[32:35], a[204:207], v[60:63], v[32:35]\n\t" \
  "v_max_f32 v192, v192, v203\n\t" \
  "v_cmp_lt_f32 vcc, 0x42800000, v192\n\t" \
  "v_mfma_f32_16x16x32_bf16 v[36:39], a[220:223], v[60:63], v[180:183]\n\t" \
  "ds_read_b64_tr_b16 a[148:149], v210 offset:8448\n\t" \
  "v_cvt_pk_bf16_f32 v153, v98, v99\n\t" \
  "v_mfma_f32_16x16x32_bf16 v[36:39], a[216:219], v[56:59], v[36:39]\n\t" \
  "s_cbranch_vccnz .Lsf_c40\n\t" \
  ".Lsf_r40:\n\t" \
  "v_mfma_f32_16x16x32_bf16 v[36:39], a[212:215], v[52:55], v[36:39]\n\t" \
  "ds_read_b64_tr_b16 a[150:151], v210 offset:12544\n\t" \
  "v_max3_f32 v192, v32, v33, v34\n\t" \
  "v_mfma_f32_16x16x32_bf16 v[36:39], a[208:211], v[48:51], v[36:39]\n\t" \
  "v_exp_f32 v100, v100\n\t" \
  "v_mfma_f32_16x16x32_bf16 v[40:43], a[224:227], v[48:51], v[180:183]\n\t" \
  "v_exp_f32 v101, v101\n\t" \
  "v_mfma_f32_16x16x32_bf16 v[40:43], a[228:231], v[52:55], v[40:43]\n\t" \
  "ds_read_b64_tr_b16 a[152:153], v211 offset:256\n\t" \
  "v_cvt_pk_bf16_f32 v154, v100, v101\n\t" \
  "v_mfma_f32_16x16x32_bf16 v[40:43], a[232:235], v[56:59], v[40:43]\n\t" \
  "ds_read_b64_tr_b16 a[154:155], v211 offset:4352\n\t" \
  "v_max3_f32 v203, v35, v36, v37\n\t" \
  "v_mfma_f32_16x16x32_bf16 v[40:43], a[236:239], v[60:63], v[40:43]\n\t" \
  "ds_read_b64_tr_b16 a[156:157], v211 offset:8448\n\t" \
  "v_max3_f32 v192, v192, v38, v39\n\t" \
  "v_mfma_f32_16x16x32_bf16 v[44:47], a[252:255], v[60:63], v[180:183]\n\t" \
  "v_exp_f32 v102, v102\n\t" \
  "v_mfma_f32_16x16x32_bf16 v[44:47], a[248:251], v[56:59], v[44:47]\n\t" \
  "v_exp_f32 v103, v103\n\t" \
  "v_mfma_f32_16x16x32_bf16 v[44:47], a[244:247], v[52:55], v[44:47]\n\t" \
  "ds_read_b64_tr_b16 a[158:159], v211 offset:12544\n\t" \
  "v_max3_f32 v203, v203, v40, v41\n\t" \
  "v_mfma_f32_16x16x32_bf16 v[44:47], a[240:243], v[48:51], v[44:47]\n\t" \
  "ds_read_b64_tr_b16 a[160:161], v210 offset:512\n\t" \
  "v_max3_f32 v192, v192, v42, v43\n\t" \
  "v_mfma_f32_16x16x32_bf16 v[64:67], a[192:195], v[80:83], v[184:187]\n\t" \
  "ds_read_b64_tr_b16 a[162:163], v210 offset:4608\n\t" \
  "v_cvt_pk_bf16_f32 v155, v102, v103\n\t" \
  "v_mfma_f32_16x16x32_bf16 v[64:67], a[196:199], v[84:87], v[64:67]\n\t" \
  "v_exp_f32 v104, v104\n\t" \
  "v_mfma_f32_16x16x32_bf16 v[64:67], a[200:203], v[88:91], v[64:67]\n\t" \
  "v_max3_f32 v203, v203, v44, v45\n\t" \
  "v_max3_f32 v192, v192, v46, v47\n\t" \
  "v_mfma_f32_16x16x32_bf16 v[64:67], a[204:207], v[92:95], v[64:67]\n\t" \
  "v_max_f32 v192, v192, v203\n\t" \
  "v_cmp_lt_f32 vcc, 0x42800000, v192\n\t" \
  "v_mfma_f32_16x16x32_bf16 v[68:71], a[220:223], v[92:95], v[184:187]\n\t" \
  "v_exp_f32 v105, v105\n\t" \
  "v_mfma_f32_16x16x32_bf16 v[68:71], a[216:219], v[88:91], v[68:71]\n\t" \
  "s_cbranch_vccnz .Lsf_c41\n\t" \
  ".Lsf_r41:\n\t" \
  "v_mfma_f32_16x16x32_bf16 v[68:71], a[212:215], v[84:87], v[68:71]\n\t" \
  "ds_read_b64_tr_b16 a[164:165], v210 offset:8704\n\t" \
  "v_max3_f32 v192, v64, v65, v66\n\t" \
  "v_mfma_f32_16x16x32_bf16 v[68:71], a[208:211], v[80:83], v[68:71]\n\t" \
  "ds_read_b64_tr_b16 a[166:167], v210 offset:12800\n\t" \
  "v_cvt_pk_bf16_f32 v156, v104, v105\n\t" \
  "v_mfma_f32_16x16x32_bf16 v[72:75], a[224:227], v[80:83], v[184:187]\n\t" \
  "v_exp_f32 v106, v106\n\t" \
  "v_mfma_f32_16x16x32_bf16 v[72:75], a[228:231], v[84:87], v[72:75]\n\t" \
  "v_exp_f32 v107, v107\n\t" \
  "v_mfma_f32_16x16x32_bf16 v[72:75], a[232:235], v[88:91], v[72:75]\n\t" \
  "ds_read_b64_tr_b16 a[168:169], v211 offset:512\n\t" \
  "v_max3_f32 v203, v67, v68, v69\n\t" \
  "v_mfma_f32_16x16x32_bf16 v[72:75], a[236:239], v[92:95], v[72:75]\n\t" \
  "ds_read_b64_tr_b16 a[170:171], v211 offset:4608\n\t" \
  "v_max3_f32 v192, v192, v70, v71\n\t" \
  "v_mfma_f32_16x16x32_bf16 v[76:79], a[252:255], v[92:95], v[184:187]\n\t" \
  "ds_read_b64_tr_b16 a[172:173], v211 offset:8704\n\t" \
  "v_cvt_pk_bf16_f32 v157, v106, v107\n\t" \
  "v_mfma_f32_16x16x32_bf16 v[76:79], a[248:251], v[88:91], v[76:79]\n\t" \
  "v_exp_f32 v108, v108\n\t" \
  "v_mfma_f32_16x16x32_bf16 v[76:79], a[244:247], v[84:87], v[76:79]\n\t" \
  "v_max3_f32 v203, v203, v72, v73\n\t" \
  "v_max3_f32 v192, v192, v74, v75\n\t" \
  "v_mfma_f32_16x16x32_bf16 v[76:79], a[240:243], v[80:83], v[76:79]\n\t" \
  "v_exp_f32 v109, v109\n\t" \
  "v_mfma_f32_16x16x32_bf16 v[96:99], a[192:195], v[112:115], v[188:191]\n\t" \
  "ds_read_b64_tr_b16 a[174:175], v211 offset:12800\n\t" \
  "v_cvt_pk_bf16_f32 v158, v108, v109\n\t" \
  "v_mfma_f32_16x16x32_bf16 v[96:99], a[196:199], v[116:119], v[96:99]\n\t" \
  "v_exp_f32 v110, v110\n\t" \
  "v_mfma_f32_16x16x32_bf16 v[96:99], a[200:203], v[120:123], v[96:99]\n\t" \
  "ds_read_b64_tr_b16 a[176:177], v210 offset:768\n\t" \
  "v_max3_f32 v203, v203, v76, v77\n\t" \
  "v_mfma_f32_16x16x32_bf16 v[96:99], a[204:207], v[124:127], v[96:99]\n\t" \
  "v_max3_f32 v192, v192, v78, v79\n\t" \
  "v_max_f32 v192, v192, v203\n\t" \
  "v_mfma_f32_16x16x32_bf16 v[100:103], a[220:223], v[124:127], v[188:191]\n\t" \
  "ds_read_b64_tr_b16 a[178:179], v210 offset:4864\n\t" \
  "v_cmp_lt_f32 vcc, 0x42800000, v192\n\t" \
  "v_mfma_f32_16x16x32_bf16 v[100:103], a[216:219], v[120:123], v[100:103]\n\t" \
  "ds_read_b64_tr_b16 a[180:181], v210 offset:8960\n\t" \
  "v_exp_f32 v111, v111\n\t" \
  "v_mfma_f32_16x16x32_bf16 v[100:103], a[212:215], v[116:119], v[100:103]\n\t" \
  "s_cbranch_vccnz .Lsf_c42\n\t" \
  ".Lsf_r42:\n\t" \
  "v_mfma_f32_16x16x32_bf16 v[100:103], a[208:211], v[112:115], v[100:103]\n\t" \
  "ds_read_b64_tr_b16 a[182:183], v210 offset:13056\n\t" \
  "v_max3_f32 v192, v96, v97, v98\n\t" \
  "v_mfma_f32_16x16x32_bf16 v[104:107], a[224:227], v[112:115], v[188:191]\n\t" \
  "ds_read_b64_tr_b16 a[184:185], v211 offset:768\n\t" \
  "v_cvt_pk_bf16_f32 v159, v110, v111\n\t" \
  "v_mfma_f32_16x16x32_bf16 v[104:107], a[228:231], v[116:119], v[104:107]\n\t" \
  "ds_read_b64_tr_b16 a[186:187], v211 offset:4864\n\t" \
  "v_mfma_f32_16x16x32_bf16 v[104:107], a[232:235], v[120:123], v[104:107]\n\t" \
  "ds_read_b64_tr_b16 a[188:189], v211 offset:8960\n\t" \
  "v_max3_f32 v203, v99, v100, v101\n\t" \
  "v_mfma_f32_16x16x32_bf16 v[104:107], a[236:239], v[124:127], v[104:107]\n\t" \
  "ds_read_b64_tr_b16 a[190:191], v211 offset:13056\n\t" \
  "v_max3_f32 v192, v192, v102, v103\n\t" \
  "v_mfma_f32_16x16x32_bf16 v[108:111], a[252:255], v[124:127], v[188:191]\n\t" \
  "v_exp_f32 v0, v0\n\t" \
  "v_mfma_f32_16x16x32_bf16 v[108:111], a[248:251], v[120:123], v[108:111]\n\t" \
  "v_exp_f32 v1, v1\n\t" \
  "v_mfma_f32_16x16x32_bf16 v[108:111], a[244:247], v[116:119], v[108:111]\n\t" \
  "s_waitcnt vmcnt(0) lgkmcnt(0)\n\t" \
  "v_max3_f32 v203, v203, v104, v105\n\t" \
  "v_max3_f32 v192, v192, v106, v107\n\t" \
  "v_mfma_f32_16x16x32_bf16 v[108:111], a[240:243], v[112:115], v[108:111]\n\t" \
  "s_barrier\n\t" \
  "v_mfma_f32_16x16x32_bf16 a[0:3], a[128:131], v[128:131], a[0:3]\n\t" \
  "v_exp_f32 v2, v2\n\t" \
  "v_mfma_f32_16x16x32_bf16 a[0:3], a[132:135], v[132:135], a[0:3]\n\t" \
  "v_exp_f32 v3, v3\n\t" \
  "v_mfma_f32_16x16x32_bf16 a[4:7], a[136:139], v[128:131], a[4:7]\n\t" \
  "v_exp_f32 v4, v4\n\t" \
  "v_mfma_f32_16x16x32_bf16 a[4:7], a[140:143], v[132:135], a[4:7]\n\t" \
  "v_max3_f32 v203, v203, v108, v109\n\t" \
  "v_max3_f32 v192, v192, v110, v111\n\t" \
  "v_mfma_f32_16x16x32_bf16 a[8:11], a[144:147], v[128:131], a[8:11]\n\t" \
  "v_max_f32 v192, v192, v203\n\t" \
  "v_cmp_lt_f32 vcc, 0x42800000, v192\n\t" \
  "v_mfma_f32_16x16x32_bf16 a[8:11], a[148:151], v[132:135], a[8:11]\n\t" \
  "v_exp_f32 v5, v5\n\t" \
  "v_mfma_f32_16x16x32_bf16 a[12:15], a[152:155], v[128:131], a[12:15]\n\t" \
  "s_cbranch_vccnz .Lsf_c43\n\t" \
  ".Lsf_r43:\n\t" \
  "v_mfma_f32_16x16x32_bf16 a[12:15], a[156:159], v[132:135], a[12:15]\n\t" \
  "s_add_u32 m0, s38, 49152\n\t" \
  "v_exp_f32 v6, v6\n\t" \
  "v_mfma_f32_16x16x32_bf16 a[16:19], a[160:163], v[128:131], a[16:19]\n\t" \
  "s_add_u32 s96, s38, 98304\n\t" \
  "v_exp_f32 v7, v7\n\t" \
  "v_mfma_f32_16x16x32_bf16 a[16:19], a[164:167], v[132:135], a[16:19]\n\t" \
  "v_exp_f32 v8, v8\n\t" \
  "v_mfma_f32_16x16x32_bf16 a[20:23], a[168:171], v[128:131], a[20:23]\n\t" \
  "buffer_load_dwordx4 v224, s[60:63], s96 offen offset:0 lds\n\t" \
  "v_mfma_f32_16x16x32_bf16 a[20:23], a[172:175], v[132:135], a[20:23]\n\t" \
  "v_exp_f32 v9, v9\n\t" \
  "v_mfma_f32_16x16x32_bf16 a[24:27], a[176:179], v[128:131], a[24:27]\n\t" \
  "v_exp_f32 v10, v10\n\t" \
  "v_mfma_f32_16x16x32_bf16 a[24:27], a[180:183], v[132:135], a[24:27]\n\t" \
  "v_exp_f32 v11, v11\n\t" \
  "v_mfma_f32_16x16x32_bf16 a[28:31], a[184:187], v[128:131], a[28:31]\n\t" \
  "v_exp_f32 v12, v12\n\t" \
  "v_mfma_f32_16x16x32_bf16 a[28:31], a[188:191], v[132:135], a[28:31]\n\t" \
  "v_exp_f32 v13, v13\n\t" \
  "v_mfma_f32_16x16x32_bf16 v[160:163], v[216:219], v[128:131], v[160:163]\n\t" \
  "v_exp_f32 v14, v14\n\t" \
  "v_mfma_f32_16x16x32_bf16 v[160:163], v[216:219], v[132:135], v[160:163]\n\t" \
  "buffer_load_dwordx4 v225, s[60:63], s96 offen offset:1024 lds\n\t" \
  "v_mfma_f32_16x16x32_bf16 a[32:35], a[128:131], v[136:139], a[32:35]\n\t" \
  "ds_read_b128 a[192:195], v220 offset:16384\n\t" \
  "v_cvt_pk_bf16_f32 v128, v0, v1\n\t" \
  "v_mfma_f32_16x16x32_bf16 a[32:35], a[132:135], v[140:143], a[32:35]\n\t" \
  "ds_read_b128 a[196:199], v221 offset:16384\n\t" \
  "v_cvt_pk_bf16_f32 v129, v2, v3\n\t" \
  "v_mfma_f32_16x16x32_bf16 a[36:39], a[136:139], v[136:139], a[36:39]\n\t" \
  "v_exp_f32 v15, v15\n\t" \
  "v_mfma_f32_16x16x32_bf16 a[36:39], a[140:143], v[140:143], a[36:39]\n\t" \
  "v_exp_f32 v32, v32\n\t" \
  "v_mfma_f32_16x16x32_bf16 a[40:43], a[144:147], v[136:139], a[40:43]\n\t" \
  "ds_read_b128 a[200:203], v222 offset:16384\n\t" \
  "v_cvt_pk_bf16_f32 v130, v4, v5\n\t" \
  "v_mfma_f32_16x16x32_bf16 a[40:43], a[148:151], v[140:143], a[40:43]\n\t" \
  "buffer_load_dwordx4 v226, s[60:63], s96 offen offset:2048 lds\n\t" \
  "v_mfma_f32_16x16x32_bf16 a[44:47], a[152:155], v[136:139], a[44:47]\n\t" \
  "ds_read_b128 a[204:207], v223 offset:16384\n\t" \
  "v_cvt_pk_bf16_f32 v131, v6, v7\n\t" \
  "v_mfma_f32_16x16x32_bf16 a[44:47], a[156:159], v[140:143], a[44:47]\n\t" \
  "v_exp_f32 v33, v33\n\t" \
  "v_mfma_f32_16x16x32_bf16 a[48:51], a[160:163], v[136:139], a[48:51]\n\t" \
  "v_exp_f32 v34, v34\n\t" \
  "v_mfma_f32_16x16x32_bf16 a[48:51], a[164:167], v[140:143], a[48:51]\n\t" \
  "ds_read_b128 a[208:211], v220 offset:20480\n\t" \
  "v_cvt_pk_bf16_f32 v132, v8, v9\n\t" \
  "v_mfma_f32_16x16x32_bf16 a[52:55], a[168:171], v[136:139], a[52:55]\n\t" \
  "v_exp_f32 v35, v35\n\t" \
  "v_mfma_f32_16x16x32_bf16 a[52:55], a[172:175], v[140:143], a[52:55]\n\t" \
  "buffer_load_dwordx4 v227, s[60:63], s96 offen offset:3072 lds\n\t" \
  "v_mfma_f32_16x16x32_bf16 a[56:59], a[176:179], v[136:139], a[56:59]\n\t" \
  "ds_read_b128 a[212:215], v221 offset:20480\n\t" \
  "v_cvt_pk_bf16_f32 v133, v10, v11\n\t" \
  "v_mfma_f32_16x16x32_bf16 a[56:59], a[180:183], v[140:143], a[56:59]\n\t" \
  "s_add_u32 m0, s38, 147456\n\t" \
  "v_exp_f32 v36, v36\n\t" \
  "v_mfma_f32_16x16x32_bf16 a[60:63], a[184:187], v[136:139], a[60:63]\n\t" \
  "s_add_u32 s96, s38, 81920\n\t" \
  "v_exp_f32 v37, v37\n\t" \
  "v_mfma_f32_16x16x32_bf16 a[60:63], a[188:191], v[140:143], a[60:63]\n\t" \
  "ds_read_b128 a[216:219], v222 offset:20480\n\t" \
  "v_cvt_pk_bf16_f32 v134, v12, v13\n\t" \
  "v_mfma_f32_16x16x32_bf16 v[164:167], v[216:219], v[136:139], v[164:167]\n\t" \
  "v_exp_f32 v38, v38\n\t" \
  "v_mfma_f32_16x16x32_bf16 v[164:167], v[216:219], v[140:143], v[164:167]\n\t" \
  "buffer_load_dwordx4 v230, s[64:67], s96 offen offset:0 lds\n\t" \
  "v_mfma_f32_16x16x32_bf16 a[64:67], a[128:131], v[144:147], a[64:67]\n\t" \
  "ds_read_b128 a[220:223], v223 offset:20480\n\t" \
  "v_cvt_pk_bf16_f32 v135, v14, v15\n\t" \
  "v_mfma_f32_16x16x32_bf16 a[64:67], a[132:135], v[148:151], a[64:67]\n\t" \
  "v_exp_f32 v39, v39\n\t" \
  "v_mfma_f32_16x16x32_bf16 a[68:71], a[136:139], v[144:147], a[68:71]\n\t" \
  "v_exp_f32 v40, v40\n\t" \
  "v_mfma_f32_16x16x32_bf16 a[68:71], a[140:143], v[148:151], a[68:71]\n\t" \
  "ds_read_b128 a[224:227], v220 offset:24576\n\t" \
  "v_cvt_pk_bf16_f32 v136, v32, v33\n\t" \
  "v_mfma_f32_16x16x32_bf16 a[72:75], a[144:147], v[144:147], a[72:75]\n\t" \
  "v_exp_f32 v41, v41\n\t" \
  "v_mfma_f32_16x16x32_bf16 a[72:75], a[148:151], v[148:151], a[72:75]\n\t" \
  "ds_read_b128 a[228:231], v221 offset:24576\n\t" \
  "v_cvt_pk_bf16_f32 v137, v34, v35\n\t" \
  "v_mfma_f32_16x16x32_bf16 a[76:79], a[152:155], v[144:147], a[76:79]\n\t" \
  "buffer_load_dwordx4 v231, s[64:67], s96 offen offset:1024 lds\n\t" \
  "v_mfma_f32_16x16x32_bf16 a[76:79], a[156:159], v[148:151], a[76:79]\n\t" \
  "v_exp_f32 v42, v42\n\t" \
  "v_mfma_f32_16x16x32_bf16 a[80:83], a[160:163], v[144:147], a[80:83]\n\t" \
  "ds_read_b128 a[232:235], v222 offset:24576\n\t" \
  "v_cvt_pk_bf16_f32 v138, v36, v37\n\t" \
  "v_mfma_f32_16x16x32_bf16 a[80:83], a[164:167], v[148:151], a[80:83]\n\t" \
  "v_exp_f32 v43, v43\n\t" \
  "v_mfma_f32_16x16x32_bf16 a[84:87], a[168:171], v[144:147], a[84:87]\n\t" \
  "v_exp_f32 v44, v44\n\t" \
  "v_mfma_f32_16x16x32_bf16 a[84:87], a[172:175], v[148:151], a[84:87]\n\t" \
  "ds_read_b128 a[236:239], v223 offset:24576\n\t" \
  "v_cvt_pk_bf16_f32 v139, v38, v39\n\t" \
  "v_mfma_f32_16x16x32_bf16 a[88:91], a[176:179], v[144:147], a[88:91]\n\t" \
  "buffer_load_dwordx4 v230, s[64:67], s96 offen offset:2048 lds\n\t" \
  "v_mfma_f32_16x16x32_bf16 a[88:91], a[180:183], v[148:151], a[88:91]\n\t" \
  "v_exp_f32 v45, v45\n\t" \
  "v_mfma_f32_16x16x32_bf16 a[92:95], a[184:187], v[144:147], a[92:95]\n\t" \
  "ds_read_b128 a[240:243], v220 offset:28672\n\t" \
  "v_cvt_pk_bf16_f32 v140, v40, v41\n\t" \
  "v_mfma_f32_16x16x32_bf16 a[92:95], a[188:191], v[148:151], a[92:95]\n\t" \
  "v_exp_f32 v46, v46\n\t" \
  "v_mfma_f32_16x16x32_bf16 v[168:171], v[216:219], v[144:147], v[168:171]\n\t" \
  "v_exp_f32 v47, v47\n\t" \
  "v_mfma_f32_16x16x32_bf16 v[168:171], v[216:219], v[148:151], v[168:171]\n\t" \
  "ds_read_b128 a[244:247], v221 offset:28672\n\t" \
  "v_cvt_pk_bf16_f32 v141, v42, v43\n\t" \
  "v_mfma_f32_16x16x32_bf16 a[96:99], a[128:131], v[152:155], a[96:99]\n\t" \
  "buffer_load_dwordx4 v231, s[64:67], s96 offen offset:3072 lds\n\t" \
  "v_mfma_f32_16x16x32_bf16 a[96:99], a[132:135], v[156:159], a[96:99]\n\t" \
  "v_exp_f32 v64, v64\n\t" \
  "v_mfma_f32_16x16x32_bf16 a[100:103], a[136:139], v[152:155], a[100:103]\n\t" \
  "ds_read_b128 a[248:251], v222 offset:28672\n\t" \
  "v_cvt_pk_bf16_f32 v142, v44, v45\n\t" \
  "v_mfma_f32_16x16x32_bf16 a[100:103], a[140:143], v[156:159], a[100:103]\n\t" \
  "v_exp_f32 v65, v65\n\t" \
  "v_mfma_f32_16x16x32_bf16 a[104:107], a[144:147], v[152:155], a[104:107]\n\t" \
  "v_exp_f32 v66, v66\n\t" \
  "v_mfma_f32_16x16x32_bf16 a[104:107], a[148:151], v[156:159], a[104:107]\n\t" \
  "ds_read_b128 a[252:255], v223 offset:28672\n\t" \
  "v_cvt_pk_bf16_f32 v143, v46, v47\n\t" \
  "v_mfma_f32_16x16x32_bf16 a[108:111], a[152:155], v[152:155], a[108:111]\n\t" \
  "v_exp_f32 v67, v67\n\t" \
  "v_mfma_f32_16x16x32_bf16 a[108:111], a[156:159], v[156:159], a[108:111]\n\t" \
  "v_exp_f32 v68, v68\n\t" \
  "v_mfma_f32_16x16x32_bf16 a[112:115], a[160:163], v[152:155], a[112:115]\n\t" \
  "v_exp_f32 v69, v69\n\t" \
  "v_mfma_f32_16x16x32_bf16 a[112:115], a[164:167], v[156:159], a[112:115]\n\t" \
  "v_exp_f32 v70, v70\n\t" \
  "v_mfma_f32_16x16x32_bf16 a[116:119], a[168:171], v[152:155], a[116:119]\n\t" \
  "v_exp_f32 v71, v71\n\t" \
  "v_mfma_f32_16x16x32_bf16 a[116:119], a[172:175], v[156:159], a[116:119]\n\t" \
  "v_exp_f32 v72, v72\n\t" \
  "v_mfma_f32_16x16x32_bf16 a[120:123], a[176:179], v[152:155], a[120:123]\n\t" \
  "v_exp_f32 v73, v73\n\t" \
  "v_mfma_f32_16x16x32_bf16 a[120:123], a[180:183], v[156:159], a[120:123]\n\t" \
  "v_exp_f32 v74, v74\n\t" \
  "v_mfma_f32_16x16x32_bf16 a[124:127], a[184:187], v[152:155], a[124:127]\n\t" \
  "v_exp_f32 v75, v75\n\t" \
  "v_mfma_f32_16x16x32_bf16 a[124:127], a[188:191], v[156:159], a[124:127]\n\t" \
  "v_exp_f32 v76, v76\n\t" \
  "v_mfma_f32_16x16x32_bf16 v[172:175], v[216:219], v[152:155], v[172:175]\n\t" \
  "v_exp_f32 v77, v77\n\t" \
  "v_mfma_f32_16x16x32_bf16 v[172:175], v[216:219], v[156:159], v[172:175]\n\t" \
  "s_waitcnt lgkmcnt(12)\n\t" \
  "s_cmp_lg_u32 s99, 0\n\t" \
  "s_cbranch_scc1 .Lsf_p4\n\t" \
  ".Lsf_q4:\n\t" \
  "v_mfma_f32_16x16x32_bf16 v[0:3], a[192:195], v[16:19], v[176:179]\n\t" \
  "ds_read_b64_tr_b16 a[128:129], v228 offset:0\n\t" \
  "v_cvt_pk_bf16_f32 v144, v64, v65\n\t" \
  "v_mfma_f32_16x16x32_bf16 v[0:3], a[196:199], v[20:23], v[0:3]\n\t" \
  "v_exp_f32 v78, v78\n\t" \
  "v_mfma_f32_16x16x32_bf16 v[0:3], a[200:203], v[24:27], v[0:3]\n\t" \
  "ds_read_b64_tr_b16 a[130:131], v228 offset:4096\n\t" \
  "v_cvt_pk_bf16_f32 v145, v66, v67\n\t" \
  "v_mfma_f32_16x16x32_bf16 v[0:3], a[204:207], v[28:31], v[0:3]\n\t" \
  "s_waitcnt lgkmcnt(10)\n\t" \
  "v_exp_f32 v79, v79\n\t" \
  "v_mfma_f32_16x16x32_bf16 v[4:7], a[220:223], v[28:31], v[176:179]\n\t" \
  "ds_read_b64_tr_b16 a[132:133], v228 offset:8192\n\t" \
  "v_cvt_pk_bf16_f32 v146, v68, v69\n\t" \
  "v_mfma_f32_16x16x32_bf16 v[4:7], a[216:219], v[24:27], v[4:7]\n\t" \
  "v_exp_f32 v96, v96\n\t" \
  "v_mfma_f32_16x16x32_bf16 v[4:7], a[212:215], v[20:23], v[4:7]\n\t" \
  "ds_read_b64_tr_b16 a[134:135], v228 offset:12288\n\t" \
  "v_max3_f32 v192, v0, v1, v2\n\t" \
  "v_mfma_f32_16x16x32_bf16 v[4:7], a[208:211], v[16:19], v[4:7]\n\t" \
  "s_waitcnt lgkmcnt(8)\n\t" \
  "ds_read_b64_tr_b16 a[136:137], v229 offset:0\n\t" \
  "v_cvt_pk_bf16_f32 v147, v70, v71\n\t" \
  "v_mfma_f32_16x16x32_bf16 v[8:11], a[224:227], v[16:19], v[176:179]\n\t" \
  "v_exp_f32 v97, v97\n\t" \
  "v_mfma_f32_16x16x32_bf16 v[8:11], a[228:231], v[20:23], v[8:11]\n\t" \
  "ds_read_b64_tr_b16 a[138:139], v229 offset:4096\n\t" \
  "v_cvt_pk_bf16_f32 v148, v72, v73\n\t" \
  "v_mfma_f32_16x16x32_bf16 v[8:11], a[232:235], v[24:27], v[8:11]\n\t" \
  "v_max3_f32 v203, v3, v4, v5\n\t" \
  "v_max3_f32 v192, v192, v6, v7\n\t" \
  "v_mfma_f32_16x16x32_bf16 v[8:11], a[236:239], v[28:31], v[8:11]\n\t" \
  "s_waitcnt lgkmcnt(6)\n\t" \
  "ds_read_b64_tr_b16 a[140:141], v229 offset:8192\n\t" \
  "v_cvt_pk_bf16_f32 v149, v74, v75\n\t" \
  "v_mfma_f32_16x16x32_bf16 v[12:15], a[252:255], v[28:31], v[176:179]\n\t" \
  "v_exp_f32 v98, v98\n\t" \
  "v_mfma_f32_16x16x32_bf16 v[12:15], a[248:251], v[24:27], v[12:15]\n\t" \
  "ds_read_b64_tr_b16 a[142:143], v229 offset:12288\n\t" \
  "v_cvt_pk_bf16_f32 v150, v76, v77\n\t" \
  "v_mfma_f32_16x16x32_bf16 v[12:15], a[244:247], v[20:23], v[12:15]\n\t" \
  "v_max3_f32 v203, v203, v8, v9\n\t" \
  "v_max3_f32 v192, v192, v10, v11\n\t" \
  "v_mfma_f32_16x16x32_bf16 v[12:15], a[240:243], v[16:19], v[12:15]\n\t" \
  "ds_read_b64_tr_b16 a[144:145], v228 offset:256\n\t" \
  "v_cvt_pk_bf16_f32 v151, v78, v79\n\t" \
  "v_mfma_f32_16x16x32_bf16 v[32:35], a[192:195], v[48:51], v[180:183]\n\t" \
  "ds_read_b64_tr_b16 a[146:147], v228 offset:4352\n\t" \
  "v_cvt_pk_bf16_f32 v152, v96, v97\n\t" \
  "v_mfma_f32_16x16x32_bf16 v[32:35], a[196:199], v[52:55], v[32:35]\n\t" \
  "v_exp_f32 v99, v99\n\t" \
  "v_mfma_f32_16x16x32_bf16 v[32:35], a[200:203], v[56:59], v[32:35]\n\t" \
  "v_max3_f32 v203, v203, v12, v13\n\t" \
  "v_max3_f32 v192, v192, v14, v15\n\t" \
  "v_mfma_f32_16x16x32_bf16 v[32:35], a[204:207], v[60:63], v[32:35]\n\t" \
  "v_max_f32 v192, v192, v203\n\t" \
  "v_cmp_lt_f32 vcc, 0x42800000, v192\n\t" \
  "v_mfma_f32_16x16x32_bf16 v[36:39], a[220:223], v[60:63], v[180:183]\n\t" \
  "ds_read_b64_tr_b16 a[148:149], v228 offset:8448\n\t" \
  "v_cvt_pk_bf16_f32 v153, v98, v99\n\t" \
  "v_mfma_f32_16x16x32_bf16 v[36:39], a[216:219], v[56:59], v[36:39]\n\t" \
  "s_cbranch_vccnz .Lsf_c50\n\t" \
  ".Lsf_r50:\n\t" \
  "v_mfma_f32_16x16x32_bf16 v[36:39], a[212:215], v[52:55], v[36:39]\n\t" \
  "ds_read_b64_tr_b16 a[150:151], v228 offset:12544\n\t" \
  "v_max3_f32 v192, v32, v33, v34\n\t" \
  "v_mfma_f32_16x16x32_bf16 v[36:39], a[208:211], v[48:51], v[36:39]\n\t" \
  "v_exp_f32 v100, v100\n\t" \
  "v_mfma_f32_16x16x32_bf16 v[40:43], a[224:227], v[48:51], v[180:183]\n\t" \
  "v_exp_f32 v101, v101\n\t" \
  "v_mfma_f32_16x16x32_bf16 v[40:43], a[228:231], v[52:55], v[40:43]\n\t" \
  "ds_read_b64_tr_b16 a[152:153], v229 offset:256\n\t" \
  "v_cvt_pk_bf16_f32 v154, v100, v101\n\t" \
  "v_mfma_f32_16x16x32_bf16 v[40:43], a[232:235], v[56:59], v[40:43]\n\t" \
  "ds_read_b64_tr_b16 a[154:155], v229 offset:4352\n\t" \
  "v_max3_f32 v203, v35, v36, v37\n\t" \
  "v_mfma_f32_16x16x32_bf16 v[40:43], a[236:239], v[60:63], v[40:43]\n\t" \
  "ds_read_b64_tr_b16 a[156:157], v229 offset:8448\n\t" \
  "v_max3_f32 v192, v192, v38, v39\n\t" \
  "v_mfma_f32_16x16x32_bf16 v[44:47], a[252:255], v[60:63], v[180:183]\n\t" \
  "v_exp_f32 v102, v102\n\t" \
  "v_mfma_f32_16x16x32_bf16 v[44:47], a[248:251], v[56:59], v[44:47]\n\t" \
  "v_exp_f32 v103, v103\n\t" \
  "v_mfma_f32_16x16x32_bf16 v[44:47], a[244:247], v[52:55], v[44:47]\n\t" \
  "ds_read_b64_tr_b16 a[158:159], v229 offset:12544\n\t" \
  "v_max3_f32 v203, v203, v40, v41\n\t" \
  "v_mfma_f32_16x16x32_bf16 v[44:47], a[240:243], v[48:51], v[44:47]\n\t" \
  "ds_read_b64_tr_b16 a[160:161], v228 offset:512\n\t" \
  "v_max3_f32 v192, v192, v42, v43\n\t" \
  "v_mfma_f32_16x16x32_bf16 v[64:67], a[192:195], v[80:83], v[184:187]\n\t" \
  "ds_read_b64_tr_b16 a[162:163], v228 offset:4608\n\t" \
  "v_cvt_pk_bf16_f32 v155, v102, v103\n\t" \
  "v_mfma_f32_16x16x32_bf16 v[64:67], a[196:199], v[84:87], v[64:67]\n\t" \
  "v_exp_f32 v104, v104\n\t" \
  "v_mfma_f32_16x16x32_bf16 v[64:67], a[200:203], v[88:91], v[64:67]\n\t" \
  "v_max3_f32 v203, v203, v44, v45\n\t" \
  "v_max3_f32 v192, v192, v46, v47\n\t" \
  "v_mfma_f32_16x16x32_bf16 v[64:67], a[204:207], v[92:95], v[64:67]\n\t" \
  "v_max_f32 v192, v192, v203\n\t" \
  "v_cmp_lt_f32 vcc, 0x42800000, v192\n\t" \
  "v_mfma_f32_16x16x32_bf16 v[68:71], a[220:223], v[92:95], v[184:187]\n\t" \
  "v_exp_f32 v105, v105\n\t" \
  "v_mfma_f32_16x16x32_bf16 v[68:71], a[216:219], v[88:91], v[68:71]\n\t" \
  "s_cbranch_vccnz .Lsf_c51\n\t" \
  ".Lsf_r51:\n\t" \
  "v_mfma_f32_16x16x32_bf16 v[68:71], a[212:215], v[84:87], v[68:71]\n\t" \
  "ds_read_b64_tr_b16 a[164:165], v228 offset:8704\n\t" \
  "v_max3_f32 v192, v64, v65, v66\n\t" \
  "v_mfma_f32_16x16x32_bf16 v[68:71], a[208:211], v[80:83], v[68:71]\n\t" \
  "ds_read_b64_tr_b16 a[166:167], v228 offset:12800\n\t" \
  "v_cvt_pk_bf16_f32 v156, v104, v105\n\t" \
  "v_mfma_f32_16x16x32_bf16 v[72:75], a[224:227], v[80:83], v[184:187]\n\t" \
  "v_exp_f32 v106, v106\n\t" \
  "v_mfma_f32_16x16x32_bf16 v[72:75], a[228:231], v[84:87], v[72:75]\n\t" \
  "v_exp_f32 v107, v107\n\t" \
  "v_mfma_f32_16x16x32_bf16 v[72:75], a[232:235], v[88:91], v[72:75]\n\t" \
  "ds_read_b64_tr_b16 a[168:169], v229 offset:512\n\t" \
  "v_max3_f32 v203, v67, v68, v69\n\t" \
  "v_mfma_f32_16x16x32_bf16 v[72:75], a[236:239], v[92:95], v[72:75]\n\t" \
  "ds_read_b64_tr_b16 a[170:171], v229 offset:4608\n\t" \
  "v_max3_f32 v192, v192, v70, v71\n\t" \
  "v_mfma_f32_16x16x32_bf16 v[76:79], a[252:255], v[92:95], v[184:187]\n\t" \
  "ds_read_b64_tr_b16 a[172:173], v229 offset:8704\n\t" \
  "v_cvt_pk_bf16_f32 v157, v106, v107\n\t" \
  "v_mfma_f32_16x16x32_bf16 v[76:79], a[248:251], v[88:91], v[76:79]\n\t" \
  "v_exp_f32 v108, v108\n\t" \
  "v_mfma_f32_16x16x32_bf16 v[76:79], a[244:247], v[84:87], v[76:79]\n\t" \
  "v_max3_f32 v203, v203, v72, v73\n\t" \
  "v_max3_f32 v192, v192, v74, v75\n\t" \
  "v_mfma_f32_16x16x32_bf16 v[76:79], a[240:243], v[80:83], v[76:79]\n\t" \
  "v_exp_f32 v109, v109\n\t" \
  "v_mfma_f32_16x16x32_bf16 v[96:99], a[192:195], v[112:115], v[188:191]\n\t" \
  "ds_read_b64_tr_b16 a[174:175], v229 offset:12800\n\t" \
  "v_cvt_pk_bf16_f32 v158, v108, v109\n\t" \
  "v_mfma_f32_16x16x32_bf16 v[96:99], a[196:199], v[116:119], v[96:99]\n\t" \
  "v_exp_f32 v110, v110\n\t" \
  "v_mfma_f32_16x16x32_bf16 v[96:99], a[200:203], v[120:123], v[96:99]\n\t" \
  "ds_read_b64_tr_b16 a[176:177], v228 offset:768\n\t" \
  "v_max3_f32 v203, v203, v76, v77\n\t" \
  "v_mfma_f32_16x16x32_bf16 v[96:99], a[204:207], v[124:127], v[96:99]\n\t" \
  "v_max3_f32 v192, v192, v78, v79\n\t" \
  "v_max_f32 v192, v192, v203\n\t" \
  "v_mfma_f32_16x16x32_bf16 v[100:103], a[220:223], v[124:127], v[188:191]\n\t" \
  "ds_read_b64_tr_b16 a[178:179], v228 offset:4864\n\t" \
  "v_cmp_lt_f32 vcc, 0x42800000, v192\n\t" \
  "v_mfma_f32_16x16x32_bf16 v[100:103], a[216:219], v[120:123], v[100:103]\n\t" \
  "ds_read_b64_tr_b16 a[180:181], v228 offset:8960\n\t" \
  "v_exp_f32 v111, v111\n\t" \
  "v_mfma_f32_16x16x32_bf16 v[100:103], a[212:215], v[116:119], v[100:103]\n\t" \
  "s_cbranch_vccnz .Lsf_c52\n\t" \
  ".Lsf_r52:\n\t" \
  "v_mfma_f32_16x16x32_bf16 v[100:103], a[208:211], v[112:115], v[100:103]\n\t" \
  "ds_read_b64_tr_b16 a[182:183], v228 offset:13056\n\t" \
  "v_max3_f32 v192, v96, v97, v98\n\t" \
  "v_mfma_f32_16x16x32_bf16 v[104:107], a[224:227], v[112:115], v[188:191]\n\t" \
  "ds_read_b64_tr_b16 a[184:185], v229 offset:768\n\t" \
  "v_cvt_pk_bf16_f32 v159, v110, v111\n\t" \
  "v_mfma_f32_16x16x32_bf16 v[104:107], a[228:231], v[116:119], v[104:107]\n\t" \
  "ds_read_b64_tr_b16 a[186:187], v229 offset:4864\n\t" \
  "v_mfma_f32_16x16x32_bf16 v[104:107], a[232:235], v[120:123], v[104:107]\n\t" \
  "ds_read_b64_tr_b16 a[188:189], v229 offset:8960\n\t" \
  "v_max3_f32 v203, v99, v100, v101\n\t" \
  "v_mfma_f32_16x16x32_bf16 v[104:107], a[236:239], v[124:127], v[104:107]\n\t" \
  "ds_read_b64_tr_b16 a[190:191], v229 offset:13056\n\t" \
  "v_max3_f32 v192, v192, v102, v103\n\t" \
  "v_mfma_f32_16x16x32_bf16 v[108:111], a[252:255], v[124:127], v[188:191]\n\t" \
  "v_exp_f32 v0, v0\n\t" \
  "v_mfma_f32_16x16x32_bf16 v[108:111], a[248:251], v[120:123], v[108:111]\n\t" \
  "v_exp_f32 v1, v1\n\t" \
  "v_mfma_f32_16x16x32_bf16 v[108:111], a[244:247], v[116:119], v[108:111]\n\t" \
  "s_waitcnt vmcnt(0) lgkmcnt(0)\n\t" \
  "v_max3_f32 v203, v203, v104, v105\n\t" \
  "v_max3_f32 v192, v192, v106, v107\n\t" \
  "v_mfma_f32_16x16x32_bf16 v[108:111], a[240:243], v[112:115], v[108:111]\n\t" \
  "s_barrier\n\t" \
  "v_mfma_f32_16x16x32_bf16 a[0:3], a[128:131], v[128:131], a[0:3]\n\t" \
  "v_exp_f32 v2, v2\n\t" \
  "v_mfma_f32_16x16x32_bf16 a[0:3], a[132:135], v[132:135], a[0:3]\n\t" \
  "v_exp_f32 v3, v3\n\t" \
  "v_mfma_f32_16x16x32_bf16 a[4:7], a[136:139], v[128:131], a[4:7]\n\t" \
  "v_exp_f32 v4, v4\n\t" \
  "v_mfma_f32_16x16x32_bf16 a[4:7], a[140:143], v[132:135], a[4:7]\n\t" \
  "v_max3_f32 v203, v203, v108, v109\n\t" \
  "v_max3_f32 v192, v192, v110, v111\n\t" \
  "v_mfma_f32_16x16x32_bf16 a[8:11], a[144:147], v[128:131], a[8:11]\n\t" \
  "v_max_f32 v192, v192, v203\n\t" \
  "v_cmp_lt_f32 vcc, 0x42800000, v192\n\t" \
  "v_mfma_f32_16x16x32_bf16 a[8:11], a[148:151], v[132:135], a[8:11]\n\t" \
  "v_exp_f32 v5, v5\n\t" \
  "v_mfma_f32_16x16x32_bf16 a[12:15], a[152:155], v[128:131], a[12:15]\n\t" \
  "s_cbranch_vccnz .Lsf_c53\n\t" \
  ".Lsf_r53:\n\t" \
  "v_mfma_f32_16x16x32_bf16 a[12:15], a[156:159], v[132:135], a[12:15]\n\t" \
  "s_add_u32 m0, s38, 65536\n\t" \
  "v_exp_f32 v6, v6\n\t" \
  "v_mfma_f32_16x16x32_bf16 a[16:19], a[160:163], v[128:131], a[16:19]\n\t" \
  "s_add_u32 s96, s38, 114688\n\t" \
  "v_exp_f32 v7, v7\n\t" \
  "v_mfma_f32_16x16x32_bf16 a[16:19], a[164:167], v[132:135], a[16:19]\n\t" \
  "v_exp_f32 v8, v8\n\t" \
  "v_mfma_f32_16x16x32_bf16 a[20:23], a[168:171], v[128:131], a[20:23]\n\t" \
  "buffer_load_dwordx4 v224, s[60:63], s96 offen offset:0 lds\n\t" \
  "v_mfma_f32_16x16x32_bf16 a[20:23], a[172:175], v[132:135], a[20:23]\n\t" \
  "v_exp_f32 v9, v9\n\t" \
  "v_mfma_f32_16x16x32_bf16 a[24:27], a[176:179], v[128:131], a[24:27]\n\t" \
  "v_exp_f32 v10, v10\n\t" \
  "v_mfma_f32_16x16x32_bf16 a[24:27], a[180:183], v[132:135], a[24:27]\n\t" \
  "v_exp_f32 v11, v11\n\t" \
  "v_mfma_f32_16x16x32_bf16 a[28:31], a[184:187], v[128:131], a[28:31]\n\t" \
  "v_exp_f32 v12, v12\n\t" \
  "v_mfma_f32_16x16x32_bf16 a[28:31], a[188:191], v[132:135], a[28:31]\n\t" \
  "v_exp_f32 v13, v13\n\t" \
  "v_mfma_f32_16x16x32_bf16 v[160:163], v[216:219], v[128:131], v[160:163]\n\t" \
  "v_exp_f32 v14, v14\n\t" \
  "v_mfma_f32_16x16x32_bf16 v[160:163], v[216:219], v[132:135], v[160:163]\n\t" \
  "buffer_load_dwordx4 v225, s[60:63], s96 offen offset:1024 lds\n\t" \
  "v_mfma_f32_16x16x32_bf16 a[32:35], a[128:131], v[136:139], a[32:35]\n\t" \
  "ds_read_b128 a[192:195], v220 offset:0\n\t" \
  "v_cvt_pk_bf16_f32 v128, v0, v1\n\t" \
  "v_mfma_f32_16x16x32_bf16 a[32:35], a[132:135], v[140:143], a[32:35]\n\t" \
  "ds_read_b128 a[196:199], v221 offset:0\n\t" \
  "v_cvt_pk_bf16_f32 v129, v2, v3\n\t" \
  "v_mfma_f32_16x16x32_bf16 a[36:39], a[136:139], v[136:139], a[36:39]\n\t" \
  "v_exp_f32 v15, v15\n\t" \
  "v_mfma_f32_16x16x32_bf16 a[36:39], a[140:143], v[140:143], a[36:39]\n\t" \
  "v_exp_f32 v32, v32\n\t" \
  "v_mfma_f32_16x16x32_bf16 a[40:43], a[144:147], v[136:139], a[40:43]\n\t" \
  "ds_read_b128 a[200:203], v222 offset:0\n\t" \
  "v_cvt_pk_bf16_f32 v130, v4, v5\n\t" \
  "v_mfma_f32_16x16x32_bf16 a[40:43], a[148:151], v[140:143], a[40:43]\n\t" \
  "buffer_load_dwordx4 v226, s[60:63], s96 offen offset:2048 lds\n\t" \
  "v_mfma_f32_16x16x32_bf16 a[44:47], a[152:155], v[136:139], a[44:47]\n\t" \
  "ds_read_b128 a[204:207], v223 offset:0\n\t" \
  "v_cvt_pk_bf16_f32 v131, v6, v7\n\t" \
  "v_mfma_f32_16x16x32_bf16 a[44:47], a[156:159], v[140:143], a[44:47]\n\t" \
  "v_exp_f32 v33, v33\n\t" \
  "v_mfma_f32_16x16x32_bf16 a[48:51], a[160:163], v[136:139], a[48:51]\n\t" \
  "v_exp_f32 v34, v34\n\t" \
  "v_mfma_f32_16x16x32_bf16 a[48:51], a[164:167], v[140:143], a[48:51]\n\t" \
  "ds_read_b128 a[208:211], v220 offset:4096\n\t" \
  "v_cvt_pk_bf16_f32 v132, v8, v9\n\t" \
  "v_mfma_f32_16x16x32_bf16 a[52:55], a[168:171], v[136:139], a[52:55]\n\t" \
  "v_exp_f32 v35, v35\n\t" \
  "v_mfma_f32_16x16x32_bf16 a[52:55], a[172:175], v[140:143], a[52:55]\n\t" \
  "buffer_load_dwordx4 v227, s[60:63], s96 offen offset:3072 lds\n\t" \
  "v_mfma_f32_16x16x32_bf16 a[56:59], a[176:179], v[136:139], a[56:59]\n\t" \
  "ds_read_b128 a[212:215], v221 offset:4096\n\t" \
  "v_cvt_pk_bf16_f32 v133, v10, v11\n\t" \
  "v_mfma_f32_16x16x32_bf16 a[56:59], a[180:183], v[140:143], a[56:59]\n\t" \
  "s_add_u32 m0, s38, 81920\n\t" \
  "v_exp_f32 v36, v36\n\t" \
  "v_mfma_f32_16x16x32_bf16 a[60:63], a[184:187], v[136:139], a[60:63]\n\t" \
  "s_add_u32 s96, s38, 98304\n\t" \
  "v_exp_f32 v37, v37\n\t" \
  "v_mfma_f32_16x16x32_bf16 a[60:63], a[188:191], v[140:143], a[60:63]\n\t" \
  "ds_read_b128 a[216:219], v222 offset:4096\n\t" \
  "v_cvt_pk_bf16_f32 v134, v12, v13\n\t" \
  "v_mfma_f32_16x16x32_bf16 v[164:167], v[216:219], v[136:139], v[164:167]\n\t" \
  "v_exp_f32 v38, v38\n\t" \
  "v_mfma_f32_16x16x32_bf16 v[164:167], v[216:219], v[140:143], v[164:167]\n\t" \
  "buffer_load_dwordx4 v230, s[64:67], s96 offen offset:0 lds\n\t" \
  "v_mfma_f32_16x16x32_bf16 a[64:67], a[128:131], v[144:147], a[64:67]\n\t" \
  "ds_read_b128 a[220:223], v223 offset:4096\n\t" \
  "v_cvt_pk_bf16_f32 v135, v14, v15\n\t" \
  "v_mfma_f32_16x16x32_bf16 a[64:67], a[132:135], v[148:151], a[64:67]\n\t" \
  "v_exp_f32 v39, v39\n\t" \
  "v_mfma_f32_16x16x32_bf16 a[68:71], a[136:139], v[144:147], a[68:71]\n\t" \
  "v_exp_f32 v40, v40\n\t" \
  "v_mfma_f32_16x16x32_bf16 a[68:71], a[140:143], v[148:151], a[68:71]\n\t" \
  "ds_read_b128 a[224:227], v220 offset:8192\n\t" \
  "v_cvt_pk_bf16_f32 v136, v32, v33\n\t" \
  "v_mfma_f32_16x16x32_bf16 a[72:75], a[144:147], v[144:147], a[72:75]\n\t" \
  "v_exp_f32 v41, v41\n\t" \
  "v_mfma_f32_16x16x32_bf16 a[72:75], a[148:151], v[148:151], a[72:75]\n\t" \
  "ds_read_b128 a[228:231], v221 offset:8192\n\t" \
  "v_cvt_pk_bf16_f32 v137, v34, v35\n\t" \
  "v_mfma_f32_16x16x32_bf16 a[76:79], a[152:155], v[144:147], a[76:79]\n\t" \
  "buffer_load_dwordx4 v231, s[64:67], s96 offen offset:1024 lds\n\t" \
  "v_mfma_f32_16x16x32_bf16 a[76:79], a[156:159], v[148:151], a[76:79]\n\t" \
  "v_exp_f32 v42, v42\n\t" \
  "v_mfma_f32_16x16x32_bf16 a[80:83], a[160:163], v[144:147], a[80:83]\n\t" \
  "ds_read_b128 a[232:235], v222 offset:8192\n\t" \
  "v_cvt_pk_bf16_f32 v138, v36, v37\n\t" \
  "v_mfma_f32_16x16x32_bf16 a[80:83], a[164:167], v[148:151], a[80:83]\n\t" \
  "v_exp_f32 v43, v43\n\t" \
  "v_mfma_f32_16x16x32_bf16 a[84:87], a[168:171], v[144:147], a[84:87]\n\t" \
  "v_exp_f32 v44, v44\n\t" \
  "v_mfma_f32_16x16x32_bf16 a[84:87], a[172:175], v[148:151], a[84:87]\n\t" \
  "ds_read_b128 a[236:239], v223 offset:8192\n\t" \
  "v_cvt_pk_bf16_f32 v139, v38, v39\n\t" \
  "v_mfma_f32_16x16x32_bf16 a[88:91], a[176:179], v[144:147], a[88:91]\n\t" \
  "buffer_load_dwordx4 v230, s[64:67], s96 offen offset:2048 lds\n\t" \
  "v_mfma_f32_16x16x32_bf16 a[88:91], a[180:183], v[148:151], a[88:91]\n\t" \
  "v_exp_f32 v45, v45\n\t" \
  "v_mfma_f32_16x16x32_bf16 a[92:95], a[184:187], v[144:147], a[92:95]\n\t" \
  "ds_read_b128 a[240:243], v220 offset:12288\n\t" \
  "v_cvt_pk_bf16_f32 v140, v40, v41\n\t" \
  "v_mfma_f32_16x16x32_bf16 a[92:95], a[188:191], v[148:151], a[92:95]\n\t" \
  "v_exp_f32 v46, v46\n\t" \
  "v_mfma_f32_16x16x32_bf16 v[168:171], v[216:219], v[144:147], v[168:171]\n\t" \
  "v_exp_f32 v47, v47\n\t" \
  "v_mfma_f32_16x16x32_bf16 v[168:171], v[216:219], v[148:151], v[168:171]\n\t" \
  "ds_read_b128 a[244:247], v221 offset:12288\n\t" \
  "v_cvt_pk_bf16_f32 v141, v42, v43\n\t" \
  "v_mfma_f32_16x16x32_bf16 a[96:99], a[128:131], v[152:155], a[96:99]\n\t" \
  "buffer_load_dwordx4 v231, s[64:67], s96 offen offset:3072 lds\n\t" \
  "v_mfma_f32_16x16x32_bf16 a[96:99], a[132:135], v[156:159], a[96:99]\n\t" \
  "v_exp_f32 v64, v64\n\t" \
  "v_mfma_f32_16x16x32_bf16 a[100:103], a[136:139], v[152:155], a[100:103]\n\t" \
  "ds_read_b128 a[248:251], v222 offset:12288\n\t" \
  "v_cvt_pk_bf16_f32 v142, v44, v45\n\t" \
  "v_mfma_f32_16x16x32_bf16 a[100:103], a[140:143], v[156:159], a[100:103]\n\t" \
  "v_exp_f32 v65, v65\n\t" \
  "v_mfma_f32_16x16x32_bf16 a[104:107], a[144:147], v[152:155], a[104:107]\n\t" \
  "v_exp_f32 v66, v66\n\t" \
  "v_mfma_f32_16x16x32_bf16 a[104:107], a[148:151], v[156:159], a[104:107]\n\t" \
  "ds_read_b128 a[252:255], v223 offset:12288\n\t" \
  "v_cvt_pk_bf16_f32 v143, v46, v47\n\t" \
  "v_mfma_f32_16x16x32_bf16 a[108:111], a[152:155], v[152:155], a[108:111]\n\t" \
  "v_exp_f32 v67, v67\n\t" \
  "v_mfma_f32_16x16x32_bf16 a[108:111], a[156:159], v[156:159], a[108:111]\n\t" \
  "v_exp_f32 v68, v68\n\t" \
  "v_mfma_f32_16x16x32_bf16 a[112:115], a[160:163], v[152:155], a[112:115]\n\t" \
  "v_exp_f32 v69, v69\n\t" \
  "v_mfma_f32_16x16x32_bf16 a[112:115], a[164:167], v[156:159], a[112:115]\n\t" \
  "v_exp_f32 v70, v70\n\t" \
  "v_mfma_f32_16x16x32_bf16 a[116:119], a[168:171], v[152:155], a[116:119]\n\t" \
  "v_exp_f32 v71, v71\n\t" \
  "v_mfma_f32_16x16x32_bf16 a[116:119], a[172:175], v[156:159], a[116:119]\n\t" \
  "v_exp_f32 v72, v72\n\t" \
  "v_mfma_f32_16x16x32_bf16 a[120:123], a[176:179], v[152:155], a[120:123]\n\t" \
  "v_exp_f32 v73, v73\n\t" \
  "v_mfma_f32_16x16x32_bf16 a[120:123], a[180:183], v[156:159], a[120:123]\n\t" \
  "v_exp_f32 v74, v74\n\t" \
  "v_mfma_f32_16x16x32_bf16 a[124:127], a[184:187], v[152:155], a[124:127]\n\t" \
  "v_exp_f32 v75, v75\n\t" \
  "v_mfma_f32_16x16x32_bf16 a[124:127], a[188:191], v[156:159], a[124:127]\n\t" \
  "v_exp_f32 v76, v76\n\t" \
  "v_mfma_f32_16x16x32_bf16 v[172:175], v[216:219], v[152:155], v[172:175]\n\t" \
  "v_exp_f32 v77, v77\n\t" \
  "v_mfma_f32_16x16x32_bf16 v[172:175], v[216:219], v[156:159], v[172:175]\n\t" \
  "s_waitcnt lgkmcnt(12)\n\t" \
  "s_cmp_lg_u32 s99, 0\n\t" \
  "s_cbranch_scc1 .Lsf_p5\n\t" \
  ".Lsf_q5:\n\t" \
  "v_mfma_f32_16x16x32_bf16 v[0:3], a[192:195], v[16:19], v[176:179]\n\t" \
  "ds_read_b64_tr_b16 a[128:129], v210 offset:0\n\t" \
  "v_cvt_pk_bf16_f32 v144, v64, v65\n\t" \
  "v_mfma_f32_16x16x32_bf16 v[0:3], a[196:199], v[20:23], v[0:3]\n\t" \
  "v_exp_f32 v78, v78\n\t" \
  "v_mfma_f32_16x16x32_bf16 v[0:3], a[200:203], v[24:27], v[0:3]\n\t" \
  "ds_read_b64_tr_b16 a[130:131], v210 offset:4096\n\t" \
  "v_cvt_pk_bf16_f32 v145, v66, v67\n\t" \
  "v_mfma_f32_16x16x32_bf16 v[0:3], a[204:207], v[28:31], v[0:3]\n\t" \
  "s_waitcnt lgkmcnt(10)\n\t" \
  "v_exp_f32 v79, v79\n\t" \
  "v_mfma_f32_16x16x32_bf16 v[4:7], a[220:223], v[28:31], v[176:179]\n\t" \
  "ds_read_b64_tr_b16 a[132:133], v210 offset:8192\n\t" \
  "v_cvt_pk_bf16_f32 v146, v68, v69\n\t" \
  "v_mfma_f32_16x16x32_bf16 v[4:7], a[216:219], v[24:27], v[4:7]\n\t" \
  "v_exp_f32 v96, v96\n\t" \
  "v_mfma_f32_16x16x32_bf16 v[4:7], a[212:215], v[20:23], v[4:7]\n\t" \
  "ds_read_b64_tr_b16 a[134:135], v210 offset:12288\n\t" \
  "v_max3_f32 v192, v0, v1, v2\n\t" \
  "v_mfma_f32_16x16x32_bf16 v[4:7], a[208:211], v[16:19], v[4:7]\n\t" \
  "s_waitcnt lgkmcnt(8)\n\t" \
  "ds_read_b64_tr_b16 a[136:137], v211 offset:0\n\t" \
  "v_cvt_pk_bf16_f32 v147, v70, v71\n\t" \
  "v_mfma_f32_16x16x32_bf16 v[8:11], a[224:227], v[16:19], v[176:179]\n\t" \
  "v_exp_f32 v97, v97\n\t" \
  "v_mfma_f32_16x16x32_bf16 v[8:11], a[228:231], v[20:23], v[8:11]\n\t" \
  "ds_read_b64_tr_b16 a[138:139], v211 offset:4096\n\t" \
  "v_cvt_pk_bf16_f32 v148, v72, v73\n\t" \
  "v_mfma_f32_16x16x32_bf16 v[8:11], a[232:235], v[24:27], v[8:11]\n\t" \
  "v_max3_f32 v203, v3, v4, v5\n\t" \
  "v_max3_f32 v192, v192, v6, v7\n\t" \
  "v_mfma_f32_16x16x32_bf16 v[8:11], a[236:239], v[28:31], v[8:11]\n\t" \
  "s_waitcnt lgkmcnt(6)\n\t" \
  "ds_read_b64_tr_b16 a[140:141], v211 offset:8192\n\t" \
  "v_cvt_pk_bf16_f32 v149, v74, v75\n\t" \
  "v_mfma_f32_16x16x32_bf16 v[12:15], a[252:255], v[28:31], v[176:179]\n\t" \
  "v_exp_f32 v98, v98\n\t" \
  "v_mfma_f32_16x16x32_bf16 v[12:15], a[248:251], v[24:27], v[12:15]\n\t" \
  "ds_read_b64_tr_b16 a[142:143], v211 offset:12288\n\t" \
  "v_cvt_pk_bf16_f32 v150, v76, v77\n\t" \
  "v_mfma_f32_16x16x32_bf16 v[12:15], a[244:247], v[20:23], v[12:15]\n\t" \
  "v_max3_f32 v203, v203, v8, v9\n\t" \
  "v_max3_f32 v192, v192, v10, v11\n\t" \
  "v_mfma_f32_16x16x32_bf16 v[12:15], a[240:243], v[16:19], v[12:15]\n\t" \
  "ds_read_b64_tr_b16 a[144:145], v210 offset:256\n\t" \
  "v_cvt_pk_bf16_f32 v151, v78, v79\n\t" \
  "v_mfma_f32_16x16x32_bf16 v[32:35], a[192:195], v[48:51], v[180:183]\n\t" \
  "ds_read_b64_tr_b16 a[146:147], v210 offset:4352\n\t" \
  "v_cvt_pk_bf16_f32 v152, v96, v97\n\t" \
  "v_mfma_f32_16x16x32_bf16 v[32:35], a[196:199], v[52:55], v[32:35]\n\t" \
  "v_exp_f32 v99, v99\n\t" \
  "v_mfma_f32_16x16x32_bf16 v[32:35], a[200:203], v[56:59], v[32:35]\n\t" \
  "v_max3_f32 v203, v203, v12, v13\n\t" \
  "v_max3_f32 v192, v192, v14, v15\n\t" \
  "v_mfma_f32_16x16x32_bf16 v[32:35], a[204:207], v[60:63], v[32:35]\n\t" \
  "v_max_f32 v192, v192, v203\n\t" \
  "v_cmp_lt_f32 vcc, 0x42800000, v192\n\t" \
  "v_mfma_f32_16x16x32_bf16 v[36:39], a[220:223], v[60:63], v[180:183]\n\t" \
  "ds_read_b64_tr_b16 a[148:149], v210 offset:8448\n\t" \
  "v_cvt_pk_bf16_f32 v153, v98, v99\n\t" \
  "v_mfma_f32_16x16x32_bf16 v[36:39], a[216:219], v[56:59], v[36:39]\n\t" \
  "s_cbranch_vccnz .Lsf_c60\n\t" \
  ".Lsf_r60:\n\t" \
  "v_mfma_f32_16x16x32_bf16 v[36:39], a[212:215], v[52:55], v[36:39]\n\t" \
  "ds_read_b64_tr_b16 a[150:151], v210 offset:12544\n\t" \
  "v_max3_f32 v192, v32, v33, v34\n\t" \
  "v_mfma_f32_16x16x32_bf16 v[36:39], a[208:211], v[48:51], v[36:39]\n\t" \
  "v_exp_f32 v100, v100\n\t" \
  "v_mfma_f32_16x16x32_bf16 v[40:43], a[224:227], v[48:51], v[180:183]\n\t" \
  "v_exp_f32 v101, v101\n\t" \
  "v_mfma_f32_16x16x32_bf16 v[40:43], a[228:231], v[52:55], v[40:43]\n\t" \
  "ds_read_b64_tr_b16 a[152:153], v211 offset:256\n\t" \
  "v_cvt_pk_bf16_f32 v154, v100, v101\n\t" \
  "v_mfma_f32_16x16x32_bf16 v[40:43], a[232:235], v[56:59], v[40:43]\n\t" \
  "ds_read_b64_tr_b16 a[154:155], v211 offset:4352\n\t" \
  "v_max3_f32 v203, v35, v36, v37\n\t" \
  "v_mfma_f32_16x16x32_bf16 v[40:43], a[236:239], v[60:63], v[40:43]\n\t" \
  "ds_read_b64_tr_b16 a[156:157], v211 offset:8448\n\t" \
  "v_max3_f32 v192, v192, v38, v39\n\t" \
  "v_mfma_f32_16x16x32_bf16 v[44:47], a[252:255], v[60:63], v[180:183]\n\t" \
  "v_exp_f32 v102, v102\n\t" \
  "v_mfma_f32_16x16x32_bf16 v[44:47], a[248:251], v[56:59], v[44:47]\n\t" \
  "v_exp_f32 v103, v103\n\t" \
  "v_mfma_f32_16x16x32_bf16 v[44:47], a[244:247], v[52:55], v[44:47]\n\t" \
  "ds_read_b64_tr_b16 a[158:159], v211 offset:12544\n\t" \
  "v_max3_f32 v203, v203, v40, v41\n\t" \
  "v_mfma_f32_16x16x32_bf16 v[44:47], a[240:243], v[48:51], v[44:47]\n\t" \
  "ds_read_b64_tr_b16 a[160:161], v210 offset:512\n\t" \
  "v_max3_f32 v192, v192, v42, v43\n\t" \
  "v_mfma_f32_16x16x32_bf16 v[64:67], a[192:195], v[80:83], v[184:187]\n\t" \
  "ds_read_b64_tr_b16 a[162:163], v210 offset:4608\n\t" \
  "v_cvt_pk_bf16_f32 v155, v102, v103\n\t" \
  "v_mfma_f32_16x16x32_bf16 v[64:67], a[196:199], v[84:87], v[64:67]\n\t" \
  "v_exp_f32 v104, v104\n\t" \
  "v_mfma_f32_16x16x32_bf16 v[64:67], a[200:203], v[88:91], v[64:67]\n\t" \
  "v_max3_f32 v203, v203, v44, v45\n\t" \
  "v_max3_f32 v192, v192, v46, v47\n\t" \
  "v_mfma_f32_16x16x32_bf16 v[64:67], a[204:207], v[92:95], v[64:67]\n\t" \
  "v_max_f32 v192, v192, v203\n\t" \
  "v_cmp_lt_f32 vcc, 0x42800000, v192\n\t" \
  "v_mfma_f32_16x16x32_bf16 v[68:71], a[220:223], v[92:95], v[184:187]\n\t" \
  "v_exp_f32 v105, v105\n\t" \
  "v_mfma_f32_16x16x32_bf16 v[68:71], a[216:219], v[88:91], v[68:71]\n\t" \
  "s_cbranch_vccnz .Lsf_c61\n\t" \
  ".Lsf_r61:\n\t" \
  "v_mfma_f32_16x16x32_bf16 v[68:71], a[212:215], v[84:87], v[68:71]\n\t" \
  "ds_read_b64_tr_b16 a[164:165], v210 offset:8704\n\t" \
  "v_max3_f32 v192, v64, v65, v66\n\t" \
  "v_mfma_f32_16x16x32_bf16 v[68:71], a[208:211], v[80:83], v[68:71]\n\t" \
  "ds_read_b64_tr_b16 a[166:167], v210 offset:12800\n\t" \
  "v_cvt_pk_bf16_f32 v156, v104, v105\n\t" \
  "v_mfma_f32_16x16x32_bf16 v[72:75], a[224:227], v[80:83], v[184:187]\n\t" \
  "v_exp_f32 v106, v106\n\t" \
  "v_mfma_f32_16x16x32_bf16 v[72:75], a[228:231], v[84:87], v[72:75]\n\t" \
  "v_exp_f32 v107, v107\n\t" \
  "v_mfma_f32_16x16x32_bf16 v[72:75], a[232:235], v[88:91], v[72:75]\n\t" \
  "ds_read_b64_tr_b16 a[168:169], v211 offset:512\n\t" \
  "v_max3_f32 v203, v67, v68, v69\n\t" \
  "v_mfma_f32_16x16x32_bf16 v[72:75], a[236:239], v[92:95], v[72:75]\n\t" \
  "ds_read_b64_tr_b16 a[170:171], v211 offset:4608\n\t" \
  "v_max3_f32 v192, v192, v70, v71\n\t" \
  "v_mfma_f32_16x16x32_bf16 v[76:79], a[252:255], v[92:95], v[184:187]\n\t" \
  "ds_read_b64_tr_b16 a[172:173], v211 offset:8704\n\t" \
  "v_cvt_pk_bf16_f32 v157, v106, v107\n\t" \
  "v_mfma_f32_16x16x32_bf16 v[76:79], a[248:251], v[88:91], v[76:79]\n\t" \
  "v_exp_f32 v108, v108\n\t" \
  "v_mfma_f32_16x16x32_bf16 v[76:79], a[244:247], v[84:87], v[76:79]\n\t" \
  "v_max3_f32 v203, v203, v72, v73\n\t" \
  "v_max3_f32 v192, v192, v74, v75\n\t" \
  "v_mfma_f32_16x16x32_bf16 v[76:79], a[240:243], v[80:83], v[76:79]\n\t" \
  "v_exp_f32 v109, v109\n\t" \
  "v_mfma_f32_16x16x32_bf16 v[96:99], a[192:195], v[112:115], v[188:191]\n\t" \
  "ds_read_b64_tr_b16 a[174:175], v211 offset:12800\n\t" \
  "v_cvt_pk_bf16_f32 v158, v108, v109\n\t" \
  "v_mfma_f32_16x16x32_bf16 v[96:99], a[196:199], v[116:119], v[96:99]\n\t" \
  "v_exp_f32 v110, v110\n\t" \
  "v_mfma_f32_16x16x32_bf16 v[96:99], a[200:203], v[120:123], v[96:99]\n\t" \
  "ds_read_b64_tr_b16 a[176:177], v210 offset:768\n\t" \
  "v_max3_f32 v203, v203, v76, v77\n\t" \
  "v_mfma_f32_16x16x32_bf16 v[96:99], a[204:207], v[124:127], v[96:99]\n\t" \
  "v_max3_f32 v192, v192, v78, v79\n\t" \
  "v_max_f32 v192, v192, v203\n\t" \
  "v_mfma_f32_16x16x32_bf16 v[100:103], a[220:223], v[124:127], v[188:191]\n\t" \
  "ds_read_b64_tr_b16 a[178:179], v210 offset:4864\n\t" \
  "v_cmp_lt_f32 vcc, 0x42800000, v192\n\t" \
  "v_mfma_f32_16x16x32_bf16 v[100:103], a[216:219], v[120:123], v[100:103]\n\t" \
  "ds_read_b64_tr_b16 a[180:181], v210 offset:8960\n\t" \
  "v_exp_f32 v111, v111\n\t" \
  "v_mfma_f32_16x16x32_bf16 v[100:103], a[212:215], v[116:119], v[100:103]\n\t" \
  "s_cbranch_vccnz .Lsf_c62\n\t" \
  ".Lsf_r62:\n\t" \
  "v_mfma_f32_16x16x32_bf16 v[100:103], a[208:211], v[112:115], v[100:103]\n\t" \
  "ds_read_b64_tr_b16 a[182:183], v210 offset:13056\n\t" \
  "v_max3_f32 v192, v96, v97, v98\n\t" \
  "v_mfma_f32_16x16x32_bf16 v[104:107], a[224:227], v[112:115], v[188:191]\n\t" \
  "ds_read_b64_tr_b16 a[184:185], v211 offset:768\n\t" \
  "v_cvt_pk_bf16_f32 v159, v110, v111\n\t" \
  "v_mfma_f32_16x16x32_bf16 v[104:107], a[228:231], v[116:119], v[104:107]\n\t" \
  "ds_read_b64_tr_b16 a[186:187], v211 offset:4864\n\t" \
  "v_mfma_f32_16x16x32_bf16 v[104:107], a[232:235], v[120:123], v[104:107]\n\t" \
  "ds_read_b64_tr_b16 a[188:189], v211 offset:8960\n\t" \
  "v_max3_f32 v203, v99, v100, v101\n\t" \
  "v_mfma_f32_16x16x32_bf16 v[104:107], a[236:239], v[124:127], v[104:107]\n\t" \
  "ds_read_b64_tr_b16 a[190:191], v211 offset:13056\n\t" \
  "v_max3_f32 v192, v192, v102, v103\n\t" \
  "v_mfma_f32_16x16x32_bf16 v[108:111], a[252:255], v[124:127], v[188:191]\n\t" \
  "v_exp_f32 v0, v0\n\t" \
  "v_mfma_f32_16x16x32_bf16 v[108:111], a[248:251], v[120:123], v[108:111]\n\t" \
  "v_exp_f32 v1, v1\n\t" \
  "v_mfma_f32_16x16x32_bf16 v[108:111], a[244:247], v[116:119], v[108:111]\n\t" \
  "s_waitcnt vmcnt(0) lgkmcnt(0)\n\t" \
  "v_max3_f32 v203, v203, v104, v105\n\t" \
  "v_max3_f32 v192, v192, v106, v107\n\t" \
  "v_mfma_f32_16x16x32_bf16 v[108:111], a[240:243], v[112:115], v[108:111]\n\t" \
  "s_barrier\n\t" \
  "v_mfma_f32_16x16x32_bf16 a[0:3], a[128:131], v[128:131], a[0:3]\n\t" \
  "v_exp_f32 v2, v2\n\t" \
  "v_mfma_f32_16x16x32_bf16 a[0:3], a[132:135], v[132:135], a[0:3]\n\t" \
  "v_exp_f32 v3, v3\n\t" \
  "v_mfma_f32_16x16x32_bf16 a[4:7], a[136:139], v[128:131], a[4:7]\n\t" \
  "v_exp_f32 v4, v4\n\t" \
  "v_mfma_f32_16x16x32_bf16 a[4:7], a[140:143], v[132:135], a[4:7]\n\t" \
  "v_max3_f32 v203, v203, v108, v109\n\t" \
  "v_max3_f32 v192, v192, v110, v111\n\t" \
  "v_mfma_f32_16x16x32_bf16 a[8:11], a[144:147], v[128:131], a[8:11]\n\t" \
  "v_max_f32 v192, v192, v203\n\t" \
  "v_cmp_lt_f32 vcc, 0x42800000, v192\n\t" \
  "v_mfma_f32_16x16x32_bf16 a[8:11], a[148:151], v[132:135], a[8:11]\n\t" \
  "v_exp_f32 v5, v5\n\t" \
  "v_mfma_f32_16x16x32_bf16 a[12:15], a[152:155], v[128:131], a[12:15]\n\t" \
  "s_cbranch_vccnz .Lsf_c63\n\t" \
  ".Lsf_r63:\n\t" \
  "v_mfma_f32_16x16x32_bf16 a[12:15], a[156:159], v[132:135], a[12:15]\n\t" \
  "s_add_u32 m0, s38, 147456\n\t" \
  "v_exp_f32 v6, v6\n\t" \
  "v_mfma_f32_16x16x32_bf16 a[16:19], a[160:163], v[128:131], a[16:19]\n\t" \
  "s_add_u32 s96, s38, 114688\n\t" \
  "v_exp_f32 v7, v7\n\t" \
  "v_mfma_f32_16x16x32_bf16 a[16:19], a[164:167], v[132:135], a[16:19]\n\t" \
  "v_exp_f32 v8, v8\n\t" \
  "v_mfma_f32_16x16x32_bf16 a[20:23], a[168:171], v[128:131], a[20:23]\n\t" \
  "buffer_load_dwordx4 v230, s[64:67], s96 offen offset:0 lds\n\t" \
  "v_mfma_f32_16x16x32_bf16 a[20:23], a[172:175], v[132:135], a[20:23]\n\t" \
  "v_exp_f32 v9, v9\n\t" \
  "v_mfma_f32_16x16x32_bf16 a[24:27], a[176:179], v[128:131], a[24:27]\n\t" \
  "v_exp_f32 v10, v10\n\t" \
  "v_mfma_f32_16x16x32_bf16 a[24:27], a[180:183], v[132:135], a[24:27]\n\t" \
  "v_exp_f32 v11, v11\n\t" \
  "v_mfma_f32_16x16x32_bf16 a[28:31], a[184:187], v[128:131], a[28:31]\n\t" \
  "v_exp_f32 v12, v12\n\t" \
  "v_mfma_f32_16x16x32_bf16 a[28:31], a[188:191], v[132:135], a[28:31]\n\t" \
  "v_exp_f32 v13, v13\n\t" \
  "v_mfma_f32_16x16x32_bf16 v[160:163], v[216:219], v[128:131], v[160:163]\n\t" \
  "v_exp_f32 v14, v14\n\t" \
  "v_mfma_f32_16x16x32_bf16 v[160:163], v[216:219], v[132:135], v[160:163]\n\t" \
  "ds_read_b128 a[192:195], v220 offset:16384\n\t" \
  "v_cvt_pk_bf16_f32 v128, v0, v1\n\t" \
  "v_mfma_f32_16x16x32_bf16 a[32:35], a[128:131], v[136:139], a[32:35]\n\t" \
  "ds_read_b128 a[196:199], v221 offset:16384\n\t" \
  "v_cvt_pk_bf16_f32 v129, v2, v3\n\t" \
  "v_mfma_f32_16x16x32_bf16 a[32:35], a[132:135], v[140:143], a[32:35]\n\t" \
  "v_exp_f32 v15, v15\n\t" \
  "v_mfma_f32_16x16x32_bf16 a[36:39], a[136:139], v[136:139], a[36:39]\n\t" \
  "v_exp_f32 v32, v32\n\t" \
  "v_mfma_f32_16x16x32_bf16 a[36:39], a[140:143], v[140:143], a[36:39]\n\t" \
  "ds_read_b128 a[200:203], v222 offset:16384\n\t" \
  "v_cvt_pk_bf16_f32 v130, v4, v5\n\t" \
  "v_mfma_f32_16x16x32_bf16 a[40:43], a[144:147], v[136:139], a[40:43]\n\t" \
  "v_exp_f32 v33, v33\n\t" \
  "v_mfma_f32_16x16x32_bf16 a[40:43], a[148:151], v[140:143], a[40:43]\n\t" \
  "buffer_load_dwordx4 v231, s[64:67], s96 offen offset:1024 lds\n\t" \
  "v_mfma_f32_16x16x32_bf16 a[44:47], a[152:155], v[136:139], a[44:47]\n\t" \
  "ds_read_b128 a[204:207], v223 offset:16384\n\t" \
  "v_cvt_pk_bf16_f32 v131, v6, v7\n\t" \
  "v_mfma_f32_16x16x32_bf16 a[44:47], a[156:159], v[140:143], a[44:47]\n\t" \
  "v_exp_f32 v34, v34\n\t" \
  "v_mfma_f32_16x16x32_bf16 a[48:51], a[160:163], v[136:139], a[48:51]\n\t" \
  "v_exp_f32 v35, v35\n\t" \
  "v_mfma_f32_16x16x32_bf16 a[48:51], a[164:167], v[140:143], a[48:51]\n\t" \
  "ds_read_b128 a[208:211], v220 offset:20480\n\t" \
  "v_cvt_pk_bf16_f32 v132, v8, v9\n\t" \
  "v_mfma_f32_16x16x32_bf16 a[52:55], a[168:171], v[136:139], a[52:55]\n\t" \
  "v_exp_f32 v36, v36\n\t" \
  "v_mfma_f32_16x16x32_bf16 a[52:55], a[172:175], v[140:143], a[52:55]\n\t" \
  "v_exp_f32 v37, v37\n\t" \
  "v_mfma_f32_16x16x32_bf16 a[56:59], a[176:179], v[136:139], a[56:59]\n\t" \
  "ds_read_b128 a[212:215], v221 offset:20480\n\t" \
  "v_cvt_pk_bf16_f32 v133, v10, v11\n\t" \
  "v_mfma_f32_16x16x32_bf16 a[56:59], a[180:183], v[140:143], a[56:59]\n\t" \
  "v_exp_f32 v38, v38\n\t" \
  "v_mfma_f32_16x16x32_bf16 a[60:63], a[184:187], v[136:139], a[60:63]\n\t" \
  "ds_read_b128 a[216:219], v222 offset:20480\n\t" \
  "v_cvt_pk_bf16_f32 v134, v12, v13\n\t" \
  "v_mfma_f32_16x16x32_bf16 a[60:63], a[188:191], v[140:143], a[60:63]\n\t" \
  "v_exp_f32 v39, v39\n\t" \
  "v_mfma_f32_16x16x32_bf16 v[164:167], v[216:219], v[136:139], v[164:167]\n\t" \
  "v_exp_f32 v40, v40\n\t" \
  "v_mfma_f32_16x16x32_bf16 v[164:167], v[216:219], v[140:143], v[164:167]\n\t" \
  "buffer_load_dwordx4 v230, s[64:67], s96 offen offset:2048 lds\n\t" \
  "v_mfma_f32_16x16x32_bf16 a[64:67], a[128:131], v[144:147], a[64:67]\n\t" \
  "ds_read_b128 a[220:223], v223 offset:20480\n\t" \
  "v_cvt_pk_bf16_f32 v135, v14, v15\n\t" \
  "v_mfma_f32_16x16x32_bf16 a[64:67], a[132:135], v[148:151], a[64:67]\n\t" \
  "v_exp_f32 v41, v41\n\t" \
  "v_mfma_f32_16x16x32_bf16 a[68:71], a[136:139], v[144:147], a[68:71]\n\t" \
  "ds_read_b128 a[224:227], v220 offset:24576\n\t" \
  "v_cvt_pk_bf16_f32 v136, v32, v33\n\t" \
  "v_mfma_f32_16x16x32_bf16 a[68:71], a[140:143], v[148:151], a[68:71]\n\t" \
  "v_exp_f32 v42, v42\n\t" \
  "v_mfma_f32_16x16x32_bf16 a[72:75], a[144:147], v[144:147], a[72:75]\n\t" \
  "v_exp_f32 v43, v43\n\t" \
  "v_mfma_f32_16x16x32_bf16 a[72:75], a[148:151], v[148:151], a[72:75]\n\t" \
  "ds_read_b128 a[228:231], v221 offset:24576\n\t" \
  "v_cvt_pk_bf16_f32 v137, v34, v35\n\t" \
  "v_mfma_f32_16x16x32_bf16 a[76:79], a[152:155], v[144:147], a[76:79]\n\t" \
  "v_exp_f32 v44, v44\n\t" \
  "v_mfma_f32_16x16x32_bf16 a[76:79], a[156:159], v[148:151], a[76:79]\n\t" \
  "v_exp_f32 v45, v45\n\t" \
  "v_mfma_f32_16x16x32_bf16 a[80:83], a[160:163], v[144:147], a[80:83]\n\t" \
  "ds_read_b128 a[232:235], v222 offset:24576\n\t" \
  "v_cvt_pk_bf16_f32 v138, v36, v37\n\t" \
  "v_mfma_f32_16x16x32_bf16 a[80:83], a[164:167], v[148:151], a[80:83]\n\t" \
  "v_exp_f32 v46, v46\n\t" \
  "v_mfma_f32_16x16x32_bf16 a[84:87], a[168:171], v[144:147], a[84:87]\n\t" \
  "v_exp_f32 v47, v47\n\t" \
  "v_mfma_f32_16x16x32_bf16 a[84:87], a[172:175], v[148:151], a[84:87]\n\t" \
  "ds_read_b128 a[236:239], v223 offset:24576\n\t" \
  "v_cvt_pk_bf16_f32 v139, v38, v39\n\t" \
  "v_mfma_f32_16x16x32_bf16 a[88:91], a[176:179], v[144:147], a[88:91]\n\t" \
  "buffer_load_dwordx4 v231, s[64:67], s96 offen offset:3072 lds\n\t" \
  "v_mfma_f32_16x16x32_bf16 a[88:91], a[180:183], v[148:151], a[88:91]\n\t" \
  "v_exp_f32 v64, v64\n\t" \
  "v_mfma_f32_16x16x32_bf16 a[92:95], a[184:187], v[144:147], a[92:95]\n\t" \
  "ds_read_b128 a[240:243], v220 offset:28672\n\t" \
  "v_cvt_pk_bf16_f32 v140, v40, v41\n\t" \
  "v_mfma_f32_16x16x32_bf16 a[92:95], a[188:191], v[148:151], a[92:95]\n\t" \
  "v_exp_f32 v65, v65\n\t" \
  "v_mfma_f32_16x16x32_bf16 v[168:171], v[216:219], v[144:147], v[168:171]\n\t" \
  "v_exp_f32 v66, v66\n\t" \
  "v_mfma_f32_16x16x32_bf16 v[168:171], v[216:219], v[148:151], v[168:171]\n\t" \
  "ds_read_b128 a[244:247], v221 offset:28672\n\t" \
  "v_cvt_pk_bf16_f32 v141, v42, v43\n\t" \
  "v_mfma_f32_16x16x32_bf16 a[96:99], a[128:131], v[152:155], a[96:99]\n\t" \
  "v_exp_f32 v67, v67\n\t" \
  "v_mfma_f32_16x16x32_bf16 a[96:99], a[132:135], v[156:159], a[96:99]\n\t" \
  "v_exp_f32 v68, v68\n\t" \
  "v_mfma_f32_16x16x32_bf16 a[100:103], a[136:139], v[152:155], a[100:103]\n\t" \
  "ds_read_b128 a[248:251], v222 offset:28672\n\t" \
  "v_cvt_pk_bf16_f32 v142, v44, v45\n\t" \
  "v_mfma_f32_16x16x32_bf16 a[100:103], a[140:143], v[156:159], a[100:103]\n\t" \
  "v_exp_f32 v69, v69\n\t" \
  "v_mfma_f32_16x16x32_bf16 a[104:107], a[144:147], v[152:155], a[104:107]\n\t" \
  "v_exp_f32 v70, v70\n\t" \
  "v_mfma_f32_16x16x32_bf16 a[104:107], a[148:151], v[156:159], a[104:107]\n\t" \
  "ds_read_b128 a[252:255], v223 offset:28672\n\t" \
  "v_cvt_pk_bf16_f32 v143, v46, v47\n\t" \
  "v_mfma_f32_16x16x32_bf16 a[108:111], a[152:155], v[152:155], a[108:111]\n\t" \
  "v_exp_f32 v71, v71\n\t" \
  "v_mfma_f32_16x16x32_bf16 a[108:111], a[156:159], v[156:159], a[108:111]\n\t" \
  "v_exp_f32 v72, v72\n\t" \
  "v_mfma_f32_16x16x32_bf16 a[112:115], a[160:163], v[152:155], a[112:115]\n\t" \
  "v_exp_f32 v73, v73\n\t" \
  "v_mfma_f32_16x16x32_bf16 a[112:115], a[164:167], v[156:159], a[112:115]\n\t" \
  "v_exp_f32 v74, v74\n\t" \
  "v_mfma_f32_16x16x32_bf16 a[116:119], a[168:171], v[152:155], a[116:119]\n\t" \
  "v_exp_f32 v75, v75\n\t" \
  "v_mfma_f32_16x16x32_bf16 a[116:119], a[172:175], v[156:159], a[116:119]\n\t" \
  "v_exp_f32 v76, v76\n\t" \
  "v_mfma_f32_16x16x32_bf16 a[120:123], a[176:179], v[152:155], a[120:123]\n\t" \
  "v_exp_f32 v77, v77\n\t" \
  "v_mfma_f32_16x16x32_bf16 a[120:123], a[180:183], v[156:159], a[120:123]\n\t" \
  "v_exp_f32 v78, v78\n\t" \
  "v_mfma_f32_16x16x32_bf16 a[124:127], a[184:187], v[152:155], a[124:127]\n\t" \
  "v_exp_f32 v79, v79\n\t" \
  "v_mfma_f32_16x16x32_bf16 a[124:127], a[188:191], v[156:159], a[124:127]\n\t" \
  "v_exp_f32 v96, v96\n\t" \
  "v_mfma_f32_16x16x32_bf16 v[172:175], v[216:219], v[152:155], v[172:175]\n\t" \
  "v_cvt_pk_bf16_f32 v144, v64, v65\n\t" \
  "v_cvt_pk_bf16_f32 v145, v66, v67\n\t" \
  "v_mfma_f32_16x16x32_bf16 v[172:175], v[216:219], v[156:159], v[172:175]\n\t" \
  "s_waitcnt lgkmcnt(12)\n\t" \
  "s_cmp_lg_u32 s99, 0\n\t" \
  "s_cbranch_scc1 .Lsf_p6\n\t" \
  ".Lsf_q6:\n\t" \
  "v_mfma_f32_16x16x32_bf16 v[0:3], a[192:195], v[16:19], v[176:179]\n\t" \
  "ds_read_b64_tr_b16 a[128:129], v228 offset:0\n\t" \
  "v_cvt_pk_bf16_f32 v146, v68, v69\n\t" \
  "v_mfma_f32_16x16x32_bf16 v[0:3], a[196:199], v[20:23], v[0:3]\n\t" \
  "v_exp_f32 v97, v97\n\t" \
  "v_mfma_f32_16x16x32_bf16 v[0:3], a[200:203], v[24:27], v[0:3]\n\t" \
  "ds_read_b64_tr_b16 a[130:131], v228 offset:4096\n\t" \
  "v_cvt_pk_bf16_f32 v147, v70, v71\n\t" \
  "v_mfma_f32_16x16x32_bf16 v[0:3], a[204:207], v[28:31], v[0:3]\n\t" \
  "s_waitcnt lgkmcnt(10)\n\t" \
  "v_exp_f32 v98, v98\n\t" \
  "v_mfma_f32_16x16x32_bf16 v[4:7], a[220:223], v[28:31], v[176:179]\n\t" \
  "ds_read_b64_tr_b16 a[132:133], v228 offset:8192\n\t" \
  "v_cvt_pk_bf16_f32 v148, v72, v73\n\t" \
  "v_mfma_f32_16x16x32_bf16 v[4:7], a[216:219], v[24:27], v[4:7]\n\t" \
  "v_exp_f32 v99, v99\n\t" \
  "v_mfma_f32_16x16x32_bf16 v[4:7], a[212:215], v[20:23], v[4:7]\n\t" \
  "ds_read_b64_tr_b16 a[134:135], v228 offset:12288\n\t" \
  "v_max3_f32 v192, v0, v1, v2\n\t" \
  "v_mfma_f32_16x16x32_bf16 v[4:7], a[208:211], v[16:19], v[4:7]\n\t" \
  "s_waitcnt lgkmcnt(8)\n\t" \
  "ds_read_b64_tr_b16 a[136:137], v229 offset:0\n\t" \
  "v_cvt_pk_bf16_f32 v149, v74, v75\n\t" \
  "v_mfma_f32_16x16x32_bf16 v[8:11], a[224:227], v[16:19], v[176:179]\n\t" \
  "v_exp_f32 v100, v100\n\t" \
  "v_mfma_f32_16x16x32_bf16 v[8:11], a[228:231], v[20:23], v[8:11]\n\t" \
  "ds_read_b64_tr_b16 a[138:139], v229 offset:4096\n\t" \
  "v_cvt_pk_bf16_f32 v150, v76, v77\n\t" \
  "v_mfma_f32_16x16x32_bf16 v[8:11], a[232:235], v[24:27], v[8:11]\n\t" \
  "v_max3_f32 v203, v3, v4, v5\n\t" \
  "v_max3_f32 v192, v192, v6, v7\n\t" \
  "v_mfma_f32_16x16x32_bf16 v[8:11], a[236:239], v[28:31], v[8:11]\n\t" \
  "s_waitcnt lgkmcnt(6)\n\t" \
  "ds_read_b64_tr_b16 a[140:141], v229 offset:8192\n\t" \
  "v_cvt_pk_bf16_f32 v151, v78, v79\n\t" \
  "v_mfma_f32_16x16x32_bf16 v[12:15], a[252:255], v[28:31], v[176:179]\n\t" \
  "v_exp_f32 v101, v101\n\t" \
  "v_mfma_f32_16x16x32_bf16 v[12:15], a[248:251], v[24:27], v[12:15]\n\t" \
  "ds_read_b64_tr_b16 a[142:143], v229 offset:12288\n\t" \
  "v_cvt_pk_bf16_f32 v152, v96, v97\n\t" \
  "v_mfma_f32_16x16x32_bf16 v[12:15], a[244:247], v[20:23], v[12:15]\n\t" \
  "v_max3_f32 v203, v203, v8, v9\n\t" \
  "v_max3_f32 v192, v192, v10, v11\n\t" \
  "v_mfma_f32_16x16x32_bf16 v[12:15], a[240:243], v[16:19], v[12:15]\n\t" \
  "ds_read_b64_tr_b16 a[144:145], v228 offset:256\n\t" \
  "v_cvt_pk_bf16_f32 v153, v98, v99\n\t" \
  "v_mfma_f32_16x16x32_bf16 v[32:35], a[192:195], v[48:51], v[180:183]\n\t" \
  "ds_read_b64_tr_b16 a[146:147], v228 offset:4352\n\t" \
  "v_cvt_pk_bf16_f32 v154, v100, v101\n\t" \
  "v_mfma_f32_16x16x32_bf16 v[32:35], a[196:199], v[52:55], v[32:35]\n\t" \
  "v_exp_f32 v102, v102\n\t" \
  "v_mfma_f32_16x16x32_bf16 v[32:35], a[200:203], v[56:59], v[32:35]\n\t" \
  "v_max3_f32 v203, v203, v12, v13\n\t" \
  "v_max3_f32 v192, v192, v14, v15\n\t" \
  "v_mfma_f32_16x16x32_bf16 v[32:35], a[204:207], v[60:63], v[32:35]\n\t" \
  "v_max_f32 v192, v192, v203\n\t" \
  "v_cmp_lt_f32 vcc, 0x42800000, v192\n\t" \
  "v_mfma_f32_16x16x32_bf16 v[36:39], a[220:223], v[60:63], v[180:183]\n\t" \
  "v_exp_f32 v103, v103\n\t" \
  "v_mfma_f32_16x16x32_bf16 v[36:39], a[216:219], v[56:59], v[36:39]\n\t" \
  "s_cbranch_vccnz .Lsf_c70\n\t" \
  ".Lsf_r70:\n\t" \
  "v_mfma_f32_16x16x32_bf16 v[36:39], a[212:215], v[52:55], v[36:39]\n\t" \
  "ds_read_b64_tr_b16 a[148:149], v228 offset:8448\n\t" \
  "v_max3_f32 v192, v32, v33, v34\n\t" \
  "v_mfma_f32_16x16x32_bf16 v[36:39], a[208:211], v[48:51], v[36:39]\n\t" \
  "ds_read_b64_tr_b16 a[150:151], v228 offset:12544\n\t" \
  "v_cvt_pk_bf16_f32 v155, v102, v103\n\t" \
  "v_mfma_f32_16x16x32_bf16 v[40:43], a[224:227], v[48:51], v[180:183]\n\t" \
  "v_exp_f32 v104, v104\n\t" \
  "v_mfma_f32_16x16x32_bf16 v[40:43], a[228:231], v[52:55], v[40:43]\n\t" \
  "v_exp_f32 v105, v105\n\t" \
  "v_mfma_f32_16x16x32_bf16 v[40:43], a[232:235], v[56:59], v[40:43]\n\t" \
  "ds_read_b64_tr_b16 a[152:153], v229 offset:256\n\t" \
  "v_max3_f32 v203, v35, v36, v37\n\t" \
  "v_mfma_f32_16x16x32_bf16 v[40:43], a[236:239], v[60:63], v[40:43]\n\t" \
  "ds_read_b64_tr_b16 a[154:155], v229 offset:4352\n\t" \
  "v_max3_f32 v192, v192, v38, v39\n\t" \
  "v_mfma_f32_16x16x32_bf16 v[44:47], a[252:255], v[60:63], v[180:183]\n\t" \
  "ds_read_b64_tr_b16 a[156:157], v229 offset:8448\n\t" \
  "v_cvt_pk_bf16_f32 v156, v104, v105\n\t" \
  "v_mfma_f32_16x16x32_bf16 v[44:47], a[248:251], v[56:59], v[44:47]\n\t" \
  "v_exp_f32 v106, v106\n\t" \
  "v_mfma_f32_16x16x32_bf16 v[44:47], a[244:247], v[52:55], v[44:47]\n\t" \
  "v_max3_f32 v203, v203, v40, v41\n\t" \
  "v_max3_f32 v192, v192, v42, v43\n\t" \
  "v_mfma_f32_16x16x32_bf16 v[44:47], a[240:243], v[48:51], v[44:47]\n\t" \
  "v_exp_f32 v107, v107\n\t" \
  "v_mfma_f32_16x16x32_bf16 v[64:67], a[192:195], v[80:83], v[184:187]\n\t" \
  "ds_read_b64_tr_b16 a[158:159], v229 offset:12544\n\t" \
  "v_cvt_pk_bf16_f32 v157, v106, v107\n\t" \
  "v_mfma_f32_16x16x32_bf16 v[64:67], a[196:199], v[84:87], v[64:67]\n\t" \
  "v_exp_f32 v108, v108\n\t" \
  "v_mfma_f32_16x16x32_bf16 v[64:67], a[200:203], v[88:91], v[64:67]\n\t" \
  "ds_read_b64_tr_b16 a[160:161], v228 offset:512\n\t" \
  "v_max3_f32 v203, v203, v44, v45\n\t" \
  "v_mfma_f32_16x16x32_bf16 v[64:67], a[204:207], v[92:95], v[64:67]\n\t" \
  "v_max3_f32 v192, v192, v46, v47\n\t" \
  "v_max_f32 v192, v192, v203\n\t" \
  "v_mfma_f32_16x16x32_bf16 v[68:71], a[220:223], v[92:95], v[184:187]\n\t" \
  "ds_read_b64_tr_b16 a[162:163], v228 offset:4608\n\t" \
  "v_cmp_lt_f32 vcc, 0x42800000, v192\n\t" \
  "v_mfma_f32_16x16x32_bf16 v[68:71], a[216:219], v[88:91], v[68:71]\n\t" \
  "v_exp_f32 v109, v109\n\t" \
  "v_mfma_f32_16x16x32_bf16 v[68:71], a[212:215], v[84:87], v[68:71]\n\t" \
  "s_cbranch_vccnz .Lsf_c71\n\t" \
  ".Lsf_r71:\n\t" \
  "v_mfma_f32_16x16x32_bf16 v[68:71], a[208:211], v[80:83], v[68:71]\n\t" \
  "ds_read_b64_tr_b16 a[164:165], v228 offset:8704\n\t" \
  "v_max3_f32 v192, v64, v65, v66\n\t" \
  "v_mfma_f32_16x16x32_bf16 v[72:75], a[224:227], v[80:83], v[184:187]\n\t" \
  "ds_read_b64_tr_b16 a[166:167], v228 offset:12800\n\t" \
  "v_cvt_pk_bf16_f32 v158, v108, v109\n\t" \
  "v_mfma_f32_16x16x32_bf16 v[72:75], a[228:231], v[84:87], v[72:75]\n\t" \
  "v_exp_f32 v110, v110\n\t" \
  "v_mfma_f32_16x16x32_bf16 v[72:75], a[232:235], v[88:91], v[72:75]\n\t" \
  "v_max3_f32 v203, v67, v68, v69\n\t" \
  "v_max3_f32 v192, v192, v70, v71\n\t" \
  "v_mfma_f32_16x16x32_bf16 v[72:75], a[236:239], v[92:95], v[72:75]\n\t" \
  "ds_read_b64_tr_b16 a[168:169], v229 offset:512\n\t" \
  "v_mfma_f32_16x16x32_bf16 v[76:79], a[252:255], v[92:95], v[184:187]\n\t" \
  "v_exp_f32 v111, v111\n\t" \
  "v_mfma_f32_16x16x32_bf16 v[76:79], a[248:251], v[88:91], v[76:79]\n\t" \
  "ds_read_b64_tr_b16 a[170:171], v229 offset:4608\n\t" \
  "v_cvt_pk_bf16_f32 v159, v110, v111\n\t" \
  "v_mfma_f32_16x16x32_bf16 v[76:79], a[244:247], v[84:87], v[76:79]\n\t" \
  "ds_read_b64_tr_b16 a[172:173], v229 offset:8704\n\t" \
  "v_max3_f32 v203, v203, v72, v73\n\t" \
  "v_mfma_f32_16x16x32_bf16 v[76:79], a[240:243], v[80:83], v[76:79]\n\t" \
  "ds_read_b64_tr_b16 a[174:175], v229 offset:12800\n\t" \
  "v_max3_f32 v192, v192, v74, v75\n\t" \
  "v_mfma_f32_16x16x32_bf16 v[96:99], a[192:195], v[112:115], v[188:191]\n\t" \
  "ds_read_b64_tr_b16 a[176:177], v228 offset:768\n\t" \
  "v_mfma_f32_16x16x32_bf16 v[96:99], a[196:199], v[116:119], v[96:99]\n\t" \
  "ds_read_b64_tr_b16 a[178:179], v228 offset:4864\n\t" \
  "v_mfma_f32_16x16x32_bf16 v[96:99], a[200:203], v[120:123], v[96:99]\n\t" \
  "ds_read_b64_tr_b16 a[180:181], v228 offset:8960\n\t" \
  "v_max3_f32 v203, v203, v76, v77\n\t" \
  "v_mfma_f32_16x16x32_bf16 v[96:99], a[204:207], v[124:127], v[96:99]\n\t" \
  "v_max3_f32 v192, v192, v78, v79\n\t" \
  "v_max_f32 v192, v192, v203\n\t" \
  "v_mfma_f32_16x16x32_bf16 v[100:103], a[220:223], v[124:127], v[188:191]\n\t" \
  "ds_read_b64_tr_b16 a[182:183], v228 offset:13056\n\t" \
  "v_cmp_lt_f32 vcc, 0x42800000, v192\n\t" \
  "v_mfma_f32_16x16x32_bf16 v[100:103], a[216:219], v[120:123], v[100:103]\n\t" \
  "ds_read_b64_tr_b16 a[184:185], v229 offset:768\n\t" \
  "v_mfma_f32_16x16x32_bf16 v[100:103], a[212:215], v[116:119], v[100:103]\n\t" \
  "s_cbranch_vccnz .Lsf_c72\n\t" \
  ".Lsf_r72:\n\t" \
  "v_mfma_f32_16x16x32_bf16 v[100:103], a[208:211], v[112:115], v[100:103]\n\t" \
  "ds_read_b64_tr_b16 a[186:187], v229 offset:4864\n\t" \
  "v_max3_f32 v192, v96, v97, v98\n\t" \
  "v_mfma_f32_16x16x32_bf16 v[104:107], a[224:227], v[112:115], v[188:191]\n\t" \
  "ds_read_b64_tr_b16 a[188:189], v229 offset:8960\n\t" \
  "v_mfma_f32_16x16x32_bf16 v[104:107], a[228:231], v[116:119], v[104:107]\n\t" \
  "ds_read_b64_tr_b16 a[190:191], v229 offset:13056\n\t" \
  "v_mfma_f32_16x16x32_bf16 v[104:107], a[232:235], v[120:123], v[104:107]\n\t" \
  "v_exp_f32 v0, v0\n\t" \
  "v_mfma_f32_16x16x32_bf16 v[104:107], a[236:239], v[124:127], v[104:107]\n\t" \
  "v_max3_f32 v203, v99, v100, v101\n\t" \
  "v_max3_f32 v192, v192, v102, v103\n\t" \
  "v_mfma_f32_16x16x32_bf16 v[108:111], a[252:255], v[124:127], v[188:191]\n\t" \
  "v_exp_f32 v1, v1\n\t" \
  "v_mfma_f32_16x16x32_bf16 v[108:111], a[248:251], v[120:123], v[108:111]\n\t" \
  "v_exp_f32 v2, v2\n\t" \
  "v_mfma_f32_16x16x32_bf16 v[108:111], a[244:247], v[116:119], v[108:111]\n\t" \
  "s_waitcnt vmcnt(0) lgkmcnt(0)\n\t" \
  "v_max3_f32 v203, v203, v104, v105\n\t" \
  "v_max3_f32 v192, v192, v106, v107\n\t" \
  "v_mfma_f32_16x16x32_bf16 v[108:111], a[240:243], v[112:115], v[108:111]\n\t" \
  "s_barrier\n\t" \
  "v_mfma_f32_16x16x32_bf16 a[0:3], a[128:131], v[128:131], a[0:3]\n\t" \
  "ds_read_b64_tr_b16 v[16:17], v210 offset:0\n\t" \
  "v_cvt_pk_bf16_f32 v0, v0, v1\n\t" \
  "v_mfma_f32_16x16x32_bf16 a[0:3], a[132:135], v[132:135], a[0:3]\n\t" \
  "v_exp_f32 v3, v3\n\t" \
  "v_mfma_f32_16x16x32_bf16 a[4:7], a[136:139], v[128:131], a[4:7]\n\t" \
  "ds_read_b64_tr_b16 v[18:19], v210 offset:4096\n\t" \
  "v_max3_f32 v203, v203, v108, v109\n\t" \
  "v_mfma_f32_16x16x32_bf16 a[4:7], a[140:143], v[132:135], a[4:7]\n\t" \
  "v_max3_f32 v192, v192, v110, v111\n\t" \
  "v_max_f32 v192, v192, v203\n\t" \
  "v_mfma_f32_16x16x32_bf16 a[8:11], a[144:147], v[128:131], a[8:11]\n\t" \
  "ds_read_b64_tr_b16 v[20:21], v210 offset:8192\n\t" \
  "v_cmp_lt_f32 vcc, 0x42800000, v192\n\t" \
  "v_mfma_f32_16x16x32_bf16 a[8:11], a[148:151], v[132:135], a[8:11]\n\t" \
  "ds_read_b64_tr_b16 v[22:23], v210 offset:12288\n\t" \
  "v_cvt_pk_bf16_f32 v1, v2, v3\n\t" \
  "v_mfma_f32_16x16x32_bf16 a[12:15], a[152:155], v[128:131], a[12:15]\n\t" \
  "s_cbranch_vccnz .Lsf_c73\n\t" \
  ".Lsf_r73:\n\t" \
  "v_mfma_f32_16x16x32_bf16 a[12:15], a[156:159], v[132:135], a[12:15]\n\t" \
  "v_exp_f32 v4, v4\n\t" \
  "v_mfma_f32_16x16x32_bf16 a[16:19], a[160:163], v[128:131], a[16:19]\n\t" \
  "v_exp_f32 v5, v5\n\t" \
  "v_mfma_f32_16x16x32_bf16 a[16:19], a[164:167], v[132:135], a[16:19]\n\t" \
  "ds_read_b64_tr_b16 v[24:25], v211 offset:0\n\t" \
  "v_cvt_pk_bf16_f32 v2, v4, v5\n\t" \
  "v_mfma_f32_16x16x32_bf16 a[20:23], a[168:171], v[128:131], a[20:23]\n\t" \
  "v_exp_f32 v6, v6\n\t" \
  "v_mfma_f32_16x16x32_bf16 a[20:23], a[172:175], v[132:135], a[20:23]\n\t" \
  "v_exp_f32 v7, v7\n\t" \
  "v_mfma_f32_16x16x32_bf16 a[24:27], a[176:179], v[128:131], a[24:27]\n\t" \
  "ds_read_b64_tr_b16 v[26:27], v211 offset:4096\n\t" \
  "v_cvt_pk_bf16_f32 v3, v6, v7\n\t" \
  "v_mfma_f32_16x16x32_bf16 a[24:27], a[180:183], v[132:135], a[24:27]\n\t" \
  "v_exp_f32 v8, v8\n\t" \
  "v_mfma_f32_16x16x32_bf16 a[28:31], a[184:187], v[128:131], a[28:31]\n\t" \
  "ds_read_b64_tr_b16 v[28:29], v211 offset:8192\n\t" \
  "v_mfma_f32_16x16x32_bf16 a[28:31], a[188:191], v[132:135], a[28:31]\n\t" \
  "ds_read_b64_tr_b16 v[30:31], v211 offset:12288\n\t" \
  "v_mfma_f32_16x16x32_bf16 v[160:163], v[216:219], v[128:131], v[160:163]\n\t" \
  "ds_read_b64_tr_b16 v[48:49], v210 offset:256\n\t" \
  "v_mfma_f32_16x16x32_bf16 v[160:163], v[216:219], v[132:135], v[160:163]\n\t" \
  "ds_read_b64_tr_b16 v[50:51], v210 offset:4352\n\t" \
  "v_mfma_f32_16x16x32_bf16 a[32:35], a[128:131], v[136:139], a[32:35]\n\t" \
  "v_exp_f32 v9, v9\n\t" \
  "v_mfma_f32_16x16x32_bf16 a[32:35], a[132:135], v[140:143], a[32:35]\n\t" \
  "ds_read_b64_tr_b16 v[52:53], v210 offset:8448\n\t" \
  "v_cvt_pk_bf16_f32 v8, v8, v9\n\t" \
  "v_mfma_f32_16x16x32_bf16 a[36:39], a[136:139], v[136:139], a[36:39]\n\t" \
  "ds_read_b64_tr_b16 v[54:55], v210 offset:12544\n\t" \
  "v_mfma_f32_16x16x32_bf16 a[36:39], a[140:143], v[140:143], a[36:39]\n\t" \
  "ds_read_b64_tr_b16 v[56:57], v211 offset:256\n\t" \
  "v_mfma_f32_16x16x32_bf16 a[40:43], a[144:147], v[136:139], a[40:43]\n\t" \
  "v_exp_f32 v10, v10\n\t" \
  "v_mfma_f32_16x16x32_bf16 a[40:43], a[148:151], v[140:143], a[40:43]\n\t" \
  "ds_read_b64_tr_b16 v[58:59], v211 offset:4352\n\t" \
  "v_mfma_f32_16x16x32_bf16 a[44:47], a[152:155], v[136:139], a[44:47]\n\t" \
  "ds_read_b64_tr_b16 v[60:61], v211 offset:8448\n\t" \
  "v_mfma_f32_16x16x32_bf16 a[44:47], a[156:159], v[140:143], a[44:47]\n\t" \
  "ds_read_b64_tr_b16 v[62:63], v211 offset:12544\n\t" \
  "v_mfma_f32_16x16x32_bf16 a[48:51], a[160:163], v[136:139], a[48:51]\n\t" \
  "v_exp_f32 v11, v11\n\t" \
  "v_mfma_f32_16x16x32_bf16 a[48:51], a[164:167], v[140:143], a[48:51]\n\t" \
  "ds_read_b64_tr_b16 v[80:81], v210 offset:512\n\t" \
  "v_cvt_pk_bf16_f32 v9, v10, v11\n\t" \
  "v_mfma_f32_16x16x32_bf16 a[52:55], a[168:171], v[136:139], a[52:55]\n\t" \
  "ds_read_b64_tr_b16 v[82:83], v210 offset:4608\n\t" \
  "v_mfma_f32_16x16x32_bf16 a[52:55], a[172:175], v[140:143], a[52:55]\n\t" \
  "v_exp_f32 v12, v12\n\t" \
  "v_mfma_f32_16x16x32_bf16 a[56:59], a[176:179], v[136:139], a[56:59]\n\t" \
  "v_exp_f32 v13, v13\n\t" \
  "v_mfma_f32_16x16x32_bf16 a[56:59], a[180:183], v[140:143], a[56:59]\n\t" \
  "ds_read_b64_tr_b16 v[84:85], v210 offset:8704\n\t" \
  "v_cvt_pk_bf16_f32 v10, v12, v13\n\t" \
  "v_mfma_f32_16x16x32_bf16 a[60:63], a[184:187], v[136:139], a[60:63]\n\t" \
  "ds_read_b64_tr_b16 v[86:87], v210 offset:12800\n\t" \
  "v_mfma_f32_16x16x32_bf16 a[60:63], a[188:191], v[140:143], a[60:63]\n\t" \
  "v_exp_f32 v14, v14\n\t" \
  "v_mfma_f32_16x16x32_bf16 v[164:167], v[216:219], v[136:139], v[164:167]\n\t" \
  "ds_read_b64_tr_b16 v[88:89], v211 offset:512\n\t" \
  "v_mfma_f32_16x16x32_bf16 v[164:167], v[216:219], v[140:143], v[164:167]\n\t" \
  "v_exp_f32 v15, v15\n\t" \
  "v_mfma_f32_16x16x32_bf16 a[64:67], a[128:131], v[144:147], a[64:67]\n\t" \
  "ds_read_b64_tr_b16 v[90:91], v211 offset:4608\n\t" \
  "v_cvt_pk_bf16_f32 v11, v14, v15\n\t" \
  "v_mfma_f32_16x16x32_bf16 a[64:67], a[132:135], v[148:151], a[64:67]\n\t" \
  "v_exp_f32 v32, v32\n\t" \
  "v_mfma_f32_16x16x32_bf16 a[68:71], a[136:139], v[144:147], a[68:71]\n\t" \
  "v_exp_f32 v33, v33\n\t" \
  "v_mfma_f32_16x16x32_bf16 a[68:71], a[140:143], v[148:151], a[68:71]\n\t" \
  "ds_read_b64_tr_b16 v[92:93], v211 offset:8704\n\t" \
  "v_cvt_pk_bf16_f32 v32, v32, v33\n\t" \
  "v_mfma_f32_16x16x32_bf16 a[72:75], a[144:147], v[144:147], a[72:75]\n\t" \
  "ds_read_b64_tr_b16 v[94:95], v211 offset:12800\n\t" \
  "v_mfma_f32_16x16x32_bf16 a[72:75], a[148:151], v[148:151], a[72:75]\n\t" \
  "v_exp_f32 v34, v34\n\t" \
  "v_mfma_f32_16x16x32_bf16 a[76:79], a[152:155], v[144:147], a[76:79]\n\t" \
  "v_exp_f32 v35, v35\n\t" \
  "v_mfma_f32_16x16x32_bf16 a[76:79], a[156:159], v[148:151], a[76:79]\n\t" \
  "ds_read_b64_tr_b16 v[112:113], v210 offset:768\n\t" \
  "v_cvt_pk_bf16_f32 v33, v34, v35\n\t" \
  "v_mfma_f32_16x16x32_bf16 a[80:83], a[160:163], v[144:147], a[80:83]\n\t" \
  "v_exp_f32 v36, v36\n\t" \
  "v_mfma_f32_16x16x32_bf16 a[80:83], a[164:167], v[148:151], a[80:83]\n\t" \
  "ds_read_b64_tr_b16 v[114:115], v210 offset:4864\n\t" \
  "v_mfma_f32_16x16x32_bf16 a[84:87], a[168:171], v[144:147], a[84:87]\n\t" \
  "v_exp_f32 v37, v37\n\t" \
  "v_mfma_f32_16x16x32_bf16 a[84:87], a[172:175], v[148:151], a[84:87]\n\t" \
  "ds_read_b64_tr_b16 v[116:117], v210 offset:8960\n\t" \
  "v_cvt_pk_bf16_f32 v34, v36, v37\n\t" \
  "v_mfma_f32_16x16x32_bf16 a[88:91], a[176:179], v[144:147], a[88:91]\n\t" \
  "v_exp_f32 v38, v38\n\t" \
  "v_mfma_f32_16x16x32_bf16 a[88:91], a[180:183], v[148:151], a[88:91]\n\t" \
  "ds_read_b64_tr_b16 v[118:119], v210 offset:13056\n\t" \
  "v_mfma_f32_16x16x32_bf16 a[92:95], a[184:187], v[144:147], a[92:95]\n\t" \
  "v_exp_f32 v39, v39\n\t" \
  "v_mfma_f32_16x16x32_bf16 a[92:95], a[188:191], v[148:151], a[92:95]\n\t" \
  "ds_read_b64_tr_b16 v[120:121], v211 offset:768\n\t" \
  "v_cvt_pk_bf16_f32 v35, v38, v39\n\t" \
  "v_mfma_f32_16x16x32_bf16 v[168:171], v[216:219], v[144:147], v[168:171]\n\t" \
  "v_exp_f32 v40, v40\n\t" \
  "v_mfma_f32_16x16x32_bf16 v[168:171], v[216:219], v[148:151], v[168:171]\n\t" \
  "ds_read_b64_tr_b16 v[122:123], v211 offset:4864\n\t" \
  "v_mfma_f32_16x16x32_bf16 a[96:99], a[128:131], v[152:155], a[96:99]\n\t" \
  "v_exp_f32 v41, v41\n\t" \
  "v_mfma_f32_16x16x32_bf16 a[96:99], a[132:135], v[156:159], a[96:99]\n\t" \
  "ds_read_b64_tr_b16 v[124:125], v211 offset:8960\n\t" \
  "v_cvt_pk_bf16_f32 v40, v40, v41\n\t" \
  "v_mfma_f32_16x16x32_bf16 a[100:103], a[136:139], v[152:155], a[100:103]\n\t" \
  "v_exp_f32 v42, v42\n\t" \
  "v_mfma_f32_16x16x32_bf16 a[100:103], a[140:143], v[156:159], a[100:103]\n\t" \
  "ds_read_b64_tr_b16 v[126:127], v211 offset:13056\n\t" \
  "v_mfma_f32_16x16x32_bf16 a[104:107], a[144:147], v[152:155], a[104:107]\n\t" \
  "v_exp_f32 v43, v43\n\t" \
  "v_mfma_f32_16x16x32_bf16 a[104:107], a[148:151], v[156:159], a[104:107]\n\t" \
  "v_exp_f32 v44, v44\n\t" \
  "v_mfma_f32_16x16x32_bf16 a[108:111], a[152:155], v[152:155], a[108:111]\n\t" \
  "v_exp_f32 v45, v45\n\t" \
  "v_mfma_f32_16x16x32_bf16 a[108:111], a[156:159], v[156:159], a[108:111]\n\t" \
  "v_exp_f32 v46, v46\n\t" \
  "v_mfma_f32_16x16x32_bf16 a[112:115], a[160:163], v[152:155], a[112:115]\n\t" \
  "v_exp_f32 v47, v47\n\t" \
  "v_mfma_f32_16x16x32_bf16 a[112:115], a[164:167], v[156:159], a[112:115]\n\t" \
  "v_exp_f32 v64, v64\n\t" \
  "v_mfma_f32_16x16x32_bf16 a[116:119], a[168:171], v[152:155], a[116:119]\n\t" \
  "v_cvt_pk_bf16_f32 v41, v42, v43\n\t" \
  "v_cvt_pk_bf16_f32 v42, v44, v45\n\t" \
  "v_mfma_f32_16x16x32_bf16 a[116:119], a[172:175], v[156:159], a[116:119]\n\t" \
  "v_exp_f32 v65, v65\n\t" \
  "v_mfma_f32_16x16x32_bf16 a[120:123], a[176:179], v[152:155], a[120:123]\n\t" \
  "v_exp_f32 v66, v66\n\t" \
  "v_mfma_f32_16x16x32_bf16 a[120:123], a[180:183], v[156:159], a[120:123]\n\t" \
  "v_exp_f32 v67, v67\n\t" \
  "v_mfma_f32_16x16x32_bf16 a[124:127], a[184:187], v[152:155], a[124:127]\n\t" \
  "v_exp_f32 v68, v68\n\t" \
  "v_mfma_f32_16x16x32_bf16 a[124:127], a[188:191], v[156:159], a[124:127]\n\t" \
  "v_exp_f32 v69, v69\n\t" \
  "v_mfma_f32_16x16x32_bf16 v[172:175], v[216:219], v[152:155], v[172:175]\n\t" \
  "v_cvt_pk_bf16_f32 v43, v46, v47\n\t" \
  "v_cvt_pk_bf16_f32 v64, v64, v65\n\t" \
  "v_mfma_f32_16x16x32_bf16 v[172:175], v[216:219], v[156:159], v[172:175]\n\t" \
  "s_waitcnt lgkmcnt(15)\n\t" \
  "s_cmp_lg_u32 s99, 0\n\t" \
  "s_cbranch_scc1 .Lsf_p7\n\t" \
  ".Lsf_q7:\n\t" \
  "v_mfma_f32_16x16x32_bf16 a[0:3], v[16:19], v[0:3], a[0:3]\n\t" \
  "v_exp_f32 v70, v70\n\t" \
  "v_mfma_f32_16x16x32_bf16 a[0:3], v[20:23], v[8:11], a[0:3]\n\t" \
  "v_exp_f32 v71, v71\n\t" \
  "v_mfma_f32_16x16x32_bf16 v[160:163], v[216:219], v[0:3], v[160:163]\n\t" \
  "v_exp_f32 v72, v72\n\t" \
  "v_mfma_f32_16x16x32_bf16 v[160:163], v[216:219], v[8:11], v[160:163]\n\t" \
  "v_exp_f32 v73, v73\n\t" \
  "v_mfma_f32_16x16x32_bf16 a[4:7], v[24:27], v[0:3], a[4:7]\n\t" \
  "v_exp_f32 v74, v74\n\t" \
  "v_mfma_f32_16x16x32_bf16 a[4:7], v[28:31], v[8:11], a[4:7]\n\t" \
  "v_cvt_pk_bf16_f32 v65, v66, v67\n\t" \
  "v_cvt_pk_bf16_f32 v66, v68, v69\n\t" \
  "v_mfma_f32_16x16x32_bf16 a[8:11], v[48:51], v[0:3], a[8:11]\n\t" \
  "v_exp_f32 v75, v75\n\t" \
  "v_mfma_f32_16x16x32_bf16 a[8:11], v[52:55], v[8:11], a[8:11]\n\t" \
  "v_exp_f32 v76, v76\n\t" \
  "v_mfma_f32_16x16x32_bf16 a[12:15], v[56:59], v[0:3], a[12:15]\n\t" \
  "v_cvt_pk_bf16_f32 v67, v70, v71\n\t" \
  "v_cvt_pk_bf16_f32 v72, v72, v73\n\t" \
  "v_mfma_f32_16x16x32_bf16 a[12:15], v[60:63], v[8:11], a[12:15]\n\t" \
  "s_waitcnt lgkmcnt(8)\n\t" \
  "v_exp_f32 v77, v77\n\t" \
  "v_mfma_f32_16x16x32_bf16 a[16:19], v[80:83], v[0:3], a[16:19]\n\t" \
  "v_exp_f32 v78, v78\n\t" \
  "v_mfma_f32_16x16x32_bf16 a[16:19], v[84:87], v[8:11], a[16:19]\n\t" \
  "v_cvt_pk_bf16_f32 v73, v74, v75\n\t" \
  "v_cvt_pk_bf16_f32 v74, v76, v77\n\t" \
  "v_mfma_f32_16x16x32_bf16 a[20:23], v[88:91], v[0:3], a[20:23]\n\t" \
  "v_exp_f32 v79, v79\n\t" \
  "v_mfma_f32_16x16x32_bf16 a[20:23], v[92:95], v[8:11], a[20:23]\n\t" \
  "s_waitcnt lgkmcnt(0)\n\t" \
  "v_exp_f32 v96, v96\n\t" \
  "v_mfma_f32_16x16x32_bf16 a[24:27], v[112:115], v[0:3], a[24:27]\n\t" \
  "v_exp_f32 v97, v97\n\t" \
  "v_mfma_f32_16x16x32_bf16 a[24:27], v[116:119], v[8:11], a[24:27]\n\t" \
  "v_exp_f32 v98, v98\n\t" \
  "v_mfma_f32_16x16x32_bf16 a[28:31], v[120:123], v[0:3], a[28:31]\n\t" \
  "v_exp_f32 v99, v99\n\t" \
  "v_mfma_f32_16x16x32_bf16 a[28:31], v[124:127], v[8:11], a[28:31]\n\t" \
  "v_exp_f32 v100, v100\n\t" \
  "v_mfma_f32_16x16x32_bf16 a[32:35], v[16:19], v[32:35], a[32:35]\n\t" \
  "v_cvt_pk_bf16_f32 v75, v78, v79\n\t" \
  "v_cvt_pk_bf16_f32 v96, v96, v97\n\t" \
  "v_mfma_f32_16x16x32_bf16 a[32:35], v[20:23], v[40:43], a[32:35]\n\t" \
  "v_exp_f32 v101, v101\n\t" \
  "v_mfma_f32_16x16x32_bf16 v[164:167], v[216:219], v[32:35], v[164:167]\n\t" \
  "v_exp_f32 v102, v102\n\t" \
  "v_mfma_f32_16x16x32_bf16 v[164:167], v[216:219], v[40:43], v[164:167]\n\t" \
  "v_exp_f32 v103, v103\n\t" \
  "v_mfma_f32_16x16x32_bf16 a[36:39], v[24:27], v[32:35], a[36:39]\n\t" \
  "v_exp_f32 v104, v104\n\t" \
  "v_mfma_f32_16x16x32_bf16 a[36:39], v[28:31], v[40:43], a[36:39]\n\t" \
  "v_exp_f32 v105, v105\n\t" \
  "v_mfma_f32_16x16x32_bf16 a[40:43], v[48:51], v[32:35], a[40:43]\n\t" \
  "v_cvt_pk_bf16_f32 v97, v98, v99\n\t" \
  "v_cvt_pk_bf16_f32 v98, v100, v101\n\t" \
  "v_mfma_f32_16x16x32_bf16 a[40:43], v[52:55], v[40:43], a[40:43]\n\t" \
  "v_exp_f32 v106, v106\n\t" \
  "v_mfma_f32_16x16x32_bf16 a[44:47], v[56:59], v[32:35], a[44:47]\n\t" \
  "v_exp_f32 v107, v107\n\t" \
  "v_mfma_f32_16x16x32_bf16 a[44:47], v[60:63], v[40:43], a[44:47]\n\t" \
  "v_cvt_pk_bf16_f32 v99, v102, v103\n\t" \
  "v_cvt_pk_bf16_f32 v104, v104, v105\n\t" \
  "v_mfma_f32_16x16x32_bf16 a[48:51], v[80:83], v[32:35], a[48:51]\n\t" \
  "v_exp_f32 v108, v108\n\t" \
  "v_mfma_f32_16x16x32_bf16 a[48:51], v[84:87], v[40:43], a[48:51]\n\t" \
  "v_exp_f32 v109, v109\n\t" \
  "v_mfma_f32_16x16x32_bf16 a[52:55], v[88:91], v[32:35], a[52:55]\n\t" \
  "v_cvt_pk_bf16_f32 v105, v106, v107\n\t" \
  "v_cvt_pk_bf16_f32 v106, v108, v109\n\t" \
  "v_mfma_f32_16x16x32_bf16 a[52:55], v[92:95], v[40:43], a[52:55]\n\t" \
  "v_exp_f32 v110, v110\n\t" \
  "v_mfma_f32_16x16x32_bf16 a[56:59], v[112:115], v[32:35], a[56:59]\n\t" \
  "v_exp_f32 v111, v111\n\t" \
  "v_mfma_f32_16x16x32_bf16 a[56:59], v[116:119], v[40:43], a[56:59]\n\t" \
  "v_cvt_pk_bf16_f32 v107, v110, v111\n\t" \
  "v_mfma_f32_16x16x32_bf16 a[60:63], v[120:123], v[32:35], a[60:63]\n\t" \
  "v_mfma_f32_16x16x32_bf16 a[60:63], v[124:127], v[40:43], a[60:63]\n\t" \
  "v_mfma_f32_16x16x32_bf16 a[64:67], v[16:19], v[64:67], a[64:67]\n\t" \
  "v_mfma_f32_16x16x32_bf16 a[64:67], v[20:23], v[72:75], a[64:67]\n\t" \
  "v_mfma_f32_16x16x32_bf16 v[168:171], v[216:219], v[64:67], v[168:171]\n\t" \
  "v_mfma_f32_16x16x32_bf16 v[168:171], v[216:219], v[72:75], v[168:171]\n\t" \
  "v_mfma_f32_16x16x32_bf16 a[68:71], v[24:27], v[64:67], a[68:71]\n\t" \
  "v_mfma_f32_16x16x32_bf16 a[68:71], v[28:31], v[72:75], a[68:71]\n\t" \
  "v_mfma_f32_16x16x32_bf16 a[72:75], v[48:51], v[64:67], a[72:75]\n\t" \
  "v_mfma_f32_16x16x32_bf16 a[72:75], v[52:55], v[72:75], a[72:75]\n\t" \
  "v_mfma_f32_16x16x32_bf16 a[76:79], v[56:59], v[64:67], a[76:79]\n\t" \
  "v_mfma_f32_16x16x32_bf16 a[76:79], v[60:63], v[72:75], a[76:79]\n\t" \
  "v_mfma_f32_16x16x32_bf16 a[80:83], v[80:83], v[64:67], a[80:83]\n\t" \
  "v_mfma_f32_16x16x32_bf16 a[80:83], v[84:87], v[72:75], a[80:83]\n\t" \
  "v_mfma_f32_16x16x32_bf16 a[84:87], v[88:91], v[64:67], a[84:87]\n\t" \
  "v_mfma_f32_16x16x32_bf16 a[84:87], v[92:95], v[72:75], a[84:87]\n\t" \
  "v_mfma_f32_16x16x32_bf16 a[88:91], v[112:115], v[64:67], a[88:91]\n\t" \
  "v_mfma_f32_16x16x32_bf16 a[88:91], v[116:119], v[72:75], a[88:91]\n\t" \
  "v_mfma_f32_16x16x32_bf16 a[92:95], v[120:123], v[64:67], a[92:95]\n\t" \
  "v_mfma_f32_16x16x32_bf16 a[92:95], v[124:127], v[72:75], a[92:95]\n\t" \
  "v_mfma_f32_16x16x32_bf16 a[96:99], v[16:19], v[96:99], a[96:99]\n\t" \
  "v_mfma_f32_16x16x32_bf16 a[96:99], v[20:23], v[104:107], a[96:99]\n\t" \
  "v_mfma_f32_16x16x32_bf16 v[172:175], v[216:219], v[96:99], v[172:175]\n\t" \
  "v_mfma_f32_16x16x32_bf16 v[172:175], v[216:219], v[104:107], v[172:175]\n\t" \
  "v_mfma_f32_16x16x32_bf16 a[100:103], v[24:27], v[96:99], a[100:103]\n\t" \
  "v_mfma_f32_16x16x32_bf16 a[100:103], v[28:31], v[104:107], a[100:103]\n\t" \
  "v_mfma_f32_16x16x32_bf16 a[104:107], v[48:51], v[96:99], a[104:107]\n\t" \
  "v_mfma_f32_16x16x32_bf16 a[104:107], v[52:55], v[104:107], a[104:107]\n\t" \
  "v_mfma_f32_16x16x32_bf16 a[108:111], v[56:59], v[96:99], a[108:111]\n\t" \
  "v_mfma_f32_16x16x32_bf16 a[108:111], v[60:63], v[104:107], a[108:111]\n\t" \
  "v_mfma_f32_16x16x32_bf16 a[112:115], v[80:83], v[96:99], a[112:115]\n\t" \
  "v_mfma_f32_16x16x32_bf16 a[112:115], v[84:87], v[104:107], a[112:115]\n\t" \
  "v_mfma_f32_16x16x32_bf16 a[116:119], v[88:91], v[96:99], a[116:119]\n\t" \
  "v_mfma_f32_16x16x32_bf16 a[116:119], v[92:95], v[104:107], a[116:119]\n\t" \
  "v_mfma_f32_16x16x32_bf16 a[120:123], v[112:115], v[96:99], a[120:123]\n\t" \
  "v_mfma_f32_16x16x32_bf16 a[120:123], v[116:119], v[104:107], a[120:123]\n\t" \
  "v_mfma_f32_16x16x32_bf16 a[124:127], v[120:123], v[96:99], a[124:127]\n\t" \
  "v_mfma_f32_16x16x32_bf16 a[124:127], v[124:127], v[104:107], a[124:127]\n\t" \
  "s_nop 7\n\t" \
  "s_waitcnt vmcnt(0)\n\t" \
  "s_barrier\n\t" \
  "s_add_u32 s77, s51, 131072\n\t" \
  "v_add_u32 v248, 14336, v240\n\t" \
  "v_add_u32 v249, 21504, v241\n\t" \
  "v_add_u32 v241, 7168, v241\n\t" \
  "s_mov_b32 s89, 0\n\t" \
  "v_lshlrev_b32 v212, 7, v233\n\t" \
  "s_waitcnt vmcnt(0) lgkmcnt(0)\n\t" \
  "s_barrier\n\t" \
  "v_cmp_gt_u32 vcc, 16, v233\n\t" \
  "v_bfrev_b32 v214, 1\n\t" \
  "s_waitcnt vmcnt(0) lgkmcnt(0)\n\t" \
  "s_barrier\n\t" \
  "v_cndmask_b32 v212, v214, v212, vcc\n\t" \
  "s_waitcnt vmcnt(0) lgkmcnt(0)\n\t" \
  "s_barrier\n\t" \
  "s_add_u32 m0, s38, 16384\n\t" \
  "s_mov_b32 s96, 0\n\t" \
  "buffer_load_dwordx4 v232, s[68:71], s96 offen offset:0 lds\n\t" \
  "s_mov_b32 s96, 6144\n\t" \
  "buffer_load_dwordx4 v232, s[68:71], s96 offen offset:1024 lds\n\t" \
  "s_mov_b32 s96, 12288\n\t" \
  "buffer_load_dwordx4 v232, s[68:71], s96 offen offset:2048 lds\n\t" \
  "s_mov_b32 s96, 18432\n\t" \
  "buffer_load_dwordx4 v232, s[68:71], s96 offen offset:3072 lds\n\t" \
  "s_waitcnt vmcnt(0) lgkmcnt(0)\n\t" \
  "s_barrier\n\t" \
  "s_add_u32 m0, s38, 32768\n\t" \
  "s_mov_b32 s96, 28672\n\t" \
  "buffer_load_dwordx4 v232, s[68:71], s96 offen offset:0 lds\n\t" \
  "s_mov_b32 s96, 34816\n\t" \
  "buffer_load_dwordx4 v232, s[68:71], s96 offen offset:1024 lds\n\t" \
  "s_mov_b32 s96, 40960\n\t" \
  "buffer_load_dwordx4 v232, s[68:71], s96 offen offset:2048 lds\n\t" \
  "s_mov_b32 s96, 47104\n\t" \
  "buffer_load_dwordx4 v232, s[68:71], s96 offen offset:3072 lds\n\t" \
  "s_add_u32 m0, s38, 114688\n\t" \
  "s_mov_b32 s96, 0\n\t" \
  "buffer_load_dwordx4 v232, s[72:75], s96 offen offset:0 lds\n\t" \
  "s_mov_b32 s96, 6144\n\t" \
  "buffer_load_dwordx4 v232, s[72:75], s96 offen offset:1024 lds\n\t" \
  "s_mov_b32 s96, 12288\n\t" \
  "buffer_load_dwordx4 v232, s[72:75], s96 offen offset:2048 lds\n\t" \
  "s_mov_b32 s96, 18432\n\t" \
  "buffer_load_dwordx4 v232, s[72:75], s96 offen offset:3072 lds\n\t" \
  "s_waitcnt vmcnt(0) lgkmcnt(0)\n\t" \
  "s_barrier\n\t" \
  "s_add_u32 m0, s38, 131072\n\t" \
  "s_mov_b32 s96, 28672\n\t" \
  "buffer_load_dwordx4 v232, s[72:75], s96 offen offset:0 lds\n\t" \
  "s_mov_b32 s96, 34816\n\t" \
  "buffer_load_dwordx4 v232, s[72:75], s96 offen offset:1024 lds\n\t" \
  "s_mov_b32 s96, 40960\n\t" \
  "buffer_load_dwordx4 v232, s[72:75], s96 offen offset:2048 lds\n\t" \
  "s_mov_b32 s96, 47104\n\t" \
  "buffer_load_dwordx4 v232, s[72:75], s96 offen offset:3072 lds\n\t" \
  "s_lshr_b32 s78, s38, 6\n\t" \
  "v_xor_b32 v234, s78, v244\n\t" \
  "v_xor_b32 v235, 32, v234\n\t" \
  "v_add_u32 v236, 65536, v234\n\t" \
  "v_add_u32 v237, 65536, v235\n\t" \
  "v_add_u32 v238, 131072, v234\n\t" \
  "v_add_u32 v239, 131072, v235\n\t" \
  "v_lshlrev_b32 v242, 4, v233\n\t" \
  "v_add_u32 v213, s38, v242\n\t" \
  "v_add_u32 v215, 98304, v213\n\t" \
  "v_mov_b32 v243, s36\n\t" \
  "v_lshlrev_b32 v254, 9, v233\n\t" \
  "v_and_b32 v244, 0x38, v233\n\t" \
  "v_lshl_or_b32 v254, v244, 26, v254\n\t" \
  "s_brev_b32 s78, 1\n\t" \
  "v_mov_b32 v244, s78\n\t" \
  "v_cmp_eq_u32 vcc, s39, v233\n\t" \
  "v_add_u32 v245, s39, v233\n\t" \
  "v_bfi_b32 v245, 7, v245, v233\n\t" \
  "v_cndmask_b32 v254, v254, v244, vcc\n\t" \
  "v_and_b32 v245, 31, v245\n\t" \
  "s_waitcnt vmcnt(0) lgkmcnt(0)\n\t" \
  "s_barrier\n\t" \
  "v_lshlrev_b32 v245, 2, v245\n\t" \
  "v_writelane_b32 v244, 0, 0\n\t" \
  "s_waitcnt vmcnt(0) lgkmcnt(0)\n\t" \
  "s_barrier\n\t" \
  "ds_read_b128 v[192:195], v215 offset:32768\n\t" \
  "ds_read_b128 v[196:199], v215 offset:33792\n\t" \
  "ds_read_b128 v[200:203], v215 offset:34816\n\t" \
  "ds_read_b128 v[204:207], v215 offset:35840\n\t" \
  "s_add_u32 s96, s40, 524288\n\t" \
  "s_and_b32 s96, s96, 0x1c0000\n\t" \
  "s_add_u32 s96, s96, s51\n\t" \
  "s_add_u32 m0, s38, 131072\n\t" \
  "s_waitcnt vmcnt(0) lgkmcnt(0)\n\t" \
  "buffer_load_dwordx4 v240, s[24:27], s96 offen offset:0 lds\n\t" \
  "buffer_load_dwordx4 v241, s[24:27], s96 offen offset:1024 lds\n\t" \
  "buffer_load_dwordx4 v248, s[24:27], s96 offen offset:2048 lds\n\t" \
  "buffer_load_dwordx4 v249, s[24:27], s96 offen offset:3072 lds\n\t" \
  "v_lshrrev_b32 v250, 1, v233\n\t" \
  "v_lshlrev_b32 v250, 2, v250\n\t" \
  "v_add_u32 v250, 8192, v250\n\t" \
  "buffer_load_dword v209, v250, s[16:19], 0 offen sc1\n\t" \
  "s_add_u32 s96, s40, 0\n\t" \
  "s_and_b32 s96, s96, 0x1c0000\n\t" \
  "s_add_u32 s96, s96, s51\n\t" \
  "s_add_u32 m0, s38, 0\n\t" \
  "s_nop 0\n\t" \
  "buffer_load_dwordx4 v240, s[24:27], s96 offen offset:0 lds\n\t" \
  "buffer_load_dwordx4 v241, s[24:27], s96 offen offset:1024 lds\n\t" \
  "buffer_load_dwordx4 v248, s[24:27], s96 offen offset:2048 lds\n\t" \
  "buffer_load_dwordx4 v249, s[24:27], s96 offen offset:3072 lds\n\t" \
  "s_waitcnt vmcnt(0) lgkmcnt(0)\n\t" \
  "s_barrier\n\t" \
  "s_add_u32 s96, s40, 0\n\t" \
  "s_and_b32 s96, s96, 0x1c0000\n\t" \
  "s_add_u32 s96, s96, s77\n\t" \
  "s_add_u32 m0, s38, 98304\n\t" \
  "s_nop 0\n\t" \
  "buffer_load_dwordx4 v240, s[24:27], s96 offen offset:0 lds\n\t" \
  "buffer_load_dwordx4 v241, s[24:27], s96 offen offset:1024 lds\n\t" \
  "s_waitcnt vmcnt(0) lgkmcnt(0)\n\t" \
  "v_and_b32 v251, 0xbfffbfff, v209\n\t" \
  "v_cmp_ne_u32_e64 s[84:85], s36, v251\n\t" \
  "s_cmp_lg_u64 s[84:85], 0\n\t" \
  "s_cbranch_scc1 .Lsf_v\n\t" \
  ".Lsf_w:\n\t" \
  "v_and_b32 v251, 1, v233\n\t" \
  "v_lshlrev_b32 v251, 4, v251\n\t" \
  "buffer_load_dwordx4 v248, s[24:27], s96 offen offset:2048 lds\n\t" \
  "v_lshrrev_b32 v251, v251, v209\n\t" \
  "v_and_b32 v251, 0x4000, v251\n\t" \
  "v_cmp_ne_u32_e64 s[84:85], 0, v251\n\t" \
  "s_bcnt1_i32_b32 s98, s84\n\t" \
  "s_bcnt1_i32_b32 s54, s85\n\t" \
  "buffer_load_dwordx4 v249, s[24:27], s96 offen offset:3072 lds\n\t" \
  "s_cmp_gt_u32 s98, s54\n\t" \
  "s_cselect_b32 s55, 0x2000, 0\n\t" \
  "s_cselect_b32 s98, s84, s85\n\t" \
  "s_add_u32 s96, s40, 262144\n\t" \
  "s_and_b32 s96, s96, 0x1c0000\n\t" \
  "s_add_u32 s96, s96, s51\n\t" \
  "s_add_u32 m0, s38, 49152\n\t" \
  "s_cmp_lg_u32 s55, 0\n\t" \
  "s_cselect_b32 s84, s85, s84\n\t" \
  "s_lshr_b32 s85, s98, 16\n\t" \
  "s_bcnt1_i32_b32 s85, s85\n\t" \
  "s_lshr_b32 s84, s84, 16\n\t" \
  "buffer_load_dwordx4 v240, s[24:27], s96 offen offset:0 lds\n\t" \
  "s_bcnt1_i32_b32 s84, s84\n\t" \
  "s_add_u32 s84, s84, 7\n\t" \
  "s_cmp_ge_u32 s85, s84\n\t" \
  "s_cselect_b32 s54, -1, 0\n\t" \
  "s_xor_b32 s98, s42, s55\n\t" \
  "s_and_b32 s98, s98, s54\n\t" \
  "s_xor_b32 s45, s45, s98\n\t" \
  "s_xor_b32 s46, s46, s98\n\t" \
  "buffer_load_dwordx4 v241, s[24:27], s96 offen offset:1024 lds\n\t" \
  "s_xor_b32 s98, s43, s55\n\t" \
  "s_and_b32 s98, s98, s54\n\t" \
  "s_xor_b32 s47, s47, s98\n\t" \
  "s_xor_b32 s48, s48, s55\n\t" \
  "s_xor_b32 s49, s49, s55\n\t" \
  "buffer_load_dwordx4 v248, s[24:27], s96 offen offset:2048 lds\n\t" \
  "s_xor_b32 s98, s45, 0\n\t" \
  "s_lshl_b32 s64, s39, 17\n\t" \
  "s_bfe_u32 s70, s46, 0x20014\n\t" \
  "buffer_load_dwordx4 v249, s[24:27], s96 offen offset:3072 lds\n\t" \
  "s_lshl_b32 s70, s70, 14\n\t" \
  "s_xor_b32 s67, s70, 49152\n\t" \
  "s_lshl1_add_u32 s60, s67, s46\n\t" \
  "s_xor_b32 s68, s70, 32768\n\t" \
  "s_lshl1_add_u32 s61, s68, s46\n\t" \
  "s_xor_b32 s69, s70, 16384\n\t" \
  "s_lshl1_add_u32 s62, s69, s46\n\t" \
  "s_lshl1_add_u32 s63, s70, s46\n\t" \
  "s_mul_i32 s65, s39, 508\n\t" \
  "s_add_u32 s65, s65, s49\n\t" \
  "s_lshl_b32 s80, s39, 2\n\t" \
  "s_add_u32 s80, s80, s3\n\t" \
  "s_waitcnt vmcnt(0) lgkmcnt(0)\n\t" \
  "s_barrier\n\t" \
  "s_lshl_b32 s80, s80, 6\n\t" \
  "s_add_u32 s76, s38, 147456\n\t" \
  "s_lshl_b32 s75, s80, 2\n\t" \
  "s_add_u32 s75, s75, 2228224\n\t" \
  "s_add_u32 s96, s40, 262144\n\t" \
  "s_and_b32 s96, s96, 0x1c0000\n\t" \
  "s_add_u32 s96, s96, s77\n\t" \
  "s_add_u32 m0, s38, 65536\n\t" \
  "s_nop 0\n\t" \
  "buffer_load_dwordx4 v240, s[24:27], s96 offen offset:0 lds\n\t" \
  "s_lshr_b32 s71, s67, 7\n\t" \
  "s_add_u32 s71, s71, s48\n\t" \
  "s_lshr_b32 s72, s68, 7\n\t" \
  "s_add_u32 s72, s72, s48\n\t" \
  "s_lshr_b32 s66, s69, 7\n\t" \
  "s_add_u32 s66, s66, s48\n\t" \
  "s_lshr_b32 s73, s70, 7\n\t" \
  "s_add_u32 s73, s73, s48\n\t" \
  "s_lshl3_add_u32 s67, s67, s47\n\t" \
  "buffer_load_dwordx4 v241, s[24:27], s96 offen offset:1024 lds\n\t" \
  "s_lshl3_add_u32 s68, s68, s47\n\t" \
  "s_add_u32 s68, s68, 4096\n\t" \
  "s_lshl3_add_u32 s69, s69, s47\n\t" \
  "s_add_u32 s69, s69, 16384\n\t" \
  "s_lshl3_add_u32 s70, s70, s47\n\t" \
  "buffer_load_dwordx4 v248, s[24:27], s96 offen offset:2048 lds\n\t" \
  "buffer_load_dwordx4 v249, s[24:27], s96 offen offset:3072 lds\n\t" \
  "s_add_u32 s96, s40, 524288\n\t" \
  "s_and_b32 s96, s96, 0x1c0000\n\t" \
  "s_add_u32 s96, s96, s77\n\t" \
  "s_add_u32 m0, s38, 81920\n\t" \
  "s_waitcnt vmcnt(0) lgkmcnt(0)\n\t" \
  "s_barrier\n\t" \
  "buffer_load_dwordx4 v240, s[24:27], s96 offen offset:0 lds\n\t" \
  "ds_read_b128 v[176:179], v215 offset:16384\n\t" \
  "ds_read_b128 v[180:183], v215 offset:17408\n\t" \
  "ds_read_b128 v[184:187], v215 offset:18432\n\t" \
  "buffer_load_dwordx4 v241, s[24:27], s96 offen offset:1024 lds\n\t" \
  "ds_read_b128 v[188:191], v215 offset:19456\n\t" \
  "buffer_load_dwordx4 v248, s[24:27], s96 offen offset:2048 lds\n\t" \
  "buffer_load_dwordx4 v249, s[24:27], s96 offen offset:3072 lds\n\t" \
  "s_add_u32 s96, s40, 786432\n\t" \
  "s_and_b32 s96, s96, 0x1c0000\n\t" \
  "s_add_u32 s96, s96, s51\n\t" \
  "s_add_u32 m0, s38, 114688\n\t" \
  "s_waitcnt vmcnt(0) lgkmcnt(0)\n\t" \
  "buffer_load_dwordx4 v240, s[24:27], s96 offen offset:0 lds\n\t" \
  "v_add_u32 v230, 0x10000, v242\n\t" \
  "v_add_u32 v232, 0x20000, v242\n\t" \
  "buffer_load_dwordx4 v241, s[24:27], s96 offen offset:1024 lds\n\t" \
  "buffer_load_dwordx4 v248, s[24:27], s96 offen offset:2048 lds\n\t" \
  "buffer_load_dwordx4 v249, s[24:27], s96 offen offset:3072 lds\n\t" \
  "ds_read_b64_tr_b16 a[192:193], v234 offset:0\n\t" \
  "ds_read_b64_tr_b16 a[194:195], v234 offset:4096\n\t" \
  "ds_read_b64_tr_b16 a[196:197], v235 offset:0\n\t" \
  "ds_read_b64_tr_b16 a[198:199], v235 offset:4096\n\t" \
  "v_rcp_f32 v224, v160\n\t" \
  "v_accvgpr_read_b32 v128, a0\n\t" \
  "v_accvgpr_read_b32 v129, a1\n\t" \
  "v_accvgpr_read_b32 v130, a2\n\t" \
  "v_accvgpr_read_b32 v131, a3\n\t" \
  "v_accvgpr_read_b32 v132, a4\n\t" \
  "v_accvgpr_read_b32 v133, a5\n\t" \
  "v_accvgpr_read_b32 v134, a6\n\t" \
  "v_accvgpr_read_b32 v135, a7\n\t" \
  "v_pk_mul_f32 v[128:129], v[128:129], v[224:225] op_sel_hi:[1,0]\n\t" \
  "v_pk_mul_f32 v[130:131], v[130:131], v[224:225] op_sel_hi:[1,0]\n\t" \
  "v_pk_mul_f32 v[132:133], v[132:133], v[224:225] op_sel_hi:[1,0]\n\t" \
  "v_pk_mul_f32 v[134:135], v[134:135], v[224:225] op_sel_hi:[1,0]\n\t" \
  "v_cvt_pk_bf16_f32 v128, v128, v129\n\t" \
  "v_cvt_pk_bf16_f32 v129, v130, v131\n\t" \
  "v_cvt_pk_bf16_f32 v130, v132, v133\n\t" \
  "v_cvt_pk_bf16_f32 v131, v134, v135\n\t" \
  "buffer_store_dwordx4 v[128:131], v242, s[16:19], s98 offen offset:0 sc1\n\t" \
  "ds_read_b64_tr_b16 a[200:201], v234 offset:8192\n\t" \
  "ds_read_b64_tr_b16 a[202:203], v234 offset:12288\n\t" \
  "v_accvgpr_read_b32 v136, a8\n\t" \
  "v_accvgpr_read_b32 v137, a9\n\t" \
  "v_accvgpr_read_b32 v138, a10\n\t" \
  "v_accvgpr_read_b32 v139, a11\n\t" \
  "v_accvgpr_read_b32 v140, a12\n\t" \
  "v_accvgpr_read_b32 v141, a13\n\t" \
  "v_accvgpr_read_b32 v142, a14\n\t" \
  "v_accvgpr_read_b32 v143, a15\n\t" \
  "v_pk_mul_f32 v[136:137], v[136:137], v[224:225] op_sel_hi:[1,0]\n\t" \
  "v_pk_mul_f32 v[138:139], v[138:139], v[224:225] op_sel_hi:[1,0]\n\t" \
  "v_pk_mul_f32 v[140:141], v[140:141], v[224:225] op_sel_hi:[1,0]\n\t" \
  "v_pk_mul_f32 v[142:143], v[142:143], v[224:225] op_sel_hi:[1,0]\n\t" \
  "v_cvt_pk_bf16_f32 v136, v136, v137\n\t" \
  "v_cvt_pk_bf16_f32 v137, v138, v139\n\t" \
  "v_cvt_pk_bf16_f32 v138, v140, v141\n\t" \
  "v_cvt_pk_bf16_f32 v139, v142, v143\n\t" \
  "buffer_store_dwordx4 v[136:139], v242, s[16:19], s98 offen offset:1024 sc1\n\t" \
  "ds_read_b64_tr_b16 a[204:205], v235 offset:8192\n\t" \
  "ds_read_b128 a[224:227], v213 offset:16384\n\t" \
  "ds_read_b64_tr_b16 a[206:207], v235 offset:12288\n\t" \
  "ds_read_b128 a[228:231], v213 offset:17408\n\t" \
  "ds_read_b128 a[232:235], v213 offset:18432\n\t" \
  "ds_read_b128 a[236:239], v213 offset:19456\n\t" \
  "ds_write_b128 v213, v[128:131] offset:16384\n\t" \
  "s_waitcnt vmcnt(0) lgkmcnt(0)\n\t" \
  "s_barrier\n\t" \
  "ds_write_b128 v213, v[136:139] offset:17408\n\t" \
  "v_accvgpr_read_b32 v0, a16\n\t" \
  "v_accvgpr_read_b32 v1, a17\n\t" \
  "v_accvgpr_read_b32 v2, a18\n\t" \
  "v_accvgpr_read_b32 v3, a19\n\t" \
  "v_accvgpr_read_b32 v4, a20\n\t" \
  "v_accvgpr_read_b32 v5, a21\n\t" \
  "v_accvgpr_read_b32 v6, a22\n\t" \
  "v_accvgpr_read_b32 v7, a23\n\t" \
  "v_pk_mul_f32 v[0:1], v[0:1], v[224:225] op_sel_hi:[1,0]\n\t" \
  "v_pk_mul_f32 v[2:3], v[2:3], v[224:225] op_sel_hi:[1,0]\n\t" \
  "v_pk_mul_f32 v[4:5], v[4:5], v[224:225] op_sel_hi:[1,0]\n\t" \
  "v_pk_mul_f32 v[6:7], v[6:7], v[224:225] op_sel_hi:[1,0]\n\t" \
  "v_cvt_pk_bf16_f32 v0, v0, v1\n\t" \
  "v_cvt_pk_bf16_f32 v1, v2, v3\n\t" \
  "v_cvt_pk_bf16_f32 v2, v4, v5\n\t" \
  "v_cvt_pk_bf16_f32 v3, v6, v7\n\t" \
  "buffer_store_dwordx4 v[0:3], v242, s[16:19], s98 offen offset:2048 sc1\n\t" \
  "ds_write_b128 v213, v[0:3] offset:18432\n\t" \
  "v_accvgpr_read_b32 v8, a24\n\t" \
  "v_accvgpr_read_b32 v9, a25\n\t" \
  "v_accvgpr_read_b32 v10, a26\n\t" \
  "v_accvgpr_read_b32 v11, a27\n\t" \
  "v_accvgpr_read_b32 v12, a28\n\t" \
  "v_accvgpr_read_b32 v13, a29\n\t" \
  "v_accvgpr_read_b32 v14, a30\n\t" \
  "v_accvgpr_read_b32 v15, a31\n\t" \
  "v_pk_mul_f32 v[8:9], v[8:9], v[224:225] op_sel_hi:[1,0]\n\t" \
  "v_pk_mul_f32 v[10:11], v[10:11], v[224:225] op_sel_hi:[1,0]\n\t" \
  "v_pk_mul_f32 v[12:13], v[12:13], v[224:225] op_sel_hi:[1,0]\n\t" \
  "v_pk_mul_f32 v[14:15], v[14:15], v[224:225] op_sel_hi:[1,0]\n\t" \
  "v_cvt_pk_bf16_f32 v8, v8, v9\n\t" \
  "v_cvt_pk_bf16_f32 v9, v10, v11\n\t" \
  "v_cvt_pk_bf16_f32 v10, v12, v13\n\t" \
  "v_cvt_pk_bf16_f32 v11, v14, v15\n\t" \
  "buffer_store_dwordx4 v[8:11], v242, s[16:19], s98 offen offset:3072 sc1\n\t" \
  "ds_write_b128 v213, v[8:11] offset:19456\n\t" \
  "s_xor_b32 s98, s45, 32768\n\t" \
  "ds_read_b64_tr_b16 a[208:209], v236 offset:32768\n\t" \
  "ds_read_b64_tr_b16 a[210:211], v236 offset:36864\n\t" \
  "ds_read_b64_tr_b16 a[212:213], v237 offset:32768\n\t" \
  "ds_read_b64_tr_b16 a[214:215], v237 offset:36864\n\t" \
  "s_add_u32 s96, s40, 786432\n\t" \
  "s_and_b32 s96, s96, 0x1c0000\n\t" \
  "ds_read_b64_tr_b16 a[216:217], v236 offset:40960\n\t" \
  "s_add_u32 s96, s96, s77\n\t" \
  "s_add_u32 m0, s38, 147456\n\t" \
  "ds_read_b64_tr_b16 a[218:219], v236 offset:45056\n\t" \
  "v_rcp_f32 v226, v164\n\t" \
  "buffer_load_dwordx4 v240, s[24:27], s96 offen offset:0 lds\n\t" \
  "v_accvgpr_read_b32 v0, a32\n\t" \
  "v_accvgpr_read_b32 v1, a33\n\t" \
  "v_accvgpr_read_b32 v2, a34\n\t" \
  "v_accvgpr_read_b32 v3, a35\n\t" \
  "buffer_load_dwordx4 v241, s[24:27], s96 offen offset:1024 lds\n\t" \
  "v_accvgpr_read_b32 v4, a36\n\t" \
  "v_accvgpr_read_b32 v5, a37\n\t" \
  "ds_read_b64_tr_b16 a[220:221], v237 offset:40960\n\t" \
  "v_accvgpr_read_b32 v6, a38\n\t" \
  "v_accvgpr_read_b32 v7, a39\n\t" \
  "buffer_load_dwordx4 v248, s[24:27], s96 offen offset:2048 lds\n\t" \
  "ds_read_b64_tr_b16 a[222:223], v237 offset:45056\n\t" \
  "v_mul_f32 v0, v0, v226\n\t" \
  "v_mul_f32 v1, v1, v226\n\t" \
  "v_mul_f32 v2, v2, v226\n\t" \
  "v_mul_f32 v3, v3, v226\n\t" \
  "buffer_load_dwordx4 v249, s[24:27], s96 offen offset:3072 lds\n\t" \
  "v_mul_f32 v4, v4, v226\n\t" \
  "v_mul_f32 v5, v5, v226\n\t" \
  "v_mul_f32 v6, v6, v226\n\t" \
  "v_mul_f32 v7, v7, v226\n\t" \
  "v_cvt_pk_bf16_f32 v32, v0, v1\n\t" \
  "v_cvt_pk_bf16_f32 v33, v2, v3\n\t" \
  "v_cvt_pk_bf16_f32 v34, v4, v5\n\t" \
  "v_cvt_pk_bf16_f32 v35, v6, v7\n\t" \
  "v_accvgpr_read_b32 v8, a40\n\t" \
  "v_accvgpr_read_b32 v9, a41\n\t" \
  "v_accvgpr_read_b32 v10, a42\n\t" \
  "v_accvgpr_read_b32 v11, a43\n\t" \
  "v_accvgpr_read_b32 v12, a44\n\t" \
  "v_accvgpr_read_b32 v13, a45\n\t" \
  "v_accvgpr_read_b32 v14, a46\n\t" \
  "v_accvgpr_read_b32 v15, a47\n\t" \
  "v_mul_f32 v8, v8, v226\n\t" \
  "v_mul_f32 v9, v9, v226\n\t" \
  "v_mul_f32 v10, v10, v226\n\t" \
  "v_mul_f32 v11, v11, v226\n\t" \
  "v_mul_f32 v12, v12, v226\n\t" \
  "v_mul_f32 v13, v13, v226\n\t" \
  "v_mul_f32 v14, v14, v226\n\t" \
  "v_mul_f32 v15, v15, v226\n\t" \
  "v_cvt_pk_bf16_f32 v36, v8, v9\n\t" \
  "v_cvt_pk_bf16_f32 v37, v10, v11\n\t" \
  "v_cvt_pk_bf16_f32 v38, v12, v13\n\t" \
  "v_cvt_pk_bf16_f32 v39, v14, v15\n\t" \
  "v_accvgpr_read_b32 v0, a48\n\t" \
  "v_accvgpr_read_b32 v1, a49\n\t" \
  "s_waitcnt vmcnt(0) lgkmcnt(0)\n\t" \
  "s_barrier\n\t" \
  "buffer_store_dwordx4 v[32:35], v242, s[16:19], s98 offen offset:0 sc1\n\t" \
  "v_accvgpr_read_b32 v2, a50\n\t" \
  "v_accvgpr_read_b32 v3, a51\n\t" \
  "v_accvgpr_read_b32 v4, a52\n\t" \
  "s_add_u32 s96, s40, 1048576\n\t" \
  "s_and_b32 s96, s96, 0x1c0000\n\t" \
  "s_add_u32 s96, s96, s51\n\t" \
  "v_accvgpr_read_b32 v5, a53\n\t" \
  "s_add_u32 m0, s38, 0\n\t" \
  "v_accvgpr_read_b32 v6, a54\n\t" \
  "v_accvgpr_read_b32 v7, a55\n\t" \
  "v_mul_f32 v0, v0, v226\n\t" \
  "buffer_load_dwordx4 v240, s[24:27], s96 offen offset:0 lds\n\t" \
  "v_mul_f32 v1, v1, v226\n\t" \
  "v_mul_f32 v2, v2, v226\n\t" \
  "v_mul_f32 v3, v3, v226\n\t" \
  "v_mul_f32 v4, v4, v226\n\t" \
  "v_mul_f32 v5, v5, v226\n\t" \
  "v_mul_f32 v6, v6, v226\n\t" \
  "v_mul_f32 v7, v7, v226\n\t" \
  "buffer_load_dwordx4 v241, s[24:27], s96 offen offset:1024 lds\n\t" \
  "v_cvt_pk_bf16_f32 v40, v0, v1\n\t" \
  "v_cvt_pk_bf16_f32 v41, v2, v3\n\t" \
  "v_cvt_pk_bf16_f32 v42, v4, v5\n\t" \
  "v_cvt_pk_bf16_f32 v43, v6, v7\n\t" \
  "v_accvgpr_read_b32 v8, a56\n\t" \
  "v_accvgpr_read_b32 v9, a57\n\t" \
  "v_accvgpr_read_b32 v10, a58\n\t" \
  "v_accvgpr_read_b32 v11, a59\n\t" \
  "v_accvgpr_read_b32 v12, a60\n\t" \
  "buffer_load_dwordx4 v248, s[24:27], s96 offen offset:2048 lds\n\t" \
  "v_accvgpr_read_b32 v13, a61\n\t" \
  "v_accvgpr_read_b32 v14, a62\n\t" \
  "v_accvgpr_read_b32 v15, a63\n\t" \
  "v_mul_f32 v8, v8, v226\n\t" \
  "v_mul_f32 v9, v9, v226\n\t" \
  "v_mul_f32 v10, v10, v226\n\t" \
  "v_mul_f32 v11, v11, v226\n\t" \
  "v_mul_f32 v12, v12, v226\n\t" \
  "buffer_store_dwordx4 v[36:39], v242, s[16:19], s98 offen offset:1024 sc1\n\t" \
  "v_mul_f32 v13, v13, v226\n\t" \
  "v_mul_f32 v14, v14, v226\n\t" \
  "v_mul_f32 v15, v15, v226\n\t" \
  "v_cvt_pk_bf16_f32 v44, v8, v9\n\t" \
  "v_cvt_pk_bf16_f32 v45, v10, v11\n\t" \
  "v_rcp_f32 v220, v168\n\t" \
  "buffer_load_dwordx4 v249, s[24:27], s96 offen offset:3072 lds\n\t" \
  "ds_read_b64_tr_b16 a[128:129], v234 offset:49152\n\t" \
  "v_cvt_pk_bf16_f32 v46, v12, v13\n\t" \
  "ds_read_b64_tr_b16 a[130:131], v234 offset:53248\n\t" \
  "v_cvt_pk_bf16_f32 v47, v14, v15\n\t" \
  "ds_read_b64_tr_b16 a[132:133], v235 offset:49152\n\t" \
  "ds_read_b128 a[240:243], v213 offset:32768\n\t" \
  "s_waitcnt vmcnt(0) lgkmcnt(0)\n\t" \
  "buffer_store_dword v243, v254, s[16:19], s49 offen offset:0 sc1\n\t" \
  "ds_read_b64_tr_b16 a[134:135], v235 offset:53248\n\t" \
  "ds_read_b128 a[244:247], v213 offset:33792\n\t" \
  "buffer_store_dwordx4 v[40:43], v242, s[16:19], s98 offen offset:2048 sc1\n\t" \
  "v_accvgpr_read_b32 v64, a64\n\t" \
  "v_accvgpr_read_b32 v65, a65\n\t" \
  "v_accvgpr_read_b32 v66, a66\n\t" \
  "v_accvgpr_read_b32 v67, a67\n\t" \
  "v_accvgpr_read_b32 v68, a68\n\t" \
  "s_waitcnt vmcnt(0) lgkmcnt(0)\n\t" \
  "s_barrier\n\t" \
  "ds_read_b128 v[128:131], v242 offset:16384\n\t" \
  "v_accvgpr_read_b32 v69, a69\n\t" \
  "v_accvgpr_read_b32 v70, a70\n\t" \
  "v_accvgpr_read_b32 v71, a71\n\t" \
  "buffer_store_dwordx4 v[44:47], v242, s[16:19], s98 offen offset:3072 sc1\n\t" \
  "v_mul_f32 v64, v64, v220\n\t" \
  "ds_read_b128 v[132:135], v242 offset:17408\n\t" \
  "v_mul_f32 v65, v65, v220\n\t" \
  "s_xor_b32 s98, s45, 65536\n\t" \
  "v_rcp_f32 v222, v172\n\t" \
  "ds_read_b128 v[136:139], v242 offset:18432\n\t" \
  "ds_read_b128 v[140:143], v242 offset:19456\n\t" \
  "v_mul_f32 v66, v66, v220\n\t" \
  "ds_read_b128 v[144:147], v242 offset:20480\n\t" \
  "v_mul_f32 v67, v67, v220\n\t" \
  "ds_read_b64_tr_b16 a[136:137], v234 offset:57344\n\t" \
  "v_mul_f32 v68, v68, v220\n\t" \
  "ds_read_b128 v[148:151], v242 offset:21504\n\t" \
  "v_mul_f32 v69, v69, v220\n\t" \
  "ds_read_b64_tr_b16 a[138:139], v234 offset:61440\n\t" \
  "v_mul_f32 v70, v70, v220\n\t" \
  "ds_read_b128 v[152:155], v242 offset:22528\n\t" \
  "v_mul_f32 v71, v71, v220\n\t" \
  "ds_read_b128 v[156:159], v242 offset:23552\n\t" \
  "ds_read_b64_tr_b16 a[140:141], v235 offset:57344\n\t" \
  "ds_read_b64_tr_b16 a[142:143], v235 offset:61440\n\t" \
  "ds_read_b128 a[248:251], v213 offset:34816\n\t" \
  "s_waitcnt vmcnt(0) lgkmcnt(0)\n\t" \
  "s_nop 1\n\t" \
  "s_branch .Lat_join\n\t" \
  ".Lsf_c00:\n\t" \
  "s_mov_b32 vcc_lo, 0\n\t" \
  "s_branch .Lsf_s0\n\t" \
  ".Lsf_c10:\n\t" \
  "s_mov_b32 vcc_lo, 1\n\t" \
  "s_branch .Lsf_s0\n\t" \
  ".Lsf_c20:\n\t" \
  "s_mov_b32 vcc_lo, 2\n\t" \
  "s_branch .Lsf_s0\n\t" \
  ".Lsf_c30:\n\t" \
  "s_mov_b32 vcc_lo, 3\n\t" \
  "s_branch .Lsf_s0\n\t" \
  ".Lsf_c40:\n\t" \
  "s_mov_b32 vcc_lo, 4\n\t" \
  "s_branch .Lsf_s0\n\t" \
  ".Lsf_c50:\n\t" \
  "s_mov_b32 vcc_lo, 5\n\t" \
  "s_branch .Lsf_s0\n\t" \
  ".Lsf_c60:\n\t" \
  "s_mov_b32 vcc_lo, 6\n\t" \
  "s_branch .Lsf_s0\n\t" \
  ".Lsf_c70:\n\t" \
  "s_mov_b32 vcc_lo, 7\n\t" \
  "s_branch .Lsf_s0\n\t" \
  ".Lsf_s0:\n\t" \
  "s_nop 7\n\t" \
  "s_nop 1\n\t" \
  "v_mov_b32 v193, v192\n\t" \
  "s_nop 1\n\t" \
  "v_permlane32_swap_b32 v192, v193\n\t" \
  "s_nop 1\n\t" \
  "v_max_f32 v192, v192, v193\n\t" \
  "s_nop 1\n\t" \
  "v_mov_b32 v193, v192\n\t" \
  "s_nop 1\n\t" \
  "v_permlane16_swap_b32 v192, v193\n\t" \
  "s_nop 1\n\t" \
  "v_max_f32 v192, v192, v193\n\t" \
  "v_max_f32 v192, 0, v192\n\t" \
  "v_sub_f32 v0, v0, v192\n\t" \
  "v_sub_f32 v1, v1, v192\n\t" \
  "v_sub_f32 v2, v2, v192\n\t" \
  "v_sub_f32 v3, v3, v192\n\t" \
  "v_sub_f32 v4, v4, v192\n\t" \
  "v_sub_f32 v5, v5, v192\n\t" \
  "v_sub_f32 v6, v6, v192\n\t" \
  "v_sub_f32 v7, v7, v192\n\t" \
  "v_sub_f32 v8, v8, v192\n\t" \
  "v_sub_f32 v9, v9, v192\n\t" \
  "v_sub_f32 v10, v10, v192\n\t" \
  "v_sub_f32 v11, v11, v192\n\t" \
  "v_sub_f32 v12, v12, v192\n\t" \
  "v_sub_f32 v13, v13, v192\n\t" \
  "v_sub_f32 v14, v14, v192\n\t" \
  "v_sub_f32 v15, v15, v192\n\t" \
  "v_sub_f32 v176, v176, v192\n\t" \
  "v_sub_f32 v177, v177, v192\n\t" \
  "v_sub_f32 v178, v178, v192\n\t" \
  "v_sub_f32 v179, v179, v192\n\t" \
  "v_exp_f32 v196, -v192\n\t" \
  "s_mov_b32 s99, 1\n\t" \
  "s_nop 1\n\t" \
  "s_cmp_eq_u32 vcc_lo, 0\n\t" \
  "s_cbranch_scc1 .Lsf_r00\n\t" \
  "s_cmp_eq_u32 vcc_lo, 1\n\t" \
  "s_cbranch_scc1 .Lsf_r10\n\t" \
  "s_cmp_eq_u32 vcc_lo, 2\n\t" \
  "s_cbranch_scc1 .Lsf_r20\n\t" \
  "s_cmp_eq_u32 vcc_lo, 3\n\t" \
  "s_cbranch_scc1 .Lsf_r30\n\t" \
  "s_cmp_eq_u32 vcc_lo, 4\n\t" \
  "s_cbranch_scc1 .Lsf_r40\n\t" \
  "s_cmp_eq_u32 vcc_lo, 5\n\t" \
  "s_cbranch_scc1 .Lsf_r50\n\t" \
  "s_cmp_eq_u32 vcc_lo, 6\n\t" \
  "s_cbranch_scc1 .Lsf_r60\n\t" \
  "s_cmp_eq_u32 vcc_lo, 7\n\t" \
  "s_cbranch_scc1 .Lsf_r70\n\t" \
  ".Lsf_c01:\n\t" \
  "s_mov_b32 vcc_lo, 0\n\t" \
  "s_branch .Lsf_s1\n\t" \
  ".Lsf_c11:\n\t" \
  "s_mov_b32 vcc_lo, 1\n\t" \
  "s_branch .Lsf_s1\n\t" \
  ".Lsf_c21:\n\t" \
  "s_mov_b32 vcc_lo, 2\n\t" \
  "s_branch .Lsf_s1\n\t" \
  ".Lsf_c31:\n\t" \
  "s_mov_b32 vcc_lo, 3\n\t" \
  "s_branch .Lsf_s1\n\t" \
  ".Lsf_c41:\n\t" \
  "s_mov_b32 vcc_lo, 4\n\t" \
  "s_branch .Lsf_s1\n\t" \
  ".Lsf_c51:\n\t" \
  "s_mov_b32 vcc_lo, 5\n\t" \
  "s_branch .Lsf_s1\n\t" \
  ".Lsf_c61:\n\t" \
  "s_mov_b32 vcc_lo, 6\n\t" \
  "s_branch .Lsf_s1\n\t" \
  ".Lsf_c71:\n\t" \
  "s_mov_b32 vcc_lo, 7\n\t" \
  "s_branch .Lsf_s1\n\t" \
  ".Lsf_s1:\n\t" \
  "s_nop 7\n\t" \
  "s_nop 1\n\t" \
  "v_mov_b32 v193, v192\n\t" \
  "s_nop 1\n\t" \
  "v_permlane32_swap_b32 v192, v193\n\t" \
  "s_nop 1\n\t" \
  "v_max_f32 v192, v192, v193\n\t" \
  "s_nop 1\n\t" \
  "v_mov_b32 v193, v192\n\t" \
  "s_nop 1\n\t" \
  "v_permlane16_swap_b32 v192, v193\n\t" \
  "s_nop 1\n\t" \
  "v_max_f32 v192, v192, v193\n\t" \
  "v_max_f32 v192, 0, v192\n\t" \
  "v_sub_f32 v32, v32, v192\n\t" \
  "v_sub_f32 v33, v33, v192\n\t" \
  "v_sub_f32 v34, v34, v192\n\t" \
  "v_sub_f32 v35, v35, v192\n\t" \
  "v_sub_f32 v36, v36, v192\n\t" \
  "v_sub_f32 v37, v37, v192\n\t" \
  "v_sub_f32 v38, v38, v192\n\t" \
  "v_sub_f32 v39, v39, v192\n\t" \
  "v_sub_f32 v40, v40, v192\n\t" \
  "v_sub_f32 v41, v41, v192\n\t" \
  "v_sub_f32 v42, v42, v192\n\t" \
  "v_sub_f32 v43, v43, v192\n\t" \
  "v_sub_f32 v44, v44, v192\n\t" \
  "v_sub_f32 v45, v45, v192\n\t" \
  "v_sub_f32 v46, v46, v192\n\t" \
  "v_sub_f32 v47, v47, v192\n\t" \
  "v_sub_f32 v180, v180, v192\n\t" \
  "v_sub_f32 v181, v181, v192\n\t" \
  "v_sub_f32 v182, v182, v192\n\t" \
  "v_sub_f32 v183, v183, v192\n\t" \
  "v_exp_f32 v197, -v192\n\t" \
  "s_mov_b32 s99, 1\n\t" \
  "s_nop 1\n\t" \
  "s_cmp_eq_u32 vcc_lo, 0\n\t" \
  "s_cbranch_scc1 .Lsf_r01\n\t" \
  "s_cmp_eq_u32 vcc_lo, 1\n\t" \
  "s_cbranch_scc1 .Lsf_r11\n\t" \
  "s_cmp_eq_u32 vcc_lo, 2\n\t" \
  "s_cbranch_scc1 .Lsf_r21\n\t" \
  "s_cmp_eq_u32 vcc_lo, 3\n\t" \
  "s_cbranch_scc1 .Lsf_r31\n\t" \
  "s_cmp_eq_u32 vcc_lo, 4\n\t" \
  "s_cbranch_scc1 .Lsf_r41\n\t" \
  "s_cmp_eq_u32 vcc_lo, 5\n\t" \
  "s_cbranch_scc1 .Lsf_r51\n\t" \
  "s_cmp_eq_u32 vcc_lo, 6\n\t" \
  "s_cbranch_scc1 .Lsf_r61\n\t" \
  "s_cmp_eq_u32 vcc_lo, 7\n\t" \
  "s_cbranch_scc1 .Lsf_r71\n\t" \
  ".Lsf_c02:\n\t" \
  "s_mov_b32 vcc_lo, 0\n\t" \
  "s_branch .Lsf_s2\n\t" \
  ".Lsf_c12:\n\t" \
  "s_mov_b32 vcc_lo, 1\n\t" \
  "s_branch .Lsf_s2\n\t" \
  ".Lsf_c22:\n\t" \
  "s_mov_b32 vcc_lo, 2\n\t" \
  "s_branch .Lsf_s2\n\t" \
  ".Lsf_c32:\n\t" \
  "s_mov_b32 vcc_lo, 3\n\t" \
  "s_branch .Lsf_s2\n\t" \
  ".Lsf_c42:\n\t" \
  "s_mov_b32 vcc_lo, 4\n\t" \
  "s_branch .Lsf_s2\n\t" \
  ".Lsf_c52:\n\t" \
  "s_mov_b32 vcc_lo, 5\n\t" \
  "s_branch .Lsf_s2\n\t" \
  ".Lsf_c62:\n\t" \
  "s_mov_b32 vcc_lo, 6\n\t" \
  "s_branch .Lsf_s2\n\t" \
  ".Lsf_c72:\n\t" \
  "s_mov_b32 vcc_lo, 7\n\t" \
  "s_branch .Lsf_s2\n\t" \
  ".Lsf_s2:\n\t" \
  "s_nop 7\n\t" \
  "s_nop 1\n\t" \
  "v_mov_b32 v193, v192\n\t" \
  "s_nop 1\n\t" \
  "v_permlane32_swap_b32 v192, v193\n\t" \
  "s_nop 1\n\t" \
  "v_max_f32 v192, v192, v193\n\t" \
  "s_nop 1\n\t" \
  "v_mov_b32 v193, v192\n\t" \
  "s_nop 1\n\t" \
  "v_permlane16_swap_b32 v192, v193\n\t" \
  "s_nop 1\n\t" \
  "v_max_f32 v192, v192, v193\n\t" \
  "v_max_f32 v192, 0, v192\n\t" \
  "v_sub_f32 v64, v64, v192\n\t" \
  "v_sub_f32 v65, v65, v192\n\t" \
  "v_sub_f32 v66, v66, v192\n\t" \
  "v_sub_f32 v67, v67, v192\n\t" \
  "v_sub_f32 v68, v68, v192\n\t" \
  "v_sub_f32 v69, v69, v192\n\t" \
  "v_sub_f32 v70, v70, v192\n\t" \
  "v_sub_f32 v71, v71, v192\n\t" \
  "v_sub_f32 v72, v72, v192\n\t" \
  "v_sub_f32 v73, v73, v192\n\t" \
  "v_sub_f32 v74, v74, v192\n\t" \
  "v_sub_f32 v75, v75, v192\n\t" \
  "v_sub_f32 v76, v76, v192\n\t" \
  "v_sub_f32 v77, v77, v192\n\t" \
  "v_sub_f32 v78, v78, v192\n\t" \
  "v_sub_f32 v79, v79, v192\n\t" \
  "v_sub_f32 v184, v184, v192\n\t" \
  "v_sub_f32 v185, v185, v192\n\t" \
  "v_sub_f32 v186, v186, v192\n\t" \
  "v_sub_f32 v187, v187, v192\n\t" \
  "v_exp_f32 v198, -v192\n\t" \
  "s_mov_b32 s99, 1\n\t" \
  "s_nop 1\n\t" \
  "s_cmp_eq_u32 vcc_lo, 0\n\t" \
  "s_cbranch_scc1 .Lsf_r02\n\t" \
  "s_cmp_eq_u32 vcc_lo, 1\n\t" \
  "s_cbranch_scc1 .Lsf_r12\n\t" \
  "s_cmp_eq_u32 vcc_lo, 2\n\t" \
  "s_cbranch_scc1 .Lsf_r22\n\t" \
  "s_cmp_eq_u32 vcc_lo, 3\n\t" \
  "s_cbranch_scc1 .Lsf_r32\n\t" \
  "s_cmp_eq_u32 vcc_lo, 4\n\t" \
  "s_cbranch_scc1 .Lsf_r42\n\t" \
  "s_cmp_eq_u32 vcc_lo, 5\n\t" \
  "s_cbranch_scc1 .Lsf_r52\n\t" \
  "s_cmp_eq_u32 vcc_lo, 6\n\t" \
  "s_cbranch_scc1 .Lsf_r62\n\t" \
  "s_cmp_eq_u32 vcc_lo, 7\n\t" \
  "s_cbranch_scc1 .Lsf_r72\n\t" \
  ".Lsf_c03:\n\t" \
  "s_mov_b32 vcc_lo, 0\n\t" \
  "s_branch .Lsf_s3\n\t" \
  ".Lsf_c13:\n\t" \
  "s_mov_b32 vcc_lo, 1\n\t" \
  "s_branch .Lsf_s3\n\t" \
  ".Lsf_c23:\n\t" \
  "s_mov_b32 vcc_lo, 2\n\t" \
  "s_branch .Lsf_s3\n\t" \
  ".Lsf_c33:\n\t" \
  "s_mov_b32 vcc_lo, 3\n\t" \
  "s_branch .Lsf_s3\n\t" \
  ".Lsf_c43:\n\t" \
  "s_mov_b32 vcc_lo, 4\n\t" \
  "s_branch .Lsf_s3\n\t" \
  ".Lsf_c53:\n\t" \
  "s_mov_b32 vcc_lo, 5\n\t" \
  "s_branch .Lsf_s3\n\t" \
  ".Lsf_c63:\n\t" \
  "s_mov_b32 vcc_lo, 6\n\t" \
  "s_branch .Lsf_s3\n\t" \
  ".Lsf_c73:\n\t" \
  "s_mov_b32 vcc_lo, 7\n\t" \
  "s_branch .Lsf_s3\n\t" \
  ".Lsf_s3:\n\t" \
  "s_nop 7\n\t" \
  "s_nop 1\n\t" \
  "v_mov_b32 v193, v192\n\t" \
  "s_nop 1\n\t" \
  "v_permlane32_swap_b32 v192, v193\n\t" \
  "s_nop 1\n\t" \
  "v_max_f32 v192, v192, v193\n\t" \
  "s_nop 1\n\t" \
  "v_mov_b32 v193, v192\n\t" \
  "s_nop 1\n\t" \
  "v_permlane16_swap_b32 v192, v193\n\t" \
  "s_nop 1\n\t" \
  "v_max_f32 v192, v192, v193\n\t" \
  "v_max_f32 v192, 0, v192\n\t" \
  "v_sub_f32 v96, v96, v192\n\t" \
  "v_sub_f32 v97, v97, v192\n\t" \
  "v_sub_f32 v98, v98, v192\n\t" \
  "v_sub_f32 v99, v99, v192\n\t" \
  "v_sub_f32 v100, v100, v192\n\t" \
  "v_sub_f32 v101, v101, v192\n\t" \
  "v_sub_f32 v102, v102, v192\n\t" \
  "v_sub_f32 v103, v103, v192\n\t" \
  "v_sub_f32 v104, v104, v192\n\t" \
  "v_sub_f32 v105, v105, v192\n\t" \
  "v_sub_f32 v106, v106, v192\n\t" \
  "v_sub_f32 v107, v107, v192\n\t" \
  "v_sub_f32 v108, v108, v192\n\t" \
  "v_sub_f32 v109, v109, v192\n\t" \
  "v_sub_f32 v110, v110, v192\n\t" \
  "v_sub_f32 v111, v111, v192\n\t" \
  "v_sub_f32 v188, v188, v192\n\t" \
  "v_sub_f32 v189, v189, v192\n\t" \
  "v_sub_f32 v190, v190, v192\n\t" \
  "v_sub_f32 v191, v191, v192\n\t" \
  "v_exp_f32 v199, -v192\n\t" \
  "s_mov_b32 s99, 1\n\t" \
  "s_nop 1\n\t" \
  "s_cmp_eq_u32 vcc_lo, 0\n\t" \
  "s_cbranch_scc1 .Lsf_r03\n\t" \
  "s_cmp_eq_u32 vcc_lo, 1\n\t" \
  "s_cbranch_scc1 .Lsf_r13\n\t" \
  "s_cmp_eq_u32 vcc_lo, 2\n\t" \
  "s_cbranch_scc1 .Lsf_r23\n\t" \
  "s_cmp_eq_u32 vcc_lo, 3\n\t" \
  "s_cbranch_scc1 .Lsf_r33\n\t" \
  "s_cmp_eq_u32 vcc_lo, 4\n\t" \
  "s_cbranch_scc1 .Lsf_r43\n\t" \
  "s_cmp_eq_u32 vcc_lo, 5\n\t" \
  "s_cbranch_scc1 .Lsf_r53\n\t" \
  "s_cmp_eq_u32 vcc_lo, 6\n\t" \
  "s_cbranch_scc1 .Lsf_r63\n\t" \
  "s_cmp_eq_u32 vcc_lo, 7\n\t" \
  "s_cbranch_scc1 .Lsf_r73\n\t" \
  ".Lsf_p0:\n\t" \
  "s_mov_b32 vcc_lo, 0\n\t" \
  "s_branch .Lsf_rs\n\t" \
  ".Lsf_p1:\n\t" \
  "s_mov_b32 vcc_lo, 1\n\t" \
  "s_branch .Lsf_rs\n\t" \
  ".Lsf_p2:\n\t" \
  "s_mov_b32 vcc_lo, 2\n\t" \
  "s_branch .Lsf_rs\n\t" \
  ".Lsf_p3:\n\t" \
  "s_mov_b32 vcc_lo, 3\n\t" \
  "s_branch .Lsf_rs\n\t" \
  ".Lsf_p4:\n\t" \
  "s_mov_b32 vcc_lo, 4\n\t" \
  "s_branch .Lsf_rs\n\t" \
  ".Lsf_p5:\n\t" \
  "s_mov_b32 vcc_lo, 5\n\t" \
  "s_branch .Lsf_rs\n\t" \
  ".Lsf_p6:\n\t" \
  "s_mov_b32 vcc_lo, 6\n\t" \
  "s_branch .Lsf_rs\n\t" \
  ".Lsf_p7:\n\t" \
  "s_mov_b32 vcc_lo, 7\n\t" \
  "s_branch .Lsf_rs\n\t" \
  ".Lsf_rs:\n\t" \
  "s_nop 7\n\t" \
  "s_nop 7\n\t" \
  "v_accvgpr_read_b32 v193, a0\n\t" \
  "v_mul_f32 v193, v196, v193\n\t" \
  "v_accvgpr_write_b32 a0, v193\n\t" \
  "v_accvgpr_read_b32 v193, a1\n\t" \
  "v_mul_f32 v193, v196, v193\n\t" \
  "v_accvgpr_write_b32 a1, v193\n\t" \
  "v_accvgpr_read_b32 v193, a2\n\t" \
  "v_mul_f32 v193, v196, v193\n\t" \
  "v_accvgpr_write_b32 a2, v193\n\t" \
  "v_accvgpr_read_b32 v193, a3\n\t" \
  "v_mul_f32 v193, v196, v193\n\t" \
  "v_accvgpr_write_b32 a3, v193\n\t" \
  "v_accvgpr_read_b32 v193, a4\n\t" \
  "v_mul_f32 v193, v196, v193\n\t" \
  "v_accvgpr_write_b32 a4, v193\n\t" \
  "v_accvgpr_read_b32 v193, a5\n\t" \
  "v_mul_f32 v193, v196, v193\n\t" \
  "v_accvgpr_write_b32 a5, v193\n\t" \
  "v_accvgpr_read_b32 v193, a6\n\t" \
  "v_mul_f32 v193, v196, v193\n\t" \
  "v_accvgpr_write_b32 a6, v193\n\t" \
  "v_accvgpr_read_b32 v193, a7\n\t" \
  "v_mul_f32 v193, v196, v193\n\t" \
  "v_accvgpr_write_b32 a7, v193\n\t" \
  "v_accvgpr_read_b32 v193, a8\n\t" \
  "v_mul_f32 v193, v196, v193\n\t" \
  "v_accvgpr_write_b32 a8, v193\n\t" \
  "v_accvgpr_read_b32 v193, a9\n\t" \
  "v_mul_f32 v193, v196, v193\n\t" \
  "v_accvgpr_write_b32 a9, v193\n\t" \
  "v_accvgpr_read_b32 v193, a10\n\t" \
  "v_mul_f32 v193, v196, v193\n\t" \
  "v_accvgpr_write_b32 a10, v193\n\t" \
  "v_accvgpr_read_b32 v193, a11\n\t" \
  "v_mul_f32 v193, v196, v193\n\t" \
  "v_accvgpr_write_b32 a11, v193\n\t" \
  "v_accvgpr_read_b32 v193, a12\n\t" \
  "v_mul_f32 v193, v196, v193\n\t" \
  "v_accvgpr_write_b32 a12, v193\n\t" \
  "v_accvgpr_read_b32 v193, a13\n\t" \
  "v_mul_f32 v193, v196, v193\n\t" \
  "v_accvgpr_write_b32 a13, v193\n\t" \
  "v_accvgpr_read_b32 v193, a14\n\t" \
  "v_mul_f32 v193, v196, v193\n\t" \
  "v_accvgpr_write_b32 a14, v193\n\t" \
  "v_accvgpr_read_b32 v193, a15\n\t" \
  "v_mul_f32 v193, v196, v193\n\t" \
  "v_accvgpr_write_b32 a15, v193\n\t" \
  "v_accvgpr_read_b32 v193, a16\n\t" \
  "v_mul_f32 v193, v196, v193\n\t" \
  "v_accvgpr_write_b32 a16, v193\n\t" \
  "v_accvgpr_read_b32 v193, a17\n\t" \
  "v_mul_f32 v193, v196, v193\n\t" \
  "v_accvgpr_write_b32 a17, v193\n\t" \
  "v_accvgpr_read_b32 v193, a18\n\t" \
  "v_mul_f32 v193, v196, v193\n\t" \
  "v_accvgpr_write_b32 a18, v193\n\t" \
  "v_accvgpr_read_b32 v193, a19\n\t" \
  "v_mul_f32 v193, v196, v193\n\t" \
  "v_accvgpr_write_b32 a19, v193\n\t" \
  "v_accvgpr_read_b32 v193, a20\n\t" \
  "v_mul_f32 v193, v196, v193\n\t" \
  "v_accvgpr_write_b32 a20, v193\n\t" \
  "v_accvgpr_read_b32 v193, a21\n\t" \
  "v_mul_f32 v193, v196, v193\n\t" \
  "v_accvgpr_write_b32 a21, v193\n\t" \
  "v_accvgpr_read_b32 v193, a22\n\t" \
  "v_mul_f32 v193, v196, v193\n\t" \
  "v_accvgpr_write_b32 a22, v193\n\t" \
  "v_accvgpr_read_b32 v193, a23\n\t" \
  "v_mul_f32 v193, v196, v193\n\t" \
  "v_accvgpr_write_b32 a23, v193\n\t" \
  "v_accvgpr_read_b32 v193, a24\n\t" \
  "v_mul_f32 v193, v196, v193\n\t" \
  "v_accvgpr_write_b32 a24, v193\n\t" \
  "v_accvgpr_read_b32 v193, a25\n\t" \
  "v_mul_f32 v193, v196, v193\n\t" \
  "v_accvgpr_write_b32 a25, v193\n\t" \
  "v_accvgpr_read_b32 v193, a26\n\t" \
  "v_mul_f32 v193, v196, v193\n\t" \
  "v_accvgpr_write_b32 a26, v193\n\t" \
  "v_accvgpr_read_b32 v193, a27\n\t" \
  "v_mul_f32 v193, v196, v193\n\t" \
  "v_accvgpr_write_b32 a27, v193\n\t" \
  "v_accvgpr_read_b32 v193, a28\n\t" \
  "v_mul_f32 v193, v196, v193\n\t" \
  "v_accvgpr_write_b32 a28, v193\n\t" \
  "v_accvgpr_read_b32 v193, a29\n\t" \
  "v_mul_f32 v193, v196, v193\n\t" \
  "v_accvgpr_write_b32 a29, v193\n\t" \
  "v_accvgpr_read_b32 v193, a30\n\t" \
  "v_mul_f32 v193, v196, v193\n\t" \
  "v_accvgpr_write_b32 a30, v193\n\t" \
  "v_accvgpr_read_b32 v193, a31\n\t" \
  "v_mul_f32 v193, v196, v193\n\t" \
  "v_accvgpr_write_b32 a31, v193\n\t" \
  "v_accvgpr_read_b32 v193, a32\n\t" \
  "v_mul_f32 v193, v197, v193\n\t" \
  "v_accvgpr_write_b32 a32, v193\n\t" \
  "v_accvgpr_read_b32 v193, a33\n\t" \
  "v_mul_f32 v193, v197, v193\n\t" \
  "v_accvgpr_write_b32 a33, v193\n\t" \
  "v_accvgpr_read_b32 v193, a34\n\t" \
  "v_mul_f32 v193, v197, v193\n\t" \
  "v_accvgpr_write_b32 a34, v193\n\t" \
  "v_accvgpr_read_b32 v193, a35\n\t" \
  "v_mul_f32 v193, v197, v193\n\t" \
  "v_accvgpr_write_b32 a35, v193\n\t" \
  "v_accvgpr_read_b32 v193, a36\n\t" \
  "v_mul_f32 v193, v197, v193\n\t" \
  "v_accvgpr_write_b32 a36, v193\n\t" \
  "v_accvgpr_read_b32 v193, a37\n\t" \
  "v_mul_f32 v193, v197, v193\n\t" \
  "v_accvgpr_write_b32 a37, v193\n\t" \
  "v_accvgpr_read_b32 v193, a38\n\t" \
  "v_mul_f32 v193, v197, v193\n\t" \
  "v_accvgpr_write_b32 a38, v193\n\t" \
  "v_accvgpr_read_b32 v193, a39\n\t" \
  "v_mul_f32 v193, v197, v193\n\t" \
  "v_accvgpr_write_b32 a39, v193\n\t" \
  "v_accvgpr_read_b32 v193, a40\n\t" \
  "v_mul_f32 v193, v197, v193\n\t" \
  "v_accvgpr_write_b32 a40, v193\n\t" \
  "v_accvgpr_read_b32 v193, a41\n\t" \
  "v_mul_f32 v193, v197, v193\n\t" \
  "v_accvgpr_write_b32 a41, v193\n\t" \
  "v_accvgpr_read_b32 v193, a42\n\t" \
  "v_mul_f32 v193, v197, v193\n\t" \
  "v_accvgpr_write_b32 a42, v193\n\t" \
  "v_accvgpr_read_b32 v193, a43\n\t" \
  "v_mul_f32 v193, v197, v193\n\t" \
  "v_accvgpr_write_b32 a43, v193\n\t" \
  "v_accvgpr_read_b32 v193, a44\n\t" \
  "v_mul_f32 v193, v197, v193\n\t" \
  "v_accvgpr_write_b32 a44, v193\n\t" \
  "v_accvgpr_read_b32 v193, a45\n\t" \
  "v_mul_f32 v193, v197, v193\n\t" \
  "v_accvgpr_write_b32 a45, v193\n\t" \
  "v_accvgpr_read_b32 v193, a46\n\t" \
  "v_mul_f32 v193, v197, v193\n\t" \
  "v_accvgpr_write_b32 a46, v193\n\t" \
  "v_accvgpr_read_b32 v193, a47\n\t" \
  "v_mul_f32 v193, v197, v193\n\t" \
  "v_accvgpr_write_b32 a47, v193\n\t" \
  "v_accvgpr_read_b32 v193, a48\n\t" \
  "v_mul_f32 v193, v197, v193\n\t" \
  "v_accvgpr_write_b32 a48, v193\n\t" \
  "v_accvgpr_read_b32 v193, a49\n\t" \
  "v_mul_f32 v193, v197, v193\n\t" \
  "v_accvgpr_write_b32 a49, v193\n\t" \
  "v_accvgpr_read_b32 v193, a50\n\t" \
  "v_mul_f32 v193, v197, v193\n\t" \
  "v_accvgpr_write_b32 a50, v193\n\t" \
  "v_accvgpr_read_b32 v193, a51\n\t" \
  "v_mul_f32 v193, v197, v193\n\t" \
  "v_accvgpr_write_b32 a51, v193\n\t" \
  "v_accvgpr_read_b32 v193, a52\n\t" \
  "v_mul_f32 v193, v197, v193\n\t" \
  "v_accvgpr_write_b32 a52, v193\n\t" \
  "v_accvgpr_read_b32 v193, a53\n\t" \
  "v_mul_f32 v193, v197, v193\n\t" \
  "v_accvgpr_write_b32 a53, v193\n\t" \
  "v_accvgpr_read_b32 v193, a54\n\t" \
  "v_mul_f32 v193, v197, v193\n\t" \
  "v_accvgpr_write_b32 a54, v193\n\t" \
  "v_accvgpr_read_b32 v193, a55\n\t" \
  "v_mul_f32 v193, v197, v193\n\t" \
  "v_accvgpr_write_b32 a55, v193\n\t" \
  "v_accvgpr_read_b32 v193, a56\n\t" \
  "v_mul_f32 v193, v197, v193\n\t" \
  "v_accvgpr_write_b32 a56, v193\n\t" \
  "v_accvgpr_read_b32 v193, a57\n\t" \
  "v_mul_f32 v193, v197, v193\n\t" \
  "v_accvgpr_write_b32 a57, v193\n\t" \
  "v_accvgpr_read_b32 v193, a58\n\t" \
  "v_mul_f32 v193, v197, v193\n\t" \
  "v_accvgpr_write_b32 a58, v193\n\t" \
  "v_accvgpr_read_b32 v193, a59\n\t" \
  "v_mul_f32 v193, v197, v193\n\t" \
  "v_accvgpr_write_b32 a59, v193\n\t" \
  "v_accvgpr_read_b32 v193, a60\n\t" \
  "v_mul_f32 v193, v197, v193\n\t" \
  "v_accvgpr_write_b32 a60, v193\n\t" \
  "v_accvgpr_read_b32 v193, a61\n\t" \
  "v_mul_f32 v193, v197, v193\n\t" \
  "v_accvgpr_write_b32 a61, v193\n\t" \
  "v_accvgpr_read_b32 v193, a62\n\t" \
  "v_mul_f32 v193, v197, v193\n\t" \
  "v_accvgpr_write_b32 a62, v193\n\t" \
  "v_accvgpr_read_b32 v193, a63\n\t" \
  "v_mul_f32 v193, v197, v193\n\t" \
  "v_accvgpr_write_b32 a63, v193\n\t" \
  "v_accvgpr_read_b32 v193, a64\n\t" \
  "v_mul_f32 v193, v198, v193\n\t" \
  "v_accvgpr_write_b32 a64, v193\n\t" \
  "v_accvgpr_read_b32 v193, a65\n\t" \
  "v_mul_f32 v193, v198, v193\n\t" \
  "v_accvgpr_write_b32 a65, v193\n\t" \
  "v_accvgpr_read_b32 v193, a66\n\t" \
  "v_mul_f32 v193, v198, v193\n\t" \
  "v_accvgpr_write_b32 a66, v193\n\t" \
  "v_accvgpr_read_b32 v193, a67\n\t" \
  "v_mul_f32 v193, v198, v193\n\t" \
  "v_accvgpr_write_b32 a67, v193\n\t" \
  "v_accvgpr_read_b32 v193, a68\n\t" \
  "v_mul_f32 v193, v198, v193\n\t" \
  "v_accvgpr_write_b32 a68, v193\n\t" \
  "v_accvgpr_read_b32 v193, a69\n\t" \
  "v_mul_f32 v193, v198, v193\n\t" \
  "v_accvgpr_write_b32 a69, v193\n\t" \
  "v_accvgpr_read_b32 v193, a70\n\t" \
  "v_mul_f32 v193, v198, v193\n\t" \
  "v_accvgpr_write_b32 a70, v193\n\t" \
  "v_accvgpr_read_b32 v193, a71\n\t" \
  "v_mul_f32 v193, v198, v193\n\t" \
  "v_accvgpr_write_b32 a71, v193\n\t" \
  "v_accvgpr_read_b32 v193, a72\n\t" \
  "v_mul_f32 v193, v198, v193\n\t" \
  "v_accvgpr_write_b32 a72, v193\n\t" \
  "v_accvgpr_read_b32 v193, a73\n\t" \
  "v_mul_f32 v193, v198, v193\n\t" \
  "v_accvgpr_write_b32 a73, v193\n\t" \
  "v_accvgpr_read_b32 v193, a74\n\t" \
  "v_mul_f32 v193, v198, v193\n\t" \
  "v_accvgpr_write_b32 a74, v193\n\t" \
  "v_accvgpr_read_b32 v193, a75\n\t" \
  "v_mul_f32 v193, v198, v193\n\t" \
  "v_accvgpr_write_b32 a75, v193\n\t" \
  "v_accvgpr_read_b32 v193, a76\n\t" \
  "v_mul_f32 v193, v198, v193\n\t" \
  "v_accvgpr_write_b32 a76, v193\n\t" \
  "v_accvgpr_read_b32 v193, a77\n\t" \
  "v_mul_f32 v193, v198, v193\n\t" \
  "v_accvgpr_write_b32 a77, v193\n\t" \
  "v_accvgpr_read_b32 v193, a78\n\t" \
  "v_mul_f32 v193, v198, v193\n\t" \
  "v_accvgpr_write_b32 a78, v193\n\t" \
  "v_accvgpr_read_b32 v193, a79\n\t" \
  "v_mul_f32 v193, v198, v193\n\t" \
  "v_accvgpr_write_b32 a79, v193\n\t" \
  "v_accvgpr_read_b32 v193, a80\n\t" \
  "v_mul_f32 v193, v198, v193\n\t" \
  "v_accvgpr_write_b32 a80, v193\n\t" \
  "v_accvgpr_read_b32 v193, a81\n\t" \
  "v_mul_f32 v193, v198, v193\n\t" \
  "v_accvgpr_write_b32 a81, v193\n\t" \
  "v_accvgpr_read_b32 v193, a82\n\t" \
  "v_mul_f32 v193, v198, v193\n\t" \
  "v_accvgpr_write_b32 a82, v193\n\t" \
  "v_accvgpr_read_b32 v193, a83\n\t" \
  "v_mul_f32 v193, v198, v193\n\t" \
  "v_accvgpr_write_b32 a83, v193\n\t" \
  "v_accvgpr_read_b32 v193, a84\n\t" \
  "v_mul_f32 v193, v198, v193\n\t" \
  "v_accvgpr_write_b32 a84, v193\n\t" \
  "v_accvgpr_read_b32 v193, a85\n\t" \
  "v_mul_f32 v193, v198, v193\n\t" \
  "v_accvgpr_write_b32 a85, v193\n\t" \
  "v_accvgpr_read_b32 v193, a86\n\t" \
  "v_mul_f32 v193, v198, v193\n\t" \
  "v_accvgpr_write_b32 a86, v193\n\t" \
  "v_accvgpr_read_b32 v193, a87\n\t" \
  "v_mul_f32 v193, v198, v193\n\t" \
  "v_accvgpr_write_b32 a87, v193\n\t" \
  "v_accvgpr_read_b32 v193, a88\n\t" \
  "v_mul_f32 v193, v198, v193\n\t" \
  "v_accvgpr_write_b32 a88, v193\n\t" \
  "v_accvgpr_read_b32 v193, a89\n\t" \
  "v_mul_f32 v193, v198, v193\n\t" \
  "v_accvgpr_write_b32 a89, v193\n\t" \
  "v_accvgpr_read_b32 v193, a90\n\t" \
  "v_mul_f32 v193, v198, v193\n\t" \
  "v_accvgpr_write_b32 a90, v193\n\t" \
  "v_accvgpr_read_b32 v193, a91\n\t" \
  "v_mul_f32 v193, v198, v193\n\t" \
  "v_accvgpr_write_b32 a91, v193\n\t" \
  "v_accvgpr_read_b32 v193, a92\n\t" \
  "v_mul_f32 v193, v198, v193\n\t" \
  "v_accvgpr_write_b32 a92, v193\n\t" \
  "v_accvgpr_read_b32 v193, a93\n\t" \
  "v_mul_f32 v193, v198, v193\n\t" \
  "v_accvgpr_write_b32 a93, v193\n\t" \
  "v_accvgpr_read_b32 v193, a94\n\t" \
  "v_mul_f32 v193, v198, v193\n\t" \
  "v_accvgpr_write_b32 a94, v193\n\t" \
  "v_accvgpr_read_b32 v193, a95\n\t" \
  "v_mul_f32 v193, v198, v193\n\t" \
  "v_accvgpr_write_b32 a95, v193\n\t" \
  "v_accvgpr_read_b32 v193, a96\n\t" \
  "v_mul_f32 v193, v199, v193\n\t" \
  "v_accvgpr_write_b32 a96, v193\n\t" \
  "v_accvgpr_read_b32 v193, a97\n\t" \
  "v_mul_f32 v193, v199, v193\n\t" \
  "v_accvgpr_write_b32 a97, v193\n\t" \
  "v_accvgpr_read_b32 v193, a98\n\t" \
  "v_mul_f32 v193, v199, v193\n\t" \
  "v_accvgpr_write_b32 a98, v193\n\t" \
  "v_accvgpr_read_b32 v193, a99\n\t" \
  "v_mul_f32 v193, v199, v193\n\t" \
  "v_accvgpr_write_b32 a99, v193\n\t" \
  "v_accvgpr_read_b32 v193, a100\n\t" \
  "v_mul_f32 v193, v199, v193\n\t" \
  "v_accvgpr_write_b32 a100, v193\n\t" \
  "v_accvgpr_read_b32 v193, a101\n\t" \
  "v_mul_f32 v193, v199, v193\n\t" \
  "v_accvgpr_write_b32 a101, v193\n\t" \
  "v_accvgpr_read_b32 v193, a102\n\t" \
  "v_mul_f32 v193, v199, v193\n\t" \
  "v_accvgpr_write_b32 a102, v193\n\t" \
  "v_accvgpr_read_b32 v193, a103\n\t" \
  "v_mul_f32 v193, v199, v193\n\t" \
  "v_accvgpr_write_b32 a103, v193\n\t" \
  "v_accvgpr_read_b32 v193, a104\n\t" \
  "v_mul_f32 v193, v199, v193\n\t" \
  "v_accvgpr_write_b32 a104, v193\n\t" \
  "v_accvgpr_read_b32 v193, a105\n\t" \
  "v_mul_f32 v193, v199, v193\n\t" \
  "v_accvgpr_write_b32 a105, v193\n\t" \
  "v_accvgpr_read_b32 v193, a106\n\t" \
  "v_mul_f32 v193, v199, v193\n\t" \
  "v_accvgpr_write_b32 a106, v193\n\t" \
  "v_accvgpr_read_b32 v193, a107\n\t" \
  "v_mul_f32 v193, v199, v193\n\t" \
  "v_accvgpr_write_b32 a107, v193\n\t" \
  "v_accvgpr_read_b32 v193, a108\n\t" \
  "v_mul_f32 v193, v199, v193\n\t" \
  "v_accvgpr_write_b32 a108, v193\n\t" \
  "v_accvgpr_read_b32 v193, a109\n\t" \
  "v_mul_f32 v193, v199, v193\n\t" \
  "v_accvgpr_write_b32 a109, v193\n\t" \
  "v_accvgpr_read_b32 v193, a110\n\t" \
  "v_mul_f32 v193, v199, v193\n\t" \
  "v_accvgpr_write_b32 a110, v193\n\t" \
  "v_accvgpr_read_b32 v193, a111\n\t" \
  "v_mul_f32 v193, v199, v193\n\t" \
  "v_accvgpr_write_b32 a111, v193\n\t" \
  "v_accvgpr_read_b32 v193, a112\n\t" \
  "v_mul_f32 v193, v199, v193\n\t" \
  "v_accvgpr_write_b32 a112, v193\n\t" \
  "v_accvgpr_read_b32 v193, a113\n\t" \
  "v_mul_f32 v193, v199, v193\n\t" \
  "v_accvgpr_write_b32 a113, v193\n\t" \
  "v_accvgpr_read_b32 v193, a114\n\t" \
  "v_mul_f32 v193, v199, v193\n\t" \
  "v_accvgpr_write_b32 a114, v193\n\t" \
  "v_accvgpr_read_b32 v193, a115\n\t" \
  "v_mul_f32 v193, v199, v193\n\t" \
  "v_accvgpr_write_b32 a115, v193\n\t" \
  "v_accvgpr_read_b32 v193, a116\n\t" \
  "v_mul_f32 v193, v199, v193\n\t" \
  "v_accvgpr_write_b32 a116, v193\n\t" \
  "v_accvgpr_read_b32 v193, a117\n\t" \
  "v_mul_f32 v193, v199, v193\n\t" \
  "v_accvgpr_write_b32 a117, v193\n\t" \
  "v_accvgpr_read_b32 v193, a118\n\t" \
  "v_mul_f32 v193, v199, v193\n\t" \
  "v_accvgpr_write_b32 a118, v193\n\t" \
  "v_accvgpr_read_b32 v193, a119\n\t" \
  "v_mul_f32 v193, v199, v193\n\t" \
  "v_accvgpr_write_b32 a119, v193\n\t" \
  "v_accvgpr_read_b32 v193, a120\n\t" \
  "v_mul_f32 v193, v199, v193\n\t" \
  "v_accvgpr_write_b32 a120, v193\n\t" \
  "v_accvgpr_read_b32 v193, a121\n\t" \
  "v_mul_f32 v193, v199, v193\n\t" \
  "v_accvgpr_write_b32 a121, v193\n\t" \
  "v_accvgpr_read_b32 v193, a122\n\t" \
  "v_mul_f32 v193, v199, v193\n\t" \
  "v_accvgpr_write_b32 a122, v193\n\t" \
  "v_accvgpr_read_b32 v193, a123\n\t" \
  "v_mul_f32 v193, v199, v193\n\t" \
  "v_accvgpr_write_b32 a123, v193\n\t" \
  "v_accvgpr_read_b32 v193, a124\n\t" \
  "v_mul_f32 v193, v199, v193\n\t" \
  "v_accvgpr_write_b32 a124, v193\n\t" \
  "v_accvgpr_read_b32 v193, a125\n\t" \
  "v_mul_f32 v193, v199, v193\n\t" \
  "v_accvgpr_write_b32 a125, v193\n\t" \
  "v_accvgpr_read_b32 v193, a126\n\t" \
  "v_mul_f32 v193, v199, v193\n\t" \
  "v_accvgpr_write_b32 a126, v193\n\t" \
  "v_accvgpr_read_b32 v193, a127\n\t" \
  "v_mul_f32 v193, v199, v193\n\t" \
  "v_accvgpr_write_b32 a127, v193\n\t" \
  "v_mul_f32 v160, v196, v160\n\t" \
  "v_mul_f32 v161, v196, v161\n\t" \
  "v_mul_f32 v162, v196, v162\n\t" \
  "v_mul_f32 v163, v196, v163\n\t" \
  "v_mov_b32 v196, 1.0\n\t" \
  "v_mul_f32 v164, v197, v164\n\t" \
  "v_mul_f32 v165, v197, v165\n\t" \
  "v_mul_f32 v166, v197, v166\n\t" \
  "v_mul_f32 v167, v197, v167\n\t" \
  "v_mov_b32 v197, 1.0\n\t" \
  "v_mul_f32 v168, v198, v168\n\t" \
  "v_mul_f32 v169, v198, v169\n\t" \
  "v_mul_f32 v170, v198, v170\n\t" \
  "v_mul_f32 v171, v198, v171\n\t" \
  "v_mov_b32 v198, 1.0\n\t" \
  "v_mul_f32 v172, v199, v172\n\t" \
  "v_mul_f32 v173, v199, v173\n\t" \
  "v_mul_f32 v174, v199, v174\n\t" \
  "v_mul_f32 v175, v199, v175\n\t" \
  "v_mov_b32 v199, 1.0\n\t" \
  "s_mov_b32 s99, 0\n\t" \
  "s_nop 1\n\t" \
  "s_cmp_eq_u32 vcc_lo, 0\n\t" \
  "s_cbranch_scc1 .Lsf_q0\n\t" \
  "s_cmp_eq_u32 vcc_lo, 1\n\t" \
  "s_cbranch_scc1 .Lsf_q1\n\t" \
  "s_cmp_eq_u32 vcc_lo, 2\n\t" \
  "s_cbranch_scc1 .Lsf_q2\n\t" \
  "s_cmp_eq_u32 vcc_lo, 3\n\t" \
  "s_cbranch_scc1 .Lsf_q3\n\t" \
  "s_cmp_eq_u32 vcc_lo, 4\n\t" \
  "s_cbranch_scc1 .Lsf_q4\n\t" \
  "s_cmp_eq_u32 vcc_lo, 5\n\t" \
  "s_cbranch_scc1 .Lsf_q5\n\t" \
  "s_cmp_eq_u32 vcc_lo, 6\n\t" \
  "s_cbranch_scc1 .Lsf_q6\n\t" \
  "s_cmp_eq_u32 vcc_lo, 7\n\t" \
  "s_cbranch_scc1 .Lsf_q7\n\t" \
  "s_endpgm\n\t" \
  ".Lsf_v:\n\t" \
  "s_mov_b32 s98, 0\n\t" \
  ".Lsf_v1:\n\t" \
  "s_sleep 4\n\t" \
  "buffer_load_dword v209, v250, s[16:19], 0 offen sc1\n\t" \
  "s_waitcnt vmcnt(0)\n\t" \
  "v_and_b32 v251, 0xbfffbfff, v209\n\t" \
  "v_cmp_ne_u32_e64 s[84:85], s36, v251\n\t" \
  "s_cmp_lg_u64 s[84:85], 0\n\t" \
  "s_cbranch_scc0 .Lsf_w\n\t" \
  "s_add_u32 s98, s98, 1\n\t" \
  "s_cmp_lt_u32 s98, " FQ_CAP "\n\t" \
  "s_cbranch_scc1 .Lsf_v1\n\t" \
  "v_mov_b32 v251, s36\n\t" \
  "buffer_store_dword v251, off, s[16:19], 0 offset:256 sc1\n\t" \
  "s_branch .Lsf_w\n\t" \
  ".subsection 0\n\t" \
  ".Lop_end:\n\t" \
  ""
#define OP_GA0 "=&{v[160:163]}"
#define OP_GA1 "=&{v[164:167]}"
#define OP_CW "=&{s82}"
#define OP_BB "=&{s83}"
#define AO_CLOB "v128", "v129", "v130", "v131", "v132", "v133", "v134", "v135", "v136", "v137", "v138", "v139", "v140", "v141", "v142", "v143", "v144", "v145", "v146", "v147", "v148", "v149", "v150", "v151", "v152", "v153", "v154", "v155", "v156", "v157", "v158", "v159", "v168", "v169", "v170", "v171", "v172", "v173", "v174", "v175", "v208", "v209", "v210", "v211", "v212", "v213", "v214", "v215", "v216", "v217", "v218", "v219", "v234", "v235", "v236", "v237", "v238", "v239", "v242", "v243", "v248", "v249", "v250", "v251", "v252", "v254", "s76", "s77", "s78", "s79", "s80", "s81", "s84", "s85", "s86", "s87", "s88", "s89", "s90", "s91", "s92", "s93", "s94", "s95", "s96", "s97", "s98", "s99", "vcc", "scc", "memory"
#define OP_DRD "s_waitcnt vmcnt(5)\n\t" "ds_read2st64_b64 v[32:35], %1 offset0:64 offset1:65\n\t" "ds_read2st64_b64 v[36:39], %1 offset0:66 offset1:67\n\t" "ds_read2st64_b64 v[40:43], %1 offset0:68 offset1:69\n\t" "ds_read2st64_b64 v[44:47], %1 offset0:70 offset1:71\n\t" "ds_read2st64_b64 v[48:51], %1 offset0:96 offset1:97\n\t" "ds_read2st64_b64 v[52:55], %1 offset0:98 offset1:99\n\t" "ds_read2st64_b64 v[56:59], %1 offset0:100 offset1:101\n\t" "ds_read2st64_b64 v[60:63], %1 offset0:102 offset1:103\n\t" "s_waitcnt lgkmcnt(0)"
#define ST_ASM "s_nop 1\n\t" "v_mfma_f32_16x16x32_bf16 v[80:83], a[176:179], v[160:163], 0\n\t" "v_mfma_f32_16x16x32_bf16 v[80:83], a[180:183], v[164:167], v[80:83]\n\t" "v_mfma_f32_16x16x32_bf16 v[80:83], a[184:187], v[168:171], v[80:83]\n\t" "v_mfma_f32_16x16x32_bf16 v[80:83], a[188:191], v[172:175], v[80:83]\n\t" "v_dot2_f32_bf16 v64, v160, v160, 0\n\t" "v_dot2_f32_bf16 v65, v164, v164, 0\n\t" "v_dot2_f32_bf16 v66, v168, v168, 0\n\t" "v_dot2_f32_bf16 v67, v172, v172, 0\n\t" "v_dot2_f32_bf16 v64, v161, v161, v64\n\t" "v_dot2_f32_bf16 v65, v165, v165, v65\n\t" "v_dot2_f32_bf16 v66, v169, v169, v66\n\t" "v_dot2_f32_bf16 v67, v173, v173, v67\n\t" "v_dot2_f32_bf16 v64, v162, v162, v64\n\t" "v_dot2_f32_bf16 v65, v166, v166, v65\n\t" "v_dot2_f32_bf16 v66, v170, v170, v66\n\t" "v_dot2_f32_bf16 v67, v174, v174, v67\n\t" "v_dot2_f32_bf16 v64, v163, v163, v64\n\t" "v_dot2_f32_bf16 v65, v167, v167, v65\n\t" "v_dot2_f32_bf16 v66, v171, v171, v66\n\t" "v_dot2_f32_bf16 v67, v175, v175, v67\n\t" "s_nop 2\n\t"
static_assert(W_MODB == 2228224 && W_VOTE == 8192);
#define AO_T0
#define MAIN_CLOB "v32", "v33", "v34", "v35", "v36", "v37", "v38", "v39", "v40", "v41", "v42", "v43", "v44", "v45", "v46", "v47", "v48", "v49", "v50", "v51", "v52", "v53", "v54", "v55", "v56", "v57", "v58", "v59", "v60", "v61", "v62", "v63", "v64", "v65", "v66", "v67", "v68", "v69", "v70", "v71", "v72", "v73", "v74", "v75", "v76", "v77", "v78", "v79", "v80", "v81", "v82", "v83", "v84", "v85", "v86", "v87", "v88", "v89", "v90", "v91", "v92", "v93", "v94", "v95", "v128", "v129", "v130", "v131", "v132", "v133", "v134", "v135", "v136", "v137", "v138", "v139", "v140", "v141", "v142", "v143", "v144", "v145", "v146", "v147", "v148", "v149", "v150", "v151", "v152", "v153", "v154", "v155", "v156", "v157", "v158", "v159", "v168", "v169", "v170", "v171", "v172", "v173", "v174", "v175", "v208", "v209", "v210", "v211", "v212", "v213", "v214", "v215", "v216", "v217", "v218", "v219", "v220", "v221", "v222", "v223", "v224", "v225", "v226", "v227", "v228", "v229", "v230", "v231", "v232", "v233", "v234", "v235", "v236", "v237", "v238", "v239", "v240", "v241", "v242", "v243", "v244", "v245", "v246", "v248", "v249", "v250", "v251", "v252", "v253", "v254", "s4", "s5", "s6", "s7", "s8", "s9", "s10", "s11", "s12", "s13", "s14", "s15", "s20", "s21", "s22", "s23", "s24", "s25", "s26", "s27", "s28", "s29", "s30", "s31", "s32", "s33", "s34", "s35", "s37", "s38", "s39", "s40", "s41", "s42", "s43", "s44", "s45", "s46", "s47", "s48", "s49", "s50", "s51", "s52", "s53", "s54", "s57", "s58", "s59", "s60", "s61", "s62", "s63", "s64", "s65", "s66", "s67", "s68", "s69", "s70", "s71", "s72", "s73", "s74", "s75", "s76", "s77", "s78", "s79", "s80", "s81", "s84", "s85", "s86", "s87", "s88", "s89", "s90", "s91", "s92", "s93", "s94", "s95", "s96", "s97", "s98", "s99", "vcc", "scc", "memory"
constexpr unsigned TABU = 9 * UNIT;
#define OAUX 17
DI int key_row(unsigned k) { return S - 1 - (int)(k & 0xffffu); }
DI float key_val(unsigned k) { unsigned b = k >> 16; b = (b & 0x8000u) ? (b & 0x7fffu) : (~b & 0xffffu); return bf2f((u16)b); }
template <int R> DI unsigned acc_readu() { unsigned r; asm volatile("v_accvgpr_read_b32 %0, a%c1" : "=v"(r) : "i"(R)); return r; }
template <int... Is> DI void gate_read(u32x32& g, ilist<Is...>) { ((g[Is] = acc_readu<224 + Is>()), ...); }
#define RC 16
#define TP 1
#define TA 0
#define TB 33
#define TE 0
#define MAIN_STV
DI void body(Ctx& k STAMP_ARG) {
  u32x32 UP, DN, q0; f32x32 A3; f32x4 ga0, ga1; float cww, bbw; unsigned dra;
  argp_t ap = (argp_t)__builtin_amdgcn_kernarg_segment_ptr();
  q0[0] = threadIdx.x; k.bid = blockIdx.x;
  asm volatile(FQ_ASM AT_ASM
               : MAIN_STV "+{v[0:31]}"(q0), "=&{v[96:127]}"(A3), "=&{v[176:207]}"(UP), OP_GA0(ga0), OP_GA1(ga1), OP_CW(cww), OP_BB(bbw), "=&{v247}"(dra),
                 "+{s[0:1]}"(ap), "+{s2}"(k.bid), "=&{s3}"(k.wid), "=&{s[16:19]}"(k.ws), "=&{s36}"(k.tag), "=&{s55}"(k.nb), "=&{s56}"(k.t0)
               :: MAIN_CLOB, ACLOB_LO, ACLOB_HI);
  const unsigned wid = k.wid, tag = k.tag, bid = k.bid;
  k.xcd = ((bid & 3) << 1) | ((bid >> 2) & 1); k.j = bid >> 3; k.kvh = bid & 3; k.hl = k.j & 7; k.rblk = (bid & 4) + (bid >> 6); k.cs = ((bid & 4) << 11) ^ k.nb;
  const unsigned grp = k.rblk * 4 + k.kvh, cblk = k.hl;
  f32x32 XT = __builtin_bit_cast(f32x32, q0);
  const unsigned lane = lane_id(), r16 = lane & 15, g = lane >> 4, tid = wid * 64 + lane;
  const unsigned sc = k.ldsb, sto = W_STAT + ((grp & 15) << 15) + k.cs, stw = ((wid >> 1) << 14) + ((wid & 1) << 12);
  const bool own0 = TA && cblk == 2 * wid, own1 = TA && cblk == 2 * wid + 1;
  const unsigned o0 = (sto + stw + lane * 16) | (own0 ? 0x80000000u : 0u), o1 = (sto + stw + 2048 + lane * 16) | (own1 ? 0x80000000u : 0u);
  u32x4 a0, a1, b0, b1;
#define STATS() do { a0 = ld16(k.ws, o0); b0 = ld16(k.ws, o0 + 1024); a1 = ld16(k.ws, o1); b1 = ld16(k.ws, o1 + 1024); } while (0)
  u32x16 xp; f32x4 q, sr;
#pragma unroll
  for (int rb = 0; rb < 4; ++rb)
#pragma unroll
    for (int e = 0; e < 4; ++e) {
      const int i = ((e >> 1) * 4 + rb) * 4 + (e & 1) * 2;
      const f32x4 gq = (e >> 1) ? ga1 : ga0;
      const f32x2 t = __builtin_elementwise_fma(f32x2{gq[(e & 1) * 2], gq[(e & 1) * 2 + 1]}, f32x2{A3[i], A3[i + 1]}, f32x2{XT[i], XT[i + 1]});
      xp[4 * rb + e] = cvtpk(t[0], t[1]);
    }
  asm volatile(ST_ASM : "=&{v[64:67]}"(q), "=&{v[80:83]}"(sr) : "{v[160:175]}"(xp));
  lds_st(sc + wid * 1024 + lane * 16, f32x4{sr[0], fold16(fold32(q[0], q[2]), fold32(q[1], q[3])), sr[1] + sr[2], (lane & 1) ? bbw : cww});
  BARRIER_LGKM();
  f32x4 so;
  {
    f32x4 p[4];
#pragma unroll
    for (int w = 0; w < 4; ++w) p[w] = lds_ld<f32x4>(sc + w * 1024 + lane * 16);
    so = (p[0] + p[1]) + (p[2] + p[3]);
    st16(k.ws, (sto + ((cblk >> 2) << 14) + ((cblk & 3) << 11) + wid * 1024 + lane * 16) | (wid << 30 & 0x80000000u), u32x4{__float_as_uint(wid ? so[2] : so[0]), tag, __float_as_uint(wid ? so[3] : so[1]), tag});
  }
  STAMPO(20);
  f32x2 xq[4][4];
#define XQ(rb, e) xq[rb][e]
  f32x4 tsb[2], tc2[2], tgb[2];
#pragma unroll
  for (int nb = 0; nb < 2; ++nb) {
    const unsigned ta = k.ldsb + TABU + wid * 4096 + 2560 + nb * 64 + g * 16;
    tsb[nb] = lds_ld<f32x4>(ta + 128); tc2[nb] = lds_ld<f32x4>(ta + 256); tgb[nb] = lds_ld<f32x4>(ta + 384);
  }
  float mu_d, rstd; unsigned gbest;
  srd_t srdo = mk_srd(ap->out.p, S * DM * 2);
  asm volatile("" : "+s"(srdo));
  const unsigned ri = ((bid & 3) | ((bid >> 3) << 2)) & (RC - 1);
  const unsigned ko = W_GKEY + ((ri >> 5) << 14) + k.cs + (ri & 31) * 256 + (lane & 31) * 8, ho = W_CANDH + k.cs + (tid >> 1) * 32;
  u32x2 kk; u32x4 h0, h1; unsigned spec;
#define KEY1() asm volatile("buffer_load_dwordx2 %0, %1, %2, 0 offen sc1" : "={v[224:225]}"(kk) : "v"(ko), "s"(k.ws) : "memory")
#define KEYS() asm volatile("buffer_load_dwordx2 %0, %3, %5, 0 offen sc1\n\tbuffer_load_dwordx4 %1, %4, %5, %6 offen sc1\n\tbuffer_load_dwordx4 %2, %4, %5, %6 offen offset:16 sc1" \
                            : "=&{v[224:225]}"(kk), "=&{v[208:211]}"(h0), "=&{v[212:215]}"(h1) : "v"(ko), "v"(ho), "s"(k.ws), "s"(((spec >> 1) << 14) + ((spec & 1) << 12)) : "memory")
#define KEYS_WAIT(N) asm volatile("s_waitcnt vmcnt(%3)" : "+{v[224:225]}"(kk), "+{v[208:211]}"(h0), "+{v[212:215]}"(h1) : "i"(N) : "memory")
#define PERMS() do { \
      u32x32 gt; gate_read(gt, iseq<32>{}); \
      _Pragma("unroll") for (int rp = 0; rp < 4; ++rp) _Pragma("unroll") for (int e = 0; e < 4; ++e) { \
          const unsigned u0 = UP[8 * rp + e], u1 = UP[8 * rp + 4 + e]; \
          UP[8 * rp + e] = plo(u0, u1); UP[8 * rp + 4 + e] = phi(u0, u1); \
          A3[8 * rp + e] = __uint_as_float(plo(gt[8 * rp + e], gt[8 * rp + 4 + e])); A3[8 * rp + 4 + e] = __uint_as_float(phi(gt[8 * rp + e], gt[8 * rp + 4 + e])); } \
      asm volatile("" : "+v"(UP), "+v"(A3)); \
      asm volatile(OP_DRD : "=&{v[32:63]}"(DN) : "v"(dra) : "memory"); \
      _Pragma("unroll") for (int rp = 0; rp < 8; ++rp) _Pragma("unroll") for (int e = 0; e < 2; ++e) { \
          const unsigned u0 = DN[4 * rp + e], u1 = DN[4 * rp + 2 + e]; DN[4 * rp + e] = plo(u0, u1); DN[4 * rp + 2 + e] = phi(u0, u1); } \
      asm volatile("" : "+v"(UP), "+v"(DN), "+v"(A3)); } while (0)
  {
    unsigned spins = 0;
    STATS();
    GAP();
    HOP_PAGE(1);
    GAP();
    PERMS();
#pragma unroll
    for (int i = 0; i < 16; ++i) { xq[i >> 2][i & 3] = up2(xp[i]); asm volatile("" : "+v"(xq[i >> 2][i & 3])); }
    while (!__all((own0 | (tags2(a0, tag) & tags2(b0, tag))) & (own1 | (tags2(a1, tag) & tags2(b1, tag))))) {
      if (spin_fail<SL1>(spins, k.ws, tag)) break;
      STATS();
    }
    const f32x4 s0 = own0 ? so : f32x4{__uint_as_float(a0[0]), __uint_as_float(a0[2]), __uint_as_float(b0[0]), __uint_as_float(b0[2])};
    const f32x4 s1 = own1 ? so : f32x4{__uint_as_float(a1[0]), __uint_as_float(a1[2]), __uint_as_float(b1[0]), __uint_as_float(b1[2])};
    lds_st(sc + 4096 + wid * 1024 + lane * 16, s0 + s1);
    BARRIER_LGKM();
    KEY1();
    GAP();
    STAMPO(21);
    f32x4 p[4];
#pragma unroll
    for (int w = 0; w < 4; ++w) p[w] = lds_ld<f32x4>(sc + 4096 + w * 1024 + lane * 16);
    const f32x4 s = (p[0] + p[1]) + (p[2] + p[3]);
    const float mu = s[0] * (1.f / DM);
    rstd = rsqrtf(fmaxf(fmaf(-mu, mu, s[1] * (1.f / DM)), 0.f) + 1e-5f);
    const float cw = __uint_as_float(__builtin_amdgcn_readlane(__float_as_uint(s[3]), 0)), bb = __uint_as_float(__builtin_amdgcn_readlane(__float_as_uint(s[3]), 1));
    const float r = fmaf(rstd, fmaf(-mu, cw, s[2]), bb);
    unsigned kb = cvtpk(r, r) & 0xffffu; kb = (kb & 0x8000u) ? (~kb & 0xffffu) : (kb | 0x8000u);
    gbest = rfl(wave_maxu((kb << 16) | (unsigned)(S - 1 - (grp * 64 + lane))));
    const unsigned rp = cblk * 4 + wid + 32 * lane, wi = rp >> 1;
    st8(k.ws, (W_GKEY + ((wi >> 5) << 14) + (((rp & 1) << 13) ^ k.nb) + (wi & 31) * 256 + grp * 8) | (lane < RC / 16 ? 0u : 0x80000000u), u32x2{gbest, tag});
    mu_d = -mu * rstd;
  }
  STAMPO(22);
  const unsigned lrow = (unsigned)key_row(gbest) & 63u;
  const unsigned oo = (grp * 64 + r16) * (DM * 2) + cblk * 256 + wid * 64 + (g & 1) * 32 + (g >> 1) * 16;
  u32x4 hp[4], ow[4]; unsigned best; bool hold, early;
  {
    float ra[4], rb_[4];
    bcast4(rstd, ra); bcast4(mu_d, rb_);
#pragma unroll
    for (int rb = 0; rb < 4; ++rb)
#pragma unroll
      for (int e = 0; e < 4; ++e) {
        const int nb = e >> 1, h = (e & 1) * 2;
        const f32x2 n = __builtin_elementwise_fma(XQ(rb, e), f32x2{ra[rb], ra[rb]}, f32x2{rb_[rb], rb_[rb]});
        const f32x2 hh = __builtin_elementwise_fma(n, f32x2{tc2[nb][h], tc2[nb][h + 1]}, f32x2{tsb[nb][h], tsb[nb][h + 1]});
        hp[rb][e] = cvtpk(hh[0], hh[1]);
      }
    u32x4 hc = hp[0], xc = {xp[0], xp[1], xp[2], xp[3]};
#pragma unroll
    for (int rb = 1; rb < 4; ++rb) if ((lrow >> 4) == (unsigned)rb) { hc = hp[rb]; xc = u32x4{xp[4 * rb], xp[4 * rb + 1], xp[4 * rb + 2], xp[4 * rb + 3]}; }
    GAP();
#define CAND(H) do { const unsigned off = ((H) && r16 == (lrow & 15) ? 0u : 0x80000000u) | ((wid * 16 + g * 2) * 8); \
    const unsigned co = (W_CANDH + ((grp >> 1) << 14) + ((grp & 1) << 12) + cblk * 512 + k.cs) + off, cx = (W_CANDX + ((grp >> 2) << 14) + (((cblk >> 2) << 13) ^ k.nb) + ((grp & 3) << 11) + (cblk & 3) * 512) + off; \
    st16(k.ws, co, u32x4{hc[0], tag, hc[1], tag}); st16(k.ws, co + 64, u32x4{hc[2], tag, hc[3], tag}); \
    st16(k.ws, co ^ 8192, u32x4{hc[0], tag, hc[1], tag}); st16(k.ws, (co ^ 8192) + 64, u32x4{hc[2], tag, hc[3], tag}); \
    st16(k.ws, cx, u32x4{xc[0], tag, xc[1], tag}); st16(k.ws, cx + 64, u32x4{xc[2], tag, xc[3], tag}); } while (0)
    asm volatile("s_waitcnt vmcnt(%1)" : "+{v[224:225]}"(kk) : "i"(TE ? 7 : 1) : "memory");
    best = max(rfl(wave_maxu(kk[1] == tag ? kk[0] : 0u)), gbest);
    hold = best == gbest;
    early = __builtin_popcountll(__ballot(kk[1] == tag)) < 2 * TB;
    spec = ((unsigned)key_row(best) >> 6) & 31u;
    KEYS();
    GAP();
    CAND(hold);
    GAP();
#pragma unroll
    for (int rb = 0; rb < 4; ++rb) {
      u32x4 op;
#pragma unroll
      for (int e = 0; e < 4; ++e) {
        const int nb = e >> 1, h = (e & 1) * 2;
        const f32x2 o = __builtin_elementwise_fma(f32x2{tgb[nb][h], tgb[nb][h + 1]}, up2(hp[rb][e]), XQ(rb, e));
        op[e] = cvtpk(o[0], o[1]);
      }
      auto s0 = __builtin_amdgcn_permlane16_swap(op[0], op[2], false, false);
      auto s1 = __builtin_amdgcn_permlane16_swap(op[1], op[3], false, false);
      ow[rb] = u32x4{s0[0], s1[0], s0[1], s1[1]};
      __builtin_amdgcn_raw_buffer_store_b128(ow[rb], srdo, (oo + rb * (16 * DM * 2)) | (!early || (hold && lrow == 16 * rb + r16) ? 0x80000000u : 0u), 0, OAUX);
    }
    GAP();
  }
  STAMPO(23);
  const unsigned fb = k.xcd * 28 + ffn_j(k);
  unsigned ga_ = sc + 8192 + wid * 128 + (lane >> 5) * 64 + (lane & 1) * 32 + ((lane >> 4) & 1) * 16, gr_ = sc + 8192 + (lane & 31) * 4, gy_ = W_YG + ((fb >> 2) << 14) + (((wid >> 1) << 13) ^ k.nb) + ((fb & 3) << 11) + (tid & 127) * 16;
  asm volatile("" : "+v"(ga_), "+v"(gr_), "+v"(gy_));
  HOP_PAGE(0);
  KEYS_WAIT(TE ? 4 : 10);
  for (unsigned spins = 0;;) {
    if ((lane & 31) == grp) kk = u32x2{gbest, tag};
    const bool kv = kk[1] == tag;
    best = rfl(wave_maxu(kv ? kk[0] : 0u));
    const unsigned w = ((unsigned)key_row(best) >> 6) & 31u;
    if (__all(kv & tags2(h0, tag) & tags2(h1, tag)) && w == spec) break;
    spec = w;
    if (spin_fail<SL2>(spins, k.ws, tag)) break;
    KEYS(); KEYS_WAIT(0);
  }
  STAMPO(24);
  const unsigned idx = (unsigned)key_row(best) & (S - 1), wgrp = idx >> 6;
  if (!ffn_idle(k)) {
    float a[16];
    const unsigned hk[4] = {h0[0], h0[2], h1[0], h1[2]};
#pragma unroll
    for (int e = 0; e < 8; ++e) {
      float sg = 0.f, su = 0.f;
#pragma unroll
      for (int rp = 0; rp < 4; ++rp) { sg = dot2(__float_as_uint(A3[8 * rp + (e & 1) * 4 + (e >> 1)]), hk[rp], sg); su = dot2(UP[8 * rp + (e & 1) * 4 + (e >> 1)], hk[rp], su); }
      a[e] = sg; a[8 + e] = su;
    }
    float b[8], cc[4];
#pragma unroll
    for (int e = 0; e < 8; ++e) b[e] = fold32(a[e], a[8 + e]);
#pragma unroll
    for (int e = 0; e < 4; ++e) cc[e] = fold16(b[e], b[4 + e]);
#pragma unroll
    for (int e = 0; e < 4; ++e) { cc[e] += dpp_f<0x128>(cc[e]); cc[e] += dpp_f<0x124>(cc[e]); cc[e] += dpp_f<0x122>(cc[e]); }
    if ((lane & 14) == 0) lds_st(ga_, f32x4{cc[0], cc[1], cc[2], cc[3]});
    BARRIER_LGKM();
    const unsigned ra = gr_;
    const float s = rbf((lds_ld<float>(ra) + lds_ld<float>(ra + 128)) + (lds_ld<float>(ra + 256) + lds_ld<float>(ra + 384)));
    auto su = __builtin_amdgcn_permlane16_swap(__float_as_uint(s), __float_as_uint(s), false, false);
    const float gg = __uint_as_float(su[0]);
    const float act = silu_bf(gg) * __uint_as_float(su[1]);
    const unsigned ap = cvtpk(act, dpp_f<0xB1>(act));
    f32x4 y = {0.f, 0.f, 0.f, 0.f};
#pragma unroll
    for (int rp = 0; rp < 8; ++rp) {
      const unsigned w = __builtin_amdgcn_readlane(ap, 2 * rp);
#pragma unroll
      for (int e = 0; e < 4; ++e) y[e] = dot2(DN[4 * rp + (e & 1) * 2 + (e >> 1)], w, y[e]);
    }
    st16(k.ws, gy_, u32x4{cvtpk(y[0], y[1]), tag, cvtpk(y[2], y[3]), tag});
  }
  STAMPO(25);
  GAP();
  if (!early || hold) {
#pragma unroll
    for (int rb = 0; rb < 4; ++rb) {
      const bool cr = lrow == 16 * rb + r16;
      __builtin_amdgcn_raw_buffer_store_b128(ow[rb], srdo, (oo + rb * (16 * DM * 2)) | ((wgrp == grp && cr) || (early && !cr) ? 0x80000000u : 0u), 0, OAUX);
    }
  }
  GAP();
  if (bid == 0 && tid == 0) st4(k.ws, W_EPOCH, tag);
  constexpr unsigned NFIN = 16, FST = 128;
  if (ffn_idle(k)) {
    const unsigned cl = 32 * k.kvh + 2 * (tid & 15), col = cblk * 128 + cl;
    const unsigned yo = W_YG + ((tid >> 5) << 14) + k.cs + (((tid >> 3) & 3) << 11) + (cblk & 3) * 512 + k.kvh * 128 + (tid & 7) * 16;
    const unsigned co = W_CANDH + ((wgrp >> 1) << 14) + k.cs + ((wgrp & 1) << 12) + col * 4, cx = W_CANDX + ((wgrp >> 2) << 14) + k.cs + ((wgrp & 3) << 11) + (col & 511) * 4;
    f32x2 gb2 = lds_ld<f32x2>(k.ldsb + TABU + k.kvh * 4096 + 2560 + 384 + (cl & 31) * 4);
    float rs = rbf(__builtin_amdgcn_rcpf(1.f + __expf(-key_val(best))));
    unsigned oaddr = idx * (DM * 2) + col * 2, fa = sc + 12288 + tid * 8, fw = sc + 12288 + wid * 128 + (lane & 7) * 16 + (lane >> 4) * 4;
    u32x4 p[7]; u32x2 hh, xx;
    asm volatile("" : "+v"(rs), "+v"(oaddr), "+v"(fa), "+v"(fw), "+v"(gb2));
    for (unsigned spins = 0;;) {
      hh = ld8(k.ws, co); xx = ld8(k.ws, cx);
#pragma unroll
      for (int r = 0; r < 7; ++r) p[r] = __builtin_amdgcn_raw_buffer_load_b128(k.ws, yo, r * 131072, 16);
      bool ok = (hh[1] == tag) & (xx[1] == tag);
#pragma unroll
      for (int r = 0; r < 7; ++r) ok &= tags2(p[r], tag);
      if (__all(ok)) break;
      if (spin_fail<SL3>(spins, k.ws, tag)) break;
    }
    f32x2 ya = up2(p[0][0]), yb = up2(p[0][2]);
#pragma unroll
    for (int r = 1; r < 7; ++r) { ya += up2(p[r][0]); yb += up2(p[r][2]); }
    float z = fold16(fold32(ya[0], yb[0]), fold32(ya[1], yb[1]));
    z += dpp_f<0x128>(z);
    if (!(lane & 8)) lds_st(fw, z);
    BARRIER_LGKM();
    if (tid < NFIN) {
      f32x2 yy = (lds_ld<f32x2>(fa) + lds_ld<f32x2>(fa + FST)) + (lds_ld<f32x2>(fa + 2 * FST) + lds_ld<f32x2>(fa + 3 * FST));
      yy = up2(cvtpk(yy[0], yy[1])) * rs;
      yy = up2(cvtpk(yy[0], yy[1])) + up2(hh[0]);
      yy = up2(cvtpk(yy[0], yy[1])) * gb2;
      yy = up2(cvtpk(yy[0], yy[1])) + up2(xx[0]);
      __builtin_amdgcn_raw_buffer_store_b32(cvtpk(yy[0], yy[1]), srdo, oaddr, 0, OAUX);
    }
  }
  STAMPO(26);
}
__global__ __launch_bounds__(256, 1) __attribute__((aligned(4096))) void k_fused(Args a) {
  Ctx k;
  STAMP_DECL;
  body(k STAMP_PASS);
  asm volatile(HOP_DEFS);
}

extern "C" void kernel_launch(void* const* d_in, const int* in_sizes, int n_in, void* d_out, int out_size, void* d_ws, size_t ws_size,
                              hipStream_t stream) {
  auto D = [](const void* p, unsigned bytes) { Desc d; d.p = p; d.bytes = bytes; d.w3 = SRD3; return d; };
  const size_t win = (size_t)(SKV - WIN) * DH * 2;
  Args a;
  a.x = D(d_in[0], S * DM * 2); a.w_mod = D(d_in[11], DM * 6 * DM * 2); a.c = D(d_in[10], DM * 2); a.ws = D(d_ws, W_END);
  a.wq = D(d_in[3], DM * QD * 2); a.wo = D(d_in[4], QD * DM * 2); a.wd = D(d_in[9], 0); a.w_r = D(d_in[6], DM * 2);
  a.kc = D((const char*)d_in[1] + win, WIN * DH * 2); a.vc = D((const char*)d_in[2] + win, WIN * DH * 2); a.wg = D(d_in[7], DM * FF * 2); a.wu = D(d_in[8], DM * FF * 2);
  a.out = D(d_out, S * DM * 2);
  (void)hipFuncSetAttribute((const void*)k_fused, hipFuncAttributeMaxDynamicSharedMemorySize, 163840);
  k_fused<<<256, 256, 163840, stream>>>(a);
}
```

```cpp
#include <hip/hip_runtime.h>
#include <hip/hip_bf16.h>
#include <stdint.h>
#include <utility>

typedef unsigned short u16;
using bf16x8 = __attribute__((ext_vector_type(8))) short;
using s16x4  = __attribute__((ext_vector_type(4))) short;
using f32x16 = __attribute__((ext_vector_type(16))) float;
using u32x16 = __attribute__((ext_vector_type(16))) unsigned;
using f32x4  = __attribute__((ext_vector_type(4))) float;
using f32x2  = __attribute__((ext_vector_type(2))) float;
using u32x4  = __attribute__((ext_vector_type(4))) unsigned;
using u32x2  = __attribute__((ext_vector_type(2))) unsigned;
using u32x8 = __attribute__((ext_vector_type(8))) unsigned;
using f32x8 = __attribute__((ext_vector_type(8))) float;
using f32x32 = __attribute__((ext_vector_type(32))) float;
using u32x32 = __attribute__((ext_vector_type(32))) unsigned;
typedef __bf16 bf16x2_t __attribute__((ext_vector_type(2)));
#define DI __device__ __forceinline__
#define NTMASK 0
#define NT_SEL(bit) ((NTMASK >> (bit)) & 1)
#if NT_SEL(0)
#define NTX " nt"
#else
#define NTX ""
#endif
#if NT_SEL(1)
#define NTM " nt"
#else
#define NTM ""
#endif
#if NT_SEL(2)
#define NTQ " nt"
#else
#define NTQ ""
#endif
#if NT_SEL(3)
#define NTO " nt"
#else
#define NTO ""
#endif
#if NT_SEL(4)
#define NTG " nt"
#else
#define NTG ""
#endif
#if NT_SEL(5)
#define NTD " nt"
#else
#define NTD ""
#endif
#if NT_SEL(6)
#define NTT " nt"
#else
#define NTT ""
#endif
#if NT_SEL(7)
#define NTF " nt"
#else
#define NTF ""
#endif

constexpr int S = 2048, DM = 1024, HQ = 32, HKV = 4, DH = 128, SKV = 2048, WIN = 512, FF = 3584;
constexpr int QD = HQ * DH;
constexpr float QSCALE = 0.12751743082f;

DI float bf2f(u16 v) { return __uint_as_float(((unsigned)v) << 16); }
DI u16 f2bf(float x) { unsigned u = __float_as_uint(x); u += 0x7fffu + ((u >> 16) & 1u); return (u16)(u >> 16); }
DI unsigned cvtpk(float lo, float hi) { f32x2 v = {lo, hi}; bf16x2_t b = __builtin_convertvector(v, bf16x2_t); return __builtin_bit_cast(unsigned, b); }
DI float lo16f(unsigned w) { return __uint_as_float(w << 16); }
DI float hi16f(unsigned w) { return __uint_as_float(w & 0xffff0000u); }
DI float rbf(float x) { return lo16f(cvtpk(x, x)); }
DI f32x2 up2(unsigned w) { return f32x2{lo16f(w), hi16f(w)}; }
DI float dot2(unsigned a, unsigned b, float c) { return __builtin_amdgcn_fdot2_f32_bf16(__builtin_bit_cast(bf16x2_t, a), __builtin_bit_cast(bf16x2_t, b), c, false); }
DI unsigned plo(unsigned a, unsigned b) { return __builtin_amdgcn_perm(b, a, 0x05040100u); }
DI unsigned phi(unsigned a, unsigned b) { return __builtin_amdgcn_perm(b, a, 0x07060302u); }
DI unsigned rfl(unsigned x) { return __builtin_amdgcn_readfirstlane(x); }
template <int CTRL> DI float dpp_f(float v) { return __int_as_float(__builtin_amdgcn_update_dpp(0, __float_as_int(v), CTRL, 0xf, 0xf, true)); }
template <int CTRL> DI unsigned dpp_u(unsigned v) { return (unsigned)__builtin_amdgcn_update_dpp(0, (int)v, CTRL, 0xf, 0xf, true); }
DI float fold16(float a, float b) { auto r = __builtin_amdgcn_permlane16_swap(__float_as_uint(a), __float_as_uint(b), false, false); return __uint_as_float(r[0]) + __uint_as_float(r[1]); }
DI float fold32(float a, float b) { auto r = __builtin_amdgcn_permlane32_swap(__float_as_uint(a), __float_as_uint(b), false, false); return __uint_as_float(r[0]) + __uint_as_float(r[1]); }
DI float sum16(float v) { return fold16(v, v); }
DI float sum32(float v) { return fold32(v, v); }
DI float sum_row16(float v) { v += dpp_f<0xB1>(v); v += dpp_f<0x4E>(v); v += dpp_f<0x141>(v); v += dpp_f<0x140>(v); return v; }
DI float wave_sum(float v) { return sum32(sum16(sum_row16(v))); }
DI unsigned wave_maxu(unsigned v) {
  v = max(v, dpp_u<0xB1>(v)); v = max(v, dpp_u<0x4E>(v)); v = max(v, dpp_u<0x141>(v)); v = max(v, dpp_u<0x140>(v));
  { auto r = __builtin_amdgcn_permlane16_swap(v, v, false, false); v = max(r[0], r[1]); }
  { auto r = __builtin_amdgcn_permlane32_swap(v, v, false, false); v = max(r[0], r[1]); }
  return v;
}
#define GAP() __builtin_amdgcn_sched_barrier(0)
template <int N> using iseq = std::make_integer_sequence<int, N>;
template <int... Is> using ilist = std::integer_sequence<int, Is...>;
typedef __amdgpu_buffer_rsrc_t srd_t;
#define SRD3 0x00020000
DI srd_t mk_srd(const void* p, unsigned bytes) { return __builtin_amdgcn_make_buffer_rsrc((void*)p, (short)0, (int)bytes, SRD3); }
DI unsigned lane_id() { unsigned l; asm volatile("v_mbcnt_lo_u32_b32 %0, -1, 0\n\tv_mbcnt_hi_u32_b32 %0, -1, %0" : "=v"(l)); return l; }
struct Desc { const void* p; unsigned bytes, w3; };
struct Args { Desc x, w_mod, c, ws, wq, wo, wd, w_r, kc, vc, wg, wu, out; };
typedef const __attribute__((address_space(4))) Args* argp_t;
DI argp_t args() { argp_t p = (argp_t)__builtin_amdgcn_kernarg_segment_ptr(); asm volatile("" : "+s"(p)); return p; }
#define BARRIER_LGKM() do { asm volatile("s_waitcnt lgkmcnt(0)\n\ts_barrier" ::: "memory"); } while (0)

constexpr unsigned W_EPOCH = 0x260000, W_TMO = 256;
constexpr unsigned W_FLAGH = 0x261000;
constexpr unsigned W_VOTE = 0x2000, W_PROBE = 0x4000;
constexpr unsigned W_GKEY = 0x10000;
constexpr unsigned W_FLAGR = 0x8000;
constexpr unsigned W_FLAGO = 0x20000;
constexpr unsigned W_CANDH = 0x40000;
constexpr unsigned W_STAT = 0x80000;
constexpr unsigned W_YG = 0x100000;
constexpr unsigned W_CANDX = 0x1E0000;
constexpr unsigned W_ST1 = 0x200000;
constexpr unsigned W_MODA = 0x210000;
constexpr unsigned W_MODB = 0x220000;
constexpr unsigned W_SIL = 0x240000;
constexpr unsigned W_HT = 4u << 20;
constexpr unsigned W_OT = 8u << 20;
constexpr unsigned W_XCH = 24u << 20;
constexpr unsigned W_END = 40u << 20;
constexpr unsigned TAGM = 0x40004000u, TAGC = 0x9E3739B9u;
#define SPINCAP 262144
constexpr unsigned kSpinCap = SPINCAP;
#define SL0 1
#define SL1 1
#define SL2 1
#define SL3 1
template <int SL = 1> DI bool spin_fail(unsigned& spins, srd_t ws, unsigned tag) {
  __builtin_amdgcn_s_sleep(SL);
  if (++spins > kSpinCap) { __builtin_amdgcn_raw_buffer_store_b32(tag, ws, W_TMO, 0, 16); return true; }
  return (spins & 255u) == 0u && rfl(__builtin_amdgcn_raw_buffer_load_b32(ws, W_TMO, 0, 16)) == tag;
}
DI u32x4 ld16(srd_t ws, unsigned off) { return __builtin_amdgcn_raw_buffer_load_b128(ws, off, 0, 16); }
DI u32x2 ld8(srd_t ws, unsigned off) { return __builtin_amdgcn_raw_buffer_load_b64(ws, off, 0, 16); }
DI void st16(srd_t ws, unsigned off, u32x4 v) { __builtin_amdgcn_raw_buffer_store_b128(v, ws, off, 0, 16); }
DI void st8(srd_t ws, unsigned off, u32x2 v) { __builtin_amdgcn_raw_buffer_store_b64(v, ws, off, 0, 16); }
DI void st4(srd_t ws, unsigned off, unsigned v) { __builtin_amdgcn_raw_buffer_store_b32(v, ws, off, 0, 16); }
DI void wait_flags(srd_t ws, unsigned off, unsigned tag) {
  const unsigned o = off + (lane_id() & 31u) * 4u;
  for (unsigned spins = 0;;) {
    const unsigned f = __builtin_amdgcn_raw_buffer_load_b32(ws, o, 0, 16);
    if (__all(f == tag)) break;
    if (spin_fail(spins, ws, tag)) break;
  }
  asm volatile("" ::: "memory");
}
template <int OFF, bool DEV = false> DI u32x4 ald16(srd_t s, unsigned vo, unsigned so) {
  u32x4 r;
  if constexpr (DEV) asm volatile("buffer_load_dwordx4 %0, %1, %2, %3 offen offset:%4 sc1" : "=v"(r) : "v"(vo), "s"(s), "s"(so), "i"(OFF) : "memory");
  else               asm volatile("buffer_load_dwordx4 %0, %1, %2, %3 offen offset:%4" NTF : "=v"(r) : "v"(vo), "s"(s), "s"(so), "i"(OFF) : "memory");
  return r;
}
template <int OFF> DI u32x2 ald8(srd_t s, unsigned vo, unsigned so) {
  u32x2 r; asm volatile("buffer_load_dwordx2 %0, %1, %2, %3 offen offset:%4" : "=v"(r) : "v"(vo), "s"(s), "s"(so), "i"(OFF) : "memory"); return r;
}
DI u32x2 ald8d(srd_t s, unsigned vo) { u32x2 r; asm volatile("buffer_load_dwordx2 %0, %1, %2, 0 offen sc1" : "=v"(r) : "v"(vo), "s"(s) : "memory"); return r; }
template <int OFF, bool DEV = false> DI unsigned ald4(srd_t s, unsigned vo, unsigned so) {
  unsigned r;
  if constexpr (DEV) asm volatile("buffer_load_dword %0, %1, %2, %3 offen offset:%4 sc1" : "=v"(r) : "v"(vo), "s"(s), "s"(so), "i"(OFF) : "memory");
  else               asm volatile("buffer_load_dword %0, %1, %2, %3 offen offset:%4" : "=v"(r) : "v"(vo), "s"(s), "s"(so), "i"(OFF) : "memory");
  return r;
}
DI void touch(unsigned& d, const void* base, unsigned vo) { asm volatile("global_load_dword %0, %1, %2" : "+v"(d) : "v"(vo), "s"(base) : "memory"); }
DI void touchq(unsigned& d, const void* base, unsigned vo) { asm volatile("global_load_dword %0, %1, %2" NTQ : "+v"(d) : "v"(vo), "s"(base) : "memory"); }
template <class T> DI T lds_ld(unsigned a) { return *reinterpret_cast<const __attribute__((address_space(3))) T*>(a); }
template <class T> DI void lds_st(unsigned a, T v) { *reinterpret_cast<__attribute__((address_space(3))) T*>(a) = v; }
DI float silu_bf(float v) { return rbf(v * __builtin_amdgcn_rcpf(1.f + __expf(-v))); }
struct Ctx { static constexpr unsigned ldsb = 0; unsigned t0, pm, bid, wid, xcd, j, kvh, hl, rblk, tag, cs, nb; srd_t ws; };
#define TAIL2 1
DI unsigned ffn_j(const Ctx& k) { return TAIL2 ? (k.j < 24 ? k.j : 24 + (k.j & 3)) : min(k.j, 27u); }
DI bool ffn_idle(const Ctx& k) { return TAIL2 ? (k.j >> 2) == 6 + (k.xcd & 1) : k.j >= 28; }
constexpr unsigned UNIT = 16384;
#define STAMPA(I) do { } while (0)
#define STAMPO(I) do { } while (0)
#define STAMP_DECL
#define STAMP(I) do { } while (0)
#define STAMP_FLUSH() do { } while (0)
#define STAMP_ARG
#define STAMP_PASS
#define ACLOB_LO "a0","a1","a2","a3","a4","a5","a6","a7","a8","a9","a10","a11","a12","a13","a14","a15","a16","a17","a18","a19","a20","a21","a22","a23","a24","a25","a26","a27","a28","a29","a30","a31","a32","a33","a34","a35","a36","a37","a38","a39","a40","a41","a42","a43","a44","a45","a46","a47","a48","a49","a50","a51","a52","a53","a54","a55","a56","a57","a58","a59","a60","a61","a62","a63","a64","a65","a66","a67","a68","a69","a70","a71","a72","a73","a74","a75","a76","a77","a78","a79","a80","a81","a82","a83","a84","a85","a86","a87","a88","a89","a90","a91","a92","a93","a94","a95","a96","a97","a98","a99","a100","a101","a102","a103","a104","a105","a106","a107","a108","a109","a110","a111","a112","a113","a114","a115","a116","a117","a118","a119","a120","a121","a122","a123","a124","a125","a126","a127"
#define ACLOB_HI "a128","a129","a130","a131","a132","a133","a134","a135","a136","a137","a138","a139","a140","a141","a142","a143","a144","a145","a146","a147","a148","a149","a150","a151","a152","a153","a154","a155","a156","a157","a158","a159","a160","a161","a162","a163","a164","a165","a166","a167","a168","a169","a170","a171","a172","a173","a174","a175","a176","a177","a178","a179","a180","a181","a182","a183","a184","a185","a186","a187","a188","a189","a190","a191","a192","a193","a194","a195","a196","a197","a198","a199","a200","a201","a202","a203","a204","a205","a206","a207","a208","a209","a210","a211","a212","a213","a214","a215","a216","a217","a218","a219","a220","a221","a222","a223","a224","a225","a226","a227","a228","a229","a230","a231","a232","a233","a234","a235","a236","a237","a238","a239","a240","a241","a242","a243","a244","a245","a246","a247","a248","a249","a250","a251","a252","a253","a254","a255"
#define HOP_DEFS ".set .Lhc00, .Lhs0\n\t.set .Lhs0n, .Lhs4\n\t.set .Lhs4n, .Lhs8\n\t.set .Lhs8n, .Lhr0\n\t.set .Lhc01, .Lhs1\n\t.set .Lhs1n, .Lhs5\n\t.set .Lhs5n, .Lhs9\n\t.set .Lhs9n, .Lhr0\n\t.set .Lhc02, .Lhs2\n\t.set .Lhs2n, .Lhs6\n\t.set .Lhs6n, .Lhs10\n\t.set .Lhs10n, .Lhr0\n\t.set .Lhc03, .Lhs3\n\t.set .Lhs3n, .Lhs7\n\t.set .Lhs7n, .Lhr0\n\t.set .Lhc10, .Lhs11\n\t.set .Lhs11n, .Lhs15\n\t.set .Lhs15n, .Lhr1\n\t.set .Lhc11, .Lhs12\n\t.set .Lhs12n, .Lhs16\n\t.set .Lhs16n, .Lhr1\n\t.set .Lhc12, .Lhs13\n\t.set .Lhs13n, .Lhr1\n\t.set .Lhc13, .Lhs14\n\t.set .Lhs14n, .Lhr1"
#define HOP_PAGE(n) asm volatile("s_branch .Lht" #n "\n\t.p2align 12\n.Lht" #n ":\n\ts_cbranch_execz .Lht" #n "n")
#define PAGE_ALIGN() asm volatile("s_branch .Lpa%=\n\t.p2align 12\n.Lpa%=:" ::: "memory")
#define HOP_FRONT() asm volatile("s_branch .Lhf\n\t.p2align 12\n.Lhf:\n\ts_cbranch_execz .Lhfn")
#define HOP_GO(g, w) asm volatile("s_mov_b64 exec, 0\n\ts_cmp_eq_u32 %0, 0\n\ts_cbranch_scc1 .Lhc" #g "0\n\ts_cmp_eq_u32 %0, 1\n\ts_cbranch_scc1 .Lhc" #g "1\n\ts_cmp_eq_u32 %0, 2\n\ts_cbranch_scc1 .Lhc" #g "2\n\ts_branch .Lhc" #g "3\n.Lhr" #g ":\n\ts_mov_b64 exec, -1" :: "s"(w) : "scc", "memory")
#define FQ_STR2(x) #x
#define FQ_STR(x) FQ_STR2(x)
#define FQ_CAP FQ_STR(SPINCAP)
DI bool tags2(const u32x4& a, unsigned tag) { return (a[1] == tag) & (a[3] == tag); }
DI void bcast4(float v, float (&o)[4]) {
  auto a = __builtin_amdgcn_permlane32_swap(__float_as_uint(v), __float_as_uint(v), false, false);
  auto b = __builtin_amdgcn_permlane16_swap(a[0], a[0], false, false);
  auto c = __builtin_amdgcn_permlane16_swap(a[1], a[1], false, false);
  o[0] = __uint_as_float(b[0]); o[1] = __uint_as_float(b[1]); o[2] = __uint_as_float(c[0]); o[3] = __uint_as_float(c[1]);
}
#define FSTP(x)
#define FSTS(x)
#define FSTA(x)
#define FQ_ASM \
  FSTA("v_mov_b32 v255, 0\n\t") \
  FSTA("s_memrealtime s[94:95]\n\t") \
  FSTA("s_memtime s[92:93]\n\t") \
  FSTA("s_waitcnt lgkmcnt(0)\n\t") \
  FSTA("s_or_b32 s94, s94, 1\n\t") \
  FSTA("v_writelane_b32 v255, s94, 0\n\t") \
  FSTA("v_writelane_b32 v255, s92, 1\n\t") \
  "s_mov_b32 s37, 1\n\t" \
  ".Lfr_again:\n\t" \
  "s_load_dwordx16 s[4:19], s[0:1], 0x0\n\t" \
  "s_load_dwordx16 s[20:35], s[0:1], 0x40\n\t" \
  "s_load_dwordx16 s[60:75], s[0:1], 0x80\n\t" \
  "v_readfirstlane_b32 s3, v0\n\t" \
  "v_and_b32 v254, 63, v0\n\t" \
  "s_lshr_b32 s3, s3, 6\n\t" \
  "s_lshr_b32 s39, s2, 3\n\t" \
  "s_lshl_b32 s54, s3, 5\n\t" \
  "s_add_u32 s54, s54, s39\n\t" \
  "s_lshl_b32 s54, s54, 7\n\t" \
  "v_lshl_add_u32 v48, v254, 14, s54\n\t" \
  "v_min_u32 v48, .Lfz_z-.Lfz_a-4, v48\n\t" \
  "s_getpc_b64 s[96:97]\n\t" \
  ".Lfz_a:\n\t" \
  "global_load_dword v246, v48, s[96:97]\n\t" \
  "s_and_b32 s40, s2, 3\n\t" \
  "v_lshrrev_b32 v52, 2, v0\n\t" \
  "v_lshlrev_b32 v244, 4, v254\n\t" \
  "v_lshlrev_b32 v53, 3, v52\n\t" \
  "v_mul_u32_u24 v52, 49152, v52\n\t" \
  "v_and_or_b32 v52, v244, 48, v52\n\t" \
  "s_lshl_b32 s44, s40, 9\n\t" \
  "s_mul_i32 s45, s40, 3145728\n\t" \
  "s_bfe_u32 s52, s2, 0x10002\n\t" \
  "s_lshl_b32 s52, s52, 5\n\t" \
  "s_add_u32 s52, s52, s39\n\t" \
  "s_lshl_b32 s54, s52, 6\n\t" \
  "s_add_u32 s45, s45, s54\n\t" \
  "s_add_u32 s96, s45, 12288\n\t" \
  "s_add_u32 s97, s45, 24576\n\t" \
  "s_add_u32 s99, s45, 36864\n\t" \
  "v_mov_b32 v1, 2490368\n\t" \
  "s_and_b32 s98, s2, 4\n\t" \
  "s_lshr_b32 s54, s2, 6\n\t" \
  "s_or_b32 s98, s98, s54\n\t" \
  "s_lshl_b32 s41, s98, 19\n\t" \
  "s_lshl_b32 s38, s3, 12\n\t" \
  "s_lshl_b32 s77, s3, 13\n\t" \
  "s_and_b32 s54, s2, 0x38\n\t" \
  "s_lshl_b32 s54, s54, 11\n\t" \
  "s_lshl_b32 s55, s40, 17\n\t" \
  "s_or_b32 s46, s41, s38\n\t" \
  "s_or_b32 s46, s46, s54\n\t" \
  "s_or_b32 s46, s46, s55\n\t" \
  "s_xor_b32 s87, s55, 393216\n\t" \
  "v_lshrrev_b32 v245, 4, v254\n\t" \
  "v_lshrrev_b32 v49, 3, v254\n\t" \
  "v_and_b32 v50, 7, v254\n\t" \
  "v_xor_b32 v51, v50, v245\n\t" \
  "v_lshlrev_b32 v51, 4, v51\n\t" \
  "v_lshl_add_u32 v212, v49, 11, v51\n\t" \
  "v_xor_b32 v213, 64, v212\n\t" \
  "v_add_u32 v213, 15360, v213\n\t" \
  "s_bfe_u32 s42, s2, 0x20004\n\t" \
  "s_lshl_b32 s54, s3, 15\n\t" \
  "s_or_b32 s47, s41, s54\n\t" \
  "s_or_b32 s47, s47, s44\n\t" \
  "s_lshl_b32 s54, s42, 7\n\t" \
  "s_or_b32 s47, s47, s54\n\t" \
  "s_and_b32 s43, s2, 8\n\t" \
  "s_lshl_b32 s54, s43, 15\n\t" \
  "s_or_b32 s57, s47, s87\n\t" \
  "s_xor_b32 s57, s57, s54\n\t" \
  "s_xor_b32 s58, s57, 131072\n\t" \
  "s_lshl_b32 s54, s43, 9\n\t" \
  "s_add_u32 s90, s77, s54\n\t" \
  "s_lshl_b32 s78, s40, 21\n\t" \
  "s_lshl_b32 s50, s39, 14\n\t" \
  "s_lshl2_add_u32 s50, s44, s50\n\t" \
  "s_mul_i32 s54, s3, 0x1080\n\t" \
  "s_and_b32 s54, s54, 0x2080\n\t" \
  "s_or_b32 s50, s50, s54\n\t" \
  "v_lshl_add_u32 v54, v50, 8, s50\n\t" \
  "v_and_b32 v51, 3, v49\n\t" \
  "v_lshl_add_u32 v51, v51, 19, v54\n\t" \
  "s_mov_b32 m0, s90\n\t" \
  "s_waitcnt lgkmcnt(0)\n\t" \
  "buffer_load_dwordx2 v[2:3], v53, s[12:15], s44 offen\n\t" \
  "buffer_load_dwordx4 v[4:7], v52, s[8:11], s45 offen nt\n\t" \
  "buffer_load_dwordx4 v[8:11], v52, s[8:11], s96 offen nt\n\t" \
  "buffer_load_dwordx4 v[12:15], v52, s[8:11], s97 offen nt\n\t" \
  "buffer_load_dwordx4 v[16:19], v52, s[8:11], s99 offen nt\n\t" \
  "s_add_u32 s16, s16, 0x1fffff\n\t" \
  "s_addc_u32 s17, s17, 0\n\t" \
  "s_and_b32 s16, s16, 0xffe00000\n\t" \
  "buffer_load_dword v1, v1, s[16:19], 0 offen sc1\n\t" \
  "s_mov_b32 s96, s46\n\t" \
  "v_mov_b32 v253, v0\n\t" \
  "v_and_b32 v247, 15, v254\n\t" \
  "s_bfe_u32 s43, s2, 0x10003\n\t" \
  "v_and_b32 v48, 3, v254\n\t" \
  "v_lshl_add_u32 v48, v48, 2, v245\n\t" \
  "s_lshl_b32 s54, s3, 8\n\t" \
  "s_add_u32 s55, s54, 147456\n\t" \
  "v_lshl_add_u32 v55, v48, 3, s55\n\t" \
  "v_lshlrev_b32 v56, 3, v247\n\t" \
  "v_add_u32 v56, 147456, v56\n\t" \
  "v_and_b32 v48, 0xf0, v0\n\t" \
  "v_lshl_or_b32 v57, v48, 24, v244\n\t" \
  "s_lshl_b32 s52, s52, 8\n\t" \
  "s_lshl_b32 s55, s40, 14\n\t" \
  "s_add_u32 s52, s52, s55\n\t" \
  "s_add_u32 s52, s52, 2162688\n\t" \
  "v_and_b32 v210, 31, v254\n\t" \
  "v_lshrrev_b32 v48, 5, v254\n\t" \
  "v_lshlrev_b32 v58, 4, v48\n\t" \
  "v_lshl_or_b32 v58, v210, 26, v58\n\t" \
  "s_lshr_b32 s46, s46, 7\n\t" \
  "s_add_u32 s46, s46, 2097152\n\t" \
  "v_cvt_f32_u32 v251, v48\n\t" \
  "v_lshlrev_b32 v248, 4, v210\n\t" \
  "v_lshl_or_b32 v248, v48, 13, v248\n\t" \
  "v_lshlrev_b32 v210, 2, v210\n\t" \
  "v_lshlrev_b32 v249, 4, v247\n\t" \
  "s_lshl_b32 s55, s3, 17\n\t" \
  "s_lshl_b32 s49, s43, 18\n\t" \
  "s_xor_b32 s55, s55, s49\n\t" \
  "buffer_load_dwordx4 v[32:35], v244, s[4:7], s96 offen offset:0\n\t" \
  "buffer_load_dwordx4 v[36:39], v244, s[4:7], s96 offen offset:1024\n\t" \
  "buffer_load_dwordx4 v[40:43], v244, s[4:7], s96 offen offset:2048\n\t" \
  "buffer_load_dwordx4 v[44:47], v244, s[4:7], s96 offen offset:3072\n\t" \
  "s_and_b32 s49, s2, 0x38\n\t" \
  "s_lshl2_add_u32 s55, s44, s55\n\t" \
  "s_lshl_b32 s49, s49, 5\n\t" \
  "s_add_u32 s55, s55, s49\n\t" \
  "v_lshlrev_b32 v48, 5, v245\n\t" \
  "v_xor_b32 v48, v48, v249\n\t" \
  "v_lshl_add_u32 v48, v245, 13, v48\n\t" \
  "v_add_u32 v200, s55, v48\n\t" \
  "v_xor_b32 v48, 0x80, v200\n\t" \
  "v_add_u32 v201, 31744, v200\n\t" \
  "v_add_u32 v202, 63488, v48\n\t" \
  "v_add_u32 v203, 95232, v48\n\t" \
  "s_add_u32 s76, s38, 32768\n\t" \
  "s_lshl_b32 s49, s42, 19\n\t" \
  "s_or_b32 s49, s49, s78\n\t" \
  "s_xor_b32 s80, s49, 1048576\n\t" \
  "s_add_u32 s55, s42, 1\n\t" \
  "s_and_b32 s55, s55, 3\n\t" \
  "s_xor_b32 s55, s55, s42\n\t" \
  "s_lshl_b32 s79, s55, 19\n\t" \
  "s_xor_b32 s79, s79, s49\n\t" \
  "s_xor_b32 s81, s79, 1048576\n\t" \
  "s_lshl_b32 s48, s55, 7\n\t" \
  "s_xor_b32 s48, s48, s47\n\t" \
  "s_xor_b32 s88, s47, 256\n\t" \
  "s_xor_b32 s89, s48, 256\n\t" \
  "v_lshl_add_u32 v48, s43, 2, v245\n\t" \
  "v_bfe_u32 v50, v254, 1, 3\n\t" \
  "v_xor_b32 v48, v48, v50\n\t" \
  "v_lshlrev_b32 v48, 4, v48\n\t" \
  "v_lshl_add_u32 v48, v247, 7, v48\n\t" \
  "v_add_u32 v204, s77, v48\n\t" \
  "v_xor_b32 v205, 64, v204\n\t" \
  "v_add_u32 v206, 98304, v204\n\t" \
  "v_add_u32 v207, 98304, v205\n\t" \
  "v_lshlrev_b32 v208, 5, v245\n\t" \
  "v_add_u32 v208, 148480, v208\n\t" \
  "v_lshlrev_b32 v50, 3, v254\n\t" \
  "s_lshl_b32 s55, s43, 7\n\t" \
  "s_or_b32 s55, s55, 256\n\t" \
  "v_xor_b32 v250, s55, v50\n\t" \
  "s_lshl1_add_u32 s55, s54, 148480\n\t" \
  "v_add_u32 v250, s55, v250\n\t" \
  "v_lshl_add_u32 v214, v245, 10, v249\n\t" \
  "v_lshl_add_u32 v214, s43, 12, v214\n\t" \
  "s_lshr_b32 s55, s2, 2\n\t" \
  "s_xor_b32 s55, s55, s3\n\t" \
  "s_and_b32 s55, s55, 1\n\t" \
  "s_lshl1_add_u32 s55, s3, s55\n\t" \
  "s_lshl_b32 s82, s55, 13\n\t" \
  "s_lshr_b32 s55, s2, 6\n\t" \
  "s_lshl_b32 s55, s55, 20\n\t" \
  "s_add_u32 s82, s82, s55\n\t" \
  "s_add_u32 s82, s82, 4194304\n\t" \
  "s_lshr_b32 s86, s49, 3\n\t" \
  "s_add_u32 s86, s86, s82\n\t" \
  "s_lshr_b32 s55, s87, 9\n\t" \
  "s_add_u32 s86, s86, s55\n\t" \
  "s_lshl2_add_u32 s84, s98, s3\n\t" \
  "s_lshl_b32 s84, s84, 7\n\t" \
  "s_add_u32 s84, s84, 2494464\n\t" \
  "s_bfe_u32 s55, s2, 0x30003\n\t" \
  "s_lshl3_add_u32 s55, s40, s55\n\t" \
  "s_lshl2_add_u32 s85, s55, s84\n\t" \
  "s_lshl_b32 s55, s43, 13\n\t" \
  "s_sub_u32 s83, 4096, s55\n\t" \
  "v_and_b32 v50, 0xf8, v50\n\t" \
  "v_bfe_u32 v48, v254, 2, 2\n\t" \
  "v_lshl_add_u32 v50, v48, 8, v50\n\t" \
  "v_lshl_add_u32 v50, v245, 11, v50\n\t" \
  "v_add_u32 v184, 32768, v50\n\t" \
  "v_xor_b32 v185, 32, v184\n\t" \
  "v_xor_b32 v186, 64, v184\n\t" \
  "v_xor_b32 v187, 96, v184\n\t" \
  "v_xor_b32 v188, 128, v184\n\t" \
  "v_xor_b32 v189, 160, v184\n\t" \
  "v_xor_b32 v190, 192, v184\n\t" \
  "v_xor_b32 v191, 224, v184\n\t" \
  "s_lshl_b32 s51, s98, 12\n\t" \
  "s_add_u32 s51, s51, s54\n\t" \
  "s_add_u32 s51, s51, 2097152\n\t" \
  "s_lshr_b32 s55, s87, 7\n\t" \
  "s_add_u32 s51, s51, s55\n\t" \
  "s_add_u32 s55, s42, s3\n\t" \
  "s_and_b32 s55, s55, 3\n\t" \
  "s_lshl2_add_u32 s55, s40, s55\n\t" \
  "s_lshl_b32 s55, s55, 9\n\t" \
  "s_add_u32 s53, s55, 2162688\n\t" \
  "v_min_u32 v49, 2, v49\n\t" \
  "s_bfe_u32 s55, s2, 0x10002\n\t" \
  "s_lshl2_add_u32 s55, s40, s55\n\t" \
  "s_add_u32 s55, s55, 4\n\t" \
  "v_lshl_add_u32 v49, v49, 1, s55\n\t" \
  "v_lshl_add_u32 v215, v49, 19, v54\n\t" \
  "v_add_u32 v216, 3145728, v215\n\t" \
  "v_and_b32 v215, 0x7fffff, v215\n\t" \
  "v_and_b32 v216, 0x7fffff, v216\n\t" \
  "s_mov_b32 s98, 0xffff0000\n\t" \
  "s_mov_b32 s45, 0x05040100\n\t" \
  "s_mov_b32 s50, 0x07060302\n\t" \
  "s_lshr_b32 s44, s2, 6\n\t" \
  "s_lshl_b32 s44, s44, 9\n\t" \
  "v_lshlrev_b32 v53, 1, v0\n\t" \
  "s_waitcnt vmcnt(9)\n\t" \
  "v_lshlrev_b32 v20, 16, v2\n\t" \
  "v_and_b32 v21, s98, v2\n\t" \
  "v_lshlrev_b32 v22, 16, v3\n\t" \
  "v_and_b32 v23, s98, v3\n\t" \
  "v_mul_f32 v24, 0xbfb8aa3b, v20\n\t" \
  "v_mul_f32 v25, 0xbfb8aa3b, v21\n\t" \
  "v_mul_f32 v26, 0xbfb8aa3b, v22\n\t" \
  "v_mul_f32 v27, 0xbfb8aa3b, v23\n\t" \
  "v_exp_f32 v24, v24\n\t" \
  "v_exp_f32 v25, v25\n\t" \
  "v_exp_f32 v26, v26\n\t" \
  "v_exp_f32 v27, v27\n\t" \
  "v_add_f32 v24, 1.0, v24\n\t" \
  "v_add_f32 v25, 1.0, v25\n\t" \
  "v_add_f32 v26, 1.0, v26\n\t" \
  "v_add_f32 v27, 1.0, v27\n\t" \
  "v_rcp_f32 v24, v24\n\t" \
  "v_rcp_f32 v25, v25\n\t" \
  "v_rcp_f32 v26, v26\n\t" \
  "v_rcp_f32 v27, v27\n\t" \
  "v_mul_f32 v20, v24, v20\n\t" \
  "v_mul_f32 v21, v25, v21\n\t" \
  "v_mul_f32 v22, v26, v22\n\t" \
  "v_mul_f32 v23, v27, v23\n\t" \
  "v_cvt_pk_bf16_f32 v2, v20, v21\n\t" \
  "v_cvt_pk_bf16_f32 v3, v22, v23\n\t" \
  FSTP("s_memrealtime s[94:95]\n\t") \
  FSTP("s_memtime s[92:93]\n\t") \
  FSTP("s_waitcnt lgkmcnt(0)\n\t") \
  FSTP("s_or_b32 s94, s94, 1\n\t") \
  FSTP("v_writelane_b32 v255, s94, 54\n\t") \
  FSTP("v_writelane_b32 v255, s92, 55\n\t") \
  "s_waitcnt vmcnt(5)\n\t" \
  FSTA("s_memrealtime s[94:95]\n\t") \
  FSTA("s_memtime s[92:93]\n\t") \
  FSTA("s_waitcnt lgkmcnt(0)\n\t") \
  FSTA("s_or_b32 s94, s94, 1\n\t") \
  FSTA("v_writelane_b32 v255, s94, 2\n\t") \
  FSTA("v_writelane_b32 v255, s92, 3\n\t") \
  "v_perm_b32 v28, v8, v4, s45\n\t" \
  "v_perm_b32 v29, v9, v5, s45\n\t" \
  "v_perm_b32 v30, v10, v6, s45\n\t" \
  "v_perm_b32 v31, v11, v7, s45\n\t" \
  "v_perm_b32 v4, v8, v4, s50\n\t" \
  "v_perm_b32 v5, v9, v5, s50\n\t" \
  "v_perm_b32 v6, v10, v6, s50\n\t" \
  "v_perm_b32 v7, v11, v7, s50\n\t" \
  "v_dot2_f32_bf16 v20, v28, v2, 0\n\t" \
  "v_dot2_f32_bf16 v21, v29, v2, 0\n\t" \
  "v_dot2_f32_bf16 v22, v30, v2, 0\n\t" \
  "v_dot2_f32_bf16 v23, v31, v2, 0\n\t" \
  "v_dot2_f32_bf16 v24, v4, v2, 0\n\t" \
  "v_dot2_f32_bf16 v25, v5, v2, 0\n\t" \
  "v_dot2_f32_bf16 v26, v6, v2, 0\n\t" \
  "v_dot2_f32_bf16 v27, v7, v2, 0\n\t" \
  "v_perm_b32 v28, v16, v12, s45\n\t" \
  "v_perm_b32 v29, v17, v13, s45\n\t" \
  "v_perm_b32 v30, v18, v14, s45\n\t" \
  "v_perm_b32 v31, v19, v15, s45\n\t" \
  "v_perm_b32 v12, v16, v12, s50\n\t" \
  "v_perm_b32 v13, v17, v13, s50\n\t" \
  "v_perm_b32 v14, v18, v14, s50\n\t" \
  "v_perm_b32 v15, v19, v15, s50\n\t" \
  "v_dot2_f32_bf16 v20, v28, v3, v20\n\t" \
  "v_dot2_f32_bf16 v21, v29, v3, v21\n\t" \
  "v_dot2_f32_bf16 v22, v30, v3, v22\n\t" \
  "v_dot2_f32_bf16 v23, v31, v3, v23\n\t" \
  "v_dot2_f32_bf16 v24, v12, v3, v24\n\t" \
  "v_dot2_f32_bf16 v25, v13, v3, v25\n\t" \
  "v_dot2_f32_bf16 v26, v14, v3, v26\n\t" \
  "v_dot2_f32_bf16 v27, v15, v3, v27\n\t" \
  "v_permlane32_swap_b32 v20, v22\n\t" \
  "v_permlane32_swap_b32 v21, v23\n\t" \
  "v_permlane32_swap_b32 v24, v26\n\t" \
  "v_permlane32_swap_b32 v25, v27\n\t" \
  "v_add_f32 v20, v20, v22\n\t" \
  "v_add_f32 v21, v21, v23\n\t" \
  "v_add_f32 v24, v24, v26\n\t" \
  "v_add_f32 v25, v25, v27\n\t" \
  "v_permlane16_swap_b32 v20, v21\n\t" \
  "s_nop 0\n\t" \
  "v_permlane16_swap_b32 v24, v25\n\t" \
  "v_add_f32 v20, v20, v21\n\t" \
  "v_add_f32 v21, v24, v25\n\t" \
  "s_nop 0\n\t" \
  "v_add_f32_dpp v20, v20, v20 row_ror:8 row_mask:0xf bank_mask:0xf\n\t" \
  "v_add_f32_dpp v21, v21, v21 row_ror:8 row_mask:0xf bank_mask:0xf\n\t" \
  "s_nop 0\n\t" \
  "v_add_f32_dpp v20, v20, v20 row_ror:4 row_mask:0xf bank_mask:0xf\n\t" \
  "v_add_f32_dpp v21, v21, v21 row_ror:4 row_mask:0xf bank_mask:0xf\n\t" \
  "ds_write_b64 v55, v[20:21]\n\t" \
  "buffer_load_dwordx4 v212, s[4:7], s57 offen lds\n\t" \
  "buffer_load_dwordx4 v213, s[4:7], s57 offen offset:1024 lds\n\t" \
  "s_add_u32 m0, s90, 2048\n\t" \
  "s_nop 0\n\t" \
  "buffer_load_dwordx4 v212, s[4:7], s58 offen lds\n\t" \
  "buffer_load_dwordx4 v213, s[4:7], s58 offen offset:1024 lds\n\t" \
  "s_xor_b32 m0, s90, 4096\n\t" \
  "s_xor_b32 s97, s57, 262144\n\t" \
  "buffer_load_dwordx4 v212, s[4:7], s97 offen lds\n\t" \
  "buffer_load_dwordx4 v213, s[4:7], s97 offen offset:1024 lds\n\t" \
  "s_xor_b32 m0, s90, 6144\n\t" \
  "s_xor_b32 s97, s57, 393216\n\t" \
  "buffer_load_dwordx4 v212, s[4:7], s97 offen lds\n\t" \
  "buffer_load_dwordx4 v213, s[4:7], s97 offen offset:1024 lds\n\t" \
  "v_mov_b32 v4, 0\n\t" \
  "v_mov_b32 v5, 0\n\t" \
  "v_mov_b32 v6, 0\n\t" \
  "v_mov_b32 v7, 0\n\t" \
  "s_waitcnt vmcnt(12) lgkmcnt(0)\n\t" \
  "s_barrier\n\t" \
  "ds_read_b64 v[8:9], v56 offset:0\n\t" \
  "ds_read_b64 v[10:11], v56 offset:256\n\t" \
  "ds_read_b64 v[12:13], v56 offset:512\n\t" \
  "ds_read_b64 v[14:15], v56 offset:768\n\t" \
  "v_readfirstlane_b32 s36, v1\n\t" \
  "s_or_b32 s36, s36, 0x40004000\n\t" \
  "s_add_u32 s36, s36, 0x9e3739b9\n\t" \
  "s_and_b32 s36, s36, 0xbfffbfff\n\t" \
  "v_mov_b32 v211, s36\n\t" \
  "v_mov_b32 v17, s36\n\t" \
  "v_mov_b32 v19, s36\n\t" \
  "s_waitcnt lgkmcnt(0)\n\t" \
  "v_add_f32 v8, v8, v10\n\t" \
  "v_add_f32 v12, v12, v14\n\t" \
  "v_add_f32 v9, v9, v11\n\t" \
  "v_add_f32 v13, v13, v15\n\t" \
  "v_add_f32 v16, v8, v12\n\t" \
  "v_add_f32 v18, v9, v13\n\t" \
  "buffer_store_dwordx4 v[16:19], v57, s[16:19], s52 offen sc1\n\t" \
  "s_waitcnt vmcnt(9)\n\t" \
  FSTA("s_memrealtime s[94:95]\n\t") \
  FSTA("s_memtime s[92:93]\n\t") \
  FSTA("s_waitcnt lgkmcnt(0)\n\t") \
  FSTA("s_or_b32 s94, s94, 1\n\t") \
  FSTA("v_writelane_b32 v255, s94, 4\n\t") \
  FSTA("v_writelane_b32 v255, s92, 5\n\t") \
  "v_dot2c_f32_bf16 v4, 0x3f803f80, v32\n\t" \
  "v_dot2c_f32_bf16 v6, v32, v32\n\t" \
  "v_dot2c_f32_bf16 v5, 0x3f803f80, v40\n\t" \
  "v_dot2c_f32_bf16 v7, v40, v40\n\t" \
  "v_dot2c_f32_bf16 v4, 0x3f803f80, v33\n\t" \
  "v_dot2c_f32_bf16 v6, v33, v33\n\t" \
  "v_dot2c_f32_bf16 v5, 0x3f803f80, v41\n\t" \
  "v_dot2c_f32_bf16 v7, v41, v41\n\t" \
  "v_dot2c_f32_bf16 v4, 0x3f803f80, v34\n\t" \
  "v_dot2c_f32_bf16 v6, v34, v34\n\t" \
  "v_dot2c_f32_bf16 v5, 0x3f803f80, v42\n\t" \
  "v_dot2c_f32_bf16 v7, v42, v42\n\t" \
  "v_dot2c_f32_bf16 v4, 0x3f803f80, v35\n\t" \
  "v_dot2c_f32_bf16 v6, v35, v35\n\t" \
  "v_dot2c_f32_bf16 v5, 0x3f803f80, v43\n\t" \
  "v_dot2c_f32_bf16 v7, v43, v43\n\t" \
  "v_dot2c_f32_bf16 v4, 0x3f803f80, v36\n\t" \
  "v_dot2c_f32_bf16 v6, v36, v36\n\t" \
  "v_dot2c_f32_bf16 v5, 0x3f803f80, v44\n\t" \
  "v_dot2c_f32_bf16 v7, v44, v44\n\t" \
  "v_dot2c_f32_bf16 v4, 0x3f803f80, v37\n\t" \
  "v_dot2c_f32_bf16 v6, v37, v37\n\t" \
  "v_dot2c_f32_bf16 v5, 0x3f803f80, v45\n\t" \
  "v_dot2c_f32_bf16 v7, v45, v45\n\t" \
  "v_dot2c_f32_bf16 v4, 0x3f803f80, v38\n\t" \
  "v_dot2c_f32_bf16 v6, v38, v38\n\t" \
  "v_dot2c_f32_bf16 v5, 0x3f803f80, v46\n\t" \
  "v_dot2c_f32_bf16 v7, v46, v46\n\t" \
  "v_dot2c_f32_bf16 v4, 0x3f803f80, v39\n\t" \
  "v_dot2c_f32_bf16 v6, v39, v39\n\t" \
  "v_dot2c_f32_bf16 v5, 0x3f803f80, v47\n\t" \
  "v_dot2c_f32_bf16 v7, v47, v47\n\t" \
  "v_mov_b32 v21, s36\n\t" \
  "v_mov_b32 v23, s36\n\t" \
  "v_permlane32_swap_b32 v4, v5\n\t" \
  "v_permlane32_swap_b32 v6, v7\n\t" \
  "v_add_f32 v4, v4, v5\n\t" \
  "v_add_f32 v6, v6, v7\n\t" \
  "s_nop 1\n\t" \
  "v_permlane16_swap_b32 v4, v6\n\t" \
  "v_add_f32 v4, v4, v6\n\t" \
  "s_nop 1\n\t" \
  "v_add_f32_dpp v4, v4, v4 quad_perm:[1,0,3,2] row_mask:0xf bank_mask:0xf\n\t" \
  "s_nop 1\n\t" \
  "v_add_f32_dpp v4, v4, v4 quad_perm:[2,3,0,1] row_mask:0xf bank_mask:0xf\n\t" \
  "s_nop 1\n\t" \
  "v_add_f32_dpp v4, v4, v4 row_half_mirror row_mask:0xf bank_mask:0xf\n\t" \
  "s_nop 1\n\t" \
  "v_add_f32_dpp v4, v4, v4 row_mirror row_mask:0xf bank_mask:0xf\n\t" \
  "v_mul_f32 v4, 0x3a800000, v4\n\t" \
  "v_mov_b32 v5, v4\n\t" \
  "s_nop 1\n\t" \
  "v_permlane16_swap_b32 v4, v5\n\t" \
  "v_fma_f32 v5, -v4, v4, v5\n\t" \
  "v_max_f32 v5, 0, v5\n\t" \
  "v_add_f32 v5, 0x3727c5ac, v5\n\t" \
  "v_rsq_f32 v20, v5\n\t" \
  "s_nop 0\n\t" \
  "v_mul_f32_e64 v22, v20, -v4\n\t" \
  "buffer_store_dwordx4 v[20:23], v58, s[16:19], s46 offen sc1\n\t" \
  FSTA("s_memrealtime s[94:95]\n\t") \
  FSTA("s_memtime s[92:93]\n\t") \
  FSTA("s_waitcnt lgkmcnt(0)\n\t") \
  FSTA("s_or_b32 s94, s94, 1\n\t") \
  FSTA("v_writelane_b32 v255, s94, 6\n\t") \
  FSTA("v_writelane_b32 v255, s92, 7\n\t") \
  "buffer_load_ushort v218, v53, s[12:15], s44 offen\n\t" \
  "s_add_u32 m0, s76, 0\n\t" \
  "s_nop 0\n\t" \
  "buffer_load_dwordx4 v200, s[20:23], s49 offen offset:0 lds\n\t" \
  "buffer_load_dwordx4 v201, s[20:23], s49 offen offset:1024 lds\n\t" \
  "buffer_load_dwordx4 v202, s[20:23], s49 offen offset:2048 lds\n\t" \
  "buffer_load_dwordx4 v203, s[20:23], s49 offen offset:3072 lds\n\t" \
  "s_add_u32 m0, s77, 49152\n\t" \
  "s_xor_b32 s54, s87, 0\n\t" \
  "s_or_b32 s54, s54, s48\n\t" \
  "buffer_load_dwordx4 v212, s[4:7], s54 offen lds\n\t" \
  "buffer_load_dwordx4 v213, s[4:7], s54 offen offset:1024 lds\n\t" \
  "s_add_u32 m0, s77, 51200\n\t" \
  "s_xor_b32 s54, s87, 131072\n\t" \
  "s_or_b32 s54, s54, s48\n\t" \
  "buffer_load_dwordx4 v212, s[4:7], s54 offen lds\n\t" \
  "buffer_load_dwordx4 v213, s[4:7], s54 offen offset:1024 lds\n\t" \
  "s_add_u32 m0, s77, 53248\n\t" \
  "s_xor_b32 s54, s87, 262144\n\t" \
  "s_or_b32 s54, s54, s48\n\t" \
  "buffer_load_dwordx4 v212, s[4:7], s54 offen lds\n\t" \
  "buffer_load_dwordx4 v213, s[4:7], s54 offen offset:1024 lds\n\t" \
  "s_add_u32 m0, s77, 55296\n\t" \
  "s_xor_b32 s54, s87, 393216\n\t" \
  "s_or_b32 s54, s54, s48\n\t" \
  "buffer_load_dwordx4 v212, s[4:7], s54 offen lds\n\t" \
  "buffer_load_dwordx4 v213, s[4:7], s54 offen offset:1024 lds\n\t" \
  "s_add_u32 m0, s76, 49152\n\t" \
  "s_nop 0\n\t" \
  "buffer_load_dwordx4 v200, s[20:23], s79 offen offset:0 lds\n\t" \
  "buffer_load_dwordx4 v201, s[20:23], s79 offen offset:1024 lds\n\t" \
  "buffer_load_dwordx4 v202, s[20:23], s79 offen offset:2048 lds\n\t" \
  "buffer_load_dwordx4 v203, s[20:23], s79 offen offset:3072 lds\n\t" \
  "v_add_u32 v192, 65536, v184\n\t" \
  "v_add_u32 v193, 65536, v185\n\t" \
  "v_add_u32 v194, 65536, v186\n\t" \
  "v_add_u32 v195, 65536, v187\n\t" \
  "v_add_u32 v196, 65536, v188\n\t" \
  "v_add_u32 v197, 65536, v189\n\t" \
  "v_add_u32 v198, 65536, v190\n\t" \
  "v_add_u32 v199, 65536, v191\n\t" \
  "v_lshrrev_b32 v48, 3, v253\n\t" \
  "s_lshr_b32 s54, s2, 6\n\t" \
  "v_lshl_add_u32 v48, s54, 5, v48\n\t" \
  "v_mul_u32_u24 v48, 98304, v48\n\t" \
  "v_and_b32 v49, 0x70, v244\n\t" \
  "s_bfe_u32 s54, s2, 0x10002\n\t" \
  "s_lshl1_add_u32 s54, s40, s54\n\t" \
  "s_lshl_b32 s54, s54, 10\n\t" \
  "s_and_b32 s55, s2, 0x38\n\t" \
  "s_lshl4_add_u32 s54, s55, s54\n\t" \
  "s_add_u32 s54, s54, 4096\n\t" \
  "v_add3_u32 v209, v48, v49, s54\n\t" \
  "v_lshlrev_b32 v219, 2, v253\n\t" \
  "v_add_u32 v219, 151552, v219\n\t" \
  "s_and_b32 s54, s39, 15\n\t" \
  "s_lshl2_add_u32 s54, s54, s3\n\t" \
  "s_lshl_b32 s54, s54, 11\n\t" \
  "s_lshl_b32 s55, s40, 19\n\t" \
  "s_add_u32 s54, s54, s55\n\t" \
  "v_lshl_add_u32 v217, v247, 7, s54\n\t" \
  "s_mov_b64 exec, 0\n\t" \
  "s_cmp_eq_u32 s3, 0\n\t" \
  "s_cbranch_scc1 .Lhc00\n\t" \
  "s_cmp_eq_u32 s3, 1\n\t" \
  "s_cbranch_scc1 .Lhc01\n\t" \
  "s_cmp_eq_u32 s3, 2\n\t" \
  "s_cbranch_scc1 .Lhc02\n\t" \
  "s_branch .Lhc03\n\t" \
  ".Lhr0:\n\t" \
  "s_mov_b64 exec, -1\n\t" \
  "s_sleep 6\n\t" \
  "s_add_u32 s58, s53, 16384\n\t" \
  "s_add_u32 s90, s53, 32768\n\t" \
  "s_add_u32 s91, s53, 49152\n\t" \
  "s_xor_b32 s96, s51, 0\n\t" \
  "s_xor_b32 s97, s51, 1024\n\t" \
  "s_xor_b32 s99, s51, 2048\n\t" \
  "s_xor_b32 s57, s51, 3072\n\t" \
  "buffer_load_dwordx4 v[128:131], v248, s[16:19], s53 offen sc1\n\t" \
  "buffer_load_dwordx4 v[132:135], v248, s[16:19], s58 offen sc1\n\t" \
  "buffer_load_dwordx4 v[136:139], v248, s[16:19], s90 offen sc1\n\t" \
  "buffer_load_dwordx4 v[140:143], v248, s[16:19], s91 offen sc1\n\t" \
  "buffer_load_dwordx4 v[160:163], v249, s[16:19], s96 offen sc1\n\t" \
  "buffer_load_dwordx4 v[164:167], v249, s[16:19], s97 offen sc1\n\t" \
  "buffer_load_dwordx4 v[168:171], v249, s[16:19], s99 offen sc1\n\t" \
  "buffer_load_dwordx4 v[172:175], v249, s[16:19], s57 offen sc1\n\t" \
  "s_bitcmp1_b32 s2, 7\n\t" \
  "s_cselect_b64 s[12:13], s[64:65], s[60:61]\n\t" \
  FSTP("s_memrealtime s[94:95]\n\t") \
  FSTP("s_memtime s[92:93]\n\t") \
  FSTP("s_waitcnt lgkmcnt(0)\n\t") \
  FSTP("s_or_b32 s94, s94, 1\n\t") \
  FSTP("v_writelane_b32 v255, s94, 56\n\t") \
  FSTP("v_writelane_b32 v255, s92, 57\n\t") \
  "s_waitcnt vmcnt(12)\n\t" \
  "ds_read_b128 v[16:19], v204 offset:0\n\t" \
  "ds_read_b128 v[48:51], v204 offset:2048\n\t" \
  "ds_read_b128 v[80:83], v204 offset:4096\n\t" \
  "ds_read_b128 v[112:115], v204 offset:6144\n\t" \
  "ds_read_b128 v[20:23], v205 offset:0\n\t" \
  "ds_read_b128 v[52:55], v205 offset:2048\n\t" \
  "ds_read_b128 v[84:87], v205 offset:4096\n\t" \
  "ds_read_b128 v[116:119], v205 offset:6144\n\t" \
  "ds_read_b128 v[24:27], v204 offset:49152\n\t" \
  "ds_read_b128 v[56:59], v204 offset:51200\n\t" \
  "ds_read_b128 v[88:91], v204 offset:53248\n\t" \
  "ds_read_b128 v[120:123], v204 offset:55296\n\t" \
  "ds_read_b128 v[28:31], v205 offset:49152\n\t" \
  "ds_read_b128 v[60:63], v205 offset:51200\n\t" \
  "ds_read_b128 v[92:95], v205 offset:53248\n\t" \
  "ds_read_b128 v[124:127], v205 offset:55296\n\t" \
  "v_lshlrev_b32 v218, 16, v218\n\t" \
  "v_mul_f32 v145, 0xbfb8aa3b, v218\n\t" \
  "v_exp_f32 v145, v145\n\t" \
  "s_nop 0\n\t" \
  "v_add_f32 v145, 1.0, v145\n\t" \
  "v_rcp_f32 v145, v145\n\t" \
  "s_nop 0\n\t" \
  "v_mul_f32 v218, v145, v218\n\t" \
  "v_cvt_pk_bf16_f32 v218, v218, v218\n\t" \
  "v_lshlrev_b32 v218, 16, v218\n\t" \
  "s_waitcnt lgkmcnt(12)\n\t" \
  "v_lshlrev_b32 v0, 16, v16\n\t" \
  "v_and_b32 v1, s98, v16\n\t" \
  "v_lshlrev_b32 v2, 16, v17\n\t" \
  "v_and_b32 v3, s98, v17\n\t" \
  "v_lshlrev_b32 v4, 16, v18\n\t" \
  "v_and_b32 v5, s98, v18\n\t" \
  "v_lshlrev_b32 v6, 16, v19\n\t" \
  "v_and_b32 v7, s98, v19\n\t" \
  "v_lshlrev_b32 v32, 16, v48\n\t" \
  "v_and_b32 v33, s98, v48\n\t" \
  "v_lshlrev_b32 v34, 16, v49\n\t" \
  "v_and_b32 v35, s98, v49\n\t" \
  "v_lshlrev_b32 v36, 16, v50\n\t" \
  "v_and_b32 v37, s98, v50\n\t" \
  "v_lshlrev_b32 v38, 16, v51\n\t" \
  "v_and_b32 v39, s98, v51\n\t" \
  "v_lshlrev_b32 v64, 16, v80\n\t" \
  "v_and_b32 v65, s98, v80\n\t" \
  "v_lshlrev_b32 v66, 16, v81\n\t" \
  "v_and_b32 v67, s98, v81\n\t" \
  "v_lshlrev_b32 v68, 16, v82\n\t" \
  "v_and_b32 v69, s98, v82\n\t" \
  "v_lshlrev_b32 v70, 16, v83\n\t" \
  "v_and_b32 v71, s98, v83\n\t" \
  "v_lshlrev_b32 v96, 16, v112\n\t" \
  "v_and_b32 v97, s98, v112\n\t" \
  "v_lshlrev_b32 v98, 16, v113\n\t" \
  "v_and_b32 v99, s98, v113\n\t" \
  "v_lshlrev_b32 v100, 16, v114\n\t" \
  "v_and_b32 v101, s98, v114\n\t" \
  "v_lshlrev_b32 v102, 16, v115\n\t" \
  "v_and_b32 v103, s98, v115\n\t" \
  "s_waitcnt lgkmcnt(8)\n\t" \
  "v_lshlrev_b32 v8, 16, v20\n\t" \
  "v_and_b32 v9, s98, v20\n\t" \
  "v_lshlrev_b32 v10, 16, v21\n\t" \
  "v_and_b32 v11, s98, v21\n\t" \
  "v_lshlrev_b32 v12, 16, v22\n\t" \
  "v_and_b32 v13, s98, v22\n\t" \
  "v_lshlrev_b32 v14, 16, v23\n\t" \
  "v_and_b32 v15, s98, v23\n\t" \
  "v_lshlrev_b32 v40, 16, v52\n\t" \
  "v_and_b32 v41, s98, v52\n\t" \
  "v_lshlrev_b32 v42, 16, v53\n\t" \
  "v_and_b32 v43, s98, v53\n\t" \
  "v_lshlrev_b32 v44, 16, v54\n\t" \
  "v_and_b32 v45, s98, v54\n\t" \
  "v_lshlrev_b32 v46, 16, v55\n\t" \
  "v_and_b32 v47, s98, v55\n\t" \
  "v_lshlrev_b32 v72, 16, v84\n\t" \
  "v_and_b32 v73, s98, v84\n\t" \
  "v_lshlrev_b32 v74, 16, v85\n\t" \
  "v_and_b32 v75, s98, v85\n\t" \
  "v_lshlrev_b32 v76, 16, v86\n\t" \
  "v_and_b32 v77, s98, v86\n\t" \
  "v_lshlrev_b32 v78, 16, v87\n\t" \
  "v_and_b32 v79, s98, v87\n\t" \
  "v_lshlrev_b32 v104, 16, v116\n\t" \
  "v_and_b32 v105, s98, v116\n\t" \
  "v_lshlrev_b32 v106, 16, v117\n\t" \
  "v_and_b32 v107, s98, v117\n\t" \
  "v_lshlrev_b32 v108, 16, v118\n\t" \
  "v_and_b32 v109, s98, v118\n\t" \
  "v_lshlrev_b32 v110, 16, v119\n\t" \
  "v_and_b32 v111, s98, v119\n\t" \
  FSTP("s_memrealtime s[94:95]\n\t") \
  FSTP("s_memtime s[92:93]\n\t") \
  FSTP("s_waitcnt lgkmcnt(0)\n\t") \
  FSTP("s_or_b32 s94, s94, 1\n\t") \
  FSTP("v_writelane_b32 v255, s94, 58\n\t") \
  FSTP("v_writelane_b32 v255, s92, 59\n\t") \
  "s_waitcnt vmcnt(0)\n\t" \
  "v_xor_b32 v144, v211, v129\n\t" \
  "v_bitop3_b32 v144, v144, v131, v211 bitop3:0xf6\n\t" \
  "v_bitop3_b32 v144, v144, v133, v211 bitop3:0xf6\n\t" \
  "v_bitop3_b32 v144, v144, v135, v211 bitop3:0xf6\n\t" \
  "v_bitop3_b32 v144, v144, v137, v211 bitop3:0xf6\n\t" \
  "v_bitop3_b32 v144, v144, v139, v211 bitop3:0xf6\n\t" \
  "v_bitop3_b32 v144, v144, v141, v211 bitop3:0xf6\n\t" \
  "v_bitop3_b32 v144, v144, v143, v211 bitop3:0xf6\n\t" \
  "v_bitop3_b32 v144, v144, v161, v211 bitop3:0xf6\n\t" \
  "v_bitop3_b32 v144, v144, v163, v211 bitop3:0xf6\n\t" \
  "v_bitop3_b32 v144, v144, v165, v211 bitop3:0xf6\n\t" \
  "v_bitop3_b32 v144, v144, v167, v211 bitop3:0xf6\n\t" \
  "v_bitop3_b32 v144, v144, v169, v211 bitop3:0xf6\n\t" \
  "v_bitop3_b32 v144, v144, v171, v211 bitop3:0xf6\n\t" \
  "v_bitop3_b32 v144, v144, v173, v211 bitop3:0xf6\n\t" \
  "v_bitop3_b32 v144, v144, v175, v211 bitop3:0xf6\n\t" \
  "v_cmp_ne_u32 vcc, 0, v144\n\t" \
  "v_add_f32 v146, v128, v132\n\t" \
  "v_add_f32 v148, v136, v140\n\t" \
  "v_add_f32 v147, v130, v134\n\t" \
  "v_add_f32 v149, v138, v142\n\t" \
  "v_add_f32 v146, v146, v148\n\t" \
  "v_add_f32 v147, v147, v149\n\t" \
  "s_cbranch_vccnz .Lfr_s\n\t" \
  ".Lfr_k:\n\t" \
  FSTA("s_memrealtime s[94:95]\n\t") \
  FSTA("s_memtime s[92:93]\n\t") \
  FSTA("s_waitcnt lgkmcnt(0)\n\t") \
  FSTA("s_or_b32 s94, s94, 1\n\t") \
  FSTA("v_writelane_b32 v255, s94, 8\n\t") \
  FSTA("v_writelane_b32 v255, s92, 9\n\t") \
  "v_cvt_pk_bf16_f32 v147, v146, v147\n\t" \
  "v_lshlrev_b32 v146, 16, v147\n\t" \
  "v_and_b32 v147, s98, v147\n\t" \
  "v_add_f32 v146, v146, v251\n\t" \
  "v_add_f32 v147, v147, v251\n\t" \
  "v_cvt_pk_bf16_f32 v147, v146, v147\n\t" \
  "v_lshlrev_b32 v146, 16, v147\n\t" \
  "v_and_b32 v147, s98, v147\n\t" \
  "v_mul_f32 v146, 0x3e0293ee, v146\n\t" \
  "v_mul_f32 v147, 0x3e0293ee, v147\n\t" \
  "ds_write_b64 v250, v[146:147]\n\t" \
  "v_pk_fma_f32 v[0:1], v[0:1], v[160:161], v[162:163] op_sel_hi:[1,0,0]\n\t" \
  "v_pk_fma_f32 v[2:3], v[2:3], v[160:161], v[162:163] op_sel_hi:[1,0,0]\n\t" \
  "v_pk_fma_f32 v[4:5], v[4:5], v[160:161], v[162:163] op_sel_hi:[1,0,0]\n\t" \
  "v_pk_fma_f32 v[6:7], v[6:7], v[160:161], v[162:163] op_sel_hi:[1,0,0]\n\t" \
  "v_pk_fma_f32 v[32:33], v[32:33], v[164:165], v[166:167] op_sel_hi:[1,0,0]\n\t" \
  "v_pk_fma_f32 v[34:35], v[34:35], v[164:165], v[166:167] op_sel_hi:[1,0,0]\n\t" \
  "v_pk_fma_f32 v[36:37], v[36:37], v[164:165], v[166:167] op_sel_hi:[1,0,0]\n\t" \
  "v_pk_fma_f32 v[38:39], v[38:39], v[164:165], v[166:167] op_sel_hi:[1,0,0]\n\t" \
  "v_pk_fma_f32 v[64:65], v[64:65], v[168:169], v[170:171] op_sel_hi:[1,0,0]\n\t" \
  "v_pk_fma_f32 v[66:67], v[66:67], v[168:169], v[170:171] op_sel_hi:[1,0,0]\n\t" \
  "v_pk_fma_f32 v[68:69], v[68:69], v[168:169], v[170:171] op_sel_hi:[1,0,0]\n\t" \
  "v_pk_fma_f32 v[70:71], v[70:71], v[168:169], v[170:171] op_sel_hi:[1,0,0]\n\t" \
  "v_pk_fma_f32 v[96:97], v[96:97], v[172:173], v[174:175] op_sel_hi:[1,0,0]\n\t" \
  "v_pk_fma_f32 v[98:99], v[98:99], v[172:173], v[174:175] op_sel_hi:[1,0,0]\n\t" \
  "v_pk_fma_f32 v[100:101], v[100:101], v[172:173], v[174:175] op_sel_hi:[1,0,0]\n\t" \
  "v_pk_fma_f32 v[102:103], v[102:103], v[172:173], v[174:175] op_sel_hi:[1,0,0]\n\t" \
  FSTP("s_memrealtime s[94:95]\n\t") \
  FSTP("s_memtime s[92:93]\n\t") \
  FSTP("s_waitcnt lgkmcnt(0)\n\t") \
  FSTP("s_or_b32 s94, s94, 1\n\t") \
  FSTP("v_writelane_b32 v255, s94, 24\n\t") \
  FSTP("v_writelane_b32 v255, s92, 25\n\t") \
  "s_waitcnt lgkmcnt(0)\n\t" \
  "s_barrier\n\t" \
  FSTP("s_memrealtime s[94:95]\n\t") \
  FSTP("s_memtime s[92:93]\n\t") \
  FSTP("s_waitcnt lgkmcnt(0)\n\t") \
  FSTP("s_or_b32 s94, s94, 1\n\t") \
  FSTP("v_writelane_b32 v255, s94, 26\n\t") \
  FSTP("v_writelane_b32 v255, s92, 27\n\t") \
  "ds_read_b128 v[128:131], v208 offset:0\n\t" \
  "ds_read_b128 v[132:135], v208 offset:16\n\t" \
  "ds_read_b128 v[136:139], v208 offset:256\n\t" \
  "ds_read_b128 v[140:143], v208 offset:272\n\t" \
  "ds_read_b128 v[144:147], v208 offset:128\n\t" \
  "ds_read_b128 v[148:151], v208 offset:144\n\t" \
  "ds_read_b128 v[152:155], v208 offset:384\n\t" \
  "ds_read_b128 v[156:159], v208 offset:400\n\t" \
  "global_load_dword v246, v215, s[20:21]\n\t" \
  "v_pk_fma_f32 v[8:9], v[8:9], v[160:161], v[162:163] op_sel_hi:[1,0,0]\n\t" \
  "v_pk_fma_f32 v[10:11], v[10:11], v[160:161], v[162:163] op_sel_hi:[1,0,0]\n\t" \
  "v_pk_fma_f32 v[12:13], v[12:13], v[160:161], v[162:163] op_sel_hi:[1,0,0]\n\t" \
  "v_pk_fma_f32 v[14:15], v[14:15], v[160:161], v[162:163] op_sel_hi:[1,0,0]\n\t" \
  "v_pk_fma_f32 v[40:41], v[40:41], v[164:165], v[166:167] op_sel_hi:[1,0,0]\n\t" \
  "v_pk_fma_f32 v[42:43], v[42:43], v[164:165], v[166:167] op_sel_hi:[1,0,0]\n\t" \
  "v_pk_fma_f32 v[44:45], v[44:45], v[164:165], v[166:167] op_sel_hi:[1,0,0]\n\t" \
  "v_pk_fma_f32 v[46:47], v[46:47], v[164:165], v[166:167] op_sel_hi:[1,0,0]\n\t" \
  "v_pk_fma_f32 v[72:73], v[72:73], v[168:169], v[170:171] op_sel_hi:[1,0,0]\n\t" \
  "v_pk_fma_f32 v[74:75], v[74:75], v[168:169], v[170:171] op_sel_hi:[1,0,0]\n\t" \
  "v_pk_fma_f32 v[76:77], v[76:77], v[168:169], v[170:171] op_sel_hi:[1,0,0]\n\t" \
  "v_pk_fma_f32 v[78:79], v[78:79], v[168:169], v[170:171] op_sel_hi:[1,0,0]\n\t" \
  "v_pk_fma_f32 v[104:105], v[104:105], v[172:173], v[174:175] op_sel_hi:[1,0,0]\n\t" \
  "v_pk_fma_f32 v[106:107], v[106:107], v[172:173], v[174:175] op_sel_hi:[1,0,0]\n\t" \
  "v_pk_fma_f32 v[108:109], v[108:109], v[172:173], v[174:175] op_sel_hi:[1,0,0]\n\t" \
  "v_pk_fma_f32 v[110:111], v[110:111], v[172:173], v[174:175] op_sel_hi:[1,0,0]\n\t" \
  "s_xor_b32 s97, s86, 0\n\t" \
  "s_waitcnt lgkmcnt(4)\n\t" \
  "v_pk_fma_f32 v[0:1], v[0:1], v[128:129], v[136:137]\n\t" \
  "v_pk_fma_f32 v[2:3], v[2:3], v[130:131], v[138:139]\n\t" \
  "v_pk_fma_f32 v[4:5], v[4:5], v[132:133], v[140:141]\n\t" \
  "v_pk_fma_f32 v[6:7], v[6:7], v[134:135], v[142:143]\n\t" \
  "v_cvt_pk_bf16_f32 v16, v0, v1\n\t" \
  "v_cvt_pk_bf16_f32 v17, v2, v3\n\t" \
  "v_cvt_pk_bf16_f32 v18, v4, v5\n\t" \
  "v_cvt_pk_bf16_f32 v19, v6, v7\n\t" \
  "buffer_store_dwordx4 v[16:19], v214, s[16:19], s97 offen sc1\n\t" \
  "s_xor_b32 s97, s86, 256\n\t" \
  "v_pk_fma_f32 v[32:33], v[32:33], v[128:129], v[136:137]\n\t" \
  "v_pk_fma_f32 v[34:35], v[34:35], v[130:131], v[138:139]\n\t" \
  "v_pk_fma_f32 v[36:37], v[36:37], v[132:133], v[140:141]\n\t" \
  "v_pk_fma_f32 v[38:39], v[38:39], v[134:135], v[142:143]\n\t" \
  "v_cvt_pk_bf16_f32 v48, v32, v33\n\t" \
  "v_cvt_pk_bf16_f32 v49, v34, v35\n\t" \
  "v_cvt_pk_bf16_f32 v50, v36, v37\n\t" \
  "v_cvt_pk_bf16_f32 v51, v38, v39\n\t" \
  "buffer_store_dwordx4 v[48:51], v214, s[16:19], s97 offen sc1\n\t" \
  "s_xor_b32 s97, s86, 512\n\t" \
  "v_pk_fma_f32 v[64:65], v[64:65], v[128:129], v[136:137]\n\t" \
  "v_pk_fma_f32 v[66:67], v[66:67], v[130:131], v[138:139]\n\t" \
  "v_pk_fma_f32 v[68:69], v[68:69], v[132:133], v[140:141]\n\t" \
  "v_pk_fma_f32 v[70:71], v[70:71], v[134:135], v[142:143]\n\t" \
  "v_cvt_pk_bf16_f32 v80, v64, v65\n\t" \
  "v_cvt_pk_bf16_f32 v81, v66, v67\n\t" \
  "v_cvt_pk_bf16_f32 v82, v68, v69\n\t" \
  "v_cvt_pk_bf16_f32 v83, v70, v71\n\t" \
  ".Lhs0:\n\ts_cbranch_execz .Lhs0n\n\t" \
  "buffer_store_dwordx4 v[80:83], v214, s[16:19], s97 offen sc1\n\t" \
  "s_xor_b32 s97, s86, 768\n\t" \
  "v_pk_fma_f32 v[96:97], v[96:97], v[128:129], v[136:137]\n\t" \
  "v_pk_fma_f32 v[98:99], v[98:99], v[130:131], v[138:139]\n\t" \
  "v_pk_fma_f32 v[100:101], v[100:101], v[132:133], v[140:141]\n\t" \
  "v_pk_fma_f32 v[102:103], v[102:103], v[134:135], v[142:143]\n\t" \
  "v_cvt_pk_bf16_f32 v112, v96, v97\n\t" \
  "v_cvt_pk_bf16_f32 v113, v98, v99\n\t" \
  "v_cvt_pk_bf16_f32 v114, v100, v101\n\t" \
  "v_cvt_pk_bf16_f32 v115, v102, v103\n\t" \
  "buffer_store_dwordx4 v[112:115], v214, s[16:19], s97 offen sc1\n\t" \
  "ds_read_b128 v[128:131], v208 offset:512\n\t" \
  "ds_read_b128 v[132:135], v208 offset:528\n\t" \
  "ds_read_b128 v[136:139], v208 offset:768\n\t" \
  "ds_read_b128 v[140:143], v208 offset:784\n\t" \
  FSTP("s_memrealtime s[94:95]\n\t") \
  FSTP("s_memtime s[92:93]\n\t") \
  "s_add_u32 m0, s77, 98304\n\t" \
  "s_xor_b32 s96, s87, 0\n\t" \
  "s_add_u32 s96, s96, s88\n\t" \
  "s_waitcnt lgkmcnt(4)\n\t" \
  "v_pk_fma_f32 v[8:9], v[8:9], v[144:145], v[152:153]\n\t" \
  "v_pk_fma_f32 v[10:11], v[10:11], v[146:147], v[154:155]\n\t" \
  "v_pk_fma_f32 v[12:13], v[12:13], v[148:149], v[156:157]\n\t" \
  "v_pk_fma_f32 v[14:15], v[14:15], v[150:151], v[158:159]\n\t" \
  "v_cvt_pk_bf16_f32 v20, v8, v9\n\t" \
  "v_cvt_pk_bf16_f32 v21, v10, v11\n\t" \
  "v_cvt_pk_bf16_f32 v22, v12, v13\n\t" \
  "v_cvt_pk_bf16_f32 v23, v14, v15\n\t" \
  "buffer_load_dwordx4 v212, s[4:7], s96 offen lds\n\t" \
  "v_pk_fma_f32 v[40:41], v[40:41], v[144:145], v[152:153]\n\t" \
  "v_pk_fma_f32 v[42:43], v[42:43], v[146:147], v[154:155]\n\t" \
  "v_pk_fma_f32 v[44:45], v[44:45], v[148:149], v[156:157]\n\t" \
  "v_pk_fma_f32 v[46:47], v[46:47], v[150:151], v[158:159]\n\t" \
  "v_cvt_pk_bf16_f32 v52, v40, v41\n\t" \
  "v_cvt_pk_bf16_f32 v53, v42, v43\n\t" \
  "v_cvt_pk_bf16_f32 v54, v44, v45\n\t" \
  "v_cvt_pk_bf16_f32 v55, v46, v47\n\t" \
  "buffer_load_dwordx4 v213, s[4:7], s96 offen offset:1024 lds\n\t" \
  "s_add_u32 m0, s77, 100352\n\t" \
  "s_xor_b32 s96, s87, 131072\n\t" \
  "s_add_u32 s96, s96, s88\n\t" \
  "v_pk_fma_f32 v[72:73], v[72:73], v[144:145], v[152:153]\n\t" \
  "v_pk_fma_f32 v[74:75], v[74:75], v[146:147], v[154:155]\n\t" \
  "v_pk_fma_f32 v[76:77], v[76:77], v[148:149], v[156:157]\n\t" \
  "v_pk_fma_f32 v[78:79], v[78:79], v[150:151], v[158:159]\n\t" \
  "v_cvt_pk_bf16_f32 v84, v72, v73\n\t" \
  "v_cvt_pk_bf16_f32 v85, v74, v75\n\t" \
  "v_cvt_pk_bf16_f32 v86, v76, v77\n\t" \
  "v_cvt_pk_bf16_f32 v87, v78, v79\n\t" \
  "buffer_load_dwordx4 v212, s[4:7], s96 offen lds\n\t" \
  "v_pk_fma_f32 v[104:105], v[104:105], v[144:145], v[152:153]\n\t" \
  "v_pk_fma_f32 v[106:107], v[106:107], v[146:147], v[154:155]\n\t" \
  "v_pk_fma_f32 v[108:109], v[108:109], v[148:149], v[156:157]\n\t" \
  "v_pk_fma_f32 v[110:111], v[110:111], v[150:151], v[158:159]\n\t" \
  "v_cvt_pk_bf16_f32 v116, v104, v105\n\t" \
  "v_cvt_pk_bf16_f32 v117, v106, v107\n\t" \
  "v_cvt_pk_bf16_f32 v118, v108, v109\n\t" \
  "v_cvt_pk_bf16_f32 v119, v110, v111\n\t" \
  "buffer_load_dwordx4 v213, s[4:7], s96 offen offset:1024 lds\n\t" \
  "ds_read_b128 v[144:147], v208 offset:640\n\t" \
  "ds_read_b128 v[148:151], v208 offset:656\n\t" \
  "ds_read_b128 v[152:155], v208 offset:896\n\t" \
  "ds_read_b128 v[156:159], v208 offset:912\n\t" \
  FSTP("v_writelane_b32 v255, s94, 28\n\t") \
  FSTP("v_writelane_b32 v255, s92, 29\n\t") \
  FSTP("s_memrealtime s[94:95]\n\t") \
  FSTP("s_memtime s[92:93]\n\t") \
  "s_add_u32 m0, s77, 102400\n\t" \
  "s_xor_b32 s96, s87, 262144\n\t" \
  "s_add_u32 s96, s96, s88\n\t" \
  "v_lshlrev_b32 v176, 16, v24\n\t" \
  "v_and_b32 v177, s98, v24\n\t" \
  "v_lshlrev_b32 v178, 16, v25\n\t" \
  "v_and_b32 v179, s98, v25\n\t" \
  "v_lshlrev_b32 v180, 16, v26\n\t" \
  "v_and_b32 v181, s98, v26\n\t" \
  "v_lshlrev_b32 v182, 16, v27\n\t" \
  "v_and_b32 v183, s98, v27\n\t" \
  "v_pk_fma_f32 v[176:177], v[176:177], v[160:161], v[162:163] op_sel_hi:[1,0,0]\n\t" \
  "v_pk_fma_f32 v[178:179], v[178:179], v[160:161], v[162:163] op_sel_hi:[1,0,0]\n\t" \
  "v_pk_fma_f32 v[180:181], v[180:181], v[160:161], v[162:163] op_sel_hi:[1,0,0]\n\t" \
  "v_pk_fma_f32 v[182:183], v[182:183], v[160:161], v[162:163] op_sel_hi:[1,0,0]\n\t" \
  "s_waitcnt lgkmcnt(4)\n\t" \
  "v_pk_fma_f32 v[176:177], v[176:177], v[128:129], v[136:137]\n\t" \
  "v_pk_fma_f32 v[178:179], v[178:179], v[130:131], v[138:139]\n\t" \
  "v_pk_fma_f32 v[180:181], v[180:181], v[132:133], v[140:141]\n\t" \
  "v_pk_fma_f32 v[182:183], v[182:183], v[134:135], v[142:143]\n\t" \
  "v_cvt_pk_bf16_f32 v24, v176, v177\n\t" \
  "v_cvt_pk_bf16_f32 v25, v178, v179\n\t" \
  "v_cvt_pk_bf16_f32 v26, v180, v181\n\t" \
  "v_cvt_pk_bf16_f32 v27, v182, v183\n\t" \
  "buffer_load_dwordx4 v212, s[4:7], s96 offen lds\n\t" \
  "v_lshlrev_b32 v176, 16, v56\n\t" \
  "v_and_b32 v177, s98, v56\n\t" \
  "v_lshlrev_b32 v178, 16, v57\n\t" \
  "v_and_b32 v179, s98, v57\n\t" \
  "v_lshlrev_b32 v180, 16, v58\n\t" \
  "v_and_b32 v181, s98, v58\n\t" \
  "v_lshlrev_b32 v182, 16, v59\n\t" \
  "v_and_b32 v183, s98, v59\n\t" \
  "v_pk_fma_f32 v[176:177], v[176:177], v[164:165], v[166:167] op_sel_hi:[1,0,0]\n\t" \
  "v_pk_fma_f32 v[178:179], v[178:179], v[164:165], v[166:167] op_sel_hi:[1,0,0]\n\t" \
  "v_pk_fma_f32 v[180:181], v[180:181], v[164:165], v[166:167] op_sel_hi:[1,0,0]\n\t" \
  "v_pk_fma_f32 v[182:183], v[182:183], v[164:165], v[166:167] op_sel_hi:[1,0,0]\n\t" \
  "v_pk_fma_f32 v[176:177], v[176:177], v[128:129], v[136:137]\n\t" \
  "v_pk_fma_f32 v[178:179], v[178:179], v[130:131], v[138:139]\n\t" \
  "v_pk_fma_f32 v[180:181], v[180:181], v[132:133], v[140:141]\n\t" \
  "v_pk_fma_f32 v[182:183], v[182:183], v[134:135], v[142:143]\n\t" \
  "v_cvt_pk_bf16_f32 v56, v176, v177\n\t" \
  "v_cvt_pk_bf16_f32 v57, v178, v179\n\t" \
  "v_cvt_pk_bf16_f32 v58, v180, v181\n\t" \
  "v_cvt_pk_bf16_f32 v59, v182, v183\n\t" \
  "buffer_load_dwordx4 v213, s[4:7], s96 offen offset:1024 lds\n\t" \
  "s_add_u32 m0, s77, 104448\n\t" \
  "s_xor_b32 s96, s87, 393216\n\t" \
  "s_add_u32 s96, s96, s88\n\t" \
  "v_lshlrev_b32 v176, 16, v88\n\t" \
  "v_and_b32 v177, s98, v88\n\t" \
  "v_lshlrev_b32 v178, 16, v89\n\t" \
  "v_and_b32 v179, s98, v89\n\t" \
  "v_lshlrev_b32 v180, 16, v90\n\t" \
  "v_and_b32 v181, s98, v90\n\t" \
  "v_lshlrev_b32 v182, 16, v91\n\t" \
  "v_and_b32 v183, s98, v91\n\t" \
  "v_pk_fma_f32 v[176:177], v[176:177], v[168:169], v[170:171] op_sel_hi:[1,0,0]\n\t" \
  "v_pk_fma_f32 v[178:179], v[178:179], v[168:169], v[170:171] op_sel_hi:[1,0,0]\n\t" \
  "v_pk_fma_f32 v[180:181], v[180:181], v[168:169], v[170:171] op_sel_hi:[1,0,0]\n\t" \
  "v_pk_fma_f32 v[182:183], v[182:183], v[168:169], v[170:171] op_sel_hi:[1,0,0]\n\t" \
  "v_pk_fma_f32 v[176:177], v[176:177], v[128:129], v[136:137]\n\t" \
  "v_pk_fma_f32 v[178:179], v[178:179], v[130:131], v[138:139]\n\t" \
  "v_pk_fma_f32 v[180:181], v[180:181], v[132:133], v[140:141]\n\t" \
  "v_pk_fma_f32 v[182:183], v[182:183], v[134:135], v[142:143]\n\t" \
  "v_cvt_pk_bf16_f32 v88, v176, v177\n\t" \
  "v_cvt_pk_bf16_f32 v89, v178, v179\n\t" \
  "v_cvt_pk_bf16_f32 v90, v180, v181\n\t" \
  "v_cvt_pk_bf16_f32 v91, v182, v183\n\t" \
  "buffer_load_dwordx4 v212, s[4:7], s96 offen lds\n\t" \
  "v_lshlrev_b32 v176, 16, v120\n\t" \
  "v_and_b32 v177, s98, v120\n\t" \
  "v_lshlrev_b32 v178, 16, v121\n\t" \
  "v_and_b32 v179, s98, v121\n\t" \
  "v_lshlrev_b32 v180, 16, v122\n\t" \
  "v_and_b32 v181, s98, v122\n\t" \
  "v_lshlrev_b32 v182, 16, v123\n\t" \
  "v_and_b32 v183, s98, v123\n\t" \
  "v_pk_fma_f32 v[176:177], v[176:177], v[172:173], v[174:175] op_sel_hi:[1,0,0]\n\t" \
  "v_pk_fma_f32 v[178:179], v[178:179], v[172:173], v[174:175] op_sel_hi:[1,0,0]\n\t" \
  "v_pk_fma_f32 v[180:181], v[180:181], v[172:173], v[174:175] op_sel_hi:[1,0,0]\n\t" \
  "v_pk_fma_f32 v[182:183], v[182:183], v[172:173], v[174:175] op_sel_hi:[1,0,0]\n\t" \
  "v_pk_fma_f32 v[176:177], v[176:177], v[128:129], v[136:137]\n\t" \
  "v_pk_fma_f32 v[178:179], v[178:179], v[130:131], v[138:139]\n\t" \
  "v_pk_fma_f32 v[180:181], v[180:181], v[132:133], v[140:141]\n\t" \
  "v_pk_fma_f32 v[182:183], v[182:183], v[134:135], v[142:143]\n\t" \
  "v_cvt_pk_bf16_f32 v120, v176, v177\n\t" \
  "v_cvt_pk_bf16_f32 v121, v178, v179\n\t" \
  "v_cvt_pk_bf16_f32 v122, v180, v181\n\t" \
  "v_cvt_pk_bf16_f32 v123, v182, v183\n\t" \
  "buffer_load_dwordx4 v213, s[4:7], s96 offen offset:1024 lds\n\t" \
  "ds_read_b128 v[128:131], v208 offset:1024\n\t" \
  "ds_read_b128 v[132:135], v208 offset:1040\n\t" \
  "ds_read_b128 v[136:139], v208 offset:1280\n\t" \
  "ds_read_b128 v[140:143], v208 offset:1296\n\t" \
  FSTP("v_writelane_b32 v255, s94, 30\n\t") \
  FSTP("v_writelane_b32 v255, s92, 31\n\t") \
  FSTP("s_memrealtime s[94:95]\n\t") \
  FSTP("s_memtime s[92:93]\n\t") \
  "s_add_u32 m0, s76, 98304\n\t" \
  "v_lshlrev_b32 v176, 16, v28\n\t" \
  "v_and_b32 v177, s98, v28\n\t" \
  "v_lshlrev_b32 v178, 16, v29\n\t" \
  "v_and_b32 v179, s98, v29\n\t" \
  "v_lshlrev_b32 v180, 16, v30\n\t" \
  "v_and_b32 v181, s98, v30\n\t" \
  "v_lshlrev_b32 v182, 16, v31\n\t" \
  "v_and_b32 v183, s98, v31\n\t" \
  "v_pk_fma_f32 v[176:177], v[176:177], v[160:161], v[162:163] op_sel_hi:[1,0,0]\n\t" \
  "v_pk_fma_f32 v[178:179], v[178:179], v[160:161], v[162:163] op_sel_hi:[1,0,0]\n\t" \
  "v_pk_fma_f32 v[180:181], v[180:181], v[160:161], v[162:163] op_sel_hi:[1,0,0]\n\t" \
  "v_pk_fma_f32 v[182:183], v[182:183], v[160:161], v[162:163] op_sel_hi:[1,0,0]\n\t" \
  "s_waitcnt lgkmcnt(4)\n\t" \
  "v_pk_fma_f32 v[176:177], v[176:177], v[144:145], v[152:153]\n\t" \
  "v_pk_fma_f32 v[178:179], v[178:179], v[146:147], v[154:155]\n\t" \
  "v_pk_fma_f32 v[180:181], v[180:181], v[148:149], v[156:157]\n\t" \
  "v_pk_fma_f32 v[182:183], v[182:183], v[150:151], v[158:159]\n\t" \
  "v_cvt_pk_bf16_f32 v28, v176, v177\n\t" \
  "v_cvt_pk_bf16_f32 v29, v178, v179\n\t" \
  "v_cvt_pk_bf16_f32 v30, v180, v181\n\t" \
  "v_cvt_pk_bf16_f32 v31, v182, v183\n\t" \
  "buffer_load_dwordx4 v200, s[20:23], s80 offen offset:0 lds\n\t" \
  "ds_read_b64_tr_b16 a[128:129], v184 offset:0\n\t" \
  "ds_read_b64_tr_b16 a[130:131], v184 offset:1024\n\t" \
  "ds_read_b64_tr_b16 a[132:133], v185 offset:0\n\t" \
  "ds_read_b64_tr_b16 a[134:135], v185 offset:1024\n\t" \
  "v_lshlrev_b32 v176, 16, v60\n\t" \
  "v_and_b32 v177, s98, v60\n\t" \
  "v_lshlrev_b32 v178, 16, v61\n\t" \
  "v_and_b32 v179, s98, v61\n\t" \
  "v_lshlrev_b32 v180, 16, v62\n\t" \
  "v_and_b32 v181, s98, v62\n\t" \
  "v_lshlrev_b32 v182, 16, v63\n\t" \
  "v_and_b32 v183, s98, v63\n\t" \
  "v_pk_fma_f32 v[176:177], v[176:177], v[164:165], v[166:167] op_sel_hi:[1,0,0]\n\t" \
  "v_pk_fma_f32 v[178:179], v[178:179], v[164:165], v[166:167] op_sel_hi:[1,0,0]\n\t" \
  "v_pk_fma_f32 v[180:181], v[180:181], v[164:165], v[166:167] op_sel_hi:[1,0,0]\n\t" \
  "v_pk_fma_f32 v[182:183], v[182:183], v[164:165], v[166:167] op_sel_hi:[1,0,0]\n\t" \
  "v_pk_fma_f32 v[176:177], v[176:177], v[144:145], v[152:153]\n\t" \
  "v_pk_fma_f32 v[178:179], v[178:179], v[146:147], v[154:155]\n\t" \
  "v_pk_fma_f32 v[180:181], v[180:181], v[148:149], v[156:157]\n\t" \
  "v_pk_fma_f32 v[182:183], v[182:183], v[150:151], v[158:159]\n\t" \
  "v_cvt_pk_bf16_f32 v60, v176, v177\n\t" \
  "v_cvt_pk_bf16_f32 v61, v178, v179\n\t" \
  "v_cvt_pk_bf16_f32 v62, v180, v181\n\t" \
  "v_cvt_pk_bf16_f32 v63, v182, v183\n\t" \
  "buffer_load_dwordx4 v201, s[20:23], s80 offen offset:1024 lds\n\t" \
  "ds_read_b64_tr_b16 a[136:137], v186 offset:0\n\t" \
  "ds_read_b64_tr_b16 a[138:139], v186 offset:1024\n\t" \
  "ds_read_b64_tr_b16 a[140:141], v187 offset:0\n\t" \
  "ds_read_b64_tr_b16 a[142:143], v187 offset:1024\n\t" \
  "v_lshlrev_b32 v176, 16, v92\n\t" \
  "v_and_b32 v177, s98, v92\n\t" \
  "v_lshlrev_b32 v178, 16, v93\n\t" \
  "v_and_b32 v179, s98, v93\n\t" \
  "v_lshlrev_b32 v180, 16, v94\n\t" \
  "v_and_b32 v181, s98, v94\n\t" \
  "v_lshlrev_b32 v182, 16, v95\n\t" \
  "v_and_b32 v183, s98, v95\n\t" \
  "v_pk_fma_f32 v[176:177], v[176:177], v[168:169], v[170:171] op_sel_hi:[1,0,0]\n\t" \
  "v_pk_fma_f32 v[178:179], v[178:179], v[168:169], v[170:171] op_sel_hi:[1,0,0]\n\t" \
  "v_pk_fma_f32 v[180:181], v[180:181], v[168:169], v[170:171] op_sel_hi:[1,0,0]\n\t" \
  "v_pk_fma_f32 v[182:183], v[182:183], v[168:169], v[170:171] op_sel_hi:[1,0,0]\n\t" \
  "v_pk_fma_f32 v[176:177], v[176:177], v[144:145], v[152:153]\n\t" \
  "v_pk_fma_f32 v[178:179], v[178:179], v[146:147], v[154:155]\n\t" \
  "v_pk_fma_f32 v[180:181], v[180:181], v[148:149], v[156:157]\n\t" \
  "v_pk_fma_f32 v[182:183], v[182:183], v[150:151], v[158:159]\n\t" \
  "v_cvt_pk_bf16_f32 v92, v176, v177\n\t" \
  "v_cvt_pk_bf16_f32 v93, v178, v179\n\t" \
  "v_cvt_pk_bf16_f32 v94, v180, v181\n\t" \
  "v_cvt_pk_bf16_f32 v95, v182, v183\n\t" \
  "buffer_load_dwordx4 v202, s[20:23], s80 offen offset:2048 lds\n\t" \
  "ds_read_b64_tr_b16 a[144:145], v188 offset:0\n\t" \
  "ds_read_b64_tr_b16 a[146:147], v188 offset:1024\n\t" \
  "ds_read_b64_tr_b16 a[148:149], v189 offset:0\n\t" \
  "ds_read_b64_tr_b16 a[150:151], v189 offset:1024\n\t" \
  "v_lshlrev_b32 v176, 16, v124\n\t" \
  "v_and_b32 v177, s98, v124\n\t" \
  "v_lshlrev_b32 v178, 16, v125\n\t" \
  "v_and_b32 v179, s98, v125\n\t" \
  "v_lshlrev_b32 v180, 16, v126\n\t" \
  "v_and_b32 v181, s98, v126\n\t" \
  "v_lshlrev_b32 v182, 16, v127\n\t" \
  "v_and_b32 v183, s98, v127\n\t" \
  "v_pk_fma_f32 v[176:177], v[176:177], v[172:173], v[174:175] op_sel_hi:[1,0,0]\n\t" \
  "v_pk_fma_f32 v[178:179], v[178:179], v[172:173], v[174:175] op_sel_hi:[1,0,0]\n\t" \
  "v_pk_fma_f32 v[180:181], v[180:181], v[172:173], v[174:175] op_sel_hi:[1,0,0]\n\t" \
  "v_pk_fma_f32 v[182:183], v[182:183], v[172:173], v[174:175] op_sel_hi:[1,0,0]\n\t" \
  "v_pk_fma_f32 v[176:177], v[176:177], v[144:145], v[152:153]\n\t" \
  "v_pk_fma_f32 v[178:179], v[178:179], v[146:147], v[154:155]\n\t" \
  "v_pk_fma_f32 v[180:181], v[180:181], v[148:149], v[156:157]\n\t" \
  "v_pk_fma_f32 v[182:183], v[182:183], v[150:151], v[158:159]\n\t" \
  "v_cvt_pk_bf16_f32 v124, v176, v177\n\t" \
  "v_cvt_pk_bf16_f32 v125, v178, v179\n\t" \
  "v_cvt_pk_bf16_f32 v126, v180, v181\n\t" \
  "v_cvt_pk_bf16_f32 v127, v182, v183\n\t" \
  "buffer_load_dwordx4 v203, s[20:23], s80 offen offset:3072 lds\n\t" \
  "ds_read_b64_tr_b16 a[152:153], v190 offset:0\n\t" \
  "ds_read_b64_tr_b16 a[154:155], v190 offset:1024\n\t" \
  "ds_read_b64_tr_b16 a[156:157], v191 offset:0\n\t" \
  "ds_read_b64_tr_b16 a[158:159], v191 offset:1024\n\t" \
  "ds_read_b128 v[144:147], v208 offset:1152\n\t" \
  "ds_read_b128 v[148:151], v208 offset:1168\n\t" \
  "ds_read_b128 v[152:155], v208 offset:1408\n\t" \
  "ds_read_b128 v[156:159], v208 offset:1424\n\t" \
  FSTP("v_writelane_b32 v255, s94, 32\n\t") \
  FSTP("v_writelane_b32 v255, s92, 33\n\t") \
  FSTP("s_memrealtime s[94:95]\n\t") \
  FSTP("s_memtime s[92:93]\n\t") \
  "s_waitcnt vmcnt(12)\n\t" \
  "s_mov_b64 exec, 1\n\t" \
  "buffer_store_dword v211, off, s[16:19], s85 sc1\n\t" \
  "s_mov_b64 exec, -1\n\t" \
  FSTP("v_writelane_b32 v255, s94, 34\n\t") \
  FSTP("v_writelane_b32 v255, s92, 35\n\t") \
  FSTP("s_memrealtime s[94:95]\n\t") \
  FSTP("s_memtime s[92:93]\n\t") \
  "s_waitcnt lgkmcnt(4)\n\t" \
  "v_mfma_f32_16x16x32_bf16 v[0:3], a[128:131], v[16:19], 0\n\t" \
  "s_add_u32 m0, s77, 0\n\t" \
  "s_xor_b32 s96, s87, 0\n\t" \
  "s_add_u32 s96, s96, s89\n\t" \
  "v_mfma_f32_16x16x32_bf16 v[32:35], a[128:131], v[48:51], 0\n\t" \
  "buffer_load_dwordx4 v212, s[4:7], s96 offen lds\n\t" \
  "v_mfma_f32_16x16x32_bf16 v[64:67], a[128:131], v[80:83], 0\n\t" \
  "ds_read_b64_tr_b16 a[160:161], v184 offset:8192\n\t" \
  "v_mfma_f32_16x16x32_bf16 v[96:99], a[128:131], v[112:115], 0\n\t" \
  "ds_read_b64_tr_b16 a[162:163], v184 offset:9216\n\t" \
  "v_mfma_f32_16x16x32_bf16 v[100:103], a[132:135], v[112:115], 0\n\t" \
  "buffer_load_dwordx4 v213, s[4:7], s96 offen offset:1024 lds\n\t" \
  "v_mfma_f32_16x16x32_bf16 v[68:71], a[132:135], v[80:83], 0\n\t" \
  "s_add_u32 m0, s77, 2048\n\t" \
  "ds_read_b64_tr_b16 a[164:165], v185 offset:8192\n\t" \
  "s_xor_b32 s96, s87, 131072\n\t" \
  "v_mfma_f32_16x16x32_bf16 v[36:39], a[132:135], v[48:51], 0\n\t" \
  "ds_read_b64_tr_b16 a[166:167], v185 offset:9216\n\t" \
  "s_add_u32 s96, s96, s89\n\t" \
  "v_mfma_f32_16x16x32_bf16 v[4:7], a[132:135], v[16:19], 0\n\t" \
  "ds_read_b64_tr_b16 a[168:169], v186 offset:8192\n\t" \
  "v_mfma_f32_16x16x32_bf16 v[8:11], a[136:139], v[16:19], 0\n\t" \
  "buffer_load_dwordx4 v212, s[4:7], s96 offen lds\n\t" \
  "v_mfma_f32_16x16x32_bf16 v[40:43], a[136:139], v[48:51], 0\n\t" \
  "ds_read_b64_tr_b16 a[170:171], v186 offset:9216\n\t" \
  "v_mfma_f32_16x16x32_bf16 v[72:75], a[136:139], v[80:83], 0\n\t" \
  "ds_read_b64_tr_b16 a[172:173], v187 offset:8192\n\t" \
  "v_mfma_f32_16x16x32_bf16 v[104:107], a[136:139], v[112:115], 0\n\t" \
  "buffer_load_dwordx4 v213, s[4:7], s96 offen offset:1024 lds\n\t" \
  "v_mfma_f32_16x16x32_bf16 v[108:111], a[140:143], v[112:115], 0\n\t" \
  "s_add_u32 m0, s77, 4096\n\t" \
  "ds_read_b64_tr_b16 a[174:175], v187 offset:9216\n\t" \
  "s_xor_b32 s96, s87, 262144\n\t" \
  "v_mfma_f32_16x16x32_bf16 v[76:79], a[140:143], v[80:83], 0\n\t" \
  "ds_read_b64_tr_b16 a[176:177], v188 offset:8192\n\t" \
  "s_add_u32 s96, s96, s89\n\t" \
  "v_mfma_f32_16x16x32_bf16 v[44:47], a[140:143], v[48:51], 0\n\t" \
  "buffer_load_dwordx4 v212, s[4:7], s96 offen lds\n\t" \
  "v_mfma_f32_16x16x32_bf16 v[12:15], a[140:143], v[16:19], 0\n\t" \
  "ds_read_b64_tr_b16 a[178:179], v188 offset:9216\n\t" \
  "v_mfma_f32_16x16x32_bf16 a[64:67], a[144:147], v[16:19], 0\n\t" \
  "ds_read_b64_tr_b16 a[180:181], v189 offset:8192\n\t" \
  "v_mfma_f32_16x16x32_bf16 a[68:71], a[144:147], v[48:51], 0\n\t" \
  "buffer_load_dwordx4 v213, s[4:7], s96 offen offset:1024 lds\n\t" \
  "v_mfma_f32_16x16x32_bf16 a[72:75], a[144:147], v[80:83], 0\n\t" \
  "s_add_u32 m0, s77, 6144\n\t" \
  "ds_read_b64_tr_b16 a[182:183], v189 offset:9216\n\t" \
  "s_xor_b32 s96, s87, 393216\n\t" \
  "v_mfma_f32_16x16x32_bf16 a[76:79], a[144:147], v[112:115], 0\n\t" \
  "ds_read_b64_tr_b16 a[184:185], v190 offset:8192\n\t" \
  "s_add_u32 s96, s96, s89\n\t" \
  "v_mfma_f32_16x16x32_bf16 a[92:95], a[148:151], v[112:115], 0\n\t" \
  "ds_read_b64_tr_b16 a[186:187], v190 offset:9216\n\t" \
  "v_mfma_f32_16x16x32_bf16 a[88:91], a[148:151], v[80:83], 0\n\t" \
  "buffer_load_dwordx4 v212, s[4:7], s96 offen lds\n\t" \
  "v_mfma_f32_16x16x32_bf16 a[84:87], a[148:151], v[48:51], 0\n\t" \
  "ds_read_b64_tr_b16 a[188:189], v191 offset:8192\n\t" \
  "v_mfma_f32_16x16x32_bf16 a[80:83], a[148:151], v[16:19], 0\n\t" \
  "ds_read_b64_tr_b16 a[190:191], v191 offset:9216\n\t" \
  "v_mfma_f32_16x16x32_bf16 a[96:99], a[152:155], v[16:19], 0\n\t" \
  "v_mfma_f32_16x16x32_bf16 a[100:103], a[152:155], v[48:51], 0\n\t" \
  "buffer_load_dwordx4 v213, s[4:7], s96 offen offset:1024 lds\n\t" \
  "v_mfma_f32_16x16x32_bf16 a[104:107], a[152:155], v[80:83], 0\n\t" \
  "v_mfma_f32_16x16x32_bf16 a[108:111], a[152:155], v[112:115], 0\n\t" \
  "v_mfma_f32_16x16x32_bf16 a[124:127], a[156:159], v[112:115], 0\n\t" \
  "v_mfma_f32_16x16x32_bf16 a[120:123], a[156:159], v[80:83], 0\n\t" \
  "s_add_u32 m0, s76, 0\n\t" \
  "v_mfma_f32_16x16x32_bf16 a[116:119], a[156:159], v[48:51], 0\n\t" \
  "s_waitcnt lgkmcnt(0)\n\t" \
  "v_mfma_f32_16x16x32_bf16 a[112:115], a[156:159], v[16:19], 0\n\t" \
  "s_waitcnt vmcnt(13)\n\t" \
  "ds_read_b128 v[16:19], v206 offset:0\n\t" \
  "ds_read_b128 v[48:51], v206 offset:2048\n\t" \
  "s_barrier\n\t" \
  FSTP("v_writelane_b32 v255, s94, 36\n\t") \
  FSTP("v_writelane_b32 v255, s92, 37\n\t") \
  FSTP("s_memrealtime s[94:95]\n\t") \
  FSTP("s_memtime s[92:93]\n\t") \
  "v_mfma_f32_16x16x32_bf16 v[0:3], a[160:163], v[20:23], v[0:3]\n\t" \
  "buffer_load_dwordx4 v200, s[20:23], s81 offen offset:0 lds\n\t" \
  "ds_read_b128 v[80:83], v206 offset:4096\n\t" \
  "ds_read_b128 v[112:115], v206 offset:6144\n\t" \
  "v_mfma_f32_16x16x32_bf16 v[32:35], a[160:163], v[52:55], v[32:35]\n\t" \
  "ds_read_b64_tr_b16 a[128:129], v184 offset:49152\n\t" \
  "v_mfma_f32_16x16x32_bf16 v[64:67], a[160:163], v[84:87], v[64:67]\n\t" \
  "ds_read_b64_tr_b16 a[130:131], v184 offset:50176\n\t" \
  "v_mfma_f32_16x16x32_bf16 v[96:99], a[160:163], v[116:119], v[96:99]\n\t" \
  "buffer_load_dwordx4 v201, s[20:23], s81 offen offset:1024 lds\n\t" \
  "v_mfma_f32_16x16x32_bf16 v[100:103], a[164:167], v[116:119], v[100:103]\n\t" \
  "ds_read_b64_tr_b16 a[132:133], v185 offset:49152\n\t" \
  "v_mfma_f32_16x16x32_bf16 v[68:71], a[164:167], v[84:87], v[68:71]\n\t" \
  "s_waitcnt lgkmcnt(3)\n\t" \
  "v_lshlrev_b32 v176, 16, v16\n\t" \
  "v_and_b32 v177, s98, v16\n\t" \
  "v_mfma_f32_16x16x32_bf16 v[36:39], a[164:167], v[52:55], v[36:39]\n\t" \
  "ds_read_b64_tr_b16 a[134:135], v185 offset:50176\n\t" \
  "v_lshlrev_b32 v178, 16, v17\n\t" \
  "v_mfma_f32_16x16x32_bf16 v[4:7], a[164:167], v[20:23], v[4:7]\n\t" \
  "v_and_b32 v179, s98, v17\n\t" \
  "v_lshlrev_b32 v180, 16, v18\n\t" \
  "v_mfma_f32_16x16x32_bf16 v[8:11], a[168:171], v[20:23], v[8:11]\n\t" \
  "buffer_load_dwordx4 v202, s[20:23], s81 offen offset:2048 lds\n\t" \
  "v_mfma_f32_16x16x32_bf16 v[40:43], a[168:171], v[52:55], v[40:43]\n\t" \
  "ds_read_b64_tr_b16 a[136:137], v186 offset:49152\n\t" \
  "v_and_b32 v181, s98, v18\n\t" \
  "v_mfma_f32_16x16x32_bf16 v[72:75], a[168:171], v[84:87], v[72:75]\n\t" \
  "ds_read_b64_tr_b16 a[138:139], v186 offset:50176\n\t" \
  "v_lshlrev_b32 v182, 16, v19\n\t" \
  "v_mfma_f32_16x16x32_bf16 v[104:107], a[168:171], v[116:119], v[104:107]\n\t" \
  "ds_read_b64_tr_b16 a[140:141], v187 offset:49152\n\t" \
  "v_and_b32 v183, s98, v19\n\t" \
  "v_mfma_f32_16x16x32_bf16 v[108:111], a[172:175], v[116:119], v[108:111]\n\t" \
  "buffer_load_dwordx4 v203, s[20:23], s81 offen offset:3072 lds\n\t" \
  "v_mfma_f32_16x16x32_bf16 v[76:79], a[172:175], v[84:87], v[76:79]\n\t" \
  "s_add_u32 s96, s78, 2097152\n\t" \
  "s_and_b32 s96, s96, 0x780000\n\t" \
  "s_add_u32 m0, s76, 16384\n\t" \
  "v_mfma_f32_16x16x32_bf16 v[44:47], a[172:175], v[52:55], v[44:47]\n\t" \
  "ds_read_b64_tr_b16 a[142:143], v187 offset:50176\n\t" \
  "v_lshlrev_b32 v220, 16, v48\n\t" \
  "v_and_b32 v221, s98, v48\n\t" \
  "v_mfma_f32_16x16x32_bf16 v[12:15], a[172:175], v[20:23], v[12:15]\n\t" \
  "buffer_load_dwordx4 v200, s[20:23], s96 offen offset:0 lds\n\t" \
  "v_mfma_f32_16x16x32_bf16 a[64:67], a[176:179], v[20:23], a[64:67]\n\t" \
  "ds_read_b64_tr_b16 a[144:145], v188 offset:49152\n\t" \
  "v_lshlrev_b32 v222, 16, v49\n\t" \
  "v_mfma_f32_16x16x32_bf16 a[68:71], a[176:179], v[52:55], a[68:71]\n\t" \
  "ds_read_b64_tr_b16 a[146:147], v188 offset:50176\n\t" \
  "v_and_b32 v223, s98, v49\n\t" \
  "v_mfma_f32_16x16x32_bf16 a[72:75], a[176:179], v[84:87], a[72:75]\n\t" \
  "ds_read_b64_tr_b16 a[148:149], v189 offset:49152\n\t" \
  "v_lshlrev_b32 v224, 16, v50\n\t" \
  "v_mfma_f32_16x16x32_bf16 a[76:79], a[176:179], v[116:119], a[76:79]\n\t" \
  "buffer_load_dwordx4 v201, s[20:23], s96 offen offset:1024 lds\n\t" \
  "v_mfma_f32_16x16x32_bf16 a[92:95], a[180:183], v[116:119], a[92:95]\n\t" \
  "ds_read_b64_tr_b16 a[150:151], v189 offset:50176\n\t" \
  "v_and_b32 v225, s98, v50\n\t" \
  "v_mfma_f32_16x16x32_bf16 a[88:91], a[180:183], v[84:87], a[88:91]\n\t" \
  "ds_read_b64_tr_b16 a[152:153], v190 offset:49152\n\t" \
  "v_lshlrev_b32 v226, 16, v51\n\t" \
  "v_mfma_f32_16x16x32_bf16 a[84:87], a[180:183], v[52:55], a[84:87]\n\t" \
  "ds_read_b64_tr_b16 a[154:155], v190 offset:50176\n\t" \
  "v_and_b32 v227, s98, v51\n\t" \
  "v_mfma_f32_16x16x32_bf16 a[80:83], a[180:183], v[20:23], a[80:83]\n\t" \
  "buffer_load_dwordx4 v202, s[20:23], s96 offen offset:2048 lds\n\t" \
  "ds_read_b64_tr_b16 a[156:157], v191 offset:49152\n\t" \
  "ds_read_b64_tr_b16 a[158:159], v191 offset:50176\n\t" \
  "v_lshlrev_b32 v228, 16, v80\n\t" \
  "v_pk_fma_f32 v[176:177], v[176:177], v[160:161], v[162:163] op_sel_hi:[1,0,0]\n\t" \
  "v_pk_fma_f32 v[178:179], v[178:179], v[160:161], v[162:163] op_sel_hi:[1,0,0]\n\t" \
  "v_pk_fma_f32 v[180:181], v[180:181], v[160:161], v[162:163] op_sel_hi:[1,0,0]\n\t" \
  "v_pk_fma_f32 v[182:183], v[182:183], v[160:161], v[162:163] op_sel_hi:[1,0,0]\n\t" \
  "v_pk_fma_f32 v[176:177], v[176:177], v[128:129], v[136:137]\n\t" \
  "v_pk_fma_f32 v[178:179], v[178:179], v[130:131], v[138:139]\n\t" \
  "v_pk_fma_f32 v[180:181], v[180:181], v[132:133], v[140:141]\n\t" \
  "v_pk_fma_f32 v[182:183], v[182:183], v[134:135], v[142:143]\n\t" \
  "v_pk_fma_f32 v[220:221], v[220:221], v[164:165], v[166:167] op_sel_hi:[1,0,0]\n\t" \
  "v_pk_fma_f32 v[222:223], v[222:223], v[164:165], v[166:167] op_sel_hi:[1,0,0]\n\t" \
  "v_pk_fma_f32 v[224:225], v[224:225], v[164:165], v[166:167] op_sel_hi:[1,0,0]\n\t" \
  "v_pk_fma_f32 v[226:227], v[226:227], v[164:165], v[166:167] op_sel_hi:[1,0,0]\n\t" \
  "v_pk_fma_f32 v[220:221], v[220:221], v[128:129], v[136:137]\n\t" \
  "v_pk_fma_f32 v[222:223], v[222:223], v[130:131], v[138:139]\n\t" \
  "v_pk_fma_f32 v[224:225], v[224:225], v[132:133], v[140:141]\n\t" \
  "v_pk_fma_f32 v[226:227], v[226:227], v[134:135], v[142:143]\n\t" \
  "v_cvt_pk_bf16_f32 v16, v176, v177\n\t" \
  "v_mfma_f32_16x16x32_bf16 a[96:99], a[184:187], v[20:23], a[96:99]\n\t" \
  "v_cvt_pk_bf16_f32 v17, v178, v179\n\t" \
  "v_cvt_pk_bf16_f32 v18, v180, v181\n\t" \
  "v_and_b32 v229, s98, v80\n\t" \
  "v_mfma_f32_16x16x32_bf16 a[100:103], a[184:187], v[52:55], a[100:103]\n\t" \
  "v_cvt_pk_bf16_f32 v19, v182, v183\n\t" \
  "v_cvt_pk_bf16_f32 v48, v220, v221\n\t" \
  "v_lshlrev_b32 v230, 16, v81\n\t" \
  "v_and_b32 v231, s98, v81\n\t" \
  "v_mfma_f32_16x16x32_bf16 a[104:107], a[184:187], v[84:87], a[104:107]\n\t" \
  "v_lshlrev_b32 v232, 16, v82\n\t" \
  "v_and_b32 v233, s98, v82\n\t" \
  "v_mfma_f32_16x16x32_bf16 a[108:111], a[184:187], v[116:119], a[108:111]\n\t" \
  "buffer_load_dwordx4 v203, s[20:23], s96 offen offset:3072 lds\n\t" \
  "v_mfma_f32_16x16x32_bf16 a[124:127], a[188:191], v[116:119], a[124:127]\n\t" \
  "v_lshlrev_b32 v234, 16, v83\n\t" \
  "v_and_b32 v235, s98, v83\n\t" \
  "v_mfma_f32_16x16x32_bf16 a[120:123], a[188:191], v[84:87], a[120:123]\n\t" \
  "s_add_u32 s96, s78, 2621440\n\t" \
  "v_lshlrev_b32 v236, 16, v112\n\t" \
  "v_and_b32 v237, s98, v112\n\t" \
  "v_mfma_f32_16x16x32_bf16 a[116:119], a[188:191], v[52:55], a[116:119]\n\t" \
  "s_and_b32 s96, s96, 0x780000\n\t" \
  "s_add_u32 m0, s76, 32768\n\t" \
  "v_lshlrev_b32 v238, 16, v113\n\t" \
  "v_mfma_f32_16x16x32_bf16 a[112:115], a[188:191], v[20:23], a[112:115]\n\t" \
  "s_waitcnt lgkmcnt(0)\n\t" \
  "ds_read_b128 v[20:23], v207 offset:0\n\t" \
  "ds_read_b128 v[52:55], v207 offset:2048\n\t" \
  "ds_read_b128 v[84:87], v207 offset:4096\n\t" \
  "v_and_b32 v239, s98, v113\n\t" \
  FSTP("v_writelane_b32 v255, s94, 38\n\t") \
  FSTP("v_writelane_b32 v255, s92, 39\n\t") \
  FSTP("s_memrealtime s[94:95]\n\t") \
  FSTP("s_memtime s[92:93]\n\t") \
  "v_mfma_f32_16x16x32_bf16 v[0:3], a[128:131], v[24:27], v[0:3]\n\t" \
  "buffer_load_dwordx4 v200, s[20:23], s96 offen offset:0 lds\n\t" \
  "ds_read_b128 v[116:119], v207 offset:6144\n\t" \
  "v_mfma_f32_16x16x32_bf16 v[32:35], a[128:131], v[56:59], v[32:35]\n\t" \
  "v_lshlrev_b32 v240, 16, v114\n\t" \
  "v_and_b32 v241, s98, v114\n\t" \
  "v_mfma_f32_16x16x32_bf16 v[64:67], a[128:131], v[88:91], v[64:67]\n\t" \
  "v_lshlrev_b32 v242, 16, v115\n\t" \
  "v_and_b32 v243, s98, v115\n\t" \
  "v_mfma_f32_16x16x32_bf16 v[96:99], a[128:131], v[120:123], v[96:99]\n\t" \
  "ds_read_b64_tr_b16 a[160:161], v184 offset:57344\n\t" \
  "ds_read_b64_tr_b16 a[162:163], v184 offset:58368\n\t" \
  "v_cvt_pk_bf16_f32 v49, v222, v223\n\t" \
  "v_cvt_pk_bf16_f32 v50, v224, v225\n\t" \
  "v_cvt_pk_bf16_f32 v51, v226, v227\n\t" \
  "v_pk_fma_f32 v[228:229], v[228:229], v[168:169], v[170:171] op_sel_hi:[1,0,0]\n\t" \
  "v_pk_fma_f32 v[230:231], v[230:231], v[168:169], v[170:171] op_sel_hi:[1,0,0]\n\t" \
  "v_pk_fma_f32 v[232:233], v[232:233], v[168:169], v[170:171] op_sel_hi:[1,0,0]\n\t" \
  "v_pk_fma_f32 v[234:235], v[234:235], v[168:169], v[170:171] op_sel_hi:[1,0,0]\n\t" \
  "v_pk_fma_f32 v[228:229], v[228:229], v[128:129], v[136:137]\n\t" \
  "v_pk_fma_f32 v[230:231], v[230:231], v[130:131], v[138:139]\n\t" \
  "v_pk_fma_f32 v[232:233], v[232:233], v[132:133], v[140:141]\n\t" \
  "v_pk_fma_f32 v[234:235], v[234:235], v[134:135], v[142:143]\n\t" \
  "v_pk_fma_f32 v[236:237], v[236:237], v[172:173], v[174:175] op_sel_hi:[1,0,0]\n\t" \
  "v_pk_fma_f32 v[238:239], v[238:239], v[172:173], v[174:175] op_sel_hi:[1,0,0]\n\t" \
  "v_pk_fma_f32 v[240:241], v[240:241], v[172:173], v[174:175] op_sel_hi:[1,0,0]\n\t" \
  "v_pk_fma_f32 v[242:243], v[242:243], v[172:173], v[174:175] op_sel_hi:[1,0,0]\n\t" \
  "v_pk_fma_f32 v[236:237], v[236:237], v[128:129], v[136:137]\n\t" \
  "v_pk_fma_f32 v[238:239], v[238:239], v[130:131], v[138:139]\n\t" \
  "v_pk_fma_f32 v[240:241], v[240:241], v[132:133], v[140:141]\n\t" \
  "v_pk_fma_f32 v[242:243], v[242:243], v[134:135], v[142:143]\n\t" \
  "v_cvt_pk_bf16_f32 v80, v228, v229\n\t" \
  "v_mfma_f32_16x16x32_bf16 v[100:103], a[132:135], v[120:123], v[100:103]\n\t" \
  "ds_read_b64_tr_b16 a[164:165], v185 offset:57344\n\t" \
  "v_cvt_pk_bf16_f32 v81, v230, v231\n\t" \
  "v_mfma_f32_16x16x32_bf16 v[68:71], a[132:135], v[88:91], v[68:71]\n\t" \
  "s_waitcnt lgkmcnt(3)\n\t" \
  "v_lshlrev_b32 v176, 16, v20\n\t" \
  "v_and_b32 v177, s98, v20\n\t" \
  "v_mfma_f32_16x16x32_bf16 v[36:39], a[132:135], v[56:59], v[36:39]\n\t" \
  "buffer_load_dwordx4 v201, s[20:23], s96 offen offset:1024 lds\n\t" \
  "v_mfma_f32_16x16x32_bf16 v[4:7], a[132:135], v[24:27], v[4:7]\n\t" \
  "ds_read_b64_tr_b16 a[166:167], v185 offset:58368\n\t" \
  "v_lshlrev_b32 v178, 16, v21\n\t" \
  "v_mfma_f32_16x16x32_bf16 v[8:11], a[136:139], v[24:27], v[8:11]\n\t" \
  "ds_read_b64_tr_b16 a[168:169], v186 offset:57344\n\t" \
  "v_and_b32 v179, s98, v21\n\t" \
  "v_mfma_f32_16x16x32_bf16 v[40:43], a[136:139], v[56:59], v[40:43]\n\t" \
  "v_lshlrev_b32 v180, 16, v22\n\t" \
  "v_and_b32 v181, s98, v22\n\t" \
  "v_mfma_f32_16x16x32_bf16 v[72:75], a[136:139], v[88:91], v[72:75]\n\t" \
  "ds_read_b64_tr_b16 a[170:171], v186 offset:58368\n\t" \
  "v_lshlrev_b32 v182, 16, v23\n\t" \
  "v_mfma_f32_16x16x32_bf16 v[104:107], a[136:139], v[120:123], v[104:107]\n\t" \
  "ds_read_b64_tr_b16 a[172:173], v187 offset:57344\n\t" \
  "v_and_b32 v183, s98, v23\n\t" \
  "v_mfma_f32_16x16x32_bf16 v[108:111], a[140:143], v[120:123], v[108:111]\n\t" \
  "buffer_load_dwordx4 v202, s[20:23], s96 offen offset:2048 lds\n\t" \
  "v_mfma_f32_16x16x32_bf16 v[76:79], a[140:143], v[88:91], v[76:79]\n\t" \
  "ds_read_b64_tr_b16 a[174:175], v187 offset:58368\n\t" \
  "v_lshlrev_b32 v220, 16, v52\n\t" \
  "v_mfma_f32_16x16x32_bf16 v[44:47], a[140:143], v[56:59], v[44:47]\n\t" \
  "v_and_b32 v221, s98, v52\n\t" \
  "v_lshlrev_b32 v222, 16, v53\n\t" \
  "v_mfma_f32_16x16x32_bf16 v[12:15], a[140:143], v[24:27], v[12:15]\n\t" \
  "ds_read_b64_tr_b16 a[176:177], v188 offset:57344\n\t" \
  "v_and_b32 v223, s98, v53\n\t" \
  "v_mfma_f32_16x16x32_bf16 a[64:67], a[144:147], v[24:27], a[64:67]\n\t" \
  "ds_read_b64_tr_b16 a[178:179], v188 offset:58368\n\t" \
  "v_lshlrev_b32 v224, 16, v54\n\t" \
  "v_mfma_f32_16x16x32_bf16 a[68:71], a[144:147], v[56:59], a[68:71]\n\t" \
  "ds_read_b64_tr_b16 a[180:181], v189 offset:57344\n\t" \
  "v_and_b32 v225, s98, v54\n\t" \
  "v_mfma_f32_16x16x32_bf16 a[72:75], a[144:147], v[88:91], a[72:75]\n\t" \
  "buffer_load_dwordx4 v203, s[20:23], s96 offen offset:3072 lds\n\t" \
  "v_mfma_f32_16x16x32_bf16 a[76:79], a[144:147], v[120:123], a[76:79]\n\t" \
  "ds_read_b64_tr_b16 a[182:183], v189 offset:58368\n\t" \
  "v_cvt_pk_bf16_f32 v82, v232, v233\n\t" \
  "v_mfma_f32_16x16x32_bf16 a[92:95], a[148:151], v[120:123], a[92:95]\n\t" \
  "v_lshlrev_b32 v226, 16, v55\n\t" \
  "v_and_b32 v227, s98, v55\n\t" \
  "v_mfma_f32_16x16x32_bf16 a[88:91], a[148:151], v[88:91], a[88:91]\n\t" \
  "ds_read_b64_tr_b16 a[184:185], v190 offset:57344\n\t" \
  "v_cvt_pk_bf16_f32 v83, v234, v235\n\t" \
  "v_cvt_pk_bf16_f32 v112, v236, v237\n\t" \
  "v_cvt_pk_bf16_f32 v113, v238, v239\n\t" \
  "v_pk_fma_f32 v[176:177], v[176:177], v[160:161], v[162:163] op_sel_hi:[1,0,0]\n\t" \
  "v_pk_fma_f32 v[178:179], v[178:179], v[160:161], v[162:163] op_sel_hi:[1,0,0]\n\t" \
  "v_pk_fma_f32 v[180:181], v[180:181], v[160:161], v[162:163] op_sel_hi:[1,0,0]\n\t" \
  "v_pk_fma_f32 v[182:183], v[182:183], v[160:161], v[162:163] op_sel_hi:[1,0,0]\n\t" \
  "v_pk_fma_f32 v[176:177], v[176:177], v[144:145], v[152:153]\n\t" \
  "v_pk_fma_f32 v[178:179], v[178:179], v[146:147], v[154:155]\n\t" \
  "v_pk_fma_f32 v[180:181], v[180:181], v[148:149], v[156:157]\n\t" \
  "v_pk_fma_f32 v[182:183], v[182:183], v[150:151], v[158:159]\n\t" \
  "v_pk_fma_f32 v[220:221], v[220:221], v[164:165], v[166:167] op_sel_hi:[1,0,0]\n\t" \
  "v_pk_fma_f32 v[222:223], v[222:223], v[164:165], v[166:167] op_sel_hi:[1,0,0]\n\t" \
  "v_pk_fma_f32 v[224:225], v[224:225], v[164:165], v[166:167] op_sel_hi:[1,0,0]\n\t" \
  "v_pk_fma_f32 v[226:227], v[226:227], v[164:165], v[166:167] op_sel_hi:[1,0,0]\n\t" \
  "v_pk_fma_f32 v[220:221], v[220:221], v[144:145], v[152:153]\n\t" \
  "v_pk_fma_f32 v[222:223], v[222:223], v[146:147], v[154:155]\n\t" \
  "v_pk_fma_f32 v[224:225], v[224:225], v[148:149], v[156:157]\n\t" \
  "v_pk_fma_f32 v[226:227], v[226:227], v[150:151], v[158:159]\n\t" \
  "v_cvt_pk_bf16_f32 v20, v176, v177\n\t" \
  "v_mfma_f32_16x16x32_bf16 a[84:87], a[148:151], v[56:59], a[84:87]\n\t" \
  "ds_read_b64_tr_b16 a[186:187], v190 offset:58368\n\t" \
  "v_lshlrev_b32 v228, 16, v84\n\t" \
  "v_mfma_f32_16x16x32_bf16 a[80:83], a[148:151], v[24:27], a[80:83]\n\t" \
  "ds_read_b64_tr_b16 a[188:189], v191 offset:57344\n\t" \
  "v_and_b32 v229, s98, v84\n\t" \
  "v_mfma_f32_16x16x32_bf16 a[96:99], a[152:155], v[24:27], a[96:99]\n\t" \
  "global_load_dword v246, v217, s[12:13]\n\t" \
  "v_mfma_f32_16x16x32_bf16 a[100:103], a[152:155], v[56:59], a[100:103]\n\t" \
  "ds_read_b64_tr_b16 a[190:191], v191 offset:58368\n\t" \
  "v_lshlrev_b32 v230, 16, v85\n\t" \
  "v_mfma_f32_16x16x32_bf16 a[104:107], a[152:155], v[88:91], a[104:107]\n\t" \
  "v_and_b32 v231, s98, v85\n\t" \
  "v_lshlrev_b32 v232, 16, v86\n\t" \
  "v_mfma_f32_16x16x32_bf16 a[108:111], a[152:155], v[120:123], a[108:111]\n\t" \
  ".Lhs1:\n\ts_cbranch_execz .Lhs1n\n\t" \
  "v_and_b32 v233, s98, v86\n\t" \
  "v_lshlrev_b32 v234, 16, v87\n\t" \
  "v_mfma_f32_16x16x32_bf16 a[124:127], a[156:159], v[120:123], a[124:127]\n\t" \
  "v_cvt_pk_bf16_f32 v114, v240, v241\n\t" \
  "v_cvt_pk_bf16_f32 v115, v242, v243\n\t" \
  "v_mfma_f32_16x16x32_bf16 a[120:123], a[156:159], v[88:91], a[120:123]\n\t" \
  "s_add_u32 s96, s78, 3145728\n\t" \
  "v_and_b32 v235, s98, v87\n\t" \
  "v_lshlrev_b32 v236, 16, v116\n\t" \
  "v_mfma_f32_16x16x32_bf16 a[116:119], a[156:159], v[56:59], a[116:119]\n\t" \
  "s_waitcnt vmcnt(22) lgkmcnt(0)\n\t" \
  "s_and_b32 s96, s96, 0x780000\n\t" \
  "s_add_u32 m0, s76, 49152\n\t" \
  "v_mfma_f32_16x16x32_bf16 a[112:115], a[156:159], v[24:27], a[112:115]\n\t" \
  "s_barrier\n\t" \
  FSTP("v_writelane_b32 v255, s94, 40\n\t") \
  FSTP("v_writelane_b32 v255, s92, 41\n\t") \
  FSTP("s_memrealtime s[94:95]\n\t") \
  FSTP("s_memtime s[92:93]\n\t") \
  "v_mfma_f32_16x16x32_bf16 v[0:3], a[160:163], v[28:31], v[0:3]\n\t" \
  "buffer_load_dwordx4 v200, s[20:23], s96 offen offset:0 lds\n\t" \
  "ds_read_b64_tr_b16 a[128:129], v192 offset:32768\n\t" \
  "v_mfma_f32_16x16x32_bf16 v[32:35], a[160:163], v[60:63], v[32:35]\n\t" \
  "s_waitcnt vmcnt(14)\n\t" \
  "ds_read_b128 v[24:27], v204 offset:0\n\t" \
  "ds_read_b64_tr_b16 a[130:131], v192 offset:33792\n\t" \
  "v_and_b32 v237, s98, v116\n\t" \
  "v_mfma_f32_16x16x32_bf16 v[64:67], a[160:163], v[92:95], v[64:67]\n\t" \
  "ds_read_b128 v[56:59], v204 offset:2048\n\t" \
  "ds_read_b64_tr_b16 a[132:133], v193 offset:32768\n\t" \
  "v_cvt_pk_bf16_f32 v21, v178, v179\n\t" \
  "v_mfma_f32_16x16x32_bf16 v[96:99], a[160:163], v[124:127], v[96:99]\n\t" \
  "ds_read_b128 v[88:91], v204 offset:4096\n\t" \
  "v_lshlrev_b32 v238, 16, v117\n\t" \
  "v_mfma_f32_16x16x32_bf16 v[100:103], a[164:167], v[124:127], v[100:103]\n\t" \
  "ds_read_b128 v[120:123], v204 offset:6144\n\t" \
  "v_cvt_pk_bf16_f32 v22, v180, v181\n\t" \
  "v_mfma_f32_16x16x32_bf16 v[68:71], a[164:167], v[92:95], v[68:71]\n\t" \
  "buffer_load_dwordx4 v201, s[20:23], s96 offen offset:1024 lds\n\t" \
  "v_mfma_f32_16x16x32_bf16 v[36:39], a[164:167], v[60:63], v[36:39]\n\t" \
  "ds_read_b64_tr_b16 a[134:135], v193 offset:33792\n\t" \
  "v_and_b32 v239, s98, v117\n\t" \
  "v_mfma_f32_16x16x32_bf16 v[4:7], a[164:167], v[28:31], v[4:7]\n\t" \
  "ds_read_b64_tr_b16 a[136:137], v194 offset:32768\n\t" \
  "v_cvt_pk_bf16_f32 v23, v182, v183\n\t" \
  "v_mfma_f32_16x16x32_bf16 v[8:11], a[168:171], v[28:31], v[8:11]\n\t" \
  "buffer_load_dwordx4 v202, s[20:23], s96 offen offset:2048 lds\n\t" \
  "v_mfma_f32_16x16x32_bf16 v[40:43], a[168:171], v[60:63], v[40:43]\n\t" \
  "ds_read_b64_tr_b16 a[138:139], v194 offset:33792\n\t" \
  "v_lshlrev_b32 v240, 16, v118\n\t" \
  "v_mfma_f32_16x16x32_bf16 v[72:75], a[168:171], v[92:95], v[72:75]\n\t" \
  "ds_read_b64_tr_b16 a[140:141], v195 offset:32768\n\t" \
  "v_and_b32 v241, s98, v118\n\t" \
  "v_mfma_f32_16x16x32_bf16 v[104:107], a[168:171], v[124:127], v[104:107]\n\t" \
  "v_lshlrev_b32 v242, 16, v119\n\t" \
  "v_and_b32 v243, s98, v119\n\t" \
  "v_mfma_f32_16x16x32_bf16 v[108:111], a[172:175], v[124:127], v[108:111]\n\t" \
  "buffer_load_dwordx4 v203, s[20:23], s96 offen offset:3072 lds\n\t" \
  "ds_read_b64_tr_b16 a[142:143], v195 offset:33792\n\t" \
  "ds_read_b128 v[128:131], v208 offset:1536\n\t" \
  "v_cvt_pk_bf16_f32 v52, v220, v221\n\t" \
  "v_cvt_pk_bf16_f32 v53, v222, v223\n\t" \
  "v_pk_fma_f32 v[228:229], v[228:229], v[168:169], v[170:171] op_sel_hi:[1,0,0]\n\t" \
  "v_pk_fma_f32 v[230:231], v[230:231], v[168:169], v[170:171] op_sel_hi:[1,0,0]\n\t" \
  "v_pk_fma_f32 v[232:233], v[232:233], v[168:169], v[170:171] op_sel_hi:[1,0,0]\n\t" \
  "v_pk_fma_f32 v[234:235], v[234:235], v[168:169], v[170:171] op_sel_hi:[1,0,0]\n\t" \
  "v_pk_fma_f32 v[228:229], v[228:229], v[144:145], v[152:153]\n\t" \
  "v_pk_fma_f32 v[230:231], v[230:231], v[146:147], v[154:155]\n\t" \
  "v_pk_fma_f32 v[232:233], v[232:233], v[148:149], v[156:157]\n\t" \
  "v_pk_fma_f32 v[234:235], v[234:235], v[150:151], v[158:159]\n\t" \
  "v_pk_fma_f32 v[236:237], v[236:237], v[172:173], v[174:175] op_sel_hi:[1,0,0]\n\t" \
  "v_pk_fma_f32 v[238:239], v[238:239], v[172:173], v[174:175] op_sel_hi:[1,0,0]\n\t" \
  "v_pk_fma_f32 v[240:241], v[240:241], v[172:173], v[174:175] op_sel_hi:[1,0,0]\n\t" \
  "v_pk_fma_f32 v[242:243], v[242:243], v[172:173], v[174:175] op_sel_hi:[1,0,0]\n\t" \
  "v_pk_fma_f32 v[236:237], v[236:237], v[144:145], v[152:153]\n\t" \
  "v_pk_fma_f32 v[238:239], v[238:239], v[146:147], v[154:155]\n\t" \
  "v_pk_fma_f32 v[240:241], v[240:241], v[148:149], v[156:157]\n\t" \
  "v_pk_fma_f32 v[242:243], v[242:243], v[150:151], v[158:159]\n\t" \
  "v_cvt_pk_bf16_f32 v84, v228, v229\n\t" \
  "v_mfma_f32_16x16x32_bf16 v[76:79], a[172:175], v[92:95], v[76:79]\n\t" \
  "s_add_u32 s96, s78, 3670016\n\t" \
  "s_and_b32 s96, s96, 0x780000\n\t" \
  "s_add_u32 m0, s76, 65536\n\t" \
  "v_mfma_f32_16x16x32_bf16 v[44:47], a[172:175], v[60:63], v[44:47]\n\t" \
  "ds_read_b64_tr_b16 a[144:145], v196 offset:32768\n\t" \
  "v_cvt_pk_bf16_f32 v54, v224, v225\n\t" \
  "v_mfma_f32_16x16x32_bf16 v[12:15], a[172:175], v[28:31], v[12:15]\n\t" \
  "buffer_load_dwordx4 v200, s[20:23], s96 offen offset:0 lds\n\t" \
  "v_mfma_f32_16x16x32_bf16 a[64:67], a[176:179], v[28:31], a[64:67]\n\t" \
  "s_waitcnt lgkmcnt(7)\n\t" \
  "v_lshlrev_b32 v176, 16, v24\n\t" \
  "v_and_b32 v177, s98, v24\n\t" \
  "v_mfma_f32_16x16x32_bf16 a[68:71], a[176:179], v[60:63], a[68:71]\n\t" \
  "ds_read_b64_tr_b16 a[146:147], v196 offset:33792\n\t" \
  "v_lshlrev_b32 v178, 16, v25\n\t" \
  "v_mfma_f32_16x16x32_bf16 a[72:75], a[176:179], v[92:95], a[72:75]\n\t" \
  "ds_read_b64_tr_b16 a[148:149], v197 offset:32768\n\t" \
  "v_and_b32 v179, s98, v25\n\t" \
  "v_mfma_f32_16x16x32_bf16 a[76:79], a[176:179], v[124:127], a[76:79]\n\t" \
  "buffer_load_dwordx4 v201, s[20:23], s96 offen offset:1024 lds\n\t" \
  "v_mfma_f32_16x16x32_bf16 a[92:95], a[180:183], v[124:127], a[92:95]\n\t" \
  "ds_read_b64_tr_b16 a[150:151], v197 offset:33792\n\t" \
  "v_lshlrev_b32 v180, 16, v26\n\t" \
  "v_mfma_f32_16x16x32_bf16 a[88:91], a[180:183], v[92:95], a[88:91]\n\t" \
  "ds_read_b64_tr_b16 a[152:153], v198 offset:32768\n\t" \
  "v_and_b32 v181, s98, v26\n\t" \
  "v_mfma_f32_16x16x32_bf16 a[84:87], a[180:183], v[60:63], a[84:87]\n\t" \
  "ds_read_b64_tr_b16 a[154:155], v198 offset:33792\n\t" \
  "v_lshlrev_b32 v182, 16, v27\n\t" \
  "v_mfma_f32_16x16x32_bf16 a[80:83], a[180:183], v[28:31], a[80:83]\n\t" \
  "buffer_load_dwordx4 v202, s[20:23], s96 offen offset:2048 lds\n\t" \
  "v_mfma_f32_16x16x32_bf16 a[96:99], a[184:187], v[28:31], a[96:99]\n\t" \
  "ds_read_b64_tr_b16 a[156:157], v199 offset:32768\n\t" \
  "v_cvt_pk_bf16_f32 v55, v226, v227\n\t" \
  "v_mfma_f32_16x16x32_bf16 a[100:103], a[184:187], v[60:63], a[100:103]\n\t" \
  "ds_read_b64_tr_b16 a[158:159], v199 offset:33792\n\t" \
  "v_and_b32 v183, s98, v27\n\t" \
  "v_mfma_f32_16x16x32_bf16 a[104:107], a[184:187], v[92:95], a[104:107]\n\t" \
  "v_lshlrev_b32 v220, 16, v56\n\t" \
  "v_and_b32 v221, s98, v56\n\t" \
  "v_mfma_f32_16x16x32_bf16 a[108:111], a[184:187], v[124:127], a[108:111]\n\t" \
  "buffer_load_dwordx4 v203, s[20:23], s96 offen offset:3072 lds\n\t" \
  "v_mfma_f32_16x16x32_bf16 a[124:127], a[188:191], v[124:127], a[124:127]\n\t" \
  "v_lshlrev_b32 v222, 16, v57\n\t" \
  "v_and_b32 v223, s98, v57\n\t" \
  "v_mfma_f32_16x16x32_bf16 a[120:123], a[188:191], v[92:95], a[120:123]\n\t" \
  "v_lshlrev_b32 v224, 16, v58\n\t" \
  "v_cvt_pk_bf16_f32 v85, v230, v231\n\t" \
  "v_mfma_f32_16x16x32_bf16 a[116:119], a[188:191], v[60:63], a[116:119]\n\t" \
  "v_and_b32 v225, s98, v58\n\t" \
  "v_lshlrev_b32 v226, 16, v59\n\t" \
  "v_mfma_f32_16x16x32_bf16 a[112:115], a[188:191], v[28:31], a[112:115]\n\t" \
  "s_waitcnt lgkmcnt(0)\n\t" \
  "v_cvt_pk_bf16_f32 v86, v232, v233\n\t" \
  "v_cvt_pk_bf16_f32 v87, v234, v235\n\t" \
  FSTP("v_writelane_b32 v255, s94, 42\n\t") \
  FSTP("v_writelane_b32 v255, s92, 43\n\t") \
  FSTP("s_memrealtime s[94:95]\n\t") \
  FSTP("s_memtime s[92:93]\n\t") \
  "v_mfma_f32_16x16x32_bf16 v[0:3], a[128:131], v[16:19], v[0:3]\n\t" \
  "buffer_load_dword v252, v210, s[16:19], s84 offen sc1\n\t" \
  "v_mfma_f32_16x16x32_bf16 v[32:35], a[128:131], v[48:51], v[32:35]\n\t" \
  "ds_read_b128 v[28:31], v205 offset:0\n\t" \
  "v_and_b32 v227, s98, v59\n\t" \
  "v_mfma_f32_16x16x32_bf16 v[64:67], a[128:131], v[80:83], v[64:67]\n\t" \
  "s_lshr_b32 s99, s87, 9\n\t" \
  "ds_read_b128 v[60:63], v205 offset:2048\n\t" \
  "ds_read_b64_tr_b16 a[160:161], v192 offset:40960\n\t" \
  "v_lshlrev_b32 v228, 16, v88\n\t" \
  "v_mfma_f32_16x16x32_bf16 v[96:99], a[128:131], v[112:115], v[96:99]\n\t" \
  "ds_read_b128 v[92:95], v205 offset:4096\n\t" \
  "ds_read_b64_tr_b16 a[162:163], v192 offset:41984\n\t" \
  "v_xor_b32 v248, s99, v214\n\t" \
  "v_mfma_f32_16x16x32_bf16 v[100:103], a[132:135], v[112:115], v[100:103]\n\t" \
  "v_xor_b32 v249, 256, v248\n\t" \
  "v_xor_b32 v250, 512, v248\n\t" \
  "v_mfma_f32_16x16x32_bf16 v[68:71], a[132:135], v[80:83], v[68:71]\n\t" \
  "v_xor_b32 v251, 768, v248\n\t" \
  "s_add_u32 s96, s78, 4194304\n\t" \
  "v_mfma_f32_16x16x32_bf16 v[36:39], a[132:135], v[48:51], v[36:39]\n\t" \
  "s_and_b32 s96, s96, 0x780000\n\t" \
  "ds_read_b128 v[124:127], v205 offset:6144\n\t" \
  "ds_read_b64_tr_b16 a[164:165], v193 offset:40960\n\t" \
  "s_add_u32 m0, s76, 81920\n\t" \
  "v_mfma_f32_16x16x32_bf16 v[4:7], a[132:135], v[16:19], v[4:7]\n\t" \
  "buffer_load_dwordx4 v200, s[20:23], s96 offen offset:0 lds\n\t" \
  "v_mfma_f32_16x16x32_bf16 v[8:11], a[136:139], v[16:19], v[8:11]\n\t" \
  "ds_read_b64_tr_b16 a[166:167], v193 offset:41984\n\t" \
  "v_and_b32 v229, s98, v88\n\t" \
  "v_mfma_f32_16x16x32_bf16 v[40:43], a[136:139], v[48:51], v[40:43]\n\t" \
  "ds_read_b64_tr_b16 a[168:169], v194 offset:40960\n\t" \
  "v_lshlrev_b32 v230, 16, v89\n\t" \
  "v_mfma_f32_16x16x32_bf16 v[72:75], a[136:139], v[80:83], v[72:75]\n\t" \
  "buffer_load_dwordx4 v201, s[20:23], s96 offen offset:1024 lds\n\t" \
  "v_mfma_f32_16x16x32_bf16 v[104:107], a[136:139], v[112:115], v[104:107]\n\t" \
  "ds_read_b64_tr_b16 a[170:171], v194 offset:41984\n\t" \
  "v_and_b32 v231, s98, v89\n\t" \
  "v_mfma_f32_16x16x32_bf16 v[108:111], a[140:143], v[112:115], v[108:111]\n\t" \
  "ds_read_b64_tr_b16 a[172:173], v195 offset:40960\n\t" \
  "v_cvt_pk_bf16_f32 v116, v236, v237\n\t" \
  "v_mfma_f32_16x16x32_bf16 v[76:79], a[140:143], v[80:83], v[76:79]\n\t" \
  "ds_read_b64_tr_b16 a[174:175], v195 offset:41984\n\t" \
  "v_lshlrev_b32 v232, 16, v90\n\t" \
  "v_mfma_f32_16x16x32_bf16 v[44:47], a[140:143], v[48:51], v[44:47]\n\t" \
  "ds_read_b64_tr_b16 a[176:177], v196 offset:40960\n\t" \
  "v_cvt_pk_bf16_f32 v117, v238, v239\n\t" \
  "v_mfma_f32_16x16x32_bf16 v[12:15], a[140:143], v[16:19], v[12:15]\n\t" \
  "buffer_load_dwordx4 v202, s[20:23], s96 offen offset:2048 lds\n\t" \
  "v_mfma_f32_16x16x32_bf16 a[64:67], a[144:147], v[16:19], a[64:67]\n\t" \
  "ds_read_b64_tr_b16 a[178:179], v196 offset:41984\n\t" \
  "v_and_b32 v233, s98, v90\n\t" \
  "v_mfma_f32_16x16x32_bf16 a[68:71], a[144:147], v[48:51], a[68:71]\n\t" \
  "ds_read_b64_tr_b16 a[180:181], v197 offset:40960\n\t" \
  "v_cvt_pk_bf16_f32 v118, v240, v241\n\t" \
  "v_mfma_f32_16x16x32_bf16 a[72:75], a[144:147], v[80:83], a[72:75]\n\t" \
  "ds_read_b64_tr_b16 a[182:183], v197 offset:41984\n\t" \
  "v_lshlrev_b32 v234, 16, v91\n\t" \
  "v_mfma_f32_16x16x32_bf16 a[76:79], a[144:147], v[112:115], a[76:79]\n\t" \
  "ds_read_b128 v[132:135], v208 offset:1552\n\t" \
  "v_cvt_pk_bf16_f32 v119, v242, v243\n\t" \
  "v_mfma_f32_16x16x32_bf16 a[92:95], a[148:151], v[112:115], a[92:95]\n\t" \
  "buffer_load_dwordx4 v203, s[20:23], s96 offen offset:3072 lds\n\t" \
  "v_mfma_f32_16x16x32_bf16 a[88:91], a[148:151], v[80:83], a[88:91]\n\t" \
  "ds_read_b64_tr_b16 a[184:185], v198 offset:40960\n\t" \
  "v_and_b32 v235, s98, v91\n\t" \
  "v_mfma_f32_16x16x32_bf16 a[84:87], a[148:151], v[48:51], a[84:87]\n\t" \
  "ds_read_b64_tr_b16 a[186:187], v198 offset:41984\n\t" \
  "v_lshlrev_b32 v236, 16, v120\n\t" \
  "v_mfma_f32_16x16x32_bf16 a[80:83], a[148:151], v[16:19], a[80:83]\n\t" \
  "ds_read_b64_tr_b16 a[188:189], v199 offset:40960\n\t" \
  "v_and_b32 v237, s98, v120\n\t" \
  "v_mfma_f32_16x16x32_bf16 a[96:99], a[152:155], v[16:19], a[96:99]\n\t" \
  "ds_read_b128 v[136:139], v208 offset:1792\n\t" \
  "v_lshlrev_b32 v238, 16, v121\n\t" \
  "v_mfma_f32_16x16x32_bf16 a[100:103], a[152:155], v[48:51], a[100:103]\n\t" \
  "global_load_dword v246, v216, s[20:21]\n\t" \
  "v_mfma_f32_16x16x32_bf16 a[104:107], a[152:155], v[80:83], a[104:107]\n\t" \
  "ds_read_b64_tr_b16 a[190:191], v199 offset:41984\n\t" \
  "v_and_b32 v239, s98, v121\n\t" \
  "v_mfma_f32_16x16x32_bf16 a[108:111], a[152:155], v[112:115], a[108:111]\n\t" \
  "ds_read_b128 v[140:143], v208 offset:1808\n\t" \
  "v_lshlrev_b32 v240, 16, v122\n\t" \
  "v_mfma_f32_16x16x32_bf16 a[124:127], a[156:159], v[112:115], a[124:127]\n\t" \
  "ds_read_b128 v[144:147], v208 offset:1664\n\t" \
  "v_and_b32 v241, s98, v122\n\t" \
  "v_lshlrev_b32 v242, 16, v123\n\t" \
  "v_and_b32 v243, s98, v123\n\t" \
  "s_waitcnt lgkmcnt(1)\n\t" \
  "v_pk_fma_f32 v[176:177], v[176:177], v[160:161], v[162:163] op_sel_hi:[1,0,0]\n\t" \
  "v_pk_fma_f32 v[178:179], v[178:179], v[160:161], v[162:163] op_sel_hi:[1,0,0]\n\t" \
  "v_pk_fma_f32 v[180:181], v[180:181], v[160:161], v[162:163] op_sel_hi:[1,0,0]\n\t" \
  "v_pk_fma_f32 v[182:183], v[182:183], v[160:161], v[162:163] op_sel_hi:[1,0,0]\n\t" \
  "v_pk_fma_f32 v[176:177], v[176:177], v[128:129], v[136:137]\n\t" \
  "v_pk_fma_f32 v[178:179], v[178:179], v[130:131], v[138:139]\n\t" \
  "v_pk_fma_f32 v[180:181], v[180:181], v[132:133], v[140:141]\n\t" \
  "v_pk_fma_f32 v[182:183], v[182:183], v[134:135], v[142:143]\n\t" \
  "v_pk_fma_f32 v[220:221], v[220:221], v[164:165], v[166:167] op_sel_hi:[1,0,0]\n\t" \
  "v_pk_fma_f32 v[222:223], v[222:223], v[164:165], v[166:167] op_sel_hi:[1,0,0]\n\t" \
  "v_pk_fma_f32 v[224:225], v[224:225], v[164:165], v[166:167] op_sel_hi:[1,0,0]\n\t" \
  "v_pk_fma_f32 v[226:227], v[226:227], v[164:165], v[166:167] op_sel_hi:[1,0,0]\n\t" \
  "v_pk_fma_f32 v[220:221], v[220:221], v[128:129], v[136:137]\n\t" \
  "v_pk_fma_f32 v[222:223], v[222:223], v[130:131], v[138:139]\n\t" \
  "v_pk_fma_f32 v[224:225], v[224:225], v[132:133], v[140:141]\n\t" \
  "v_pk_fma_f32 v[226:227], v[226:227], v[134:135], v[142:143]\n\t" \
  "v_cvt_pk_bf16_f32 v24, v176, v177\n\t" \
  "v_mfma_f32_16x16x32_bf16 a[120:123], a[156:159], v[80:83], a[120:123]\n\t" \
  "v_cvt_pk_bf16_f32 v25, v178, v179\n\t" \
  "v_cvt_pk_bf16_f32 v26, v180, v181\n\t" \
  "v_cvt_pk_bf16_f32 v27, v182, v183\n\t" \
  "v_cvt_pk_bf16_f32 v56, v220, v221\n\t" \
  "v_cvt_pk_bf16_f32 v57, v222, v223\n\t" \
  "v_pk_fma_f32 v[228:229], v[228:229], v[168:169], v[170:171] op_sel_hi:[1,0,0]\n\t" \
  "v_pk_fma_f32 v[230:231], v[230:231], v[168:169], v[170:171] op_sel_hi:[1,0,0]\n\t" \
  "v_pk_fma_f32 v[232:233], v[232:233], v[168:169], v[170:171] op_sel_hi:[1,0,0]\n\t" \
  "v_pk_fma_f32 v[234:235], v[234:235], v[168:169], v[170:171] op_sel_hi:[1,0,0]\n\t" \
  "v_pk_fma_f32 v[228:229], v[228:229], v[128:129], v[136:137]\n\t" \
  "v_pk_fma_f32 v[230:231], v[230:231], v[130:131], v[138:139]\n\t" \
  "v_pk_fma_f32 v[232:233], v[232:233], v[132:133], v[140:141]\n\t" \
  "v_pk_fma_f32 v[234:235], v[234:235], v[134:135], v[142:143]\n\t" \
  "v_pk_fma_f32 v[236:237], v[236:237], v[172:173], v[174:175] op_sel_hi:[1,0,0]\n\t" \
  "v_pk_fma_f32 v[238:239], v[238:239], v[172:173], v[174:175] op_sel_hi:[1,0,0]\n\t" \
  "v_pk_fma_f32 v[240:241], v[240:241], v[172:173], v[174:175] op_sel_hi:[1,0,0]\n\t" \
  "v_pk_fma_f32 v[242:243], v[242:243], v[172:173], v[174:175] op_sel_hi:[1,0,0]\n\t" \
  "v_pk_fma_f32 v[236:237], v[236:237], v[128:129], v[136:137]\n\t" \
  "v_pk_fma_f32 v[238:239], v[238:239], v[130:131], v[138:139]\n\t" \
  "v_pk_fma_f32 v[240:241], v[240:241], v[132:133], v[140:141]\n\t" \
  "v_pk_fma_f32 v[242:243], v[242:243], v[134:135], v[142:143]\n\t" \
  "v_cvt_pk_bf16_f32 v88, v228, v229\n\t" \
  "v_mfma_f32_16x16x32_bf16 a[116:119], a[156:159], v[48:51], a[116:119]\n\t" \
  "s_waitcnt vmcnt(23)\n\t" \
  "v_cvt_pk_bf16_f32 v58, v224, v225\n\t" \
  "v_cvt_pk_bf16_f32 v59, v226, v227\n\t" \
  "v_mfma_f32_16x16x32_bf16 a[112:115], a[156:159], v[16:19], a[112:115]\n\t" \
  "s_barrier\n\t" \
  FSTP("v_writelane_b32 v255, s94, 44\n\t") \
  FSTP("v_writelane_b32 v255, s92, 45\n\t") \
  FSTP("s_memrealtime s[94:95]\n\t") \
  FSTP("s_memtime s[92:93]\n\t") \
  "v_mfma_f32_16x16x32_bf16 v[0:3], a[160:163], v[20:23], v[0:3]\n\t" \
  "s_waitcnt vmcnt(5)\n\t" \
  "v_cmp_ne_u32 vcc, v211, v252\n\t" \
  "v_cvt_pk_bf16_f32 v89, v230, v231\n\t" \
  "v_mfma_f32_16x16x32_bf16 v[32:35], a[160:163], v[52:55], v[32:35]\n\t" \
  "ds_read_b64_tr_b16 a[128:129], v184 offset:0\n\t" \
  "v_cvt_pk_bf16_f32 v90, v232, v233\n\t" \
  "v_mfma_f32_16x16x32_bf16 v[64:67], a[160:163], v[84:87], v[64:67]\n\t" \
  "s_cbranch_vccnz .Lfq_s\n\t" \
  ".Lfq_k:\n\t" \
  "v_mfma_f32_16x16x32_bf16 v[96:99], a[160:163], v[116:119], v[96:99]\n\t" \
  "s_add_u32 s97, s78, 2097152\n\t" \
  "s_bfe_u32 s97, s97, 0x140003\n\t" \
  "s_add_u32 s97, s97, s82\n\t" \
  "v_mfma_f32_16x16x32_bf16 v[100:103], a[164:167], v[116:119], v[100:103]\n\t" \
  "buffer_load_dwordx4 a[0:3], v248, s[16:19], s97 offen sc1\n\t" \
  "v_mfma_f32_16x16x32_bf16 v[68:71], a[164:167], v[84:87], v[68:71]\n\t" \
  "buffer_load_dwordx4 a[4:7], v249, s[16:19], s97 offen sc1\n\t" \
  "v_mfma_f32_16x16x32_bf16 v[36:39], a[164:167], v[52:55], v[36:39]\n\t" \
  "ds_read_b64_tr_b16 a[130:131], v184 offset:1024\n\t" \
  "v_cvt_pk_bf16_f32 v91, v234, v235\n\t" \
  "v_mfma_f32_16x16x32_bf16 v[4:7], a[164:167], v[20:23], v[4:7]\n\t" \
  "ds_read_b64_tr_b16 a[132:133], v185 offset:0\n\t" \
  "v_cvt_pk_bf16_f32 v120, v236, v237\n\t" \
  "v_mfma_f32_16x16x32_bf16 v[8:11], a[168:171], v[20:23], v[8:11]\n\t" \
  "buffer_load_dwordx4 a[8:11], v250, s[16:19], s97 offen sc1\n\t" \
  "v_mfma_f32_16x16x32_bf16 v[40:43], a[168:171], v[52:55], v[40:43]\n\t" \
  "ds_read_b64_tr_b16 a[134:135], v185 offset:1024\n\t" \
  "v_cvt_pk_bf16_f32 v121, v238, v239\n\t" \
  "v_mfma_f32_16x16x32_bf16 v[72:75], a[168:171], v[84:87], v[72:75]\n\t" \
  "v_lshlrev_b32 v176, 16, v28\n\t" \
  "v_and_b32 v177, s98, v28\n\t" \
  "v_mfma_f32_16x16x32_bf16 v[104:107], a[168:171], v[116:119], v[104:107]\n\t" \
  "ds_read_b64_tr_b16 a[136:137], v186 offset:0\n\t" \
  "v_lshlrev_b32 v178, 16, v29\n\t" \
  "v_mfma_f32_16x16x32_bf16 v[108:111], a[172:175], v[116:119], v[108:111]\n\t" \
  "buffer_load_dwordx4 a[12:15], v251, s[16:19], s97 offen sc1\n\t" \
  "v_mfma_f32_16x16x32_bf16 v[76:79], a[172:175], v[84:87], v[76:79]\n\t" \
  "s_add_u32 s97, s97, s83\n\t" \
  "ds_read_b128 v[148:151], v208 offset:1680\n\t" \
  "ds_read_b64_tr_b16 a[138:139], v186 offset:1024\n\t" \
  "v_and_b32 v179, s98, v29\n\t" \
  "v_mfma_f32_16x16x32_bf16 v[44:47], a[172:175], v[52:55], v[44:47]\n\t" \
  "ds_read_b128 v[152:155], v208 offset:1920\n\t" \
  "ds_read_b64_tr_b16 a[140:141], v187 offset:0\n\t" \
  "v_lshlrev_b32 v180, 16, v30\n\t" \
  "v_mfma_f32_16x16x32_bf16 v[12:15], a[172:175], v[20:23], v[12:15]\n\t" \
  "buffer_load_dwordx4 a[16:19], v248, s[16:19], s97 offen sc1\n\t" \
  "v_mfma_f32_16x16x32_bf16 a[64:67], a[176:179], v[20:23], a[64:67]\n\t" \
  "ds_read_b64_tr_b16 a[142:143], v187 offset:1024\n\t" \
  "v_and_b32 v181, s98, v30\n\t" \
  "v_mfma_f32_16x16x32_bf16 a[68:71], a[176:179], v[52:55], a[68:71]\n\t" \
  "ds_read_b64_tr_b16 a[144:145], v188 offset:0\n\t" \
  "v_lshlrev_b32 v182, 16, v31\n\t" \
  "v_mfma_f32_16x16x32_bf16 a[72:75], a[176:179], v[84:87], a[72:75]\n\t" \
  "ds_read_b64_tr_b16 a[146:147], v188 offset:1024\n\t" \
  "v_and_b32 v183, s98, v31\n\t" \
  "v_mfma_f32_16x16x32_bf16 a[76:79], a[176:179], v[116:119], a[76:79]\n\t" \
  "buffer_load_dwordx4 a[20:23], v249, s[16:19], s97 offen sc1\n\t" \
  "v_mfma_f32_16x16x32_bf16 a[92:95], a[180:183], v[116:119], a[92:95]\n\t" \
  "ds_read_b64_tr_b16 a[148:149], v189 offset:0\n\t" \
  "v_lshlrev_b32 v220, 16, v60\n\t" \
  "v_mfma_f32_16x16x32_bf16 a[88:91], a[180:183], v[84:87], a[88:91]\n\t" \
  "ds_read_b64_tr_b16 a[150:151], v189 offset:1024\n\t" \
  "v_and_b32 v221, s98, v60\n\t" \
  "v_mfma_f32_16x16x32_bf16 a[84:87], a[180:183], v[52:55], a[84:87]\n\t" \
  "ds_read_b64_tr_b16 a[152:153], v190 offset:0\n\t" \
  "v_lshlrev_b32 v222, 16, v61\n\t" \
  "v_mfma_f32_16x16x32_bf16 a[80:83], a[180:183], v[20:23], a[80:83]\n\t" \
  "buffer_load_dwordx4 a[24:27], v250, s[16:19], s97 offen sc1\n\t" \
  "v_mfma_f32_16x16x32_bf16 a[96:99], a[184:187], v[20:23], a[96:99]\n\t" \
  "ds_read_b64_tr_b16 a[154:155], v190 offset:1024\n\t" \
  "v_cvt_pk_bf16_f32 v122, v240, v241\n\t" \
  "v_mfma_f32_16x16x32_bf16 a[100:103], a[184:187], v[52:55], a[100:103]\n\t" \
  "ds_read_b64_tr_b16 a[156:157], v191 offset:0\n\t" \
  "v_and_b32 v223, s98, v61\n\t" \
  "v_mfma_f32_16x16x32_bf16 a[104:107], a[184:187], v[84:87], a[104:107]\n\t" \
  "ds_read_b64_tr_b16 a[158:159], v191 offset:1024\n\t" \
  "v_cvt_pk_bf16_f32 v123, v242, v243\n\t" \
  "v_mfma_f32_16x16x32_bf16 a[108:111], a[184:187], v[116:119], a[108:111]\n\t" \
  "buffer_load_dwordx4 a[28:31], v251, s[16:19], s97 offen sc1\n\t" \
  "v_mfma_f32_16x16x32_bf16 a[124:127], a[188:191], v[116:119], a[124:127]\n\t" \
  "ds_read_b128 v[156:159], v208 offset:1936\n\t" \
  "v_lshlrev_b32 v224, 16, v62\n\t" \
  "v_mfma_f32_16x16x32_bf16 a[120:123], a[188:191], v[84:87], a[120:123]\n\t" \
  "s_add_u32 s97, s78, 2621440\n\t" \
  "v_and_b32 v225, s98, v62\n\t" \
  "v_lshlrev_b32 v226, 16, v63\n\t" \
  "v_mfma_f32_16x16x32_bf16 a[116:119], a[188:191], v[52:55], a[116:119]\n\t" \
  "s_bfe_u32 s97, s97, 0x140003\n\t" \
  "s_add_u32 s97, s97, s82\n\t" \
  "v_and_b32 v227, s98, v63\n\t" \
  "v_mfma_f32_16x16x32_bf16 a[112:115], a[188:191], v[20:23], a[112:115]\n\t" \
  "s_waitcnt lgkmcnt(1)\n\t" \
  "v_lshlrev_b32 v228, 16, v92\n\t" \
  "v_and_b32 v229, s98, v92\n\t" \
  "v_lshlrev_b32 v230, 16, v93\n\t" \
  "v_and_b32 v231, s98, v93\n\t" \
  "s_waitcnt lgkmcnt(0)\n\t" \
  "v_pk_fma_f32 v[176:177], v[176:177], v[160:161], v[162:163] op_sel_hi:[1,0,0]\n\t" \
  "v_pk_fma_f32 v[178:179], v[178:179], v[160:161], v[162:163] op_sel_hi:[1,0,0]\n\t" \
  "v_pk_fma_f32 v[180:181], v[180:181], v[160:161], v[162:163] op_sel_hi:[1,0,0]\n\t" \
  "v_pk_fma_f32 v[182:183], v[182:183], v[160:161], v[162:163] op_sel_hi:[1,0,0]\n\t" \
  "v_pk_fma_f32 v[176:177], v[176:177], v[144:145], v[152:153]\n\t" \
  "v_pk_fma_f32 v[178:179], v[178:179], v[146:147], v[154:155]\n\t" \
  "v_pk_fma_f32 v[180:181], v[180:181], v[148:149], v[156:157]\n\t" \
  "v_pk_fma_f32 v[182:183], v[182:183], v[150:151], v[158:159]\n\t" \
  "v_pk_fma_f32 v[220:221], v[220:221], v[164:165], v[166:167] op_sel_hi:[1,0,0]\n\t" \
  "v_pk_fma_f32 v[222:223], v[222:223], v[164:165], v[166:167] op_sel_hi:[1,0,0]\n\t" \
  "v_pk_fma_f32 v[224:225], v[224:225], v[164:165], v[166:167] op_sel_hi:[1,0,0]\n\t" \
  "v_pk_fma_f32 v[226:227], v[226:227], v[164:165], v[166:167] op_sel_hi:[1,0,0]\n\t" \
  "v_pk_fma_f32 v[220:221], v[220:221], v[144:145], v[152:153]\n\t" \
  "v_pk_fma_f32 v[222:223], v[222:223], v[146:147], v[154:155]\n\t" \
  "v_pk_fma_f32 v[224:225], v[224:225], v[148:149], v[156:157]\n\t" \
  "v_pk_fma_f32 v[226:227], v[226:227], v[150:151], v[158:159]\n\t" \
  "v_cvt_pk_bf16_f32 v28, v176, v177\n\t" \
  FSTP("v_writelane_b32 v255, s94, 46\n\t") \
  FSTP("v_writelane_b32 v255, s92, 47\n\t") \
  FSTP("s_memrealtime s[94:95]\n\t") \
  FSTP("s_memtime s[92:93]\n\t") \
  "v_mfma_f32_16x16x32_bf16 v[0:3], a[128:131], v[24:27], v[0:3]\n\t" \
  "buffer_load_dwordx4 a[32:35], v248, s[16:19], s97 offen sc1\n\t" \
  "v_mfma_f32_16x16x32_bf16 v[32:35], a[128:131], v[56:59], v[32:35]\n\t" \
  "ds_read_b64_tr_b16 a[160:161], v184 offset:8192\n\t" \
  "v_lshlrev_b32 v232, 16, v94\n\t" \
  "v_mfma_f32_16x16x32_bf16 v[64:67], a[128:131], v[88:91], v[64:67]\n\t" \
  "ds_read_b64_tr_b16 a[162:163], v184 offset:9216\n\t" \
  "v_and_b32 v233, s98, v94\n\t" \
  "v_mfma_f32_16x16x32_bf16 v[96:99], a[128:131], v[120:123], v[96:99]\n\t" \
  "v_lshlrev_b32 v234, 16, v95\n\t" \
  "v_and_b32 v235, s98, v95\n\t" \
  "v_mfma_f32_16x16x32_bf16 v[100:103], a[132:135], v[120:123], v[100:103]\n\t" \
  "buffer_load_dwordx4 a[36:39], v249, s[16:19], s97 offen sc1\n\t" \
  "v_mfma_f32_16x16x32_bf16 v[68:71], a[132:135], v[88:91], v[68:71]\n\t" \
  "ds_read_b64_tr_b16 a[164:165], v185 offset:8192\n\t" \
  "v_lshlrev_b32 v236, 16, v124\n\t" \
  "v_mfma_f32_16x16x32_bf16 v[36:39], a[132:135], v[56:59], v[36:39]\n\t" \
  "ds_read_b64_tr_b16 a[166:167], v185 offset:9216\n\t" \
  "v_and_b32 v237, s98, v124\n\t" \
  "v_mfma_f32_16x16x32_bf16 v[4:7], a[132:135], v[24:27], v[4:7]\n\t" \
  "ds_read_b64_tr_b16 a[168:169], v186 offset:8192\n\t" \
  "v_cvt_pk_bf16_f32 v29, v178, v179\n\t" \
  "v_mfma_f32_16x16x32_bf16 v[8:11], a[136:139], v[24:27], v[8:11]\n\t" \
  "buffer_load_dwordx4 a[40:43], v250, s[16:19], s97 offen sc1\n\t" \
  "v_mfma_f32_16x16x32_bf16 v[40:43], a[136:139], v[56:59], v[40:43]\n\t" \
  "ds_read_b64_tr_b16 a[170:171], v186 offset:9216\n\t" \
  "v_lshlrev_b32 v238, 16, v125\n\t" \
  "v_mfma_f32_16x16x32_bf16 v[72:75], a[136:139], v[88:91], v[72:75]\n\t" \
  "ds_read_b64_tr_b16 a[172:173], v187 offset:8192\n\t" \
  "v_and_b32 v239, s98, v125\n\t" \
  "v_mfma_f32_16x16x32_bf16 v[104:107], a[136:139], v[120:123], v[104:107]\n\t" \
  "v_cvt_pk_bf16_f32 v30, v180, v181\n\t" \
  "v_cvt_pk_bf16_f32 v31, v182, v183\n\t" \
  "v_mfma_f32_16x16x32_bf16 v[108:111], a[140:143], v[120:123], v[108:111]\n\t" \
  "buffer_load_dwordx4 a[44:47], v251, s[16:19], s97 offen sc1\n\t" \
  "v_mfma_f32_16x16x32_bf16 v[76:79], a[140:143], v[88:91], v[76:79]\n\t" \
  "s_add_u32 s97, s97, s83\n\t" \
  "ds_read_b64_tr_b16 a[174:175], v187 offset:9216\n\t" \
  "v_lshlrev_b32 v240, 16, v126\n\t" \
  "v_mfma_f32_16x16x32_bf16 v[44:47], a[140:143], v[56:59], v[44:47]\n\t" \
  "ds_read_b64_tr_b16 a[176:177], v188 offset:8192\n\t" \
  "v_and_b32 v241, s98, v126\n\t" \
  "v_mfma_f32_16x16x32_bf16 v[12:15], a[140:143], v[24:27], v[12:15]\n\t" \
  "buffer_load_dwordx4 a[48:51], v248, s[16:19], s97 offen sc1\n\t" \
  "v_mfma_f32_16x16x32_bf16 a[64:67], a[144:147], v[24:27], a[64:67]\n\t" \
  "ds_read_b64_tr_b16 a[178:179], v188 offset:9216\n\t" \
  "v_cvt_pk_bf16_f32 v60, v220, v221\n\t" \
  "v_mfma_f32_16x16x32_bf16 a[68:71], a[144:147], v[56:59], a[68:71]\n\t" \
  "ds_read_b64_tr_b16 a[180:181], v189 offset:8192\n\t" \
  "v_cvt_pk_bf16_f32 v61, v222, v223\n\t" \
  "v_mfma_f32_16x16x32_bf16 a[72:75], a[144:147], v[88:91], a[72:75]\n\t" \
  "ds_read_b64_tr_b16 a[182:183], v189 offset:9216\n\t" \
  "v_lshlrev_b32 v242, 16, v127\n\t" \
  "v_mfma_f32_16x16x32_bf16 a[76:79], a[144:147], v[120:123], a[76:79]\n\t" \
  "buffer_load_dwordx4 a[52:55], v249, s[16:19], s97 offen sc1\n\t" \
  "v_mfma_f32_16x16x32_bf16 a[92:95], a[148:151], v[120:123], a[92:95]\n\t" \
  "ds_read_b64_tr_b16 a[184:185], v190 offset:8192\n\t" \
  "v_and_b32 v243, s98, v127\n\t" \
  "v_mfma_f32_16x16x32_bf16 a[88:91], a[148:151], v[88:91], a[88:91]\n\t" \
  "buffer_load_dwordx4 a[56:59], v250, s[16:19], s97 offen sc1\n\t" \
  "ds_read_b64_tr_b16 a[186:187], v190 offset:9216\n\t" \
  "ds_read_b64_tr_b16 a[188:189], v191 offset:8192\n\t" \
  "v_cvt_pk_bf16_f32 v62, v224, v225\n\t" \
  "v_cvt_pk_bf16_f32 v63, v226, v227\n\t" \
  "v_pk_fma_f32 v[228:229], v[228:229], v[168:169], v[170:171] op_sel_hi:[1,0,0]\n\t" \
  "v_pk_fma_f32 v[230:231], v[230:231], v[168:169], v[170:171] op_sel_hi:[1,0,0]\n\t" \
  "v_pk_fma_f32 v[232:233], v[232:233], v[168:169], v[170:171] op_sel_hi:[1,0,0]\n\t" \
  "v_pk_fma_f32 v[234:235], v[234:235], v[168:169], v[170:171] op_sel_hi:[1,0,0]\n\t" \
  "v_pk_fma_f32 v[228:229], v[228:229], v[144:145], v[152:153]\n\t" \
  "v_pk_fma_f32 v[230:231], v[230:231], v[146:147], v[154:155]\n\t" \
  "v_pk_fma_f32 v[232:233], v[232:233], v[148:149], v[156:157]\n\t" \
  "v_pk_fma_f32 v[234:235], v[234:235], v[150:151], v[158:159]\n\t" \
  "v_pk_fma_f32 v[236:237], v[236:237], v[172:173], v[174:175] op_sel_hi:[1,0,0]\n\t" \
  "v_pk_fma_f32 v[238:239], v[238:239], v[172:173], v[174:175] op_sel_hi:[1,0,0]\n\t" \
  "v_pk_fma_f32 v[240:241], v[240:241], v[172:173], v[174:175] op_sel_hi:[1,0,0]\n\t" \
  "v_pk_fma_f32 v[242:243], v[242:243], v[172:173], v[174:175] op_sel_hi:[1,0,0]\n\t" \
  "v_pk_fma_f32 v[236:237], v[236:237], v[144:145], v[152:153]\n\t" \
  "v_pk_fma_f32 v[238:239], v[238:239], v[146:147], v[154:155]\n\t" \
  "v_pk_fma_f32 v[240:241], v[240:241], v[148:149], v[156:157]\n\t" \
  "v_pk_fma_f32 v[242:243], v[242:243], v[150:151], v[158:159]\n\t" \
  "v_cvt_pk_bf16_f32 v92, v228, v229\n\t" \
  "v_mfma_f32_16x16x32_bf16 a[84:87], a[148:151], v[56:59], a[84:87]\n\t" \
  "v_cvt_pk_bf16_f32 v93, v230, v231\n\t" \
  "v_cvt_pk_bf16_f32 v94, v232, v233\n\t" \
  "v_mfma_f32_16x16x32_bf16 a[80:83], a[148:151], v[24:27], a[80:83]\n\t" \
  "v_cvt_pk_bf16_f32 v95, v234, v235\n\t" \
  "v_cvt_pk_bf16_f32 v124, v236, v237\n\t" \
  "v_mfma_f32_16x16x32_bf16 a[96:99], a[152:155], v[24:27], a[96:99]\n\t" \
  "v_cvt_pk_bf16_f32 v125, v238, v239\n\t" \
  "v_cvt_pk_bf16_f32 v126, v240, v241\n\t" \
  "v_mfma_f32_16x16x32_bf16 a[100:103], a[152:155], v[56:59], a[100:103]\n\t" \
  "ds_read_b64_tr_b16 a[190:191], v191 offset:9216\n\t" \
  "v_cvt_pk_bf16_f32 v127, v242, v243\n\t" \
  "v_mfma_f32_16x16x32_bf16 a[104:107], a[152:155], v[88:91], a[104:107]\n\t" \
  "buffer_load_dwordx4 a[60:63], v251, s[16:19], s97 offen sc1\n\t" \
  "v_mfma_f32_16x16x32_bf16 a[108:111], a[152:155], v[120:123], a[108:111]\n\t" \
  "v_mfma_f32_16x16x32_bf16 a[124:127], a[156:159], v[120:123], a[124:127]\n\t" \
  "v_mfma_f32_16x16x32_bf16 a[120:123], a[156:159], v[88:91], a[120:123]\n\t" \
  "s_add_u32 s97, s78, 3145728\n\t" \
  "s_bfe_u32 s97, s97, 0x140003\n\t" \
  "s_add_u32 s97, s97, s82\n\t" \
  "v_mfma_f32_16x16x32_bf16 a[116:119], a[156:159], v[56:59], a[116:119]\n\t" \
  "s_waitcnt lgkmcnt(0)\n\t" \
  "v_mfma_f32_16x16x32_bf16 a[112:115], a[156:159], v[24:27], a[112:115]\n\t" \
  "s_barrier\n\t" \
  FSTP("v_writelane_b32 v255, s94, 48\n\t") \
  FSTP("v_writelane_b32 v255, s92, 49\n\t") \
  FSTP("s_memrealtime s[94:95]\n\t") \
  FSTP("s_memtime s[92:93]\n\t") \
  "v_mfma_f32_16x16x32_bf16 v[0:3], a[160:163], v[28:31], v[0:3]\n\t" \
  "buffer_load_dwordx4 a[192:195], v248, s[16:19], s97 offen sc1\n\t" \
  "v_mfma_f32_16x16x32_bf16 v[32:35], a[160:163], v[60:63], v[32:35]\n\t" \
  "ds_read_b64_tr_b16 a[128:129], v184 offset:16384\n\t" \
  "v_mfma_f32_16x16x32_bf16 v[64:67], a[160:163], v[92:95], v[64:67]\n\t" \
  "ds_read_b64_tr_b16 a[130:131], v184 offset:17408\n\t" \
  "v_mfma_f32_16x16x32_bf16 v[96:99], a[160:163], v[124:127], v[96:99]\n\t" \
  "ds_write_b32 v219, v218\n\t" \
  "v_mfma_f32_16x16x32_bf16 v[100:103], a[164:167], v[124:127], v[100:103]\n\t" \
  "buffer_load_dwordx4 a[196:199], v249, s[16:19], s97 offen sc1\n\t" \
  "v_mfma_f32_16x16x32_bf16 v[68:71], a[164:167], v[92:95], v[68:71]\n\t" \
  "ds_read_b64_tr_b16 a[132:133], v185 offset:16384\n\t" \
  "v_and_b32 v219, 0x7fffffe0, v219\n\t" \
  "v_mfma_f32_16x16x32_bf16 v[36:39], a[164:167], v[60:63], v[36:39]\n\t" \
  "ds_read_b64_tr_b16 a[134:135], v185 offset:17408\n\t" \
  "v_mfma_f32_16x16x32_bf16 v[4:7], a[164:167], v[28:31], v[4:7]\n\t" \
  "ds_read_b64_tr_b16 a[136:137], v186 offset:16384\n\t" \
  "v_mfma_f32_16x16x32_bf16 v[8:11], a[168:171], v[28:31], v[8:11]\n\t" \
  "buffer_load_dwordx4 a[200:203], v250, s[16:19], s97 offen sc1\n\t" \
  "v_mfma_f32_16x16x32_bf16 v[40:43], a[168:171], v[60:63], v[40:43]\n\t" \
  "ds_read_b64_tr_b16 a[138:139], v186 offset:17408\n\t" \
  "v_mfma_f32_16x16x32_bf16 v[72:75], a[168:171], v[92:95], v[72:75]\n\t" \
  "ds_read_b64_tr_b16 a[140:141], v187 offset:16384\n\t" \
  "v_mfma_f32_16x16x32_bf16 v[104:107], a[168:171], v[124:127], v[104:107]\n\t" \
  "ds_read_b128 v[168:171], v219 offset:0\n\t" \
  "v_mfma_f32_16x16x32_bf16 v[108:111], a[172:175], v[124:127], v[108:111]\n\t" \
  "buffer_load_dwordx4 a[204:207], v251, s[16:19], s97 offen sc1\n\t" \
  "v_mfma_f32_16x16x32_bf16 v[76:79], a[172:175], v[92:95], v[76:79]\n\t" \
  "ds_read_b64_tr_b16 a[142:143], v187 offset:17408\n\t" \
  "s_add_u32 s97, s97, s83\n\t" \
  "v_mfma_f32_16x16x32_bf16 v[44:47], a[172:175], v[60:63], v[44:47]\n\t" \
  "ds_read_b64_tr_b16 a[144:145], v188 offset:16384\n\t" \
  "v_mfma_f32_16x16x32_bf16 v[12:15], a[172:175], v[28:31], v[12:15]\n\t" \
  "buffer_load_dwordx4 a[208:211], v248, s[16:19], s97 offen sc1\n\t" \
  "v_mfma_f32_16x16x32_bf16 a[64:67], a[176:179], v[28:31], a[64:67]\n\t" \
  "ds_read_b64_tr_b16 a[146:147], v188 offset:17408\n\t" \
  "v_mfma_f32_16x16x32_bf16 a[68:71], a[176:179], v[60:63], a[68:71]\n\t" \
  "ds_read_b64_tr_b16 a[148:149], v189 offset:16384\n\t" \
  "v_mfma_f32_16x16x32_bf16 a[72:75], a[176:179], v[92:95], a[72:75]\n\t" \
  "ds_read_b64_tr_b16 a[150:151], v189 offset:17408\n\t" \
  "v_mfma_f32_16x16x32_bf16 a[76:79], a[176:179], v[124:127], a[76:79]\n\t" \
  "buffer_load_dwordx4 a[212:215], v249, s[16:19], s97 offen sc1\n\t" \
  "v_mfma_f32_16x16x32_bf16 a[92:95], a[180:183], v[124:127], a[92:95]\n\t" \
  "ds_read_b64_tr_b16 a[152:153], v190 offset:16384\n\t" \
  "v_mfma_f32_16x16x32_bf16 a[88:91], a[180:183], v[92:95], a[88:91]\n\t" \
  "ds_read_b128 v[172:175], v219 offset:16\n\t" \
  "v_mfma_f32_16x16x32_bf16 a[84:87], a[180:183], v[60:63], a[84:87]\n\t" \
  "ds_read_b64_tr_b16 a[154:155], v190 offset:17408\n\t" \
  "v_mfma_f32_16x16x32_bf16 a[80:83], a[180:183], v[28:31], a[80:83]\n\t" \
  "buffer_load_dwordx4 a[216:219], v250, s[16:19], s97 offen sc1\n\t" \
  "v_mfma_f32_16x16x32_bf16 a[96:99], a[184:187], v[28:31], a[96:99]\n\t" \
  "ds_read_b64_tr_b16 a[156:157], v191 offset:16384\n\t" \
  "v_mfma_f32_16x16x32_bf16 a[100:103], a[184:187], v[60:63], a[100:103]\n\t" \
  "ds_read_b64_tr_b16 a[158:159], v191 offset:17408\n\t" \
  "v_mfma_f32_16x16x32_bf16 a[104:107], a[184:187], v[92:95], a[104:107]\n\t" \
  "buffer_load_dwordx4 a[220:223], v251, s[16:19], s97 offen sc1\n\t" \
  "v_mfma_f32_16x16x32_bf16 a[108:111], a[184:187], v[124:127], a[108:111]\n\t" \
  "v_mfma_f32_16x16x32_bf16 a[124:127], a[188:191], v[124:127], a[124:127]\n\t" \
  "v_mfma_f32_16x16x32_bf16 a[120:123], a[188:191], v[92:95], a[120:123]\n\t" \
  "s_add_u32 s97, s78, 3670016\n\t" \
  "s_bfe_u32 s97, s97, 0x140003\n\t" \
  "s_add_u32 s97, s97, s82\n\t" \
  "v_mfma_f32_16x16x32_bf16 a[116:119], a[188:191], v[60:63], a[116:119]\n\t" \
  "v_mfma_f32_16x16x32_bf16 a[112:115], a[188:191], v[28:31], a[112:115]\n\t" \
  "s_waitcnt vmcnt(20) lgkmcnt(0)\n\t" \
  FSTP("v_writelane_b32 v255, s94, 50\n\t") \
  FSTP("v_writelane_b32 v255, s92, 51\n\t") \
  FSTP("s_memrealtime s[94:95]\n\t") \
  FSTP("s_memtime s[92:93]\n\t") \
  FSTS("s_memrealtime s[94:95]\n\t") \
  FSTS("s_waitcnt lgkmcnt(0)\n\t") \
  FSTS("v_writelane_b32 v255, s94, 14\n\t") \
  "v_mfma_f32_16x16x32_bf16 a[64:67], a[144:147], a[0:3], a[64:67]\n\t" \
  "buffer_load_dwordx4 a[224:227], v248, s[16:19], s97 offen sc1\n\t" \
  "v_mfma_f32_16x16x32_bf16 a[68:71], a[144:147], a[4:7], a[68:71]\n\t" \
  "s_bfe_u32 s39, s2, 0x30003\n\t" \
  "ds_read_b64_tr_b16 a[160:161], v184 offset:24576\n\t" \
  "s_lshl_b32 s40, s39, 18\n\t" \
  "v_mfma_f32_16x16x32_bf16 a[72:75], a[144:147], a[8:11], a[72:75]\n\t" \
  "s_lshr_b32 s98, s2, 3\n\t" \
  "ds_read_b64_tr_b16 a[162:163], v184 offset:25600\n\t" \
  "v_accvgpr_read_b32 v16, a64\n\t" \
  "v_mfma_f32_16x16x32_bf16 a[76:79], a[144:147], a[12:15], a[76:79]\n\t" \
  "s_lshl_b32 s41, s3, 5\n\t" \
  "v_accvgpr_read_b32 v17, a65\n\t" \
  "v_accvgpr_read_b32 v18, a66\n\t" \
  "v_mfma_f32_16x16x32_bf16 a[92:95], a[148:151], a[12:15], a[92:95]\n\t" \
  ".Lhs2:\n\ts_cbranch_execz .Lhs2n\n\t" \
  "buffer_load_dwordx4 a[228:231], v249, s[16:19], s97 offen sc1\n\t" \
  "v_mfma_f32_16x16x32_bf16 a[88:91], a[148:151], a[8:11], a[88:91]\n\t" \
  "s_add_u32 s41, s41, s98\n\t" \
  "ds_read_b64_tr_b16 a[164:165], v185 offset:24576\n\t" \
  "v_accvgpr_read_b32 v19, a67\n\t" \
  "v_mfma_f32_16x16x32_bf16 a[84:87], a[148:151], a[4:7], a[84:87]\n\t" \
  "s_lshl_b32 s41, s41, 7\n\t" \
  "ds_read_b64_tr_b16 a[166:167], v185 offset:25600\n\t" \
  "v_accvgpr_read_b32 v48, a68\n\t" \
  "v_mfma_f32_16x16x32_bf16 a[80:83], a[148:151], a[0:3], a[80:83]\n\t" \
  "s_and_b32 s12, s2, 3\n\t" \
  "ds_read_b64_tr_b16 a[168:169], v186 offset:24576\n\t" \
  "v_accvgpr_read_b32 v49, a69\n\t" \
  "v_mfma_f32_16x16x32_bf16 a[96:99], a[152:155], a[0:3], a[96:99]\n\t" \
  "buffer_load_dwordx4 a[232:235], v250, s[16:19], s97 offen sc1\n\t" \
  "v_mfma_f32_16x16x32_bf16 a[100:103], a[152:155], a[4:7], a[100:103]\n\t" \
  "s_bfe_u32 s13, s2, 0x10002\n\t" \
  "ds_read_b64_tr_b16 a[170:171], v186 offset:25600\n\t" \
  "v_accvgpr_read_b32 v50, a70\n\t" \
  "v_mfma_f32_16x16x32_bf16 a[104:107], a[152:155], a[8:11], a[104:107]\n\t" \
  "s_lshl1_add_u32 s12, s12, s13\n\t" \
  "ds_read_b64_tr_b16 a[172:173], v187 offset:24576\n\t" \
  "v_accvgpr_read_b32 v51, a71\n\t" \
  "v_mfma_f32_16x16x32_bf16 a[108:111], a[152:155], a[12:15], a[108:111]\n\t" \
  "s_bfe_u32 s59, s2, 0x20003\n\t" \
  "v_accvgpr_read_b32 v80, a72\n\t" \
  "v_accvgpr_read_b32 v81, a73\n\t" \
  "v_mfma_f32_16x16x32_bf16 a[124:127], a[156:159], a[12:15], a[124:127]\n\t" \
  "buffer_load_dwordx4 a[236:239], v251, s[16:19], s97 offen sc1\n\t" \
  "v_mfma_f32_16x16x32_bf16 a[120:123], a[156:159], a[8:11], a[120:123]\n\t" \
  "s_add_u32 s96, s78, 4718592\n\t" \
  "s_and_b32 s96, s96, 0x780000\n\t" \
  "s_add_u32 m0, s76, 98304\n\t" \
  "v_mfma_f32_16x16x32_bf16 a[116:119], a[156:159], a[4:7], a[116:119]\n\t" \
  "s_or_b32 s59, s59, 24\n\t" \
  "ds_read_b64_tr_b16 a[174:175], v187 offset:25600\n\t" \
  "v_accvgpr_read_b32 v82, a74\n\t" \
  "v_mfma_f32_16x16x32_bf16 a[112:115], a[156:159], a[0:3], a[112:115]\n\t" \
  "buffer_load_dwordx4 v200, s[20:23], s96 offen offset:0 lds\n\t" \
  "v_mfma_f32_16x16x32_bf16 v[0:3], a[128:131], a[0:3], v[0:3]\n\t" \
  "ds_read_b64_tr_b16 a[176:177], v188 offset:24576\n\t" \
  "v_accvgpr_read_b32 v83, a75\n\t" \
  "v_mfma_f32_16x16x32_bf16 v[32:35], a[128:131], a[4:7], v[32:35]\n\t" \
  "ds_read_b64_tr_b16 a[178:179], v188 offset:25600\n\t" \
  "v_accvgpr_read_b32 v112, a76\n\t" \
  "v_mfma_f32_16x16x32_bf16 v[64:67], a[128:131], a[8:11], v[64:67]\n\t" \
  "ds_read_b64_tr_b16 a[180:181], v189 offset:24576\n\t" \
  "v_accvgpr_read_b32 v113, a77\n\t" \
  "v_mfma_f32_16x16x32_bf16 v[96:99], a[128:131], a[12:15], v[96:99]\n\t" \
  "buffer_load_dwordx4 v201, s[20:23], s96 offen offset:1024 lds\n\t" \
  "v_mfma_f32_16x16x32_bf16 v[100:103], a[132:135], a[12:15], v[100:103]\n\t" \
  "ds_read_b64_tr_b16 a[182:183], v189 offset:25600\n\t" \
  "v_accvgpr_read_b32 v114, a78\n\t" \
  "v_mfma_f32_16x16x32_bf16 v[68:71], a[132:135], a[8:11], v[68:71]\n\t" \
  "ds_read_b64_tr_b16 a[184:185], v190 offset:24576\n\t" \
  "v_accvgpr_read_b32 v115, a79\n\t" \
  "v_mfma_f32_16x16x32_bf16 v[36:39], a[132:135], a[4:7], v[36:39]\n\t" \
  "ds_read_b64_tr_b16 a[186:187], v190 offset:25600\n\t" \
  "v_accvgpr_read_b32 v116, a92\n\t" \
  "v_mfma_f32_16x16x32_bf16 v[4:7], a[132:135], a[0:3], v[4:7]\n\t" \
  "buffer_load_dwordx4 v202, s[20:23], s96 offen offset:2048 lds\n\t" \
  "v_mfma_f32_16x16x32_bf16 v[8:11], a[136:139], a[0:3], v[8:11]\n\t" \
  "ds_read_b64_tr_b16 a[188:189], v191 offset:24576\n\t" \
  "v_accvgpr_read_b32 v117, a93\n\t" \
  "v_mfma_f32_16x16x32_bf16 v[40:43], a[136:139], a[4:7], v[40:43]\n\t" \
  "ds_read_b64_tr_b16 a[190:191], v191 offset:25600\n\t" \
  "v_accvgpr_read_b32 v118, a94\n\t" \
  "v_mfma_f32_16x16x32_bf16 v[72:75], a[136:139], a[8:11], v[72:75]\n\t" \
  "v_accvgpr_read_b32 v119, a95\n\t" \
  "v_accvgpr_read_b32 v84, a88\n\t" \
  "v_mfma_f32_16x16x32_bf16 v[104:107], a[136:139], a[12:15], v[104:107]\n\t" \
  "buffer_load_dwordx4 v203, s[20:23], s96 offen offset:3072 lds\n\t" \
  "v_mfma_f32_16x16x32_bf16 v[108:111], a[140:143], a[12:15], v[108:111]\n\t" \
  "v_accvgpr_read_b32 v85, a89\n\t" \
  "v_accvgpr_read_b32 v86, a90\n\t" \
  "v_mfma_f32_16x16x32_bf16 v[76:79], a[140:143], a[8:11], v[76:79]\n\t" \
  "s_add_u32 s97, s97, s83\n\t" \
  "v_accvgpr_read_b32 v87, a91\n\t" \
  "v_accvgpr_read_b32 v52, a84\n\t" \
  "v_mfma_f32_16x16x32_bf16 v[44:47], a[140:143], a[4:7], v[44:47]\n\t" \
  "s_waitcnt vmcnt(24) lgkmcnt(0)\n\t" \
  "v_accvgpr_read_b32 v53, a85\n\t" \
  "v_accvgpr_read_b32 v54, a86\n\t" \
  "v_mfma_f32_16x16x32_bf16 v[12:15], a[140:143], a[0:3], v[12:15]\n\t" \
  "s_barrier\n\t" \
  "v_mfma_f32_16x16x32_bf16 a[96:99], a[184:187], a[16:19], a[96:99]\n\t" \
  "buffer_load_dwordx4 a[240:243], v248, s[16:19], s97 offen sc1\n\t" \
  "v_mfma_f32_16x16x32_bf16 a[100:103], a[184:187], a[20:23], a[100:103]\n\t" \
  "ds_read_b64_tr_b16 a[128:129], v184 offset:32768\n\t" \
  "v_accvgpr_read_b32 v55, a87\n\t" \
  "v_mfma_f32_16x16x32_bf16 a[104:107], a[184:187], a[24:27], a[104:107]\n\t" \
  "ds_read_b64_tr_b16 a[130:131], v184 offset:33792\n\t" \
  "v_accvgpr_read_b32 v20, a80\n\t" \
  "v_mfma_f32_16x16x32_bf16 a[108:111], a[184:187], a[28:31], a[108:111]\n\t" \
  "v_accvgpr_read_b32 v21, a81\n\t" \
  "v_accvgpr_read_b32 v22, a82\n\t" \
  "v_mfma_f32_16x16x32_bf16 a[124:127], a[188:191], a[28:31], a[124:127]\n\t" \
  "buffer_load_dwordx4 a[244:247], v249, s[16:19], s97 offen sc1\n\t" \
  "v_mfma_f32_16x16x32_bf16 a[120:123], a[188:191], a[24:27], a[120:123]\n\t" \
  "ds_read_b64_tr_b16 a[132:133], v185 offset:32768\n\t" \
  "v_accvgpr_read_b32 v23, a83\n\t" \
  "v_mfma_f32_16x16x32_bf16 a[116:119], a[188:191], a[20:23], a[116:119]\n\t" \
  "ds_read_b64_tr_b16 a[134:135], v185 offset:33792\n\t" \
  "v_accvgpr_read_b32 v24, a96\n\t" \
  "v_mfma_f32_16x16x32_bf16 a[112:115], a[188:191], a[16:19], a[112:115]\n\t" \
  "ds_read_b64_tr_b16 a[136:137], v186 offset:32768\n\t" \
  "v_accvgpr_read_b32 v25, a97\n\t" \
  "v_mfma_f32_16x16x32_bf16 v[0:3], a[160:163], a[16:19], v[0:3]\n\t" \
  "buffer_load_dwordx4 a[248:251], v250, s[16:19], s97 offen sc1\n\t" \
  "v_mfma_f32_16x16x32_bf16 v[32:35], a[160:163], a[20:23], v[32:35]\n\t" \
  "ds_read_b64_tr_b16 a[138:139], v186 offset:33792\n\t" \
  "v_accvgpr_read_b32 v26, a98\n\t" \
  "v_mfma_f32_16x16x32_bf16 v[64:67], a[160:163], a[24:27], v[64:67]\n\t" \
  "ds_read_b64_tr_b16 a[140:141], v187 offset:32768\n\t" \
  "v_accvgpr_read_b32 v27, a99\n\t" \
  "v_mfma_f32_16x16x32_bf16 v[96:99], a[160:163], a[28:31], v[96:99]\n\t" \
  "v_accvgpr_read_b32 v56, a100\n\t" \
  "v_accvgpr_read_b32 v57, a101\n\t" \
  "v_mfma_f32_16x16x32_bf16 v[100:103], a[164:167], a[28:31], v[100:103]\n\t" \
  "buffer_load_dwordx4 a[252:255], v251, s[16:19], s97 offen sc1\n\t" \
  "v_mfma_f32_16x16x32_bf16 v[68:71], a[164:167], a[24:27], v[68:71]\n\t" \
  "s_add_u32 s97, s78, 4194304\n\t" \
  "s_bfe_u32 s97, s97, 0x140003\n\t" \
  "s_add_u32 s97, s97, s82\n\t" \
  "v_mfma_f32_16x16x32_bf16 v[36:39], a[164:167], a[20:23], v[36:39]\n\t" \
  "ds_read_b64_tr_b16 a[142:143], v187 offset:33792\n\t" \
  "v_accvgpr_read_b32 v58, a102\n\t" \
  "v_mfma_f32_16x16x32_bf16 v[4:7], a[164:167], a[16:19], v[4:7]\n\t" \
  "buffer_load_dwordx4 a[0:3], v248, s[16:19], s97 offen sc1\n\t" \
  "v_mfma_f32_16x16x32_bf16 v[8:11], a[168:171], a[16:19], v[8:11]\n\t" \
  "ds_read_b64_tr_b16 a[144:145], v188 offset:32768\n\t" \
  "v_accvgpr_read_b32 v59, a103\n\t" \
  "v_mfma_f32_16x16x32_bf16 v[40:43], a[168:171], a[20:23], v[40:43]\n\t" \
  "ds_read_b64_tr_b16 a[146:147], v188 offset:33792\n\t" \
  "v_accvgpr_read_b32 v88, a104\n\t" \
  "v_mfma_f32_16x16x32_bf16 v[72:75], a[168:171], a[24:27], v[72:75]\n\t" \
  "ds_read_b64_tr_b16 a[148:149], v189 offset:32768\n\t" \
  "v_accvgpr_read_b32 v89, a105\n\t" \
  "v_mfma_f32_16x16x32_bf16 v[104:107], a[168:171], a[28:31], v[104:107]\n\t" \
  "buffer_load_dwordx4 a[4:7], v249, s[16:19], s97 offen sc1\n\t" \
  "v_mfma_f32_16x16x32_bf16 v[108:111], a[172:175], a[28:31], v[108:111]\n\t" \
  "ds_read_b64_tr_b16 a[150:151], v189 offset:33792\n\t" \
  "v_accvgpr_read_b32 v90, a106\n\t" \
  "v_mfma_f32_16x16x32_bf16 v[76:79], a[172:175], a[24:27], v[76:79]\n\t" \
  "ds_read_b64_tr_b16 a[152:153], v190 offset:32768\n\t" \
  "v_accvgpr_read_b32 v91, a107\n\t" \
  "v_mfma_f32_16x16x32_bf16 v[44:47], a[172:175], a[20:23], v[44:47]\n\t" \
  "ds_read_b64_tr_b16 a[154:155], v190 offset:33792\n\t" \
  "v_accvgpr_read_b32 v120, a108\n\t" \
  "v_mfma_f32_16x16x32_bf16 v[12:15], a[172:175], a[16:19], v[12:15]\n\t" \
  "buffer_load_dwordx4 a[8:11], v250, s[16:19], s97 offen sc1\n\t" \
  "v_mfma_f32_16x16x32_bf16 v[16:19], a[176:179], a[16:19], v[16:19]\n\t" \
  "ds_read_b64_tr_b16 a[156:157], v191 offset:32768\n\t" \
  "v_accvgpr_read_b32 v121, a109\n\t" \
  "v_mfma_f32_16x16x32_bf16 v[48:51], a[176:179], a[20:23], v[48:51]\n\t" \
  "ds_read_b64_tr_b16 a[158:159], v191 offset:33792\n\t" \
  "v_accvgpr_read_b32 v122, a110\n\t" \
  "v_mfma_f32_16x16x32_bf16 v[80:83], a[176:179], a[24:27], v[80:83]\n\t" \
  "v_accvgpr_read_b32 v123, a111\n\t" \
  "v_accvgpr_read_b32 v124, a124\n\t" \
  "v_mfma_f32_16x16x32_bf16 v[112:115], a[176:179], a[28:31], v[112:115]\n\t" \
  "buffer_load_dwordx4 a[12:15], v251, s[16:19], s97 offen sc1\n\t" \
  "v_mfma_f32_16x16x32_bf16 v[116:119], a[180:183], a[28:31], v[116:119]\n\t" \
  "v_mbcnt_lo_u32_b32 v217, -1, 0\n\t" \
  "v_mbcnt_hi_u32_b32 v217, -1, v217\n\t" \
  "v_mfma_f32_16x16x32_bf16 v[84:87], a[180:183], a[24:27], v[84:87]\n\t" \
  "v_cmp_gt_u32 vcc, 16, v217\n\t" \
  "v_lshlrev_b32 v217, 7, v217\n\t" \
  "v_mfma_f32_16x16x32_bf16 v[52:55], a[180:183], a[20:23], v[52:55]\n\t" \
  "v_bfrev_b32 v215, 1\n\t" \
  "v_cndmask_b32 v217, v215, v217, vcc\n\t" \
  "v_mfma_f32_16x16x32_bf16 v[20:23], a[180:183], a[16:19], v[20:23]\n\t" \
  "s_waitcnt vmcnt(28) lgkmcnt(0)\n\t" \
  "v_bfe_u32 v215, v217, 10, 1\n\t" \
  "s_mov_b32 s99, 11264\n\t" \
  "v_mfma_f32_16x16x32_bf16 v[0:3], a[128:131], a[32:35], v[0:3]\n\t" \
  "v_mad_u32_u24 v215, v215, s99, v217\n\t" \
  "s_add_u32 s97, s97, s83\n\t" \
  "v_mfma_f32_16x16x32_bf16 v[32:35], a[128:131], a[36:39], v[32:35]\n\t" \
  "buffer_load_dwordx4 a[16:19], v248, s[16:19], s97 offen sc1\n\t" \
  "v_mfma_f32_16x16x32_bf16 v[64:67], a[128:131], a[40:43], v[64:67]\n\t" \
  "ds_read_b64_tr_b16 a[160:161], v184 offset:40960\n\t" \
  "v_accvgpr_read_b32 v125, a125\n\t" \
  "v_mfma_f32_16x16x32_bf16 v[96:99], a[128:131], a[44:47], v[96:99]\n\t" \
  "ds_read_b64_tr_b16 a[162:163], v184 offset:41984\n\t" \
  "v_accvgpr_read_b32 v126, a126\n\t" \
  "v_mfma_f32_16x16x32_bf16 v[100:103], a[132:135], a[44:47], v[100:103]\n\t" \
  "buffer_load_dwordx4 a[20:23], v249, s[16:19], s97 offen sc1\n\t" \
  "v_mfma_f32_16x16x32_bf16 v[68:71], a[132:135], a[40:43], v[68:71]\n\t" \
  "ds_read_b64_tr_b16 a[164:165], v185 offset:40960\n\t" \
  "v_accvgpr_read_b32 v127, a127\n\t" \
  "v_mfma_f32_16x16x32_bf16 v[36:39], a[132:135], a[36:39], v[36:39]\n\t" \
  "ds_read_b64_tr_b16 a[166:167], v185 offset:41984\n\t" \
  "v_accvgpr_read_b32 v92, a120\n\t" \
  "v_mfma_f32_16x16x32_bf16 v[4:7], a[132:135], a[32:35], v[4:7]\n\t" \
  "ds_read_b64_tr_b16 a[168:169], v186 offset:40960\n\t" \
  "v_accvgpr_read_b32 v93, a121\n\t" \
  "v_mfma_f32_16x16x32_bf16 v[8:11], a[136:139], a[32:35], v[8:11]\n\t" \
  "buffer_load_dwordx4 a[24:27], v250, s[16:19], s97 offen sc1\n\t" \
  "v_mfma_f32_16x16x32_bf16 v[40:43], a[136:139], a[36:39], v[40:43]\n\t" \
  "ds_read_b64_tr_b16 a[170:171], v186 offset:41984\n\t" \
  "v_accvgpr_read_b32 v94, a122\n\t" \
  "v_mfma_f32_16x16x32_bf16 v[72:75], a[136:139], a[40:43], v[72:75]\n\t" \
  "ds_read_b64_tr_b16 a[172:173], v187 offset:40960\n\t" \
  "v_accvgpr_read_b32 v95, a123\n\t" \
  "v_mfma_f32_16x16x32_bf16 v[104:107], a[136:139], a[44:47], v[104:107]\n\t" \
  "v_accvgpr_read_b32 v60, a116\n\t" \
  "v_accvgpr_read_b32 v61, a117\n\t" \
  "v_mfma_f32_16x16x32_bf16 v[108:111], a[140:143], a[44:47], v[108:111]\n\t" \
  "buffer_load_dwordx4 a[28:31], v251, s[16:19], s97 offen sc1\n\t" \
  "v_mfma_f32_16x16x32_bf16 v[76:79], a[140:143], a[40:43], v[76:79]\n\t" \
  "s_add_u32 s96, s78, 5242880\n\t" \
  "s_and_b32 s96, s96, 0x780000\n\t" \
  "s_add_u32 m0, s76, 0\n\t" \
  "v_mfma_f32_16x16x32_bf16 v[44:47], a[140:143], a[36:39], v[44:47]\n\t" \
  "ds_read_b64_tr_b16 a[174:175], v187 offset:41984\n\t" \
  "v_accvgpr_read_b32 v62, a118\n\t" \
  "v_mfma_f32_16x16x32_bf16 v[12:15], a[140:143], a[32:35], v[12:15]\n\t" \
  "buffer_load_dwordx4 v200, s[20:23], s96 offen offset:0 lds\n\t" \
  "v_mfma_f32_16x16x32_bf16 v[16:19], a[144:147], a[32:35], v[16:19]\n\t" \
  "ds_read_b64_tr_b16 a[176:177], v188 offset:40960\n\t" \
  "v_accvgpr_read_b32 v63, a119\n\t" \
  "v_mfma_f32_16x16x32_bf16 v[48:51], a[144:147], a[36:39], v[48:51]\n\t" \
  "ds_read_b64_tr_b16 a[178:179], v188 offset:41984\n\t" \
  "v_accvgpr_read_b32 v28, a112\n\t" \
  "v_mfma_f32_16x16x32_bf16 v[80:83], a[144:147], a[40:43], v[80:83]\n\t" \
  "ds_read_b64_tr_b16 a[180:181], v189 offset:40960\n\t" \
  "v_accvgpr_read_b32 v29, a113\n\t" \
  "v_mfma_f32_16x16x32_bf16 v[112:115], a[144:147], a[44:47], v[112:115]\n\t" \
  "buffer_load_dwordx4 v201, s[20:23], s96 offen offset:1024 lds\n\t" \
  "v_mfma_f32_16x16x32_bf16 v[116:119], a[148:151], a[44:47], v[116:119]\n\t" \
  "ds_read_b64_tr_b16 a[182:183], v189 offset:41984\n\t" \
  "v_accvgpr_read_b32 v30, a114\n\t" \
  "v_mfma_f32_16x16x32_bf16 v[84:87], a[148:151], a[40:43], v[84:87]\n\t" \
  "ds_read_b64_tr_b16 a[184:185], v190 offset:40960\n\t" \
  "v_accvgpr_read_b32 v31, a115\n\t" \
  "v_mfma_f32_16x16x32_bf16 v[52:55], a[148:151], a[36:39], v[52:55]\n\t" \
  "ds_read_b64_tr_b16 a[186:187], v190 offset:41984\n\t" \
  "v_mfma_f32_16x16x32_bf16 v[20:23], a[148:151], a[32:35], v[20:23]\n\t" \
  "buffer_load_dwordx4 v202, s[20:23], s96 offen offset:2048 lds\n\t" \
  "v_mfma_f32_16x16x32_bf16 v[24:27], a[152:155], a[32:35], v[24:27]\n\t" \
  "ds_read_b64_tr_b16 a[188:189], v191 offset:40960\n\t" \
  "v_mfma_f32_16x16x32_bf16 v[56:59], a[152:155], a[36:39], v[56:59]\n\t" \
  "ds_read_b64_tr_b16 a[190:191], v191 offset:41984\n\t" \
  "v_mfma_f32_16x16x32_bf16 v[88:91], a[152:155], a[40:43], v[88:91]\n\t" \
  "s_cmp_lt_u32 s98, 24\n\t" \
  "s_cselect_b32 s59, s98, s59\n\t" \
  "s_mul_i32 s53, s98, 57344\n\t" \
  "v_mfma_f32_16x16x32_bf16 v[120:123], a[152:155], a[44:47], v[120:123]\n\t" \
  "buffer_load_dwordx4 v203, s[20:23], s96 offen offset:3072 lds\n\t" \
  "v_mfma_f32_16x16x32_bf16 v[124:127], a[156:159], a[44:47], v[124:127]\n\t" \
  "s_lshl_b32 s14, s59, 5\n\t" \
  "s_sub_u32 s53, s53, s14\n\t" \
  "s_lshl_b32 s52, s98, 16\n\t" \
  "v_mfma_f32_16x16x32_bf16 v[92:95], a[156:159], a[40:43], v[92:95]\n\t" \
  "s_lshl_b32 s14, s3, 14\n\t" \
  "s_add_u32 s52, s52, s14\n\t" \
  "s_lshl_b32 s14, s13, 13\n\t" \
  "v_mfma_f32_16x16x32_bf16 v[60:63], a[156:159], a[36:39], v[60:63]\n\t" \
  "s_waitcnt vmcnt(32) lgkmcnt(0)\n\t" \
  "s_mov_b32 s99, 0\n\t" \
  "s_add_u32 s52, s52, s14\n\t" \
  "v_mfma_f32_16x16x32_bf16 v[28:31], a[156:159], a[32:35], v[28:31]\n\t" \
  "s_barrier\n\t" \
  "v_mfma_f32_16x16x32_bf16 v[0:3], a[160:163], a[48:51], v[0:3]\n\t" \
  "buffer_load_dwordx4 v[136:139], v209, s[8:11], s99 offen nt\n\t" \
  "v_mfma_f32_16x16x32_bf16 v[32:35], a[160:163], a[52:55], v[32:35]\n\t" \
  "s_add_u32 s52, s52, s78\n\t" \
  "ds_read_b64_tr_b16 a[128:129], v184 offset:49152\n\t" \
  "s_lshl_b32 s51, s3, 15\n\t" \
  "v_mfma_f32_16x16x32_bf16 v[64:67], a[160:163], a[56:59], v[64:67]\n\t" \
  "s_add_u32 s51, s51, s78\n\t" \
  "ds_read_b64_tr_b16 a[130:131], v184 offset:50176\n\t" \
  "s_lshl_b32 s14, s39, 8\n\t" \
  "v_mfma_f32_16x16x32_bf16 v[96:99], a[160:163], a[60:63], v[96:99]\n\t" \
  "s_add_u32 s51, s51, s14\n\t" \
  "ds_read_b64_tr_b16 a[132:133], v185 offset:49152\n\t" \
  "s_lshr_b32 s98, s98, 2\n\t" \
  "v_mfma_f32_16x16x32_bf16 v[100:103], a[164:167], a[60:63], v[100:103]\n\t" \
  "s_add_u32 s13, s13, 6\n\t" \
  "ds_read_b64_tr_b16 a[134:135], v185 offset:50176\n\t" \
  "s_mov_b32 s99, 12288\n\t" \
  "v_mfma_f32_16x16x32_bf16 v[68:71], a[164:167], a[56:59], v[68:71]\n\t" \
  "s_cmp_eq_u32 s98, s13\n\t" \
  "s_cselect_b32 s57, 0x80000000, 0\n\t" \
  "s_cselect_b32 s30, 0, 0x8000\n\t" \
  "v_mfma_f32_16x16x32_bf16 v[36:39], a[164:167], a[52:55], v[36:39]\n\t" \
  "s_mul_i32 s14, s12, 28\n\t" \
  "ds_read_b64_tr_b16 a[136:137], v186 offset:49152\n\t" \
  "s_add_u32 s59, s59, s14\n\t" \
  "v_mfma_f32_16x16x32_bf16 v[4:7], a[164:167], a[48:51], v[4:7]\n\t" \
  "s_lshl_b32 s14, s59, 15\n\t" \
  "s_add_u32 s28, s28, s14\n\t" \
  "s_addc_u32 s29, s29, 0\n\t" \
  "v_mfma_f32_16x16x32_bf16 v[8:11], a[168:171], a[48:51], v[8:11]\n\t" \
  "buffer_load_dwordx4 v[140:143], v209, s[8:11], s99 offen nt\n\t" \
  "v_mfma_f32_16x16x32_bf16 v[40:43], a[168:171], a[52:55], v[40:43]\n\t" \
  "s_mul_i32 s14, s3, 1835008\n\t" \
  "ds_read_b64_tr_b16 a[138:139], v186 offset:50176\n\t" \
  "s_lshl_b32 s59, s59, 5\n\t" \
  "v_mfma_f32_16x16x32_bf16 v[72:75], a[168:171], a[56:59], v[72:75]\n\t" \
  "s_mov_b32 s99, 24576\n\t" \
  "ds_read_b64_tr_b16 a[140:141], v187 offset:49152\n\t" \
  "s_add_u32 s14, s14, s59\n\t" \
  "v_mfma_f32_16x16x32_bf16 v[104:107], a[168:171], a[60:63], v[104:107]\n\t" \
  "s_add_u32 s68, s68, s14\n\t" \
  "s_addc_u32 s69, s69, 0\n\t" \
  "v_mfma_f32_16x16x32_bf16 v[108:111], a[172:175], a[60:63], v[108:111]\n\t" \
  "ds_read_b64_tr_b16 a[142:143], v187 offset:50176\n\t" \
  "v_mfma_f32_16x16x32_bf16 v[76:79], a[172:175], a[56:59], v[76:79]\n\t" \
  "ds_read_b64_tr_b16 a[144:145], v188 offset:49152\n\t" \
  "v_mfma_f32_16x16x32_bf16 v[44:47], a[172:175], a[52:55], v[44:47]\n\t" \
  "s_add_u32 s72, s72, s14\n\t" \
  "s_addc_u32 s73, s73, 0\n\t" \
  "s_sub_u32 s70, s70, s14\n\t" \
  "v_mfma_f32_16x16x32_bf16 v[12:15], a[172:175], a[48:51], v[12:15]\n\t" \
  "buffer_load_dwordx4 v[144:147], v209, s[8:11], s99 offen nt\n\t" \
  "v_mfma_f32_16x16x32_bf16 v[16:19], a[176:179], a[48:51], v[16:19]\n\t" \
  "s_sub_u32 s74, s74, s14\n\t" \
  "ds_read_b64_tr_b16 a[146:147], v188 offset:50176\n\t" \
  "s_lshr_b32 s14, s78, 2\n\t" \
  "v_mfma_f32_16x16x32_bf16 v[48:51], a[176:179], a[52:55], v[48:51]\n\t" \
  "ds_read_b64_tr_b16 a[148:149], v189 offset:49152\n\t" \
  "s_mov_b32 s99, 36864\n\t" \
  "v_mfma_f32_16x16x32_bf16 v[80:83], a[176:179], a[56:59], v[80:83]\n\t" \
  "ds_read_b64_tr_b16 a[150:151], v189 offset:50176\n\t" \
  "v_mfma_f32_16x16x32_bf16 v[112:115], a[176:179], a[60:63], v[112:115]\n\t" \
  "s_add_u32 s60, s60, s14\n\t" \
  "s_addc_u32 s61, s61, 0\n\t" \
  "v_mfma_f32_16x16x32_bf16 v[116:119], a[180:183], a[60:63], v[116:119]\n\t" \
  "ds_read_b64_tr_b16 a[152:153], v190 offset:49152\n\t" \
  "v_mfma_f32_16x16x32_bf16 v[84:87], a[180:183], a[56:59], v[84:87]\n\t" \
  "s_add_u32 s64, s64, s14\n\t" \
  "s_addc_u32 s65, s65, 0\n\t" \
  "s_lshl2_add_u32 s58, s2, s3\n\t" \
  "v_mfma_f32_16x16x32_bf16 v[52:55], a[180:183], a[52:55], v[52:55]\n\t" \
  "s_lshl_b32 s58, s58, 11\n\t" \
  "ds_read_b64_tr_b16 a[154:155], v190 offset:50176\n\t" \
  "s_bfe_u32 s13, s2, 0x10004\n\t" \
  "v_mfma_f32_16x16x32_bf16 v[20:23], a[180:183], a[48:51], v[20:23]\n\t" \
  "buffer_load_dwordx4 v[148:151], v209, s[8:11], s99 offen nt\n\t" \
  "v_mfma_f32_16x16x32_bf16 v[24:27], a[184:187], a[48:51], v[24:27]\n\t" \
  "s_lshl1_add_u32 s12, s12, s13\n\t" \
  "ds_read_b64_tr_b16 a[156:157], v191 offset:49152\n\t" \
  "s_lshl_b32 s13, s13, 13\n\t" \
  "v_mfma_f32_16x16x32_bf16 v[56:59], a[184:187], a[52:55], v[56:59]\n\t" \
  "s_lshl_b32 s14, s12, 7\n\t" \
  "ds_read_b64_tr_b16 a[158:159], v191 offset:50176\n\t" \
  "s_add_u32 s13, s13, s14\n\t" \
  "v_mfma_f32_16x16x32_bf16 v[88:91], a[184:187], a[56:59], v[88:91]\n\t" \
  "s_add_u32 s13, s13, 16384\n\t" \
  "s_lshl_b32 s12, s12, 21\n\t" \
  "s_add_u32 s12, s12, 8388608\n\t" \
  "v_mfma_f32_16x16x32_bf16 v[120:123], a[184:187], a[60:63], v[120:123]\n\t" \
  "s_bitcmp1_b32 s2, 3\n\t" \
  "s_cselect_b32 s91, s12, s13\n\t" \
  "s_lshr_b32 s12, s57, s3\n\t" \
  "v_mfma_f32_16x16x32_bf16 v[124:127], a[188:191], a[60:63], v[124:127]\n\t" \
  "s_cmp_lt_i32 s12, 0\n\t" \
  "s_cselect_b32 s91, s91, 0x80000000\n\t" \
  "s_lshl_b32 s90, s3, 7\n\t" \
  "v_mfma_f32_16x16x32_bf16 v[92:95], a[188:191], a[56:59], v[92:95]\n\t" \
  "s_add_u32 s90, s90, 0x220000\n\t" \
  "s_and_b32 s98, s2, 0xc0\n\t" \
  "s_lshl_b32 s98, s98, 9\n\t" \
  "v_mfma_f32_16x16x32_bf16 v[60:63], a[188:191], a[52:55], v[60:63]\n\t" \
  "s_add_u32 s90, s90, s98\n\t" \
  "s_and_b32 s98, s2, 0x3\n\t" \
  "s_lshl_b32 s98, s98, 13\n\t" \
  "v_mfma_f32_16x16x32_bf16 v[28:31], a[188:191], a[48:51], v[28:31]\n\t" \
  "s_waitcnt vmcnt(32) lgkmcnt(0)\n\t" \
  "s_add_u32 s90, s90, s98\n\t" \
  "s_and_b32 s98, s2, 0x4\n\t" \
  "v_mfma_f32_16x16x32_bf16 v[0:3], a[128:131], a[192:195], v[0:3]\n\t" \
  "s_lshl_b32 s98, s98, 10\n\t" \
  "ds_read_b64_tr_b16 a[160:161], v184 offset:57344\n\t" \
  "s_add_u32 s90, s90, s98\n\t" \
  "v_mfma_f32_16x16x32_bf16 v[32:35], a[128:131], a[196:199], v[32:35]\n\t" \
  "s_and_b32 s98, s2, 0x38\n\t" \
  "ds_read_b64_tr_b16 a[162:163], v184 offset:58368\n\t" \
  "s_lshl_b32 s98, s98, 6\n\t" \
  "v_mfma_f32_16x16x32_bf16 v[64:67], a[128:131], a[200:203], v[64:67]\n\t" \
  "s_add_u32 s90, s90, s98\n\t" \
  "ds_read_b64_tr_b16 a[164:165], v185 offset:57344\n\t" \
  "s_lshr_b32 s98, s2, 2\n\t" \
  "v_mfma_f32_16x16x32_bf16 v[96:99], a[128:131], a[204:207], v[96:99]\n\t" \
  "s_xor_b32 s98, s98, s3\n\t" \
  "ds_read_b64_tr_b16 a[166:167], v185 offset:58368\n\t" \
  "s_lshl_b32 s98, s98, 13\n\t" \
  "v_mfma_f32_16x16x32_bf16 v[100:103], a[132:135], a[204:207], v[100:103]\n\t" \
  "s_and_b32 s42, s98, 0x2000\n\t" \
  "ds_read_b64_tr_b16 a[168:169], v186 offset:57344\n\t" \
  "s_lshr_b32 s98, s2, 6\n\t" \
  "v_mfma_f32_16x16x32_bf16 v[68:71], a[132:135], a[200:203], v[68:71]\n\t" \
  "s_lshl_b32 s98, s98, 22\n\t" \
  "ds_read_b64_tr_b16 a[170:171], v186 offset:58368\n\t" \
  "s_add_u32 s42, s42, s98\n\t" \
  "v_mfma_f32_16x16x32_bf16 v[36:39], a[132:135], a[196:199], v[36:39]\n\t" \
  "s_lshl_b32 s47, s3, 15\n\t" \
  "ds_read_b64_tr_b16 a[172:173], v187 offset:57344\n\t" \
  "s_add_u32 s47, s47, 0x1800000\n\t" \
  "v_mfma_f32_16x16x32_bf16 v[4:7], a[132:135], a[192:195], v[4:7]\n\t" \
  "s_add_u32 s47, s47, s42\n\t" \
  "ds_read_b64_tr_b16 a[174:175], v187 offset:58368\n\t" \
  "s_and_b32 s98, s2, 0x38\n\t" \
  "v_mfma_f32_16x16x32_bf16 v[8:11], a[136:139], a[192:195], v[8:11]\n\t" \
  "s_lshl_b32 s98, s98, 16\n\t" \
  "ds_read_b64_tr_b16 a[176:177], v188 offset:57344\n\t" \
  "s_add_u32 s47, s47, s98\n\t" \
  "v_mfma_f32_16x16x32_bf16 v[40:43], a[136:139], a[196:199], v[40:43]\n\t" \
  "s_add_u32 s46, s42, 0x800000\n\t" \
  "ds_read_b64_tr_b16 a[178:179], v188 offset:58368\n\t" \
  "s_and_b32 s98, s2, 0x3\n\t" \
  "v_mfma_f32_16x16x32_bf16 v[72:75], a[136:139], a[200:203], v[72:75]\n\t" \
  "s_lshl_b32 s98, s98, 20\n\t" \
  "ds_read_b64_tr_b16 a[180:181], v189 offset:57344\n\t" \
  "s_add_u32 s46, s46, s98\n\t" \
  "v_mfma_f32_16x16x32_bf16 v[104:107], a[136:139], a[204:207], v[104:107]\n\t" \
  "s_and_b32 s98, s3, 2\n\t" \
  "ds_read_b64_tr_b16 a[182:183], v189 offset:58368\n\t" \
  "s_lshl_b32 s98, s98, 13\n\t" \
  "v_mfma_f32_16x16x32_bf16 v[108:111], a[140:143], a[204:207], v[108:111]\n\t" \
  "s_add_u32 s46, s46, s98\n\t" \
  "ds_read_b64_tr_b16 a[184:185], v190 offset:57344\n\t" \
  "s_and_b32 s98, s3, 1\n\t" \
  "v_mfma_f32_16x16x32_bf16 v[76:79], a[140:143], a[200:203], v[76:79]\n\t" \
  "s_lshl_b32 s98, s98, 12\n\t" \
  "ds_read_b64_tr_b16 a[186:187], v190 offset:58368\n\t" \
  "s_add_u32 s46, s46, s98\n\t" \
  "v_mfma_f32_16x16x32_bf16 v[44:47], a[140:143], a[196:199], v[44:47]\n\t" \
  "s_lshr_b32 s45, s87, 2\n\t" \
  "ds_read_b64_tr_b16 a[188:189], v191 offset:57344\n\t" \
  "s_add_u32 s45, s45, s46\n\t" \
  "v_mfma_f32_16x16x32_bf16 v[12:15], a[140:143], a[192:195], v[12:15]\n\t" \
  "s_and_b32 s98, s2, 0x38\n\t" \
  "ds_read_b64_tr_b16 a[190:191], v191 offset:58368\n\t" \
  "s_lshl_b32 s98, s98, 14\n\t" \
  "v_mfma_f32_16x16x32_bf16 v[16:19], a[144:147], a[192:195], v[16:19]\n\t" \
  "s_add_u32 s45, s45, s98\n\t" \
  "s_lshr_b32 s98, s2, 1\n\t" \
  "s_xor_b32 s98, s98, s3\n\t" \
  "v_mfma_f32_16x16x32_bf16 v[48:51], a[144:147], a[196:199], v[48:51]\n\t" \
  "s_lshl_b32 s98, s98, 13\n\t" \
  "s_and_b32 s42, s98, 0x2000\n\t" \
  "s_lshr_b32 s98, s2, 5\n\t" \
  "v_mfma_f32_16x16x32_bf16 v[80:83], a[144:147], a[200:203], v[80:83]\n\t" \
  "s_xor_b32 s98, s98, s3\n\t" \
  "s_lshl_b32 s98, s98, 13\n\t" \
  "s_and_b32 s43, s98, 0x2000\n\t" \
  "v_mfma_f32_16x16x32_bf16 v[112:115], a[144:147], a[204:207], v[112:115]\n\t" \
  "s_lshl_b32 s48, s3, 2\n\t" \
  "s_add_u32 s48, s48, 0x8000\n\t" \
  "s_and_b32 s98, s2, 0x80\n\t" \
  "v_mfma_f32_16x16x32_bf16 v[116:119], a[148:151], a[204:207], v[116:119]\n\t" \
  "s_lshl_b32 s98, s98, 7\n\t" \
  "s_add_u32 s48, s48, s98\n\t" \
  "s_and_b32 s98, s2, 0x4\n\t" \
  "v_mfma_f32_16x16x32_bf16 v[84:87], a[148:151], a[200:203], v[84:87]\n\t" \
  "s_lshl_b32 s98, s98, 11\n\t" \
  "s_add_u32 s48, s48, s98\n\t" \
  "s_and_b32 s98, s2, 0x78\n\t" \
  "v_mfma_f32_16x16x32_bf16 v[52:55], a[148:151], a[196:199], v[52:55]\n\t" \
  "s_lshl_b32 s98, s98, 6\n\t" \
  "s_add_u32 s48, s48, s98\n\t" \
  "s_lshl_b32 s49, s3, 7\n\t" \
  "v_mfma_f32_16x16x32_bf16 v[20:23], a[148:151], a[192:195], v[20:23]\n\t" \
  "s_add_u32 s49, s49, 0x20000\n\t" \
  "s_and_b32 s98, s2, 0xc0\n\t" \
  "s_lshl_b32 s98, s98, 9\n\t" \
  "v_mfma_f32_16x16x32_bf16 v[24:27], a[152:155], a[192:195], v[24:27]\n\t" \
  "s_add_u32 s49, s49, s98\n\t" \
  "s_and_b32 s98, s2, 0x2\n\t" \
  "s_lshl_b32 s98, s98, 13\n\t" \
  "v_mfma_f32_16x16x32_bf16 v[56:59], a[152:155], a[196:199], v[56:59]\n\t" \
  "s_add_u32 s49, s49, s98\n\t" \
  "s_and_b32 s98, s2, 0x4\n\t" \
  "s_lshl_b32 s98, s98, 11\n\t" \
  "v_mfma_f32_16x16x32_bf16 v[88:91], a[152:155], a[200:203], v[88:91]\n\t" \
  "s_add_u32 s49, s49, s98\n\t" \
  "s_and_b32 s98, s2, 0x1\n\t" \
  "s_lshl_b32 s98, s98, 12\n\t" \
  "v_mfma_f32_16x16x32_bf16 v[120:123], a[152:155], a[204:207], v[120:123]\n\t" \
  "s_add_u32 s49, s49, s98\n\t" \
  "s_lshl2_add_u32 s49, s39, s49\n\t" \
  "s_lshl_b32 s50, s3, 15\n\t" \
  "v_mfma_f32_16x16x32_bf16 v[124:127], a[156:159], a[204:207], v[124:127]\n\t" \
  "s_and_b32 s98, s2, 0x4\n\t" \
  "s_lshl_b32 s98, s98, 19\n\t" \
  "s_add_u32 s50, s50, s98\n\t" \
  "v_mfma_f32_16x16x32_bf16 v[92:95], a[156:159], a[200:203], v[92:95]\n\t" \
  "s_and_b32 s98, s2, 0xc0\n\t" \
  "s_lshl_b32 s98, s98, 13\n\t" \
  "s_add_u32 s50, s50, s98\n\t" \
  "v_mfma_f32_16x16x32_bf16 v[60:63], a[156:159], a[196:199], v[60:63]\n\t" \
  "s_waitcnt vmcnt(28) lgkmcnt(0)\n\t" \
  "s_add_u32 s97, s78, 4718592\n\t" \
  "s_bfe_u32 s97, s97, 0x140003\n\t" \
  "v_mfma_f32_16x16x32_bf16 v[28:31], a[156:159], a[192:195], v[28:31]\n\t" \
  "s_barrier\n\t" \
  "v_mfma_f32_16x16x32_bf16 v[0:3], a[160:163], a[208:211], v[0:3]\n\t" \
  "ds_read_b64_tr_b16 a[128:129], v192 offset:0\n\t" \
  "ds_read_b64_tr_b16 a[130:131], v192 offset:1024\n\t" \
  "ds_read_b64_tr_b16 a[132:133], v193 offset:0\n\t" \
  "s_add_u32 s97, s97, s82\n\t" \
  "v_mfma_f32_16x16x32_bf16 v[32:35], a[160:163], a[212:215], v[32:35]\n\t" \
  "buffer_load_dwordx4 a[32:35], v248, s[16:19], s97 offen sc1\n\t" \
  "ds_read_b64_tr_b16 a[134:135], v193 offset:1024\n\t" \
  "ds_read_b64_tr_b16 a[136:137], v194 offset:0\n\t" \
  "ds_read_b64_tr_b16 a[138:139], v194 offset:1024\n\t" \
  "v_mfma_f32_16x16x32_bf16 v[64:67], a[160:163], a[216:219], v[64:67]\n\t" \
  "ds_read_b64_tr_b16 a[140:141], v195 offset:0\n\t" \
  "ds_read_b64_tr_b16 a[142:143], v195 offset:1024\n\t" \
  "ds_read_b64_tr_b16 a[144:145], v196 offset:0\n\t" \
  "ds_read_b64_tr_b16 a[146:147], v196 offset:1024\n\t" \
  "v_mfma_f32_16x16x32_bf16 v[96:99], a[160:163], a[220:223], v[96:99]\n\t" \
  "ds_read_b64_tr_b16 a[148:149], v197 offset:0\n\t" \
  "ds_read_b64_tr_b16 a[150:151], v197 offset:1024\n\t" \
  "ds_read_b64_tr_b16 a[152:153], v198 offset:0\n\t" \
  "v_mbcnt_lo_u32_b32 v233, -1, 0\n\t" \
  "v_mfma_f32_16x16x32_bf16 v[100:103], a[164:167], a[220:223], v[100:103]\n\t" \
  "buffer_load_dwordx4 a[36:39], v249, s[16:19], s97 offen sc1\n\t" \
  "ds_read_b64_tr_b16 a[154:155], v198 offset:1024\n\t" \
  "s_and_b32 s98, s2, 0x3\n\t" \
  "v_mfma_f32_16x16x32_bf16 v[68:71], a[164:167], a[216:219], v[68:71]\n\t" \
  "s_lshl_b32 s98, s98, 17\n\t" \
  "ds_read_b64_tr_b16 a[156:157], v199 offset:0\n\t" \
  "v_mbcnt_hi_u32_b32 v233, -1, v233\n\t" \
  "v_mfma_f32_16x16x32_bf16 v[36:39], a[164:167], a[212:215], v[36:39]\n\t" \
  "s_add_u32 s50, s50, s98\n\t" \
  "ds_read_b64_tr_b16 a[158:159], v199 offset:1024\n\t" \
  "v_lshrrev_b32 v129, 4, v233\n\t" \
  "v_mfma_f32_16x16x32_bf16 v[4:7], a[164:167], a[208:211], v[4:7]\n\t" \
  "s_and_b32 s98, s2, 0x38\n\t" \
  "ds_read_b64_tr_b16 a[64:65], v192 offset:8192\n\t" \
  "v_and_b32 v130, 15, v233\n\t" \
  "v_mfma_f32_16x16x32_bf16 v[8:11], a[168:171], a[208:211], v[8:11]\n\t" \
  "buffer_load_dwordx4 a[40:43], v250, s[16:19], s97 offen sc1\n\t" \
  "v_mfma_f32_16x16x32_bf16 v[40:43], a[168:171], a[212:215], v[40:43]\n\t" \
  "s_lshl_b32 s98, s98, 5\n\t" \
  "ds_read_b64_tr_b16 a[66:67], v192 offset:9216\n\t" \
  "v_bfe_u32 v131, v233, 2, 2\n\t" \
  "v_mfma_f32_16x16x32_bf16 v[72:75], a[168:171], a[216:219], v[72:75]\n\t" \
  "s_add_u32 s50, s50, s98\n\t" \
  "ds_read_b64_tr_b16 a[68:69], v193 offset:8192\n\t" \
  "v_and_b32 v132, 3, v233\n\t" \
  "v_mfma_f32_16x16x32_bf16 v[104:107], a[168:171], a[220:223], v[104:107]\n\t" \
  "s_lshr_b32 s98, s2, 2\n\t" \
  "ds_read_b64_tr_b16 a[70:71], v193 offset:9216\n\t" \
  "v_cmp_lt_u32 vcc, 6, v233\n\t" \
  "v_mfma_f32_16x16x32_bf16 v[108:111], a[172:175], a[220:223], v[108:111]\n\t" \
  "buffer_load_dwordx4 a[44:47], v251, s[16:19], s97 offen sc1\n\t" \
  "v_mfma_f32_16x16x32_bf16 v[76:79], a[172:175], a[216:219], v[76:79]\n\t" \
  "s_add_u32 s96, s78, 5767168\n\t" \
  "s_and_b32 s96, s96, 0x780000\n\t" \
  "ds_read_b64_tr_b16 a[72:73], v194 offset:8192\n\t" \
  "s_add_u32 m0, s76, 16384\n\t" \
  "v_mfma_f32_16x16x32_bf16 v[44:47], a[172:175], a[212:215], v[44:47]\n\t" \
  "s_xor_b32 s98, s98, s2\n\t" \
  "ds_read_b64_tr_b16 a[74:75], v194 offset:9216\n\t" \
  "v_mov_b32 v133, 6272\n\t" \
  "v_mfma_f32_16x16x32_bf16 v[12:15], a[172:175], a[208:211], v[12:15]\n\t" \
  "buffer_load_dwordx4 v200, s[20:23], s96 offen offset:0 lds\n\t" \
  "v_mfma_f32_16x16x32_bf16 v[16:19], a[176:179], a[208:211], v[16:19]\n\t" \
  "s_and_b32 s98, s98, 4\n\t" \
  "ds_read_b64_tr_b16 a[76:77], v195 offset:8192\n\t" \
  "v_cndmask_b32 v133, 0, v133, vcc\n\t" \
  "v_mfma_f32_16x16x32_bf16 v[48:51], a[176:179], a[212:215], v[48:51]\n\t" \
  "s_lshl_b32 s44, s98, 4\n\t" \
  "ds_read_b64_tr_b16 a[78:79], v195 offset:9216\n\t" \
  "v_lshl_add_u32 v133, v233, 7, v133\n\t" \
  "v_mfma_f32_16x16x32_bf16 v[80:83], a[176:179], a[216:219], v[80:83]\n\t" \
  "s_and_b32 s98, s2, 0x8\n\t" \
  "ds_read_b64_tr_b16 a[80:81], v196 offset:8192\n\t" \
  "v_cmp_gt_u32 vcc, 14, v233\n\t" \
  "v_mfma_f32_16x16x32_bf16 v[112:115], a[176:179], a[220:223], v[112:115]\n\t" \
  "buffer_load_dwordx4 v201, s[20:23], s96 offen offset:1024 lds\n\t" \
  "v_mfma_f32_16x16x32_bf16 v[116:119], a[180:183], a[220:223], v[116:119]\n\t" \
  "s_lshl_b32 s98, s98, 2\n\t" \
  "ds_read_b64_tr_b16 a[82:83], v196 offset:9216\n\t" \
  "v_bfrev_b32 v247, 1\n\t" \
  "v_mfma_f32_16x16x32_bf16 v[84:87], a[180:183], a[216:219], v[84:87]\n\t" \
  "s_add_u32 s44, s44, s98\n\t" \
  "ds_read_b64_tr_b16 a[84:85], v197 offset:8192\n\t" \
  "v_cndmask_b32 v245, v247, v133, vcc\n\t" \
  "v_mfma_f32_16x16x32_bf16 v[52:55], a[180:183], a[212:215], v[52:55]\n\t" \
  "s_and_b32 s98, s2, 0x3\n\t" \
  "ds_read_b64_tr_b16 a[86:87], v197 offset:9216\n\t" \
  "v_writelane_b32 v247, s91, 0\n\t" \
  "v_mfma_f32_16x16x32_bf16 v[20:23], a[180:183], a[208:211], v[20:23]\n\t" \
  ".Lhs3:\n\ts_cbranch_execz .Lhs3n\n\t" \
  "buffer_load_dwordx4 v202, s[20:23], s96 offen offset:2048 lds\n\t" \
  "v_mfma_f32_16x16x32_bf16 v[24:27], a[184:187], a[208:211], v[24:27]\n\t" \
  "s_lshl_b32 s98, s98, 3\n\t" \
  "ds_read_b64_tr_b16 a[88:89], v198 offset:8192\n\t" \
  "v_lshrrev_b32 v133, 1, v129\n\t" \
  "v_mfma_f32_16x16x32_bf16 v[56:59], a[184:187], a[212:215], v[56:59]\n\t" \
  "s_add_u32 s44, s44, s98\n\t" \
  "ds_read_b64_tr_b16 a[90:91], v198 offset:9216\n\t" \
  "v_and_b32 v134, 1, v129\n\t" \
  "v_mfma_f32_16x16x32_bf16 v[88:91], a[184:187], a[216:219], v[88:91]\n\t" \
  "s_and_b32 s98, s2, 0x4\n\t" \
  "ds_read_b64_tr_b16 a[92:93], v199 offset:8192\n\t" \
  "v_lshl_or_b32 v133, v134, 1, v133\n\t" \
  "v_mfma_f32_16x16x32_bf16 v[120:123], a[184:187], a[220:223], v[120:123]\n\t" \
  "buffer_load_dwordx4 v203, s[20:23], s96 offen offset:3072 lds\n\t" \
  "v_mfma_f32_16x16x32_bf16 v[124:127], a[188:191], a[220:223], v[124:127]\n\t" \
  "s_lshl_b32 s98, s98, 0\n\t" \
  "ds_read_b64_tr_b16 a[94:95], v199 offset:9216\n\t" \
  "v_xor_b32 v133, v133, v130\n\t" \
  "v_mfma_f32_16x16x32_bf16 v[92:95], a[188:191], a[216:219], v[92:95]\n\t" \
  "s_add_u32 s44, s44, s98\n\t" \
  "v_lshlrev_b32 v133, 4, v133\n\t" \
  "v_lshl_add_u32 v133, v130, 8, v133\n\t" \
  "v_mfma_f32_16x16x32_bf16 v[60:63], a[188:191], a[212:215], v[60:63]\n\t" \
  "s_waitcnt vmcnt(24) lgkmcnt(0)\n\t" \
  "s_add_u32 s97, s97, s83\n\t" \
  "v_add_u32 v220, 49152, v133\n\t" \
  "v_mfma_f32_16x16x32_bf16 v[28:31], a[188:191], a[208:211], v[28:31]\n\t" \
  "s_barrier\n\t" \
  "v_mfma_f32_16x16x32_bf16 v[0:3], a[128:131], a[224:227], v[0:3]\n\t" \
  "buffer_load_dwordx4 a[48:51], v248, s[16:19], s97 offen sc1\n\t" \
  "v_mfma_f32_16x16x32_bf16 v[0:3], a[64:67], a[240:243], v[0:3]\n\t" \
  "s_add_u32 s44, s44, 0x2000\n\t" \
  "ds_read_b64_tr_b16 a[160:161], v192 offset:16384\n\t" \
  "v_xor_b32 v221, 64, v220\n\t" \
  "v_mfma_f32_16x16x32_bf16 v[32:35], a[64:67], a[244:247], v[32:35]\n\t" \
  "ds_read_b64_tr_b16 a[162:163], v192 offset:17408\n\t" \
  "v_xor_b32 v222, 128, v220\n\t" \
  "v_mfma_f32_16x16x32_bf16 v[32:35], a[128:131], a[228:231], v[32:35]\n\t" \
  "v_xor_b32 v223, 192, v220\n\t" \
  "v_xor_b32 v133, v130, v129\n\t" \
  "v_mfma_f32_16x16x32_bf16 v[64:67], a[128:131], a[232:235], v[64:67]\n\t" \
  "buffer_load_dwordx4 a[52:55], v249, s[16:19], s97 offen sc1\n\t" \
  "v_mfma_f32_16x16x32_bf16 v[64:67], a[64:67], a[248:251], v[64:67]\n\t" \
  "ds_read_b64_tr_b16 a[164:165], v193 offset:16384\n\t" \
  "v_lshlrev_b32 v133, 4, v133\n\t" \
  "v_mfma_f32_16x16x32_bf16 v[96:99], a[64:67], a[252:255], v[96:99]\n\t" \
  "ds_read_b64_tr_b16 a[166:167], v193 offset:17408\n\t" \
  "v_lshl_add_u32 v224, v129, 8, v133\n\t" \
  "v_mfma_f32_16x16x32_bf16 v[96:99], a[128:131], a[236:239], v[96:99]\n\t" \
  "ds_read_b64_tr_b16 a[168:169], v194 offset:16384\n\t" \
  "v_xor_b32 v225, 64, v224\n\t" \
  "v_mfma_f32_16x16x32_bf16 v[4:7], a[132:135], a[224:227], v[4:7]\n\t" \
  "buffer_load_dwordx4 a[56:59], v250, s[16:19], s97 offen sc1\n\t" \
  "v_mfma_f32_16x16x32_bf16 v[4:7], a[68:71], a[240:243], v[4:7]\n\t" \
  "ds_read_b64_tr_b16 a[170:171], v194 offset:17408\n\t" \
  "v_xor_b32 v226, 128, v224\n\t" \
  "v_mfma_f32_16x16x32_bf16 v[36:39], a[68:71], a[244:247], v[36:39]\n\t" \
  "ds_read_b64_tr_b16 a[172:173], v195 offset:16384\n\t" \
  "v_xor_b32 v227, 192, v224\n\t" \
  "v_mfma_f32_16x16x32_bf16 v[36:39], a[132:135], a[228:231], v[36:39]\n\t" \
  "v_lshlrev_b32 v133, 3, v132\n\t" \
  "v_lshl_add_u32 v133, v129, 10, v133\n\t" \
  "v_mfma_f32_16x16x32_bf16 v[68:71], a[132:135], a[232:235], v[68:71]\n\t" \
  "buffer_load_dwordx4 a[60:63], v251, s[16:19], s97 offen sc1\n\t" \
  "v_mfma_f32_16x16x32_bf16 v[68:71], a[68:71], a[248:251], v[68:71]\n\t" \
  "s_add_u32 s97, s78, 5242880\n\t" \
  "s_bfe_u32 s97, s97, 0x140003\n\t" \
  "s_add_u32 s97, s97, s82\n\t" \
  "v_mfma_f32_16x16x32_bf16 v[100:103], a[68:71], a[252:255], v[100:103]\n\t" \
  "ds_read_b64_tr_b16 a[174:175], v195 offset:17408\n\t" \
  "v_lshl_add_u32 v228, v131, 6, v133\n\t" \
  "v_mfma_f32_16x16x32_bf16 v[100:103], a[132:135], a[236:239], v[100:103]\n\t" \
  "buffer_load_dwordx4 a[192:195], v248, s[16:19], s97 offen sc1\n\t" \
  "v_mfma_f32_16x16x32_bf16 v[8:11], a[136:139], a[224:227], v[8:11]\n\t" \
  "ds_read_b64_tr_b16 a[176:177], v196 offset:16384\n\t" \
  "v_lshl_add_u32 v228, v134, 5, v228\n\t" \
  "v_mfma_f32_16x16x32_bf16 v[8:11], a[72:75], a[240:243], v[8:11]\n\t" \
  "ds_read_b64_tr_b16 a[178:179], v196 offset:17408\n\t" \
  "v_add_u32 v228, 81920, v228\n\t" \
  "v_mfma_f32_16x16x32_bf16 v[40:43], a[72:75], a[244:247], v[40:43]\n\t" \
  "ds_read_b64_tr_b16 a[180:181], v197 offset:16384\n\t" \
  "v_xor_b32 v229, 32, v228\n\t" \
  "v_mfma_f32_16x16x32_bf16 v[40:43], a[136:139], a[228:231], v[40:43]\n\t" \
  "buffer_load_dwordx4 a[196:199], v249, s[16:19], s97 offen sc1\n\t" \
  "v_mfma_f32_16x16x32_bf16 v[72:75], a[136:139], a[232:235], v[72:75]\n\t" \
  "ds_read_b64_tr_b16 a[182:183], v197 offset:17408\n\t" \
  "v_lshl_add_u32 v244, v131, 8, v133\n\t" \
  "v_mfma_f32_16x16x32_bf16 v[72:75], a[72:75], a[248:251], v[72:75]\n\t" \
  "ds_read_b64_tr_b16 a[184:185], v198 offset:16384\n\t" \
  "v_lshl_add_u32 v244, v131, 5, v244\n\t" \
  "v_mfma_f32_16x16x32_bf16 v[104:107], a[72:75], a[252:255], v[104:107]\n\t" \
  "ds_read_b64_tr_b16 a[186:187], v198 offset:17408\n\t" \
  "v_lshl_add_u32 v244, v134, 7, v244\n\t" \
  "v_mfma_f32_16x16x32_bf16 v[104:107], a[136:139], a[236:239], v[104:107]\n\t" \
  "buffer_load_dwordx4 a[200:203], v250, s[16:19], s97 offen sc1\n\t" \
  "v_mfma_f32_16x16x32_bf16 v[12:15], a[140:143], a[224:227], v[12:15]\n\t" \
  "ds_read_b64_tr_b16 a[188:189], v199 offset:16384\n\t" \
  "v_lshlrev_b32 v133, 4, v132\n\t" \
  "v_mfma_f32_16x16x32_bf16 v[12:15], a[76:79], a[240:243], v[12:15]\n\t" \
  "ds_read_b64_tr_b16 a[190:191], v199 offset:17408\n\t" \
  "v_lshl_add_u32 v133, v129, 6, v133\n\t" \
  "v_mfma_f32_16x16x32_bf16 v[44:47], a[76:79], a[244:247], v[44:47]\n\t" \
  "ds_read_b64_tr_b16 a[96:97], v192 offset:24576\n\t" \
  "v_lshl_add_u32 v230, v131, 8, v133\n\t" \
  "v_mfma_f32_16x16x32_bf16 v[44:47], a[140:143], a[228:231], v[44:47]\n\t" \
  "buffer_load_dwordx4 a[204:207], v251, s[16:19], s97 offen sc1\n\t" \
  "v_mfma_f32_16x16x32_bf16 v[76:79], a[140:143], a[232:235], v[76:79]\n\t" \
  "ds_read_b64_tr_b16 a[98:99], v192 offset:25600\n\t" \
  "v_xor_b32 v231, 32, v230\n\t" \
  "v_mfma_f32_16x16x32_bf16 v[76:79], a[76:79], a[248:251], v[76:79]\n\t" \
  "s_add_u32 s97, s97, s83\n\t" \
  "v_lshlrev_b32 v133, 1, v129\n\t" \
  "v_xor_b32 v133, v133, v130\n\t" \
  "v_mfma_f32_16x16x32_bf16 v[108:111], a[76:79], a[252:255], v[108:111]\n\t" \
  "ds_read_b64_tr_b16 a[100:101], v193 offset:24576\n\t" \
  "v_lshlrev_b32 v133, 4, v133\n\t" \
  "v_mfma_f32_16x16x32_bf16 v[108:111], a[140:143], a[236:239], v[108:111]\n\t" \
  "ds_read_b64_tr_b16 a[102:103], v193 offset:25600\n\t" \
  "v_lshl_add_u32 v240, v129, 11, v133\n\t" \
  "v_mfma_f32_16x16x32_bf16 v[16:19], a[144:147], a[224:227], v[16:19]\n\t" \
  "buffer_load_dwordx4 a[208:211], v248, s[16:19], s97 offen sc1\n\t" \
  "v_mfma_f32_16x16x32_bf16 v[16:19], a[80:83], a[240:243], v[16:19]\n\t" \
  "ds_read_b64_tr_b16 a[104:105], v194 offset:24576\n\t" \
  "v_xor_b32 v241, 0x80, v240\n\t" \
  "v_mfma_f32_16x16x32_bf16 v[48:51], a[80:83], a[244:247], v[48:51]\n\t" \
  "s_waitcnt vmcnt(20)\n\t" \
  "v_lshlrev_b32 v128, 16, v136\n\t" \
  "v_lshrrev_b32 v133, 1, v233\n\t" \
  "v_mfma_f32_16x16x32_bf16 v[48:51], a[144:147], a[228:231], v[48:51]\n\t" \
  "ds_read_b64_tr_b16 a[106:107], v194 offset:25600\n\t" \
  "v_mul_u32_u24 v133, 57344, v133\n\t" \
  "v_mfma_f32_16x16x32_bf16 v[80:83], a[144:147], a[232:235], v[80:83]\n\t" \
  "buffer_load_dwordx4 a[212:215], v249, s[16:19], s97 offen sc1\n\t" \
  "v_mfma_f32_16x16x32_bf16 v[80:83], a[80:83], a[248:251], v[80:83]\n\t" \
  "ds_read_b64_tr_b16 a[108:109], v195 offset:24576\n\t" \
  "v_and_b32 v134, 1, v233\n\t" \
  "v_mfma_f32_16x16x32_bf16 v[112:115], a[80:83], a[252:255], v[112:115]\n\t" \
  "ds_read_b64_tr_b16 a[110:111], v195 offset:25600\n\t" \
  "v_lshl_add_u32 v133, v134, 4, v133\n\t" \
  "v_mfma_f32_16x16x32_bf16 v[112:115], a[144:147], a[236:239], v[112:115]\n\t" \
  "ds_read_b64_tr_b16 a[112:113], v196 offset:24576\n\t" \
  "v_or_b32 v232, s57, v133\n\t" \
  "v_mfma_f32_16x16x32_bf16 v[20:23], a[148:151], a[224:227], v[20:23]\n\t" \
  "buffer_load_dwordx4 a[216:219], v250, s[16:19], s97 offen sc1\n\t" \
  "v_mfma_f32_16x16x32_bf16 v[20:23], a[84:87], a[240:243], v[20:23]\n\t" \
  "ds_read_b64_tr_b16 a[114:115], v196 offset:25600\n\t" \
  "v_and_b32 v136, 0xffff0000, v136\n\t" \
  "v_mfma_f32_16x16x32_bf16 v[52:55], a[84:87], a[244:247], v[52:55]\n\t" \
  "v_mul_f32 v176, v128, v168\n\t" \
  "v_mul_f32 v180, v136, v168\n\t" \
  "v_mfma_f32_16x16x32_bf16 v[52:55], a[148:151], a[228:231], v[52:55]\n\t" \
  "ds_read_b64_tr_b16 a[116:117], v197 offset:24576\n\t" \
  "v_lshlrev_b32 v128, 16, v137\n\t" \
  "v_mfma_f32_16x16x32_bf16 v[84:87], a[148:151], a[232:235], v[84:87]\n\t" \
  "buffer_load_dwordx4 a[220:223], v251, s[16:19], s97 offen sc1\n\t" \
  "v_mfma_f32_16x16x32_bf16 v[84:87], a[84:87], a[248:251], v[84:87]\n\t" \
  "s_add_u32 s96, s78, 6291456\n\t" \
  "s_and_b32 s96, s96, 0x780000\n\t" \
  "s_add_u32 m0, s76, 32768\n\t" \
  "v_mfma_f32_16x16x32_bf16 v[116:119], a[84:87], a[252:255], v[116:119]\n\t" \
  "ds_read_b64_tr_b16 a[118:119], v197 offset:25600\n\t" \
  "v_and_b32 v137, 0xffff0000, v137\n\t" \
  "v_mfma_f32_16x16x32_bf16 v[116:119], a[148:151], a[236:239], v[116:119]\n\t" \
  "buffer_load_dwordx4 v200, s[20:23], s96 offen offset:0 lds\n\t" \
  "v_mfma_f32_16x16x32_bf16 v[24:27], a[152:155], a[224:227], v[24:27]\n\t" \
  "ds_read_b64_tr_b16 a[120:121], v198 offset:24576\n\t" \
  "v_mul_f32 v177, v128, v168\n\t" \
  "v_mfma_f32_16x16x32_bf16 v[24:27], a[88:91], a[240:243], v[24:27]\n\t" \
  "ds_read_b64_tr_b16 a[122:123], v198 offset:25600\n\t" \
  "v_mul_f32 v181, v137, v168\n\t" \
  "v_mfma_f32_16x16x32_bf16 v[56:59], a[88:91], a[244:247], v[56:59]\n\t" \
  "ds_read_b64_tr_b16 a[124:125], v199 offset:24576\n\t" \
  "v_lshlrev_b32 v128, 16, v138\n\t" \
  "v_mfma_f32_16x16x32_bf16 v[56:59], a[152:155], a[228:231], v[56:59]\n\t" \
  "buffer_load_dwordx4 v201, s[20:23], s96 offen offset:1024 lds\n\t" \
  "v_mfma_f32_16x16x32_bf16 v[88:91], a[152:155], a[232:235], v[88:91]\n\t" \
  "ds_read_b64_tr_b16 a[126:127], v199 offset:25600\n\t" \
  "v_and_b32 v138, 0xffff0000, v138\n\t" \
  "v_mfma_f32_16x16x32_bf16 v[88:91], a[88:91], a[248:251], v[88:91]\n\t" \
  "v_mul_f32 v178, v128, v168\n\t" \
  "v_mul_f32 v182, v138, v168\n\t" \
  "v_mfma_f32_16x16x32_bf16 v[120:123], a[88:91], a[252:255], v[120:123]\n\t" \
  "v_lshlrev_b32 v128, 16, v139\n\t" \
  "v_and_b32 v139, 0xffff0000, v139\n\t" \
  "v_mfma_f32_16x16x32_bf16 v[120:123], a[152:155], a[236:239], v[120:123]\n\t" \
  "buffer_load_dwordx4 v202, s[20:23], s96 offen offset:2048 lds\n\t" \
  "v_mfma_f32_16x16x32_bf16 v[28:31], a[156:159], a[224:227], v[28:31]\n\t" \
  "v_mul_f32 v179, v128, v168\n\t" \
  "v_mul_f32 v183, v139, v168\n\t" \
  "v_mfma_f32_16x16x32_bf16 v[28:31], a[92:95], a[240:243], v[28:31]\n\t" \
  "s_waitcnt vmcnt(25)\n\t" \
  "v_lshlrev_b32 v128, 16, v140\n\t" \
  "v_and_b32 v140, 0xffff0000, v140\n\t" \
  "v_mfma_f32_16x16x32_bf16 v[60:63], a[92:95], a[244:247], v[60:63]\n\t" \
  "v_fma_f32 v176, v128, v169, v176\n\t" \
  "v_fma_f32 v180, v140, v169, v180\n\t" \
  "v_mfma_f32_16x16x32_bf16 v[60:63], a[156:159], a[228:231], v[60:63]\n\t" \
  "buffer_load_dwordx4 v203, s[20:23], s96 offen offset:3072 lds\n\t" \
  "v_mfma_f32_16x16x32_bf16 v[92:95], a[156:159], a[232:235], v[92:95]\n\t" \
  "v_lshlrev_b32 v128, 16, v141\n\t" \
  "v_and_b32 v141, 0xffff0000, v141\n\t" \
  "v_mfma_f32_16x16x32_bf16 v[92:95], a[92:95], a[248:251], v[92:95]\n\t" \
  "s_add_u32 s97, s78, 5767168\n\t" \
  "v_fma_f32 v177, v128, v169, v177\n\t" \
  "v_fma_f32 v181, v141, v169, v181\n\t" \
  "v_mfma_f32_16x16x32_bf16 v[124:127], a[92:95], a[252:255], v[124:127]\n\t" \
  "s_waitcnt lgkmcnt(0)\n\t" \
  "s_bfe_u32 s97, s97, 0x140003\n\t" \
  "s_add_u32 s97, s97, s82\n\t" \
  "v_mfma_f32_16x16x32_bf16 v[124:127], a[156:159], a[236:239], v[124:127]\n\t" \
  "s_barrier\n\t" \
  "v_mfma_f32_16x16x32_bf16 v[0:3], a[160:163], a[0:3], v[0:3]\n\t" \
  "buffer_load_dwordx4 a[224:227], v248, s[16:19], s97 offen sc1\n\t" \
  "v_mfma_f32_16x16x32_bf16 v[0:3], a[96:99], a[16:19], v[0:3]\n\t" \
  "ds_read_b64_tr_b16 a[128:129], v192 offset:32768\n\t" \
  "v_lshlrev_b32 v128, 16, v142\n\t" \
  "v_mfma_f32_16x16x32_bf16 v[32:35], a[96:99], a[20:23], v[32:35]\n\t" \
  "ds_read_b64_tr_b16 a[130:131], v192 offset:33792\n\t" \
  "v_and_b32 v142, 0xffff0000, v142\n\t" \
  "v_mfma_f32_16x16x32_bf16 v[32:35], a[160:163], a[4:7], v[32:35]\n\t" \
  "v_fma_f32 v178, v128, v169, v178\n\t" \
  "v_fma_f32 v182, v142, v169, v182\n\t" \
  "v_mfma_f32_16x16x32_bf16 v[64:67], a[160:163], a[8:11], v[64:67]\n\t" \
  "buffer_load_dwordx4 a[228:231], v249, s[16:19], s97 offen sc1\n\t" \
  "v_mfma_f32_16x16x32_bf16 v[64:67], a[96:99], a[24:27], v[64:67]\n\t" \
  "ds_read_b64_tr_b16 a[132:133], v193 offset:32768\n\t" \
  "v_lshlrev_b32 v128, 16, v143\n\t" \
  "v_mfma_f32_16x16x32_bf16 v[96:99], a[96:99], a[28:31], v[96:99]\n\t" \
  "ds_read_b64_tr_b16 a[134:135], v193 offset:33792\n\t" \
  "v_and_b32 v143, 0xffff0000, v143\n\t" \
  "v_mfma_f32_16x16x32_bf16 v[96:99], a[160:163], a[12:15], v[96:99]\n\t" \
  "ds_read_b64_tr_b16 a[136:137], v194 offset:32768\n\t" \
  "v_fma_f32 v179, v128, v169, v179\n\t" \
  "v_mfma_f32_16x16x32_bf16 v[4:7], a[164:167], a[0:3], v[4:7]\n\t" \
  "buffer_load_dwordx4 a[232:235], v250, s[16:19], s97 offen sc1\n\t" \
  "v_mfma_f32_16x16x32_bf16 v[4:7], a[100:103], a[16:19], v[4:7]\n\t" \
  "ds_read_b64_tr_b16 a[138:139], v194 offset:33792\n\t" \
  "v_fma_f32 v183, v143, v169, v183\n\t" \
  "v_mfma_f32_16x16x32_bf16 v[36:39], a[100:103], a[20:23], v[36:39]\n\t" \
  "s_waitcnt vmcnt(28)\n\t" \
  "v_lshlrev_b32 v128, 16, v144\n\t" \
  "v_and_b32 v144, 0xffff0000, v144\n\t" \
  "v_mfma_f32_16x16x32_bf16 v[36:39], a[164:167], a[4:7], v[36:39]\n\t" \
  "ds_read_b64_tr_b16 a[140:141], v195 offset:32768\n\t" \
  "v_fma_f32 v176, v128, v170, v176\n\t" \
  "v_mfma_f32_16x16x32_bf16 v[68:71], a[164:167], a[8:11], v[68:71]\n\t" \
  "buffer_load_dwordx4 a[236:239], v251, s[16:19], s97 offen sc1\n\t" \
  "v_mfma_f32_16x16x32_bf16 v[68:71], a[100:103], a[24:27], v[68:71]\n\t" \
  "s_add_u32 s96, s78, 6815744\n\t" \
  "s_and_b32 s96, s96, 0x780000\n\t" \
  "s_add_u32 m0, s76, 65536\n\t" \
  "v_mfma_f32_16x16x32_bf16 v[100:103], a[100:103], a[28:31], v[100:103]\n\t" \
  "ds_read_b64_tr_b16 a[142:143], v195 offset:33792\n\t" \
  "v_fma_f32 v180, v144, v170, v180\n\t" \
  "v_mfma_f32_16x16x32_bf16 v[100:103], a[164:167], a[12:15], v[100:103]\n\t" \
  "buffer_load_dwordx4 v200, s[20:23], s96 offen offset:0 lds\n\t" \
  "v_mfma_f32_16x16x32_bf16 v[8:11], a[168:171], a[0:3], v[8:11]\n\t" \
  "ds_read_b64_tr_b16 a[144:145], v196 offset:32768\n\t" \
  "v_lshlrev_b32 v128, 16, v145\n\t" \
  "v_mfma_f32_16x16x32_bf16 v[8:11], a[104:107], a[16:19], v[8:11]\n\t" \
  "ds_read_b64_tr_b16 a[146:147], v196 offset:33792\n\t" \
  "v_and_b32 v145, 0xffff0000, v145\n\t" \
  "v_mfma_f32_16x16x32_bf16 v[40:43], a[104:107], a[20:23], v[40:43]\n\t" \
  "ds_read_b64_tr_b16 a[148:149], v197 offset:32768\n\t" \
  "v_fma_f32 v177, v128, v170, v177\n\t" \
  "v_mfma_f32_16x16x32_bf16 v[40:43], a[168:171], a[4:7], v[40:43]\n\t" \
  "buffer_load_dwordx4 v201, s[20:23], s96 offen offset:1024 lds\n\t" \
  "v_mfma_f32_16x16x32_bf16 v[72:75], a[168:171], a[8:11], v[72:75]\n\t" \
  "ds_read_b64_tr_b16 a[150:151], v197 offset:33792\n\t" \
  "v_fma_f32 v181, v145, v170, v181\n\t" \
  "v_mfma_f32_16x16x32_bf16 v[72:75], a[104:107], a[24:27], v[72:75]\n\t" \
  "ds_read_b64_tr_b16 a[152:153], v198 offset:32768\n\t" \
  "v_lshlrev_b32 v128, 16, v146\n\t" \
  "v_mfma_f32_16x16x32_bf16 v[104:107], a[104:107], a[28:31], v[104:107]\n\t" \
  "ds_read_b64_tr_b16 a[154:155], v198 offset:33792\n\t" \
  "v_and_b32 v146, 0xffff0000, v146\n\t" \
  "v_mfma_f32_16x16x32_bf16 v[104:107], a[168:171], a[12:15], v[104:107]\n\t" \
  "buffer_load_dwordx4 v202, s[20:23], s96 offen offset:2048 lds\n\t" \
  "v_mfma_f32_16x16x32_bf16 v[12:15], a[172:175], a[0:3], v[12:15]\n\t" \
  "ds_read_b64_tr_b16 a[156:157], v199 offset:32768\n\t" \
  "v_fma_f32 v178, v128, v170, v178\n\t" \
  "v_mfma_f32_16x16x32_bf16 v[12:15], a[108:111], a[16:19], v[12:15]\n\t" \
  "ds_read_b64_tr_b16 a[158:159], v199 offset:33792\n\t" \
  "v_fma_f32 v182, v146, v170, v182\n\t" \
  "v_mfma_f32_16x16x32_bf16 v[44:47], a[108:111], a[20:23], v[44:47]\n\t" \
  "ds_read_b64_tr_b16 a[64:65], v192 offset:40960\n\t" \
  "v_lshlrev_b32 v128, 16, v147\n\t" \
  "v_mfma_f32_16x16x32_bf16 v[44:47], a[172:175], a[4:7], v[44:47]\n\t" \
  "buffer_load_dwordx4 v203, s[20:23], s96 offen offset:3072 lds\n\t" \
  "v_mfma_f32_16x16x32_bf16 v[76:79], a[172:175], a[8:11], v[76:79]\n\t" \
  "ds_read_b64_tr_b16 a[66:67], v192 offset:41984\n\t" \
  "v_and_b32 v147, 0xffff0000, v147\n\t" \
  "v_mfma_f32_16x16x32_bf16 v[76:79], a[108:111], a[24:27], v[76:79]\n\t" \
  "s_add_u32 s97, s97, s83\n\t" \
  "v_fma_f32 v179, v128, v170, v179\n\t" \
  "v_fma_f32 v183, v147, v170, v183\n\t" \
  "v_mfma_f32_16x16x32_bf16 v[108:111], a[108:111], a[28:31], v[108:111]\n\t" \
  "ds_read_b64_tr_b16 a[68:69], v193 offset:40960\n\t" \
  "v_mfma_f32_16x16x32_bf16 v[108:111], a[172:175], a[12:15], v[108:111]\n\t" \
  "s_waitcnt vmcnt(32)\n\t" \
  "v_lshlrev_b32 v128, 16, v148\n\t" \
  "v_and_b32 v148, 0xffff0000, v148\n\t" \
  "v_mfma_f32_16x16x32_bf16 v[16:19], a[176:179], a[0:3], v[16:19]\n\t" \
  "buffer_load_dwordx4 a[240:243], v248, s[16:19], s97 offen sc1\n\t" \
  "v_mfma_f32_16x16x32_bf16 v[16:19], a[112:115], a[16:19], v[16:19]\n\t" \
  "ds_read_b64_tr_b16 a[70:71], v193 offset:41984\n\t" \
  "v_fma_f32 v176, v128, v171, v176\n\t" \
  "v_mfma_f32_16x16x32_bf16 v[48:51], a[112:115], a[20:23], v[48:51]\n\t" \
  "ds_read_b64_tr_b16 a[72:73], v194 offset:40960\n\t" \
  "v_fma_f32 v180, v148, v171, v180\n\t" \
  "v_mfma_f32_16x16x32_bf16 v[48:51], a[176:179], a[4:7], v[48:51]\n\t" \
  "ds_read_b64_tr_b16 a[74:75], v194 offset:41984\n\t" \
  "v_lshlrev_b32 v128, 16, v149\n\t" \
  "v_mfma_f32_16x16x32_bf16 v[80:83], a[176:179], a[8:11], v[80:83]\n\t" \
  "ds_read_b64_tr_b16 a[76:77], v195 offset:40960\n\t" \
  "v_and_b32 v149, 0xffff0000, v149\n\t" \
  "v_mfma_f32_16x16x32_bf16 v[80:83], a[112:115], a[24:27], v[80:83]\n\t" \
  "v_fma_f32 v177, v128, v171, v177\n\t" \
  "v_fma_f32 v181, v149, v171, v181\n\t" \
  "v_mfma_f32_16x16x32_bf16 v[112:115], a[112:115], a[28:31], v[112:115]\n\t" \
  "ds_read_b64_tr_b16 a[78:79], v195 offset:41984\n\t" \
  "v_lshlrev_b32 v128, 16, v150\n\t" \
  "v_mfma_f32_16x16x32_bf16 v[112:115], a[176:179], a[12:15], v[112:115]\n\t" \
  "ds_read_b64_tr_b16 a[80:81], v196 offset:40960\n\t" \
  "v_and_b32 v150, 0xffff0000, v150\n\t" \
  "v_mfma_f32_16x16x32_bf16 v[20:23], a[180:183], a[0:3], v[20:23]\n\t" \
  "buffer_load_dwordx4 a[244:247], v249, s[16:19], s97 offen sc1\n\t" \
  "v_mfma_f32_16x16x32_bf16 v[20:23], a[116:119], a[16:19], v[20:23]\n\t" \
  "ds_read_b64_tr_b16 a[82:83], v196 offset:41984\n\t" \
  "v_fma_f32 v178, v128, v171, v178\n\t" \
  "v_mfma_f32_16x16x32_bf16 v[52:55], a[116:119], a[20:23], v[52:55]\n\t" \
  "v_fma_f32 v182, v150, v171, v182\n\t" \
  "v_lshlrev_b32 v128, 16, v151\n\t" \
  "v_mfma_f32_16x16x32_bf16 v[52:55], a[180:183], a[4:7], v[52:55]\n\t" \
  "ds_read_b64_tr_b16 a[84:85], v197 offset:40960\n\t" \
  "v_and_b32 v151, 0xffff0000, v151\n\t" \
  "v_mfma_f32_16x16x32_bf16 v[84:87], a[180:183], a[8:11], v[84:87]\n\t" \
  "ds_read_b64_tr_b16 a[86:87], v197 offset:41984\n\t" \
  "v_fma_f32 v179, v128, v171, v179\n\t" \
  "v_mfma_f32_16x16x32_bf16 v[84:87], a[116:119], a[24:27], v[84:87]\n\t" \
  "s_cmp_lt_i32 s91, 0\n\t" \
  "s_cselect_b32 s44, s91, s44\n\t" \
  "v_fma_f32 v183, v151, v171, v183\n\t" \
  "v_mfma_f32_16x16x32_bf16 v[116:119], a[116:119], a[28:31], v[116:119]\n\t" \
  "ds_read_b64_tr_b16 a[88:89], v198 offset:40960\n\t" \
  "v_mfma_f32_16x16x32_bf16 v[116:119], a[180:183], a[12:15], v[116:119]\n\t" \
  "buffer_load_dwordx4 a[248:251], v250, s[16:19], s97 offen sc1\n\t" \
  "v_mfma_f32_16x16x32_bf16 v[24:27], a[184:187], a[0:3], v[24:27]\n\t" \
  "ds_read_b64_tr_b16 a[90:91], v198 offset:41984\n\t" \
  "v_mfma_f32_16x16x32_bf16 v[24:27], a[120:123], a[16:19], v[24:27]\n\t" \
  "ds_read_b64_tr_b16 a[92:93], v199 offset:40960\n\t" \
  "v_mfma_f32_16x16x32_bf16 v[56:59], a[120:123], a[20:23], v[56:59]\n\t" \
  "ds_read_b64_tr_b16 a[94:95], v199 offset:41984\n\t" \
  "v_mfma_f32_16x16x32_bf16 v[56:59], a[184:187], a[4:7], v[56:59]\n\t" \
  "v_mfma_f32_16x16x32_bf16 v[88:91], a[184:187], a[8:11], v[88:91]\n\t" \
  "v_mfma_f32_16x16x32_bf16 v[88:91], a[120:123], a[24:27], v[88:91]\n\t" \
  "buffer_load_dwordx4 a[252:255], v251, s[16:19], s97 offen sc1\n\t" \
  "v_mfma_f32_16x16x32_bf16 v[120:123], a[120:123], a[28:31], v[120:123]\n\t" \
  "v_mfma_f32_16x16x32_bf16 v[120:123], a[184:187], a[12:15], v[120:123]\n\t" \
  "v_mfma_f32_16x16x32_bf16 v[28:31], a[188:191], a[0:3], v[28:31]\n\t" \
  "v_mfma_f32_16x16x32_bf16 v[28:31], a[124:127], a[16:19], v[28:31]\n\t" \
  "v_mfma_f32_16x16x32_bf16 v[60:63], a[124:127], a[20:23], v[60:63]\n\t" \
  "v_mfma_f32_16x16x32_bf16 v[60:63], a[188:191], a[4:7], v[60:63]\n\t" \
  "v_mfma_f32_16x16x32_bf16 v[92:95], a[188:191], a[8:11], v[92:95]\n\t" \
  "v_mfma_f32_16x16x32_bf16 v[92:95], a[124:127], a[24:27], v[92:95]\n\t" \
  "s_add_u32 s97, s78, 6291456\n\t" \
  "s_bfe_u32 s97, s97, 0x140003\n\t" \
  "s_add_u32 s97, s97, s82\n\t" \
  "v_mfma_f32_16x16x32_bf16 v[124:127], a[124:127], a[28:31], v[124:127]\n\t" \
  "s_waitcnt vmcnt(24) lgkmcnt(0)\n\t" \
  "v_mfma_f32_16x16x32_bf16 v[124:127], a[188:191], a[12:15], v[124:127]\n\t" \
  "s_barrier\n\t" \
  "v_mfma_f32_16x16x32_bf16 v[0:3], a[128:131], a[32:35], v[0:3]\n\t" \
  "buffer_load_dwordx4 a[0:3], v248, s[16:19], s97 offen sc1\n\t" \
  "v_mfma_f32_16x16x32_bf16 v[0:3], a[64:67], a[48:51], v[0:3]\n\t" \
  "ds_read_b64_tr_b16 a[160:161], v184 offset:0\n\t" \
  "v_mfma_f32_16x16x32_bf16 v[32:35], a[64:67], a[52:55], v[32:35]\n\t" \
  "ds_read_b64_tr_b16 a[162:163], v184 offset:1024\n\t" \
  "v_mfma_f32_16x16x32_bf16 v[32:35], a[128:131], a[36:39], v[32:35]\n\t" \
  "buffer_load_dwordx4 a[4:7], v249, s[16:19], s97 offen sc1\n\t" \
  "v_mfma_f32_16x16x32_bf16 v[64:67], a[128:131], a[40:43], v[64:67]\n\t" \
  "ds_read_b64_tr_b16 a[164:165], v185 offset:0\n\t" \
  "v_mfma_f32_16x16x32_bf16 v[64:67], a[64:67], a[56:59], v[64:67]\n\t" \
  "buffer_load_dwordx4 a[8:11], v250, s[16:19], s97 offen sc1\n\t" \
  "v_mfma_f32_16x16x32_bf16 v[96:99], a[64:67], a[60:63], v[96:99]\n\t" \
  "ds_read_b64_tr_b16 a[166:167], v185 offset:1024\n\t" \
  "v_mfma_f32_16x16x32_bf16 v[96:99], a[128:131], a[44:47], v[96:99]\n\t" \
  "ds_read_b64_tr_b16 a[168:169], v186 offset:0\n\t" \
  "v_mfma_f32_16x16x32_bf16 v[4:7], a[132:135], a[32:35], v[4:7]\n\t" \
  "buffer_load_dwordx4 a[12:15], v251, s[16:19], s97 offen sc1\n\t" \
  "v_mfma_f32_16x16x32_bf16 v[4:7], a[68:71], a[48:51], v[4:7]\n\t" \
  "s_add_u32 s96, s78, 7340032\n\t" \
  "s_and_b32 s96, s96, 0x780000\n\t" \
  "s_add_u32 m0, s76, 81920\n\t" \
  "v_mfma_f32_16x16x32_bf16 v[36:39], a[68:71], a[52:55], v[36:39]\n\t" \
  "buffer_load_dwordx4 v200, s[20:23], s96 offen offset:0 lds\n\t" \
  "v_mfma_f32_16x16x32_bf16 v[36:39], a[132:135], a[36:39], v[36:39]\n\t" \
  "ds_read_b64_tr_b16 a[170:171], v186 offset:1024\n\t" \
  "v_mfma_f32_16x16x32_bf16 v[68:71], a[132:135], a[40:43], v[68:71]\n\t" \
  "ds_read_b64_tr_b16 a[172:173], v187 offset:0\n\t" \
  "v_mfma_f32_16x16x32_bf16 v[68:71], a[68:71], a[56:59], v[68:71]\n\t" \
  "buffer_load_dwordx4 v201, s[20:23], s96 offen offset:1024 lds\n\t" \
  "v_mfma_f32_16x16x32_bf16 v[100:103], a[68:71], a[60:63], v[100:103]\n\t" \
  "ds_read_b64_tr_b16 a[174:175], v187 offset:1024\n\t" \
  "v_mfma_f32_16x16x32_bf16 v[100:103], a[132:135], a[44:47], v[100:103]\n\t" \
  "buffer_load_dwordx4 v202, s[20:23], s96 offen offset:2048 lds\n\t" \
  "v_mfma_f32_16x16x32_bf16 v[8:11], a[136:139], a[32:35], v[8:11]\n\t" \
  "ds_read_b64_tr_b16 a[176:177], v188 offset:0\n\t" \
  "v_mfma_f32_16x16x32_bf16 v[8:11], a[72:75], a[48:51], v[8:11]\n\t" \
  "ds_read_b64_tr_b16 a[178:179], v188 offset:1024\n\t" \
  "v_mfma_f32_16x16x32_bf16 v[40:43], a[72:75], a[52:55], v[40:43]\n\t" \
  "buffer_load_dwordx4 v203, s[20:23], s96 offen offset:3072 lds\n\t" \
  "v_mfma_f32_16x16x32_bf16 v[40:43], a[136:139], a[36:39], v[40:43]\n\t" \
  "ds_read_b64_tr_b16 a[180:181], v189 offset:0\n\t" \
  "s_add_u32 s97, s97, s83\n\t" \
  "v_mfma_f32_16x16x32_bf16 v[72:75], a[136:139], a[40:43], v[72:75]\n\t" \
  "buffer_load_dwordx4 a[16:19], v248, s[16:19], s97 offen sc1\n\t" \
  "v_mfma_f32_16x16x32_bf16 v[72:75], a[72:75], a[56:59], v[72:75]\n\t" \
  "ds_read_b64_tr_b16 a[182:183], v189 offset:1024\n\t" \
  "v_mfma_f32_16x16x32_bf16 v[104:107], a[72:75], a[60:63], v[104:107]\n\t" \
  "ds_read_b64_tr_b16 a[184:185], v190 offset:0\n\t" \
  "v_mfma_f32_16x16x32_bf16 v[104:107], a[136:139], a[44:47], v[104:107]\n\t" \
  "buffer_load_dwordx4 a[20:23], v249, s[16:19], s97 offen sc1\n\t" \
  "v_mfma_f32_16x16x32_bf16 v[12:15], a[140:143], a[32:35], v[12:15]\n\t" \
  "ds_read_b64_tr_b16 a[186:187], v190 offset:1024\n\t" \
  "v_mfma_f32_16x16x32_bf16 v[12:15], a[76:79], a[48:51], v[12:15]\n\t" \
  "buffer_load_dwordx4 a[24:27], v250, s[16:19], s97 offen sc1\n\t" \
  "v_mfma_f32_16x16x32_bf16 v[44:47], a[76:79], a[52:55], v[44:47]\n\t" \
  "ds_read_b64_tr_b16 a[188:189], v191 offset:0\n\t" \
  "v_mfma_f32_16x16x32_bf16 v[44:47], a[140:143], a[36:39], v[44:47]\n\t" \
  "ds_read_b64_tr_b16 a[190:191], v191 offset:1024\n\t" \
  "v_mfma_f32_16x16x32_bf16 v[76:79], a[140:143], a[40:43], v[76:79]\n\t" \
  "buffer_load_dwordx4 a[28:31], v251, s[16:19], s97 offen sc1\n\t" \
  "v_mfma_f32_16x16x32_bf16 v[76:79], a[76:79], a[56:59], v[76:79]\n\t" \
  "ds_read_b64_tr_b16 a[96:97], v184 offset:8192\n\t" \
  "s_mov_b32 s99, 49152\n\t" \
  "v_mfma_f32_16x16x32_bf16 v[108:111], a[76:79], a[60:63], v[108:111]\n\t" \
  "ds_read_b64_tr_b16 a[98:99], v184 offset:9216\n\t" \
  "v_mfma_f32_16x16x32_bf16 v[108:111], a[140:143], a[44:47], v[108:111]\n\t" \
  "ds_read_b64_tr_b16 a[100:101], v185 offset:8192\n\t" \
  "v_mfma_f32_16x16x32_bf16 v[16:19], a[144:147], a[32:35], v[16:19]\n\t" \
  "buffer_load_dwordx4 v[152:155], v209, s[8:11], s99 offen nt\n\t" \
  "v_mfma_f32_16x16x32_bf16 v[16:19], a[80:83], a[48:51], v[16:19]\n\t" \
  "ds_read_b64_tr_b16 a[102:103], v185 offset:9216\n\t" \
  "s_mov_b32 s99, 61440\n\t" \
  "v_mfma_f32_16x16x32_bf16 v[48:51], a[80:83], a[52:55], v[48:51]\n\t" \
  "ds_read_b64_tr_b16 a[104:105], v186 offset:8192\n\t" \
  "v_mfma_f32_16x16x32_bf16 v[48:51], a[144:147], a[36:39], v[48:51]\n\t" \
  "ds_read_b64_tr_b16 a[106:107], v186 offset:9216\n\t" \
  "v_mfma_f32_16x16x32_bf16 v[80:83], a[144:147], a[40:43], v[80:83]\n\t" \
  "ds_read_b64_tr_b16 a[108:109], v187 offset:8192\n\t" \
  "v_mfma_f32_16x16x32_bf16 v[80:83], a[80:83], a[56:59], v[80:83]\n\t" \
  "ds_read_b64_tr_b16 a[110:111], v187 offset:9216\n\t" \
  "v_mfma_f32_16x16x32_bf16 v[112:115], a[80:83], a[60:63], v[112:115]\n\t" \
  "buffer_load_dwordx4 v[156:159], v209, s[8:11], s99 offen nt\n\t" \
  "v_mfma_f32_16x16x32_bf16 v[112:115], a[144:147], a[44:47], v[112:115]\n\t" \
  "ds_read_b64_tr_b16 a[112:113], v188 offset:8192\n\t" \
  "s_mov_b32 s99, 73728\n\t" \
  "v_mfma_f32_16x16x32_bf16 v[20:23], a[148:151], a[32:35], v[20:23]\n\t" \
  "ds_read_b64_tr_b16 a[114:115], v188 offset:9216\n\t" \
  "v_mfma_f32_16x16x32_bf16 v[20:23], a[84:87], a[48:51], v[20:23]\n\t" \
  "ds_read_b64_tr_b16 a[116:117], v189 offset:8192\n\t" \
  "v_mfma_f32_16x16x32_bf16 v[52:55], a[84:87], a[52:55], v[52:55]\n\t" \
  "ds_read_b64_tr_b16 a[118:119], v189 offset:9216\n\t" \
  "v_mfma_f32_16x16x32_bf16 v[52:55], a[148:151], a[36:39], v[52:55]\n\t" \
  "ds_read_b64_tr_b16 a[120:121], v190 offset:8192\n\t" \
  "v_mfma_f32_16x16x32_bf16 v[84:87], a[148:151], a[40:43], v[84:87]\n\t" \
  "ds_read_b64_tr_b16 a[122:123], v190 offset:9216\n\t" \
  "v_mfma_f32_16x16x32_bf16 v[84:87], a[84:87], a[56:59], v[84:87]\n\t" \
  "ds_read_b64_tr_b16 a[124:125], v191 offset:8192\n\t" \
  "v_mfma_f32_16x16x32_bf16 v[116:119], a[84:87], a[60:63], v[116:119]\n\t" \
  "buffer_load_dwordx4 v[160:163], v209, s[8:11], s99 offen nt\n\t" \
  "v_mfma_f32_16x16x32_bf16 v[116:119], a[148:151], a[44:47], v[116:119]\n\t" \
  "ds_read_b64_tr_b16 a[126:127], v191 offset:9216\n\t" \
  "s_mov_b32 s99, 86016\n\t" \
  "v_mfma_f32_16x16x32_bf16 v[24:27], a[152:155], a[32:35], v[24:27]\n\t" \
  "v_mfma_f32_16x16x32_bf16 v[24:27], a[88:91], a[48:51], v[24:27]\n\t" \
  "v_mfma_f32_16x16x32_bf16 v[56:59], a[88:91], a[52:55], v[56:59]\n\t" \
  "v_mfma_f32_16x16x32_bf16 v[56:59], a[152:155], a[36:39], v[56:59]\n\t" \
  "v_mfma_f32_16x16x32_bf16 v[88:91], a[152:155], a[40:43], v[88:91]\n\t" \
  "v_mfma_f32_16x16x32_bf16 v[88:91], a[88:91], a[56:59], v[88:91]\n\t" \
  "buffer_load_dwordx4 v[164:167], v209, s[8:11], s99 offen nt\n\t" \
  "v_mfma_f32_16x16x32_bf16 v[120:123], a[88:91], a[60:63], v[120:123]\n\t" \
  "v_mfma_f32_16x16x32_bf16 v[120:123], a[152:155], a[44:47], v[120:123]\n\t" \
  "v_mfma_f32_16x16x32_bf16 v[28:31], a[156:159], a[32:35], v[28:31]\n\t" \
  "v_mfma_f32_16x16x32_bf16 v[28:31], a[92:95], a[48:51], v[28:31]\n\t" \
  "v_mfma_f32_16x16x32_bf16 v[60:63], a[92:95], a[52:55], v[60:63]\n\t" \
  "v_mfma_f32_16x16x32_bf16 v[60:63], a[156:159], a[36:39], v[60:63]\n\t" \
  "v_mfma_f32_16x16x32_bf16 v[92:95], a[156:159], a[40:43], v[92:95]\n\t" \
  "v_mfma_f32_16x16x32_bf16 v[92:95], a[92:95], a[56:59], v[92:95]\n\t" \
  "v_mfma_f32_16x16x32_bf16 v[124:127], a[92:95], a[60:63], v[124:127]\n\t" \
  "s_waitcnt vmcnt(32) lgkmcnt(0)\n\t" \
  "v_mfma_f32_16x16x32_bf16 v[124:127], a[156:159], a[44:47], v[124:127]\n\t" \
  "s_barrier\n\t" \
  "v_mfma_f32_16x16x32_bf16 v[0:3], a[160:163], a[192:195], v[0:3]\n\t" \
  "ds_read_b64_tr_b16 a[128:129], v184 offset:16384\n\t" \
  "v_mfma_f32_16x16x32_bf16 v[0:3], a[96:99], a[208:211], v[0:3]\n\t" \
  "ds_read_b64_tr_b16 a[130:131], v184 offset:17408\n\t" \
  "v_mfma_f32_16x16x32_bf16 v[32:35], a[96:99], a[212:215], v[32:35]\n\t" \
  "ds_read_b64_tr_b16 a[132:133], v185 offset:16384\n\t" \
  "v_mfma_f32_16x16x32_bf16 v[32:35], a[160:163], a[196:199], v[32:35]\n\t" \
  "ds_read_b64_tr_b16 a[134:135], v185 offset:17408\n\t" \
  "v_mfma_f32_16x16x32_bf16 v[64:67], a[160:163], a[200:203], v[64:67]\n\t" \
  "ds_read_b64_tr_b16 a[136:137], v186 offset:16384\n\t" \
  "v_mfma_f32_16x16x32_bf16 v[64:67], a[96:99], a[216:219], v[64:67]\n\t" \
  "ds_read_b64_tr_b16 a[138:139], v186 offset:17408\n\t" \
  "v_mfma_f32_16x16x32_bf16 v[96:99], a[96:99], a[220:223], v[96:99]\n\t" \
  "ds_read_b64_tr_b16 a[140:141], v187 offset:16384\n\t" \
  "v_mfma_f32_16x16x32_bf16 v[96:99], a[160:163], a[204:207], v[96:99]\n\t" \
  "ds_read_b64_tr_b16 a[142:143], v187 offset:17408\n\t" \
  "v_mfma_f32_16x16x32_bf16 v[4:7], a[164:167], a[192:195], v[4:7]\n\t" \
  "ds_read_b64_tr_b16 a[144:145], v188 offset:16384\n\t" \
  "v_mfma_f32_16x16x32_bf16 v[4:7], a[100:103], a[208:211], v[4:7]\n\t" \
  "ds_read_b64_tr_b16 a[146:147], v188 offset:17408\n\t" \
  "v_mfma_f32_16x16x32_bf16 v[36:39], a[100:103], a[212:215], v[36:39]\n\t" \
  "ds_read_b64_tr_b16 a[148:149], v189 offset:16384\n\t" \
  "v_mfma_f32_16x16x32_bf16 v[36:39], a[164:167], a[196:199], v[36:39]\n\t" \
  "ds_read_b64_tr_b16 a[150:151], v189 offset:17408\n\t" \
  "v_mfma_f32_16x16x32_bf16 v[68:71], a[164:167], a[200:203], v[68:71]\n\t" \
  "ds_read_b64_tr_b16 a[152:153], v190 offset:16384\n\t" \
  "v_mfma_f32_16x16x32_bf16 v[68:71], a[100:103], a[216:219], v[68:71]\n\t" \
  "ds_read_b64_tr_b16 a[154:155], v190 offset:17408\n\t" \
  "v_mfma_f32_16x16x32_bf16 v[100:103], a[100:103], a[220:223], v[100:103]\n\t" \
  "ds_read_b64_tr_b16 a[156:157], v191 offset:16384\n\t" \
  "v_mfma_f32_16x16x32_bf16 v[100:103], a[164:167], a[204:207], v[100:103]\n\t" \
  "ds_read_b64_tr_b16 a[158:159], v191 offset:17408\n\t" \
  "v_mfma_f32_16x16x32_bf16 v[8:11], a[168:171], a[192:195], v[8:11]\n\t" \
  "ds_read_b64_tr_b16 a[64:65], v184 offset:24576\n\t" \
  "v_mfma_f32_16x16x32_bf16 v[8:11], a[104:107], a[208:211], v[8:11]\n\t" \
  "v_mfma_f32_16x16x32_bf16 v[40:43], a[104:107], a[212:215], v[40:43]\n\t" \
  "ds_read_b64_tr_b16 a[66:67], v184 offset:25600\n\t" \
  "v_mfma_f32_16x16x32_bf16 v[40:43], a[168:171], a[196:199], v[40:43]\n\t" \
  "v_mfma_f32_16x16x32_bf16 v[72:75], a[168:171], a[200:203], v[72:75]\n\t" \
  "ds_read_b64_tr_b16 a[68:69], v185 offset:24576\n\t" \
  "v_mfma_f32_16x16x32_bf16 v[72:75], a[104:107], a[216:219], v[72:75]\n\t" \
  "ds_read_b64_tr_b16 a[70:71], v185 offset:25600\n\t" \
  "v_mfma_f32_16x16x32_bf16 v[104:107], a[104:107], a[220:223], v[104:107]\n\t" \
  "v_mfma_f32_16x16x32_bf16 v[104:107], a[168:171], a[204:207], v[104:107]\n\t" \
  "ds_read_b64_tr_b16 a[72:73], v186 offset:24576\n\t" \
  "v_mfma_f32_16x16x32_bf16 v[12:15], a[172:175], a[192:195], v[12:15]\n\t" \
  "v_mfma_f32_16x16x32_bf16 v[12:15], a[108:111], a[208:211], v[12:15]\n\t" \
  "ds_read_b64_tr_b16 a[74:75], v186 offset:25600\n\t" \
  "v_mfma_f32_16x16x32_bf16 v[44:47], a[108:111], a[212:215], v[44:47]\n\t" \
  "ds_read_b64_tr_b16 a[76:77], v187 offset:24576\n\t" \
  "v_mfma_f32_16x16x32_bf16 v[44:47], a[172:175], a[196:199], v[44:47]\n\t" \
  ".Lhs4:\n\ts_cbranch_execz .Lhs4n\n\t" \
  "v_mfma_f32_16x16x32_bf16 v[76:79], a[172:175], a[200:203], v[76:79]\n\t" \
  "ds_read_b64_tr_b16 a[78:79], v187 offset:25600\n\t" \
  "v_mfma_f32_16x16x32_bf16 v[76:79], a[108:111], a[216:219], v[76:79]\n\t" \
  "s_add_u32 s97, s78, 6815744\n\t" \
  "ds_read_b64_tr_b16 a[80:81], v188 offset:24576\n\t" \
  "s_bfe_u32 s97, s97, 0x140003\n\t" \
  "v_mfma_f32_16x16x32_bf16 v[108:111], a[108:111], a[220:223], v[108:111]\n\t" \
  "s_add_u32 s97, s97, s82\n\t" \
  "v_mfma_f32_16x16x32_bf16 v[108:111], a[172:175], a[204:207], v[108:111]\n\t" \
  "buffer_load_dwordx4 a[32:35], v248, s[16:19], s97 offen sc1\n\t" \
  "v_mfma_f32_16x16x32_bf16 v[16:19], a[176:179], a[192:195], v[16:19]\n\t" \
  "ds_read_b64_tr_b16 a[82:83], v188 offset:25600\n\t" \
  "v_mfma_f32_16x16x32_bf16 v[16:19], a[112:115], a[208:211], v[16:19]\n\t" \
  "ds_read_b64_tr_b16 a[84:85], v189 offset:24576\n\t" \
  "v_mfma_f32_16x16x32_bf16 v[48:51], a[112:115], a[212:215], v[48:51]\n\t" \
  "buffer_load_dwordx4 a[36:39], v249, s[16:19], s97 offen sc1\n\t" \
  "v_mfma_f32_16x16x32_bf16 v[48:51], a[176:179], a[196:199], v[48:51]\n\t" \
  "ds_read_b64_tr_b16 a[86:87], v189 offset:25600\n\t" \
  "v_mfma_f32_16x16x32_bf16 v[80:83], a[176:179], a[200:203], v[80:83]\n\t" \
  "ds_read_b64_tr_b16 a[88:89], v190 offset:24576\n\t" \
  "v_mfma_f32_16x16x32_bf16 v[80:83], a[112:115], a[216:219], v[80:83]\n\t" \
  "v_mfma_f32_16x16x32_bf16 v[112:115], a[112:115], a[220:223], v[112:115]\n\t" \
  "buffer_load_dwordx4 a[40:43], v250, s[16:19], s97 offen sc1\n\t" \
  "v_mfma_f32_16x16x32_bf16 v[112:115], a[176:179], a[204:207], v[112:115]\n\t" \
  "ds_read_b64_tr_b16 a[90:91], v190 offset:25600\n\t" \
  "v_mfma_f32_16x16x32_bf16 v[20:23], a[180:183], a[192:195], v[20:23]\n\t" \
  "ds_read_b64_tr_b16 a[92:93], v191 offset:24576\n\t" \
  "v_mfma_f32_16x16x32_bf16 v[20:23], a[116:119], a[208:211], v[20:23]\n\t" \
  "ds_read_b64_tr_b16 a[94:95], v191 offset:25600\n\t" \
  "v_mfma_f32_16x16x32_bf16 v[52:55], a[116:119], a[212:215], v[52:55]\n\t" \
  "buffer_load_dwordx4 a[44:47], v251, s[16:19], s97 offen sc1\n\t" \
  "v_mfma_f32_16x16x32_bf16 v[52:55], a[180:183], a[196:199], v[52:55]\n\t" \
  "s_add_u32 s96, s78, 7864320\n\t" \
  "s_and_b32 s96, s96, 0x780000\n\t" \
  "s_add_u32 m0, s76, 98304\n\t" \
  "v_mfma_f32_16x16x32_bf16 v[84:87], a[180:183], a[200:203], v[84:87]\n\t" \
  "v_mfma_f32_16x16x32_bf16 v[84:87], a[116:119], a[216:219], v[84:87]\n\t" \
  "v_mfma_f32_16x16x32_bf16 v[116:119], a[116:119], a[220:223], v[116:119]\n\t" \
  "buffer_load_dwordx4 v200, s[20:23], s96 offen offset:0 lds\n\t" \
  "v_mfma_f32_16x16x32_bf16 v[116:119], a[180:183], a[204:207], v[116:119]\n\t" \
  "v_mfma_f32_16x16x32_bf16 v[24:27], a[184:187], a[192:195], v[24:27]\n\t" \
  "v_mfma_f32_16x16x32_bf16 v[24:27], a[120:123], a[208:211], v[24:27]\n\t" \
  "buffer_load_dwordx4 v201, s[20:23], s96 offen offset:1024 lds\n\t" \
  "v_mfma_f32_16x16x32_bf16 v[56:59], a[120:123], a[212:215], v[56:59]\n\t" \
  "v_mfma_f32_16x16x32_bf16 v[56:59], a[184:187], a[196:199], v[56:59]\n\t" \
  "v_mfma_f32_16x16x32_bf16 v[88:91], a[184:187], a[200:203], v[88:91]\n\t" \
  "v_mfma_f32_16x16x32_bf16 v[88:91], a[120:123], a[216:219], v[88:91]\n\t" \
  "buffer_load_dwordx4 v202, s[20:23], s96 offen offset:2048 lds\n\t" \
  "v_mfma_f32_16x16x32_bf16 v[120:123], a[120:123], a[220:223], v[120:123]\n\t" \
  "v_mfma_f32_16x16x32_bf16 v[120:123], a[184:187], a[204:207], v[120:123]\n\t" \
  "v_mfma_f32_16x16x32_bf16 v[28:31], a[188:191], a[192:195], v[28:31]\n\t" \
  "v_mfma_f32_16x16x32_bf16 v[28:31], a[124:127], a[208:211], v[28:31]\n\t" \
  "buffer_load_dwordx4 v203, s[20:23], s96 offen offset:3072 lds\n\t" \
  "v_mfma_f32_16x16x32_bf16 v[60:63], a[124:127], a[212:215], v[60:63]\n\t" \
  "v_mfma_f32_16x16x32_bf16 v[60:63], a[188:191], a[196:199], v[60:63]\n\t" \
  "v_mfma_f32_16x16x32_bf16 v[92:95], a[188:191], a[200:203], v[92:95]\n\t" \
  "v_mfma_f32_16x16x32_bf16 v[92:95], a[124:127], a[216:219], v[92:95]\n\t" \
  "s_add_u32 s97, s97, s83\n\t" \
  "v_mfma_f32_16x16x32_bf16 v[124:127], a[124:127], a[220:223], v[124:127]\n\t" \
  "s_waitcnt vmcnt(24) lgkmcnt(0)\n\t" \
  "v_mfma_f32_16x16x32_bf16 v[124:127], a[188:191], a[204:207], v[124:127]\n\t" \
  "s_barrier\n\t" \
  "v_mfma_f32_16x16x32_bf16 v[0:3], a[128:131], a[224:227], v[0:3]\n\t" \
  "buffer_load_dwordx4 a[48:51], v248, s[16:19], s97 offen sc1\n\t" \
  "v_mfma_f32_16x16x32_bf16 v[0:3], a[64:67], a[240:243], v[0:3]\n\t" \
  "ds_read_b64_tr_b16 a[160:161], v184 offset:32768\n\t" \
  "v_mfma_f32_16x16x32_bf16 v[32:35], a[64:67], a[244:247], v[32:35]\n\t" \
  "ds_read_b64_tr_b16 a[162:163], v184 offset:33792\n\t" \
  "v_mfma_f32_16x16x32_bf16 v[32:35], a[128:131], a[228:231], v[32:35]\n\t" \
  "buffer_load_dwordx4 a[52:55], v249, s[16:19], s97 offen sc1\n\t" \
  "v_mfma_f32_16x16x32_bf16 v[64:67], a[128:131], a[232:235], v[64:67]\n\t" \
  "ds_read_b64_tr_b16 a[164:165], v185 offset:32768\n\t" \
  "v_mfma_f32_16x16x32_bf16 v[64:67], a[64:67], a[248:251], v[64:67]\n\t" \
  "ds_read_b64_tr_b16 a[166:167], v185 offset:33792\n\t" \
  "v_mfma_f32_16x16x32_bf16 v[96:99], a[64:67], a[252:255], v[96:99]\n\t" \
  "buffer_load_dwordx4 a[56:59], v250, s[16:19], s97 offen sc1\n\t" \
  "v_mfma_f32_16x16x32_bf16 v[96:99], a[128:131], a[236:239], v[96:99]\n\t" \
  "ds_read_b64_tr_b16 a[168:169], v186 offset:32768\n\t" \
  "v_mfma_f32_16x16x32_bf16 v[4:7], a[132:135], a[224:227], v[4:7]\n\t" \
  "ds_read_b64_tr_b16 a[170:171], v186 offset:33792\n\t" \
  "v_mfma_f32_16x16x32_bf16 v[4:7], a[68:71], a[240:243], v[4:7]\n\t" \
  "ds_read_b64_tr_b16 a[172:173], v187 offset:32768\n\t" \
  "v_mfma_f32_16x16x32_bf16 v[36:39], a[68:71], a[244:247], v[36:39]\n\t" \
  "buffer_load_dwordx4 a[60:63], v251, s[16:19], s97 offen sc1\n\t" \
  "v_mfma_f32_16x16x32_bf16 v[36:39], a[132:135], a[228:231], v[36:39]\n\t" \
  "s_add_u32 s97, s78, 7340032\n\t" \
  "ds_read_b64_tr_b16 a[174:175], v187 offset:33792\n\t" \
  "s_bfe_u32 s97, s97, 0x140003\n\t" \
  "v_mfma_f32_16x16x32_bf16 v[68:71], a[132:135], a[232:235], v[68:71]\n\t" \
  "ds_read_b64_tr_b16 a[176:177], v188 offset:32768\n\t" \
  "s_add_u32 s97, s97, s82\n\t" \
  "v_mfma_f32_16x16x32_bf16 v[68:71], a[68:71], a[248:251], v[68:71]\n\t" \
  "ds_read_b64_tr_b16 a[178:179], v188 offset:33792\n\t" \
  "v_mfma_f32_16x16x32_bf16 v[100:103], a[68:71], a[252:255], v[100:103]\n\t" \
  "buffer_load_dwordx4 a[192:195], v248, s[16:19], s97 offen sc1\n\t" \
  "v_mfma_f32_16x16x32_bf16 v[100:103], a[132:135], a[236:239], v[100:103]\n\t" \
  "ds_read_b64_tr_b16 a[180:181], v189 offset:32768\n\t" \
  "v_mfma_f32_16x16x32_bf16 v[8:11], a[136:139], a[224:227], v[8:11]\n\t" \
  "ds_read_b64_tr_b16 a[182:183], v189 offset:33792\n\t" \
  "v_mfma_f32_16x16x32_bf16 v[8:11], a[72:75], a[240:243], v[8:11]\n\t" \
  "buffer_load_dwordx4 a[196:199], v249, s[16:19], s97 offen sc1\n\t" \
  "v_mfma_f32_16x16x32_bf16 v[40:43], a[72:75], a[244:247], v[40:43]\n\t" \
  "ds_read_b64_tr_b16 a[184:185], v190 offset:32768\n\t" \
  "v_mfma_f32_16x16x32_bf16 v[40:43], a[136:139], a[228:231], v[40:43]\n\t" \
  "ds_read_b64_tr_b16 a[186:187], v190 offset:33792\n\t" \
  "v_mfma_f32_16x16x32_bf16 v[72:75], a[136:139], a[232:235], v[72:75]\n\t" \
  "ds_read_b64_tr_b16 a[188:189], v191 offset:32768\n\t" \
  "v_mfma_f32_16x16x32_bf16 v[72:75], a[72:75], a[248:251], v[72:75]\n\t" \
  "buffer_load_dwordx4 a[200:203], v250, s[16:19], s97 offen sc1\n\t" \
  "v_mfma_f32_16x16x32_bf16 v[104:107], a[72:75], a[252:255], v[104:107]\n\t" \
  "ds_read_b64_tr_b16 a[190:191], v191 offset:33792\n\t" \
  "v_mfma_f32_16x16x32_bf16 v[104:107], a[136:139], a[236:239], v[104:107]\n\t" \
  "ds_read_b64_tr_b16 a[96:97], v184 offset:40960\n\t" \
  "v_mfma_f32_16x16x32_bf16 v[12:15], a[140:143], a[224:227], v[12:15]\n\t" \
  "ds_read_b64_tr_b16 a[98:99], v184 offset:41984\n\t" \
  "v_mfma_f32_16x16x32_bf16 v[12:15], a[76:79], a[240:243], v[12:15]\n\t" \
  "buffer_load_dwordx4 a[204:207], v251, s[16:19], s97 offen sc1\n\t" \
  "v_mfma_f32_16x16x32_bf16 v[44:47], a[76:79], a[244:247], v[44:47]\n\t" \
  "ds_read_b64_tr_b16 a[100:101], v185 offset:40960\n\t" \
  "v_mfma_f32_16x16x32_bf16 v[44:47], a[140:143], a[228:231], v[44:47]\n\t" \
  "ds_read_b64_tr_b16 a[102:103], v185 offset:41984\n\t" \
  "v_mfma_f32_16x16x32_bf16 v[76:79], a[140:143], a[232:235], v[76:79]\n\t" \
  "ds_read_b64_tr_b16 a[104:105], v186 offset:40960\n\t" \
  "v_mfma_f32_16x16x32_bf16 v[76:79], a[76:79], a[248:251], v[76:79]\n\t" \
  "ds_read_b64_tr_b16 a[106:107], v186 offset:41984\n\t" \
  "s_add_u32 s97, s97, s83\n\t" \
  "v_mfma_f32_16x16x32_bf16 v[108:111], a[76:79], a[252:255], v[108:111]\n\t" \
  "ds_read_b64_tr_b16 a[108:109], v187 offset:40960\n\t" \
  "v_mfma_f32_16x16x32_bf16 v[108:111], a[140:143], a[236:239], v[108:111]\n\t" \
  "buffer_load_dwordx4 a[208:211], v248, s[16:19], s97 offen sc1\n\t" \
  "v_mfma_f32_16x16x32_bf16 v[16:19], a[144:147], a[224:227], v[16:19]\n\t" \
  "ds_read_b64_tr_b16 a[110:111], v187 offset:41984\n\t" \
  "v_mfma_f32_16x16x32_bf16 v[16:19], a[80:83], a[240:243], v[16:19]\n\t" \
  "ds_read_b64_tr_b16 a[112:113], v188 offset:40960\n\t" \
  "v_mfma_f32_16x16x32_bf16 v[48:51], a[80:83], a[244:247], v[48:51]\n\t" \
  "s_waitcnt vmcnt(20)\n\t" \
  "v_lshlrev_b32 v128, 16, v152\n\t" \
  "v_and_b32 v152, 0xffff0000, v152\n\t" \
  "v_mfma_f32_16x16x32_bf16 v[48:51], a[144:147], a[228:231], v[48:51]\n\t" \
  "v_fma_f32 v176, v128, v172, v176\n\t" \
  "v_fma_f32 v180, v152, v172, v180\n\t" \
  "v_mfma_f32_16x16x32_bf16 v[80:83], a[144:147], a[232:235], v[80:83]\n\t" \
  "v_lshlrev_b32 v128, 16, v153\n\t" \
  "v_and_b32 v153, 0xffff0000, v153\n\t" \
  "v_mfma_f32_16x16x32_bf16 v[80:83], a[80:83], a[248:251], v[80:83]\n\t" \
  "v_fma_f32 v177, v128, v172, v177\n\t" \
  "v_fma_f32 v181, v153, v172, v181\n\t" \
  "v_mfma_f32_16x16x32_bf16 v[112:115], a[80:83], a[252:255], v[112:115]\n\t" \
  "v_lshlrev_b32 v128, 16, v154\n\t" \
  "v_and_b32 v154, 0xffff0000, v154\n\t" \
  "v_mfma_f32_16x16x32_bf16 v[112:115], a[144:147], a[236:239], v[112:115]\n\t" \
  "v_fma_f32 v178, v128, v172, v178\n\t" \
  "v_fma_f32 v182, v154, v172, v182\n\t" \
  "v_mfma_f32_16x16x32_bf16 v[20:23], a[148:151], a[224:227], v[20:23]\n\t" \
  "buffer_load_dwordx4 a[212:215], v249, s[16:19], s97 offen sc1\n\t" \
  "v_mfma_f32_16x16x32_bf16 v[20:23], a[84:87], a[240:243], v[20:23]\n\t" \
  "ds_read_b64_tr_b16 a[114:115], v188 offset:41984\n\t" \
  "v_lshlrev_b32 v128, 16, v155\n\t" \
  "v_mfma_f32_16x16x32_bf16 v[52:55], a[84:87], a[244:247], v[52:55]\n\t" \
  "v_and_b32 v155, 0xffff0000, v155\n\t" \
  "v_fma_f32 v179, v128, v172, v179\n\t" \
  "v_mfma_f32_16x16x32_bf16 v[52:55], a[148:151], a[228:231], v[52:55]\n\t" \
  "ds_read_b64_tr_b16 a[116:117], v189 offset:40960\n\t" \
  "v_fma_f32 v183, v155, v172, v183\n\t" \
  "v_mfma_f32_16x16x32_bf16 v[84:87], a[148:151], a[232:235], v[84:87]\n\t" \
  "s_waitcnt vmcnt(20)\n\t" \
  "v_lshlrev_b32 v128, 16, v156\n\t" \
  "v_and_b32 v156, 0xffff0000, v156\n\t" \
  "v_mfma_f32_16x16x32_bf16 v[84:87], a[84:87], a[248:251], v[84:87]\n\t" \
  "ds_read_b64_tr_b16 a[118:119], v189 offset:41984\n\t" \
  "v_fma_f32 v176, v128, v173, v176\n\t" \
  "v_mfma_f32_16x16x32_bf16 v[116:119], a[84:87], a[252:255], v[116:119]\n\t" \
  "ds_read_b64_tr_b16 a[120:121], v190 offset:40960\n\t" \
  "v_fma_f32 v180, v156, v173, v180\n\t" \
  "v_mfma_f32_16x16x32_bf16 v[116:119], a[148:151], a[236:239], v[116:119]\n\t" \
  "buffer_load_dwordx4 a[216:219], v250, s[16:19], s97 offen sc1\n\t" \
  "v_mfma_f32_16x16x32_bf16 v[24:27], a[152:155], a[224:227], v[24:27]\n\t" \
  "ds_read_b64_tr_b16 a[122:123], v190 offset:41984\n\t" \
  "v_lshlrev_b32 v128, 16, v157\n\t" \
  "v_mfma_f32_16x16x32_bf16 v[24:27], a[88:91], a[240:243], v[24:27]\n\t" \
  "ds_read_b64_tr_b16 a[124:125], v191 offset:40960\n\t" \
  "v_and_b32 v157, 0xffff0000, v157\n\t" \
  "v_mfma_f32_16x16x32_bf16 v[56:59], a[88:91], a[244:247], v[56:59]\n\t" \
  "v_fma_f32 v177, v128, v173, v177\n\t" \
  "v_fma_f32 v181, v157, v173, v181\n\t" \
  "v_mfma_f32_16x16x32_bf16 v[56:59], a[152:155], a[228:231], v[56:59]\n\t" \
  "ds_read_b64_tr_b16 a[126:127], v191 offset:41984\n\t" \
  "v_lshlrev_b32 v128, 16, v158\n\t" \
  "v_mfma_f32_16x16x32_bf16 v[88:91], a[152:155], a[232:235], v[88:91]\n\t" \
  "v_and_b32 v158, 0xffff0000, v158\n\t" \
  "v_fma_f32 v178, v128, v173, v178\n\t" \
  "v_mfma_f32_16x16x32_bf16 v[88:91], a[88:91], a[248:251], v[88:91]\n\t" \
  "v_fma_f32 v182, v158, v173, v182\n\t" \
  "v_lshlrev_b32 v128, 16, v159\n\t" \
  "v_mfma_f32_16x16x32_bf16 v[120:123], a[88:91], a[252:255], v[120:123]\n\t" \
  "v_and_b32 v159, 0xffff0000, v159\n\t" \
  "v_fma_f32 v179, v128, v173, v179\n\t" \
  "v_mfma_f32_16x16x32_bf16 v[120:123], a[152:155], a[236:239], v[120:123]\n\t" \
  "buffer_load_dwordx4 a[220:223], v251, s[16:19], s97 offen sc1\n\t" \
  "v_mfma_f32_16x16x32_bf16 v[28:31], a[156:159], a[224:227], v[28:31]\n\t" \
  "v_fma_f32 v183, v159, v173, v183\n\t" \
  "v_mfma_f32_16x16x32_bf16 v[28:31], a[92:95], a[240:243], v[28:31]\n\t" \
  "s_waitcnt vmcnt(21)\n\t" \
  "v_lshlrev_b32 v128, 16, v160\n\t" \
  "v_and_b32 v160, 0xffff0000, v160\n\t" \
  "v_mfma_f32_16x16x32_bf16 v[60:63], a[92:95], a[244:247], v[60:63]\n\t" \
  "v_fma_f32 v176, v128, v174, v176\n\t" \
  "v_fma_f32 v180, v160, v174, v180\n\t" \
  "v_mfma_f32_16x16x32_bf16 v[60:63], a[156:159], a[228:231], v[60:63]\n\t" \
  "v_lshlrev_b32 v128, 16, v161\n\t" \
  "v_and_b32 v161, 0xffff0000, v161\n\t" \
  "v_mfma_f32_16x16x32_bf16 v[92:95], a[156:159], a[232:235], v[92:95]\n\t" \
  "v_fma_f32 v177, v128, v174, v177\n\t" \
  "v_fma_f32 v181, v161, v174, v181\n\t" \
  "v_mfma_f32_16x16x32_bf16 v[92:95], a[92:95], a[248:251], v[92:95]\n\t" \
  "s_add_u32 s97, s78, 7864320\n\t" \
  "v_lshlrev_b32 v128, 16, v162\n\t" \
  "v_and_b32 v162, 0xffff0000, v162\n\t" \
  "v_mfma_f32_16x16x32_bf16 v[124:127], a[92:95], a[252:255], v[124:127]\n\t" \
  "s_waitcnt lgkmcnt(0)\n\t" \
  "s_bfe_u32 s97, s97, 0x140003\n\t" \
  "s_add_u32 s97, s97, s82\n\t" \
  "v_mfma_f32_16x16x32_bf16 v[124:127], a[156:159], a[236:239], v[124:127]\n\t" \
  "s_barrier\n\t" \
  "v_mfma_f32_16x16x32_bf16 v[0:3], a[160:163], a[0:3], v[0:3]\n\t" \
  "buffer_load_dwordx4 a[224:227], v248, s[16:19], s97 offen sc1\n\t" \
  "v_mfma_f32_16x16x32_bf16 v[0:3], a[96:99], a[16:19], v[0:3]\n\t" \
  "ds_read_b64_tr_b16 a[128:129], v192 offset:0\n\t" \
  "v_fma_f32 v178, v128, v174, v178\n\t" \
  "v_mfma_f32_16x16x32_bf16 v[32:35], a[96:99], a[20:23], v[32:35]\n\t" \
  "ds_read_b64_tr_b16 a[130:131], v192 offset:1024\n\t" \
  "v_fma_f32 v182, v162, v174, v182\n\t" \
  "v_mfma_f32_16x16x32_bf16 v[32:35], a[160:163], a[4:7], v[32:35]\n\t" \
  "v_lshlrev_b32 v128, 16, v163\n\t" \
  "v_and_b32 v163, 0xffff0000, v163\n\t" \
  "v_mfma_f32_16x16x32_bf16 v[64:67], a[160:163], a[8:11], v[64:67]\n\t" \
  "ds_read_b64_tr_b16 a[132:133], v193 offset:0\n\t" \
  "v_fma_f32 v179, v128, v174, v179\n\t" \
  "v_mfma_f32_16x16x32_bf16 v[64:67], a[96:99], a[24:27], v[64:67]\n\t" \
  "ds_read_b64_tr_b16 a[134:135], v193 offset:1024\n\t" \
  "v_fma_f32 v183, v163, v174, v183\n\t" \
  "v_mfma_f32_16x16x32_bf16 v[96:99], a[96:99], a[28:31], v[96:99]\n\t" \
  "s_waitcnt vmcnt(21)\n\t" \
  "v_lshlrev_b32 v128, 16, v164\n\t" \
  "v_and_b32 v164, 0xffff0000, v164\n\t" \
  "v_mfma_f32_16x16x32_bf16 v[96:99], a[160:163], a[12:15], v[96:99]\n\t" \
  "ds_read_b64_tr_b16 a[136:137], v194 offset:0\n\t" \
  "v_fma_f32 v176, v128, v175, v176\n\t" \
  "v_mfma_f32_16x16x32_bf16 v[4:7], a[164:167], a[0:3], v[4:7]\n\t" \
  "buffer_load_dwordx4 a[228:231], v249, s[16:19], s97 offen sc1\n\t" \
  "v_mfma_f32_16x16x32_bf16 v[4:7], a[100:103], a[16:19], v[4:7]\n\t" \
  "ds_read_b64_tr_b16 a[138:139], v194 offset:1024\n\t" \
  "v_fma_f32 v180, v164, v175, v180\n\t" \
  "v_mfma_f32_16x16x32_bf16 v[36:39], a[100:103], a[20:23], v[36:39]\n\t" \
  "ds_read_b64_tr_b16 a[140:141], v195 offset:0\n\t" \
  "v_lshlrev_b32 v128, 16, v165\n\t" \
  "v_mfma_f32_16x16x32_bf16 v[36:39], a[164:167], a[4:7], v[36:39]\n\t" \
  "v_and_b32 v165, 0xffff0000, v165\n\t" \
  "v_fma_f32 v177, v128, v175, v177\n\t" \
  "v_mfma_f32_16x16x32_bf16 v[68:71], a[164:167], a[8:11], v[68:71]\n\t" \
  "ds_read_b64_tr_b16 a[142:143], v195 offset:1024\n\t" \
  "v_fma_f32 v181, v165, v175, v181\n\t" \
  "v_mfma_f32_16x16x32_bf16 v[68:71], a[100:103], a[24:27], v[68:71]\n\t" \
  "ds_read_b64_tr_b16 a[144:145], v196 offset:0\n\t" \
  "v_lshlrev_b32 v128, 16, v166\n\t" \
  "v_mfma_f32_16x16x32_bf16 v[100:103], a[100:103], a[28:31], v[100:103]\n\t" \
  "v_and_b32 v166, 0xffff0000, v166\n\t" \
  "v_fma_f32 v178, v128, v175, v178\n\t" \
  "v_mfma_f32_16x16x32_bf16 v[100:103], a[164:167], a[12:15], v[100:103]\n\t" \
  "buffer_load_dwordx4 a[232:235], v250, s[16:19], s97 offen sc1\n\t" \
  "v_mfma_f32_16x16x32_bf16 v[8:11], a[168:171], a[0:3], v[8:11]\n\t" \
  "ds_read_b64_tr_b16 a[146:147], v196 offset:1024\n\t" \
  "v_fma_f32 v182, v166, v175, v182\n\t" \
  "v_mfma_f32_16x16x32_bf16 v[8:11], a[104:107], a[16:19], v[8:11]\n\t" \
  "ds_read_b64_tr_b16 a[148:149], v197 offset:0\n\t" \
  "v_lshlrev_b32 v128, 16, v167\n\t" \
  "v_mfma_f32_16x16x32_bf16 v[40:43], a[104:107], a[20:23], v[40:43]\n\t" \
  "ds_read_b64_tr_b16 a[150:151], v197 offset:1024\n\t" \
  "v_and_b32 v167, 0xffff0000, v167\n\t" \
  "v_mfma_f32_16x16x32_bf16 v[40:43], a[168:171], a[4:7], v[40:43]\n\t" \
  "v_fma_f32 v179, v128, v175, v179\n\t" \
  "v_fma_f32 v183, v167, v175, v183\n\t" \
  "v_mfma_f32_16x16x32_bf16 v[72:75], a[168:171], a[8:11], v[72:75]\n\t" \
  "ds_read_b64_tr_b16 a[152:153], v198 offset:0\n\t" \
  "v_and_b32 v138, 7, v233\n\t" \
  "v_mfma_f32_16x16x32_bf16 v[72:75], a[104:107], a[24:27], v[72:75]\n\t" \
  "ds_read_b64_tr_b16 a[154:155], v198 offset:1024\n\t" \
  "v_lshlrev_b32 v138, 5, v138\n\t" \
  "v_mfma_f32_16x16x32_bf16 v[104:107], a[104:107], a[28:31], v[104:107]\n\t" \
  "v_permlane32_swap_b32 v176, v178\n\t" \
  "v_mfma_f32_16x16x32_bf16 v[104:107], a[168:171], a[12:15], v[104:107]\n\t" \
  "buffer_load_dwordx4 a[236:239], v251, s[16:19], s97 offen sc1\n\t" \
  "v_mfma_f32_16x16x32_bf16 v[12:15], a[172:175], a[0:3], v[12:15]\n\t" \
  "v_permlane32_swap_b32 v180, v182\n\t" \
  "v_mfma_f32_16x16x32_bf16 v[12:15], a[108:111], a[16:19], v[12:15]\n\t" \
  "ds_read_b64_tr_b16 a[156:157], v199 offset:0\n\t" \
  "v_mfma_f32_16x16x32_bf16 v[44:47], a[108:111], a[20:23], v[44:47]\n\t" \
  "ds_read_b64_tr_b16 a[158:159], v199 offset:1024\n\t" \
  "v_mfma_f32_16x16x32_bf16 v[44:47], a[172:175], a[4:7], v[44:47]\n\t" \
  "v_permlane32_swap_b32 v177, v179\n\t" \
  "v_mfma_f32_16x16x32_bf16 v[76:79], a[172:175], a[8:11], v[76:79]\n\t" \
  "ds_read_b64_tr_b16 a[64:65], v192 offset:8192\n\t" \
  "v_mfma_f32_16x16x32_bf16 v[76:79], a[108:111], a[24:27], v[76:79]\n\t" \
  "s_add_u32 s97, s97, s83\n\t" \
  "v_permlane32_swap_b32 v181, v183\n\t" \
  "v_mfma_f32_16x16x32_bf16 v[108:111], a[108:111], a[28:31], v[108:111]\n\t" \
  "ds_read_b64_tr_b16 a[66:67], v192 offset:9216\n\t" \
  "v_add_f32 v176, v176, v178\n\t" \
  "v_mfma_f32_16x16x32_bf16 v[108:111], a[172:175], a[12:15], v[108:111]\n\t" \
  "ds_read_b64_tr_b16 a[68:69], v193 offset:8192\n\t" \
  "v_add_f32 v177, v177, v179\n\t" \
  "v_mfma_f32_16x16x32_bf16 v[16:19], a[176:179], a[0:3], v[16:19]\n\t" \
  "buffer_load_dwordx4 a[240:243], v248, s[16:19], s97 offen sc1\n\t" \
  "v_mfma_f32_16x16x32_bf16 v[16:19], a[112:115], a[16:19], v[16:19]\n\t" \
  "ds_read_b64_tr_b16 a[70:71], v193 offset:9216\n\t" \
  "v_add_f32 v180, v180, v182\n\t" \
  "v_mfma_f32_16x16x32_bf16 v[48:51], a[112:115], a[20:23], v[48:51]\n\t" \
  "ds_read_b64_tr_b16 a[72:73], v194 offset:8192\n\t" \
  "v_add_f32 v181, v181, v183\n\t" \
  "v_mfma_f32_16x16x32_bf16 v[48:51], a[176:179], a[4:7], v[48:51]\n\t" \
  "ds_read_b64_tr_b16 a[74:75], v194 offset:9216\n\t" \
  "v_lshrrev_b32 v139, 4, v233\n\t" \
  "v_mfma_f32_16x16x32_bf16 v[80:83], a[176:179], a[8:11], v[80:83]\n\t" \
  "v_permlane16_swap_b32 v176, v177\n\t" \
  "v_mfma_f32_16x16x32_bf16 v[80:83], a[112:115], a[24:27], v[80:83]\n\t" \
  "buffer_load_dwordx4 a[244:247], v249, s[16:19], s97 offen sc1\n\t" \
  "v_mfma_f32_16x16x32_bf16 v[112:115], a[112:115], a[28:31], v[112:115]\n\t" \
  "ds_read_b64_tr_b16 a[76:77], v195 offset:8192\n\t" \
  "v_mfma_f32_16x16x32_bf16 v[112:115], a[176:179], a[12:15], v[112:115]\n\t" \
  "ds_read_b64_tr_b16 a[78:79], v195 offset:9216\n\t" \
  "v_mfma_f32_16x16x32_bf16 v[20:23], a[180:183], a[0:3], v[20:23]\n\t" \
  "ds_read_b64_tr_b16 a[80:81], v196 offset:8192\n\t" \
  "v_mfma_f32_16x16x32_bf16 v[20:23], a[116:119], a[16:19], v[20:23]\n\t" \
  "v_permlane16_swap_b32 v180, v181\n\t" \
  "v_mfma_f32_16x16x32_bf16 v[52:55], a[116:119], a[20:23], v[52:55]\n\t" \
  "buffer_load_dwordx4 a[248:251], v250, s[16:19], s97 offen sc1\n\t" \
  "v_mfma_f32_16x16x32_bf16 v[52:55], a[180:183], a[4:7], v[52:55]\n\t" \
  "ds_read_b64_tr_b16 a[82:83], v196 offset:9216\n\t" \
  "v_add_f32 v176, v176, v177\n\t" \
  "v_mfma_f32_16x16x32_bf16 v[84:87], a[180:183], a[8:11], v[84:87]\n\t" \
  "ds_read_b64_tr_b16 a[84:85], v197 offset:8192\n\t" \
  "v_add_f32 v180, v180, v181\n\t" \
  "v_mfma_f32_16x16x32_bf16 v[84:87], a[116:119], a[24:27], v[84:87]\n\t" \
  "ds_read_b64_tr_b16 a[86:87], v197 offset:9216\n\t" \
  "v_lshl_add_u32 v138, v139, 3, v138\n\t" \
  "v_mfma_f32_16x16x32_bf16 v[116:119], a[116:119], a[28:31], v[116:119]\n\t" \
  "ds_read_b64_tr_b16 a[88:89], v198 offset:8192\n\t" \
  "v_and_b32 v139, 0x7fffff00, v219\n\t" \
  "v_mfma_f32_16x16x32_bf16 v[116:119], a[180:183], a[12:15], v[116:119]\n\t" \
  "buffer_load_dwordx4 a[252:255], v251, s[16:19], s97 offen sc1\n\t" \
  "v_mfma_f32_16x16x32_bf16 v[24:27], a[184:187], a[0:3], v[24:27]\n\t" \
  "ds_read_b64_tr_b16 a[90:91], v198 offset:9216\n\t" \
  "v_add_f32_dpp v136, v176, v176 row_ror:8 row_mask:0xf bank_mask:0xf\n\t" \
  "v_mfma_f32_16x16x32_bf16 v[24:27], a[120:123], a[16:19], v[24:27]\n\t" \
  "ds_read_b64_tr_b16 a[92:93], v199 offset:8192\n\t" \
  "v_add_f32_dpp v137, v180, v180 row_ror:8 row_mask:0xf bank_mask:0xf\n\t" \
  "v_mfma_f32_16x16x32_bf16 v[56:59], a[120:123], a[20:23], v[56:59]\n\t" \
  "ds_read_b64_tr_b16 a[94:95], v199 offset:9216\n\t" \
  "v_add_u32 v138, v139, v138\n\t" \
  "v_mfma_f32_16x16x32_bf16 v[56:59], a[184:187], a[4:7], v[56:59]\n\t" \
  "ds_write_b64 v138, v[136:137] offset:1024\n\t" \
  "v_mfma_f32_16x16x32_bf16 v[88:91], a[184:187], a[8:11], v[88:91]\n\t" \
  "buffer_load_dword v246, v217, s[16:19], s58 offen sc1\n\t" \
  "v_mfma_f32_16x16x32_bf16 v[88:91], a[120:123], a[24:27], v[88:91]\n\t" \
  "s_add_u32 s99, s53, 0\n\t" \
  "v_and_b32 v140, 3, v233\n\t" \
  "v_bfe_u32 v141, v233, 4, 1\n\t" \
  "v_mfma_f32_16x16x32_bf16 v[120:123], a[120:123], a[28:31], v[120:123]\n\t" \
  "v_lshl_add_u32 v140, v141, 2, v140\n\t" \
  "v_bfe_u32 v141, v233, 2, 2\n\t" \
  "v_mfma_f32_16x16x32_bf16 v[120:123], a[184:187], a[12:15], v[120:123]\n\t" \
  "v_lshlrev_b32 v141, 8, v141\n\t" \
  "v_lshl_add_u32 v141, v140, 3, v141\n\t" \
  "v_mfma_f32_16x16x32_bf16 v[28:31], a[188:191], a[0:3], v[28:31]\n\t" \
  "v_lshl_add_u32 v141, s3, 6, v141\n\t" \
  "v_lshlrev_b32 v140, 4, v140\n\t" \
  "v_mfma_f32_16x16x32_bf16 v[28:31], a[124:127], a[16:19], v[28:31]\n\t" \
  "buffer_load_dword v246, v245, s[68:71], s99 offen\n\t" \
  "v_mfma_f32_16x16x32_bf16 v[60:63], a[124:127], a[20:23], v[60:63]\n\t" \
  "v_and_b32 v142, 0x2c, v233\n\t" \
  "v_lshl_or_b32 v140, v142, 26, v140\n\t" \
  "v_mfma_f32_16x16x32_bf16 v[60:63], a[188:191], a[4:7], v[60:63]\n\t" \
  "v_add_u32 v141, 151552, v141\n\t" \
  "v_mov_b32 v145, v211\n\t" \
  "v_mfma_f32_16x16x32_bf16 v[92:95], a[188:191], a[8:11], v[92:95]\n\t" \
  "v_mov_b32 v147, v211\n\t" \
  "v_mfma_f32_16x16x32_bf16 v[92:95], a[124:127], a[24:27], v[92:95]\n\t" \
  "s_add_u32 s99, s53, 14336\n\t" \
  "v_mfma_f32_16x16x32_bf16 v[124:127], a[124:127], a[28:31], v[124:127]\n\t" \
  "s_waitcnt vmcnt(18) lgkmcnt(1)\n\t" \
  "v_mfma_f32_16x16x32_bf16 v[124:127], a[188:191], a[12:15], v[124:127]\n\t" \
  "s_barrier\n\t" \
  "v_mfma_f32_16x16x32_bf16 v[0:3], a[128:131], a[32:35], v[0:3]\n\t" \
  "buffer_load_dword v246, v245, s[68:71], s99 offen\n\t" \
  "v_mfma_f32_16x16x32_bf16 v[0:3], a[64:67], a[48:51], v[0:3]\n\t" \
  "ds_read_b64_tr_b16 a[160:161], v192 offset:16384\n\t" \
  "v_mfma_f32_16x16x32_bf16 v[32:35], a[64:67], a[52:55], v[32:35]\n\t" \
  "ds_read_b64_tr_b16 a[162:163], v192 offset:17408\n\t" \
  "v_mfma_f32_16x16x32_bf16 v[32:35], a[128:131], a[36:39], v[32:35]\n\t" \
  "ds_read_b64_tr_b16 a[164:165], v193 offset:16384\n\t" \
  "v_mfma_f32_16x16x32_bf16 v[64:67], a[128:131], a[40:43], v[64:67]\n\t" \
  "ds_read_b64_tr_b16 a[166:167], v193 offset:17408\n\t" \
  "v_mfma_f32_16x16x32_bf16 v[64:67], a[64:67], a[56:59], v[64:67]\n\t" \
  "ds_read_b64_tr_b16 a[168:169], v194 offset:16384\n\t" \
  "v_mfma_f32_16x16x32_bf16 v[96:99], a[64:67], a[60:63], v[96:99]\n\t" \
  "ds_read_b64_tr_b16 a[170:171], v194 offset:17408\n\t" \
  "v_mfma_f32_16x16x32_bf16 v[96:99], a[128:131], a[44:47], v[96:99]\n\t" \
  "ds_read_b64_tr_b16 a[172:173], v195 offset:16384\n\t" \
  "v_mfma_f32_16x16x32_bf16 v[4:7], a[132:135], a[32:35], v[4:7]\n\t" \
  "ds_read_b64_tr_b16 a[174:175], v195 offset:17408\n\t" \
  "v_mfma_f32_16x16x32_bf16 v[4:7], a[68:71], a[48:51], v[4:7]\n\t" \
  "ds_read_b64_tr_b16 a[176:177], v196 offset:16384\n\t" \
  "v_mfma_f32_16x16x32_bf16 v[36:39], a[68:71], a[52:55], v[36:39]\n\t" \
  "ds_read_b64_tr_b16 a[178:179], v196 offset:17408\n\t" \
  "v_mfma_f32_16x16x32_bf16 v[36:39], a[132:135], a[36:39], v[36:39]\n\t" \
  "ds_read_b64_tr_b16 a[180:181], v197 offset:16384\n\t" \
  "v_mfma_f32_16x16x32_bf16 v[68:71], a[132:135], a[40:43], v[68:71]\n\t" \
  "ds_read_b64_tr_b16 a[182:183], v197 offset:17408\n\t" \
  "v_mfma_f32_16x16x32_bf16 v[68:71], a[68:71], a[56:59], v[68:71]\n\t" \
  "ds_read_b64_tr_b16 a[184:185], v198 offset:16384\n\t" \
  "v_mfma_f32_16x16x32_bf16 v[100:103], a[68:71], a[60:63], v[100:103]\n\t" \
  "ds_read_b64_tr_b16 a[186:187], v198 offset:17408\n\t" \
  "v_mfma_f32_16x16x32_bf16 v[100:103], a[132:135], a[44:47], v[100:103]\n\t" \
  "ds_read_b64_tr_b16 a[188:189], v199 offset:16384\n\t" \
  "v_mfma_f32_16x16x32_bf16 v[8:11], a[136:139], a[32:35], v[8:11]\n\t" \
  "ds_read_b64_tr_b16 a[190:191], v199 offset:17408\n\t" \
  "v_mfma_f32_16x16x32_bf16 v[8:11], a[72:75], a[48:51], v[8:11]\n\t" \
  "ds_read_b64_tr_b16 a[96:97], v192 offset:24576\n\t" \
  "v_mfma_f32_16x16x32_bf16 v[40:43], a[72:75], a[52:55], v[40:43]\n\t" \
  "ds_read_b64_tr_b16 a[98:99], v192 offset:25600\n\t" \
  "v_mfma_f32_16x16x32_bf16 v[40:43], a[136:139], a[36:39], v[40:43]\n\t" \
  "v_mfma_f32_16x16x32_bf16 v[72:75], a[136:139], a[40:43], v[72:75]\n\t" \
  "ds_read_b64_tr_b16 a[100:101], v193 offset:24576\n\t" \
  "v_mfma_f32_16x16x32_bf16 v[72:75], a[72:75], a[56:59], v[72:75]\n\t" \
  "ds_read_b64_tr_b16 a[102:103], v193 offset:25600\n\t" \
  "v_mfma_f32_16x16x32_bf16 v[104:107], a[72:75], a[60:63], v[104:107]\n\t" \
  "v_mfma_f32_16x16x32_bf16 v[104:107], a[136:139], a[44:47], v[104:107]\n\t" \
  "ds_read_b64_tr_b16 a[104:105], v194 offset:24576\n\t" \
  "v_mfma_f32_16x16x32_bf16 v[12:15], a[140:143], a[32:35], v[12:15]\n\t" \
  "v_mfma_f32_16x16x32_bf16 v[12:15], a[76:79], a[48:51], v[12:15]\n\t" \
  "ds_read_b64_tr_b16 a[106:107], v194 offset:25600\n\t" \
  "v_mfma_f32_16x16x32_bf16 v[44:47], a[76:79], a[52:55], v[44:47]\n\t" \
  "ds_read_b64_tr_b16 a[108:109], v195 offset:24576\n\t" \
  "v_mfma_f32_16x16x32_bf16 v[44:47], a[140:143], a[36:39], v[44:47]\n\t" \
  "v_mfma_f32_16x16x32_bf16 v[76:79], a[140:143], a[40:43], v[76:79]\n\t" \
  "ds_read_b64_tr_b16 a[110:111], v195 offset:25600\n\t" \
  "v_mfma_f32_16x16x32_bf16 v[76:79], a[76:79], a[56:59], v[76:79]\n\t" \
  "ds_read_b64_tr_b16 a[112:113], v196 offset:24576\n\t" \
  "s_add_u32 m0, s76, 16384\n\t" \
  "v_mfma_f32_16x16x32_bf16 v[108:111], a[76:79], a[60:63], v[108:111]\n\t" \
  "v_mfma_f32_16x16x32_bf16 v[108:111], a[140:143], a[44:47], v[108:111]\n\t" \
  "buffer_load_dwordx4 v224, s[60:63], s38 offen offset:0 lds\n\t" \
  "v_mfma_f32_16x16x32_bf16 v[16:19], a[144:147], a[32:35], v[16:19]\n\t" \
  "ds_read_b64_tr_b16 a[114:115], v196 offset:25600\n\t" \
  "v_mfma_f32_16x16x32_bf16 v[16:19], a[80:83], a[48:51], v[16:19]\n\t" \
  "ds_read_b64_tr_b16 a[116:117], v197 offset:24576\n\t" \
  "v_mfma_f32_16x16x32_bf16 v[48:51], a[80:83], a[52:55], v[48:51]\n\t" \
  "ds_read_b64_tr_b16 a[118:119], v197 offset:25600\n\t" \
  "v_mfma_f32_16x16x32_bf16 v[48:51], a[144:147], a[36:39], v[48:51]\n\t" \
  "v_mfma_f32_16x16x32_bf16 v[80:83], a[144:147], a[40:43], v[80:83]\n\t" \
  "buffer_load_dwordx4 v225, s[60:63], s38 offen offset:1024 lds\n\t" \
  "v_mfma_f32_16x16x32_bf16 v[80:83], a[80:83], a[56:59], v[80:83]\n\t" \
  "ds_read_b64_tr_b16 a[120:121], v198 offset:24576\n\t" \
  "v_mfma_f32_16x16x32_bf16 v[112:115], a[80:83], a[60:63], v[112:115]\n\t" \
  "ds_read_b64_tr_b16 a[122:123], v198 offset:25600\n\t" \
  "v_mfma_f32_16x16x32_bf16 v[112:115], a[144:147], a[44:47], v[112:115]\n\t" \
  "ds_read_b64_tr_b16 a[124:125], v199 offset:24576\n\t" \
  "v_mfma_f32_16x16x32_bf16 v[20:23], a[148:151], a[32:35], v[20:23]\n\t" \
  "v_mfma_f32_16x16x32_bf16 v[20:23], a[84:87], a[48:51], v[20:23]\n\t" \
  "buffer_load_dwordx4 v226, s[60:63], s38 offen offset:2048 lds\n\t" \
  "v_mfma_f32_16x16x32_bf16 v[52:55], a[84:87], a[52:55], v[52:55]\n\t" \
  "ds_read_b64_tr_b16 a[126:127], v199 offset:25600\n\t" \
  "v_mfma_f32_16x16x32_bf16 v[52:55], a[148:151], a[36:39], v[52:55]\n\t" \
  "v_mfma_f32_16x16x32_bf16 v[84:87], a[148:151], a[40:43], v[84:87]\n\t" \
  "v_mfma_f32_16x16x32_bf16 v[84:87], a[84:87], a[56:59], v[84:87]\n\t" \
  "v_mfma_f32_16x16x32_bf16 v[116:119], a[84:87], a[60:63], v[116:119]\n\t" \
  "buffer_load_dwordx4 v227, s[60:63], s38 offen offset:3072 lds\n\t" \
  "v_mfma_f32_16x16x32_bf16 v[116:119], a[148:151], a[44:47], v[116:119]\n\t" \
  "s_add_u32 m0, s76, 49152\n\t" \
  "v_mfma_f32_16x16x32_bf16 v[24:27], a[152:155], a[32:35], v[24:27]\n\t" \
  "v_mfma_f32_16x16x32_bf16 v[24:27], a[88:91], a[48:51], v[24:27]\n\t" \
  "v_mfma_f32_16x16x32_bf16 v[56:59], a[88:91], a[52:55], v[56:59]\n\t" \
  "v_mfma_f32_16x16x32_bf16 v[56:59], a[152:155], a[36:39], v[56:59]\n\t" \
  "buffer_load_dwordx4 v230, s[64:67], s38 offen offset:0 lds\n\t" \
  "v_mfma_f32_16x16x32_bf16 v[88:91], a[152:155], a[40:43], v[88:91]\n\t" \
  "v_mfma_f32_16x16x32_bf16 v[88:91], a[88:91], a[56:59], v[88:91]\n\t" \
  "v_mfma_f32_16x16x32_bf16 v[120:123], a[88:91], a[60:63], v[120:123]\n\t" \
  "v_mfma_f32_16x16x32_bf16 v[120:123], a[152:155], a[44:47], v[120:123]\n\t" \
  "v_mfma_f32_16x16x32_bf16 v[28:31], a[156:159], a[32:35], v[28:31]\n\t" \
  "buffer_load_dwordx4 v231, s[64:67], s38 offen offset:1024 lds\n\t" \
  "v_mfma_f32_16x16x32_bf16 v[28:31], a[92:95], a[48:51], v[28:31]\n\t" \
  "v_mfma_f32_16x16x32_bf16 v[60:63], a[92:95], a[52:55], v[60:63]\n\t" \
  "v_mfma_f32_16x16x32_bf16 v[60:63], a[156:159], a[36:39], v[60:63]\n\t" \
  "v_mfma_f32_16x16x32_bf16 v[92:95], a[156:159], a[40:43], v[92:95]\n\t" \
  "v_mfma_f32_16x16x32_bf16 v[92:95], a[92:95], a[56:59], v[92:95]\n\t" \
  "v_mfma_f32_16x16x32_bf16 v[124:127], a[92:95], a[60:63], v[124:127]\n\t" \
  "s_waitcnt vmcnt(17) lgkmcnt(0)\n\t" \
  "v_mfma_f32_16x16x32_bf16 v[124:127], a[156:159], a[44:47], v[124:127]\n\t" \
  "s_barrier\n\t" \
  "v_mfma_f32_16x16x32_bf16 v[0:3], a[160:163], a[192:195], v[0:3]\n\t" \
  "buffer_load_dwordx4 v230, s[64:67], s38 offen offset:2048 lds\n\t" \
  "v_mfma_f32_16x16x32_bf16 v[0:3], a[96:99], a[208:211], v[0:3]\n\t" \
  "ds_read_b64_tr_b16 a[128:129], v192 offset:32768\n\t" \
  "v_mfma_f32_16x16x32_bf16 v[32:35], a[96:99], a[212:215], v[32:35]\n\t" \
  "ds_read_b64_tr_b16 a[130:131], v192 offset:33792\n\t" \
  "v_mfma_f32_16x16x32_bf16 v[32:35], a[160:163], a[196:199], v[32:35]\n\t" \
  "ds_read_b64 v[142:143], v141 offset:1024\n\t" \
  "v_mfma_f32_16x16x32_bf16 v[64:67], a[160:163], a[200:203], v[64:67]\n\t" \
  "ds_read_b64_tr_b16 a[132:133], v193 offset:32768\n\t" \
  "v_mfma_f32_16x16x32_bf16 v[64:67], a[96:99], a[216:219], v[64:67]\n\t" \
  "buffer_load_dwordx4 v231, s[64:67], s38 offen offset:3072 lds\n\t" \
  "v_mfma_f32_16x16x32_bf16 v[96:99], a[96:99], a[220:223], v[96:99]\n\t" \
  "s_add_u32 m0, s76, 32768\n\t" \
  "ds_read_b64_tr_b16 a[134:135], v193 offset:33792\n\t" \
  "s_add_u32 s96, s38, 16384\n\t" \
  "v_mfma_f32_16x16x32_bf16 v[96:99], a[160:163], a[204:207], v[96:99]\n\t" \
  "ds_read_b64_tr_b16 a[136:137], v194 offset:32768\n\t" \
  "v_mfma_f32_16x16x32_bf16 v[4:7], a[164:167], a[192:195], v[4:7]\n\t" \
  "buffer_load_dwordx4 v224, s[60:63], s96 offen offset:0 lds\n\t" \
  "v_mfma_f32_16x16x32_bf16 v[4:7], a[100:103], a[208:211], v[4:7]\n\t" \
  "s_waitcnt lgkmcnt(3)\n\t" \
  "v_add_f32_dpp v142, v142, v142 row_ror:4 row_mask:0xf bank_mask:0xf\n\t" \
  "v_add_f32_dpp v143, v143, v143 row_ror:4 row_mask:0xf bank_mask:0xf\n\t" \
  "v_mfma_f32_16x16x32_bf16 v[36:39], a[100:103], a[212:215], v[36:39]\n\t" \
  "ds_read_b64_tr_b16 a[138:139], v194 offset:33792\n\t" \
  "v_add_f32_dpp v144, v142, v142 row_ror:8 row_mask:0xf bank_mask:0xf\n\t" \
  "v_mfma_f32_16x16x32_bf16 v[36:39], a[164:167], a[196:199], v[36:39]\n\t" \
  "ds_read_b64_tr_b16 a[140:141], v195 offset:32768\n\t" \
  "v_add_f32_dpp v146, v143, v143 row_ror:8 row_mask:0xf bank_mask:0xf\n\t" \
  "v_mfma_f32_16x16x32_bf16 v[68:71], a[164:167], a[200:203], v[68:71]\n\t" \
  "buffer_store_dwordx4 v[144:147], v140, s[16:19], s90 offen sc1\n\t" \
  "ds_read_b64_tr_b16 a[142:143], v195 offset:33792\n\t" \
  "ds_read_b64_tr_b16 a[144:145], v196 offset:32768\n\t" \
  "ds_read_b64_tr_b16 a[146:147], v196 offset:33792\n\t" \
  "v_mfma_f32_16x16x32_bf16 v[68:71], a[100:103], a[216:219], v[68:71]\n\t" \
  "buffer_load_dwordx4 v225, s[60:63], s96 offen offset:1024 lds\n\t" \
  "v_mfma_f32_16x16x32_bf16 v[100:103], a[100:103], a[220:223], v[100:103]\n\t" \
  "ds_read_b64_tr_b16 a[148:149], v197 offset:32768\n\t" \
  "v_mfma_f32_16x16x32_bf16 v[100:103], a[164:167], a[204:207], v[100:103]\n\t" \
  ".Lhs5:\n\ts_cbranch_execz .Lhs5n\n\t" \
  "ds_read_b64_tr_b16 a[150:151], v197 offset:33792\n\t" \
  "v_mfma_f32_16x16x32_bf16 v[8:11], a[168:171], a[192:195], v[8:11]\n\t" \
  "buffer_load_dwordx4 v226, s[60:63], s96 offen offset:2048 lds\n\t" \
  "v_mfma_f32_16x16x32_bf16 v[8:11], a[104:107], a[208:211], v[8:11]\n\t" \
  "ds_read_b64_tr_b16 a[152:153], v198 offset:32768\n\t" \
  "v_mfma_f32_16x16x32_bf16 v[40:43], a[104:107], a[212:215], v[40:43]\n\t" \
  "ds_read_b64_tr_b16 a[154:155], v198 offset:33792\n\t" \
  "v_mfma_f32_16x16x32_bf16 v[40:43], a[168:171], a[196:199], v[40:43]\n\t" \
  "ds_read_b64_tr_b16 a[156:157], v199 offset:32768\n\t" \
  "v_mfma_f32_16x16x32_bf16 v[72:75], a[168:171], a[200:203], v[72:75]\n\t" \
  "buffer_load_dwordx4 v227, s[60:63], s96 offen offset:3072 lds\n\t" \
  "v_mfma_f32_16x16x32_bf16 v[72:75], a[104:107], a[216:219], v[72:75]\n\t" \
  "ds_read_b64_tr_b16 a[158:159], v199 offset:33792\n\t" \
  "s_add_u32 s99, s53, 28672\n\t" \
  "v_mfma_f32_16x16x32_bf16 v[104:107], a[104:107], a[220:223], v[104:107]\n\t" \
  "ds_read_b64_tr_b16 a[64:65], v192 offset:40960\n\t" \
  "v_mfma_f32_16x16x32_bf16 v[104:107], a[168:171], a[204:207], v[104:107]\n\t" \
  "ds_read_b64_tr_b16 a[66:67], v192 offset:41984\n\t" \
  "v_mfma_f32_16x16x32_bf16 v[12:15], a[172:175], a[192:195], v[12:15]\n\t" \
  "buffer_load_dword v246, v245, s[68:71], s99 offen\n\t" \
  "v_mfma_f32_16x16x32_bf16 v[12:15], a[108:111], a[208:211], v[12:15]\n\t" \
  "ds_read_b64_tr_b16 a[68:69], v193 offset:40960\n\t" \
  "v_mfma_f32_16x16x32_bf16 v[44:47], a[108:111], a[212:215], v[44:47]\n\t" \
  "ds_read_b64_tr_b16 a[70:71], v193 offset:41984\n\t" \
  "v_mfma_f32_16x16x32_bf16 v[44:47], a[172:175], a[196:199], v[44:47]\n\t" \
  "ds_read_b64_tr_b16 a[72:73], v194 offset:40960\n\t" \
  "v_mfma_f32_16x16x32_bf16 v[76:79], a[172:175], a[200:203], v[76:79]\n\t" \
  "ds_read_b64_tr_b16 a[74:75], v194 offset:41984\n\t" \
  "v_mfma_f32_16x16x32_bf16 v[76:79], a[108:111], a[216:219], v[76:79]\n\t" \
  "ds_read_b64_tr_b16 a[76:77], v195 offset:40960\n\t" \
  "s_add_u32 s99, s53, 43008\n\t" \
  "v_mfma_f32_16x16x32_bf16 v[108:111], a[108:111], a[220:223], v[108:111]\n\t" \
  "ds_read_b64_tr_b16 a[78:79], v195 offset:41984\n\t" \
  "v_mfma_f32_16x16x32_bf16 v[108:111], a[172:175], a[204:207], v[108:111]\n\t" \
  "buffer_load_dword v246, v245, s[68:71], s99 offen\n\t" \
  "v_mfma_f32_16x16x32_bf16 v[16:19], a[176:179], a[192:195], v[16:19]\n\t" \
  "ds_read_b64_tr_b16 a[80:81], v196 offset:40960\n\t" \
  "v_mfma_f32_16x16x32_bf16 v[16:19], a[112:115], a[208:211], v[16:19]\n\t" \
  "ds_read_b64_tr_b16 a[82:83], v196 offset:41984\n\t" \
  "v_mfma_f32_16x16x32_bf16 v[48:51], a[112:115], a[212:215], v[48:51]\n\t" \
  "ds_read_b64_tr_b16 a[84:85], v197 offset:40960\n\t" \
  "v_mfma_f32_16x16x32_bf16 v[48:51], a[176:179], a[196:199], v[48:51]\n\t" \
  "ds_read_b64_tr_b16 a[86:87], v197 offset:41984\n\t" \
  "v_mfma_f32_16x16x32_bf16 v[80:83], a[176:179], a[200:203], v[80:83]\n\t" \
  "ds_read_b64_tr_b16 a[88:89], v198 offset:40960\n\t" \
  "v_mfma_f32_16x16x32_bf16 v[80:83], a[112:115], a[216:219], v[80:83]\n\t" \
  "v_mfma_f32_16x16x32_bf16 v[112:115], a[112:115], a[220:223], v[112:115]\n\t" \
  "ds_read_b64_tr_b16 a[90:91], v198 offset:41984\n\t" \
  "v_mfma_f32_16x16x32_bf16 v[112:115], a[176:179], a[204:207], v[112:115]\n\t" \
  "ds_read_b64_tr_b16 a[92:93], v199 offset:40960\n\t" \
  "v_mfma_f32_16x16x32_bf16 v[20:23], a[180:183], a[192:195], v[20:23]\n\t" \
  "v_mfma_f32_16x16x32_bf16 v[20:23], a[116:119], a[208:211], v[20:23]\n\t" \
  "ds_read_b64_tr_b16 a[94:95], v199 offset:41984\n\t" \
  "v_mfma_f32_16x16x32_bf16 v[52:55], a[116:119], a[212:215], v[52:55]\n\t" \
  "v_mfma_f32_16x16x32_bf16 v[52:55], a[180:183], a[196:199], v[52:55]\n\t" \
  "v_mfma_f32_16x16x32_bf16 v[84:87], a[180:183], a[200:203], v[84:87]\n\t" \
  "v_mfma_f32_16x16x32_bf16 v[84:87], a[116:119], a[216:219], v[84:87]\n\t" \
  "v_mfma_f32_16x16x32_bf16 v[116:119], a[116:119], a[220:223], v[116:119]\n\t" \
  "v_mfma_f32_16x16x32_bf16 v[116:119], a[180:183], a[204:207], v[116:119]\n\t" \
  "v_mfma_f32_16x16x32_bf16 v[24:27], a[184:187], a[192:195], v[24:27]\n\t" \
  "v_mfma_f32_16x16x32_bf16 v[24:27], a[120:123], a[208:211], v[24:27]\n\t" \
  "v_mfma_f32_16x16x32_bf16 v[56:59], a[120:123], a[212:215], v[56:59]\n\t" \
  "v_mfma_f32_16x16x32_bf16 v[56:59], a[184:187], a[196:199], v[56:59]\n\t" \
  "v_mfma_f32_16x16x32_bf16 v[88:91], a[184:187], a[200:203], v[88:91]\n\t" \
  "v_mfma_f32_16x16x32_bf16 v[88:91], a[120:123], a[216:219], v[88:91]\n\t" \
  "v_mfma_f32_16x16x32_bf16 v[120:123], a[120:123], a[220:223], v[120:123]\n\t" \
  "v_mfma_f32_16x16x32_bf16 v[120:123], a[184:187], a[204:207], v[120:123]\n\t" \
  "v_mfma_f32_16x16x32_bf16 v[28:31], a[188:191], a[192:195], v[28:31]\n\t" \
  "v_mfma_f32_16x16x32_bf16 v[28:31], a[124:127], a[208:211], v[28:31]\n\t" \
  "v_mfma_f32_16x16x32_bf16 v[60:63], a[124:127], a[212:215], v[60:63]\n\t" \
  "v_mfma_f32_16x16x32_bf16 v[60:63], a[188:191], a[196:199], v[60:63]\n\t" \
  "v_mfma_f32_16x16x32_bf16 v[92:95], a[188:191], a[200:203], v[92:95]\n\t" \
  "v_mfma_f32_16x16x32_bf16 v[92:95], a[124:127], a[216:219], v[92:95]\n\t" \
  "v_mfma_f32_16x16x32_bf16 v[124:127], a[124:127], a[220:223], v[124:127]\n\t" \
  "v_mfma_f32_16x16x32_bf16 v[124:127], a[188:191], a[204:207], v[124:127]\n\t" \
  "s_waitcnt vmcnt(18) lgkmcnt(0)\n\t" \
  FSTP("s_waitcnt lgkmcnt(0)\n\t") \
  FSTP("v_writelane_b32 v255, s94, 52\n\t") \
  FSTP("v_writelane_b32 v255, s92, 53\n\t") \
  FSTA("s_memrealtime s[94:95]\n\t") \
  FSTA("s_memtime s[92:93]\n\t") \
  FSTA("s_waitcnt lgkmcnt(0)\n\t") \
  FSTA("v_writelane_b32 v255, s94, 16\n\t") \
  FSTA("v_writelane_b32 v255, s92, 17\n\t") \
  ".subsection 1\n\t" \
  ".Lfr_s:\n\t" \
  "s_mov_b32 s54, 0\n\t" \
  ".Lfr_r:\n\t" \
  "s_sleep 1\n\t" \
  "buffer_load_dwordx4 v[128:131], v248, s[16:19], s53 offen sc1\n\t" \
  "buffer_load_dwordx4 v[132:135], v248, s[16:19], s58 offen sc1\n\t" \
  "buffer_load_dwordx4 v[136:139], v248, s[16:19], s90 offen sc1\n\t" \
  "buffer_load_dwordx4 v[140:143], v248, s[16:19], s91 offen sc1\n\t" \
  "buffer_load_dwordx4 v[160:163], v249, s[16:19], s96 offen sc1\n\t" \
  "buffer_load_dwordx4 v[164:167], v249, s[16:19], s97 offen sc1\n\t" \
  "buffer_load_dwordx4 v[168:171], v249, s[16:19], s99 offen sc1\n\t" \
  "buffer_load_dwordx4 v[172:175], v249, s[16:19], s57 offen sc1\n\t" \
  "s_waitcnt vmcnt(0)\n\t" \
  "v_xor_b32 v144, v211, v129\n\t" \
  "v_bitop3_b32 v144, v144, v131, v211 bitop3:0xf6\n\t" \
  "v_bitop3_b32 v144, v144, v133, v211 bitop3:0xf6\n\t" \
  "v_bitop3_b32 v144, v144, v135, v211 bitop3:0xf6\n\t" \
  "v_bitop3_b32 v144, v144, v137, v211 bitop3:0xf6\n\t" \
  "v_bitop3_b32 v144, v144, v139, v211 bitop3:0xf6\n\t" \
  "v_bitop3_b32 v144, v144, v141, v211 bitop3:0xf6\n\t" \
  "v_bitop3_b32 v144, v144, v143, v211 bitop3:0xf6\n\t" \
  "v_bitop3_b32 v144, v144, v161, v211 bitop3:0xf6\n\t" \
  "v_bitop3_b32 v144, v144, v163, v211 bitop3:0xf6\n\t" \
  "v_bitop3_b32 v144, v144, v165, v211 bitop3:0xf6\n\t" \
  "v_bitop3_b32 v144, v144, v167, v211 bitop3:0xf6\n\t" \
  "v_bitop3_b32 v144, v144, v169, v211 bitop3:0xf6\n\t" \
  "v_bitop3_b32 v144, v144, v171, v211 bitop3:0xf6\n\t" \
  "v_bitop3_b32 v144, v144, v173, v211 bitop3:0xf6\n\t" \
  "v_bitop3_b32 v144, v144, v175, v211 bitop3:0xf6\n\t" \
  "v_cmp_ne_u32 vcc, 0, v144\n\t" \
  "v_add_f32 v146, v128, v132\n\t" \
  "v_add_f32 v148, v136, v140\n\t" \
  "v_add_f32 v147, v130, v134\n\t" \
  "v_add_f32 v149, v138, v142\n\t" \
  "v_add_f32 v146, v146, v148\n\t" \
  "v_add_f32 v147, v147, v149\n\t" \
  "s_cbranch_vccz .Lfr_k\n\t" \
  "s_add_u32 s54, s54, 1\n\t" \
  "s_cmp_lt_u32 s54, " FQ_CAP "\n\t" \
  "s_cbranch_scc1 .Lfr_r\n\t" \
  "buffer_store_dword v211, off, s[16:19], 0 offset:256 sc1\n\t" \
  "s_branch .Lfr_k\n\t" \
  ".Lfq_s:\n\t" \
  "s_mov_b32 s97, 0\n\t" \
  ".Lfq_r:\n\t" \
  "buffer_load_dword v252, v210, s[16:19], s84 offen sc1\n\t" \
  "s_waitcnt vmcnt(0)\n\t" \
  "v_cmp_ne_u32 vcc, v211, v252\n\t" \
  "s_cbranch_vccz .Lfq_k\n\t" \
  "s_add_u32 s97, s97, 1\n\t" \
  "s_cmp_lt_u32 s97, " FQ_CAP "\n\t" \
  "s_cbranch_scc1 .Lfq_r\n\t" \
  "buffer_store_dword v211, off, s[16:19], 0 offset:256 sc1\n\t" \
  "s_branch .Lfq_k\n\t" \
  ".subsection 0\n\t"
static_assert(W_VOTE == 8192 && W_FLAGR == 32768 && W_FLAGO == 131072 && W_MODB == 2228224 && W_OT == 8388608 && W_XCH == 25165824 && W_EPOCH == 2490368 && W_FLAGH == 2494464 && W_ST1 == 2097152 && W_MODA == 2162688 && W_HT == 4194304 && W_PROBE == 16384);
#define STS(x)
#define AO_STV
#define STA(x)
#define STP(x)
#define AT_ASM \
  "s_cmp_eq_u32 s37, 0\n\t" \
  "s_cbranch_scc1 .Lsf_go\n\t" \
  "v_mfma_f32_16x16x32_bf16 v[28:31], a[156:159], a[224:227], v[28:31]\n\t" \
  "s_add_u32 s77, s51, 131072\n\t" \
  "v_add_u32 v210, 65536, v228\n\t" \
  "v_add_u32 v248, 14336, v240\n\t" \
  "v_mfma_f32_16x16x32_bf16 v[28:31], a[92:95], a[240:243], v[28:31]\n\t" \
  "v_add_u32 v211, 65536, v229\n\t" \
  "v_add_u32 v249, 21504, v241\n\t" \
  "v_mfma_f32_16x16x32_bf16 v[24:27], a[152:155], a[224:227], v[24:27]\n\t" \
  "v_mov_b32 v216, 0x3f803f80\n\t" \
  "v_mov_b32 v217, 0x3f803f80\n\t" \
  "v_mfma_f32_16x16x32_bf16 v[24:27], a[88:91], a[240:243], v[24:27]\n\t" \
  "v_mov_b32 v218, 0x3f803f80\n\t" \
  "v_add_u32 v241, 7168, v241\n\t" \
  "v_mfma_f32_16x16x32_bf16 v[20:23], a[148:151], a[224:227], v[20:23]\n\t" \
  "s_mov_b32 s89, 0\n\t" \
  "v_cvt_pk_bf16_f32 v31, v30, v31\n\t" \
  "v_cvt_pk_bf16_f32 v30, v28, v29\n\t" \
  "v_mfma_f32_16x16x32_bf16 v[20:23], a[84:87], a[240:243], v[20:23]\n\t" \
  "v_mov_b32 v219, 0x3f803f80\n\t" \
  "v_cvt_pk_bf16_f32 v28, v24, v25\n\t" \
  "v_mfma_f32_16x16x32_bf16 v[16:19], a[144:147], a[224:227], v[16:19]\n\t" \
  "v_cvt_pk_bf16_f32 v29, v26, v27\n\t" \
  "v_lshlrev_b32 v212, 7, v233\n\t" \
  "v_mfma_f32_16x16x32_bf16 v[16:19], a[80:83], a[240:243], v[16:19]\n\t" \
  "v_permlane16_swap_b32 v28, v30\n\t" \
  "v_mfma_f32_16x16x32_bf16 v[12:15], a[140:143], a[224:227], v[12:15]\n\t" \
  "v_cvt_pk_bf16_f32 v27, v22, v23\n\t" \
  "v_cvt_pk_bf16_f32 v26, v20, v21\n\t" \
  "v_mfma_f32_16x16x32_bf16 v[12:15], a[76:79], a[240:243], v[12:15]\n\t" \
  "v_permlane16_swap_b32 v29, v31\n\t" \
  "v_mfma_f32_16x16x32_bf16 v[8:11], a[136:139], a[224:227], v[8:11]\n\t" \
  "s_mov_b32 s54, 0\n\t" \
  "v_cvt_pk_bf16_f32 v24, v16, v17\n\t" \
  "v_cvt_pk_bf16_f32 v25, v18, v19\n\t" \
  "v_mfma_f32_16x16x32_bf16 v[8:11], a[72:75], a[240:243], v[8:11]\n\t" \
  "v_permlane16_swap_b32 v24, v26\n\t" \
  "v_mfma_f32_16x16x32_bf16 v[4:7], a[132:135], a[224:227], v[4:7]\n\t" \
  "v_cvt_pk_bf16_f32 v23, v14, v15\n\t" \
  "v_cvt_pk_bf16_f32 v22, v12, v13\n\t" \
  "v_mfma_f32_16x16x32_bf16 v[4:7], a[68:71], a[240:243], v[4:7]\n\t" \
  "v_permlane16_swap_b32 v25, v27\n\t" \
  "v_mfma_f32_16x16x32_bf16 v[0:3], a[128:131], a[224:227], v[0:3]\n\t" \
  "s_waitcnt vmcnt(2)\n\t" \
  "v_cvt_pk_bf16_f32 v20, v8, v9\n\t" \
  "v_cvt_pk_bf16_f32 v21, v10, v11\n\t" \
  "v_mfma_f32_16x16x32_bf16 v[0:3], a[64:67], a[240:243], v[0:3]\n\t" \
  "s_barrier\n\t" \
  "v_mfma_f32_16x16x32_bf16 v[60:63], a[156:159], a[228:231], v[60:63]\n\t" \
  "v_cvt_pk_bf16_f32 v19, v6, v7\n\t" \
  "v_cvt_pk_bf16_f32 v18, v4, v5\n\t" \
  "v_mfma_f32_16x16x32_bf16 v[60:63], a[92:95], a[244:247], v[60:63]\n\t" \
  "v_permlane16_swap_b32 v20, v22\n\t" \
  "v_mfma_f32_16x16x32_bf16 v[56:59], a[152:155], a[228:231], v[56:59]\n\t" \
  "ds_read_b128 a[0:3], v220 offset:0\n\t" \
  "v_cvt_pk_bf16_f32 v16, v0, v1\n\t" \
  "v_mfma_f32_16x16x32_bf16 v[56:59], a[88:91], a[244:247], v[56:59]\n\t" \
  "ds_read_b128 a[4:7], v221 offset:0\n\t" \
  "v_cvt_pk_bf16_f32 v17, v2, v3\n\t" \
  "v_mfma_f32_16x16x32_bf16 v[52:55], a[148:151], a[228:231], v[52:55]\n\t" \
  "v_cvt_pk_bf16_f32 v63, v62, v63\n\t" \
  "v_cvt_pk_bf16_f32 v62, v60, v61\n\t" \
  "v_mfma_f32_16x16x32_bf16 v[52:55], a[84:87], a[244:247], v[52:55]\n\t" \
  "v_permlane16_swap_b32 v21, v23\n\t" \
  "v_mfma_f32_16x16x32_bf16 v[48:51], a[144:147], a[228:231], v[48:51]\n\t" \
  "v_cvt_pk_bf16_f32 v60, v56, v57\n\t" \
  "v_cvt_pk_bf16_f32 v61, v58, v59\n\t" \
  "v_mfma_f32_16x16x32_bf16 v[48:51], a[80:83], a[244:247], v[48:51]\n\t" \
  "ds_read_b128 a[8:11], v222 offset:0\n\t" \
  "v_cmp_gt_u32 vcc, 16, v233\n\t" \
  "v_mfma_f32_16x16x32_bf16 v[44:47], a[140:143], a[228:231], v[44:47]\n\t" \
  "v_cvt_pk_bf16_f32 v59, v54, v55\n\t" \
  "v_cvt_pk_bf16_f32 v58, v52, v53\n\t" \
  "v_mfma_f32_16x16x32_bf16 v[44:47], a[76:79], a[244:247], v[44:47]\n\t" \
  "ds_read_b128 a[12:15], v223 offset:0\n\t" \
  "v_bfrev_b32 v214, 1\n\t" \
  "v_mfma_f32_16x16x32_bf16 v[40:43], a[136:139], a[228:231], v[40:43]\n\t" \
  "v_cvt_pk_bf16_f32 v56, v48, v49\n\t" \
  "v_cvt_pk_bf16_f32 v57, v50, v51\n\t" \
  "v_mfma_f32_16x16x32_bf16 v[40:43], a[72:75], a[244:247], v[40:43]\n\t" \
  "s_add_u32 s99, s53, 0\n\t" \
  "ds_read_b128 a[16:19], v220 offset:4096\n\t" \
  "v_cvt_pk_bf16_f32 v55, v46, v47\n\t" \
  "v_mfma_f32_16x16x32_bf16 v[36:39], a[132:135], a[228:231], v[36:39]\n\t" \
  "ds_read_b128 a[20:23], v221 offset:4096\n\t" \
  "v_cvt_pk_bf16_f32 v54, v44, v45\n\t" \
  "v_mfma_f32_16x16x32_bf16 v[36:39], a[68:71], a[244:247], v[36:39]\n\t" \
  "ds_read_b128 a[24:27], v222 offset:4096\n\t" \
  "v_cvt_pk_bf16_f32 v52, v40, v41\n\t" \
  "v_mfma_f32_16x16x32_bf16 v[32:35], a[128:131], a[228:231], v[32:35]\n\t" \
  "buffer_load_dword v246, v245, s[72:75], s99 offen\n\t" \
  "v_mfma_f32_16x16x32_bf16 v[32:35], a[64:67], a[244:247], v[32:35]\n\t" \
  "v_cvt_pk_bf16_f32 v53, v42, v43\n\t" \
  "v_permlane16_swap_b32 v16, v18\n\t" \
  "v_mfma_f32_16x16x32_bf16 v[92:95], a[156:159], a[232:235], v[92:95]\n\t" \
  "v_cvt_pk_bf16_f32 v51, v38, v39\n\t" \
  "v_cvt_pk_bf16_f32 v50, v36, v37\n\t" \
  "v_mfma_f32_16x16x32_bf16 v[92:95], a[92:95], a[248:251], v[92:95]\n\t" \
  "ds_read_b128 a[28:31], v223 offset:4096\n\t" \
  "v_permlane16_swap_b32 v17, v19\n\t" \
  "v_mfma_f32_16x16x32_bf16 v[88:91], a[152:155], a[232:235], v[88:91]\n\t" \
  "v_cvt_pk_bf16_f32 v48, v32, v33\n\t" \
  "v_permlane16_swap_b32 v60, v62\n\t" \
  "v_mfma_f32_16x16x32_bf16 v[88:91], a[88:91], a[248:251], v[88:91]\n\t" \
  "ds_read_b128 a[32:35], v220 offset:8192\n\t" \
  "v_cvt_pk_bf16_f32 v49, v34, v35\n\t" \
  "v_mfma_f32_16x16x32_bf16 v[84:87], a[148:151], a[232:235], v[84:87]\n\t" \
  "v_cvt_pk_bf16_f32 v95, v94, v95\n\t" \
  "v_cvt_pk_bf16_f32 v94, v92, v93\n\t" \
  "v_mfma_f32_16x16x32_bf16 v[84:87], a[84:87], a[248:251], v[84:87]\n\t" \
  "ds_read_b128 a[36:39], v221 offset:8192\n\t" \
  "v_permlane16_swap_b32 v61, v63\n\t" \
  "v_mfma_f32_16x16x32_bf16 v[80:83], a[144:147], a[232:235], v[80:83]\n\t" \
  "v_cvt_pk_bf16_f32 v92, v88, v89\n\t" \
  "v_cvt_pk_bf16_f32 v93, v90, v91\n\t" \
  "v_mfma_f32_16x16x32_bf16 v[80:83], a[80:83], a[248:251], v[80:83]\n\t" \
  "ds_read_b128 a[40:43], v222 offset:8192\n\t" \
  "v_permlane16_swap_b32 v56, v58\n\t" \
  "v_cvt_pk_bf16_f32 v91, v86, v87\n\t" \
  "v_mfma_f32_16x16x32_bf16 v[76:79], a[140:143], a[232:235], v[76:79]\n\t" \
  "buffer_load_dword v246, v247, s[16:19], 0 offen sc1\n\t" \
  "v_mfma_f32_16x16x32_bf16 v[76:79], a[76:79], a[248:251], v[76:79]\n\t" \
  "ds_read_b128 a[44:47], v223 offset:8192\n\t" \
  "v_permlane16_swap_b32 v57, v59\n\t" \
  "v_mfma_f32_16x16x32_bf16 v[72:75], a[136:139], a[232:235], v[72:75]\n\t" \
  "v_cvt_pk_bf16_f32 v90, v84, v85\n\t" \
  "v_cvt_pk_bf16_f32 v88, v80, v81\n\t" \
  "v_mfma_f32_16x16x32_bf16 v[72:75], a[72:75], a[248:251], v[72:75]\n\t" \
  "ds_read_b128 a[48:51], v220 offset:12288\n\t" \
  "v_permlane16_swap_b32 v52, v54\n\t" \
  "v_mfma_f32_16x16x32_bf16 v[68:71], a[132:135], a[232:235], v[68:71]\n\t" \
  "ds_read_b128 a[52:55], v221 offset:12288\n\t" \
  "v_cvt_pk_bf16_f32 v89, v82, v83\n\t" \
  "v_mfma_f32_16x16x32_bf16 v[68:71], a[68:71], a[248:251], v[68:71]\n\t" \
  "v_cvt_pk_bf16_f32 v87, v78, v79\n\t" \
  "v_permlane16_swap_b32 v53, v55\n\t" \
  "v_mfma_f32_16x16x32_bf16 v[64:67], a[128:131], a[232:235], v[64:67]\n\t" \
  "ds_read_b128 a[56:59], v222 offset:12288\n\t" \
  "v_cvt_pk_bf16_f32 v86, v76, v77\n\t" \
  "v_mfma_f32_16x16x32_bf16 v[64:67], a[64:67], a[248:251], v[64:67]\n\t" \
  "v_cvt_pk_bf16_f32 v84, v72, v73\n\t" \
  "v_permlane16_swap_b32 v48, v50\n\t" \
  "v_mfma_f32_16x16x32_bf16 v[124:127], a[156:159], a[236:239], v[124:127]\n\t" \
  "ds_read_b128 a[60:63], v223 offset:12288\n\t" \
  "v_cvt_pk_bf16_f32 v85, v74, v75\n\t" \
  "v_mfma_f32_16x16x32_bf16 v[124:127], a[92:95], a[252:255], v[124:127]\n\t" \
  "v_cvt_pk_bf16_f32 v83, v70, v71\n\t" \
  "v_permlane16_swap_b32 v49, v51\n\t" \
  "v_mfma_f32_16x16x32_bf16 v[120:123], a[152:155], a[236:239], v[120:123]\n\t" \
  "v_cvt_pk_bf16_f32 v82, v68, v69\n\t" \
  "v_cvt_pk_bf16_f32 v80, v64, v65\n\t" \
  "v_mfma_f32_16x16x32_bf16 v[120:123], a[88:91], a[252:255], v[120:123]\n\t" \
  "v_cvt_pk_bf16_f32 v81, v66, v67\n\t" \
  "v_permlane16_swap_b32 v92, v94\n\t" \
  "v_mfma_f32_16x16x32_bf16 v[116:119], a[148:151], a[236:239], v[116:119]\n\t" \
  "v_cvt_pk_bf16_f32 v127, v126, v127\n\t" \
  "v_cvt_pk_bf16_f32 v126, v124, v125\n\t" \
  "v_mfma_f32_16x16x32_bf16 v[116:119], a[84:87], a[252:255], v[116:119]\n\t" \
  "v_permlane16_swap_b32 v93, v95\n\t" \
  "v_mfma_f32_16x16x32_bf16 v[112:115], a[144:147], a[236:239], v[112:115]\n\t" \
  "v_cvt_pk_bf16_f32 v124, v120, v121\n\t" \
  "v_permlane16_swap_b32 v88, v90\n\t" \
  "v_mfma_f32_16x16x32_bf16 v[112:115], a[80:83], a[252:255], v[112:115]\n\t" \
  "v_permlane16_swap_b32 v89, v91\n\t" \
  "v_mfma_f32_16x16x32_bf16 v[108:111], a[140:143], a[236:239], v[108:111]\n\t" \
  "v_cvt_pk_bf16_f32 v125, v122, v123\n\t" \
  "v_cvt_pk_bf16_f32 v123, v118, v119\n\t" \
  "v_mfma_f32_16x16x32_bf16 v[108:111], a[76:79], a[252:255], v[108:111]\n\t" \
  "v_cvt_pk_bf16_f32 v122, v116, v117\n\t" \
  "v_permlane16_swap_b32 v84, v86\n\t" \
  "v_mfma_f32_16x16x32_bf16 v[104:107], a[136:139], a[236:239], v[104:107]\n\t" \
  "v_cvt_pk_bf16_f32 v120, v112, v113\n\t" \
  "v_cvt_pk_bf16_f32 v121, v114, v115\n\t" \
  "v_mfma_f32_16x16x32_bf16 v[104:107], a[72:75], a[252:255], v[104:107]\n\t" \
  "v_permlane16_swap_b32 v85, v87\n\t" \
  "v_mfma_f32_16x16x32_bf16 v[100:103], a[132:135], a[236:239], v[100:103]\n\t" \
  "s_add_u32 m0, s38, 49152\n\t" \
  "v_permlane16_swap_b32 v80, v82\n\t" \
  "v_mfma_f32_16x16x32_bf16 v[100:103], a[68:71], a[252:255], v[100:103]\n\t" \
  "s_add_u32 s96, s38, 32768\n\t" \
  "v_permlane16_swap_b32 v81, v83\n\t" \
  "v_mfma_f32_16x16x32_bf16 v[96:99], a[128:131], a[236:239], v[96:99]\n\t" \
  "s_waitcnt lgkmcnt(0)\n\t" \
  "v_cvt_pk_bf16_f32 v119, v110, v111\n\t" \
  "v_cvt_pk_bf16_f32 v118, v108, v109\n\t" \
  "v_mfma_f32_16x16x32_bf16 v[96:99], a[64:67], a[252:255], v[96:99]\n\t" \
  "s_barrier\n\t" \
  STS("s_memrealtime s[94:95]\n\t") \
  STS("s_memtime s[92:93]\n\t") \
  "v_mfma_f32_16x16x32_bf16 v[0:3], a[0:3], v[16:19], 0\n\t" \
  "buffer_load_dwordx4 v224, s[60:63], s96 offen offset:0 lds\n\t" \
  "v_mfma_f32_16x16x32_bf16 v[0:3], a[4:7], v[20:23], v[0:3]\n\t" \
  "v_permlane16_swap_b32 v124, v126\n\t" \
  "v_mfma_f32_16x16x32_bf16 v[0:3], a[8:11], v[24:27], v[0:3]\n\t" \
  "ds_read_b128 a[192:195], v220 offset:16384\n\t" \
  "v_cvt_pk_bf16_f32 v116, v104, v105\n\t" \
  "v_mfma_f32_16x16x32_bf16 v[0:3], a[12:15], v[28:31], v[0:3]\n\t" \
  "v_permlane16_swap_b32 v125, v127\n\t" \
  "v_mfma_f32_16x16x32_bf16 v[32:35], a[0:3], v[48:51], 0\n\t" \
  "ds_read_b128 a[196:199], v221 offset:16384\n\t" \
  "v_cvt_pk_bf16_f32 v117, v106, v107\n\t" \
  "v_mfma_f32_16x16x32_bf16 v[32:35], a[4:7], v[52:55], v[32:35]\n\t" \
  "v_cvt_pk_bf16_f32 v115, v102, v103\n\t" \
  "v_cvt_pk_bf16_f32 v114, v100, v101\n\t" \
  "s_nop 0\n\t" \
  "v_max3_f32 v194, v0, v1, v2\n\t" \
  "v_max_f32 v194, v194, v3\n\t" \
  "v_mfma_f32_16x16x32_bf16 v[32:35], a[8:11], v[56:59], v[32:35]\n\t" \
  "v_mov_b32 v195, v194\n\t" \
  "v_cvt_pk_bf16_f32 v112, v96, v97\n\t" \
  "s_nop 0\n\t" \
  "v_permlane32_swap_b32 v194, v195\n\t" \
  "v_mfma_f32_16x16x32_bf16 v[32:35], a[12:15], v[60:63], v[32:35]\n\t" \
  "v_permlane16_swap_b32 v120, v122\n\t" \
  "v_max_f32 v194, v194, v195\n\t" \
  "v_mov_b32 v195, v194\n\t" \
  "s_nop 1\n\t" \
  "v_permlane16_swap_b32 v194, v195\n\t" \
  "v_max_f32 v194, v194, v195\n\t" \
  "v_sub_f32 v176, 0xc2000000, v194\n\t" \
  "v_sub_f32 v177, 0xc2000000, v194\n\t" \
  "v_sub_f32 v178, 0xc2000000, v194\n\t" \
  "v_sub_f32 v179, 0xc2000000, v194\n\t" \
  "s_nop 1\n\t" \
  "v_mfma_f32_16x16x32_bf16 v[4:7], a[16:19], v[16:19], v[176:179]\n\t" \
  "v_add_f32 v0, v0, v176\n\t" \
  "v_add_f32 v1, v1, v176\n\t" \
  "v_add_f32 v2, v2, v176\n\t" \
  "v_add_f32 v3, v3, v176\n\t" \
  "v_mfma_f32_16x16x32_bf16 v[4:7], a[20:23], v[20:23], v[4:7]\n\t" \
  "v_max3_f32 v194, v32, v33, v34\n\t" \
  "v_max_f32 v194, v194, v35\n\t" \
  "v_mov_b32 v195, v194\n\t" \
  "v_exp_f32 v0, v0\n\t" \
  "v_mfma_f32_16x16x32_bf16 v[4:7], a[24:27], v[24:27], v[4:7]\n\t" \
  "buffer_load_dwordx4 v225, s[60:63], s96 offen offset:1024 lds\n\t" \
  "v_permlane32_swap_b32 v194, v195\n\t" \
  "v_max_f32 v194, v194, v195\n\t" \
  "v_mfma_f32_16x16x32_bf16 v[4:7], a[28:31], v[28:31], v[4:7]\n\t" \
  "v_mov_b32 v195, v194\n\t" \
  "v_exp_f32 v1, v1\n\t" \
  "v_exp_f32 v2, v2\n\t" \
  "v_mfma_f32_16x16x32_bf16 v[8:11], a[32:35], v[16:19], v[176:179]\n\t" \
  STS("v_writelane_b32 v255, s94, 18\n\t") \
  STS("v_writelane_b32 v255, s92, 19\n\t") \
  "v_permlane16_swap_b32 v194, v195\n\t" \
  "v_max_f32 v194, v194, v195\n\t" \
  "v_sub_f32 v180, 0xc2000000, v194\n\t" \
  "v_sub_f32 v181, 0xc2000000, v194\n\t" \
  "v_mfma_f32_16x16x32_bf16 v[8:11], a[36:39], v[20:23], v[8:11]\n\t" \
  "v_sub_f32 v182, 0xc2000000, v194\n\t" \
  "v_sub_f32 v183, 0xc2000000, v194\n\t" \
  "v_add_f32 v32, v32, v180\n\t" \
  "v_exp_f32 v3, v3\n\t" \
  "v_mfma_f32_16x16x32_bf16 v[8:11], a[40:43], v[24:27], v[8:11]\n\t" \
  "s_add_u32 s99, s53, 14336\n\t" \
  "v_add_f32 v33, v33, v180\n\t" \
  "v_exp_f32 v4, v4\n\t" \
  "v_exp_f32 v5, v5\n\t" \
  "v_mfma_f32_16x16x32_bf16 v[8:11], a[44:47], v[28:31], v[8:11]\n\t" \
  "v_exp_f32 v6, v6\n\t" \
  "v_exp_f32 v7, v7\n\t" \
  "v_add_f32 v34, v34, v180\n\t" \
  "v_mfma_f32_16x16x32_bf16 v[12:15], a[48:51], v[16:19], v[176:179]\n\t" \
  "buffer_load_dword v246, v245, s[72:75], s99 offen\n\t" \
  "v_mfma_f32_16x16x32_bf16 v[12:15], a[52:55], v[20:23], v[12:15]\n\t" \
  "ds_read_b128 a[200:203], v222 offset:16384\n\t" \
  "v_cvt_pk_bf16_f32 v113, v98, v99\n\t" \
  "v_add_f32 v35, v35, v180\n\t" \
  "v_exp_f32 v8, v8\n\t" \
  "v_mfma_f32_16x16x32_bf16 v[12:15], a[56:59], v[24:27], v[12:15]\n\t" \
  "ds_read_b128 a[204:207], v223 offset:16384\n\t" \
  "v_exp_f32 v9, v9\n\t" \
  "v_exp_f32 v10, v10\n\t" \
  "v_mfma_f32_16x16x32_bf16 v[12:15], a[60:63], v[28:31], v[12:15]\n\t" \
  "buffer_load_dwordx4 v226, s[60:63], s96 offen offset:2048 lds\n\t" \
  "v_exp_f32 v11, v11\n\t" \
  "v_mfma_f32_16x16x32_bf16 v[64:67], a[0:3], v[80:83], 0\n\t" \
  "ds_read_b128 a[208:211], v220 offset:20480\n\t" \
  "v_permlane16_swap_b32 v121, v123\n\t" \
  "v_mfma_f32_16x16x32_bf16 v[64:67], a[4:7], v[84:87], v[64:67]\n\t" \
  "ds_read_b128 a[212:215], v221 offset:20480\n\t" \
  "v_permlane16_swap_b32 v116, v118\n\t" \
  "v_exp_f32 v12, v12\n\t" \
  "v_mfma_f32_16x16x32_bf16 v[64:67], a[8:11], v[88:91], v[64:67]\n\t" \
  "ds_read_b128 a[216:219], v222 offset:20480\n\t" \
  "v_exp_f32 v13, v13\n\t" \
  "v_exp_f32 v14, v14\n\t" \
  "v_mfma_f32_16x16x32_bf16 v[64:67], a[12:15], v[92:95], v[64:67]\n\t" \
  "ds_read_b128 a[220:223], v223 offset:20480\n\t" \
  "v_exp_f32 v15, v15\n\t" \
  "v_exp_f32 v32, v32\n\t" \
  "v_mfma_f32_16x16x32_bf16 v[36:39], a[16:19], v[48:51], v[180:183]\n\t" \
  "v_exp_f32 v33, v33\n\t" \
  "v_exp_f32 v34, v34\n\t" \
  "v_mfma_f32_16x16x32_bf16 v[36:39], a[20:23], v[52:55], v[36:39]\n\t" \
  "s_nop 0\n\t" \
  "v_max3_f32 v194, v64, v65, v66\n\t" \
  "v_max_f32 v194, v194, v67\n\t" \
  "v_mov_b32 v195, v194\n\t" \
  "v_exp_f32 v35, v35\n\t" \
  "v_mfma_f32_16x16x32_bf16 v[36:39], a[24:27], v[56:59], v[36:39]\n\t" \
  "ds_read_b128 a[224:227], v220 offset:24576\n\t" \
  "v_permlane32_swap_b32 v194, v195\n\t" \
  "v_max_f32 v194, v194, v195\n\t" \
  "v_mov_b32 v195, v194\n\t" \
  "v_mfma_f32_16x16x32_bf16 v[36:39], a[28:31], v[60:63], v[36:39]\n\t" \
  "s_nop 0\n\t" \
  "v_permlane16_swap_b32 v194, v195\n\t" \
  "v_max_f32 v194, v194, v195\n\t" \
  "v_sub_f32 v184, 0xc2000000, v194\n\t" \
  "v_sub_f32 v185, 0xc2000000, v194\n\t" \
  "v_mfma_f32_16x16x32_bf16 v[40:43], a[32:35], v[48:51], v[180:183]\n\t" \
  "buffer_load_dwordx4 v227, s[60:63], s96 offen offset:3072 lds\n\t" \
  "v_sub_f32 v186, 0xc2000000, v194\n\t" \
  "v_sub_f32 v187, 0xc2000000, v194\n\t" \
  "v_mfma_f32_16x16x32_bf16 v[40:43], a[36:39], v[52:55], v[40:43]\n\t" \
  "v_add_f32 v64, v64, v184\n\t" \
  "v_permlane16_swap_b32 v117, v119\n\t" \
  "v_exp_f32 v36, v36\n\t" \
  "v_mfma_f32_16x16x32_bf16 v[40:43], a[40:43], v[56:59], v[40:43]\n\t" \
  "v_add_f32 v65, v65, v184\n\t" \
  "v_exp_f32 v37, v37\n\t" \
  "v_exp_f32 v38, v38\n\t" \
  "v_mfma_f32_16x16x32_bf16 v[40:43], a[44:47], v[60:63], v[40:43]\n\t" \
  "v_add_f32 v66, v66, v184\n\t" \
  "v_exp_f32 v39, v39\n\t" \
  "v_permlane16_swap_b32 v112, v114\n\t" \
  "v_mfma_f32_16x16x32_bf16 v[44:47], a[48:51], v[48:51], v[180:183]\n\t" \
  "s_add_u32 m0, s38, 147456\n\t" \
  "v_add_f32 v67, v67, v184\n\t" \
  "v_permlane16_swap_b32 v113, v115\n\t" \
  "v_mfma_f32_16x16x32_bf16 v[44:47], a[52:55], v[52:55], v[44:47]\n\t" \
  "s_add_u32 s96, s38, 16384\n\t" \
  "ds_read_b128 a[228:231], v221 offset:24576\n\t" \
  "v_exp_f32 v40, v40\n\t" \
  "v_mfma_f32_16x16x32_bf16 v[44:47], a[56:59], v[56:59], v[44:47]\n\t" \
  "ds_read_b128 a[232:235], v222 offset:24576\n\t" \
  "v_exp_f32 v41, v41\n\t" \
  "v_mfma_f32_16x16x32_bf16 v[44:47], a[60:63], v[60:63], v[44:47]\n\t" \
  "v_exp_f32 v42, v42\n\t" \
  "v_mfma_f32_16x16x32_bf16 v[96:99], a[0:3], v[112:115], 0\n\t" \
  "buffer_load_dwordx4 v230, s[64:67], s96 offen offset:0 lds\n\t" \
  "v_mfma_f32_16x16x32_bf16 v[96:99], a[4:7], v[116:119], v[96:99]\n\t" \
  "ds_read_b128 a[236:239], v223 offset:24576\n\t" \
  "v_exp_f32 v43, v43\n\t" \
  "v_mfma_f32_16x16x32_bf16 v[96:99], a[8:11], v[120:123], v[96:99]\n\t" \
  "v_cvt_pk_bf16_f32 v128, v0, v1\n\t" \
  "v_exp_f32 v44, v44\n\t" \
  "v_mfma_f32_16x16x32_bf16 v[96:99], a[12:15], v[124:127], v[96:99]\n\t" \
  "v_exp_f32 v45, v45\n\t" \
  "v_mfma_f32_16x16x32_bf16 v[68:71], a[16:19], v[80:83], v[184:187]\n\t" \
  "v_exp_f32 v46, v46\n\t" \
  "v_mfma_f32_16x16x32_bf16 v[68:71], a[20:23], v[84:87], v[68:71]\n\t" \
  "s_nop 3\n\t" \
  "v_max3_f32 v194, v96, v97, v98\n\t" \
  "v_mfma_f32_16x16x32_bf16 v[68:71], a[24:27], v[88:91], v[68:71]\n\t" \
  "ds_read_b128 a[240:243], v220 offset:28672\n\t" \
  "v_cvt_pk_bf16_f32 v129, v2, v3\n\t" \
  "v_max_f32 v194, v194, v99\n\t" \
  "v_mov_b32 v195, v194\n\t" \
  "v_mfma_f32_16x16x32_bf16 v[68:71], a[28:31], v[92:95], v[68:71]\n\t" \
  "v_exp_f32 v47, v47\n\t" \
  "v_permlane32_swap_b32 v194, v195\n\t" \
  "v_mfma_f32_16x16x32_bf16 v[72:75], a[32:35], v[80:83], v[184:187]\n\t" \
  "v_max_f32 v194, v194, v195\n\t" \
  "v_mov_b32 v195, v194\n\t" \
  "v_mfma_f32_16x16x32_bf16 v[72:75], a[36:39], v[84:87], v[72:75]\n\t" \
  "ds_read_b128 a[244:247], v221 offset:28672\n\t" \
  "v_permlane16_swap_b32 v194, v195\n\t" \
  "v_mfma_f32_16x16x32_bf16 v[72:75], a[40:43], v[88:91], v[72:75]\n\t" \
  "v_max_f32 v194, v194, v195\n\t" \
  "v_sub_f32 v188, 0xc2000000, v194\n\t" \
  "v_mfma_f32_16x16x32_bf16 v[72:75], a[44:47], v[92:95], v[72:75]\n\t" \
  "v_sub_f32 v189, 0xc2000000, v194\n\t" \
  "v_sub_f32 v190, 0xc2000000, v194\n\t" \
  "v_mfma_f32_16x16x32_bf16 v[76:79], a[48:51], v[80:83], v[184:187]\n\t" \
  "buffer_load_dwordx4 v231, s[64:67], s96 offen offset:1024 lds\n\t" \
  "v_mfma_f32_16x16x32_bf16 v[76:79], a[52:55], v[84:87], v[76:79]\n\t" \
  "v_sub_f32 v191, 0xc2000000, v194\n\t" \
  "v_add_f32 v96, v96, v188\n\t" \
  "v_mfma_f32_16x16x32_bf16 v[76:79], a[56:59], v[88:91], v[76:79]\n\t" \
  "v_add_f32 v97, v97, v188\n\t" \
  "v_exp_f32 v64, v64\n\t" \
  "v_mfma_f32_16x16x32_bf16 v[76:79], a[60:63], v[92:95], v[76:79]\n\t" \
  "ds_read_b128 a[248:251], v222 offset:28672\n\t" \
  "v_exp_f32 v65, v65\n\t" \
  "v_mfma_f32_16x16x32_bf16 v[100:103], a[16:19], v[112:115], v[188:191]\n\t" \
  "v_add_f32 v98, v98, v188\n\t" \
  "v_add_f32 v99, v99, v188\n\t" \
  "v_mfma_f32_16x16x32_bf16 v[100:103], a[20:23], v[116:119], v[100:103]\n\t" \
  "ds_read_b128 a[252:255], v223 offset:28672\n\t" \
  "v_exp_f32 v66, v66\n\t" \
  "v_mfma_f32_16x16x32_bf16 v[100:103], a[24:27], v[120:123], v[100:103]\n\t" \
  "buffer_load_dwordx4 v230, s[64:67], s96 offen offset:2048 lds\n\t" \
  "v_mfma_f32_16x16x32_bf16 v[100:103], a[28:31], v[124:127], v[100:103]\n\t" \
  "v_cvt_pk_bf16_f32 v130, v4, v5\n\t" \
  "v_exp_f32 v67, v67\n\t" \
  "v_mfma_f32_16x16x32_bf16 v[104:107], a[32:35], v[112:115], v[188:191]\n\t" \
  "v_cvt_pk_bf16_f32 v131, v6, v7\n\t" \
  "v_cndmask_b32 v212, v214, v212, vcc\n\t" \
  "v_mfma_f32_16x16x32_bf16 v[104:107], a[36:39], v[116:119], v[104:107]\n\t" \
  "v_cvt_pk_bf16_f32 v132, v8, v9\n\t" \
  "v_exp_f32 v68, v68\n\t" \
  "v_mfma_f32_16x16x32_bf16 v[104:107], a[40:43], v[120:123], v[104:107]\n\t" \
  "v_exp_f32 v69, v69\n\t" \
  "v_mfma_f32_16x16x32_bf16 v[104:107], a[44:47], v[124:127], v[104:107]\n\t" \
  "v_cvt_pk_bf16_f32 v133, v10, v11\n\t" \
  "v_cvt_pk_bf16_f32 v134, v12, v13\n\t" \
  "v_mfma_f32_16x16x32_bf16 v[108:111], a[48:51], v[112:115], v[188:191]\n\t" \
  "v_exp_f32 v70, v70\n\t" \
  "v_mfma_f32_16x16x32_bf16 v[108:111], a[52:55], v[116:119], v[108:111]\n\t" \
  "v_exp_f32 v71, v71\n\t" \
  "v_mfma_f32_16x16x32_bf16 v[108:111], a[56:59], v[120:123], v[108:111]\n\t" \
  "v_cvt_pk_bf16_f32 v135, v14, v15\n\t" \
  "v_cvt_pk_bf16_f32 v136, v32, v33\n\t" \
  "v_mfma_f32_16x16x32_bf16 v[108:111], a[60:63], v[124:127], v[108:111]\n\t" \
  "s_waitcnt lgkmcnt(0)\n\t" \
  "buffer_load_dwordx4 v231, s[64:67], s96 offen offset:3072 lds\n\t" \
  STA("s_memrealtime s[94:95]\n\t") \
  STA("s_memtime s[92:93]\n\t") \
  "v_mfma_f32_16x16x32_bf16 v[0:3], a[192:195], v[16:19], v[176:179]\n\t" \
  "s_add_u32 s99, s53, 28672\n\t" \
  "ds_read_b64_tr_b16 a[128:129], v228 offset:0\n\t" \
  "v_cvt_pk_bf16_f32 v137, v34, v35\n\t" \
  "v_mfma_f32_16x16x32_bf16 v[0:3], a[196:199], v[20:23], v[0:3]\n\t" \
  "v_exp_f32 v72, v72\n\t" \
  "v_mfma_f32_16x16x32_bf16 v[0:3], a[200:203], v[24:27], v[0:3]\n\t" \
  "buffer_load_dword v246, v245, s[72:75], s99 offen\n\t" \
  "v_mfma_f32_16x16x32_bf16 v[0:3], a[204:207], v[28:31], v[0:3]\n\t" \
  "ds_read_b64_tr_b16 a[130:131], v228 offset:4096\n\t" \
  "v_cvt_pk_bf16_f32 v138, v36, v37\n\t" \
  "v_mfma_f32_16x16x32_bf16 v[4:7], a[220:223], v[28:31], v[176:179]\n\t" \
  "ds_read_b64_tr_b16 a[132:133], v228 offset:8192\n\t" \
  "v_cvt_pk_bf16_f32 v139, v38, v39\n\t" \
  "v_mfma_f32_16x16x32_bf16 v[4:7], a[216:219], v[24:27], v[4:7]\n\t" \
  "v_exp_f32 v73, v73\n\t" \
  "v_mfma_f32_16x16x32_bf16 v[4:7], a[212:215], v[20:23], v[4:7]\n\t" \
  "ds_read_b64_tr_b16 a[134:135], v228 offset:12288\n\t" \
  "v_cvt_pk_bf16_f32 v140, v40, v41\n\t" \
  "v_mfma_f32_16x16x32_bf16 v[4:7], a[208:211], v[16:19], v[4:7]\n\t" \
  "ds_read_b64_tr_b16 a[136:137], v229 offset:0\n\t" \
  "v_cvt_pk_bf16_f32 v141, v42, v43\n\t" \
  "v_mfma_f32_16x16x32_bf16 v[8:11], a[224:227], v[16:19], v[176:179]\n\t" \
  "v_exp_f32 v74, v74\n\t" \
  "v_mfma_f32_16x16x32_bf16 v[8:11], a[228:231], v[20:23], v[8:11]\n\t" \
  "ds_read_b64_tr_b16 a[138:139], v229 offset:4096\n\t" \
  "v_cvt_pk_bf16_f32 v142, v44, v45\n\t" \
  "v_mfma_f32_16x16x32_bf16 v[8:11], a[232:235], v[24:27], v[8:11]\n\t" \
  "v_exp_f32 v75, v75\n\t" \
  "v_mfma_f32_16x16x32_bf16 v[8:11], a[236:239], v[28:31], v[8:11]\n\t" \
  "ds_read_b64_tr_b16 a[140:141], v229 offset:8192\n\t" \
  "v_lshl_add_u32 v214, v233, 14, s41\n\t" \
  "v_mfma_f32_16x16x32_bf16 v[12:15], a[252:255], v[28:31], v[176:179]\n\t" \
  STA("v_writelane_b32 v255, s94, 26\n\t") \
  STA("v_writelane_b32 v255, s92, 27\n\t") \
  "v_exp_f32 v76, v76\n\t" \
  "v_mfma_f32_16x16x32_bf16 v[12:15], a[248:251], v[24:27], v[12:15]\n\t" \
  "ds_read_b64_tr_b16 a[142:143], v229 offset:12288\n\t" \
  "v_min_u32 v214, .Lfz_z-.Lat_t0-4, v214\n\t" \
  "v_mfma_f32_16x16x32_bf16 v[12:15], a[244:247], v[20:23], v[12:15]\n\t" \
  "s_getpc_b64 s[84:85]\n\t" \
  ".Lat_t0:\n\t" \
  "ds_read_b64_tr_b16 a[144:145], v228 offset:256\n\t" \
  "v_cvt_pk_bf16_f32 v143, v46, v47\n\t" \
  "v_mfma_f32_16x16x32_bf16 v[12:15], a[240:243], v[16:19], v[12:15]\n\t" \
  "global_load_dword v246, v214, s[84:85]\n\t" \
  "v_mfma_f32_16x16x32_bf16 v[32:35], a[192:195], v[48:51], v[180:183]\n\t" \
  "v_exp_f32 v77, v77\n\t" \
  "v_mfma_f32_16x16x32_bf16 v[32:35], a[196:199], v[52:55], v[32:35]\n\t" \
  "ds_read_b64_tr_b16 a[146:147], v228 offset:4352\n\t" \
  "v_cvt_pk_bf16_f32 v144, v64, v65\n\t" \
  "v_mfma_f32_16x16x32_bf16 v[32:35], a[200:203], v[56:59], v[32:35]\n\t" \
  "ds_read_b64_tr_b16 a[148:149], v228 offset:8448\n\t" \
  "v_exp_f32 v78, v78\n\t" \
  "v_mfma_f32_16x16x32_bf16 v[32:35], a[204:207], v[60:63], v[32:35]\n\t" \
  "v_exp_f32 v79, v79\n\t" \
  "v_mfma_f32_16x16x32_bf16 v[36:39], a[220:223], v[60:63], v[180:183]\n\t" \
  "ds_read_b64_tr_b16 a[150:151], v228 offset:12544\n\t" \
  "v_cvt_pk_bf16_f32 v145, v66, v67\n\t" \
  "v_mfma_f32_16x16x32_bf16 v[36:39], a[216:219], v[56:59], v[36:39]\n\t" \
  "v_exp_f32 v96, v96\n\t" \
  "v_mfma_f32_16x16x32_bf16 v[36:39], a[212:215], v[52:55], v[36:39]\n\t" \
  "ds_read_b64_tr_b16 a[152:153], v229 offset:256\n\t" \
  "v_cvt_pk_bf16_f32 v146, v68, v69\n\t" \
  "v_mfma_f32_16x16x32_bf16 v[36:39], a[208:211], v[48:51], v[36:39]\n\t" \
  "ds_read_b64_tr_b16 a[154:155], v229 offset:4352\n\t" \
  "v_exp_f32 v97, v97\n\t" \
  "v_mfma_f32_16x16x32_bf16 v[40:43], a[224:227], v[48:51], v[180:183]\n\t" \
  "v_exp_f32 v98, v98\n\t" \
  "v_mfma_f32_16x16x32_bf16 v[40:43], a[228:231], v[52:55], v[40:43]\n\t" \
  "ds_read_b64_tr_b16 a[156:157], v229 offset:8448\n\t" \
  "v_cvt_pk_bf16_f32 v147, v70, v71\n\t" \
  "v_mfma_f32_16x16x32_bf16 v[40:43], a[232:235], v[56:59], v[40:43]\n\t" \
  "v_exp_f32 v99, v99\n\t" \
  "v_mfma_f32_16x16x32_bf16 v[40:43], a[236:239], v[60:63], v[40:43]\n\t" \
  "ds_read_b64_tr_b16 a[158:159], v229 offset:12544\n\t" \
  "v_cvt_pk_bf16_f32 v148, v72, v73\n\t" \
  "v_mfma_f32_16x16x32_bf16 v[44:47], a[252:255], v[60:63], v[180:183]\n\t" \
  "ds_read_b64_tr_b16 a[160:161], v228 offset:512\n\t" \
  "v_cvt_pk_bf16_f32 v149, v74, v75\n\t" \
  "v_mfma_f32_16x16x32_bf16 v[44:47], a[248:251], v[56:59], v[44:47]\n\t" \
  "v_exp_f32 v100, v100\n\t" \
  "v_mfma_f32_16x16x32_bf16 v[44:47], a[244:247], v[52:55], v[44:47]\n\t" \
  "v_exp_f32 v101, v101\n\t" \
  "v_mfma_f32_16x16x32_bf16 v[44:47], a[240:243], v[48:51], v[44:47]\n\t" \
  "ds_read_b64_tr_b16 a[162:163], v228 offset:4608\n\t" \
  "v_cvt_pk_bf16_f32 v150, v76, v77\n\t" \
  "v_mfma_f32_16x16x32_bf16 v[64:67], a[192:195], v[80:83], v[184:187]\n\t" \
  "ds_read_b64_tr_b16 a[164:165], v228 offset:8704\n\t" \
  "v_cvt_pk_bf16_f32 v151, v78, v79\n\t" \
  "v_mfma_f32_16x16x32_bf16 v[64:67], a[196:199], v[84:87], v[64:67]\n\t" \
  "v_exp_f32 v102, v102\n\t" \
  "v_mfma_f32_16x16x32_bf16 v[64:67], a[200:203], v[88:91], v[64:67]\n\t" \
  "ds_read_b64_tr_b16 a[166:167], v228 offset:12800\n\t" \
  "v_cvt_pk_bf16_f32 v152, v96, v97\n\t" \
  "v_mfma_f32_16x16x32_bf16 v[64:67], a[204:207], v[92:95], v[64:67]\n\t" \
  "ds_read_b64_tr_b16 a[168:169], v229 offset:512\n\t" \
  "v_exp_f32 v103, v103\n\t" \
  "v_mfma_f32_16x16x32_bf16 v[68:71], a[220:223], v[92:95], v[184:187]\n\t" \
  "v_exp_f32 v104, v104\n\t" \
  "v_mfma_f32_16x16x32_bf16 v[68:71], a[216:219], v[88:91], v[68:71]\n\t" \
  ".Lhs6:\n\ts_cbranch_execz .Lhs6n\n\t" \
  "ds_read_b64_tr_b16 a[170:171], v229 offset:4608\n\t" \
  "v_cvt_pk_bf16_f32 v153, v98, v99\n\t" \
  "v_mfma_f32_16x16x32_bf16 v[68:71], a[212:215], v[84:87], v[68:71]\n\t" \
  "v_exp_f32 v105, v105\n\t" \
  "v_mfma_f32_16x16x32_bf16 v[68:71], a[208:211], v[80:83], v[68:71]\n\t" \
  "ds_read_b64_tr_b16 a[172:173], v229 offset:8704\n\t" \
  "v_cvt_pk_bf16_f32 v154, v100, v101\n\t" \
  "v_mfma_f32_16x16x32_bf16 v[72:75], a[224:227], v[80:83], v[184:187]\n\t" \
  "v_exp_f32 v106, v106\n\t" \
  "v_mfma_f32_16x16x32_bf16 v[72:75], a[228:231], v[84:87], v[72:75]\n\t" \
  "ds_read_b64_tr_b16 a[174:175], v229 offset:12800\n\t" \
  "v_cvt_pk_bf16_f32 v155, v102, v103\n\t" \
  "v_mfma_f32_16x16x32_bf16 v[72:75], a[232:235], v[88:91], v[72:75]\n\t" \
  "v_exp_f32 v107, v107\n\t" \
  "v_mfma_f32_16x16x32_bf16 v[72:75], a[236:239], v[92:95], v[72:75]\n\t" \
  "ds_read_b64_tr_b16 a[176:177], v228 offset:768\n\t" \
  "v_cvt_pk_bf16_f32 v156, v104, v105\n\t" \
  "v_mfma_f32_16x16x32_bf16 v[76:79], a[252:255], v[92:95], v[184:187]\n\t" \
  "v_exp_f32 v108, v108\n\t" \
  "v_mfma_f32_16x16x32_bf16 v[76:79], a[248:251], v[88:91], v[76:79]\n\t" \
  "ds_read_b64_tr_b16 a[178:179], v228 offset:4864\n\t" \
  "v_cvt_pk_bf16_f32 v157, v106, v107\n\t" \
  "v_mfma_f32_16x16x32_bf16 v[76:79], a[244:247], v[84:87], v[76:79]\n\t" \
  "v_exp_f32 v109, v109\n\t" \
  "v_mfma_f32_16x16x32_bf16 v[76:79], a[240:243], v[80:83], v[76:79]\n\t" \
  "ds_read_b64_tr_b16 a[180:181], v228 offset:8960\n\t" \
  "v_cvt_pk_bf16_f32 v158, v108, v109\n\t" \
  "v_mfma_f32_16x16x32_bf16 v[96:99], a[192:195], v[112:115], v[188:191]\n\t" \
  "v_exp_f32 v110, v110\n\t" \
  "v_mfma_f32_16x16x32_bf16 v[96:99], a[196:199], v[116:119], v[96:99]\n\t" \
  "s_add_u32 s99, s53, 43008\n\t" \
  "v_exp_f32 v111, v111\n\t" \
  "v_mfma_f32_16x16x32_bf16 v[96:99], a[200:203], v[120:123], v[96:99]\n\t" \
  "ds_read_b64_tr_b16 a[182:183], v228 offset:13056\n\t" \
  "v_cvt_pk_bf16_f32 v159, v110, v111\n\t" \
  "v_mfma_f32_16x16x32_bf16 v[96:99], a[204:207], v[124:127], v[96:99]\n\t" \
  "v_exp_f32 v0, v0\n\t" \
  "v_mfma_f32_16x16x32_bf16 v[100:103], a[220:223], v[124:127], v[188:191]\n\t" \
  "buffer_load_dword v246, v245, s[72:75], s99 offen\n\t" \
  "v_mfma_f32_16x16x32_bf16 v[100:103], a[216:219], v[120:123], v[100:103]\n\t" \
  "ds_read_b64_tr_b16 a[184:185], v229 offset:768\n\t" \
  "v_mfma_f32_16x16x32_bf16 v[100:103], a[212:215], v[116:119], v[100:103]\n\t" \
  "ds_read_b64_tr_b16 a[186:187], v229 offset:4864\n\t" \
  "v_mfma_f32_16x16x32_bf16 v[100:103], a[208:211], v[112:115], v[100:103]\n\t" \
  "ds_read_b64_tr_b16 a[188:189], v229 offset:8960\n\t" \
  "v_mfma_f32_16x16x32_bf16 v[104:107], a[224:227], v[112:115], v[188:191]\n\t" \
  "v_exp_f32 v1, v1\n\t" \
  "v_mfma_f32_16x16x32_bf16 v[104:107], a[228:231], v[116:119], v[104:107]\n\t" \
  "ds_read_b64_tr_b16 a[190:191], v229 offset:13056\n\t" \
  "v_mfma_f32_16x16x32_bf16 v[104:107], a[232:235], v[120:123], v[104:107]\n\t" \
  "v_exp_f32 v2, v2\n\t" \
  "v_mfma_f32_16x16x32_bf16 v[104:107], a[236:239], v[124:127], v[104:107]\n\t" \
  "v_exp_f32 v3, v3\n\t" \
  "v_mfma_f32_16x16x32_bf16 v[108:111], a[252:255], v[124:127], v[188:191]\n\t" \
  "v_exp_f32 v4, v4\n\t" \
  "v_mfma_f32_16x16x32_bf16 v[108:111], a[248:251], v[120:123], v[108:111]\n\t" \
  "v_exp_f32 v5, v5\n\t" \
  "v_mfma_f32_16x16x32_bf16 v[108:111], a[244:247], v[116:119], v[108:111]\n\t" \
  "s_waitcnt vmcnt(3) lgkmcnt(0)\n\t" \
  "v_exp_f32 v6, v6\n\t" \
  "v_mfma_f32_16x16x32_bf16 v[108:111], a[240:243], v[112:115], v[108:111]\n\t" \
  "s_barrier\n\t" \
  STA("s_memrealtime s[94:95]\n\t") \
  STA("s_memtime s[92:93]\n\t") \
  "v_mfma_f32_16x16x32_bf16 a[0:3], a[128:131], v[128:131], 0\n\t" \
  "v_exp_f32 v7, v7\n\t" \
  "v_mfma_f32_16x16x32_bf16 a[0:3], a[132:135], v[132:135], a[0:3]\n\t" \
  "v_exp_f32 v8, v8\n\t" \
  "v_mfma_f32_16x16x32_bf16 a[4:7], a[136:139], v[128:131], 0\n\t" \
  "v_exp_f32 v9, v9\n\t" \
  "v_mfma_f32_16x16x32_bf16 a[4:7], a[140:143], v[132:135], a[4:7]\n\t" \
  "v_exp_f32 v10, v10\n\t" \
  "v_mfma_f32_16x16x32_bf16 a[8:11], a[144:147], v[128:131], 0\n\t" \
  "v_exp_f32 v11, v11\n\t" \
  "v_mfma_f32_16x16x32_bf16 a[8:11], a[148:151], v[132:135], a[8:11]\n\t" \
  "v_exp_f32 v12, v12\n\t" \
  "v_mfma_f32_16x16x32_bf16 a[12:15], a[152:155], v[128:131], 0\n\t" \
  "v_exp_f32 v13, v13\n\t" \
  "v_mfma_f32_16x16x32_bf16 a[12:15], a[156:159], v[132:135], a[12:15]\n\t" \
  "v_exp_f32 v14, v14\n\t" \
  "v_mfma_f32_16x16x32_bf16 a[16:19], a[160:163], v[128:131], 0\n\t" \
  "v_exp_f32 v15, v15\n\t" \
  "v_mfma_f32_16x16x32_bf16 a[16:19], a[164:167], v[132:135], a[16:19]\n\t" \
  "v_exp_f32 v32, v32\n\t" \
  "v_mfma_f32_16x16x32_bf16 a[20:23], a[168:171], v[128:131], 0\n\t" \
  "v_exp_f32 v33, v33\n\t" \
  "v_mfma_f32_16x16x32_bf16 a[20:23], a[172:175], v[132:135], a[20:23]\n\t" \
  "v_exp_f32 v34, v34\n\t" \
  "v_mfma_f32_16x16x32_bf16 a[24:27], a[176:179], v[128:131], 0\n\t" \
  STA("v_writelane_b32 v255, s94, 28\n\t") \
  STA("v_writelane_b32 v255, s92, 29\n\t") \
  "v_exp_f32 v35, v35\n\t" \
  "v_mfma_f32_16x16x32_bf16 a[24:27], a[180:183], v[132:135], a[24:27]\n\t" \
  "v_exp_f32 v36, v36\n\t" \
  "v_mfma_f32_16x16x32_bf16 a[28:31], a[184:187], v[128:131], 0\n\t" \
  "v_exp_f32 v37, v37\n\t" \
  "v_mfma_f32_16x16x32_bf16 a[28:31], a[188:191], v[132:135], a[28:31]\n\t" \
  "v_exp_f32 v38, v38\n\t" \
  "v_mfma_f32_16x16x32_bf16 v[160:163], v[216:219], v[128:131], 0\n\t" \
  "s_add_u32 m0, s38, 65536\n\t" \
  "v_exp_f32 v39, v39\n\t" \
  "v_mfma_f32_16x16x32_bf16 v[160:163], v[216:219], v[132:135], v[160:163]\n\t" \
  "s_add_u32 s96, s38, 49152\n\t" \
  "ds_read_b128 a[192:195], v220 offset:0\n\t" \
  "v_cvt_pk_bf16_f32 v128, v0, v1\n\t" \
  "v_mfma_f32_16x16x32_bf16 a[32:35], a[128:131], v[136:139], 0\n\t" \
  "ds_read_b128 a[196:199], v221 offset:0\n\t" \
  "v_cvt_pk_bf16_f32 v129, v2, v3\n\t" \
  "v_mfma_f32_16x16x32_bf16 a[32:35], a[132:135], v[140:143], a[32:35]\n\t" \
  "v_exp_f32 v40, v40\n\t" \
  "v_mfma_f32_16x16x32_bf16 a[36:39], a[136:139], v[136:139], 0\n\t" \
  "buffer_load_dwordx4 v224, s[60:63], s96 offen offset:0 lds\n\t" \
  "v_mfma_f32_16x16x32_bf16 a[36:39], a[140:143], v[140:143], a[36:39]\n\t" \
  "ds_read_b128 a[200:203], v222 offset:0\n\t" \
  "v_cvt_pk_bf16_f32 v130, v4, v5\n\t" \
  "v_mfma_f32_16x16x32_bf16 a[40:43], a[144:147], v[136:139], 0\n\t" \
  "v_exp_f32 v41, v41\n\t" \
  "v_mfma_f32_16x16x32_bf16 a[40:43], a[148:151], v[140:143], a[40:43]\n\t" \
  "ds_read_b128 a[204:207], v223 offset:0\n\t" \
  "v_cvt_pk_bf16_f32 v131, v6, v7\n\t" \
  "v_mfma_f32_16x16x32_bf16 a[44:47], a[152:155], v[136:139], 0\n\t" \
  "v_exp_f32 v42, v42\n\t" \
  "v_mfma_f32_16x16x32_bf16 a[44:47], a[156:159], v[140:143], a[44:47]\n\t" \
  "v_exp_f32 v43, v43\n\t" \
  "v_mfma_f32_16x16x32_bf16 a[48:51], a[160:163], v[136:139], 0\n\t" \
  "buffer_load_dwordx4 v225, s[60:63], s96 offen offset:1024 lds\n\t" \
  "v_mfma_f32_16x16x32_bf16 a[48:51], a[164:167], v[140:143], a[48:51]\n\t" \
  "ds_read_b128 a[208:211], v220 offset:4096\n\t" \
  "v_cvt_pk_bf16_f32 v132, v8, v9\n\t" \
  "v_mfma_f32_16x16x32_bf16 a[52:55], a[168:171], v[136:139], 0\n\t" \
  "v_exp_f32 v44, v44\n\t" \
  "v_mfma_f32_16x16x32_bf16 a[52:55], a[172:175], v[140:143], a[52:55]\n\t" \
  "ds_read_b128 a[212:215], v221 offset:4096\n\t" \
  "v_cvt_pk_bf16_f32 v133, v10, v11\n\t" \
  "v_mfma_f32_16x16x32_bf16 a[56:59], a[176:179], v[136:139], 0\n\t" \
  "v_exp_f32 v45, v45\n\t" \
  "v_mfma_f32_16x16x32_bf16 a[56:59], a[180:183], v[140:143], a[56:59]\n\t" \
  "v_exp_f32 v46, v46\n\t" \
  "v_mfma_f32_16x16x32_bf16 a[60:63], a[184:187], v[136:139], 0\n\t" \
  "buffer_load_dwordx4 v226, s[60:63], s96 offen offset:2048 lds\n\t" \
  "v_mfma_f32_16x16x32_bf16 a[60:63], a[188:191], v[140:143], a[60:63]\n\t" \
  "ds_read_b128 a[216:219], v222 offset:4096\n\t" \
  "v_cvt_pk_bf16_f32 v134, v12, v13\n\t" \
  "v_mfma_f32_16x16x32_bf16 v[164:167], v[216:219], v[136:139], 0\n\t" \
  "v_exp_f32 v47, v47\n\t" \
  "v_mfma_f32_16x16x32_bf16 v[164:167], v[216:219], v[140:143], v[164:167]\n\t" \
  "ds_read_b128 a[220:223], v223 offset:4096\n\t" \
  "v_cvt_pk_bf16_f32 v135, v14, v15\n\t" \
  "v_mfma_f32_16x16x32_bf16 a[64:67], a[128:131], v[144:147], 0\n\t" \
  "v_exp_f32 v64, v64\n\t" \
  "v_mfma_f32_16x16x32_bf16 a[64:67], a[132:135], v[148:151], a[64:67]\n\t" \
  "v_exp_f32 v65, v65\n\t" \
  "v_mfma_f32_16x16x32_bf16 a[68:71], a[136:139], v[144:147], 0\n\t" \
  "buffer_load_dwordx4 v227, s[60:63], s96 offen offset:3072 lds\n\t" \
  "v_mfma_f32_16x16x32_bf16 a[68:71], a[140:143], v[148:151], a[68:71]\n\t" \
  "ds_read_b128 a[224:227], v220 offset:8192\n\t" \
  "v_cvt_pk_bf16_f32 v136, v32, v33\n\t" \
  "v_mfma_f32_16x16x32_bf16 a[72:75], a[144:147], v[144:147], 0\n\t" \
  "s_add_u32 m0, s38, 81920\n\t" \
  "v_exp_f32 v66, v66\n\t" \
  "v_mfma_f32_16x16x32_bf16 a[72:75], a[148:151], v[148:151], a[72:75]\n\t" \
  "s_add_u32 s96, s38, 32768\n\t" \
  "ds_read_b128 a[228:231], v221 offset:8192\n\t" \
  "v_cvt_pk_bf16_f32 v137, v34, v35\n\t" \
  "v_mfma_f32_16x16x32_bf16 a[76:79], a[152:155], v[144:147], 0\n\t" \
  "v_exp_f32 v67, v67\n\t" \
  "v_mfma_f32_16x16x32_bf16 a[76:79], a[156:159], v[148:151], a[76:79]\n\t" \
  "v_exp_f32 v68, v68\n\t" \
  "v_mfma_f32_16x16x32_bf16 a[80:83], a[160:163], v[144:147], 0\n\t" \
  "buffer_load_dwordx4 v230, s[64:67], s96 offen offset:0 lds\n\t" \
  "v_mfma_f32_16x16x32_bf16 a[80:83], a[164:167], v[148:151], a[80:83]\n\t" \
  "ds_read_b128 a[232:235], v222 offset:8192\n\t" \
  "v_cvt_pk_bf16_f32 v138, v36, v37\n\t" \
  "v_mfma_f32_16x16x32_bf16 a[84:87], a[168:171], v[144:147], 0\n\t" \
  "v_exp_f32 v69, v69\n\t" \
  "v_mfma_f32_16x16x32_bf16 a[84:87], a[172:175], v[148:151], a[84:87]\n\t" \
  "ds_read_b128 a[236:239], v223 offset:8192\n\t" \
  "v_cvt_pk_bf16_f32 v139, v38, v39\n\t" \
  "v_mfma_f32_16x16x32_bf16 a[88:91], a[176:179], v[144:147], 0\n\t" \
  "v_exp_f32 v70, v70\n\t" \
  "v_mfma_f32_16x16x32_bf16 a[88:91], a[180:183], v[148:151], a[88:91]\n\t" \
  "v_exp_f32 v71, v71\n\t" \
  "v_mfma_f32_16x16x32_bf16 a[92:95], a[184:187], v[144:147], 0\n\t" \
  "buffer_load_dwordx4 v231, s[64:67], s96 offen offset:1024 lds\n\t" \
  "v_mfma_f32_16x16x32_bf16 a[92:95], a[188:191], v[148:151], a[92:95]\n\t" \
  "ds_read_b128 a[240:243], v220 offset:12288\n\t" \
  "v_cvt_pk_bf16_f32 v140, v40, v41\n\t" \
  "v_mfma_f32_16x16x32_bf16 v[168:171], v[216:219], v[144:147], 0\n\t" \
  "v_exp_f32 v72, v72\n\t" \
  "v_mfma_f32_16x16x32_bf16 v[168:171], v[216:219], v[148:151], v[168:171]\n\t" \
  "ds_read_b128 a[244:247], v221 offset:12288\n\t" \
  "v_cvt_pk_bf16_f32 v141, v42, v43\n\t" \
  "v_mfma_f32_16x16x32_bf16 a[96:99], a[128:131], v[152:155], 0\n\t" \
  "v_exp_f32 v73, v73\n\t" \
  "v_mfma_f32_16x16x32_bf16 a[96:99], a[132:135], v[156:159], a[96:99]\n\t" \
  "v_exp_f32 v74, v74\n\t" \
  "v_mfma_f32_16x16x32_bf16 a[100:103], a[136:139], v[152:155], 0\n\t" \
  "buffer_load_dwordx4 v230, s[64:67], s96 offen offset:2048 lds\n\t" \
  "v_mfma_f32_16x16x32_bf16 a[100:103], a[140:143], v[156:159], a[100:103]\n\t" \
  "ds_read_b128 a[248:251], v222 offset:12288\n\t" \
  "v_cvt_pk_bf16_f32 v142, v44, v45\n\t" \
  "v_mfma_f32_16x16x32_bf16 a[104:107], a[144:147], v[152:155], 0\n\t" \
  "v_exp_f32 v75, v75\n\t" \
  "v_mfma_f32_16x16x32_bf16 a[104:107], a[148:151], v[156:159], a[104:107]\n\t" \
  "ds_read_b128 a[252:255], v223 offset:12288\n\t" \
  "v_cvt_pk_bf16_f32 v143, v46, v47\n\t" \
  "v_mfma_f32_16x16x32_bf16 a[108:111], a[152:155], v[152:155], 0\n\t" \
  "v_exp_f32 v76, v76\n\t" \
  "v_mfma_f32_16x16x32_bf16 a[108:111], a[156:159], v[156:159], a[108:111]\n\t" \
  "v_exp_f32 v77, v77\n\t" \
  "v_mfma_f32_16x16x32_bf16 a[112:115], a[160:163], v[152:155], 0\n\t" \
  "buffer_load_dwordx4 v231, s[64:67], s96 offen offset:3072 lds\n\t" \
  "v_mfma_f32_16x16x32_bf16 a[112:115], a[164:167], v[156:159], a[112:115]\n\t" \
  "v_exp_f32 v78, v78\n\t" \
  "v_mfma_f32_16x16x32_bf16 a[116:119], a[168:171], v[152:155], 0\n\t" \
  "v_exp_f32 v79, v79\n\t" \
  "v_mfma_f32_16x16x32_bf16 a[116:119], a[172:175], v[156:159], a[116:119]\n\t" \
  "v_exp_f32 v96, v96\n\t" \
  "v_mfma_f32_16x16x32_bf16 a[120:123], a[176:179], v[152:155], 0\n\t" \
  "v_cvt_pk_bf16_f32 v144, v64, v65\n\t" \
  "v_cvt_pk_bf16_f32 v145, v66, v67\n\t" \
  "v_mfma_f32_16x16x32_bf16 a[120:123], a[180:183], v[156:159], a[120:123]\n\t" \
  "v_exp_f32 v97, v97\n\t" \
  "v_mfma_f32_16x16x32_bf16 a[124:127], a[184:187], v[152:155], 0\n\t" \
  "v_exp_f32 v98, v98\n\t" \
  "v_mfma_f32_16x16x32_bf16 a[124:127], a[188:191], v[156:159], a[124:127]\n\t" \
  "v_cvt_pk_bf16_f32 v146, v68, v69\n\t" \
  "v_cvt_pk_bf16_f32 v147, v70, v71\n\t" \
  "v_mfma_f32_16x16x32_bf16 v[172:175], v[216:219], v[152:155], 0\n\t" \
  "v_exp_f32 v99, v99\n\t" \
  "v_mfma_f32_16x16x32_bf16 v[172:175], v[216:219], v[156:159], v[172:175]\n\t" \
  "s_waitcnt lgkmcnt(12)\n\t" \
  "v_exp_f32 v100, v100\n\t" \
  STA("s_memrealtime s[94:95]\n\t") \
  STA("s_memtime s[92:93]\n\t") \
  "v_mfma_f32_16x16x32_bf16 v[0:3], a[192:195], v[16:19], v[176:179]\n\t" \
  "ds_read_b64_tr_b16 a[128:129], v210 offset:0\n\t" \
  "v_cvt_pk_bf16_f32 v148, v72, v73\n\t" \
  "v_mfma_f32_16x16x32_bf16 v[0:3], a[196:199], v[20:23], v[0:3]\n\t" \
  "v_exp_f32 v101, v101\n\t" \
  "v_mfma_f32_16x16x32_bf16 v[0:3], a[200:203], v[24:27], v[0:3]\n\t" \
  "ds_read_b64_tr_b16 a[130:131], v210 offset:4096\n\t" \
  "v_cvt_pk_bf16_f32 v149, v74, v75\n\t" \
  "v_mfma_f32_16x16x32_bf16 v[0:3], a[204:207], v[28:31], v[0:3]\n\t" \
  "s_waitcnt lgkmcnt(10)\n\t" \
  "v_exp_f32 v102, v102\n\t" \
  "v_mfma_f32_16x16x32_bf16 v[4:7], a[220:223], v[28:31], v[176:179]\n\t" \
  "ds_read_b64_tr_b16 a[132:133], v210 offset:8192\n\t" \
  "v_cvt_pk_bf16_f32 v150, v76, v77\n\t" \
  "v_mfma_f32_16x16x32_bf16 v[4:7], a[216:219], v[24:27], v[4:7]\n\t" \
  "v_exp_f32 v103, v103\n\t" \
  "v_mfma_f32_16x16x32_bf16 v[4:7], a[212:215], v[20:23], v[4:7]\n\t" \
  "ds_read_b64_tr_b16 a[134:135], v210 offset:12288\n\t" \
  "v_cvt_pk_bf16_f32 v151, v78, v79\n\t" \
  "v_mfma_f32_16x16x32_bf16 v[4:7], a[208:211], v[16:19], v[4:7]\n\t" \
  "s_waitcnt lgkmcnt(8)\n\t" \
  "ds_read_b64_tr_b16 a[136:137], v211 offset:0\n\t" \
  "v_cvt_pk_bf16_f32 v152, v96, v97\n\t" \
  "v_mfma_f32_16x16x32_bf16 v[8:11], a[224:227], v[16:19], v[176:179]\n\t" \
  "v_exp_f32 v104, v104\n\t" \
  "v_mfma_f32_16x16x32_bf16 v[8:11], a[228:231], v[20:23], v[8:11]\n\t" \
  "ds_read_b64_tr_b16 a[138:139], v211 offset:4096\n\t" \
  "v_cvt_pk_bf16_f32 v153, v98, v99\n\t" \
  "v_mfma_f32_16x16x32_bf16 v[8:11], a[232:235], v[24:27], v[8:11]\n\t" \
  "v_exp_f32 v105, v105\n\t" \
  "v_mfma_f32_16x16x32_bf16 v[8:11], a[236:239], v[28:31], v[8:11]\n\t" \
  "s_waitcnt lgkmcnt(6)\n\t" \
  "ds_read_b64_tr_b16 a[140:141], v211 offset:8192\n\t" \
  "v_cvt_pk_bf16_f32 v154, v100, v101\n\t" \
  "v_mfma_f32_16x16x32_bf16 v[12:15], a[252:255], v[28:31], v[176:179]\n\t" \
  STA("v_writelane_b32 v255, s94, 30\n\t") \
  STA("v_writelane_b32 v255, s92, 31\n\t") \
  "v_exp_f32 v106, v106\n\t" \
  "v_mfma_f32_16x16x32_bf16 v[12:15], a[248:251], v[24:27], v[12:15]\n\t" \
  "ds_read_b64_tr_b16 a[142:143], v211 offset:12288\n\t" \
  "v_cvt_pk_bf16_f32 v155, v102, v103\n\t" \
  "v_mfma_f32_16x16x32_bf16 v[12:15], a[244:247], v[20:23], v[12:15]\n\t" \
  "ds_read_b64_tr_b16 a[144:145], v210 offset:256\n\t" \
  "v_cvt_pk_bf16_f32 v156, v104, v105\n\t" \
  "v_mfma_f32_16x16x32_bf16 v[12:15], a[240:243], v[16:19], v[12:15]\n\t" \
  "v_exp_f32 v107, v107\n\t" \
  "v_mfma_f32_16x16x32_bf16 v[32:35], a[192:195], v[48:51], v[180:183]\n\t" \
  "s_add_u32 m0, s38, 16384\n\t" \
  "ds_read_b64_tr_b16 a[146:147], v210 offset:4352\n\t" \
  "v_cvt_pk_bf16_f32 v157, v106, v107\n\t" \
  "v_mfma_f32_16x16x32_bf16 v[32:35], a[196:199], v[52:55], v[32:35]\n\t" \
  "s_mov_b32 s96, 0\n\t" \
  "v_exp_f32 v108, v108\n\t" \
  "v_mfma_f32_16x16x32_bf16 v[32:35], a[200:203], v[56:59], v[32:35]\n\t" \
  "v_exp_f32 v109, v109\n\t" \
  "v_mfma_f32_16x16x32_bf16 v[32:35], a[204:207], v[60:63], v[32:35]\n\t" \
  "ds_read_b64_tr_b16 a[148:149], v210 offset:8448\n\t" \
  "v_cvt_pk_bf16_f32 v158, v108, v109\n\t" \
  "v_mfma_f32_16x16x32_bf16 v[36:39], a[220:223], v[60:63], v[180:183]\n\t" \
  "buffer_load_dwordx4 v232, s[68:71], s96 offen offset:0 lds\n\t" \
  "v_mfma_f32_16x16x32_bf16 v[36:39], a[216:219], v[56:59], v[36:39]\n\t" \
  "v_exp_f32 v110, v110\n\t" \
  "v_mfma_f32_16x16x32_bf16 v[36:39], a[212:215], v[52:55], v[36:39]\n\t" \
  "v_exp_f32 v111, v111\n\t" \
  "v_mfma_f32_16x16x32_bf16 v[36:39], a[208:211], v[48:51], v[36:39]\n\t" \
  "ds_read_b64_tr_b16 a[150:151], v210 offset:12544\n\t" \
  "v_cvt_pk_bf16_f32 v159, v110, v111\n\t" \
  "v_mfma_f32_16x16x32_bf16 v[40:43], a[224:227], v[48:51], v[180:183]\n\t" \
  "v_exp_f32 v0, v0\n\t" \
  "v_mfma_f32_16x16x32_bf16 v[40:43], a[228:231], v[52:55], v[40:43]\n\t" \
  "ds_read_b64_tr_b16 a[152:153], v211 offset:256\n\t" \
  "v_mfma_f32_16x16x32_bf16 v[40:43], a[232:235], v[56:59], v[40:43]\n\t" \
  "ds_read_b64_tr_b16 a[154:155], v211 offset:4352\n\t" \
  "s_mov_b32 s96, 6144\n\t" \
  "v_mfma_f32_16x16x32_bf16 v[40:43], a[236:239], v[60:63], v[40:43]\n\t" \
  "v_exp_f32 v1, v1\n\t" \
  "v_mfma_f32_16x16x32_bf16 v[44:47], a[252:255], v[60:63], v[180:183]\n\t" \
  "ds_read_b64_tr_b16 a[156:157], v211 offset:8448\n\t" \
  "v_mfma_f32_16x16x32_bf16 v[44:47], a[248:251], v[56:59], v[44:47]\n\t" \
  "v_exp_f32 v2, v2\n\t" \
  "v_mfma_f32_16x16x32_bf16 v[44:47], a[244:247], v[52:55], v[44:47]\n\t" \
  "buffer_load_dwordx4 v232, s[68:71], s96 offen offset:1024 lds\n\t" \
  "v_mfma_f32_16x16x32_bf16 v[44:47], a[240:243], v[48:51], v[44:47]\n\t" \
  "ds_read_b64_tr_b16 a[158:159], v211 offset:12544\n\t" \
  "v_mfma_f32_16x16x32_bf16 v[64:67], a[192:195], v[80:83], v[184:187]\n\t" \
  "ds_read_b64_tr_b16 a[160:161], v210 offset:512\n\t" \
  "v_mfma_f32_16x16x32_bf16 v[64:67], a[196:199], v[84:87], v[64:67]\n\t" \
  "ds_read_b64_tr_b16 a[162:163], v210 offset:4608\n\t" \
  "v_mfma_f32_16x16x32_bf16 v[64:67], a[200:203], v[88:91], v[64:67]\n\t" \
  "v_exp_f32 v3, v3\n\t" \
  "v_mfma_f32_16x16x32_bf16 v[64:67], a[204:207], v[92:95], v[64:67]\n\t" \
  "ds_read_b64_tr_b16 a[164:165], v210 offset:8704\n\t" \
  "v_mfma_f32_16x16x32_bf16 v[68:71], a[220:223], v[92:95], v[184:187]\n\t" \
  "ds_read_b64_tr_b16 a[166:167], v210 offset:12800\n\t" \
  "s_mov_b32 s96, 12288\n\t" \
  "v_mfma_f32_16x16x32_bf16 v[68:71], a[216:219], v[88:91], v[68:71]\n\t" \
  "v_exp_f32 v4, v4\n\t" \
  "v_mfma_f32_16x16x32_bf16 v[68:71], a[212:215], v[84:87], v[68:71]\n\t" \
  "ds_read_b64_tr_b16 a[168:169], v211 offset:512\n\t" \
  "v_mfma_f32_16x16x32_bf16 v[68:71], a[208:211], v[80:83], v[68:71]\n\t" \
  "v_exp_f32 v5, v5\n\t" \
  "v_mfma_f32_16x16x32_bf16 v[72:75], a[224:227], v[80:83], v[184:187]\n\t" \
  "buffer_load_dwordx4 v232, s[68:71], s96 offen offset:2048 lds\n\t" \
  "v_mfma_f32_16x16x32_bf16 v[72:75], a[228:231], v[84:87], v[72:75]\n\t" \
  "ds_read_b64_tr_b16 a[170:171], v211 offset:4608\n\t" \
  "v_mfma_f32_16x16x32_bf16 v[72:75], a[232:235], v[88:91], v[72:75]\n\t" \
  "ds_read_b64_tr_b16 a[172:173], v211 offset:8704\n\t" \
  "v_mfma_f32_16x16x32_bf16 v[72:75], a[236:239], v[92:95], v[72:75]\n\t" \
  "ds_read_b64_tr_b16 a[174:175], v211 offset:12800\n\t" \
  "v_mfma_f32_16x16x32_bf16 v[76:79], a[252:255], v[92:95], v[184:187]\n\t" \
  "v_exp_f32 v6, v6\n\t" \
  "v_mfma_f32_16x16x32_bf16 v[76:79], a[248:251], v[88:91], v[76:79]\n\t" \
  "ds_read_b64_tr_b16 a[176:177], v210 offset:768\n\t" \
  "v_mfma_f32_16x16x32_bf16 v[76:79], a[244:247], v[84:87], v[76:79]\n\t" \
  "s_mov_b32 s96, 18432\n\t" \
  "v_exp_f32 v7, v7\n\t" \
  "v_mfma_f32_16x16x32_bf16 v[76:79], a[240:243], v[80:83], v[76:79]\n\t" \
  "ds_read_b64_tr_b16 a[178:179], v210 offset:4864\n\t" \
  "v_mfma_f32_16x16x32_bf16 v[96:99], a[192:195], v[112:115], v[188:191]\n\t" \
  "ds_read_b64_tr_b16 a[180:181], v210 offset:8960\n\t" \
  "v_mfma_f32_16x16x32_bf16 v[96:99], a[196:199], v[116:119], v[96:99]\n\t" \
  "v_exp_f32 v8, v8\n\t" \
  "v_mfma_f32_16x16x32_bf16 v[96:99], a[200:203], v[120:123], v[96:99]\n\t" \
  "buffer_load_dwordx4 v232, s[68:71], s96 offen offset:3072 lds\n\t" \
  "v_mfma_f32_16x16x32_bf16 v[96:99], a[204:207], v[124:127], v[96:99]\n\t" \
  "ds_read_b64_tr_b16 a[182:183], v210 offset:13056\n\t" \
  "v_mfma_f32_16x16x32_bf16 v[100:103], a[220:223], v[124:127], v[188:191]\n\t" \
  "ds_read_b64_tr_b16 a[184:185], v211 offset:768\n\t" \
  "v_mfma_f32_16x16x32_bf16 v[100:103], a[216:219], v[120:123], v[100:103]\n\t" \
  "ds_read_b64_tr_b16 a[186:187], v211 offset:4864\n\t" \
  "v_mfma_f32_16x16x32_bf16 v[100:103], a[212:215], v[116:119], v[100:103]\n\t" \
  "ds_read_b64_tr_b16 a[188:189], v211 offset:8960\n\t" \
  "v_mfma_f32_16x16x32_bf16 v[100:103], a[208:211], v[112:115], v[100:103]\n\t" \
  "v_exp_f32 v9, v9\n\t" \
  "v_mfma_f32_16x16x32_bf16 v[104:107], a[224:227], v[112:115], v[188:191]\n\t" \
  "ds_read_b64_tr_b16 a[190:191], v211 offset:13056\n\t" \
  "v_mfma_f32_16x16x32_bf16 v[104:107], a[228:231], v[116:119], v[104:107]\n\t" \
  "v_exp_f32 v10, v10\n\t" \
  "v_mfma_f32_16x16x32_bf16 v[104:107], a[232:235], v[120:123], v[104:107]\n\t" \
  "v_exp_f32 v11, v11\n\t" \
  "v_mfma_f32_16x16x32_bf16 v[104:107], a[236:239], v[124:127], v[104:107]\n\t" \
  "v_exp_f32 v12, v12\n\t" \
  "v_mfma_f32_16x16x32_bf16 v[108:111], a[252:255], v[124:127], v[188:191]\n\t" \
  "v_exp_f32 v13, v13\n\t" \
  "v_mfma_f32_16x16x32_bf16 v[108:111], a[248:251], v[120:123], v[108:111]\n\t" \
  "v_exp_f32 v14, v14\n\t" \
  "v_mfma_f32_16x16x32_bf16 v[108:111], a[244:247], v[116:119], v[108:111]\n\t" \
  "s_waitcnt vmcnt(4) lgkmcnt(0)\n\t" \
  "v_exp_f32 v15, v15\n\t" \
  "v_mfma_f32_16x16x32_bf16 v[108:111], a[240:243], v[112:115], v[108:111]\n\t" \
  "s_barrier\n\t" \
  STA("s_memrealtime s[94:95]\n\t") \
  STA("s_memtime s[92:93]\n\t") \
  "v_mfma_f32_16x16x32_bf16 a[0:3], a[128:131], v[128:131], a[0:3]\n\t" \
  "v_exp_f32 v32, v32\n\t" \
  "v_mfma_f32_16x16x32_bf16 a[0:3], a[132:135], v[132:135], a[0:3]\n\t" \
  "v_exp_f32 v33, v33\n\t" \
  "v_mfma_f32_16x16x32_bf16 a[4:7], a[136:139], v[128:131], a[4:7]\n\t" \
  "v_exp_f32 v34, v34\n\t" \
  "v_mfma_f32_16x16x32_bf16 a[4:7], a[140:143], v[132:135], a[4:7]\n\t" \
  "v_exp_f32 v35, v35\n\t" \
  "v_mfma_f32_16x16x32_bf16 a[8:11], a[144:147], v[128:131], a[8:11]\n\t" \
  "v_exp_f32 v36, v36\n\t" \
  "v_mfma_f32_16x16x32_bf16 a[8:11], a[148:151], v[132:135], a[8:11]\n\t" \
  "v_exp_f32 v37, v37\n\t" \
  "v_mfma_f32_16x16x32_bf16 a[12:15], a[152:155], v[128:131], a[12:15]\n\t" \
  "v_exp_f32 v38, v38\n\t" \
  "v_mfma_f32_16x16x32_bf16 a[12:15], a[156:159], v[132:135], a[12:15]\n\t" \
  "v_exp_f32 v39, v39\n\t" \
  "v_mfma_f32_16x16x32_bf16 a[16:19], a[160:163], v[128:131], a[16:19]\n\t" \
  "v_exp_f32 v40, v40\n\t" \
  "v_mfma_f32_16x16x32_bf16 a[16:19], a[164:167], v[132:135], a[16:19]\n\t" \
  "v_exp_f32 v41, v41\n\t" \
  "v_mfma_f32_16x16x32_bf16 a[20:23], a[168:171], v[128:131], a[20:23]\n\t" \
  "v_exp_f32 v42, v42\n\t" \
  "v_mfma_f32_16x16x32_bf16 a[20:23], a[172:175], v[132:135], a[20:23]\n\t" \
  "v_exp_f32 v43, v43\n\t" \
  "v_mfma_f32_16x16x32_bf16 a[24:27], a[176:179], v[128:131], a[24:27]\n\t" \
  STA("v_writelane_b32 v255, s94, 32\n\t") \
  STA("v_writelane_b32 v255, s92, 33\n\t") \
  "buffer_store_dword v233, v247, s[16:19], 0 offen sc1\n\t" \
  "ds_read_b128 a[192:195], v220 offset:16384\n\t" \
  "v_mfma_f32_16x16x32_bf16 a[24:27], a[180:183], v[132:135], a[24:27]\n\t" \
  "v_exp_f32 v44, v44\n\t" \
  "v_mfma_f32_16x16x32_bf16 a[28:31], a[184:187], v[128:131], a[28:31]\n\t" \
  "v_exp_f32 v45, v45\n\t" \
  "v_mfma_f32_16x16x32_bf16 a[28:31], a[188:191], v[132:135], a[28:31]\n\t" \
  "v_exp_f32 v46, v46\n\t" \
  "v_mfma_f32_16x16x32_bf16 v[160:163], v[216:219], v[128:131], v[160:163]\n\t" \
  "s_add_u32 m0, s38, 49152\n\t" \
  "v_exp_f32 v47, v47\n\t" \
  "v_mfma_f32_16x16x32_bf16 v[160:163], v[216:219], v[132:135], v[160:163]\n\t" \
  "s_add_u32 s96, s38, 65536\n\t" \
  "ds_read_b128 a[196:199], v221 offset:16384\n\t" \
  "v_cvt_pk_bf16_f32 v128, v0, v1\n\t" \
  "v_mfma_f32_16x16x32_bf16 a[32:35], a[128:131], v[136:139], a[32:35]\n\t" \
  "v_exp_f32 v64, v64\n\t" \
  "v_mfma_f32_16x16x32_bf16 a[32:35], a[132:135], v[140:143], a[32:35]\n\t" \
  "v_exp_f32 v65, v65\n\t" \
  "v_mfma_f32_16x16x32_bf16 a[36:39], a[136:139], v[136:139], a[36:39]\n\t" \
  "buffer_load_dwordx4 v224, s[60:63], s96 offen offset:0 lds\n\t" \
  "v_mfma_f32_16x16x32_bf16 a[36:39], a[140:143], v[140:143], a[36:39]\n\t" \
  "ds_read_b128 a[200:203], v222 offset:16384\n\t" \
  "v_cvt_pk_bf16_f32 v129, v2, v3\n\t" \
  "v_mfma_f32_16x16x32_bf16 a[40:43], a[144:147], v[136:139], a[40:43]\n\t" \
  "v_exp_f32 v66, v66\n\t" \
  "v_mfma_f32_16x16x32_bf16 a[40:43], a[148:151], v[140:143], a[40:43]\n\t" \
  "ds_read_b128 a[204:207], v223 offset:16384\n\t" \
  "v_cvt_pk_bf16_f32 v130, v4, v5\n\t" \
  "v_mfma_f32_16x16x32_bf16 a[44:47], a[152:155], v[136:139], a[44:47]\n\t" \
  "buffer_load_dwordx4 v225, s[60:63], s96 offen offset:1024 lds\n\t" \
  "v_mfma_f32_16x16x32_bf16 a[44:47], a[156:159], v[140:143], a[44:47]\n\t" \
  "v_exp_f32 v67, v67\n\t" \
  "v_mfma_f32_16x16x32_bf16 a[48:51], a[160:163], v[136:139], a[48:51]\n\t" \
  "ds_read_b128 a[208:211], v220 offset:20480\n\t" \
  "v_cvt_pk_bf16_f32 v131, v6, v7\n\t" \
  "v_mfma_f32_16x16x32_bf16 a[48:51], a[164:167], v[140:143], a[48:51]\n\t" \
  "v_exp_f32 v68, v68\n\t" \
  "v_mfma_f32_16x16x32_bf16 a[52:55], a[168:171], v[136:139], a[52:55]\n\t" \
  "buffer_load_dwordx4 v226, s[60:63], s96 offen offset:2048 lds\n\t" \
  "v_mfma_f32_16x16x32_bf16 a[52:55], a[172:175], v[140:143], a[52:55]\n\t" \
  "ds_read_b128 a[212:215], v221 offset:20480\n\t" \
  "v_cvt_pk_bf16_f32 v132, v8, v9\n\t" \
  "v_mfma_f32_16x16x32_bf16 a[56:59], a[176:179], v[136:139], a[56:59]\n\t" \
  "v_exp_f32 v69, v69\n\t" \
  "v_mfma_f32_16x16x32_bf16 a[56:59], a[180:183], v[140:143], a[56:59]\n\t" \
  "v_cvt_pk_bf16_f32 v133, v10, v11\n\t" \
  "v_cvt_pk_bf16_f32 v134, v12, v13\n\t" \
  "v_mfma_f32_16x16x32_bf16 a[60:63], a[184:187], v[136:139], a[60:63]\n\t" \
  "buffer_load_dwordx4 v227, s[60:63], s96 offen offset:3072 lds\n\t" \
  "v_mfma_f32_16x16x32_bf16 a[60:63], a[188:191], v[140:143], a[60:63]\n\t" \
  "s_add_u32 m0, s38, 147456\n\t" \
  "ds_read_b128 a[216:219], v222 offset:20480\n\t" \
  "v_cvt_pk_bf16_f32 v135, v14, v15\n\t" \
  "v_mfma_f32_16x16x32_bf16 v[164:167], v[216:219], v[136:139], v[164:167]\n\t" \
  "s_add_u32 s96, s38, 49152\n\t" \
  "v_exp_f32 v70, v70\n\t" \
  "v_mfma_f32_16x16x32_bf16 v[164:167], v[216:219], v[140:143], v[164:167]\n\t" \
  "ds_read_b128 a[220:223], v223 offset:20480\n\t" \
  "v_cvt_pk_bf16_f32 v136, v32, v33\n\t" \
  "v_mfma_f32_16x16x32_bf16 a[64:67], a[128:131], v[144:147], a[64:67]\n\t" \
  "buffer_load_dwordx4 v230, s[64:67], s96 offen offset:0 lds\n\t" \
  "v_mfma_f32_16x16x32_bf16 a[64:67], a[132:135], v[148:151], a[64:67]\n\t" \
  "v_exp_f32 v71, v71\n\t" \
  "v_mfma_f32_16x16x32_bf16 a[68:71], a[136:139], v[144:147], a[68:71]\n\t" \
  "ds_read_b128 a[224:227], v220 offset:24576\n\t" \
  "v_cvt_pk_bf16_f32 v137, v34, v35\n\t" \
  "v_mfma_f32_16x16x32_bf16 a[68:71], a[140:143], v[148:151], a[68:71]\n\t" \
  "v_exp_f32 v72, v72\n\t" \
  "v_mfma_f32_16x16x32_bf16 a[72:75], a[144:147], v[144:147], a[72:75]\n\t" \
  "buffer_load_dwordx4 v231, s[64:67], s96 offen offset:1024 lds\n\t" \
  "v_mfma_f32_16x16x32_bf16 a[72:75], a[148:151], v[148:151], a[72:75]\n\t" \
  "ds_read_b128 a[228:231], v221 offset:24576\n\t" \
  "v_cvt_pk_bf16_f32 v138, v36, v37\n\t" \
  "v_mfma_f32_16x16x32_bf16 a[76:79], a[152:155], v[144:147], a[76:79]\n\t" \
  "v_exp_f32 v73, v73\n\t" \
  "v_mfma_f32_16x16x32_bf16 a[76:79], a[156:159], v[148:151], a[76:79]\n\t" \
  "v_exp_f32 v74, v74\n\t" \
  "v_mfma_f32_16x16x32_bf16 a[80:83], a[160:163], v[144:147], a[80:83]\n\t" \
  "buffer_load_dwordx4 v230, s[64:67], s96 offen offset:2048 lds\n\t" \
  "v_mfma_f32_16x16x32_bf16 a[80:83], a[164:167], v[148:151], a[80:83]\n\t" \
  "ds_read_b128 a[232:235], v222 offset:24576\n\t" \
  "v_cvt_pk_bf16_f32 v139, v38, v39\n\t" \
  "v_mfma_f32_16x16x32_bf16 a[84:87], a[168:171], v[144:147], a[84:87]\n\t" \
  "v_exp_f32 v75, v75\n\t" \
  "v_mfma_f32_16x16x32_bf16 a[84:87], a[172:175], v[148:151], a[84:87]\n\t" \
  "ds_read_b128 a[236:239], v223 offset:24576\n\t" \
  "v_cvt_pk_bf16_f32 v140, v40, v41\n\t" \
  "v_mfma_f32_16x16x32_bf16 a[88:91], a[176:179], v[144:147], a[88:91]\n\t" \
  "buffer_load_dwordx4 v231, s[64:67], s96 offen offset:3072 lds\n\t" \
  "v_mfma_f32_16x16x32_bf16 a[88:91], a[180:183], v[148:151], a[88:91]\n\t" \
  "s_add_u32 m0, s38, 32768\n\t" \
  "v_exp_f32 v76, v76\n\t" \
  "v_mfma_f32_16x16x32_bf16 a[92:95], a[184:187], v[144:147], a[92:95]\n\t" \
  "s_mov_b32 s96, 28672\n\t" \
  "ds_read_b128 a[240:243], v220 offset:28672\n\t" \
  "v_cvt_pk_bf16_f32 v141, v42, v43\n\t" \
  "v_mfma_f32_16x16x32_bf16 a[92:95], a[188:191], v[148:151], a[92:95]\n\t" \
  "v_exp_f32 v77, v77\n\t" \
  "v_mfma_f32_16x16x32_bf16 v[168:171], v[216:219], v[144:147], v[168:171]\n\t" \
  "buffer_load_dwordx4 v232, s[68:71], s96 offen offset:0 lds\n\t" \
  "v_mfma_f32_16x16x32_bf16 v[168:171], v[216:219], v[148:151], v[168:171]\n\t" \
  "s_mov_b32 s96, 34816\n\t" \
  "ds_read_b128 a[244:247], v221 offset:28672\n\t" \
  "v_cvt_pk_bf16_f32 v142, v44, v45\n\t" \
  "v_mfma_f32_16x16x32_bf16 a[96:99], a[128:131], v[152:155], a[96:99]\n\t" \
  "v_exp_f32 v78, v78\n\t" \
  "v_mfma_f32_16x16x32_bf16 a[96:99], a[132:135], v[156:159], a[96:99]\n\t" \
  "v_exp_f32 v79, v79\n\t" \
  "v_mfma_f32_16x16x32_bf16 a[100:103], a[136:139], v[152:155], a[100:103]\n\t" \
  "buffer_load_dwordx4 v232, s[68:71], s96 offen offset:1024 lds\n\t" \
  "v_mfma_f32_16x16x32_bf16 a[100:103], a[140:143], v[156:159], a[100:103]\n\t" \
  "s_mov_b32 s96, 40960\n\t" \
  "ds_read_b128 a[248:251], v222 offset:28672\n\t" \
  "v_cvt_pk_bf16_f32 v143, v46, v47\n\t" \
  "v_mfma_f32_16x16x32_bf16 a[104:107], a[144:147], v[152:155], a[104:107]\n\t" \
  "v_exp_f32 v96, v96\n\t" \
  "v_mfma_f32_16x16x32_bf16 a[104:107], a[148:151], v[156:159], a[104:107]\n\t" \
  "ds_read_b128 a[252:255], v223 offset:28672\n\t" \
  "v_cvt_pk_bf16_f32 v144, v64, v65\n\t" \
  "v_mfma_f32_16x16x32_bf16 a[108:111], a[152:155], v[152:155], a[108:111]\n\t" \
  "buffer_load_dwordx4 v232, s[68:71], s96 offen offset:2048 lds\n\t" \
  "v_mfma_f32_16x16x32_bf16 a[108:111], a[156:159], v[156:159], a[108:111]\n\t" \
  "s_mov_b32 s96, 47104\n\t" \
  "v_exp_f32 v97, v97\n\t" \
  "v_mfma_f32_16x16x32_bf16 a[112:115], a[160:163], v[152:155], a[112:115]\n\t" \
  "v_cvt_pk_bf16_f32 v145, v66, v67\n\t" \
  "v_cvt_pk_bf16_f32 v146, v68, v69\n\t" \
  "v_mfma_f32_16x16x32_bf16 a[112:115], a[164:167], v[156:159], a[112:115]\n\t" \
  "v_exp_f32 v98, v98\n\t" \
  "v_mfma_f32_16x16x32_bf16 a[116:119], a[168:171], v[152:155], a[116:119]\n\t" \
  "buffer_load_dwordx4 v232, s[68:71], s96 offen offset:3072 lds\n\t" \
  "v_mfma_f32_16x16x32_bf16 a[116:119], a[172:175], v[156:159], a[116:119]\n\t" \
  "v_exp_f32 v99, v99\n\t" \
  "v_mfma_f32_16x16x32_bf16 a[120:123], a[176:179], v[152:155], a[120:123]\n\t" \
  "v_cvt_pk_bf16_f32 v147, v70, v71\n\t" \
  "v_cvt_pk_bf16_f32 v148, v72, v73\n\t" \
  "v_mfma_f32_16x16x32_bf16 a[120:123], a[180:183], v[156:159], a[120:123]\n\t" \
  "v_exp_f32 v100, v100\n\t" \
  "v_mfma_f32_16x16x32_bf16 a[124:127], a[184:187], v[152:155], a[124:127]\n\t" \
  "v_exp_f32 v101, v101\n\t" \
  "v_mfma_f32_16x16x32_bf16 a[124:127], a[188:191], v[156:159], a[124:127]\n\t" \
  "v_cvt_pk_bf16_f32 v149, v74, v75\n\t" \
  "v_cvt_pk_bf16_f32 v150, v76, v77\n\t" \
  "v_mfma_f32_16x16x32_bf16 v[172:175], v[216:219], v[152:155], v[172:175]\n\t" \
  "v_exp_f32 v102, v102\n\t" \
  "v_mfma_f32_16x16x32_bf16 v[172:175], v[216:219], v[156:159], v[172:175]\n\t" \
  "s_waitcnt lgkmcnt(12)\n\t" \
  "v_exp_f32 v103, v103\n\t" \
  STA("s_memrealtime s[94:95]\n\t") \
  STA("s_memtime s[92:93]\n\t") \
  "v_mfma_f32_16x16x32_bf16 v[0:3], a[192:195], v[16:19], v[176:179]\n\t" \
  "ds_read_b64_tr_b16 a[128:129], v228 offset:0\n\t" \
  "v_cvt_pk_bf16_f32 v151, v78, v79\n\t" \
  "v_mfma_f32_16x16x32_bf16 v[0:3], a[196:199], v[20:23], v[0:3]\n\t" \
  "v_exp_f32 v104, v104\n\t" \
  "v_mfma_f32_16x16x32_bf16 v[0:3], a[200:203], v[24:27], v[0:3]\n\t" \
  "ds_read_b64_tr_b16 a[130:131], v228 offset:4096\n\t" \
  "v_cvt_pk_bf16_f32 v152, v96, v97\n\t" \
  "v_mfma_f32_16x16x32_bf16 v[0:3], a[204:207], v[28:31], v[0:3]\n\t" \
  "s_waitcnt lgkmcnt(10)\n\t" \
  "v_exp_f32 v105, v105\n\t" \
  "v_mfma_f32_16x16x32_bf16 v[4:7], a[220:223], v[28:31], v[176:179]\n\t" \
  "ds_read_b64_tr_b16 a[132:133], v228 offset:8192\n\t" \
  "v_cvt_pk_bf16_f32 v153, v98, v99\n\t" \
  "v_mfma_f32_16x16x32_bf16 v[4:7], a[216:219], v[24:27], v[4:7]\n\t" \
  "v_exp_f32 v106, v106\n\t" \
  "v_mfma_f32_16x16x32_bf16 v[4:7], a[212:215], v[20:23], v[4:7]\n\t" \
  "ds_read_b64_tr_b16 a[134:135], v228 offset:12288\n\t" \
  "v_cvt_pk_bf16_f32 v154, v100, v101\n\t" \
  "v_mfma_f32_16x16x32_bf16 v[4:7], a[208:211], v[16:19], v[4:7]\n\t" \
  "s_waitcnt lgkmcnt(8)\n\t" \
  "ds_read_b64_tr_b16 a[136:137], v229 offset:0\n\t" \
  "v_cvt_pk_bf16_f32 v155, v102, v103\n\t" \
  "v_mfma_f32_16x16x32_bf16 v[8:11], a[224:227], v[16:19], v[176:179]\n\t" \
  "v_exp_f32 v107, v107\n\t" \
  "v_mfma_f32_16x16x32_bf16 v[8:11], a[228:231], v[20:23], v[8:11]\n\t" \
  "ds_read_b64_tr_b16 a[138:139], v229 offset:4096\n\t" \
  "v_cvt_pk_bf16_f32 v156, v104, v105\n\t" \
  "v_mfma_f32_16x16x32_bf16 v[8:11], a[232:235], v[24:27], v[8:11]\n\t" \
  "v_exp_f32 v108, v108\n\t" \
  "v_mfma_f32_16x16x32_bf16 v[8:11], a[236:239], v[28:31], v[8:11]\n\t" \
  "s_waitcnt lgkmcnt(6)\n\t" \
  "ds_read_b64_tr_b16 a[140:141], v229 offset:8192\n\t" \
  "v_cvt_pk_bf16_f32 v157, v106, v107\n\t" \
  "v_mfma_f32_16x16x32_bf16 v[12:15], a[252:255], v[28:31], v[176:179]\n\t" \
  STA("v_writelane_b32 v255, s94, 34\n\t") \
  STA("v_writelane_b32 v255, s92, 35\n\t") \
  ".Lhs7:\n\ts_cbranch_execz .Lhs7n\n\t" \
  "v_exp_f32 v109, v109\n\t" \
  "v_mfma_f32_16x16x32_bf16 v[12:15], a[248:251], v[24:27], v[12:15]\n\t" \
  "ds_read_b64_tr_b16 a[142:143], v229 offset:12288\n\t" \
  "v_cvt_pk_bf16_f32 v158, v108, v109\n\t" \
  "v_mfma_f32_16x16x32_bf16 v[12:15], a[244:247], v[20:23], v[12:15]\n\t" \
  "v_exp_f32 v110, v110\n\t" \
  "v_mfma_f32_16x16x32_bf16 v[12:15], a[240:243], v[16:19], v[12:15]\n\t" \
  "v_exp_f32 v111, v111\n\t" \
  "v_mfma_f32_16x16x32_bf16 v[32:35], a[192:195], v[48:51], v[180:183]\n\t" \
  "s_add_u32 m0, s38, 114688\n\t" \
  "ds_read_b64_tr_b16 a[144:145], v228 offset:256\n\t" \
  "v_cvt_pk_bf16_f32 v159, v110, v111\n\t" \
  "v_mfma_f32_16x16x32_bf16 v[32:35], a[196:199], v[52:55], v[32:35]\n\t" \
  "s_mov_b32 s96, 0\n\t" \
  "v_exp_f32 v0, v0\n\t" \
  "v_mfma_f32_16x16x32_bf16 v[32:35], a[200:203], v[56:59], v[32:35]\n\t" \
  "v_exp_f32 v1, v1\n\t" \
  "v_mfma_f32_16x16x32_bf16 v[32:35], a[204:207], v[60:63], v[32:35]\n\t" \
  "ds_read_b64_tr_b16 a[146:147], v228 offset:4352\n\t" \
  "buffer_load_dword v246, v247, s[16:19], 0 offen sc1\n\t" \
  "v_mfma_f32_16x16x32_bf16 v[36:39], a[220:223], v[60:63], v[180:183]\n\t" \
  "buffer_load_dwordx4 v232, s[72:75], s96 offen offset:0 lds\n\t" \
  "v_mfma_f32_16x16x32_bf16 v[36:39], a[216:219], v[56:59], v[36:39]\n\t" \
  "ds_read_b64_tr_b16 a[148:149], v228 offset:8448\n\t" \
  "v_mfma_f32_16x16x32_bf16 v[36:39], a[212:215], v[52:55], v[36:39]\n\t" \
  "v_exp_f32 v2, v2\n\t" \
  "v_mfma_f32_16x16x32_bf16 v[36:39], a[208:211], v[48:51], v[36:39]\n\t" \
  "ds_read_b64_tr_b16 a[150:151], v228 offset:12544\n\t" \
  "v_mfma_f32_16x16x32_bf16 v[40:43], a[224:227], v[48:51], v[180:183]\n\t" \
  "ds_read_b64_tr_b16 a[152:153], v229 offset:256\n\t" \
  "v_mfma_f32_16x16x32_bf16 v[40:43], a[228:231], v[52:55], v[40:43]\n\t" \
  "v_exp_f32 v3, v3\n\t" \
  "v_mfma_f32_16x16x32_bf16 v[40:43], a[232:235], v[56:59], v[40:43]\n\t" \
  "ds_read_b64_tr_b16 a[154:155], v229 offset:4352\n\t" \
  "s_mov_b32 s96, 6144\n\t" \
  "v_mfma_f32_16x16x32_bf16 v[40:43], a[236:239], v[60:63], v[40:43]\n\t" \
  "v_exp_f32 v4, v4\n\t" \
  "v_mfma_f32_16x16x32_bf16 v[44:47], a[252:255], v[60:63], v[180:183]\n\t" \
  "ds_read_b64_tr_b16 a[156:157], v229 offset:8448\n\t" \
  "v_mfma_f32_16x16x32_bf16 v[44:47], a[248:251], v[56:59], v[44:47]\n\t" \
  "ds_read_b64_tr_b16 a[158:159], v229 offset:12544\n\t" \
  "v_mfma_f32_16x16x32_bf16 v[44:47], a[244:247], v[52:55], v[44:47]\n\t" \
  "buffer_load_dwordx4 v232, s[72:75], s96 offen offset:1024 lds\n\t" \
  "v_mfma_f32_16x16x32_bf16 v[44:47], a[240:243], v[48:51], v[44:47]\n\t" \
  "ds_read_b64_tr_b16 a[160:161], v228 offset:512\n\t" \
  "v_mfma_f32_16x16x32_bf16 v[64:67], a[192:195], v[80:83], v[184:187]\n\t" \
  "v_exp_f32 v5, v5\n\t" \
  "v_mfma_f32_16x16x32_bf16 v[64:67], a[196:199], v[84:87], v[64:67]\n\t" \
  "ds_read_b64_tr_b16 a[162:163], v228 offset:4608\n\t" \
  "v_mfma_f32_16x16x32_bf16 v[64:67], a[200:203], v[88:91], v[64:67]\n\t" \
  "ds_read_b64_tr_b16 a[164:165], v228 offset:8704\n\t" \
  "v_mfma_f32_16x16x32_bf16 v[64:67], a[204:207], v[92:95], v[64:67]\n\t" \
  "v_exp_f32 v6, v6\n\t" \
  "v_mfma_f32_16x16x32_bf16 v[68:71], a[220:223], v[92:95], v[184:187]\n\t" \
  "ds_read_b64_tr_b16 a[166:167], v228 offset:12800\n\t" \
  "s_mov_b32 s96, 12288\n\t" \
  "v_mfma_f32_16x16x32_bf16 v[68:71], a[216:219], v[88:91], v[68:71]\n\t" \
  "v_exp_f32 v7, v7\n\t" \
  "v_mfma_f32_16x16x32_bf16 v[68:71], a[212:215], v[84:87], v[68:71]\n\t" \
  "ds_read_b64_tr_b16 a[168:169], v229 offset:512\n\t" \
  "v_mfma_f32_16x16x32_bf16 v[68:71], a[208:211], v[80:83], v[68:71]\n\t" \
  "ds_read_b64_tr_b16 a[170:171], v229 offset:4608\n\t" \
  "v_mfma_f32_16x16x32_bf16 v[72:75], a[224:227], v[80:83], v[184:187]\n\t" \
  "buffer_load_dwordx4 v232, s[72:75], s96 offen offset:2048 lds\n\t" \
  "v_mfma_f32_16x16x32_bf16 v[72:75], a[228:231], v[84:87], v[72:75]\n\t" \
  "ds_read_b64_tr_b16 a[172:173], v229 offset:8704\n\t" \
  "v_mfma_f32_16x16x32_bf16 v[72:75], a[232:235], v[88:91], v[72:75]\n\t" \
  "v_exp_f32 v8, v8\n\t" \
  "v_mfma_f32_16x16x32_bf16 v[72:75], a[236:239], v[92:95], v[72:75]\n\t" \
  "ds_read_b64_tr_b16 a[174:175], v229 offset:12800\n\t" \
  "v_mfma_f32_16x16x32_bf16 v[76:79], a[252:255], v[92:95], v[184:187]\n\t" \
  "ds_read_b64_tr_b16 a[176:177], v228 offset:768\n\t" \
  "v_mfma_f32_16x16x32_bf16 v[76:79], a[248:251], v[88:91], v[76:79]\n\t" \
  "v_exp_f32 v9, v9\n\t" \
  "v_mfma_f32_16x16x32_bf16 v[76:79], a[244:247], v[84:87], v[76:79]\n\t" \
  "ds_read_b64_tr_b16 a[178:179], v228 offset:4864\n\t" \
  "s_mov_b32 s96, 18432\n\t" \
  "v_mfma_f32_16x16x32_bf16 v[76:79], a[240:243], v[80:83], v[76:79]\n\t" \
  "v_exp_f32 v10, v10\n\t" \
  "v_mfma_f32_16x16x32_bf16 v[96:99], a[192:195], v[112:115], v[188:191]\n\t" \
  "ds_read_b64_tr_b16 a[180:181], v228 offset:8960\n\t" \
  "v_mfma_f32_16x16x32_bf16 v[96:99], a[196:199], v[116:119], v[96:99]\n\t" \
  "ds_read_b64_tr_b16 a[182:183], v228 offset:13056\n\t" \
  "v_mfma_f32_16x16x32_bf16 v[96:99], a[200:203], v[120:123], v[96:99]\n\t" \
  "buffer_load_dwordx4 v232, s[72:75], s96 offen offset:3072 lds\n\t" \
  "v_mfma_f32_16x16x32_bf16 v[96:99], a[204:207], v[124:127], v[96:99]\n\t" \
  "ds_read_b64_tr_b16 a[184:185], v229 offset:768\n\t" \
  "v_mfma_f32_16x16x32_bf16 v[100:103], a[220:223], v[124:127], v[188:191]\n\t" \
  "ds_read_b64_tr_b16 a[186:187], v229 offset:4864\n\t" \
  "v_mfma_f32_16x16x32_bf16 v[100:103], a[216:219], v[120:123], v[100:103]\n\t" \
  "v_exp_f32 v11, v11\n\t" \
  "v_mfma_f32_16x16x32_bf16 v[100:103], a[212:215], v[116:119], v[100:103]\n\t" \
  "ds_read_b64_tr_b16 a[188:189], v229 offset:8960\n\t" \
  "v_mfma_f32_16x16x32_bf16 v[100:103], a[208:211], v[112:115], v[100:103]\n\t" \
  "ds_read_b64_tr_b16 a[190:191], v229 offset:13056\n\t" \
  "v_mfma_f32_16x16x32_bf16 v[104:107], a[224:227], v[112:115], v[188:191]\n\t" \
  "v_exp_f32 v12, v12\n\t" \
  "v_mfma_f32_16x16x32_bf16 v[104:107], a[228:231], v[116:119], v[104:107]\n\t" \
  "s_getreg_b32 s98, hwreg(HW_REG_IB_STS, 0, 4)\n\t" \
  "s_cmp_lg_u32 s98, 0\n\t" \
  "s_addc_u32 s54, s54, 0\n\t" \
  "v_mfma_f32_16x16x32_bf16 v[104:107], a[232:235], v[120:123], v[104:107]\n\t" \
  "v_exp_f32 v13, v13\n\t" \
  "v_mfma_f32_16x16x32_bf16 v[104:107], a[236:239], v[124:127], v[104:107]\n\t" \
  "s_getreg_b32 s98, hwreg(HW_REG_IB_STS, 0, 4)\n\t" \
  "s_cmp_lg_u32 s98, 0\n\t" \
  "s_addc_u32 s54, s54, 0\n\t" \
  "v_mfma_f32_16x16x32_bf16 v[108:111], a[252:255], v[124:127], v[188:191]\n\t" \
  "v_exp_f32 v14, v14\n\t" \
  "v_mfma_f32_16x16x32_bf16 v[108:111], a[248:251], v[120:123], v[108:111]\n\t" \
  "s_getreg_b32 s98, hwreg(HW_REG_IB_STS, 0, 4)\n\t" \
  "s_cmp_lg_u32 s98, 0\n\t" \
  "s_addc_u32 s54, s54, 0\n\t" \
  "v_mfma_f32_16x16x32_bf16 v[108:111], a[244:247], v[116:119], v[108:111]\n\t" \
  "s_waitcnt vmcnt(9) lgkmcnt(0)\n\t" \
  "s_getreg_b32 s98, hwreg(HW_REG_IB_STS, 0, 4)\n\t" \
  "s_cmp_lg_u32 s98, 0\n\t" \
  "s_addc_u32 s54, s54, 0\n\t" \
  "v_mfma_f32_16x16x32_bf16 v[108:111], a[240:243], v[112:115], v[108:111]\n\t" \
  "s_barrier\n\t" \
  STA("s_memrealtime s[94:95]\n\t") \
  STA("s_memtime s[92:93]\n\t") \
  "v_mfma_f32_16x16x32_bf16 a[0:3], a[128:131], v[128:131], a[0:3]\n\t" \
  "v_exp_f32 v15, v15\n\t" \
  "v_mfma_f32_16x16x32_bf16 a[0:3], a[132:135], v[132:135], a[0:3]\n\t" \
  "v_exp_f32 v32, v32\n\t" \
  "v_mfma_f32_16x16x32_bf16 a[4:7], a[136:139], v[128:131], a[4:7]\n\t" \
  "v_exp_f32 v33, v33\n\t" \
  "v_mfma_f32_16x16x32_bf16 a[4:7], a[140:143], v[132:135], a[4:7]\n\t" \
  "v_exp_f32 v34, v34\n\t" \
  "v_mfma_f32_16x16x32_bf16 a[8:11], a[144:147], v[128:131], a[8:11]\n\t" \
  STS("v_writelane_b32 v255, s54, 58\n\t") \
  "s_cmp_ge_u32 s54, 2\n\t" \
  "s_cselect_b32 s98, 0x4000, 0\n\t" \
  "s_or_b32 s98, s98, s36\n\t" \
  "v_mfma_f32_16x16x32_bf16 a[8:11], a[148:151], v[132:135], a[8:11]\n\t" \
  "s_cmp_ge_u32 s54, 3\n\t" \
  "s_cselect_b32 s54, 2.0, 0\n\t" \
  "s_or_b32 s98, s98, s54\n\t" \
  "v_mfma_f32_16x16x32_bf16 a[12:15], a[152:155], v[128:131], a[12:15]\n\t" \
  "ds_read_b128 a[192:195], v220 offset:0\n\t" \
  "v_mov_b32 v209, s98\n\t" \
  "v_mfma_f32_16x16x32_bf16 a[12:15], a[156:159], v[132:135], a[12:15]\n\t" \
  "ds_read_b128 a[196:199], v221 offset:0\n\t" \
  "v_bfrev_b32 v250, 1\n\t" \
  "v_mfma_f32_16x16x32_bf16 a[16:19], a[160:163], v[128:131], a[16:19]\n\t" \
  "v_writelane_b32 v250, s44, 0\n\t" \
  "v_mfma_f32_16x16x32_bf16 a[16:19], a[164:167], v[132:135], a[16:19]\n\t" \
  "v_exp_f32 v35, v35\n\t" \
  "v_mfma_f32_16x16x32_bf16 a[20:23], a[168:171], v[128:131], a[20:23]\n\t" \
  "buffer_store_dword v209, v250, s[16:19], 0 offen sc1\n\t" \
  "ds_read_b128 a[200:203], v222 offset:0\n\t" \
  "v_mfma_f32_16x16x32_bf16 a[20:23], a[172:175], v[132:135], a[20:23]\n\t" \
  "v_exp_f32 v36, v36\n\t" \
  "v_mfma_f32_16x16x32_bf16 a[24:27], a[176:179], v[128:131], a[24:27]\n\t" \
  STA("v_writelane_b32 v255, s94, 36\n\t") \
  STA("v_writelane_b32 v255, s92, 37\n\t") \
  "v_exp_f32 v37, v37\n\t" \
  "v_mfma_f32_16x16x32_bf16 a[24:27], a[180:183], v[132:135], a[24:27]\n\t" \
  "v_exp_f32 v38, v38\n\t" \
  "v_mfma_f32_16x16x32_bf16 a[28:31], a[184:187], v[128:131], a[28:31]\n\t" \
  "v_exp_f32 v39, v39\n\t" \
  "v_mfma_f32_16x16x32_bf16 a[28:31], a[188:191], v[132:135], a[28:31]\n\t" \
  "v_exp_f32 v40, v40\n\t" \
  "v_mfma_f32_16x16x32_bf16 v[160:163], v[216:219], v[128:131], v[160:163]\n\t" \
  "s_add_u32 m0, s38, 65536\n\t" \
  "v_exp_f32 v41, v41\n\t" \
  "v_mfma_f32_16x16x32_bf16 v[160:163], v[216:219], v[132:135], v[160:163]\n\t" \
  "s_add_u32 s96, s38, 81920\n\t" \
  "v_exp_f32 v42, v42\n\t" \
  "v_mfma_f32_16x16x32_bf16 a[32:35], a[128:131], v[136:139], a[32:35]\n\t" \
  "v_exp_f32 v43, v43\n\t" \
  "v_mfma_f32_16x16x32_bf16 a[32:35], a[132:135], v[140:143], a[32:35]\n\t" \
  "v_exp_f32 v44, v44\n\t" \
  "v_mfma_f32_16x16x32_bf16 a[36:39], a[136:139], v[136:139], a[36:39]\n\t" \
  "buffer_load_dwordx4 v224, s[60:63], s96 offen offset:0 lds\n\t" \
  "v_mfma_f32_16x16x32_bf16 a[36:39], a[140:143], v[140:143], a[36:39]\n\t" \
  "v_exp_f32 v45, v45\n\t" \
  "v_mfma_f32_16x16x32_bf16 a[40:43], a[144:147], v[136:139], a[40:43]\n\t" \
  "v_exp_f32 v46, v46\n\t" \
  "v_mfma_f32_16x16x32_bf16 a[40:43], a[148:151], v[140:143], a[40:43]\n\t" \
  "ds_read_b128 a[204:207], v223 offset:0\n\t" \
  "v_cvt_pk_bf16_f32 v128, v0, v1\n\t" \
  "v_mfma_f32_16x16x32_bf16 a[44:47], a[152:155], v[136:139], a[44:47]\n\t" \
  "buffer_load_dwordx4 v225, s[60:63], s96 offen offset:1024 lds\n\t" \
  "v_mfma_f32_16x16x32_bf16 a[44:47], a[156:159], v[140:143], a[44:47]\n\t" \
  "v_exp_f32 v47, v47\n\t" \
  "v_mfma_f32_16x16x32_bf16 a[48:51], a[160:163], v[136:139], a[48:51]\n\t" \
  "ds_read_b128 a[208:211], v220 offset:4096\n\t" \
  "v_cvt_pk_bf16_f32 v129, v2, v3\n\t" \
  "v_mfma_f32_16x16x32_bf16 a[48:51], a[164:167], v[140:143], a[48:51]\n\t" \
  "v_exp_f32 v64, v64\n\t" \
  "v_mfma_f32_16x16x32_bf16 a[52:55], a[168:171], v[136:139], a[52:55]\n\t" \
  "buffer_load_dwordx4 v226, s[60:63], s96 offen offset:2048 lds\n\t" \
  "v_mfma_f32_16x16x32_bf16 a[52:55], a[172:175], v[140:143], a[52:55]\n\t" \
  "ds_read_b128 a[212:215], v221 offset:4096\n\t" \
  "v_cvt_pk_bf16_f32 v130, v4, v5\n\t" \
  "v_mfma_f32_16x16x32_bf16 a[56:59], a[176:179], v[136:139], a[56:59]\n\t" \
  "v_exp_f32 v65, v65\n\t" \
  "v_mfma_f32_16x16x32_bf16 a[56:59], a[180:183], v[140:143], a[56:59]\n\t" \
  "v_exp_f32 v66, v66\n\t" \
  "v_mfma_f32_16x16x32_bf16 a[60:63], a[184:187], v[136:139], a[60:63]\n\t" \
  "buffer_load_dwordx4 v227, s[60:63], s96 offen offset:3072 lds\n\t" \
  "v_mfma_f32_16x16x32_bf16 a[60:63], a[188:191], v[140:143], a[60:63]\n\t" \
  "s_add_u32 m0, s38, 81920\n\t" \
  "ds_read_b128 a[216:219], v222 offset:4096\n\t" \
  "v_cvt_pk_bf16_f32 v131, v6, v7\n\t" \
  "v_mfma_f32_16x16x32_bf16 v[164:167], v[216:219], v[136:139], v[164:167]\n\t" \
  "s_add_u32 s96, s38, 65536\n\t" \
  "v_exp_f32 v67, v67\n\t" \
  "v_mfma_f32_16x16x32_bf16 v[164:167], v[216:219], v[140:143], v[164:167]\n\t" \
  "ds_read_b128 a[220:223], v223 offset:4096\n\t" \
  "v_cvt_pk_bf16_f32 v132, v8, v9\n\t" \
  "v_mfma_f32_16x16x32_bf16 a[64:67], a[128:131], v[144:147], a[64:67]\n\t" \
  "buffer_load_dwordx4 v230, s[64:67], s96 offen offset:0 lds\n\t" \
  "v_mfma_f32_16x16x32_bf16 a[64:67], a[132:135], v[148:151], a[64:67]\n\t" \
  "v_exp_f32 v68, v68\n\t" \
  "v_mfma_f32_16x16x32_bf16 a[68:71], a[136:139], v[144:147], a[68:71]\n\t" \
  "ds_read_b128 a[224:227], v220 offset:8192\n\t" \
  "v_cvt_pk_bf16_f32 v133, v10, v11\n\t" \
  "v_mfma_f32_16x16x32_bf16 a[68:71], a[140:143], v[148:151], a[68:71]\n\t" \
  "v_exp_f32 v69, v69\n\t" \
  "v_mfma_f32_16x16x32_bf16 a[72:75], a[144:147], v[144:147], a[72:75]\n\t" \
  "buffer_load_dwordx4 v231, s[64:67], s96 offen offset:1024 lds\n\t" \
  "v_mfma_f32_16x16x32_bf16 a[72:75], a[148:151], v[148:151], a[72:75]\n\t" \
  "ds_read_b128 a[228:231], v221 offset:8192\n\t" \
  "v_cvt_pk_bf16_f32 v134, v12, v13\n\t" \
  "v_mfma_f32_16x16x32_bf16 a[76:79], a[152:155], v[144:147], a[76:79]\n\t" \
  "v_exp_f32 v70, v70\n\t" \
  "v_mfma_f32_16x16x32_bf16 a[76:79], a[156:159], v[148:151], a[76:79]\n\t" \
  "v_exp_f32 v71, v71\n\t" \
  "v_mfma_f32_16x16x32_bf16 a[80:83], a[160:163], v[144:147], a[80:83]\n\t" \
  "buffer_load_dwordx4 v230, s[64:67], s96 offen offset:2048 lds\n\t" \
  "v_mfma_f32_16x16x32_bf16 a[80:83], a[164:167], v[148:151], a[80:83]\n\t" \
  "ds_read_b128 a[232:235], v222 offset:8192\n\t" \
  "v_cvt_pk_bf16_f32 v135, v14, v15\n\t" \
  "v_mfma_f32_16x16x32_bf16 a[84:87], a[168:171], v[144:147], a[84:87]\n\t" \
  "v_exp_f32 v72, v72\n\t" \
  "v_mfma_f32_16x16x32_bf16 a[84:87], a[172:175], v[148:151], a[84:87]\n\t" \
  "ds_read_b128 a[236:239], v223 offset:8192\n\t" \
  "v_cvt_pk_bf16_f32 v136, v32, v33\n\t" \
  "v_mfma_f32_16x16x32_bf16 a[88:91], a[176:179], v[144:147], a[88:91]\n\t" \
  "buffer_load_dwordx4 v231, s[64:67], s96 offen offset:3072 lds\n\t" \
  "v_mfma_f32_16x16x32_bf16 a[88:91], a[180:183], v[148:151], a[88:91]\n\t" \
  "s_add_u32 m0, s38, 131072\n\t" \
  "v_exp_f32 v73, v73\n\t" \
  "v_mfma_f32_16x16x32_bf16 a[92:95], a[184:187], v[144:147], a[92:95]\n\t" \
  "s_mov_b32 s96, 28672\n\t" \
  "ds_read_b128 a[240:243], v220 offset:12288\n\t" \
  "v_cvt_pk_bf16_f32 v137, v34, v35\n\t" \
  "v_mfma_f32_16x16x32_bf16 a[92:95], a[188:191], v[148:151], a[92:95]\n\t" \
  "v_exp_f32 v74, v74\n\t" \
  "v_mfma_f32_16x16x32_bf16 v[168:171], v[216:219], v[144:147], v[168:171]\n\t" \
  "buffer_load_dwordx4 v232, s[72:75], s96 offen offset:0 lds\n\t" \
  "v_mfma_f32_16x16x32_bf16 v[168:171], v[216:219], v[148:151], v[168:171]\n\t" \
  "s_mov_b32 s96, 34816\n\t" \
  "ds_read_b128 a[244:247], v221 offset:12288\n\t" \
  "v_cvt_pk_bf16_f32 v138, v36, v37\n\t" \
  "v_mfma_f32_16x16x32_bf16 a[96:99], a[128:131], v[152:155], a[96:99]\n\t" \
  "v_exp_f32 v75, v75\n\t" \
  "v_mfma_f32_16x16x32_bf16 a[96:99], a[132:135], v[156:159], a[96:99]\n\t" \
  "v_cvt_pk_bf16_f32 v139, v38, v39\n\t" \
  "v_cvt_pk_bf16_f32 v140, v40, v41\n\t" \
  "v_mfma_f32_16x16x32_bf16 a[100:103], a[136:139], v[152:155], a[100:103]\n\t" \
  "buffer_load_dwordx4 v232, s[72:75], s96 offen offset:1024 lds\n\t" \
  "v_mfma_f32_16x16x32_bf16 a[100:103], a[140:143], v[156:159], a[100:103]\n\t" \
  "s_mov_b32 s96, 40960\n\t" \
  "ds_read_b128 a[248:251], v222 offset:12288\n\t" \
  "v_cvt_pk_bf16_f32 v141, v42, v43\n\t" \
  "v_mfma_f32_16x16x32_bf16 a[104:107], a[144:147], v[152:155], a[104:107]\n\t" \
  "v_exp_f32 v76, v76\n\t" \
  "v_mfma_f32_16x16x32_bf16 a[104:107], a[148:151], v[156:159], a[104:107]\n\t" \
  "ds_read_b128 a[252:255], v223 offset:12288\n\t" \
  "v_cvt_pk_bf16_f32 v142, v44, v45\n\t" \
  "v_mfma_f32_16x16x32_bf16 a[108:111], a[152:155], v[152:155], a[108:111]\n\t" \
  "buffer_load_dwordx4 v232, s[72:75], s96 offen offset:2048 lds\n\t" \
  "v_mfma_f32_16x16x32_bf16 a[108:111], a[156:159], v[156:159], a[108:111]\n\t" \
  "s_mov_b32 s96, 47104\n\t" \
  "v_exp_f32 v77, v77\n\t" \
  "v_mfma_f32_16x16x32_bf16 a[112:115], a[160:163], v[152:155], a[112:115]\n\t" \
  "v_exp_f32 v78, v78\n\t" \
  "v_mfma_f32_16x16x32_bf16 a[112:115], a[164:167], v[156:159], a[112:115]\n\t" \
  "v_exp_f32 v79, v79\n\t" \
  "v_mfma_f32_16x16x32_bf16 a[116:119], a[168:171], v[152:155], a[116:119]\n\t" \
  "buffer_load_dwordx4 v232, s[72:75], s96 offen offset:3072 lds\n\t" \
  "v_mfma_f32_16x16x32_bf16 a[116:119], a[172:175], v[156:159], a[116:119]\n\t" \
  "v_cvt_pk_bf16_f32 v143, v46, v47\n\t" \
  "v_cvt_pk_bf16_f32 v144, v64, v65\n\t" \
  "v_mfma_f32_16x16x32_bf16 a[120:123], a[176:179], v[152:155], a[120:123]\n\t" \
  "v_exp_f32 v96, v96\n\t" \
  "v_mfma_f32_16x16x32_bf16 a[120:123], a[180:183], v[156:159], a[120:123]\n\t" \
  "s_add_u32 s99, s52, 0\n\t" \
  "v_exp_f32 v97, v97\n\t" \
  "v_mfma_f32_16x16x32_bf16 a[124:127], a[184:187], v[152:155], a[124:127]\n\t" \
  "v_cvt_pk_bf16_f32 v145, v66, v67\n\t" \
  "v_cvt_pk_bf16_f32 v146, v68, v69\n\t" \
  "v_mfma_f32_16x16x32_bf16 a[124:127], a[188:191], v[156:159], a[124:127]\n\t" \
  "v_exp_f32 v98, v98\n\t" \
  "v_mfma_f32_16x16x32_bf16 v[172:175], v[216:219], v[152:155], v[172:175]\n\t" \
  "buffer_load_dword v246, v212, s[24:27], s99 offen\n\t" \
  "v_mfma_f32_16x16x32_bf16 v[172:175], v[216:219], v[156:159], v[172:175]\n\t" \
  "s_waitcnt lgkmcnt(12)\n\t" \
  "v_exp_f32 v99, v99\n\t" \
  STA("s_memrealtime s[94:95]\n\t") \
  STA("s_memtime s[92:93]\n\t") \
  "v_mfma_f32_16x16x32_bf16 v[0:3], a[192:195], v[16:19], v[176:179]\n\t" \
  "s_lshr_b32 s78, s38, 6\n\t" \
  "ds_read_b64_tr_b16 a[128:129], v210 offset:0\n\t" \
  "v_cvt_pk_bf16_f32 v147, v70, v71\n\t" \
  "v_mfma_f32_16x16x32_bf16 v[0:3], a[196:199], v[20:23], v[0:3]\n\t" \
  "v_exp_f32 v100, v100\n\t" \
  "v_mfma_f32_16x16x32_bf16 v[0:3], a[200:203], v[24:27], v[0:3]\n\t" \
  "ds_read_b64_tr_b16 a[130:131], v210 offset:4096\n\t" \
  "v_cvt_pk_bf16_f32 v148, v72, v73\n\t" \
  "v_mfma_f32_16x16x32_bf16 v[0:3], a[204:207], v[28:31], v[0:3]\n\t" \
  "s_waitcnt lgkmcnt(10)\n\t" \
  "v_exp_f32 v101, v101\n\t" \
  "v_mfma_f32_16x16x32_bf16 v[4:7], a[220:223], v[28:31], v[176:179]\n\t" \
  "ds_read_b64_tr_b16 a[132:133], v210 offset:8192\n\t" \
  "v_cvt_pk_bf16_f32 v149, v74, v75\n\t" \
  "v_mfma_f32_16x16x32_bf16 v[4:7], a[216:219], v[24:27], v[4:7]\n\t" \
  "v_exp_f32 v102, v102\n\t" \
  "v_mfma_f32_16x16x32_bf16 v[4:7], a[212:215], v[20:23], v[4:7]\n\t" \
  "ds_read_b64_tr_b16 a[134:135], v210 offset:12288\n\t" \
  "v_cvt_pk_bf16_f32 v150, v76, v77\n\t" \
  "v_mfma_f32_16x16x32_bf16 v[4:7], a[208:211], v[16:19], v[4:7]\n\t" \
  "s_waitcnt lgkmcnt(8)\n\t" \
  "ds_read_b64_tr_b16 a[136:137], v211 offset:0\n\t" \
  "v_cvt_pk_bf16_f32 v151, v78, v79\n\t" \
  "v_mfma_f32_16x16x32_bf16 v[8:11], a[224:227], v[16:19], v[176:179]\n\t" \
  "v_exp_f32 v103, v103\n\t" \
  "v_mfma_f32_16x16x32_bf16 v[8:11], a[228:231], v[20:23], v[8:11]\n\t" \
  "s_add_u32 s99, s52, 2048\n\t" \
  "ds_read_b64_tr_b16 a[138:139], v211 offset:4096\n\t" \
  "v_cvt_pk_bf16_f32 v152, v96, v97\n\t" \
  "v_mfma_f32_16x16x32_bf16 v[8:11], a[232:235], v[24:27], v[8:11]\n\t" \
  "v_exp_f32 v104, v104\n\t" \
  "v_mfma_f32_16x16x32_bf16 v[8:11], a[236:239], v[28:31], v[8:11]\n\t" \
  "s_waitcnt lgkmcnt(6)\n\t" \
  "ds_read_b64_tr_b16 a[140:141], v211 offset:8192\n\t" \
  "v_cvt_pk_bf16_f32 v153, v98, v99\n\t" \
  "v_mfma_f32_16x16x32_bf16 v[12:15], a[252:255], v[28:31], v[176:179]\n\t" \
  STA("v_writelane_b32 v255, s94, 38\n\t") \
  STA("v_writelane_b32 v255, s92, 39\n\t") \
  "buffer_load_dword v246, v212, s[24:27], s99 offen\n\t" \
  "v_mfma_f32_16x16x32_bf16 v[12:15], a[248:251], v[24:27], v[12:15]\n\t" \
  "ds_read_b64_tr_b16 a[142:143], v211 offset:12288\n\t" \
  "v_cvt_pk_bf16_f32 v154, v100, v101\n\t" \
  "v_mfma_f32_16x16x32_bf16 v[12:15], a[244:247], v[20:23], v[12:15]\n\t" \
  "ds_read_b64_tr_b16 a[144:145], v210 offset:256\n\t" \
  "v_cvt_pk_bf16_f32 v155, v102, v103\n\t" \
  "v_mfma_f32_16x16x32_bf16 v[12:15], a[240:243], v[16:19], v[12:15]\n\t" \
  "v_exp_f32 v105, v105\n\t" \
  "v_mfma_f32_16x16x32_bf16 v[32:35], a[192:195], v[48:51], v[180:183]\n\t" \
  "ds_read_b64_tr_b16 a[146:147], v210 offset:4352\n\t" \
  "v_cvt_pk_bf16_f32 v156, v104, v105\n\t" \
  "v_mfma_f32_16x16x32_bf16 v[32:35], a[196:199], v[52:55], v[32:35]\n\t" \
  "v_exp_f32 v106, v106\n\t" \
  "v_mfma_f32_16x16x32_bf16 v[32:35], a[200:203], v[56:59], v[32:35]\n\t" \
  "ds_read_b64_tr_b16 a[148:149], v210 offset:8448\n\t" \
  "v_xor_b32 v234, s78, v244\n\t" \
  "v_mfma_f32_16x16x32_bf16 v[32:35], a[204:207], v[60:63], v[32:35]\n\t" \
  "v_exp_f32 v107, v107\n\t" \
  "v_mfma_f32_16x16x32_bf16 v[36:39], a[220:223], v[60:63], v[180:183]\n\t" \
  "ds_read_b64_tr_b16 a[150:151], v210 offset:12544\n\t" \
  "v_cvt_pk_bf16_f32 v157, v106, v107\n\t" \
  "v_mfma_f32_16x16x32_bf16 v[36:39], a[216:219], v[56:59], v[36:39]\n\t" \
  "ds_read_b64_tr_b16 a[152:153], v211 offset:256\n\t" \
  "v_xor_b32 v235, 32, v234\n\t" \
  "v_mfma_f32_16x16x32_bf16 v[36:39], a[212:215], v[52:55], v[36:39]\n\t" \
  "v_exp_f32 v108, v108\n\t" \
  "v_mfma_f32_16x16x32_bf16 v[36:39], a[208:211], v[48:51], v[36:39]\n\t" \
  "ds_read_b64_tr_b16 a[154:155], v211 offset:4352\n\t" \
  "v_add_u32 v236, 65536, v234\n\t" \
  "v_mfma_f32_16x16x32_bf16 v[40:43], a[224:227], v[48:51], v[180:183]\n\t" \
  "v_exp_f32 v109, v109\n\t" \
  "v_mfma_f32_16x16x32_bf16 v[40:43], a[228:231], v[52:55], v[40:43]\n\t" \
  "ds_read_b64_tr_b16 a[156:157], v211 offset:8448\n\t" \
  "v_cvt_pk_bf16_f32 v158, v108, v109\n\t" \
  "v_mfma_f32_16x16x32_bf16 v[40:43], a[232:235], v[56:59], v[40:43]\n\t" \
  "v_exp_f32 v110, v110\n\t" \
  "v_mfma_f32_16x16x32_bf16 v[40:43], a[236:239], v[60:63], v[40:43]\n\t" \
  "ds_read_b64_tr_b16 a[158:159], v211 offset:12544\n\t" \
  "v_add_u32 v237, 65536, v235\n\t" \
  "v_mfma_f32_16x16x32_bf16 v[44:47], a[252:255], v[60:63], v[180:183]\n\t" \
  "v_exp_f32 v111, v111\n\t" \
  "v_mfma_f32_16x16x32_bf16 v[44:47], a[248:251], v[56:59], v[44:47]\n\t" \
  "ds_read_b64_tr_b16 a[160:161], v210 offset:512\n\t" \
  "v_cvt_pk_bf16_f32 v159, v110, v111\n\t" \
  "v_mfma_f32_16x16x32_bf16 v[44:47], a[244:247], v[52:55], v[44:47]\n\t" \
  "ds_read_b64_tr_b16 a[162:163], v210 offset:4608\n\t" \
  "v_add_u32 v238, 131072, v234\n\t" \
  "v_mfma_f32_16x16x32_bf16 v[44:47], a[240:243], v[48:51], v[44:47]\n\t" \
  "v_exp_f32 v0, v0\n\t" \
  "v_mfma_f32_16x16x32_bf16 v[64:67], a[192:195], v[80:83], v[184:187]\n\t" \
  "ds_read_b64_tr_b16 a[164:165], v210 offset:8704\n\t" \
  "v_add_u32 v239, 131072, v235\n\t" \
  "v_mfma_f32_16x16x32_bf16 v[64:67], a[196:199], v[84:87], v[64:67]\n\t" \
  "v_exp_f32 v1, v1\n\t" \
  "v_mfma_f32_16x16x32_bf16 v[64:67], a[200:203], v[88:91], v[64:67]\n\t" \
  "ds_read_b64_tr_b16 a[166:167], v210 offset:12800\n\t" \
  "v_lshlrev_b32 v242, 4, v233\n\t" \
  "v_mfma_f32_16x16x32_bf16 v[64:67], a[204:207], v[92:95], v[64:67]\n\t" \
  "ds_read_b64_tr_b16 a[168:169], v211 offset:512\n\t" \
  "v_add_u32 v213, s38, v242\n\t" \
  "v_mfma_f32_16x16x32_bf16 v[68:71], a[220:223], v[92:95], v[184:187]\n\t" \
  "v_exp_f32 v2, v2\n\t" \
  "v_mfma_f32_16x16x32_bf16 v[68:71], a[216:219], v[88:91], v[68:71]\n\t" \
  "ds_read_b64_tr_b16 a[170:171], v211 offset:4608\n\t" \
  "v_add_u32 v215, 98304, v213\n\t" \
  "v_mfma_f32_16x16x32_bf16 v[68:71], a[212:215], v[84:87], v[68:71]\n\t" \
  "v_exp_f32 v3, v3\n\t" \
  "v_mfma_f32_16x16x32_bf16 v[68:71], a[208:211], v[80:83], v[68:71]\n\t" \
  "ds_read_b64_tr_b16 a[172:173], v211 offset:8704\n\t" \
  "v_mov_b32 v243, s36\n\t" \
  "v_mfma_f32_16x16x32_bf16 v[72:75], a[224:227], v[80:83], v[184:187]\n\t" \
  "v_exp_f32 v4, v4\n\t" \
  "v_mfma_f32_16x16x32_bf16 v[72:75], a[228:231], v[84:87], v[72:75]\n\t" \
  "ds_read_b64_tr_b16 a[174:175], v211 offset:12800\n\t" \
  "v_lshlrev_b32 v254, 9, v233\n\t" \
  "v_mfma_f32_16x16x32_bf16 v[72:75], a[232:235], v[88:91], v[72:75]\n\t" \
  "ds_read_b64_tr_b16 a[176:177], v210 offset:768\n\t" \
  "v_and_b32 v244, 0x38, v233\n\t" \
  "v_mfma_f32_16x16x32_bf16 v[72:75], a[236:239], v[92:95], v[72:75]\n\t" \
  "v_exp_f32 v5, v5\n\t" \
  "v_mfma_f32_16x16x32_bf16 v[76:79], a[252:255], v[92:95], v[184:187]\n\t" \
  "ds_read_b64_tr_b16 a[178:179], v210 offset:4864\n\t" \
  "v_lshl_or_b32 v254, v244, 26, v254\n\t" \
  "v_mfma_f32_16x16x32_bf16 v[76:79], a[248:251], v[88:91], v[76:79]\n\t" \
  "s_brev_b32 s78, 1\n\t" \
  "v_exp_f32 v6, v6\n\t" \
  "v_mfma_f32_16x16x32_bf16 v[76:79], a[244:247], v[84:87], v[76:79]\n\t" \
  "ds_read_b64_tr_b16 a[180:181], v210 offset:8960\n\t" \
  "v_mov_b32 v244, s78\n\t" \
  "v_mfma_f32_16x16x32_bf16 v[76:79], a[240:243], v[80:83], v[76:79]\n\t" \
  "v_exp_f32 v7, v7\n\t" \
  "v_mfma_f32_16x16x32_bf16 v[96:99], a[192:195], v[112:115], v[188:191]\n\t" \
  "ds_read_b64_tr_b16 a[182:183], v210 offset:13056\n\t" \
  "v_cmp_eq_u32 vcc, s39, v233\n\t" \
  "v_mfma_f32_16x16x32_bf16 v[96:99], a[196:199], v[116:119], v[96:99]\n\t" \
  "ds_read_b64_tr_b16 a[184:185], v211 offset:768\n\t" \
  "v_add_u32 v245, s39, v233\n\t" \
  "v_mfma_f32_16x16x32_bf16 v[96:99], a[200:203], v[120:123], v[96:99]\n\t" \
  "v_exp_f32 v8, v8\n\t" \
  "v_mfma_f32_16x16x32_bf16 v[96:99], a[204:207], v[124:127], v[96:99]\n\t" \
  "ds_read_b64_tr_b16 a[186:187], v211 offset:4864\n\t" \
  "v_bfi_b32 v245, 7, v245, v233\n\t" \
  "v_mfma_f32_16x16x32_bf16 v[100:103], a[220:223], v[124:127], v[188:191]\n\t" \
  "v_exp_f32 v9, v9\n\t" \
  "v_mfma_f32_16x16x32_bf16 v[100:103], a[216:219], v[120:123], v[100:103]\n\t" \
  "ds_read_b64_tr_b16 a[188:189], v211 offset:8960\n\t" \
  "v_cndmask_b32 v254, v254, v244, vcc\n\t" \
  "v_mfma_f32_16x16x32_bf16 v[100:103], a[212:215], v[116:119], v[100:103]\n\t" \
  "v_exp_f32 v10, v10\n\t" \
  "v_mfma_f32_16x16x32_bf16 v[100:103], a[208:211], v[112:115], v[100:103]\n\t" \
  "ds_read_b64_tr_b16 a[190:191], v211 offset:13056\n\t" \
  "v_and_b32 v245, 31, v245\n\t" \
  "v_mfma_f32_16x16x32_bf16 v[104:107], a[224:227], v[112:115], v[188:191]\n\t" \
  "v_exp_f32 v11, v11\n\t" \
  "v_mfma_f32_16x16x32_bf16 v[104:107], a[228:231], v[116:119], v[104:107]\n\t" \
  "v_exp_f32 v12, v12\n\t" \
  "v_mfma_f32_16x16x32_bf16 v[104:107], a[232:235], v[120:123], v[104:107]\n\t" \
  "v_exp_f32 v13, v13\n\t" \
  "v_mfma_f32_16x16x32_bf16 v[104:107], a[236:239], v[124:127], v[104:107]\n\t" \
  "v_exp_f32 v14, v14\n\t" \
  "v_mfma_f32_16x16x32_bf16 v[108:111], a[252:255], v[124:127], v[188:191]\n\t" \
  "v_exp_f32 v15, v15\n\t" \
  "v_mfma_f32_16x16x32_bf16 v[108:111], a[248:251], v[120:123], v[108:111]\n\t" \
  "v_exp_f32 v32, v32\n\t" \
  "v_mfma_f32_16x16x32_bf16 v[108:111], a[244:247], v[116:119], v[108:111]\n\t" \
  "s_waitcnt vmcnt(2) lgkmcnt(0)\n\t" \
  "v_exp_f32 v33, v33\n\t" \
  "v_mfma_f32_16x16x32_bf16 v[108:111], a[240:243], v[112:115], v[108:111]\n\t" \
  "s_barrier\n\t" \
  STA("s_memrealtime s[94:95]\n\t") \
  STA("s_memtime s[92:93]\n\t") \
  "v_mfma_f32_16x16x32_bf16 a[0:3], a[128:131], v[128:131], a[0:3]\n\t" \
  "v_exp_f32 v34, v34\n\t" \
  "v_mfma_f32_16x16x32_bf16 a[0:3], a[132:135], v[132:135], a[0:3]\n\t" \
  "v_exp_f32 v35, v35\n\t" \
  "v_mfma_f32_16x16x32_bf16 a[4:7], a[136:139], v[128:131], a[4:7]\n\t" \
  "v_exp_f32 v36, v36\n\t" \
  "v_mfma_f32_16x16x32_bf16 a[4:7], a[140:143], v[132:135], a[4:7]\n\t" \
  "v_exp_f32 v37, v37\n\t" \
  "v_mfma_f32_16x16x32_bf16 a[8:11], a[144:147], v[128:131], a[8:11]\n\t" \
  "v_exp_f32 v38, v38\n\t" \
  "v_mfma_f32_16x16x32_bf16 a[8:11], a[148:151], v[132:135], a[8:11]\n\t" \
  "v_exp_f32 v39, v39\n\t" \
  "v_mfma_f32_16x16x32_bf16 a[12:15], a[152:155], v[128:131], a[12:15]\n\t" \
  "v_exp_f32 v40, v40\n\t" \
  "v_mfma_f32_16x16x32_bf16 a[12:15], a[156:159], v[132:135], a[12:15]\n\t" \
  "v_exp_f32 v41, v41\n\t" \
  "v_mfma_f32_16x16x32_bf16 a[16:19], a[160:163], v[128:131], a[16:19]\n\t" \
  "v_exp_f32 v42, v42\n\t" \
  "v_mfma_f32_16x16x32_bf16 a[16:19], a[164:167], v[132:135], a[16:19]\n\t" \
  "v_exp_f32 v43, v43\n\t" \
  "v_mfma_f32_16x16x32_bf16 a[20:23], a[168:171], v[128:131], a[20:23]\n\t" \
  "v_exp_f32 v44, v44\n\t" \
  "v_mfma_f32_16x16x32_bf16 a[20:23], a[172:175], v[132:135], a[20:23]\n\t" \
  "v_exp_f32 v45, v45\n\t" \
  "v_mfma_f32_16x16x32_bf16 a[24:27], a[176:179], v[128:131], a[24:27]\n\t" \
  STA("v_writelane_b32 v255, s94, 40\n\t") \
  STA("v_writelane_b32 v255, s92, 41\n\t") \
  "v_exp_f32 v46, v46\n\t" \
  "v_mfma_f32_16x16x32_bf16 a[24:27], a[180:183], v[132:135], a[24:27]\n\t" \
  "v_exp_f32 v47, v47\n\t" \
  "v_mfma_f32_16x16x32_bf16 a[28:31], a[184:187], v[128:131], a[28:31]\n\t" \
  "ds_read_b128 a[192:195], v220 offset:16384\n\t" \
  "v_lshlrev_b32 v245, 2, v245\n\t" \
  "v_mfma_f32_16x16x32_bf16 a[28:31], a[188:191], v[132:135], a[28:31]\n\t" \
  "v_exp_f32 v64, v64\n\t" \
  "v_mfma_f32_16x16x32_bf16 v[160:163], v[216:219], v[128:131], v[160:163]\n\t" \
  "s_add_u32 m0, s38, 49152\n\t" \
  "v_exp_f32 v65, v65\n\t" \
  "v_mfma_f32_16x16x32_bf16 v[160:163], v[216:219], v[132:135], v[160:163]\n\t" \
  "s_add_u32 s96, s38, 98304\n\t" \
  "ds_read_b128 a[196:199], v221 offset:16384\n\t" \
  "v_cvt_pk_bf16_f32 v128, v0, v1\n\t" \
  "v_mfma_f32_16x16x32_bf16 a[32:35], a[128:131], v[136:139], a[32:35]\n\t" \
  "v_cvt_pk_bf16_f32 v129, v2, v3\n\t" \
  "v_cvt_pk_bf16_f32 v130, v4, v5\n\t" \
  "v_mfma_f32_16x16x32_bf16 a[32:35], a[132:135], v[140:143], a[32:35]\n\t" \
  "v_exp_f32 v66, v66\n\t" \
  "v_mfma_f32_16x16x32_bf16 a[36:39], a[136:139], v[136:139], a[36:39]\n\t" \
  "buffer_load_dwordx4 v224, s[60:63], s96 offen offset:0 lds\n\t" \
  "v_mfma_f32_16x16x32_bf16 a[36:39], a[140:143], v[140:143], a[36:39]\n\t" \
  "ds_read_b128 a[200:203], v222 offset:16384\n\t" \
  "v_cvt_pk_bf16_f32 v131, v6, v7\n\t" \
  "v_mfma_f32_16x16x32_bf16 a[40:43], a[144:147], v[136:139], a[40:43]\n\t" \
  "v_exp_f32 v67, v67\n\t" \
  "v_mfma_f32_16x16x32_bf16 a[40:43], a[148:151], v[140:143], a[40:43]\n\t" \
  "ds_read_b128 a[204:207], v223 offset:16384\n\t" \
  "v_cvt_pk_bf16_f32 v132, v8, v9\n\t" \
  "v_mfma_f32_16x16x32_bf16 a[44:47], a[152:155], v[136:139], a[44:47]\n\t" \
  "v_exp_f32 v68, v68\n\t" \
  "v_mfma_f32_16x16x32_bf16 a[44:47], a[156:159], v[140:143], a[44:47]\n\t" \
  "v_exp_f32 v69, v69\n\t" \
  "v_mfma_f32_16x16x32_bf16 a[48:51], a[160:163], v[136:139], a[48:51]\n\t" \
  "buffer_load_dwordx4 v225, s[60:63], s96 offen offset:1024 lds\n\t" \
  "v_mfma_f32_16x16x32_bf16 a[48:51], a[164:167], v[140:143], a[48:51]\n\t" \
  "ds_read_b128 a[208:211], v220 offset:20480\n\t" \
  "v_cvt_pk_bf16_f32 v133, v10, v11\n\t" \
  "v_mfma_f32_16x16x32_bf16 a[52:55], a[168:171], v[136:139], a[52:55]\n\t" \
  "v_exp_f32 v70, v70\n\t" \
  "v_mfma_f32_16x16x32_bf16 a[52:55], a[172:175], v[140:143], a[52:55]\n\t" \
  "ds_read_b128 a[212:215], v221 offset:20480\n\t" \
  "v_cvt_pk_bf16_f32 v134, v12, v13\n\t" \
  "v_mfma_f32_16x16x32_bf16 a[56:59], a[176:179], v[136:139], a[56:59]\n\t" \
  "v_exp_f32 v71, v71\n\t" \
  "v_mfma_f32_16x16x32_bf16 a[56:59], a[180:183], v[140:143], a[56:59]\n\t" \
  "v_exp_f32 v72, v72\n\t" \
  "v_mfma_f32_16x16x32_bf16 a[60:63], a[184:187], v[136:139], a[60:63]\n\t" \
  "buffer_load_dwordx4 v226, s[60:63], s96 offen offset:2048 lds\n\t" \
  "v_mfma_f32_16x16x32_bf16 a[60:63], a[188:191], v[140:143], a[60:63]\n\t" \
  "ds_read_b128 a[216:219], v222 offset:20480\n\t" \
  "v_cvt_pk_bf16_f32 v135, v14, v15\n\t" \
  "v_mfma_f32_16x16x32_bf16 v[164:167], v[216:219], v[136:139], v[164:167]\n\t" \
  "v_exp_f32 v73, v73\n\t" \
  "v_mfma_f32_16x16x32_bf16 v[164:167], v[216:219], v[140:143], v[164:167]\n\t" \
  "ds_read_b128 a[220:223], v223 offset:20480\n\t" \
  "v_cvt_pk_bf16_f32 v136, v32, v33\n\t" \
  "v_mfma_f32_16x16x32_bf16 a[64:67], a[128:131], v[144:147], a[64:67]\n\t" \
  "v_cvt_pk_bf16_f32 v137, v34, v35\n\t" \
  "v_cvt_pk_bf16_f32 v138, v36, v37\n\t" \
  "v_mfma_f32_16x16x32_bf16 a[64:67], a[132:135], v[148:151], a[64:67]\n\t" \
  "v_exp_f32 v74, v74\n\t" \
  "v_mfma_f32_16x16x32_bf16 a[68:71], a[136:139], v[144:147], a[68:71]\n\t" \
  "buffer_load_dwordx4 v227, s[60:63], s96 offen offset:3072 lds\n\t" \
  "v_mfma_f32_16x16x32_bf16 a[68:71], a[140:143], v[148:151], a[68:71]\n\t" \
  "ds_read_b128 a[224:227], v220 offset:24576\n\t" \
  "v_cvt_pk_bf16_f32 v139, v38, v39\n\t" \
  "v_mfma_f32_16x16x32_bf16 a[72:75], a[144:147], v[144:147], a[72:75]\n\t" \
  "s_add_u32 m0, s38, 147456\n\t" \
  "v_exp_f32 v75, v75\n\t" \
  "v_mfma_f32_16x16x32_bf16 a[72:75], a[148:151], v[148:151], a[72:75]\n\t" \
  "s_add_u32 s96, s38, 81920\n\t" \
  "ds_read_b128 a[228:231], v221 offset:24576\n\t" \
  "v_cvt_pk_bf16_f32 v140, v40, v41\n\t" \
  "v_mfma_f32_16x16x32_bf16 a[76:79], a[152:155], v[144:147], a[76:79]\n\t" \
  "v_exp_f32 v76, v76\n\t" \
  "v_mfma_f32_16x16x32_bf16 a[76:79], a[156:159], v[148:151], a[76:79]\n\t" \
  "v_exp_f32 v77, v77\n\t" \
  "v_mfma_f32_16x16x32_bf16 a[80:83], a[160:163], v[144:147], a[80:83]\n\t" \
  "buffer_load_dwordx4 v230, s[64:67], s96 offen offset:0 lds\n\t" \
  "v_mfma_f32_16x16x32_bf16 a[80:83], a[164:167], v[148:151], a[80:83]\n\t" \
  "ds_read_b128 a[232:235], v222 offset:24576\n\t" \
  "v_cvt_pk_bf16_f32 v141, v42, v43\n\t" \
  "v_mfma_f32_16x16x32_bf16 a[84:87], a[168:171], v[144:147], a[84:87]\n\t" \
  "v_exp_f32 v78, v78\n\t" \
  "v_mfma_f32_16x16x32_bf16 a[84:87], a[172:175], v[148:151], a[84:87]\n\t" \
  "ds_read_b128 a[236:239], v223 offset:24576\n\t" \
  "v_cvt_pk_bf16_f32 v142, v44, v45\n\t" \
  "v_mfma_f32_16x16x32_bf16 a[88:91], a[176:179], v[144:147], a[88:91]\n\t" \
  "v_exp_f32 v79, v79\n\t" \
  "v_mfma_f32_16x16x32_bf16 a[88:91], a[180:183], v[148:151], a[88:91]\n\t" \
  "v_exp_f32 v96, v96\n\t" \
  "v_mfma_f32_16x16x32_bf16 a[92:95], a[184:187], v[144:147], a[92:95]\n\t" \
  "buffer_load_dwordx4 v231, s[64:67], s96 offen offset:1024 lds\n\t" \
  "v_mfma_f32_16x16x32_bf16 a[92:95], a[188:191], v[148:151], a[92:95]\n\t" \
  "ds_read_b128 a[240:243], v220 offset:28672\n\t" \
  "v_cvt_pk_bf16_f32 v143, v46, v47\n\t" \
  "v_mfma_f32_16x16x32_bf16 v[168:171], v[216:219], v[144:147], v[168:171]\n\t" \
  ".Lhs8:\n\ts_cbranch_execz .Lhs8n\n\t" \
  "v_exp_f32 v97, v97\n\t" \
  "v_mfma_f32_16x16x32_bf16 v[168:171], v[216:219], v[148:151], v[168:171]\n\t" \
  "ds_read_b128 a[244:247], v221 offset:28672\n\t" \
  "v_cvt_pk_bf16_f32 v144, v64, v65\n\t" \
  "v_mfma_f32_16x16x32_bf16 a[96:99], a[128:131], v[152:155], a[96:99]\n\t" \
  "v_cvt_pk_bf16_f32 v145, v66, v67\n\t" \
  "v_cvt_pk_bf16_f32 v146, v68, v69\n\t" \
  "v_mfma_f32_16x16x32_bf16 a[96:99], a[132:135], v[156:159], a[96:99]\n\t" \
  "v_exp_f32 v98, v98\n\t" \
  "v_mfma_f32_16x16x32_bf16 a[100:103], a[136:139], v[152:155], a[100:103]\n\t" \
  "buffer_load_dwordx4 v230, s[64:67], s96 offen offset:2048 lds\n\t" \
  "v_mfma_f32_16x16x32_bf16 a[100:103], a[140:143], v[156:159], a[100:103]\n\t" \
  "ds_read_b128 a[248:251], v222 offset:28672\n\t" \
  "v_cvt_pk_bf16_f32 v147, v70, v71\n\t" \
  "v_mfma_f32_16x16x32_bf16 a[104:107], a[144:147], v[152:155], a[104:107]\n\t" \
  "v_exp_f32 v99, v99\n\t" \
  "v_mfma_f32_16x16x32_bf16 a[104:107], a[148:151], v[156:159], a[104:107]\n\t" \
  "ds_read_b128 a[252:255], v223 offset:28672\n\t" \
  "v_cvt_pk_bf16_f32 v148, v72, v73\n\t" \
  "v_mfma_f32_16x16x32_bf16 a[108:111], a[152:155], v[152:155], a[108:111]\n\t" \
  "v_exp_f32 v100, v100\n\t" \
  "v_mfma_f32_16x16x32_bf16 a[108:111], a[156:159], v[156:159], a[108:111]\n\t" \
  "v_exp_f32 v101, v101\n\t" \
  "v_mfma_f32_16x16x32_bf16 a[112:115], a[160:163], v[152:155], a[112:115]\n\t" \
  "buffer_load_dwordx4 v231, s[64:67], s96 offen offset:3072 lds\n\t" \
  "v_mfma_f32_16x16x32_bf16 a[112:115], a[164:167], v[156:159], a[112:115]\n\t" \
  "v_cvt_pk_bf16_f32 v149, v74, v75\n\t" \
  "v_cvt_pk_bf16_f32 v150, v76, v77\n\t" \
  "v_mfma_f32_16x16x32_bf16 a[116:119], a[168:171], v[152:155], a[116:119]\n\t" \
  "v_exp_f32 v102, v102\n\t" \
  "v_mfma_f32_16x16x32_bf16 a[116:119], a[172:175], v[156:159], a[116:119]\n\t" \
  "v_exp_f32 v103, v103\n\t" \
  "v_mfma_f32_16x16x32_bf16 a[120:123], a[176:179], v[152:155], a[120:123]\n\t" \
  "v_exp_f32 v104, v104\n\t" \
  "v_mfma_f32_16x16x32_bf16 a[120:123], a[180:183], v[156:159], a[120:123]\n\t" \
  "s_add_u32 s99, s52, 4096\n\t" \
  "v_exp_f32 v105, v105\n\t" \
  "v_mfma_f32_16x16x32_bf16 a[124:127], a[184:187], v[152:155], a[124:127]\n\t" \
  "v_exp_f32 v106, v106\n\t" \
  "v_mfma_f32_16x16x32_bf16 a[124:127], a[188:191], v[156:159], a[124:127]\n\t" \
  "v_exp_f32 v107, v107\n\t" \
  "v_mfma_f32_16x16x32_bf16 v[172:175], v[216:219], v[152:155], v[172:175]\n\t" \
  "buffer_load_dword v246, v212, s[24:27], s99 offen\n\t" \
  "v_mfma_f32_16x16x32_bf16 v[172:175], v[216:219], v[156:159], v[172:175]\n\t" \
  "s_waitcnt lgkmcnt(12)\n\t" \
  "v_cvt_pk_bf16_f32 v151, v78, v79\n\t" \
  "v_cvt_pk_bf16_f32 v152, v96, v97\n\t" \
  STA("s_memrealtime s[94:95]\n\t") \
  STA("s_memtime s[92:93]\n\t") \
  "v_mfma_f32_16x16x32_bf16 v[0:3], a[192:195], v[16:19], v[176:179]\n\t" \
  "ds_read_b64_tr_b16 a[128:129], v228 offset:0\n\t" \
  "v_cvt_pk_bf16_f32 v153, v98, v99\n\t" \
  "v_mfma_f32_16x16x32_bf16 v[0:3], a[196:199], v[20:23], v[0:3]\n\t" \
  "v_cvt_pk_bf16_f32 v154, v100, v101\n\t" \
  "v_cvt_pk_bf16_f32 v155, v102, v103\n\t" \
  "v_mfma_f32_16x16x32_bf16 v[0:3], a[200:203], v[24:27], v[0:3]\n\t" \
  "ds_read_b64_tr_b16 a[130:131], v228 offset:4096\n\t" \
  "v_cvt_pk_bf16_f32 v156, v104, v105\n\t" \
  "v_mfma_f32_16x16x32_bf16 v[0:3], a[204:207], v[28:31], v[0:3]\n\t" \
  "s_waitcnt lgkmcnt(10)\n\t" \
  "v_exp_f32 v108, v108\n\t" \
  "v_mfma_f32_16x16x32_bf16 v[4:7], a[220:223], v[28:31], v[176:179]\n\t" \
  "ds_read_b64_tr_b16 a[132:133], v228 offset:8192\n\t" \
  "v_cvt_pk_bf16_f32 v157, v106, v107\n\t" \
  "v_mfma_f32_16x16x32_bf16 v[4:7], a[216:219], v[24:27], v[4:7]\n\t" \
  "v_exp_f32 v109, v109\n\t" \
  "v_mfma_f32_16x16x32_bf16 v[4:7], a[212:215], v[20:23], v[4:7]\n\t" \
  "ds_read_b64_tr_b16 a[134:135], v228 offset:12288\n\t" \
  "v_cvt_pk_bf16_f32 v158, v108, v109\n\t" \
  "v_mfma_f32_16x16x32_bf16 v[4:7], a[208:211], v[16:19], v[4:7]\n\t" \
  "s_waitcnt lgkmcnt(8)\n\t" \
  "ds_read_b64_tr_b16 a[136:137], v229 offset:0\n\t" \
  "v_writelane_b32 v244, 0, 0\n\t" \
  "v_mfma_f32_16x16x32_bf16 v[8:11], a[224:227], v[16:19], v[176:179]\n\t" \
  "v_exp_f32 v110, v110\n\t" \
  "v_mfma_f32_16x16x32_bf16 v[8:11], a[228:231], v[20:23], v[8:11]\n\t" \
  "s_add_u32 s99, s52, 6144\n\t" \
  "v_exp_f32 v111, v111\n\t" \
  "v_mfma_f32_16x16x32_bf16 v[8:11], a[232:235], v[24:27], v[8:11]\n\t" \
  "ds_read_b64_tr_b16 a[138:139], v229 offset:4096\n\t" \
  "v_cvt_pk_bf16_f32 v159, v110, v111\n\t" \
  "v_mfma_f32_16x16x32_bf16 v[8:11], a[236:239], v[28:31], v[8:11]\n\t" \
  "s_waitcnt lgkmcnt(6)\n\t" \
  "v_exp_f32 v0, v0\n\t" \
  "v_mfma_f32_16x16x32_bf16 v[12:15], a[252:255], v[28:31], v[176:179]\n\t" \
  STA("v_writelane_b32 v255, s94, 42\n\t") \
  STA("v_writelane_b32 v255, s92, 43\n\t") \
  "buffer_load_dword v246, v212, s[24:27], s99 offen\n\t" \
  "v_mfma_f32_16x16x32_bf16 v[12:15], a[248:251], v[24:27], v[12:15]\n\t" \
  "v_exp_f32 v1, v1\n\t" \
  "v_mfma_f32_16x16x32_bf16 v[12:15], a[244:247], v[20:23], v[12:15]\n\t" \
  "v_exp_f32 v2, v2\n\t" \
  "v_mfma_f32_16x16x32_bf16 v[12:15], a[240:243], v[16:19], v[12:15]\n\t" \
  "ds_read_b64_tr_b16 a[140:141], v229 offset:8192\n\t" \
  "v_mfma_f32_16x16x32_bf16 v[32:35], a[192:195], v[48:51], v[180:183]\n\t" \
  "ds_read_b64_tr_b16 a[142:143], v229 offset:12288\n\t" \
  "v_mfma_f32_16x16x32_bf16 v[32:35], a[196:199], v[52:55], v[32:35]\n\t" \
  "v_exp_f32 v3, v3\n\t" \
  "v_mfma_f32_16x16x32_bf16 v[32:35], a[200:203], v[56:59], v[32:35]\n\t" \
  "ds_read_b64_tr_b16 a[144:145], v228 offset:256\n\t" \
  "v_mfma_f32_16x16x32_bf16 v[32:35], a[204:207], v[60:63], v[32:35]\n\t" \
  "v_exp_f32 v4, v4\n\t" \
  "v_mfma_f32_16x16x32_bf16 v[36:39], a[220:223], v[60:63], v[180:183]\n\t" \
  "ds_read_b64_tr_b16 a[146:147], v228 offset:4352\n\t" \
  "v_mfma_f32_16x16x32_bf16 v[36:39], a[216:219], v[56:59], v[36:39]\n\t" \
  "ds_read_b64_tr_b16 a[148:149], v228 offset:8448\n\t" \
  "v_mfma_f32_16x16x32_bf16 v[36:39], a[212:215], v[52:55], v[36:39]\n\t" \
  "v_exp_f32 v5, v5\n\t" \
  "v_mfma_f32_16x16x32_bf16 v[36:39], a[208:211], v[48:51], v[36:39]\n\t" \
  "ds_read_b64_tr_b16 a[150:151], v228 offset:12544\n\t" \
  "v_mfma_f32_16x16x32_bf16 v[40:43], a[224:227], v[48:51], v[180:183]\n\t" \
  "v_exp_f32 v6, v6\n\t" \
  "v_mfma_f32_16x16x32_bf16 v[40:43], a[228:231], v[52:55], v[40:43]\n\t" \
  "ds_read_b64_tr_b16 a[152:153], v229 offset:256\n\t" \
  "v_mfma_f32_16x16x32_bf16 v[40:43], a[232:235], v[56:59], v[40:43]\n\t" \
  "v_exp_f32 v7, v7\n\t" \
  "v_mfma_f32_16x16x32_bf16 v[40:43], a[236:239], v[60:63], v[40:43]\n\t" \
  "ds_read_b64_tr_b16 a[154:155], v229 offset:4352\n\t" \
  "v_mfma_f32_16x16x32_bf16 v[44:47], a[252:255], v[60:63], v[180:183]\n\t" \
  "ds_read_b64_tr_b16 a[156:157], v229 offset:8448\n\t" \
  "v_mfma_f32_16x16x32_bf16 v[44:47], a[248:251], v[56:59], v[44:47]\n\t" \
  "v_exp_f32 v8, v8\n\t" \
  "v_mfma_f32_16x16x32_bf16 v[44:47], a[244:247], v[52:55], v[44:47]\n\t" \
  "ds_read_b64_tr_b16 a[158:159], v229 offset:12544\n\t" \
  "v_mfma_f32_16x16x32_bf16 v[44:47], a[240:243], v[48:51], v[44:47]\n\t" \
  "v_exp_f32 v9, v9\n\t" \
  "v_mfma_f32_16x16x32_bf16 v[64:67], a[192:195], v[80:83], v[184:187]\n\t" \
  "ds_read_b64_tr_b16 a[160:161], v228 offset:512\n\t" \
  "v_mfma_f32_16x16x32_bf16 v[64:67], a[196:199], v[84:87], v[64:67]\n\t" \
  "ds_read_b64_tr_b16 a[162:163], v228 offset:4608\n\t" \
  "v_mfma_f32_16x16x32_bf16 v[64:67], a[200:203], v[88:91], v[64:67]\n\t" \
  "v_exp_f32 v10, v10\n\t" \
  "v_mfma_f32_16x16x32_bf16 v[64:67], a[204:207], v[92:95], v[64:67]\n\t" \
  "ds_read_b64_tr_b16 a[164:165], v228 offset:8704\n\t" \
  "v_mfma_f32_16x16x32_bf16 v[68:71], a[220:223], v[92:95], v[184:187]\n\t" \
  "v_exp_f32 v11, v11\n\t" \
  "v_mfma_f32_16x16x32_bf16 v[68:71], a[216:219], v[88:91], v[68:71]\n\t" \
  "ds_read_b64_tr_b16 a[166:167], v228 offset:12800\n\t" \
  "v_mfma_f32_16x16x32_bf16 v[68:71], a[212:215], v[84:87], v[68:71]\n\t" \
  "ds_read_b64_tr_b16 a[168:169], v229 offset:512\n\t" \
  "v_mfma_f32_16x16x32_bf16 v[68:71], a[208:211], v[80:83], v[68:71]\n\t" \
  "v_exp_f32 v12, v12\n\t" \
  "v_mfma_f32_16x16x32_bf16 v[72:75], a[224:227], v[80:83], v[184:187]\n\t" \
  "ds_read_b64_tr_b16 a[170:171], v229 offset:4608\n\t" \
  "v_mfma_f32_16x16x32_bf16 v[72:75], a[228:231], v[84:87], v[72:75]\n\t" \
  "ds_read_b64_tr_b16 a[172:173], v229 offset:8704\n\t" \
  "v_mfma_f32_16x16x32_bf16 v[72:75], a[232:235], v[88:91], v[72:75]\n\t" \
  "v_exp_f32 v13, v13\n\t" \
  "v_mfma_f32_16x16x32_bf16 v[72:75], a[236:239], v[92:95], v[72:75]\n\t" \
  "ds_read_b64_tr_b16 a[174:175], v229 offset:12800\n\t" \
  "v_mfma_f32_16x16x32_bf16 v[76:79], a[252:255], v[92:95], v[184:187]\n\t" \
  "v_exp_f32 v14, v14\n\t" \
  "v_mfma_f32_16x16x32_bf16 v[76:79], a[248:251], v[88:91], v[76:79]\n\t" \
  "ds_read_b64_tr_b16 a[176:177], v228 offset:768\n\t" \
  "v_mfma_f32_16x16x32_bf16 v[76:79], a[244:247], v[84:87], v[76:79]\n\t" \
  "ds_read_b64_tr_b16 a[178:179], v228 offset:4864\n\t" \
  "v_mfma_f32_16x16x32_bf16 v[76:79], a[240:243], v[80:83], v[76:79]\n\t" \
  "v_exp_f32 v15, v15\n\t" \
  "v_mfma_f32_16x16x32_bf16 v[96:99], a[192:195], v[112:115], v[188:191]\n\t" \
  "ds_read_b64_tr_b16 a[180:181], v228 offset:8960\n\t" \
  "v_mfma_f32_16x16x32_bf16 v[96:99], a[196:199], v[116:119], v[96:99]\n\t" \
  "ds_read_b64_tr_b16 a[182:183], v228 offset:13056\n\t" \
  "v_mfma_f32_16x16x32_bf16 v[96:99], a[200:203], v[120:123], v[96:99]\n\t" \
  "v_exp_f32 v32, v32\n\t" \
  "v_mfma_f32_16x16x32_bf16 v[96:99], a[204:207], v[124:127], v[96:99]\n\t" \
  "ds_read_b64_tr_b16 a[184:185], v229 offset:768\n\t" \
  "v_mfma_f32_16x16x32_bf16 v[100:103], a[220:223], v[124:127], v[188:191]\n\t" \
  "v_exp_f32 v33, v33\n\t" \
  "v_mfma_f32_16x16x32_bf16 v[100:103], a[216:219], v[120:123], v[100:103]\n\t" \
  "ds_read_b64_tr_b16 a[186:187], v229 offset:4864\n\t" \
  "v_mfma_f32_16x16x32_bf16 v[100:103], a[212:215], v[116:119], v[100:103]\n\t" \
  "ds_read_b64_tr_b16 a[188:189], v229 offset:8960\n\t" \
  "v_mfma_f32_16x16x32_bf16 v[100:103], a[208:211], v[112:115], v[100:103]\n\t" \
  "v_exp_f32 v34, v34\n\t" \
  "v_mfma_f32_16x16x32_bf16 v[104:107], a[224:227], v[112:115], v[188:191]\n\t" \
  "ds_read_b64_tr_b16 a[190:191], v229 offset:13056\n\t" \
  "v_mfma_f32_16x16x32_bf16 v[104:107], a[228:231], v[116:119], v[104:107]\n\t" \
  "v_exp_f32 v35, v35\n\t" \
  "v_mfma_f32_16x16x32_bf16 v[104:107], a[232:235], v[120:123], v[104:107]\n\t" \
  "v_exp_f32 v36, v36\n\t" \
  "v_mfma_f32_16x16x32_bf16 v[104:107], a[236:239], v[124:127], v[104:107]\n\t" \
  "v_exp_f32 v37, v37\n\t" \
  "v_mfma_f32_16x16x32_bf16 v[108:111], a[252:255], v[124:127], v[188:191]\n\t" \
  "v_exp_f32 v38, v38\n\t" \
  "v_mfma_f32_16x16x32_bf16 v[108:111], a[248:251], v[120:123], v[108:111]\n\t" \
  "v_exp_f32 v39, v39\n\t" \
  "v_mfma_f32_16x16x32_bf16 v[108:111], a[244:247], v[116:119], v[108:111]\n\t" \
  "s_waitcnt vmcnt(2) lgkmcnt(0)\n\t" \
  "v_exp_f32 v40, v40\n\t" \
  "v_mfma_f32_16x16x32_bf16 v[108:111], a[240:243], v[112:115], v[108:111]\n\t" \
  "s_barrier\n\t" \
  STA("s_memrealtime s[94:95]\n\t") \
  STA("s_memtime s[92:93]\n\t") \
  "v_mfma_f32_16x16x32_bf16 a[0:3], a[128:131], v[128:131], a[0:3]\n\t" \
  "v_exp_f32 v41, v41\n\t" \
  "v_mfma_f32_16x16x32_bf16 a[0:3], a[132:135], v[132:135], a[0:3]\n\t" \
  "v_exp_f32 v42, v42\n\t" \
  "v_mfma_f32_16x16x32_bf16 a[4:7], a[136:139], v[128:131], a[4:7]\n\t" \
  "v_exp_f32 v43, v43\n\t" \
  "v_mfma_f32_16x16x32_bf16 a[4:7], a[140:143], v[132:135], a[4:7]\n\t" \
  "v_exp_f32 v44, v44\n\t" \
  "v_mfma_f32_16x16x32_bf16 a[8:11], a[144:147], v[128:131], a[8:11]\n\t" \
  "v_exp_f32 v45, v45\n\t" \
  "v_mfma_f32_16x16x32_bf16 a[8:11], a[148:151], v[132:135], a[8:11]\n\t" \
  "v_exp_f32 v46, v46\n\t" \
  "v_mfma_f32_16x16x32_bf16 a[12:15], a[152:155], v[128:131], a[12:15]\n\t" \
  "v_exp_f32 v47, v47\n\t" \
  "v_mfma_f32_16x16x32_bf16 a[12:15], a[156:159], v[132:135], a[12:15]\n\t" \
  "v_exp_f32 v64, v64\n\t" \
  "v_mfma_f32_16x16x32_bf16 a[16:19], a[160:163], v[128:131], a[16:19]\n\t" \
  "v_exp_f32 v65, v65\n\t" \
  "v_mfma_f32_16x16x32_bf16 a[16:19], a[164:167], v[132:135], a[16:19]\n\t" \
  "v_exp_f32 v66, v66\n\t" \
  "v_mfma_f32_16x16x32_bf16 a[20:23], a[168:171], v[128:131], a[20:23]\n\t" \
  "v_exp_f32 v67, v67\n\t" \
  "v_mfma_f32_16x16x32_bf16 a[20:23], a[172:175], v[132:135], a[20:23]\n\t" \
  "v_exp_f32 v68, v68\n\t" \
  "v_mfma_f32_16x16x32_bf16 a[24:27], a[176:179], v[128:131], a[24:27]\n\t" \
  STA("v_writelane_b32 v255, s94, 44\n\t") \
  STA("v_writelane_b32 v255, s92, 45\n\t") \
  "v_exp_f32 v69, v69\n\t" \
  "v_mfma_f32_16x16x32_bf16 a[24:27], a[180:183], v[132:135], a[24:27]\n\t" \
  "v_exp_f32 v70, v70\n\t" \
  "v_mfma_f32_16x16x32_bf16 a[28:31], a[184:187], v[128:131], a[28:31]\n\t" \
  "v_exp_f32 v71, v71\n\t" \
  "v_mfma_f32_16x16x32_bf16 a[28:31], a[188:191], v[132:135], a[28:31]\n\t" \
  "v_exp_f32 v72, v72\n\t" \
  "v_mfma_f32_16x16x32_bf16 v[160:163], v[216:219], v[128:131], v[160:163]\n\t" \
  "s_add_u32 m0, s38, 65536\n\t" \
  "v_exp_f32 v73, v73\n\t" \
  "v_mfma_f32_16x16x32_bf16 v[160:163], v[216:219], v[132:135], v[160:163]\n\t" \
  "s_add_u32 s96, s38, 114688\n\t" \
  "ds_read_b128 a[192:195], v220 offset:0\n\t" \
  "v_cvt_pk_bf16_f32 v128, v0, v1\n\t" \
  "v_mfma_f32_16x16x32_bf16 a[32:35], a[128:131], v[136:139], a[32:35]\n\t" \
  "ds_read_b128 a[196:199], v221 offset:0\n\t" \
  "v_cvt_pk_bf16_f32 v129, v2, v3\n\t" \
  "v_mfma_f32_16x16x32_bf16 a[32:35], a[132:135], v[140:143], a[32:35]\n\t" \
  "ds_read_b128 v[192:195], v215 offset:32768\n\t" \
  "v_cvt_pk_bf16_f32 v130, v4, v5\n\t" \
  "v_mfma_f32_16x16x32_bf16 a[36:39], a[136:139], v[136:139], a[36:39]\n\t" \
  "buffer_load_dwordx4 v224, s[60:63], s96 offen offset:0 lds\n\t" \
  "v_mfma_f32_16x16x32_bf16 a[36:39], a[140:143], v[140:143], a[36:39]\n\t" \
  "ds_read_b128 a[200:203], v222 offset:0\n\t" \
  "v_cvt_pk_bf16_f32 v131, v6, v7\n\t" \
  "v_mfma_f32_16x16x32_bf16 a[40:43], a[144:147], v[136:139], a[40:43]\n\t" \
  "ds_read_b128 v[196:199], v215 offset:33792\n\t" \
  "v_cvt_pk_bf16_f32 v132, v8, v9\n\t" \
  "v_mfma_f32_16x16x32_bf16 a[40:43], a[148:151], v[140:143], a[40:43]\n\t" \
  "ds_read_b128 v[200:203], v215 offset:34816\n\t" \
  "v_cvt_pk_bf16_f32 v133, v10, v11\n\t" \
  "v_mfma_f32_16x16x32_bf16 a[44:47], a[152:155], v[136:139], a[44:47]\n\t" \
  "buffer_load_dwordx4 v225, s[60:63], s96 offen offset:1024 lds\n\t" \
  "v_mfma_f32_16x16x32_bf16 a[44:47], a[156:159], v[140:143], a[44:47]\n\t" \
  "ds_read_b128 a[204:207], v223 offset:0\n\t" \
  "v_cvt_pk_bf16_f32 v134, v12, v13\n\t" \
  "v_mfma_f32_16x16x32_bf16 a[48:51], a[160:163], v[136:139], a[48:51]\n\t" \
  "ds_read_b128 v[204:207], v215 offset:35840\n\t" \
  "v_cvt_pk_bf16_f32 v135, v14, v15\n\t" \
  "v_mfma_f32_16x16x32_bf16 a[48:51], a[164:167], v[140:143], a[48:51]\n\t" \
  "v_exp_f32 v74, v74\n\t" \
  "v_mfma_f32_16x16x32_bf16 a[52:55], a[168:171], v[136:139], a[52:55]\n\t" \
  "buffer_load_dwordx4 v226, s[60:63], s96 offen offset:2048 lds\n\t" \
  "v_mfma_f32_16x16x32_bf16 a[52:55], a[172:175], v[140:143], a[52:55]\n\t" \
  "v_exp_f32 v75, v75\n\t" \
  "v_mfma_f32_16x16x32_bf16 a[56:59], a[176:179], v[136:139], a[56:59]\n\t" \
  "v_exp_f32 v76, v76\n\t" \
  "v_mfma_f32_16x16x32_bf16 a[56:59], a[180:183], v[140:143], a[56:59]\n\t" \
  "ds_read_b128 a[208:211], v220 offset:4096\n\t" \
  "v_mfma_f32_16x16x32_bf16 a[60:63], a[184:187], v[136:139], a[60:63]\n\t" \
  "buffer_load_dwordx4 v227, s[60:63], s96 offen offset:3072 lds\n\t" \
  "v_mfma_f32_16x16x32_bf16 a[60:63], a[188:191], v[140:143], a[60:63]\n\t" \
  "s_add_u32 m0, s38, 81920\n\t" \
  "ds_read_b128 a[212:215], v221 offset:4096\n\t" \
  "s_add_u32 s96, s38, 98304\n\t" \
  "v_mfma_f32_16x16x32_bf16 v[164:167], v[216:219], v[136:139], v[164:167]\n\t" \
  "v_exp_f32 v77, v77\n\t" \
  "v_mfma_f32_16x16x32_bf16 v[164:167], v[216:219], v[140:143], v[164:167]\n\t" \
  "ds_read_b128 a[216:219], v222 offset:4096\n\t" \
  "v_cvt_pk_bf16_f32 v136, v32, v33\n\t" \
  "v_mfma_f32_16x16x32_bf16 a[64:67], a[128:131], v[144:147], a[64:67]\n\t" \
  "buffer_load_dwordx4 v230, s[64:67], s96 offen offset:0 lds\n\t" \
  "v_mfma_f32_16x16x32_bf16 a[64:67], a[132:135], v[148:151], a[64:67]\n\t" \
  "ds_read_b128 a[220:223], v223 offset:4096\n\t" \
  "v_cvt_pk_bf16_f32 v137, v34, v35\n\t" \
  "v_mfma_f32_16x16x32_bf16 a[68:71], a[136:139], v[144:147], a[68:71]\n\t" \
  "ds_read_b128 a[224:227], v220 offset:8192\n\t" \
  "v_cvt_pk_bf16_f32 v138, v36, v37\n\t" \
  "v_mfma_f32_16x16x32_bf16 a[68:71], a[140:143], v[148:151], a[68:71]\n\t" \
  "v_cvt_pk_bf16_f32 v139, v38, v39\n\t" \
  "v_cvt_pk_bf16_f32 v140, v40, v41\n\t" \
  "v_mfma_f32_16x16x32_bf16 a[72:75], a[144:147], v[144:147], a[72:75]\n\t" \
  "buffer_load_dwordx4 v231, s[64:67], s96 offen offset:1024 lds\n\t" \
  "v_mfma_f32_16x16x32_bf16 a[72:75], a[148:151], v[148:151], a[72:75]\n\t" \
  "ds_read_b128 a[228:231], v221 offset:8192\n\t" \
  "v_cvt_pk_bf16_f32 v141, v42, v43\n\t" \
  "v_mfma_f32_16x16x32_bf16 a[76:79], a[152:155], v[144:147], a[76:79]\n\t" \
  "v_exp_f32 v78, v78\n\t" \
  "v_mfma_f32_16x16x32_bf16 a[76:79], a[156:159], v[148:151], a[76:79]\n\t" \
  "v_cvt_pk_bf16_f32 v142, v44, v45\n\t" \
  "v_cvt_pk_bf16_f32 v143, v46, v47\n\t" \
  "v_mfma_f32_16x16x32_bf16 a[80:83], a[160:163], v[144:147], a[80:83]\n\t" \
  "buffer_load_dwordx4 v230, s[64:67], s96 offen offset:2048 lds\n\t" \
  "v_mfma_f32_16x16x32_bf16 a[80:83], a[164:167], v[148:151], a[80:83]\n\t" \
  "v_exp_f32 v79, v79\n\t" \
  "v_mfma_f32_16x16x32_bf16 a[84:87], a[168:171], v[144:147], a[84:87]\n\t" \
  "v_exp_f32 v96, v96\n\t" \
  "v_mfma_f32_16x16x32_bf16 a[84:87], a[172:175], v[148:151], a[84:87]\n\t" \
  "v_exp_f32 v97, v97\n\t" \
  "v_mfma_f32_16x16x32_bf16 a[88:91], a[176:179], v[144:147], a[88:91]\n\t" \
  "buffer_load_dwordx4 v231, s[64:67], s96 offen offset:3072 lds\n\t" \
  "v_mfma_f32_16x16x32_bf16 a[88:91], a[180:183], v[148:151], a[88:91]\n\t" \
  "s_add_u32 s96, s40, 524288\n\t" \
  "ds_read_b128 a[232:235], v222 offset:8192\n\t" \
  "s_and_b32 s96, s96, 0x1c0000\n\t" \
  "v_mfma_f32_16x16x32_bf16 a[92:95], a[184:187], v[144:147], a[92:95]\n\t" \
  "s_add_u32 s96, s96, s51\n\t" \
  "ds_read_b128 a[236:239], v223 offset:8192\n\t" \
  "s_add_u32 m0, s38, 131072\n\t" \
  "v_mfma_f32_16x16x32_bf16 a[92:95], a[188:191], v[148:151], a[92:95]\n\t" \
  "v_exp_f32 v98, v98\n\t" \
  "v_mfma_f32_16x16x32_bf16 v[168:171], v[216:219], v[144:147], v[168:171]\n\t" \
  "s_waitcnt lgkmcnt(8)\n\t" \
  "buffer_load_dwordx4 v240, s[24:27], s96 offen offset:0 lds\n\t" \
  "v_mfma_f32_16x16x32_bf16 v[168:171], v[216:219], v[148:151], v[168:171]\n\t" \
  "ds_read_b128 a[240:243], v220 offset:12288\n\t" \
  "v_cvt_pk_bf16_f32 v144, v64, v65\n\t" \
  "v_mfma_f32_16x16x32_bf16 a[96:99], a[128:131], v[152:155], a[96:99]\n\t" \
  "ds_read_b128 a[244:247], v221 offset:12288\n\t" \
  "v_cvt_pk_bf16_f32 v145, v66, v67\n\t" \
  "v_mfma_f32_16x16x32_bf16 a[96:99], a[132:135], v[156:159], a[96:99]\n\t" \
  "v_cvt_pk_bf16_f32 v146, v68, v69\n\t" \
  "v_cvt_pk_bf16_f32 v147, v70, v71\n\t" \
  "v_mfma_f32_16x16x32_bf16 a[100:103], a[136:139], v[152:155], a[100:103]\n\t" \
  "buffer_load_dwordx4 v241, s[24:27], s96 offen offset:1024 lds\n\t" \
  "v_mfma_f32_16x16x32_bf16 a[100:103], a[140:143], v[156:159], a[100:103]\n\t" \
  "ds_read_b128 a[248:251], v222 offset:12288\n\t" \
  "v_cvt_pk_bf16_f32 v148, v72, v73\n\t" \
  "v_mfma_f32_16x16x32_bf16 a[104:107], a[144:147], v[152:155], a[104:107]\n\t" \
  "v_exp_f32 v99, v99\n\t" \
  "v_mfma_f32_16x16x32_bf16 a[104:107], a[148:151], v[156:159], a[104:107]\n\t" \
  "ds_read_b128 a[252:255], v223 offset:12288\n\t" \
  "v_cvt_pk_bf16_f32 v149, v74, v75\n\t" \
  "v_mfma_f32_16x16x32_bf16 a[108:111], a[152:155], v[152:155], a[108:111]\n\t" \
  "buffer_load_dwordx4 v248, s[24:27], s96 offen offset:2048 lds\n\t" \
  "v_mfma_f32_16x16x32_bf16 a[108:111], a[156:159], v[156:159], a[108:111]\n\t" \
  "v_exp_f32 v100, v100\n\t" \
  "v_mfma_f32_16x16x32_bf16 a[112:115], a[160:163], v[152:155], a[112:115]\n\t" \
  "v_exp_f32 v101, v101\n\t" \
  "v_mfma_f32_16x16x32_bf16 a[112:115], a[164:167], v[156:159], a[112:115]\n\t" \
  "v_exp_f32 v102, v102\n\t" \
  "v_mfma_f32_16x16x32_bf16 a[116:119], a[168:171], v[152:155], a[116:119]\n\t" \
  "buffer_load_dwordx4 v249, s[24:27], s96 offen offset:3072 lds\n\t" \
  "v_mfma_f32_16x16x32_bf16 a[116:119], a[172:175], v[156:159], a[116:119]\n\t" \
  "v_cvt_pk_bf16_f32 v150, v76, v77\n\t" \
  "v_cvt_pk_bf16_f32 v151, v78, v79\n\t" \
  "v_mfma_f32_16x16x32_bf16 a[120:123], a[176:179], v[152:155], a[120:123]\n\t" \
  "v_exp_f32 v103, v103\n\t" \
  "v_mfma_f32_16x16x32_bf16 a[120:123], a[180:183], v[156:159], a[120:123]\n\t" \
  "s_lshl1_add_u32 s99, s38, 0\n\t" \
  "v_exp_f32 v104, v104\n\t" \
  "v_mfma_f32_16x16x32_bf16 a[124:127], a[184:187], v[152:155], a[124:127]\n\t" \
  "v_exp_f32 v105, v105\n\t" \
  "v_mfma_f32_16x16x32_bf16 a[124:127], a[188:191], v[156:159], a[124:127]\n\t" \
  "v_exp_f32 v106, v106\n\t" \
  "v_mfma_f32_16x16x32_bf16 v[172:175], v[216:219], v[152:155], v[172:175]\n\t" \
  "buffer_load_dword v246, v212, s[28:31], s99 offen\n\t" \
  "v_mfma_f32_16x16x32_bf16 v[172:175], v[216:219], v[156:159], v[172:175]\n\t" \
  "v_cvt_pk_bf16_f32 v152, v96, v97\n\t" \
  "v_cvt_pk_bf16_f32 v153, v98, v99\n\t" \
  STA("s_memrealtime s[94:95]\n\t") \
  STA("s_memtime s[92:93]\n\t") \
  "v_mfma_f32_16x16x32_bf16 v[0:3], a[192:195], v[16:19], v[176:179]\n\t" \
  "ds_read_b64_tr_b16 a[128:129], v210 offset:0\n\t" \
  "v_cvt_pk_bf16_f32 v154, v100, v101\n\t" \
  "v_mfma_f32_16x16x32_bf16 v[0:3], a[196:199], v[20:23], v[0:3]\n\t" \
  "v_exp_f32 v107, v107\n\t" \
  "v_mfma_f32_16x16x32_bf16 v[0:3], a[200:203], v[24:27], v[0:3]\n\t" \
  "ds_read_b64_tr_b16 a[130:131], v210 offset:4096\n\t" \
  "v_cvt_pk_bf16_f32 v155, v102, v103\n\t" \
  "v_mfma_f32_16x16x32_bf16 v[0:3], a[204:207], v[28:31], v[0:3]\n\t" \
  "s_waitcnt lgkmcnt(10)\n\t" \
  "v_exp_f32 v108, v108\n\t" \
  "v_mfma_f32_16x16x32_bf16 v[4:7], a[220:223], v[28:31], v[176:179]\n\t" \
  "ds_read_b64_tr_b16 a[132:133], v210 offset:8192\n\t" \
  "v_lshrrev_b32 v250, 1, v233\n\t" \
  "v_mfma_f32_16x16x32_bf16 v[4:7], a[216:219], v[24:27], v[4:7]\n\t" \
  "v_lshl_add_u32 v214, v233, 14, s41\n\t" \
  "v_lshlrev_b32 v250, 2, v250\n\t" \
  "v_mfma_f32_16x16x32_bf16 v[4:7], a[212:215], v[20:23], v[4:7]\n\t" \
  "v_min_u32 v214, .Lfz_z-.Lat_t1-4, v214\n\t" \
  "v_add_u32 v250, 8192, v250\n\t" \
  "v_mfma_f32_16x16x32_bf16 v[4:7], a[208:211], v[16:19], v[4:7]\n\t" \
  "s_waitcnt lgkmcnt(7)\n\t" \
  "ds_read_b64_tr_b16 a[134:135], v210 offset:12288\n\t" \
  "v_cvt_pk_bf16_f32 v156, v104, v105\n\t" \
  "v_mfma_f32_16x16x32_bf16 v[8:11], a[224:227], v[16:19], v[176:179]\n\t" \
  "buffer_load_dword v209, v250, s[16:19], 0 offen sc1\n\t" \
  "v_mfma_f32_16x16x32_bf16 v[8:11], a[228:231], v[20:23], v[8:11]\n\t" \
  "s_getpc_b64 s[84:85]\n\t" \
  ".Lat_t1:\n\t" \
  "ds_read_b64_tr_b16 a[136:137], v211 offset:0\n\t" \
  "v_cvt_pk_bf16_f32 v157, v106, v107\n\t" \
  "v_mfma_f32_16x16x32_bf16 v[8:11], a[232:235], v[24:27], v[8:11]\n\t" \
  "global_load_dword v246, v214, s[84:85]\n\t" \
  "v_mfma_f32_16x16x32_bf16 v[8:11], a[236:239], v[28:31], v[8:11]\n\t" \
  "s_waitcnt lgkmcnt(5)\n\t" \
  "ds_read_b64_tr_b16 a[138:139], v211 offset:4096\n\t" \
  "s_lshl1_add_u32 s99, s38, 2048\n\t" \
  "v_mfma_f32_16x16x32_bf16 v[12:15], a[252:255], v[28:31], v[176:179]\n\t" \
  STA("v_writelane_b32 v255, s94, 46\n\t") \
  STA("v_writelane_b32 v255, s92, 47\n\t") \
  "buffer_load_dword v246, v212, s[28:31], s99 offen\n\t" \
  "v_mfma_f32_16x16x32_bf16 v[12:15], a[248:251], v[24:27], v[12:15]\n\t" \
  "v_exp_f32 v109, v109\n\t" \
  "v_mfma_f32_16x16x32_bf16 v[12:15], a[244:247], v[20:23], v[12:15]\n\t" \
  "ds_read_b64_tr_b16 a[140:141], v211 offset:8192\n\t" \
  "v_cvt_pk_bf16_f32 v158, v108, v109\n\t" \
  "v_mfma_f32_16x16x32_bf16 v[12:15], a[240:243], v[16:19], v[12:15]\n\t" \
  "v_exp_f32 v110, v110\n\t" \
  "v_mfma_f32_16x16x32_bf16 v[32:35], a[192:195], v[48:51], v[180:183]\n\t" \
  "s_add_u32 s96, s40, 0\n\t" \
  "v_exp_f32 v111, v111\n\t" \
  "v_mfma_f32_16x16x32_bf16 v[32:35], a[196:199], v[52:55], v[32:35]\n\t" \
  "s_and_b32 s96, s96, 0x1c0000\n\t" \
  "ds_read_b64_tr_b16 a[142:143], v211 offset:12288\n\t" \
  "v_cvt_pk_bf16_f32 v159, v110, v111\n\t" \
  "v_mfma_f32_16x16x32_bf16 v[32:35], a[200:203], v[56:59], v[32:35]\n\t" \
  "s_add_u32 s96, s96, s51\n\t" \
  "ds_read_b64_tr_b16 a[144:145], v210 offset:256\n\t" \
  "s_add_u32 m0, s38, 0\n\t" \
  "v_mfma_f32_16x16x32_bf16 v[32:35], a[204:207], v[60:63], v[32:35]\n\t" \
  "v_exp_f32 v0, v0\n\t" \
  "v_mfma_f32_16x16x32_bf16 v[36:39], a[220:223], v[60:63], v[180:183]\n\t" \
  "buffer_load_dwordx4 v240, s[24:27], s96 offen offset:0 lds\n\t" \
  "v_mfma_f32_16x16x32_bf16 v[36:39], a[216:219], v[56:59], v[36:39]\n\t" \
  "ds_read_b64_tr_b16 a[146:147], v210 offset:4352\n\t" \
  "v_mfma_f32_16x16x32_bf16 v[36:39], a[212:215], v[52:55], v[36:39]\n\t" \
  "ds_read_b64_tr_b16 a[148:149], v210 offset:8448\n\t" \
  "v_mfma_f32_16x16x32_bf16 v[36:39], a[208:211], v[48:51], v[36:39]\n\t" \
  "ds_read_b64_tr_b16 a[150:151], v210 offset:12544\n\t" \
  "v_mfma_f32_16x16x32_bf16 v[40:43], a[224:227], v[48:51], v[180:183]\n\t" \
  "v_exp_f32 v1, v1\n\t" \
  "v_mfma_f32_16x16x32_bf16 v[40:43], a[228:231], v[52:55], v[40:43]\n\t" \
  "ds_read_b64_tr_b16 a[152:153], v211 offset:256\n\t" \
  "v_mfma_f32_16x16x32_bf16 v[40:43], a[232:235], v[56:59], v[40:43]\n\t" \
  "v_exp_f32 v2, v2\n\t" \
  "v_mfma_f32_16x16x32_bf16 v[40:43], a[236:239], v[60:63], v[40:43]\n\t" \
  "ds_read_b64_tr_b16 a[154:155], v211 offset:4352\n\t" \
  "v_mfma_f32_16x16x32_bf16 v[44:47], a[252:255], v[60:63], v[180:183]\n\t" \
  "v_exp_f32 v3, v3\n\t" \
  "v_mfma_f32_16x16x32_bf16 v[44:47], a[248:251], v[56:59], v[44:47]\n\t" \
  "ds_read_b64_tr_b16 a[156:157], v211 offset:8448\n\t" \
  "v_mfma_f32_16x16x32_bf16 v[44:47], a[244:247], v[52:55], v[44:47]\n\t" \
  "buffer_load_dwordx4 v241, s[24:27], s96 offen offset:1024 lds\n\t" \
  "v_mfma_f32_16x16x32_bf16 v[44:47], a[240:243], v[48:51], v[44:47]\n\t" \
  "ds_read_b64_tr_b16 a[158:159], v211 offset:12544\n\t" \
  "v_mfma_f32_16x16x32_bf16 v[64:67], a[192:195], v[80:83], v[184:187]\n\t" \
  "ds_read_b64_tr_b16 a[160:161], v210 offset:512\n\t" \
  "v_mfma_f32_16x16x32_bf16 v[64:67], a[196:199], v[84:87], v[64:67]\n\t" \
  "v_exp_f32 v4, v4\n\t" \
  "v_mfma_f32_16x16x32_bf16 v[64:67], a[200:203], v[88:91], v[64:67]\n\t" \
  "ds_read_b64_tr_b16 a[162:163], v210 offset:4608\n\t" \
  "v_mfma_f32_16x16x32_bf16 v[64:67], a[204:207], v[92:95], v[64:67]\n\t" \
  "ds_read_b64_tr_b16 a[164:165], v210 offset:8704\n\t" \
  "v_mfma_f32_16x16x32_bf16 v[68:71], a[220:223], v[92:95], v[184:187]\n\t" \
  "v_exp_f32 v5, v5\n\t" \
  "v_mfma_f32_16x16x32_bf16 v[68:71], a[216:219], v[88:91], v[68:71]\n\t" \
  "ds_read_b64_tr_b16 a[166:167], v210 offset:12800\n\t" \
  "v_mfma_f32_16x16x32_bf16 v[68:71], a[212:215], v[84:87], v[68:71]\n\t" \
  "v_exp_f32 v6, v6\n\t" \
  "v_mfma_f32_16x16x32_bf16 v[68:71], a[208:211], v[80:83], v[68:71]\n\t" \
  "ds_read_b64_tr_b16 a[168:169], v211 offset:512\n\t" \
  "v_mfma_f32_16x16x32_bf16 v[72:75], a[224:227], v[80:83], v[184:187]\n\t" \
  "buffer_load_dwordx4 v248, s[24:27], s96 offen offset:2048 lds\n\t" \
  "v_mfma_f32_16x16x32_bf16 v[72:75], a[228:231], v[84:87], v[72:75]\n\t" \
  "ds_read_b64_tr_b16 a[170:171], v211 offset:4608\n\t" \
  "v_mfma_f32_16x16x32_bf16 v[72:75], a[232:235], v[88:91], v[72:75]\n\t" \
  "ds_read_b64_tr_b16 a[172:173], v211 offset:8704\n\t" \
  "v_mfma_f32_16x16x32_bf16 v[72:75], a[236:239], v[92:95], v[72:75]\n\t" \
  "v_exp_f32 v7, v7\n\t" \
  "v_mfma_f32_16x16x32_bf16 v[76:79], a[252:255], v[92:95], v[184:187]\n\t" \
  "ds_read_b64_tr_b16 a[174:175], v211 offset:12800\n\t" \
  "v_mfma_f32_16x16x32_bf16 v[76:79], a[248:251], v[88:91], v[76:79]\n\t" \
  "v_exp_f32 v8, v8\n\t" \
  "v_mfma_f32_16x16x32_bf16 v[76:79], a[244:247], v[84:87], v[76:79]\n\t" \
  "ds_read_b64_tr_b16 a[176:177], v210 offset:768\n\t" \
  "v_mfma_f32_16x16x32_bf16 v[76:79], a[240:243], v[80:83], v[76:79]\n\t" \
  "ds_read_b64_tr_b16 a[178:179], v210 offset:4864\n\t" \
  "v_mfma_f32_16x16x32_bf16 v[96:99], a[192:195], v[112:115], v[188:191]\n\t" \
  "v_exp_f32 v9, v9\n\t" \
  "v_mfma_f32_16x16x32_bf16 v[96:99], a[196:199], v[116:119], v[96:99]\n\t" \
  "ds_read_b64_tr_b16 a[180:181], v210 offset:8960\n\t" \
  "v_mfma_f32_16x16x32_bf16 v[96:99], a[200:203], v[120:123], v[96:99]\n\t" \
  "buffer_load_dwordx4 v249, s[24:27], s96 offen offset:3072 lds\n\t" \
  "v_mfma_f32_16x16x32_bf16 v[96:99], a[204:207], v[124:127], v[96:99]\n\t" \
  "ds_read_b64_tr_b16 a[182:183], v210 offset:13056\n\t" \
  "v_mfma_f32_16x16x32_bf16 v[100:103], a[220:223], v[124:127], v[188:191]\n\t" \
  "v_exp_f32 v10, v10\n\t" \
  "v_mfma_f32_16x16x32_bf16 v[100:103], a[216:219], v[120:123], v[100:103]\n\t" \
  "ds_read_b64_tr_b16 a[184:185], v211 offset:768\n\t" \
  "v_mfma_f32_16x16x32_bf16 v[100:103], a[212:215], v[116:119], v[100:103]\n\t" \
  "ds_read_b64_tr_b16 a[186:187], v211 offset:4864\n\t" \
  "v_mfma_f32_16x16x32_bf16 v[100:103], a[208:211], v[112:115], v[100:103]\n\t" \
  "v_exp_f32 v11, v11\n\t" \
  "v_mfma_f32_16x16x32_bf16 v[104:107], a[224:227], v[112:115], v[188:191]\n\t" \
  "ds_read_b64_tr_b16 a[188:189], v211 offset:8960\n\t" \
  "v_mfma_f32_16x16x32_bf16 v[104:107], a[228:231], v[116:119], v[104:107]\n\t" \
  "v_exp_f32 v12, v12\n\t" \
  "v_mfma_f32_16x16x32_bf16 v[104:107], a[232:235], v[120:123], v[104:107]\n\t" \
  "ds_read_b64_tr_b16 a[190:191], v211 offset:13056\n\t" \
  "v_mfma_f32_16x16x32_bf16 v[104:107], a[236:239], v[124:127], v[104:107]\n\t" \
  "v_exp_f32 v13, v13\n\t" \
  "v_mfma_f32_16x16x32_bf16 v[108:111], a[252:255], v[124:127], v[188:191]\n\t" \
  "v_exp_f32 v14, v14\n\t" \
  "v_mfma_f32_16x16x32_bf16 v[108:111], a[248:251], v[120:123], v[108:111]\n\t" \
  "v_exp_f32 v15, v15\n\t" \
  "v_mfma_f32_16x16x32_bf16 v[108:111], a[244:247], v[116:119], v[108:111]\n\t" \
  "s_waitcnt vmcnt(12) lgkmcnt(0)\n\t" \
  "v_exp_f32 v32, v32\n\t" \
  "v_mfma_f32_16x16x32_bf16 v[108:111], a[240:243], v[112:115], v[108:111]\n\t" \
  "s_barrier\n\t" \
  STA("s_memrealtime s[94:95]\n\t") \
  STA("s_memtime s[92:93]\n\t") \
  "v_mfma_f32_16x16x32_bf16 a[0:3], a[128:131], v[128:131], a[0:3]\n\t" \
  "v_exp_f32 v33, v33\n\t" \
  "v_mfma_f32_16x16x32_bf16 a[0:3], a[132:135], v[132:135], a[0:3]\n\t" \
  "v_exp_f32 v34, v34\n\t" \
  "v_mfma_f32_16x16x32_bf16 a[4:7], a[136:139], v[128:131], a[4:7]\n\t" \
  "v_exp_f32 v35, v35\n\t" \
  "v_mfma_f32_16x16x32_bf16 a[4:7], a[140:143], v[132:135], a[4:7]\n\t" \
  "v_exp_f32 v36, v36\n\t" \
  "v_mfma_f32_16x16x32_bf16 a[8:11], a[144:147], v[128:131], a[8:11]\n\t" \
  "v_exp_f32 v37, v37\n\t" \
  "v_mfma_f32_16x16x32_bf16 a[8:11], a[148:151], v[132:135], a[8:11]\n\t" \
  "v_exp_f32 v38, v38\n\t" \
  "v_mfma_f32_16x16x32_bf16 a[12:15], a[152:155], v[128:131], a[12:15]\n\t" \
  "v_exp_f32 v39, v39\n\t" \
  "v_mfma_f32_16x16x32_bf16 a[12:15], a[156:159], v[132:135], a[12:15]\n\t" \
  "v_exp_f32 v40, v40\n\t" \
  "v_mfma_f32_16x16x32_bf16 a[16:19], a[160:163], v[128:131], a[16:19]\n\t" \
  "v_exp_f32 v41, v41\n\t" \
  "v_mfma_f32_16x16x32_bf16 a[16:19], a[164:167], v[132:135], a[16:19]\n\t" \
  "v_exp_f32 v42, v42\n\t" \
  "v_mfma_f32_16x16x32_bf16 a[20:23], a[168:171], v[128:131], a[20:23]\n\t" \
  "v_exp_f32 v43, v43\n\t" \
  "v_mfma_f32_16x16x32_bf16 a[20:23], a[172:175], v[132:135], a[20:23]\n\t" \
  "v_exp_f32 v44, v44\n\t" \
  "v_mfma_f32_16x16x32_bf16 a[24:27], a[176:179], v[128:131], a[24:27]\n\t" \
  STA("v_writelane_b32 v255, s94, 48\n\t") \
  STA("v_writelane_b32 v255, s92, 49\n\t") \
  "v_exp_f32 v45, v45\n\t" \
  "v_mfma_f32_16x16x32_bf16 a[24:27], a[180:183], v[132:135], a[24:27]\n\t" \
  "v_exp_f32 v46, v46\n\t" \
  "v_mfma_f32_16x16x32_bf16 a[28:31], a[184:187], v[128:131], a[28:31]\n\t" \
  "v_exp_f32 v47, v47\n\t" \
  "v_mfma_f32_16x16x32_bf16 a[28:31], a[188:191], v[132:135], a[28:31]\n\t" \
  "v_exp_f32 v64, v64\n\t" \
  "v_mfma_f32_16x16x32_bf16 v[160:163], v[216:219], v[128:131], v[160:163]\n\t" \
  "s_add_u32 m0, s38, 147456\n\t" \
  "v_exp_f32 v65, v65\n\t" \
  "v_mfma_f32_16x16x32_bf16 v[160:163], v[216:219], v[132:135], v[160:163]\n\t" \
  "s_add_u32 s96, s38, 114688\n\t" \
  "ds_read_b128 a[192:195], v220 offset:16384\n\t" \
  "v_cvt_pk_bf16_f32 v128, v0, v1\n\t" \
  "v_mfma_f32_16x16x32_bf16 a[32:35], a[128:131], v[136:139], a[32:35]\n\t" \
  "v_exp_f32 v66, v66\n\t" \
  "v_mfma_f32_16x16x32_bf16 a[32:35], a[132:135], v[140:143], a[32:35]\n\t" \
  "ds_read_b128 a[196:199], v221 offset:16384\n\t" \
  "v_cvt_pk_bf16_f32 v129, v2, v3\n\t" \
  "v_mfma_f32_16x16x32_bf16 a[36:39], a[136:139], v[136:139], a[36:39]\n\t" \
  "buffer_load_dwordx4 v230, s[64:67], s96 offen offset:0 lds\n\t" \
  "v_mfma_f32_16x16x32_bf16 a[36:39], a[140:143], v[140:143], a[36:39]\n\t" \
  "v_exp_f32 v67, v67\n\t" \
  "v_mfma_f32_16x16x32_bf16 a[40:43], a[144:147], v[136:139], a[40:43]\n\t" \
  "ds_read_b128 a[200:203], v222 offset:16384\n\t" \
  "v_cvt_pk_bf16_f32 v130, v4, v5\n\t" \
  "v_mfma_f32_16x16x32_bf16 a[40:43], a[148:151], v[140:143], a[40:43]\n\t" \
  "v_exp_f32 v68, v68\n\t" \
  "v_mfma_f32_16x16x32_bf16 a[44:47], a[152:155], v[136:139], a[44:47]\n\t" \
  "buffer_load_dwordx4 v231, s[64:67], s96 offen offset:1024 lds\n\t" \
  "v_mfma_f32_16x16x32_bf16 a[44:47], a[156:159], v[140:143], a[44:47]\n\t" \
  "ds_read_b128 a[204:207], v223 offset:16384\n\t" \
  "v_cvt_pk_bf16_f32 v131, v6, v7\n\t" \
  "v_mfma_f32_16x16x32_bf16 a[48:51], a[160:163], v[136:139], a[48:51]\n\t" \
  "v_exp_f32 v69, v69\n\t" \
  "v_mfma_f32_16x16x32_bf16 a[48:51], a[164:167], v[140:143], a[48:51]\n\t" \
  "ds_read_b128 a[208:211], v220 offset:20480\n\t" \
  "v_cvt_pk_bf16_f32 v132, v8, v9\n\t" \
  "v_mfma_f32_16x16x32_bf16 a[52:55], a[168:171], v[136:139], a[52:55]\n\t" \
  "buffer_load_dwordx4 v230, s[64:67], s96 offen offset:2048 lds\n\t" \
  "v_mfma_f32_16x16x32_bf16 a[52:55], a[172:175], v[140:143], a[52:55]\n\t" \
  "v_exp_f32 v70, v70\n\t" \
  "v_mfma_f32_16x16x32_bf16 a[56:59], a[176:179], v[136:139], a[56:59]\n\t" \
  "ds_read_b128 a[212:215], v221 offset:20480\n\t" \
  "v_cvt_pk_bf16_f32 v133, v10, v11\n\t" \
  "v_mfma_f32_16x16x32_bf16 a[56:59], a[180:183], v[140:143], a[56:59]\n\t" \
  ".Lhs9:\n\ts_cbranch_execz .Lhs9n\n\t" \
  "v_exp_f32 v71, v71\n\t" \
  "v_mfma_f32_16x16x32_bf16 a[60:63], a[184:187], v[136:139], a[60:63]\n\t" \
  "buffer_load_dwordx4 v231, s[64:67], s96 offen offset:3072 lds\n\t" \
  "v_mfma_f32_16x16x32_bf16 a[60:63], a[188:191], v[140:143], a[60:63]\n\t" \
  "s_add_u32 s96, s40, 0\n\t" \
  "v_exp_f32 v72, v72\n\t" \
  "v_mfma_f32_16x16x32_bf16 v[164:167], v[216:219], v[136:139], v[164:167]\n\t" \
  "s_and_b32 s96, s96, 0x1c0000\n\t" \
  "s_add_u32 s96, s96, s77\n\t" \
  "s_add_u32 m0, s38, 98304\n\t" \
  "v_mfma_f32_16x16x32_bf16 v[164:167], v[216:219], v[140:143], v[164:167]\n\t" \
  "ds_read_b128 a[216:219], v222 offset:20480\n\t" \
  "v_cvt_pk_bf16_f32 v134, v12, v13\n\t" \
  "v_mfma_f32_16x16x32_bf16 a[64:67], a[128:131], v[144:147], a[64:67]\n\t" \
  "buffer_load_dwordx4 v240, s[24:27], s96 offen offset:0 lds\n\t" \
  "v_mfma_f32_16x16x32_bf16 a[64:67], a[132:135], v[148:151], a[64:67]\n\t" \
  "ds_read_b128 a[220:223], v223 offset:20480\n\t" \
  "v_cvt_pk_bf16_f32 v135, v14, v15\n\t" \
  "v_mfma_f32_16x16x32_bf16 a[68:71], a[136:139], v[144:147], a[68:71]\n\t" \
  "v_exp_f32 v73, v73\n\t" \
  "v_mfma_f32_16x16x32_bf16 a[68:71], a[140:143], v[148:151], a[68:71]\n\t" \
  "v_exp_f32 v74, v74\n\t" \
  "v_mfma_f32_16x16x32_bf16 a[72:75], a[144:147], v[144:147], a[72:75]\n\t" \
  "buffer_load_dwordx4 v241, s[24:27], s96 offen offset:1024 lds\n\t" \
  "v_mfma_f32_16x16x32_bf16 a[72:75], a[148:151], v[148:151], a[72:75]\n\t" \
  "s_waitcnt vmcnt(12)\n\t" \
  "v_and_b32 v251, 0xbfffbfff, v209\n\t" \
  "v_cmp_ne_u32_e64 s[84:85], s36, v251\n\t" \
  "v_mfma_f32_16x16x32_bf16 a[76:79], a[152:155], v[144:147], a[76:79]\n\t" \
  "s_cmp_lg_u64 s[84:85], 0\n\t" \
  "s_cbranch_scc1 .Lat_v\n\t" \
  ".Lat_w:\n\t" \
  "v_mfma_f32_16x16x32_bf16 a[76:79], a[156:159], v[148:151], a[76:79]\n\t" \
  "v_and_b32 v251, 1, v233\n\t" \
  "v_lshlrev_b32 v251, 4, v251\n\t" \
  "v_mfma_f32_16x16x32_bf16 a[80:83], a[160:163], v[144:147], a[80:83]\n\t" \
  "buffer_load_dwordx4 v248, s[24:27], s96 offen offset:2048 lds\n\t" \
  "v_mfma_f32_16x16x32_bf16 a[80:83], a[164:167], v[148:151], a[80:83]\n\t" \
  "v_lshrrev_b32 v251, v251, v209\n\t" \
  "v_and_b32 v251, 0x4000, v251\n\t" \
  "v_mfma_f32_16x16x32_bf16 a[84:87], a[168:171], v[144:147], a[84:87]\n\t" \
  "ds_read_b128 a[224:227], v220 offset:24576\n\t" \
  "v_cmp_ne_u32_e64 s[84:85], 0, v251\n\t" \
  STS("s_nop 4\n\t") \
  STS("v_writelane_b32 v255, s84, 60\n\t") \
  STS("v_writelane_b32 v255, s85, 61\n\t") \
  "v_mfma_f32_16x16x32_bf16 a[84:87], a[172:175], v[148:151], a[84:87]\n\t" \
  "s_bcnt1_i32_b32 s98, s84\n\t" \
  "ds_read_b128 a[228:231], v221 offset:24576\n\t" \
  "s_bcnt1_i32_b32 s54, s85\n\t" \
  "v_mfma_f32_16x16x32_bf16 a[88:91], a[176:179], v[144:147], a[88:91]\n\t" \
  "buffer_load_dwordx4 v249, s[24:27], s96 offen offset:3072 lds\n\t" \
  "v_mfma_f32_16x16x32_bf16 a[88:91], a[180:183], v[148:151], a[88:91]\n\t" \
  "s_cmp_gt_u32 s98, s54\n\t" \
  "s_cselect_b32 s55, 0x2000, 0\n\t" \
  "s_cselect_b32 s98, s84, s85\n\t" \
  "v_mfma_f32_16x16x32_bf16 a[92:95], a[184:187], v[144:147], a[92:95]\n\t" \
  "s_add_u32 s96, s40, 262144\n\t" \
  "s_and_b32 s96, s96, 0x1c0000\n\t" \
  "s_add_u32 s96, s96, s51\n\t" \
  "v_mfma_f32_16x16x32_bf16 a[92:95], a[188:191], v[148:151], a[92:95]\n\t" \
  "s_add_u32 m0, s38, 49152\n\t" \
  "s_cmp_lg_u32 s55, 0\n\t" \
  "s_cselect_b32 s84, s85, s84\n\t" \
  "v_mfma_f32_16x16x32_bf16 v[168:171], v[216:219], v[144:147], v[168:171]\n\t" \
  "s_lshr_b32 s85, s98, 16\n\t" \
  "s_bcnt1_i32_b32 s85, s85\n\t" \
  "s_lshr_b32 s84, s84, 16\n\t" \
  "v_mfma_f32_16x16x32_bf16 v[168:171], v[216:219], v[148:151], v[168:171]\n\t" \
  "buffer_load_dwordx4 v240, s[24:27], s96 offen offset:0 lds\n\t" \
  "v_mfma_f32_16x16x32_bf16 a[96:99], a[128:131], v[152:155], a[96:99]\n\t" \
  "s_bcnt1_i32_b32 s84, s84\n\t" \
  "ds_read_b128 a[232:235], v222 offset:24576\n\t" \
  "s_add_u32 s84, s84, 7\n\t" \
  "v_mfma_f32_16x16x32_bf16 a[96:99], a[132:135], v[156:159], a[96:99]\n\t" \
  "s_cmp_ge_u32 s85, s84\n\t" \
  "s_cselect_b32 s54, -1, 0\n\t" \
  "s_xor_b32 s98, s42, s55\n\t" \
  "v_mfma_f32_16x16x32_bf16 a[100:103], a[136:139], v[152:155], a[100:103]\n\t" \
  "s_and_b32 s98, s98, s54\n\t" \
  "s_xor_b32 s45, s45, s98\n\t" \
  "s_xor_b32 s46, s46, s98\n\t" \
  STS("v_writelane_b32 v255, s54, 57\n\t") \
  STS("v_writelane_b32 v255, s55, 59\n\t") \
  "v_mfma_f32_16x16x32_bf16 a[100:103], a[140:143], v[156:159], a[100:103]\n\t" \
  "buffer_load_dwordx4 v241, s[24:27], s96 offen offset:1024 lds\n\t" \
  "v_mfma_f32_16x16x32_bf16 a[104:107], a[144:147], v[152:155], a[104:107]\n\t" \
  "s_xor_b32 s98, s43, s55\n\t" \
  "s_and_b32 s98, s98, s54\n\t" \
  "s_xor_b32 s47, s47, s98\n\t" \
  "v_mfma_f32_16x16x32_bf16 a[104:107], a[148:151], v[156:159], a[104:107]\n\t" \
  "s_xor_b32 s48, s48, s55\n\t" \
  "ds_read_b128 a[236:239], v223 offset:24576\n\t" \
  "s_xor_b32 s49, s49, s55\n\t" \
  "v_mfma_f32_16x16x32_bf16 a[108:111], a[152:155], v[152:155], a[108:111]\n\t" \
  "buffer_load_dwordx4 v248, s[24:27], s96 offen offset:2048 lds\n\t" \
  "v_mfma_f32_16x16x32_bf16 a[108:111], a[156:159], v[156:159], a[108:111]\n\t" \
  "s_xor_b32 s98, s45, 0\n\t" \
  "ds_read_b128 a[240:243], v220 offset:28672\n\t" \
  "v_exp_f32 v75, v75\n\t" \
  "v_mfma_f32_16x16x32_bf16 a[112:115], a[160:163], v[152:155], a[112:115]\n\t" \
  "s_lshl_b32 s64, s39, 17\n\t" \
  "ds_read_b128 a[244:247], v221 offset:28672\n\t" \
  "v_exp_f32 v76, v76\n\t" \
  "v_mfma_f32_16x16x32_bf16 a[112:115], a[164:167], v[156:159], a[112:115]\n\t" \
  "s_bfe_u32 s70, s46, 0x20014\n\t" \
  "ds_read_b128 a[248:251], v222 offset:28672\n\t" \
  "v_exp_f32 v77, v77\n\t" \
  "v_mfma_f32_16x16x32_bf16 a[116:119], a[168:171], v[152:155], a[116:119]\n\t" \
  "buffer_load_dwordx4 v249, s[24:27], s96 offen offset:3072 lds\n\t" \
  "v_mfma_f32_16x16x32_bf16 a[116:119], a[172:175], v[156:159], a[116:119]\n\t" \
  "s_lshl_b32 s70, s70, 14\n\t" \
  "ds_read_b128 a[252:255], v223 offset:28672\n\t" \
  "v_exp_f32 v78, v78\n\t" \
  "v_mfma_f32_16x16x32_bf16 a[120:123], a[176:179], v[152:155], a[120:123]\n\t" \
  "s_xor_b32 s67, s70, 49152\n\t" \
  "v_cvt_pk_bf16_f32 v136, v32, v33\n\t" \
  "v_exp_f32 v79, v79\n\t" \
  "v_mfma_f32_16x16x32_bf16 a[120:123], a[180:183], v[156:159], a[120:123]\n\t" \
  "s_lshl1_add_u32 s60, s67, s46\n\t" \
  "v_exp_f32 v96, v96\n\t" \
  "v_mfma_f32_16x16x32_bf16 a[124:127], a[184:187], v[152:155], a[124:127]\n\t" \
  "s_lshl1_add_u32 s99, s38, 4096\n\t" \
  "v_exp_f32 v97, v97\n\t" \
  "v_mfma_f32_16x16x32_bf16 a[124:127], a[188:191], v[156:159], a[124:127]\n\t" \
  "s_xor_b32 s68, s70, 32768\n\t" \
  "v_exp_f32 v98, v98\n\t" \
  "v_mfma_f32_16x16x32_bf16 v[172:175], v[216:219], v[152:155], v[172:175]\n\t" \
  "buffer_load_dword v246, v212, s[28:31], s99 offen\n\t" \
  "v_mfma_f32_16x16x32_bf16 v[172:175], v[216:219], v[156:159], v[172:175]\n\t" \
  "s_waitcnt lgkmcnt(12)\n\t" \
  "v_exp_f32 v99, v99\n\t" \
  STA("s_memrealtime s[94:95]\n\t") \
  STA("s_memtime s[92:93]\n\t") \
  "v_mfma_f32_16x16x32_bf16 v[0:3], a[192:195], v[16:19], v[176:179]\n\t" \
  "s_lshl1_add_u32 s61, s68, s46\n\t" \
  "ds_read_b64_tr_b16 a[128:129], v228 offset:0\n\t" \
  "v_cvt_pk_bf16_f32 v137, v34, v35\n\t" \
  "v_mfma_f32_16x16x32_bf16 v[0:3], a[196:199], v[20:23], v[0:3]\n\t" \
  "s_xor_b32 s69, s70, 16384\n\t" \
  "ds_read_b64_tr_b16 a[130:131], v228 offset:4096\n\t" \
  "v_cvt_pk_bf16_f32 v138, v36, v37\n\t" \
  "v_mfma_f32_16x16x32_bf16 v[0:3], a[200:203], v[24:27], v[0:3]\n\t" \
  "s_lshl1_add_u32 s62, s69, s46\n\t" \
  "ds_read_b64_tr_b16 a[132:133], v228 offset:8192\n\t" \
  "v_exp_f32 v100, v100\n\t" \
  "v_mfma_f32_16x16x32_bf16 v[0:3], a[204:207], v[28:31], v[0:3]\n\t" \
  "s_waitcnt lgkmcnt(11)\n\t" \
  "ds_read_b64_tr_b16 a[134:135], v228 offset:12288\n\t" \
  "v_cvt_pk_bf16_f32 v139, v38, v39\n\t" \
  "v_mfma_f32_16x16x32_bf16 v[4:7], a[220:223], v[28:31], v[176:179]\n\t" \
  "s_lshl1_add_u32 s63, s70, s46\n\t" \
  "ds_read_b64_tr_b16 a[136:137], v229 offset:0\n\t" \
  "v_cvt_pk_bf16_f32 v140, v40, v41\n\t" \
  "v_mfma_f32_16x16x32_bf16 v[4:7], a[216:219], v[24:27], v[4:7]\n\t" \
  "s_mul_i32 s65, s39, 508\n\t" \
  "ds_read_b64_tr_b16 a[138:139], v229 offset:4096\n\t" \
  "v_exp_f32 v101, v101\n\t" \
  "v_mfma_f32_16x16x32_bf16 v[4:7], a[212:215], v[20:23], v[4:7]\n\t" \
  "s_add_u32 s65, s65, s49\n\t" \
  "ds_read_b64_tr_b16 a[140:141], v229 offset:8192\n\t" \
  "v_cvt_pk_bf16_f32 v141, v42, v43\n\t" \
  "v_mfma_f32_16x16x32_bf16 v[4:7], a[208:211], v[16:19], v[4:7]\n\t" \
  "s_waitcnt lgkmcnt(11)\n\t" \
  "ds_read_b64_tr_b16 a[142:143], v229 offset:12288\n\t" \
  "v_cvt_pk_bf16_f32 v142, v44, v45\n\t" \
  "v_mfma_f32_16x16x32_bf16 v[8:11], a[224:227], v[16:19], v[176:179]\n\t" \
  "s_lshl_b32 s80, s39, 2\n\t" \
  "v_exp_f32 v102, v102\n\t" \
  "v_mfma_f32_16x16x32_bf16 v[8:11], a[228:231], v[20:23], v[8:11]\n\t" \
  "s_add_u32 s80, s80, s3\n\t" \
  "v_exp_f32 v103, v103\n\t" \
  "v_mfma_f32_16x16x32_bf16 v[8:11], a[232:235], v[24:27], v[8:11]\n\t" \
  "s_lshl1_add_u32 s99, s38, 6144\n\t" \
  "v_exp_f32 v104, v104\n\t" \
  "v_mfma_f32_16x16x32_bf16 v[8:11], a[236:239], v[28:31], v[8:11]\n\t" \
  "s_waitcnt lgkmcnt(8)\n\t" \
  "v_exp_f32 v105, v105\n\t" \
  "v_mfma_f32_16x16x32_bf16 v[12:15], a[252:255], v[28:31], v[176:179]\n\t" \
  STA("v_writelane_b32 v255, s94, 50\n\t") \
  STA("v_writelane_b32 v255, s92, 51\n\t") \
  "buffer_load_dword v246, v212, s[28:31], s99 offen\n\t" \
  "v_mfma_f32_16x16x32_bf16 v[12:15], a[248:251], v[24:27], v[12:15]\n\t" \
  "s_barrier\n\t" \
  "v_mfma_f32_16x16x32_bf16 v[12:15], a[244:247], v[20:23], v[12:15]\n\t" \
  "s_lshl_b32 s80, s80, 6\n\t" \
  "ds_read_b64_tr_b16 a[144:145], v228 offset:256\n\t" \
  "v_cvt_pk_bf16_f32 v143, v46, v47\n\t" \
  "v_mfma_f32_16x16x32_bf16 v[12:15], a[240:243], v[16:19], v[12:15]\n\t" \
  "s_add_u32 s76, s38, 147456\n\t" \
  "ds_read_b64_tr_b16 a[146:147], v228 offset:4352\n\t" \
  "v_cvt_pk_bf16_f32 v144, v64, v65\n\t" \
  "v_mfma_f32_16x16x32_bf16 v[32:35], a[192:195], v[48:51], v[180:183]\n\t" \
  "s_lshl_b32 s75, s80, 2\n\t" \
  "v_exp_f32 v106, v106\n\t" \
  "v_mfma_f32_16x16x32_bf16 v[32:35], a[196:199], v[52:55], v[32:35]\n\t" \
  "s_add_u32 s75, s75, 2228224\n\t" \
  "ds_read_b64_tr_b16 a[148:149], v228 offset:8448\n\t" \
  "v_cvt_pk_bf16_f32 v145, v66, v67\n\t" \
  "v_mfma_f32_16x16x32_bf16 v[32:35], a[200:203], v[56:59], v[32:35]\n\t" \
  "s_add_u32 s96, s40, 262144\n\t" \
  "s_and_b32 s96, s96, 0x1c0000\n\t" \
  "s_add_u32 s96, s96, s77\n\t" \
  "v_mfma_f32_16x16x32_bf16 v[32:35], a[204:207], v[60:63], v[32:35]\n\t" \
  "s_add_u32 m0, s38, 65536\n\t" \
  "ds_read_b64_tr_b16 a[150:151], v228 offset:12544\n\t" \
  "v_cvt_pk_bf16_f32 v146, v68, v69\n\t" \
  "v_mfma_f32_16x16x32_bf16 v[36:39], a[220:223], v[60:63], v[180:183]\n\t" \
  "buffer_load_dwordx4 v240, s[24:27], s96 offen offset:0 lds\n\t" \
  "v_mfma_f32_16x16x32_bf16 v[36:39], a[216:219], v[56:59], v[36:39]\n\t" \
  "s_lshr_b32 s71, s67, 7\n\t" \
  "ds_read_b64_tr_b16 a[152:153], v229 offset:256\n\t" \
  "v_exp_f32 v107, v107\n\t" \
  "v_mfma_f32_16x16x32_bf16 v[36:39], a[212:215], v[52:55], v[36:39]\n\t" \
  "s_add_u32 s71, s71, s48\n\t" \
  "ds_read_b64_tr_b16 a[154:155], v229 offset:4352\n\t" \
  "v_exp_f32 v108, v108\n\t" \
  "v_mfma_f32_16x16x32_bf16 v[36:39], a[208:211], v[48:51], v[36:39]\n\t" \
  "s_lshr_b32 s72, s68, 7\n\t" \
  "ds_read_b64_tr_b16 a[156:157], v229 offset:8448\n\t" \
  "v_cvt_pk_bf16_f32 v147, v70, v71\n\t" \
  "v_mfma_f32_16x16x32_bf16 v[40:43], a[224:227], v[48:51], v[180:183]\n\t" \
  "s_add_u32 s72, s72, s48\n\t" \
  "v_cvt_pk_bf16_f32 v148, v72, v73\n\t" \
  "v_exp_f32 v109, v109\n\t" \
  "v_mfma_f32_16x16x32_bf16 v[40:43], a[228:231], v[52:55], v[40:43]\n\t" \
  "s_lshr_b32 s66, s69, 7\n\t" \
  "v_exp_f32 v110, v110\n\t" \
  "v_mfma_f32_16x16x32_bf16 v[40:43], a[232:235], v[56:59], v[40:43]\n\t" \
  "s_add_u32 s66, s66, s48\n\t" \
  "ds_read_b64_tr_b16 a[158:159], v229 offset:12544\n\t" \
  "v_cvt_pk_bf16_f32 v149, v74, v75\n\t" \
  "v_mfma_f32_16x16x32_bf16 v[40:43], a[236:239], v[60:63], v[40:43]\n\t" \
  "s_lshr_b32 s73, s70, 7\n\t" \
  "ds_read_b64_tr_b16 a[160:161], v228 offset:512\n\t" \
  "v_cvt_pk_bf16_f32 v150, v76, v77\n\t" \
  "v_mfma_f32_16x16x32_bf16 v[44:47], a[252:255], v[60:63], v[180:183]\n\t" \
  "s_add_u32 s73, s73, s48\n\t" \
  "v_exp_f32 v111, v111\n\t" \
  "v_mfma_f32_16x16x32_bf16 v[44:47], a[248:251], v[56:59], v[44:47]\n\t" \
  "s_lshl3_add_u32 s67, s67, s47\n\t" \
  "v_exp_f32 v0, v0\n\t" \
  "v_mfma_f32_16x16x32_bf16 v[44:47], a[244:247], v[52:55], v[44:47]\n\t" \
  "buffer_load_dwordx4 v241, s[24:27], s96 offen offset:1024 lds\n\t" \
  "v_mfma_f32_16x16x32_bf16 v[44:47], a[240:243], v[48:51], v[44:47]\n\t" \
  "s_lshl3_add_u32 s68, s68, s47\n\t" \
  "ds_read_b64_tr_b16 a[162:163], v228 offset:4608\n\t" \
  "v_cvt_pk_bf16_f32 v151, v78, v79\n\t" \
  "v_mfma_f32_16x16x32_bf16 v[64:67], a[192:195], v[80:83], v[184:187]\n\t" \
  "s_add_u32 s68, s68, 4096\n\t" \
  "ds_read_b64_tr_b16 a[164:165], v228 offset:8704\n\t" \
  "v_cvt_pk_bf16_f32 v152, v96, v97\n\t" \
  "v_mfma_f32_16x16x32_bf16 v[64:67], a[196:199], v[84:87], v[64:67]\n\t" \
  "s_lshl3_add_u32 s69, s69, s47\n\t" \
  "v_cvt_pk_bf16_f32 v153, v98, v99\n\t" \
  "v_exp_f32 v1, v1\n\t" \
  "v_mfma_f32_16x16x32_bf16 v[64:67], a[200:203], v[88:91], v[64:67]\n\t" \
  "s_add_u32 s69, s69, 16384\n\t" \
  "ds_read_b64_tr_b16 a[166:167], v228 offset:12800\n\t" \
  "v_cvt_pk_bf16_f32 v154, v100, v101\n\t" \
  "v_mfma_f32_16x16x32_bf16 v[64:67], a[204:207], v[92:95], v[64:67]\n\t" \
  "s_lshl3_add_u32 s70, s70, s47\n\t" \
  "ds_read_b64_tr_b16 a[168:169], v229 offset:512\n\t" \
  "v_cvt_pk_bf16_f32 v155, v102, v103\n\t" \
  "v_mfma_f32_16x16x32_bf16 v[68:71], a[220:223], v[92:95], v[184:187]\n\t" \
  "v_exp_f32 v2, v2\n\t" \
  "v_mfma_f32_16x16x32_bf16 v[68:71], a[216:219], v[88:91], v[68:71]\n\t" \
  "v_exp_f32 v3, v3\n\t" \
  "v_mfma_f32_16x16x32_bf16 v[68:71], a[212:215], v[84:87], v[68:71]\n\t" \
  "ds_read_b64_tr_b16 a[170:171], v229 offset:4608\n\t" \
  "v_cvt_pk_bf16_f32 v156, v104, v105\n\t" \
  "v_mfma_f32_16x16x32_bf16 v[68:71], a[208:211], v[80:83], v[68:71]\n\t" \
  "v_exp_f32 v4, v4\n\t" \
  "v_mfma_f32_16x16x32_bf16 v[72:75], a[224:227], v[80:83], v[184:187]\n\t" \
  "buffer_load_dwordx4 v248, s[24:27], s96 offen offset:2048 lds\n\t" \
  "v_mfma_f32_16x16x32_bf16 v[72:75], a[228:231], v[84:87], v[72:75]\n\t" \
  "ds_read_b64_tr_b16 a[172:173], v229 offset:8704\n\t" \
  "v_cvt_pk_bf16_f32 v157, v106, v107\n\t" \
  "v_mfma_f32_16x16x32_bf16 v[72:75], a[232:235], v[88:91], v[72:75]\n\t" \
  "ds_read_b64_tr_b16 a[174:175], v229 offset:12800\n\t" \
  "v_cvt_pk_bf16_f32 v158, v108, v109\n\t" \
  "v_mfma_f32_16x16x32_bf16 v[72:75], a[236:239], v[92:95], v[72:75]\n\t" \
  "ds_read_b64_tr_b16 a[176:177], v228 offset:768\n\t" \
  "v_exp_f32 v5, v5\n\t" \
  "v_mfma_f32_16x16x32_bf16 v[76:79], a[252:255], v[92:95], v[184:187]\n\t" \
  "v_exp_f32 v6, v6\n\t" \
  "v_mfma_f32_16x16x32_bf16 v[76:79], a[248:251], v[88:91], v[76:79]\n\t" \
  "ds_read_b64_tr_b16 a[178:179], v228 offset:4864\n\t" \
  "v_cvt_pk_bf16_f32 v159, v110, v111\n\t" \
  "v_mfma_f32_16x16x32_bf16 v[76:79], a[244:247], v[84:87], v[76:79]\n\t" \
  "v_exp_f32 v7, v7\n\t" \
  "v_mfma_f32_16x16x32_bf16 v[76:79], a[240:243], v[80:83], v[76:79]\n\t" \
  "v_exp_f32 v8, v8\n\t" \
  "v_mfma_f32_16x16x32_bf16 v[96:99], a[192:195], v[112:115], v[188:191]\n\t" \
  "v_exp_f32 v9, v9\n\t" \
  "v_mfma_f32_16x16x32_bf16 v[96:99], a[196:199], v[116:119], v[96:99]\n\t" \
  "ds_read_b64_tr_b16 a[180:181], v228 offset:8960\n\t" \
  "v_cvt_pk_bf16_f32 v0, v0, v1\n\t" \
  "v_mfma_f32_16x16x32_bf16 v[96:99], a[200:203], v[120:123], v[96:99]\n\t" \
  "buffer_load_dwordx4 v249, s[24:27], s96 offen offset:3072 lds\n\t" \
  "v_mfma_f32_16x16x32_bf16 v[96:99], a[204:207], v[124:127], v[96:99]\n\t" \
  "ds_read_b64_tr_b16 a[182:183], v228 offset:13056\n\t" \
  "v_cvt_pk_bf16_f32 v1, v2, v3\n\t" \
  "v_mfma_f32_16x16x32_bf16 v[100:103], a[220:223], v[124:127], v[188:191]\n\t" \
  "v_exp_f32 v10, v10\n\t" \
  "v_mfma_f32_16x16x32_bf16 v[100:103], a[216:219], v[120:123], v[100:103]\n\t" \
  "ds_read_b64_tr_b16 a[184:185], v229 offset:768\n\t" \
  "v_cvt_pk_bf16_f32 v2, v4, v5\n\t" \
  "v_mfma_f32_16x16x32_bf16 v[100:103], a[212:215], v[116:119], v[100:103]\n\t" \
  "v_exp_f32 v11, v11\n\t" \
  "v_mfma_f32_16x16x32_bf16 v[100:103], a[208:211], v[112:115], v[100:103]\n\t" \
  "ds_read_b64_tr_b16 a[186:187], v229 offset:4864\n\t" \
  "v_cvt_pk_bf16_f32 v3, v6, v7\n\t" \
  "v_mfma_f32_16x16x32_bf16 v[104:107], a[224:227], v[112:115], v[188:191]\n\t" \
  "ds_read_b64_tr_b16 a[188:189], v229 offset:8960\n\t" \
  "v_cvt_pk_bf16_f32 v8, v8, v9\n\t" \
  "v_mfma_f32_16x16x32_bf16 v[104:107], a[228:231], v[116:119], v[104:107]\n\t" \
  "ds_read_b64_tr_b16 a[190:191], v229 offset:13056\n\t" \
  "v_exp_f32 v12, v12\n\t" \
  "v_mfma_f32_16x16x32_bf16 v[104:107], a[232:235], v[120:123], v[104:107]\n\t" \
  "s_add_u32 s96, s40, 524288\n\t" \
  "v_exp_f32 v13, v13\n\t" \
  "v_mfma_f32_16x16x32_bf16 v[104:107], a[236:239], v[124:127], v[104:107]\n\t" \
  "s_and_b32 s96, s96, 0x1c0000\n\t" \
  "v_exp_f32 v14, v14\n\t" \
  "v_mfma_f32_16x16x32_bf16 v[108:111], a[252:255], v[124:127], v[188:191]\n\t" \
  "s_add_u32 s96, s96, s77\n\t" \
  "v_exp_f32 v15, v15\n\t" \
  "v_mfma_f32_16x16x32_bf16 v[108:111], a[248:251], v[120:123], v[108:111]\n\t" \
  "s_add_u32 m0, s38, 81920\n\t" \
  "v_exp_f32 v32, v32\n\t" \
  "v_mfma_f32_16x16x32_bf16 v[108:111], a[244:247], v[116:119], v[108:111]\n\t" \
  "s_waitcnt vmcnt(6) lgkmcnt(0)\n\t" \
  "v_exp_f32 v33, v33\n\t" \
  "v_mfma_f32_16x16x32_bf16 v[108:111], a[240:243], v[112:115], v[108:111]\n\t" \
  "s_barrier\n\t" \
  STS("s_memrealtime s[94:95]\n\t") \
  STS("s_memtime s[92:93]\n\t") \
  "v_mfma_f32_16x16x32_bf16 a[0:3], a[128:131], v[128:131], a[0:3]\n\t" \
  "buffer_load_dwordx4 v240, s[24:27], s96 offen offset:0 lds\n\t" \
  "ds_read_b64_tr_b16 v[16:17], v210 offset:0\n\t" \
  "ds_read_b128 v[176:179], v215 offset:16384\n\t" \
  "ds_read_b128 v[180:183], v215 offset:17408\n\t" \
  "v_mfma_f32_16x16x32_bf16 a[0:3], a[132:135], v[132:135], a[0:3]\n\t" \
  "ds_read_b64_tr_b16 v[18:19], v210 offset:4096\n\t" \
  "ds_read_b64_tr_b16 v[20:21], v210 offset:8192\n\t" \
  "ds_read_b64_tr_b16 v[22:23], v210 offset:12288\n\t" \
  "ds_read_b64_tr_b16 v[24:25], v211 offset:0\n\t" \
  "v_mfma_f32_16x16x32_bf16 a[4:7], a[136:139], v[128:131], a[4:7]\n\t" \
  "ds_read_b64_tr_b16 v[26:27], v211 offset:4096\n\t" \
  "ds_read_b64_tr_b16 v[28:29], v211 offset:8192\n\t" \
  "ds_read_b64_tr_b16 v[30:31], v211 offset:12288\n\t" \
  "ds_read_b128 v[184:187], v215 offset:18432\n\t" \
  "v_mfma_f32_16x16x32_bf16 a[4:7], a[140:143], v[132:135], a[4:7]\n\t" \
  "buffer_load_dwordx4 v241, s[24:27], s96 offen offset:1024 lds\n\t" \
  "v_mfma_f32_16x16x32_bf16 a[8:11], a[144:147], v[128:131], a[8:11]\n\t" \
  "ds_read_b128 v[188:191], v215 offset:19456\n\t" \
  "ds_read_b64_tr_b16 v[48:49], v210 offset:256\n\t" \
  "v_cvt_pk_bf16_f32 v9, v10, v11\n\t" \
  "v_mfma_f32_16x16x32_bf16 a[8:11], a[148:151], v[132:135], a[8:11]\n\t" \
  "ds_read_b64_tr_b16 v[50:51], v210 offset:4352\n\t" \
  "v_cvt_pk_bf16_f32 v10, v12, v13\n\t" \
  "v_mfma_f32_16x16x32_bf16 a[12:15], a[152:155], v[128:131], a[12:15]\n\t" \
  "buffer_load_dwordx4 v248, s[24:27], s96 offen offset:2048 lds\n\t" \
  "v_mfma_f32_16x16x32_bf16 a[12:15], a[156:159], v[132:135], a[12:15]\n\t" \
  "v_exp_f32 v34, v34\n\t" \
  "v_mfma_f32_16x16x32_bf16 a[16:19], a[160:163], v[128:131], a[16:19]\n\t" \
  "ds_read_b64_tr_b16 v[52:53], v210 offset:8448\n\t" \
  "v_cvt_pk_bf16_f32 v11, v14, v15\n\t" \
  "v_mfma_f32_16x16x32_bf16 a[16:19], a[164:167], v[132:135], a[16:19]\n\t" \
  "buffer_load_dwordx4 v249, s[24:27], s96 offen offset:3072 lds\n\t" \
  "v_mfma_f32_16x16x32_bf16 a[20:23], a[168:171], v[128:131], a[20:23]\n\t" \
  "s_add_u32 s96, s40, 786432\n\t" \
  "ds_read_b64_tr_b16 v[54:55], v210 offset:12544\n\t" \
  "v_cvt_pk_bf16_f32 v32, v32, v33\n\t" \
  "v_mfma_f32_16x16x32_bf16 a[20:23], a[172:175], v[132:135], a[20:23]\n\t" \
  "s_and_b32 s96, s96, 0x1c0000\n\t" \
  "v_exp_f32 v35, v35\n\t" \
  "v_mfma_f32_16x16x32_bf16 a[24:27], a[176:179], v[128:131], a[24:27]\n\t" \
  STS("v_writelane_b32 v255, s94, 20\n\t") \
  STS("v_writelane_b32 v255, s92, 21\n\t") \
  "s_add_u32 s96, s96, s51\n\t" \
  "v_cvt_pk_bf16_f32 v33, v34, v35\n\t" \
  "s_add_u32 m0, s38, 114688\n\t" \
  "v_mfma_f32_16x16x32_bf16 a[24:27], a[180:183], v[132:135], a[24:27]\n\t" \
  "v_exp_f32 v36, v36\n\t" \
  "v_mfma_f32_16x16x32_bf16 a[28:31], a[184:187], v[128:131], a[28:31]\n\t" \
  "s_waitcnt lgkmcnt(4)\n\t" \
  "buffer_load_dwordx4 v240, s[24:27], s96 offen offset:0 lds\n\t" \
  "v_mfma_f32_16x16x32_bf16 a[28:31], a[188:191], v[132:135], a[28:31]\n\t" \
  "ds_read_b64_tr_b16 v[56:57], v211 offset:256\n\t" \
  "v_add_u32 v230, 0x10000, v242\n\t" \
  "v_mfma_f32_16x16x32_bf16 v[160:163], v[216:219], v[128:131], v[160:163]\n\t" \
  "ds_read_b64_tr_b16 v[58:59], v211 offset:4352\n\t" \
  "v_add_u32 v232, 0x20000, v242\n\t" \
  "v_mfma_f32_16x16x32_bf16 v[160:163], v[216:219], v[132:135], v[160:163]\n\t" \
  "buffer_load_dwordx4 v241, s[24:27], s96 offen offset:1024 lds\n\t" \
  "v_mfma_f32_16x16x32_bf16 a[32:35], a[128:131], v[136:139], a[32:35]\n\t" \
  "v_exp_f32 v37, v37\n\t" \
  "v_mfma_f32_16x16x32_bf16 a[32:35], a[132:135], v[140:143], a[32:35]\n\t" \
  "ds_read_b64_tr_b16 v[60:61], v211 offset:8448\n\t" \
  "v_cvt_pk_bf16_f32 v34, v36, v37\n\t" \
  "v_mfma_f32_16x16x32_bf16 a[36:39], a[136:139], v[136:139], a[36:39]\n\t" \
  "buffer_load_dwordx4 v248, s[24:27], s96 offen offset:2048 lds\n\t" \
  "v_mfma_f32_16x16x32_bf16 a[36:39], a[140:143], v[140:143], a[36:39]\n\t" \
  "v_exp_f32 v38, v38\n\t" \
  "v_mfma_f32_16x16x32_bf16 a[40:43], a[144:147], v[136:139], a[40:43]\n\t" \
  "ds_read_b64_tr_b16 v[62:63], v211 offset:12544\n\t" \
  "v_mfma_f32_16x16x32_bf16 a[40:43], a[148:151], v[140:143], a[40:43]\n\t" \
  "buffer_load_dwordx4 v249, s[24:27], s96 offen offset:3072 lds\n\t" \
  "v_mfma_f32_16x16x32_bf16 a[44:47], a[152:155], v[136:139], a[44:47]\n\t" \
  "ds_read_b64_tr_b16 v[80:81], v210 offset:512\n\t" \
  "v_mfma_f32_16x16x32_bf16 a[44:47], a[156:159], v[140:143], a[44:47]\n\t" \
  "ds_read_b64_tr_b16 a[192:193], v234 offset:0\n\t" \
  "ds_read_b64_tr_b16 v[82:83], v210 offset:4608\n\t" \
  "v_exp_f32 v39, v39\n\t" \
  "v_mfma_f32_16x16x32_bf16 a[48:51], a[160:163], v[136:139], a[48:51]\n\t" \
  "ds_read_b64_tr_b16 v[84:85], v210 offset:8704\n\t" \
  "v_exp_f32 v40, v40\n\t" \
  "v_mfma_f32_16x16x32_bf16 a[48:51], a[164:167], v[140:143], a[48:51]\n\t" \
  "ds_read_b64_tr_b16 a[194:195], v234 offset:4096\n\t" \
  "ds_read_b64_tr_b16 v[86:87], v210 offset:12800\n\t" \
  "v_cvt_pk_bf16_f32 v35, v38, v39\n\t" \
  "v_mfma_f32_16x16x32_bf16 a[52:55], a[168:171], v[136:139], a[52:55]\n\t" \
  "v_exp_f32 v41, v41\n\t" \
  "v_mfma_f32_16x16x32_bf16 a[52:55], a[172:175], v[140:143], a[52:55]\n\t" \
  "ds_read_b64_tr_b16 v[88:89], v211 offset:512\n\t" \
  "v_cvt_pk_bf16_f32 v40, v40, v41\n\t" \
  "v_mfma_f32_16x16x32_bf16 a[56:59], a[176:179], v[136:139], a[56:59]\n\t" \
  "v_exp_f32 v42, v42\n\t" \
  "v_mfma_f32_16x16x32_bf16 a[56:59], a[180:183], v[140:143], a[56:59]\n\t" \
  "ds_read_b64_tr_b16 v[90:91], v211 offset:4608\n\t" \
  "v_mfma_f32_16x16x32_bf16 a[60:63], a[184:187], v[136:139], a[60:63]\n\t" \
  "ds_read_b64_tr_b16 v[92:93], v211 offset:8704\n\t" \
  "v_mfma_f32_16x16x32_bf16 a[60:63], a[188:191], v[140:143], a[60:63]\n\t" \
  "v_exp_f32 v43, v43\n\t" \
  "v_mfma_f32_16x16x32_bf16 v[164:167], v[216:219], v[136:139], v[164:167]\n\t" \
  "ds_read_b64_tr_b16 a[196:197], v235 offset:0\n\t" \
  "ds_read_b64_tr_b16 v[94:95], v211 offset:12800\n\t" \
  "v_cvt_pk_bf16_f32 v41, v42, v43\n\t" \
  "v_mfma_f32_16x16x32_bf16 v[164:167], v[216:219], v[140:143], v[164:167]\n\t" \
  "ds_read_b64_tr_b16 a[198:199], v235 offset:4096\n\t" \
  "ds_read_b64_tr_b16 v[112:113], v210 offset:768\n\t" \
  "v_exp_f32 v44, v44\n\t" \
  "v_mfma_f32_16x16x32_bf16 a[0:3], v[16:19], v[0:3], a[0:3]\n\t" \
  "ds_read_b64_tr_b16 v[114:115], v210 offset:4864\n\t" \
  "v_mfma_f32_16x16x32_bf16 a[0:3], v[20:23], v[8:11], a[0:3]\n\t" \
  "v_exp_f32 v45, v45\n\t" \
  "v_mfma_f32_16x16x32_bf16 v[160:163], v[216:219], v[0:3], v[160:163]\n\t" \
  "ds_read_b64_tr_b16 v[116:117], v210 offset:8960\n\t" \
  "v_cvt_pk_bf16_f32 v42, v44, v45\n\t" \
  "v_mfma_f32_16x16x32_bf16 v[160:163], v[216:219], v[8:11], v[160:163]\n\t" \
  "ds_read_b64_tr_b16 v[118:119], v210 offset:13056\n\t" \
  "v_mfma_f32_16x16x32_bf16 a[4:7], v[24:27], v[0:3], a[4:7]\n\t" \
  "v_exp_f32 v46, v46\n\t" \
  "v_mfma_f32_16x16x32_bf16 a[4:7], v[28:31], v[8:11], a[4:7]\n\t" \
  "s_waitcnt lgkmcnt(15)\n\t" \
  "ds_read_b64_tr_b16 v[120:121], v211 offset:768\n\t" \
  "v_mfma_f32_16x16x32_bf16 a[8:11], v[48:51], v[0:3], a[8:11]\n\t" \
  "ds_read_b64_tr_b16 v[122:123], v211 offset:4864\n\t" \
  "v_exp_f32 v47, v47\n\t" \
  "v_mfma_f32_16x16x32_bf16 a[8:11], v[52:55], v[8:11], a[8:11]\n\t" \
  "v_rcp_f32 v224, v160\n\t" \
  "v_mfma_f32_16x16x32_bf16 a[12:15], v[56:59], v[0:3], a[12:15]\n\t" \
  "ds_read_b64_tr_b16 v[124:125], v211 offset:8960\n\t" \
  "v_accvgpr_read_b32 v128, a0\n\t" \
  "v_accvgpr_read_b32 v129, a1\n\t" \
  "v_accvgpr_read_b32 v130, a2\n\t" \
  "v_accvgpr_read_b32 v131, a3\n\t" \
  "v_accvgpr_read_b32 v132, a4\n\t" \
  "v_accvgpr_read_b32 v133, a5\n\t" \
  "v_accvgpr_read_b32 v134, a6\n\t" \
  "v_accvgpr_read_b32 v135, a7\n\t" \
  "v_pk_mul_f32 v[128:129], v[128:129], v[224:225] op_sel_hi:[1,0]\n\t" \
  "v_pk_mul_f32 v[130:131], v[130:131], v[224:225] op_sel_hi:[1,0]\n\t" \
  "v_pk_mul_f32 v[132:133], v[132:133], v[224:225] op_sel_hi:[1,0]\n\t" \
  "v_pk_mul_f32 v[134:135], v[134:135], v[224:225] op_sel_hi:[1,0]\n\t" \
  "v_cvt_pk_bf16_f32 v128, v128, v129\n\t" \
  "v_cvt_pk_bf16_f32 v129, v130, v131\n\t" \
  "v_cvt_pk_bf16_f32 v130, v132, v133\n\t" \
  "v_cvt_pk_bf16_f32 v131, v134, v135\n\t" \
  "buffer_store_dwordx4 v[128:131], v242, s[16:19], s98 offen offset:0 sc1\n\t" \
  "v_mfma_f32_16x16x32_bf16 a[12:15], v[60:63], v[8:11], a[12:15]\n\t" \
  "s_waitcnt lgkmcnt(8)\n\t" \
  "ds_read_b64_tr_b16 a[200:201], v234 offset:8192\n\t" \
  "ds_read_b64_tr_b16 v[126:127], v211 offset:13056\n\t" \
  "v_cmp_lt_f32 vcc, 0x5f800000, v160\n\t" \
  "v_mfma_f32_16x16x32_bf16 a[16:19], v[80:83], v[0:3], a[16:19]\n\t" \
  "s_cmp_lg_u64 vcc, 0\n\t" \
  "s_cselect_b32 s89, s78, s89\n\t" \
  "v_or_b32 v254, s89, v254\n\t" \
  "v_mfma_f32_16x16x32_bf16 a[16:19], v[84:87], v[8:11], a[16:19]\n\t" \
  "ds_read_b64_tr_b16 a[202:203], v234 offset:12288\n\t" \
  "v_exp_f32 v64, v64\n\t" \
  "v_mfma_f32_16x16x32_bf16 a[20:23], v[88:91], v[0:3], a[20:23]\n\t" \
  "v_accvgpr_read_b32 v136, a8\n\t" \
  "v_accvgpr_read_b32 v137, a9\n\t" \
  "v_accvgpr_read_b32 v138, a10\n\t" \
  "v_accvgpr_read_b32 v139, a11\n\t" \
  "v_accvgpr_read_b32 v140, a12\n\t" \
  "v_accvgpr_read_b32 v141, a13\n\t" \
  "v_accvgpr_read_b32 v142, a14\n\t" \
  "v_accvgpr_read_b32 v143, a15\n\t" \
  "v_pk_mul_f32 v[136:137], v[136:137], v[224:225] op_sel_hi:[1,0]\n\t" \
  "v_pk_mul_f32 v[138:139], v[138:139], v[224:225] op_sel_hi:[1,0]\n\t" \
  "v_pk_mul_f32 v[140:141], v[140:141], v[224:225] op_sel_hi:[1,0]\n\t" \
  "v_pk_mul_f32 v[142:143], v[142:143], v[224:225] op_sel_hi:[1,0]\n\t" \
  "v_cvt_pk_bf16_f32 v136, v136, v137\n\t" \
  "v_cvt_pk_bf16_f32 v137, v138, v139\n\t" \
  "v_cvt_pk_bf16_f32 v138, v140, v141\n\t" \
  "v_cvt_pk_bf16_f32 v139, v142, v143\n\t" \
  "buffer_store_dwordx4 v[136:139], v242, s[16:19], s98 offen offset:1024 sc1\n\t" \
  "v_mfma_f32_16x16x32_bf16 a[20:23], v[92:95], v[8:11], a[20:23]\n\t" \
  "s_waitcnt lgkmcnt(1)\n\t" \
  "ds_read_b64_tr_b16 a[204:205], v235 offset:8192\n\t" \
  "v_exp_f32 v65, v65\n\t" \
  "v_mfma_f32_16x16x32_bf16 a[24:27], v[112:115], v[0:3], a[24:27]\n\t" \
  "ds_read_b128 a[224:227], v213 offset:16384\n\t" \
  "v_exp_f32 v66, v66\n\t" \
  "v_mfma_f32_16x16x32_bf16 a[24:27], v[116:119], v[8:11], a[24:27]\n\t" \
  "ds_read_b64_tr_b16 a[206:207], v235 offset:12288\n\t" \
  "v_exp_f32 v67, v67\n\t" \
  "v_mfma_f32_16x16x32_bf16 a[28:31], v[120:123], v[0:3], a[28:31]\n\t" \
  "ds_read_b128 a[228:231], v213 offset:17408\n\t" \
  "v_exp_f32 v68, v68\n\t" \
  "v_mfma_f32_16x16x32_bf16 a[28:31], v[124:127], v[8:11], a[28:31]\n\t" \
  "ds_read_b128 a[232:235], v213 offset:18432\n\t" \
  "v_exp_f32 v69, v69\n\t" \
  "v_mfma_f32_16x16x32_bf16 a[64:67], a[128:131], v[144:147], a[64:67]\n\t" \
  "ds_read_b128 a[236:239], v213 offset:19456\n\t" \
  "v_exp_f32 v70, v70\n\t" \
  "v_mfma_f32_16x16x32_bf16 a[64:67], a[132:135], v[148:151], a[64:67]\n\t" \
  "ds_write_b128 v213, v[128:131] offset:16384\n\t" \
  "s_barrier\n\t" \
  "v_mfma_f32_16x16x32_bf16 a[68:71], a[136:139], v[144:147], a[68:71]\n\t" \
  "ds_write_b128 v213, v[136:139] offset:17408\n\t" \
  "v_accvgpr_read_b32 v0, a16\n\t" \
  "v_accvgpr_read_b32 v1, a17\n\t" \
  "v_accvgpr_read_b32 v2, a18\n\t" \
  "v_accvgpr_read_b32 v3, a19\n\t" \
  "v_accvgpr_read_b32 v4, a20\n\t" \
  "v_accvgpr_read_b32 v5, a21\n\t" \
  "v_accvgpr_read_b32 v6, a22\n\t" \
  "v_accvgpr_read_b32 v7, a23\n\t" \
  "v_pk_mul_f32 v[0:1], v[0:1], v[224:225] op_sel_hi:[1,0]\n\t" \
  "v_pk_mul_f32 v[2:3], v[2:3], v[224:225] op_sel_hi:[1,0]\n\t" \
  "v_pk_mul_f32 v[4:5], v[4:5], v[224:225] op_sel_hi:[1,0]\n\t" \
  "v_pk_mul_f32 v[6:7], v[6:7], v[224:225] op_sel_hi:[1,0]\n\t" \
  "v_cvt_pk_bf16_f32 v0, v0, v1\n\t" \
  "v_cvt_pk_bf16_f32 v1, v2, v3\n\t" \
  "v_cvt_pk_bf16_f32 v2, v4, v5\n\t" \
  "v_cvt_pk_bf16_f32 v3, v6, v7\n\t" \
  "buffer_store_dwordx4 v[0:3], v242, s[16:19], s98 offen offset:2048 sc1\n\t" \
  "v_mfma_f32_16x16x32_bf16 a[68:71], a[140:143], v[148:151], a[68:71]\n\t" \
  "ds_write_b128 v213, v[0:3] offset:18432\n\t" \
  "v_accvgpr_read_b32 v8, a24\n\t" \
  "v_accvgpr_read_b32 v9, a25\n\t" \
  "v_accvgpr_read_b32 v10, a26\n\t" \
  "v_accvgpr_read_b32 v11, a27\n\t" \
  "v_accvgpr_read_b32 v12, a28\n\t" \
  "v_accvgpr_read_b32 v13, a29\n\t" \
  "v_accvgpr_read_b32 v14, a30\n\t" \
  "v_accvgpr_read_b32 v15, a31\n\t" \
  "v_pk_mul_f32 v[8:9], v[8:9], v[224:225] op_sel_hi:[1,0]\n\t" \
  "v_pk_mul_f32 v[10:11], v[10:11], v[224:225] op_sel_hi:[1,0]\n\t" \
  "v_pk_mul_f32 v[12:13], v[12:13], v[224:225] op_sel_hi:[1,0]\n\t" \
  "v_pk_mul_f32 v[14:15], v[14:15], v[224:225] op_sel_hi:[1,0]\n\t" \
  "v_cvt_pk_bf16_f32 v8, v8, v9\n\t" \
  "v_cvt_pk_bf16_f32 v9, v10, v11\n\t" \
  "v_cvt_pk_bf16_f32 v10, v12, v13\n\t" \
  "v_cvt_pk_bf16_f32 v11, v14, v15\n\t" \
  "buffer_store_dwordx4 v[8:11], v242, s[16:19], s98 offen offset:3072 sc1\n\t" \
  "v_mfma_f32_16x16x32_bf16 a[72:75], a[144:147], v[144:147], a[72:75]\n\t" \
  "ds_write_b128 v213, v[8:11] offset:19456\n\t" \
  "v_mfma_f32_16x16x32_bf16 a[72:75], a[148:151], v[148:151], a[72:75]\n\t" \
  "s_xor_b32 s98, s45, 32768\n\t" \
  "ds_read_b64_tr_b16 a[208:209], v236 offset:32768\n\t" \
  "v_exp_f32 v71, v71\n\t" \
  "v_mfma_f32_16x16x32_bf16 a[76:79], a[152:155], v[144:147], a[76:79]\n\t" \
  "v_exp_f32 v72, v72\n\t" \
  "v_mfma_f32_16x16x32_bf16 a[76:79], a[156:159], v[148:151], a[76:79]\n\t" \
  "v_exp_f32 v73, v73\n\t" \
  "v_mfma_f32_16x16x32_bf16 a[80:83], a[160:163], v[144:147], a[80:83]\n\t" \
  "ds_read_b64_tr_b16 a[210:211], v236 offset:36864\n\t" \
  "v_cvt_pk_bf16_f32 v43, v46, v47\n\t" \
  "v_mfma_f32_16x16x32_bf16 a[80:83], a[164:167], v[148:151], a[80:83]\n\t" \
  "v_exp_f32 v74, v74\n\t" \
  "v_mfma_f32_16x16x32_bf16 a[84:87], a[168:171], v[144:147], a[84:87]\n\t" \
  "v_exp_f32 v75, v75\n\t" \
  "v_mfma_f32_16x16x32_bf16 a[84:87], a[172:175], v[148:151], a[84:87]\n\t" \
  "v_exp_f32 v76, v76\n\t" \
  "v_mfma_f32_16x16x32_bf16 a[88:91], a[176:179], v[144:147], a[88:91]\n\t" \
  "v_exp_f32 v77, v77\n\t" \
  "v_mfma_f32_16x16x32_bf16 a[88:91], a[180:183], v[148:151], a[88:91]\n\t" \
  "ds_read_b64_tr_b16 a[212:213], v237 offset:32768\n\t" \
  "v_cvt_pk_bf16_f32 v64, v64, v65\n\t" \
  "v_mfma_f32_16x16x32_bf16 a[92:95], a[184:187], v[144:147], a[92:95]\n\t" \
  ".Lhs10:\n\ts_cbranch_execz .Lhs10n\n\t" \
  "v_exp_f32 v78, v78\n\t" \
  "v_mfma_f32_16x16x32_bf16 a[92:95], a[188:191], v[148:151], a[92:95]\n\t" \
  "ds_read_b64_tr_b16 a[214:215], v237 offset:36864\n\t" \
  "v_cvt_pk_bf16_f32 v65, v66, v67\n\t" \
  "v_mfma_f32_16x16x32_bf16 v[168:171], v[216:219], v[144:147], v[168:171]\n\t" \
  "v_exp_f32 v79, v79\n\t" \
  "v_mfma_f32_16x16x32_bf16 v[168:171], v[216:219], v[148:151], v[168:171]\n\t" \
  "s_add_u32 s96, s40, 786432\n\t" \
  "v_exp_f32 v96, v96\n\t" \
  "v_mfma_f32_16x16x32_bf16 a[32:35], v[16:19], v[32:35], a[32:35]\n\t" \
  "s_and_b32 s96, s96, 0x1c0000\n\t" \
  "ds_read_b64_tr_b16 a[216:217], v236 offset:40960\n\t" \
  "v_cvt_pk_bf16_f32 v66, v68, v69\n\t" \
  "v_mfma_f32_16x16x32_bf16 a[32:35], v[20:23], v[40:43], a[32:35]\n\t" \
  "s_add_u32 s96, s96, s77\n\t" \
  "v_exp_f32 v97, v97\n\t" \
  "v_mfma_f32_16x16x32_bf16 v[164:167], v[216:219], v[32:35], v[164:167]\n\t" \
  "s_add_u32 m0, s38, 147456\n\t" \
  "v_exp_f32 v98, v98\n\t" \
  "v_mfma_f32_16x16x32_bf16 v[164:167], v[216:219], v[40:43], v[164:167]\n\t" \
  "ds_read_b64_tr_b16 a[218:219], v236 offset:45056\n\t" \
  "v_cvt_pk_bf16_f32 v67, v70, v71\n\t" \
  "v_mfma_f32_16x16x32_bf16 a[36:39], v[24:27], v[32:35], a[36:39]\n\t" \
  "v_exp_f32 v99, v99\n\t" \
  "v_mfma_f32_16x16x32_bf16 a[36:39], v[28:31], v[40:43], a[36:39]\n\t" \
  "v_cvt_pk_bf16_f32 v72, v72, v73\n\t" \
  "v_cvt_pk_bf16_f32 v73, v74, v75\n\t" \
  "s_nop 0\n\t" \
  "v_rcp_f32 v226, v164\n\t" \
  "v_mfma_f32_16x16x32_bf16 a[40:43], v[48:51], v[32:35], a[40:43]\n\t" \
  "buffer_load_dwordx4 v240, s[24:27], s96 offen offset:0 lds\n\t" \
  "v_accvgpr_read_b32 v0, a32\n\t" \
  "v_accvgpr_read_b32 v1, a33\n\t" \
  "v_mfma_f32_16x16x32_bf16 a[40:43], v[52:55], v[40:43], a[40:43]\n\t" \
  "v_cmp_lt_f32 vcc, 0x5f800000, v164\n\t" \
  "v_accvgpr_read_b32 v2, a34\n\t" \
  "v_accvgpr_read_b32 v3, a35\n\t" \
  "v_mfma_f32_16x16x32_bf16 a[44:47], v[56:59], v[32:35], a[44:47]\n\t" \
  "s_cmp_lg_u64 vcc, 0\n\t" \
  "s_cselect_b32 s89, s78, s89\n\t" \
  "v_or_b32 v254, s89, v254\n\t" \
  "v_mfma_f32_16x16x32_bf16 a[44:47], v[60:63], v[40:43], a[44:47]\n\t" \
  "buffer_load_dwordx4 v241, s[24:27], s96 offen offset:1024 lds\n\t" \
  "v_accvgpr_read_b32 v4, a36\n\t" \
  "v_accvgpr_read_b32 v5, a37\n\t" \
  "v_mfma_f32_16x16x32_bf16 a[48:51], v[80:83], v[32:35], a[48:51]\n\t" \
  "ds_read_b64_tr_b16 a[220:221], v237 offset:40960\n\t" \
  "v_accvgpr_read_b32 v6, a38\n\t" \
  "v_accvgpr_read_b32 v7, a39\n\t" \
  "v_mfma_f32_16x16x32_bf16 a[48:51], v[84:87], v[40:43], a[48:51]\n\t" \
  "buffer_load_dwordx4 v248, s[24:27], s96 offen offset:2048 lds\n\t" \
  "v_mfma_f32_16x16x32_bf16 a[52:55], v[88:91], v[32:35], a[52:55]\n\t" \
  "ds_read_b64_tr_b16 a[222:223], v237 offset:45056\n\t" \
  "v_mul_f32 v0, v0, v226\n\t" \
  "v_mul_f32 v1, v1, v226\n\t" \
  "v_mfma_f32_16x16x32_bf16 a[52:55], v[92:95], v[40:43], a[52:55]\n\t" \
  "v_exp_f32 v100, v100\n\t" \
  "v_mul_f32 v2, v2, v226\n\t" \
  "v_mul_f32 v3, v3, v226\n\t" \
  "v_mfma_f32_16x16x32_bf16 a[56:59], v[112:115], v[32:35], a[56:59]\n\t" \
  "buffer_load_dwordx4 v249, s[24:27], s96 offen offset:3072 lds\n\t" \
  "v_mfma_f32_16x16x32_bf16 a[56:59], v[116:119], v[40:43], a[56:59]\n\t" \
  "v_exp_f32 v101, v101\n\t" \
  "v_mul_f32 v4, v4, v226\n\t" \
  "v_mul_f32 v5, v5, v226\n\t" \
  "v_mfma_f32_16x16x32_bf16 a[60:63], v[120:123], v[32:35], a[60:63]\n\t" \
  "v_exp_f32 v102, v102\n\t" \
  "v_mul_f32 v6, v6, v226\n\t" \
  "v_mul_f32 v7, v7, v226\n\t" \
  "v_mfma_f32_16x16x32_bf16 a[60:63], v[124:127], v[40:43], a[60:63]\n\t" \
  "v_cvt_pk_bf16_f32 v32, v0, v1\n\t" \
  "v_cvt_pk_bf16_f32 v33, v2, v3\n\t" \
  "v_cvt_pk_bf16_f32 v34, v4, v5\n\t" \
  "v_cvt_pk_bf16_f32 v35, v6, v7\n\t" \
  "v_mfma_f32_16x16x32_bf16 a[96:99], a[128:131], v[152:155], a[96:99]\n\t" \
  "v_accvgpr_read_b32 v8, a40\n\t" \
  "v_accvgpr_read_b32 v9, a41\n\t" \
  "v_accvgpr_read_b32 v10, a42\n\t" \
  "v_accvgpr_read_b32 v11, a43\n\t" \
  "v_mfma_f32_16x16x32_bf16 a[96:99], a[132:135], v[156:159], a[96:99]\n\t" \
  "v_accvgpr_read_b32 v12, a44\n\t" \
  "v_accvgpr_read_b32 v13, a45\n\t" \
  "v_accvgpr_read_b32 v14, a46\n\t" \
  "v_accvgpr_read_b32 v15, a47\n\t" \
  "v_mfma_f32_16x16x32_bf16 a[100:103], a[136:139], v[152:155], a[100:103]\n\t" \
  "v_mul_f32 v8, v8, v226\n\t" \
  "v_mul_f32 v9, v9, v226\n\t" \
  "v_mul_f32 v10, v10, v226\n\t" \
  "v_mul_f32 v11, v11, v226\n\t" \
  "v_mfma_f32_16x16x32_bf16 a[100:103], a[140:143], v[156:159], a[100:103]\n\t" \
  "v_mul_f32 v12, v12, v226\n\t" \
  "v_mul_f32 v13, v13, v226\n\t" \
  "v_mul_f32 v14, v14, v226\n\t" \
  "v_mul_f32 v15, v15, v226\n\t" \
  "v_mfma_f32_16x16x32_bf16 a[104:107], a[144:147], v[152:155], a[104:107]\n\t" \
  "s_waitcnt vmcnt(8) lgkmcnt(0)\n\t" \
  "v_cvt_pk_bf16_f32 v36, v8, v9\n\t" \
  "v_cvt_pk_bf16_f32 v37, v10, v11\n\t" \
  "v_cvt_pk_bf16_f32 v38, v12, v13\n\t" \
  "v_cvt_pk_bf16_f32 v39, v14, v15\n\t" \
  "v_mfma_f32_16x16x32_bf16 a[104:107], a[148:151], v[156:159], a[104:107]\n\t" \
  "v_accvgpr_read_b32 v0, a48\n\t" \
  "v_accvgpr_read_b32 v1, a49\n\t" \
  "s_barrier\n\t" \
  "v_mfma_f32_16x16x32_bf16 a[108:111], a[152:155], v[152:155], a[108:111]\n\t" \
  "buffer_store_dwordx4 v[32:35], v242, s[16:19], s98 offen offset:0 sc1\n\t" \
  "v_accvgpr_read_b32 v2, a50\n\t" \
  "v_accvgpr_read_b32 v3, a51\n\t" \
  "v_accvgpr_read_b32 v4, a52\n\t" \
  "v_mfma_f32_16x16x32_bf16 a[108:111], a[156:159], v[156:159], a[108:111]\n\t" \
  "s_add_u32 s96, s40, 1048576\n\t" \
  "s_and_b32 s96, s96, 0x1c0000\n\t" \
  "s_add_u32 s96, s96, s51\n\t" \
  "v_accvgpr_read_b32 v5, a53\n\t" \
  "v_mfma_f32_16x16x32_bf16 a[112:115], a[160:163], v[152:155], a[112:115]\n\t" \
  "s_add_u32 m0, s38, 0\n\t" \
  "v_accvgpr_read_b32 v6, a54\n\t" \
  "v_accvgpr_read_b32 v7, a55\n\t" \
  "v_mul_f32 v0, v0, v226\n\t" \
  "v_mfma_f32_16x16x32_bf16 a[112:115], a[164:167], v[156:159], a[112:115]\n\t" \
  "buffer_load_dwordx4 v240, s[24:27], s96 offen offset:0 lds\n\t" \
  "v_mul_f32 v1, v1, v226\n\t" \
  "v_mul_f32 v2, v2, v226\n\t" \
  "v_mul_f32 v3, v3, v226\n\t" \
  "v_mfma_f32_16x16x32_bf16 a[116:119], a[168:171], v[152:155], a[116:119]\n\t" \
  "v_mul_f32 v4, v4, v226\n\t" \
  "v_mul_f32 v5, v5, v226\n\t" \
  "v_mul_f32 v6, v6, v226\n\t" \
  "v_mul_f32 v7, v7, v226\n\t" \
  "v_mfma_f32_16x16x32_bf16 a[116:119], a[172:175], v[156:159], a[116:119]\n\t" \
  "buffer_load_dwordx4 v241, s[24:27], s96 offen offset:1024 lds\n\t" \
  "v_cvt_pk_bf16_f32 v40, v0, v1\n\t" \
  "v_cvt_pk_bf16_f32 v41, v2, v3\n\t" \
  "v_cvt_pk_bf16_f32 v42, v4, v5\n\t" \
  "v_mfma_f32_16x16x32_bf16 a[120:123], a[176:179], v[152:155], a[120:123]\n\t" \
  "v_cvt_pk_bf16_f32 v74, v76, v77\n\t" \
  "v_cvt_pk_bf16_f32 v43, v6, v7\n\t" \
  "v_accvgpr_read_b32 v8, a56\n\t" \
  "v_accvgpr_read_b32 v9, a57\n\t" \
  "v_mfma_f32_16x16x32_bf16 a[120:123], a[180:183], v[156:159], a[120:123]\n\t" \
  "v_accvgpr_read_b32 v10, a58\n\t" \
  "v_accvgpr_read_b32 v11, a59\n\t" \
  "v_accvgpr_read_b32 v12, a60\n\t" \
  "v_exp_f32 v103, v103\n\t" \
  "v_mfma_f32_16x16x32_bf16 a[124:127], a[184:187], v[152:155], a[124:127]\n\t" \
  "buffer_load_dwordx4 v248, s[24:27], s96 offen offset:2048 lds\n\t" \
  "v_accvgpr_read_b32 v13, a61\n\t" \
  "v_accvgpr_read_b32 v14, a62\n\t" \
  "v_accvgpr_read_b32 v15, a63\n\t" \
  "v_mfma_f32_16x16x32_bf16 a[124:127], a[188:191], v[156:159], a[124:127]\n\t" \
  "v_cvt_pk_bf16_f32 v75, v78, v79\n\t" \
  "v_mul_f32 v8, v8, v226\n\t" \
  "v_mul_f32 v9, v9, v226\n\t" \
  "v_mul_f32 v10, v10, v226\n\t" \
  "v_mfma_f32_16x16x32_bf16 v[172:175], v[216:219], v[152:155], v[172:175]\n\t" \
  "v_cvt_pk_bf16_f32 v96, v96, v97\n\t" \
  "v_exp_f32 v104, v104\n\t" \
  "v_mul_f32 v11, v11, v226\n\t" \
  "v_mul_f32 v12, v12, v226\n\t" \
  "v_mfma_f32_16x16x32_bf16 v[172:175], v[216:219], v[156:159], v[172:175]\n\t" \
  "buffer_store_dwordx4 v[36:39], v242, s[16:19], s98 offen offset:1024 sc1\n\t" \
  "v_exp_f32 v105, v105\n\t" \
  "v_mul_f32 v13, v13, v226\n\t" \
  "v_exp_f32 v106, v106\n\t" \
  "v_mfma_f32_16x16x32_bf16 a[64:67], v[16:19], v[64:67], a[64:67]\n\t" \
  "v_cvt_pk_bf16_f32 v97, v98, v99\n\t" \
  "v_exp_f32 v107, v107\n\t" \
  "v_mfma_f32_16x16x32_bf16 a[64:67], v[20:23], v[72:75], a[64:67]\n\t" \
  "v_cvt_pk_bf16_f32 v98, v100, v101\n\t" \
  "v_mul_f32 v14, v14, v226\n\t" \
  "v_mfma_f32_16x16x32_bf16 v[168:171], v[216:219], v[64:67], v[168:171]\n\t" \
  "v_mul_f32 v15, v15, v226\n\t" \
  "v_exp_f32 v108, v108\n\t" \
  "v_mfma_f32_16x16x32_bf16 v[168:171], v[216:219], v[72:75], v[168:171]\n\t" \
  "v_cvt_pk_bf16_f32 v99, v102, v103\n\t" \
  "v_cvt_pk_bf16_f32 v44, v8, v9\n\t" \
  "v_mfma_f32_16x16x32_bf16 a[68:71], v[24:27], v[64:67], a[68:71]\n\t" \
  "v_cvt_pk_bf16_f32 v104, v104, v105\n\t" \
  "v_cvt_pk_bf16_f32 v45, v10, v11\n\t" \
  "v_mfma_f32_16x16x32_bf16 a[68:71], v[28:31], v[72:75], a[68:71]\n\t" \
  "v_cvt_pk_bf16_f32 v105, v106, v107\n\t" \
  "v_exp_f32 v109, v109\n\t" \
  "v_mfma_f32_16x16x32_bf16 a[72:75], v[48:51], v[64:67], a[72:75]\n\t" \
  "v_rcp_f32 v220, v168\n\t" \
  "v_mfma_f32_16x16x32_bf16 a[72:75], v[52:55], v[72:75], a[72:75]\n\t" \
  "v_cmp_lt_f32 vcc, 0x5f800000, v168\n\t" \
  "v_exp_f32 v110, v110\n\t" \
  "v_mfma_f32_16x16x32_bf16 a[76:79], v[56:59], v[64:67], a[76:79]\n\t" \
  "s_cmp_lg_u64 vcc, 0\n\t" \
  "s_cselect_b32 s89, s78, s89\n\t" \
  "v_or_b32 v254, s89, v254\n\t" \
  "v_mfma_f32_16x16x32_bf16 a[76:79], v[60:63], v[72:75], a[76:79]\n\t" \
  "buffer_load_dwordx4 v249, s[24:27], s96 offen offset:3072 lds\n\t" \
  "v_mfma_f32_16x16x32_bf16 a[80:83], v[80:83], v[64:67], a[80:83]\n\t" \
  "ds_read_b64_tr_b16 a[128:129], v234 offset:49152\n\t" \
  "v_cvt_pk_bf16_f32 v46, v12, v13\n\t" \
  "v_mfma_f32_16x16x32_bf16 a[80:83], v[84:87], v[72:75], a[80:83]\n\t" \
  "v_cvt_pk_bf16_f32 v106, v108, v109\n\t" \
  "v_exp_f32 v111, v111\n\t" \
  "v_mfma_f32_16x16x32_bf16 a[84:87], v[88:91], v[64:67], a[84:87]\n\t" \
  "ds_read_b64_tr_b16 a[130:131], v234 offset:53248\n\t" \
  "v_cvt_pk_bf16_f32 v47, v14, v15\n\t" \
  "v_mfma_f32_16x16x32_bf16 a[84:87], v[92:95], v[72:75], a[84:87]\n\t" \
  "ds_read_b64_tr_b16 a[132:133], v235 offset:49152\n\t" \
  "v_cvt_pk_bf16_f32 v107, v110, v111\n\t" \
  "v_mfma_f32_16x16x32_bf16 a[88:91], v[112:115], v[64:67], a[88:91]\n\t" \
  "ds_read_b128 a[240:243], v213 offset:32768\n\t" \
  "v_mfma_f32_16x16x32_bf16 a[88:91], v[116:119], v[72:75], a[88:91]\n\t" \
  "s_waitcnt vmcnt(10)\n\t" \
  "buffer_store_dword v243, v254, s[16:19], s49 offen offset:0 sc1\n\t" \
  "ds_read_b64_tr_b16 a[134:135], v235 offset:53248\n\t" \
  "v_mfma_f32_16x16x32_bf16 a[92:95], v[120:123], v[64:67], a[92:95]\n\t" \
  "ds_read_b128 a[244:247], v213 offset:33792\n\t" \
  "v_mfma_f32_16x16x32_bf16 a[92:95], v[124:127], v[72:75], a[92:95]\n\t" \
  "buffer_store_dwordx4 v[40:43], v242, s[16:19], s98 offen offset:2048 sc1\n\t" \
  "v_accvgpr_read_b32 v64, a64\n\t" \
  "v_accvgpr_read_b32 v65, a65\n\t" \
  "v_accvgpr_read_b32 v66, a66\n\t" \
  "v_mfma_f32_16x16x32_bf16 a[96:99], v[16:19], v[96:99], a[96:99]\n\t" \
  "v_accvgpr_read_b32 v67, a67\n\t" \
  "v_accvgpr_read_b32 v68, a68\n\t" \
  "v_mfma_f32_16x16x32_bf16 a[96:99], v[20:23], v[104:107], a[96:99]\n\t" \
  "s_barrier\n\t" \
  "v_mfma_f32_16x16x32_bf16 v[172:175], v[216:219], v[96:99], v[172:175]\n\t" \
  "ds_read_b128 v[128:131], v242 offset:16384\n\t" \
  "v_accvgpr_read_b32 v69, a69\n\t" \
  "v_mfma_f32_16x16x32_bf16 v[172:175], v[216:219], v[104:107], v[172:175]\n\t" \
  "v_accvgpr_read_b32 v70, a70\n\t" \
  "v_accvgpr_read_b32 v71, a71\n\t" \
  "v_mfma_f32_16x16x32_bf16 a[100:103], v[24:27], v[96:99], a[100:103]\n\t" \
  "buffer_store_dwordx4 v[44:47], v242, s[16:19], s98 offen offset:3072 sc1\n\t" \
  "v_mul_f32 v64, v64, v220\n\t" \
  "ds_read_b128 v[132:135], v242 offset:17408\n\t" \
  "v_mul_f32 v65, v65, v220\n\t" \
  "v_mfma_f32_16x16x32_bf16 a[100:103], v[28:31], v[104:107], a[100:103]\n\t" \
  "s_xor_b32 s98, s45, 65536\n\t" \
  "v_rcp_f32 v222, v172\n\t" \
  "v_mfma_f32_16x16x32_bf16 a[104:107], v[48:51], v[96:99], a[104:107]\n\t" \
  "ds_read_b128 v[136:139], v242 offset:18432\n\t" \
  "v_cmp_lt_f32 vcc, 0x5f800000, v172\n\t" \
  "v_mfma_f32_16x16x32_bf16 a[104:107], v[52:55], v[104:107], a[104:107]\n\t" \
  "s_cmp_lg_u64 vcc, 0\n\t" \
  "s_cselect_b32 s89, s78, s89\n\t" \
  "v_or_b32 v254, s89, v254\n\t" \
  "v_mfma_f32_16x16x32_bf16 a[108:111], v[56:59], v[96:99], a[108:111]\n\t" \
  "s_cmp_lg_u32 s89, 0\n\t" \
  "s_cbranch_scc1 .Lat_x\n\t" \
  ".Lat_y:\n\t" \
  "v_mfma_f32_16x16x32_bf16 a[108:111], v[60:63], v[104:107], a[108:111]\n\t" \
  "ds_read_b128 v[140:143], v242 offset:19456\n\t" \
  "v_mul_f32 v66, v66, v220\n\t" \
  "v_mfma_f32_16x16x32_bf16 a[112:115], v[80:83], v[96:99], a[112:115]\n\t" \
  "ds_read_b128 v[144:147], v242 offset:20480\n\t" \
  "v_mul_f32 v67, v67, v220\n\t" \
  "v_mfma_f32_16x16x32_bf16 a[112:115], v[84:87], v[104:107], a[112:115]\n\t" \
  "ds_read_b64_tr_b16 a[136:137], v234 offset:57344\n\t" \
  "v_mul_f32 v68, v68, v220\n\t" \
  "v_mfma_f32_16x16x32_bf16 a[116:119], v[88:91], v[96:99], a[116:119]\n\t" \
  "ds_read_b128 v[148:151], v242 offset:21504\n\t" \
  "v_mul_f32 v69, v69, v220\n\t" \
  "v_mfma_f32_16x16x32_bf16 a[116:119], v[92:95], v[104:107], a[116:119]\n\t" \
  "ds_read_b64_tr_b16 a[138:139], v234 offset:61440\n\t" \
  "v_mul_f32 v70, v70, v220\n\t" \
  "v_mfma_f32_16x16x32_bf16 a[120:123], v[112:115], v[96:99], a[120:123]\n\t" \
  "ds_read_b128 v[152:155], v242 offset:22528\n\t" \
  "v_mul_f32 v71, v71, v220\n\t" \
  "v_mfma_f32_16x16x32_bf16 a[120:123], v[116:119], v[104:107], a[120:123]\n\t" \
  "ds_read_b128 v[156:159], v242 offset:23552\n\t" \
  "v_mfma_f32_16x16x32_bf16 a[124:127], v[120:123], v[96:99], a[124:127]\n\t" \
  "ds_read_b64_tr_b16 a[140:141], v235 offset:57344\n\t" \
  "v_mfma_f32_16x16x32_bf16 a[124:127], v[124:127], v[104:107], a[124:127]\n\t" \
  "s_waitcnt lgkmcnt(7)\n\t" \
  "ds_read_b64_tr_b16 a[142:143], v235 offset:61440\n\t" \
  "ds_read_b128 a[248:251], v213 offset:34816\n\t" \
  ".Lat_join:\n\t" \
  STS("s_memrealtime s[94:95]\n\t") \
  STS("s_memtime s[92:93]\n\t") \
  "v_mfma_f32_16x16x32_bf16 v[0:3], a[192:195], v[128:131], 0\n\t" \
  "ds_read_b128 v[160:163], v242 offset:24576\n\t" \
  "v_cvt_pk_bf16_f32 v112, v64, v65\n\t" \
  "v_cvt_pk_bf16_f32 v113, v66, v67\n\t" \
  "v_cvt_pk_bf16_f32 v114, v68, v69\n\t" \
  "v_mfma_f32_16x16x32_bf16 v[0:3], a[200:203], v[132:135], v[0:3]\n\t" \
  "ds_read_b128 a[252:255], v213 offset:35840\n\t" \
  "v_cvt_pk_bf16_f32 v115, v70, v71\n\t" \
  "v_accvgpr_read_b32 v72, a72\n\t" \
  "v_accvgpr_read_b32 v73, a73\n\t" \
  "v_mfma_f32_16x16x32_bf16 v[0:3], a[208:211], v[136:139], v[0:3]\n\t" \
  "ds_read_b128 v[164:167], v242 offset:25600\n\t" \
  "v_accvgpr_read_b32 v74, a74\n\t" \
  "v_accvgpr_read_b32 v75, a75\n\t" \
  "v_accvgpr_read_b32 v76, a76\n\t" \
  "v_mfma_f32_16x16x32_bf16 v[0:3], a[216:219], v[140:143], v[0:3]\n\t" \
  "v_accvgpr_read_b32 v77, a77\n\t" \
  "v_accvgpr_read_b32 v78, a78\n\t" \
  "v_accvgpr_read_b32 v79, a79\n\t" \
  "v_mul_f32 v72, v72, v220\n\t" \
  "v_mfma_f32_16x16x32_bf16 v[16:19], a[220:223], v[140:143], 0\n\t" \
  "v_mul_f32 v73, v73, v220\n\t" \
  "ds_read_b128 v[168:171], v242 offset:26624\n\t" \
  "ds_write_b128 v213, v[32:35] offset:32768\n\t" \
  "v_mfma_f32_16x16x32_bf16 v[16:19], a[212:215], v[136:139], v[16:19]\n\t" \
  "v_mul_f32 v74, v74, v220\n\t" \
  "v_mul_f32 v75, v75, v220\n\t" \
  "v_mul_f32 v76, v76, v220\n\t" \
  "v_mul_f32 v77, v77, v220\n\t" \
  "v_mfma_f32_16x16x32_bf16 v[16:19], a[204:207], v[132:135], v[16:19]\n\t" \
  "v_mul_f32 v78, v78, v220\n\t" \
  "ds_read_b128 v[172:175], v242 offset:27648\n\t" \
  "ds_write_b128 v213, v[36:39] offset:33792\n\t" \
  "v_mfma_f32_16x16x32_bf16 v[16:19], a[196:199], v[128:131], v[16:19]\n\t" \
  "s_waitcnt lgkmcnt(10)\n\t" \
  "v_mul_f32 v79, v79, v220\n\t" \
  "v_cvt_pk_bf16_f32 v116, v72, v73\n\t" \
  "v_cvt_pk_bf16_f32 v117, v74, v75\n\t" \
  "v_cvt_pk_bf16_f32 v118, v76, v77\n\t" \
  "v_mfma_f32_16x16x32_bf16 v[4:7], a[192:195], v[144:147], 0\n\t" \
  "v_cvt_pk_bf16_f32 v119, v78, v79\n\t" \
  "ds_read_b128 v[128:131], v242 offset:28672\n\t" \
  "ds_read_b128 v[132:135], v242 offset:29696\n\t" \
  "ds_write_b128 v213, v[40:43] offset:34816\n\t" \
  "v_mfma_f32_16x16x32_bf16 v[4:7], a[200:203], v[148:151], v[4:7]\n\t" \
  "v_accvgpr_read_b32 v64, a80\n\t" \
  "ds_read_b128 v[136:139], v242 offset:30720\n\t" \
  "ds_write_b128 v213, v[44:47] offset:35840\n\t" \
  "v_mfma_f32_16x16x32_bf16 v[4:7], a[208:211], v[152:155], v[4:7]\n\t" \
  "ds_read_b64_tr_b16 a[144:145], v236 offset:0\n\t" \
  "v_accvgpr_read_b32 v65, a81\n\t" \
  "v_accvgpr_read_b32 v66, a82\n\t" \
  "v_accvgpr_read_b32 v67, a83\n\t" \
  "v_mfma_f32_16x16x32_bf16 v[4:7], a[216:219], v[156:159], v[4:7]\n\t" \
  "v_accvgpr_read_b32 v68, a84\n\t" \
  "v_accvgpr_read_b32 v69, a85\n\t" \
  "v_accvgpr_read_b32 v70, a86\n\t" \
  "v_accvgpr_read_b32 v71, a87\n\t" \
  "v_mfma_f32_16x16x32_bf16 v[20:23], a[220:223], v[156:159], 0\n\t" \
  STS("v_writelane_b32 v255, s94, 22\n\t") \
  STS("v_writelane_b32 v255, s92, 23\n\t") \
  "buffer_load_dword v246, v245, s[16:19], s65 offen sc1\n\t" \
  "v_mul_f32 v64, v64, v220\n\t" \
  "v_mul_f32 v65, v65, v220\n\t" \
  "v_mul_f32 v66, v66, v220\n\t" \
  "v_mfma_f32_16x16x32_bf16 v[20:23], a[212:215], v[152:155], v[20:23]\n\t" \
  "ds_read_b64_tr_b16 a[146:147], v236 offset:4096\n\t" \
  "v_mul_f32 v67, v67, v220\n\t" \
  "v_mul_f32 v68, v68, v220\n\t" \
  "v_mul_f32 v69, v69, v220\n\t" \
  "v_mfma_f32_16x16x32_bf16 v[20:23], a[204:207], v[148:151], v[20:23]\n\t" \
  "s_waitcnt vmcnt(10) lgkmcnt(2)\n\t" \
  "buffer_store_dwordx4 v[112:115], v242, s[16:19], s98 offen offset:0 sc1\n\t" \
  "v_mul_f32 v70, v70, v220\n\t" \
  "ds_read_b128 v[140:143], v242 offset:31744\n\t" \
  "v_mul_f32 v71, v71, v220\n\t" \
  "v_mfma_f32_16x16x32_bf16 v[20:23], a[196:199], v[144:147], v[20:23]\n\t" \
  "v_cvt_pk_bf16_f32 v120, v64, v65\n\t" \
  "v_cvt_pk_bf16_f32 v121, v66, v67\n\t" \
  "s_barrier\n\t" \
  "v_mfma_f32_16x16x32_bf16 v[8:11], a[192:195], v[160:163], 0\n\t" \
  "ds_read_b128 v[144:147], v242 offset:32768\n\t" \
  "v_cvt_pk_bf16_f32 v122, v68, v69\n\t" \
  "v_cvt_pk_bf16_f32 v123, v70, v71\n\t" \
  "v_accvgpr_read_b32 v72, a88\n\t" \
  "v_mfma_f32_16x16x32_bf16 v[8:11], a[200:203], v[164:167], v[8:11]\n\t" \
  "ds_read_b64_tr_b16 a[148:149], v237 offset:0\n\t" \
  "v_accvgpr_read_b32 v73, a89\n\t" \
  "v_accvgpr_read_b32 v74, a90\n\t" \
  "v_accvgpr_read_b32 v75, a91\n\t" \
  "v_mfma_f32_16x16x32_bf16 v[8:11], a[208:211], v[168:171], v[8:11]\n\t" \
  "buffer_store_dwordx4 v[116:119], v242, s[16:19], s98 offen offset:1024 sc1\n\t" \
  "ds_read_b32 v209, v215 offset:0\n\t" \
  "ds_read_b128 v[148:151], v242 offset:33792\n\t" \
  "v_accvgpr_read_b32 v76, a92\n\t" \
  "v_mfma_f32_16x16x32_bf16 v[8:11], a[216:219], v[172:175], v[8:11]\n\t" \
  "ds_read_b64_tr_b16 a[150:151], v237 offset:4096\n\t" \
  "v_accvgpr_read_b32 v77, a93\n\t" \
  "v_mfma_f32_16x16x32_bf16 v[24:27], a[220:223], v[172:175], 0\n\t" \
  "ds_read_b64_tr_b16 a[152:153], v236 offset:8192\n\t" \
  "ds_read_b128 v[152:155], v242 offset:34816\n\t" \
  "v_accvgpr_read_b32 v78, a94\n\t" \
  "v_mfma_f32_16x16x32_bf16 v[24:27], a[212:215], v[168:171], v[24:27]\n\t" \
  "ds_read_b64_tr_b16 a[154:155], v236 offset:12288\n\t" \
  "v_accvgpr_read_b32 v79, a95\n\t" \
  "v_mfma_f32_16x16x32_bf16 v[24:27], a[204:207], v[164:167], v[24:27]\n\t" \
  "ds_read_b64_tr_b16 a[156:157], v237 offset:8192\n\t" \
  "ds_read_b128 v[156:159], v242 offset:35840\n\t" \
  "v_mul_f32 v72, v72, v220\n\t" \
  "v_mfma_f32_16x16x32_bf16 v[24:27], a[196:199], v[160:163], v[24:27]\n\t" \
  "s_waitcnt lgkmcnt(10)\n\t" \
  "s_waitcnt lgkmcnt(7)\n\t" \
  "v_cmp_eq_u32 vcc, s36, v209\n\t" \
  "v_mfma_f32_16x16x32_bf16 v[12:15], a[192:195], v[128:131], 0\n\t" \
  "s_and_b32 vcc_lo, vcc_lo, s37\n\t" \
  "s_cbranch_scc1 .Lat_rd\n\t" \
  "v_mfma_f32_16x16x32_bf16 v[12:15], a[200:203], v[132:135], v[12:15]\n\t" \
  "ds_read_b128 v[160:163], v242 offset:36864\n\t" \
  "v_mul_f32 v73, v73, v220\n\t" \
  "v_mul_f32 v74, v74, v220\n\t" \
  "v_mfma_f32_16x16x32_bf16 v[12:15], a[208:211], v[136:139], v[12:15]\n\t" \
  "ds_write_b128 v215, v[112:115] offset:0\n\t" \
  "v_mfma_f32_16x16x32_bf16 v[12:15], a[216:219], v[140:143], v[12:15]\n\t" \
  "ds_read_b64_tr_b16 a[158:159], v237 offset:12288\n\t" \
  "ds_read_b128 v[164:167], v242 offset:37888\n\t" \
  "v_mul_f32 v75, v75, v220\n\t" \
  "v_mul_f32 v76, v76, v220\n\t" \
  "v_mfma_f32_16x16x32_bf16 v[28:31], a[220:223], v[140:143], 0\n\t" \
  "ds_write_b128 v215, v[116:119] offset:1024\n\t" \
  "v_mfma_f32_16x16x32_bf16 v[28:31], a[212:215], v[136:139], v[28:31]\n\t" \
  "ds_read_b64_tr_b16 a[160:161], v238 offset:0\n\t" \
  "ds_read_b128 v[168:171], v242 offset:38912\n\t" \
  "v_mul_f32 v77, v77, v220\n\t" \
  "v_mfma_f32_16x16x32_bf16 v[28:31], a[204:207], v[132:135], v[28:31]\n\t" \
  "ds_read_b64_tr_b16 a[162:163], v238 offset:4096\n\t" \
  "ds_read_b128 v[172:175], v242 offset:39936\n\t" \
  "v_mul_f32 v78, v78, v220\n\t" \
  "v_mfma_f32_16x16x32_bf16 v[28:31], a[196:199], v[128:131], v[28:31]\n\t" \
  "s_waitcnt lgkmcnt(9)\n\t" \
  "ds_write_b128 v215, v[120:123] offset:2048\n\t" \
  STP("s_memrealtime s[94:95]\n\t") \
  STP("s_memtime s[92:93]\n\t") \
  "v_mfma_f32_16x16x32_bf16 v[32:35], a[192:195], v[144:147], 0\n\t" \
  "ds_read_b128 v[128:131], v242 offset:40960\n\t" \
  "v_mul_f32 v79, v79, v220\n\t" \
  "v_mfma_f32_16x16x32_bf16 v[32:35], a[200:203], v[148:151], v[32:35]\n\t" \
  "ds_read_b64_tr_b16 a[164:165], v239 offset:0\n\t" \
  "v_cvt_pk_bf16_f32 v124, v72, v73\n\t" \
  "v_mfma_f32_16x16x32_bf16 v[32:35], a[208:211], v[152:155], v[32:35]\n\t" \
  "ds_read_b64_tr_b16 a[166:167], v239 offset:4096\n\t" \
  "ds_read_b128 v[132:135], v242 offset:41984\n\t" \
  "v_cvt_pk_bf16_f32 v125, v74, v75\n\t" \
  "v_mfma_f32_16x16x32_bf16 v[32:35], a[216:219], v[156:159], v[32:35]\n\t" \
  "ds_read_b64_tr_b16 a[168:169], v238 offset:8192\n\t" \
  "ds_read_b128 v[136:139], v242 offset:43008\n\t" \
  "v_cvt_pk_bf16_f32 v126, v76, v77\n\t" \
  "v_mfma_f32_16x16x32_bf16 v[48:51], a[220:223], v[156:159], 0\n\t" \
  "ds_read_b64_tr_b16 a[170:171], v238 offset:12288\n\t" \
  "v_cvt_pk_bf16_f32 v127, v78, v79\n\t" \
  "v_mfma_f32_16x16x32_bf16 v[48:51], a[212:215], v[152:155], v[48:51]\n\t" \
  "ds_write_b128 v215, v[124:127] offset:3072\n\t" \
  "v_mfma_f32_16x16x32_bf16 v[48:51], a[204:207], v[148:151], v[48:51]\n\t" \
  "s_waitcnt vmcnt(3)\n\t" \
  "buffer_store_dword v243, v254, s[16:19], s49 offen offset:32 sc1\n\t" \
  "ds_read_b128 v[140:143], v242 offset:44032\n\t" \
  "v_mfma_f32_16x16x32_bf16 v[48:51], a[196:199], v[144:147], v[48:51]\n\t" \
  "s_waitcnt lgkmcnt(10)\n\t" \
  "ds_read_b64_tr_b16 a[172:173], v239 offset:8192\n\t" \
  "s_add_u32 s97, s64, 131072\n\t" \
  "v_mfma_f32_16x16x32_bf16 v[36:39], a[192:195], v[160:163], 0\n\t" \
  "s_and_b32 s97, s97, 0xe0000\n\t" \
  "ds_read_b128 v[144:147], v242 offset:45056\n\t" \
  "s_add_u32 s97, s97, s60\n\t" \
  "v_mfma_f32_16x16x32_bf16 v[36:39], a[200:203], v[164:167], v[36:39]\n\t" \
  "s_add_u32 m0, s38, 49152\n\t" \
  "s_waitcnt vmcnt(3)\n\t" \
  "v_cmp_ne_u32 vcc, s36, v246\n\t" \
  "v_mfma_f32_16x16x32_bf16 v[36:39], a[208:211], v[168:171], v[36:39]\n\t" \
  "s_and_b32 vcc_lo, vcc_lo, 0xe\n\t" \
  "s_cbranch_scc1 .Lat_s0\n\t" \
  ".Lat_k0:\n\t" \
  "v_mfma_f32_16x16x32_bf16 v[36:39], a[216:219], v[172:175], v[36:39]\n\t" \
  "ds_read_b64_tr_b16 a[174:175], v239 offset:12288\n\t" \
  "ds_read_b128 v[148:151], v242 offset:46080\n\t" \
  "v_mfma_f32_16x16x32_bf16 v[52:55], a[220:223], v[172:175], 0\n\t" \
  STP("v_writelane_b32 v255, s94, 24\n\t") \
  STP("v_writelane_b32 v255, s92, 25\n\t") \
  "buffer_load_dwordx4 v242, s[16:19], s97 offen offset:0 sc1 lds\n\t" \
  "v_mfma_f32_16x16x32_bf16 v[52:55], a[212:215], v[168:171], v[52:55]\n\t" \
  "ds_read_b64_tr_b16 a[176:177], v236 offset:16384\n\t" \
  "ds_read_b128 v[152:155], v242 offset:47104\n\t" \
  "v_mfma_f32_16x16x32_bf16 v[52:55], a[204:207], v[164:167], v[52:55]\n\t" \
  "s_waitcnt lgkmcnt(6)\n\t" \
  "ds_read_b64_tr_b16 a[178:179], v236 offset:20480\n\t" \
  "ds_read_b128 v[156:159], v242 offset:48128\n\t" \
  "v_mfma_f32_16x16x32_bf16 v[52:55], a[196:199], v[160:163], v[52:55]\n\t" \
  "s_barrier\n\t" \
  "v_mfma_f32_16x16x32_bf16 v[40:43], a[192:195], v[128:131], 0\n\t" \
  "buffer_load_dwordx4 v242, s[16:19], s97 offen offset:1024 sc1 lds\n\t" \
  "v_mfma_f32_16x16x32_bf16 v[40:43], a[200:203], v[132:135], v[40:43]\n\t" \
  "ds_read_b128 v[160:163], v230 offset:32768\n\t" \
  "v_mfma_f32_16x16x32_bf16 v[40:43], a[208:211], v[136:139], v[40:43]\n\t" \
  "ds_read_b128 v[164:167], v230 offset:33792\n\t" \
  "v_mfma_f32_16x16x32_bf16 v[40:43], a[216:219], v[140:143], v[40:43]\n\t" \
  "buffer_load_dwordx4 v242, s[16:19], s97 offen offset:2048 sc1 lds\n\t" \
  "v_mfma_f32_16x16x32_bf16 v[56:59], a[220:223], v[140:143], 0\n\t" \
  "ds_read_b128 v[168:171], v230 offset:34816\n\t" \
  "v_mfma_f32_16x16x32_bf16 v[56:59], a[212:215], v[136:139], v[56:59]\n\t" \
  "ds_read_b64_tr_b16 a[180:181], v237 offset:16384\n\t" \
  "v_mfma_f32_16x16x32_bf16 v[56:59], a[204:207], v[132:135], v[56:59]\n\t" \
  "buffer_load_dwordx4 v242, s[16:19], s97 offen offset:3072 sc1 lds\n\t" \
  "v_mfma_f32_16x16x32_bf16 v[56:59], a[196:199], v[128:131], v[56:59]\n\t" \
  "s_waitcnt lgkmcnt(4)\n\t" \
  "s_add_u32 s97, s64, 262144\n\t" \
  "s_and_b32 s97, s97, 0xe0000\n\t" \
  "ds_read_b128 v[172:175], v230 offset:35840\n\t" \
  "s_add_u32 s97, s97, s60\n\t" \
  "v_mfma_f32_16x16x32_bf16 v[44:47], a[192:195], v[144:147], 0\n\t" \
  "s_add_u32 m0, s38, 65536\n\t" \
  "ds_read_b64_tr_b16 a[182:183], v237 offset:20480\n\t" \
  "ds_read_b128 v[128:131], v230 offset:36864\n\t" \
  "ds_read_b128 v[132:135], v230 offset:37888\n\t" \
  "v_mfma_f32_16x16x32_bf16 v[44:47], a[200:203], v[148:151], v[44:47]\n\t" \
  "buffer_load_dwordx4 v242, s[16:19], s97 offen offset:0 sc1 lds\n\t" \
  "v_mfma_f32_16x16x32_bf16 v[44:47], a[208:211], v[152:155], v[44:47]\n\t" \
  "ds_read_b64_tr_b16 a[184:185], v236 offset:24576\n\t" \
  "ds_read_b128 v[136:139], v230 offset:38912\n\t" \
  "v_mfma_f32_16x16x32_bf16 v[44:47], a[216:219], v[156:159], v[44:47]\n\t" \
  "ds_read_b64_tr_b16 a[186:187], v236 offset:28672\n\t" \
  "v_mfma_f32_16x16x32_bf16 v[60:63], a[220:223], v[156:159], 0\n\t" \
  "buffer_load_dwordx4 v242, s[16:19], s97 offen offset:1024 sc1 lds\n\t" \
  "v_mfma_f32_16x16x32_bf16 v[60:63], a[212:215], v[152:155], v[60:63]\n\t" \
  "ds_read_b128 v[140:143], v230 offset:39936\n\t" \
  "v_mfma_f32_16x16x32_bf16 v[60:63], a[204:207], v[148:151], v[60:63]\n\t" \
  "ds_read_b64_tr_b16 a[188:189], v237 offset:24576\n\t" \
  "v_mfma_f32_16x16x32_bf16 v[60:63], a[196:199], v[144:147], v[60:63]\n\t" \
  "s_waitcnt lgkmcnt(8)\n\t" \
  "buffer_load_dwordx4 v242, s[16:19], s97 offen offset:2048 sc1 lds\n\t" \
  STP("s_memrealtime s[94:95]\n\t") \
  STP("s_memtime s[92:93]\n\t") \
  "v_mfma_f32_16x16x32_bf16 v[64:67], a[192:195], v[160:163], 0\n\t" \
  "ds_read_b128 v[144:147], v230 offset:40960\n\t" \
  "v_mfma_f32_16x16x32_bf16 v[64:67], a[200:203], v[164:167], v[64:67]\n\t" \
  "ds_read_b64_tr_b16 a[190:191], v237 offset:28672\n\t" \
  "v_mfma_f32_16x16x32_bf16 v[64:67], a[208:211], v[168:171], v[64:67]\n\t" \
  "buffer_load_dwordx4 v242, s[16:19], s97 offen offset:3072 sc1 lds\n\t" \
  "v_mfma_f32_16x16x32_bf16 v[64:67], a[216:219], v[172:175], v[64:67]\n\t" \
  "s_add_u32 s97, s64, 393216\n\t" \
  "s_and_b32 s97, s97, 0xe0000\n\t" \
  "ds_read_b128 v[148:151], v230 offset:41984\n\t" \
  "s_add_u32 s97, s97, s60\n\t" \
  "v_mfma_f32_16x16x32_bf16 v[80:83], a[220:223], v[172:175], 0\n\t" \
  "s_add_u32 m0, s38, 16384\n\t" \
  "ds_read_b128 v[152:155], v230 offset:43008\n\t" \
  "v_mfma_f32_16x16x32_bf16 v[80:83], a[212:215], v[168:171], v[80:83]\n\t" \
  "buffer_load_dwordx4 v242, s[16:19], s97 offen offset:0 sc1 lds\n\t" \
  "v_mfma_f32_16x16x32_bf16 v[80:83], a[204:207], v[164:167], v[80:83]\n\t" \
  "ds_read_b128 v[156:159], v230 offset:44032\n\t" \
  "v_mfma_f32_16x16x32_bf16 v[80:83], a[196:199], v[160:163], v[80:83]\n\t" \
  "s_waitcnt lgkmcnt(6)\n\t" \
  "ds_read_b64_tr_b16 a[0:1], v236 offset:49152\n\t" \
  "v_mfma_f32_16x16x32_bf16 v[68:71], a[192:195], v[128:131], 0\n\t" \
  "s_mov_b64 exec, 0\n\t" \
  "s_cmp_eq_u32 s3, 0\n\t" \
  "s_cbranch_scc1 .Lhc10\n\t" \
  "s_cmp_eq_u32 s3, 1\n\t" \
  "s_cbranch_scc1 .Lhc11\n\t" \
  "s_cmp_eq_u32 s3, 2\n\t" \
  "s_cbranch_scc1 .Lhc12\n\t" \
  "s_branch .Lhc13\n\t" \
  ".Lhr1:\n\t" \
  "s_mov_b64 exec, -1\n\t" \
  "v_mfma_f32_16x16x32_bf16 v[68:71], a[200:203], v[132:135], v[68:71]\n\t" \
  "buffer_load_dwordx4 v242, s[16:19], s97 offen offset:1024 sc1 lds\n\t" \
  "v_mfma_f32_16x16x32_bf16 v[68:71], a[208:211], v[136:139], v[68:71]\n\t" \
  "ds_read_b64_tr_b16 a[2:3], v236 offset:53248\n\t" \
  "ds_read_b128 v[160:163], v230 offset:45056\n\t" \
  "v_mfma_f32_16x16x32_bf16 v[68:71], a[216:219], v[140:143], v[68:71]\n\t" \
  "buffer_load_dwordx4 v242, s[16:19], s97 offen offset:2048 sc1 lds\n\t" \
  "v_mfma_f32_16x16x32_bf16 v[84:87], a[220:223], v[140:143], 0\n\t" \
  STP("v_writelane_b32 v255, s94, 26\n\t") \
  STP("v_writelane_b32 v255, s92, 27\n\t") \
  "ds_read_b128 v[164:167], v230 offset:46080\n\t" \
  "v_mfma_f32_16x16x32_bf16 v[84:87], a[212:215], v[136:139], v[84:87]\n\t" \
  "ds_read_b128 v[168:171], v230 offset:47104\n\t" \
  "v_mfma_f32_16x16x32_bf16 v[84:87], a[204:207], v[132:135], v[84:87]\n\t" \
  "s_waitcnt vmcnt(7) lgkmcnt(5)\n\t" \
  "buffer_load_dwordx4 v242, s[16:19], s97 offen offset:3072 sc1 lds\n\t" \
  "v_mfma_f32_16x16x32_bf16 v[84:87], a[196:199], v[128:131], v[84:87]\n\t" \
  "s_barrier\n\t" \
  "v_mfma_f32_16x16x32_bf16 v[72:75], a[192:195], v[144:147], 0\n\t" \
  "ds_read_b128 v[172:175], v230 offset:48128\n\t" \
  "v_mfma_f32_16x16x32_bf16 v[72:75], a[200:203], v[148:151], v[72:75]\n\t" \
  "ds_read_b128 v[128:131], v242 offset:49152\n\t" \
  "v_mfma_f32_16x16x32_bf16 v[72:75], a[208:211], v[152:155], v[72:75]\n\t" \
  "ds_read_b64_tr_b16 a[4:5], v237 offset:49152\n\t" \
  "v_mfma_f32_16x16x32_bf16 v[72:75], a[216:219], v[156:159], v[72:75]\n\t" \
  "ds_read_b64_tr_b16 a[6:7], v237 offset:53248\n\t" \
  "ds_read_b128 v[132:135], v242 offset:50176\n\t" \
  "v_mfma_f32_16x16x32_bf16 v[88:91], a[220:223], v[156:159], 0\n\t" \
  "s_add_u32 s96, s40, 1048576\n\t" \
  "ds_read_b64_tr_b16 a[8:9], v236 offset:57344\n\t" \
  "s_and_b32 s96, s96, 0x1c0000\n\t" \
  "v_mfma_f32_16x16x32_bf16 v[88:91], a[212:215], v[152:155], v[88:91]\n\t" \
  "s_add_u32 s96, s96, s77\n\t" \
  "ds_read_b128 v[136:139], v242 offset:51200\n\t" \
  "s_add_u32 m0, s38, 81920\n\t" \
  "v_mfma_f32_16x16x32_bf16 v[88:91], a[204:207], v[148:151], v[88:91]\n\t" \
  "ds_read_b128 v[140:143], v242 offset:52224\n\t" \
  "v_mfma_f32_16x16x32_bf16 v[88:91], a[196:199], v[144:147], v[88:91]\n\t" \
  "s_waitcnt lgkmcnt(7)\n\t" \
  "ds_read_b64_tr_b16 a[10:11], v236 offset:61440\n\t" \
  "v_mfma_f32_16x16x32_bf16 v[76:79], a[192:195], v[160:163], 0\n\t" \
  "buffer_load_dword v246, v245, s[16:19], s65 offen sc1\n\t" \
  "v_mfma_f32_16x16x32_bf16 v[76:79], a[200:203], v[164:167], v[76:79]\n\t" \
  "ds_read_b64_tr_b16 a[12:13], v237 offset:57344\n\t" \
  "ds_read_b128 v[144:147], v242 offset:53248\n\t" \
  "v_mfma_f32_16x16x32_bf16 v[76:79], a[208:211], v[168:171], v[76:79]\n\t" \
  "buffer_load_dwordx4 v240, s[24:27], s96 offen offset:0 lds\n\t" \
  "v_mfma_f32_16x16x32_bf16 v[76:79], a[216:219], v[172:175], v[76:79]\n\t" \
  "ds_read_b64_tr_b16 a[14:15], v237 offset:61440\n\t" \
  "ds_read_b128 v[148:151], v242 offset:54272\n\t" \
  ".Lhs11:\n\ts_cbranch_execz .Lhs11n\n\t" \
  "v_mfma_f32_16x16x32_bf16 v[92:95], a[220:223], v[172:175], 0\n\t" \
  "ds_read_b128 v[152:155], v242 offset:55296\n\t" \
  "v_mfma_f32_16x16x32_bf16 v[92:95], a[212:215], v[168:171], v[92:95]\n\t" \
  "buffer_load_dwordx4 v241, s[24:27], s96 offen offset:1024 lds\n\t" \
  "v_mfma_f32_16x16x32_bf16 v[92:95], a[204:207], v[164:167], v[92:95]\n\t" \
  "ds_read_b128 v[156:159], v242 offset:56320\n\t" \
  "v_mfma_f32_16x16x32_bf16 v[92:95], a[196:199], v[160:163], v[92:95]\n\t" \
  "s_waitcnt lgkmcnt(7)\n\t" \
  "ds_read_b64_tr_b16 a[16:17], v238 offset:16384\n\t" \
  STP("s_memrealtime s[94:95]\n\t") \
  STP("s_memtime s[92:93]\n\t") \
  "v_mfma_f32_16x16x32_bf16 v[0:3], a[128:131], v[128:131], v[0:3]\n\t" \
  "buffer_load_dwordx4 v248, s[24:27], s96 offen offset:2048 lds\n\t" \
  "v_mfma_f32_16x16x32_bf16 v[0:3], a[136:139], v[132:135], v[0:3]\n\t" \
  "ds_read_b64_tr_b16 a[18:19], v238 offset:20480\n\t" \
  "ds_read_b128 v[160:163], v242 offset:57344\n\t" \
  "v_accvgpr_read_b32 v96, a96\n\t" \
  "v_mfma_f32_16x16x32_bf16 v[0:3], a[144:147], v[136:139], v[0:3]\n\t" \
  "ds_read_b64_tr_b16 a[20:21], v239 offset:16384\n\t" \
  "ds_read_b128 v[164:167], v242 offset:58368\n\t" \
  "v_accvgpr_read_b32 v97, a97\n\t" \
  "v_mfma_f32_16x16x32_bf16 v[0:3], a[152:155], v[140:143], v[0:3]\n\t" \
  "buffer_load_dwordx4 v249, s[24:27], s96 offen offset:3072 lds\n\t" \
  "v_mfma_f32_16x16x32_bf16 v[16:19], a[156:159], v[140:143], v[16:19]\n\t" \
  "s_add_u32 s96, s40, 1310720\n\t" \
  "ds_read_b128 v[168:171], v242 offset:59392\n\t" \
  "v_accvgpr_read_b32 v98, a98\n\t" \
  "v_mfma_f32_16x16x32_bf16 v[16:19], a[148:151], v[136:139], v[16:19]\n\t" \
  "s_and_b32 s96, s96, 0x1c0000\n\t" \
  "ds_read_b64_tr_b16 a[22:23], v239 offset:20480\n\t" \
  "v_accvgpr_read_b32 v99, a99\n\t" \
  "v_mfma_f32_16x16x32_bf16 v[16:19], a[140:143], v[132:135], v[16:19]\n\t" \
  "buffer_store_dwordx4 v[120:123], v242, s[16:19], s98 offen offset:2048 sc1\n\t" \
  "ds_read_b64_tr_b16 a[24:25], v238 offset:24576\n\t" \
  "ds_read_b64_tr_b16 a[26:27], v238 offset:28672\n\t" \
  "ds_read_b128 v[172:175], v242 offset:60416\n\t" \
  "v_mfma_f32_16x16x32_bf16 v[16:19], a[132:135], v[128:131], v[16:19]\n\t" \
  "s_waitcnt lgkmcnt(10)\n\t" \
  "ds_read_b64_tr_b16 a[28:29], v239 offset:24576\n\t" \
  "v_accvgpr_read_b32 v100, a100\n\t" \
  "v_mfma_f32_16x16x32_bf16 v[4:7], a[128:131], v[144:147], v[4:7]\n\t" \
  "s_add_u32 s96, s96, s51\n\t" \
  "ds_read_b128 v[128:131], v242 offset:61440\n\t" \
  "s_add_u32 m0, s38, 131072\n\t" \
  "v_mfma_f32_16x16x32_bf16 v[4:7], a[136:139], v[148:151], v[4:7]\n\t" \
  "ds_read_b64_tr_b16 a[30:31], v239 offset:28672\n\t" \
  "v_accvgpr_read_b32 v101, a101\n\t" \
  "v_mfma_f32_16x16x32_bf16 v[4:7], a[144:147], v[152:155], v[4:7]\n\t" \
  "buffer_load_dwordx4 v240, s[24:27], s96 offen offset:0 lds\n\t" \
  "v_mfma_f32_16x16x32_bf16 v[4:7], a[152:155], v[156:159], v[4:7]\n\t" \
  "ds_read_b128 v[132:135], v242 offset:62464\n\t" \
  "v_accvgpr_read_b32 v102, a102\n\t" \
  "v_mfma_f32_16x16x32_bf16 v[20:23], a[156:159], v[156:159], v[20:23]\n\t" \
  STP("v_writelane_b32 v255, s94, 28\n\t") \
  STP("v_writelane_b32 v255, s92, 29\n\t") \
  "ds_read_b128 v[136:139], v242 offset:63488\n\t" \
  "v_accvgpr_read_b32 v103, a103\n\t" \
  "v_mfma_f32_16x16x32_bf16 v[20:23], a[148:151], v[152:155], v[20:23]\n\t" \
  "buffer_load_dwordx4 v241, s[24:27], s96 offen offset:1024 lds\n\t" \
  "v_mfma_f32_16x16x32_bf16 v[20:23], a[140:143], v[148:151], v[20:23]\n\t" \
  "s_waitcnt vmcnt(12) lgkmcnt(5)\n\t" \
  "ds_read_b128 v[140:143], v242 offset:64512\n\t" \
  "v_mul_f32 v96, v96, v222\n\t" \
  "v_mfma_f32_16x16x32_bf16 v[20:23], a[132:135], v[144:147], v[20:23]\n\t" \
  "s_barrier\n\t" \
  "v_mfma_f32_16x16x32_bf16 v[8:11], a[128:131], v[160:163], v[8:11]\n\t" \
  "buffer_load_dwordx4 v248, s[24:27], s96 offen offset:2048 lds\n\t" \
  "v_mfma_f32_16x16x32_bf16 v[8:11], a[136:139], v[164:167], v[8:11]\n\t" \
  "ds_read_b128 v[144:147], v230 offset:0\n\t" \
  "v_mul_f32 v97, v97, v222\n\t" \
  "v_mfma_f32_16x16x32_bf16 v[8:11], a[144:147], v[168:171], v[8:11]\n\t" \
  "ds_read_b64_tr_b16 a[32:33], v234 offset:0\n\t" \
  "v_mul_f32 v98, v98, v222\n\t" \
  "v_mfma_f32_16x16x32_bf16 v[8:11], a[152:155], v[172:175], v[8:11]\n\t" \
  "ds_read_b128 v[148:151], v230 offset:1024\n\t" \
  "v_mul_f32 v99, v99, v222\n\t" \
  "v_mfma_f32_16x16x32_bf16 v[24:27], a[156:159], v[172:175], v[24:27]\n\t" \
  "buffer_load_dwordx4 v249, s[24:27], s96 offen offset:3072 lds\n\t" \
  "v_mfma_f32_16x16x32_bf16 v[24:27], a[148:151], v[168:171], v[24:27]\n\t" \
  "ds_read_b128 v[152:155], v230 offset:2048\n\t" \
  "v_mul_f32 v100, v100, v222\n\t" \
  "v_mfma_f32_16x16x32_bf16 v[24:27], a[140:143], v[164:167], v[24:27]\n\t" \
  "s_add_u32 s97, s64, 131072\n\t" \
  "ds_read_b128 v[156:159], v230 offset:3072\n\t" \
  "v_mul_f32 v101, v101, v222\n\t" \
  "v_mfma_f32_16x16x32_bf16 v[24:27], a[132:135], v[160:163], v[24:27]\n\t" \
  "s_waitcnt lgkmcnt(5)\n\t" \
  "ds_read_b64_tr_b16 a[34:35], v234 offset:4096\n\t" \
  "v_mul_f32 v102, v102, v222\n\t" \
  "v_mfma_f32_16x16x32_bf16 v[12:15], a[128:131], v[128:131], v[12:15]\n\t" \
  "s_and_b32 s97, s97, 0xe0000\n\t" \
  "ds_read_b128 v[160:163], v230 offset:4096\n\t" \
  "v_mul_f32 v103, v103, v222\n\t" \
  "v_mfma_f32_16x16x32_bf16 v[12:15], a[136:139], v[132:135], v[12:15]\n\t" \
  "s_waitcnt vmcnt(9)\n\t" \
  "v_cmp_ne_u32 vcc, s36, v246\n\t" \
  "v_cvt_pk_bf16_f32 v96, v96, v97\n\t" \
  "v_mfma_f32_16x16x32_bf16 v[12:15], a[144:147], v[136:139], v[12:15]\n\t" \
  "s_add_u32 s97, s97, s61\n\t" \
  "ds_read_b64_tr_b16 a[36:37], v235 offset:0\n\t" \
  "s_add_u32 m0, s38, 98304\n\t" \
  "v_mfma_f32_16x16x32_bf16 v[12:15], a[152:155], v[140:143], v[12:15]\n\t" \
  "s_and_b32 vcc_lo, vcc_lo, 0xfefe\n\t" \
  "s_cbranch_scc1 .Lat_s1\n\t" \
  ".Lat_k1:\n\t" \
  "ds_read_b128 v[164:167], v230 offset:5120\n\t" \
  "v_cvt_pk_bf16_f32 v97, v98, v99\n\t" \
  "v_mfma_f32_16x16x32_bf16 v[28:31], a[156:159], v[140:143], v[28:31]\n\t" \
  "buffer_load_dwordx4 v242, s[16:19], s97 offen offset:0 sc1 lds\n\t" \
  "v_mfma_f32_16x16x32_bf16 v[28:31], a[148:151], v[136:139], v[28:31]\n\t" \
  "ds_read_b128 v[168:171], v230 offset:6144\n\t" \
  "v_cvt_pk_bf16_f32 v98, v100, v101\n\t" \
  "v_mfma_f32_16x16x32_bf16 v[28:31], a[140:143], v[132:135], v[28:31]\n\t" \
  "ds_read_b64_tr_b16 a[38:39], v235 offset:4096\n\t" \
  "v_cvt_pk_bf16_f32 v99, v102, v103\n\t" \
  "v_mfma_f32_16x16x32_bf16 v[28:31], a[132:135], v[128:131], v[28:31]\n\t" \
  "s_waitcnt lgkmcnt(6)\n\t" \
  "buffer_load_dwordx4 v242, s[16:19], s97 offen offset:1024 sc1 lds\n\t" \
  STP("s_memrealtime s[94:95]\n\t") \
  STP("s_memtime s[92:93]\n\t") \
  "v_mfma_f32_16x16x32_bf16 v[0:3], a[160:163], v[144:147], v[0:3]\n\t" \
  "ds_write_b128 v213, v[96:99] offset:32768\n\t" \
  "v_mfma_f32_16x16x32_bf16 v[0:3], a[168:171], v[148:151], v[0:3]\n\t" \
  "ds_read_b64_tr_b16 a[40:41], v234 offset:8192\n\t" \
  "ds_read_b128 v[172:175], v230 offset:7168\n\t" \
  "v_accvgpr_read_b32 v104, a104\n\t" \
  "v_mfma_f32_16x16x32_bf16 v[0:3], a[176:179], v[152:155], v[0:3]\n\t" \
  "ds_read_b64_tr_b16 a[42:43], v234 offset:12288\n\t" \
  "ds_read_b128 v[128:131], v230 offset:8192\n\t" \
  "v_accvgpr_read_b32 v105, a105\n\t" \
  "v_mfma_f32_16x16x32_bf16 v[0:3], a[184:187], v[156:159], v[0:3]\n\t" \
  "ds_read_b128 v[132:135], v230 offset:9216\n\t" \
  "v_accvgpr_read_b32 v106, a106\n\t" \
  "v_mfma_f32_16x16x32_bf16 v[16:19], a[188:191], v[156:159], v[16:19]\n\t" \
  "ds_read_b64_tr_b16 a[44:45], v235 offset:8192\n\t" \
  "v_accvgpr_read_b32 v107, a107\n\t" \
  "v_mfma_f32_16x16x32_bf16 v[16:19], a[180:183], v[152:155], v[16:19]\n\t" \
  "ds_read_b128 v[136:139], v230 offset:10240\n\t" \
  "v_accvgpr_read_b32 v108, a108\n\t" \
  "v_mfma_f32_16x16x32_bf16 v[16:19], a[172:175], v[148:151], v[16:19]\n\t" \
  "ds_read_b64_tr_b16 a[46:47], v235 offset:12288\n\t" \
  "v_accvgpr_read_b32 v109, a109\n\t" \
  "v_mfma_f32_16x16x32_bf16 v[16:19], a[164:167], v[144:147], v[16:19]\n\t" \
  "s_waitcnt lgkmcnt(6)\n\t" \
  "ds_read_b128 v[140:143], v230 offset:11264\n\t" \
  "v_accvgpr_read_b32 v110, a110\n\t" \
  "v_mfma_f32_16x16x32_bf16 v[4:7], a[160:163], v[160:163], v[4:7]\n\t" \
  "buffer_load_dwordx4 v242, s[16:19], s97 offen offset:2048 sc1 lds\n\t" \
  "v_mfma_f32_16x16x32_bf16 v[4:7], a[168:171], v[164:167], v[4:7]\n\t" \
  "ds_read_b128 v[144:147], v230 offset:12288\n\t" \
  "v_accvgpr_read_b32 v111, a111\n\t" \
  "v_mfma_f32_16x16x32_bf16 v[4:7], a[176:179], v[168:171], v[4:7]\n\t" \
  "ds_read_b128 v[148:151], v230 offset:13312\n\t" \
  "v_mul_f32 v104, v104, v222\n\t" \
  "v_mfma_f32_16x16x32_bf16 v[4:7], a[184:187], v[172:175], v[4:7]\n\t" \
  "buffer_load_dwordx4 v242, s[16:19], s97 offen offset:3072 sc1 lds\n\t" \
  "v_mfma_f32_16x16x32_bf16 v[20:23], a[188:191], v[172:175], v[20:23]\n\t" \
  STP("v_writelane_b32 v255, s94, 30\n\t") \
  STP("v_writelane_b32 v255, s92, 31\n\t") \
  "ds_read_b128 v[152:155], v230 offset:14336\n\t" \
  "v_mul_f32 v105, v105, v222\n\t" \
  "v_mfma_f32_16x16x32_bf16 v[20:23], a[180:183], v[168:171], v[20:23]\n\t" \
  "v_mul_f32 v106, v106, v222\n\t" \
  "v_mul_f32 v107, v107, v222\n\t" \
  "v_mfma_f32_16x16x32_bf16 v[20:23], a[172:175], v[164:167], v[20:23]\n\t" \
  "s_waitcnt lgkmcnt(3)\n\t" \
  "ds_read_b128 v[156:159], v230 offset:15360\n\t" \
  "v_mul_f32 v108, v108, v222\n\t" \
  "v_mfma_f32_16x16x32_bf16 v[20:23], a[164:167], v[160:163], v[20:23]\n\t" \
  "s_barrier\n\t" \
  "v_mfma_f32_16x16x32_bf16 v[8:11], a[160:163], v[128:131], v[8:11]\n\t" \
  "buffer_store_dwordx4 v[124:127], v242, s[16:19], s98 offen offset:3072 sc1\n\t" \
  "v_mul_f32 v109, v109, v222\n\t" \
  "ds_read_b128 v[160:163], v242 offset:16384\n\t" \
  "v_mul_f32 v110, v110, v222\n\t" \
  "v_mfma_f32_16x16x32_bf16 v[8:11], a[168:171], v[132:135], v[8:11]\n\t" \
  "s_xor_b32 s98, s45, 98304\n\t" \
  "v_mul_f32 v111, v111, v222\n\t" \
  "v_cvt_pk_bf16_f32 v104, v104, v105\n\t" \
  "v_mfma_f32_16x16x32_bf16 v[8:11], a[176:179], v[136:139], v[8:11]\n\t" \
  "s_add_u32 s97, s64, 524288\n\t" \
  "ds_read_b128 v[164:167], v242 offset:17408\n\t" \
  "v_cvt_pk_bf16_f32 v105, v106, v107\n\t" \
  "v_mfma_f32_16x16x32_bf16 v[8:11], a[184:187], v[140:143], v[8:11]\n\t" \
  "s_and_b32 s97, s97, 0xe0000\n\t" \
  "v_cvt_pk_bf16_f32 v106, v108, v109\n\t" \
  "v_cvt_pk_bf16_f32 v107, v110, v111\n\t" \
  "v_mfma_f32_16x16x32_bf16 v[24:27], a[188:191], v[140:143], v[24:27]\n\t" \
  "s_add_u32 s97, s97, s60\n\t" \
  "ds_read_b128 v[168:171], v242 offset:18432\n\t" \
  "s_add_u32 m0, s38, 0\n\t" \
  "v_mfma_f32_16x16x32_bf16 v[24:27], a[180:183], v[136:139], v[24:27]\n\t" \
  "ds_write_b128 v213, v[104:107] offset:33792\n\t" \
  "v_mfma_f32_16x16x32_bf16 v[24:27], a[172:175], v[132:135], v[24:27]\n\t" \
  "buffer_load_dwordx4 v242, s[16:19], s97 offen offset:0 sc1 lds\n\t" \
  "v_mfma_f32_16x16x32_bf16 v[24:27], a[164:167], v[128:131], v[24:27]\n\t" \
  "s_waitcnt lgkmcnt(4)\n\t" \
  "ds_read_b128 v[172:175], v242 offset:19456\n\t" \
  "v_mfma_f32_16x16x32_bf16 v[12:15], a[160:163], v[144:147], v[12:15]\n\t" \
  "ds_read_b128 v[128:131], v242 offset:20480\n\t" \
  "v_mfma_f32_16x16x32_bf16 v[12:15], a[168:171], v[148:151], v[12:15]\n\t" \
  "buffer_load_dwordx4 v242, s[16:19], s97 offen offset:1024 sc1 lds\n\t" \
  "v_mfma_f32_16x16x32_bf16 v[12:15], a[176:179], v[152:155], v[12:15]\n\t" \
  "ds_read_b128 v[132:135], v242 offset:21504\n\t" \
  "v_mfma_f32_16x16x32_bf16 v[12:15], a[184:187], v[156:159], v[12:15]\n\t" \
  "ds_read_b128 v[136:139], v242 offset:22528\n\t" \
  "v_mfma_f32_16x16x32_bf16 v[28:31], a[188:191], v[156:159], v[28:31]\n\t" \
  "ds_read_b128 v[140:143], v242 offset:23552\n\t" \
  "v_mfma_f32_16x16x32_bf16 v[28:31], a[180:183], v[152:155], v[28:31]\n\t" \
  "v_mfma_f32_16x16x32_bf16 v[28:31], a[172:175], v[148:151], v[28:31]\n\t" \
  "v_mfma_f32_16x16x32_bf16 v[28:31], a[164:167], v[144:147], v[28:31]\n\t" \
  "s_waitcnt lgkmcnt(4)\n\t" \
  "v_mfma_f32_16x16x32_bf16 v[0:3], a[0:3], v[160:163], v[0:3]\n\t" \
  "ds_read_b128 v[144:147], v242 offset:24576\n\t" \
  "v_mfma_f32_16x16x32_bf16 v[0:3], a[8:11], v[164:167], v[0:3]\n\t" \
  "ds_read_b128 v[148:151], v242 offset:25600\n\t" \
  "v_mfma_f32_16x16x32_bf16 v[0:3], a[16:19], v[168:171], v[0:3]\n\t" \
  "buffer_load_dwordx4 v242, s[16:19], s97 offen offset:2048 sc1 lds\n\t" \
  "v_mfma_f32_16x16x32_bf16 v[0:3], a[24:27], v[172:175], v[0:3]\n\t" \
  "ds_read_b128 v[152:155], v242 offset:26624\n\t" \
  "v_mfma_f32_16x16x32_bf16 v[16:19], a[28:31], v[172:175], v[16:19]\n\t" \
  "ds_read_b128 v[156:159], v242 offset:27648\n\t" \
  "v_mfma_f32_16x16x32_bf16 v[16:19], a[20:23], v[168:171], v[16:19]\n\t" \
  "buffer_load_dwordx4 v242, s[16:19], s97 offen offset:3072 sc1 lds\n\t" \
  "v_mfma_f32_16x16x32_bf16 v[16:19], a[12:15], v[164:167], v[16:19]\n\t" \
  "v_mfma_f32_16x16x32_bf16 v[16:19], a[4:7], v[160:163], v[16:19]\n\t" \
  "s_waitcnt lgkmcnt(4)\n\t" \
  "v_mfma_f32_16x16x32_bf16 v[4:7], a[0:3], v[128:131], v[4:7]\n\t" \
  "buffer_store_dwordx4 v[96:99], v242, s[16:19], s98 offen offset:0 sc1\n\t" \
  "ds_read_b128 v[160:163], v242 offset:28672\n\t" \
  "ds_read_b128 v[164:167], v242 offset:29696\n\t" \
  "ds_read_b128 v[168:171], v242 offset:30720\n\t" \
  "v_mfma_f32_16x16x32_bf16 v[4:7], a[8:11], v[132:135], v[4:7]\n\t" \
  "s_add_u32 s97, s64, 262144\n\t" \
  "v_accvgpr_read_b32 v96, a112\n\t" \
  "v_accvgpr_read_b32 v97, a113\n\t" \
  "v_mfma_f32_16x16x32_bf16 v[4:7], a[16:19], v[136:139], v[4:7]\n\t" \
  "s_and_b32 s97, s97, 0xe0000\n\t" \
  "v_accvgpr_read_b32 v98, a114\n\t" \
  "v_accvgpr_read_b32 v99, a115\n\t" \
  "v_mfma_f32_16x16x32_bf16 v[4:7], a[24:27], v[140:143], v[4:7]\n\t" \
  "s_add_u32 s97, s97, s61\n\t" \
  "ds_read_b128 v[172:175], v242 offset:31744\n\t" \
  "v_accvgpr_read_b32 v100, a116\n\t" \
  "v_mfma_f32_16x16x32_bf16 v[20:23], a[28:31], v[140:143], v[20:23]\n\t" \
  "s_add_u32 m0, s38, 147456\n\t" \
  "v_accvgpr_read_b32 v101, a117\n\t" \
  "v_accvgpr_read_b32 v102, a118\n\t" \
  "v_mfma_f32_16x16x32_bf16 v[20:23], a[20:23], v[136:139], v[20:23]\n\t" \
  "v_accvgpr_read_b32 v103, a119\n\t" \
  "v_mul_f32 v96, v96, v222\n\t" \
  "v_mfma_f32_16x16x32_bf16 v[20:23], a[12:15], v[132:135], v[20:23]\n\t" \
  "s_waitcnt vmcnt(6) lgkmcnt(4)\n\t" \
  "buffer_load_dwordx4 v242, s[16:19], s97 offen offset:0 sc1 lds\n\t" \
  "v_mfma_f32_16x16x32_bf16 v[20:23], a[4:7], v[128:131], v[20:23]\n\t" \
  "s_barrier\n\t" \
  "v_mfma_f32_16x16x32_bf16 v[8:11], a[0:3], v[144:147], v[8:11]\n\t" \
  "ds_read_b64_tr_b16 a[48:49], v236 offset:16384\n\t" \
  "ds_read_b128 v[128:131], v230 offset:32768\n\t" \
  "v_mul_f32 v97, v97, v222\n\t" \
  "v_mfma_f32_16x16x32_bf16 v[8:11], a[8:11], v[148:151], v[8:11]\n\t" \
  "buffer_load_dwordx4 v242, s[16:19], s97 offen offset:1024 sc1 lds\n\t" \
  "v_mfma_f32_16x16x32_bf16 v[8:11], a[16:19], v[152:155], v[8:11]\n\t" \
  "ds_read_b128 v[132:135], v230 offset:33792\n\t" \
  "v_mul_f32 v98, v98, v222\n\t" \
  "v_mfma_f32_16x16x32_bf16 v[8:11], a[24:27], v[156:159], v[8:11]\n\t" \
  "ds_read_b64_tr_b16 a[50:51], v236 offset:20480\n\t" \
  "ds_read_b128 v[136:139], v230 offset:34816\n\t" \
  "v_mul_f32 v99, v99, v222\n\t" \
  "v_mfma_f32_16x16x32_bf16 v[24:27], a[28:31], v[156:159], v[24:27]\n\t" \
  "buffer_load_dwordx4 v242, s[16:19], s97 offen offset:2048 sc1 lds\n\t" \
  "v_mfma_f32_16x16x32_bf16 v[24:27], a[20:23], v[152:155], v[24:27]\n\t" \
  "ds_read_b128 v[140:143], v230 offset:35840\n\t" \
  "v_mul_f32 v100, v100, v222\n\t" \
  "v_mfma_f32_16x16x32_bf16 v[24:27], a[12:15], v[148:151], v[24:27]\n\t" \
  "ds_read_b64_tr_b16 a[52:53], v237 offset:16384\n\t" \
  "v_mul_f32 v101, v101, v222\n\t" \
  "v_mfma_f32_16x16x32_bf16 v[24:27], a[4:7], v[144:147], v[24:27]\n\t" \
  "s_waitcnt lgkmcnt(7)\n\t" \
  "buffer_load_dwordx4 v242, s[16:19], s97 offen offset:3072 sc1 lds\n\t" \
  "v_mfma_f32_16x16x32_bf16 v[12:15], a[0:3], v[160:163], v[12:15]\n\t" \
  "s_add_u32 s96, s40, 1310720\n\t" \
  "ds_read_b128 v[144:147], v230 offset:36864\n\t" \
  "v_mul_f32 v102, v102, v222\n\t" \
  "v_mfma_f32_16x16x32_bf16 v[12:15], a[8:11], v[164:167], v[12:15]\n\t" \
  "s_and_b32 s96, s96, 0x1c0000\n\t" \
  "ds_read_b128 v[148:151], v230 offset:37888\n\t" \
  "v_mul_f32 v103, v103, v222\n\t" \
  "v_mfma_f32_16x16x32_bf16 v[12:15], a[16:19], v[168:171], v[12:15]\n\t" \
  "buffer_store_dwordx4 v[104:107], v242, s[16:19], s98 offen offset:1024 sc1\n\t" \
  "ds_read_b64_tr_b16 a[54:55], v237 offset:20480\n\t" \
  "ds_read_b128 v[152:155], v230 offset:38912\n\t" \
  "v_cvt_pk_bf16_f32 v96, v96, v97\n\t" \
  "v_mfma_f32_16x16x32_bf16 v[12:15], a[24:27], v[172:175], v[12:15]\n\t" \
  "s_add_u32 s96, s96, s77\n\t" \
  "ds_read_b128 v[156:159], v230 offset:39936\n\t" \
  "v_cvt_pk_bf16_f32 v97, v98, v99\n\t" \
  "v_mfma_f32_16x16x32_bf16 v[28:31], a[28:31], v[172:175], v[28:31]\n\t" \
  "s_add_u32 m0, s38, 65536\n\t" \
  "ds_read_b64_tr_b16 a[56:57], v236 offset:24576\n\t" \
  "v_cvt_pk_bf16_f32 v98, v100, v101\n\t" \
  "v_mfma_f32_16x16x32_bf16 v[28:31], a[20:23], v[168:171], v[28:31]\n\t" \
  "v_cvt_pk_bf16_f32 v99, v102, v103\n\t" \
  "v_accvgpr_read_b32 v104, a120\n\t" \
  "v_mfma_f32_16x16x32_bf16 v[28:31], a[12:15], v[164:167], v[28:31]\n\t" \
  "buffer_load_dwordx4 v240, s[24:27], s96 offen offset:0 lds\n\t" \
  "v_mfma_f32_16x16x32_bf16 v[28:31], a[4:7], v[160:163], v[28:31]\n\t" \
  "s_waitcnt lgkmcnt(7)\n\t" \
  "ds_write_b128 v213, v[96:99] offset:34816\n\t" \
  "v_mfma_f32_16x16x32_bf16 v[32:35], a[128:131], v[128:131], v[32:35]\n\t" \
  "ds_read_b128 v[160:163], v230 offset:40960\n\t" \
  "v_accvgpr_read_b32 v105, a121\n\t" \
  "v_mfma_f32_16x16x32_bf16 v[32:35], a[136:139], v[132:135], v[32:35]\n\t" \
  "buffer_load_dwordx4 v241, s[24:27], s96 offen offset:1024 lds\n\t" \
  "v_mfma_f32_16x16x32_bf16 v[32:35], a[144:147], v[136:139], v[32:35]\n\t" \
  "ds_read_b128 v[164:167], v230 offset:41984\n\t" \
  "v_accvgpr_read_b32 v106, a122\n\t" \
  "v_mfma_f32_16x16x32_bf16 v[32:35], a[152:155], v[140:143], v[32:35]\n\t" \
  "ds_read_b64_tr_b16 a[58:59], v236 offset:28672\n\t" \
  "v_accvgpr_read_b32 v107, a123\n\t" \
  "v_mfma_f32_16x16x32_bf16 v[48:51], a[156:159], v[140:143], v[48:51]\n\t" \
  "buffer_load_dwordx4 v248, s[24:27], s96 offen offset:2048 lds\n\t" \
  "v_mfma_f32_16x16x32_bf16 v[48:51], a[148:151], v[136:139], v[48:51]\n\t" \
  "ds_read_b128 v[168:171], v230 offset:43008\n\t" \
  "v_accvgpr_read_b32 v108, a124\n\t" \
  "v_mfma_f32_16x16x32_bf16 v[48:51], a[140:143], v[132:135], v[48:51]\n\t" \
  "ds_read_b64_tr_b16 a[60:61], v237 offset:24576\n\t" \
  "v_accvgpr_read_b32 v109, a125\n\t" \
  "v_mfma_f32_16x16x32_bf16 v[48:51], a[132:135], v[128:131], v[48:51]\n\t" \
  "s_waitcnt lgkmcnt(7)\n\t" \
  "ds_read_b128 v[172:175], v230 offset:44032\n\t" \
  "v_accvgpr_read_b32 v110, a126\n\t" \
  "v_mfma_f32_16x16x32_bf16 v[36:39], a[128:131], v[144:147], v[36:39]\n\t" \
  "buffer_load_dwordx4 v249, s[24:27], s96 offen offset:3072 lds\n\t" \
  "v_mfma_f32_16x16x32_bf16 v[36:39], a[136:139], v[148:151], v[36:39]\n\t" \
  "ds_read_b64_tr_b16 a[62:63], v237 offset:28672\n\t" \
  "v_accvgpr_read_b32 v111, a127\n\t" \
  "v_mfma_f32_16x16x32_bf16 v[36:39], a[144:147], v[152:155], v[36:39]\n\t" \
  "ds_read_b128 v[128:131], v230 offset:45056\n\t" \
  "v_mul_f32 v104, v104, v222\n\t" \
  "v_mfma_f32_16x16x32_bf16 v[36:39], a[152:155], v[156:159], v[36:39]\n\t" \
  "buffer_store_dwordx4 v[96:99], v242, s[16:19], s98 offen offset:2048 sc1\n\t" \
  "ds_read_b64_tr_b16 a[64:65], v238 offset:0\n\t" \
  "ds_read_b64_tr_b16 a[66:67], v238 offset:4096\n\t" \
  "ds_read_b128 v[132:135], v230 offset:46080\n\t" \
  "v_mfma_f32_16x16x32_bf16 v[52:55], a[156:159], v[156:159], v[52:55]\n\t" \
  "ds_read_b128 v[136:139], v230 offset:47104\n\t" \
  "v_mul_f32 v105, v105, v222\n\t" \
  "v_mfma_f32_16x16x32_bf16 v[52:55], a[148:151], v[152:155], v[52:55]\n\t" \
  "ds_read_b64_tr_b16 a[68:69], v239 offset:0\n\t" \
  "v_mul_f32 v106, v106, v222\n\t" \
  "v_mfma_f32_16x16x32_bf16 v[52:55], a[140:143], v[148:151], v[52:55]\n\t" \
  "s_waitcnt vmcnt(11) lgkmcnt(6)\n\t" \
  "ds_read_b128 v[140:143], v230 offset:48128\n\t" \
  "v_mul_f32 v107, v107, v222\n\t" \
  "v_mfma_f32_16x16x32_bf16 v[52:55], a[132:135], v[144:147], v[52:55]\n\t" \
  "s_barrier\n\t" \
  "v_mfma_f32_16x16x32_bf16 v[40:43], a[128:131], v[160:163], v[40:43]\n\t" \
  "ds_read_b128 v[144:147], v242 offset:0\n\t" \
  "v_mul_f32 v108, v108, v222\n\t" \
  "v_mfma_f32_16x16x32_bf16 v[40:43], a[136:139], v[164:167], v[40:43]\n\t" \
  "s_add_u32 s96, s40, 1572864\n\t" \
  "ds_read_b64_tr_b16 a[70:71], v239 offset:4096\n\t" \
  "v_mul_f32 v109, v109, v222\n\t" \
  "v_mfma_f32_16x16x32_bf16 v[40:43], a[144:147], v[168:171], v[40:43]\n\t" \
  "s_and_b32 s96, s96, 0x1c0000\n\t" \
  "ds_read_b128 v[148:151], v242 offset:1024\n\t" \
  "v_mul_f32 v110, v110, v222\n\t" \
  "v_mfma_f32_16x16x32_bf16 v[40:43], a[152:155], v[172:175], v[40:43]\n\t" \
  "s_add_u32 s96, s96, s51\n\t" \
  "ds_read_b64_tr_b16 a[72:73], v238 offset:8192\n\t" \
  "v_mul_f32 v111, v111, v222\n\t" \
  "v_mfma_f32_16x16x32_bf16 v[56:59], a[156:159], v[172:175], v[56:59]\n\t" \
  "s_add_u32 m0, s38, 81920\n\t" \
  "ds_read_b128 v[152:155], v242 offset:2048\n\t" \
  "v_cvt_pk_bf16_f32 v104, v104, v105\n\t" \
  "v_mfma_f32_16x16x32_bf16 v[56:59], a[148:151], v[168:171], v[56:59]\n\t" \
  "ds_read_b64_tr_b16 a[74:75], v238 offset:12288\n\t" \
  "v_cvt_pk_bf16_f32 v105, v106, v107\n\t" \
  "v_mfma_f32_16x16x32_bf16 v[56:59], a[140:143], v[164:167], v[56:59]\n\t" \
  "ds_read_b128 v[156:159], v242 offset:3072\n\t" \
  "v_cvt_pk_bf16_f32 v106, v108, v109\n\t" \
  "v_mfma_f32_16x16x32_bf16 v[56:59], a[132:135], v[160:163], v[56:59]\n\t" \
  "s_waitcnt lgkmcnt(7)\n\t" \
  "buffer_load_dwordx4 v240, s[24:27], s96 offen offset:0 lds\n\t" \
  "v_mfma_f32_16x16x32_bf16 v[44:47], a[128:131], v[128:131], v[44:47]\n\t" \
  "ds_read_b128 v[160:163], v242 offset:4096\n\t" \
  "v_cvt_pk_bf16_f32 v107, v110, v111\n\t" \
  "v_mfma_f32_16x16x32_bf16 v[44:47], a[136:139], v[132:135], v[44:47]\n\t" \
  "ds_write_b128 v213, v[104:107] offset:35840\n\t" \
  "v_mfma_f32_16x16x32_bf16 v[44:47], a[144:147], v[136:139], v[44:47]\n\t" \
  "buffer_load_dwordx4 v241, s[24:27], s96 offen offset:1024 lds\n\t" \
  "v_mfma_f32_16x16x32_bf16 v[44:47], a[152:155], v[140:143], v[44:47]\n\t" \
  "ds_read_b128 v[164:167], v242 offset:5120\n\t" \
  "v_mfma_f32_16x16x32_bf16 v[60:63], a[156:159], v[140:143], v[60:63]\n\t" \
  "ds_read_b128 v[168:171], v242 offset:6144\n\t" \
  "v_mfma_f32_16x16x32_bf16 v[60:63], a[148:151], v[136:139], v[60:63]\n\t" \
  "ds_read_b64_tr_b16 a[76:77], v239 offset:8192\n\t" \
  "v_mfma_f32_16x16x32_bf16 v[60:63], a[140:143], v[132:135], v[60:63]\n\t" \
  "buffer_load_dwordx4 v248, s[24:27], s96 offen offset:2048 lds\n\t" \
  "v_mfma_f32_16x16x32_bf16 v[60:63], a[132:135], v[128:131], v[60:63]\n\t" \
  "s_waitcnt lgkmcnt(5)\n\t" \
  "ds_read_b128 v[172:175], v242 offset:7168\n\t" \
  "v_mfma_f32_16x16x32_bf16 v[0:3], a[32:35], v[144:147], v[0:3]\n\t" \
  "ds_read_b64_tr_b16 a[78:79], v239 offset:12288\n\t" \
  "v_mfma_f32_16x16x32_bf16 v[0:3], a[40:43], v[148:151], v[0:3]\n\t" \
  "buffer_load_dwordx4 v249, s[24:27], s96 offen offset:3072 lds\n\t" \
  "v_mfma_f32_16x16x32_bf16 v[0:3], a[48:51], v[152:155], v[0:3]\n\t" \
  "ds_read_b128 v[128:131], v242 offset:8192\n\t" \
  "v_mfma_f32_16x16x32_bf16 v[0:3], a[56:59], v[156:159], v[0:3]\n\t" \
  "ds_read_b128 v[132:135], v242 offset:9216\n\t" \
  "v_mfma_f32_16x16x32_bf16 v[16:19], a[60:63], v[156:159], v[16:19]\n\t" \
  "ds_read_b128 v[136:139], v242 offset:10240\n\t" \
  "v_mfma_f32_16x16x32_bf16 v[16:19], a[52:55], v[152:155], v[16:19]\n\t" \
  "buffer_store_dwordx4 v[104:107], v242, s[16:19], s98 offen offset:3072 sc1\n\t" \
  "ds_read_b128 v[140:143], v242 offset:11264\n\t" \
  "v_mfma_f32_16x16x32_bf16 v[16:19], a[44:47], v[148:151], v[16:19]\n\t" \
  "v_mfma_f32_16x16x32_bf16 v[16:19], a[36:39], v[144:147], v[16:19]\n\t" \
  "s_waitcnt lgkmcnt(5)\n\t" \
  "v_mfma_f32_16x16x32_bf16 v[4:7], a[32:35], v[160:163], v[4:7]\n\t" \
  "ds_read_b128 v[144:147], v242 offset:12288\n\t" \
  "v_mfma_f32_16x16x32_bf16 v[4:7], a[40:43], v[164:167], v[4:7]\n\t" \
  "ds_read_b128 v[148:151], v242 offset:13312\n\t" \
  "v_mfma_f32_16x16x32_bf16 v[4:7], a[48:51], v[168:171], v[4:7]\n\t" \
  "ds_read_b128 v[152:155], v242 offset:14336\n\t" \
  "v_mfma_f32_16x16x32_bf16 v[4:7], a[56:59], v[172:175], v[4:7]\n\t" \
  "s_add_u32 s97, s64, 655360\n\t" \
  "ds_read_b128 v[156:159], v242 offset:15360\n\t" \
  "s_and_b32 s97, s97, 0xe0000\n\t" \
  "v_mfma_f32_16x16x32_bf16 v[20:23], a[60:63], v[172:175], v[20:23]\n\t" \
  "s_add_u32 s97, s97, s60\n\t" \
  "s_add_u32 m0, s38, 131072\n\t" \
  "v_mfma_f32_16x16x32_bf16 v[20:23], a[52:55], v[168:171], v[20:23]\n\t" \
  "v_mfma_f32_16x16x32_bf16 v[20:23], a[44:47], v[164:167], v[20:23]\n\t" \
  "s_waitcnt vmcnt(11) lgkmcnt(4)\n\t" \
  "v_mfma_f32_16x16x32_bf16 v[20:23], a[36:39], v[160:163], v[20:23]\n\t" \
  "s_barrier\n\t" \
  "v_mfma_f32_16x16x32_bf16 v[8:11], a[32:35], v[128:131], v[8:11]\n\t" \
  "ds_read_b128 v[160:163], v232 offset:16384\n\t" \
  "v_mfma_f32_16x16x32_bf16 v[8:11], a[40:43], v[132:135], v[8:11]\n\t" \
  "buffer_load_dwordx4 v242, s[16:19], s97 offen offset:0 sc1 lds\n\t" \
  "v_mfma_f32_16x16x32_bf16 v[8:11], a[48:51], v[136:139], v[8:11]\n\t" \
  "ds_read_b128 v[164:167], v232 offset:17408\n\t" \
  "v_mfma_f32_16x16x32_bf16 v[8:11], a[56:59], v[140:143], v[8:11]\n\t" \
  "ds_read_b128 v[168:171], v232 offset:18432\n\t" \
  "v_mfma_f32_16x16x32_bf16 v[24:27], a[60:63], v[140:143], v[24:27]\n\t" \
  "ds_read_b128 v[172:175], v232 offset:19456\n\t" \
  "v_mfma_f32_16x16x32_bf16 v[24:27], a[52:55], v[136:139], v[24:27]\n\t" \
  "v_mfma_f32_16x16x32_bf16 v[24:27], a[44:47], v[132:135], v[24:27]\n\t" \
  "buffer_load_dwordx4 v242, s[16:19], s97 offen offset:1024 sc1 lds\n\t" \
  "v_mfma_f32_16x16x32_bf16 v[24:27], a[36:39], v[128:131], v[24:27]\n\t" \
  "s_waitcnt lgkmcnt(4)\n\t" \
  "v_mfma_f32_16x16x32_bf16 v[12:15], a[32:35], v[144:147], v[12:15]\n\t" \
  "ds_read_b128 v[128:131], v232 offset:20480\n\t" \
  "v_mfma_f32_16x16x32_bf16 v[12:15], a[40:43], v[148:151], v[12:15]\n\t" \
  "buffer_load_dwordx4 v242, s[16:19], s97 offen offset:2048 sc1 lds\n\t" \
  "v_mfma_f32_16x16x32_bf16 v[12:15], a[48:51], v[152:155], v[12:15]\n\t" \
  "ds_read_b128 v[132:135], v232 offset:21504\n\t" \
  "v_mfma_f32_16x16x32_bf16 v[12:15], a[56:59], v[156:159], v[12:15]\n\t" \
  "ds_read_b128 v[136:139], v232 offset:22528\n\t" \
  "v_mfma_f32_16x16x32_bf16 v[28:31], a[60:63], v[156:159], v[28:31]\n\t" \
  "buffer_load_dwordx4 v242, s[16:19], s97 offen offset:3072 sc1 lds\n\t" \
  "v_mfma_f32_16x16x32_bf16 v[28:31], a[52:55], v[152:155], v[28:31]\n\t" \
  "s_add_u32 s96, s40, 1572864\n\t" \
  "ds_read_b128 v[140:143], v232 offset:23552\n\t" \
  "s_and_b32 s96, s96, 0x1c0000\n\t" \
  "v_mfma_f32_16x16x32_bf16 v[28:31], a[44:47], v[148:151], v[28:31]\n\t" \
  "s_add_u32 s96, s96, s77\n\t" \
  "s_add_u32 m0, s38, 16384\n\t" \
  "v_mfma_f32_16x16x32_bf16 v[28:31], a[36:39], v[144:147], v[28:31]\n\t" \
  "s_waitcnt lgkmcnt(4)\n\t" \
  "v_mfma_f32_16x16x32_bf16 v[32:35], a[160:163], v[160:163], v[32:35]\n\t" \
  "buffer_load_dwordx4 v240, s[24:27], s96 offen offset:0 lds\n\t" \
  "v_mfma_f32_16x16x32_bf16 v[32:35], a[168:171], v[164:167], v[32:35]\n\t" \
  "ds_read_b128 v[144:147], v232 offset:24576\n\t" \
  "v_mfma_f32_16x16x32_bf16 v[32:35], a[176:179], v[168:171], v[32:35]\n\t" \
  "ds_read_b128 v[148:151], v232 offset:25600\n\t" \
  "v_mfma_f32_16x16x32_bf16 v[32:35], a[184:187], v[172:175], v[32:35]\n\t" \
  "ds_read_b128 v[152:155], v232 offset:26624\n\t" \
  "v_mfma_f32_16x16x32_bf16 v[48:51], a[188:191], v[172:175], v[48:51]\n\t" \
  "buffer_load_dwordx4 v241, s[24:27], s96 offen offset:1024 lds\n\t" \
  "v_mfma_f32_16x16x32_bf16 v[48:51], a[180:183], v[168:171], v[48:51]\n\t" \
  "ds_read_b128 v[156:159], v232 offset:27648\n\t" \
  "v_mfma_f32_16x16x32_bf16 v[48:51], a[172:175], v[164:167], v[48:51]\n\t" \
  "v_mfma_f32_16x16x32_bf16 v[48:51], a[164:167], v[160:163], v[48:51]\n\t" \
  "s_waitcnt lgkmcnt(4)\n\t" \
  "v_mfma_f32_16x16x32_bf16 v[36:39], a[160:163], v[128:131], v[36:39]\n\t" \
  "ds_read_b128 v[160:163], v232 offset:28672\n\t" \
  "v_mfma_f32_16x16x32_bf16 v[36:39], a[168:171], v[132:135], v[36:39]\n\t" \
  "ds_read_b128 v[164:167], v232 offset:29696\n\t" \
  "v_mfma_f32_16x16x32_bf16 v[36:39], a[176:179], v[136:139], v[36:39]\n\t" \
  "ds_read_b128 v[168:171], v232 offset:30720\n\t" \
  "v_mfma_f32_16x16x32_bf16 v[36:39], a[184:187], v[140:143], v[36:39]\n\t" \
  "ds_read_b128 v[172:175], v232 offset:31744\n\t" \
  "v_mfma_f32_16x16x32_bf16 v[52:55], a[188:191], v[140:143], v[52:55]\n\t" \
  "v_mfma_f32_16x16x32_bf16 v[52:55], a[180:183], v[136:139], v[52:55]\n\t" \
  "v_mfma_f32_16x16x32_bf16 v[52:55], a[172:175], v[132:135], v[52:55]\n\t" \
  "s_waitcnt vmcnt(12) lgkmcnt(4)\n\t" \
  "v_mfma_f32_16x16x32_bf16 v[52:55], a[164:167], v[128:131], v[52:55]\n\t" \
  "s_barrier\n\t" \
  "v_mfma_f32_16x16x32_bf16 v[40:43], a[160:163], v[144:147], v[40:43]\n\t" \
  "buffer_load_dwordx4 v248, s[24:27], s96 offen offset:2048 lds\n\t" \
  "v_mfma_f32_16x16x32_bf16 v[40:43], a[168:171], v[148:151], v[40:43]\n\t" \
  "ds_read_b128 v[128:131], v242 offset:32768\n\t" \
  "v_mfma_f32_16x16x32_bf16 v[40:43], a[176:179], v[152:155], v[40:43]\n\t" \
  "ds_read_b128 v[132:135], v242 offset:33792\n\t" \
  "v_mfma_f32_16x16x32_bf16 v[40:43], a[184:187], v[156:159], v[40:43]\n\t" \
  "ds_read_b64_tr_b16 a[80:81], v236 offset:0\n\t" \
  "v_mfma_f32_16x16x32_bf16 v[56:59], a[188:191], v[156:159], v[56:59]\n\t" \
  "buffer_load_dwordx4 v249, s[24:27], s96 offen offset:3072 lds\n\t" \
  "v_mfma_f32_16x16x32_bf16 v[56:59], a[180:183], v[152:155], v[56:59]\n\t" \
  "s_add_u32 s97, s64, 393216\n\t" \
  "ds_read_b128 v[136:139], v242 offset:34816\n\t" \
  "s_and_b32 s97, s97, 0xe0000\n\t" \
  "v_mfma_f32_16x16x32_bf16 v[56:59], a[172:175], v[148:151], v[56:59]\n\t" \
  "s_add_u32 s97, s97, s61\n\t" \
  "ds_read_b128 v[140:143], v242 offset:35840\n\t" \
  "s_add_u32 m0, s38, 0\n\t" \
  "v_mfma_f32_16x16x32_bf16 v[56:59], a[164:167], v[144:147], v[56:59]\n\t" \
  "s_waitcnt lgkmcnt(5)\n\t" \
  "ds_read_b64_tr_b16 a[82:83], v236 offset:4096\n\t" \
  "v_mfma_f32_16x16x32_bf16 v[44:47], a[160:163], v[160:163], v[44:47]\n\t" \
  "buffer_load_dwordx4 v242, s[16:19], s97 offen offset:0 sc1 lds\n\t" \
  "v_mfma_f32_16x16x32_bf16 v[44:47], a[168:171], v[164:167], v[44:47]\n\t" \
  "ds_read_b64_tr_b16 a[84:85], v237 offset:0\n\t" \
  "ds_read_b128 v[144:147], v242 offset:36864\n\t" \
  "v_mfma_f32_16x16x32_bf16 v[44:47], a[176:179], v[168:171], v[44:47]\n\t" \
  "ds_read_b64_tr_b16 a[86:87], v237 offset:4096\n\t" \
  "ds_read_b128 v[148:151], v242 offset:37888\n\t" \
  "v_mfma_f32_16x16x32_bf16 v[44:47], a[184:187], v[172:175], v[44:47]\n\t" \
  "buffer_load_dwordx4 v242, s[16:19], s97 offen offset:1024 sc1 lds\n\t" \
  "v_mfma_f32_16x16x32_bf16 v[60:63], a[188:191], v[172:175], v[60:63]\n\t" \
  "ds_read_b128 v[152:155], v242 offset:38912\n\t" \
  "v_mfma_f32_16x16x32_bf16 v[60:63], a[180:183], v[168:171], v[60:63]\n\t" \
  "ds_read_b64_tr_b16 a[88:89], v236 offset:8192\n\t" \
  "v_mfma_f32_16x16x32_bf16 v[60:63], a[172:175], v[164:167], v[60:63]\n\t" \
  "ds_read_b128 v[156:159], v242 offset:39936\n\t" \
  "v_mfma_f32_16x16x32_bf16 v[60:63], a[164:167], v[160:163], v[60:63]\n\t" \
  "s_waitcnt lgkmcnt(8)\n\t" \
  "ds_read_b64_tr_b16 a[90:91], v236 offset:12288\n\t" \
  "v_mfma_f32_16x16x32_bf16 v[96:99], a[192:195], v[128:131], 0\n\t" \
  "buffer_load_dwordx4 v242, s[16:19], s97 offen offset:2048 sc1 lds\n\t" \
  "v_mfma_f32_16x16x32_bf16 v[96:99], a[200:203], v[132:135], v[96:99]\n\t" \
  "ds_read_b128 v[160:163], v242 offset:40960\n\t" \
  "v_mfma_f32_16x16x32_bf16 v[96:99], a[208:211], v[136:139], v[96:99]\n\t" \
  "ds_read_b128 v[164:167], v242 offset:41984\n\t" \
  "v_mfma_f32_16x16x32_bf16 v[96:99], a[216:219], v[140:143], v[96:99]\n\t" \
  "buffer_load_dwordx4 v242, s[16:19], s97 offen offset:3072 sc1 lds\n\t" \
  "v_mfma_f32_16x16x32_bf16 v[112:115], a[220:223], v[140:143], 0\n\t" \
  "s_add_u32 s96, s40, 1835008\n\t" \
  "ds_read_b128 v[168:171], v242 offset:43008\n\t" \
  "s_and_b32 s96, s96, 0x1c0000\n\t" \
  "v_mfma_f32_16x16x32_bf16 v[112:115], a[212:215], v[136:139], v[112:115]\n\t" \
  "s_add_u32 s96, s96, s51\n\t" \
  "ds_read_b64_tr_b16 a[92:93], v237 offset:8192\n\t" \
  "s_add_u32 m0, s38, 49152\n\t" \
  "v_mfma_f32_16x16x32_bf16 v[112:115], a[204:207], v[132:135], v[112:115]\n\t" \
  "s_waitcnt vmcnt(12)\n\t" \
  "buffer_store_dword v243, v254, s[16:19], s49 offen offset:64 sc1\n\t" \
  "ds_read_b128 v[172:175], v242 offset:44032\n\t" \
  "v_mfma_f32_16x16x32_bf16 v[112:115], a[196:199], v[128:131], v[112:115]\n\t" \
  "s_waitcnt lgkmcnt(6)\n\t" \
  "ds_read_b64_tr_b16 a[94:95], v237 offset:12288\n\t" \
  ".Lhs12:\n\ts_cbranch_execz .Lhs12n\n\t" \
  "v_mfma_f32_16x16x32_bf16 v[100:103], a[192:195], v[144:147], 0\n\t" \
  "buffer_load_dwordx4 v240, s[24:27], s96 offen offset:0 lds\n\t" \
  "v_mfma_f32_16x16x32_bf16 v[100:103], a[200:203], v[148:151], v[100:103]\n\t" \
  "ds_read_b128 v[128:131], v242 offset:45056\n\t" \
  "v_mfma_f32_16x16x32_bf16 v[100:103], a[208:211], v[152:155], v[100:103]\n\t" \
  "ds_read_b128 v[132:135], v242 offset:46080\n\t" \
  "v_mfma_f32_16x16x32_bf16 v[100:103], a[216:219], v[156:159], v[100:103]\n\t" \
  "buffer_load_dwordx4 v241, s[24:27], s96 offen offset:1024 lds\n\t" \
  "v_mfma_f32_16x16x32_bf16 v[116:119], a[220:223], v[156:159], 0\n\t" \
  "ds_read_b128 v[136:139], v242 offset:47104\n\t" \
  "v_mfma_f32_16x16x32_bf16 v[116:119], a[212:215], v[152:155], v[116:119]\n\t" \
  "ds_read_b128 v[140:143], v242 offset:48128\n\t" \
  "v_mfma_f32_16x16x32_bf16 v[116:119], a[204:207], v[148:151], v[116:119]\n\t" \
  "s_waitcnt vmcnt(11) lgkmcnt(4)\n\t" \
  "v_mfma_f32_16x16x32_bf16 v[116:119], a[196:199], v[144:147], v[116:119]\n\t" \
  "s_barrier\n\t" \
  "v_mfma_f32_16x16x32_bf16 v[104:107], a[192:195], v[160:163], 0\n\t" \
  "ds_read_b128 v[144:147], v232 offset:0\n\t" \
  "v_mfma_f32_16x16x32_bf16 v[104:107], a[200:203], v[164:167], v[104:107]\n\t" \
  "ds_read_b64_tr_b16 a[96:97], v236 offset:16384\n\t" \
  "v_mfma_f32_16x16x32_bf16 v[104:107], a[208:211], v[168:171], v[104:107]\n\t" \
  "ds_read_b128 v[148:151], v232 offset:1024\n\t" \
  "v_mfma_f32_16x16x32_bf16 v[104:107], a[216:219], v[172:175], v[104:107]\n\t" \
  "ds_read_b64_tr_b16 a[98:99], v236 offset:20480\n\t" \
  "v_mfma_f32_16x16x32_bf16 v[120:123], a[220:223], v[172:175], 0\n\t" \
  "ds_read_b128 v[152:155], v232 offset:2048\n\t" \
  "v_mfma_f32_16x16x32_bf16 v[120:123], a[212:215], v[168:171], v[120:123]\n\t" \
  "ds_read_b128 v[156:159], v232 offset:3072\n\t" \
  "v_mfma_f32_16x16x32_bf16 v[120:123], a[204:207], v[164:167], v[120:123]\n\t" \
  "ds_read_b64_tr_b16 a[100:101], v237 offset:16384\n\t" \
  "v_mfma_f32_16x16x32_bf16 v[120:123], a[196:199], v[160:163], v[120:123]\n\t" \
  "s_waitcnt lgkmcnt(7)\n\t" \
  "ds_read_b64_tr_b16 a[102:103], v237 offset:20480\n\t" \
  "v_mfma_f32_16x16x32_bf16 v[108:111], a[192:195], v[128:131], 0\n\t" \
  "ds_read_b128 v[160:163], v232 offset:4096\n\t" \
  "v_mfma_f32_16x16x32_bf16 v[108:111], a[200:203], v[132:135], v[108:111]\n\t" \
  "ds_read_b128 v[164:167], v232 offset:5120\n\t" \
  "v_mfma_f32_16x16x32_bf16 v[108:111], a[208:211], v[136:139], v[108:111]\n\t" \
  "ds_read_b128 v[168:171], v232 offset:6144\n\t" \
  "v_mfma_f32_16x16x32_bf16 v[108:111], a[216:219], v[140:143], v[108:111]\n\t" \
  "buffer_load_dwordx4 v248, s[24:27], s96 offen offset:2048 lds\n\t" \
  "v_mfma_f32_16x16x32_bf16 v[124:127], a[220:223], v[140:143], 0\n\t" \
  "ds_read_b64_tr_b16 a[104:105], v236 offset:24576\n\t" \
  "v_mfma_f32_16x16x32_bf16 v[124:127], a[212:215], v[136:139], v[124:127]\n\t" \
  "ds_read_b128 v[172:175], v232 offset:7168\n\t" \
  "v_mfma_f32_16x16x32_bf16 v[124:127], a[204:207], v[132:135], v[124:127]\n\t" \
  "buffer_load_dwordx4 v249, s[24:27], s96 offen offset:3072 lds\n\t" \
  "v_mfma_f32_16x16x32_bf16 v[124:127], a[196:199], v[128:131], v[124:127]\n\t" \
  "s_waitcnt lgkmcnt(7)\n\t" \
  "ds_read_b64_tr_b16 a[106:107], v236 offset:28672\n\t" \
  "s_add_u32 s97, s64, 786432\n\t" \
  "v_mfma_f32_16x16x32_bf16 v[0:3], a[64:67], v[144:147], v[0:3]\n\t" \
  "s_and_b32 s97, s97, 0xe0000\n\t" \
  "s_add_u32 s97, s97, s60\n\t" \
  "s_add_u32 m0, s38, 65536\n\t" \
  "v_mfma_f32_16x16x32_bf16 v[0:3], a[72:75], v[148:151], v[0:3]\n\t" \
  "ds_read_b128 v[128:131], v232 offset:8192\n\t" \
  "v_lshrrev_b32 v252, 5, v233\n\t" \
  "v_mfma_f32_16x16x32_bf16 v[0:3], a[80:83], v[152:155], v[0:3]\n\t" \
  "buffer_load_dwordx4 v242, s[16:19], s97 offen offset:0 sc1 lds\n\t" \
  "v_mfma_f32_16x16x32_bf16 v[0:3], a[88:91], v[156:159], v[0:3]\n\t" \
  "ds_read_b128 v[132:135], v232 offset:9216\n\t" \
  "v_mul_u32_u24 v252, 1536, v252\n\t" \
  "v_mfma_f32_16x16x32_bf16 v[16:19], a[92:95], v[156:159], v[16:19]\n\t" \
  "ds_read_b128 v[136:139], v232 offset:10240\n\t" \
  "v_lshl_add_u32 v252, s3, 9, v252\n\t" \
  "v_mfma_f32_16x16x32_bf16 v[16:19], a[84:87], v[152:155], v[16:19]\n\t" \
  "ds_read_b64_tr_b16 a[108:109], v237 offset:24576\n\t" \
  "v_add_u32 v252, v252, v242\n\t" \
  "v_mfma_f32_16x16x32_bf16 v[16:19], a[76:79], v[148:151], v[16:19]\n\t" \
  "buffer_load_dwordx4 v242, s[16:19], s97 offen offset:1024 sc1 lds\n\t" \
  "v_mfma_f32_16x16x32_bf16 v[16:19], a[68:71], v[144:147], v[16:19]\n\t" \
  "s_waitcnt lgkmcnt(5)\n\t" \
  "ds_read_b128 v[140:143], v232 offset:11264\n\t" \
  "v_lshrrev_b32 v247, 1, v242\n\t" \
  "v_mfma_f32_16x16x32_bf16 v[4:7], a[64:67], v[160:163], v[4:7]\n\t" \
  "ds_read_b64_tr_b16 a[110:111], v237 offset:28672\n\t" \
  "v_add_u32 v247, s38, v247\n\t" \
  "v_mfma_f32_16x16x32_bf16 v[4:7], a[72:75], v[164:167], v[4:7]\n\t" \
  "buffer_load_dwordx4 v242, s[16:19], s97 offen offset:2048 sc1 lds\n\t" \
  "v_mfma_f32_16x16x32_bf16 v[4:7], a[80:83], v[168:171], v[4:7]\n\t" \
  "ds_read_b128 v[144:147], v232 offset:12288\n\t" \
  "v_mfma_f32_16x16x32_bf16 v[4:7], a[88:91], v[172:175], v[4:7]\n\t" \
  "ds_read_b128 v[148:151], v232 offset:13312\n\t" \
  "v_mfma_f32_16x16x32_bf16 v[20:23], a[92:95], v[172:175], v[20:23]\n\t" \
  "buffer_load_dwordx4 v242, s[16:19], s97 offen offset:3072 sc1 lds\n\t" \
  "v_mfma_f32_16x16x32_bf16 v[20:23], a[84:87], v[168:171], v[20:23]\n\t" \
  "s_add_u32 s96, s40, 1835008\n\t" \
  "ds_read_b128 v[152:155], v232 offset:14336\n\t" \
  "s_and_b32 s96, s96, 0x1c0000\n\t" \
  "v_mfma_f32_16x16x32_bf16 v[20:23], a[76:79], v[164:167], v[20:23]\n\t" \
  "s_waitcnt vmcnt(9) lgkmcnt(3)\n\t" \
  "s_add_u32 s96, s96, s77\n\t" \
  "s_add_u32 m0, s38, 81920\n\t" \
  "v_mfma_f32_16x16x32_bf16 v[20:23], a[68:71], v[160:163], v[20:23]\n\t" \
  "s_barrier\n\t" \
  "v_mfma_f32_16x16x32_bf16 v[8:11], a[64:67], v[128:131], v[8:11]\n\t" \
  "buffer_load_dwordx4 v240, s[24:27], s96 offen offset:0 lds\n\t" \
  "v_mfma_f32_16x16x32_bf16 v[8:11], a[72:75], v[132:135], v[8:11]\n\t" \
  "ds_read_b128 v[156:159], v232 offset:15360\n\t" \
  "v_mfma_f32_16x16x32_bf16 v[8:11], a[80:83], v[136:139], v[8:11]\n\t" \
  "ds_read_b128 v[160:163], v242 offset:0\n\t" \
  "v_mfma_f32_16x16x32_bf16 v[8:11], a[88:91], v[140:143], v[8:11]\n\t" \
  "buffer_load_dwordx4 v241, s[24:27], s96 offen offset:1024 lds\n\t" \
  "v_mfma_f32_16x16x32_bf16 v[24:27], a[92:95], v[140:143], v[24:27]\n\t" \
  "ds_read_b128 v[164:167], v242 offset:1024\n\t" \
  "v_mfma_f32_16x16x32_bf16 v[24:27], a[84:87], v[136:139], v[24:27]\n\t" \
  "ds_read_b128 v[168:171], v242 offset:2048\n\t" \
  "v_mfma_f32_16x16x32_bf16 v[24:27], a[76:79], v[132:135], v[24:27]\n\t" \
  "buffer_load_dwordx4 v248, s[24:27], s96 offen offset:2048 lds\n\t" \
  "v_mfma_f32_16x16x32_bf16 v[24:27], a[68:71], v[128:131], v[24:27]\n\t" \
  "s_waitcnt lgkmcnt(3)\n\t" \
  "ds_read_b128 v[172:175], v242 offset:3072\n\t" \
  "v_mfma_f32_16x16x32_bf16 v[12:15], a[64:67], v[144:147], v[12:15]\n\t" \
  "ds_read_b64_tr_b16 a[112:113], v234 offset:16384\n\t" \
  "v_mfma_f32_16x16x32_bf16 v[12:15], a[72:75], v[148:151], v[12:15]\n\t" \
  "ds_read_b128 v[128:131], v242 offset:4096\n\t" \
  "v_mfma_f32_16x16x32_bf16 v[12:15], a[80:83], v[152:155], v[12:15]\n\t" \
  "ds_read_b128 v[132:135], v242 offset:5120\n\t" \
  "v_mfma_f32_16x16x32_bf16 v[12:15], a[88:91], v[156:159], v[12:15]\n\t" \
  "buffer_load_dwordx4 v249, s[24:27], s96 offen offset:3072 lds\n\t" \
  "v_mfma_f32_16x16x32_bf16 v[28:31], a[92:95], v[156:159], v[28:31]\n\t" \
  "s_add_u32 s97, s64, 524288\n\t" \
  "s_and_b32 s97, s97, 0xe0000\n\t" \
  "s_add_u32 s97, s97, s61\n\t" \
  "v_mfma_f32_16x16x32_bf16 v[28:31], a[84:87], v[152:155], v[28:31]\n\t" \
  "s_add_u32 m0, s38, 147456\n\t" \
  "ds_read_b128 v[136:139], v242 offset:6144\n\t" \
  "v_mfma_f32_16x16x32_bf16 v[28:31], a[76:79], v[148:151], v[28:31]\n\t" \
  "buffer_load_dwordx4 v242, s[16:19], s97 offen offset:0 sc1 lds\n\t" \
  "v_mfma_f32_16x16x32_bf16 v[28:31], a[68:71], v[144:147], v[28:31]\n\t" \
  "s_waitcnt lgkmcnt(4)\n\t" \
  "ds_read_b64_tr_b16 a[114:115], v234 offset:20480\n\t" \
  STP("s_memrealtime s[94:95]\n\t") \
  STP("s_memtime s[92:93]\n\t") \
  "v_mfma_f32_16x16x32_bf16 v[32:35], a[0:3], v[160:163], v[32:35]\n\t" \
  "ds_read_b128 v[140:143], v242 offset:7168\n\t" \
  "v_mfma_f32_16x16x32_bf16 v[32:35], a[8:11], v[164:167], v[32:35]\n\t" \
  "ds_read_b64_tr_b16 a[116:117], v235 offset:16384\n\t" \
  "v_mfma_f32_16x16x32_bf16 v[32:35], a[16:19], v[168:171], v[32:35]\n\t" \
  "ds_read_b128 v[144:147], v242 offset:8192\n\t" \
  "v_mfma_f32_16x16x32_bf16 v[32:35], a[24:27], v[172:175], v[32:35]\n\t" \
  "ds_read_b64_tr_b16 a[118:119], v235 offset:20480\n\t" \
  "v_mfma_f32_16x16x32_bf16 v[48:51], a[28:31], v[172:175], v[48:51]\n\t" \
  "ds_read_b128 v[148:151], v242 offset:9216\n\t" \
  "v_mfma_f32_16x16x32_bf16 v[48:51], a[20:23], v[168:171], v[48:51]\n\t" \
  "ds_read_b64_tr_b16 a[120:121], v234 offset:24576\n\t" \
  "v_mfma_f32_16x16x32_bf16 v[48:51], a[12:15], v[164:167], v[48:51]\n\t" \
  "ds_read_b128 v[152:155], v242 offset:10240\n\t" \
  "v_mfma_f32_16x16x32_bf16 v[48:51], a[4:7], v[160:163], v[48:51]\n\t" \
  "s_waitcnt lgkmcnt(6)\n\t" \
  "ds_read_b64_tr_b16 a[122:123], v234 offset:28672\n\t" \
  "v_mfma_f32_16x16x32_bf16 v[36:39], a[0:3], v[128:131], v[36:39]\n\t" \
  "ds_read_b128 v[156:159], v242 offset:11264\n\t" \
  "v_mfma_f32_16x16x32_bf16 v[36:39], a[8:11], v[132:135], v[36:39]\n\t" \
  "ds_read_b64_tr_b16 a[124:125], v235 offset:24576\n\t" \
  "v_mfma_f32_16x16x32_bf16 v[36:39], a[16:19], v[136:139], v[36:39]\n\t" \
  "ds_read_b128 v[160:163], v242 offset:12288\n\t" \
  "v_mfma_f32_16x16x32_bf16 v[36:39], a[24:27], v[140:143], v[36:39]\n\t" \
  "ds_read_b128 v[164:167], v242 offset:13312\n\t" \
  "ds_read_b64_tr_b16 a[126:127], v235 offset:28672\n\t" \
  "v_mfma_f32_16x16x32_bf16 v[52:55], a[28:31], v[140:143], v[52:55]\n\t" \
  STP("v_writelane_b32 v255, s94, 32\n\t") \
  STP("v_writelane_b32 v255, s92, 33\n\t") \
  "ds_read_b128 v[168:171], v242 offset:14336\n\t" \
  "v_mfma_f32_16x16x32_bf16 v[52:55], a[20:23], v[136:139], v[52:55]\n\t" \
  "buffer_load_dwordx4 v242, s[16:19], s97 offen offset:1024 sc1 lds\n\t" \
  "v_mfma_f32_16x16x32_bf16 v[52:55], a[12:15], v[132:135], v[52:55]\n\t" \
  "s_waitcnt vmcnt(6) lgkmcnt(1)\n\t" \
  "ds_read_b128 v[172:175], v242 offset:15360\n\t" \
  "v_mfma_f32_16x16x32_bf16 v[52:55], a[4:7], v[128:131], v[52:55]\n\t" \
  "s_barrier\n\t" \
  "v_mfma_f32_16x16x32_bf16 v[40:43], a[0:3], v[144:147], v[40:43]\n\t" \
  "buffer_load_dwordx4 v242, s[16:19], s97 offen offset:2048 sc1 lds\n\t" \
  "v_mfma_f32_16x16x32_bf16 v[40:43], a[8:11], v[148:151], v[40:43]\n\t" \
  "ds_read_b128 v[128:131], v230 offset:0\n\t" \
  "v_mfma_f32_16x16x32_bf16 v[40:43], a[16:19], v[152:155], v[40:43]\n\t" \
  "ds_read_b128 v[132:135], v230 offset:1024\n\t" \
  "v_mfma_f32_16x16x32_bf16 v[40:43], a[24:27], v[156:159], v[40:43]\n\t" \
  "buffer_load_dwordx4 v242, s[16:19], s97 offen offset:3072 sc1 lds\n\t" \
  "v_mfma_f32_16x16x32_bf16 v[56:59], a[28:31], v[156:159], v[56:59]\n\t" \
  "s_add_u32 s97, s64, 917504\n\t" \
  "s_and_b32 s97, s97, 0xe0000\n\t" \
  "s_add_u32 s97, s97, s60\n\t" \
  "v_mfma_f32_16x16x32_bf16 v[56:59], a[20:23], v[152:155], v[56:59]\n\t" \
  "s_add_u32 m0, s38, 131072\n\t" \
  "ds_read_b128 v[136:139], v230 offset:2048\n\t" \
  "v_mfma_f32_16x16x32_bf16 v[56:59], a[12:15], v[148:151], v[56:59]\n\t" \
  "buffer_load_dwordx4 v242, s[16:19], s97 offen offset:0 sc1 lds\n\t" \
  "v_mfma_f32_16x16x32_bf16 v[56:59], a[4:7], v[144:147], v[56:59]\n\t" \
  "s_waitcnt lgkmcnt(3)\n\t" \
  "ds_read_b128 v[140:143], v230 offset:3072\n\t" \
  "v_mfma_f32_16x16x32_bf16 v[44:47], a[0:3], v[160:163], v[44:47]\n\t" \
  "ds_read_b128 v[144:147], v230 offset:4096\n\t" \
  "v_mfma_f32_16x16x32_bf16 v[44:47], a[8:11], v[164:167], v[44:47]\n\t" \
  "buffer_load_dwordx4 v242, s[16:19], s97 offen offset:1024 sc1 lds\n\t" \
  "v_mfma_f32_16x16x32_bf16 v[44:47], a[16:19], v[168:171], v[44:47]\n\t" \
  "ds_read_b128 v[148:151], v230 offset:5120\n\t" \
  "v_mfma_f32_16x16x32_bf16 v[44:47], a[24:27], v[172:175], v[44:47]\n\t" \
  "ds_read_b128 v[152:155], v230 offset:6144\n\t" \
  "v_mfma_f32_16x16x32_bf16 v[60:63], a[28:31], v[172:175], v[60:63]\n\t" \
  "buffer_load_dwordx4 v242, s[16:19], s97 offen offset:2048 sc1 lds\n\t" \
  "v_mfma_f32_16x16x32_bf16 v[60:63], a[20:23], v[168:171], v[60:63]\n\t" \
  "ds_read_b128 v[156:159], v230 offset:7168\n\t" \
  "v_mfma_f32_16x16x32_bf16 v[60:63], a[12:15], v[164:167], v[60:63]\n\t" \
  "v_mfma_f32_16x16x32_bf16 v[60:63], a[4:7], v[160:163], v[60:63]\n\t" \
  "s_waitcnt lgkmcnt(4)\n\t" \
  "v_mfma_f32_16x16x32_bf16 v[0:3], a[96:99], v[128:131], v[0:3]\n\t" \
  "buffer_load_dwordx4 v242, s[16:19], s97 offen offset:3072 sc1 lds\n\t" \
  "v_mfma_f32_16x16x32_bf16 v[0:3], a[104:107], v[132:135], v[0:3]\n\t" \
  "s_add_u32 s97, s64, 655360\n\t" \
  "ds_read_b128 v[160:163], v230 offset:8192\n\t" \
  "s_and_b32 s97, s97, 0xe0000\n\t" \
  "v_mfma_f32_16x16x32_bf16 v[0:3], a[112:115], v[136:139], v[0:3]\n\t" \
  "s_add_u32 s97, s97, s61\n\t" \
  "ds_read_b128 v[164:167], v230 offset:9216\n\t" \
  "s_add_u32 m0, s38, 16384\n\t" \
  "v_mfma_f32_16x16x32_bf16 v[0:3], a[120:123], v[140:143], v[0:3]\n\t" \
  "ds_read_b128 v[168:171], v230 offset:10240\n\t" \
  "v_mfma_f32_16x16x32_bf16 v[16:19], a[124:127], v[140:143], v[16:19]\n\t" \
  "buffer_load_dwordx4 v242, s[16:19], s97 offen offset:0 sc1 lds\n\t" \
  "v_mfma_f32_16x16x32_bf16 v[16:19], a[116:119], v[136:139], v[16:19]\n\t" \
  "ds_read_b128 v[172:175], v230 offset:11264\n\t" \
  "v_mfma_f32_16x16x32_bf16 v[16:19], a[108:111], v[132:135], v[16:19]\n\t" \
  "v_mfma_f32_16x16x32_bf16 v[16:19], a[100:103], v[128:131], v[16:19]\n\t" \
  "s_waitcnt lgkmcnt(4)\n\t" \
  "buffer_load_dword v246, v245, s[16:19], s65 offen sc1\n\t" \
  "v_mfma_f32_16x16x32_bf16 v[4:7], a[96:99], v[144:147], v[4:7]\n\t" \
  "ds_read_b128 v[128:131], v230 offset:12288\n\t" \
  "v_mfma_f32_16x16x32_bf16 v[4:7], a[104:107], v[148:151], v[4:7]\n\t" \
  "ds_read_b128 v[132:135], v230 offset:13312\n\t" \
  "v_mfma_f32_16x16x32_bf16 v[4:7], a[112:115], v[152:155], v[4:7]\n\t" \
  "ds_read_b128 v[136:139], v230 offset:14336\n\t" \
  "v_mfma_f32_16x16x32_bf16 v[4:7], a[120:123], v[156:159], v[4:7]\n\t" \
  "buffer_load_dwordx4 v242, s[16:19], s97 offen offset:1024 sc1 lds\n\t" \
  "v_mfma_f32_16x16x32_bf16 v[20:23], a[124:127], v[156:159], v[20:23]\n\t" \
  "ds_read_b128 v[140:143], v230 offset:15360\n\t" \
  "v_mfma_f32_16x16x32_bf16 v[20:23], a[116:119], v[152:155], v[20:23]\n\t" \
  "v_mfma_f32_16x16x32_bf16 v[20:23], a[108:111], v[148:151], v[20:23]\n\t" \
  "s_waitcnt vmcnt(7) lgkmcnt(4)\n\t" \
  "buffer_load_dwordx4 v242, s[16:19], s97 offen offset:2048 sc1 lds\n\t" \
  "v_mfma_f32_16x16x32_bf16 v[20:23], a[100:103], v[144:147], v[20:23]\n\t" \
  "s_barrier\n\t" \
  "v_mfma_f32_16x16x32_bf16 v[8:11], a[96:99], v[160:163], v[8:11]\n\t" \
  "ds_read_b64_tr_b16 a[192:193], v234 offset:49152\n\t" \
  "ds_read_b128 v[144:147], v232 offset:16384\n\t" \
  "ds_read_b128 v[148:151], v232 offset:17408\n\t" \
  "v_mfma_f32_16x16x32_bf16 v[8:11], a[104:107], v[164:167], v[8:11]\n\t" \
  "buffer_load_dwordx4 v242, s[16:19], s97 offen offset:3072 sc1 lds\n\t" \
  "v_mfma_f32_16x16x32_bf16 v[8:11], a[112:115], v[168:171], v[8:11]\n\t" \
  "s_add_u32 s97, s64, 786432\n\t" \
  "s_and_b32 s97, s97, 0xe0000\n\t" \
  "ds_read_b128 v[152:155], v232 offset:18432\n\t" \
  "s_add_u32 s97, s97, s61\n\t" \
  "v_mfma_f32_16x16x32_bf16 v[8:11], a[120:123], v[172:175], v[8:11]\n\t" \
  "s_add_u32 m0, s38, 0\n\t" \
  "ds_read_b128 v[156:159], v232 offset:19456\n\t" \
  "v_mfma_f32_16x16x32_bf16 v[24:27], a[124:127], v[172:175], v[24:27]\n\t" \
  "buffer_load_dwordx4 v242, s[16:19], s97 offen offset:0 sc1 lds\n\t" \
  "v_mfma_f32_16x16x32_bf16 v[24:27], a[116:119], v[168:171], v[24:27]\n\t" \
  "ds_read_b64_tr_b16 a[194:195], v234 offset:53248\n\t" \
  "v_mfma_f32_16x16x32_bf16 v[24:27], a[108:111], v[164:167], v[24:27]\n\t" \
  "ds_read_b64_tr_b16 a[196:197], v235 offset:49152\n\t" \
  "v_mfma_f32_16x16x32_bf16 v[24:27], a[100:103], v[160:163], v[24:27]\n\t" \
  "s_waitcnt lgkmcnt(7)\n\t" \
  "buffer_load_dwordx4 v242, s[16:19], s97 offen offset:1024 sc1 lds\n\t" \
  "v_mfma_f32_16x16x32_bf16 v[12:15], a[96:99], v[128:131], v[12:15]\n\t" \
  "ds_read_b64_tr_b16 a[198:199], v235 offset:53248\n\t" \
  "ds_read_b128 v[160:163], v232 offset:20480\n\t" \
  "v_mfma_f32_16x16x32_bf16 v[12:15], a[104:107], v[132:135], v[12:15]\n\t" \
  "ds_read_b128 v[164:167], v232 offset:21504\n\t" \
  "v_mfma_f32_16x16x32_bf16 v[12:15], a[112:115], v[136:139], v[12:15]\n\t" \
  "buffer_load_dwordx4 v242, s[16:19], s97 offen offset:2048 sc1 lds\n\t" \
  "v_mfma_f32_16x16x32_bf16 v[12:15], a[120:123], v[140:143], v[12:15]\n\t" \
  "ds_read_b64_tr_b16 a[200:201], v234 offset:57344\n\t" \
  "ds_read_b128 v[168:171], v232 offset:22528\n\t" \
  "v_mfma_f32_16x16x32_bf16 v[28:31], a[124:127], v[140:143], v[28:31]\n\t" \
  "ds_read_b128 v[172:175], v232 offset:23552\n\t" \
  "v_mfma_f32_16x16x32_bf16 v[28:31], a[116:119], v[136:139], v[28:31]\n\t" \
  "buffer_load_dwordx4 v242, s[16:19], s97 offen offset:3072 sc1 lds\n\t" \
  "v_mfma_f32_16x16x32_bf16 v[28:31], a[108:111], v[132:135], v[28:31]\n\t" \
  "s_add_u32 s97, s64, 917504\n\t" \
  "s_and_b32 s97, s97, 0xe0000\n\t" \
  "s_add_u32 s97, s97, s61\n\t" \
  "v_mfma_f32_16x16x32_bf16 v[28:31], a[100:103], v[128:131], v[28:31]\n\t" \
  "s_waitcnt lgkmcnt(8)\n\t" \
  "ds_read_b64_tr_b16 a[202:203], v234 offset:61440\n\t" \
  "s_add_u32 m0, s38, 32768\n\t" \
  "v_mfma_f32_16x16x32_bf16 v[32:35], a[32:35], v[144:147], v[32:35]\n\t" \
  "buffer_load_dwordx4 v242, s[16:19], s97 offen offset:0 sc1 lds\n\t" \
  "ds_read_b128 v[128:131], v232 offset:24576\n\t" \
  "v_mfma_f32_16x16x32_bf16 v[32:35], a[40:43], v[148:151], v[32:35]\n\t" \
  "ds_read_b64_tr_b16 a[204:205], v235 offset:57344\n\t" \
  "ds_read_b128 v[132:135], v232 offset:25600\n\t" \
  "v_mfma_f32_16x16x32_bf16 v[32:35], a[48:51], v[152:155], v[32:35]\n\t" \
  "ds_read_b128 v[136:139], v232 offset:26624\n\t" \
  "v_mfma_f32_16x16x32_bf16 v[32:35], a[56:59], v[156:159], v[32:35]\n\t" \
  "buffer_load_dwordx4 v242, s[16:19], s97 offen offset:1024 sc1 lds\n\t" \
  "v_mfma_f32_16x16x32_bf16 v[48:51], a[60:63], v[156:159], v[48:51]\n\t" \
  "ds_read_b128 v[140:143], v232 offset:27648\n\t" \
  "v_mfma_f32_16x16x32_bf16 v[48:51], a[52:55], v[152:155], v[48:51]\n\t" \
  "ds_read_b64_tr_b16 a[206:207], v235 offset:61440\n\t" \
  "v_mfma_f32_16x16x32_bf16 v[48:51], a[44:47], v[148:151], v[48:51]\n\t" \
  "buffer_load_dwordx4 v242, s[16:19], s97 offen offset:2048 sc1 lds\n\t" \
  "v_mfma_f32_16x16x32_bf16 v[48:51], a[36:39], v[144:147], v[48:51]\n\t" \
  "s_waitcnt lgkmcnt(7)\n\t" \
  "ds_read_b64_tr_b16 a[208:209], v236 offset:16384\n\t" \
  "v_mfma_f32_16x16x32_bf16 v[36:39], a[32:35], v[160:163], v[36:39]\n\t" \
  "ds_read_b64_tr_b16 a[210:211], v236 offset:20480\n\t" \
  "ds_read_b128 v[144:147], v232 offset:28672\n\t" \
  "ds_read_b128 v[148:151], v232 offset:29696\n\t" \
  "v_mfma_f32_16x16x32_bf16 v[36:39], a[40:43], v[164:167], v[36:39]\n\t" \
  "buffer_load_dwordx4 v242, s[16:19], s97 offen offset:3072 sc1 lds\n\t" \
  "v_mfma_f32_16x16x32_bf16 v[36:39], a[48:51], v[168:171], v[36:39]\n\t" \
  "s_add_u32 s97, s64, 131072\n\t" \
  "ds_read_b128 v[152:155], v232 offset:30720\n\t" \
  "s_and_b32 s97, s97, 0xe0000\n\t" \
  "v_mfma_f32_16x16x32_bf16 v[36:39], a[56:59], v[172:175], v[36:39]\n\t" \
  "s_add_u32 s97, s97, s62\n\t" \
  "ds_read_b128 v[156:159], v232 offset:31744\n\t" \
  "s_add_u32 m0, s38, 65536\n\t" \
  "v_mfma_f32_16x16x32_bf16 v[52:55], a[60:63], v[172:175], v[52:55]\n\t" \
  "ds_read_b64_tr_b16 a[212:213], v237 offset:16384\n\t" \
  "v_mfma_f32_16x16x32_bf16 v[52:55], a[52:55], v[168:171], v[52:55]\n\t" \
  "s_waitcnt vmcnt(11)\n\t" \
  "v_cmp_ne_u32 vcc, s36, v246\n\t" \
  "v_mfma_f32_16x16x32_bf16 v[52:55], a[44:47], v[164:167], v[52:55]\n\t" \
  "s_waitcnt lgkmcnt(7)\n\t" \
  "s_and_b32 vcc_lo, vcc_lo, 0xfe0000\n\t" \
  "s_cbranch_scc1 .Lat_s2\n\t" \
  ".Lat_k2:\n\t" \
  "v_mfma_f32_16x16x32_bf16 v[52:55], a[36:39], v[160:163], v[52:55]\n\t" \
  "s_barrier\n\t" \
  "v_mfma_f32_16x16x32_bf16 v[40:43], a[32:35], v[128:131], v[40:43]\n\t" \
  "buffer_load_dwordx4 v242, s[16:19], s97 offen offset:0 sc1 lds\n\t" \
  "ds_read_b128 v[160:163], v232 offset:0\n\t" \
  "v_mfma_f32_16x16x32_bf16 v[40:43], a[40:43], v[132:135], v[40:43]\n\t" \
  "ds_read_b128 v[164:167], v232 offset:1024\n\t" \
  "ds_read_b64_tr_b16 a[214:215], v237 offset:20480\n\t" \
  "v_mfma_f32_16x16x32_bf16 v[40:43], a[48:51], v[136:139], v[40:43]\n\t" \
  "ds_read_b64_tr_b16 a[216:217], v236 offset:24576\n\t" \
  "v_mfma_f32_16x16x32_bf16 v[40:43], a[56:59], v[140:143], v[40:43]\n\t" \
  "buffer_load_dwordx4 v242, s[16:19], s97 offen offset:1024 sc1 lds\n\t" \
  "v_mfma_f32_16x16x32_bf16 v[56:59], a[60:63], v[140:143], v[56:59]\n\t" \
  "ds_read_b128 v[168:171], v232 offset:2048\n\t" \
  "v_mfma_f32_16x16x32_bf16 v[56:59], a[52:55], v[136:139], v[56:59]\n\t" \
  "ds_read_b64_tr_b16 a[218:219], v236 offset:28672\n\t" \
  "v_mfma_f32_16x16x32_bf16 v[56:59], a[44:47], v[132:135], v[56:59]\n\t" \
  "buffer_load_dwordx4 v242, s[16:19], s97 offen offset:2048 sc1 lds\n\t" \
  "v_mfma_f32_16x16x32_bf16 v[56:59], a[36:39], v[128:131], v[56:59]\n\t" \
  "s_waitcnt lgkmcnt(7)\n\t" \
  "ds_read_b128 v[172:175], v232 offset:3072\n\t" \
  "v_mfma_f32_16x16x32_bf16 v[44:47], a[32:35], v[144:147], v[44:47]\n\t" \
  "ds_read_b64_tr_b16 a[220:221], v237 offset:24576\n\t" \
  "v_mfma_f32_16x16x32_bf16 v[44:47], a[40:43], v[148:151], v[44:47]\n\t" \
  "buffer_load_dwordx4 v242, s[16:19], s97 offen offset:3072 sc1 lds\n\t" \
  "v_mfma_f32_16x16x32_bf16 v[44:47], a[48:51], v[152:155], v[44:47]\n\t" \
  "s_add_u32 s97, s64, 262144\n\t" \
  "ds_read_b128 v[128:131], v232 offset:4096\n\t" \
  "s_and_b32 s97, s97, 0xe0000\n\t" \
  "v_mfma_f32_16x16x32_bf16 v[44:47], a[56:59], v[156:159], v[44:47]\n\t" \
  "s_add_u32 s97, s97, s62\n\t" \
  "ds_read_b128 v[132:135], v232 offset:5120\n\t" \
  "ds_read_b64_tr_b16 a[222:223], v237 offset:28672\n\t" \
  "s_add_u32 m0, s38, 49152\n\t" \
  "v_mfma_f32_16x16x32_bf16 v[60:63], a[60:63], v[156:159], v[60:63]\n\t" \
  "ds_read_b128 v[136:139], v232 offset:6144\n\t" \
  "v_mfma_f32_16x16x32_bf16 v[60:63], a[52:55], v[152:155], v[60:63]\n\t" \
  "buffer_load_dwordx4 v242, s[16:19], s97 offen offset:0 sc1 lds\n\t" \
  "v_mfma_f32_16x16x32_bf16 v[60:63], a[44:47], v[148:151], v[60:63]\n\t" \
  "ds_read_b128 v[140:143], v232 offset:7168\n\t" \
  "v_mfma_f32_16x16x32_bf16 v[60:63], a[36:39], v[144:147], v[60:63]\n\t" \
  "s_waitcnt lgkmcnt(2)\n\t" \
  "v_mfma_f32_16x16x32_bf16 v[0:3], a[192:195], v[160:163], v[0:3]\n\t" \
  "buffer_load_dwordx4 v242, s[16:19], s97 offen offset:1024 sc1 lds\n\t" \
  "v_mfma_f32_16x16x32_bf16 v[0:3], a[200:203], v[164:167], v[0:3]\n\t" \
  "ds_read_b128 v[144:147], v232 offset:8192\n\t" \
  "v_mfma_f32_16x16x32_bf16 v[0:3], a[208:211], v[168:171], v[0:3]\n\t" \
  "ds_read_b128 v[148:151], v232 offset:9216\n\t" \
  "v_mfma_f32_16x16x32_bf16 v[0:3], a[216:219], v[172:175], v[0:3]\n\t" \
  "ds_read_b128 v[152:155], v232 offset:10240\n\t" \
  "v_mfma_f32_16x16x32_bf16 v[16:19], a[220:223], v[172:175], v[16:19]\n\t" \
  "buffer_load_dwordx4 v242, s[16:19], s97 offen offset:2048 sc1 lds\n\t" \
  "v_mfma_f32_16x16x32_bf16 v[16:19], a[212:215], v[168:171], v[16:19]\n\t" \
  "ds_read_b128 v[156:159], v232 offset:11264\n\t" \
  "v_mfma_f32_16x16x32_bf16 v[16:19], a[204:207], v[164:167], v[16:19]\n\t" \
  "v_mfma_f32_16x16x32_bf16 v[16:19], a[196:199], v[160:163], v[16:19]\n\t" \
  "s_waitcnt lgkmcnt(4)\n\t" \
  "v_cvt_pk_bf16_f32 v208, v0, v1\n\t" \
  "v_cvt_pk_bf16_f32 v209, v2, v3\n\t" \
  "v_mfma_f32_16x16x32_bf16 v[4:7], a[192:195], v[128:131], v[4:7]\n\t" \
  "buffer_load_dwordx4 v242, s[16:19], s97 offen offset:3072 sc1 lds\n\t" \
  "v_mfma_f32_16x16x32_bf16 v[4:7], a[200:203], v[132:135], v[4:7]\n\t" \
  "s_add_u32 s97, s64, 393216\n\t" \
  "s_and_b32 s97, s97, 0xe0000\n\t" \
  "s_add_u32 s97, s97, s62\n\t" \
  "v_mfma_f32_16x16x32_bf16 v[4:7], a[208:211], v[136:139], v[4:7]\n\t" \
  "s_add_u32 m0, s38, 98304\n\t" \
  "ds_read_b128 v[160:163], v232 offset:12288\n\t" \
  "v_cvt_pk_bf16_f32 v210, v16, v17\n\t" \
  "v_mfma_f32_16x16x32_bf16 v[4:7], a[216:219], v[140:143], v[4:7]\n\t" \
  "buffer_load_dwordx4 v242, s[16:19], s97 offen offset:0 sc1 lds\n\t" \
  "v_mfma_f32_16x16x32_bf16 v[20:23], a[220:223], v[140:143], v[20:23]\n\t" \
  "ds_read_b128 v[164:167], v232 offset:13312\n\t" \
  "v_cvt_pk_bf16_f32 v211, v18, v19\n\t" \
  "v_mfma_f32_16x16x32_bf16 v[20:23], a[212:215], v[136:139], v[20:23]\n\t" \
  "ds_read_b128 v[168:171], v232 offset:14336\n\t" \
  "v_mfma_f32_16x16x32_bf16 v[20:23], a[204:207], v[132:135], v[20:23]\n\t" \
  "s_waitcnt vmcnt(17) lgkmcnt(3)\n\t" \
  "ds_read_b128 v[172:175], v232 offset:15360\n\t" \
  "v_mfma_f32_16x16x32_bf16 v[20:23], a[196:199], v[128:131], v[20:23]\n\t" \
  "s_barrier\n\t" \
  "v_mfma_f32_16x16x32_bf16 v[8:11], a[192:195], v[144:147], v[8:11]\n\t" \
  "buffer_store_dwordx4 v[208:211], v242, s[16:19], s67 offen offset:0 sc1\n\t" \
  "ds_read_b128 v[128:131], v242 offset:16384\n\t" \
  "v_cvt_pk_bf16_f32 v212, v4, v5\n\t" \
  "v_cvt_pk_bf16_f32 v213, v6, v7\n\t" \
  "v_mfma_f32_16x16x32_bf16 v[8:11], a[200:203], v[148:151], v[8:11]\n\t" \
  "ds_read_b128 v[132:135], v242 offset:17408\n\t" \
  "v_cvt_pk_bf16_f32 v214, v20, v21\n\t" \
  "v_mfma_f32_16x16x32_bf16 v[8:11], a[208:211], v[152:155], v[8:11]\n\t" \
  "ds_read_b128 v[136:139], v242 offset:18432\n\t" \
  "v_cvt_pk_bf16_f32 v215, v22, v23\n\t" \
  "v_mfma_f32_16x16x32_bf16 v[8:11], a[216:219], v[156:159], v[8:11]\n\t" \
  "ds_read_b128 v[140:143], v242 offset:19456\n\t" \
  "v_mfma_f32_16x16x32_bf16 v[24:27], a[220:223], v[156:159], v[24:27]\n\t" \
  "v_mfma_f32_16x16x32_bf16 v[24:27], a[212:215], v[152:155], v[24:27]\n\t" \
  "v_mfma_f32_16x16x32_bf16 v[24:27], a[204:207], v[148:151], v[24:27]\n\t" \
  "v_mfma_f32_16x16x32_bf16 v[24:27], a[196:199], v[144:147], v[24:27]\n\t" \
  "s_waitcnt lgkmcnt(4)\n\t" \
  "v_mfma_f32_16x16x32_bf16 v[12:15], a[192:195], v[160:163], v[12:15]\n\t" \
  "ds_read_b128 v[144:147], v242 offset:20480\n\t" \
  "v_cvt_pk_bf16_f32 v216, v8, v9\n\t" \
  "v_mfma_f32_16x16x32_bf16 v[12:15], a[200:203], v[164:167], v[12:15]\n\t" \
  "ds_read_b128 v[148:151], v242 offset:21504\n\t" \
  "v_cvt_pk_bf16_f32 v217, v10, v11\n\t" \
  "v_mfma_f32_16x16x32_bf16 v[12:15], a[208:211], v[168:171], v[12:15]\n\t" \
  "v_cvt_pk_bf16_f32 v218, v24, v25\n\t" \
  "v_cvt_pk_bf16_f32 v219, v26, v27\n\t" \
  "v_mfma_f32_16x16x32_bf16 v[12:15], a[216:219], v[172:175], v[12:15]\n\t" \
  "ds_read_b128 v[152:155], v242 offset:22528\n\t" \
  "v_mfma_f32_16x16x32_bf16 v[28:31], a[220:223], v[172:175], v[28:31]\n\t" \
  "buffer_store_dwordx4 v[212:215], v242, s[16:19], s67 offen offset:1024 sc1\n\t" \
  "ds_read_b128 v[156:159], v242 offset:23552\n\t" \
  "v_mfma_f32_16x16x32_bf16 v[28:31], a[212:215], v[168:171], v[28:31]\n\t" \
  "v_mfma_f32_16x16x32_bf16 v[28:31], a[204:207], v[164:167], v[28:31]\n\t" \
  "v_mfma_f32_16x16x32_bf16 v[28:31], a[196:199], v[160:163], v[28:31]\n\t" \
  "s_waitcnt lgkmcnt(4)\n\t" \
  "v_cvt_pk_bf16_f32 v220, v12, v13\n\t" \
  "v_cvt_pk_bf16_f32 v221, v14, v15\n\t" \
  "v_mfma_f32_16x16x32_bf16 v[32:35], a[64:67], v[128:131], v[32:35]\n\t" \
  "ds_read_b128 v[160:163], v242 offset:24576\n\t" \
  "v_mfma_f32_16x16x32_bf16 v[32:35], a[72:75], v[132:135], v[32:35]\n\t" \
  "ds_read_b128 v[164:167], v242 offset:25600\n\t" \
  "v_mfma_f32_16x16x32_bf16 v[32:35], a[80:83], v[136:139], v[32:35]\n\t" \
  "v_cvt_pk_bf16_f32 v222, v28, v29\n\t" \
  "v_cvt_pk_bf16_f32 v223, v30, v31\n\t" \
  "v_mfma_f32_16x16x32_bf16 v[32:35], a[88:91], v[140:143], v[32:35]\n\t" \
  "ds_read_b128 v[168:171], v242 offset:26624\n\t" \
  "v_mfma_f32_16x16x32_bf16 v[48:51], a[92:95], v[140:143], v[48:51]\n\t" \
  "ds_read_b128 v[172:175], v242 offset:27648\n\t" \
  "v_mfma_f32_16x16x32_bf16 v[48:51], a[84:87], v[136:139], v[48:51]\n\t" \
  "v_mfma_f32_16x16x32_bf16 v[48:51], a[76:79], v[132:135], v[48:51]\n\t" \
  "v_mfma_f32_16x16x32_bf16 v[48:51], a[68:71], v[128:131], v[48:51]\n\t" \
  "s_waitcnt lgkmcnt(4)\n\t" \
  "v_mfma_f32_16x16x32_bf16 v[36:39], a[64:67], v[144:147], v[36:39]\n\t" \
  "ds_read_b128 v[128:131], v242 offset:28672\n\t" \
  "v_mfma_f32_16x16x32_bf16 v[36:39], a[72:75], v[148:151], v[36:39]\n\t" \
  "buffer_store_dwordx4 v[216:219], v242, s[16:19], s67 offen offset:2048 sc1\n\t" \
  "ds_read_b128 v[132:135], v242 offset:29696\n\t" \
  "ds_read_b128 v[136:139], v242 offset:30720\n\t" \
  "ds_read_b128 v[140:143], v242 offset:31744\n\t" \
  "v_mfma_f32_16x16x32_bf16 v[36:39], a[80:83], v[152:155], v[36:39]\n\t" \
  "v_mfma_f32_16x16x32_bf16 v[36:39], a[88:91], v[156:159], v[36:39]\n\t" \
  "v_mfma_f32_16x16x32_bf16 v[52:55], a[92:95], v[156:159], v[52:55]\n\t" \
  "v_mfma_f32_16x16x32_bf16 v[52:55], a[84:87], v[152:155], v[52:55]\n\t" \
  "v_mfma_f32_16x16x32_bf16 v[52:55], a[76:79], v[148:151], v[52:55]\n\t" \
  "s_waitcnt vmcnt(16) lgkmcnt(4)\n\t" \
  "v_mfma_f32_16x16x32_bf16 v[52:55], a[68:71], v[144:147], v[52:55]\n\t" \
  "s_barrier\n\t" \
  "v_mfma_f32_16x16x32_bf16 v[40:43], a[64:67], v[160:163], v[40:43]\n\t" \
  "ds_read_b128 v[144:147], v242 offset:0\n\t" \
  "v_mfma_f32_16x16x32_bf16 v[40:43], a[72:75], v[164:167], v[40:43]\n\t" \
  "ds_read_b128 v[148:151], v242 offset:1024\n\t" \
  "v_mfma_f32_16x16x32_bf16 v[40:43], a[80:83], v[168:171], v[40:43]\n\t" \
  "ds_read_b128 v[152:155], v242 offset:2048\n\t" \
  "v_mfma_f32_16x16x32_bf16 v[40:43], a[88:91], v[172:175], v[40:43]\n\t" \
  "ds_read_b128 v[156:159], v242 offset:3072\n\t" \
  "v_mfma_f32_16x16x32_bf16 v[56:59], a[92:95], v[172:175], v[56:59]\n\t" \
  "v_mfma_f32_16x16x32_bf16 v[56:59], a[84:87], v[168:171], v[56:59]\n\t" \
  "v_mfma_f32_16x16x32_bf16 v[56:59], a[76:79], v[164:167], v[56:59]\n\t" \
  "buffer_store_dwordx4 v[220:223], v242, s[16:19], s67 offen offset:3072 sc1\n\t" \
  "v_mfma_f32_16x16x32_bf16 v[56:59], a[68:71], v[160:163], v[56:59]\n\t" \
  "s_waitcnt lgkmcnt(4)\n\t" \
  "v_mfma_f32_16x16x32_bf16 v[44:47], a[64:67], v[128:131], v[44:47]\n\t" \
  "ds_read_b128 v[160:163], v242 offset:4096\n\t" \
  "v_mfma_f32_16x16x32_bf16 v[44:47], a[72:75], v[132:135], v[44:47]\n\t" \
  "ds_read_b128 v[164:167], v242 offset:5120\n\t" \
  "v_mfma_f32_16x16x32_bf16 v[44:47], a[80:83], v[136:139], v[44:47]\n\t" \
  "ds_read_b128 v[168:171], v242 offset:6144\n\t" \
  "v_mfma_f32_16x16x32_bf16 v[44:47], a[88:91], v[140:143], v[44:47]\n\t" \
  "ds_read_b128 v[172:175], v242 offset:7168\n\t" \
  "v_mfma_f32_16x16x32_bf16 v[60:63], a[92:95], v[140:143], v[60:63]\n\t" \
  "v_mfma_f32_16x16x32_bf16 v[60:63], a[84:87], v[136:139], v[60:63]\n\t" \
  "v_mfma_f32_16x16x32_bf16 v[60:63], a[76:79], v[132:135], v[60:63]\n\t" \
  "v_mfma_f32_16x16x32_bf16 v[60:63], a[68:71], v[128:131], v[60:63]\n\t" \
  "s_waitcnt lgkmcnt(4)\n\t" \
  "v_mfma_f32_16x16x32_bf16 v[32:35], a[96:99], v[144:147], v[32:35]\n\t" \
  "ds_read_b128 v[128:131], v242 offset:8192\n\t" \
  "v_mfma_f32_16x16x32_bf16 v[32:35], a[104:107], v[148:151], v[32:35]\n\t" \
  "ds_read_b128 v[132:135], v242 offset:9216\n\t" \
  "v_mfma_f32_16x16x32_bf16 v[32:35], a[112:115], v[152:155], v[32:35]\n\t" \
  "ds_read_b128 v[136:139], v242 offset:10240\n\t" \
  "v_mfma_f32_16x16x32_bf16 v[32:35], a[120:123], v[156:159], v[32:35]\n\t" \
  "buffer_load_dwordx4 v242, s[16:19], s97 offen offset:1024 sc1 lds\n\t" \
  "v_mfma_f32_16x16x32_bf16 v[48:51], a[124:127], v[156:159], v[48:51]\n\t" \
  "ds_read_b128 v[140:143], v242 offset:11264\n\t" \
  "v_mfma_f32_16x16x32_bf16 v[48:51], a[116:119], v[152:155], v[48:51]\n\t" \
  "v_mfma_f32_16x16x32_bf16 v[48:51], a[108:111], v[148:151], v[48:51]\n\t" \
  "v_mfma_f32_16x16x32_bf16 v[48:51], a[100:103], v[144:147], v[48:51]\n\t" \
  "s_waitcnt lgkmcnt(4)\n\t" \
  "buffer_load_dwordx4 v242, s[16:19], s97 offen offset:2048 sc1 lds\n\t" \
  "v_mfma_f32_16x16x32_bf16 v[36:39], a[96:99], v[160:163], v[36:39]\n\t" \
  "ds_read_b128 v[144:147], v242 offset:12288\n\t" \
  "v_mfma_f32_16x16x32_bf16 v[36:39], a[104:107], v[164:167], v[36:39]\n\t" \
  "ds_read_b128 v[148:151], v242 offset:13312\n\t" \
  "v_mfma_f32_16x16x32_bf16 v[36:39], a[112:115], v[168:171], v[36:39]\n\t" \
  "buffer_load_dwordx4 v242, s[16:19], s97 offen offset:3072 sc1 lds\n\t" \
  "v_mfma_f32_16x16x32_bf16 v[36:39], a[120:123], v[172:175], v[36:39]\n\t" \
  "s_add_u32 s97, s64, 524288\n\t" \
  "ds_read_b128 v[152:155], v242 offset:14336\n\t" \
  "s_and_b32 s97, s97, 0xe0000\n\t" \
  "v_mfma_f32_16x16x32_bf16 v[52:55], a[124:127], v[172:175], v[52:55]\n\t" \
  "s_add_u32 s97, s97, s62\n\t" \
  "ds_read_b128 v[156:159], v242 offset:15360\n\t" \
  "s_add_u32 m0, s38, 81920\n\t" \
  "v_mfma_f32_16x16x32_bf16 v[52:55], a[116:119], v[168:171], v[52:55]\n\t" \
  "v_mfma_f32_16x16x32_bf16 v[52:55], a[108:111], v[164:167], v[52:55]\n\t" \
  "s_waitcnt vmcnt(16) lgkmcnt(4)\n\t" \
  "buffer_load_dwordx4 v242, s[16:19], s97 offen offset:0 sc1 lds\n\t" \
  "v_mfma_f32_16x16x32_bf16 v[52:55], a[100:103], v[160:163], v[52:55]\n\t" \
  "s_barrier\n\t" \
  ".Lhs13:\n\ts_cbranch_execz .Lhs13n\n\t" \
  "v_mfma_f32_16x16x32_bf16 v[40:43], a[96:99], v[128:131], v[40:43]\n\t" \
  "ds_read_b128 v[160:163], v242 offset:32768\n\t" \
  "v_mfma_f32_16x16x32_bf16 v[40:43], a[104:107], v[132:135], v[40:43]\n\t" \
  "ds_read_b128 v[164:167], v242 offset:33792\n\t" \
  "v_mfma_f32_16x16x32_bf16 v[40:43], a[112:115], v[136:139], v[40:43]\n\t" \
  "buffer_load_dwordx4 v242, s[16:19], s97 offen offset:1024 sc1 lds\n\t" \
  "v_mfma_f32_16x16x32_bf16 v[40:43], a[120:123], v[140:143], v[40:43]\n\t" \
  "ds_read_b128 v[168:171], v242 offset:34816\n\t" \
  "v_mfma_f32_16x16x32_bf16 v[56:59], a[124:127], v[140:143], v[56:59]\n\t" \
  "ds_read_b128 v[172:175], v242 offset:35840\n\t" \
  "v_mfma_f32_16x16x32_bf16 v[56:59], a[116:119], v[136:139], v[56:59]\n\t" \
  "buffer_load_dwordx4 v242, s[16:19], s97 offen offset:2048 sc1 lds\n\t" \
  "v_mfma_f32_16x16x32_bf16 v[56:59], a[108:111], v[132:135], v[56:59]\n\t" \
  "v_mfma_f32_16x16x32_bf16 v[56:59], a[100:103], v[128:131], v[56:59]\n\t" \
  "s_waitcnt lgkmcnt(4)\n\t" \
  "v_mfma_f32_16x16x32_bf16 v[44:47], a[96:99], v[144:147], v[44:47]\n\t" \
  "ds_read_b128 v[128:131], v242 offset:36864\n\t" \
  "v_mfma_f32_16x16x32_bf16 v[44:47], a[104:107], v[148:151], v[44:47]\n\t" \
  "buffer_load_dwordx4 v242, s[16:19], s97 offen offset:3072 sc1 lds\n\t" \
  "v_mfma_f32_16x16x32_bf16 v[44:47], a[112:115], v[152:155], v[44:47]\n\t" \
  "s_add_u32 s97, s64, 655360\n\t" \
  "ds_read_b128 v[132:135], v242 offset:37888\n\t" \
  "s_and_b32 s97, s97, 0xe0000\n\t" \
  "v_mfma_f32_16x16x32_bf16 v[44:47], a[120:123], v[156:159], v[44:47]\n\t" \
  "s_add_u32 s97, s97, s62\n\t" \
  "ds_read_b128 v[136:139], v242 offset:38912\n\t" \
  "s_add_u32 m0, s38, 131072\n\t" \
  "v_mfma_f32_16x16x32_bf16 v[60:63], a[124:127], v[156:159], v[60:63]\n\t" \
  "ds_read_b128 v[140:143], v242 offset:39936\n\t" \
  "v_mfma_f32_16x16x32_bf16 v[60:63], a[116:119], v[152:155], v[60:63]\n\t" \
  "buffer_load_dwordx4 v242, s[16:19], s97 offen offset:0 sc1 lds\n\t" \
  "v_mfma_f32_16x16x32_bf16 v[60:63], a[108:111], v[148:151], v[60:63]\n\t" \
  "v_mfma_f32_16x16x32_bf16 v[60:63], a[100:103], v[144:147], v[60:63]\n\t" \
  "s_waitcnt lgkmcnt(4)\n\t" \
  "v_mfma_f32_16x16x32_bf16 v[32:35], a[192:195], v[160:163], v[32:35]\n\t" \
  "buffer_load_dwordx4 v242, s[16:19], s97 offen offset:1024 sc1 lds\n\t" \
  "v_mfma_f32_16x16x32_bf16 v[32:35], a[200:203], v[164:167], v[32:35]\n\t" \
  "ds_read_b128 v[144:147], v242 offset:40960\n\t" \
  "v_mfma_f32_16x16x32_bf16 v[32:35], a[208:211], v[168:171], v[32:35]\n\t" \
  "ds_read_b128 v[148:151], v242 offset:41984\n\t" \
  "v_mfma_f32_16x16x32_bf16 v[32:35], a[216:219], v[172:175], v[32:35]\n\t" \
  "ds_read_b128 v[152:155], v242 offset:43008\n\t" \
  "v_mfma_f32_16x16x32_bf16 v[48:51], a[220:223], v[172:175], v[48:51]\n\t" \
  "ds_read_b128 v[156:159], v242 offset:44032\n\t" \
  "v_mfma_f32_16x16x32_bf16 v[48:51], a[212:215], v[168:171], v[48:51]\n\t" \
  "v_mfma_f32_16x16x32_bf16 v[48:51], a[204:207], v[164:167], v[48:51]\n\t" \
  "buffer_load_dwordx4 v242, s[16:19], s97 offen offset:2048 sc1 lds\n\t" \
  "v_mfma_f32_16x16x32_bf16 v[48:51], a[196:199], v[160:163], v[48:51]\n\t" \
  "s_waitcnt lgkmcnt(4)\n\t" \
  "v_cvt_pk_bf16_f32 v0, v32, v33\n\t" \
  "v_cvt_pk_bf16_f32 v1, v34, v35\n\t" \
  "v_mfma_f32_16x16x32_bf16 v[36:39], a[192:195], v[128:131], v[36:39]\n\t" \
  "ds_read_b128 v[160:163], v242 offset:45056\n\t" \
  "v_mfma_f32_16x16x32_bf16 v[36:39], a[200:203], v[132:135], v[36:39]\n\t" \
  "ds_read_b128 v[164:167], v242 offset:46080\n\t" \
  "v_mfma_f32_16x16x32_bf16 v[36:39], a[208:211], v[136:139], v[36:39]\n\t" \
  "v_cvt_pk_bf16_f32 v2, v48, v49\n\t" \
  "v_cvt_pk_bf16_f32 v3, v50, v51\n\t" \
  "v_mfma_f32_16x16x32_bf16 v[36:39], a[216:219], v[140:143], v[36:39]\n\t" \
  "ds_read_b128 v[168:171], v242 offset:47104\n\t" \
  "v_mfma_f32_16x16x32_bf16 v[52:55], a[220:223], v[140:143], v[52:55]\n\t" \
  "ds_read_b128 v[172:175], v242 offset:48128\n\t" \
  "v_mfma_f32_16x16x32_bf16 v[52:55], a[212:215], v[136:139], v[52:55]\n\t" \
  "v_mfma_f32_16x16x32_bf16 v[52:55], a[204:207], v[132:135], v[52:55]\n\t" \
  "s_waitcnt vmcnt(15) lgkmcnt(4)\n\t" \
  "v_mfma_f32_16x16x32_bf16 v[52:55], a[196:199], v[128:131], v[52:55]\n\t" \
  "s_barrier\n\t" \
  "v_mfma_f32_16x16x32_bf16 v[40:43], a[192:195], v[144:147], v[40:43]\n\t" \
  "ds_read_b128 v[128:131], v230 offset:0\n\t" \
  "v_cvt_pk_bf16_f32 v4, v36, v37\n\t" \
  "v_mfma_f32_16x16x32_bf16 v[40:43], a[200:203], v[148:151], v[40:43]\n\t" \
  "v_cvt_pk_bf16_f32 v5, v38, v39\n\t" \
  "v_and_b32 v235, 15, v233\n\t" \
  "v_mfma_f32_16x16x32_bf16 v[40:43], a[208:211], v[152:155], v[40:43]\n\t" \
  "v_cvt_pk_bf16_f32 v6, v52, v53\n\t" \
  "v_cvt_pk_bf16_f32 v7, v54, v55\n\t" \
  "v_mfma_f32_16x16x32_bf16 v[40:43], a[216:219], v[156:159], v[40:43]\n\t" \
  "ds_read_b128 v[132:135], v230 offset:1024\n\t" \
  "v_lshrrev_b32 v236, 4, v233\n\t" \
  "v_mfma_f32_16x16x32_bf16 v[56:59], a[220:223], v[156:159], v[56:59]\n\t" \
  "buffer_store_dwordx4 v[0:3], v242, s[16:19], s68 offen offset:0 sc1\n\t" \
  "ds_read_b128 v[136:139], v230 offset:2048\n\t" \
  "v_lshlrev_b32 v234, 13, v236\n\t" \
  "v_lshl_add_u32 v234, v235, 4, v234\n\t" \
  "v_mfma_f32_16x16x32_bf16 v[56:59], a[212:215], v[152:155], v[56:59]\n\t" \
  "v_lshlrev_b32 v240, 7, v236\n\t" \
  "v_lshl_add_u32 v240, v235, 3, v240\n\t" \
  "v_mfma_f32_16x16x32_bf16 v[56:59], a[204:207], v[148:151], v[56:59]\n\t" \
  "buffer_load_dwordx4 v242, s[16:19], s97 offen offset:3072 sc1 lds\n\t" \
  "v_mfma_f32_16x16x32_bf16 v[56:59], a[196:199], v[144:147], v[56:59]\n\t" \
  "s_waitcnt lgkmcnt(3)\n\t" \
  "ds_read_b128 v[140:143], v230 offset:3072\n\t" \
  "v_cvt_pk_bf16_f32 v8, v40, v41\n\t" \
  "v_mfma_f32_16x16x32_bf16 v[44:47], a[192:195], v[160:163], v[44:47]\n\t" \
  "ds_read_b128 v[144:147], v230 offset:4096\n\t" \
  "v_cvt_pk_bf16_f32 v9, v42, v43\n\t" \
  "v_mfma_f32_16x16x32_bf16 v[44:47], a[200:203], v[164:167], v[44:47]\n\t" \
  "s_waitcnt vmcnt(12)\n\t" \
  "buffer_store_dword v243, v244, s[16:19], s71 offen offset:0 sc1\n\t" \
  "ds_read_b128 v[148:151], v230 offset:5120\n\t" \
  "v_mfma_f32_16x16x32_bf16 v[44:47], a[208:211], v[168:171], v[44:47]\n\t" \
  "v_cvt_pk_bf16_f32 v10, v56, v57\n\t" \
  "v_cvt_pk_bf16_f32 v11, v58, v59\n\t" \
  "v_mfma_f32_16x16x32_bf16 v[44:47], a[216:219], v[172:175], v[44:47]\n\t" \
  "buffer_store_dwordx4 v[4:7], v242, s[16:19], s68 offen offset:1024 sc1\n\t" \
  "ds_read_b128 v[152:155], v230 offset:6144\n\t" \
  "v_add_u32 v240, s76, v240\n\t" \
  "v_lshlrev_b32 v235, 2, v235\n\t" \
  "v_mfma_f32_16x16x32_bf16 v[60:63], a[220:223], v[172:175], v[60:63]\n\t" \
  "ds_read_b128 v[156:159], v230 offset:7168\n\t" \
  "v_mfma_f32_16x16x32_bf16 v[60:63], a[212:215], v[168:171], v[60:63]\n\t" \
  "v_mfma_f32_16x16x32_bf16 v[60:63], a[204:207], v[164:167], v[60:63]\n\t" \
  "v_mfma_f32_16x16x32_bf16 v[60:63], a[196:199], v[160:163], v[60:63]\n\t" \
  "s_waitcnt lgkmcnt(4)\n\t" \
  "v_cvt_pk_bf16_f32 v12, v44, v45\n\t" \
  "v_cvt_pk_bf16_f32 v13, v46, v47\n\t" \
  STP("s_memrealtime s[94:95]\n\t") \
  STP("s_memtime s[92:93]\n\t") \
  "v_mfma_f32_16x16x32_bf16 v[64:67], a[128:131], v[128:131], v[64:67]\n\t" \
  "ds_read_b128 v[160:163], v230 offset:8192\n\t" \
  "v_mfma_f32_16x16x32_bf16 v[64:67], a[136:139], v[132:135], v[64:67]\n\t" \
  "ds_read_b128 v[164:167], v230 offset:9216\n\t" \
  "v_mfma_f32_16x16x32_bf16 v[64:67], a[144:147], v[136:139], v[64:67]\n\t" \
  "v_cvt_pk_bf16_f32 v14, v60, v61\n\t" \
  "v_cvt_pk_bf16_f32 v15, v62, v63\n\t" \
  "v_mfma_f32_16x16x32_bf16 v[64:67], a[152:155], v[140:143], v[64:67]\n\t" \
  "ds_read_b128 v[168:171], v230 offset:10240\n\t" \
  "v_mfma_f32_16x16x32_bf16 v[80:83], a[156:159], v[140:143], v[80:83]\n\t" \
  "ds_read_b128 v[172:175], v230 offset:11264\n\t" \
  "s_add_u32 s78, s75, 0\n\t" \
  "v_mfma_f32_16x16x32_bf16 v[80:83], a[148:151], v[136:139], v[80:83]\n\t" \
  "v_mfma_f32_16x16x32_bf16 v[80:83], a[140:143], v[132:135], v[80:83]\n\t" \
  "v_mfma_f32_16x16x32_bf16 v[80:83], a[132:135], v[128:131], v[80:83]\n\t" \
  "s_waitcnt lgkmcnt(4)\n\t" \
  "v_mfma_f32_16x16x32_bf16 v[68:71], a[128:131], v[144:147], v[68:71]\n\t" \
  "buffer_load_dwordx4 v[208:211], v234, s[16:19], s78 offen sc1\n\t" \
  "v_mfma_f32_16x16x32_bf16 v[68:71], a[136:139], v[148:151], v[68:71]\n\t" \
  "ds_read_b128 v[128:131], v230 offset:12288\n\t" \
  "v_mfma_f32_16x16x32_bf16 v[68:71], a[144:147], v[152:155], v[68:71]\n\t" \
  "ds_read_b128 v[132:135], v230 offset:13312\n\t" \
  "s_add_u32 s78, s75, 32768\n\t" \
  "v_mfma_f32_16x16x32_bf16 v[68:71], a[152:155], v[156:159], v[68:71]\n\t" \
  "ds_read_b128 v[136:139], v230 offset:14336\n\t" \
  "v_mfma_f32_16x16x32_bf16 v[84:87], a[156:159], v[156:159], v[84:87]\n\t" \
  STP("v_writelane_b32 v255, s94, 34\n\t") \
  STP("v_writelane_b32 v255, s92, 35\n\t") \
  "ds_read_b128 v[140:143], v230 offset:15360\n\t" \
  "v_mfma_f32_16x16x32_bf16 v[84:87], a[148:151], v[152:155], v[84:87]\n\t" \
  "v_mfma_f32_16x16x32_bf16 v[84:87], a[140:143], v[148:151], v[84:87]\n\t" \
  "buffer_load_dwordx4 v[212:215], v234, s[16:19], s78 offen sc1\n\t" \
  "v_mfma_f32_16x16x32_bf16 v[84:87], a[132:135], v[144:147], v[84:87]\n\t" \
  "s_waitcnt lgkmcnt(4)\n\t" \
  "s_add_u32 s78, s75, 65536\n\t" \
  "v_mfma_f32_16x16x32_bf16 v[72:75], a[128:131], v[160:163], v[72:75]\n\t" \
  "ds_read_b128 v[144:147], v242 offset:49152\n\t" \
  "v_mfma_f32_16x16x32_bf16 v[72:75], a[136:139], v[164:167], v[72:75]\n\t" \
  "ds_read_b128 v[148:151], v242 offset:50176\n\t" \
  "v_mfma_f32_16x16x32_bf16 v[72:75], a[144:147], v[168:171], v[72:75]\n\t" \
  "ds_read_b128 v[152:155], v242 offset:51200\n\t" \
  "v_mfma_f32_16x16x32_bf16 v[72:75], a[152:155], v[172:175], v[72:75]\n\t" \
  "buffer_load_dwordx4 v[216:219], v234, s[16:19], s78 offen sc1\n\t" \
  "v_mfma_f32_16x16x32_bf16 v[88:91], a[156:159], v[172:175], v[88:91]\n\t" \
  "ds_read_b128 v[156:159], v242 offset:52224\n\t" \
  "s_add_u32 s78, s75, 98304\n\t" \
  "v_mfma_f32_16x16x32_bf16 v[88:91], a[148:151], v[168:171], v[88:91]\n\t" \
  "v_mfma_f32_16x16x32_bf16 v[88:91], a[140:143], v[164:167], v[88:91]\n\t" \
  "v_mfma_f32_16x16x32_bf16 v[88:91], a[132:135], v[160:163], v[88:91]\n\t" \
  "s_waitcnt lgkmcnt(4)\n\t" \
  "v_mfma_f32_16x16x32_bf16 v[76:79], a[128:131], v[128:131], v[76:79]\n\t" \
  "buffer_load_dwordx4 v[220:223], v234, s[16:19], s78 offen sc1\n\t" \
  "v_mfma_f32_16x16x32_bf16 v[76:79], a[136:139], v[132:135], v[76:79]\n\t" \
  "ds_read_b128 v[160:163], v242 offset:53248\n\t" \
  "v_mfma_f32_16x16x32_bf16 v[76:79], a[144:147], v[136:139], v[76:79]\n\t" \
  "ds_read_b128 v[164:167], v242 offset:54272\n\t" \
  "v_mfma_f32_16x16x32_bf16 v[76:79], a[152:155], v[140:143], v[76:79]\n\t" \
  "ds_read_b128 v[168:171], v242 offset:55296\n\t" \
  "v_mfma_f32_16x16x32_bf16 v[92:95], a[156:159], v[140:143], v[92:95]\n\t" \
  "ds_read_b128 v[172:175], v242 offset:56320\n\t" \
  "v_mfma_f32_16x16x32_bf16 v[92:95], a[148:151], v[136:139], v[92:95]\n\t" \
  "buffer_load_dword v224, v235, s[32:35], s80 offen\n\t" \
  "v_mfma_f32_16x16x32_bf16 v[92:95], a[140:143], v[132:135], v[92:95]\n\t" \
  "v_lshrrev_b32 v250, 4, v233\n\t" \
  "v_and_b32 v238, 15, v233\n\t" \
  "v_mfma_f32_16x16x32_bf16 v[92:95], a[132:135], v[128:131], v[92:95]\n\t" \
  "s_waitcnt lgkmcnt(4)\n\t" \
  "v_xor_b32 v26, v238, v250\n\t" \
  "v_lshlrev_b32 v26, 4, v26\n\t" \
  "v_mfma_f32_16x16x32_bf16 v[64:67], a[160:163], v[144:147], v[64:67]\n\t" \
  "ds_read_b128 v[128:131], v242 offset:57344\n\t" \
  "v_lshl_add_u32 v26, v250, 11, v26\n\t" \
  "v_mfma_f32_16x16x32_bf16 v[64:67], a[168:171], v[148:151], v[64:67]\n\t" \
  "v_xor_b32 v27, 64, v26\n\t" \
  "v_xor_b32 v28, 0x80, v26\n\t" \
  "v_mfma_f32_16x16x32_bf16 v[64:67], a[176:179], v[152:155], v[64:67]\n\t" \
  "buffer_store_dwordx4 v[8:11], v242, s[16:19], s68 offen offset:2048 sc1\n\t" \
  "ds_read_b128 v[132:135], v242 offset:58368\n\t" \
  "v_xor_b32 v29, 0xc0, v26\n\t" \
  "v_lshrrev_b32 v30, 5, v233\n\t" \
  "v_mfma_f32_16x16x32_bf16 v[64:67], a[184:187], v[156:159], v[64:67]\n\t" \
  "v_lshl_add_u32 v30, s3, 2, v30\n\t" \
  "v_xor_b32 v30, v30, v238\n\t" \
  "v_mfma_f32_16x16x32_bf16 v[80:83], a[188:191], v[156:159], v[80:83]\n\t" \
  "ds_read_b128 v[136:139], v242 offset:59392\n\t" \
  "v_lshlrev_b32 v30, 4, v30\n\t" \
  "v_mfma_f32_16x16x32_bf16 v[80:83], a[180:183], v[152:155], v[80:83]\n\t" \
  "v_lshl_add_u32 v30, v238, 8, v30\n\t" \
  "v_and_b32 v250, 1, v250\n\t" \
  "v_mfma_f32_16x16x32_bf16 v[80:83], a[172:175], v[148:151], v[80:83]\n\t" \
  "ds_read_b128 v[140:143], v242 offset:60416\n\t" \
  "v_lshl_add_u32 v30, v250, 3, v30\n\t" \
  "v_mfma_f32_16x16x32_bf16 v[80:83], a[164:167], v[144:147], v[80:83]\n\t" \
  "s_waitcnt lgkmcnt(4)\n\t" \
  "v_add_u32 v238, 0, v30\n\t" \
  "v_xor_b32 v239, 32, v238\n\t" \
  "v_mfma_f32_16x16x32_bf16 v[68:71], a[160:163], v[160:163], v[68:71]\n\t" \
  "ds_read_b128 v[144:147], v242 offset:61440\n\t" \
  "v_bfe_u32 v237, v233, 1, 2\n\t" \
  "v_mfma_f32_16x16x32_bf16 v[68:71], a[168:171], v[164:167], v[68:71]\n\t" \
  "v_bfe_u32 v16, v233, 3, 1\n\t" \
  "v_lshlrev_b32 v237, 4, v237\n\t" \
  "v_mfma_f32_16x16x32_bf16 v[68:71], a[176:179], v[168:171], v[68:71]\n\t" \
  "s_add_u32 s97, s64, 786432\n\t" \
  "ds_read_b128 v[148:151], v242 offset:62464\n\t" \
  "v_lshl_add_u32 v237, v16, 3, v237\n\t" \
  "v_mfma_f32_16x16x32_bf16 v[68:71], a[184:187], v[172:175], v[68:71]\n\t" \
  "s_and_b32 s97, s97, 0xe0000\n\t" \
  "v_and_b32 v16, 1, v233\n\t" \
  "v_lshl_add_u32 v237, v16, 2, v237\n\t" \
  "v_mfma_f32_16x16x32_bf16 v[84:87], a[188:191], v[172:175], v[84:87]\n\t" \
  "s_add_u32 s97, s97, s62\n\t" \
  "ds_read_b128 v[152:155], v242 offset:63488\n\t" \
  "v_add_u32 v237, s76, v237\n\t" \
  "v_mfma_f32_16x16x32_bf16 v[84:87], a[180:183], v[168:171], v[84:87]\n\t" \
  "s_add_u32 m0, s38, 65536\n\t" \
  "v_cmp_eq_u32 vcc, 2, v236\n\t" \
  "v_mov_b32 v16, 0\n\t" \
  "v_mfma_f32_16x16x32_bf16 v[84:87], a[172:175], v[164:167], v[84:87]\n\t" \
  "s_waitcnt vmcnt(13) lgkmcnt(3)\n\t" \
  "ds_read_b128 v[156:159], v242 offset:64512\n\t" \
  "v_mov_b32 v17, 0\n\t" \
  "v_mfma_f32_16x16x32_bf16 v[84:87], a[164:167], v[160:163], v[84:87]\n\t" \
  "s_barrier\n\t" \
  "v_mfma_f32_16x16x32_bf16 v[72:75], a[160:163], v[128:131], v[72:75]\n\t" \
  "buffer_load_dwordx4 v242, s[16:19], s97 offen offset:0 sc1 lds\n\t" \
  "v_mfma_f32_16x16x32_bf16 v[72:75], a[168:171], v[132:135], v[72:75]\n\t" \
  "ds_read_b128 v[160:163], v230 offset:32768\n\t" \
  "v_mov_b32 v18, 0\n\t" \
  "v_mfma_f32_16x16x32_bf16 v[72:75], a[176:179], v[136:139], v[72:75]\n\t" \
  "ds_read_b128 v[164:167], v230 offset:33792\n\t" \
  "v_mov_b32 v19, 0\n\t" \
  "v_mfma_f32_16x16x32_bf16 v[72:75], a[184:187], v[140:143], v[72:75]\n\t" \
  "buffer_load_dwordx4 v242, s[16:19], s97 offen offset:1024 sc1 lds\n\t" \
  "v_mfma_f32_16x16x32_bf16 v[88:91], a[188:191], v[140:143], v[88:91]\n\t" \
  "ds_read_b128 v[168:171], v230 offset:34816\n\t" \
  "v_cndmask_b32 v236, 0, 1.0, vcc\n\t" \
  "v_mfma_f32_16x16x32_bf16 v[88:91], a[180:183], v[136:139], v[88:91]\n\t" \
  "v_mov_b32 v20, 0x3f803f80\n\t" \
  "v_add_u32 v21, s76, v242\n\t" \
  "v_mfma_f32_16x16x32_bf16 v[88:91], a[172:175], v[132:135], v[88:91]\n\t" \
  "ds_write_b128 v21, v[16:19] offset:0\n\t" \
  "v_mfma_f32_16x16x32_bf16 v[88:91], a[164:167], v[128:131], v[88:91]\n\t" \
  "s_waitcnt lgkmcnt(4)\n\t" \
  "buffer_load_dwordx4 v242, s[16:19], s97 offen offset:2048 sc1 lds\n\t" \
  "v_mfma_f32_16x16x32_bf16 v[76:79], a[160:163], v[144:147], v[76:79]\n\t" \
  "ds_read_b128 v[172:175], v230 offset:35840\n\t" \
  "v_mfma_f32_16x16x32_bf16 v[76:79], a[168:171], v[148:151], v[76:79]\n\t" \
  "ds_write_b128 v21, v[16:19] offset:1024\n\t" \
  "v_mfma_f32_16x16x32_bf16 v[76:79], a[176:179], v[152:155], v[76:79]\n\t" \
  "buffer_load_dwordx4 v242, s[16:19], s97 offen offset:3072 sc1 lds\n\t" \
  "v_mfma_f32_16x16x32_bf16 v[76:79], a[184:187], v[156:159], v[76:79]\n\t" \
  "ds_read_b128 v[128:131], v230 offset:36864\n\t" \
  "v_mfma_f32_16x16x32_bf16 v[92:95], a[188:191], v[156:159], v[92:95]\n\t" \
  "ds_read_b128 v[132:135], v230 offset:37888\n\t" \
  "v_mfma_f32_16x16x32_bf16 v[92:95], a[180:183], v[152:155], v[92:95]\n\t" \
  "ds_write_b128 v21, v[16:19] offset:1536\n\t" \
  "v_mfma_f32_16x16x32_bf16 v[92:95], a[172:175], v[148:151], v[92:95]\n\t" \
  "buffer_store_dwordx4 v[12:15], v242, s[16:19], s68 offen offset:3072 sc1\n\t" \
  "ds_read_b128 v[136:139], v230 offset:38912\n\t" \
  "ds_read_b128 v[140:143], v230 offset:39936\n\t" \
  "v_mfma_f32_16x16x32_bf16 v[92:95], a[164:167], v[144:147], v[92:95]\n\t" \
  "s_waitcnt lgkmcnt(6)\n\t" \
  "s_mov_b64 exec, 0xffff\n\t" \
  "ds_write_b32 v237, v20 offset:960\n\t" \
  "s_mov_b64 exec, -1\n\t" \
  "v_mfma_f32_16x16x32_bf16 v[64:67], a[0:3], v[160:163], v[64:67]\n\t" \
  "ds_read_b128 v[144:147], v230 offset:40960\n\t" \
  "v_mfma_f32_16x16x32_bf16 v[64:67], a[8:11], v[164:167], v[64:67]\n\t" \
  "ds_read_b128 v[148:151], v230 offset:41984\n\t" \
  "v_mfma_f32_16x16x32_bf16 v[64:67], a[16:19], v[168:171], v[64:67]\n\t" \
  "ds_read_b128 v[152:155], v230 offset:43008\n\t" \
  "v_mfma_f32_16x16x32_bf16 v[64:67], a[24:27], v[172:175], v[64:67]\n\t" \
  "ds_read_b128 v[156:159], v230 offset:44032\n\t" \
  "v_mfma_f32_16x16x32_bf16 v[80:83], a[28:31], v[172:175], v[80:83]\n\t" \
  "v_mfma_f32_16x16x32_bf16 v[80:83], a[20:23], v[168:171], v[80:83]\n\t" \
  "s_add_u32 m0, s38, 0\n\t" \
  "s_add_u32 s96, s50, 0\n\t" \
  "v_mfma_f32_16x16x32_bf16 v[80:83], a[12:15], v[164:167], v[80:83]\n\t" \
  "v_mfma_f32_16x16x32_bf16 v[80:83], a[4:7], v[160:163], v[80:83]\n\t" \
  "s_waitcnt lgkmcnt(5)\n\t" \
  "v_mfma_f32_16x16x32_bf16 v[68:71], a[0:3], v[128:131], v[68:71]\n\t" \
  "ds_read_b128 v[160:163], v230 offset:45056\n\t" \
  "v_mfma_f32_16x16x32_bf16 v[68:71], a[8:11], v[132:135], v[68:71]\n\t" \
  "ds_read_b128 v[164:167], v230 offset:46080\n\t" \
  "v_mfma_f32_16x16x32_bf16 v[68:71], a[16:19], v[136:139], v[68:71]\n\t" \
  "ds_read_b128 v[168:171], v230 offset:47104\n\t" \
  "v_mfma_f32_16x16x32_bf16 v[68:71], a[24:27], v[140:143], v[68:71]\n\t" \
  "buffer_load_dwordx4 v26, s[4:7], s96 offen offset:0 lds\n\t" \
  "v_mfma_f32_16x16x32_bf16 v[84:87], a[28:31], v[140:143], v[84:87]\n\t" \
  "s_add_u32 s96, s50, 7168\n\t" \
  "ds_read_b128 v[172:175], v230 offset:48128\n\t" \
  "v_mfma_f32_16x16x32_bf16 v[84:87], a[20:23], v[136:139], v[84:87]\n\t" \
  "v_mfma_f32_16x16x32_bf16 v[84:87], a[12:15], v[132:135], v[84:87]\n\t" \
  "v_mfma_f32_16x16x32_bf16 v[84:87], a[4:7], v[128:131], v[84:87]\n\t" \
  "s_waitcnt lgkmcnt(4)\n\t" \
  "v_mfma_f32_16x16x32_bf16 v[72:75], a[0:3], v[144:147], v[72:75]\n\t" \
  "buffer_load_dwordx4 v27, s[4:7], s96 offen offset:1024 lds\n\t" \
  "v_mfma_f32_16x16x32_bf16 v[72:75], a[8:11], v[148:151], v[72:75]\n\t" \
  "s_add_u32 s96, s50, 14336\n\t" \
  "ds_read_b128 v[128:131], v230 offset:16384\n\t" \
  "v_mfma_f32_16x16x32_bf16 v[72:75], a[16:19], v[152:155], v[72:75]\n\t" \
  "ds_read_b128 v[132:135], v230 offset:17408\n\t" \
  "v_mfma_f32_16x16x32_bf16 v[72:75], a[24:27], v[156:159], v[72:75]\n\t" \
  "ds_read_b128 v[136:139], v230 offset:18432\n\t" \
  "v_mfma_f32_16x16x32_bf16 v[88:91], a[28:31], v[156:159], v[88:91]\n\t" \
  "ds_read_b128 v[140:143], v230 offset:19456\n\t" \
  "v_mfma_f32_16x16x32_bf16 v[88:91], a[20:23], v[152:155], v[88:91]\n\t" \
  "buffer_load_dwordx4 v28, s[4:7], s96 offen offset:2048 lds\n\t" \
  "v_mfma_f32_16x16x32_bf16 v[88:91], a[12:15], v[148:151], v[88:91]\n\t" \
  "s_add_u32 s96, s50, 21504\n\t" \
  "v_mfma_f32_16x16x32_bf16 v[88:91], a[4:7], v[144:147], v[88:91]\n\t" \
  "s_waitcnt lgkmcnt(4)\n\t" \
  "v_mfma_f32_16x16x32_bf16 v[76:79], a[0:3], v[160:163], v[76:79]\n\t" \
  "ds_read_b128 v[144:147], v230 offset:20480\n\t" \
  "v_mfma_f32_16x16x32_bf16 v[76:79], a[8:11], v[164:167], v[76:79]\n\t" \
  "ds_read_b128 v[148:151], v230 offset:21504\n\t" \
  "v_mfma_f32_16x16x32_bf16 v[76:79], a[16:19], v[168:171], v[76:79]\n\t" \
  "buffer_load_dwordx4 v29, s[4:7], s96 offen offset:3072 lds\n\t" \
  "v_mfma_f32_16x16x32_bf16 v[76:79], a[24:27], v[172:175], v[76:79]\n\t" \
  "s_mov_b32 s40, 0x05040100\n\t" \
  "ds_read_b128 v[152:155], v230 offset:22528\n\t" \
  "s_mov_b32 s41, 0x07060302\n\t" \
  "v_mfma_f32_16x16x32_bf16 v[92:95], a[28:31], v[172:175], v[92:95]\n\t" \
  "s_load_dwordx4 s[4:7], s[0:1], 0xc0\n\t" \
  "ds_read_b128 v[156:159], v230 offset:23552\n\t" \
  "s_and_b32 s42, s2, 4\n\t" \
  "v_mfma_f32_16x16x32_bf16 v[92:95], a[20:23], v[168:171], v[92:95]\n\t" \
  "s_lshl_b32 s9, s42, 11\n\t" \
  "s_xor_b32 s9, s9, s55\n\t" \
  "s_lshr_b32 s43, s2, 6\n\t" \
  "v_mfma_f32_16x16x32_bf16 v[92:95], a[12:15], v[164:167], v[92:95]\n\t" \
  "s_add_u32 s97, s64, 917504\n\t" \
  "s_and_b32 s97, s97, 0xe0000\n\t" \
  "s_add_u32 s97, s97, s62\n\t" \
  "v_mfma_f32_16x16x32_bf16 v[92:95], a[4:7], v[160:163], v[92:95]\n\t" \
  "s_waitcnt lgkmcnt(4)\n\t" \
  "s_add_u32 m0, s38, 16384\n\t" \
  "s_add_u32 s42, s42, s43\n\t" \
  "v_mfma_f32_16x16x32_bf16 v[64:67], a[32:35], v[128:131], v[64:67]\n\t" \
  "s_waitcnt vmcnt(10)\n\t" \
  "v_xor_b32 v246, v243, v209\n\t" \
  "v_bitop3_b32 v246, v246, v211, v243 bitop3:0xf6\n\t" \
  "v_mfma_f32_16x16x32_bf16 v[64:67], a[40:43], v[132:135], v[64:67]\n\t" \
  "s_and_b32 s43, s2, 3\n\t" \
  "v_bitop3_b32 v246, v246, v213, v243 bitop3:0xf6\n\t" \
  "v_bitop3_b32 v246, v246, v215, v243 bitop3:0xf6\n\t" \
  "v_mfma_f32_16x16x32_bf16 v[64:67], a[48:51], v[136:139], v[64:67]\n\t" \
  "s_lshl2_add_u32 s8, s42, s43\n\t" \
  "v_bitop3_b32 v246, v246, v217, v243 bitop3:0xf6\n\t" \
  "v_bitop3_b32 v246, v246, v219, v243 bitop3:0xf6\n\t" \
  "v_mfma_f32_16x16x32_bf16 v[64:67], a[56:59], v[140:143], v[64:67]\n\t" \
  "buffer_load_dwordx4 v242, s[16:19], s97 offen offset:0 sc1 lds\n\t" \
  "v_mfma_f32_16x16x32_bf16 v[80:83], a[60:63], v[140:143], v[80:83]\n\t" \
  "s_and_b32 s11, s8, 15\n\t" \
  "v_bitop3_b32 v246, v246, v221, v243 bitop3:0xf6\n\t" \
  "v_bitop3_b32 v246, v246, v223, v243 bitop3:0xf6\n\t" \
  "v_mfma_f32_16x16x32_bf16 v[80:83], a[52:55], v[136:139], v[80:83]\n\t" \
  "buffer_load_dwordx4 v242, s[16:19], s97 offen offset:1024 sc1 lds\n\t" \
  "ds_read_b128 v[160:163], v230 offset:24576\n\t" \
  "s_lshl_b32 s11, s11, 15\n\t" \
  "v_mfma_f32_16x16x32_bf16 v[80:83], a[44:47], v[132:135], v[80:83]\n\t" \
  "s_add_u32 s11, s11, s9\n\t" \
  "ds_read_b128 v[164:167], v230 offset:25600\n\t" \
  "v_cmp_ne_u32 vcc, 0, v246\n\t" \
  "v_mfma_f32_16x16x32_bf16 v[80:83], a[36:39], v[128:131], v[80:83]\n\t" \
  "s_waitcnt lgkmcnt(2)\n\t" \
  "buffer_load_dwordx4 v242, s[16:19], s97 offen offset:2048 sc1 lds\n\t" \
  "v_mfma_f32_16x16x32_bf16 v[68:71], a[32:35], v[144:147], v[68:71]\n\t" \
  "s_cbranch_vccnz .Lat_s3\n\t" \
  ".Lat_k3:\n\t" \
  "v_mfma_f32_16x16x32_bf16 v[68:71], a[40:43], v[148:151], v[68:71]\n\t" \
  "s_add_u32 s11, s11, 524288\n\t" \
  "ds_read_b128 v[168:171], v230 offset:26624\n\t" \
  "v_add_f32 v208, v208, v212\n\t" \
  "v_mfma_f32_16x16x32_bf16 v[68:71], a[48:51], v[152:155], v[68:71]\n\t" \
  "s_and_b32 s42, s2, 0x20\n\t" \
  "ds_read_b128 v[172:175], v230 offset:27648\n\t" \
  "v_add_f32 v216, v216, v220\n\t" \
  "v_mfma_f32_16x16x32_bf16 v[68:71], a[56:59], v[156:159], v[68:71]\n\t" \
  "buffer_load_dwordx4 v242, s[16:19], s97 offen offset:3072 sc1 lds\n\t" \
  "v_mfma_f32_16x16x32_bf16 v[84:87], a[60:63], v[156:159], v[84:87]\n\t" \
  "s_add_u32 s97, s64, 131072\n\t" \
  "s_and_b32 s97, s97, 0xe0000\n\t" \
  "s_add_u32 s97, s97, s63\n\t" \
  "v_mfma_f32_16x16x32_bf16 v[84:87], a[52:55], v[152:155], v[84:87]\n\t" \
  "s_add_u32 m0, s38, 32768\n\t" \
  "ds_read_b128 v[128:131], v230 offset:28672\n\t" \
  "v_add_f32 v210, v210, v214\n\t" \
  "v_mfma_f32_16x16x32_bf16 v[84:87], a[44:47], v[148:151], v[84:87]\n\t" \
  "s_waitcnt vmcnt(9) lgkmcnt(1)\n\t" \
  "buffer_load_dwordx4 v242, s[16:19], s97 offen offset:0 sc1 lds\n\t" \
  "v_mfma_f32_16x16x32_bf16 v[84:87], a[36:39], v[144:147], v[84:87]\n\t" \
  "s_barrier\n\t" \
  "v_mfma_f32_16x16x32_bf16 v[72:75], a[32:35], v[160:163], v[72:75]\n\t" \
  "s_lshl_b32 s42, s42, 9\n\t" \
  "ds_read_b128 v[132:135], v230 offset:29696\n\t" \
  "v_add_f32 v218, v218, v222\n\t" \
  "v_mfma_f32_16x16x32_bf16 v[72:75], a[40:43], v[164:167], v[72:75]\n\t" \
  "s_add_u32 s10, s11, s42\n\t" \
  "ds_read_b128 v[136:139], v230 offset:30720\n\t" \
  "v_add_f32 v208, v208, v216\n\t" \
  "v_mfma_f32_16x16x32_bf16 v[72:75], a[48:51], v[168:171], v[72:75]\n\t" \
  "s_and_b32 s42, s2, 0x18\n\t" \
  "ds_read_b128 v[140:143], v230 offset:31744\n\t" \
  "v_add_f32 v210, v210, v218\n\t" \
  "v_mfma_f32_16x16x32_bf16 v[72:75], a[56:59], v[172:175], v[72:75]\n\t" \
  "buffer_load_dwordx4 v242, s[16:19], s97 offen offset:1024 sc1 lds\n\t" \
  "v_mfma_f32_16x16x32_bf16 v[88:91], a[60:63], v[172:175], v[88:91]\n\t" \
  "s_lshl_b32 s42, s42, 8\n\t" \
  "ds_read_b128 v[144:147], v232 offset:0\n\t" \
  "v_lshlrev_b32 v19, 16, v224\n\t" \
  "v_mfma_f32_16x16x32_bf16 v[88:91], a[52:55], v[168:171], v[88:91]\n\t" \
  "s_add_u32 s10, s10, s42\n\t" \
  "ds_read_b128 v[148:151], v232 offset:1024\n\t" \
  "v_and_b32 v20, 0xffff0000, v224\n\t" \
  "v_mfma_f32_16x16x32_bf16 v[88:91], a[44:47], v[164:167], v[88:91]\n\t" \
  "s_mul_i32 s42, s3, 0x3000\n\t" \
  "ds_read_b128 v[152:155], v232 offset:2048\n\t" \
  "v_cvt_pk_bf16_f32 v18, v208, v210\n\t" \
  "v_mfma_f32_16x16x32_bf16 v[88:91], a[36:39], v[160:163], v[88:91]\n\t" \
  "s_waitcnt lgkmcnt(3)\n\t" \
  "ds_read_b128 v[156:159], v232 offset:3072\n\t" \
  "v_lshlrev_b32 v16, 16, v18\n\t" \
  "v_mfma_f32_16x16x32_bf16 v[76:79], a[32:35], v[128:131], v[76:79]\n\t" \
  "s_and_b32 s42, s42, 0x1000\n\t" \
  "ds_read_b128 v[160:163], v232 offset:4096\n\t" \
  "v_and_b32 v17, 0xffff0000, v18\n\t" \
  "v_mfma_f32_16x16x32_bf16 v[76:79], a[40:43], v[132:135], v[76:79]\n\t" \
  "s_add_u32 s11, s11, s42\n\t" \
  "v_add_f32 v16, v16, v236\n\t" \
  "v_add_f32 v17, v17, v236\n\t" \
  "v_mfma_f32_16x16x32_bf16 v[76:79], a[48:51], v[136:139], v[76:79]\n\t" \
  "s_and_b32 s42, s3, 2\n\t" \
  "ds_read_b128 v[164:167], v232 offset:5120\n\t" \
  "v_cvt_pk_bf16_f32 v18, v16, v17\n\t" \
  "v_mfma_f32_16x16x32_bf16 v[76:79], a[56:59], v[140:143], v[76:79]\n\t" \
  "s_lshl_b32 s42, s42, 13\n\t" \
  "v_lshlrev_b32 v16, 16, v18\n\t" \
  "v_and_b32 v17, 0xffff0000, v18\n\t" \
  "v_mfma_f32_16x16x32_bf16 v[92:95], a[60:63], v[140:143], v[92:95]\n\t" \
  "buffer_load_dwordx4 v242, s[16:19], s97 offen offset:2048 sc1 lds\n\t" \
  "v_mfma_f32_16x16x32_bf16 v[92:95], a[52:55], v[136:139], v[92:95]\n\t" \
  "s_add_u32 s11, s11, s42\n\t" \
  "ds_read_b128 v[168:171], v232 offset:6144\n\t" \
  "s_add_u32 s12, s11, 2048\n\t" \
  "v_mfma_f32_16x16x32_bf16 v[92:95], a[44:47], v[132:135], v[92:95]\n\t" \
  "ds_read_b128 v[172:175], v232 offset:7168\n\t" \
  "s_sub_u32 s39, s10, s11\n\t" \
  "v_mfma_f32_16x16x32_bf16 v[92:95], a[36:39], v[128:131], v[92:95]\n\t" \
  "s_waitcnt lgkmcnt(4)\n\t" \
  "ds_write_b64 v240, v[16:17] offset:2560\n\t" \
  "v_mfma_f32_16x16x32_bf16 v[64:67], a[64:67], v[144:147], v[64:67]\n\t" \
  "ds_read_b128 v[128:131], v232 offset:8192\n\t" \
  "v_mul_f32 v22, v16, v19\n\t" \
  "v_mfma_f32_16x16x32_bf16 v[64:67], a[72:75], v[148:151], v[64:67]\n\t" \
  "s_waitcnt vmcnt(11)\n\t" \
  "buffer_store_dword v243, v244, s[16:19], s72 offen offset:16 sc1\n\t" \
  "ds_read_b128 v[132:135], v232 offset:9216\n\t" \
  "v_mfma_f32_16x16x32_bf16 v[64:67], a[80:83], v[152:155], v[64:67]\n\t" \
  "v_mul_f32 v23, v17, v20\n\t" \
  "v_cvt_pk_bf16_f32 v24, v22, v23\n\t" \
  "v_mfma_f32_16x16x32_bf16 v[64:67], a[88:91], v[156:159], v[64:67]\n\t" \
  "v_add_f32 v18, v22, v23\n\t" \
  "v_lshlrev_b32 v19, 16, v24\n\t" \
  "v_mfma_f32_16x16x32_bf16 v[80:83], a[92:95], v[156:159], v[80:83]\n\t" \
  "ds_read_b128 v[136:139], v232 offset:10240\n\t" \
  "v_and_b32 v20, 0xffff0000, v24\n\t" \
  "v_mfma_f32_16x16x32_bf16 v[80:83], a[84:87], v[152:155], v[80:83]\n\t" \
  "v_add_f32_dpp v18, v18, v18 row_ror:8 row_mask:0xf bank_mask:0xf\n\t" \
  "v_sub_f32 v22, v22, v19\n\t" \
  "v_mfma_f32_16x16x32_bf16 v[80:83], a[76:79], v[148:151], v[80:83]\n\t" \
  "ds_read_b128 v[140:143], v232 offset:11264\n\t" \
  "v_sub_f32 v23, v23, v20\n\t" \
  "v_mfma_f32_16x16x32_bf16 v[80:83], a[68:71], v[144:147], v[80:83]\n\t" \
  "s_waitcnt lgkmcnt(5)\n\t" \
  "v_add_f32_dpp v18, v18, v18 row_ror:4 row_mask:0xf bank_mask:0xf\n\t" \
  "v_cvt_pk_bf16_f32 v25, v22, v23\n\t" \
  "v_mfma_f32_16x16x32_bf16 v[68:71], a[64:67], v[160:163], v[68:71]\n\t" \
  "s_mov_b32 s84, 0\n\t" \
  "ds_read_b128 v[144:147], v232 offset:12288\n\t" \
  "v_add_f32_dpp v18, v18, v18 row_ror:2 row_mask:0xf bank_mask:0xf\n\t" \
  "v_mfma_f32_16x16x32_bf16 v[68:71], a[72:75], v[164:167], v[68:71]\n\t" \
  "s_mov_b32 s85, 0xffff\n\t" \
  "ds_read_b128 v[148:151], v232 offset:13312\n\t" \
  "v_add_f32_dpp v18, v18, v18 row_ror:1 row_mask:0xf bank_mask:0xf\n\t" \
  "v_mfma_f32_16x16x32_bf16 v[68:71], a[80:83], v[168:171], v[68:71]\n\t" \
  "s_mov_b64 exec, s[84:85]\n\t" \
  "ds_write_b32 v237, v24 offset:1040\n\t" \
  "ds_write_b32 v237, v25 offset:1120\n\t" \
  "s_mov_b64 exec, -1\n\t" \
  "v_mfma_f32_16x16x32_bf16 v[68:71], a[88:91], v[172:175], v[68:71]\n\t" \
  "v_readlane_b32 s82, v18, 32\n\t" \
  "v_readlane_b32 s83, v18, 16\n\t" \
  "v_mfma_f32_16x16x32_bf16 v[84:87], a[92:95], v[172:175], v[84:87]\n\t" \
  "buffer_load_dwordx4 v242, s[16:19], s97 offen offset:3072 sc1 lds\n\t" \
  "v_mfma_f32_16x16x32_bf16 v[84:87], a[84:87], v[168:171], v[84:87]\n\t" \
  "s_add_u32 s97, s64, 262144\n\t" \
  "ds_read_b128 v[152:155], v232 offset:14336\n\t" \
  "v_and_b32 v235, 16, v242\n\t" \
  "v_mfma_f32_16x16x32_bf16 v[84:87], a[76:79], v[164:167], v[84:87]\n\t" \
  "s_and_b32 s97, s97, 0xe0000\n\t" \
  "ds_read_b128 v[156:159], v232 offset:15360\n\t" \
  "s_add_u32 s97, s97, s63\n\t" \
  "v_mfma_f32_16x16x32_bf16 v[84:87], a[68:71], v[160:163], v[84:87]\n\t" \
  "s_waitcnt lgkmcnt(6)\n\t" \
  "s_add_u32 m0, s38, 49152\n\t" \
  "v_mfma_f32_16x16x32_bf16 v[72:75], a[64:67], v[128:131], v[72:75]\n\t" \
  "ds_read_b128 v[160:163], v230 offset:0\n\t" \
  "v_mfma_f32_16x16x32_bf16 v[72:75], a[72:75], v[132:135], v[72:75]\n\t" \
  "buffer_load_dwordx4 v242, s[16:19], s97 offen offset:0 sc1 lds\n\t" \
  "v_mfma_f32_16x16x32_bf16 v[72:75], a[80:83], v[136:139], v[72:75]\n\t" \
  "ds_read_b128 v[164:167], v230 offset:1024\n\t" \
  "v_mfma_f32_16x16x32_bf16 v[72:75], a[88:91], v[140:143], v[72:75]\n\t" \
  "s_cmp_eq_u32 s39, 0\n\t" \
  "s_cselect_b32 s11, s12, s11\n\t" \
  "v_mfma_f32_16x16x32_bf16 v[88:91], a[92:95], v[140:143], v[88:91]\n\t" \
  "ds_read_b128 v[168:171], v230 offset:2048\n\t" \
  "v_mfma_f32_16x16x32_bf16 v[88:91], a[84:87], v[136:139], v[88:91]\n\t" \
  "s_cmp_eq_u32 s39, 2048\n\t" \
  "s_cselect_b32 s12, s11, s12\n\t" \
  "s_lshl_b32 s42, s3, 10\n\t" \
  "v_mfma_f32_16x16x32_bf16 v[88:91], a[76:79], v[132:135], v[88:91]\n\t" \
  "buffer_load_dwordx4 v242, s[16:19], s97 offen offset:1024 sc1 lds\n\t" \
  "v_mfma_f32_16x16x32_bf16 v[88:91], a[68:71], v[128:131], v[88:91]\n\t" \
  "s_waitcnt lgkmcnt(3)\n\t" \
  "ds_read_b128 v[172:175], v230 offset:3072\n\t" \
  "s_add_u32 s10, s10, s42\n\t" \
  "v_mfma_f32_16x16x32_bf16 v[76:79], a[64:67], v[144:147], v[76:79]\n\t" \
  "s_lshr_b32 s42, s2, 1\n\t" \
  "ds_read_b128 v[128:131], v230 offset:4096\n\t" \
  "s_and_b32 s42, s42, 12\n\t" \
  "v_mfma_f32_16x16x32_bf16 v[76:79], a[72:75], v[148:151], v[76:79]\n\t" \
  "buffer_load_dwordx4 v242, s[16:19], s97 offen offset:2048 sc1 lds\n\t" \
  "v_mfma_f32_16x16x32_bf16 v[76:79], a[80:83], v[152:155], v[76:79]\n\t" \
  "s_or_b32 s42, s42, s43\n\t" \
  "ds_read_b128 v[132:135], v230 offset:5120\n\t" \
  "s_lshl_b32 s13, s42, 8\n\t" \
  "v_mfma_f32_16x16x32_bf16 v[76:79], a[88:91], v[156:159], v[76:79]\n\t" \
  "s_add_u32 s13, s13, s9\n\t" \
  "ds_read_b128 v[136:139], v230 offset:6144\n\t" \
  "s_add_u32 s13, s13, 65536\n\t" \
  "v_mfma_f32_16x16x32_bf16 v[92:95], a[92:95], v[156:159], v[92:95]\n\t" \
  "buffer_load_dwordx4 v242, s[16:19], s97 offen offset:3072 sc1 lds\n\t" \
  "v_mfma_f32_16x16x32_bf16 v[92:95], a[84:87], v[152:155], v[92:95]\n\t" \
  "s_add_u32 s97, s64, 393216\n\t" \
  "s_and_b32 s97, s97, 0xe0000\n\t" \
  "s_add_u32 s97, s97, s63\n\t" \
  "v_mfma_f32_16x16x32_bf16 v[92:95], a[76:79], v[148:151], v[92:95]\n\t" \
  "s_add_u32 m0, s38, 98304\n\t" \
  "ds_read_b128 v[140:143], v230 offset:7168\n\t" \
  "s_bfe_u32 s44, s2, 0x30003\n\t" \
  "v_mfma_f32_16x16x32_bf16 v[92:95], a[68:71], v[144:147], v[92:95]\n\t" \
  "s_waitcnt lgkmcnt(4)\n\t" \
  "buffer_load_dwordx4 v242, s[16:19], s97 offen offset:0 sc1 lds\n\t" \
  "v_mfma_f32_16x16x32_bf16 v[64:67], a[96:99], v[160:163], v[64:67]\n\t" \
  "s_lshl_b32 s42, s3, 13\n\t" \
  "ds_read_b128 v[144:147], v230 offset:8192\n\t" \
  "s_and_b32 s42, s42, 0x2000\n\t" \
  "v_mfma_f32_16x16x32_bf16 v[64:67], a[104:107], v[164:167], v[64:67]\n\t" \
  "s_xor_b32 s14, s42, s55\n\t" \
  "ds_read_b128 v[148:151], v230 offset:9216\n\t" \
  "s_lshr_b32 s42, s3, 1\n\t" \
  "v_mfma_f32_16x16x32_bf16 v[64:67], a[112:115], v[168:171], v[64:67]\n\t" \
  ".Lhs14:\n\ts_cbranch_execz .Lhs14n\n\t" \
  "buffer_load_dwordx4 v242, s[16:19], s97 offen offset:1024 sc1 lds\n\t" \
  "v_mfma_f32_16x16x32_bf16 v[64:67], a[120:123], v[172:175], v[64:67]\n\t" \
  "s_lshl1_add_u32 s42, s44, s42\n\t" \
  "ds_read_b128 v[152:155], v230 offset:10240\n\t" \
  "s_lshl_b32 s42, s42, 8\n\t" \
  "v_mfma_f32_16x16x32_bf16 v[80:83], a[124:127], v[172:175], v[80:83]\n\t" \
  "s_add_u32 s14, s14, s42\n\t" \
  "ds_read_b128 v[156:159], v230 offset:11264\n\t" \
  "s_lshl3_add_u32 s14, s8, s14\n\t" \
  "v_mfma_f32_16x16x32_bf16 v[80:83], a[116:119], v[168:171], v[80:83]\n\t" \
  "s_add_u32 s14, s14, 65536\n\t" \
  "s_add_u32 s15, s9, 262144\n\t" \
  "s_lshl_b32 s42, s8, 13\n\t" \
  "v_mfma_f32_16x16x32_bf16 v[80:83], a[108:111], v[164:167], v[80:83]\n\t" \
  "s_and_b32 s42, s42, 0x3c000\n\t" \
  "s_add_u32 s20, s15, s42\n\t" \
  "s_and_b32 s42, s8, 1\n\t" \
  "v_mfma_f32_16x16x32_bf16 v[80:83], a[100:103], v[160:163], v[80:83]\n\t" \
  "s_waitcnt lgkmcnt(4)\n\t" \
  "s_lshl_b32 s42, s42, 12\n\t" \
  "s_add_u32 s20, s20, s42\n\t" \
  "v_mfma_f32_16x16x32_bf16 v[68:71], a[96:99], v[128:131], v[68:71]\n\t" \
  "s_lshl2_add_u32 s42, s44, s3\n\t" \
  "ds_read_b128 v[160:163], v230 offset:12288\n\t" \
  "s_lshl_b32 s42, s42, 7\n\t" \
  "v_mfma_f32_16x16x32_bf16 v[68:71], a[104:107], v[132:135], v[68:71]\n\t" \
  "s_add_u32 s20, s20, s42\n\t" \
  "ds_read_b128 v[164:167], v230 offset:13312\n\t" \
  "s_and_b32 s21, s42, 0x7ff\n\t" \
  "v_mfma_f32_16x16x32_bf16 v[68:71], a[112:115], v[136:139], v[68:71]\n\t" \
  "buffer_load_dwordx4 v242, s[16:19], s97 offen offset:2048 sc1 lds\n\t" \
  "v_mfma_f32_16x16x32_bf16 v[68:71], a[120:123], v[140:143], v[68:71]\n\t" \
  "s_lshl_b32 s22, s8, 17\n\t" \
  "ds_read_b128 v[168:171], v230 offset:14336\n\t" \
  "s_lshr_b32 s42, s42, 1\n\t" \
  "v_mfma_f32_16x16x32_bf16 v[84:87], a[124:127], v[140:143], v[84:87]\n\t" \
  "s_add_u32 s22, s22, s42\n\t" \
  "ds_read_b128 v[172:175], v230 offset:15360\n\t" \
  "s_and_b32 s42, s2, 0x20\n\t" \
  "v_mfma_f32_16x16x32_bf16 v[84:87], a[116:119], v[136:139], v[84:87]\n\t" \
  "s_lshl_b32 s42, s42, 8\n\t" \
  "s_xor_b32 s42, s42, s55\n\t" \
  "s_add_u32 s21, s21, s42\n\t" \
  "v_mfma_f32_16x16x32_bf16 v[84:87], a[108:111], v[132:135], v[84:87]\n\t" \
  "s_waitcnt vmcnt(7) lgkmcnt(4)\n\t" \
  "buffer_load_dwordx4 v242, s[16:19], s97 offen offset:3072 sc1 lds\n\t" \
  "v_mfma_f32_16x16x32_bf16 v[84:87], a[100:103], v[128:131], v[84:87]\n\t" \
  "s_barrier\n\t" \
  "v_mfma_f32_16x16x32_bf16 v[72:75], a[96:99], v[144:147], v[72:75]\n\t" \
  "s_add_u32 s97, s64, 524288\n\t" \
  "s_and_b32 s97, s97, 0xe0000\n\t" \
  "s_add_u32 s97, s97, s63\n\t" \
  "v_mfma_f32_16x16x32_bf16 v[72:75], a[104:107], v[148:151], v[72:75]\n\t" \
  "s_add_u32 m0, s38, 81920\n\t" \
  "ds_read_b128 v[128:131], v242 offset:16384\n\t" \
  "s_lshl_b32 s42, s43, 11\n\t" \
  "v_mfma_f32_16x16x32_bf16 v[72:75], a[112:115], v[152:155], v[72:75]\n\t" \
  "buffer_load_dwordx4 v242, s[16:19], s97 offen offset:0 sc1 lds\n\t" \
  "v_mfma_f32_16x16x32_bf16 v[72:75], a[120:123], v[156:159], v[72:75]\n\t" \
  "s_add_u32 s21, s21, s42\n\t" \
  "ds_read_b128 v[132:135], v242 offset:17408\n\t" \
  "s_lshr_b32 s42, s8, 2\n\t" \
  "v_mfma_f32_16x16x32_bf16 v[88:91], a[124:127], v[156:159], v[88:91]\n\t" \
  "buffer_load_dwordx4 v242, s[16:19], s97 offen offset:1024 sc1 lds\n\t" \
  "v_mfma_f32_16x16x32_bf16 v[88:91], a[116:119], v[152:155], v[88:91]\n\t" \
  "s_lshl_b32 s42, s42, 14\n\t" \
  "ds_read_b128 v[136:139], v242 offset:18432\n\t" \
  "s_add_u32 s21, s21, s42\n\t" \
  "v_mfma_f32_16x16x32_bf16 v[88:91], a[108:111], v[148:151], v[88:91]\n\t" \
  "s_add_u32 s21, s21, 1966080\n\t" \
  "ds_read_b128 v[140:143], v242 offset:19456\n\t" \
  "s_lshl_b32 s42, s59, 7\n\t" \
  "v_mfma_f32_16x16x32_bf16 v[88:91], a[100:103], v[144:147], v[88:91]\n\t" \
  "s_waitcnt lgkmcnt(4)\n\t" \
  "buffer_load_dwordx4 v242, s[16:19], s97 offen offset:2048 sc1 lds\n\t" \
  "v_mfma_f32_16x16x32_bf16 v[76:79], a[96:99], v[160:163], v[76:79]\n\t" \
  "s_and_b32 s23, s42, 0xffffc000\n\t" \
  "ds_read_b128 v[144:147], v242 offset:20480\n\t" \
  "s_lshl_b32 s42, s59, 6\n\t" \
  "v_mfma_f32_16x16x32_bf16 v[76:79], a[104:107], v[164:167], v[76:79]\n\t" \
  "s_and_b32 s42, s42, 0x1800\n\t" \
  "ds_read_b128 v[148:151], v242 offset:21504\n\t" \
  "s_add_u32 s23, s23, s42\n\t" \
  "v_mfma_f32_16x16x32_bf16 v[76:79], a[112:115], v[168:171], v[76:79]\n\t" \
  "buffer_load_dwordx4 v242, s[16:19], s97 offen offset:3072 sc1 lds\n\t" \
  "v_mfma_f32_16x16x32_bf16 v[76:79], a[120:123], v[172:175], v[76:79]\n\t" \
  "s_add_u32 s97, s64, 655360\n\t" \
  "s_and_b32 s97, s97, 0xe0000\n\t" \
  "s_add_u32 s97, s97, s63\n\t" \
  "v_mfma_f32_16x16x32_bf16 v[92:95], a[124:127], v[172:175], v[92:95]\n\t" \
  "s_add_u32 m0, s38, 131072\n\t" \
  "ds_read_b128 v[152:155], v242 offset:22528\n\t" \
  "s_mul_i32 s42, s3, 0x1400\n\t" \
  "v_mfma_f32_16x16x32_bf16 v[92:95], a[116:119], v[168:171], v[92:95]\n\t" \
  "buffer_load_dwordx4 v242, s[16:19], s97 offen offset:0 sc1 lds\n\t" \
  "v_mfma_f32_16x16x32_bf16 v[92:95], a[108:111], v[164:167], v[92:95]\n\t" \
  "s_and_b32 s42, s42, 0x2400\n\t" \
  "ds_read_b128 v[156:159], v242 offset:23552\n\t" \
  "s_xor_b32 s42, s42, s55\n\t" \
  "v_mfma_f32_16x16x32_bf16 v[92:95], a[100:103], v[160:163], v[92:95]\n\t" \
  "s_waitcnt lgkmcnt(4)\n\t" \
  "s_add_u32 s23, s23, s42\n\t" \
  "s_add_u32 s23, s23, 1048576\n\t" \
  "v_mfma_f32_16x16x32_bf16 v[64:67], a[192:195], v[128:131], v[64:67]\n\t" \
  "buffer_load_dwordx4 v242, s[16:19], s97 offen offset:1024 sc1 lds\n\t" \
  "v_mfma_f32_16x16x32_bf16 v[64:67], a[200:203], v[132:135], v[64:67]\n\t" \
  "ds_read_b128 v[160:163], v242 offset:24576\n\t" \
  "v_mfma_f32_16x16x32_bf16 v[64:67], a[208:211], v[136:139], v[64:67]\n\t" \
  "ds_read_b128 v[164:167], v242 offset:25600\n\t" \
  "v_mfma_f32_16x16x32_bf16 v[64:67], a[216:219], v[140:143], v[64:67]\n\t" \
  "buffer_load_dwordx4 v242, s[16:19], s97 offen offset:2048 sc1 lds\n\t" \
  "v_mfma_f32_16x16x32_bf16 v[80:83], a[220:223], v[140:143], v[80:83]\n\t" \
  "ds_read_b128 v[168:171], v242 offset:26624\n\t" \
  "v_mfma_f32_16x16x32_bf16 v[80:83], a[212:215], v[136:139], v[80:83]\n\t" \
  "ds_read_b128 v[172:175], v242 offset:27648\n\t" \
  "v_mfma_f32_16x16x32_bf16 v[80:83], a[204:207], v[132:135], v[80:83]\n\t" \
  "buffer_load_dwordx4 v242, s[16:19], s97 offen offset:3072 sc1 lds\n\t" \
  "v_mfma_f32_16x16x32_bf16 v[80:83], a[196:199], v[128:131], v[80:83]\n\t" \
  "s_waitcnt lgkmcnt(4)\n\t" \
  "v_cvt_pk_bf16_f32 v0, v64, v65\n\t" \
  "v_cvt_pk_bf16_f32 v1, v66, v67\n\t" \
  "v_mfma_f32_16x16x32_bf16 v[68:71], a[192:195], v[144:147], v[68:71]\n\t" \
  "buffer_load_dword v246, v235, s[16:19], s73 offen sc1\n\t" \
  "v_mfma_f32_16x16x32_bf16 v[68:71], a[200:203], v[148:151], v[68:71]\n\t" \
  "ds_read_b128 v[128:131], v242 offset:28672\n\t" \
  "v_mov_b32 v250, 0x3f80\n\t" \
  "v_mfma_f32_16x16x32_bf16 v[68:71], a[208:211], v[152:155], v[68:71]\n\t" \
  "v_cvt_pk_bf16_f32 v2, v80, v81\n\t" \
  "v_cvt_pk_bf16_f32 v3, v82, v83\n\t" \
  "v_mfma_f32_16x16x32_bf16 v[68:71], a[216:219], v[156:159], v[68:71]\n\t" \
  "ds_read_b128 v[132:135], v242 offset:29696\n\t" \
  "v_lshlrev_b32 v250, v235, v250\n\t" \
  "v_mfma_f32_16x16x32_bf16 v[84:87], a[220:223], v[156:159], v[84:87]\n\t" \
  "s_mov_b32 s84, 0x300003\n\t" \
  "ds_read_b128 v[136:139], v242 offset:30720\n\t" \
  "s_mov_b32 s85, 0x30000300\n\t" \
  "v_mfma_f32_16x16x32_bf16 v[84:87], a[212:215], v[152:155], v[84:87]\n\t" \
  "s_mov_b32 s86, 0xc0000c\n\t" \
  "s_mov_b32 s87, 0xc0000c00\n\t" \
  "v_cndmask_b32 v248, 0, v250, s[84:85]\n\t" \
  "v_mfma_f32_16x16x32_bf16 v[84:87], a[204:207], v[148:151], v[84:87]\n\t" \
  "ds_read_b128 v[140:143], v242 offset:31744\n\t" \
  "v_cndmask_b32 v249, 0, v250, s[86:87]\n\t" \
  "v_mfma_f32_16x16x32_bf16 v[84:87], a[196:199], v[144:147], v[84:87]\n\t" \
  "s_waitcnt lgkmcnt(4)\n\t" \
  "v_cvt_pk_bf16_f32 v4, v68, v69\n\t" \
  "v_cvt_pk_bf16_f32 v5, v70, v71\n\t" \
  "v_mfma_f32_16x16x32_bf16 v[72:75], a[192:195], v[160:163], v[72:75]\n\t" \
  "buffer_store_dwordx4 v[0:3], v242, s[16:19], s69 offen offset:0 sc1\n\t" \
  "ds_read_b128 v[144:147], v242 offset:32768\n\t" \
  "v_and_b32 v236, 15, v233\n\t" \
  "v_lshrrev_b32 v250, 4, v233\n\t" \
  "v_mfma_f32_16x16x32_bf16 v[72:75], a[200:203], v[164:167], v[72:75]\n\t" \
  "v_cvt_pk_bf16_f32 v6, v84, v85\n\t" \
  "v_cvt_pk_bf16_f32 v7, v86, v87\n\t" \
  "v_mfma_f32_16x16x32_bf16 v[72:75], a[208:211], v[168:171], v[72:75]\n\t" \
  "ds_read_b128 v[148:151], v242 offset:33792\n\t" \
  "v_mul_u32_u24 v236, 80, v236\n\t" \
  "v_mfma_f32_16x16x32_bf16 v[72:75], a[216:219], v[172:175], v[72:75]\n\t" \
  "v_lshlrev_b32 v237, 4, v250\n\t" \
  "v_add_u32 v237, s76, v237\n\t" \
  "v_mfma_f32_16x16x32_bf16 v[88:91], a[220:223], v[172:175], v[88:91]\n\t" \
  "ds_read_b128 v[152:155], v242 offset:34816\n\t" \
  "v_add_u32 v236, v237, v236\n\t" \
  "v_mfma_f32_16x16x32_bf16 v[88:91], a[212:215], v[168:171], v[88:91]\n\t" \
  "s_add_u32 s78, s70, 4096\n\t" \
  "ds_read_b128 v[156:159], v242 offset:35840\n\t" \
  "s_add_u32 s79, s70, 16384\n\t" \
  "v_mfma_f32_16x16x32_bf16 v[88:91], a[204:207], v[164:167], v[88:91]\n\t" \
  "v_mfma_f32_16x16x32_bf16 v[88:91], a[196:199], v[160:163], v[88:91]\n\t" \
  "s_waitcnt lgkmcnt(4)\n\t" \
  "v_cvt_pk_bf16_f32 v8, v72, v73\n\t" \
  "v_cvt_pk_bf16_f32 v9, v74, v75\n\t" \
  "v_mfma_f32_16x16x32_bf16 v[76:79], a[192:195], v[128:131], v[76:79]\n\t" \
  "buffer_store_dwordx4 v[4:7], v242, s[16:19], s69 offen offset:1024 sc1\n\t" \
  "ds_read_b128 v[160:163], v242 offset:36864\n\t" \
  "ds_read_b128 v[164:167], v242 offset:37888\n\t" \
  "ds_read_b128 v[168:171], v242 offset:38912\n\t" \
  "v_mfma_f32_16x16x32_bf16 v[76:79], a[200:203], v[132:135], v[76:79]\n\t" \
  "v_cvt_pk_bf16_f32 v10, v88, v89\n\t" \
  "v_cvt_pk_bf16_f32 v11, v90, v91\n\t" \
  "v_mfma_f32_16x16x32_bf16 v[76:79], a[208:211], v[136:139], v[76:79]\n\t" \
  "ds_read_b128 v[172:175], v242 offset:39936\n\t" \
  "v_mfma_f32_16x16x32_bf16 v[76:79], a[216:219], v[140:143], v[76:79]\n\t" \
  "v_mfma_f32_16x16x32_bf16 v[92:95], a[220:223], v[140:143], v[92:95]\n\t" \
  "v_mfma_f32_16x16x32_bf16 v[92:95], a[212:215], v[136:139], v[92:95]\n\t" \
  "v_mfma_f32_16x16x32_bf16 v[92:95], a[204:207], v[132:135], v[92:95]\n\t" \
  "v_mfma_f32_16x16x32_bf16 v[92:95], a[196:199], v[128:131], v[92:95]\n\t" \
  "s_waitcnt lgkmcnt(4)\n\t" \
  STP("s_memrealtime s[94:95]\n\t") \
  STP("s_memtime s[92:93]\n\t") \
  "v_mfma_f32_16x16x32_bf16 v[96:99], a[128:131], v[144:147], v[96:99]\n\t" \
  "buffer_store_dwordx4 v[8:11], v242, s[16:19], s69 offen offset:2048 sc1\n\t" \
  "ds_read_b128 v[128:131], v242 offset:40960\n\t" \
  "v_cvt_pk_bf16_f32 v12, v76, v77\n\t" \
  "v_cvt_pk_bf16_f32 v13, v78, v79\n\t" \
  "v_mfma_f32_16x16x32_bf16 v[96:99], a[136:139], v[148:151], v[96:99]\n\t" \
  "ds_read_b64 v[208:209], v238 offset:0\n\t" \
  "v_cvt_pk_bf16_f32 v14, v92, v93\n\t" \
  "v_mfma_f32_16x16x32_bf16 v[96:99], a[144:147], v[152:155], v[96:99]\n\t" \
  "ds_read_b128 v[132:135], v242 offset:41984\n\t" \
  "v_cvt_pk_bf16_f32 v15, v94, v95\n\t" \
  "v_mfma_f32_16x16x32_bf16 v[96:99], a[152:155], v[156:159], v[96:99]\n\t" \
  "ds_read_b64 v[210:211], v238 offset:4096\n\t" \
  "v_mfma_f32_16x16x32_bf16 v[112:115], a[156:159], v[156:159], v[112:115]\n\t" \
  "ds_read_b128 v[136:139], v242 offset:43008\n\t" \
  "v_mfma_f32_16x16x32_bf16 v[112:115], a[148:151], v[152:155], v[112:115]\n\t" \
  "ds_read_b64 v[212:213], v238 offset:8192\n\t" \
  "v_mfma_f32_16x16x32_bf16 v[112:115], a[140:143], v[148:151], v[112:115]\n\t" \
  "ds_read_b128 v[140:143], v242 offset:44032\n\t" \
  "v_mfma_f32_16x16x32_bf16 v[112:115], a[132:135], v[144:147], v[112:115]\n\t" \
  "s_waitcnt lgkmcnt(7)\n\t" \
  "ds_read_b64 v[214:215], v238 offset:12288\n\t" \
  "v_mfma_f32_16x16x32_bf16 v[100:103], a[128:131], v[160:163], v[100:103]\n\t" \
  "buffer_store_dwordx4 v[12:15], v242, s[16:19], s69 offen offset:3072 sc1\n\t" \
  "ds_read_b128 v[144:147], v242 offset:45056\n\t" \
  "ds_read_b128 v[148:151], v242 offset:46080\n\t" \
  "ds_read_b64 v[216:217], v239 offset:0\n\t" \
  "v_mfma_f32_16x16x32_bf16 v[100:103], a[136:139], v[164:167], v[100:103]\n\t" \
  "ds_read_b128 v[152:155], v242 offset:47104\n\t" \
  "v_mfma_f32_16x16x32_bf16 v[100:103], a[144:147], v[168:171], v[100:103]\n\t" \
  "ds_read_b64 v[218:219], v239 offset:4096\n\t" \
  "v_mfma_f32_16x16x32_bf16 v[100:103], a[152:155], v[172:175], v[100:103]\n\t" \
  "ds_read_b128 v[156:159], v242 offset:48128\n\t" \
  "v_mfma_f32_16x16x32_bf16 v[116:119], a[156:159], v[172:175], v[116:119]\n\t" \
  STP("v_writelane_b32 v255, s94, 36\n\t") \
  STP("v_writelane_b32 v255, s92, 37\n\t") \
  "ds_read_b64 v[220:221], v239 offset:8192\n\t" \
  "v_mfma_f32_16x16x32_bf16 v[116:119], a[148:151], v[168:171], v[116:119]\n\t" \
  "ds_read_b64 v[222:223], v239 offset:12288\n\t" \
  "v_mfma_f32_16x16x32_bf16 v[116:119], a[140:143], v[164:167], v[116:119]\n\t" \
  "s_waitcnt vmcnt(13) lgkmcnt(9)\n\t" \
  "v_lshl_add_u32 v224, s3, 10, v242\n\t" \
  "v_and_b32 v225, 31, v233\n\t" \
  "v_mfma_f32_16x16x32_bf16 v[116:119], a[132:135], v[160:163], v[116:119]\n\t" \
  "s_barrier\n\t" \
  "v_mfma_f32_16x16x32_bf16 v[104:107], a[128:131], v[128:131], v[104:107]\n\t" \
  "ds_read_b128 v[160:163], v242 offset:49152\n\t" \
  "v_lshlrev_b32 v245, 2, v225\n\t" \
  "v_mfma_f32_16x16x32_bf16 v[104:107], a[136:139], v[132:135], v[104:107]\n\t" \
  "s_waitcnt lgkmcnt(1)\n\t" \
  "v_lshlrev_b32 v0, 16, v208\n\t" \
  "v_lshlrev_b32 v225, 3, v225\n\t" \
  "v_mfma_f32_16x16x32_bf16 v[104:107], a[144:147], v[136:139], v[104:107]\n\t" \
  "ds_read_b128 v[164:167], v242 offset:50176\n\t" \
  "v_and_b32 v226, 0xffffffe0, v224\n\t" \
  "v_mfma_f32_16x16x32_bf16 v[104:107], a[152:155], v[140:143], v[104:107]\n\t" \
  "v_lshl_add_u32 v227, s8, 6, v233\n\t" \
  "v_sub_u32 v227, 0x7ff, v227\n\t" \
  "v_mfma_f32_16x16x32_bf16 v[120:123], a[156:159], v[140:143], v[120:123]\n\t" \
  "s_add_u32 s97, s64, 786432\n\t" \
  "ds_read_b128 v[168:171], v242 offset:51200\n\t" \
  "v_and_b32 v228, 0x30, v233\n\t" \
  "v_mfma_f32_16x16x32_bf16 v[120:123], a[148:151], v[136:139], v[120:123]\n\t" \
  "s_and_b32 s97, s97, 0xe0000\n\t" \
  "v_and_b32 v239, 15, v233\n\t" \
  "v_and_b32 v241, 16, v233\n\t" \
  "v_mfma_f32_16x16x32_bf16 v[120:123], a[140:143], v[132:135], v[120:123]\n\t" \
  "s_add_u32 s97, s97, s63\n\t" \
  "ds_read_b128 v[172:175], v242 offset:52224\n\t" \
  "v_lshlrev_b32 v231, 11, v239\n\t" \
  "v_mfma_f32_16x16x32_bf16 v[120:123], a[132:135], v[128:131], v[120:123]\n\t" \
  "s_add_u32 m0, s38, 65536\n\t" \
  "v_lshl_add_u32 v231, v241, 1, v231\n\t" \
  "v_lshrrev_b32 v239, 5, v233\n\t" \
  "v_mfma_f32_16x16x32_bf16 v[108:111], a[128:131], v[144:147], v[108:111]\n\t" \
  "ds_read_b128 v[128:131], v242 offset:53248\n\t" \
  "v_lshl_add_u32 v231, v239, 4, v231\n\t" \
  "v_mfma_f32_16x16x32_bf16 v[108:111], a[136:139], v[148:151], v[108:111]\n\t" \
  "v_lshl_add_u32 v253, v239, 6, v241\n\t" \
  "v_and_b32 v239, 1, v233\n\t" \
  "v_mfma_f32_16x16x32_bf16 v[108:111], a[144:147], v[152:155], v[108:111]\n\t" \
  "buffer_load_dwordx4 v242, s[16:19], s97 offen offset:0 sc1 lds\n\t" \
  "v_mfma_f32_16x16x32_bf16 v[108:111], a[152:155], v[156:159], v[108:111]\n\t" \
  "ds_read_b128 v[132:135], v242 offset:54272\n\t" \
  "v_lshl_add_u32 v253, v239, 5, v253\n\t" \
  "v_mfma_f32_16x16x32_bf16 v[124:127], a[156:159], v[156:159], v[124:127]\n\t" \
  "ds_read_b128 v[136:139], v242 offset:55296\n\t" \
  "v_lshl_add_u32 v253, s3, 7, v253\n\t" \
  "v_mfma_f32_16x16x32_bf16 v[124:127], a[148:151], v[152:155], v[124:127]\n\t" \
  "buffer_load_dwordx4 v242, s[16:19], s97 offen offset:1024 sc1 lds\n\t" \
  "v_mfma_f32_16x16x32_bf16 v[124:127], a[140:143], v[148:151], v[124:127]\n\t" \
  "ds_read_b128 v[140:143], v242 offset:56320\n\t" \
  "v_and_b32 v239, 0x800, v224\n\t" \
  "v_mfma_f32_16x16x32_bf16 v[124:127], a[132:135], v[144:147], v[124:127]\n\t" \
  "s_waitcnt lgkmcnt(4)\n\t" \
  "v_lshl_or_b32 v240, v239, 20, v242\n\t" \
  "v_mov_b32 v238, s82\n\t" \
  "v_mfma_f32_16x16x32_bf16 v[96:99], a[160:163], v[160:163], v[96:99]\n\t" \
  "ds_read_b128 v[144:147], v242 offset:57344\n\t" \
  "v_writelane_b32 v238, s83, 1\n\t" \
  "v_mfma_f32_16x16x32_bf16 v[96:99], a[168:171], v[164:167], v[96:99]\n\t" \
  "buffer_load_dwordx4 v242, s[16:19], s97 offen offset:2048 sc1 lds\n\t" \
  "v_mfma_f32_16x16x32_bf16 v[96:99], a[176:179], v[168:171], v[96:99]\n\t" \
  "ds_read_b128 v[148:151], v242 offset:58368\n\t" \
  "v_perm_b32 v239, v180, v176, s40\n\t" \
  "v_mfma_f32_16x16x32_bf16 v[96:99], a[184:187], v[172:175], v[96:99]\n\t" \
  "v_perm_b32 v180, v180, v176, s41\n\t" \
  "v_mov_b32 v176, v239\n\t" \
  "v_mfma_f32_16x16x32_bf16 v[112:115], a[188:191], v[172:175], v[112:115]\n\t" \
  "ds_read_b128 v[152:155], v242 offset:59392\n\t" \
  "v_perm_b32 v239, v181, v177, s40\n\t" \
  "v_mfma_f32_16x16x32_bf16 v[112:115], a[180:183], v[168:171], v[112:115]\n\t" \
  "buffer_load_dwordx4 v242, s[16:19], s97 offen offset:3072 sc1 lds\n\t" \
  "v_mfma_f32_16x16x32_bf16 v[112:115], a[172:175], v[164:167], v[112:115]\n\t" \
  "s_add_u32 s97, s64, 917504\n\t" \
  "s_and_b32 s97, s97, 0xe0000\n\t" \
  "s_add_u32 s97, s97, s63\n\t" \
  "v_mfma_f32_16x16x32_bf16 v[112:115], a[164:167], v[160:163], v[112:115]\n\t" \
  "s_waitcnt lgkmcnt(3)\n\t" \
  "ds_read_b128 v[156:159], v242 offset:60416\n\t" \
  "s_add_u32 m0, s38, 16384\n\t" \
  "v_mfma_f32_16x16x32_bf16 v[100:103], a[160:163], v[128:131], v[100:103]\n\t" \
  "buffer_load_dwordx4 v242, s[16:19], s97 offen offset:0 sc1 lds\n\t" \
  "v_mfma_f32_16x16x32_bf16 v[100:103], a[168:171], v[132:135], v[100:103]\n\t" \
  "ds_read_b128 v[160:163], v242 offset:61440\n\t" \
  "v_perm_b32 v181, v181, v177, s41\n\t" \
  "v_mfma_f32_16x16x32_bf16 v[100:103], a[176:179], v[136:139], v[100:103]\n\t" \
  "ds_read_b128 v[164:167], v242 offset:62464\n\t" \
  "v_mov_b32 v177, v239\n\t" \
  "v_mfma_f32_16x16x32_bf16 v[100:103], a[184:187], v[140:143], v[100:103]\n\t" \
  "v_perm_b32 v239, v182, v178, s40\n\t" \
  "v_perm_b32 v182, v182, v178, s41\n\t" \
  "v_mfma_f32_16x16x32_bf16 v[116:119], a[188:191], v[140:143], v[116:119]\n\t" \
  "buffer_load_dwordx4 v242, s[16:19], s97 offen offset:1024 sc1 lds\n\t" \
  "v_mfma_f32_16x16x32_bf16 v[116:119], a[180:183], v[136:139], v[116:119]\n\t" \
  "ds_read_b128 v[168:171], v242 offset:63488\n\t" \
  "v_mov_b32 v178, v239\n\t" \
  "v_mfma_f32_16x16x32_bf16 v[116:119], a[172:175], v[132:135], v[116:119]\n\t" \
  "ds_read_b128 v[172:175], v242 offset:64512\n\t" \
  "v_perm_b32 v239, v183, v179, s40\n\t" \
  "v_mfma_f32_16x16x32_bf16 v[116:119], a[164:167], v[128:131], v[116:119]\n\t" \
  "s_waitcnt lgkmcnt(4)\n\t" \
  "v_perm_b32 v183, v183, v179, s41\n\t" \
  "v_mov_b32 v179, v239\n\t" \
  "v_mfma_f32_16x16x32_bf16 v[104:107], a[160:163], v[144:147], v[104:107]\n\t" \
  "buffer_load_dwordx4 v242, s[16:19], s97 offen offset:2048 sc1 lds\n\t" \
  "v_mfma_f32_16x16x32_bf16 v[104:107], a[168:171], v[148:151], v[104:107]\n\t" \
  "s_waitcnt vmcnt(11)\n\t" \
  "v_cmp_ne_u32 vcc, s36, v246\n\t" \
  "v_perm_b32 v239, v188, v184, s40\n\t" \
  "v_mfma_f32_16x16x32_bf16 v[104:107], a[176:179], v[152:155], v[104:107]\n\t" \
  "ds_read_b128 v[128:131], v230 offset:32768\n\t" \
  "v_perm_b32 v188, v188, v184, s41\n\t" \
  "v_mfma_f32_16x16x32_bf16 v[104:107], a[184:187], v[156:159], v[104:107]\n\t" \
  "s_cbranch_vccnz .Lat_s4\n\t" \
  ".Lat_k4:\n\t" \
  "v_mfma_f32_16x16x32_bf16 v[120:123], a[188:191], v[156:159], v[120:123]\n\t" \
  "buffer_load_dwordx4 v[32:35], v242, s[16:19], s70 offen offset:0 sc1\n\t" \
  "v_mfma_f32_16x16x32_bf16 v[120:123], a[180:183], v[152:155], v[120:123]\n\t" \
  "ds_read_b128 v[132:135], v230 offset:33792\n\t" \
  "v_mov_b32 v184, v239\n\t" \
  "v_mfma_f32_16x16x32_bf16 v[120:123], a[172:175], v[148:151], v[120:123]\n\t" \
  "ds_read_b128 v[136:139], v230 offset:34816\n\t" \
  "v_perm_b32 v239, v189, v185, s40\n\t" \
  "v_mfma_f32_16x16x32_bf16 v[120:123], a[164:167], v[144:147], v[120:123]\n\t" \
  "s_waitcnt lgkmcnt(3)\n\t" \
  "ds_read_b128 v[140:143], v230 offset:35840\n\t" \
  "v_perm_b32 v189, v189, v185, s41\n\t" \
  "v_mfma_f32_16x16x32_bf16 v[108:111], a[160:163], v[160:163], v[108:111]\n\t" \
  "buffer_load_dwordx4 v242, s[16:19], s97 offen offset:3072 sc1 lds\n\t" \
  "v_mfma_f32_16x16x32_bf16 v[108:111], a[168:171], v[164:167], v[108:111]\n\t" \
  "ds_read_b128 v[144:147], v230 offset:36864\n\t" \
  "v_mov_b32 v185, v239\n\t" \
  "v_mfma_f32_16x16x32_bf16 v[108:111], a[176:179], v[168:171], v[108:111]\n\t" \
  "ds_read_b128 v[148:151], v230 offset:37888\n\t" \
  "v_and_b32 v1, 0xffff0000, v208\n\t" \
  "v_mfma_f32_16x16x32_bf16 v[108:111], a[184:187], v[172:175], v[108:111]\n\t" \
  "v_lshlrev_b32 v2, 16, v209\n\t" \
  "v_perm_b32 v239, v190, v186, s40\n\t" \
  "v_mfma_f32_16x16x32_bf16 v[124:127], a[188:191], v[172:175], v[124:127]\n\t" \
  "ds_read_b128 v[152:155], v230 offset:38912\n\t" \
  "v_perm_b32 v190, v190, v186, s41\n\t" \
  "v_mfma_f32_16x16x32_bf16 v[124:127], a[180:183], v[168:171], v[124:127]\n\t" \
  "buffer_load_dwordx4 v[36:39], v242, s[16:19], s70 offen offset:1024 sc1\n\t" \
  "v_mfma_f32_16x16x32_bf16 v[124:127], a[172:175], v[164:167], v[124:127]\n\t" \
  "ds_read_b128 v[156:159], v230 offset:39936\n\t" \
  "v_and_b32 v3, 0xffff0000, v209\n\t" \
  "v_mfma_f32_16x16x32_bf16 v[124:127], a[164:167], v[160:163], v[124:127]\n\t" \
  "s_waitcnt lgkmcnt(4)\n\t" \
  "v_lshlrev_b32 v4, 16, v210\n\t" \
  "v_mov_b32 v186, v239\n\t" \
  "v_mfma_f32_16x16x32_bf16 v[96:99], a[0:3], v[128:131], v[96:99]\n\t" \
  "ds_read_b128 v[160:163], v230 offset:40960\n\t" \
  "v_perm_b32 v239, v191, v187, s40\n\t" \
  "v_mfma_f32_16x16x32_bf16 v[96:99], a[8:11], v[132:135], v[96:99]\n\t" \
  "v_and_b32 v5, 0xffff0000, v210\n\t" \
  "v_lshlrev_b32 v6, 16, v211\n\t" \
  "v_mfma_f32_16x16x32_bf16 v[96:99], a[16:19], v[136:139], v[96:99]\n\t" \
  "ds_read_b128 v[164:167], v230 offset:41984\n\t" \
  "v_perm_b32 v191, v191, v187, s41\n\t" \
  "v_mfma_f32_16x16x32_bf16 v[96:99], a[24:27], v[140:143], v[96:99]\n\t" \
  "v_and_b32 v7, 0xffff0000, v211\n\t" \
  "v_mov_b32 v187, v239\n\t" \
  "v_mfma_f32_16x16x32_bf16 v[112:115], a[28:31], v[140:143], v[112:115]\n\t" \
  "ds_read_b128 v[168:171], v230 offset:43008\n\t" \
  "v_perm_b32 v239, v196, v192, s40\n\t" \
  "v_mfma_f32_16x16x32_bf16 v[112:115], a[20:23], v[136:139], v[112:115]\n\t" \
  "v_lshlrev_b32 v8, 16, v212\n\t" \
  "v_perm_b32 v196, v196, v192, s41\n\t" \
  "v_mfma_f32_16x16x32_bf16 v[112:115], a[12:15], v[132:135], v[112:115]\n\t" \
  "ds_read_b128 v[172:175], v230 offset:44032\n\t" \
  "v_and_b32 v9, 0xffff0000, v212\n\t" \
  "v_mfma_f32_16x16x32_bf16 v[112:115], a[4:7], v[128:131], v[112:115]\n\t" \
  "s_waitcnt lgkmcnt(4)\n\t" \
  "v_lshlrev_b32 v10, 16, v213\n\t" \
  "v_mov_b32 v192, v239\n\t" \
  "v_mfma_f32_16x16x32_bf16 v[100:103], a[0:3], v[144:147], v[100:103]\n\t" \
  "ds_read_b128 v[128:131], v230 offset:45056\n\t" \
  "v_perm_b32 v239, v197, v193, s40\n\t" \
  "v_mfma_f32_16x16x32_bf16 v[100:103], a[8:11], v[148:151], v[100:103]\n\t" \
  "v_and_b32 v11, 0xffff0000, v213\n\t" \
  "v_perm_b32 v197, v197, v193, s41\n\t" \
  "v_mfma_f32_16x16x32_bf16 v[100:103], a[16:19], v[152:155], v[100:103]\n\t" \
  "buffer_load_dwordx4 v[40:43], v242, s[16:19], s70 offen offset:2048 sc1\n\t" \
  "v_mfma_f32_16x16x32_bf16 v[100:103], a[24:27], v[156:159], v[100:103]\n\t" \
  "ds_read_b128 v[132:135], v230 offset:46080\n\t" \
  "v_lshlrev_b32 v12, 16, v214\n\t" \
  "v_mfma_f32_16x16x32_bf16 v[116:119], a[28:31], v[156:159], v[116:119]\n\t" \
  "ds_read_b128 v[136:139], v230 offset:47104\n\t" \
  "v_mov_b32 v193, v239\n\t" \
  "v_mfma_f32_16x16x32_bf16 v[116:119], a[20:23], v[152:155], v[116:119]\n\t" \
  "buffer_load_dwordx4 v[44:47], v242, s[16:19], s70 offen offset:3072 sc1\n\t" \
  "v_mfma_f32_16x16x32_bf16 v[116:119], a[12:15], v[148:151], v[116:119]\n\t" \
  "s_waitcnt lgkmcnt(3)\n\t" \
  "ds_read_b128 v[140:143], v230 offset:48128\n\t" \
  "v_and_b32 v13, 0xffff0000, v214\n\t" \
  "v_mfma_f32_16x16x32_bf16 v[116:119], a[4:7], v[144:147], v[116:119]\n\t" \
  "s_barrier\n\t" \
  "v_mfma_f32_16x16x32_bf16 v[104:107], a[0:3], v[160:163], v[104:107]\n\t" \
  "s_waitcnt vmcnt(12)\n\t" \
  "buffer_store_dword v243, v244, s[16:19], s66 offen offset:32 sc1\n\t" \
  "ds_read_b128 v[144:147], v230 offset:16384\n\t" \
  "v_mfma_f32_16x16x32_bf16 v[104:107], a[8:11], v[164:167], v[104:107]\n\t" \
  "v_lshlrev_b32 v14, 16, v215\n\t" \
  "v_perm_b32 v239, v198, v194, s40\n\t" \
  "v_mfma_f32_16x16x32_bf16 v[104:107], a[16:19], v[168:171], v[104:107]\n\t" \
  "buffer_load_dwordx4 v[48:51], v242, s[16:19], s78 offen offset:0 sc1\n\t" \
  "v_mfma_f32_16x16x32_bf16 v[104:107], a[24:27], v[172:175], v[104:107]\n\t" \
  "ds_read_b128 v[148:151], v230 offset:17408\n\t" \
  "v_perm_b32 v198, v198, v194, s41\n\t" \
  "v_mfma_f32_16x16x32_bf16 v[120:123], a[28:31], v[172:175], v[120:123]\n\t" \
  "ds_read_b128 v[152:155], v230 offset:18432\n\t" \
  "v_and_b32 v15, 0xffff0000, v215\n\t" \
  "v_mfma_f32_16x16x32_bf16 v[120:123], a[20:23], v[168:171], v[120:123]\n\t" \
  "buffer_load_dwordx4 v[52:55], v242, s[16:19], s78 offen offset:1024 sc1\n\t" \
  "v_mfma_f32_16x16x32_bf16 v[120:123], a[12:15], v[164:167], v[120:123]\n\t" \
  "ds_read_b128 v[156:159], v230 offset:19456\n\t" \
  "v_mov_b32 v194, v239\n\t" \
  "v_mfma_f32_16x16x32_bf16 v[120:123], a[4:7], v[160:163], v[120:123]\n\t" \
  "s_waitcnt lgkmcnt(4)\n\t" \
  "v_lshlrev_b32 v16, 16, v216\n\t" \
  "v_perm_b32 v239, v199, v195, s40\n\t" \
  "v_mfma_f32_16x16x32_bf16 v[108:111], a[0:3], v[128:131], v[108:111]\n\t" \
  "buffer_load_dwordx4 v[56:59], v242, s[16:19], s78 offen offset:2048 sc1\n\t" \
  "v_mfma_f32_16x16x32_bf16 v[108:111], a[8:11], v[132:135], v[108:111]\n\t" \
  "ds_read_b128 v[160:163], v230 offset:20480\n\t" \
  "v_and_b32 v17, 0xffff0000, v216\n\t" \
  "v_mfma_f32_16x16x32_bf16 v[108:111], a[16:19], v[136:139], v[108:111]\n\t" \
  "ds_read_b128 v[164:167], v230 offset:21504\n\t" \
  "v_lshlrev_b32 v18, 16, v217\n\t" \
  "v_mfma_f32_16x16x32_bf16 v[108:111], a[24:27], v[140:143], v[108:111]\n\t" \
  "v_perm_b32 v199, v199, v195, s41\n\t" \
  "v_mov_b32 v195, v239\n\t" \
  "v_mfma_f32_16x16x32_bf16 v[124:127], a[28:31], v[140:143], v[124:127]\n\t" \
  "ds_read_b128 v[168:171], v230 offset:22528\n\t" \
  "v_and_b32 v19, 0xffff0000, v217\n\t" \
  "v_mfma_f32_16x16x32_bf16 v[124:127], a[20:23], v[136:139], v[124:127]\n\t" \
  "buffer_load_dwordx4 v[60:63], v242, s[16:19], s78 offen offset:3072 sc1\n\t" \
  "v_mfma_f32_16x16x32_bf16 v[124:127], a[12:15], v[132:135], v[124:127]\n\t" \
  "ds_read_b128 v[172:175], v230 offset:23552\n\t" \
  "v_accvgpr_write_b32 a128, 0\n\t" \
  "v_mfma_f32_16x16x32_bf16 v[124:127], a[4:7], v[128:131], v[124:127]\n\t" \
  "s_waitcnt lgkmcnt(4)\n\t" \
  "v_accvgpr_write_b32 a129, 0\n\t" \
  "v_accvgpr_write_b32 a130, v248\n\t" \
  "v_mfma_f32_16x16x32_bf16 v[96:99], a[32:35], v[144:147], v[96:99]\n\t" \
  "ds_read_b128 v[128:131], v230 offset:24576\n\t" \
  "v_accvgpr_write_b32 a131, v249\n\t" \
  "v_mfma_f32_16x16x32_bf16 v[96:99], a[40:43], v[148:151], v[96:99]\n\t" \
  "v_accvgpr_write_b32 a132, 0\n\t" \
  "v_accvgpr_write_b32 a133, 0\n\t" \
  "v_mfma_f32_16x16x32_bf16 v[96:99], a[48:51], v[152:155], v[96:99]\n\t" \
  "ds_read_b128 v[132:135], v230 offset:25600\n\t" \
  "v_lshlrev_b32 v20, 16, v218\n\t" \
  "v_mfma_f32_16x16x32_bf16 v[96:99], a[56:59], v[156:159], v[96:99]\n\t" \
  "ds_read_b128 a[176:179], v236 offset:960\n\t" \
  "v_perm_b32 v239, v204, v200, s40\n\t" \
  "v_mfma_f32_16x16x32_bf16 v[112:115], a[60:63], v[156:159], v[112:115]\n\t" \
  "ds_read_b128 v[136:139], v230 offset:26624\n\t" \
  "v_and_b32 v21, 0xffff0000, v218\n\t" \
  "v_mfma_f32_16x16x32_bf16 v[112:115], a[52:55], v[152:155], v[112:115]\n\t" \
  "ds_read_b128 a[180:183], v236 offset:640\n\t" \
  "v_perm_b32 v204, v204, v200, s41\n\t" \
  "v_mfma_f32_16x16x32_bf16 v[112:115], a[44:47], v[148:151], v[112:115]\n\t" \
  "ds_read_b128 v[140:143], v230 offset:27648\n\t" \
  "v_lshlrev_b32 v22, 16, v219\n\t" \
  "v_mfma_f32_16x16x32_bf16 v[112:115], a[36:39], v[144:147], v[112:115]\n\t" \
  "s_waitcnt lgkmcnt(6)\n\t" \
  "ds_read_b128 a[184:187], v236 offset:320\n\t" \
  "v_mov_b32 v200, v239\n\t" \
  "v_mfma_f32_16x16x32_bf16 v[100:103], a[32:35], v[160:163], v[100:103]\n\t" \
  "ds_read_b128 v[144:147], v230 offset:28672\n\t" \
  "v_and_b32 v23, 0xffff0000, v219\n\t" \
  "v_mfma_f32_16x16x32_bf16 v[100:103], a[40:43], v[164:167], v[100:103]\n\t" \
  "ds_read_b128 a[188:191], v236 offset:0\n\t" \
  "v_perm_b32 v239, v205, v201, s40\n\t" \
  "v_mfma_f32_16x16x32_bf16 v[100:103], a[48:51], v[168:171], v[100:103]\n\t" \
  "ds_read_b128 v[148:151], v230 offset:29696\n\t" \
  "v_lshlrev_b32 v24, 16, v220\n\t" \
  "v_mfma_f32_16x16x32_bf16 v[100:103], a[56:59], v[172:175], v[100:103]\n\t" \
  "v_and_b32 v25, 0xffff0000, v220\n\t" \
  "v_perm_b32 v205, v205, v201, s41\n\t" \
  "v_mfma_f32_16x16x32_bf16 v[116:119], a[60:63], v[172:175], v[116:119]\n\t" \
  "ds_read_b128 v[152:155], v230 offset:30720\n\t" \
  "v_mov_b32 v201, v239\n\t" \
  "v_mfma_f32_16x16x32_bf16 v[116:119], a[52:55], v[168:171], v[116:119]\n\t" \
  "v_lshlrev_b32 v26, 16, v221\n\t" \
  "v_perm_b32 v239, v206, v202, s40\n\t" \
  "v_mfma_f32_16x16x32_bf16 v[116:119], a[44:47], v[164:167], v[116:119]\n\t" \
  "ds_read_b128 v[156:159], v230 offset:31744\n\t" \
  "v_and_b32 v27, 0xffff0000, v221\n\t" \
  "v_mfma_f32_16x16x32_bf16 v[116:119], a[36:39], v[160:163], v[116:119]\n\t" \
  "s_waitcnt lgkmcnt(6)\n\t" \
  "v_lshlrev_b32 v28, 16, v222\n\t" \
  "v_perm_b32 v206, v206, v202, s41\n\t" \
  "v_mfma_f32_16x16x32_bf16 v[104:107], a[32:35], v[128:131], v[104:107]\n\t" \
  "ds_read_b128 v[160:163], v232 offset:0\n\t" \
  "v_mov_b32 v202, v239\n\t" \
  "v_mfma_f32_16x16x32_bf16 v[104:107], a[40:43], v[132:135], v[104:107]\n\t" \
  "v_and_b32 v29, 0xffff0000, v222\n\t" \
  "v_perm_b32 v239, v207, v203, s40\n\t" \
  "v_mfma_f32_16x16x32_bf16 v[104:107], a[48:51], v[136:139], v[104:107]\n\t" \
  "ds_read_b128 v[164:167], v232 offset:1024\n\t" \
  "v_lshlrev_b32 v30, 16, v223\n\t" \
  "v_mfma_f32_16x16x32_bf16 v[104:107], a[56:59], v[140:143], v[104:107]\n\t" \
  "v_and_b32 v31, 0xffff0000, v223\n\t" \
  "v_perm_b32 v207, v207, v203, s41\n\t" \
  "v_mfma_f32_16x16x32_bf16 v[120:123], a[60:63], v[140:143], v[120:123]\n\t" \
  "ds_read_b128 v[168:171], v232 offset:2048\n\t" \
  "v_mov_b32 v203, v239\n\t" \
  "v_mfma_f32_16x16x32_bf16 v[120:123], a[52:55], v[136:139], v[120:123]\n\t" \
  "v_accvgpr_read_b32 v239, a224\n\t" \
  "v_accvgpr_read_b32 v84, a228\n\t" \
  "v_mfma_f32_16x16x32_bf16 v[120:123], a[44:47], v[132:135], v[120:123]\n\t" \
  "ds_read_b128 v[172:175], v232 offset:3072\n\t" \
  "v_perm_b32 v80, v84, v239, s40\n\t" \
  "v_mfma_f32_16x16x32_bf16 v[120:123], a[36:39], v[128:131], v[120:123]\n\t" \
  "s_waitcnt lgkmcnt(4)\n\t" \
  "v_perm_b32 v84, v84, v239, s41\n\t" \
  "v_accvgpr_read_b32 v239, a225\n\t" \
  "v_mfma_f32_16x16x32_bf16 v[108:111], a[32:35], v[144:147], v[108:111]\n\t" \
  "ds_read_b128 v[128:131], v232 offset:4096\n\t" \
  "v_accvgpr_read_b32 v85, a229\n\t" \
  "v_mfma_f32_16x16x32_bf16 v[108:111], a[40:43], v[148:151], v[108:111]\n\t" \
  "v_perm_b32 v81, v85, v239, s40\n\t" \
  "v_perm_b32 v85, v85, v239, s41\n\t" \
  "v_mfma_f32_16x16x32_bf16 v[108:111], a[48:51], v[152:155], v[108:111]\n\t" \
  "ds_read_b128 v[132:135], v232 offset:5120\n\t" \
  "v_accvgpr_read_b32 v239, a226\n\t" \
  "v_mfma_f32_16x16x32_bf16 v[108:111], a[56:59], v[156:159], v[108:111]\n\t" \
  "v_accvgpr_read_b32 v86, a230\n\t" \
  "v_perm_b32 v82, v86, v239, s40\n\t" \
  "v_mfma_f32_16x16x32_bf16 v[124:127], a[60:63], v[156:159], v[124:127]\n\t" \
  "buffer_load_dword v246, off, s[16:19], s73 offset:32 sc1\n\t" \
  "v_mfma_f32_16x16x32_bf16 v[124:127], a[52:55], v[152:155], v[124:127]\n\t" \
  "ds_read_b128 v[136:139], v232 offset:6144\n\t" \
  "v_perm_b32 v86, v86, v239, s41\n\t" \
  "v_mfma_f32_16x16x32_bf16 v[124:127], a[44:47], v[148:151], v[124:127]\n\t" \
  ".Lhs15:\n\ts_cbranch_execz .Lhs15n\n\t" \
  "ds_read_b128 v[140:143], v232 offset:7168\n\t" \
  "v_accvgpr_read_b32 v239, a227\n\t" \
  "v_mfma_f32_16x16x32_bf16 v[124:127], a[36:39], v[144:147], v[124:127]\n\t" \
  "s_waitcnt lgkmcnt(4)\n\t" \
  "v_accvgpr_read_b32 v87, a231\n\t" \
  "v_perm_b32 v83, v87, v239, s40\n\t" \
  "v_mfma_f32_16x16x32_bf16 v[96:99], a[64:67], v[160:163], v[96:99]\n\t" \
  "ds_read_b128 v[144:147], v232 offset:8192\n\t" \
  "v_perm_b32 v87, v87, v239, s41\n\t" \
  "v_mfma_f32_16x16x32_bf16 v[96:99], a[72:75], v[164:167], v[96:99]\n\t" \
  "v_accvgpr_read_b32 v239, a232\n\t" \
  "v_accvgpr_read_b32 v92, a236\n\t" \
  "v_mfma_f32_16x16x32_bf16 v[96:99], a[80:83], v[168:171], v[96:99]\n\t" \
  "ds_read_b128 v[148:151], v232 offset:9216\n\t" \
  "v_perm_b32 v88, v92, v239, s40\n\t" \
  "v_mfma_f32_16x16x32_bf16 v[96:99], a[88:91], v[172:175], v[96:99]\n\t" \
  "v_perm_b32 v92, v92, v239, s41\n\t" \
  "v_accvgpr_read_b32 v239, a233\n\t" \
  "v_mfma_f32_16x16x32_bf16 v[112:115], a[92:95], v[172:175], v[112:115]\n\t" \
  "ds_read_b128 v[152:155], v232 offset:10240\n\t" \
  "v_accvgpr_read_b32 v93, a237\n\t" \
  "v_mfma_f32_16x16x32_bf16 v[112:115], a[84:87], v[168:171], v[112:115]\n\t" \
  "v_perm_b32 v89, v93, v239, s40\n\t" \
  "v_perm_b32 v93, v93, v239, s41\n\t" \
  "v_mfma_f32_16x16x32_bf16 v[112:115], a[76:79], v[164:167], v[112:115]\n\t" \
  "ds_read_b128 v[156:159], v232 offset:11264\n\t" \
  "v_accvgpr_read_b32 v239, a234\n\t" \
  "v_mfma_f32_16x16x32_bf16 v[112:115], a[68:71], v[160:163], v[112:115]\n\t" \
  "s_waitcnt lgkmcnt(4)\n\t" \
  "v_accvgpr_read_b32 v94, a238\n\t" \
  "v_perm_b32 v90, v94, v239, s40\n\t" \
  "v_mfma_f32_16x16x32_bf16 v[100:103], a[64:67], v[128:131], v[100:103]\n\t" \
  "ds_read_b128 v[160:163], v232 offset:12288\n\t" \
  "v_perm_b32 v94, v94, v239, s41\n\t" \
  "v_mfma_f32_16x16x32_bf16 v[100:103], a[72:75], v[132:135], v[100:103]\n\t" \
  "v_accvgpr_read_b32 v239, a235\n\t" \
  "v_accvgpr_read_b32 v95, a239\n\t" \
  "v_mfma_f32_16x16x32_bf16 v[100:103], a[80:83], v[136:139], v[100:103]\n\t" \
  "ds_read_b128 v[164:167], v232 offset:13312\n\t" \
  "v_perm_b32 v91, v95, v239, s40\n\t" \
  "v_mfma_f32_16x16x32_bf16 v[100:103], a[88:91], v[140:143], v[100:103]\n\t" \
  "v_perm_b32 v95, v95, v239, s41\n\t" \
  "v_accvgpr_read_b32 v239, a240\n\t" \
  "v_mfma_f32_16x16x32_bf16 v[116:119], a[92:95], v[140:143], v[116:119]\n\t" \
  "ds_read_b128 v[168:171], v232 offset:14336\n\t" \
  "v_accvgpr_read_b32 v212, a244\n\t" \
  "v_mfma_f32_16x16x32_bf16 v[116:119], a[84:87], v[136:139], v[116:119]\n\t" \
  "v_perm_b32 v208, v212, v239, s40\n\t" \
  "v_perm_b32 v212, v212, v239, s41\n\t" \
  "v_mfma_f32_16x16x32_bf16 v[116:119], a[76:79], v[132:135], v[116:119]\n\t" \
  "s_waitcnt vmcnt(9) lgkmcnt(3)\n\t" \
  "ds_read_b128 v[172:175], v232 offset:15360\n\t" \
  "v_accvgpr_read_b32 v239, a241\n\t" \
  "v_mfma_f32_16x16x32_bf16 v[116:119], a[68:71], v[128:131], v[116:119]\n\t" \
  "s_barrier\n\t" \
  "v_mfma_f32_16x16x32_bf16 v[104:107], a[64:67], v[144:147], v[104:107]\n\t" \
  "ds_read_b128 v[128:131], v230 offset:0\n\t" \
  "v_accvgpr_read_b32 v213, a245\n\t" \
  "v_mfma_f32_16x16x32_bf16 v[104:107], a[72:75], v[148:151], v[104:107]\n\t" \
  "v_perm_b32 v209, v213, v239, s40\n\t" \
  "v_perm_b32 v213, v213, v239, s41\n\t" \
  "v_mfma_f32_16x16x32_bf16 v[104:107], a[80:83], v[152:155], v[104:107]\n\t" \
  "ds_read_b128 v[132:135], v230 offset:1024\n\t" \
  "v_accvgpr_read_b32 v239, a242\n\t" \
  "v_mfma_f32_16x16x32_bf16 v[104:107], a[88:91], v[156:159], v[104:107]\n\t" \
  "v_accvgpr_read_b32 v214, a246\n\t" \
  "v_perm_b32 v210, v214, v239, s40\n\t" \
  "v_mfma_f32_16x16x32_bf16 v[120:123], a[92:95], v[156:159], v[120:123]\n\t" \
  "ds_read_b128 v[136:139], v230 offset:2048\n\t" \
  "v_perm_b32 v214, v214, v239, s41\n\t" \
  "v_mfma_f32_16x16x32_bf16 v[120:123], a[84:87], v[152:155], v[120:123]\n\t" \
  "v_accvgpr_read_b32 v239, a243\n\t" \
  "v_accvgpr_read_b32 v215, a247\n\t" \
  "v_mfma_f32_16x16x32_bf16 v[120:123], a[76:79], v[148:151], v[120:123]\n\t" \
  "ds_read_b128 v[140:143], v230 offset:3072\n\t" \
  "v_perm_b32 v211, v215, v239, s40\n\t" \
  "v_mfma_f32_16x16x32_bf16 v[120:123], a[68:71], v[144:147], v[120:123]\n\t" \
  "s_waitcnt lgkmcnt(4)\n\t" \
  "v_perm_b32 v215, v215, v239, s41\n\t" \
  "v_accvgpr_read_b32 v239, a248\n\t" \
  "v_mfma_f32_16x16x32_bf16 v[108:111], a[64:67], v[160:163], v[108:111]\n\t" \
  "ds_read_b128 v[144:147], v230 offset:4096\n\t" \
  "v_accvgpr_read_b32 v220, a252\n\t" \
  "v_mfma_f32_16x16x32_bf16 v[108:111], a[72:75], v[164:167], v[108:111]\n\t" \
  "v_perm_b32 v216, v220, v239, s40\n\t" \
  "v_perm_b32 v220, v220, v239, s41\n\t" \
  "v_mfma_f32_16x16x32_bf16 v[108:111], a[80:83], v[168:171], v[108:111]\n\t" \
  "ds_read_b128 v[148:151], v230 offset:5120\n\t" \
  "v_accvgpr_read_b32 v239, a249\n\t" \
  "v_mfma_f32_16x16x32_bf16 v[108:111], a[88:91], v[172:175], v[108:111]\n\t" \
  "buffer_load_dword v251, off, s[16:19], s73 offset:32 sc1\n\t" \
  "v_mfma_f32_16x16x32_bf16 v[124:127], a[92:95], v[172:175], v[124:127]\n\t" \
  "ds_read_b128 v[152:155], v230 offset:6144\n\t" \
  "v_accvgpr_read_b32 v221, a253\n\t" \
  "v_mfma_f32_16x16x32_bf16 v[124:127], a[84:87], v[168:171], v[124:127]\n\t" \
  "v_perm_b32 v217, v221, v239, s40\n\t" \
  "v_perm_b32 v221, v221, v239, s41\n\t" \
  "v_mfma_f32_16x16x32_bf16 v[124:127], a[76:79], v[164:167], v[124:127]\n\t" \
  "ds_read_b128 v[156:159], v230 offset:7168\n\t" \
  "v_accvgpr_read_b32 v239, a250\n\t" \
  "v_mfma_f32_16x16x32_bf16 v[124:127], a[68:71], v[160:163], v[124:127]\n\t" \
  "s_waitcnt lgkmcnt(4)\n\t" \
  "v_accvgpr_read_b32 v222, a254\n\t" \
  "v_perm_b32 v218, v222, v239, s40\n\t" \
  "v_mfma_f32_16x16x32_bf16 v[96:99], a[96:99], v[128:131], v[96:99]\n\t" \
  "s_waitcnt vmcnt(1)\n\t" \
  "v_cmp_ne_u32 vcc, s36, v246\n\t" \
  "v_perm_b32 v222, v222, v239, s41\n\t" \
  "v_mfma_f32_16x16x32_bf16 v[96:99], a[104:107], v[132:135], v[96:99]\n\t" \
  "ds_read_b128 v[160:163], v230 offset:8192\n\t" \
  "v_accvgpr_read_b32 v239, a251\n\t" \
  "v_mfma_f32_16x16x32_bf16 v[96:99], a[112:115], v[136:139], v[96:99]\n\t" \
  "s_mov_b32 s88, vcc_lo\n\t" \
  "s_cbranch_vccnz .Lat_n2\n\t" \
  "buffer_load_dwordx4 v[64:67], v242, s[16:19], s79 offen offset:0 sc1\n\t" \
  "buffer_load_dwordx4 v[68:71], v242, s[16:19], s79 offen offset:1024 sc1\n\t" \
  "buffer_load_dwordx4 v[72:75], v242, s[16:19], s79 offen offset:2048 sc1\n\t" \
  "buffer_load_dwordx4 v[76:79], v242, s[16:19], s79 offen offset:3072 sc1\n\t" \
  ".Lat_n2:\n\t" \
  "ds_read_b128 v[164:167], v230 offset:9216\n\t" \
  "v_accvgpr_read_b32 v223, a255\n\t" \
  "v_mfma_f32_16x16x32_bf16 v[96:99], a[120:123], v[140:143], v[96:99]\n\t" \
  "v_perm_b32 v219, v223, v239, s40\n\t" \
  "v_perm_b32 v223, v223, v239, s41\n\t" \
  "v_mfma_f32_16x16x32_bf16 v[112:115], a[124:127], v[140:143], v[112:115]\n\t" \
  "ds_read_b128 v[168:171], v230 offset:10240\n\t" \
  "v_mfma_f32_16x16x32_bf16 v[112:115], a[116:119], v[136:139], v[112:115]\n\t" \
  "ds_read_b128 v[172:175], v230 offset:11264\n\t" \
  "v_mfma_f32_16x16x32_bf16 v[112:115], a[108:111], v[132:135], v[112:115]\n\t" \
  "v_mfma_f32_16x16x32_bf16 v[112:115], a[100:103], v[128:131], v[112:115]\n\t" \
  "s_waitcnt lgkmcnt(4)\n\t" \
  "v_mfma_f32_16x16x32_bf16 v[100:103], a[96:99], v[144:147], v[100:103]\n\t" \
  "ds_read_b128 v[128:131], v230 offset:12288\n\t" \
  "v_mfma_f32_16x16x32_bf16 v[100:103], a[104:107], v[148:151], v[100:103]\n\t" \
  "ds_read_b128 v[132:135], v230 offset:13312\n\t" \
  "v_mfma_f32_16x16x32_bf16 v[100:103], a[112:115], v[152:155], v[100:103]\n\t" \
  "ds_read_b128 v[136:139], v230 offset:14336\n\t" \
  "v_mfma_f32_16x16x32_bf16 v[100:103], a[120:123], v[156:159], v[100:103]\n\t" \
  "ds_read_b128 v[140:143], v230 offset:15360\n\t" \
  "v_mfma_f32_16x16x32_bf16 v[116:119], a[124:127], v[156:159], v[116:119]\n\t" \
  "v_mfma_f32_16x16x32_bf16 v[116:119], a[116:119], v[152:155], v[116:119]\n\t" \
  "v_mfma_f32_16x16x32_bf16 v[116:119], a[108:111], v[148:151], v[116:119]\n\t" \
  "v_mfma_f32_16x16x32_bf16 v[116:119], a[100:103], v[144:147], v[116:119]\n\t" \
  "s_waitcnt lgkmcnt(4)\n\t" \
  "v_mfma_f32_16x16x32_bf16 v[104:107], a[96:99], v[160:163], v[104:107]\n\t" \
  "ds_read_b128 v[144:147], v242 offset:16384\n\t" \
  "v_mfma_f32_16x16x32_bf16 v[104:107], a[104:107], v[164:167], v[104:107]\n\t" \
  "ds_read_b128 v[148:151], v242 offset:17408\n\t" \
  "v_mfma_f32_16x16x32_bf16 v[104:107], a[112:115], v[168:171], v[104:107]\n\t" \
  "ds_read_b128 v[152:155], v242 offset:18432\n\t" \
  "v_mfma_f32_16x16x32_bf16 v[104:107], a[120:123], v[172:175], v[104:107]\n\t" \
  "ds_read_b128 v[156:159], v242 offset:19456\n\t" \
  "v_mfma_f32_16x16x32_bf16 v[120:123], a[124:127], v[172:175], v[120:123]\n\t" \
  "v_mfma_f32_16x16x32_bf16 v[120:123], a[116:119], v[168:171], v[120:123]\n\t" \
  "v_mfma_f32_16x16x32_bf16 v[120:123], a[108:111], v[164:167], v[120:123]\n\t" \
  "v_mfma_f32_16x16x32_bf16 v[120:123], a[100:103], v[160:163], v[120:123]\n\t" \
  "s_waitcnt lgkmcnt(4)\n\t" \
  "v_mfma_f32_16x16x32_bf16 v[108:111], a[96:99], v[128:131], v[108:111]\n\t" \
  "ds_read_b128 v[160:163], v242 offset:20480\n\t" \
  "v_mfma_f32_16x16x32_bf16 v[108:111], a[104:107], v[132:135], v[108:111]\n\t" \
  "ds_read_b128 v[164:167], v242 offset:21504\n\t" \
  "v_mfma_f32_16x16x32_bf16 v[108:111], a[112:115], v[136:139], v[108:111]\n\t" \
  "ds_read_b128 v[168:171], v242 offset:22528\n\t" \
  "v_mfma_f32_16x16x32_bf16 v[108:111], a[120:123], v[140:143], v[108:111]\n\t" \
  "ds_read_b128 v[172:175], v242 offset:23552\n\t" \
  "v_mfma_f32_16x16x32_bf16 v[124:127], a[124:127], v[140:143], v[124:127]\n\t" \
  "v_mfma_f32_16x16x32_bf16 v[124:127], a[116:119], v[136:139], v[124:127]\n\t" \
  "v_mfma_f32_16x16x32_bf16 v[124:127], a[108:111], v[132:135], v[124:127]\n\t" \
  "v_mfma_f32_16x16x32_bf16 v[124:127], a[100:103], v[128:131], v[124:127]\n\t" \
  "v_mfma_f32_16x16x32_bf16 v[96:99], a[130:133], v[32:35], v[96:99]\n\t" \
  "v_mfma_f32_16x16x32_bf16 v[100:103], a[130:133], v[36:39], v[100:103]\n\t" \
  "v_mfma_f32_16x16x32_bf16 v[104:107], a[130:133], v[40:43], v[104:107]\n\t" \
  "v_mfma_f32_16x16x32_bf16 v[108:111], a[130:133], v[44:47], v[108:111]\n\t" \
  "v_mfma_f32_16x16x32_bf16 v[112:115], a[128:131], v[32:35], v[112:115]\n\t" \
  "v_mfma_f32_16x16x32_bf16 v[116:119], a[128:131], v[36:39], v[116:119]\n\t" \
  "v_mfma_f32_16x16x32_bf16 v[120:123], a[128:131], v[40:43], v[120:123]\n\t" \
  "v_mfma_f32_16x16x32_bf16 v[124:127], a[128:131], v[44:47], v[124:127]\n\t" \
  "s_waitcnt lgkmcnt(4)\n\t" \
  "v_mfma_f32_16x16x32_bf16 v[96:99], a[192:195], v[144:147], v[96:99]\n\t" \
  "s_cmp_lg_u32 s88, 0\n\t" \
  "s_cbranch_scc1 .Lat_m3\n\t" \
  ".Lat_n3:\n\t" \
  "v_mfma_f32_16x16x32_bf16 v[96:99], a[200:203], v[148:151], v[96:99]\n\t" \
  "s_add_u32 m0, s38, 32768\n\t" \
  "ds_read_b128 v[128:131], v242 offset:24576\n\t" \
  "s_mov_b32 s96, 0\n\t" \
  "v_mfma_f32_16x16x32_bf16 v[96:99], a[208:211], v[152:155], v[96:99]\n\t" \
  "buffer_load_dwordx4 v252, s[28:31], s96 offen offset:0 lds\n\t" \
  "v_mfma_f32_16x16x32_bf16 v[96:99], a[216:219], v[156:159], v[96:99]\n\t" \
  "ds_read_b128 v[132:135], v242 offset:25600\n\t" \
  "s_mov_b32 s96, 3072\n\t" \
  "v_mfma_f32_16x16x32_bf16 v[112:115], a[220:223], v[156:159], v[112:115]\n\t" \
  "buffer_load_dwordx4 v252, s[28:31], s96 offen offset:1024 lds\n\t" \
  "v_mfma_f32_16x16x32_bf16 v[112:115], a[212:215], v[152:155], v[112:115]\n\t" \
  "ds_read_b128 v[136:139], v242 offset:26624\n\t" \
  "s_mov_b32 s96, 6144\n\t" \
  "v_mfma_f32_16x16x32_bf16 v[112:115], a[204:207], v[148:151], v[112:115]\n\t" \
  "buffer_load_dwordx4 v252, s[28:31], s96 offen offset:2048 lds\n\t" \
  "v_mfma_f32_16x16x32_bf16 v[112:115], a[196:199], v[144:147], v[112:115]\n\t" \
  "ds_read_b128 v[140:143], v242 offset:27648\n\t" \
  "s_mov_b32 s96, 9216\n\t" \
  "v_mfma_f32_16x16x32_bf16 v[96:99], a[130:133], v[48:51], v[96:99]\n\t" \
  "buffer_load_dwordx4 v252, s[28:31], s96 offen offset:3072 lds\n\t" \
  "v_mfma_f32_16x16x32_bf16 v[100:103], a[130:133], v[52:55], v[100:103]\n\t" \
  "s_add_u32 m0, s38, 49152\n\t" \
  "s_mov_b32 s96, 16384\n\t" \
  "v_mfma_f32_16x16x32_bf16 v[104:107], a[130:133], v[56:59], v[104:107]\n\t" \
  "buffer_load_dwordx4 v252, s[28:31], s96 offen offset:0 lds\n\t" \
  "v_mfma_f32_16x16x32_bf16 v[108:111], a[130:133], v[60:63], v[108:111]\n\t" \
  "s_mov_b32 s96, 19456\n\t" \
  "v_mfma_f32_16x16x32_bf16 v[112:115], a[128:131], v[48:51], v[112:115]\n\t" \
  "buffer_load_dwordx4 v252, s[28:31], s96 offen offset:1024 lds\n\t" \
  "v_mfma_f32_16x16x32_bf16 v[116:119], a[128:131], v[52:55], v[116:119]\n\t" \
  "s_mov_b32 s96, 22528\n\t" \
  "v_mfma_f32_16x16x32_bf16 v[120:123], a[128:131], v[56:59], v[120:123]\n\t" \
  "buffer_load_dwordx4 v252, s[28:31], s96 offen offset:2048 lds\n\t" \
  "v_mfma_f32_16x16x32_bf16 v[124:127], a[128:131], v[60:63], v[124:127]\n\t" \
  "s_waitcnt lgkmcnt(4)\n\t" \
  "s_mov_b32 s96, 25600\n\t" \
  "v_mfma_f32_16x16x32_bf16 v[100:103], a[192:195], v[160:163], v[100:103]\n\t" \
  "buffer_load_dwordx4 v252, s[28:31], s96 offen offset:3072 lds\n\t" \
  "v_mfma_f32_16x16x32_bf16 v[100:103], a[200:203], v[164:167], v[100:103]\n\t" \
  "ds_read_b128 v[144:147], v242 offset:28672\n\t" \
  "v_mfma_f32_16x16x32_bf16 v[100:103], a[208:211], v[168:171], v[100:103]\n\t" \
  "ds_read_b128 v[148:151], v242 offset:29696\n\t" \
  "v_mfma_f32_16x16x32_bf16 v[100:103], a[216:219], v[172:175], v[100:103]\n\t" \
  "ds_read_b128 v[152:155], v242 offset:30720\n\t" \
  "v_mfma_f32_16x16x32_bf16 v[116:119], a[220:223], v[172:175], v[116:119]\n\t" \
  "ds_read_b128 v[156:159], v242 offset:31744\n\t" \
  "v_mfma_f32_16x16x32_bf16 v[116:119], a[212:215], v[168:171], v[116:119]\n\t" \
  "v_mfma_f32_16x16x32_bf16 v[116:119], a[204:207], v[164:167], v[116:119]\n\t" \
  "v_mfma_f32_16x16x32_bf16 v[116:119], a[196:199], v[160:163], v[116:119]\n\t" \
  "s_waitcnt lgkmcnt(4)\n\t" \
  "v_mfma_f32_16x16x32_bf16 v[104:107], a[192:195], v[128:131], v[104:107]\n\t" \
  "ds_read_b128 v[160:163], v237 offset:2560\n\t" \
  "v_mfma_f32_16x16x32_bf16 v[104:107], a[200:203], v[132:135], v[104:107]\n\t" \
  "ds_read_b128 v[164:167], v237 offset:2624\n\t" \
  "v_mfma_f32_16x16x32_bf16 v[104:107], a[208:211], v[136:139], v[104:107]\n\t" \
  "v_mfma_f32_16x16x32_bf16 v[104:107], a[216:219], v[140:143], v[104:107]\n\t" \
  "v_mfma_f32_16x16x32_bf16 v[120:123], a[220:223], v[140:143], v[120:123]\n\t" \
  "v_mfma_f32_16x16x32_bf16 v[120:123], a[212:215], v[136:139], v[120:123]\n\t" \
  "v_mfma_f32_16x16x32_bf16 v[120:123], a[204:207], v[132:135], v[120:123]\n\t" \
  "v_mfma_f32_16x16x32_bf16 v[120:123], a[196:199], v[128:131], v[120:123]\n\t" \
  "s_waitcnt lgkmcnt(2)\n\t" \
  "v_mfma_f32_16x16x32_bf16 v[108:111], a[192:195], v[144:147], v[108:111]\n\t" \
  "v_mfma_f32_16x16x32_bf16 v[108:111], a[200:203], v[148:151], v[108:111]\n\t" \
  "v_mfma_f32_16x16x32_bf16 v[108:111], a[208:211], v[152:155], v[108:111]\n\t" \
  "v_mfma_f32_16x16x32_bf16 v[108:111], a[216:219], v[156:159], v[108:111]\n\t" \
  "v_mfma_f32_16x16x32_bf16 v[124:127], a[220:223], v[156:159], v[124:127]\n\t" \
  "v_mfma_f32_16x16x32_bf16 v[124:127], a[212:215], v[152:155], v[124:127]\n\t" \
  "v_mfma_f32_16x16x32_bf16 v[124:127], a[204:207], v[148:151], v[124:127]\n\t" \
  "v_mfma_f32_16x16x32_bf16 v[124:127], a[196:199], v[144:147], v[124:127]\n\t" \
  STP("s_memrealtime s[94:95]\n\t") \
  STP("s_memtime s[92:93]\n\t") \
  "s_cmp_lg_u32 s88, 0\n\t" \
  "s_cbranch_scc1 .Lat_s99\n\t" \
  ".Lat_d2:\n\t" \
  "s_waitcnt vmcnt(8)\n\t" \
  "v_mfma_f32_16x16x32_bf16 v[96:99], a[130:133], v[64:67], v[96:99]\n\t" \
  "v_mfma_f32_16x16x32_bf16 v[100:103], a[130:133], v[68:71], v[100:103]\n\t" \
  "v_mfma_f32_16x16x32_bf16 v[104:107], a[130:133], v[72:75], v[104:107]\n\t" \
  "v_mfma_f32_16x16x32_bf16 v[108:111], a[130:133], v[76:79], v[108:111]\n\t" \
  "v_mfma_f32_16x16x32_bf16 v[112:115], a[128:131], v[64:67], v[112:115]\n\t" \
  "v_mfma_f32_16x16x32_bf16 v[116:119], a[128:131], v[68:71], v[116:119]\n\t" \
  "v_mfma_f32_16x16x32_bf16 v[120:123], a[128:131], v[72:75], v[120:123]\n\t" \
  "v_mfma_f32_16x16x32_bf16 v[124:127], a[128:131], v[76:79], v[124:127]\n\t" \
  "s_waitcnt lgkmcnt(0)\n\t" \
  STP("v_writelane_b32 v255, s94, 38\n\t") \
  STP("v_writelane_b32 v255, s92, 39\n\t") \
  "v_pk_fma_f32 v[0:1], v[160:161], v[96:97], v[0:1]\n\t" \
  "v_pk_fma_f32 v[4:5], v[160:161], v[100:101], v[4:5]\n\t" \
  "v_pk_fma_f32 v[8:9], v[160:161], v[104:105], v[8:9]\n\t" \
  "v_pk_fma_f32 v[12:13], v[160:161], v[108:109], v[12:13]\n\t" \
  "v_pk_fma_f32 v[2:3], v[162:163], v[98:99], v[2:3]\n\t" \
  "v_pk_fma_f32 v[6:7], v[162:163], v[102:103], v[6:7]\n\t" \
  "v_pk_fma_f32 v[10:11], v[162:163], v[106:107], v[10:11]\n\t" \
  "v_pk_fma_f32 v[14:15], v[162:163], v[110:111], v[14:15]\n\t" \
  "v_pk_fma_f32 v[16:17], v[164:165], v[112:113], v[16:17]\n\t" \
  "v_pk_fma_f32 v[20:21], v[164:165], v[116:117], v[20:21]\n\t" \
  "v_pk_fma_f32 v[24:25], v[164:165], v[120:121], v[24:25]\n\t" \
  "v_pk_fma_f32 v[28:29], v[164:165], v[124:125], v[28:29]\n\t" \
  "v_pk_fma_f32 v[18:19], v[166:167], v[114:115], v[18:19]\n\t" \
  "v_pk_fma_f32 v[22:23], v[166:167], v[118:119], v[22:23]\n\t" \
  "v_pk_fma_f32 v[26:27], v[166:167], v[122:123], v[26:27]\n\t" \
  "v_pk_fma_f32 v[30:31], v[166:167], v[126:127], v[30:31]\n\t" \
  "v_cvt_pk_bf16_f32 v128, v0, v1\n\t" \
  "v_cvt_pk_bf16_f32 v132, v4, v5\n\t" \
  "v_cvt_pk_bf16_f32 v136, v8, v9\n\t" \
  "v_cvt_pk_bf16_f32 v140, v12, v13\n\t" \
  "v_cvt_pk_bf16_f32 v129, v2, v3\n\t" \
  "v_cvt_pk_bf16_f32 v133, v6, v7\n\t" \
  "v_cvt_pk_bf16_f32 v137, v10, v11\n\t" \
  "v_cvt_pk_bf16_f32 v141, v14, v15\n\t" \
  "v_cvt_pk_bf16_f32 v130, v16, v17\n\t" \
  "v_cvt_pk_bf16_f32 v134, v20, v21\n\t" \
  "v_cvt_pk_bf16_f32 v138, v24, v25\n\t" \
  "v_cvt_pk_bf16_f32 v142, v28, v29\n\t" \
  "v_cvt_pk_bf16_f32 v131, v18, v19\n\t" \
  "v_cvt_pk_bf16_f32 v135, v22, v23\n\t" \
  "v_cvt_pk_bf16_f32 v139, v26, v27\n\t" \
  "v_cvt_pk_bf16_f32 v143, v30, v31\n\t" \
  "v_dot2_f32_bf16 v64, v128, v128, 0\n\t" \
  "v_dot2_f32_bf16 v65, v132, v132, 0\n\t" \
  "v_dot2_f32_bf16 v66, v136, v136, 0\n\t" \
  "v_dot2_f32_bf16 v67, v140, v140, 0\n\t" \
  "v_dot2_f32_bf16 v64, v129, v129, v64\n\t" \
  "v_dot2_f32_bf16 v65, v133, v133, v65\n\t" \
  "v_dot2_f32_bf16 v66, v137, v137, v66\n\t" \
  "v_dot2_f32_bf16 v67, v141, v141, v67\n\t" \
  "v_dot2_f32_bf16 v64, v130, v130, v64\n\t" \
  "v_dot2_f32_bf16 v65, v134, v134, v65\n\t" \
  "v_dot2_f32_bf16 v66, v138, v138, v66\n\t" \
  "v_dot2_f32_bf16 v67, v142, v142, v67\n\t" \
  "v_dot2_f32_bf16 v64, v131, v131, v64\n\t" \
  "v_dot2_f32_bf16 v65, v135, v135, v65\n\t" \
  "v_dot2_f32_bf16 v66, v139, v139, v66\n\t" \
  "v_dot2_f32_bf16 v67, v143, v143, v67\n\t" \
  "v_mfma_f32_16x16x32_bf16 v[68:71], a[176:179], v[128:131], 0\n\t" \
  "v_mfma_f32_16x16x32_bf16 v[68:71], a[180:183], v[132:135], v[68:71]\n\t" \
  "v_permlane32_swap_b32 v64, v66\n\t" \
  "v_mfma_f32_16x16x32_bf16 v[68:71], a[184:187], v[136:139], v[68:71]\n\t" \
  "v_permlane32_swap_b32 v65, v67\n\t" \
  "v_mfma_f32_16x16x32_bf16 v[68:71], a[188:191], v[140:143], v[68:71]\n\t" \
  "v_add_f32 v64, v64, v66\n\t" \
  "v_add_f32 v65, v65, v67\n\t" \
  "v_mov_b32 v73, s36\n\t" \
  "v_mov_b32 v75, s36\n\t" \
  "v_permlane16_swap_b32 v64, v65\n\t" \
  "s_mov_b32 s81, 0\n\t" \
  "s_nop 1\n\t" \
  "v_add_f32 v70, v69, v70\n\t" \
  "v_add_f32 v69, v64, v65\n\t" \
  "v_mov_b32 v71, v238\n\t" \
  "ds_write_b128 v224, v[68:71]\n\t" \
  "s_waitcnt lgkmcnt(0)\n\t" \
  "s_barrier\n\t" \
  "ds_read_b128 v[96:99], v242 offset:0\n\t" \
  "ds_read_b128 v[100:103], v242 offset:1024\n\t" \
  "ds_read_b128 v[104:107], v242 offset:2048\n\t" \
  "ds_read_b128 v[108:111], v242 offset:3072\n\t" \
  "s_cmp_eq_u32 s3, 0\n\t" \
  "s_cselect_b64 vcc, -1, 0\n\t" \
  "s_waitcnt lgkmcnt(2)\n\t" \
  "v_pk_add_f32 v[96:97], v[96:97], v[100:101]\n\t" \
  "v_pk_add_f32 v[98:99], v[98:99], v[102:103]\n\t" \
  "s_waitcnt lgkmcnt(0)\n\t" \
  "v_pk_add_f32 v[104:105], v[104:105], v[108:109]\n\t" \
  "v_pk_add_f32 v[106:107], v[106:107], v[110:111]\n\t" \
  "v_pk_add_f32 v[64:65], v[96:97], v[104:105]\n\t" \
  "v_pk_add_f32 v[66:67], v[98:99], v[106:107]\n\t" \
  "v_cndmask_b32 v72, v66, v64, vcc\n\t" \
  "v_cndmask_b32 v74, v67, v65, vcc\n\t" \
  "buffer_store_dwordx4 v[72:75], v240, s[16:19], s10 offen sc1\n\t" \
  STP("s_memrealtime s[94:95]\n\t") \
  STP("s_memtime s[92:93]\n\t") \
  STP("s_waitcnt lgkmcnt(0)\n\t") \
  STP("v_writelane_b32 v255, s94, 40\n\t") \
  STP("v_writelane_b32 v255, s92, 41\n\t") \
  "buffer_load_dwordx4 v[96:99], v242, s[16:19], s11 offen offset:0 sc1\n\t" \
  "buffer_load_dwordx4 v[100:103], v242, s[16:19], s11 offen offset:1024 sc1\n\t" \
  "buffer_load_dwordx4 v[104:107], v242, s[16:19], s12 offen offset:0 sc1\n\t" \
  "buffer_load_dwordx4 v[108:111], v242, s[16:19], s12 offen offset:1024 sc1\n\t" \
  "ds_read_b128 v[144:147], v237 offset:2688\n\t" \
  "ds_read_b128 v[148:151], v237 offset:2752\n\t" \
  "ds_read_b128 v[152:155], v237 offset:2816\n\t" \
  "ds_read_b128 v[156:159], v237 offset:2880\n\t" \
  "ds_read_b128 v[168:171], v237 offset:2944\n\t" \
  "ds_read_b128 v[172:175], v237 offset:3008\n\t" \
  "v_lshlrev_b32 v0, 16, v128\n\t" \
  "v_and_b32 v1, 0xffff0000, v128\n\t" \
  "v_lshlrev_b32 v2, 16, v129\n\t" \
  "v_and_b32 v3, 0xffff0000, v129\n\t" \
  "v_lshlrev_b32 v4, 16, v130\n\t" \
  "v_and_b32 v5, 0xffff0000, v130\n\t" \
  "v_lshlrev_b32 v6, 16, v131\n\t" \
  "v_and_b32 v7, 0xffff0000, v131\n\t" \
  "v_lshlrev_b32 v8, 16, v132\n\t" \
  "v_and_b32 v9, 0xffff0000, v132\n\t" \
  "v_lshlrev_b32 v10, 16, v133\n\t" \
  "v_and_b32 v11, 0xffff0000, v133\n\t" \
  "v_lshlrev_b32 v12, 16, v134\n\t" \
  "v_and_b32 v13, 0xffff0000, v134\n\t" \
  "v_lshlrev_b32 v14, 16, v135\n\t" \
  "v_and_b32 v15, 0xffff0000, v135\n\t" \
  "v_lshlrev_b32 v16, 16, v136\n\t" \
  "v_and_b32 v17, 0xffff0000, v136\n\t" \
  "v_lshlrev_b32 v18, 16, v137\n\t" \
  "v_and_b32 v19, 0xffff0000, v137\n\t" \
  "v_lshlrev_b32 v20, 16, v138\n\t" \
  "v_and_b32 v21, 0xffff0000, v138\n\t" \
  "v_lshlrev_b32 v22, 16, v139\n\t" \
  "v_and_b32 v23, 0xffff0000, v139\n\t" \
  "v_lshlrev_b32 v24, 16, v140\n\t" \
  "v_and_b32 v25, 0xffff0000, v140\n\t" \
  "v_lshlrev_b32 v26, 16, v141\n\t" \
  "v_and_b32 v27, 0xffff0000, v141\n\t" \
  "v_lshlrev_b32 v28, 16, v142\n\t" \
  "v_and_b32 v29, 0xffff0000, v142\n\t" \
  "v_lshlrev_b32 v30, 16, v143\n\t" \
  "v_and_b32 v31, 0xffff0000, v143\n\t" \
  "s_waitcnt vmcnt(5)\n\t" \
  "ds_read2st64_b64 v[32:35], v247 offset0:64 offset1:65\n\t" \
  "ds_read2st64_b64 v[36:39], v247 offset0:66 offset1:67\n\t" \
  "ds_read2st64_b64 v[40:43], v247 offset0:68 offset1:69\n\t" \
  "ds_read2st64_b64 v[44:47], v247 offset0:70 offset1:71\n\t" \
  "ds_read2st64_b64 v[48:51], v247 offset0:96 offset1:97\n\t" \
  "ds_read2st64_b64 v[52:55], v247 offset0:98 offset1:99\n\t" \
  "ds_read2st64_b64 v[56:59], v247 offset0:100 offset1:101\n\t" \
  "ds_read2st64_b64 v[60:63], v247 offset0:102 offset1:103\n\t" \
  "s_waitcnt lgkmcnt(0)\n\t" \
  "v_perm_b32 v112, v34, v32, s40\n\t" \
  "v_perm_b32 v34, v34, v32, s41\n\t" \
  "v_perm_b32 v113, v35, v33, s40\n\t" \
  "v_perm_b32 v35, v35, v33, s41\n\t" \
  "v_perm_b32 v114, v38, v36, s40\n\t" \
  "v_perm_b32 v38, v38, v36, s41\n\t" \
  "v_perm_b32 v115, v39, v37, s40\n\t" \
  "v_perm_b32 v39, v39, v37, s41\n\t" \
  "v_perm_b32 v116, v42, v40, s40\n\t" \
  "v_perm_b32 v42, v42, v40, s41\n\t" \
  "v_perm_b32 v117, v43, v41, s40\n\t" \
  "v_perm_b32 v43, v43, v41, s41\n\t" \
  "v_perm_b32 v118, v46, v44, s40\n\t" \
  "v_perm_b32 v46, v46, v44, s41\n\t" \
  "v_perm_b32 v119, v47, v45, s40\n\t" \
  "v_perm_b32 v47, v47, v45, s41\n\t" \
  "v_perm_b32 v120, v50, v48, s40\n\t" \
  "v_perm_b32 v50, v50, v48, s41\n\t" \
  "v_perm_b32 v121, v51, v49, s40\n\t" \
  "v_perm_b32 v51, v51, v49, s41\n\t" \
  "v_perm_b32 v122, v54, v52, s40\n\t" \
  "v_perm_b32 v54, v54, v52, s41\n\t" \
  "v_perm_b32 v123, v55, v53, s40\n\t" \
  "v_perm_b32 v55, v55, v53, s41\n\t" \
  "v_perm_b32 v124, v58, v56, s40\n\t" \
  "v_perm_b32 v58, v58, v56, s41\n\t" \
  "v_perm_b32 v125, v59, v57, s40\n\t" \
  "v_perm_b32 v59, v59, v57, s41\n\t" \
  "v_perm_b32 v126, v62, v60, s40\n\t" \
  "v_perm_b32 v62, v62, v60, s41\n\t" \
  "v_perm_b32 v127, v63, v61, s40\n\t" \
  "v_perm_b32 v63, v63, v61, s41\n\t" \
  "s_cmp_lt_u32 s39, 4096\n\t" \
  "s_cselect_b64 s[52:53], -1, 0\n\t" \
  "s_branch .Ltl_w1\n\t" \
  ".Ltl_r1:\n\t" \
  "s_sleep 1\n\t" \
  "buffer_load_dwordx4 v[96:99], v242, s[16:19], s11 offen offset:0 sc1\n\t" \
  "buffer_load_dwordx4 v[100:103], v242, s[16:19], s11 offen offset:1024 sc1\n\t" \
  "buffer_load_dwordx4 v[104:107], v242, s[16:19], s12 offen offset:0 sc1\n\t" \
  "buffer_load_dwordx4 v[108:111], v242, s[16:19], s12 offen offset:1024 sc1\n\t" \
  ".Ltl_w1:\n\t" \
  "s_waitcnt vmcnt(0)\n\t" \
  "v_xor_b32 v72, s36, v97\n\t" \
  "v_bitop3_b32 v72, v72, v99, s36 bitop3:0xf6\n\t" \
  "v_bitop3_b32 v72, v72, v101, s36 bitop3:0xf6\n\t" \
  "v_bitop3_b32 v72, v72, v103, s36 bitop3:0xf6\n\t" \
  "v_bitop3_b32 v72, v72, v105, s36 bitop3:0xf6\n\t" \
  "v_bitop3_b32 v72, v72, v107, s36 bitop3:0xf6\n\t" \
  "v_bitop3_b32 v72, v72, v109, s36 bitop3:0xf6\n\t" \
  "v_bitop3_b32 v72, v72, v111, s36 bitop3:0xf6\n\t" \
  "v_cmp_ne_u32 vcc, 0, v72\n\t" \
  "v_cndmask_b32 v96, v96, v64, s[52:53]\n\t" \
  "v_cndmask_b32 v98, v98, v65, s[52:53]\n\t" \
  "v_cndmask_b32 v100, v100, v66, s[52:53]\n\t" \
  "v_cndmask_b32 v102, v102, v67, s[52:53]\n\t" \
  "v_add_f32 v68, v96, v104\n\t" \
  "v_add_f32 v69, v98, v106\n\t" \
  "v_add_f32 v70, v100, v108\n\t" \
  "v_add_f32 v71, v102, v110\n\t" \
  "s_cbranch_vccz .Ltl_k1\n\t" \
  "s_add_u32 s81, s81, 1\n\t" \
  "s_cmp_lt_u32 s81, " FQ_CAP "\n\t" \
  "s_cbranch_scc1 .Ltl_r1\n\t" \
  "buffer_store_dword v243, off, s[16:19], 0 offset:256 sc1\n\t" \
  ".Ltl_k1:\n\t" \
  "ds_write_b128 v224, v[68:71] offset:4096\n\t" \
  "s_waitcnt lgkmcnt(0)\n\t" \
  "s_barrier\n\t" \
  "ds_read_b128 v[96:99], v242 offset:4096\n\t" \
  "ds_read_b128 v[100:103], v242 offset:5120\n\t" \
  "ds_read_b128 v[104:107], v242 offset:6144\n\t" \
  "ds_read_b128 v[108:111], v242 offset:7168\n\t" \
  "buffer_load_dwordx2 v[248:249], v225, s[16:19], s13 offen sc1\n\t" \
  STP("s_memrealtime s[94:95]\n\t") \
  STP("s_memtime s[92:93]\n\t") \
  STP("s_waitcnt lgkmcnt(0)\n\t") \
  STP("v_writelane_b32 v255, s94, 42\n\t") \
  STP("v_writelane_b32 v255, s92, 43\n\t") \
  "s_waitcnt lgkmcnt(2)\n\t" \
  "v_pk_add_f32 v[96:97], v[96:97], v[100:101]\n\t" \
  "v_pk_add_f32 v[98:99], v[98:99], v[102:103]\n\t" \
  "s_waitcnt lgkmcnt(0)\n\t" \
  "v_pk_add_f32 v[104:105], v[104:105], v[108:109]\n\t" \
  "v_pk_add_f32 v[106:107], v[106:107], v[110:111]\n\t" \
  "v_pk_add_f32 v[64:65], v[96:97], v[104:105]\n\t" \
  "v_pk_add_f32 v[66:67], v[98:99], v[106:107]\n\t" \
  "v_mul_f32 v64, 0x3a800000, v64\n\t" \
  "v_mul_f32 v65, 0x3a800000, v65\n\t" \
  "v_readlane_b32 s42, v67, 0\n\t" \
  "v_readlane_b32 s43, v67, 1\n\t" \
  "v_fma_f32 v65, -v64, v64, v65\n\t" \
  "v_max_f32 v65, 0, v65\n\t" \
  "v_add_f32 v65, 0x3727c5ac, v65\n\t" \
  "v_rsq_f32 v68, v65\n\t" \
  "v_fma_f32 v66, -v64, s42, v66\n\t" \
  "v_fma_f32 v66, v68, v66, s43\n\t" \
  "v_cvt_pk_bf16_f32 v66, v66, v66\n\t" \
  "v_ashrrev_i32 v67, 31, v66\n\t" \
  "v_or_b32 v67, 0x80000000, v67\n\t" \
  "v_xor_b32 v66, v66, v67\n\t" \
  "v_and_b32 v66, 0xffff0000, v66\n\t" \
  "v_or_b32 v66, v66, v227\n\t" \
  "s_nop 1\n\t" \
  "v_max_u32_dpp v66, v66, v66 quad_perm:[1,0,3,2] row_mask:0xf bank_mask:0xf\n\t" \
  "v_mul_f32_e64 v70, v68, -v64\n\t" \
  "v_mov_b32 v65, s36\n\t" \
  "v_max_u32_dpp v66, v66, v66 quad_perm:[2,3,0,1] row_mask:0xf bank_mask:0xf\n\t" \
  "v_mov_b32 v97, s36\n\t" \
  "v_mov_b32 v99, s36\n\t" \
  "v_max_u32_dpp v66, v66, v66 row_half_mirror row_mask:0xf bank_mask:0xf\n\t" \
  "v_mov_b32 v101, s36\n\t" \
  "v_mov_b32 v103, s36\n\t" \
  "v_max_u32_dpp v66, v66, v66 row_mirror row_mask:0xf bank_mask:0xf\n\t" \
  "v_mov_b32 v105, s36\n\t" \
  "v_mov_b32 v107, s36\n\t" \
  "v_readlane_b32 s46, v66, 0\n\t" \
  "v_readlane_b32 s42, v66, 16\n\t" \
  "v_readlane_b32 s43, v66, 32\n\t" \
  "v_readlane_b32 s44, v66, 48\n\t" \
  "s_max_u32 s46, s46, s42\n\t" \
  "s_max_u32 s43, s43, s44\n\t" \
  "s_max_u32 s46, s46, s43\n\t" \
  "v_mov_b32 v64, s46\n\t" \
  "buffer_store_dwordx2 v[64:65], v244, s[16:19], s14 offen sc1\n\t" \
  STP("s_memrealtime s[94:95]\n\t") \
  STP("s_memtime s[92:93]\n\t") \
  STP("s_waitcnt lgkmcnt(0)\n\t") \
  STP("v_writelane_b32 v255, s94, 44\n\t") \
  STP("v_writelane_b32 v255, s92, 45\n\t") \
  "s_not_b32 s48, s46\n\t" \
  "s_and_b32 s48, s48, 63\n\t" \
  "s_and_b32 s44, s48, 15\n\t" \
  "s_lshl_b32 s50, 0x10001, s44\n\t" \
  "s_mov_b32 s51, s50\n\t" \
  "v_readlane_b32 s52, v68, s48\n\t" \
  "v_readlane_b32 s54, v70, s48\n\t" \
  "s_lshr_b32 s48, s48, 4\n\t" \
  "s_lshl_b32 s44, s48, 2\n\t" \
  "v_mov_b32 v109, s36\n\t" \
  "v_mov_b32 v111, s36\n\t" \
  "s_set_gpr_idx_on s44, gpr_idx(SRC0)\n\t" \
  "v_mov_b32 v104, v128\n\t" \
  "v_mov_b32 v106, v129\n\t" \
  "v_mov_b32 v108, v130\n\t" \
  "v_mov_b32 v110, v131\n\t" \
  "s_set_gpr_idx_off\n\t" \
  "v_mov_b32 v66, s54\n\t" \
  "v_lshlrev_b32 v160, 16, v104\n\t" \
  "v_and_b32 v161, 0xffff0000, v104\n\t" \
  "v_lshlrev_b32 v162, 16, v106\n\t" \
  "v_and_b32 v163, 0xffff0000, v106\n\t" \
  "v_lshlrev_b32 v164, 16, v108\n\t" \
  "v_and_b32 v165, 0xffff0000, v108\n\t" \
  "v_lshlrev_b32 v166, 16, v110\n\t" \
  "v_and_b32 v167, 0xffff0000, v110\n\t" \
  "v_fma_f32 v160, v160, s52, v66\n\t" \
  "v_fma_f32 v161, v161, s52, v66\n\t" \
  "v_fma_f32 v162, v162, s52, v66\n\t" \
  "v_fma_f32 v163, v163, s52, v66\n\t" \
  "v_fma_f32 v164, v164, s52, v66\n\t" \
  "v_fma_f32 v165, v165, s52, v66\n\t" \
  "v_fma_f32 v166, v166, s52, v66\n\t" \
  "v_fma_f32 v167, v167, s52, v66\n\t" \
  "v_pk_fma_f32 v[160:161], v[160:161], v[152:153], v[144:145]\n\t" \
  "v_pk_fma_f32 v[162:163], v[162:163], v[154:155], v[146:147]\n\t" \
  "v_pk_fma_f32 v[164:165], v[164:165], v[156:157], v[148:149]\n\t" \
  "v_pk_fma_f32 v[166:167], v[166:167], v[158:159], v[150:151]\n\t" \
  "v_cvt_pk_bf16_f32 v96, v160, v161\n\t" \
  "v_cvt_pk_bf16_f32 v98, v162, v163\n\t" \
  "v_cvt_pk_bf16_f32 v100, v164, v165\n\t" \
  "v_cvt_pk_bf16_f32 v102, v166, v167\n\t" \
  "s_waitcnt vmcnt(1)\n\t" \
  "v_cmp_eq_u32 vcc, s36, v249\n\t" \
  "v_mov_b32 v69, v68\n\t" \
  "v_mov_b32 v71, v70\n\t" \
  "v_cndmask_b32 v64, 0, v248, vcc\n\t" \
  "s_nop 1\n\t" \
  "v_max_u32_dpp v64, v64, v64 quad_perm:[1,0,3,2] row_mask:0xf bank_mask:0xf\n\t" \
  "v_permlane32_swap_b32 v68, v69\n\t" \
  "v_permlane32_swap_b32 v70, v71\n\t" \
  "v_max_u32_dpp v64, v64, v64 quad_perm:[2,3,0,1] row_mask:0xf bank_mask:0xf\n\t" \
  "v_mov_b32 v160, v68\n\t" \
  "v_mov_b32 v161, v69\n\t" \
  "v_max_u32_dpp v64, v64, v64 row_half_mirror row_mask:0xf bank_mask:0xf\n\t" \
  "v_mov_b32 v162, v70\n\t" \
  "v_mov_b32 v163, v71\n\t" \
  "v_max_u32_dpp v64, v64, v64 row_mirror row_mask:0xf bank_mask:0xf\n\t" \
  "s_nop 0\n\t" \
  "v_readlane_b32 s45, v64, 0\n\t" \
  ".Lhs16:\n\ts_cbranch_execz .Lhs16n\n\t" \
  "v_readlane_b32 s42, v64, 16\n\t" \
  "s_max_u32 s45, s45, s42\n\t" \
  "s_cmp_ge_u32 s46, s45\n\t" \
  "s_cselect_b32 s49, 1, 0\n\t" \
  "s_max_u32 s45, s45, s46\n\t" \
  "s_not_b32 s42, s45\n\t" \
  "s_bfe_u32 s47, s42, 0x50006\n\t" \
  "s_lshl_b32 s43, s42, 7\n\t" \
  "s_and_b32 s43, s43, 0x3c000\n\t" \
  "s_lshl_b32 s42, s42, 6\n\t" \
  "s_and_b32 s42, s42, 0x1000\n\t" \
  "s_add_u32 s42, s42, s43\n\t" \
  "s_add_u32 s42, s42, s15\n\t" \
  "buffer_load_dwordx2 v[248:249], v225, s[16:19], s13 offen sc1\n\t" \
  "buffer_load_dwordx4 v[72:75], v226, s[16:19], s42 offen offset:0 sc1\n\t" \
  "buffer_load_dwordx4 v[76:79], v226, s[16:19], s42 offen offset:16 sc1\n\t" \
  "s_cmp_eq_u32 s49, 0\n\t" \
  "s_cbranch_scc1 .Ltl_nc\n\t" \
  "s_xor_b32 s43, s20, 8192\n\t" \
  "s_mov_b64 exec, s[50:51]\n\t" \
  "buffer_store_dwordx4 v[96:99], v228, s[16:19], s20 offen offset:0 sc1\n\t" \
  "buffer_store_dwordx4 v[100:103], v228, s[16:19], s20 offen offset:64 sc1\n\t" \
  "buffer_store_dwordx4 v[96:99], v228, s[16:19], s43 offen offset:0 sc1\n\t" \
  "buffer_store_dwordx4 v[100:103], v228, s[16:19], s43 offen offset:64 sc1\n\t" \
  "buffer_store_dwordx4 v[104:107], v228, s[16:19], s21 offen offset:0 sc1\n\t" \
  "buffer_store_dwordx4 v[108:111], v228, s[16:19], s21 offen offset:64 sc1\n\t" \
  "s_mov_b64 exec, -1\n\t" \
  ".Ltl_nc:\n\t" \
  "v_permlane16_swap_b32 v68, v160\n\t" \
  "v_permlane16_swap_b32 v69, v161\n\t" \
  "v_permlane16_swap_b32 v70, v162\n\t" \
  "v_permlane16_swap_b32 v71, v163\n\t" \
  "v_pk_fma_f32 v[96:97], v[0:1], v[68:69], v[70:71] op_sel:[0,0,0] op_sel_hi:[1,0,0]\n\t" \
  "v_pk_fma_f32 v[98:99], v[2:3], v[68:69], v[70:71] op_sel:[0,0,0] op_sel_hi:[1,0,0]\n\t" \
  "v_pk_fma_f32 v[100:101], v[4:5], v[68:69], v[70:71] op_sel:[0,0,0] op_sel_hi:[1,0,0]\n\t" \
  "v_pk_fma_f32 v[102:103], v[6:7], v[68:69], v[70:71] op_sel:[0,0,0] op_sel_hi:[1,0,0]\n\t" \
  "v_pk_fma_f32 v[96:97], v[96:97], v[152:153], v[144:145]\n\t" \
  "v_pk_fma_f32 v[98:99], v[98:99], v[154:155], v[146:147]\n\t" \
  "v_pk_fma_f32 v[100:101], v[100:101], v[156:157], v[148:149]\n\t" \
  "v_pk_fma_f32 v[102:103], v[102:103], v[158:159], v[150:151]\n\t" \
  "v_cvt_pk_bf16_f32 v97, v96, v97\n\t" \
  "v_cvt_pk_bf16_f32 v99, v98, v99\n\t" \
  "v_cvt_pk_bf16_f32 v101, v100, v101\n\t" \
  "v_cvt_pk_bf16_f32 v103, v102, v103\n\t" \
  "v_lshlrev_b32 v96, 16, v97\n\t" \
  "v_and_b32 v97, 0xffff0000, v97\n\t" \
  "v_lshlrev_b32 v98, 16, v99\n\t" \
  "v_and_b32 v99, 0xffff0000, v99\n\t" \
  "v_lshlrev_b32 v100, 16, v101\n\t" \
  "v_and_b32 v101, 0xffff0000, v101\n\t" \
  "v_lshlrev_b32 v102, 16, v103\n\t" \
  "v_and_b32 v103, 0xffff0000, v103\n\t" \
  "v_pk_fma_f32 v[96:97], v[168:169], v[96:97], v[0:1]\n\t" \
  "v_pk_fma_f32 v[98:99], v[170:171], v[98:99], v[2:3]\n\t" \
  "v_pk_fma_f32 v[100:101], v[172:173], v[100:101], v[4:5]\n\t" \
  "v_pk_fma_f32 v[102:103], v[174:175], v[102:103], v[6:7]\n\t" \
  "v_cvt_pk_bf16_f32 v128, v96, v97\n\t" \
  "v_cvt_pk_bf16_f32 v129, v98, v99\n\t" \
  "v_cvt_pk_bf16_f32 v130, v100, v101\n\t" \
  "v_cvt_pk_bf16_f32 v131, v102, v103\n\t" \
  "v_pk_fma_f32 v[104:105], v[8:9], v[160:161], v[162:163] op_sel:[0,0,0] op_sel_hi:[1,0,0]\n\t" \
  "v_pk_fma_f32 v[106:107], v[10:11], v[160:161], v[162:163] op_sel:[0,0,0] op_sel_hi:[1,0,0]\n\t" \
  "v_pk_fma_f32 v[108:109], v[12:13], v[160:161], v[162:163] op_sel:[0,0,0] op_sel_hi:[1,0,0]\n\t" \
  "v_pk_fma_f32 v[110:111], v[14:15], v[160:161], v[162:163] op_sel:[0,0,0] op_sel_hi:[1,0,0]\n\t" \
  "v_pk_fma_f32 v[104:105], v[104:105], v[152:153], v[144:145]\n\t" \
  "v_pk_fma_f32 v[106:107], v[106:107], v[154:155], v[146:147]\n\t" \
  "v_pk_fma_f32 v[108:109], v[108:109], v[156:157], v[148:149]\n\t" \
  "v_pk_fma_f32 v[110:111], v[110:111], v[158:159], v[150:151]\n\t" \
  "v_cvt_pk_bf16_f32 v105, v104, v105\n\t" \
  "v_cvt_pk_bf16_f32 v107, v106, v107\n\t" \
  "v_cvt_pk_bf16_f32 v109, v108, v109\n\t" \
  "v_cvt_pk_bf16_f32 v111, v110, v111\n\t" \
  "v_lshlrev_b32 v104, 16, v105\n\t" \
  "v_and_b32 v105, 0xffff0000, v105\n\t" \
  "v_lshlrev_b32 v106, 16, v107\n\t" \
  "v_and_b32 v107, 0xffff0000, v107\n\t" \
  "v_lshlrev_b32 v108, 16, v109\n\t" \
  "v_and_b32 v109, 0xffff0000, v109\n\t" \
  "v_lshlrev_b32 v110, 16, v111\n\t" \
  "v_and_b32 v111, 0xffff0000, v111\n\t" \
  "v_pk_fma_f32 v[104:105], v[168:169], v[104:105], v[8:9]\n\t" \
  "v_pk_fma_f32 v[106:107], v[170:171], v[106:107], v[10:11]\n\t" \
  "v_pk_fma_f32 v[108:109], v[172:173], v[108:109], v[12:13]\n\t" \
  "v_pk_fma_f32 v[110:111], v[174:175], v[110:111], v[14:15]\n\t" \
  "v_cvt_pk_bf16_f32 v132, v104, v105\n\t" \
  "v_cvt_pk_bf16_f32 v133, v106, v107\n\t" \
  "v_cvt_pk_bf16_f32 v134, v108, v109\n\t" \
  "v_cvt_pk_bf16_f32 v135, v110, v111\n\t" \
  "v_pk_fma_f32 v[96:97], v[16:17], v[68:69], v[70:71] op_sel:[0,1,1] op_sel_hi:[1,1,1]\n\t" \
  "v_pk_fma_f32 v[98:99], v[18:19], v[68:69], v[70:71] op_sel:[0,1,1] op_sel_hi:[1,1,1]\n\t" \
  "v_pk_fma_f32 v[100:101], v[20:21], v[68:69], v[70:71] op_sel:[0,1,1] op_sel_hi:[1,1,1]\n\t" \
  "v_pk_fma_f32 v[102:103], v[22:23], v[68:69], v[70:71] op_sel:[0,1,1] op_sel_hi:[1,1,1]\n\t" \
  "v_pk_fma_f32 v[96:97], v[96:97], v[152:153], v[144:145]\n\t" \
  "v_pk_fma_f32 v[98:99], v[98:99], v[154:155], v[146:147]\n\t" \
  "v_pk_fma_f32 v[100:101], v[100:101], v[156:157], v[148:149]\n\t" \
  "v_pk_fma_f32 v[102:103], v[102:103], v[158:159], v[150:151]\n\t" \
  "v_cvt_pk_bf16_f32 v97, v96, v97\n\t" \
  "v_cvt_pk_bf16_f32 v99, v98, v99\n\t" \
  "v_cvt_pk_bf16_f32 v101, v100, v101\n\t" \
  "v_cvt_pk_bf16_f32 v103, v102, v103\n\t" \
  "v_lshlrev_b32 v96, 16, v97\n\t" \
  "v_and_b32 v97, 0xffff0000, v97\n\t" \
  "v_lshlrev_b32 v98, 16, v99\n\t" \
  "v_and_b32 v99, 0xffff0000, v99\n\t" \
  "v_lshlrev_b32 v100, 16, v101\n\t" \
  "v_and_b32 v101, 0xffff0000, v101\n\t" \
  "v_lshlrev_b32 v102, 16, v103\n\t" \
  "v_and_b32 v103, 0xffff0000, v103\n\t" \
  "v_pk_fma_f32 v[96:97], v[168:169], v[96:97], v[16:17]\n\t" \
  "v_pk_fma_f32 v[98:99], v[170:171], v[98:99], v[18:19]\n\t" \
  "v_pk_fma_f32 v[100:101], v[172:173], v[100:101], v[20:21]\n\t" \
  "v_pk_fma_f32 v[102:103], v[174:175], v[102:103], v[22:23]\n\t" \
  "v_cvt_pk_bf16_f32 v136, v96, v97\n\t" \
  "v_cvt_pk_bf16_f32 v137, v98, v99\n\t" \
  "v_cvt_pk_bf16_f32 v138, v100, v101\n\t" \
  "v_cvt_pk_bf16_f32 v139, v102, v103\n\t" \
  "v_pk_fma_f32 v[104:105], v[24:25], v[160:161], v[162:163] op_sel:[0,1,1] op_sel_hi:[1,1,1]\n\t" \
  "v_pk_fma_f32 v[106:107], v[26:27], v[160:161], v[162:163] op_sel:[0,1,1] op_sel_hi:[1,1,1]\n\t" \
  "v_pk_fma_f32 v[108:109], v[28:29], v[160:161], v[162:163] op_sel:[0,1,1] op_sel_hi:[1,1,1]\n\t" \
  "v_pk_fma_f32 v[110:111], v[30:31], v[160:161], v[162:163] op_sel:[0,1,1] op_sel_hi:[1,1,1]\n\t" \
  "v_pk_fma_f32 v[104:105], v[104:105], v[152:153], v[144:145]\n\t" \
  "v_pk_fma_f32 v[106:107], v[106:107], v[154:155], v[146:147]\n\t" \
  "v_pk_fma_f32 v[108:109], v[108:109], v[156:157], v[148:149]\n\t" \
  "v_pk_fma_f32 v[110:111], v[110:111], v[158:159], v[150:151]\n\t" \
  "v_cvt_pk_bf16_f32 v105, v104, v105\n\t" \
  "v_cvt_pk_bf16_f32 v107, v106, v107\n\t" \
  "v_cvt_pk_bf16_f32 v109, v108, v109\n\t" \
  "v_cvt_pk_bf16_f32 v111, v110, v111\n\t" \
  "v_lshlrev_b32 v104, 16, v105\n\t" \
  "v_and_b32 v105, 0xffff0000, v105\n\t" \
  "v_lshlrev_b32 v106, 16, v107\n\t" \
  "v_and_b32 v107, 0xffff0000, v107\n\t" \
  "v_lshlrev_b32 v108, 16, v109\n\t" \
  "v_and_b32 v109, 0xffff0000, v109\n\t" \
  "v_lshlrev_b32 v110, 16, v111\n\t" \
  "v_and_b32 v111, 0xffff0000, v111\n\t" \
  "v_pk_fma_f32 v[104:105], v[168:169], v[104:105], v[24:25]\n\t" \
  "v_pk_fma_f32 v[106:107], v[170:171], v[106:107], v[26:27]\n\t" \
  "v_pk_fma_f32 v[108:109], v[172:173], v[108:109], v[28:29]\n\t" \
  "v_pk_fma_f32 v[110:111], v[174:175], v[110:111], v[30:31]\n\t" \
  "v_cvt_pk_bf16_f32 v140, v104, v105\n\t" \
  "v_cvt_pk_bf16_f32 v141, v106, v107\n\t" \
  "v_cvt_pk_bf16_f32 v142, v108, v109\n\t" \
  "v_cvt_pk_bf16_f32 v143, v110, v111\n\t" \
  "v_permlane16_swap_b32 v128, v130\n\t" \
  "v_permlane16_swap_b32 v129, v131\n\t" \
  "v_permlane16_swap_b32 v132, v134\n\t" \
  "v_permlane16_swap_b32 v133, v135\n\t" \
  "v_permlane16_swap_b32 v136, v138\n\t" \
  "v_permlane16_swap_b32 v137, v139\n\t" \
  "v_permlane16_swap_b32 v140, v142\n\t" \
  "v_permlane16_swap_b32 v141, v143\n\t" \
  STP("s_memrealtime s[94:95]\n\t") \
  STP("s_memtime s[92:93]\n\t") \
  STP("s_waitcnt lgkmcnt(0)\n\t") \
  STP("v_writelane_b32 v255, s94, 46\n\t") \
  STP("v_writelane_b32 v255, s92, 47\n\t") \
  "s_mov_b32 s98, 0\n\t" \
  "s_mov_b32 s81, 0\n\t" \
  "s_lshl_b32 s99, 1, s8\n\t" \
  "s_cmp_eq_u32 s49, 0\n\t" \
  "s_cbranch_scc1 .Ltl_w2\n\t" \
  "s_waitcnt vmcnt(6)\n\t" \
  "s_branch .Ltl_t2\n\t" \
  ".Ltl_s2:\n\t" \
  "s_bcnt1_i32_b32 s54, s54\n\t" \
  "s_cmp_ge_u32 s54, 24\n\t" \
  "s_cselect_b32 s54, 1, s98\n\t" \
  "s_cmp_lg_u32 s54, 0\n\t" \
  "s_cbranch_scc1 .Ltl_q2\n\t" \
  "s_mov_b32 s98, 1\n\t" \
  "s_mov_b64 s[96:97], -1\n\t" \
  "s_cmp_eq_u32 s49, 0\n\t" \
  "s_cselect_b64 s[52:53], 0, s[50:51]\n\t" \
  "s_not_b64 s[52:53], s[52:53]\n\t" \
  "s_cmp_eq_u32 s48, 0\n\t" \
  "s_cselect_b64 exec, s[52:53], s[96:97]\n\t" \
  "s_add_u32 s44, s22, 0\n\t" \
  "buffer_store_dwordx4 v[128:131], v231, s[4:7], s44 offen sc0 sc1\n\t" \
  "s_cmp_eq_u32 s48, 1\n\t" \
  "s_cselect_b64 exec, s[52:53], s[96:97]\n\t" \
  "s_add_u32 s44, s22, 32768\n\t" \
  "buffer_store_dwordx4 v[132:135], v231, s[4:7], s44 offen sc0 sc1\n\t" \
  "s_cmp_eq_u32 s48, 2\n\t" \
  "s_cselect_b64 exec, s[52:53], s[96:97]\n\t" \
  "s_add_u32 s44, s22, 65536\n\t" \
  "buffer_store_dwordx4 v[136:139], v231, s[4:7], s44 offen sc0 sc1\n\t" \
  "s_cmp_eq_u32 s48, 3\n\t" \
  "s_cselect_b64 exec, s[52:53], s[96:97]\n\t" \
  "s_add_u32 s44, s22, 98304\n\t" \
  "buffer_store_dwordx4 v[140:143], v231, s[4:7], s44 offen sc0 sc1\n\t" \
  "s_mov_b64 exec, -1\n\t" \
  ".Ltl_q2:\n\t" \
  "s_add_u32 s81, s81, 1\n\t" \
  "s_cmp_lt_u32 s81, " FQ_CAP "\n\t" \
  "s_cbranch_scc0 .Ltl_g2\n\t" \
  "s_sleep 1\n\t" \
  "s_not_b32 s42, s45\n\t" \
  "s_bfe_u32 s47, s42, 0x50006\n\t" \
  "s_lshl_b32 s43, s42, 7\n\t" \
  "s_and_b32 s43, s43, 0x3c000\n\t" \
  "s_lshl_b32 s42, s42, 6\n\t" \
  "s_and_b32 s42, s42, 0x1000\n\t" \
  "s_add_u32 s42, s42, s43\n\t" \
  "s_add_u32 s42, s42, s15\n\t" \
  "buffer_load_dwordx2 v[248:249], v225, s[16:19], s13 offen sc1\n\t" \
  "buffer_load_dwordx4 v[72:75], v226, s[16:19], s42 offen offset:0 sc1\n\t" \
  "buffer_load_dwordx4 v[76:79], v226, s[16:19], s42 offen offset:16 sc1\n\t" \
  ".Ltl_w2:\n\t" \
  "s_waitcnt vmcnt(0)\n\t" \
  ".Ltl_t2:\n\t" \
  "v_cmp_eq_u32 vcc, s36, v249\n\t" \
  "v_xor_b32 v65, s36, v73\n\t" \
  "v_bitop3_b32 v65, v65, v75, s36 bitop3:0xf6\n\t" \
  "v_cndmask_b32 v64, 0, v248, vcc\n\t" \
  "s_or_b32 s54, vcc_lo, s99\n\t" \
  "s_nop 0\n\t" \
  "v_max_u32_dpp v64, v64, v64 quad_perm:[1,0,3,2] row_mask:0xf bank_mask:0xf\n\t" \
  "v_bitop3_b32 v65, v65, v77, s36 bitop3:0xf6\n\t" \
  "v_bitop3_b32 v65, v65, v79, s36 bitop3:0xf6\n\t" \
  "v_max_u32_dpp v64, v64, v64 quad_perm:[2,3,0,1] row_mask:0xf bank_mask:0xf\n\t" \
  "v_cmp_ne_u32 vcc, 0, v65\n\t" \
  "s_nop 0\n\t" \
  "v_max_u32_dpp v64, v64, v64 row_half_mirror row_mask:0xf bank_mask:0xf\n\t" \
  "s_nop 1\n\t" \
  "v_max_u32_dpp v64, v64, v64 row_mirror row_mask:0xf bank_mask:0xf\n\t" \
  "s_nop 0\n\t" \
  "v_readlane_b32 s45, v64, 0\n\t" \
  "v_readlane_b32 s42, v64, 16\n\t" \
  "s_max_u32 s45, s45, s42\n\t" \
  "s_max_u32 s45, s45, s46\n\t" \
  "s_not_b32 s42, s45\n\t" \
  "s_bfe_u32 s42, s42, 0x50006\n\t" \
  "s_xor_b32 s42, s42, s47\n\t" \
  "s_orn2_b32 s42, s42, s54\n\t" \
  "s_or_b32 s42, s42, vcc_lo\n\t" \
  "s_or_b32 s42, s42, vcc_hi\n\t" \
  "s_cbranch_scc1 .Ltl_s2\n\t" \
  ".Ltl_k2:\n\t" \
  STP("s_memrealtime s[94:95]\n\t") \
  STP("s_memtime s[92:93]\n\t") \
  STP("s_waitcnt lgkmcnt(0)\n\t") \
  STP("v_writelane_b32 v255, s94, 48\n\t") \
  STP("v_writelane_b32 v255, s92, 49\n\t") \
  "s_cmp_lt_i32 s57, 0\n\t" \
  "s_cbranch_scc1 .Ltl_nf\n\t" \
  "v_dot2_f32_bf16 v96, v80, v72, 0\n\t" \
  "v_dot2_f32_bf16 v97, v84, v72, 0\n\t" \
  "v_dot2_f32_bf16 v98, v81, v72, 0\n\t" \
  "v_dot2_f32_bf16 v99, v85, v72, 0\n\t" \
  "v_dot2_f32_bf16 v100, v82, v72, 0\n\t" \
  "v_dot2_f32_bf16 v101, v86, v72, 0\n\t" \
  "v_dot2_f32_bf16 v102, v83, v72, 0\n\t" \
  "v_dot2_f32_bf16 v103, v87, v72, 0\n\t" \
  "v_dot2_f32_bf16 v104, v176, v72, 0\n\t" \
  "v_dot2_f32_bf16 v105, v180, v72, 0\n\t" \
  "v_dot2_f32_bf16 v106, v177, v72, 0\n\t" \
  "v_dot2_f32_bf16 v107, v181, v72, 0\n\t" \
  "v_dot2_f32_bf16 v108, v178, v72, 0\n\t" \
  "v_dot2_f32_bf16 v109, v182, v72, 0\n\t" \
  "v_dot2_f32_bf16 v110, v179, v72, 0\n\t" \
  "v_dot2_f32_bf16 v111, v183, v72, 0\n\t" \
  "v_dot2_f32_bf16 v96, v88, v74, v96\n\t" \
  "v_dot2_f32_bf16 v97, v92, v74, v97\n\t" \
  "v_dot2_f32_bf16 v98, v89, v74, v98\n\t" \
  "v_dot2_f32_bf16 v99, v93, v74, v99\n\t" \
  "v_dot2_f32_bf16 v100, v90, v74, v100\n\t" \
  "v_dot2_f32_bf16 v101, v94, v74, v101\n\t" \
  "v_dot2_f32_bf16 v102, v91, v74, v102\n\t" \
  "v_dot2_f32_bf16 v103, v95, v74, v103\n\t" \
  "v_dot2_f32_bf16 v104, v184, v74, v104\n\t" \
  "v_dot2_f32_bf16 v105, v188, v74, v105\n\t" \
  "v_dot2_f32_bf16 v106, v185, v74, v106\n\t" \
  "v_dot2_f32_bf16 v107, v189, v74, v107\n\t" \
  "v_dot2_f32_bf16 v108, v186, v74, v108\n\t" \
  "v_dot2_f32_bf16 v109, v190, v74, v109\n\t" \
  "v_dot2_f32_bf16 v110, v187, v74, v110\n\t" \
  "v_dot2_f32_bf16 v111, v191, v74, v111\n\t" \
  "v_dot2_f32_bf16 v96, v208, v76, v96\n\t" \
  "v_dot2_f32_bf16 v97, v212, v76, v97\n\t" \
  "v_dot2_f32_bf16 v98, v209, v76, v98\n\t" \
  "v_dot2_f32_bf16 v99, v213, v76, v99\n\t" \
  "v_dot2_f32_bf16 v100, v210, v76, v100\n\t" \
  "v_dot2_f32_bf16 v101, v214, v76, v101\n\t" \
  "v_dot2_f32_bf16 v102, v211, v76, v102\n\t" \
  "v_dot2_f32_bf16 v103, v215, v76, v103\n\t" \
  "v_dot2_f32_bf16 v104, v192, v76, v104\n\t" \
  "v_dot2_f32_bf16 v105, v196, v76, v105\n\t" \
  "v_dot2_f32_bf16 v106, v193, v76, v106\n\t" \
  "v_dot2_f32_bf16 v107, v197, v76, v107\n\t" \
  "v_dot2_f32_bf16 v108, v194, v76, v108\n\t" \
  "v_dot2_f32_bf16 v109, v198, v76, v109\n\t" \
  "v_dot2_f32_bf16 v110, v195, v76, v110\n\t" \
  "v_dot2_f32_bf16 v111, v199, v76, v111\n\t" \
  "v_dot2_f32_bf16 v96, v216, v78, v96\n\t" \
  "v_dot2_f32_bf16 v97, v220, v78, v97\n\t" \
  "v_dot2_f32_bf16 v98, v217, v78, v98\n\t" \
  "v_dot2_f32_bf16 v99, v221, v78, v99\n\t" \
  "v_dot2_f32_bf16 v100, v218, v78, v100\n\t" \
  "v_dot2_f32_bf16 v101, v222, v78, v101\n\t" \
  "v_dot2_f32_bf16 v102, v219, v78, v102\n\t" \
  "v_dot2_f32_bf16 v103, v223, v78, v103\n\t" \
  "v_dot2_f32_bf16 v104, v200, v78, v104\n\t" \
  "v_dot2_f32_bf16 v105, v204, v78, v105\n\t" \
  "v_dot2_f32_bf16 v106, v201, v78, v106\n\t" \
  "v_dot2_f32_bf16 v107, v205, v78, v107\n\t" \
  "v_dot2_f32_bf16 v108, v202, v78, v108\n\t" \
  "v_dot2_f32_bf16 v109, v206, v78, v109\n\t" \
  "v_dot2_f32_bf16 v110, v203, v78, v110\n\t" \
  "v_dot2_f32_bf16 v111, v207, v78, v111\n\t" \
  "v_permlane32_swap_b32 v96, v104\n\t" \
  "v_permlane32_swap_b32 v97, v105\n\t" \
  "v_permlane32_swap_b32 v98, v106\n\t" \
  "v_permlane32_swap_b32 v99, v107\n\t" \
  "v_permlane32_swap_b32 v100, v108\n\t" \
  "v_permlane32_swap_b32 v101, v109\n\t" \
  "v_permlane32_swap_b32 v102, v110\n\t" \
  "v_permlane32_swap_b32 v103, v111\n\t" \
  "v_add_f32 v96, v96, v104\n\t" \
  "v_add_f32 v97, v97, v105\n\t" \
  "v_add_f32 v98, v98, v106\n\t" \
  "v_add_f32 v99, v99, v107\n\t" \
  "v_add_f32 v100, v100, v108\n\t" \
  "v_add_f32 v101, v101, v109\n\t" \
  "v_add_f32 v102, v102, v110\n\t" \
  "v_add_f32 v103, v103, v111\n\t" \
  "v_permlane16_swap_b32 v96, v100\n\t" \
  "v_permlane16_swap_b32 v97, v101\n\t" \
  "v_permlane16_swap_b32 v98, v102\n\t" \
  "v_permlane16_swap_b32 v99, v103\n\t" \
  "v_add_f32 v96, v96, v100\n\t" \
  "v_add_f32 v97, v97, v101\n\t" \
  "v_add_f32 v98, v98, v102\n\t" \
  "v_add_f32 v99, v99, v103\n\t" \
  "v_add_f32_dpp v96, v96, v96 row_ror:8 row_mask:0xf bank_mask:0xf\n\t" \
  "v_add_f32_dpp v97, v97, v97 row_ror:8 row_mask:0xf bank_mask:0xf\n\t" \
  "v_add_f32_dpp v98, v98, v98 row_ror:8 row_mask:0xf bank_mask:0xf\n\t" \
  "v_add_f32_dpp v99, v99, v99 row_ror:8 row_mask:0xf bank_mask:0xf\n\t" \
  "v_add_f32_dpp v96, v96, v96 row_ror:4 row_mask:0xf bank_mask:0xf\n\t" \
  "v_add_f32_dpp v97, v97, v97 row_ror:4 row_mask:0xf bank_mask:0xf\n\t" \
  "v_add_f32_dpp v98, v98, v98 row_ror:4 row_mask:0xf bank_mask:0xf\n\t" \
  "v_add_f32_dpp v99, v99, v99 row_ror:4 row_mask:0xf bank_mask:0xf\n\t" \
  "v_add_f32_dpp v96, v96, v96 row_ror:2 row_mask:0xf bank_mask:0xf\n\t" \
  "v_add_f32_dpp v97, v97, v97 row_ror:2 row_mask:0xf bank_mask:0xf\n\t" \
  "v_add_f32_dpp v98, v98, v98 row_ror:2 row_mask:0xf bank_mask:0xf\n\t" \
  "v_add_f32_dpp v99, v99, v99 row_ror:2 row_mask:0xf bank_mask:0xf\n\t" \
  "s_mov_b32 exec_lo, 0x00030003\n\t" \
  "s_mov_b32 exec_hi, 0x00030003\n\t" \
  "ds_write_b128 v253, v[96:99] offset:8192\n\t" \
  "s_mov_b64 exec, -1\n\t" \
  "v_mov_b32 v101, s36\n\t" \
  "v_mov_b32 v103, s36\n\t" \
  "s_waitcnt lgkmcnt(0)\n\t" \
  "s_barrier\n\t" \
  "v_add_u32 v104, 8192, v245\n\t" \
  "ds_read2_b32 v[96:97], v104 offset1:64\n\t" \
  "ds_read2_b32 v[98:99], v104 offset0:32 offset1:96\n\t" \
  "s_waitcnt lgkmcnt(0)\n\t" \
  "v_pk_add_f32 v[96:97], v[96:97], v[98:99]\n\t" \
  "v_add_f32 v96, v96, v97\n\t" \
  "v_cvt_pk_bf16_f32 v96, v96, v96\n\t" \
  "v_lshlrev_b32 v96, 16, v96\n\t" \
  "v_mov_b32 v97, v96\n\t" \
  "s_nop 1\n\t" \
  "v_permlane16_swap_b32 v96, v97\n\t" \
  "v_mul_f32 v98, 0xbfb8aa3b, v96\n\t" \
  "v_exp_f32 v98, v98\n\t" \
  "s_nop 0\n\t" \
  "v_add_f32 v98, 1.0, v98\n\t" \
  "v_rcp_f32 v98, v98\n\t" \
  "s_nop 0\n\t" \
  "v_mul_f32 v96, v98, v96\n\t" \
  "v_cvt_pk_bf16_f32 v96, v96, v96\n\t" \
  "v_lshlrev_b32 v96, 16, v96\n\t" \
  "v_mul_f32 v96, v97, v96\n\t" \
  "s_nop 1\n\t" \
  "v_mov_b32_dpp v97, v96 quad_perm:[1,0,3,2] row_mask:0xf bank_mask:0xf\n\t" \
  "v_cvt_pk_bf16_f32 v96, v96, v97\n\t" \
  "s_nop 0\n\t" \
  "v_readlane_b32 s60, v96, 0\n\t" \
  "v_readlane_b32 s61, v96, 2\n\t" \
  "v_readlane_b32 s62, v96, 4\n\t" \
  "v_readlane_b32 s63, v96, 6\n\t" \
  "v_readlane_b32 s64, v96, 8\n\t" \
  "v_readlane_b32 s65, v96, 10\n\t" \
  "v_readlane_b32 s66, v96, 12\n\t" \
  "v_readlane_b32 s67, v96, 14\n\t" \
  "v_dot2_f32_bf16 v104, v112, s60, 0\n\t" \
  "v_dot2_f32_bf16 v105, v34, s60, 0\n\t" \
  "v_dot2_f32_bf16 v106, v113, s60, 0\n\t" \
  "v_dot2_f32_bf16 v107, v35, s60, 0\n\t" \
  "v_dot2_f32_bf16 v104, v114, s61, v104\n\t" \
  "v_dot2_f32_bf16 v105, v38, s61, v105\n\t" \
  "v_dot2_f32_bf16 v106, v115, s61, v106\n\t" \
  "v_dot2_f32_bf16 v107, v39, s61, v107\n\t" \
  "v_dot2_f32_bf16 v104, v116, s62, v104\n\t" \
  "v_dot2_f32_bf16 v105, v42, s62, v105\n\t" \
  "v_dot2_f32_bf16 v106, v117, s62, v106\n\t" \
  "v_dot2_f32_bf16 v107, v43, s62, v107\n\t" \
  "v_dot2_f32_bf16 v104, v118, s63, v104\n\t" \
  "v_dot2_f32_bf16 v105, v46, s63, v105\n\t" \
  "v_dot2_f32_bf16 v106, v119, s63, v106\n\t" \
  "v_dot2_f32_bf16 v107, v47, s63, v107\n\t" \
  "v_dot2_f32_bf16 v104, v120, s64, v104\n\t" \
  "v_dot2_f32_bf16 v105, v50, s64, v105\n\t" \
  "v_dot2_f32_bf16 v106, v121, s64, v106\n\t" \
  "v_dot2_f32_bf16 v107, v51, s64, v107\n\t" \
  "v_dot2_f32_bf16 v104, v122, s65, v104\n\t" \
  "v_dot2_f32_bf16 v105, v54, s65, v105\n\t" \
  "v_dot2_f32_bf16 v106, v123, s65, v106\n\t" \
  "v_dot2_f32_bf16 v107, v55, s65, v107\n\t" \
  "v_dot2_f32_bf16 v104, v124, s66, v104\n\t" \
  "v_dot2_f32_bf16 v105, v58, s66, v105\n\t" \
  "v_dot2_f32_bf16 v106, v125, s66, v106\n\t" \
  "v_dot2_f32_bf16 v107, v59, s66, v107\n\t" \
  "v_dot2_f32_bf16 v104, v126, s67, v104\n\t" \
  "v_dot2_f32_bf16 v105, v62, s67, v105\n\t" \
  "v_dot2_f32_bf16 v106, v127, s67, v106\n\t" \
  "v_dot2_f32_bf16 v107, v63, s67, v107\n\t" \
  "s_nop 0\n\t" \
  "v_cvt_pk_bf16_f32 v100, v104, v105\n\t" \
  "s_nop 0\n\t" \
  "v_cvt_pk_bf16_f32 v102, v106, v107\n\t" \
  "buffer_store_dwordx4 v[100:103], v242, s[16:19], s23 offen sc1\n\t" \
  ".Ltl_nf:\n\t" \
  STP("s_memrealtime s[94:95]\n\t") \
  STP("s_memtime s[92:93]\n\t") \
  STP("s_waitcnt lgkmcnt(0)\n\t") \
  STP("v_writelane_b32 v255, s94, 50\n\t") \
  STP("v_writelane_b32 v255, s92, 51\n\t") \
  "s_cmp_eq_u32 s47, s8\n\t" \
  "s_cselect_b64 s[52:53], s[50:51], 0\n\t" \
  "s_cselect_b32 s54, 0, s49\n\t" \
  "s_not_b64 s[52:53], s[52:53]\n\t" \
  "s_mov_b64 s[96:97], -1\n\t" \
  "s_cmp_eq_u32 s98, 0\n\t" \
  "s_cbranch_scc1 .Ltl_so\n\t" \
  "s_cmp_eq_u32 s54, 0\n\t" \
  "s_cbranch_scc1 .Ltl_ns\n\t" \
  "s_mov_b64 s[52:53], s[50:51]\n\t" \
  "s_mov_b64 s[96:97], 0\n\t" \
  ".Ltl_so:\n\t" \
  "s_cmp_eq_u32 s48, 0\n\t" \
  "s_cselect_b64 exec, s[52:53], s[96:97]\n\t" \
  "s_add_u32 s44, s22, 0\n\t" \
  "buffer_store_dwordx4 v[128:131], v231, s[4:7], s44 offen sc0 sc1\n\t" \
  "s_cmp_eq_u32 s48, 1\n\t" \
  "s_cselect_b64 exec, s[52:53], s[96:97]\n\t" \
  "s_add_u32 s44, s22, 32768\n\t" \
  "buffer_store_dwordx4 v[132:135], v231, s[4:7], s44 offen sc0 sc1\n\t" \
  "s_cmp_eq_u32 s48, 2\n\t" \
  "s_cselect_b64 exec, s[52:53], s[96:97]\n\t" \
  "s_add_u32 s44, s22, 65536\n\t" \
  "buffer_store_dwordx4 v[136:139], v231, s[4:7], s44 offen sc0 sc1\n\t" \
  "s_cmp_eq_u32 s48, 3\n\t" \
  "s_cselect_b64 exec, s[52:53], s[96:97]\n\t" \
  "s_add_u32 s44, s22, 98304\n\t" \
  "buffer_store_dwordx4 v[140:143], v231, s[4:7], s44 offen sc0 sc1\n\t" \
  "s_mov_b64 exec, -1\n\t" \
  ".Ltl_ns:\n\t" \
  "s_or_b32 s42, s2, s3\n\t" \
  "s_cbranch_scc1 .Ltl_ne\n\t" \
  "s_mov_b32 s42, 2490368\n\t" \
  "buffer_store_dword v243, v244, s[16:19], s42 offen sc1\n\t" \
  ".Ltl_ne:\n\t" \
  "s_cmp_lt_i32 s57, 0\n\t" \
  "s_cbranch_scc0 .Ltl_end\n\t" \
  "s_and_b32 s44, s2, 3\n\t" \
  "v_lshrrev_b32 v4, 4, v224\n\t" \
  "v_lshrrev_b32 v0, 5, v4\n\t" \
  "v_lshlrev_b32 v0, 14, v0\n\t" \
  "v_bfe_u32 v5, v4, 3, 2\n\t" \
  "v_lshl_add_u32 v0, v5, 11, v0\n\t" \
  "v_and_b32 v5, 0x70, v242\n\t" \
  "v_add_u32 v0, v0, v5\n\t" \
  "v_and_b32 v1, 15, v233\n\t" \
  "v_lshlrev_b32 v1, 3, v1\n\t" \
  "s_lshl_b32 s42, s44, 12\n\t" \
  "s_add_u32 s42, s42, 150400\n\t" \
  "v_add_u32 v6, s42, v1\n\t" \
  "ds_read_b64 v[6:7], v6\n\t" \
  "v_lshrrev_b32 v3, 2, v228\n\t" \
  "v_add_u32 v3, v3, v5\n\t" \
  "v_lshl_add_u32 v3, s3, 7, v3\n\t" \
  "s_and_b32 s42, s2, 0x38\n\t" \
  "s_lshl_b32 s42, s42, 6\n\t" \
  "s_lshl_b32 s44, s44, 7\n\t" \
  "s_add_u32 s44, s44, s42\n\t" \
  "s_and_b32 s60, s44, 0x7ff\n\t" \
  "s_add_u32 s61, s60, s9\n\t" \
  "s_add_u32 s62, s61, 1048576\n\t" \
  "s_add_u32 s63, s62, 131072\n\t" \
  "s_add_u32 s64, s62, 262144\n\t" \
  "s_add_u32 s65, s62, 393216\n\t" \
  "s_add_u32 s66, s62, 524288\n\t" \
  "s_add_u32 s67, s62, 655360\n\t" \
  "s_add_u32 s68, s62, 786432\n\t" \
  "s_not_b32 s42, s45\n\t" \
  "s_bfe_u32 s47, s42, 0x50006\n\t" \
  "s_lshl_b32 s43, s42, 7\n\t" \
  "s_and_b32 s43, s43, 0x3c000\n\t" \
  "s_lshl_b32 s42, s42, 6\n\t" \
  "s_and_b32 s42, s42, 0x1000\n\t" \
  "s_add_u32 s42, s42, s43\n\t" \
  "s_add_u32 s42, s42, s15\n\t" \
  "s_add_u32 s69, s42, s44\n\t" \
  "s_lshl_b32 s42, s47, 12\n\t" \
  "s_and_b32 s42, s42, 0x1c000\n\t" \
  "s_add_u32 s70, s61, s42\n\t" \
  "s_and_b32 s42, s47, 3\n\t" \
  "s_lshl_b32 s42, s42, 11\n\t" \
  "s_add_u32 s70, s70, s42\n\t" \
  "s_add_u32 s70, s70, 1966080\n\t" \
  "s_not_b32 s42, s45\n\t" \
  "s_and_b32 s42, s42, 0x7ff\n\t" \
  "s_lshl_b32 s42, s42, 11\n\t" \
  "s_lshr_b32 s71, s44, 1\n\t" \
  "s_add_u32 s71, s71, s42\n\t" \
  "s_ashr_i32 s42, s45, 31\n\t" \
  "s_orn2_b32 s42, 0x80000000, s42\n\t" \
  "s_xor_b32 s42, s42, s45\n\t" \
  "s_and_b32 s42, s42, 0xffff0000\n\t" \
  "v_mov_b32 v2, s42\n\t" \
  "v_mul_f32 v2, 0xbfb8aa3b, v2\n\t" \
  "v_exp_f32 v2, v2\n\t" \
  "s_nop 0\n\t" \
  "v_add_f32 v2, 1.0, v2\n\t" \
  "v_rcp_f32 v2, v2\n\t" \
  "s_nop 0\n\t" \
  "v_cvt_pk_bf16_f32 v2, v2, v2\n\t" \
  "v_lshlrev_b32 v2, 16, v2\n\t" \
  "s_mov_b32 s81, 0\n\t" \
  "s_sleep 8\n\t" \
  ".Ltl_r3:\n\t" \
  "s_sleep 1\n\t" \
  "buffer_load_dwordx4 v[12:15], v0, s[16:19], s62 offen sc1\n\t" \
  "buffer_load_dwordx4 v[16:19], v0, s[16:19], s63 offen sc1\n\t" \
  "buffer_load_dwordx4 v[20:23], v0, s[16:19], s64 offen sc1\n\t" \
  "buffer_load_dwordx4 v[24:27], v0, s[16:19], s65 offen sc1\n\t" \
  "buffer_load_dwordx4 v[28:31], v0, s[16:19], s66 offen sc1\n\t" \
  "buffer_load_dwordx4 v[32:35], v0, s[16:19], s67 offen sc1\n\t" \
  "buffer_load_dwordx4 v[36:39], v0, s[16:19], s68 offen sc1\n\t" \
  "buffer_load_dwordx2 v[8:9], v1, s[16:19], s69 offen sc1\n\t" \
  "buffer_load_dwordx2 v[10:11], v1, s[16:19], s70 offen sc1\n\t" \
  "s_waitcnt vmcnt(0) lgkmcnt(0)\n\t" \
  "v_xor_b32 v4, s36, v9\n\t" \
  "v_bitop3_b32 v4, v4, v11, s36 bitop3:0xf6\n\t" \
  "v_bitop3_b32 v4, v4, v13, s36 bitop3:0xf6\n\t" \
  "v_bitop3_b32 v4, v4, v15, s36 bitop3:0xf6\n\t" \
  "v_bitop3_b32 v4, v4, v17, s36 bitop3:0xf6\n\t" \
  "v_bitop3_b32 v4, v4, v19, s36 bitop3:0xf6\n\t" \
  "v_bitop3_b32 v4, v4, v21, s36 bitop3:0xf6\n\t" \
  "v_bitop3_b32 v4, v4, v23, s36 bitop3:0xf6\n\t" \
  "v_bitop3_b32 v4, v4, v25, s36 bitop3:0xf6\n\t" \
  "v_bitop3_b32 v4, v4, v27, s36 bitop3:0xf6\n\t" \
  "v_bitop3_b32 v4, v4, v29, s36 bitop3:0xf6\n\t" \
  "v_bitop3_b32 v4, v4, v31, s36 bitop3:0xf6\n\t" \
  "v_bitop3_b32 v4, v4, v33, s36 bitop3:0xf6\n\t" \
  "v_bitop3_b32 v4, v4, v35, s36 bitop3:0xf6\n\t" \
  "v_bitop3_b32 v4, v4, v37, s36 bitop3:0xf6\n\t" \
  "v_bitop3_b32 v4, v4, v39, s36 bitop3:0xf6\n\t" \
  "v_cmp_ne_u32 vcc, 0, v4\n\t" \
  "v_lshlrev_b32 v40, 16, v12\n\t" \
  "v_and_b32 v41, 0xffff0000, v12\n\t" \
  "v_lshlrev_b32 v42, 16, v14\n\t" \
  "v_and_b32 v43, 0xffff0000, v14\n\t" \
  "v_lshlrev_b32 v44, 16, v16\n\t" \
  "v_and_b32 v45, 0xffff0000, v16\n\t" \
  "v_lshlrev_b32 v46, 16, v18\n\t" \
  "v_and_b32 v47, 0xffff0000, v18\n\t" \
  "s_cbranch_vccz .Ltl_k3\n\t" \
  "s_add_u32 s81, s81, 1\n\t" \
  "s_cmp_lt_u32 s81, " FQ_CAP "\n\t" \
  "s_cbranch_scc1 .Ltl_r3\n\t" \
  "buffer_store_dword v243, off, s[16:19], 0 offset:256 sc1\n\t" \
  ".Ltl_k3:\n\t" \
  "v_pk_add_f32 v[40:41], v[40:41], v[44:45]\n\t" \
  "v_pk_add_f32 v[42:43], v[42:43], v[46:47]\n\t" \
  "v_lshlrev_b32 v44, 16, v20\n\t" \
  "v_and_b32 v45, 0xffff0000, v20\n\t" \
  "v_lshlrev_b32 v46, 16, v22\n\t" \
  "v_and_b32 v47, 0xffff0000, v22\n\t" \
  "v_pk_add_f32 v[40:41], v[40:41], v[44:45]\n\t" \
  "v_pk_add_f32 v[42:43], v[42:43], v[46:47]\n\t" \
  "v_lshlrev_b32 v44, 16, v24\n\t" \
  "v_and_b32 v45, 0xffff0000, v24\n\t" \
  "v_lshlrev_b32 v46, 16, v26\n\t" \
  "v_and_b32 v47, 0xffff0000, v26\n\t" \
  "v_pk_add_f32 v[40:41], v[40:41], v[44:45]\n\t" \
  "v_pk_add_f32 v[42:43], v[42:43], v[46:47]\n\t" \
  "v_lshlrev_b32 v44, 16, v28\n\t" \
  "v_and_b32 v45, 0xffff0000, v28\n\t" \
  "v_lshlrev_b32 v46, 16, v30\n\t" \
  "v_and_b32 v47, 0xffff0000, v30\n\t" \
  "v_pk_add_f32 v[40:41], v[40:41], v[44:45]\n\t" \
  "v_pk_add_f32 v[42:43], v[42:43], v[46:47]\n\t" \
  "v_lshlrev_b32 v44, 16, v32\n\t" \
  "v_and_b32 v45, 0xffff0000, v32\n\t" \
  "v_lshlrev_b32 v46, 16, v34\n\t" \
  "v_and_b32 v47, 0xffff0000, v34\n\t" \
  "v_pk_add_f32 v[40:41], v[40:41], v[44:45]\n\t" \
  "v_pk_add_f32 v[42:43], v[42:43], v[46:47]\n\t" \
  "v_lshlrev_b32 v44, 16, v36\n\t" \
  "v_and_b32 v45, 0xffff0000, v36\n\t" \
  "v_lshlrev_b32 v46, 16, v38\n\t" \
  "v_and_b32 v47, 0xffff0000, v38\n\t" \
  "v_pk_add_f32 v[40:41], v[40:41], v[44:45]\n\t" \
  "v_pk_add_f32 v[42:43], v[42:43], v[46:47]\n\t" \
  "s_nop 1\n\t" \
  "v_permlane32_swap_b32 v40, v42\n\t" \
  "v_permlane32_swap_b32 v41, v43\n\t" \
  "v_add_f32 v40, v40, v42\n\t" \
  "v_add_f32 v41, v41, v43\n\t" \
  "s_nop 1\n\t" \
  "v_permlane16_swap_b32 v40, v41\n\t" \
  "v_add_f32 v40, v40, v41\n\t" \
  "s_nop 1\n\t" \
  "v_add_f32_dpp v40, v40, v40 row_ror:8 row_mask:0xf bank_mask:0xf\n\t" \
  "ds_write_b32 v3, v40 offset:12288\n\t" \
  "v_lshlrev_b32 v44, 16, v8\n\t" \
  "v_and_b32 v45, 0xffff0000, v8\n\t" \
  "v_lshlrev_b32 v46, 16, v10\n\t" \
  "v_and_b32 v47, 0xffff0000, v10\n\t" \
  "s_waitcnt lgkmcnt(0)\n\t" \
  "s_barrier\n\t" \
  "s_cmp_lg_u32 s3, 0\n\t" \
  "s_cbranch_scc1 .Ltl_end\n\t" \
  "v_add_u32 v4, 12288, v1\n\t" \
  "ds_read2_b64 v[12:15], v4 offset1:16\n\t" \
  "ds_read2_b64 v[16:19], v4 offset0:32 offset1:48\n\t" \
  "v_lshrrev_b32 v1, 1, v1\n\t" \
  "s_waitcnt lgkmcnt(0)\n\t" \
  "v_pk_add_f32 v[12:13], v[12:13], v[14:15]\n\t" \
  "v_pk_add_f32 v[16:17], v[16:17], v[18:19]\n\t" \
  "v_pk_add_f32 v[12:13], v[12:13], v[16:17]\n\t" \
  "v_cvt_pk_bf16_f32 v13, v12, v13\n\t" \
  "v_lshlrev_b32 v12, 16, v13\n\t" \
  "v_and_b32 v13, 0xffff0000, v13\n\t" \
  "v_mul_f32 v12, v12, v2\n\t" \
  "v_mul_f32 v13, v13, v2\n\t" \
  "v_cvt_pk_bf16_f32 v13, v12, v13\n\t" \
  "v_lshlrev_b32 v12, 16, v13\n\t" \
  "v_and_b32 v13, 0xffff0000, v13\n\t" \
  "v_pk_add_f32 v[12:13], v[12:13], v[44:45]\n\t" \
  "v_cvt_pk_bf16_f32 v13, v12, v13\n\t" \
  "v_lshlrev_b32 v12, 16, v13\n\t" \
  "v_and_b32 v13, 0xffff0000, v13\n\t" \
  "v_pk_mul_f32 v[12:13], v[12:13], v[6:7]\n\t" \
  "v_cvt_pk_bf16_f32 v13, v12, v13\n\t" \
  "v_lshlrev_b32 v12, 16, v13\n\t" \
  "v_and_b32 v13, 0xffff0000, v13\n\t" \
  "v_pk_add_f32 v[12:13], v[12:13], v[46:47]\n\t" \
  "v_cvt_pk_bf16_f32 v12, v12, v13\n\t" \
  "s_mov_b64 exec, 0xffff\n\t" \
  "buffer_store_dword v12, v1, s[4:7], s71 offen sc0 sc1\n\t" \
  "s_mov_b64 exec, -1\n\t" \
  ".Ltl_end:\n\t" \
  STP("s_memrealtime s[94:95]\n\t") \
  STP("s_memtime s[92:93]\n\t") \
  STP("s_waitcnt lgkmcnt(0)\n\t") \
  STP("v_writelane_b32 v255, s94, 52\n\t") \
  STP("v_writelane_b32 v255, s92, 53\n\t") \
  ".subsection 1\n\t" \
  ".Lat_x:\n\t" \
  "v_lshlrev_b32 v209, 12, v233\n\t" \
  "v_add_u32 v209, 98304, v209\n\t" \
  "s_mov_b64 exec, 15\n\t" \
  "ds_write_b32 v209, v243\n\t" \
  "s_mov_b64 exec, -1\n\t" \
  "s_waitcnt lgkmcnt(0)\n\t" \
  "s_branch .Lat_y\n\t" \
  ".Lat_rd:\n\t" \
  "s_waitcnt vmcnt(0) lgkmcnt(0)\n\t" \
  "s_barrier\n\t" \
  "s_nop 7\n\t" \
  "v_lshl_add_u32 v0, s3, 6, v233\n\t" \
  "s_mov_b32 s37, 0\n\t" \
  "s_branch .Lfr_again\n\t" \
  ".Lat_s0:\n\t" \
  "s_mov_b32 s81, 0\n\t" \
  ".Lat_r0:\n\t" \
  "s_sleep 4\n\t" \
  "buffer_load_dword v246, v245, s[16:19], s65 offen sc1\n\t" \
  "s_waitcnt vmcnt(0)\n\t" \
  "v_cmp_ne_u32 vcc, s36, v246\n\t" \
  "s_and_b32 vcc_lo, vcc_lo, 0xe\n\t" \
  "s_cbranch_scc0 .Lat_k0\n\t" \
  "s_add_u32 s81, s81, 1\n\t" \
  "s_cmp_lt_u32 s81, " FQ_CAP "\n\t" \
  "s_cbranch_scc1 .Lat_r0\n\t" \
  "buffer_store_dword v243, off, s[16:19], 0 offset:256 sc1\n\t" \
  "s_branch .Lat_k0\n\t" \
  ".Lat_s1:\n\t" \
  "s_mov_b32 s81, 0\n\t" \
  ".Lat_r1:\n\t" \
  "s_sleep 4\n\t" \
  "buffer_load_dword v246, v245, s[16:19], s65 offen sc1\n\t" \
  "s_waitcnt vmcnt(0)\n\t" \
  "v_cmp_ne_u32 vcc, s36, v246\n\t" \
  "s_and_b32 vcc_lo, vcc_lo, 0xfefe\n\t" \
  "s_cbranch_scc0 .Lat_k1\n\t" \
  "s_add_u32 s81, s81, 1\n\t" \
  "s_cmp_lt_u32 s81, " FQ_CAP "\n\t" \
  "s_cbranch_scc1 .Lat_r1\n\t" \
  "buffer_store_dword v243, off, s[16:19], 0 offset:256 sc1\n\t" \
  "s_branch .Lat_k1\n\t" \
  ".Lat_s2:\n\t" \
  "s_mov_b32 s81, 0\n\t" \
  ".Lat_r2:\n\t" \
  "s_sleep 4\n\t" \
  "buffer_load_dword v246, v245, s[16:19], s65 offen sc1\n\t" \
  "s_waitcnt vmcnt(0)\n\t" \
  "v_cmp_ne_u32 vcc, s36, v246\n\t" \
  "s_and_b32 vcc_lo, vcc_lo, 0xfe0000\n\t" \
  "s_cbranch_scc0 .Lat_k2\n\t" \
  "s_add_u32 s81, s81, 1\n\t" \
  "s_cmp_lt_u32 s81, " FQ_CAP "\n\t" \
  "s_cbranch_scc1 .Lat_r2\n\t" \
  "buffer_store_dword v243, off, s[16:19], 0 offset:256 sc1\n\t" \
  "s_branch .Lat_k2\n\t" \
  ".Lat_s3:\n\t" \
  "s_mov_b32 s81, 0\n\t" \
  ".Lat_r3:\n\t" \
  "s_sleep 4\n\t" \
  "s_add_u32 s78, s75, 0\n\t" \
  "buffer_load_dwordx4 v[208:211], v234, s[16:19], s78 offen sc1\n\t" \
  "s_add_u32 s78, s75, 32768\n\t" \
  "buffer_load_dwordx4 v[212:215], v234, s[16:19], s78 offen sc1\n\t" \
  "s_add_u32 s78, s75, 65536\n\t" \
  "buffer_load_dwordx4 v[216:219], v234, s[16:19], s78 offen sc1\n\t" \
  "s_add_u32 s78, s75, 98304\n\t" \
  "buffer_load_dwordx4 v[220:223], v234, s[16:19], s78 offen sc1\n\t" \
  "s_waitcnt vmcnt(0)\n\t" \
  "v_xor_b32 v246, v243, v209\n\t" \
  "v_bitop3_b32 v246, v246, v211, v243 bitop3:0xf6\n\t" \
  "v_bitop3_b32 v246, v246, v213, v243 bitop3:0xf6\n\t" \
  "v_bitop3_b32 v246, v246, v215, v243 bitop3:0xf6\n\t" \
  "v_bitop3_b32 v246, v246, v217, v243 bitop3:0xf6\n\t" \
  "v_bitop3_b32 v246, v246, v219, v243 bitop3:0xf6\n\t" \
  "v_bitop3_b32 v246, v246, v221, v243 bitop3:0xf6\n\t" \
  "v_bitop3_b32 v246, v246, v223, v243 bitop3:0xf6\n\t" \
  "v_cmp_ne_u32 vcc, 0, v246\n\t" \
  "s_cbranch_vccz .Lat_k3\n\t" \
  "s_add_u32 s81, s81, 1\n\t" \
  "s_cmp_lt_u32 s81, " FQ_CAP "\n\t" \
  "s_cbranch_scc1 .Lat_r3\n\t" \
  "buffer_store_dword v243, off, s[16:19], 0 offset:256 sc1\n\t" \
  "s_branch .Lat_k3\n\t" \
  ".Lat_s4:\n\t" \
  "s_mov_b32 s81, 0\n\t" \
  ".Lat_r4:\n\t" \
  "s_sleep 4\n\t" \
  "buffer_load_dword v246, v235, s[16:19], s73 offen sc1\n\t" \
  "s_waitcnt vmcnt(0)\n\t" \
  "v_cmp_ne_u32 vcc, s36, v246\n\t" \
  "s_cbranch_vccz .Lat_k4\n\t" \
  "s_add_u32 s81, s81, 1\n\t" \
  "s_cmp_lt_u32 s81, " FQ_CAP "\n\t" \
  "s_cbranch_scc1 .Lat_r4\n\t" \
  "buffer_store_dword v243, off, s[16:19], 0 offset:256 sc1\n\t" \
  "s_branch .Lat_k4\n\t" \
  ".Lat_m3:\n\t" \
  "s_waitcnt vmcnt(0)\n\t" \
  "v_cmp_ne_u32 vcc, s36, v251\n\t" \
  "s_mov_b32 s88, vcc_lo\n\t" \
  "s_cbranch_vccnz .Lat_n3\n\t" \
  "buffer_load_dwordx4 v[64:67], v242, s[16:19], s79 offen offset:0 sc1\n\t" \
  "buffer_load_dwordx4 v[68:71], v242, s[16:19], s79 offen offset:1024 sc1\n\t" \
  "buffer_load_dwordx4 v[72:75], v242, s[16:19], s79 offen offset:2048 sc1\n\t" \
  "buffer_load_dwordx4 v[76:79], v242, s[16:19], s79 offen offset:3072 sc1\n\t" \
  "s_branch .Lat_n3\n\t" \
  ".Lat_v:\n\t" \
  "s_mov_b32 s98, 0\n\t" \
  ".Lat_v1:\n\t" \
  "s_sleep 4\n\t" \
  "buffer_load_dword v209, v250, s[16:19], 0 offen sc1\n\t" \
  "s_waitcnt vmcnt(0)\n\t" \
  "v_and_b32 v251, 0xbfffbfff, v209\n\t" \
  "v_cmp_ne_u32_e64 s[84:85], s36, v251\n\t" \
  "s_cmp_lg_u64 s[84:85], 0\n\t" \
  "s_cbranch_scc0 .Lat_w\n\t" \
  "s_add_u32 s98, s98, 1\n\t" \
  "s_cmp_lt_u32 s98, " FQ_CAP "\n\t" \
  "s_cbranch_scc1 .Lat_v1\n\t" \
  "v_mov_b32 v251, s36\n\t" \
  "buffer_store_dword v251, off, s[16:19], 0 offset:256 sc1\n\t" \
  "s_branch .Lat_w\n\t" \
  ".Lat_s99:\n\t" \
  "s_mov_b32 s81, 0\n\t" \
  ".Lat_r99:\n\t" \
  "buffer_load_dword v246, off, s[16:19], s73 offset:32 sc1\n\t" \
  "s_waitcnt vmcnt(0)\n\t" \
  "v_cmp_ne_u32 vcc, s36, v246\n\t" \
  "s_cbranch_vccz .Lat_k99\n\t" \
  "s_add_u32 s81, s81, 1\n\t" \
  "s_cmp_lt_u32 s81, " FQ_CAP "\n\t" \
  "s_cbranch_scc1 .Lat_r99\n\t" \
  "buffer_store_dword v243, off, s[16:19], 0 offset:256 sc1\n\t" \
  ".Lat_k99:\n\t" \
  "buffer_load_dwordx4 v[64:67], v242, s[16:19], s79 offen offset:0 sc1\n\t" \
  "buffer_load_dwordx4 v[68:71], v242, s[16:19], s79 offen offset:1024 sc1\n\t" \
  "buffer_load_dwordx4 v[72:75], v242, s[16:19], s79 offen offset:2048 sc1\n\t" \
  "buffer_load_dwordx4 v[76:79], v242, s[16:19], s79 offen offset:3072 sc1\n\t" \
  "s_waitcnt vmcnt(0)\n\t" \
  "s_branch .Lat_d2\n\t" \
  ".Ltl_g2:\n\t" \
  "buffer_store_dword v243, off, s[16:19], 0 offset:256 sc1\n\t" \
  "s_branch .Ltl_k2\n\t" \
  "s_endpgm\n\t" \
  ".Lfz_z:\n\t" \
  ".fill 128, 4, 0xBF800000\n\t" \
  ".subsection 2\n\t" \
  ".Lsf_go:\n\t" \
  "v_mfma_f32_16x16x32_bf16 v[28:31], a[156:159], a[224:227], v[28:31]\n\t" \
  "v_mov_b32 v196, 1.0\n\t" \
  "v_mov_b32 v197, 1.0\n\t" \
  "v_mfma_f32_16x16x32_bf16 v[28:31], a[92:95], a[240:243], v[28:31]\n\t" \
  "v_mov_b32 v198, 1.0\n\t" \
  "v_mov_b32 v199, 1.0\n\t" \
  "v_mfma_f32_16x16x32_bf16 v[24:27], a[152:155], a[224:227], v[24:27]\n\t" \
  "s_mov_b32 s99, 0\n\t" \
  "v_add_u32 v210, 65536, v228\n\t" \
  "v_add_u32 v211, 65536, v229\n\t" \
  "v_mfma_f32_16x16x32_bf16 v[24:27], a[88:91], a[240:243], v[24:27]\n\t" \
  "v_mov_b32 v216, 0x3f803f80\n\t" \
  "v_cvt_pk_bf16_f32 v31, v30, v31\n\t" \
  "v_mfma_f32_16x16x32_bf16 v[20:23], a[148:151], a[224:227], v[20:23]\n\t" \
  "v_mov_b32 v217, 0x3f803f80\n\t" \
  "v_cvt_pk_bf16_f32 v30, v28, v29\n\t" \
  "v_mfma_f32_16x16x32_bf16 v[20:23], a[84:87], a[240:243], v[20:23]\n\t" \
  "v_mov_b32 v218, 0x3f803f80\n\t" \
  "v_mov_b32 v219, 0x3f803f80\n\t" \
  "v_mfma_f32_16x16x32_bf16 v[16:19], a[144:147], a[224:227], v[16:19]\n\t" \
  "v_cvt_pk_bf16_f32 v28, v24, v25\n\t" \
  "v_cvt_pk_bf16_f32 v29, v26, v27\n\t" \
  "v_mfma_f32_16x16x32_bf16 v[16:19], a[80:83], a[240:243], v[16:19]\n\t" \
  "v_permlane16_swap_b32 v28, v30\n\t" \
  "v_mfma_f32_16x16x32_bf16 v[12:15], a[140:143], a[224:227], v[12:15]\n\t" \
  "v_cvt_pk_bf16_f32 v27, v22, v23\n\t" \
  "v_cvt_pk_bf16_f32 v26, v20, v21\n\t" \
  "v_mfma_f32_16x16x32_bf16 v[12:15], a[76:79], a[240:243], v[12:15]\n\t" \
  "v_permlane16_swap_b32 v29, v31\n\t" \
  "v_mfma_f32_16x16x32_bf16 v[8:11], a[136:139], a[224:227], v[8:11]\n\t" \
  "v_mfma_f32_16x16x32_bf16 v[8:11], a[72:75], a[240:243], v[8:11]\n\t" \
  "v_cvt_pk_bf16_f32 v24, v16, v17\n\t" \
  "v_cvt_pk_bf16_f32 v25, v18, v19\n\t" \
  "v_mfma_f32_16x16x32_bf16 v[4:7], a[132:135], a[224:227], v[4:7]\n\t" \
  "v_permlane16_swap_b32 v24, v26\n\t" \
  "v_mfma_f32_16x16x32_bf16 v[4:7], a[68:71], a[240:243], v[4:7]\n\t" \
  "v_cvt_pk_bf16_f32 v23, v14, v15\n\t" \
  "v_cvt_pk_bf16_f32 v22, v12, v13\n\t" \
  "v_mfma_f32_16x16x32_bf16 v[0:3], a[128:131], a[224:227], v[0:3]\n\t" \
  "s_waitcnt vmcnt(2)\n\t" \
  "v_cvt_pk_bf16_f32 v20, v8, v9\n\t" \
  "v_cvt_pk_bf16_f32 v21, v10, v11\n\t" \
  "v_mfma_f32_16x16x32_bf16 v[0:3], a[64:67], a[240:243], v[0:3]\n\t" \
  "s_barrier\n\t" \
  "v_mfma_f32_16x16x32_bf16 v[60:63], a[156:159], a[228:231], v[60:63]\n\t" \
  "v_cvt_pk_bf16_f32 v19, v6, v7\n\t" \
  "v_cvt_pk_bf16_f32 v18, v4, v5\n\t" \
  "v_mfma_f32_16x16x32_bf16 v[60:63], a[92:95], a[244:247], v[60:63]\n\t" \
  "v_permlane16_swap_b32 v25, v27\n\t" \
  "v_mfma_f32_16x16x32_bf16 v[56:59], a[152:155], a[228:231], v[56:59]\n\t" \
  "ds_read_b128 a[0:3], v220 offset:0\n\t" \
  "v_cvt_pk_bf16_f32 v16, v0, v1\n\t" \
  "v_mfma_f32_16x16x32_bf16 v[56:59], a[88:91], a[244:247], v[56:59]\n\t" \
  "ds_read_b128 a[4:7], v221 offset:0\n\t" \
  "v_cvt_pk_bf16_f32 v17, v2, v3\n\t" \
  "v_mfma_f32_16x16x32_bf16 v[52:55], a[148:151], a[228:231], v[52:55]\n\t" \
  "v_cvt_pk_bf16_f32 v63, v62, v63\n\t" \
  "v_cvt_pk_bf16_f32 v62, v60, v61\n\t" \
  "v_mfma_f32_16x16x32_bf16 v[52:55], a[84:87], a[244:247], v[52:55]\n\t" \
  "v_permlane16_swap_b32 v20, v22\n\t" \
  "v_mfma_f32_16x16x32_bf16 v[48:51], a[144:147], a[228:231], v[48:51]\n\t" \
  "ds_read_b128 a[8:11], v222 offset:0\n\t" \
  "v_cvt_pk_bf16_f32 v60, v56, v57\n\t" \
  "v_mfma_f32_16x16x32_bf16 v[48:51], a[80:83], a[244:247], v[48:51]\n\t" \
  "ds_read_b128 a[12:15], v223 offset:0\n\t" \
  "v_cvt_pk_bf16_f32 v61, v58, v59\n\t" \
  "v_mfma_f32_16x16x32_bf16 v[44:47], a[140:143], a[228:231], v[44:47]\n\t" \
  "v_cvt_pk_bf16_f32 v59, v54, v55\n\t" \
  "v_cvt_pk_bf16_f32 v58, v52, v53\n\t" \
  "v_mfma_f32_16x16x32_bf16 v[44:47], a[76:79], a[244:247], v[44:47]\n\t" \
  "v_permlane16_swap_b32 v21, v23\n\t" \
  "v_mfma_f32_16x16x32_bf16 v[40:43], a[136:139], a[228:231], v[40:43]\n\t" \
  "ds_read_b128 a[16:19], v220 offset:4096\n\t" \
  "v_cvt_pk_bf16_f32 v56, v48, v49\n\t" \
  "v_mfma_f32_16x16x32_bf16 v[40:43], a[72:75], a[244:247], v[40:43]\n\t" \
  "ds_read_b128 a[20:23], v221 offset:4096\n\t" \
  "v_cvt_pk_bf16_f32 v57, v50, v51\n\t" \
  "v_mfma_f32_16x16x32_bf16 v[36:39], a[132:135], a[228:231], v[36:39]\n\t" \
  "v_cvt_pk_bf16_f32 v55, v46, v47\n\t" \
  "v_cvt_pk_bf16_f32 v54, v44, v45\n\t" \
  "v_mfma_f32_16x16x32_bf16 v[36:39], a[68:71], a[244:247], v[36:39]\n\t" \
  "v_permlane16_swap_b32 v16, v18\n\t" \
  "v_mfma_f32_16x16x32_bf16 v[32:35], a[128:131], a[228:231], v[32:35]\n\t" \
  "ds_read_b128 a[24:27], v222 offset:4096\n\t" \
  "v_cvt_pk_bf16_f32 v52, v40, v41\n\t" \
  "v_mfma_f32_16x16x32_bf16 v[32:35], a[64:67], a[244:247], v[32:35]\n\t" \
  "ds_read_b128 a[28:31], v223 offset:4096\n\t" \
  "v_cvt_pk_bf16_f32 v53, v42, v43\n\t" \
  "v_mfma_f32_16x16x32_bf16 v[92:95], a[156:159], a[232:235], v[92:95]\n\t" \
  "v_cvt_pk_bf16_f32 v51, v38, v39\n\t" \
  "v_cvt_pk_bf16_f32 v50, v36, v37\n\t" \
  "v_mfma_f32_16x16x32_bf16 v[92:95], a[92:95], a[248:251], v[92:95]\n\t" \
  "v_permlane16_swap_b32 v17, v19\n\t" \
  "v_mfma_f32_16x16x32_bf16 v[88:91], a[152:155], a[232:235], v[88:91]\n\t" \
  "v_cvt_pk_bf16_f32 v48, v32, v33\n\t" \
  "v_cvt_pk_bf16_f32 v49, v34, v35\n\t" \
  "v_mfma_f32_16x16x32_bf16 v[88:91], a[88:91], a[248:251], v[88:91]\n\t" \
  "v_permlane16_swap_b32 v60, v62\n\t" \
  "v_mfma_f32_16x16x32_bf16 v[84:87], a[148:151], a[232:235], v[84:87]\n\t" \
  "ds_read_b128 a[32:35], v220 offset:8192\n\t" \
  "v_cvt_pk_bf16_f32 v95, v94, v95\n\t" \
  "v_mfma_f32_16x16x32_bf16 v[84:87], a[84:87], a[248:251], v[84:87]\n\t" \
  "ds_read_b128 a[36:39], v221 offset:8192\n\t" \
  "v_cvt_pk_bf16_f32 v94, v92, v93\n\t" \
  "v_mfma_f32_16x16x32_bf16 v[80:83], a[144:147], a[232:235], v[80:83]\n\t" \
  "v_cvt_pk_bf16_f32 v92, v88, v89\n\t" \
  "v_permlane16_swap_b32 v61, v63\n\t" \
  "v_mfma_f32_16x16x32_bf16 v[80:83], a[80:83], a[248:251], v[80:83]\n\t" \
  "ds_read_b128 a[40:43], v222 offset:8192\n\t" \
  "v_cvt_pk_bf16_f32 v93, v90, v91\n\t" \
  "v_mfma_f32_16x16x32_bf16 v[76:79], a[140:143], a[232:235], v[76:79]\n\t" \
  "v_cvt_pk_bf16_f32 v91, v86, v87\n\t" \
  "v_cvt_pk_bf16_f32 v90, v84, v85\n\t" \
  "v_mfma_f32_16x16x32_bf16 v[76:79], a[76:79], a[248:251], v[76:79]\n\t" \
  "ds_read_b128 a[44:47], v223 offset:8192\n\t" \
  "v_permlane16_swap_b32 v56, v58\n\t" \
  "v_mfma_f32_16x16x32_bf16 v[72:75], a[136:139], a[232:235], v[72:75]\n\t" \
  "v_cvt_pk_bf16_f32 v88, v80, v81\n\t" \
  "v_permlane16_swap_b32 v57, v59\n\t" \
  "v_mfma_f32_16x16x32_bf16 v[72:75], a[72:75], a[248:251], v[72:75]\n\t" \
  "ds_read_b128 a[48:51], v220 offset:12288\n\t" \
  "v_cvt_pk_bf16_f32 v89, v82, v83\n\t" \
  "v_mfma_f32_16x16x32_bf16 v[68:71], a[132:135], a[232:235], v[68:71]\n\t" \
  "ds_read_b128 a[52:55], v221 offset:12288\n\t" \
  "v_cvt_pk_bf16_f32 v87, v78, v79\n\t" \
  "v_mfma_f32_16x16x32_bf16 v[68:71], a[68:71], a[248:251], v[68:71]\n\t" \
  "v_cvt_pk_bf16_f32 v86, v76, v77\n\t" \
  "v_permlane16_swap_b32 v52, v54\n\t" \
  "v_mfma_f32_16x16x32_bf16 v[64:67], a[128:131], a[232:235], v[64:67]\n\t" \
  "ds_read_b128 a[56:59], v222 offset:12288\n\t" \
  "v_permlane16_swap_b32 v53, v55\n\t" \
  "v_mfma_f32_16x16x32_bf16 v[64:67], a[64:67], a[248:251], v[64:67]\n\t" \
  "v_cvt_pk_bf16_f32 v84, v72, v73\n\t" \
  "v_cvt_pk_bf16_f32 v85, v74, v75\n\t" \
  "v_mfma_f32_16x16x32_bf16 v[124:127], a[156:159], a[236:239], v[124:127]\n\t" \
  "ds_read_b128 a[60:63], v223 offset:12288\n\t" \
  "v_cvt_pk_bf16_f32 v83, v70, v71\n\t" \
  "v_mfma_f32_16x16x32_bf16 v[124:127], a[92:95], a[252:255], v[124:127]\n\t" \
  "v_cvt_pk_bf16_f32 v82, v68, v69\n\t" \
  "v_permlane16_swap_b32 v48, v50\n\t" \
  "v_mfma_f32_16x16x32_bf16 v[120:123], a[152:155], a[236:239], v[120:123]\n\t" \
  "v_cvt_pk_bf16_f32 v80, v64, v65\n\t" \
  "v_permlane16_swap_b32 v49, v51\n\t" \
  "v_mfma_f32_16x16x32_bf16 v[120:123], a[88:91], a[252:255], v[120:123]\n\t" \
  "v_permlane16_swap_b32 v92, v94\n\t" \
  "v_mfma_f32_16x16x32_bf16 v[116:119], a[148:151], a[236:239], v[116:119]\n\t" \
  "v_cvt_pk_bf16_f32 v81, v66, v67\n\t" \
  "v_cvt_pk_bf16_f32 v127, v126, v127\n\t" \
  "v_mfma_f32_16x16x32_bf16 v[116:119], a[84:87], a[252:255], v[116:119]\n\t" \
  "v_permlane16_swap_b32 v93, v95\n\t" \
  "v_mfma_f32_16x16x32_bf16 v[112:115], a[144:147], a[236:239], v[112:115]\n\t" \
  "v_cvt_pk_bf16_f32 v126, v124, v125\n\t" \
  "v_cvt_pk_bf16_f32 v124, v120, v121\n\t" \
  "v_mfma_f32_16x16x32_bf16 v[112:115], a[80:83], a[252:255], v[112:115]\n\t" \
  "v_cvt_pk_bf16_f32 v125, v122, v123\n\t" \
  "v_permlane16_swap_b32 v88, v90\n\t" \
  "v_mfma_f32_16x16x32_bf16 v[108:111], a[140:143], a[236:239], v[108:111]\n\t" \
  "v_permlane16_swap_b32 v89, v91\n\t" \
  "v_mfma_f32_16x16x32_bf16 v[108:111], a[76:79], a[252:255], v[108:111]\n\t" \
  "v_cvt_pk_bf16_f32 v123, v118, v119\n\t" \
  "v_cvt_pk_bf16_f32 v122, v116, v117\n\t" \
  "v_mfma_f32_16x16x32_bf16 v[104:107], a[136:139], a[236:239], v[104:107]\n\t" \
  "v_permlane16_swap_b32 v84, v86\n\t" \
  "v_mfma_f32_16x16x32_bf16 v[104:107], a[72:75], a[252:255], v[104:107]\n\t" \
  "v_cvt_pk_bf16_f32 v120, v112, v113\n\t" \
  "v_cvt_pk_bf16_f32 v121, v114, v115\n\t" \
  "v_mfma_f32_16x16x32_bf16 v[100:103], a[132:135], a[236:239], v[100:103]\n\t" \
  "s_add_u32 m0, s38, 49152\n\t" \
  "v_permlane16_swap_b32 v85, v87\n\t" \
  "v_mfma_f32_16x16x32_bf16 v[100:103], a[68:71], a[252:255], v[100:103]\n\t" \
  "s_add_u32 s96, s38, 32768\n\t" \
  "v_permlane16_swap_b32 v80, v82\n\t" \
  "v_mfma_f32_16x16x32_bf16 v[96:99], a[128:131], a[236:239], v[96:99]\n\t" \
  "s_waitcnt lgkmcnt(0)\n\t" \
  "v_cvt_pk_bf16_f32 v119, v110, v111\n\t" \
  "v_cvt_pk_bf16_f32 v118, v108, v109\n\t" \
  "v_mfma_f32_16x16x32_bf16 v[96:99], a[64:67], a[252:255], v[96:99]\n\t" \
  "s_barrier\n\t" \
  "v_mfma_f32_16x16x32_bf16 v[0:3], a[0:3], v[16:19], 0\n\t" \
  "buffer_load_dwordx4 v224, s[60:63], s96 offen offset:0 lds\n\t" \
  "v_mfma_f32_16x16x32_bf16 v[0:3], a[4:7], v[20:23], v[0:3]\n\t" \
  "v_permlane16_swap_b32 v81, v83\n\t" \
  "v_mfma_f32_16x16x32_bf16 v[0:3], a[8:11], v[24:27], v[0:3]\n\t" \
  "ds_read_b128 a[192:195], v220 offset:16384\n\t" \
  "v_cvt_pk_bf16_f32 v116, v104, v105\n\t" \
  "v_mfma_f32_16x16x32_bf16 v[0:3], a[12:15], v[28:31], v[0:3]\n\t" \
  "v_permlane16_swap_b32 v124, v126\n\t" \
  "v_mfma_f32_16x16x32_bf16 v[32:35], a[0:3], v[48:51], 0\n\t" \
  "v_permlane16_swap_b32 v125, v127\n\t" \
  "v_mfma_f32_16x16x32_bf16 v[32:35], a[4:7], v[52:55], v[32:35]\n\t" \
  "s_nop 3\n\t" \
  "v_max3_f32 v194, v0, v1, v2\n\t" \
  "v_mfma_f32_16x16x32_bf16 v[32:35], a[8:11], v[56:59], v[32:35]\n\t" \
  "ds_read_b128 a[196:199], v221 offset:16384\n\t" \
  "v_cvt_pk_bf16_f32 v117, v106, v107\n\t" \
  "v_max_f32 v194, v194, v3\n\t" \
  "v_mov_b32 v195, v194\n\t" \
  "v_mfma_f32_16x16x32_bf16 v[32:35], a[12:15], v[60:63], v[32:35]\n\t" \
  "v_cvt_pk_bf16_f32 v115, v102, v103\n\t" \
  "v_permlane32_swap_b32 v194, v195\n\t" \
  "v_max_f32 v194, v194, v195\n\t" \
  "v_mov_b32 v195, v194\n\t" \
  "s_nop 1\n\t" \
  "v_permlane16_swap_b32 v194, v195\n\t" \
  "v_max_f32 v194, v194, v195\n\t" \
  "v_sub_f32 v176, 0xc2000000, v194\n\t" \
  "v_sub_f32 v177, 0xc2000000, v194\n\t" \
  "v_sub_f32 v178, 0xc2000000, v194\n\t" \
  "v_sub_f32 v179, 0xc2000000, v194\n\t" \
  "s_nop 1\n\t" \
  "v_mfma_f32_16x16x32_bf16 v[4:7], a[16:19], v[16:19], v[176:179]\n\t" \
  "v_add_f32 v0, v0, v176\n\t" \
  "v_add_f32 v1, v1, v176\n\t" \
  "v_add_f32 v2, v2, v176\n\t" \
  "v_add_f32 v3, v3, v176\n\t" \
  "v_mfma_f32_16x16x32_bf16 v[4:7], a[20:23], v[20:23], v[4:7]\n\t" \
  "buffer_load_dwordx4 v225, s[60:63], s96 offen offset:1024 lds\n\t" \
  "v_max3_f32 v194, v32, v33, v34\n\t" \
  "v_max_f32 v194, v194, v35\n\t" \
  "v_mov_b32 v195, v194\n\t" \
  "v_mfma_f32_16x16x32_bf16 v[4:7], a[24:27], v[24:27], v[4:7]\n\t" \
  "ds_read_b128 a[200:203], v222 offset:16384\n\t" \
  "v_permlane32_swap_b32 v194, v195\n\t" \
  "v_max_f32 v194, v194, v195\n\t" \
  "v_mov_b32 v195, v194\n\t" \
  "v_mfma_f32_16x16x32_bf16 v[4:7], a[28:31], v[28:31], v[4:7]\n\t" \
  "v_permlane16_swap_b32 v120, v122\n\t" \
  "v_permlane16_swap_b32 v194, v195\n\t" \
  "v_max_f32 v194, v194, v195\n\t" \
  "v_mfma_f32_16x16x32_bf16 v[8:11], a[32:35], v[16:19], v[176:179]\n\t" \
  "v_sub_f32 v180, 0xc2000000, v194\n\t" \
  "v_sub_f32 v181, 0xc2000000, v194\n\t" \
  "v_mfma_f32_16x16x32_bf16 v[8:11], a[36:39], v[20:23], v[8:11]\n\t" \
  "v_sub_f32 v182, 0xc2000000, v194\n\t" \
  "v_sub_f32 v183, 0xc2000000, v194\n\t" \
  "v_mfma_f32_16x16x32_bf16 v[8:11], a[40:43], v[24:27], v[8:11]\n\t" \
  "v_add_f32 v32, v32, v180\n\t" \
  "v_add_f32 v33, v33, v180\n\t" \
  "v_max3_f32 v192, v4, v5, v6\n\t" \
  "v_mfma_f32_16x16x32_bf16 v[8:11], a[44:47], v[28:31], v[8:11]\n\t" \
  "v_add_f32 v34, v34, v180\n\t" \
  "v_permlane16_swap_b32 v121, v123\n\t" \
  "v_mfma_f32_16x16x32_bf16 v[12:15], a[48:51], v[16:19], v[176:179]\n\t" \
  "v_add_f32 v35, v35, v180\n\t" \
  "v_permlane16_swap_b32 v116, v118\n\t" \
  "v_mfma_f32_16x16x32_bf16 v[12:15], a[52:55], v[20:23], v[12:15]\n\t" \
  "ds_read_b128 a[204:207], v223 offset:16384\n\t" \
  "v_cvt_pk_bf16_f32 v114, v100, v101\n\t" \
  "v_max3_f32 v203, v7, v8, v9\n\t" \
  "v_max3_f32 v192, v192, v10, v11\n\t" \
  "v_mfma_f32_16x16x32_bf16 v[12:15], a[56:59], v[24:27], v[12:15]\n\t" \
  "buffer_load_dwordx4 v226, s[60:63], s96 offen offset:2048 lds\n\t" \
  "v_mfma_f32_16x16x32_bf16 v[12:15], a[60:63], v[28:31], v[12:15]\n\t" \
  "ds_read_b128 a[208:211], v220 offset:20480\n\t" \
  "v_permlane16_swap_b32 v117, v119\n\t" \
  "v_mfma_f32_16x16x32_bf16 v[64:67], a[0:3], v[80:83], 0\n\t" \
  "ds_read_b128 a[212:215], v221 offset:20480\n\t" \
  "v_cvt_pk_bf16_f32 v112, v96, v97\n\t" \
  "v_mfma_f32_16x16x32_bf16 v[64:67], a[4:7], v[84:87], v[64:67]\n\t" \
  "v_cvt_pk_bf16_f32 v113, v98, v99\n\t" \
  "s_nop 0\n\t" \
  "v_max3_f32 v203, v203, v12, v13\n\t" \
  "v_max3_f32 v192, v192, v14, v15\n\t" \
  "v_max_f32 v192, v192, v203\n\t" \
  "v_mfma_f32_16x16x32_bf16 v[64:67], a[8:11], v[88:91], v[64:67]\n\t" \
  "v_permlane16_swap_b32 v112, v114\n\t" \
  "v_cmp_lt_f32 vcc, 0x42800000, v192\n\t" \
  "v_mfma_f32_16x16x32_bf16 v[64:67], a[12:15], v[92:95], v[64:67]\n\t" \
  "v_permlane16_swap_b32 v113, v115\n\t" \
  "v_mfma_f32_16x16x32_bf16 v[36:39], a[16:19], v[48:51], v[180:183]\n\t" \
  "s_cbranch_vccnz .Lsf_c00\n\t" \
  ".Lsf_r00:\n\t" \
  "v_mfma_f32_16x16x32_bf16 v[36:39], a[20:23], v[52:55], v[36:39]\n\t" \
  "ds_read_b128 a[216:219], v222 offset:20480\n\t" \
  "v_exp_f32 v0, v0\n\t" \
  "v_exp_f32 v1, v1\n\t" \
  "v_mfma_f32_16x16x32_bf16 v[36:39], a[24:27], v[56:59], v[36:39]\n\t" \
  "v_max3_f32 v194, v64, v65, v66\n\t" \
  "v_max_f32 v194, v194, v67\n\t" \
  "v_mov_b32 v195, v194\n\t" \
  "v_exp_f32 v2, v2\n\t" \
  "v_mfma_f32_16x16x32_bf16 v[36:39], a[28:31], v[60:63], v[36:39]\n\t" \
  "ds_read_b128 a[220:223], v223 offset:20480\n\t" \
  "v_permlane32_swap_b32 v194, v195\n\t" \
  "v_max_f32 v194, v194, v195\n\t" \
  "v_mov_b32 v195, v194\n\t" \
  "v_mfma_f32_16x16x32_bf16 v[40:43], a[32:35], v[48:51], v[180:183]\n\t" \
  "s_nop 0\n\t" \
  "v_permlane16_swap_b32 v194, v195\n\t" \
  "v_max_f32 v194, v194, v195\n\t" \
  "v_sub_f32 v184, 0xc2000000, v194\n\t" \
  "v_sub_f32 v185, 0xc2000000, v194\n\t" \
  "v_mfma_f32_16x16x32_bf16 v[40:43], a[36:39], v[52:55], v[40:43]\n\t" \
  "buffer_load_dwordx4 v227, s[60:63], s96 offen offset:3072 lds\n\t" \
  "v_exp_f32 v3, v3\n\t" \
  "v_sub_f32 v186, 0xc2000000, v194\n\t" \
  "v_mfma_f32_16x16x32_bf16 v[40:43], a[40:43], v[56:59], v[40:43]\n\t" \
  "v_sub_f32 v187, 0xc2000000, v194\n\t" \
  "v_add_f32 v64, v64, v184\n\t" \
  "v_max3_f32 v192, v36, v37, v38\n\t" \
  "v_exp_f32 v4, v4\n\t" \
  "v_mfma_f32_16x16x32_bf16 v[40:43], a[44:47], v[60:63], v[40:43]\n\t" \
  "v_add_f32 v65, v65, v184\n\t" \
  "v_exp_f32 v5, v5\n\t" \
  "v_exp_f32 v6, v6\n\t" \
  "v_mfma_f32_16x16x32_bf16 v[44:47], a[48:51], v[48:51], v[180:183]\n\t" \
  "s_add_u32 m0, s38, 147456\n\t" \
  "v_add_f32 v66, v66, v184\n\t" \
  "v_exp_f32 v7, v7\n\t" \
  "v_exp_f32 v8, v8\n\t" \
  "v_mfma_f32_16x16x32_bf16 v[44:47], a[52:55], v[52:55], v[44:47]\n\t" \
  "s_add_u32 s96, s38, 16384\n\t" \
  "v_max3_f32 v203, v39, v40, v41\n\t" \
  "v_exp_f32 v9, v9\n\t" \
  "v_exp_f32 v10, v10\n\t" \
  "v_mfma_f32_16x16x32_bf16 v[44:47], a[56:59], v[56:59], v[44:47]\n\t" \
  "v_max3_f32 v192, v192, v42, v43\n\t" \
  "v_exp_f32 v11, v11\n\t" \
  "v_exp_f32 v12, v12\n\t" \
  "v_mfma_f32_16x16x32_bf16 v[44:47], a[60:63], v[60:63], v[44:47]\n\t" \
  "v_add_f32 v67, v67, v184\n\t" \
  "v_exp_f32 v13, v13\n\t" \
  "v_exp_f32 v14, v14\n\t" \
  "v_mfma_f32_16x16x32_bf16 v[96:99], a[0:3], v[112:115], 0\n\t" \
  "buffer_load_dwordx4 v230, s[64:67], s96 offen offset:0 lds\n\t" \
  "v_exp_f32 v15, v15\n\t" \
  "v_mfma_f32_16x16x32_bf16 v[96:99], a[4:7], v[116:119], v[96:99]\n\t" \
  "s_nop 0\n\t" \
  "v_max3_f32 v203, v203, v44, v45\n\t" \
  "v_max3_f32 v192, v192, v46, v47\n\t" \
  "v_max_f32 v192, v192, v203\n\t" \
  "v_cmp_lt_f32 vcc, 0x42800000, v192\n\t" \
  "v_mfma_f32_16x16x32_bf16 v[96:99], a[8:11], v[120:123], v[96:99]\n\t" \
  "ds_read_b128 a[224:227], v220 offset:24576\n\t" \
  "v_cvt_pk_bf16_f32 v128, v0, v1\n\t" \
  "v_mfma_f32_16x16x32_bf16 v[96:99], a[12:15], v[124:127], v[96:99]\n\t" \
  "ds_read_b128 a[228:231], v221 offset:24576\n\t" \
  "s_cbranch_vccnz .Lsf_c01\n\t" \
  ".Lsf_r01:\n\t" \
  "v_mfma_f32_16x16x32_bf16 v[68:71], a[16:19], v[80:83], v[184:187]\n\t" \
  "ds_read_b128 a[232:235], v222 offset:24576\n\t" \
  "v_exp_f32 v32, v32\n\t" \
  "v_exp_f32 v33, v33\n\t" \
  "v_mfma_f32_16x16x32_bf16 v[68:71], a[20:23], v[84:87], v[68:71]\n\t" \
  "s_nop 0\n\t" \
  "v_max3_f32 v194, v96, v97, v98\n\t" \
  "v_max_f32 v194, v194, v99\n\t" \
  "v_mov_b32 v195, v194\n\t" \
  "v_exp_f32 v34, v34\n\t" \
  "v_mfma_f32_16x16x32_bf16 v[68:71], a[24:27], v[88:91], v[68:71]\n\t" \
  "ds_read_b128 a[236:239], v223 offset:24576\n\t" \
  "v_permlane32_swap_b32 v194, v195\n\t" \
  "v_max_f32 v194, v194, v195\n\t" \
  "v_mov_b32 v195, v194\n\t" \
  "v_mfma_f32_16x16x32_bf16 v[68:71], a[28:31], v[92:95], v[68:71]\n\t" \
  "ds_read_b128 a[240:243], v220 offset:28672\n\t" \
  "v_permlane16_swap_b32 v194, v195\n\t" \
  "v_max_f32 v194, v194, v195\n\t" \
  "v_sub_f32 v188, 0xc2000000, v194\n\t" \
  "v_mfma_f32_16x16x32_bf16 v[72:75], a[32:35], v[80:83], v[184:187]\n\t" \
  "v_sub_f32 v189, 0xc2000000, v194\n\t" \
  "v_sub_f32 v190, 0xc2000000, v194\n\t" \
  "v_exp_f32 v35, v35\n\t" \
  "v_max3_f32 v192, v68, v69, v70\n\t" \
  "v_mfma_f32_16x16x32_bf16 v[72:75], a[36:39], v[84:87], v[72:75]\n\t" \
  "v_sub_f32 v191, 0xc2000000, v194\n\t" \
  "v_exp_f32 v36, v36\n\t" \
  "v_exp_f32 v37, v37\n\t" \
  "v_mfma_f32_16x16x32_bf16 v[72:75], a[40:43], v[88:91], v[72:75]\n\t" \
  "buffer_load_dwordx4 v231, s[64:67], s96 offen offset:1024 lds\n\t" \
  "v_exp_f32 v38, v38\n\t" \
  "v_mfma_f32_16x16x32_bf16 v[72:75], a[44:47], v[92:95], v[72:75]\n\t" \
  "v_add_f32 v96, v96, v188\n\t" \
  "v_exp_f32 v39, v39\n\t" \
  "v_exp_f32 v40, v40\n\t" \
  "v_mfma_f32_16x16x32_bf16 v[76:79], a[48:51], v[80:83], v[184:187]\n\t" \
  "ds_read_b128 a[244:247], v221 offset:28672\n\t" \
  "v_exp_f32 v41, v41\n\t" \
  "v_exp_f32 v42, v42\n\t" \
  "v_mfma_f32_16x16x32_bf16 v[76:79], a[52:55], v[84:87], v[76:79]\n\t" \
  "v_add_f32 v97, v97, v188\n\t" \
  "v_max3_f32 v203, v71, v72, v73\n\t" \
  "v_exp_f32 v43, v43\n\t" \
  "v_max3_f32 v192, v192, v74, v75\n\t" \
  "v_mfma_f32_16x16x32_bf16 v[76:79], a[56:59], v[88:91], v[76:79]\n\t" \
  "v_add_f32 v98, v98, v188\n\t" \
  "v_exp_f32 v44, v44\n\t" \
  "v_exp_f32 v45, v45\n\t" \
  "v_mfma_f32_16x16x32_bf16 v[76:79], a[60:63], v[92:95], v[76:79]\n\t" \
  "ds_read_b128 a[248:251], v222 offset:28672\n\t" \
  "v_exp_f32 v46, v46\n\t" \
  "v_exp_f32 v47, v47\n\t" \
  "v_mfma_f32_16x16x32_bf16 v[100:103], a[16:19], v[112:115], v[188:191]\n\t" \
  "s_nop 3\n\t" \
  "v_max3_f32 v203, v203, v76, v77\n\t" \
  "v_mfma_f32_16x16x32_bf16 v[100:103], a[20:23], v[116:119], v[100:103]\n\t" \
  "ds_read_b128 a[252:255], v223 offset:28672\n\t" \
  "v_max3_f32 v192, v192, v78, v79\n\t" \
  "v_max_f32 v192, v192, v203\n\t" \
  "v_cmp_lt_f32 vcc, 0x42800000, v192\n\t" \
  "v_mfma_f32_16x16x32_bf16 v[100:103], a[24:27], v[120:123], v[100:103]\n\t" \
  "buffer_load_dwordx4 v230, s[64:67], s96 offen offset:2048 lds\n\t" \
  "v_mfma_f32_16x16x32_bf16 v[100:103], a[28:31], v[124:127], v[100:103]\n\t" \
  "v_add_f32 v99, v99, v188\n\t" \
  "v_cvt_pk_bf16_f32 v129, v2, v3\n\t" \
  "s_cbranch_vccnz .Lsf_c02\n\t" \
  ".Lsf_r02:\n\t" \
  "v_mfma_f32_16x16x32_bf16 v[104:107], a[32:35], v[112:115], v[188:191]\n\t" \
  "v_cvt_pk_bf16_f32 v130, v4, v5\n\t" \
  "v_cvt_pk_bf16_f32 v131, v6, v7\n\t" \
  "v_exp_f32 v64, v64\n\t" \
  "v_mfma_f32_16x16x32_bf16 v[104:107], a[36:39], v[116:119], v[104:107]\n\t" \
  "v_cvt_pk_bf16_f32 v132, v8, v9\n\t" \
  "v_exp_f32 v65, v65\n\t" \
  "v_max3_f32 v192, v100, v101, v102\n\t" \
  "v_mfma_f32_16x16x32_bf16 v[104:107], a[40:43], v[120:123], v[104:107]\n\t" \
  "v_cvt_pk_bf16_f32 v133, v10, v11\n\t" \
  "v_exp_f32 v66, v66\n\t" \
  "v_mfma_f32_16x16x32_bf16 v[104:107], a[44:47], v[124:127], v[104:107]\n\t" \
  "v_cvt_pk_bf16_f32 v134, v12, v13\n\t" \
  "v_exp_f32 v67, v67\n\t" \
  "v_mfma_f32_16x16x32_bf16 v[108:111], a[48:51], v[112:115], v[188:191]\n\t" \
  "v_cvt_pk_bf16_f32 v135, v14, v15\n\t" \
  "v_exp_f32 v68, v68\n\t" \
  "v_mfma_f32_16x16x32_bf16 v[108:111], a[52:55], v[116:119], v[108:111]\n\t" \
  "v_exp_f32 v69, v69\n\t" \
  "v_mfma_f32_16x16x32_bf16 v[108:111], a[56:59], v[120:123], v[108:111]\n\t" \
  "v_max3_f32 v203, v103, v104, v105\n\t" \
  "v_max3_f32 v192, v192, v106, v107\n\t" \
  "v_mfma_f32_16x16x32_bf16 v[108:111], a[60:63], v[124:127], v[108:111]\n\t" \
  "s_waitcnt lgkmcnt(0)\n\t" \
  "buffer_load_dwordx4 v231, s[64:67], s96 offen offset:3072 lds\n\t" \
  "v_mfma_f32_16x16x32_bf16 v[0:3], a[192:195], v[16:19], v[176:179]\n\t" \
  "ds_read_b64_tr_b16 a[128:129], v228 offset:0\n\t" \
  "v_cvt_pk_bf16_f32 v136, v32, v33\n\t" \
  "v_mfma_f32_16x16x32_bf16 v[0:3], a[196:199], v[20:23], v[0:3]\n\t" \
  "v_cvt_pk_bf16_f32 v137, v34, v35\n\t" \
  "v_exp_f32 v70, v70\n\t" \
  "v_mfma_f32_16x16x32_bf16 v[0:3], a[200:203], v[24:27], v[0:3]\n\t" \
  "v_max3_f32 v203, v203, v108, v109\n\t" \
  "v_max3_f32 v192, v192, v110, v111\n\t" \
  "v_mfma_f32_16x16x32_bf16 v[0:3], a[204:207], v[28:31], v[0:3]\n\t" \
  "v_max_f32 v192, v192, v203\n\t" \
  "v_cmp_lt_f32 vcc, 0x42800000, v192\n\t" \
  "v_mfma_f32_16x16x32_bf16 v[4:7], a[220:223], v[28:31], v[176:179]\n\t" \
  "ds_read_b64_tr_b16 a[130:131], v228 offset:4096\n\t" \
  "v_exp_f32 v71, v71\n\t" \
  "v_mfma_f32_16x16x32_bf16 v[4:7], a[216:219], v[24:27], v[4:7]\n\t" \
  "s_cbranch_vccnz .Lsf_c03\n\t" \
  ".Lsf_r03:\n\t" \
  "v_mfma_f32_16x16x32_bf16 v[4:7], a[212:215], v[20:23], v[4:7]\n\t" \
  "ds_read_b64_tr_b16 a[132:133], v228 offset:8192\n\t" \
  "v_exp_f32 v72, v72\n\t" \
  "v_mfma_f32_16x16x32_bf16 v[4:7], a[208:211], v[16:19], v[4:7]\n\t" \
  "s_cmp_lg_u32 s99, 0\n\t" \
  "s_cbranch_scc1 .Lsf_p0\n\t" \
  ".Lsf_q0:\n\t" \
  "v_mfma_f32_16x16x32_bf16 v[8:11], a[224:227], v[16:19], v[176:179]\n\t" \
  "ds_read_b64_tr_b16 a[134:135], v228 offset:12288\n\t" \
  "v_max3_f32 v192, v0, v1, v2\n\t" \
  "v_mfma_f32_16x16x32_bf16 v[8:11], a[228:231], v[20:23], v[8:11]\n\t" \
  "ds_read_b64_tr_b16 a[136:137], v229 offset:0\n\t" \
  "v_exp_f32 v73, v73\n\t" \
  "v_mfma_f32_16x16x32_bf16 v[8:11], a[232:235], v[24:27], v[8:11]\n\t" \
  "v_max3_f32 v203, v3, v4, v5\n\t" \
  "v_max3_f32 v192, v192, v6, v7\n\t" \
  "v_mfma_f32_16x16x32_bf16 v[8:11], a[236:239], v[28:31], v[8:11]\n\t" \
  "ds_read_b64_tr_b16 a[138:139], v229 offset:4096\n\t" \
  "v_exp_f32 v74, v74\n\t" \
  "v_mfma_f32_16x16x32_bf16 v[12:15], a[252:255], v[28:31], v[176:179]\n\t" \
  "ds_read_b64_tr_b16 a[140:141], v229 offset:8192\n\t" \
  "v_exp_f32 v75, v75\n\t" \
  "v_mfma_f32_16x16x32_bf16 v[12:15], a[248:251], v[24:27], v[12:15]\n\t" \
  "ds_read_b64_tr_b16 a[142:143], v229 offset:12288\n\t" \
  "v_cvt_pk_bf16_f32 v138, v36, v37\n\t" \
  "v_mfma_f32_16x16x32_bf16 v[12:15], a[244:247], v[20:23], v[12:15]\n\t" \
  "v_max3_f32 v203, v203, v8, v9\n\t" \
  "v_max3_f32 v192, v192, v10, v11\n\t" \
  "v_mfma_f32_16x16x32_bf16 v[12:15], a[240:243], v[16:19], v[12:15]\n\t" \
  "ds_read_b64_tr_b16 a[144:145], v228 offset:256\n\t" \
  "v_exp_f32 v76, v76\n\t" \
  "v_mfma_f32_16x16x32_bf16 v[32:35], a[192:195], v[48:51], v[180:183]\n\t" \
  "v_cvt_pk_bf16_f32 v139, v38, v39\n\t" \
  "v_exp_f32 v77, v77\n\t" \
  "v_mfma_f32_16x16x32_bf16 v[32:35], a[196:199], v[52:55], v[32:35]\n\t" \
  "ds_read_b64_tr_b16 a[146:147], v228 offset:4352\n\t" \
  "v_cvt_pk_bf16_f32 v140, v40, v41\n\t" \
  "v_mfma_f32_16x16x32_bf16 v[32:35], a[200:203], v[56:59], v[32:35]\n\t" \
  "v_max3_f32 v203, v203, v12, v13\n\t" \
  "v_max3_f32 v192, v192, v14, v15\n\t" \
  "v_mfma_f32_16x16x32_bf16 v[32:35], a[204:207], v[60:63], v[32:35]\n\t" \
  "v_max_f32 v192, v192, v203\n\t" \
  "v_cmp_lt_f32 vcc, 0x42800000, v192\n\t" \
  "v_mfma_f32_16x16x32_bf16 v[36:39], a[220:223], v[60:63], v[180:183]\n\t" \
  "ds_read_b64_tr_b16 a[148:149], v228 offset:8448\n\t" \
  "v_exp_f32 v78, v78\n\t" \
  "v_mfma_f32_16x16x32_bf16 v[36:39], a[216:219], v[56:59], v[36:39]\n\t" \
  "s_cbranch_vccnz .Lsf_c10\n\t" \
  ".Lsf_r10:\n\t" \
  "v_mfma_f32_16x16x32_bf16 v[36:39], a[212:215], v[52:55], v[36:39]\n\t" \
  "v_max3_f32 v192, v32, v33, v34\n\t" \
  "v_exp_f32 v79, v79\n\t" \
  "v_cvt_pk_bf16_f32 v141, v42, v43\n\t" \
  "v_mfma_f32_16x16x32_bf16 v[36:39], a[208:211], v[48:51], v[36:39]\n\t" \
  "ds_read_b64_tr_b16 a[150:151], v228 offset:12544\n\t" \
  "v_exp_f32 v96, v96\n\t" \
  "v_mfma_f32_16x16x32_bf16 v[40:43], a[224:227], v[48:51], v[180:183]\n\t" \
  "ds_read_b64_tr_b16 a[152:153], v229 offset:256\n\t" \
  "v_exp_f32 v97, v97\n\t" \
  "v_mfma_f32_16x16x32_bf16 v[40:43], a[228:231], v[52:55], v[40:43]\n\t" \
  "ds_read_b64_tr_b16 a[154:155], v229 offset:4352\n\t" \
  "v_cvt_pk_bf16_f32 v142, v44, v45\n\t" \
  "v_mfma_f32_16x16x32_bf16 v[40:43], a[232:235], v[56:59], v[40:43]\n\t" \
  "v_max3_f32 v203, v35, v36, v37\n\t" \
  "v_max3_f32 v192, v192, v38, v39\n\t" \
  "v_mfma_f32_16x16x32_bf16 v[40:43], a[236:239], v[60:63], v[40:43]\n\t" \
  "ds_read_b64_tr_b16 a[156:157], v229 offset:8448\n\t" \
  "v_exp_f32 v98, v98\n\t" \
  "v_cvt_pk_bf16_f32 v143, v46, v47\n\t" \
  "v_mfma_f32_16x16x32_bf16 v[44:47], a[252:255], v[60:63], v[180:183]\n\t" \
  "ds_read_b64_tr_b16 a[158:159], v229 offset:12544\n\t" \
  "v_exp_f32 v99, v99\n\t" \
  "v_mfma_f32_16x16x32_bf16 v[44:47], a[248:251], v[56:59], v[44:47]\n\t" \
  "v_cvt_pk_bf16_f32 v144, v64, v65\n\t" \
  "v_max3_f32 v203, v203, v40, v41\n\t" \
  "v_mfma_f32_16x16x32_bf16 v[44:47], a[244:247], v[52:55], v[44:47]\n\t" \
  "v_max3_f32 v192, v192, v42, v43\n\t" \
  "v_exp_f32 v100, v100\n\t" \
  "v_mfma_f32_16x16x32_bf16 v[44:47], a[240:243], v[48:51], v[44:47]\n\t" \
  "ds_read_b64_tr_b16 a[160:161], v228 offset:512\n\t" \
  "v_cvt_pk_bf16_f32 v145, v66, v67\n\t" \
  "v_mfma_f32_16x16x32_bf16 v[64:67], a[192:195], v[80:83], v[184:187]\n\t" \
  "ds_read_b64_tr_b16 a[162:163], v228 offset:4608\n\t" \
  "v_exp_f32 v101, v101\n\t" \
  "v_mfma_f32_16x16x32_bf16 v[64:67], a[196:199], v[84:87], v[64:67]\n\t" \
  "v_cvt_pk_bf16_f32 v146, v68, v69\n\t" \
  "v_cvt_pk_bf16_f32 v147, v70, v71\n\t" \
  "v_mfma_f32_16x16x32_bf16 v[64:67], a[200:203], v[88:91], v[64:67]\n\t" \
  "v_max3_f32 v203, v203, v44, v45\n\t" \
  "v_max3_f32 v192, v192, v46, v47\n\t" \
  "v_mfma_f32_16x16x32_bf16 v[64:67], a[204:207], v[92:95], v[64:67]\n\t" \
  "v_max_f32 v192, v192, v203\n\t" \
  "v_cmp_lt_f32 vcc, 0x42800000, v192\n\t" \
  "v_mfma_f32_16x16x32_bf16 v[68:71], a[220:223], v[92:95], v[184:187]\n\t" \
  "ds_read_b64_tr_b16 a[164:165], v228 offset:8704\n\t" \
  "v_exp_f32 v102, v102\n\t" \
  "v_mfma_f32_16x16x32_bf16 v[68:71], a[216:219], v[88:91], v[68:71]\n\t" \
  "s_cbranch_vccnz .Lsf_c11\n\t" \
  ".Lsf_r11:\n\t" \
  "v_mfma_f32_16x16x32_bf16 v[68:71], a[212:215], v[84:87], v[68:71]\n\t" \
  "v_max3_f32 v192, v64, v65, v66\n\t" \
  "v_exp_f32 v103, v103\n\t" \
  "v_cvt_pk_bf16_f32 v148, v72, v73\n\t" \
  "v_cvt_pk_bf16_f32 v149, v74, v75\n\t" \
  "v_mfma_f32_16x16x32_bf16 v[68:71], a[208:211], v[80:83], v[68:71]\n\t" \
  "ds_read_b64_tr_b16 a[166:167], v228 offset:12800\n\t" \
  "v_exp_f32 v104, v104\n\t" \
  "v_mfma_f32_16x16x32_bf16 v[72:75], a[224:227], v[80:83], v[184:187]\n\t" \
  "ds_read_b64_tr_b16 a[168:169], v229 offset:512\n\t" \
  "v_exp_f32 v105, v105\n\t" \
  "v_mfma_f32_16x16x32_bf16 v[72:75], a[228:231], v[84:87], v[72:75]\n\t" \
  "ds_read_b64_tr_b16 a[170:171], v229 offset:4608\n\t" \
  "v_cvt_pk_bf16_f32 v150, v76, v77\n\t" \
  "v_mfma_f32_16x16x32_bf16 v[72:75], a[232:235], v[88:91], v[72:75]\n\t" \
  "v_max3_f32 v203, v67, v68, v69\n\t" \
  "v_max3_f32 v192, v192, v70, v71\n\t" \
  "v_mfma_f32_16x16x32_bf16 v[72:75], a[236:239], v[92:95], v[72:75]\n\t" \
  "ds_read_b64_tr_b16 a[172:173], v229 offset:8704\n\t" \
  "v_exp_f32 v106, v106\n\t" \
  "v_cvt_pk_bf16_f32 v151, v78, v79\n\t" \
  "v_mfma_f32_16x16x32_bf16 v[76:79], a[252:255], v[92:95], v[184:187]\n\t" \
  "ds_read_b64_tr_b16 a[174:175], v229 offset:12800\n\t" \
  "v_exp_f32 v107, v107\n\t" \
  "v_mfma_f32_16x16x32_bf16 v[76:79], a[248:251], v[88:91], v[76:79]\n\t" \
  "ds_read_b64_tr_b16 a[176:177], v228 offset:768\n\t" \
  "v_max3_f32 v203, v203, v72, v73\n\t" \
  "v_mfma_f32_16x16x32_bf16 v[76:79], a[244:247], v[84:87], v[76:79]\n\t" \
  "v_max3_f32 v192, v192, v74, v75\n\t" \
  "v_exp_f32 v108, v108\n\t" \
  "v_mfma_f32_16x16x32_bf16 v[76:79], a[240:243], v[80:83], v[76:79]\n\t" \
  "ds_read_b64_tr_b16 a[178:179], v228 offset:4864\n\t" \
  "v_cvt_pk_bf16_f32 v152, v96, v97\n\t" \
  "v_cvt_pk_bf16_f32 v153, v98, v99\n\t" \
  "v_mfma_f32_16x16x32_bf16 v[96:99], a[192:195], v[112:115], v[188:191]\n\t" \
  "ds_read_b64_tr_b16 a[180:181], v228 offset:8960\n\t" \
  "v_exp_f32 v109, v109\n\t" \
  "v_mfma_f32_16x16x32_bf16 v[96:99], a[196:199], v[116:119], v[96:99]\n\t" \
  "v_cvt_pk_bf16_f32 v154, v100, v101\n\t" \
  "v_max3_f32 v203, v203, v76, v77\n\t" \
  "v_mfma_f32_16x16x32_bf16 v[96:99], a[200:203], v[120:123], v[96:99]\n\t" \
  "v_max3_f32 v192, v192, v78, v79\n\t" \
  "v_max_f32 v192, v192, v203\n\t" \
  "v_mfma_f32_16x16x32_bf16 v[96:99], a[204:207], v[124:127], v[96:99]\n\t" \
  "v_cmp_lt_f32 vcc, 0x42800000, v192\n\t" \
  "v_exp_f32 v110, v110\n\t" \
  "v_cvt_pk_bf16_f32 v155, v102, v103\n\t" \
  "v_mfma_f32_16x16x32_bf16 v[100:103], a[220:223], v[124:127], v[188:191]\n\t" \
  "ds_read_b64_tr_b16 a[182:183], v228 offset:13056\n\t" \
  "v_exp_f32 v111, v111\n\t" \
  "v_mfma_f32_16x16x32_bf16 v[100:103], a[216:219], v[120:123], v[100:103]\n\t" \
  "s_cbranch_vccnz .Lsf_c12\n\t" \
  ".Lsf_r12:\n\t" \
  "v_mfma_f32_16x16x32_bf16 v[100:103], a[212:215], v[116:119], v[100:103]\n\t" \
  "ds_read_b64_tr_b16 a[184:185], v229 offset:768\n\t" \
  "v_max3_f32 v192, v96, v97, v98\n\t" \
  "v_mfma_f32_16x16x32_bf16 v[100:103], a[208:211], v[112:115], v[100:103]\n\t" \
  "ds_read_b64_tr_b16 a[186:187], v229 offset:4864\n\t" \
  "v_cvt_pk_bf16_f32 v156, v104, v105\n\t" \
  "v_cvt_pk_bf16_f32 v157, v106, v107\n\t" \
  "v_mfma_f32_16x16x32_bf16 v[104:107], a[224:227], v[112:115], v[188:191]\n\t" \
  "ds_read_b64_tr_b16 a[188:189], v229 offset:8960\n\t" \
  "v_cvt_pk_bf16_f32 v158, v108, v109\n\t" \
  "v_mfma_f32_16x16x32_bf16 v[104:107], a[228:231], v[116:119], v[104:107]\n\t" \
  "ds_read_b64_tr_b16 a[190:191], v229 offset:13056\n\t" \
  "v_cvt_pk_bf16_f32 v159, v110, v111\n\t" \
  "v_mfma_f32_16x16x32_bf16 v[104:107], a[232:235], v[120:123], v[104:107]\n\t" \
  "v_max3_f32 v203, v99, v100, v101\n\t" \
  "v_max3_f32 v192, v192, v102, v103\n\t" \
  "v_mfma_f32_16x16x32_bf16 v[104:107], a[236:239], v[124:127], v[104:107]\n\t" \
  "v_exp_f32 v0, v0\n\t" \
  "v_mfma_f32_16x16x32_bf16 v[108:111], a[252:255], v[124:127], v[188:191]\n\t" \
  "v_exp_f32 v1, v1\n\t" \
  "v_mfma_f32_16x16x32_bf16 v[108:111], a[248:251], v[120:123], v[108:111]\n\t" \
  "v_exp_f32 v2, v2\n\t" \
  "v_mfma_f32_16x16x32_bf16 v[108:111], a[244:247], v[116:119], v[108:111]\n\t" \
  "s_waitcnt vmcnt(0) lgkmcnt(0)\n\t" \
  "v_exp_f32 v3, v3\n\t" \
  "v_mfma_f32_16x16x32_bf16 v[108:111], a[240:243], v[112:115], v[108:111]\n\t" \
  "s_barrier\n\t" \
  "v_mfma_f32_16x16x32_bf16 a[0:3], a[128:131], v[128:131], 0\n\t" \
  "v_max3_f32 v203, v203, v104, v105\n\t" \
  "v_max3_f32 v192, v192, v106, v107\n\t" \
  "v_mfma_f32_16x16x32_bf16 a[0:3], a[132:135], v[132:135], a[0:3]\n\t" \
  "v_exp_f32 v4, v4\n\t" \
  "v_mfma_f32_16x16x32_bf16 a[4:7], a[136:139], v[128:131], 0\n\t" \
  "v_exp_f32 v5, v5\n\t" \
  "v_mfma_f32_16x16x32_bf16 a[4:7], a[140:143], v[132:135], a[4:7]\n\t" \
  "v_max3_f32 v203, v203, v108, v109\n\t" \
  "v_max3_f32 v192, v192, v110, v111\n\t" \
  "v_mfma_f32_16x16x32_bf16 a[8:11], a[144:147], v[128:131], 0\n\t" \
  "v_max_f32 v192, v192, v203\n\t" \
  "v_cmp_lt_f32 vcc, 0x42800000, v192\n\t" \
  "v_mfma_f32_16x16x32_bf16 a[8:11], a[148:151], v[132:135], a[8:11]\n\t" \
  "v_exp_f32 v6, v6\n\t" \
  "v_mfma_f32_16x16x32_bf16 a[12:15], a[152:155], v[128:131], 0\n\t" \
  "s_cbranch_vccnz .Lsf_c13\n\t" \
  ".Lsf_r13:\n\t" \
  "v_mfma_f32_16x16x32_bf16 a[12:15], a[156:159], v[132:135], a[12:15]\n\t" \
  "s_add_u32 m0, s38, 65536\n\t" \
  "v_exp_f32 v7, v7\n\t" \
  "v_mfma_f32_16x16x32_bf16 a[16:19], a[160:163], v[128:131], 0\n\t" \
  "s_add_u32 s96, s38, 49152\n\t" \
  "v_exp_f32 v8, v8\n\t" \
  "v_mfma_f32_16x16x32_bf16 a[16:19], a[164:167], v[132:135], a[16:19]\n\t" \
  "v_exp_f32 v9, v9\n\t" \
  "v_mfma_f32_16x16x32_bf16 a[20:23], a[168:171], v[128:131], 0\n\t" \
  "buffer_load_dwordx4 v224, s[60:63], s96 offen offset:0 lds\n\t" \
  "v_mfma_f32_16x16x32_bf16 a[20:23], a[172:175], v[132:135], a[20:23]\n\t" \
  "v_exp_f32 v10, v10\n\t" \
  "v_mfma_f32_16x16x32_bf16 a[24:27], a[176:179], v[128:131], 0\n\t" \
  "v_exp_f32 v11, v11\n\t" \
  "v_mfma_f32_16x16x32_bf16 a[24:27], a[180:183], v[132:135], a[24:27]\n\t" \
  "v_exp_f32 v12, v12\n\t" \
  "v_mfma_f32_16x16x32_bf16 a[28:31], a[184:187], v[128:131], 0\n\t" \
  "v_exp_f32 v13, v13\n\t" \
  "v_mfma_f32_16x16x32_bf16 a[28:31], a[188:191], v[132:135], a[28:31]\n\t" \
  "v_exp_f32 v14, v14\n\t" \
  "v_mfma_f32_16x16x32_bf16 v[160:163], v[216:219], v[128:131], 0\n\t" \
  "v_exp_f32 v15, v15\n\t" \
  "v_mfma_f32_16x16x32_bf16 v[160:163], v[216:219], v[132:135], v[160:163]\n\t" \
  "buffer_load_dwordx4 v225, s[60:63], s96 offen offset:1024 lds\n\t" \
  "v_mfma_f32_16x16x32_bf16 a[32:35], a[128:131], v[136:139], 0\n\t" \
  "ds_read_b128 a[192:195], v220 offset:0\n\t" \
  "v_cvt_pk_bf16_f32 v128, v0, v1\n\t" \
  "v_mfma_f32_16x16x32_bf16 a[32:35], a[132:135], v[140:143], a[32:35]\n\t" \
  "ds_read_b128 a[196:199], v221 offset:0\n\t" \
  "v_cvt_pk_bf16_f32 v129, v2, v3\n\t" \
  "v_mfma_f32_16x16x32_bf16 a[36:39], a[136:139], v[136:139], 0\n\t" \
  "v_exp_f32 v32, v32\n\t" \
  "v_mfma_f32_16x16x32_bf16 a[36:39], a[140:143], v[140:143], a[36:39]\n\t" \
  "ds_read_b128 a[200:203], v222 offset:0\n\t" \
  "v_cvt_pk_bf16_f32 v130, v4, v5\n\t" \
  "v_mfma_f32_16x16x32_bf16 a[40:43], a[144:147], v[136:139], 0\n\t" \
  "v_exp_f32 v33, v33\n\t" \
  "v_mfma_f32_16x16x32_bf16 a[40:43], a[148:151], v[140:143], a[40:43]\n\t" \
  "buffer_load_dwordx4 v226, s[60:63], s96 offen offset:2048 lds\n\t" \
  "v_mfma_f32_16x16x32_bf16 a[44:47], a[152:155], v[136:139], 0\n\t" \
  "ds_read_b128 a[204:207], v223 offset:0\n\t" \
  "v_cvt_pk_bf16_f32 v131, v6, v7\n\t" \
  "v_mfma_f32_16x16x32_bf16 a[44:47], a[156:159], v[140:143], a[44:47]\n\t" \
  "v_exp_f32 v34, v34\n\t" \
  "v_mfma_f32_16x16x32_bf16 a[48:51], a[160:163], v[136:139], 0\n\t" \
  "v_exp_f32 v35, v35\n\t" \
  "v_mfma_f32_16x16x32_bf16 a[48:51], a[164:167], v[140:143], a[48:51]\n\t" \
  "ds_read_b128 a[208:211], v220 offset:4096\n\t" \
  "v_cvt_pk_bf16_f32 v132, v8, v9\n\t" \
  "v_mfma_f32_16x16x32_bf16 a[52:55], a[168:171], v[136:139], 0\n\t" \
  "v_exp_f32 v36, v36\n\t" \
  "v_mfma_f32_16x16x32_bf16 a[52:55], a[172:175], v[140:143], a[52:55]\n\t" \
  "buffer_load_dwordx4 v227, s[60:63], s96 offen offset:3072 lds\n\t" \
  "v_mfma_f32_16x16x32_bf16 a[56:59], a[176:179], v[136:139], 0\n\t" \
  "ds_read_b128 a[212:215], v221 offset:4096\n\t" \
  "v_cvt_pk_bf16_f32 v133, v10, v11\n\t" \
  "v_mfma_f32_16x16x32_bf16 a[56:59], a[180:183], v[140:143], a[56:59]\n\t" \
  "s_add_u32 m0, s38, 81920\n\t" \
  "v_exp_f32 v37, v37\n\t" \
  "v_mfma_f32_16x16x32_bf16 a[60:63], a[184:187], v[136:139], 0\n\t" \
  "s_add_u32 s96, s38, 32768\n\t" \
  "v_exp_f32 v38, v38\n\t" \
  "v_mfma_f32_16x16x32_bf16 a[60:63], a[188:191], v[140:143], a[60:63]\n\t" \
  "ds_read_b128 a[216:219], v222 offset:4096\n\t" \
  "v_cvt_pk_bf16_f32 v134, v12, v13\n\t" \
  "v_mfma_f32_16x16x32_bf16 v[164:167], v[216:219], v[136:139], 0\n\t" \
  "v_exp_f32 v39, v39\n\t" \
  "v_mfma_f32_16x16x32_bf16 v[164:167], v[216:219], v[140:143], v[164:167]\n\t" \
  "buffer_load_dwordx4 v230, s[64:67], s96 offen offset:0 lds\n\t" \
  "v_mfma_f32_16x16x32_bf16 a[64:67], a[128:131], v[144:147], 0\n\t" \
  "ds_read_b128 a[220:223], v223 offset:4096\n\t" \
  "v_cvt_pk_bf16_f32 v135, v14, v15\n\t" \
  "v_mfma_f32_16x16x32_bf16 a[64:67], a[132:135], v[148:151], a[64:67]\n\t" \
  "v_exp_f32 v40, v40\n\t" \
  "v_mfma_f32_16x16x32_bf16 a[68:71], a[136:139], v[144:147], 0\n\t" \
  "ds_read_b128 a[224:227], v220 offset:8192\n\t" \
  "v_cvt_pk_bf16_f32 v136, v32, v33\n\t" \
  "v_mfma_f32_16x16x32_bf16 a[68:71], a[140:143], v[148:151], a[68:71]\n\t" \
  "v_exp_f32 v41, v41\n\t" \
  "v_mfma_f32_16x16x32_bf16 a[72:75], a[144:147], v[144:147], 0\n\t" \
  "v_exp_f32 v42, v42\n\t" \
  "v_mfma_f32_16x16x32_bf16 a[72:75], a[148:151], v[148:151], a[72:75]\n\t" \
  "ds_read_b128 a[228:231], v221 offset:8192\n\t" \
  "v_cvt_pk_bf16_f32 v137, v34, v35\n\t" \
  "v_mfma_f32_16x16x32_bf16 a[76:79], a[152:155], v[144:147], 0\n\t" \
  "buffer_load_dwordx4 v231, s[64:67], s96 offen offset:1024 lds\n\t" \
  "v_mfma_f32_16x16x32_bf16 a[76:79], a[156:159], v[148:151], a[76:79]\n\t" \
  "v_exp_f32 v43, v43\n\t" \
  "v_mfma_f32_16x16x32_bf16 a[80:83], a[160:163], v[144:147], 0\n\t" \
  "ds_read_b128 a[232:235], v222 offset:8192\n\t" \
  "v_cvt_pk_bf16_f32 v138, v36, v37\n\t" \
  "v_mfma_f32_16x16x32_bf16 a[80:83], a[164:167], v[148:151], a[80:83]\n\t" \
  "v_exp_f32 v44, v44\n\t" \
  "v_mfma_f32_16x16x32_bf16 a[84:87], a[168:171], v[144:147], 0\n\t" \
  "v_exp_f32 v45, v45\n\t" \
  "v_mfma_f32_16x16x32_bf16 a[84:87], a[172:175], v[148:151], a[84:87]\n\t" \
  "ds_read_b128 a[236:239], v223 offset:8192\n\t" \
  "v_cvt_pk_bf16_f32 v139, v38, v39\n\t" \
  "v_mfma_f32_16x16x32_bf16 a[88:91], a[176:179], v[144:147], 0\n\t" \
  "buffer_load_dwordx4 v230, s[64:67], s96 offen offset:2048 lds\n\t" \
  "v_mfma_f32_16x16x32_bf16 a[88:91], a[180:183], v[148:151], a[88:91]\n\t" \
  "v_exp_f32 v46, v46\n\t" \
  "v_mfma_f32_16x16x32_bf16 a[92:95], a[184:187], v[144:147], 0\n\t" \
  "ds_read_b128 a[240:243], v220 offset:12288\n\t" \
  "v_cvt_pk_bf16_f32 v140, v40, v41\n\t" \
  "v_mfma_f32_16x16x32_bf16 a[92:95], a[188:191], v[148:151], a[92:95]\n\t" \
  "v_exp_f32 v47, v47\n\t" \
  "v_mfma_f32_16x16x32_bf16 v[168:171], v[216:219], v[144:147], 0\n\t" \
  "v_exp_f32 v64, v64\n\t" \
  "v_mfma_f32_16x16x32_bf16 v[168:171], v[216:219], v[148:151], v[168:171]\n\t" \
  "ds_read_b128 a[244:247], v221 offset:12288\n\t" \
  "v_cvt_pk_bf16_f32 v141, v42, v43\n\t" \
  "v_mfma_f32_16x16x32_bf16 a[96:99], a[128:131], v[152:155], 0\n\t" \
  "buffer_load_dwordx4 v231, s[64:67], s96 offen offset:3072 lds\n\t" \
  "v_mfma_f32_16x16x32_bf16 a[96:99], a[132:135], v[156:159], a[96:99]\n\t" \
  "v_exp_f32 v65, v65\n\t" \
  "v_mfma_f32_16x16x32_bf16 a[100:103], a[136:139], v[152:155], 0\n\t" \
  "ds_read_b128 a[248:251], v222 offset:12288\n\t" \
  "v_cvt_pk_bf16_f32 v142, v44, v45\n\t" \
  "v_mfma_f32_16x16x32_bf16 a[100:103], a[140:143], v[156:159], a[100:103]\n\t" \
  "v_exp_f32 v66, v66\n\t" \
  "v_mfma_f32_16x16x32_bf16 a[104:107], a[144:147], v[152:155], 0\n\t" \
  "v_exp_f32 v67, v67\n\t" \
  "v_mfma_f32_16x16x32_bf16 a[104:107], a[148:151], v[156:159], a[104:107]\n\t" \
  "ds_read_b128 a[252:255], v223 offset:12288\n\t" \
  "v_cvt_pk_bf16_f32 v143, v46, v47\n\t" \
  "v_mfma_f32_16x16x32_bf16 a[108:111], a[152:155], v[152:155], 0\n\t" \
  "v_exp_f32 v68, v68\n\t" \
  "v_mfma_f32_16x16x32_bf16 a[108:111], a[156:159], v[156:159], a[108:111]\n\t" \
  "v_exp_f32 v69, v69\n\t" \
  "v_mfma_f32_16x16x32_bf16 a[112:115], a[160:163], v[152:155], 0\n\t" \
  "v_exp_f32 v70, v70\n\t" \
  "v_mfma_f32_16x16x32_bf16 a[112:115], a[164:167], v[156:159], a[112:115]\n\t" \
  "v_exp_f32 v71, v71\n\t" \
  "v_mfma_f32_16x16x32_bf16 a[116:119], a[168:171], v[152:155], 0\n\t" \
  "v_exp_f32 v72, v72\n\t" \
  "v_mfma_f32_16x16x32_bf16 a[116:119], a[172:175], v[156:159], a[116:119]\n\t" \
  "v_exp_f32 v73, v73\n\t" \
  "v_mfma_f32_16x16x32_bf16 a[120:123], a[176:179], v[152:155], 0\n\t" \
  "v_exp_f32 v74, v74\n\t" \
  "v_mfma_f32_16x16x32_bf16 a[120:123], a[180:183], v[156:159], a[120:123]\n\t" \
  "v_exp_f32 v75, v75\n\t" \
  "v_mfma_f32_16x16x32_bf16 a[124:127], a[184:187], v[152:155], 0\n\t" \
  "v_exp_f32 v76, v76\n\t" \
  "v_mfma_f32_16x16x32_bf16 a[124:127], a[188:191], v[156:159], a[124:127]\n\t" \
  "v_exp_f32 v77, v77\n\t" \
  "v_mfma_f32_16x16x32_bf16 v[172:175], v[216:219], v[152:155], 0\n\t" \
  "v_exp_f32 v78, v78\n\t" \
  "v_mfma_f32_16x16x32_bf16 v[172:175], v[216:219], v[156:159], v[172:175]\n\t" \
  "s_waitcnt lgkmcnt(12)\n\t" \
  "s_cmp_lg_u32 s99, 0\n\t" \
  "s_cbranch_scc1 .Lsf_p1\n\t" \
  ".Lsf_q1:\n\t" \
  "v_mfma_f32_16x16x32_bf16 v[0:3], a[192:195], v[16:19], v[176:179]\n\t" \
  "ds_read_b64_tr_b16 a[128:129], v210 offset:0\n\t" \
  "v_cvt_pk_bf16_f32 v144, v64, v65\n\t" \
  "v_mfma_f32_16x16x32_bf16 v[0:3], a[196:199], v[20:23], v[0:3]\n\t" \
  "v_exp_f32 v79, v79\n\t" \
  "v_mfma_f32_16x16x32_bf16 v[0:3], a[200:203], v[24:27], v[0:3]\n\t" \
  "ds_read_b64_tr_b16 a[130:131], v210 offset:4096\n\t" \
  "v_cvt_pk_bf16_f32 v145, v66, v67\n\t" \
  "v_mfma_f32_16x16x32_bf16 v[0:3], a[204:207], v[28:31], v[0:3]\n\t" \
  "s_waitcnt lgkmcnt(10)\n\t" \
  "v_exp_f32 v96, v96\n\t" \
  "v_mfma_f32_16x16x32_bf16 v[4:7], a[220:223], v[28:31], v[176:179]\n\t" \
  "ds_read_b64_tr_b16 a[132:133], v210 offset:8192\n\t" \
  "v_cvt_pk_bf16_f32 v146, v68, v69\n\t" \
  "v_mfma_f32_16x16x32_bf16 v[4:7], a[216:219], v[24:27], v[4:7]\n\t" \
  "v_exp_f32 v97, v97\n\t" \
  "v_mfma_f32_16x16x32_bf16 v[4:7], a[212:215], v[20:23], v[4:7]\n\t" \
  "ds_read_b64_tr_b16 a[134:135], v210 offset:12288\n\t" \
  "v_max3_f32 v192, v0, v1, v2\n\t" \
  "v_mfma_f32_16x16x32_bf16 v[4:7], a[208:211], v[16:19], v[4:7]\n\t" \
  "s_waitcnt lgkmcnt(8)\n\t" \
  "ds_read_b64_tr_b16 a[136:137], v211 offset:0\n\t" \
  "v_cvt_pk_bf16_f32 v147, v70, v71\n\t" \
  "v_mfma_f32_16x16x32_bf16 v[8:11], a[224:227], v[16:19], v[176:179]\n\t" \
  "v_exp_f32 v98, v98\n\t" \
  "v_mfma_f32_16x16x32_bf16 v[8:11], a[228:231], v[20:23], v[8:11]\n\t" \
  "ds_read_b64_tr_b16 a[138:139], v211 offset:4096\n\t" \
  "v_cvt_pk_bf16_f32 v148, v72, v73\n\t" \
  "v_mfma_f32_16x16x32_bf16 v[8:11], a[232:235], v[24:27], v[8:11]\n\t" \
  "v_max3_f32 v203, v3, v4, v5\n\t" \
  "v_max3_f32 v192, v192, v6, v7\n\t" \
  "v_mfma_f32_16x16x32_bf16 v[8:11], a[236:239], v[28:31], v[8:11]\n\t" \
  "s_waitcnt lgkmcnt(6)\n\t" \
  "ds_read_b64_tr_b16 a[140:141], v211 offset:8192\n\t" \
  "v_cvt_pk_bf16_f32 v149, v74, v75\n\t" \
  "v_mfma_f32_16x16x32_bf16 v[12:15], a[252:255], v[28:31], v[176:179]\n\t" \
  "v_exp_f32 v99, v99\n\t" \
  "v_mfma_f32_16x16x32_bf16 v[12:15], a[248:251], v[24:27], v[12:15]\n\t" \
  "ds_read_b64_tr_b16 a[142:143], v211 offset:12288\n\t" \
  "v_cvt_pk_bf16_f32 v150, v76, v77\n\t" \
  "v_mfma_f32_16x16x32_bf16 v[12:15], a[244:247], v[20:23], v[12:15]\n\t" \
  "v_max3_f32 v203, v203, v8, v9\n\t" \
  "v_max3_f32 v192, v192, v10, v11\n\t" \
  "v_mfma_f32_16x16x32_bf16 v[12:15], a[240:243], v[16:19], v[12:15]\n\t" \
  "ds_read_b64_tr_b16 a[144:145], v210 offset:256\n\t" \
  "v_cvt_pk_bf16_f32 v151, v78, v79\n\t" \
  "v_mfma_f32_16x16x32_bf16 v[32:35], a[192:195], v[48:51], v[180:183]\n\t" \
  "ds_read_b64_tr_b16 a[146:147], v210 offset:4352\n\t" \
  "v_cvt_pk_bf16_f32 v152, v96, v97\n\t" \
  "v_mfma_f32_16x16x32_bf16 v[32:35], a[196:199], v[52:55], v[32:35]\n\t" \
  "v_exp_f32 v100, v100\n\t" \
  "v_mfma_f32_16x16x32_bf16 v[32:35], a[200:203], v[56:59], v[32:35]\n\t" \
  "v_max3_f32 v203, v203, v12, v13\n\t" \
  "v_max3_f32 v192, v192, v14, v15\n\t" \
  "v_mfma_f32_16x16x32_bf16 v[32:35], a[204:207], v[60:63], v[32:35]\n\t" \
  "v_max_f32 v192, v192, v203\n\t" \
  "v_cmp_lt_f32 vcc, 0x42800000, v192\n\t" \
  "v_mfma_f32_16x16x32_bf16 v[36:39], a[220:223], v[60:63], v[180:183]\n\t" \
  "ds_read_b64_tr_b16 a[148:149], v210 offset:8448\n\t" \
  "v_cvt_pk_bf16_f32 v153, v98, v99\n\t" \
  "v_mfma_f32_16x16x32_bf16 v[36:39], a[216:219], v[56:59], v[36:39]\n\t" \
  "s_cbranch_vccnz .Lsf_c20\n\t" \
  ".Lsf_r20:\n\t" \
  "v_mfma_f32_16x16x32_bf16 v[36:39], a[212:215], v[52:55], v[36:39]\n\t" \
  "ds_read_b64_tr_b16 a[150:151], v210 offset:12544\n\t" \
  "v_max3_f32 v192, v32, v33, v34\n\t" \
  "v_mfma_f32_16x16x32_bf16 v[36:39], a[208:211], v[48:51], v[36:39]\n\t" \
  "v_exp_f32 v101, v101\n\t" \
  "v_mfma_f32_16x16x32_bf16 v[40:43], a[224:227], v[48:51], v[180:183]\n\t" \
  "ds_read_b64_tr_b16 a[152:153], v211 offset:256\n\t" \
  "v_cvt_pk_bf16_f32 v154, v100, v101\n\t" \
  "v_mfma_f32_16x16x32_bf16 v[40:43], a[228:231], v[52:55], v[40:43]\n\t" \
  "v_exp_f32 v102, v102\n\t" \
  "v_mfma_f32_16x16x32_bf16 v[40:43], a[232:235], v[56:59], v[40:43]\n\t" \
  "ds_read_b64_tr_b16 a[154:155], v211 offset:4352\n\t" \
  "v_max3_f32 v203, v35, v36, v37\n\t" \
  "v_mfma_f32_16x16x32_bf16 v[40:43], a[236:239], v[60:63], v[40:43]\n\t" \
  "ds_read_b64_tr_b16 a[156:157], v211 offset:8448\n\t" \
  "v_max3_f32 v192, v192, v38, v39\n\t" \
  "v_mfma_f32_16x16x32_bf16 v[44:47], a[252:255], v[60:63], v[180:183]\n\t" \
  "v_exp_f32 v103, v103\n\t" \
  "v_mfma_f32_16x16x32_bf16 v[44:47], a[248:251], v[56:59], v[44:47]\n\t" \
  "ds_read_b64_tr_b16 a[158:159], v211 offset:12544\n\t" \
  "v_cvt_pk_bf16_f32 v155, v102, v103\n\t" \
  "v_mfma_f32_16x16x32_bf16 v[44:47], a[244:247], v[52:55], v[44:47]\n\t" \
  "v_max3_f32 v203, v203, v40, v41\n\t" \
  "v_max3_f32 v192, v192, v42, v43\n\t" \
  "v_mfma_f32_16x16x32_bf16 v[44:47], a[240:243], v[48:51], v[44:47]\n\t" \
  "v_exp_f32 v104, v104\n\t" \
  "v_mfma_f32_16x16x32_bf16 v[64:67], a[192:195], v[80:83], v[184:187]\n\t" \
  "v_exp_f32 v105, v105\n\t" \
  "v_mfma_f32_16x16x32_bf16 v[64:67], a[196:199], v[84:87], v[64:67]\n\t" \
  "ds_read_b64_tr_b16 a[160:161], v210 offset:512\n\t" \
  "v_cvt_pk_bf16_f32 v156, v104, v105\n\t" \
  "v_mfma_f32_16x16x32_bf16 v[64:67], a[200:203], v[88:91], v[64:67]\n\t" \
  "ds_read_b64_tr_b16 a[162:163], v210 offset:4608\n\t" \
  "v_max3_f32 v203, v203, v44, v45\n\t" \
  "v_mfma_f32_16x16x32_bf16 v[64:67], a[204:207], v[92:95], v[64:67]\n\t" \
  "v_max3_f32 v192, v192, v46, v47\n\t" \
  "v_max_f32 v192, v192, v203\n\t" \
  "v_mfma_f32_16x16x32_bf16 v[68:71], a[220:223], v[92:95], v[184:187]\n\t" \
  "ds_read_b64_tr_b16 a[164:165], v210 offset:8704\n\t" \
  "v_cmp_lt_f32 vcc, 0x42800000, v192\n\t" \
  "v_mfma_f32_16x16x32_bf16 v[68:71], a[216:219], v[88:91], v[68:71]\n\t" \
  "v_exp_f32 v106, v106\n\t" \
  "v_mfma_f32_16x16x32_bf16 v[68:71], a[212:215], v[84:87], v[68:71]\n\t" \
  "s_cbranch_vccnz .Lsf_c21\n\t" \
  ".Lsf_r21:\n\t" \
  "v_mfma_f32_16x16x32_bf16 v[68:71], a[208:211], v[80:83], v[68:71]\n\t" \
  "ds_read_b64_tr_b16 a[166:167], v210 offset:12800\n\t" \
  "v_max3_f32 v192, v64, v65, v66\n\t" \
  "v_mfma_f32_16x16x32_bf16 v[72:75], a[224:227], v[80:83], v[184:187]\n\t" \
  "v_exp_f32 v107, v107\n\t" \
  "v_mfma_f32_16x16x32_bf16 v[72:75], a[228:231], v[84:87], v[72:75]\n\t" \
  "ds_read_b64_tr_b16 a[168:169], v211 offset:512\n\t" \
  "v_cvt_pk_bf16_f32 v157, v106, v107\n\t" \
  "v_mfma_f32_16x16x32_bf16 v[72:75], a[232:235], v[88:91], v[72:75]\n\t" \
  "v_max3_f32 v203, v67, v68, v69\n\t" \
  "v_max3_f32 v192, v192, v70, v71\n\t" \
  "v_mfma_f32_16x16x32_bf16 v[72:75], a[236:239], v[92:95], v[72:75]\n\t" \
  "v_exp_f32 v108, v108\n\t" \
  "v_mfma_f32_16x16x32_bf16 v[76:79], a[252:255], v[92:95], v[184:187]\n\t" \
  "v_exp_f32 v109, v109\n\t" \
  "v_mfma_f32_16x16x32_bf16 v[76:79], a[248:251], v[88:91], v[76:79]\n\t" \
  "ds_read_b64_tr_b16 a[170:171], v211 offset:4608\n\t" \
  "v_cvt_pk_bf16_f32 v158, v108, v109\n\t" \
  "v_mfma_f32_16x16x32_bf16 v[76:79], a[244:247], v[84:87], v[76:79]\n\t" \
  "ds_read_b64_tr_b16 a[172:173], v211 offset:8704\n\t" \
  "v_max3_f32 v203, v203, v72, v73\n\t" \
  "v_mfma_f32_16x16x32_bf16 v[76:79], a[240:243], v[80:83], v[76:79]\n\t" \
  "ds_read_b64_tr_b16 a[174:175], v211 offset:12800\n\t" \
  "v_max3_f32 v192, v192, v74, v75\n\t" \
  "v_mfma_f32_16x16x32_bf16 v[96:99], a[192:195], v[112:115], v[188:191]\n\t" \
  "v_exp_f32 v110, v110\n\t" \
  "v_mfma_f32_16x16x32_bf16 v[96:99], a[196:199], v[116:119], v[96:99]\n\t" \
  "v_exp_f32 v111, v111\n\t" \
  "v_mfma_f32_16x16x32_bf16 v[96:99], a[200:203], v[120:123], v[96:99]\n\t" \
  "ds_read_b64_tr_b16 a[176:177], v210 offset:768\n\t" \
  "v_max3_f32 v203, v203, v76, v77\n\t" \
  "v_mfma_f32_16x16x32_bf16 v[96:99], a[204:207], v[124:127], v[96:99]\n\t" \
  "v_max3_f32 v192, v192, v78, v79\n\t" \
  "v_max_f32 v192, v192, v203\n\t" \
  "v_mfma_f32_16x16x32_bf16 v[100:103], a[220:223], v[124:127], v[188:191]\n\t" \
  "ds_read_b64_tr_b16 a[178:179], v210 offset:4864\n\t" \
  "v_cmp_lt_f32 vcc, 0x42800000, v192\n\t" \
  "v_mfma_f32_16x16x32_bf16 v[100:103], a[216:219], v[120:123], v[100:103]\n\t" \
  "ds_read_b64_tr_b16 a[180:181], v210 offset:8960\n\t" \
  "v_cvt_pk_bf16_f32 v159, v110, v111\n\t" \
  "v_mfma_f32_16x16x32_bf16 v[100:103], a[212:215], v[116:119], v[100:103]\n\t" \
  "s_cbranch_vccnz .Lsf_c22\n\t" \
  ".Lsf_r22:\n\t" \
  "v_mfma_f32_16x16x32_bf16 v[100:103], a[208:211], v[112:115], v[100:103]\n\t" \
  "ds_read_b64_tr_b16 a[182:183], v210 offset:13056\n\t" \
  "v_max3_f32 v192, v96, v97, v98\n\t" \
  "v_mfma_f32_16x16x32_bf16 v[104:107], a[224:227], v[112:115], v[188:191]\n\t" \
  "ds_read_b64_tr_b16 a[184:185], v211 offset:768\n\t" \
  "v_mfma_f32_16x16x32_bf16 v[104:107], a[228:231], v[116:119], v[104:107]\n\t" \
  "ds_read_b64_tr_b16 a[186:187], v211 offset:4864\n\t" \
  "v_mfma_f32_16x16x32_bf16 v[104:107], a[232:235], v[120:123], v[104:107]\n\t" \
  "ds_read_b64_tr_b16 a[188:189], v211 offset:8960\n\t" \
  "v_max3_f32 v203, v99, v100, v101\n\t" \
  "v_mfma_f32_16x16x32_bf16 v[104:107], a[236:239], v[124:127], v[104:107]\n\t" \
  "ds_read_b64_tr_b16 a[190:191], v211 offset:13056\n\t" \
  "v_max3_f32 v192, v192, v102, v103\n\t" \
  "v_mfma_f32_16x16x32_bf16 v[108:111], a[252:255], v[124:127], v[188:191]\n\t" \
  "v_exp_f32 v0, v0\n\t" \
  "v_mfma_f32_16x16x32_bf16 v[108:111], a[248:251], v[120:123], v[108:111]\n\t" \
  "v_exp_f32 v1, v1\n\t" \
  "v_mfma_f32_16x16x32_bf16 v[108:111], a[244:247], v[116:119], v[108:111]\n\t" \
  "s_waitcnt vmcnt(0) lgkmcnt(0)\n\t" \
  "v_max3_f32 v203, v203, v104, v105\n\t" \
  "v_max3_f32 v192, v192, v106, v107\n\t" \
  "v_mfma_f32_16x16x32_bf16 v[108:111], a[240:243], v[112:115], v[108:111]\n\t" \
  "s_barrier\n\t" \
  "v_mfma_f32_16x16x32_bf16 a[0:3], a[128:131], v[128:131], a[0:3]\n\t" \
  "v_exp_f32 v2, v2\n\t" \
  "v_mfma_f32_16x16x32_bf16 a[0:3], a[132:135], v[132:135], a[0:3]\n\t" \
  "v_exp_f32 v3, v3\n\t" \
  "v_mfma_f32_16x16x32_bf16 a[4:7], a[136:139], v[128:131], a[4:7]\n\t" \
  "v_exp_f32 v4, v4\n\t" \
  "v_mfma_f32_16x16x32_bf16 a[4:7], a[140:143], v[132:135], a[4:7]\n\t" \
  "v_max3_f32 v203, v203, v108, v109\n\t" \
  "v_max3_f32 v192, v192, v110, v111\n\t" \
  "v_mfma_f32_16x16x32_bf16 a[8:11], a[144:147], v[128:131], a[8:11]\n\t" \
  "v_max_f32 v192, v192, v203\n\t" \
  "v_cmp_lt_f32 vcc, 0x42800000, v192\n\t" \
  "v_mfma_f32_16x16x32_bf16 a[8:11], a[148:151], v[132:135], a[8:11]\n\t" \
  "v_exp_f32 v5, v5\n\t" \
  "v_mfma_f32_16x16x32_bf16 a[12:15], a[152:155], v[128:131], a[12:15]\n\t" \
  "s_cbranch_vccnz .Lsf_c23\n\t" \
  ".Lsf_r23:\n\t" \
  "v_mfma_f32_16x16x32_bf16 a[12:15], a[156:159], v[132:135], a[12:15]\n\t" \
  "s_add_u32 m0, s38, 49152\n\t" \
  "v_exp_f32 v6, v6\n\t" \
  "v_mfma_f32_16x16x32_bf16 a[16:19], a[160:163], v[128:131], a[16:19]\n\t" \
  "s_add_u32 s96, s38, 65536\n\t" \
  "v_exp_f32 v7, v7\n\t" \
  "v_mfma_f32_16x16x32_bf16 a[16:19], a[164:167], v[132:135], a[16:19]\n\t" \
  "v_exp_f32 v8, v8\n\t" \
  "v_mfma_f32_16x16x32_bf16 a[20:23], a[168:171], v[128:131], a[20:23]\n\t" \
  "buffer_load_dwordx4 v224, s[60:63], s96 offen offset:0 lds\n\t" \
  "v_mfma_f32_16x16x32_bf16 a[20:23], a[172:175], v[132:135], a[20:23]\n\t" \
  "v_exp_f32 v9, v9\n\t" \
  "v_mfma_f32_16x16x32_bf16 a[24:27], a[176:179], v[128:131], a[24:27]\n\t" \
  "v_exp_f32 v10, v10\n\t" \
  "v_mfma_f32_16x16x32_bf16 a[24:27], a[180:183], v[132:135], a[24:27]\n\t" \
  "v_exp_f32 v11, v11\n\t" \
  "v_mfma_f32_16x16x32_bf16 a[28:31], a[184:187], v[128:131], a[28:31]\n\t" \
  "v_exp_f32 v12, v12\n\t" \
  "v_mfma_f32_16x16x32_bf16 a[28:31], a[188:191], v[132:135], a[28:31]\n\t" \
  "v_exp_f32 v13, v13\n\t" \
  "v_mfma_f32_16x16x32_bf16 v[160:163], v[216:219], v[128:131], v[160:163]\n\t" \
  "v_exp_f32 v14, v14\n\t" \
  "v_mfma_f32_16x16x32_bf16 v[160:163], v[216:219], v[132:135], v[160:163]\n\t" \
  "buffer_load_dwordx4 v225, s[60:63], s96 offen offset:1024 lds\n\t" \
  "v_mfma_f32_16x16x32_bf16 a[32:35], a[128:131], v[136:139], a[32:35]\n\t" \
  "ds_read_b128 a[192:195], v220 offset:16384\n\t" \
  "v_cvt_pk_bf16_f32 v128, v0, v1\n\t" \
  "v_mfma_f32_16x16x32_bf16 a[32:35], a[132:135], v[140:143], a[32:35]\n\t" \
  "ds_read_b128 a[196:199], v221 offset:16384\n\t" \
  "v_cvt_pk_bf16_f32 v129, v2, v3\n\t" \
  "v_mfma_f32_16x16x32_bf16 a[36:39], a[136:139], v[136:139], a[36:39]\n\t" \
  "v_exp_f32 v15, v15\n\t" \
  "v_mfma_f32_16x16x32_bf16 a[36:39], a[140:143], v[140:143], a[36:39]\n\t" \
  "v_exp_f32 v32, v32\n\t" \
  "v_mfma_f32_16x16x32_bf16 a[40:43], a[144:147], v[136:139], a[40:43]\n\t" \
  "ds_read_b128 a[200:203], v222 offset:16384\n\t" \
  "v_cvt_pk_bf16_f32 v130, v4, v5\n\t" \
  "v_mfma_f32_16x16x32_bf16 a[40:43], a[148:151], v[140:143], a[40:43]\n\t" \
  "buffer_load_dwordx4 v226, s[60:63], s96 offen offset:2048 lds\n\t" \
  "v_mfma_f32_16x16x32_bf16 a[44:47], a[152:155], v[136:139], a[44:47]\n\t" \
  "ds_read_b128 a[204:207], v223 offset:16384\n\t" \
  "v_cvt_pk_bf16_f32 v131, v6, v7\n\t" \
  "v_mfma_f32_16x16x32_bf16 a[44:47], a[156:159], v[140:143], a[44:47]\n\t" \
  "v_exp_f32 v33, v33\n\t" \
  "v_mfma_f32_16x16x32_bf16 a[48:51], a[160:163], v[136:139], a[48:51]\n\t" \
  "v_exp_f32 v34, v34\n\t" \
  "v_mfma_f32_16x16x32_bf16 a[48:51], a[164:167], v[140:143], a[48:51]\n\t" \
  "ds_read_b128 a[208:211], v220 offset:20480\n\t" \
  "v_cvt_pk_bf16_f32 v132, v8, v9\n\t" \
  "v_mfma_f32_16x16x32_bf16 a[52:55], a[168:171], v[136:139], a[52:55]\n\t" \
  "v_exp_f32 v35, v35\n\t" \
  "v_mfma_f32_16x16x32_bf16 a[52:55], a[172:175], v[140:143], a[52:55]\n\t" \
  "buffer_load_dwordx4 v227, s[60:63], s96 offen offset:3072 lds\n\t" \
  "v_mfma_f32_16x16x32_bf16 a[56:59], a[176:179], v[136:139], a[56:59]\n\t" \
  "ds_read_b128 a[212:215], v221 offset:20480\n\t" \
  "v_cvt_pk_bf16_f32 v133, v10, v11\n\t" \
  "v_mfma_f32_16x16x32_bf16 a[56:59], a[180:183], v[140:143], a[56:59]\n\t" \
  "s_add_u32 m0, s38, 147456\n\t" \
  "v_exp_f32 v36, v36\n\t" \
  "v_mfma_f32_16x16x32_bf16 a[60:63], a[184:187], v[136:139], a[60:63]\n\t" \
  "s_add_u32 s96, s38, 49152\n\t" \
  "v_exp_f32 v37, v37\n\t" \
  "v_mfma_f32_16x16x32_bf16 a[60:63], a[188:191], v[140:143], a[60:63]\n\t" \
  "ds_read_b128 a[216:219], v222 offset:20480\n\t" \
  "v_cvt_pk_bf16_f32 v134, v12, v13\n\t" \
  "v_mfma_f32_16x16x32_bf16 v[164:167], v[216:219], v[136:139], v[164:167]\n\t" \
  "v_exp_f32 v38, v38\n\t" \
  "v_mfma_f32_16x16x32_bf16 v[164:167], v[216:219], v[140:143], v[164:167]\n\t" \
  "buffer_load_dwordx4 v230, s[64:67], s96 offen offset:0 lds\n\t" \
  "v_mfma_f32_16x16x32_bf16 a[64:67], a[128:131], v[144:147], a[64:67]\n\t" \
  "ds_read_b128 a[220:223], v223 offset:20480\n\t" \
  "v_cvt_pk_bf16_f32 v135, v14, v15\n\t" \
  "v_mfma_f32_16x16x32_bf16 a[64:67], a[132:135], v[148:151], a[64:67]\n\t" \
  "v_exp_f32 v39, v39\n\t" \
  "v_mfma_f32_16x16x32_bf16 a[68:71], a[136:139], v[144:147], a[68:71]\n\t" \
  "v_exp_f32 v40, v40\n\t" \
  "v_mfma_f32_16x16x32_bf16 a[68:71], a[140:143], v[148:151], a[68:71]\n\t" \
  "ds_read_b128 a[224:227], v220 offset:24576\n\t" \
  "v_cvt_pk_bf16_f32 v136, v32, v33\n\t" \
  "v_mfma_f32_16x16x32_bf16 a[72:75], a[144:147], v[144:147], a[72:75]\n\t" \
  "v_exp_f32 v41, v41\n\t" \
  "v_mfma_f32_16x16x32_bf16 a[72:75], a[148:151], v[148:151], a[72:75]\n\t" \
  "ds_read_b128 a[228:231], v221 offset:24576\n\t" \
  "v_cvt_pk_bf16_f32 v137, v34, v35\n\t" \
  "v_mfma_f32_16x16x32_bf16 a[76:79], a[152:155], v[144:147], a[76:79]\n\t" \
  "buffer_load_dwordx4 v231, s[64:67], s96 offen offset:1024 lds\n\t" \
  "v_mfma_f32_16x16x32_bf16 a[76:79], a[156:159], v[148:151], a[76:79]\n\t" \
  "v_exp_f32 v42, v42\n\t" \
  "v_mfma_f32_16x16x32_bf16 a[80:83], a[160:163], v[144:147], a[80:83]\n\t" \
  "ds_read_b128 a[232:235], v222 offset:24576\n\t" \
  "v_cvt_pk_bf16_f32 v138, v36, v37\n\t" \
  "v_mfma_f32_16x16x32_bf16 a[80:83], a[164:167], v[148:151], a[80:83]\n\t" \
  "v_exp_f32 v43, v43\n\t" \
  "v_mfma_f32_16x16x32_bf16 a[84:87], a[168:171], v[144:147], a[84:87]\n\t" \
  "v_exp_f32 v44, v44\n\t" \
  "v_mfma_f32_16x16x32_bf16 a[84:87], a[172:175], v[148:151], a[84:87]\n\t" \
  "ds_read_b128 a[236:239], v223 offset:24576\n\t" \
  "v_cvt_pk_bf16_f32 v139, v38, v39\n\t" \
  "v_mfma_f32_16x16x32_bf16 a[88:91], a[176:179], v[144:147], a[88:91]\n\t" \
  "buffer_load_dwordx4 v230, s[64:67], s96 offen offset:2048 lds\n\t" \
  "v_mfma_f32_16x16x32_bf16 a[88:91], a[180:183], v[148:151], a[88:91]\n\t" \
  "v_exp_f32 v45, v45\n\t" \
  "v_mfma_f32_16x16x32_bf16 a[92:95], a[184:187], v[144:147], a[92:95]\n\t" \
  "ds_read_b128 a[240:243], v220 offset:28672\n\t" \
  "v_cvt_pk_bf16_f32 v140, v40, v41\n\t" \
  "v_mfma_f32_16x16x32_bf16 a[92:95], a[188:191], v[148:151], a[92:95]\n\t" \
  "v_exp_f32 v46, v46\n\t" \
  "v_mfma_f32_16x16x32_bf16 v[168:171], v[216:219], v[144:147], v[168:171]\n\t" \
  "v_exp_f32 v47, v47\n\t" \
  "v_mfma_f32_16x16x32_bf16 v[168:171], v[216:219], v[148:151], v[168:171]\n\t" \
  "ds_read_b128 a[244:247], v221 offset:28672\n\t" \
  "v_cvt_pk_bf16_f32 v141, v42, v43\n\t" \
  "v_mfma_f32_16x16x32_bf16 a[96:99], a[128:131], v[152:155], a[96:99]\n\t" \
  "buffer_load_dwordx4 v231, s[64:67], s96 offen offset:3072 lds\n\t" \
  "v_mfma_f32_16x16x32_bf16 a[96:99], a[132:135], v[156:159], a[96:99]\n\t" \
  "v_exp_f32 v64, v64\n\t" \
  "v_mfma_f32_16x16x32_bf16 a[100:103], a[136:139], v[152:155], a[100:103]\n\t" \
  "ds_read_b128 a[248:251], v222 offset:28672\n\t" \
  "v_cvt_pk_bf16_f32 v142, v44, v45\n\t" \
  "v_mfma_f32_16x16x32_bf16 a[100:103], a[140:143], v[156:159], a[100:103]\n\t" \
  "v_exp_f32 v65, v65\n\t" \
  "v_mfma_f32_16x16x32_bf16 a[104:107], a[144:147], v[152:155], a[104:107]\n\t" \
  "v_exp_f32 v66, v66\n\t" \
  "v_mfma_f32_16x16x32_bf16 a[104:107], a[148:151], v[156:159], a[104:107]\n\t" \
  "ds_read_b128 a[252:255], v223 offset:28672\n\t" \
  "v_cvt_pk_bf16_f32 v143, v46, v47\n\t" \
  "v_mfma_f32_16x16x32_bf16 a[108:111], a[152:155], v[152:155], a[108:111]\n\t" \
  "v_exp_f32 v67, v67\n\t" \
  "v_mfma_f32_16x16x32_bf16 a[108:111], a[156:159], v[156:159], a[108:111]\n\t" \
  "v_exp_f32 v68, v68\n\t" \
  "v_mfma_f32_16x16x32_bf16 a[112:115], a[160:163], v[152:155], a[112:115]\n\t" \
  "v_exp_f32 v69, v69\n\t" \
  "v_mfma_f32_16x16x32_bf16 a[112:115], a[164:167], v[156:159], a[112:115]\n\t" \
  "v_exp_f32 v70, v70\n\t" \
  "v_mfma_f32_16x16x32_bf16 a[116:119], a[168:171], v[152:155], a[116:119]\n\t" \
  "v_exp_f32 v71, v71\n\t" \
  "v_mfma_f32_16x16x32_bf16 a[116:119], a[172:175], v[156:159], a[116:119]\n\t" \
  "v_exp_f32 v72, v72\n\t" \
  "v_mfma_f32_16x16x32_bf16 a[120:123], a[176:179], v[152:155], a[120:123]\n\t" \
  "v_exp_f32 v73, v73\n\t" \
  "v_mfma_f32_16x16x32_bf16 a[120:123], a[180:183], v[156:159], a[120:123]\n\t" \
  "v_exp_f32 v74, v74\n\t" \
  "v_mfma_f32_16x16x32_bf16 a[124:127], a[184:187], v[152:155], a[124:127]\n\t" \
  "v_exp_f32 v75, v75\n\t" \
  "v_mfma_f32_16x16x32_bf16 a[124:127], a[188:191], v[156:159], a[124:127]\n\t" \
  "v_exp_f32 v76, v76\n\t" \
  "v_mfma_f32_16x16x32_bf16 v[172:175], v[216:219], v[152:155], v[172:175]\n\t" \
  "v_exp_f32 v77, v77\n\t" \
  "v_mfma_f32_16x16x32_bf16 v[172:175], v[216:219], v[156:159], v[172:175]\n\t" \
  "s_waitcnt lgkmcnt(12)\n\t" \
  "s_cmp_lg_u32 s99, 0\n\t" \
  "s_cbranch_scc1 .Lsf_p2\n\t" \
  ".Lsf_q2:\n\t" \
  "v_mfma_f32_16x16x32_bf16 v[0:3], a[192:195], v[16:19], v[176:179]\n\t" \
  "ds_read_b64_tr_b16 a[128:129], v228 offset:0\n\t" \
  "v_cvt_pk_bf16_f32 v144, v64, v65\n\t" \
  "v_mfma_f32_16x16x32_bf16 v[0:3], a[196:199], v[20:23], v[0:3]\n\t" \
  "v_exp_f32 v78, v78\n\t" \
  "v_mfma_f32_16x16x32_bf16 v[0:3], a[200:203], v[24:27], v[0:3]\n\t" \
  "ds_read_b64_tr_b16 a[130:131], v228 offset:4096\n\t" \
  "v_cvt_pk_bf16_f32 v145, v66, v67\n\t" \
  "v_mfma_f32_16x16x32_bf16 v[0:3], a[204:207], v[28:31], v[0:3]\n\t" \
  "s_waitcnt lgkmcnt(10)\n\t" \
  "v_exp_f32 v79, v79\n\t" \
  "v_mfma_f32_16x16x32_bf16 v[4:7], a[220:223], v[28:31], v[176:179]\n\t" \
  "ds_read_b64_tr_b16 a[132:133], v228 offset:8192\n\t" \
  "v_cvt_pk_bf16_f32 v146, v68, v69\n\t" \
  "v_mfma_f32_16x16x32_bf16 v[4:7], a[216:219], v[24:27], v[4:7]\n\t" \
  "v_exp_f32 v96, v96\n\t" \
  "v_mfma_f32_16x16x32_bf16 v[4:7], a[212:215], v[20:23], v[4:7]\n\t" \
  "ds_read_b64_tr_b16 a[134:135], v228 offset:12288\n\t" \
  "v_max3_f32 v192, v0, v1, v2\n\t" \
  "v_mfma_f32_16x16x32_bf16 v[4:7], a[208:211], v[16:19], v[4:7]\n\t" \
  "s_waitcnt lgkmcnt(8)\n\t" \
  "ds_read_b64_tr_b16 a[136:137], v229 offset:0\n\t" \
  "v_cvt_pk_bf16_f32 v147, v70, v71\n\t" \
  "v_mfma_f32_16x16x32_bf16 v[8:11], a[224:227], v[16:19], v[176:179]\n\t" \
  "v_exp_f32 v97, v97\n\t" \
  "v_mfma_f32_16x16x32_bf16 v[8:11], a[228:231], v[20:23], v[8:11]\n\t" \
  "ds_read_b64_tr_b16 a[138:139], v229 offset:4096\n\t" \
  "v_cvt_pk_bf16_f32 v148, v72, v73\n\t" \
  "v_mfma_f32_16x16x32_bf16 v[8:11], a[232:235], v[24:27], v[8:11]\n\t" \
  "v_max3_f32 v203, v3, v4, v5\n\t" \
  "v_max3_f32 v192, v192, v6, v7\n\t" \
  "v_mfma_f32_16x16x32_bf16 v[8:11], a[236:239], v[28:31], v[8:11]\n\t" \
  "s_waitcnt lgkmcnt(6)\n\t" \
  "ds_read_b64_tr_b16 a[140:141], v229 offset:8192\n\t" \
  "v_cvt_pk_bf16_f32 v149, v74, v75\n\t" \
  "v_mfma_f32_16x16x32_bf16 v[12:15], a[252:255], v[28:31], v[176:179]\n\t" \
  "v_exp_f32 v98, v98\n\t" \
  "v_mfma_f32_16x16x32_bf16 v[12:15], a[248:251], v[24:27], v[12:15]\n\t" \
  "ds_read_b64_tr_b16 a[142:143], v229 offset:12288\n\t" \
  "v_cvt_pk_bf16_f32 v150, v76, v77\n\t" \
  "v_mfma_f32_16x16x32_bf16 v[12:15], a[244:247], v[20:23], v[12:15]\n\t" \
  "v_max3_f32 v203, v203, v8, v9\n\t" \
  "v_max3_f32 v192, v192, v10, v11\n\t" \
  "v_mfma_f32_16x16x32_bf16 v[12:15], a[240:243], v[16:19], v[12:15]\n\t" \
  "ds_read_b64_tr_b16 a[144:145], v228 offset:256\n\t" \
  "v_cvt_pk_bf16_f32 v151, v78, v79\n\t" \
  "v_mfma_f32_16x16x32_bf16 v[32:35], a[192:195], v[48:51], v[180:183]\n\t" \
  "ds_read_b64_tr_b16 a[146:147], v228 offset:4352\n\t" \
  "v_cvt_pk_bf16_f32 v152, v96, v97\n\t" \
  "v_mfma_f32_16x16x32_bf16 v[32:35], a[196:199], v[52:55], v[32:35]\n\t" \
  "v_exp_f32 v99, v99\n\t" \
  "v_mfma_f32_16x16x32_bf16 v[32:35], a[200:203], v[56:59], v[32:35]\n\t" \
  "v_max3_f32 v203, v203, v12, v13\n\t" \
  "v_max3_f32 v192, v192, v14, v15\n\t" \
  "v_mfma_f32_16x16x32_bf16 v[32:35], a[204:207], v[60:63], v[32:35]\n\t" \
  "v_max_f32 v192, v192, v203\n\t" \
  "v_cmp_lt_f32 vcc, 0x42800000, v192\n\t" \
  "v_mfma_f32_16x16x32_bf16 v[36:39], a[220:223], v[60:63], v[180:183]\n\t" \
  "ds_read_b64_tr_b16 a[148:149], v228 offset:8448\n\t" \
  "v_cvt_pk_bf16_f32 v153, v98, v99\n\t" \
  "v_mfma_f32_16x16x32_bf16 v[36:39], a[216:219], v[56:59], v[36:39]\n\t" \
  "s_cbranch_vccnz .Lsf_c30\n\t" \
  ".Lsf_r30:\n\t" \
  "v_mfma_f32_16x16x32_bf16 v[36:39], a[212:215], v[52:55], v[36:39]\n\t" \
  "ds_read_b64_tr_b16 a[150:151], v228 offset:12544\n\t" \
  "v_max3_f32 v192, v32, v33, v34\n\t" \
  "v_mfma_f32_16x16x32_bf16 v[36:39], a[208:211], v[48:51], v[36:39]\n\t" \
  "v_exp_f32 v100, v100\n\t" \
  "v_mfma_f32_16x16x32_bf16 v[40:43], a[224:227], v[48:51], v[180:183]\n\t" \
  "v_exp_f32 v101, v101\n\t" \
  "v_mfma_f32_16x16x32_bf16 v[40:43], a[228:231], v[52:55], v[40:43]\n\t" \
  "ds_read_b64_tr_b16 a[152:153], v229 offset:256\n\t" \
  "v_cvt_pk_bf16_f32 v154, v100, v101\n\t" \
  "v_mfma_f32_16x16x32_bf16 v[40:43], a[232:235], v[56:59], v[40:43]\n\t" \
  "ds_read_b64_tr_b16 a[154:155], v229 offset:4352\n\t" \
  "v_max3_f32 v203, v35, v36, v37\n\t" \
  "v_mfma_f32_16x16x32_bf16 v[40:43], a[236:239], v[60:63], v[40:43]\n\t" \
  "ds_read_b64_tr_b16 a[156:157], v229 offset:8448\n\t" \
  "v_max3_f32 v192, v192, v38, v39\n\t" \
  "v_mfma_f32_16x16x32_bf16 v[44:47], a[252:255], v[60:63], v[180:183]\n\t" \
  "v_exp_f32 v102, v102\n\t" \
  "v_mfma_f32_16x16x32_bf16 v[44:47], a[248:251], v[56:59], v[44:47]\n\t" \
  "v_exp_f32 v103, v103\n\t" \
  "v_mfma_f32_16x16x32_bf16 v[44:47], a[244:247], v[52:55], v[44:47]\n\t" \
  "ds_read_b64_tr_b16 a[158:159], v229 offset:12544\n\t" \
  "v_max3_f32 v203, v203, v40, v41\n\t" \
  "v_mfma_f32_16x16x32_bf16 v[44:47], a[240:243], v[48:51], v[44:47]\n\t" \
  "ds_read_b64_tr_b16 a[160:161], v228 offset:512\n\t" \
  "v_max3_f32 v192, v192, v42, v43\n\t" \
  "v_mfma_f32_16x16x32_bf16 v[64:67], a[192:195], v[80:83], v[184:187]\n\t" \
  "ds_read_b64_tr_b16 a[162:163], v228 offset:4608\n\t" \
  "v_cvt_pk_bf16_f32 v155, v102, v103\n\t" \
  "v_mfma_f32_16x16x32_bf16 v[64:67], a[196:199], v[84:87], v[64:67]\n\t" \
  "v_exp_f32 v104, v104\n\t" \
  "v_mfma_f32_16x16x32_bf16 v[64:67], a[200:203], v[88:91], v[64:67]\n\t" \
  "v_max3_f32 v203, v203, v44, v45\n\t" \
  "v_max3_f32 v192, v192, v46, v47\n\t" \
  "v_mfma_f32_16x16x32_bf16 v[64:67], a[204:207], v[92:95], v[64:67]\n\t" \
  "v_max_f32 v192, v192, v203\n\t" \
  "v_cmp_lt_f32 vcc, 0x42800000, v192\n\t" \
  "v_mfma_f32_16x16x32_bf16 v[68:71], a[220:223], v[92:95], v[184:187]\n\t" \
  "v_exp_f32 v105, v105\n\t" \
  "v_mfma_f32_16x16x32_bf16 v[68:71], a[216:219], v[88:91], v[68:71]\n\t" \
  "s_cbranch_vccnz .Lsf_c31\n\t" \
  ".Lsf_r31:\n\t" \
  "v_mfma_f32_16x16x32_bf16 v[68:71], a[212:215], v[84:87], v[68:71]\n\t" \
  "ds_read_b64_tr_b16 a[164:165], v228 offset:8704\n\t" \
  "v_max3_f32 v192, v64, v65, v66\n\t" \
  "v_mfma_f32_16x16x32_bf16 v[68:71], a[208:211], v[80:83], v[68:71]\n\t" \
  "ds_read_b64_tr_b16 a[166:167], v228 offset:12800\n\t" \
  "v_cvt_pk_bf16_f32 v156, v104, v105\n\t" \
  "v_mfma_f32_16x16x32_bf16 v[72:75], a[224:227], v[80:83], v[184:187]\n\t" \
  "v_exp_f32 v106, v106\n\t" \
  "v_mfma_f32_16x16x32_bf16 v[72:75], a[228:231], v[84:87], v[72:75]\n\t" \
  "v_exp_f32 v107, v107\n\t" \
  "v_mfma_f32_16x16x32_bf16 v[72:75], a[232:235], v[88:91], v[72:75]\n\t" \
  "ds_read_b64_tr_b16 a[168:169], v229 offset:512\n\t" \
  "v_max3_f32 v203, v67, v68, v69\n\t" \
  "v_mfma_f32_16x16x32_bf16 v[72:75], a[236:239], v[92:95], v[72:75]\n\t" \
  "ds_read_b64_tr_b16 a[170:171], v229 offset:4608\n\t" \
  "v_max3_f32 v192, v192, v70, v71\n\t" \
  "v_mfma_f32_16x16x32_bf16 v[76:79], a[252:255], v[92:95], v[184:187]\n\t" \
  "ds_read_b64_tr_b16 a[172:173], v229 offset:8704\n\t" \
  "v_cvt_pk_bf16_f32 v157, v106, v107\n\t" \
  "v_mfma_f32_16x16x32_bf16 v[76:79], a[248:251], v[88:91], v[76:79]\n\t" \
  "v_exp_f32 v108, v108\n\t" \
  "v_mfma_f32_16x16x32_bf16 v[76:79], a[244:247], v[84:87], v[76:79]\n\t" \
  "v_max3_f32 v203, v203, v72, v73\n\t" \
  "v_max3_f32 v192, v192, v74, v75\n\t" \
  "v_mfma_f32_16x16x32_bf16 v[76:79], a[240:243], v[80:83], v[76:79]\n\t" \
  "v_exp_f32 v109, v109\n\t" \
  "v_mfma_f32_16x16x32_bf16 v[96:99], a[192:195], v[112:115], v[188:191]\n\t" \
  "ds_read_b64_tr_b16 a[174:175], v229 offset:12800\n\t" \
  "v_cvt_pk_bf16_f32 v158, v108, v109\n\t" \
  "v_mfma_f32_16x16x32_bf16 v[96:99], a[196:199], v[116:119], v[96:99]\n\t" \
  "v_exp_f32 v110, v110\n\t" \
  "v_mfma_f32_16x16x32_bf16 v[96:99], a[200:203], v[120:123], v[96:99]\n\t" \
  "ds_read_b64_tr_b16 a[176:177], v228 offset:768\n\t" \
  "v_max3_f32 v203, v203, v76, v77\n\t" \
  "v_mfma_f32_16x16x32_bf16 v[96:99], a[204:207], v[124:127], v[96:99]\n\t" \
  "v_max3_f32 v192, v192, v78, v79\n\t" \
  "v_max_f32 v192, v192, v203\n\t" \
  "v_mfma_f32_16x16x32_bf16 v[100:103], a[220:223], v[124:127], v[188:191]\n\t" \
  "ds_read_b64_tr_b16 a[178:179], v228 offset:4864\n\t" \
  "v_cmp_lt_f32 vcc, 0x42800000, v192\n\t" \
  "v_mfma_f32_16x16x32_bf16 v[100:103], a[216:219], v[120:123], v[100:103]\n\t" \
  "ds_read_b64_tr_b16 a[180:181], v228 offset:8960\n\t" \
  "v_exp_f32 v111, v111\n\t" \
  "v_mfma_f32_16x16x32_bf16 v[100:103], a[212:215], v[116:119], v[100:103]\n\t" \
  "s_cbranch_vccnz .Lsf_c32\n\t" \
  ".Lsf_r32:\n\t" \
  "v_mfma_f32_16x16x32_bf16 v[100:103], a[208:211], v[112:115], v[100:103]\n\t" \
  "ds_read_b64_tr_b16 a[182:183], v228 offset:13056\n\t" \
  "v_max3_f32 v192, v96, v97, v98\n\t" \
  "v_mfma_f32_16x16x32_bf16 v[104:107], a[224:227], v[112:115], v[188:191]\n\t" \
  "ds_read_b64_tr_b16 a[184:185], v229 offset:768\n\t" \
  "v_cvt_pk_bf16_f32 v159, v110, v111\n\t" \
  "v_mfma_f32_16x16x32_bf16 v[104:107], a[228:231], v[116:119], v[104:107]\n\t" \
  "ds_read_b64_tr_b16 a[186:187], v229 offset:4864\n\t" \
  "v_mfma_f32_16x16x32_bf16 v[104:107], a[232:235], v[120:123], v[104:107]\n\t" \
  "ds_read_b64_tr_b16 a[188:189], v229 offset:8960\n\t" \
  "v_max3_f32 v203, v99, v100, v101\n\t" \
  "v_mfma_f32_16x16x32_bf16 v[104:107], a[236:239], v[124:127], v[104:107]\n\t" \
  "ds_read_b64_tr_b16 a[190:191], v229 offset:13056\n\t" \
  "v_max3_f32 v192, v192, v102, v103\n\t" \
  "v_mfma_f32_16x16x32_bf16 v[108:111], a[252:255], v[124:127], v[188:191]\n\t" \
  "v_exp_f32 v0, v0\n\t" \
  "v_mfma_f32_16x16x32_bf16 v[108:111], a[248:251], v[120:123], v[108:111]\n\t" \
  "v_exp_f32 v1, v1\n\t" \
  "v_mfma_f32_16x16x32_bf16 v[108:111], a[244:247], v[116:119], v[108:111]\n\t" \
  "s_waitcnt vmcnt(0) lgkmcnt(0)\n\t" \
  "v_max3_f32 v203, v203, v104, v105\n\t" \
  "v_max3_f32 v192, v192, v106, v107\n\t" \
  "v_mfma_f32_16x16x32_bf16 v[108:111], a[240:243], v[112:115], v[108:111]\n\t" \
  "s_barrier\n\t" \
  "v_mfma_f32_16x16x32_bf16 a[0:3], a[128:131], v[128:131], a[0:3]\n\t" \
  "v_exp_f32 v2, v2\n\t" \
  "v_mfma_f32_16x16x32_bf16 a[0:3], a[132:135], v[132:135], a[0:3]\n\t" \
  "v_exp_f32 v3, v3\n\t" \
  "v_mfma_f32_16x16x32_bf16 a[4:7], a[136:139], v[128:131], a[4:7]\n\t" \
  "v_exp_f32 v4, v4\n\t" \
  "v_mfma_f32_16x16x32_bf16 a[4:7], a[140:143], v[132:135], a[4:7]\n\t" \
  "v_max3_f32 v203, v203, v108, v109\n\t" \
  "v_max3_f32 v192, v192, v110, v111\n\t" \
  "v_mfma_f32_16x16x32_bf16 a[8:11], a[144:147], v[128:131], a[8:11]\n\t" \
  "v_max_f32 v192, v192, v203\n\t" \
  "v_cmp_lt_f32 vcc, 0x42800000, v192\n\t" \
  "v_mfma_f32_16x16x32_bf16 a[8:11], a[148:151], v[132:135], a[8:11]\n\t" \
  "v_exp_f32 v5, v5\n\t" \
  "v_mfma_f32_16x16x32_bf16 a[12:15], a[152:155], v[128:131], a[12:15]\n\t" \
  "s_cbranch_vccnz .Lsf_c33\n\t" \
  ".Lsf_r33:\n\t" \
  "v_mfma_f32_16x16x32_bf16 a[12:15], a[156:159], v[132:135], a[12:15]\n\t" \
  "s_add_u32 m0, s38, 65536\n\t" \
  "v_exp_f32 v6, v6\n\t" \
  "v_mfma_f32_16x16x32_bf16 a[16:19], a[160:163], v[128:131], a[16:19]\n\t" \
  "s_add_u32 s96, s38, 81920\n\t" \
  "v_exp_f32 v7, v7\n\t" \
  "v_mfma_f32_16x16x32_bf16 a[16:19], a[164:167], v[132:135], a[16:19]\n\t" \
  "v_exp_f32 v8, v8\n\t" \
  "v_mfma_f32_16x16x32_bf16 a[20:23], a[168:171], v[128:131], a[20:23]\n\t" \
  "buffer_load_dwordx4 v224, s[60:63], s96 offen offset:0 lds\n\t" \
  "v_mfma_f32_16x16x32_bf16 a[20:23], a[172:175], v[132:135], a[20:23]\n\t" \
  "v_exp_f32 v9, v9\n\t" \
  "v_mfma_f32_16x16x32_bf16 a[24:27], a[176:179], v[128:131], a[24:27]\n\t" \
  "v_exp_f32 v10, v10\n\t" \
  "v_mfma_f32_16x16x32_bf16 a[24:27], a[180:183], v[132:135], a[24:27]\n\t" \
  "v_exp_f32 v11, v11\n\t" \
  "v_mfma_f32_16x16x32_bf16 a[28:31], a[184:187], v[128:131], a[28:31]\n\t" \
  "v_exp_f32 v12, v12\n\t" \
  "v_mfma_f32_16x16x32_bf16 a[28:31], a[188:191], v[132:135], a[28:31]\n\t" \
  "v_exp_f32 v13, v13\n\t" \
  "v_mfma_f32_16x16x32_bf16 v[160:163], v[216:219], v[128:131], v[160:163]\n\t" \
  "v_exp_f32 v14, v14\n\t" \
  "v_mfma_f32_16x16x32_bf16 v[160:163], v[216:219], v[132:135], v[160:163]\n\t" \
  "buffer_load_dwordx4 v225, s[60:63], s96 offen offset:1024 lds\n\t" \
  "v_mfma_f32_16x16x32_bf16 a[32:35], a[128:131], v[136:139], a[32:35]\n\t" \
  "ds_read_b128 a[192:195], v220 offset:0\n\t" \
  "v_cvt_pk_bf16_f32 v128, v0, v1\n\t" \
  "v_mfma_f32_16x16x32_bf16 a[32:35], a[132:135], v[140:143], a[32:35]\n\t" \
  "ds_read_b128 a[196:199], v221 offset:0\n\t" \
  "v_cvt_pk_bf16_f32 v129, v2, v3\n\t" \
  "v_mfma_f32_16x16x32_bf16 a[36:39], a[136:139], v[136:139], a[36:39]\n\t" \
  "v_exp_f32 v15, v15\n\t" \
  "v_mfma_f32_16x16x32_bf16 a[36:39], a[140:143], v[140:143], a[36:39]\n\t" \
  "v_exp_f32 v32, v32\n\t" \
  "v_mfma_f32_16x16x32_bf16 a[40:43], a[144:147], v[136:139], a[40:43]\n\t" \
  "ds_read_b128 a[200:203], v222 offset:0\n\t" \
  "v_cvt_pk_bf16_f32 v130, v4, v5\n\t" \
  "v_mfma_f32_16x16x32_bf16 a[40:43], a[148:151], v[140:143], a[40:43]\n\t" \
  "buffer_load_dwordx4 v226, s[60:63], s96 offen offset:2048 lds\n\t" \
  "v_mfma_f32_16x16x32_bf16 a[44:47], a[152:155], v[136:139], a[44:47]\n\t" \
  "ds_read_b128 a[204:207], v223 offset:0\n\t" \
  "v_cvt_pk_bf16_f32 v131, v6, v7\n\t" \
  "v_mfma_f32_16x16x32_bf16 a[44:47], a[156:159], v[140:143], a[44:47]\n\t" \
  "v_exp_f32 v33, v33\n\t" \
  "v_mfma_f32_16x16x32_bf16 a[48:51], a[160:163], v[136:139], a[48:51]\n\t" \
  "v_exp_f32 v34, v34\n\t" \
  "v_mfma_f32_16x16x32_bf16 a[48:51], a[164:167], v[140:143], a[48:51]\n\t" \
  "ds_read_b128 a[208:211], v220 offset:4096\n\t" \
  "v_cvt_pk_bf16_f32 v132, v8, v9\n\t" \
  "v_mfma_f32_16x16x32_bf16 a[52:55], a[168:171], v[136:139], a[52:55]\n\t" \
  "v_exp_f32 v35, v35\n\t" \
  "v_mfma_f32_16x16x32_bf16 a[52:55], a[172:175], v[140:143], a[52:55]\n\t" \
  "buffer_load_dwordx4 v227, s[60:63], s96 offen offset:3072 lds\n\t" \
  "v_mfma_f32_16x16x32_bf16 a[56:59], a[176:179], v[136:139], a[56:59]\n\t" \
  "ds_read_b128 a[212:215], v221 offset:4096\n\t" \
  "v_cvt_pk_bf16_f32 v133, v10, v11\n\t" \
  "v_mfma_f32_16x16x32_bf16 a[56:59], a[180:183], v[140:143], a[56:59]\n\t" \
  "s_add_u32 m0, s38, 81920\n\t" \
  "v_exp_f32 v36, v36\n\t" \
  "v_mfma_f32_16x16x32_bf16 a[60:63], a[184:187], v[136:139], a[60:63]\n\t" \
  "s_add_u32 s96, s38, 65536\n\t" \
  "v_exp_f32 v37, v37\n\t" \
  "v_mfma_f32_16x16x32_bf16 a[60:63], a[188:191], v[140:143], a[60:63]\n\t" \
  "ds_read_b128 a[216:219], v222 offset:4096\n\t" \
  "v_cvt_pk_bf16_f32 v134, v12, v13\n\t" \
  "v_mfma_f32_16x16x32_bf16 v[164:167], v[216:219], v[136:139], v[164:167]\n\t" \
  "v_exp_f32 v38, v38\n\t" \
  "v_mfma_f32_16x16x32_bf16 v[164:167], v[216:219], v[140:143], v[164:167]\n\t" \
  "buffer_load_dwordx4 v230, s[64:67], s96 offen offset:0 lds\n\t" \
  "v_mfma_f32_16x16x32_bf16 a[64:67], a[128:131], v[144:147], a[64:67]\n\t" \
  "ds_read_b128 a[220:223], v223 offset:4096\n\t" \
  "v_cvt_pk_bf16_f32 v135, v14, v15\n\t" \
  "v_mfma_f32_16x16x32_bf16 a[64:67], a[132:135], v[148:151], a[64:67]\n\t" \
  "v_exp_f32 v39, v39\n\t" \
  "v_mfma_f32_16x16x32_bf16 a[68:71], a[136:139], v[144:147], a[68:71]\n\t" \
  "v_exp_f32 v40, v40\n\t" \
  "v_mfma_f32_16x16x32_bf16 a[68:71], a[140:143], v[148:151], a[68:71]\n\t" \
  "ds_read_b128 a[224:227], v220 offset:8192\n\t" \
  "v_cvt_pk_bf16_f32 v136, v32, v33\n\t" \
  "v_mfma_f32_16x16x32_bf16 a[72:75], a[144:147], v[144:147], a[72:75]\n\t" \
  "v_exp_f32 v41, v41\n\t" \
  "v_mfma_f32_16x16x32_bf16 a[72:75], a[148:151], v[148:151], a[72:75]\n\t" \
  "ds_read_b128 a[228:231], v221 offset:8192\n\t" \
  "v_cvt_pk_bf16_f32 v137, v34, v35\n\t" \
  "v_mfma_f32_16x16x32_bf16 a[76:79], a[152:155], v[144:147], a[76:79]\n\t" \
  "buffer_load_dwordx4 v231, s[64:67], s96 offen offset:1024 lds\n\t" \
  "v_mfma_f32_16x16x32_bf16 a[76:79], a[156:159], v[148:151], a[76:79]\n\t" \
  "v_exp_f32 v42, v42\n\t" \
  "v_mfma_f32_16x16x32_bf16 a[80:83], a[160:163], v[144:147], a[80:83]\n\t" \
  "ds_read_b128 a[232:235], v222 offset:8192\n\t" \
  "v_cvt_pk_bf16_f32 v138, v36, v37\n\t" \
  "v_mfma_f32_16x16x32_bf16 a[80:83], a[164:167], v[148:151], a[80:83]\n\t" \
  "v_exp_f32 v43, v43\n\t" \
  "v_mfma_f32_16x16x32_bf16 a[84:87], a[168:171], v[144:147], a[84:87]\n\t" \
  "v_exp_f32 v44, v44\n\t" \
  "v_mfma_f32_16x16x32_bf16 a[84:87], a[172:175], v[148:151], a[84:87]\n\t" \
  "ds_read_b128 a[236:239], v223 offset:8192\n\t" \
  "v_cvt_pk_bf16_f32 v139, v38, v39\n\t" \
  "v_mfma_f32_16x16x32_bf16 a[88:91], a[176:179], v[144:147], a[88:91]\n\t" \
  "buffer_load_dwordx4 v230, s[64:67], s96 offen offset:2048 lds\n\t" \
  "v_mfma_f32_16x16x32_bf16 a[88:91], a[180:183], v[148:151], a[88:91]\n\t" \
  "v_exp_f32 v45, v45\n\t" \
  "v_mfma_f32_16x16x32_bf16 a[92:95], a[184:187], v[144:147], a[92:95]\n\t" \
  "ds_read_b128 a[240:243], v220 offset:12288\n\t" \
  "v_cvt_pk_bf16_f32 v140, v40, v41\n\t" \
  "v_mfma_f32_16x16x32_bf16 a[92:95], a[188:191], v[148:151], a[92:95]\n\t" \
  "v_exp_f32 v46, v46\n\t" \
  "v_mfma_f32_16x16x32_bf16 v[168:171], v[216:219], v[144:147], v[168:171]\n\t" \
  "v_exp_f32 v47, v47\n\t" \
  "v_mfma_f32_16x16x32_bf16 v[168:171], v[216:219], v[148:151], v[168:171]\n\t" \
  "ds_read_b128 a[244:247], v221 offset:12288\n\t" \
  "v_cvt_pk_bf16_f32 v141, v42, v43\n\t" \
  "v_mfma_f32_16x16x32_bf16 a[96:99], a[128:131], v[152:155], a[96:99]\n\t" \
  "buffer_load_dwordx4 v231, s[64:67], s96 offen offset:3072 lds\n\t" \
  "v_mfma_f32_16x16x32_bf16 a[96:99], a[132:135], v[156:159], a[96:99]\n\t" \
  "v_exp_f32 v64, v64\n\t" \
  "v_mfma_f32_16x16x32_bf16 a[100:103], a[136:139], v[152:155], a[100:103]\n\t" \
  "ds_read_b128 a[248:251], v222 offset:12288\n\t" \
  "v_cvt_pk_bf16_f32 v142, v44, v45\n\t" \
  "v_mfma_f32_16x16x32_bf16 a[100:103], a[140:143], v[156:159], a[100:103]\n\t" \
  "v_exp_f32 v65, v65\n\t" \
  "v_mfma_f32_16x16x32_bf16 a[104:107], a[144:147], v[152:155], a[104:107]\n\t" \
  "v_exp_f32 v66, v66\n\t" \
  "v_mfma_f32_16x16x32_bf16 a[104:107], a[148:151], v[156:159], a[104:107]\n\t" \
  "ds_read_b128 a[252:255], v223 offset:12288\n\t" \
  "v_cvt_pk_bf16_f32 v143, v46, v47\n\t" \
  "v_mfma_f32_16x16x32_bf16 a[108:111], a[152:155], v[152:155], a[108:111]\n\t" \
  "v_exp_f32 v67, v67\n\t" \
  "v_mfma_f32_16x16x32_bf16 a[108:111], a[156:159], v[156:159], a[108:111]\n\t" \
  "v_exp_f32 v68, v68\n\t" \
  "v_mfma_f32_16x16x32_bf16 a[112:115], a[160:163], v[152:155], a[112:115]\n\t" \
  "v_exp_f32 v69, v69\n\t" \
  "v_mfma_f32_16x16x32_bf16 a[112:115], a[164:167], v[156:159], a[112:115]\n\t" \
  "v_exp_f32 v70, v70\n\t" \
  "v_mfma_f32_16x16x32_bf16 a[116:119], a[168:171], v[152:155], a[116:119]\n\t" \
  "v_exp_f32 v71, v71\n\t" \
  "v_mfma_f32_16x16x32_bf16 a[116:119], a[172:175], v[156:159], a[116:119]\n\t" \
  "v_exp_f32 v72, v72\n\t" \
  "v_mfma_f32_16x16x32_bf16 a[120:123], a[176:179], v[152:155], a[120:123]\n\t" \
  "v_exp_f32 v73, v73\n\t" \
  "v_mfma_f32_16x16x32_bf16 a[120:123], a[180:183], v[156:159], a[120:123]\n\t" \
  "v_exp_f32 v74, v74\n\t" \
  "v_mfma_f32_16x16x32_bf16 a[124:127], a[184:187], v[152:155], a[124:127]\n\t" \
  "v_exp_f32 v75, v75\n\t" \
  "v_mfma_f32_16x16x32_bf16 a[124:127], a[188:191], v[156:159], a[124:127]\n\t" \
  "v_exp_f32 v76, v76\n\t" \
  "v_mfma_f32_16x16x32_bf16 v[172:175], v[216:219], v[152:155], v[172:175]\n\t" \
  "v_exp_f32 v77, v77\n\t" \
  "v_mfma_f32_16x16x32_bf16 v[172:175], v[216:219], v[156:159], v[172:175]\n\t" \
  "s_waitcnt lgkmcnt(12)\n\t" \
  "s_cmp_lg_u32 s99, 0\n\t" \
  "s_cbranch_scc1 .Lsf_p3\n\t" \
  ".Lsf_q3:\n\t" \
  "v_mfma_f32_16x16x32_bf16 v[0:3], a[192:195], v[16:19], v[176:179]\n\t" \
  "ds_read_b64_tr_b16 a[128:129], v210 offset:0\n\t" \
  "v_cvt_pk_bf16_f32 v144, v64, v65\n\t" \
  "v_mfma_f32_16x16x32_bf16 v[0:3], a[196:199], v[20:23], v[0:3]\n\t" \
  "v_exp_f32 v78, v78\n\t" \
  "v_mfma_f32_16x16x32_bf16 v[0:3], a[200:203], v[24:27], v[0:3]\n\t" \
  "ds_read_b64_tr_b16 a[130:131], v210 offset:4096\n\t" \
  "v_cvt_pk_bf16_f32 v145, v66, v67\n\t" \
  "v_mfma_f32_16x16x32_bf16 v[0:3], a[204:207], v[28:31], v[0:3]\n\t" \
  "s_waitcnt lgkmcnt(10)\n\t" \
  "v_exp_f32 v79, v79\n\t" \
  "v_mfma_f32_16x16x32_bf16 v[4:7], a[220:223], v[28:31], v[176:179]\n\t" \
  "ds_read_b64_tr_b16 a[132:133], v210 offset:8192\n\t" \
  "v_cvt_pk_bf16_f32 v146, v68, v69\n\t" \
  "v_mfma_f32_16x16x32_bf16 v[4:7], a[216:219], v[24:27], v[4:7]\n\t" \
  "v_exp_f32 v96, v96\n\t" \
  "v_mfma_f32_16x16x32_bf16 v[4:7], a[212:215], v[20:23], v[4:7]\n\t" \
  "ds_read_b64_tr_b16 a[134:135], v210 offset:12288\n\t" \
  "v_max3_f32 v192, v0, v1, v2\n\t" \
  "v_mfma_f32_16x16x32_bf16 v[4:7], a[208:211], v[16:19], v[4:7]\n\t" \
  "s_waitcnt lgkmcnt(8)\n\t" \
  "ds_read_b64_tr_b16 a[136:137], v211 offset:0\n\t" \
  "v_cvt_pk_bf16_f32 v147, v70, v71\n\t" \
  "v_mfma_f32_16x16x32_bf16 v[8:11], a[224:227], v[16:19], v[176:179]\n\t" \
  "v_exp_f32 v97, v97\n\t" \
  "v_mfma_f32_16x16x32_bf16 v[8:11], a[228:231], v[20:23], v[8:11]\n\t" \
  "ds_read_b64_tr_b16 a[138:139], v211 offset:4096\n\t" \
  "v_cvt_pk_bf16_f32 v148, v72, v73\n\t" \
  "v_mfma_f32_16x16x32_bf16 v[8:11], a[232:235], v[24:27], v[8:11]\n\t" \
  "v_max3_f32 v203, v3, v4, v5\n\t" \
  "v_max3_f32 v192, v192, v6, v7\n\t" \
  "v_mfma_f32_16x16x32_bf16 v[8:11], a[236:239], v[28:31], v[8:11]\n\t" \
  "s_waitcnt lgkmcnt(6)\n\t" \
  "ds_read_b64_tr_b16 a[140:141], v211 offset:8192\n\t" \
  "v_cvt_pk_bf16_f32 v149, v74, v75\n\t" \
  "v_mfma_f32_16x16x32_bf16 v[12:15], a[252:255], v[28:31], v[176:179]\n\t" \
  "v_exp_f32 v98, v98\n\t" \
  "v_mfma_f32_16x16x32_bf16 v[12:15], a[248:251], v[24:27], v[12:15]\n\t" \
  "ds_read_b64_tr_b16 a[142:143], v211 offset:12288\n\t" \
  "v_cvt_pk_bf16_f32 v150, v76, v77\n\t" \
  "v_mfma_f32_16x16x32_bf16 v[12:15], a[244:247], v[20:23], v[12:15]\n\t" \
  "v_max3_f32 v203, v203, v8, v9\n\t" \
  "v_max3_f32 v192, v192, v10, v11\n\t" \
  "v_mfma_f32_16x16x32_bf16 v[12:15], a[240:243], v[16:19], v[12:15]\n\t" \
  "ds_read_b64_tr_b16 a[144:145], v210 offset:256\n\t" \
  "v_cvt_pk_bf16_f32 v151, v78, v79\n\t" \
  "v_mfma_f32_16x16x32_bf16 v[32:35], a[192:195], v[48:51], v[180:183]\n\t" \
  "ds_read_b64_tr_b16 a[146:147], v210 offset:4352\n\t" \
  "v_cvt_pk_bf16_f32 v152, v96, v97\n\t" \
  "v_mfma_f32_16x16x32_bf16 v[32:35], a[196:199], v[52:55], v[32:35]\n\t" \
  "v_exp_f32 v99, v99\n\t" \
  "v_mfma_f32_16x16x32_bf16 v[32:35], a[200:203], v[56:59], v[32:35]\n\t" \
  "v_max3_f32 v203, v203, v12, v13\n\t" \
  "v_max3_f32 v192, v192, v14, v15\n\t" \
  "v_mfma_f32_16x16x32_bf16 v[32:35], a[204:207], v[60:63], v[32:35]\n\t" \
  "v_max_f32 v192, v192, v203\n\t" \
  "v_cmp_lt_f32 vcc, 0x42800000, v192\n\t" \
  "v_mfma_f32_16x16x32_bf16 v[36:39], a[220:223], v[60:63], v[180:183]\n\t" \
  "ds_read_b64_tr_b16 a[148:149], v210 offset:8448\n\t" \
  "v_cvt_pk_bf16_f32 v153, v98, v99\n\t" \
  "v_mfma_f32_16x16x32_bf16 v[36:39], a[216:219], v[56:59], v[36:39]\n\t" \
  "s_cbranch_vccnz .Lsf_c40\n\t" \
  ".Lsf_r40:\n\t" \
  "v_mfma_f32_16x16x32_bf16 v[36:39], a[212:215], v[52:55], v[36:39]\n\t" \
  "ds_read_b64_tr_b16 a[150:151], v210 offset:12544\n\t" \
  "v_max3_f32 v192, v32, v33, v34\n\t" \
  "v_mfma_f32_16x16x32_bf16 v[36:39], a[208:211], v[48:51], v[36:39]\n\t" \
  "v_exp_f32 v100, v100\n\t" \
  "v_mfma_f32_16x16x32_bf16 v[40:43], a[224:227], v[48:51], v[180:183]\n\t" \
  "v_exp_f32 v101, v101\n\t" \
  "v_mfma_f32_16x16x32_bf16 v[40:43], a[228:231], v[52:55], v[40:43]\n\t" \
  "ds_read_b64_tr_b16 a[152:153], v211 offset:256\n\t" \
  "v_cvt_pk_bf16_f32 v154, v100, v101\n\t" \
  "v_mfma_f32_16x16x32_bf16 v[40:43], a[232:235], v[56:59], v[40:43]\n\t" \
  "ds_read_b64_tr_b16 a[154:155], v211 offset:4352\n\t" \
  "v_max3_f32 v203, v35, v36, v37\n\t" \
  "v_mfma_f32_16x16x32_bf16 v[40:43], a[236:239], v[60:63], v[40:43]\n\t" \
  "ds_read_b64_tr_b16 a[156:157], v211 offset:8448\n\t" \
  "v_max3_f32 v192, v192, v38, v39\n\t" \
  "v_mfma_f32_16x16x32_bf16 v[44:47], a[252:255], v[60:63], v[180:183]\n\t" \
  "v_exp_f32 v102, v102\n\t" \
  "v_mfma_f32_16x16x32_bf16 v[44:47], a[248:251], v[56:59], v[44:47]\n\t" \
  "v_exp_f32 v103, v103\n\t" \
  "v_mfma_f32_16x16x32_bf16 v[44:47], a[244:247], v[52:55], v[44:47]\n\t" \
  "ds_read_b64_tr_b16 a[158:159], v211 offset:12544\n\t" \
  "v_max3_f32 v203, v203, v40, v41\n\t" \
  "v_mfma_f32_16x16x32_bf16 v[44:47], a[240:243], v[48:51], v[44:47]\n\t" \
  "ds_read_b64_tr_b16 a[160:161], v210 offset:512\n\t" \
  "v_max3_f32 v192, v192, v42, v43\n\t" \
  "v_mfma_f32_16x16x32_bf16 v[64:67], a[192:195], v[80:83], v[184:187]\n\t" \
  "ds_read_b64_tr_b16 a[162:163], v210 offset:4608\n\t" \
  "v_cvt_pk_bf16_f32 v155, v102, v103\n\t" \
  "v_mfma_f32_16x16x32_bf16 v[64:67], a[196:199], v[84:87], v[64:67]\n\t" \
  "v_exp_f32 v104, v104\n\t" \
  "v_mfma_f32_16x16x32_bf16 v[64:67], a[200:203], v[88:91], v[64:67]\n\t" \
  "v_max3_f32 v203, v203, v44, v45\n\t" \
  "v_max3_f32 v192, v192, v46, v47\n\t" \
  "v_mfma_f32_16x16x32_bf16 v[64:67], a[204:207], v[92:95], v[64:67]\n\t" \
  "v_max_f32 v192, v192, v203\n\t" \
  "v_cmp_lt_f32 vcc, 0x42800000, v192\n\t" \
  "v_mfma_f32_16x16x32_bf16 v[68:71], a[220:223], v[92:95], v[184:187]\n\t" \
  "v_exp_f32 v105, v105\n\t" \
  "v_mfma_f32_16x16x32_bf16 v[68:71], a[216:219], v[88:91], v[68:71]\n\t" \
  "s_cbranch_vccnz .Lsf_c41\n\t" \
  ".Lsf_r41:\n\t" \
  "v_mfma_f32_16x16x32_bf16 v[68:71], a[212:215], v[84:87], v[68:71]\n\t" \
  "ds_read_b64_tr_b16 a[164:165], v210 offset:8704\n\t" \
  "v_max3_f32 v192, v64, v65, v66\n\t" \
  "v_mfma_f32_16x16x32_bf16 v[68:71], a[208:211], v[80:83], v[68:71]\n\t" \
  "ds_read_b64_tr_b16 a[166:167], v210 offset:12800\n\t" \
  "v_cvt_pk_bf16_f32 v156, v104, v105\n\t" \
  "v_mfma_f32_16x16x32_bf16 v[72:75], a[224:227], v[80:83], v[184:187]\n\t" \
  "v_exp_f32 v106, v106\n\t" \
  "v_mfma_f32_16x16x32_bf16 v[72:75], a[228:231], v[84:87], v[72:75]\n\t" \
  "v_exp_f32 v107, v107\n\t" \
  "v_mfma_f32_16x16x32_bf16 v[72:75], a[232:235], v[88:91], v[72:75]\n\t" \
  "ds_read_b64_tr_b16 a[168:169], v211 offset:512\n\t" \
  "v_max3_f32 v203, v67, v68, v69\n\t" \
  "v_mfma_f32_16x16x32_bf16 v[72:75], a[236:239], v[92:95], v[72:75]\n\t" \
  "ds_read_b64_tr_b16 a[170:171], v211 offset:4608\n\t" \
  "v_max3_f32 v192, v192, v70, v71\n\t" \
  "v_mfma_f32_16x16x32_bf16 v[76:79], a[252:255], v[92:95], v[184:187]\n\t" \
  "ds_read_b64_tr_b16 a[172:173], v211 offset:8704\n\t" \
  "v_cvt_pk_bf16_f32 v157, v106, v107\n\t" \
  "v_mfma_f32_16x16x32_bf16 v[76:79], a[248:251], v[88:91], v[76:79]\n\t" \
  "v_exp_f32 v108, v108\n\t" \
  "v_mfma_f32_16x16x32_bf16 v[76:79], a[244:247], v[84:87], v[76:79]\n\t" \
  "v_max3_f32 v203, v203, v72, v73\n\t" \
  "v_max3_f32 v192, v192, v74, v75\n\t" \
  "v_mfma_f32_16x16x32_bf16 v[76:79], a[240:243], v[80:83], v[76:79]\n\t" \
  "v_exp_f32 v109, v109\n\t" \
  "v_mfma_f32_16x16x32_bf16 v[96:99], a[192:195], v[112:115], v[188:191]\n\t" \
  "ds_read_b64_tr_b16 a[174:175], v211 offset:12800\n\t" \
  "v_cvt_pk_bf16_f32 v158, v108, v109\n\t" \
  "v_mfma_f32_16x16x32_bf16 v[96:99], a[196:199], v[116:119], v[96:99]\n\t" \
  "v_exp_f32 v110, v110\n\t" \
  "v_mfma_f32_16x16x32_bf16 v[96:99], a[200:203], v[120:123], v[96:99]\n\t" \
  "ds_read_b64_tr_b16 a[176:177], v210 offset:768\n\t" \
  "v_max3_f32 v203, v203, v76, v77\n\t" \
  "v_mfma_f32_16x16x32_bf16 v[96:99], a[204:207], v[124:127], v[96:99]\n\t" \
  "v_max3_f32 v192, v192, v78, v79\n\t" \
  "v_max_f32 v192, v192, v203\n\t" \
  "v_mfma_f32_16x16x32_bf16 v[100:103], a[220:223], v[124:127], v[188:191]\n\t" \
  "ds_read_b64_tr_b16 a[178:179], v210 offset:4864\n\t" \
  "v_cmp_lt_f32 vcc, 0x42800000, v192\n\t" \
  "v_mfma_f32_16x16x32_bf16 v[100:103], a[216:219], v[120:123], v[100:103]\n\t" \
  "ds_read_b64_tr_b16 a[180:181], v210 offset:8960\n\t" \
  "v_exp_f32 v111, v111\n\t" \
  "v_mfma_f32_16x16x32_bf16 v[100:103], a[212:215], v[116:119], v[100:103]\n\t" \
  "s_cbranch_vccnz .Lsf_c42\n\t" \
  ".Lsf_r42:\n\t" \
  "v_mfma_f32_16x16x32_bf16 v[100:103], a[208:211], v[112:115], v[100:103]\n\t" \
  "ds_read_b64_tr_b16 a[182:183], v210 offset:13056\n\t" \
  "v_max3_f32 v192, v96, v97, v98\n\t" \
  "v_mfma_f32_16x16x32_bf16 v[104:107], a[224:227], v[112:115], v[188:191]\n\t" \
  "ds_read_b64_tr_b16 a[184:185], v211 offset:768\n\t" \
  "v_cvt_pk_bf16_f32 v159, v110, v111\n\t" \
  "v_mfma_f32_16x16x32_bf16 v[104:107], a[228:231], v[116:119], v[104:107]\n\t" \
  "ds_read_b64_tr_b16 a[186:187], v211 offset:4864\n\t" \
  "v_mfma_f32_16x16x32_bf16 v[104:107], a[232:235], v[120:123], v[104:107]\n\t" \
  "ds_read_b64_tr_b16 a[188:189], v211 offset:8960\n\t" \
  "v_max3_f32 v203, v99, v100, v101\n\t" \
  "v_mfma_f32_16x16x32_bf16 v[104:107], a[236:239], v[124:127], v[104:107]\n\t" \
  "ds_read_b64_tr_b16 a[190:191], v211 offset:13056\n\t" \
  "v_max3_f32 v192, v192, v102, v103\n\t" \
  "v_mfma_f32_16x16x32_bf16 v[108:111], a[252:255], v[124:127], v[188:191]\n\t" \
  "v_exp_f32 v0, v0\n\t" \
  "v_mfma_f32_16x16x32_bf16 v[108:111], a[248:251], v[120:123], v[108:111]\n\t" \
  "v_exp_f32 v1, v1\n\t" \
  "v_mfma_f32_16x16x32_bf16 v[108:111], a[244:247], v[116:119], v[108:111]\n\t" \
  "s_waitcnt vmcnt(0) lgkmcnt(0)\n\t" \
  "v_max3_f32 v203, v203, v104, v105\n\t" \
  "v_max3_f32 v192, v192, v106, v107\n\t" \
  "v_mfma_f32_16x16x32_bf16 v[108:111], a[240:243], v[112:115], v[108:111]\n\t" \
  "s_barrier\n\t" \
  "v_mfma_f32_16x16x32_bf16 a[0:3], a[128:131], v[128:131], a[0:3]\n\t" \
  "v_exp_f32 v2, v2\n\t" \
  "v_mfma_f32_16x16x32_bf16 a[0:3], a[132:135], v[132:135], a[0:3]\n\t" \
  "v_exp_f32 v3, v3\n\t" \
  "v_mfma_f32_16x16x32_bf16 a[4:7], a[136:139], v[128:131], a[4:7]\n\t" \
  "v_exp_f32 v4, v4\n\t" \
  "v_mfma_f32_16x16x32_bf16 a[4:7], a[140:143], v[132:135], a[4:7]\n\t" \
  "v_max3_f32 v203, v203, v108, v109\n\t" \
  "v_max3_f32 v192, v192, v110, v111\n\t" \
  "v_mfma_f32_16x16x32_bf16 a[8:11], a[144:147], v[128:131], a[8:11]\n\t" \
  "v_max_f32 v192, v192, v203\n\t" \
  "v_cmp_lt_f32 vcc, 0x42800000, v192\n\t" \
  "v_mfma_f32_16x16x32_bf16 a[8:11], a[148:151], v[132:135], a[8:11]\n\t" \
  "v_exp_f32 v5, v5\n\t" \
  "v_mfma_f32_16x16x32_bf16 a[12:15], a[152:155], v[128:131], a[12:15]\n\t" \
  "s_cbranch_vccnz .Lsf_c43\n\t" \
  ".Lsf_r43:\n\t" \
  "v_mfma_f32_16x16x32_bf16 a[12:15], a[156:159], v[132:135], a[12:15]\n\t" \
  "s_add_u32 m0, s38, 49152\n\t" \
  "v_exp_f32 v6, v6\n\t" \
  "v_mfma_f32_16x16x32_bf16 a[16:19], a[160:163], v[128:131], a[16:19]\n\t" \
  "s_add_u32 s96, s38, 98304\n\t" \
  "v_exp_f32 v7, v7\n\t" \
  "v_mfma_f32_16x16x32_bf16 a[16:19], a[164:167], v[132:135], a[16:19]\n\t" \
  "v_exp_f32 v8, v8\n\t" \
  "v_mfma_f32_16x16x32_bf16 a[20:23], a[168:171], v[128:131], a[20:23]\n\t" \
  "buffer_load_dwordx4 v224, s[60:63], s96 offen offset:0 lds\n\t" \
  "v_mfma_f32_16x16x32_bf16 a[20:23], a[172:175], v[132:135], a[20:23]\n\t" \
  "v_exp_f32 v9, v9\n\t" \
  "v_mfma_f32_16x16x32_bf16 a[24:27], a[176:179], v[128:131], a[24:27]\n\t" \
  "v_exp_f32 v10, v10\n\t" \
  "v_mfma_f32_16x16x32_bf16 a[24:27], a[180:183], v[132:135], a[24:27]\n\t" \
  "v_exp_f32 v11, v11\n\t" \
  "v_mfma_f32_16x16x32_bf16 a[28:31], a[184:187], v[128:131], a[28:31]\n\t" \
  "v_exp_f32 v12, v12\n\t" \
  "v_mfma_f32_16x16x32_bf16 a[28:31], a[188:191], v[132:135], a[28:31]\n\t" \
  "v_exp_f32 v13, v13\n\t" \
  "v_mfma_f32_16x16x32_bf16 v[160:163], v[216:219], v[128:131], v[160:163]\n\t" \
  "v_exp_f32 v14, v14\n\t" \
  "v_mfma_f32_16x16x32_bf16 v[160:163], v[216:219], v[132:135], v[160:163]\n\t" \
  "buffer_load_dwordx4 v225, s[60:63], s96 offen offset:1024 lds\n\t" \
  "v_mfma_f32_16x16x32_bf16 a[32:35], a[128:131], v[136:139], a[32:35]\n\t" \
  "ds_read_b128 a[192:195], v220 offset:16384\n\t" \
  "v_cvt_pk_bf16_f32 v128, v0, v1\n\t" \
  "v_mfma_f32_16x16x32_bf16 a[32:35], a[132:135], v[140:143], a[32:35]\n\t" \
  "ds_read_b128 a[196:199], v221 offset:16384\n\t" \
  "v_cvt_pk_bf16_f32 v129, v2, v3\n\t" \
  "v_mfma_f32_16x16x32_bf16 a[36:39], a[136:139], v[136:139], a[36:39]\n\t" \
  "v_exp_f32 v15, v15\n\t" \
  "v_mfma_f32_16x16x32_bf16 a[36:39], a[140:143], v[140:143], a[36:39]\n\t" \
  "v_exp_f32 v32, v32\n\t" \
  "v_mfma_f32_16x16x32_bf16 a[40:43], a[144:147], v[136:139], a[40:43]\n\t" \
  "ds_read_b128 a[200:203], v222 offset:16384\n\t" \
  "v_cvt_pk_bf16_f32 v130, v4, v5\n\t" \
  "v_mfma_f32_16x16x32_bf16 a[40:43], a[148:151], v[140:143], a[40:43]\n\t" \
  "buffer_load_dwordx4 v226, s[60:63], s96 offen offset:2048 lds\n\t" \
  "v_mfma_f32_16x16x32_bf16 a[44:47], a[152:155], v[136:139], a[44:47]\n\t" \
  "ds_read_b128 a[204:207], v223 offset:16384\n\t" \
  "v_cvt_pk_bf16_f32 v131, v6, v7\n\t" \
  "v_mfma_f32_16x16x32_bf16 a[44:47], a[156:159], v[140:143], a[44:47]\n\t" \
  "v_exp_f32 v33, v33\n\t" \
  "v_mfma_f32_16x16x32_bf16 a[48:51], a[160:163], v[136:139], a[48:51]\n\t" \
  "v_exp_f32 v34, v34\n\t" \
  "v_mfma_f32_16x16x32_bf16 a[48:51], a[164:167], v[140:143], a[48:51]\n\t" \
  "ds_read_b128 a[208:211], v220 offset:20480\n\t" \
  "v_cvt_pk_bf16_f32 v132, v8, v9\n\t" \
  "v_mfma_f32_16x16x32_bf16 a[52:55], a[168:171], v[136:139], a[52:55]\n\t" \
  "v_exp_f32 v35, v35\n\t" \
  "v_mfma_f32_16x16x32_bf16 a[52:55], a[172:175], v[140:143], a[52:55]\n\t" \
  "buffer_load_dwordx4 v227, s[60:63], s96 offen offset:3072 lds\n\t" \
  "v_mfma_f32_16x16x32_bf16 a[56:59], a[176:179], v[136:139], a[56:59]\n\t" \
  "ds_read_b128 a[212:215], v221 offset:20480\n\t" \
  "v_cvt_pk_bf16_f32 v133, v10, v11\n\t" \
  "v_mfma_f32_16x16x32_bf16 a[56:59], a[180:183], v[140:143], a[56:59]\n\t" \
  "s_add_u32 m0, s38, 147456\n\t" \
  "v_exp_f32 v36, v36\n\t" \
  "v_mfma_f32_16x16x32_bf16 a[60:63], a[184:187], v[136:139], a[60:63]\n\t" \
  "s_add_u32 s96, s38, 81920\n\t" \
  "v_exp_f32 v37, v37\n\t" \
  "v_mfma_f32_16x16x32_bf16 a[60:63], a[188:191], v[140:143], a[60:63]\n\t" \
  "ds_read_b128 a[216:219], v222 offset:20480\n\t" \
  "v_cvt_pk_bf16_f32 v134, v12, v13\n\t" \
  "v_mfma_f32_16x16x32_bf16 v[164:167], v[216:219], v[136:139], v[164:167]\n\t" \
  "v_exp_f32 v38, v38\n\t" \
  "v_mfma_f32_16x16x32_bf16 v[164:167], v[216:219], v[140:143], v[164:167]\n\t" \
  "buffer_load_dwordx4 v230, s[64:67], s96 offen offset:0 lds\n\t" \
  "v_mfma_f32_16x16x32_bf16 a[64:67], a[128:131], v[144:147], a[64:67]\n\t" \
  "ds_read_b128 a[220:223], v223 offset:20480\n\t" \
  "v_cvt_pk_bf16_f32 v135, v14, v15\n\t" \
  "v_mfma_f32_16x16x32_bf16 a[64:67], a[132:135], v[148:151], a[64:67]\n\t" \
  "v_exp_f32 v39, v39\n\t" \
  "v_mfma_f32_16x16x32_bf16 a[68:71], a[136:139], v[144:147], a[68:71]\n\t" \
  "v_exp_f32 v40, v40\n\t" \
  "v_mfma_f32_16x16x32_bf16 a[68:71], a[140:143], v[148:151], a[68:71]\n\t" \
  "ds_read_b128 a[224:227], v220 offset:24576\n\t" \
  "v_cvt_pk_bf16_f32 v136, v32, v33\n\t" \
  "v_mfma_f32_16x16x32_bf16 a[72:75], a[144:147], v[144:147], a[72:75]\n\t" \
  "v_exp_f32 v41, v41\n\t" \
  "v_mfma_f32_16x16x32_bf16 a[72:75], a[148:151], v[148:151], a[72:75]\n\t" \
  "ds_read_b128 a[228:231], v221 offset:24576\n\t" \
  "v_cvt_pk_bf16_f32 v137, v34, v35\n\t" \
  "v_mfma_f32_16x16x32_bf16 a[76:79], a[152:155], v[144:147], a[76:79]\n\t" \
  "buffer_load_dwordx4 v231, s[64:67], s96 offen offset:1024 lds\n\t" \
  "v_mfma_f32_16x16x32_bf16 a[76:79], a[156:159], v[148:151], a[76:79]\n\t" \
  "v_exp_f32 v42, v42\n\t" \
  "v_mfma_f32_16x16x32_bf16 a[80:83], a[160:163], v[144:147], a[80:83]\n\t" \
  "ds_read_b128 a[232:235], v222 offset:24576\n\t" \
  "v_cvt_pk_bf16_f32 v138, v36, v37\n\t" \
  "v_mfma_f32_16x16x32_bf16 a[80:83], a[164:167], v[148:151], a[80:83]\n\t" \
  "v_exp_f32 v43, v43\n\t" \
  "v_mfma_f32_16x16x32_bf16 a[84:87], a[168:171], v[144:147], a[84:87]\n\t" \
  "v_exp_f32 v44, v44\n\t" \
  "v_mfma_f32_16x16x32_bf16 a[84:87], a[172:175], v[148:151], a[84:87]\n\t" \
  "ds_read_b128 a[236:239], v223 offset:24576\n\t" \
  "v_cvt_pk_bf16_f32 v139, v38, v39\n\t" \
  "v_mfma_f32_16x16x32_bf16 a[88:91], a[176:179], v[144:147], a[88:91]\n\t" \
  "buffer_load_dwordx4 v230, s[64:67], s96 offen offset:2048 lds\n\t" \
  "v_mfma_f32_16x16x32_bf16 a[88:91], a[180:183], v[148:151], a[88:91]\n\t" \
  "v_exp_f32 v45, v45\n\t" \
  "v_mfma_f32_16x16x32_bf16 a[92:95], a[184:187], v[144:147], a[92:95]\n\t" \
  "ds_read_b128 a[240:243], v220 offset:28672\n\t" \
  "v_cvt_pk_bf16_f32 v140, v40, v41\n\t" \
  "v_mfma_f32_16x16x32_bf16 a[92:95], a[188:191], v[148:151], a[92:95]\n\t" \
  "v_exp_f32 v46, v46\n\t" \
  "v_mfma_f32_16x16x32_bf16 v[168:171], v[216:219], v[144:147], v[168:171]\n\t" \
  "v_exp_f32 v47, v47\n\t" \
  "v_mfma_f32_16x16x32_bf16 v[168:171], v[216:219], v[148:151], v[168:171]\n\t" \
  "ds_read_b128 a[244:247], v221 offset:28672\n\t" \
  "v_cvt_pk_bf16_f32 v141, v42, v43\n\t" \
  "v_mfma_f32_16x16x32_bf16 a[96:99], a[128:131], v[152:155], a[96:99]\n\t" \
  "buffer_load_dwordx4 v231, s[64:67], s96 offen offset:3072 lds\n\t" \
  "v_mfma_f32_16x16x32_bf16 a[96:99], a[132:135], v[156:159], a[96:99]\n\t" \
  "v_exp_f32 v64, v64\n\t" \
  "v_mfma_f32_16x16x32_bf16 a[100:103], a[136:139], v[152:155], a[100:103]\n\t" \
  "ds_read_b128 a[248:251], v222 offset:28672\n\t" \
  "v_cvt_pk_bf16_f32 v142, v44, v45\n\t" \
  "v_mfma_f32_16x16x32_bf16 a[100:103], a[140:143], v[156:159], a[100:103]\n\t" \
  "v_exp_f32 v65, v65\n\t" \
  "v_mfma_f32_16x16x32_bf16 a[104:107], a[144:147], v[152:155], a[104:107]\n\t" \
  "v_exp_f32 v66, v66\n\t" \
  "v_mfma_f32_16x16x32_bf16 a[104:107], a[148:151], v[156:159], a[104:107]\n\t" \
  "ds_read_b128 a[252:255], v223 offset:28672\n\t" \
  "v_cvt_pk_bf16_f32 v143, v46, v47\n\t" \
  "v_mfma_f32_16x16x32_bf16 a[108:111], a[152:155], v[152:155], a[108:111]\n\t" \
  "v_exp_f32 v67, v67\n\t" \
  "v_mfma_f32_16x16x32_bf16 a[108:111], a[156:159], v[156:159], a[108:111]\n\t" \
  "v_exp_f32 v68, v68\n\t" \
  "v_mfma_f32_16x16x32_bf16 a[112:115], a[160:163], v[152:155], a[112:115]\n\t" \
  "v_exp_f32 v69, v69\n\t" \
  "v_mfma_f32_16x16x32_bf16 a[112:115], a[164:167], v[156:159], a[112:115]\n\t" \
  "v_exp_f32 v70, v70\n\t" \
  "v_mfma_f32_16x16x32_bf16 a[116:119], a[168:171], v[152:155], a[116:119]\n\t" \
  "v_exp_f32 v71, v71\n\t" \
  "v_mfma_f32_16x16x32_bf16 a[116:119], a[172:175], v[156:159], a[116:119]\n\t" \
  "v_exp_f32 v72, v72\n\t" \
  "v_mfma_f32_16x16x32_bf16 a[120:123], a[176:179], v[152:155], a[120:123]\n\t" \
  "v_exp_f32 v73, v73\n\t" \
  "v_mfma_f32_16x16x32_bf16 a[120:123], a[180:183], v[156:159], a[120:123]\n\t" \
  "v_exp_f32 v74, v74\n\t" \
  "v_mfma_f32_16x16x32_bf16 a[124:127], a[184:187], v[152:155], a[124:127]\n\t" \
  "v_exp_f32 v75, v75\n\t" \
  "v_mfma_f32_16x16x32_bf16 a[124:127], a[188:191], v[156:159], a[124:127]\n\t" \
  "v_exp_f32 v76, v76\n\t" \
  "v_mfma_f32_16x16x32_bf16 v[172:175], v[216:219], v[152:155], v[172:175]\n\t" \
  "v_exp_f32 v77, v77\n\t" \
  "v_mfma_f32_16x16x32_bf16 v[172:175], v[216:219], v[156:159], v[172:175]\n\t" \
  "s_waitcnt lgkmcnt(12)\n\t" \
  "s_cmp_lg_u32 s99, 0\n\t" \
  "s_cbranch_scc1 .Lsf_p4\n\t" \
  ".Lsf_q4:\n\t" \
  "v_mfma_f32_16x16x32_bf16 v[0:3], a[192:195], v[16:19], v[176:179]\n\t" \
  "ds_read_b64_tr_b16 a[128:129], v228 offset:0\n\t" \
  "v_cvt_pk_bf16_f32 v144, v64, v65\n\t" \
  "v_mfma_f32_16x16x32_bf16 v[0:3], a[196:199], v[20:23], v[0:3]\n\t" \
  "v_exp_f32 v78, v78\n\t" \
  "v_mfma_f32_16x16x32_bf16 v[0:3], a[200:203], v[24:27], v[0:3]\n\t" \
  "ds_read_b64_tr_b16 a[130:131], v228 offset:4096\n\t" \
  "v_cvt_pk_bf16_f32 v145, v66, v67\n\t" \
  "v_mfma_f32_16x16x32_bf16 v[0:3], a[204:207], v[28:31], v[0:3]\n\t" \
  "s_waitcnt lgkmcnt(10)\n\t" \
  "v_exp_f32 v79, v79\n\t" \
  "v_mfma_f32_16x16x32_bf16 v[4:7], a[220:223], v[28:31], v[176:179]\n\t" \
  "ds_read_b64_tr_b16 a[132:133], v228 offset:8192\n\t" \
  "v_cvt_pk_bf16_f32 v146, v68, v69\n\t" \
  "v_mfma_f32_16x16x32_bf16 v[4:7], a[216:219], v[24:27], v[4:7]\n\t" \
  "v_exp_f32 v96, v96\n\t" \
  "v_mfma_f32_16x16x32_bf16 v[4:7], a[212:215], v[20:23], v[4:7]\n\t" \
  "ds_read_b64_tr_b16 a[134:135], v228 offset:12288\n\t" \
  "v_max3_f32 v192, v0, v1, v2\n\t" \
  "v_mfma_f32_16x16x32_bf16 v[4:7], a[208:211], v[16:19], v[4:7]\n\t" \
  "s_waitcnt lgkmcnt(8)\n\t" \
  "ds_read_b64_tr_b16 a[136:137], v229 offset:0\n\t" \
  "v_cvt_pk_bf16_f32 v147, v70, v71\n\t" \
  "v_mfma_f32_16x16x32_bf16 v[8:11], a[224:227], v[16:19], v[176:179]\n\t" \
  "v_exp_f32 v97, v97\n\t" \
  "v_mfma_f32_16x16x32_bf16 v[8:11], a[228:231], v[20:23], v[8:11]\n\t" \
  "ds_read_b64_tr_b16 a[138:139], v229 offset:4096\n\t" \
  "v_cvt_pk_bf16_f32 v148, v72, v73\n\t" \
  "v_mfma_f32_16x16x32_bf16 v[8:11], a[232:235], v[24:27], v[8:11]\n\t" \
  "v_max3_f32 v203, v3, v4, v5\n\t" \
  "v_max3_f32 v192, v192, v6, v7\n\t" \
  "v_mfma_f32_16x16x32_bf16 v[8:11], a[236:239], v[28:31], v[8:11]\n\t" \
  "s_waitcnt lgkmcnt(6)\n\t" \
  "ds_read_b64_tr_b16 a[140:141], v229 offset:8192\n\t" \
  "v_cvt_pk_bf16_f32 v149, v74, v75\n\t" \
  "v_mfma_f32_16x16x32_bf16 v[12:15], a[252:255], v[28:31], v[176:179]\n\t" \
  "v_exp_f32 v98, v98\n\t" \
  "v_mfma_f32_16x16x32_bf16 v[12:15], a[248:251], v[24:27], v[12:15]\n\t" \
  "ds_read_b64_tr_b16 a[142:143], v229 offset:12288\n\t" \
  "v_cvt_pk_bf16_f32 v150, v76, v77\n\t" \
  "v_mfma_f32_16x16x32_bf16 v[12:15], a[244:247], v[20:23], v[12:15]\n\t" \
  "v_max3_f32 v203, v203, v8, v9\n\t" \
  "v_max3_f32 v192, v192, v10, v11\n\t" \
  "v_mfma_f32_16x16x32_bf16 v[12:15], a[240:243], v[16:19], v[12:15]\n\t" \
  "ds_read_b64_tr_b16 a[144:145], v228 offset:256\n\t" \
  "v_cvt_pk_bf16_f32 v151, v78, v79\n\t" \
  "v_mfma_f32_16x16x32_bf16 v[32:35], a[192:195], v[48:51], v[180:183]\n\t" \
  "ds_read_b64_tr_b16 a[146:147], v228 offset:4352\n\t" \
  "v_cvt_pk_bf16_f32 v152, v96, v97\n\t" \
  "v_mfma_f32_16x16x32_bf16 v[32:35], a[196:199], v[52:55], v[32:35]\n\t" \
  "v_exp_f32 v99, v99\n\t" \
  "v_mfma_f32_16x16x32_bf16 v[32:35], a[200:203], v[56:59], v[32:35]\n\t" \
  "v_max3_f32 v203, v203, v12, v13\n\t" \
  "v_max3_f32 v192, v192, v14, v15\n\t" \
  "v_mfma_f32_16x16x32_bf16 v[32:35], a[204:207], v[60:63], v[32:35]\n\t" \
  "v_max_f32 v192, v192, v203\n\t" \
  "v_cmp_lt_f32 vcc, 0x42800000, v192\n\t" \
  "v_mfma_f32_16x16x32_bf16 v[36:39], a[220:223], v[60:63], v[180:183]\n\t" \
  "ds_read_b64_tr_b16 a[148:149], v228 offset:8448\n\t" \
  "v_cvt_pk_bf16_f32 v153, v98, v99\n\t" \
  "v_mfma_f32_16x16x32_bf16 v[36:39], a[216:219], v[56:59], v[36:39]\n\t" \
  "s_cbranch_vccnz .Lsf_c50\n\t" \
  ".Lsf_r50:\n\t" \
  "v_mfma_f32_16x16x32_bf16 v[36:39], a[212:215], v[52:55], v[36:39]\n\t" \
  "ds_read_b64_tr_b16 a[150:151], v228 offset:12544\n\t" \
  "v_max3_f32 v192, v32, v33, v34\n\t" \
  "v_mfma_f32_16x16x32_bf16 v[36:39], a[208:211], v[48:51], v[36:39]\n\t" \
  "v_exp_f32 v100, v100\n\t" \
  "v_mfma_f32_16x16x32_bf16 v[40:43], a[224:227], v[48:51], v[180:183]\n\t" \
  "v_exp_f32 v101, v101\n\t" \
  "v_mfma_f32_16x16x32_bf16 v[40:43], a[228:231], v[52:55], v[40:43]\n\t" \
  "ds_read_b64_tr_b16 a[152:153], v229 offset:256\n\t" \
  "v_cvt_pk_bf16_f32 v154, v100, v101\n\t" \
  "v_mfma_f32_16x16x32_bf16 v[40:43], a[232:235], v[56:59], v[40:43]\n\t" \
  "ds_read_b64_tr_b16 a[154:155], v229 offset:4352\n\t" \
  "v_max3_f32 v203, v35, v36, v37\n\t" \
  "v_mfma_f32_16x16x32_bf16 v[40:43], a[236:239], v[60:63], v[40:43]\n\t" \
  "ds_read_b64_tr_b16 a[156:157], v229 offset:8448\n\t" \
  "v_max3_f32 v192, v192, v38, v39\n\t" \
  "v_mfma_f32_16x16x32_bf16 v[44:47], a[252:255], v[60:63], v[180:183]\n\t" \
  "v_exp_f32 v102, v102\n\t" \
  "v_mfma_f32_16x16x32_bf16 v[44:47], a[248:251], v[56:59], v[44:47]\n\t" \
  "v_exp_f32 v103, v103\n\t" \
  "v_mfma_f32_16x16x32_bf16 v[44:47], a[244:247], v[52:55], v[44:47]\n\t" \
  "ds_read_b64_tr_b16 a[158:159], v229 offset:12544\n\t" \
  "v_max3_f32 v203, v203, v40, v41\n\t" \
  "v_mfma_f32_16x16x32_bf16 v[44:47], a[240:243], v[48:51], v[44:47]\n\t" \
  "ds_read_b64_tr_b16 a[160:161], v228 offset:512\n\t" \
  "v_max3_f32 v192, v192, v42, v43\n\t" \
  "v_mfma_f32_16x16x32_bf16 v[64:67], a[192:195], v[80:83], v[184:187]\n\t" \
  "ds_read_b64_tr_b16 a[162:163], v228 offset:4608\n\t" \
  "v_cvt_pk_bf16_f32 v155, v102, v103\n\t" \
  "v_mfma_f32_16x16x32_bf16 v[64:67], a[196:199], v[84:87], v[64:67]\n\t" \
  "v_exp_f32 v104, v104\n\t" \
  "v_mfma_f32_16x16x32_bf16 v[64:67], a[200:203], v[88:91], v[64:67]\n\t" \
  "v_max3_f32 v203, v203, v44, v45\n\t" \
  "v_max3_f32 v192, v192, v46, v47\n\t" \
  "v_mfma_f32_16x16x32_bf16 v[64:67], a[204:207], v[92:95], v[64:67]\n\t" \
  "v_max_f32 v192, v192, v203\n\t" \
  "v_cmp_lt_f32 vcc, 0x42800000, v192\n\t" \
  "v_mfma_f32_16x16x32_bf16 v[68:71], a[220:223], v[92:95], v[184:187]\n\t" \
  "v_exp_f32 v105, v105\n\t" \
  "v_mfma_f32_16x16x32_bf16 v[68:71], a[216:219], v[88:91], v[68:71]\n\t" \
  "s_cbranch_vccnz .Lsf_c51\n\t" \
  ".Lsf_r51:\n\t" \
  "v_mfma_f32_16x16x32_bf16 v[68:71], a[212:215], v[84:87], v[68:71]\n\t" \
  "ds_read_b64_tr_b16 a[164:165], v228 offset:8704\n\t" \
  "v_max3_f32 v192, v64, v65, v66\n\t" \
  "v_mfma_f32_16x16x32_bf16 v[68:71], a[208:211], v[80:83], v[68:71]\n\t" \
  "ds_read_b64_tr_b16 a[166:167], v228 offset:12800\n\t" \
  "v_cvt_pk_bf16_f32 v156, v104, v105\n\t" \
  "v_mfma_f32_16x16x32_bf16 v[72:75], a[224:227], v[80:83], v[184:187]\n\t" \
  "v_exp_f32 v106, v106\n\t" \
  "v_mfma_f32_16x16x32_bf16 v[72:75], a[228:231], v[84:87], v[72:75]\n\t" \
  "v_exp_f32 v107, v107\n\t" \
  "v_mfma_f32_16x16x32_bf16 v[72:75], a[232:235], v[88:91], v[72:75]\n\t" \
  "ds_read_b64_tr_b16 a[168:169], v229 offset:512\n\t" \
  "v_max3_f32 v203, v67, v68, v69\n\t" \
  "v_mfma_f32_16x16x32_bf16 v[72:75], a[236:239], v[92:95], v[72:75]\n\t" \
  "ds_read_b64_tr_b16 a[170:171], v229 offset:4608\n\t" \
  "v_max3_f32 v192, v192, v70, v71\n\t" \
  "v_mfma_f32_16x16x32_bf16 v[76:79], a[252:255], v[92:95], v[184:187]\n\t" \
  "ds_read_b64_tr_b16 a[172:173], v229 offset:8704\n\t" \
  "v_cvt_pk_bf16_f32 v157, v106, v107\n\t" \
  "v_mfma_f32_16x16x32_bf16 v[76:79], a[248:251], v[88:91], v[76:79]\n\t" \
  "v_exp_f32 v108, v108\n\t" \
  "v_mfma_f32_16x16x32_bf16 v[76:79], a[244:247], v[84:87], v[76:79]\n\t" \
  "v_max3_f32 v203, v203, v72, v73\n\t" \
  "v_max3_f32 v192, v192, v74, v75\n\t" \
  "v_mfma_f32_16x16x32_bf16 v[76:79], a[240:243], v[80:83], v[76:79]\n\t" \
  "v_exp_f32 v109, v109\n\t" \
  "v_mfma_f32_16x16x32_bf16 v[96:99], a[192:195], v[112:115], v[188:191]\n\t" \
  "ds_read_b64_tr_b16 a[174:175], v229 offset:12800\n\t" \
  "v_cvt_pk_bf16_f32 v158, v108, v109\n\t" \
  "v_mfma_f32_16x16x32_bf16 v[96:99], a[196:199], v[116:119], v[96:99]\n\t" \
  "v_exp_f32 v110, v110\n\t" \
  "v_mfma_f32_16x16x32_bf16 v[96:99], a[200:203], v[120:123], v[96:99]\n\t" \
  "ds_read_b64_tr_b16 a[176:177], v228 offset:768\n\t" \
  "v_max3_f32 v203, v203, v76, v77\n\t" \
  "v_mfma_f32_16x16x32_bf16 v[96:99], a[204:207], v[124:127], v[96:99]\n\t" \
  "v_max3_f32 v192, v192, v78, v79\n\t" \
  "v_max_f32 v192, v192, v203\n\t" \
  "v_mfma_f32_16x16x32_bf16 v[100:103], a[220:223], v[124:127], v[188:191]\n\t" \
  "ds_read_b64_tr_b16 a[178:179], v228 offset:4864\n\t" \
  "v_cmp_lt_f32 vcc, 0x42800000, v192\n\t" \
  "v_mfma_f32_16x16x32_bf16 v[100:103], a[216:219], v[120:123], v[100:103]\n\t" \
  "ds_read_b64_tr_b16 a[180:181], v228 offset:8960\n\t" \
  "v_exp_f32 v111, v111\n\t" \
  "v_mfma_f32_16x16x32_bf16 v[100:103], a[212:215], v[116:119], v[100:103]\n\t" \
  "s_cbranch_vccnz .Lsf_c52\n\t" \
  ".Lsf_r52:\n\t" \
  "v_mfma_f32_16x16x32_bf16 v[100:103], a[208:211], v[112:115], v[100:103]\n\t" \
  "ds_read_b64_tr_b16 a[182:183], v228 offset:13056\n\t" \
  "v_max3_f32 v192, v96, v97, v98\n\t" \
  "v_mfma_f32_16x16x32_bf16 v[104:107], a[224:227], v[112:115], v[188:191]\n\t" \
  "ds_read_b64_tr_b16 a[184:185], v229 offset:768\n\t" \
  "v_cvt_pk_bf16_f32 v159, v110, v111\n\t" \
  "v_mfma_f32_16x16x32_bf16 v[104:107], a[228:231], v[116:119], v[104:107]\n\t" \
  "ds_read_b64_tr_b16 a[186:187], v229 offset:4864\n\t" \
  "v_mfma_f32_16x16x32_bf16 v[104:107], a[232:235], v[120:123], v[104:107]\n\t" \
  "ds_read_b64_tr_b16 a[188:189], v229 offset:8960\n\t" \
  "v_max3_f32 v203, v99, v100, v101\n\t" \
  "v_mfma_f32_16x16x32_bf16 v[104:107], a[236:239], v[124:127], v[104:107]\n\t" \
  "ds_read_b64_tr_b16 a[190:191], v229 offset:13056\n\t" \
  "v_max3_f32 v192, v192, v102, v103\n\t" \
  "v_mfma_f32_16x16x32_bf16 v[108:111], a[252:255], v[124:127], v[188:191]\n\t" \
  "v_exp_f32 v0, v0\n\t" \
  "v_mfma_f32_16x16x32_bf16 v[108:111], a[248:251], v[120:123], v[108:111]\n\t" \
  "v_exp_f32 v1, v1\n\t" \
  "v_mfma_f32_16x16x32_bf16 v[108:111], a[244:247], v[116:119], v[108:111]\n\t" \
  "s_waitcnt vmcnt(0) lgkmcnt(0)\n\t" \
  "v_max3_f32 v203, v203, v104, v105\n\t" \
  "v_max3_f32 v192, v192, v106, v107\n\t" \
  "v_mfma_f32_16x16x32_bf16 v[108:111], a[240:243], v[112:115], v[108:111]\n\t" \
  "s_barrier\n\t" \
  "v_mfma_f32_16x16x32_bf16 a[0:3], a[128:131], v[128:131], a[0:3]\n\t" \
  "v_exp_f32 v2, v2\n\t" \
  "v_mfma_f32_16x16x32_bf16 a[0:3], a[132:135], v[132:135], a[0:3]\n\t" \
  "v_exp_f32 v3, v3\n\t" \
  "v_mfma_f32_16x16x32_bf16 a[4:7], a[136:139], v[128:131], a[4:7]\n\t" \
  "v_exp_f32 v4, v4\n\t" \
  "v_mfma_f32_16x16x32_bf16 a[4:7], a[140:143], v[132:135], a[4:7]\n\t" \
  "v_max3_f32 v203, v203, v108, v109\n\t" \
  "v_max3_f32 v192, v192, v110, v111\n\t" \
  "v_mfma_f32_16x16x32_bf16 a[8:11], a[144:147], v[128:131], a[8:11]\n\t" \
  "v_max_f32 v192, v192, v203\n\t" \
  "v_cmp_lt_f32 vcc, 0x42800000, v192\n\t" \
  "v_mfma_f32_16x16x32_bf16 a[8:11], a[148:151], v[132:135], a[8:11]\n\t" \
  "v_exp_f32 v5, v5\n\t" \
  "v_mfma_f32_16x16x32_bf16 a[12:15], a[152:155], v[128:131], a[12:15]\n\t" \
  "s_cbranch_vccnz .Lsf_c53\n\t" \
  ".Lsf_r53:\n\t" \
  "v_mfma_f32_16x16x32_bf16 a[12:15], a[156:159], v[132:135], a[12:15]\n\t" \
  "s_add_u32 m0, s38, 65536\n\t" \
  "v_exp_f32 v6, v6\n\t" \
  "v_mfma_f32_16x16x32_bf16 a[16:19], a[160:163], v[128:131], a[16:19]\n\t" \
  "s_add_u32 s96, s38, 114688\n\t" \
  "v_exp_f32 v7, v7\n\t" \
  "v_mfma_f32_16x16x32_bf16 a[16:19], a[164:167], v[132:135], a[16:19]\n\t" \
  "v_exp_f32 v8, v8\n\t" \
  "v_mfma_f32_16x16x32_bf16 a[20:23], a[168:171], v[128:131], a[20:23]\n\t" \
  "buffer_load_dwordx4 v224, s[60:63], s96 offen offset:0 lds\n\t" \
  "v_mfma_f32_16x16x32_bf16 a[20:23], a[172:175], v[132:135], a[20:23]\n\t" \
  "v_exp_f32 v9, v9\n\t" \
  "v_mfma_f32_16x16x32_bf16 a[24:27], a[176:179], v[128:131], a[24:27]\n\t" \
  "v_exp_f32 v10, v10\n\t" \
  "v_mfma_f32_16x16x32_bf16 a[24:27], a[180:183], v[132:135], a[24:27]\n\t" \
  "v_exp_f32 v11, v11\n\t" \
  "v_mfma_f32_16x16x32_bf16 a[28:31], a[184:187], v[128:131], a[28:31]\n\t" \
  "v_exp_f32 v12, v12\n\t" \
  "v_mfma_f32_16x16x32_bf16 a[28:31], a[188:191], v[132:135], a[28:31]\n\t" \
  "v_exp_f32 v13, v13\n\t" \
  "v_mfma_f32_16x16x32_bf16 v[160:163], v[216:219], v[128:131], v[160:163]\n\t" \
  "v_exp_f32 v14, v14\n\t" \
  "v_mfma_f32_16x16x32_bf16 v[160:163], v[216:219], v[132:135], v[160:163]\n\t" \
  "buffer_load_dwordx4 v225, s[60:63], s96 offen offset:1024 lds\n\t" \
  "v_mfma_f32_16x16x32_bf16 a[32:35], a[128:131], v[136:139], a[32:35]\n\t" \
  "ds_read_b128 a[192:195], v220 offset:0\n\t" \
  "v_cvt_pk_bf16_f32 v128, v0, v1\n\t" \
  "v_mfma_f32_16x16x32_bf16 a[32:35], a[132:135], v[140:143], a[32:35]\n\t" \
  "ds_read_b128 a[196:199], v221 offset:0\n\t" \
  "v_cvt_pk_bf16_f32 v129, v2, v3\n\t" \
  "v_mfma_f32_16x16x32_bf16 a[36:39], a[136:139], v[136:139], a[36:39]\n\t" \
  "v_exp_f32 v15, v15\n\t" \
  "v_mfma_f32_16x16x32_bf16 a[36:39], a[140:143], v[140:143], a[36:39]\n\t" \
  "v_exp_f32 v32, v32\n\t" \
  "v_mfma_f32_16x16x32_bf16 a[40:43], a[144:147], v[136:139], a[40:43]\n\t" \
  "ds_read_b128 a[200:203], v222 offset:0\n\t" \
  "v_cvt_pk_bf16_f32 v130, v4, v5\n\t" \
  "v_mfma_f32_16x16x32_bf16 a[40:43], a[148:151], v[140:143], a[40:43]\n\t" \
  "buffer_load_dwordx4 v226, s[60:63], s96 offen offset:2048 lds\n\t" \
  "v_mfma_f32_16x16x32_bf16 a[44:47], a[152:155], v[136:139], a[44:47]\n\t" \
  "ds_read_b128 a[204:207], v223 offset:0\n\t" \
  "v_cvt_pk_bf16_f32 v131, v6, v7\n\t" \
  "v_mfma_f32_16x16x32_bf16 a[44:47], a[156:159], v[140:143], a[44:47]\n\t" \
  "v_exp_f32 v33, v33\n\t" \
  "v_mfma_f32_16x16x32_bf16 a[48:51], a[160:163], v[136:139], a[48:51]\n\t" \
  "v_exp_f32 v34, v34\n\t" \
  "v_mfma_f32_16x16x32_bf16 a[48:51], a[164:167], v[140:143], a[48:51]\n\t" \
  "ds_read_b128 a[208:211], v220 offset:4096\n\t" \
  "v_cvt_pk_bf16_f32 v132, v8, v9\n\t" \
  "v_mfma_f32_16x16x32_bf16 a[52:55], a[168:171], v[136:139], a[52:55]\n\t" \
  "v_exp_f32 v35, v35\n\t" \
  "v_mfma_f32_16x16x32_bf16 a[52:55], a[172:175], v[140:143], a[52:55]\n\t" \
  "buffer_load_dwordx4 v227, s[60:63], s96 offen offset:3072 lds\n\t" \
  "v_mfma_f32_16x16x32_bf16 a[56:59], a[176:179], v[136:139], a[56:59]\n\t" \
  "ds_read_b128 a[212:215], v221 offset:4096\n\t" \
  "v_cvt_pk_bf16_f32 v133, v10, v11\n\t" \
  "v_mfma_f32_16x16x32_bf16 a[56:59], a[180:183], v[140:143], a[56:59]\n\t" \
  "s_add_u32 m0, s38, 81920\n\t" \
  "v_exp_f32 v36, v36\n\t" \
  "v_mfma_f32_16x16x32_bf16 a[60:63], a[184:187], v[136:139], a[60:63]\n\t" \
  "s_add_u32 s96, s38, 98304\n\t" \
  "v_exp_f32 v37, v37\n\t" \
  "v_mfma_f32_16x16x32_bf16 a[60:63], a[188:191], v[140:143], a[60:63]\n\t" \
  "ds_read_b128 a[216:219], v222 offset:4096\n\t" \
  "v_cvt_pk_bf16_f32 v134, v12, v13\n\t" \
  "v_mfma_f32_16x16x32_bf16 v[164:167], v[216:219], v[136:139], v[164:167]\n\t" \
  "v_exp_f32 v38, v38\n\t" \
  "v_mfma_f32_16x16x32_bf16 v[164:167], v[216:219], v[140:143], v[164:167]\n\t" \
  "buffer_load_dwordx4 v230, s[64:67], s96 offen offset:0 lds\n\t" \
  "v_mfma_f32_16x16x32_bf16 a[64:67], a[128:131], v[144:147], a[64:67]\n\t" \
  "ds_read_b128 a[220:223], v223 offset:4096\n\t" \
  "v_cvt_pk_bf16_f32 v135, v14, v15\n\t" \
  "v_mfma_f32_16x16x32_bf16 a[64:67], a[132:135], v[148:151], a[64:67]\n\t" \
  "v_exp_f32 v39, v39\n\t" \
  "v_mfma_f32_16x16x32_bf16 a[68:71], a[136:139], v[144:147], a[68:71]\n\t" \
  "v_exp_f32 v40, v40\n\t" \
  "v_mfma_f32_16x16x32_bf16 a[68:71], a[140:143], v[148:151], a[68:71]\n\t" \
  "ds_read_b128 a[224:227], v220 offset:8192\n\t" \
  "v_cvt_pk_bf16_f32 v136, v32, v33\n\t" \
  "v_mfma_f32_16x16x32_bf16 a[72:75], a[144:147], v[144:147], a[72:75]\n\t" \
  "v_exp_f32 v41, v41\n\t" \
  "v_mfma_f32_16x16x32_bf16 a[72:75], a[148:151], v[148:151], a[72:75]\n\t" \
  "ds_read_b128 a[228:231], v221 offset:8192\n\t" \
  "v_cvt_pk_bf16_f32 v137, v34, v35\n\t" \
  "v_mfma_f32_16x16x32_bf16 a[76:79], a[152:155], v[144:147], a[76:79]\n\t" \
  "buffer_load_dwordx4 v231, s[64:67], s96 offen offset:1024 lds\n\t" \
  "v_mfma_f32_16x16x32_bf16 a[76:79], a[156:159], v[148:151], a[76:79]\n\t" \
  "v_exp_f32 v42, v42\n\t" \
  "v_mfma_f32_16x16x32_bf16 a[80:83], a[160:163], v[144:147], a[80:83]\n\t" \
  "ds_read_b128 a[232:235], v222 offset:8192\n\t" \
  "v_cvt_pk_bf16_f32 v138, v36, v37\n\t" \
  "v_mfma_f32_16x16x32_bf16 a[80:83], a[164:167], v[148:151], a[80:83]\n\t" \
  "v_exp_f32 v43, v43\n\t" \
  "v_mfma_f32_16x16x32_bf16 a[84:87], a[168:171], v[144:147], a[84:87]\n\t" \
  "v_exp_f32 v44, v44\n\t" \
  "v_mfma_f32_16x16x32_bf16 a[84:87], a[172:175], v[148:151], a[84:87]\n\t" \
  "ds_read_b128 a[236:239], v223 offset:8192\n\t" \
  "v_cvt_pk_bf16_f32 v139, v38, v39\n\t" \
  "v_mfma_f32_16x16x32_bf16 a[88:91], a[176:179], v[144:147], a[88:91]\n\t" \
  "buffer_load_dwordx4 v230, s[64:67], s96 offen offset:2048 lds\n\t" \
  "v_mfma_f32_16x16x32_bf16 a[88:91], a[180:183], v[148:151], a[88:91]\n\t" \
  "v_exp_f32 v45, v45\n\t" \
  "v_mfma_f32_16x16x32_bf16 a[92:95], a[184:187], v[144:147], a[92:95]\n\t" \
  "ds_read_b128 a[240:243], v220 offset:12288\n\t" \
  "v_cvt_pk_bf16_f32 v140, v40, v41\n\t" \
  "v_mfma_f32_16x16x32_bf16 a[92:95], a[188:191], v[148:151], a[92:95]\n\t" \
  "v_exp_f32 v46, v46\n\t" \
  "v_mfma_f32_16x16x32_bf16 v[168:171], v[216:219], v[144:147], v[168:171]\n\t" \
  "v_exp_f32 v47, v47\n\t" \
  "v_mfma_f32_16x16x32_bf16 v[168:171], v[216:219], v[148:151], v[168:171]\n\t" \
  "ds_read_b128 a[244:247], v221 offset:12288\n\t" \
  "v_cvt_pk_bf16_f32 v141, v42, v43\n\t" \
  "v_mfma_f32_16x16x32_bf16 a[96:99], a[128:131], v[152:155], a[96:99]\n\t" \
  "buffer_load_dwordx4 v231, s[64:67], s96 offen offset:3072 lds\n\t" \
  "v_mfma_f32_16x16x32_bf16 a[96:99], a[132:135], v[156:159], a[96:99]\n\t" \
  "v_exp_f32 v64, v64\n\t" \
  "v_mfma_f32_16x16x32_bf16 a[100:103], a[136:139], v[152:155], a[100:103]\n\t" \
  "ds_read_b128 a[248:251], v222 offset:12288\n\t" \
  "v_cvt_pk_bf16_f32 v142, v44, v45\n\t" \
  "v_mfma_f32_16x16x32_bf16 a[100:103], a[140:143], v[156:159], a[100:103]\n\t" \
  "v_exp_f32 v65, v65\n\t" \
  "v_mfma_f32_16x16x32_bf16 a[104:107], a[144:147], v[152:155], a[104:107]\n\t" \
  "v_exp_f32 v66, v66\n\t" \
  "v_mfma_f32_16x16x32_bf16 a[104:107], a[148:151], v[156:159], a[104:107]\n\t" \
  "ds_read_b128 a[252:255], v223 offset:12288\n\t" \
  "v_cvt_pk_bf16_f32 v143, v46, v47\n\t" \
  "v_mfma_f32_16x16x32_bf16 a[108:111], a[152:155], v[152:155], a[108:111]\n\t" \
  "v_exp_f32 v67, v67\n\t" \
  "v_mfma_f32_16x16x32_bf16 a[108:111], a[156:159], v[156:159], a[108:111]\n\t" \
  "v_exp_f32 v68, v68\n\t" \
  "v_mfma_f32_16x16x32_bf16 a[112:115], a[160:163], v[152:155], a[112:115]\n\t" \
  "v_exp_f32 v69, v69\n\t" \
  "v_mfma_f32_16x16x32_bf16 a[112:115], a[164:167], v[156:159], a[112:115]\n\t" \
  "v_exp_f32 v70, v70\n\t" \
  "v_mfma_f32_16x16x32_bf16 a[116:119], a[168:171], v[152:155], a[116:119]\n\t" \
  "v_exp_f32 v71, v71\n\t" \
  "v_mfma_f32_16x16x32_bf16 a[116:119], a[172:175], v[156:159], a[116:119]\n\t" \
  "v_exp_f32 v72, v72\n\t" \
  "v_mfma_f32_16x16x32_bf16 a[120:123], a[176:179], v[152:155], a[120:123]\n\t" \
  "v_exp_f32 v73, v73\n\t" \
  "v_mfma_f32_16x16x32_bf16 a[120:123], a[180:183], v[156:159], a[120:123]\n\t" \
  "v_exp_f32 v74, v74\n\t" \
  "v_mfma_f32_16x16x32_bf16 a[124:127], a[184:187], v[152:155], a[124:127]\n\t" \
  "v_exp_f32 v75, v75\n\t" \
  "v_mfma_f32_16x16x32_bf16 a[124:127], a[188:191], v[156:159], a[124:127]\n\t" \
  "v_exp_f32 v76, v76\n\t" \
  "v_mfma_f32_16x16x32_bf16 v[172:175], v[216:219], v[152:155], v[172:175]\n\t" \
  "v_exp_f32 v77, v77\n\t" \
  "v_mfma_f32_16x16x32_bf16 v[172:175], v[216:219], v[156:159], v[172:175]\n\t" \
  "s_waitcnt lgkmcnt(12)\n\t" \
  "s_cmp_lg_u32 s99, 0\n\t" \
  "s_cbranch_scc1 .Lsf_p5\n\t" \
  ".Lsf_q5:\n\t" \
  "v_mfma_f32_16x16x32_bf16 v[0:3], a[192:195], v[16:19], v[176:179]\n\t" \
  "ds_read_b64_tr_b16 a[128:129], v210 offset:0\n\t" \
  "v_cvt_pk_bf16_f32 v144, v64, v65\n\t" \
  "v_mfma_f32_16x16x32_bf16 v[0:3], a[196:199], v[20:23], v[0:3]\n\t" \
  "v_exp_f32 v78, v78\n\t" \
  "v_mfma_f32_16x16x32_bf16 v[0:3], a[200:203], v[24:27], v[0:3]\n\t" \
  "ds_read_b64_tr_b16 a[130:131], v210 offset:4096\n\t" \
  "v_cvt_pk_bf16_f32 v145, v66, v67\n\t" \
  "v_mfma_f32_16x16x32_bf16 v[0:3], a[204:207], v[28:31], v[0:3]\n\t" \
  "s_waitcnt lgkmcnt(10)\n\t" \
  "v_exp_f32 v79, v79\n\t" \
  "v_mfma_f32_16x16x32_bf16 v[4:7], a[220:223], v[28:31], v[176:179]\n\t" \
  "ds_read_b64_tr_b16 a[132:133], v210 offset:8192\n\t" \
  "v_cvt_pk_bf16_f32 v146, v68, v69\n\t" \
  "v_mfma_f32_16x16x32_bf16 v[4:7], a[216:219], v[24:27], v[4:7]\n\t" \
  "v_exp_f32 v96, v96\n\t" \
  "v_mfma_f32_16x16x32_bf16 v[4:7], a[212:215], v[20:23], v[4:7]\n\t" \
  "ds_read_b64_tr_b16 a[134:135], v210 offset:12288\n\t" \
  "v_max3_f32 v192, v0, v1, v2\n\t" \
  "v_mfma_f32_16x16x32_bf16 v[4:7], a[208:211], v[16:19], v[4:7]\n\t" \
  "s_waitcnt lgkmcnt(8)\n\t" \
  "ds_read_b64_tr_b16 a[136:137], v211 offset:0\n\t" \
  "v_cvt_pk_bf16_f32 v147, v70, v71\n\t" \
  "v_mfma_f32_16x16x32_bf16 v[8:11], a[224:227], v[16:19], v[176:179]\n\t" \
  "v_exp_f32 v97, v97\n\t" \
  "v_mfma_f32_16x16x32_bf16 v[8:11], a[228:231], v[20:23], v[8:11]\n\t" \
  "ds_read_b64_tr_b16 a[138:139], v211 offset:4096\n\t" \
  "v_cvt_pk_bf16_f32 v148, v72, v73\n\t" \
  "v_mfma_f32_16x16x32_bf16 v[8:11], a[232:235], v[24:27], v[8:11]\n\t" \
  "v_max3_f32 v203, v3, v4, v5\n\t" \
  "v_max3_f32 v192, v192, v6, v7\n\t" \
  "v_mfma_f32_16x16x32_bf16 v[8:11], a[236:239], v[28:31], v[8:11]\n\t" \
  "s_waitcnt lgkmcnt(6)\n\t" \
  "ds_read_b64_tr_b16 a[140:141], v211 offset:8192\n\t" \
  "v_cvt_pk_bf16_f32 v149, v74, v75\n\t" \
  "v_mfma_f32_16x16x32_bf16 v[12:15], a[252:255], v[28:31], v[176:179]\n\t" \
  "v_exp_f32 v98, v98\n\t" \
  "v_mfma_f32_16x16x32_bf16 v[12:15], a[248:251], v[24:27], v[12:15]\n\t" \
  "ds_read_b64_tr_b16 a[142:143], v211 offset:12288\n\t" \
  "v_cvt_pk_bf16_f32 v150, v76, v77\n\t" \
  "v_mfma_f32_16x16x32_bf16 v[12:15], a[244:247], v[20:23], v[12:15]\n\t" \
  "v_max3_f32 v203, v203, v8, v9\n\t" \
  "v_max3_f32 v192, v192, v10, v11\n\t" \
  "v_mfma_f32_16x16x32_bf16 v[12:15], a[240:243], v[16:19], v[12:15]\n\t" \
  "ds_read_b64_tr_b16 a[144:145], v210 offset:256\n\t" \
  "v_cvt_pk_bf16_f32 v151, v78, v79\n\t" \
  "v_mfma_f32_16x16x32_bf16 v[32:35], a[192:195], v[48:51], v[180:183]\n\t" \
  "ds_read_b64_tr_b16 a[146:147], v210 offset:4352\n\t" \
  "v_cvt_pk_bf16_f32 v152, v96, v97\n\t" \
  "v_mfma_f32_16x16x32_bf16 v[32:35], a[196:199], v[52:55], v[32:35]\n\t" \
  "v_exp_f32 v99, v99\n\t" \
  "v_mfma_f32_16x16x32_bf16 v[32:35], a[200:203], v[56:59], v[32:35]\n\t" \
  "v_max3_f32 v203, v203, v12, v13\n\t" \
  "v_max3_f32 v192, v192, v14, v15\n\t" \
  "v_mfma_f32_16x16x32_bf16 v[32:35], a[204:207], v[60:63], v[32:35]\n\t" \
  "v_max_f32 v192, v192, v203\n\t" \
  "v_cmp_lt_f32 vcc, 0x42800000, v192\n\t" \
  "v_mfma_f32_16x16x32_bf16 v[36:39], a[220:223], v[60:63], v[180:183]\n\t" \
  "ds_read_b64_tr_b16 a[148:149], v210 offset:8448\n\t" \
  "v_cvt_pk_bf16_f32 v153, v98, v99\n\t" \
  "v_mfma_f32_16x16x32_bf16 v[36:39], a[216:219], v[56:59], v[36:39]\n\t" \
  "s_cbranch_vccnz .Lsf_c60\n\t" \
  ".Lsf_r60:\n\t" \
  "v_mfma_f32_16x16x32_bf16 v[36:39], a[212:215], v[52:55], v[36:39]\n\t" \
  "ds_read_b64_tr_b16 a[150:151], v210 offset:12544\n\t" \
  "v_max3_f32 v192, v32, v33, v34\n\t" \
  "v_mfma_f32_16x16x32_bf16 v[36:39], a[208:211], v[48:51], v[36:39]\n\t" \
  "v_exp_f32 v100, v100\n\t" \
  "v_mfma_f32_16x16x32_bf16 v[40:43], a[224:227], v[48:51], v[180:183]\n\t" \
  "v_exp_f32 v101, v101\n\t" \
  "v_mfma_f32_16x16x32_bf16 v[40:43], a[228:231], v[52:55], v[40:43]\n\t" \
  "ds_read_b64_tr_b16 a[152:153], v211 offset:256\n\t" \
  "v_cvt_pk_bf16_f32 v154, v100, v101\n\t" \
  "v_mfma_f32_16x16x32_bf16 v[40:43], a[232:235], v[56:59], v[40:43]\n\t" \
  "ds_read_b64_tr_b16 a[154:155], v211 offset:4352\n\t" \
  "v_max3_f32 v203, v35, v36, v37\n\t" \
  "v_mfma_f32_16x16x32_bf16 v[40:43], a[236:239], v[60:63], v[40:43]\n\t" \
  "ds_read_b64_tr_b16 a[156:157], v211 offset:8448\n\t" \
  "v_max3_f32 v192, v192, v38, v39\n\t" \
  "v_mfma_f32_16x16x32_bf16 v[44:47], a[252:255], v[60:63], v[180:183]\n\t" \
  "v_exp_f32 v102, v102\n\t" \
  "v_mfma_f32_16x16x32_bf16 v[44:47], a[248:251], v[56:59], v[44:47]\n\t" \
  "v_exp_f32 v103, v103\n\t" \
  "v_mfma_f32_16x16x32_bf16 v[44:47], a[244:247], v[52:55], v[44:47]\n\t" \
  "ds_read_b64_tr_b16 a[158:159], v211 offset:12544\n\t" \
  "v_max3_f32 v203, v203, v40, v41\n\t" \
  "v_mfma_f32_16x16x32_bf16 v[44:47], a[240:243], v[48:51], v[44:47]\n\t" \
  "ds_read_b64_tr_b16 a[160:161], v210 offset:512\n\t" \
  "v_max3_f32 v192, v192, v42, v43\n\t" \
  "v_mfma_f32_16x16x32_bf16 v[64:67], a[192:195], v[80:83], v[184:187]\n\t" \
  "ds_read_b64_tr_b16 a[162:163], v210 offset:4608\n\t" \
  "v_cvt_pk_bf16_f32 v155, v102, v103\n\t" \
  "v_mfma_f32_16x16x32_bf16 v[64:67], a[196:199], v[84:87], v[64:67]\n\t" \
  "v_exp_f32 v104, v104\n\t" \
  "v_mfma_f32_16x16x32_bf16 v[64:67], a[200:203], v[88:91], v[64:67]\n\t" \
  "v_max3_f32 v203, v203, v44, v45\n\t" \
  "v_max3_f32 v192, v192, v46, v47\n\t" \
  "v_mfma_f32_16x16x32_bf16 v[64:67], a[204:207], v[92:95], v[64:67]\n\t" \
  "v_max_f32 v192, v192, v203\n\t" \
  "v_cmp_lt_f32 vcc, 0x42800000, v192\n\t" \
  "v_mfma_f32_16x16x32_bf16 v[68:71], a[220:223], v[92:95], v[184:187]\n\t" \
  "v_exp_f32 v105, v105\n\t" \
  "v_mfma_f32_16x16x32_bf16 v[68:71], a[216:219], v[88:91], v[68:71]\n\t" \
  "s_cbranch_vccnz .Lsf_c61\n\t" \
  ".Lsf_r61:\n\t" \
  "v_mfma_f32_16x16x32_bf16 v[68:71], a[212:215], v[84:87], v[68:71]\n\t" \
  "ds_read_b64_tr_b16 a[164:165], v210 offset:8704\n\t" \
  "v_max3_f32 v192, v64, v65, v66\n\t" \
  "v_mfma_f32_16x16x32_bf16 v[68:71], a[208:211], v[80:83], v[68:71]\n\t" \
  "ds_read_b64_tr_b16 a[166:167], v210 offset:12800\n\t" \
  "v_cvt_pk_bf16_f32 v156, v104, v105\n\t" \
  "v_mfma_f32_16x16x32_bf16 v[72:75], a[224:227], v[80:83], v[184:187]\n\t" \
  "v_exp_f32 v106, v106\n\t" \
  "v_mfma_f32_16x16x32_bf16 v[72:75], a[228:231], v[84:87], v[72:75]\n\t" \
  "v_exp_f32 v107, v107\n\t" \
  "v_mfma_f32_16x16x32_bf16 v[72:75], a[232:235], v[88:91], v[72:75]\n\t" \
  "ds_read_b64_tr_b16 a[168:169], v211 offset:512\n\t" \
  "v_max3_f32 v203, v67, v68, v69\n\t" \
  "v_mfma_f32_16x16x32_bf16 v[72:75], a[236:239], v[92:95], v[72:75]\n\t" \
  "ds_read_b64_tr_b16 a[170:171], v211 offset:4608\n\t" \
  "v_max3_f32 v192, v192, v70, v71\n\t" \
  "v_mfma_f32_16x16x32_bf16 v[76:79], a[252:255], v[92:95], v[184:187]\n\t" \
  "ds_read_b64_tr_b16 a[172:173], v211 offset:8704\n\t" \
  "v_cvt_pk_bf16_f32 v157, v106, v107\n\t" \
  "v_mfma_f32_16x16x32_bf16 v[76:79], a[248:251], v[88:91], v[76:79]\n\t" \
  "v_exp_f32 v108, v108\n\t" \
  "v_mfma_f32_16x16x32_bf16 v[76:79], a[244:247], v[84:87], v[76:79]\n\t" \
  "v_max3_f32 v203, v203, v72, v73\n\t" \
  "v_max3_f32 v192, v192, v74, v75\n\t" \
  "v_mfma_f32_16x16x32_bf16 v[76:79], a[240:243], v[80:83], v[76:79]\n\t" \
  "v_exp_f32 v109, v109\n\t" \
  "v_mfma_f32_16x16x32_bf16 v[96:99], a[192:195], v[112:115], v[188:191]\n\t" \
  "ds_read_b64_tr_b16 a[174:175], v211 offset:12800\n\t" \
  "v_cvt_pk_bf16_f32 v158, v108, v109\n\t" \
  "v_mfma_f32_16x16x32_bf16 v[96:99], a[196:199], v[116:119], v[96:99]\n\t" \
  "v_exp_f32 v110, v110\n\t" \
  "v_mfma_f32_16x16x32_bf16 v[96:99], a[200:203], v[120:123], v[96:99]\n\t" \
  "ds_read_b64_tr_b16 a[176:177], v210 offset:768\n\t" \
  "v_max3_f32 v203, v203, v76, v77\n\t" \
  "v_mfma_f32_16x16x32_bf16 v[96:99], a[204:207], v[124:127], v[96:99]\n\t" \
  "v_max3_f32 v192, v192, v78, v79\n\t" \
  "v_max_f32 v192, v192, v203\n\t" \
  "v_mfma_f32_16x16x32_bf16 v[100:103], a[220:223], v[124:127], v[188:191]\n\t" \
  "ds_read_b64_tr_b16 a[178:179], v210 offset:4864\n\t" \
  "v_cmp_lt_f32 vcc, 0x42800000, v192\n\t" \
  "v_mfma_f32_16x16x32_bf16 v[100:103], a[216:219], v[120:123], v[100:103]\n\t" \
  "ds_read_b64_tr_b16 a[180:181], v210 offset:8960\n\t" \
  "v_exp_f32 v111, v111\n\t" \
  "v_mfma_f32_16x16x32_bf16 v[100:103], a[212:215], v[116:119], v[100:103]\n\t" \
  "s_cbranch_vccnz .Lsf_c62\n\t" \
  ".Lsf_r62:\n\t" \
  "v_mfma_f32_16x16x32_bf16 v[100:103], a[208:211], v[112:115], v[100:103]\n\t" \
  "ds_read_b64_tr_b16 a[182:183], v210 offset:13056\n\t" \
  "v_max3_f32 v192, v96, v97, v98\n\t" \
  "v_mfma_f32_16x16x32_bf16 v[104:107], a[224:227], v[112:115], v[188:191]\n\t" \
  "ds_read_b64_tr_b16 a[184:185], v211 offset:768\n\t" \
  "v_cvt_pk_bf16_f32 v159, v110, v111\n\t" \
  "v_mfma_f32_16x16x32_bf16 v[104:107], a[228:231], v[116:119], v[104:107]\n\t" \
  "ds_read_b64_tr_b16 a[186:187], v211 offset:4864\n\t" \
  "v_mfma_f32_16x16x32_bf16 v[104:107], a[232:235], v[120:123], v[104:107]\n\t" \
  "ds_read_b64_tr_b16 a[188:189], v211 offset:8960\n\t" \
  "v_max3_f32 v203, v99, v100, v101\n\t" \
  "v_mfma_f32_16x16x32_bf16 v[104:107], a[236:239], v[124:127], v[104:107]\n\t" \
  "ds_read_b64_tr_b16 a[190:191], v211 offset:13056\n\t" \
  "v_max3_f32 v192, v192, v102, v103\n\t" \
  "v_mfma_f32_16x16x32_bf16 v[108:111], a[252:255], v[124:127], v[188:191]\n\t" \
  "v_exp_f32 v0, v0\n\t" \
  "v_mfma_f32_16x16x32_bf16 v[108:111], a[248:251], v[120:123], v[108:111]\n\t" \
  "v_exp_f32 v1, v1\n\t" \
  "v_mfma_f32_16x16x32_bf16 v[108:111], a[244:247], v[116:119], v[108:111]\n\t" \
  "s_waitcnt vmcnt(0) lgkmcnt(0)\n\t" \
  "v_max3_f32 v203, v203, v104, v105\n\t" \
  "v_max3_f32 v192, v192, v106, v107\n\t" \
  "v_mfma_f32_16x16x32_bf16 v[108:111], a[240:243], v[112:115], v[108:111]\n\t" \
  "s_barrier\n\t" \
  "v_mfma_f32_16x16x32_bf16 a[0:3], a[128:131], v[128:131], a[0:3]\n\t" \
  "v_exp_f32 v2, v2\n\t" \
  "v_mfma_f32_16x16x32_bf16 a[0:3], a[132:135], v[132:135], a[0:3]\n\t" \
  "v_exp_f32 v3, v3\n\t" \
  "v_mfma_f32_16x16x32_bf16 a[4:7], a[136:139], v[128:131], a[4:7]\n\t" \
  "v_exp_f32 v4, v4\n\t" \
  "v_mfma_f32_16x16x32_bf16 a[4:7], a[140:143], v[132:135], a[4:7]\n\t" \
  "v_max3_f32 v203, v203, v108, v109\n\t" \
  "v_max3_f32 v192, v192, v110, v111\n\t" \
  "v_mfma_f32_16x16x32_bf16 a[8:11], a[144:147], v[128:131], a[8:11]\n\t" \
  "v_max_f32 v192, v192, v203\n\t" \
  "v_cmp_lt_f32 vcc, 0x42800000, v192\n\t" \
  "v_mfma_f32_16x16x32_bf16 a[8:11], a[148:151], v[132:135], a[8:11]\n\t" \
  "v_exp_f32 v5, v5\n\t" \
  "v_mfma_f32_16x16x32_bf16 a[12:15], a[152:155], v[128:131], a[12:15]\n\t" \
  "s_cbranch_vccnz .Lsf_c63\n\t" \
  ".Lsf_r63:\n\t" \
  "v_mfma_f32_16x16x32_bf16 a[12:15], a[156:159], v[132:135], a[12:15]\n\t" \
  "s_add_u32 m0, s38, 147456\n\t" \
  "v_exp_f32 v6, v6\n\t" \
  "v_mfma_f32_16x16x32_bf16 a[16:19], a[160:163], v[128:131], a[16:19]\n\t" \
  "s_add_u32 s96, s38, 114688\n\t" \
  "v_exp_f32 v7, v7\n\t" \
  "v_mfma_f32_16x16x32_bf16 a[16:19], a[164:167], v[132:135], a[16:19]\n\t" \
  "v_exp_f32 v8, v8\n\t" \
  "v_mfma_f32_16x16x32_bf16 a[20:23], a[168:171], v[128:131], a[20:23]\n\t" \
  "buffer_load_dwordx4 v230, s[64:67], s96 offen offset:0 lds\n\t" \
  "v_mfma_f32_16x16x32_bf16 a[20:23], a[172:175], v[132:135], a[20:23]\n\t" \
  "v_exp_f32 v9, v9\n\t" \
  "v_mfma_f32_16x16x32_bf16 a[24:27], a[176:179], v[128:131], a[24:27]\n\t" \
  "v_exp_f32 v10, v10\n\t" \
  "v_mfma_f32_16x16x32_bf16 a[24:27], a[180:183], v[132:135], a[24:27]\n\t" \
  "v_exp_f32 v11, v11\n\t" \
  "v_mfma_f32_16x16x32_bf16 a[28:31], a[184:187], v[128:131], a[28:31]\n\t" \
  "v_exp_f32 v12, v12\n\t" \
  "v_mfma_f32_16x16x32_bf16 a[28:31], a[188:191], v[132:135], a[28:31]\n\t" \
  "v_exp_f32 v13, v13\n\t" \
  "v_mfma_f32_16x16x32_bf16 v[160:163], v[216:219], v[128:131], v[160:163]\n\t" \
  "v_exp_f32 v14, v14\n\t" \
  "v_mfma_f32_16x16x32_bf16 v[160:163], v[216:219], v[132:135], v[160:163]\n\t" \
  "ds_read_b128 a[192:195], v220 offset:16384\n\t" \
  "v_cvt_pk_bf16_f32 v128, v0, v1\n\t" \
  "v_mfma_f32_16x16x32_bf16 a[32:35], a[128:131], v[136:139], a[32:35]\n\t" \
  "ds_read_b128 a[196:199], v221 offset:16384\n\t" \
  "v_cvt_pk_bf16_f32 v129, v2, v3\n\t" \
  "v_mfma_f32_16x16x32_bf16 a[32:35], a[132:135], v[140:143], a[32:35]\n\t" \
  "v_exp_f32 v15, v15\n\t" \
  "v_mfma_f32_16x16x32_bf16 a[36:39], a[136:139], v[136:139], a[36:39]\n\t" \
  "v_exp_f32 v32, v32\n\t" \
  "v_mfma_f32_16x16x32_bf16 a[36:39], a[140:143], v[140:143], a[36:39]\n\t" \
  "ds_read_b128 a[200:203], v222 offset:16384\n\t" \
  "v_cvt_pk_bf16_f32 v130, v4, v5\n\t" \
  "v_mfma_f32_16x16x32_bf16 a[40:43], a[144:147], v[136:139], a[40:43]\n\t" \
  "v_exp_f32 v33, v33\n\t" \
  "v_mfma_f32_16x16x32_bf16 a[40:43], a[148:151], v[140:143], a[40:43]\n\t" \
  "buffer_load_dwordx4 v231, s[64:67], s96 offen offset:1024 lds\n\t" \
  "v_mfma_f32_16x16x32_bf16 a[44:47], a[152:155], v[136:139], a[44:47]\n\t" \
  "ds_read_b128 a[204:207], v223 offset:16384\n\t" \
  "v_cvt_pk_bf16_f32 v131, v6, v7\n\t" \
  "v_mfma_f32_16x16x32_bf16 a[44:47], a[156:159], v[140:143], a[44:47]\n\t" \
  "v_exp_f32 v34, v34\n\t" \
  "v_mfma_f32_16x16x32_bf16 a[48:51], a[160:163], v[136:139], a[48:51]\n\t" \
  "v_exp_f32 v35, v35\n\t" \
  "v_mfma_f32_16x16x32_bf16 a[48:51], a[164:167], v[140:143], a[48:51]\n\t" \
  "ds_read_b128 a[208:211], v220 offset:20480\n\t" \
  "v_cvt_pk_bf16_f32 v132, v8, v9\n\t" \
  "v_mfma_f32_16x16x32_bf16 a[52:55], a[168:171], v[136:139], a[52:55]\n\t" \
  "v_exp_f32 v36, v36\n\t" \
  "v_mfma_f32_16x16x32_bf16 a[52:55], a[172:175], v[140:143], a[52:55]\n\t" \
  "v_exp_f32 v37, v37\n\t" \
  "v_mfma_f32_16x16x32_bf16 a[56:59], a[176:179], v[136:139], a[56:59]\n\t" \
  "ds_read_b128 a[212:215], v221 offset:20480\n\t" \
  "v_cvt_pk_bf16_f32 v133, v10, v11\n\t" \
  "v_mfma_f32_16x16x32_bf16 a[56:59], a[180:183], v[140:143], a[56:59]\n\t" \
  "v_exp_f32 v38, v38\n\t" \
  "v_mfma_f32_16x16x32_bf16 a[60:63], a[184:187], v[136:139], a[60:63]\n\t" \
  "ds_read_b128 a[216:219], v222 offset:20480\n\t" \
  "v_cvt_pk_bf16_f32 v134, v12, v13\n\t" \
  "v_mfma_f32_16x16x32_bf16 a[60:63], a[188:191], v[140:143], a[60:63]\n\t" \
  "v_exp_f32 v39, v39\n\t" \
  "v_mfma_f32_16x16x32_bf16 v[164:167], v[216:219], v[136:139], v[164:167]\n\t" \
  "v_exp_f32 v40, v40\n\t" \
  "v_mfma_f32_16x16x32_bf16 v[164:167], v[216:219], v[140:143], v[164:167]\n\t" \
  "buffer_load_dwordx4 v230, s[64:67], s96 offen offset:2048 lds\n\t" \
  "v_mfma_f32_16x16x32_bf16 a[64:67], a[128:131], v[144:147], a[64:67]\n\t" \
  "ds_read_b128 a[220:223], v223 offset:20480\n\t" \
  "v_cvt_pk_bf16_f32 v135, v14, v15\n\t" \
  "v_mfma_f32_16x16x32_bf16 a[64:67], a[132:135], v[148:151], a[64:67]\n\t" \
  "v_exp_f32 v41, v41\n\t" \
  "v_mfma_f32_16x16x32_bf16 a[68:71], a[136:139], v[144:147], a[68:71]\n\t" \
  "ds_read_b128 a[224:227], v220 offset:24576\n\t" \
  "v_cvt_pk_bf16_f32 v136, v32, v33\n\t" \
  "v_mfma_f32_16x16x32_bf16 a[68:71], a[140:143], v[148:151], a[68:71]\n\t" \
  "v_exp_f32 v42, v42\n\t" \
  "v_mfma_f32_16x16x32_bf16 a[72:75], a[144:147], v[144:147], a[72:75]\n\t" \
  "v_exp_f32 v43, v43\n\t" \
  "v_mfma_f32_16x16x32_bf16 a[72:75], a[148:151], v[148:151], a[72:75]\n\t" \
  "ds_read_b128 a[228:231], v221 offset:24576\n\t" \
  "v_cvt_pk_bf16_f32 v137, v34, v35\n\t" \
  "v_mfma_f32_16x16x32_bf16 a[76:79], a[152:155], v[144:147], a[76:79]\n\t" \
  "v_exp_f32 v44, v44\n\t" \
  "v_mfma_f32_16x16x32_bf16 a[76:79], a[156:159], v[148:151], a[76:79]\n\t" \
  "v_exp_f32 v45, v45\n\t" \
  "v_mfma_f32_16x16x32_bf16 a[80:83], a[160:163], v[144:147], a[80:83]\n\t" \
  "ds_read_b128 a[232:235], v222 offset:24576\n\t" \
  "v_cvt_pk_bf16_f32 v138, v36, v37\n\t" \
  "v_mfma_f32_16x16x32_bf16 a[80:83], a[164:167], v[148:151], a[80:83]\n\t" \
  "v_exp_f32 v46, v46\n\t" \
  "v_mfma_f32_16x16x32_bf16 a[84:87], a[168:171], v[144:147], a[84:87]\n\t" \
  "v_exp_f32 v47, v47\n\t" \
  "v_mfma_f32_16x16x32_bf16 a[84:87], a[172:175], v[148:151], a[84:87]\n\t" \
  "ds_read_b128 a[236:239], v223 offset:24576\n\t" \
  "v_cvt_pk_bf16_f32 v139, v38, v39\n\t" \
  "v_mfma_f32_16x16x32_bf16 a[88:91], a[176:179], v[144:147], a[88:91]\n\t" \
  "buffer_load_dwordx4 v231, s[64:67], s96 offen offset:3072 lds\n\t" \
  "v_mfma_f32_16x16x32_bf16 a[88:91], a[180:183], v[148:151], a[88:91]\n\t" \
  "v_exp_f32 v64, v64\n\t" \
  "v_mfma_f32_16x16x32_bf16 a[92:95], a[184:187], v[144:147], a[92:95]\n\t" \
  "ds_read_b128 a[240:243], v220 offset:28672\n\t" \
  "v_cvt_pk_bf16_f32 v140, v40, v41\n\t" \
  "v_mfma_f32_16x16x32_bf16 a[92:95], a[188:191], v[148:151], a[92:95]\n\t" \
  "v_exp_f32 v65, v65\n\t" \
  "v_mfma_f32_16x16x32_bf16 v[168:171], v[216:219], v[144:147], v[168:171]\n\t" \
  "v_exp_f32 v66, v66\n\t" \
  "v_mfma_f32_16x16x32_bf16 v[168:171], v[216:219], v[148:151], v[168:171]\n\t" \
  "ds_read_b128 a[244:247], v221 offset:28672\n\t" \
  "v_cvt_pk_bf16_f32 v141, v42, v43\n\t" \
  "v_mfma_f32_16x16x32_bf16 a[96:99], a[128:131], v[152:155], a[96:99]\n\t" \
  "v_exp_f32 v67, v67\n\t" \
  "v_mfma_f32_16x16x32_bf16 a[96:99], a[132:135], v[156:159], a[96:99]\n\t" \
  "v_exp_f32 v68, v68\n\t" \
  "v_mfma_f32_16x16x32_bf16 a[100:103], a[136:139], v[152:155], a[100:103]\n\t" \
  "ds_read_b128 a[248:251], v222 offset:28672\n\t" \
  "v_cvt_pk_bf16_f32 v142, v44, v45\n\t" \
  "v_mfma_f32_16x16x32_bf16 a[100:103], a[140:143], v[156:159], a[100:103]\n\t" \
  "v_exp_f32 v69, v69\n\t" \
  "v_mfma_f32_16x16x32_bf16 a[104:107], a[144:147], v[152:155], a[104:107]\n\t" \
  "v_exp_f32 v70, v70\n\t" \
  "v_mfma_f32_16x16x32_bf16 a[104:107], a[148:151], v[156:159], a[104:107]\n\t" \
  "ds_read_b128 a[252:255], v223 offset:28672\n\t" \
  "v_cvt_pk_bf16_f32 v143, v46, v47\n\t" \
  "v_mfma_f32_16x16x32_bf16 a[108:111], a[152:155], v[152:155], a[108:111]\n\t" \
  "v_exp_f32 v71, v71\n\t" \
  "v_mfma_f32_16x16x32_bf16 a[108:111], a[156:159], v[156:159], a[108:111]\n\t" \
  "v_exp_f32 v72, v72\n\t" \
  "v_mfma_f32_16x16x32_bf16 a[112:115], a[160:163], v[152:155], a[112:115]\n\t" \
  "v_exp_f32 v73, v73\n\t" \
  "v_mfma_f32_16x16x32_bf16 a[112:115], a[164:167], v[156:159], a[112:115]\n\t" \
  "v_exp_f32 v74, v74\n\t" \
  "v_mfma_f32_16x16x32_bf16 a[116:119], a[168:171], v[152:155], a[116:119]\n\t" \
  "v_exp_f32 v75, v75\n\t" \
  "v_mfma_f32_16x16x32_bf16 a[116:119], a[172:175], v[156:159], a[116:119]\n\t" \
  "v_exp_f32 v76, v76\n\t" \
  "v_mfma_f32_16x16x32_bf16 a[120:123], a[176:179], v[152:155], a[120:123]\n\t" \
  "v_exp_f32 v77, v77\n\t" \
  "v_mfma_f32_16x16x32_bf16 a[120:123], a[180:183], v[156:159], a[120:123]\n\t" \
  "v_exp_f32 v78, v78\n\t" \
  "v_mfma_f32_16x16x32_bf16 a[124:127], a[184:187], v[152:155], a[124:127]\n\t" \
  "v_exp_f32 v79, v79\n\t" \
  "v_mfma_f32_16x16x32_bf16 a[124:127], a[188:191], v[156:159], a[124:127]\n\t" \
  "v_exp_f32 v96, v96\n\t" \
  "v_mfma_f32_16x16x32_bf16 v[172:175], v[216:219], v[152:155], v[172:175]\n\t" \
  "v_cvt_pk_bf16_f32 v144, v64, v65\n\t" \
  "v_cvt_pk_bf16_f32 v145, v66, v67\n\t" \
  "v_mfma_f32_16x16x32_bf16 v[172:175], v[216:219], v[156:159], v[172:175]\n\t" \
  "s_waitcnt lgkmcnt(12)\n\t" \
  "s_cmp_lg_u32 s99, 0\n\t" \
  "s_cbranch_scc1 .Lsf_p6\n\t" \
  ".Lsf_q6:\n\t" \
  "v_mfma_f32_16x16x32_bf16 v[0:3], a[192:195], v[16:19], v[176:179]\n\t" \
  "ds_read_b64_tr_b16 a[128:129], v228 offset:0\n\t" \
  "v_cvt_pk_bf16_f32 v146, v68, v69\n\t" \
  "v_mfma_f32_16x16x32_bf16 v[0:3], a[196:199], v[20:23], v[0:3]\n\t" \
  "v_exp_f32 v97, v97\n\t" \
  "v_mfma_f32_16x16x32_bf16 v[0:3], a[200:203], v[24:27], v[0:3]\n\t" \
  "ds_read_b64_tr_b16 a[130:131], v228 offset:4096\n\t" \
  "v_cvt_pk_bf16_f32 v147, v70, v71\n\t" \
  "v_mfma_f32_16x16x32_bf16 v[0:3], a[204:207], v[28:31], v[0:3]\n\t" \
  "s_waitcnt lgkmcnt(10)\n\t" \
  "v_exp_f32 v98, v98\n\t" \
  "v_mfma_f32_16x16x32_bf16 v[4:7], a[220:223], v[28:31], v[176:179]\n\t" \
  "ds_read_b64_tr_b16 a[132:133], v228 offset:8192\n\t" \
  "v_cvt_pk_bf16_f32 v148, v72, v73\n\t" \
  "v_mfma_f32_16x16x32_bf16 v[4:7], a[216:219], v[24:27], v[4:7]\n\t" \
  "v_exp_f32 v99, v99\n\t" \
  "v_mfma_f32_16x16x32_bf16 v[4:7], a[212:215], v[20:23], v[4:7]\n\t" \
  "ds_read_b64_tr_b16 a[134:135], v228 offset:12288\n\t" \
  "v_max3_f32 v192, v0, v1, v2\n\t" \
  "v_mfma_f32_16x16x32_bf16 v[4:7], a[208:211], v[16:19], v[4:7]\n\t" \
  "s_waitcnt lgkmcnt(8)\n\t" \
  "ds_read_b64_tr_b16 a[136:137], v229 offset:0\n\t" \
  "v_cvt_pk_bf16_f32 v149, v74, v75\n\t" \
  "v_mfma_f32_16x16x32_bf16 v[8:11], a[224:227], v[16:19], v[176:179]\n\t" \
  "v_exp_f32 v100, v100\n\t" \
  "v_mfma_f32_16x16x32_bf16 v[8:11], a[228:231], v[20:23], v[8:11]\n\t" \
  "ds_read_b64_tr_b16 a[138:139], v229 offset:4096\n\t" \
  "v_cvt_pk_bf16_f32 v150, v76, v77\n\t" \
  "v_mfma_f32_16x16x32_bf16 v[8:11], a[232:235], v[24:27], v[8:11]\n\t" \
  "v_max3_f32 v203, v3, v4, v5\n\t" \
  "v_max3_f32 v192, v192, v6, v7\n\t" \
  "v_mfma_f32_16x16x32_bf16 v[8:11], a[236:239], v[28:31], v[8:11]\n\t" \
  "s_waitcnt lgkmcnt(6)\n\t" \
  "ds_read_b64_tr_b16 a[140:141], v229 offset:8192\n\t" \
  "v_cvt_pk_bf16_f32 v151, v78, v79\n\t" \
  "v_mfma_f32_16x16x32_bf16 v[12:15], a[252:255], v[28:31], v[176:179]\n\t" \
  "v_exp_f32 v101, v101\n\t" \
  "v_mfma_f32_16x16x32_bf16 v[12:15], a[248:251], v[24:27], v[12:15]\n\t" \
  "ds_read_b64_tr_b16 a[142:143], v229 offset:12288\n\t" \
  "v_cvt_pk_bf16_f32 v152, v96, v97\n\t" \
  "v_mfma_f32_16x16x32_bf16 v[12:15], a[244:247], v[20:23], v[12:15]\n\t" \
  "v_max3_f32 v203, v203, v8, v9\n\t" \
  "v_max3_f32 v192, v192, v10, v11\n\t" \
  "v_mfma_f32_16x16x32_bf16 v[12:15], a[240:243], v[16:19], v[12:15]\n\t" \
  "ds_read_b64_tr_b16 a[144:145], v228 offset:256\n\t" \
  "v_cvt_pk_bf16_f32 v153, v98, v99\n\t" \
  "v_mfma_f32_16x16x32_bf16 v[32:35], a[192:195], v[48:51], v[180:183]\n\t" \
  "ds_read_b64_tr_b16 a[146:147], v228 offset:4352\n\t" \
  "v_cvt_pk_bf16_f32 v154, v100, v101\n\t" \
  "v_mfma_f32_16x16x32_bf16 v[32:35], a[196:199], v[52:55], v[32:35]\n\t" \
  "v_exp_f32 v102, v102\n\t" \
  "v_mfma_f32_16x16x32_bf16 v[32:35], a[200:203], v[56:59], v[32:35]\n\t" \
  "v_max3_f32 v203, v203, v12, v13\n\t" \
  "v_max3_f32 v192, v192, v14, v15\n\t" \
  "v_mfma_f32_16x16x32_bf16 v[32:35], a[204:207], v[60:63], v[32:35]\n\t" \
  "v_max_f32 v192, v192, v203\n\t" \
  "v_cmp_lt_f32 vcc, 0x42800000, v192\n\t" \
  "v_mfma_f32_16x16x32_bf16 v[36:39], a[220:223], v[60:63], v[180:183]\n\t" \
  "v_exp_f32 v103, v103\n\t" \
  "v_mfma_f32_16x16x32_bf16 v[36:39], a[216:219], v[56:59], v[36:39]\n\t" \
  "s_cbranch_vccnz .Lsf_c70\n\t" \
  ".Lsf_r70:\n\t" \
  "v_mfma_f32_16x16x32_bf16 v[36:39], a[212:215], v[52:55], v[36:39]\n\t" \
  "ds_read_b64_tr_b16 a[148:149], v228 offset:8448\n\t" \
  "v_max3_f32 v192, v32, v33, v34\n\t" \
  "v_mfma_f32_16x16x32_bf16 v[36:39], a[208:211], v[48:51], v[36:39]\n\t" \
  "ds_read_b64_tr_b16 a[150:151], v228 offset:12544\n\t" \
  "v_cvt_pk_bf16_f32 v155, v102, v103\n\t" \
  "v_mfma_f32_16x16x32_bf16 v[40:43], a[224:227], v[48:51], v[180:183]\n\t" \
  "v_exp_f32 v104, v104\n\t" \
  "v_mfma_f32_16x16x32_bf16 v[40:43], a[228:231], v[52:55], v[40:43]\n\t" \
  "v_exp_f32 v105, v105\n\t" \
  "v_mfma_f32_16x16x32_bf16 v[40:43], a[232:235], v[56:59], v[40:43]\n\t" \
  "ds_read_b64_tr_b16 a[152:153], v229 offset:256\n\t" \
  "v_max3_f32 v203, v35, v36, v37\n\t" \
  "v_mfma_f32_16x16x32_bf16 v[40:43], a[236:239], v[60:63], v[40:43]\n\t" \
  "ds_read_b64_tr_b16 a[154:155], v229 offset:4352\n\t" \
  "v_max3_f32 v192, v192, v38, v39\n\t" \
  "v_mfma_f32_16x16x32_bf16 v[44:47], a[252:255], v[60:63], v[180:183]\n\t" \
  "ds_read_b64_tr_b16 a[156:157], v229 offset:8448\n\t" \
  "v_cvt_pk_bf16_f32 v156, v104, v105\n\t" \
  "v_mfma_f32_16x16x32_bf16 v[44:47], a[248:251], v[56:59], v[44:47]\n\t" \
  "v_exp_f32 v106, v106\n\t" \
  "v_mfma_f32_16x16x32_bf16 v[44:47], a[244:247], v[52:55], v[44:47]\n\t" \
  "v_max3_f32 v203, v203, v40, v41\n\t" \
  "v_max3_f32 v192, v192, v42, v43\n\t" \
  "v_mfma_f32_16x16x32_bf16 v[44:47], a[240:243], v[48:51], v[44:47]\n\t" \
  "v_exp_f32 v107, v107\n\t" \
  "v_mfma_f32_16x16x32_bf16 v[64:67], a[192:195], v[80:83], v[184:187]\n\t" \
  "ds_read_b64_tr_b16 a[158:159], v229 offset:12544\n\t" \
  "v_cvt_pk_bf16_f32 v157, v106, v107\n\t" \
  "v_mfma_f32_16x16x32_bf16 v[64:67], a[196:199], v[84:87], v[64:67]\n\t" \
  "v_exp_f32 v108, v108\n\t" \
  "v_mfma_f32_16x16x32_bf16 v[64:67], a[200:203], v[88:91], v[64:67]\n\t" \
  "ds_read_b64_tr_b16 a[160:161], v228 offset:512\n\t" \
  "v_max3_f32 v203, v203, v44, v45\n\t" \
  "v_mfma_f32_16x16x32_bf16 v[64:67], a[204:207], v[92:95], v[64:67]\n\t" \
  "v_max3_f32 v192, v192, v46, v47\n\t" \
  "v_max_f32 v192, v192, v203\n\t" \
  "v_mfma_f32_16x16x32_bf16 v[68:71], a[220:223], v[92:95], v[184:187]\n\t" \
  "ds_read_b64_tr_b16 a[162:163], v228 offset:4608\n\t" \
  "v_cmp_lt_f32 vcc, 0x42800000, v192\n\t" \
  "v_mfma_f32_16x16x32_bf16 v[68:71], a[216:219], v[88:91], v[68:71]\n\t" \
  "v_exp_f32 v109, v109\n\t" \
  "v_mfma_f32_16x16x32_bf16 v[68:71], a[212:215], v[84:87], v[68:71]\n\t" \
  "s_cbranch_vccnz .Lsf_c71\n\t" \
  ".Lsf_r71:\n\t" \
  "v_mfma_f32_16x16x32_bf16 v[68:71], a[208:211], v[80:83], v[68:71]\n\t" \
  "ds_read_b64_tr_b16 a[164:165], v228 offset:8704\n\t" \
  "v_max3_f32 v192, v64, v65, v66\n\t" \
  "v_mfma_f32_16x16x32_bf16 v[72:75], a[224:227], v[80:83], v[184:187]\n\t" \
  "ds_read_b64_tr_b16 a[166:167], v228 offset:12800\n\t" \
  "v_cvt_pk_bf16_f32 v158, v108, v109\n\t" \
  "v_mfma_f32_16x16x32_bf16 v[72:75], a[228:231], v[84:87], v[72:75]\n\t" \
  "v_exp_f32 v110, v110\n\t" \
  "v_mfma_f32_16x16x32_bf16 v[72:75], a[232:235], v[88:91], v[72:75]\n\t" \
  "v_max3_f32 v203, v67, v68, v69\n\t" \
  "v_max3_f32 v192, v192, v70, v71\n\t" \
  "v_mfma_f32_16x16x32_bf16 v[72:75], a[236:239], v[92:95], v[72:75]\n\t" \
  "ds_read_b64_tr_b16 a[168:169], v229 offset:512\n\t" \
  "v_mfma_f32_16x16x32_bf16 v[76:79], a[252:255], v[92:95], v[184:187]\n\t" \
  "v_exp_f32 v111, v111\n\t" \
  "v_mfma_f32_16x16x32_bf16 v[76:79], a[248:251], v[88:91], v[76:79]\n\t" \
  "ds_read_b64_tr_b16 a[170:171], v229 offset:4608\n\t" \
  "v_cvt_pk_bf16_f32 v159, v110, v111\n\t" \
  "v_mfma_f32_16x16x32_bf16 v[76:79], a[244:247], v[84:87], v[76:79]\n\t" \
  "ds_read_b64_tr_b16 a[172:173], v229 offset:8704\n\t" \
  "v_max3_f32 v203, v203, v72, v73\n\t" \
  "v_mfma_f32_16x16x32_bf16 v[76:79], a[240:243], v[80:83], v[76:79]\n\t" \
  "ds_read_b64_tr_b16 a[174:175], v229 offset:12800\n\t" \
  "v_max3_f32 v192, v192, v74, v75\n\t" \
  "v_mfma_f32_16x16x32_bf16 v[96:99], a[192:195], v[112:115], v[188:191]\n\t" \
  "ds_read_b64_tr_b16 a[176:177], v228 offset:768\n\t" \
  "v_mfma_f32_16x16x32_bf16 v[96:99], a[196:199], v[116:119], v[96:99]\n\t" \
  "ds_read_b64_tr_b16 a[178:179], v228 offset:4864\n\t" \
  "v_mfma_f32_16x16x32_bf16 v[96:99], a[200:203], v[120:123], v[96:99]\n\t" \
  "ds_read_b64_tr_b16 a[180:181], v228 offset:8960\n\t" \
  "v_max3_f32 v203, v203, v76, v77\n\t" \
  "v_mfma_f32_16x16x32_bf16 v[96:99], a[204:207], v[124:127], v[96:99]\n\t" \
  "v_max3_f32 v192, v192, v78, v79\n\t" \
  "v_max_f32 v192, v192, v203\n\t" \
  "v_mfma_f32_16x16x32_bf16 v[100:103], a[220:223], v[124:127], v[188:191]\n\t" \
  "ds_read_b64_tr_b16 a[182:183], v228 offset:13056\n\t" \
  "v_cmp_lt_f32 vcc, 0x42800000, v192\n\t" \
  "v_mfma_f32_16x16x32_bf16 v[100:103], a[216:219], v[120:123], v[100:103]\n\t" \
  "ds_read_b64_tr_b16 a[184:185], v229 offset:768\n\t" \
  "v_mfma_f32_16x16x32_bf16 v[100:103], a[212:215], v[116:119], v[100:103]\n\t" \
  "s_cbranch_vccnz .Lsf_c72\n\t" \
  ".Lsf_r72:\n\t" \
  "v_mfma_f32_16x16x32_bf16 v[100:103], a[208:211], v[112:115], v[100:103]\n\t" \
  "ds_read_b64_tr_b16 a[186:187], v229 offset:4864\n\t" \
  "v_max3_f32 v192, v96, v97, v98\n\t" \
  "v_mfma_f32_16x16x32_bf16 v[104:107], a[224:227], v[112:115], v[188:191]\n\t" \
  "ds_read_b64_tr_b16 a[188:189], v229 offset:8960\n\t" \
  "v_mfma_f32_16x16x32_bf16 v[104:107], a[228:231], v[116:119], v[104:107]\n\t" \
  "ds_read_b64_tr_b16 a[190:191], v229 offset:13056\n\t" \
  "v_mfma_f32_16x16x32_bf16 v[104:107], a[232:235], v[120:123], v[104:107]\n\t" \
  "v_exp_f32 v0, v0\n\t" \
  "v_mfma_f32_16x16x32_bf16 v[104:107], a[236:239], v[124:127], v[104:107]\n\t" \
  "v_max3_f32 v203, v99, v100, v101\n\t" \
  "v_max3_f32 v192, v192, v102, v103\n\t" \
  "v_mfma_f32_16x16x32_bf16 v[108:111], a[252:255], v[124:127], v[188:191]\n\t" \
  "v_exp_f32 v1, v1\n\t" \
  "v_mfma_f32_16x16x32_bf16 v[108:111], a[248:251], v[120:123], v[108:111]\n\t" \
  "v_exp_f32 v2, v2\n\t" \
  "v_mfma_f32_16x16x32_bf16 v[108:111], a[244:247], v[116:119], v[108:111]\n\t" \
  "s_waitcnt vmcnt(0) lgkmcnt(0)\n\t" \
  "v_max3_f32 v203, v203, v104, v105\n\t" \
  "v_max3_f32 v192, v192, v106, v107\n\t" \
  "v_mfma_f32_16x16x32_bf16 v[108:111], a[240:243], v[112:115], v[108:111]\n\t" \
  "s_barrier\n\t" \
  "v_mfma_f32_16x16x32_bf16 a[0:3], a[128:131], v[128:131], a[0:3]\n\t" \
  "ds_read_b64_tr_b16 v[16:17], v210 offset:0\n\t" \
  "v_cvt_pk_bf16_f32 v0, v0, v1\n\t" \
  "v_mfma_f32_16x16x32_bf16 a[0:3], a[132:135], v[132:135], a[0:3]\n\t" \
  "v_exp_f32 v3, v3\n\t" \
  "v_mfma_f32_16x16x32_bf16 a[4:7], a[136:139], v[128:131], a[4:7]\n\t" \
  "ds_read_b64_tr_b16 v[18:19], v210 offset:4096\n\t" \
  "v_max3_f32 v203, v203, v108, v109\n\t" \
  "v_mfma_f32_16x16x32_bf16 a[4:7], a[140:143], v[132:135], a[4:7]\n\t" \
  "v_max3_f32 v192, v192, v110, v111\n\t" \
  "v_max_f32 v192, v192, v203\n\t" \
  "v_mfma_f32_16x16x32_bf16 a[8:11], a[144:147], v[128:131], a[8:11]\n\t" \
  "ds_read_b64_tr_b16 v[20:21], v210 offset:8192\n\t" \
  "v_cmp_lt_f32 vcc, 0x42800000, v192\n\t" \
  "v_mfma_f32_16x16x32_bf16 a[8:11], a[148:151], v[132:135], a[8:11]\n\t" \
  "ds_read_b64_tr_b16 v[22:23], v210 offset:12288\n\t" \
  "v_cvt_pk_bf16_f32 v1, v2, v3\n\t" \
  "v_mfma_f32_16x16x32_bf16 a[12:15], a[152:155], v[128:131], a[12:15]\n\t" \
  "s_cbranch_vccnz .Lsf_c73\n\t" \
  ".Lsf_r73:\n\t" \
  "v_mfma_f32_16x16x32_bf16 a[12:15], a[156:159], v[132:135], a[12:15]\n\t" \
  "v_exp_f32 v4, v4\n\t" \
  "v_mfma_f32_16x16x32_bf16 a[16:19], a[160:163], v[128:131], a[16:19]\n\t" \
  "v_exp_f32 v5, v5\n\t" \
  "v_mfma_f32_16x16x32_bf16 a[16:19], a[164:167], v[132:135], a[16:19]\n\t" \
  "ds_read_b64_tr_b16 v[24:25], v211 offset:0\n\t" \
  "v_cvt_pk_bf16_f32 v2, v4, v5\n\t" \
  "v_mfma_f32_16x16x32_bf16 a[20:23], a[168:171], v[128:131], a[20:23]\n\t" \
  "v_exp_f32 v6, v6\n\t" \
  "v_mfma_f32_16x16x32_bf16 a[20:23], a[172:175], v[132:135], a[20:23]\n\t" \
  "v_exp_f32 v7, v7\n\t" \
  "v_mfma_f32_16x16x32_bf16 a[24:27], a[176:179], v[128:131], a[24:27]\n\t" \
  "ds_read_b64_tr_b16 v[26:27], v211 offset:4096\n\t" \
  "v_cvt_pk_bf16_f32 v3, v6, v7\n\t" \
  "v_mfma_f32_16x16x32_bf16 a[24:27], a[180:183], v[132:135], a[24:27]\n\t" \
  "v_exp_f32 v8, v8\n\t" \
  "v_mfma_f32_16x16x32_bf16 a[28:31], a[184:187], v[128:131], a[28:31]\n\t" \
  "ds_read_b64_tr_b16 v[28:29], v211 offset:8192\n\t" \
  "v_mfma_f32_16x16x32_bf16 a[28:31], a[188:191], v[132:135], a[28:31]\n\t" \
  "ds_read_b64_tr_b16 v[30:31], v211 offset:12288\n\t" \
  "v_mfma_f32_16x16x32_bf16 v[160:163], v[216:219], v[128:131], v[160:163]\n\t" \
  "ds_read_b64_tr_b16 v[48:49], v210 offset:256\n\t" \
  "v_mfma_f32_16x16x32_bf16 v[160:163], v[216:219], v[132:135], v[160:163]\n\t" \
  "ds_read_b64_tr_b16 v[50:51], v210 offset:4352\n\t" \
  "v_mfma_f32_16x16x32_bf16 a[32:35], a[128:131], v[136:139], a[32:35]\n\t" \
  "v_exp_f32 v9, v9\n\t" \
  "v_mfma_f32_16x16x32_bf16 a[32:35], a[132:135], v[140:143], a[32:35]\n\t" \
  "ds_read_b64_tr_b16 v[52:53], v210 offset:8448\n\t" \
  "v_cvt_pk_bf16_f32 v8, v8, v9\n\t" \
  "v_mfma_f32_16x16x32_bf16 a[36:39], a[136:139], v[136:139], a[36:39]\n\t" \
  "ds_read_b64_tr_b16 v[54:55], v210 offset:12544\n\t" \
  "v_mfma_f32_16x16x32_bf16 a[36:39], a[140:143], v[140:143], a[36:39]\n\t" \
  "ds_read_b64_tr_b16 v[56:57], v211 offset:256\n\t" \
  "v_mfma_f32_16x16x32_bf16 a[40:43], a[144:147], v[136:139], a[40:43]\n\t" \
  "v_exp_f32 v10, v10\n\t" \
  "v_mfma_f32_16x16x32_bf16 a[40:43], a[148:151], v[140:143], a[40:43]\n\t" \
  "ds_read_b64_tr_b16 v[58:59], v211 offset:4352\n\t" \
  "v_mfma_f32_16x16x32_bf16 a[44:47], a[152:155], v[136:139], a[44:47]\n\t" \
  "ds_read_b64_tr_b16 v[60:61], v211 offset:8448\n\t" \
  "v_mfma_f32_16x16x32_bf16 a[44:47], a[156:159], v[140:143], a[44:47]\n\t" \
  "ds_read_b64_tr_b16 v[62:63], v211 offset:12544\n\t" \
  "v_mfma_f32_16x16x32_bf16 a[48:51], a[160:163], v[136:139], a[48:51]\n\t" \
  "v_exp_f32 v11, v11\n\t" \
  "v_mfma_f32_16x16x32_bf16 a[48:51], a[164:167], v[140:143], a[48:51]\n\t" \
  "ds_read_b64_tr_b16 v[80:81], v210 offset:512\n\t" \
  "v_cvt_pk_bf16_f32 v9, v10, v11\n\t" \
  "v_mfma_f32_16x16x32_bf16 a[52:55], a[168:171], v[136:139], a[52:55]\n\t" \
  "ds_read_b64_tr_b16 v[82:83], v210 offset:4608\n\t" \
  "v_mfma_f32_16x16x32_bf16 a[52:55], a[172:175], v[140:143], a[52:55]\n\t" \
  "v_exp_f32 v12, v12\n\t" \
  "v_mfma_f32_16x16x32_bf16 a[56:59], a[176:179], v[136:139], a[56:59]\n\t" \
  "v_exp_f32 v13, v13\n\t" \
  "v_mfma_f32_16x16x32_bf16 a[56:59], a[180:183], v[140:143], a[56:59]\n\t" \
  "ds_read_b64_tr_b16 v[84:85], v210 offset:8704\n\t" \
  "v_cvt_pk_bf16_f32 v10, v12, v13\n\t" \
  "v_mfma_f32_16x16x32_bf16 a[60:63], a[184:187], v[136:139], a[60:63]\n\t" \
  "ds_read_b64_tr_b16 v[86:87], v210 offset:12800\n\t" \
  "v_mfma_f32_16x16x32_bf16 a[60:63], a[188:191], v[140:143], a[60:63]\n\t" \
  "v_exp_f32 v14, v14\n\t" \
  "v_mfma_f32_16x16x32_bf16 v[164:167], v[216:219], v[136:139], v[164:167]\n\t" \
  "ds_read_b64_tr_b16 v[88:89], v211 offset:512\n\t" \
  "v_mfma_f32_16x16x32_bf16 v[164:167], v[216:219], v[140:143], v[164:167]\n\t" \
  "v_exp_f32 v15, v15\n\t" \
  "v_mfma_f32_16x16x32_bf16 a[64:67], a[128:131], v[144:147], a[64:67]\n\t" \
  "ds_read_b64_tr_b16 v[90:91], v211 offset:4608\n\t" \
  "v_cvt_pk_bf16_f32 v11, v14, v15\n\t" \
  "v_mfma_f32_16x16x32_bf16 a[64:67], a[132:135], v[148:151], a[64:67]\n\t" \
  "v_exp_f32 v32, v32\n\t" \
  "v_mfma_f32_16x16x32_bf16 a[68:71], a[136:139], v[144:147], a[68:71]\n\t" \
  "v_exp_f32 v33, v33\n\t" \
  "v_mfma_f32_16x16x32_bf16 a[68:71], a[140:143], v[148:151], a[68:71]\n\t" \
  "ds_read_b64_tr_b16 v[92:93], v211 offset:8704\n\t" \
  "v_cvt_pk_bf16_f32 v32, v32, v33\n\t" \
  "v_mfma_f32_16x16x32_bf16 a[72:75], a[144:147], v[144:147], a[72:75]\n\t" \
  "ds_read_b64_tr_b16 v[94:95], v211 offset:12800\n\t" \
  "v_mfma_f32_16x16x32_bf16 a[72:75], a[148:151], v[148:151], a[72:75]\n\t" \
  "v_exp_f32 v34, v34\n\t" \
  "v_mfma_f32_16x16x32_bf16 a[76:79], a[152:155], v[144:147], a[76:79]\n\t" \
  "v_exp_f32 v35, v35\n\t" \
  "v_mfma_f32_16x16x32_bf16 a[76:79], a[156:159], v[148:151], a[76:79]\n\t" \
  "ds_read_b64_tr_b16 v[112:113], v210 offset:768\n\t" \
  "v_cvt_pk_bf16_f32 v33, v34, v35\n\t" \
  "v_mfma_f32_16x16x32_bf16 a[80:83], a[160:163], v[144:147], a[80:83]\n\t" \
  "v_exp_f32 v36, v36\n\t" \
  "v_mfma_f32_16x16x32_bf16 a[80:83], a[164:167], v[148:151], a[80:83]\n\t" \
  "ds_read_b64_tr_b16 v[114:115], v210 offset:4864\n\t" \
  "v_mfma_f32_16x16x32_bf16 a[84:87], a[168:171], v[144:147], a[84:87]\n\t" \
  "v_exp_f32 v37, v37\n\t" \
  "v_mfma_f32_16x16x32_bf16 a[84:87], a[172:175], v[148:151], a[84:87]\n\t" \
  "ds_read_b64_tr_b16 v[116:117], v210 offset:8960\n\t" \
  "v_cvt_pk_bf16_f32 v34, v36, v37\n\t" \
  "v_mfma_f32_16x16x32_bf16 a[88:91], a[176:179], v[144:147], a[88:91]\n\t" \
  "v_exp_f32 v38, v38\n\t" \
  "v_mfma_f32_16x16x32_bf16 a[88:91], a[180:183], v[148:151], a[88:91]\n\t" \
  "ds_read_b64_tr_b16 v[118:119], v210 offset:13056\n\t" \
  "v_mfma_f32_16x16x32_bf16 a[92:95], a[184:187], v[144:147], a[92:95]\n\t" \
  "v_exp_f32 v39, v39\n\t" \
  "v_mfma_f32_16x16x32_bf16 a[92:95], a[188:191], v[148:151], a[92:95]\n\t" \
  "ds_read_b64_tr_b16 v[120:121], v211 offset:768\n\t" \
  "v_cvt_pk_bf16_f32 v35, v38, v39\n\t" \
  "v_mfma_f32_16x16x32_bf16 v[168:171], v[216:219], v[144:147], v[168:171]\n\t" \
  "v_exp_f32 v40, v40\n\t" \
  "v_mfma_f32_16x16x32_bf16 v[168:171], v[216:219], v[148:151], v[168:171]\n\t" \
  "ds_read_b64_tr_b16 v[122:123], v211 offset:4864\n\t" \
  "v_mfma_f32_16x16x32_bf16 a[96:99], a[128:131], v[152:155], a[96:99]\n\t" \
  "v_exp_f32 v41, v41\n\t" \
  "v_mfma_f32_16x16x32_bf16 a[96:99], a[132:135], v[156:159], a[96:99]\n\t" \
  "ds_read_b64_tr_b16 v[124:125], v211 offset:8960\n\t" \
  "v_cvt_pk_bf16_f32 v40, v40, v41\n\t" \
  "v_mfma_f32_16x16x32_bf16 a[100:103], a[136:139], v[152:155], a[100:103]\n\t" \
  "v_exp_f32 v42, v42\n\t" \
  "v_mfma_f32_16x16x32_bf16 a[100:103], a[140:143], v[156:159], a[100:103]\n\t" \
  "ds_read_b64_tr_b16 v[126:127], v211 offset:13056\n\t" \
  "v_mfma_f32_16x16x32_bf16 a[104:107], a[144:147], v[152:155], a[104:107]\n\t" \
  "v_exp_f32 v43, v43\n\t" \
  "v_mfma_f32_16x16x32_bf16 a[104:107], a[148:151], v[156:159], a[104:107]\n\t" \
  "v_exp_f32 v44, v44\n\t" \
  "v_mfma_f32_16x16x32_bf16 a[108:111], a[152:155], v[152:155], a[108:111]\n\t" \
  "v_exp_f32 v45, v45\n\t" \
  "v_mfma_f32_16x16x32_bf16 a[108:111], a[156:159], v[156:159], a[108:111]\n\t" \
  "v_exp_f32 v46, v46\n\t" \
  "v_mfma_f32_16x16x32_bf16 a[112:115], a[160:163], v[152:155], a[112:115]\n\t" \
  "v_exp_f32 v47, v47\n\t" \
  "v_mfma_f32_16x16x32_bf16 a[112:115], a[164:167], v[156:159], a[112:115]\n\t" \
  "v_exp_f32 v64, v64\n\t" \
  "v_mfma_f32_16x16x32_bf16 a[116:119], a[168:171], v[152:155], a[116:119]\n\t" \
  "v_cvt_pk_bf16_f32 v41, v42, v43\n\t" \
  "v_cvt_pk_bf16_f32 v42, v44, v45\n\t" \
  "v_mfma_f32_16x16x32_bf16 a[116:119], a[172:175], v[156:159], a[116:119]\n\t" \
  "v_exp_f32 v65, v65\n\t" \
  "v_mfma_f32_16x16x32_bf16 a[120:123], a[176:179], v[152:155], a[120:123]\n\t" \
  "v_exp_f32 v66, v66\n\t" \
  "v_mfma_f32_16x16x32_bf16 a[120:123], a[180:183], v[156:159], a[120:123]\n\t" \
  "v_exp_f32 v67, v67\n\t" \
  "v_mfma_f32_16x16x32_bf16 a[124:127], a[184:187], v[152:155], a[124:127]\n\t" \
  "v_exp_f32 v68, v68\n\t" \
  "v_mfma_f32_16x16x32_bf16 a[124:127], a[188:191], v[156:159], a[124:127]\n\t" \
  "v_exp_f32 v69, v69\n\t" \
  "v_mfma_f32_16x16x32_bf16 v[172:175], v[216:219], v[152:155], v[172:175]\n\t" \
  "v_cvt_pk_bf16_f32 v43, v46, v47\n\t" \
  "v_cvt_pk_bf16_f32 v64, v64, v65\n\t" \
  "v_mfma_f32_16x16x32_bf16 v[172:175], v[216:219], v[156:159], v[172:175]\n\t" \
  "s_waitcnt lgkmcnt(15)\n\t" \
  "s_cmp_lg_u32 s99, 0\n\t" \
  "s_cbranch_scc1 .Lsf_p7\n\t" \
  ".Lsf_q7:\n\t" \
  "v_mfma_f32_16x16x32_bf16 a[0:3], v[16:19], v[0:3], a[0:3]\n\t" \
  "v_exp_f32 v70, v70\n\t" \
  "v_mfma_f32_16x16x32_bf16 a[0:3], v[20:23], v[8:11], a[0:3]\n\t" \
  "v_exp_f32 v71, v71\n\t" \
  "v_mfma_f32_16x16x32_bf16 v[160:163], v[216:219], v[0:3], v[160:163]\n\t" \
  "v_exp_f32 v72, v72\n\t" \
  "v_mfma_f32_16x16x32_bf16 v[160:163], v[216:219], v[8:11], v[160:163]\n\t" \
  "v_exp_f32 v73, v73\n\t" \
  "v_mfma_f32_16x16x32_bf16 a[4:7], v[24:27], v[0:3], a[4:7]\n\t" \
  "v_exp_f32 v74, v74\n\t" \
  "v_mfma_f32_16x16x32_bf16 a[4:7], v[28:31], v[8:11], a[4:7]\n\t" \
  "v_cvt_pk_bf16_f32 v65, v66, v67\n\t" \
  "v_cvt_pk_bf16_f32 v66, v68, v69\n\t" \
  "v_mfma_f32_16x16x32_bf16 a[8:11], v[48:51], v[0:3], a[8:11]\n\t" \
  "v_exp_f32 v75, v75\n\t" \
  "v_mfma_f32_16x16x32_bf16 a[8:11], v[52:55], v[8:11], a[8:11]\n\t" \
  "v_exp_f32 v76, v76\n\t" \
  "v_mfma_f32_16x16x32_bf16 a[12:15], v[56:59], v[0:3], a[12:15]\n\t" \
  "v_cvt_pk_bf16_f32 v67, v70, v71\n\t" \
  "v_cvt_pk_bf16_f32 v72, v72, v73\n\t" \
  "v_mfma_f32_16x16x32_bf16 a[12:15], v[60:63], v[8:11], a[12:15]\n\t" \
  "s_waitcnt lgkmcnt(8)\n\t" \
  "v_exp_f32 v77, v77\n\t" \
  "v_mfma_f32_16x16x32_bf16 a[16:19], v[80:83], v[0:3], a[16:19]\n\t" \
  "v_exp_f32 v78, v78\n\t" \
  "v_mfma_f32_16x16x32_bf16 a[16:19], v[84:87], v[8:11], a[16:19]\n\t" \
  "v_cvt_pk_bf16_f32 v73, v74, v75\n\t" \
  "v_cvt_pk_bf16_f32 v74, v76, v77\n\t" \
  "v_mfma_f32_16x16x32_bf16 a[20:23], v[88:91], v[0:3], a[20:23]\n\t" \
  "v_exp_f32 v79, v79\n\t" \
  "v_mfma_f32_16x16x32_bf16 a[20:23], v[92:95], v[8:11], a[20:23]\n\t" \
  "s_waitcnt lgkmcnt(0)\n\t" \
  "v_exp_f32 v96, v96\n\t" \
  "v_mfma_f32_16x16x32_bf16 a[24:27], v[112:115], v[0:3], a[24:27]\n\t" \
  "v_exp_f32 v97, v97\n\t" \
  "v_mfma_f32_16x16x32_bf16 a[24:27], v[116:119], v[8:11], a[24:27]\n\t" \
  "v_exp_f32 v98, v98\n\t" \
  "v_mfma_f32_16x16x32_bf16 a[28:31], v[120:123], v[0:3], a[28:31]\n\t" \
  "v_exp_f32 v99, v99\n\t" \
  "v_mfma_f32_16x16x32_bf16 a[28:31], v[124:127], v[8:11], a[28:31]\n\t" \
  "v_exp_f32 v100, v100\n\t" \
  "v_mfma_f32_16x16x32_bf16 a[32:35], v[16:19], v[32:35], a[32:35]\n\t" \
  "v_cvt_pk_bf16_f32 v75, v78, v79\n\t" \
  "v_cvt_pk_bf16_f32 v96, v96, v97\n\t" \
  "v_mfma_f32_16x16x32_bf16 a[32:35], v[20:23], v[40:43], a[32:35]\n\t" \
  "v_exp_f32 v101, v101\n\t" \
  "v_mfma_f32_16x16x32_bf16 v[164:167], v[216:219], v[32:35], v[164:167]\n\t" \
  "v_exp_f32 v102, v102\n\t" \
  "v_mfma_f32_16x16x32_bf16 v[164:167], v[216:219], v[40:43], v[164:167]\n\t" \
  "v_exp_f32 v103, v103\n\t" \
  "v_mfma_f32_16x16x32_bf16 a[36:39], v[24:27], v[32:35], a[36:39]\n\t" \
  "v_exp_f32 v104, v104\n\t" \
  "v_mfma_f32_16x16x32_bf16 a[36:39], v[28:31], v[40:43], a[36:39]\n\t" \
  "v_exp_f32 v105, v105\n\t" \
  "v_mfma_f32_16x16x32_bf16 a[40:43], v[48:51], v[32:35], a[40:43]\n\t" \
  "v_cvt_pk_bf16_f32 v97, v98, v99\n\t" \
  "v_cvt_pk_bf16_f32 v98, v100, v101\n\t" \
  "v_mfma_f32_16x16x32_bf16 a[40:43], v[52:55], v[40:43], a[40:43]\n\t" \
  "v_exp_f32 v106, v106\n\t" \
  "v_mfma_f32_16x16x32_bf16 a[44:47], v[56:59], v[32:35], a[44:47]\n\t" \
  "v_exp_f32 v107, v107\n\t" \
  "v_mfma_f32_16x16x32_bf16 a[44:47], v[60:63], v[40:43], a[44:47]\n\t" \
  "v_cvt_pk_bf16_f32 v99, v102, v103\n\t" \
  "v_cvt_pk_bf16_f32 v104, v104, v105\n\t" \
  "v_mfma_f32_16x16x32_bf16 a[48:51], v[80:83], v[32:35], a[48:51]\n\t" \
  "v_exp_f32 v108, v108\n\t" \
  "v_mfma_f32_16x16x32_bf16 a[48:51], v[84:87], v[40:43], a[48:51]\n\t" \
  "v_exp_f32 v109, v109\n\t" \
  "v_mfma_f32_16x16x32_bf16 a[52:55], v[88:91], v[32:35], a[52:55]\n\t" \
  "v_cvt_pk_bf16_f32 v105, v106, v107\n\t" \
  "v_cvt_pk_bf16_f32 v106, v108, v109\n\t" \
  "v_mfma_f32_16x16x32_bf16 a[52:55], v[92:95], v[40:43], a[52:55]\n\t" \
  "v_exp_f32 v110, v110\n\t" \
  "v_mfma_f32_16x16x32_bf16 a[56:59], v[112:115], v[32:35], a[56:59]\n\t" \
  "v_exp_f32 v111, v111\n\t" \
  "v_mfma_f32_16x16x32_bf16 a[56:59], v[116:119], v[40:43], a[56:59]\n\t" \
  "v_cvt_pk_bf16_f32 v107, v110, v111\n\t" \
  "v_mfma_f32_16x16x32_bf16 a[60:63], v[120:123], v[32:35], a[60:63]\n\t" \
  "v_mfma_f32_16x16x32_bf16 a[60:63], v[124:127], v[40:43], a[60:63]\n\t" \
  "v_mfma_f32_16x16x32_bf16 a[64:67], v[16:19], v[64:67], a[64:67]\n\t" \
  "v_mfma_f32_16x16x32_bf16 a[64:67], v[20:23], v[72:75], a[64:67]\n\t" \
  "v_mfma_f32_16x16x32_bf16 v[168:171], v[216:219], v[64:67], v[168:171]\n\t" \
  "v_mfma_f32_16x16x32_bf16 v[168:171], v[216:219], v[72:75], v[168:171]\n\t" \
  "v_mfma_f32_16x16x32_bf16 a[68:71], v[24:27], v[64:67], a[68:71]\n\t" \
  "v_mfma_f32_16x16x32_bf16 a[68:71], v[28:31], v[72:75], a[68:71]\n\t" \
  "v_mfma_f32_16x16x32_bf16 a[72:75], v[48:51], v[64:67], a[72:75]\n\t" \
  "v_mfma_f32_16x16x32_bf16 a[72:75], v[52:55], v[72:75], a[72:75]\n\t" \
  "v_mfma_f32_16x16x32_bf16 a[76:79], v[56:59], v[64:67], a[76:79]\n\t" \
  "v_mfma_f32_16x16x32_bf16 a[76:79], v[60:63], v[72:75], a[76:79]\n\t" \
  "v_mfma_f32_16x16x32_bf16 a[80:83], v[80:83], v[64:67], a[80:83]\n\t" \
  "v_mfma_f32_16x16x32_bf16 a[80:83], v[84:87], v[72:75], a[80:83]\n\t" \
  "v_mfma_f32_16x16x32_bf16 a[84:87], v[88:91], v[64:67], a[84:87]\n\t" \
  "v_mfma_f32_16x16x32_bf16 a[84:87], v[92:95], v[72:75], a[84:87]\n\t" \
  "v_mfma_f32_16x16x32_bf16 a[88:91], v[112:115], v[64:67], a[88:91]\n\t" \
  "v_mfma_f32_16x16x32_bf16 a[88:91], v[116:119], v[72:75], a[88:91]\n\t" \
  "v_mfma_f32_16x16x32_bf16 a[92:95], v[120:123], v[64:67], a[92:95]\n\t" \
  "v_mfma_f32_16x16x32_bf16 a[92:95], v[124:127], v[72:75], a[92:95]\n\t" \
  "v_mfma_f32_16x16x32_bf16 a[96:99], v[16:19], v[96:99], a[96:99]\n\t" \
  "v_mfma_f32_16x16x32_bf16 a[96:99], v[20:23], v[104:107], a[96:99]\n\t" \
  "v_mfma_f32_16x16x32_bf16 v[172:175], v[216:219], v[96:99], v[172:175]\n\t" \
  "v_mfma_f32_16x16x32_bf16 v[172:175], v[216:219], v[104:107], v[172:175]\n\t" \
  "v_mfma_f32_16x16x32_bf16 a[100:103], v[24:27], v[96:99], a[100:103]\n\t" \
  "v_mfma_f32_16x16x32_bf16 a[100:103], v[28:31], v[104:107], a[100:103]\n\t" \
  "v_mfma_f32_16x16x32_bf16 a[104:107], v[48:51], v[96:99], a[104:107]\n\t" \
  "v_mfma_f32_16x16x32_bf16 a[104:107], v[52:55], v[104:107], a[104:107]\n\t" \
  "v_mfma_f32_16x16x32_bf16 a[108:111], v[56:59], v[96:99], a[108:111]\n\t" \
  "v_mfma_f32_16x16x32_bf16 a[108:111], v[60:63], v[104:107], a[108:111]\n\t" \
  "v_mfma_f32_16x16x32_bf16 a[112:115], v[80:83], v[96:99], a[112:115]\n\t" \
  "v_mfma_f32_16x16x32_bf16 a[112:115], v[84:87], v[104:107], a[112:115]\n\t" \
  "v_mfma_f32_16x16x32_bf16 a[116:119], v[88:91], v[96:99], a[116:119]\n\t" \
  "v_mfma_f32_16x16x32_bf16 a[116:119], v[92:95], v[104:107], a[116:119]\n\t" \
  "v_mfma_f32_16x16x32_bf16 a[120:123], v[112:115], v[96:99], a[120:123]\n\t" \
  "v_mfma_f32_16x16x32_bf16 a[120:123], v[116:119], v[104:107], a[120:123]\n\t" \
  "v_mfma_f32_16x16x32_bf16 a[124:127], v[120:123], v[96:99], a[124:127]\n\t" \
  "v_mfma_f32_16x16x32_bf16 a[124:127], v[124:127], v[104:107], a[124:127]\n\t" \
  "s_nop 7\n\t" \
  "s_waitcnt vmcnt(0)\n\t" \
  "s_barrier\n\t" \
  "s_add_u32 s77, s51, 131072\n\t" \
  "v_add_u32 v248, 14336, v240\n\t" \
  "v_add_u32 v249, 21504, v241\n\t" \
  "v_add_u32 v241, 7168, v241\n\t" \
  "s_mov_b32 s89, 0\n\t" \
  "v_lshlrev_b32 v212, 7, v233\n\t" \
  "s_waitcnt vmcnt(0) lgkmcnt(0)\n\t" \
  "s_barrier\n\t" \
  "v_cmp_gt_u32 vcc, 16, v233\n\t" \
  "v_bfrev_b32 v214, 1\n\t" \
  "s_waitcnt vmcnt(0) lgkmcnt(0)\n\t" \
  "s_barrier\n\t" \
  "v_cndmask_b32 v212, v214, v212, vcc\n\t" \
  "s_waitcnt vmcnt(0) lgkmcnt(0)\n\t" \
  "s_barrier\n\t" \
  "s_add_u32 m0, s38, 16384\n\t" \
  "s_mov_b32 s96, 0\n\t" \
  "buffer_load_dwordx4 v232, s[68:71], s96 offen offset:0 lds\n\t" \
  "s_mov_b32 s96, 6144\n\t" \
  "buffer_load_dwordx4 v232, s[68:71], s96 offen offset:1024 lds\n\t" \
  "s_mov_b32 s96, 12288\n\t" \
  "buffer_load_dwordx4 v232, s[68:71], s96 offen offset:2048 lds\n\t" \
  "s_mov_b32 s96, 18432\n\t" \
  "buffer_load_dwordx4 v232, s[68:71], s96 offen offset:3072 lds\n\t" \
  "s_waitcnt vmcnt(0) lgkmcnt(0)\n\t" \
  "s_barrier\n\t" \
  "s_add_u32 m0, s38, 32768\n\t" \
  "s_mov_b32 s96, 28672\n\t" \
  "buffer_load_dwordx4 v232, s[68:71], s96 offen offset:0 lds\n\t" \
  "s_mov_b32 s96, 34816\n\t" \
  "buffer_load_dwordx4 v232, s[68:71], s96 offen offset:1024 lds\n\t" \
  "s_mov_b32 s96, 40960\n\t" \
  "buffer_load_dwordx4 v232, s[68:71], s96 offen offset:2048 lds\n\t" \
  "s_mov_b32 s96, 47104\n\t" \
  "buffer_load_dwordx4 v232, s[68:71], s96 offen offset:3072 lds\n\t" \
  "s_add_u32 m0, s38, 114688\n\t" \
  "s_mov_b32 s96, 0\n\t" \
  "buffer_load_dwordx4 v232, s[72:75], s96 offen offset:0 lds\n\t" \
  "s_mov_b32 s96, 6144\n\t" \
  "buffer_load_dwordx4 v232, s[72:75], s96 offen offset:1024 lds\n\t" \
  "s_mov_b32 s96, 12288\n\t" \
  "buffer_load_dwordx4 v232, s[72:75], s96 offen offset:2048 lds\n\t" \
  "s_mov_b32 s96, 18432\n\t" \
  "buffer_load_dwordx4 v232, s[72:75], s96 offen offset:3072 lds\n\t" \
  "s_waitcnt vmcnt(0) lgkmcnt(0)\n\t" \
  "s_barrier\n\t" \
  "s_add_u32 m0, s38, 131072\n\t" \
  "s_mov_b32 s96, 28672\n\t" \
  "buffer_load_dwordx4 v232, s[72:75], s96 offen offset:0 lds\n\t" \
  "s_mov_b32 s96, 34816\n\t" \
  "buffer_load_dwordx4 v232, s[72:75], s96 offen offset:1024 lds\n\t" \
  "s_mov_b32 s96, 40960\n\t" \
  "buffer_load_dwordx4 v232, s[72:75], s96 offen offset:2048 lds\n\t" \
  "s_mov_b32 s96, 47104\n\t" \
  "buffer_load_dwordx4 v232, s[72:75], s96 offen offset:3072 lds\n\t" \
  "s_lshr_b32 s78, s38, 6\n\t" \
  "v_xor_b32 v234, s78, v244\n\t" \
  "v_xor_b32 v235, 32, v234\n\t" \
  "v_add_u32 v236, 65536, v234\n\t" \
  "v_add_u32 v237, 65536, v235\n\t" \
  "v_add_u32 v238, 131072, v234\n\t" \
  "v_add_u32 v239, 131072, v235\n\t" \
  "v_lshlrev_b32 v242, 4, v233\n\t" \
  "v_add_u32 v213, s38, v242\n\t" \
  "v_add_u32 v215, 98304, v213\n\t" \
  "v_mov_b32 v243, s36\n\t" \
  "v_lshlrev_b32 v254, 9, v233\n\t" \
  "v_and_b32 v244, 0x38, v233\n\t" \
  "v_lshl_or_b32 v254, v244, 26, v254\n\t" \
  "s_brev_b32 s78, 1\n\t" \
  "v_mov_b32 v244, s78\n\t" \
  "v_cmp_eq_u32 vcc, s39, v233\n\t" \
  "v_add_u32 v245, s39, v233\n\t" \
  "v_bfi_b32 v245, 7, v245, v233\n\t" \
  "v_cndmask_b32 v254, v254, v244, vcc\n\t" \
  "v_and_b32 v245, 31, v245\n\t" \
  "s_waitcnt vmcnt(0) lgkmcnt(0)\n\t" \
  "s_barrier\n\t" \
  "v_lshlrev_b32 v245, 2, v245\n\t" \
  "v_writelane_b32 v244, 0, 0\n\t" \
  "s_waitcnt vmcnt(0) lgkmcnt(0)\n\t" \
  "s_barrier\n\t" \
  "ds_read_b128 v[192:195], v215 offset:32768\n\t" \
  "ds_read_b128 v[196:199], v215 offset:33792\n\t" \
  "ds_read_b128 v[200:203], v215 offset:34816\n\t" \
  "ds_read_b128 v[204:207], v215 offset:35840\n\t" \
  "s_add_u32 s96, s40, 524288\n\t" \
  "s_and_b32 s96, s96, 0x1c0000\n\t" \
  "s_add_u32 s96, s96, s51\n\t" \
  "s_add_u32 m0, s38, 131072\n\t" \
  "s_waitcnt vmcnt(0) lgkmcnt(0)\n\t" \
  "buffer_load_dwordx4 v240, s[24:27], s96 offen offset:0 lds\n\t" \
  "buffer_load_dwordx4 v241, s[24:27], s96 offen offset:1024 lds\n\t" \
  "buffer_load_dwordx4 v248, s[24:27], s96 offen offset:2048 lds\n\t" \
  "buffer_load_dwordx4 v249, s[24:27], s96 offen offset:3072 lds\n\t" \
  "v_lshrrev_b32 v250, 1, v233\n\t" \
  "v_lshlrev_b32 v250, 2, v250\n\t" \
  "v_add_u32 v250, 8192, v250\n\t" \
  "buffer_load_dword v209, v250, s[16:19], 0 offen sc1\n\t" \
  "s_add_u32 s96, s40, 0\n\t" \
  "s_and_b32 s96, s96, 0x1c0000\n\t" \
  "s_add_u32 s96, s96, s51\n\t" \
  "s_add_u32 m0, s38, 0\n\t" \
  "s_nop 0\n\t" \
  "buffer_load_dwordx4 v240, s[24:27], s96 offen offset:0 lds\n\t" \
  "buffer_load_dwordx4 v241, s[24:27], s96 offen offset:1024 lds\n\t" \
  "buffer_load_dwordx4 v248, s[24:27], s96 offen offset:2048 lds\n\t" \
  "buffer_load_dwordx4 v249, s[24:27], s96 offen offset:3072 lds\n\t" \
  "s_waitcnt vmcnt(0) lgkmcnt(0)\n\t" \
  "s_barrier\n\t" \
  "s_add_u32 s96, s40, 0\n\t" \
  "s_and_b32 s96, s96, 0x1c0000\n\t" \
  "s_add_u32 s96, s96, s77\n\t" \
  "s_add_u32 m0, s38, 98304\n\t" \
  "s_nop 0\n\t" \
  "buffer_load_dwordx4 v240, s[24:27], s96 offen offset:0 lds\n\t" \
  "buffer_load_dwordx4 v241, s[24:27], s96 offen offset:1024 lds\n\t" \
  "s_waitcnt vmcnt(0) lgkmcnt(0)\n\t" \
  "v_and_b32 v251, 0xbfffbfff, v209\n\t" \
  "v_cmp_ne_u32_e64 s[84:85], s36, v251\n\t" \
  "s_cmp_lg_u64 s[84:85], 0\n\t" \
  "s_cbranch_scc1 .Lsf_v\n\t" \
  ".Lsf_w:\n\t" \
  "v_and_b32 v251, 1, v233\n\t" \
  "v_lshlrev_b32 v251, 4, v251\n\t" \
  "buffer_load_dwordx4 v248, s[24:27], s96 offen offset:2048 lds\n\t" \
  "v_lshrrev_b32 v251, v251, v209\n\t" \
  "v_and_b32 v251, 0x4000, v251\n\t" \
  "v_cmp_ne_u32_e64 s[84:85], 0, v251\n\t" \
  "s_bcnt1_i32_b32 s98, s84\n\t" \
  "s_bcnt1_i32_b32 s54, s85\n\t" \
  "buffer_load_dwordx4 v249, s[24:27], s96 offen offset:3072 lds\n\t" \
  "s_cmp_gt_u32 s98, s54\n\t" \
  "s_cselect_b32 s55, 0x2000, 0\n\t" \
  "s_cselect_b32 s98, s84, s85\n\t" \
  "s_add_u32 s96, s40, 262144\n\t" \
  "s_and_b32 s96, s96, 0x1c0000\n\t" \
  "s_add_u32 s96, s96, s51\n\t" \
  "s_add_u32 m0, s38, 49152\n\t" \
  "s_cmp_lg_u32 s55, 0\n\t" \
  "s_cselect_b32 s84, s85, s84\n\t" \
  "s_lshr_b32 s85, s98, 16\n\t" \
  "s_bcnt1_i32_b32 s85, s85\n\t" \
  "s_lshr_b32 s84, s84, 16\n\t" \
  "buffer_load_dwordx4 v240, s[24:27], s96 offen offset:0 lds\n\t" \
  "s_bcnt1_i32_b32 s84, s84\n\t" \
  "s_add_u32 s84, s84, 7\n\t" \
  "s_cmp_ge_u32 s85, s84\n\t" \
  "s_cselect_b32 s54, -1, 0\n\t" \
  "s_xor_b32 s98, s42, s55\n\t" \
  "s_and_b32 s98, s98, s54\n\t" \
  "s_xor_b32 s45, s45, s98\n\t" \
  "s_xor_b32 s46, s46, s98\n\t" \
  "buffer_load_dwordx4 v241, s[24:27], s96 offen offset:1024 lds\n\t" \
  "s_xor_b32 s98, s43, s55\n\t" \
  "s_and_b32 s98, s98, s54\n\t" \
  "s_xor_b32 s47, s47, s98\n\t" \
  "s_xor_b32 s48, s48, s55\n\t" \
  "s_xor_b32 s49, s49, s55\n\t" \
  "buffer_load_dwordx4 v248, s[24:27], s96 offen offset:2048 lds\n\t" \
  "s_xor_b32 s98, s45, 0\n\t" \
  "s_lshl_b32 s64, s39, 17\n\t" \
  "s_bfe_u32 s70, s46, 0x20014\n\t" \
  "buffer_load_dwordx4 v249, s[24:27], s96 offen offset:3072 lds\n\t" \
  "s_lshl_b32 s70, s70, 14\n\t" \
  "s_xor_b32 s67, s70, 49152\n\t" \
  "s_lshl1_add_u32 s60, s67, s46\n\t" \
  "s_xor_b32 s68, s70, 32768\n\t" \
  "s_lshl1_add_u32 s61, s68, s46\n\t" \
  "s_xor_b32 s69, s70, 16384\n\t" \
  "s_lshl1_add_u32 s62, s69, s46\n\t" \
  "s_lshl1_add_u32 s63, s70, s46\n\t" \
  "s_mul_i32 s65, s39, 508\n\t" \
  "s_add_u32 s65, s65, s49\n\t" \
  "s_lshl_b32 s80, s39, 2\n\t" \
  "s_add_u32 s80, s80, s3\n\t" \
  "s_waitcnt vmcnt(0) lgkmcnt(0)\n\t" \
  "s_barrier\n\t" \
  "s_lshl_b32 s80, s80, 6\n\t" \
  "s_add_u32 s76, s38, 147456\n\t" \
  "s_lshl_b32 s75, s80, 2\n\t" \
  "s_add_u32 s75, s75, 2228224\n\t" \
  "s_add_u32 s96, s40, 262144\n\t" \
  "s_and_b32 s96, s96, 0x1c0000\n\t" \
  "s_add_u32 s96, s96, s77\n\t" \
  "s_add_u32 m0, s38, 65536\n\t" \
  "s_nop 0\n\t" \
  "buffer_load_dwordx4 v240, s[24:27], s96 offen offset:0 lds\n\t" \
  "s_lshr_b32 s71, s67, 7\n\t" \
  "s_add_u32 s71, s71, s48\n\t" \
  "s_lshr_b32 s72, s68, 7\n\t" \
  "s_add_u32 s72, s72, s48\n\t" \
  "s_lshr_b32 s66, s69, 7\n\t" \
  "s_add_u32 s66, s66, s48\n\t" \
  "s_lshr_b32 s73, s70, 7\n\t" \
  "s_add_u32 s73, s73, s48\n\t" \
  "s_lshl3_add_u32 s67, s67, s47\n\t" \
  "buffer_load_dwordx4 v241, s[24:27], s96 offen offset:1024 lds\n\t" \
  "s_lshl3_add_u32 s68, s68, s47\n\t" \
  "s_add_u32 s68, s68, 4096\n\t" \
  "s_lshl3_add_u32 s69, s69, s47\n\t" \
  "s_add_u32 s69, s69, 16384\n\t" \
  "s_lshl3_add_u32 s70, s70, s47\n\t" \
  "buffer_load_dwordx4 v248, s[24:27], s96 offen offset:2048 lds\n\t" \
  "buffer_load_dwordx4 v249, s[24:27], s96 offen offset:3072 lds\n\t" \
  "s_add_u32 s96, s40, 524288\n\t" \
  "s_and_b32 s96, s96, 0x1c0000\n\t" \
  "s_add_u32 s96, s96, s77\n\t" \
  "s_add_u32 m0, s38, 81920\n\t" \
  "s_waitcnt vmcnt(0) lgkmcnt(0)\n\t" \
  "s_barrier\n\t" \
  "buffer_load_dwordx4 v240, s[24:27], s96 offen offset:0 lds\n\t" \
  "ds_read_b128 v[176:179], v215 offset:16384\n\t" \
  "ds_read_b128 v[180:183], v215 offset:17408\n\t" \
  "ds_read_b128 v[184:187], v215 offset:18432\n\t" \
  "buffer_load_dwordx4 v241, s[24:27], s96 offen offset:1024 lds\n\t" \
  "ds_read_b128 v[188:191], v215 offset:19456\n\t" \
  "buffer_load_dwordx4 v248, s[24:27], s96 offen offset:2048 lds\n\t" \
  "buffer_load_dwordx4 v249, s[24:27], s96 offen offset:3072 lds\n\t" \
  "s_add_u32 s96, s40, 786432\n\t" \
  "s_and_b32 s96, s96, 0x1c0000\n\t" \
  "s_add_u32 s96, s96, s51\n\t" \
  "s_add_u32 m0, s38, 114688\n\t" \
  "s_waitcnt vmcnt(0) lgkmcnt(0)\n\t" \
  "buffer_load_dwordx4 v240, s[24:27], s96 offen offset:0 lds\n\t" \
  "v_add_u32 v230, 0x10000, v242\n\t" \
  "v_add_u32 v232, 0x20000, v242\n\t" \
  "buffer_load_dwordx4 v241, s[24:27], s96 offen offset:1024 lds\n\t" \
  "buffer_load_dwordx4 v248, s[24:27], s96 offen offset:2048 lds\n\t" \
  "buffer_load_dwordx4 v249, s[24:27], s96 offen offset:3072 lds\n\t" \
  "ds_read_b64_tr_b16 a[192:193], v234 offset:0\n\t" \
  "ds_read_b64_tr_b16 a[194:195], v234 offset:4096\n\t" \
  "ds_read_b64_tr_b16 a[196:197], v235 offset:0\n\t" \
  "ds_read_b64_tr_b16 a[198:199], v235 offset:4096\n\t" \
  "v_rcp_f32 v224, v160\n\t" \
  "v_accvgpr_read_b32 v128, a0\n\t" \
  "v_accvgpr_read_b32 v129, a1\n\t" \
  "v_accvgpr_read_b32 v130, a2\n\t" \
  "v_accvgpr_read_b32 v131, a3\n\t" \
  "v_accvgpr_read_b32 v132, a4\n\t" \
  "v_accvgpr_read_b32 v133, a5\n\t" \
  "v_accvgpr_read_b32 v134, a6\n\t" \
  "v_accvgpr_read_b32 v135, a7\n\t" \
  "v_pk_mul_f32 v[128:129], v[128:129], v[224:225] op_sel_hi:[1,0]\n\t" \
  "v_pk_mul_f32 v[130:131], v[130:131], v[224:225] op_sel_hi:[1,0]\n\t" \
  "v_pk_mul_f32 v[132:133], v[132:133], v[224:225] op_sel_hi:[1,0]\n\t" \
  "v_pk_mul_f32 v[134:135], v[134:135], v[224:225] op_sel_hi:[1,0]\n\t" \
  "v_cvt_pk_bf16_f32 v128, v128, v129\n\t" \
  "v_cvt_pk_bf16_f32 v129, v130, v131\n\t" \
  "v_cvt_pk_bf16_f32 v130, v132, v133\n\t" \
  "v_cvt_pk_bf16_f32 v131, v134, v135\n\t" \
  "buffer_store_dwordx4 v[128:131], v242, s[16:19], s98 offen offset:0 sc1\n\t" \
  "ds_read_b64_tr_b16 a[200:201], v234 offset:8192\n\t" \
  "ds_read_b64_tr_b16 a[202:203], v234 offset:12288\n\t" \
  "v_accvgpr_read_b32 v136, a8\n\t" \
  "v_accvgpr_read_b32 v137, a9\n\t" \
  "v_accvgpr_read_b32 v138, a10\n\t" \
  "v_accvgpr_read_b32 v139, a11\n\t" \
  "v_accvgpr_read_b32 v140, a12\n\t" \
  "v_accvgpr_read_b32 v141, a13\n\t" \
  "v_accvgpr_read_b32 v142, a14\n\t" \
  "v_accvgpr_read_b32 v143, a15\n\t" \
  "v_pk_mul_f32 v[136:137], v[136:137], v[224:225] op_sel_hi:[1,0]\n\t" \
  "v_pk_mul_f32 v[138:139], v[138:139], v[224:225] op_sel_hi:[1,0]\n\t" \
  "v_pk_mul_f32 v[140:141], v[140:141], v[224:225] op_sel_hi:[1,0]\n\t" \
  "v_pk_mul_f32 v[142:143], v[142:143], v[224:225] op_sel_hi:[1,0]\n\t" \
  "v_cvt_pk_bf16_f32 v136, v136, v137\n\t" \
  "v_cvt_pk_bf16_f32 v137, v138, v139\n\t" \
  "v_cvt_pk_bf16_f32 v138, v140, v141\n\t" \
  "v_cvt_pk_bf16_f32 v139, v142, v143\n\t" \
  "buffer_store_dwordx4 v[136:139], v242, s[16:19], s98 offen offset:1024 sc1\n\t" \
  "ds_read_b64_tr_b16 a[204:205], v235 offset:8192\n\t" \
  "ds_read_b128 a[224:227], v213 offset:16384\n\t" \
  "ds_read_b64_tr_b16 a[206:207], v235 offset:12288\n\t" \
  "ds_read_b128 a[228:231], v213 offset:17408\n\t" \
  "ds_read_b128 a[232:235], v213 offset:18432\n\t" \
  "ds_read_b128 a[236:239], v213 offset:19456\n\t" \
  "ds_write_b128 v213, v[128:131] offset:16384\n\t" \
  "s_waitcnt vmcnt(0) lgkmcnt(0)\n\t" \
  "s_barrier\n\t" \
  "ds_write_b128 v213, v[136:139] offset:17408\n\t" \
  "v_accvgpr_read_b32 v0, a16\n\t" \
  "v_accvgpr_read_b32 v1, a17\n\t" \
  "v_accvgpr_read_b32 v2, a18\n\t" \
  "v_accvgpr_read_b32 v3, a19\n\t" \
  "v_accvgpr_read_b32 v4, a20\n\t" \
  "v_accvgpr_read_b32 v5, a21\n\t" \
  "v_accvgpr_read_b32 v6, a22\n\t" \
  "v_accvgpr_read_b32 v7, a23\n\t" \
  "v_pk_mul_f32 v[0:1], v[0:1], v[224:225] op_sel_hi:[1,0]\n\t" \
  "v_pk_mul_f32 v[2:3], v[2:3], v[224:225] op_sel_hi:[1,0]\n\t" \
  "v_pk_mul_f32 v[4:5], v[4:5], v[224:225] op_sel_hi:[1,0]\n\t" \
  "v_pk_mul_f32 v[6:7], v[6:7], v[224:225] op_sel_hi:[1,0]\n\t" \
  "v_cvt_pk_bf16_f32 v0, v0, v1\n\t" \
  "v_cvt_pk_bf16_f32 v1, v2, v3\n\t" \
  "v_cvt_pk_bf16_f32 v2, v4, v5\n\t" \
  "v_cvt_pk_bf16_f32 v3, v6, v7\n\t" \
  "buffer_store_dwordx4 v[0:3], v242, s[16:19], s98 offen offset:2048 sc1\n\t" \
  "ds_write_b128 v213, v[0:3] offset:18432\n\t" \
  "v_accvgpr_read_b32 v8, a24\n\t" \
  "v_accvgpr_read_b32 v9, a25\n\t" \
  "v_accvgpr_read_b32 v10, a26\n\t" \
  "v_accvgpr_read_b32 v11, a27\n\t" \
  "v_accvgpr_read_b32 v12, a28\n\t" \
  "v_accvgpr_read_b32 v13, a29\n\t" \
  "v_accvgpr_read_b32 v14, a30\n\t" \
  "v_accvgpr_read_b32 v15, a31\n\t" \
  "v_pk_mul_f32 v[8:9], v[8:9], v[224:225] op_sel_hi:[1,0]\n\t" \
  "v_pk_mul_f32 v[10:11], v[10:11], v[224:225] op_sel_hi:[1,0]\n\t" \
  "v_pk_mul_f32 v[12:13], v[12:13], v[224:225] op_sel_hi:[1,0]\n\t" \
  "v_pk_mul_f32 v[14:15], v[14:15], v[224:225] op_sel_hi:[1,0]\n\t" \
  "v_cvt_pk_bf16_f32 v8, v8, v9\n\t" \
  "v_cvt_pk_bf16_f32 v9, v10, v11\n\t" \
  "v_cvt_pk_bf16_f32 v10, v12, v13\n\t" \
  "v_cvt_pk_bf16_f32 v11, v14, v15\n\t" \
  "buffer_store_dwordx4 v[8:11], v242, s[16:19], s98 offen offset:3072 sc1\n\t" \
  "ds_write_b128 v213, v[8:11] offset:19456\n\t" \
  "s_xor_b32 s98, s45, 32768\n\t" \
  "ds_read_b64_tr_b16 a[208:209], v236 offset:32768\n\t" \
  "ds_read_b64_tr_b16 a[210:211], v236 offset:36864\n\t" \
  "ds_read_b64_tr_b16 a[212:213], v237 offset:32768\n\t" \
  "ds_read_b64_tr_b16 a[214:215], v237 offset:36864\n\t" \
  "s_add_u32 s96, s40, 786432\n\t" \
  "s_and_b32 s96, s96, 0x1c0000\n\t" \
  "ds_read_b64_tr_b16 a[216:217], v236 offset:40960\n\t" \
  "s_add_u32 s96, s96, s77\n\t" \
  "s_add_u32 m0, s38, 147456\n\t" \
  "ds_read_b64_tr_b16 a[218:219], v236 offset:45056\n\t" \
  "v_rcp_f32 v226, v164\n\t" \
  "buffer_load_dwordx4 v240, s[24:27], s96 offen offset:0 lds\n\t" \
  "v_accvgpr_read_b32 v0, a32\n\t" \
  "v_accvgpr_read_b32 v1, a33\n\t" \
  "v_accvgpr_read_b32 v2, a34\n\t" \
  "v_accvgpr_read_b32 v3, a35\n\t" \
  "buffer_load_dwordx4 v241, s[24:27], s96 offen offset:1024 lds\n\t" \
  "v_accvgpr_read_b32 v4, a36\n\t" \
  "v_accvgpr_read_b32 v5, a37\n\t" \
  "ds_read_b64_tr_b16 a[220:221], v237 offset:40960\n\t" \
  "v_accvgpr_read_b32 v6, a38\n\t" \
  "v_accvgpr_read_b32 v7, a39\n\t" \
  "buffer_load_dwordx4 v248, s[24:27], s96 offen offset:2048 lds\n\t" \
  "ds_read_b64_tr_b16 a[222:223], v237 offset:45056\n\t" \
  "v_mul_f32 v0, v0, v226\n\t" \
  "v_mul_f32 v1, v1, v226\n\t" \
  "v_mul_f32 v2, v2, v226\n\t" \
  "v_mul_f32 v3, v3, v226\n\t" \
  "buffer_load_dwordx4 v249, s[24:27], s96 offen offset:3072 lds\n\t" \
  "v_mul_f32 v4, v4, v226\n\t" \
  "v_mul_f32 v5, v5, v226\n\t" \
  "v_mul_f32 v6, v6, v226\n\t" \
  "v_mul_f32 v7, v7, v226\n\t" \
  "v_cvt_pk_bf16_f32 v32, v0, v1\n\t" \
  "v_cvt_pk_bf16_f32 v33, v2, v3\n\t" \
  "v_cvt_pk_bf16_f32 v34, v4, v5\n\t" \
  "v_cvt_pk_bf16_f32 v35, v6, v7\n\t" \
  "v_accvgpr_read_b32 v8, a40\n\t" \
  "v_accvgpr_read_b32 v9, a41\n\t" \
  "v_accvgpr_read_b32 v10, a42\n\t" \
  "v_accvgpr_read_b32 v11, a43\n\t" \
  "v_accvgpr_read_b32 v12, a44\n\t" \
  "v_accvgpr_read_b32 v13, a45\n\t" \
  "v_accvgpr_read_b32 v14, a46\n\t" \
  "v_accvgpr_read_b32 v15, a47\n\t" \
  "v_mul_f32 v8, v8, v226\n\t" \
  "v_mul_f32 v9, v9, v226\n\t" \
  "v_mul_f32 v10, v10, v226\n\t" \
  "v_mul_f32 v11, v11, v226\n\t" \
  "v_mul_f32 v12, v12, v226\n\t" \
  "v_mul_f32 v13, v13, v226\n\t" \
  "v_mul_f32 v14, v14, v226\n\t" \
  "v_mul_f32 v15, v15, v226\n\t" \
  "v_cvt_pk_bf16_f32 v36, v8, v9\n\t" \
  "v_cvt_pk_bf16_f32 v37, v10, v11\n\t" \
  "v_cvt_pk_bf16_f32 v38, v12, v13\n\t" \
  "v_cvt_pk_bf16_f32 v39, v14, v15\n\t" \
  "v_accvgpr_read_b32 v0, a48\n\t" \
  "v_accvgpr_read_b32 v1, a49\n\t" \
  "s_waitcnt vmcnt(0) lgkmcnt(0)\n\t" \
  "s_barrier\n\t" \
  "buffer_store_dwordx4 v[32:35], v242, s[16:19], s98 offen offset:0 sc1\n\t" \
  "v_accvgpr_read_b32 v2, a50\n\t" \
  "v_accvgpr_read_b32 v3, a51\n\t" \
  "v_accvgpr_read_b32 v4, a52\n\t" \
  "s_add_u32 s96, s40, 1048576\n\t" \
  "s_and_b32 s96, s96, 0x1c0000\n\t" \
  "s_add_u32 s96, s96, s51\n\t" \
  "v_accvgpr_read_b32 v5, a53\n\t" \
  "s_add_u32 m0, s38, 0\n\t" \
  "v_accvgpr_read_b32 v6, a54\n\t" \
  "v_accvgpr_read_b32 v7, a55\n\t" \
  "v_mul_f32 v0, v0, v226\n\t" \
  "buffer_load_dwordx4 v240, s[24:27], s96 offen offset:0 lds\n\t" \
  "v_mul_f32 v1, v1, v226\n\t" \
  "v_mul_f32 v2, v2, v226\n\t" \
  "v_mul_f32 v3, v3, v226\n\t" \
  "v_mul_f32 v4, v4, v226\n\t" \
  "v_mul_f32 v5, v5, v226\n\t" \
  "v_mul_f32 v6, v6, v226\n\t" \
  "v_mul_f32 v7, v7, v226\n\t" \
  "buffer_load_dwordx4 v241, s[24:27], s96 offen offset:1024 lds\n\t" \
  "v_cvt_pk_bf16_f32 v40, v0, v1\n\t" \
  "v_cvt_pk_bf16_f32 v41, v2, v3\n\t" \
  "v_cvt_pk_bf16_f32 v42, v4, v5\n\t" \
  "v_cvt_pk_bf16_f32 v43, v6, v7\n\t" \
  "v_accvgpr_read_b32 v8, a56\n\t" \
  "v_accvgpr_read_b32 v9, a57\n\t" \
  "v_accvgpr_read_b32 v10, a58\n\t" \
  "v_accvgpr_read_b32 v11, a59\n\t" \
  "v_accvgpr_read_b32 v12, a60\n\t" \
  "buffer_load_dwordx4 v248, s[24:27], s96 offen offset:2048 lds\n\t" \
  "v_accvgpr_read_b32 v13, a61\n\t" \
  "v_accvgpr_read_b32 v14, a62\n\t" \
  "v_accvgpr_read_b32 v15, a63\n\t" \
  "v_mul_f32 v8, v8, v226\n\t" \
  "v_mul_f32 v9, v9, v226\n\t" \
  "v_mul_f32 v10, v10, v226\n\t" \
  "v_mul_f32 v11, v11, v226\n\t" \
  "v_mul_f32 v12, v12, v226\n\t" \
  "buffer_store_dwordx4 v[36:39], v242, s[16:19], s98 offen offset:1024 sc1\n\t" \
  "v_mul_f32 v13, v13, v226\n\t" \
  "v_mul_f32 v14, v14, v226\n\t" \
  "v_mul_f32 v15, v15, v226\n\t" \
  "v_cvt_pk_bf16_f32 v44, v8, v9\n\t" \
  "v_cvt_pk_bf16_f32 v45, v10, v11\n\t" \
  "v_rcp_f32 v220, v168\n\t" \
  "buffer_load_dwordx4 v249, s[24:27], s96 offen offset:3072 lds\n\t" \
  "ds_read_b64_tr_b16 a[128:129], v234 offset:49152\n\t" \
  "v_cvt_pk_bf16_f32 v46, v12, v13\n\t" \
  "ds_read_b64_tr_b16 a[130:131], v234 offset:53248\n\t" \
  "v_cvt_pk_bf16_f32 v47, v14, v15\n\t" \
  "ds_read_b64_tr_b16 a[132:133], v235 offset:49152\n\t" \
  "ds_read_b128 a[240:243], v213 offset:32768\n\t" \
  "s_waitcnt vmcnt(0) lgkmcnt(0)\n\t" \
  "buffer_store_dword v243, v254, s[16:19], s49 offen offset:0 sc1\n\t" \
  "ds_read_b64_tr_b16 a[134:135], v235 offset:53248\n\t" \
  "ds_read_b128 a[244:247], v213 offset:33792\n\t" \
  "buffer_store_dwordx4 v[40:43], v242, s[16:19], s98 offen offset:2048 sc1\n\t" \
  "v_accvgpr_read_b32 v64, a64\n\t" \
  "v_accvgpr_read_b32 v65, a65\n\t" \
  "v_accvgpr_read_b32 v66, a66\n\t" \
  "v_accvgpr_read_b32 v67, a67\n\t" \
  "v_accvgpr_read_b32 v68, a68\n\t" \
  "s_waitcnt vmcnt(0) lgkmcnt(0)\n\t" \
  "s_barrier\n\t" \
  "ds_read_b128 v[128:131], v242 offset:16384\n\t" \
  "v_accvgpr_read_b32 v69, a69\n\t" \
  "v_accvgpr_read_b32 v70, a70\n\t" \
  "v_accvgpr_read_b32 v71, a71\n\t" \
  "buffer_store_dwordx4 v[44:47], v242, s[16:19], s98 offen offset:3072 sc1\n\t" \
  "v_mul_f32 v64, v64, v220\n\t" \
  "ds_read_b128 v[132:135], v242 offset:17408\n\t" \
  "v_mul_f32 v65, v65, v220\n\t" \
  "s_xor_b32 s98, s45, 65536\n\t" \
  "v_rcp_f32 v222, v172\n\t" \
  "ds_read_b128 v[136:139], v242 offset:18432\n\t" \
  "ds_read_b128 v[140:143], v242 offset:19456\n\t" \
  "v_mul_f32 v66, v66, v220\n\t" \
  "ds_read_b128 v[144:147], v242 offset:20480\n\t" \
  "v_mul_f32 v67, v67, v220\n\t" \
  "ds_read_b64_tr_b16 a[136:137], v234 offset:57344\n\t" \
  "v_mul_f32 v68, v68, v220\n\t" \
  "ds_read_b128 v[148:151], v242 offset:21504\n\t" \
  "v_mul_f32 v69, v69, v220\n\t" \
  "ds_read_b64_tr_b16 a[138:139], v234 offset:61440\n\t" \
  "v_mul_f32 v70, v70, v220\n\t" \
  "ds_read_b128 v[152:155], v242 offset:22528\n\t" \
  "v_mul_f32 v71, v71, v220\n\t" \
  "ds_read_b128 v[156:159], v242 offset:23552\n\t" \
  "ds_read_b64_tr_b16 a[140:141], v235 offset:57344\n\t" \
  "ds_read_b64_tr_b16 a[142:143], v235 offset:61440\n\t" \
  "ds_read_b128 a[248:251], v213 offset:34816\n\t" \
  "s_waitcnt vmcnt(0) lgkmcnt(0)\n\t" \
  "s_nop 1\n\t" \
  "s_branch .Lat_join\n\t" \
  ".Lsf_c00:\n\t" \
  "s_mov_b32 vcc_lo, 0\n\t" \
  "s_branch .Lsf_s0\n\t" \
  ".Lsf_c10:\n\t" \
  "s_mov_b32 vcc_lo, 1\n\t" \
  "s_branch .Lsf_s0\n\t" \
  ".Lsf_c20:\n\t" \
  "s_mov_b32 vcc_lo, 2\n\t" \
  "s_branch .Lsf_s0\n\t" \
  ".Lsf_c30:\n\t" \
  "s_mov_b32 vcc_lo, 3\n\t" \
  "s_branch .Lsf_s0\n\t" \
  ".Lsf_c40:\n\t" \
  "s_mov_b32 vcc_lo, 4\n\t" \
  "s_branch .Lsf_s0\n\t" \
  ".Lsf_c50:\n\t" \
  "s_mov_b32 vcc_lo, 5\n\t" \
  "s_branch .Lsf_s0\n\t" \
  ".Lsf_c60:\n\t" \
  "s_mov_b32 vcc_lo, 6\n\t" \
  "s_branch .Lsf_s0\n\t" \
  ".Lsf_c70:\n\t" \
  "s_mov_b32 vcc_lo, 7\n\t" \
  "s_branch .Lsf_s0\n\t" \
  ".Lsf_s0:\n\t" \
  "s_nop 7\n\t" \
  "s_nop 1\n\t" \
  "v_mov_b32 v193, v192\n\t" \
  "s_nop 1\n\t" \
  "v_permlane32_swap_b32 v192, v193\n\t" \
  "s_nop 1\n\t" \
  "v_max_f32 v192, v192, v193\n\t" \
  "s_nop 1\n\t" \
  "v_mov_b32 v193, v192\n\t" \
  "s_nop 1\n\t" \
  "v_permlane16_swap_b32 v192, v193\n\t" \
  "s_nop 1\n\t" \
  "v_max_f32 v192, v192, v193\n\t" \
  "v_max_f32 v192, 0, v192\n\t" \
  "v_sub_f32 v0, v0, v192\n\t" \
  "v_sub_f32 v1, v1, v192\n\t" \
  "v_sub_f32 v2, v2, v192\n\t" \
  "v_sub_f32 v3, v3, v192\n\t" \
  "v_sub_f32 v4, v4, v192\n\t" \
  "v_sub_f32 v5, v5, v192\n\t" \
  "v_sub_f32 v6, v6, v192\n\t" \
  "v_sub_f32 v7, v7, v192\n\t" \
  "v_sub_f32 v8, v8, v192\n\t" \
  "v_sub_f32 v9, v9, v192\n\t" \
  "v_sub_f32 v10, v10, v192\n\t" \
  "v_sub_f32 v11, v11, v192\n\t" \
  "v_sub_f32 v12, v12, v192\n\t" \
  "v_sub_f32 v13, v13, v192\n\t" \
  "v_sub_f32 v14, v14, v192\n\t" \
  "v_sub_f32 v15, v15, v192\n\t" \
  "v_sub_f32 v176, v176, v192\n\t" \
  "v_sub_f32 v177, v177, v192\n\t" \
  "v_sub_f32 v178, v178, v192\n\t" \
  "v_sub_f32 v179, v179, v192\n\t" \
  "v_exp_f32 v196, -v192\n\t" \
  "s_mov_b32 s99, 1\n\t" \
  "s_nop 1\n\t" \
  "s_cmp_eq_u32 vcc_lo, 0\n\t" \
  "s_cbranch_scc1 .Lsf_r00\n\t" \
  "s_cmp_eq_u32 vcc_lo, 1\n\t" \
  "s_cbranch_scc1 .Lsf_r10\n\t" \
  "s_cmp_eq_u32 vcc_lo, 2\n\t" \
  "s_cbranch_scc1 .Lsf_r20\n\t" \
  "s_cmp_eq_u32 vcc_lo, 3\n\t" \
  "s_cbranch_scc1 .Lsf_r30\n\t" \
  "s_cmp_eq_u32 vcc_lo, 4\n\t" \
  "s_cbranch_scc1 .Lsf_r40\n\t" \
  "s_cmp_eq_u32 vcc_lo, 5\n\t" \
  "s_cbranch_scc1 .Lsf_r50\n\t" \
  "s_cmp_eq_u32 vcc_lo, 6\n\t" \
  "s_cbranch_scc1 .Lsf_r60\n\t" \
  "s_cmp_eq_u32 vcc_lo, 7\n\t" \
  "s_cbranch_scc1 .Lsf_r70\n\t" \
  ".Lsf_c01:\n\t" \
  "s_mov_b32 vcc_lo, 0\n\t" \
  "s_branch .Lsf_s1\n\t" \
  ".Lsf_c11:\n\t" \
  "s_mov_b32 vcc_lo, 1\n\t" \
  "s_branch .Lsf_s1\n\t" \
  ".Lsf_c21:\n\t" \
  "s_mov_b32 vcc_lo, 2\n\t" \
  "s_branch .Lsf_s1\n\t" \
  ".Lsf_c31:\n\t" \
  "s_mov_b32 vcc_lo, 3\n\t" \
  "s_branch .Lsf_s1\n\t" \
  ".Lsf_c41:\n\t" \
  "s_mov_b32 vcc_lo, 4\n\t" \
  "s_branch .Lsf_s1\n\t" \
  ".Lsf_c51:\n\t" \
  "s_mov_b32 vcc_lo, 5\n\t" \
  "s_branch .Lsf_s1\n\t" \
  ".Lsf_c61:\n\t" \
  "s_mov_b32 vcc_lo, 6\n\t" \
  "s_branch .Lsf_s1\n\t" \
  ".Lsf_c71:\n\t" \
  "s_mov_b32 vcc_lo, 7\n\t" \
  "s_branch .Lsf_s1\n\t" \
  ".Lsf_s1:\n\t" \
  "s_nop 7\n\t" \
  "s_nop 1\n\t" \
  "v_mov_b32 v193, v192\n\t" \
  "s_nop 1\n\t" \
  "v_permlane32_swap_b32 v192, v193\n\t" \
  "s_nop 1\n\t" \
  "v_max_f32 v192, v192, v193\n\t" \
  "s_nop 1\n\t" \
  "v_mov_b32 v193, v192\n\t" \
  "s_nop 1\n\t" \
  "v_permlane16_swap_b32 v192, v193\n\t" \
  "s_nop 1\n\t" \
  "v_max_f32 v192, v192, v193\n\t" \
  "v_max_f32 v192, 0, v192\n\t" \
  "v_sub_f32 v32, v32, v192\n\t" \
  "v_sub_f32 v33, v33, v192\n\t" \
  "v_sub_f32 v34, v34, v192\n\t" \
  "v_sub_f32 v35, v35, v192\n\t" \
  "v_sub_f32 v36, v36, v192\n\t" \
  "v_sub_f32 v37, v37, v192\n\t" \
  "v_sub_f32 v38, v38, v192\n\t" \
  "v_sub_f32 v39, v39, v192\n\t" \
  "v_sub_f32 v40, v40, v192\n\t" \
  "v_sub_f32 v41, v41, v192\n\t" \
  "v_sub_f32 v42, v42, v192\n\t" \
  "v_sub_f32 v43, v43, v192\n\t" \
  "v_sub_f32 v44, v44, v192\n\t" \
  "v_sub_f32 v45, v45, v192\n\t" \
  "v_sub_f32 v46, v46, v192\n\t" \
  "v_sub_f32 v47, v47, v192\n\t" \
  "v_sub_f32 v180, v180, v192\n\t" \
  "v_sub_f32 v181, v181, v192\n\t" \
  "v_sub_f32 v182, v182, v192\n\t" \
  "v_sub_f32 v183, v183, v192\n\t" \
  "v_exp_f32 v197, -v192\n\t" \
  "s_mov_b32 s99, 1\n\t" \
  "s_nop 1\n\t" \
  "s_cmp_eq_u32 vcc_lo, 0\n\t" \
  "s_cbranch_scc1 .Lsf_r01\n\t" \
  "s_cmp_eq_u32 vcc_lo, 1\n\t" \
  "s_cbranch_scc1 .Lsf_r11\n\t" \
  "s_cmp_eq_u32 vcc_lo, 2\n\t" \
  "s_cbranch_scc1 .Lsf_r21\n\t" \
  "s_cmp_eq_u32 vcc_lo, 3\n\t" \
  "s_cbranch_scc1 .Lsf_r31\n\t" \
  "s_cmp_eq_u32 vcc_lo, 4\n\t" \
  "s_cbranch_scc1 .Lsf_r41\n\t" \
  "s_cmp_eq_u32 vcc_lo, 5\n\t" \
  "s_cbranch_scc1 .Lsf_r51\n\t" \
  "s_cmp_eq_u32 vcc_lo, 6\n\t" \
  "s_cbranch_scc1 .Lsf_r61\n\t" \
  "s_cmp_eq_u32 vcc_lo, 7\n\t" \
  "s_cbranch_scc1 .Lsf_r71\n\t" \
  ".Lsf_c02:\n\t" \
  "s_mov_b32 vcc_lo, 0\n\t" \
  "s_branch .Lsf_s2\n\t" \
  ".Lsf_c12:\n\t" \
  "s_mov_b32 vcc_lo, 1\n\t" \
  "s_branch .Lsf_s2\n\t" \
  ".Lsf_c22:\n\t" \
  "s_mov_b32 vcc_lo, 2\n\t" \
  "s_branch .Lsf_s2\n\t" \
  ".Lsf_c32:\n\t" \
  "s_mov_b32 vcc_lo, 3\n\t" \
  "s_branch .Lsf_s2\n\t" \
  ".Lsf_c42:\n\t" \
  "s_mov_b32 vcc_lo, 4\n\t" \
  "s_branch .Lsf_s2\n\t" \
  ".Lsf_c52:\n\t" \
  "s_mov_b32 vcc_lo, 5\n\t" \
  "s_branch .Lsf_s2\n\t" \
  ".Lsf_c62:\n\t" \
  "s_mov_b32 vcc_lo, 6\n\t" \
  "s_branch .Lsf_s2\n\t" \
  ".Lsf_c72:\n\t" \
  "s_mov_b32 vcc_lo, 7\n\t" \
  "s_branch .Lsf_s2\n\t" \
  ".Lsf_s2:\n\t" \
  "s_nop 7\n\t" \
  "s_nop 1\n\t" \
  "v_mov_b32 v193, v192\n\t" \
  "s_nop 1\n\t" \
  "v_permlane32_swap_b32 v192, v193\n\t" \
  "s_nop 1\n\t" \
  "v_max_f32 v192, v192, v193\n\t" \
  "s_nop 1\n\t" \
  "v_mov_b32 v193, v192\n\t" \
  "s_nop 1\n\t" \
  "v_permlane16_swap_b32 v192, v193\n\t" \
  "s_nop 1\n\t" \
  "v_max_f32 v192, v192, v193\n\t" \
  "v_max_f32 v192, 0, v192\n\t" \
  "v_sub_f32 v64, v64, v192\n\t" \
  "v_sub_f32 v65, v65, v192\n\t" \
  "v_sub_f32 v66, v66, v192\n\t" \
  "v_sub_f32 v67, v67, v192\n\t" \
  "v_sub_f32 v68, v68, v192\n\t" \
  "v_sub_f32 v69, v69, v192\n\t" \
  "v_sub_f32 v70, v70, v192\n\t" \
  "v_sub_f32 v71, v71, v192\n\t" \
  "v_sub_f32 v72, v72, v192\n\t" \
  "v_sub_f32 v73, v73, v192\n\t" \
  "v_sub_f32 v74, v74, v192\n\t" \
  "v_sub_f32 v75, v75, v192\n\t" \
  "v_sub_f32 v76, v76, v192\n\t" \
  "v_sub_f32 v77, v77, v192\n\t" \
  "v_sub_f32 v78, v78, v192\n\t" \
  "v_sub_f32 v79, v79, v192\n\t" \
  "v_sub_f32 v184, v184, v192\n\t" \
  "v_sub_f32 v185, v185, v192\n\t" \
  "v_sub_f32 v186, v186, v192\n\t" \
  "v_sub_f32 v187, v187, v192\n\t" \
  "v_exp_f32 v198, -v192\n\t" \
  "s_mov_b32 s99, 1\n\t" \
  "s_nop 1\n\t" \
  "s_cmp_eq_u32 vcc_lo, 0\n\t" \
  "s_cbranch_scc1 .Lsf_r02\n\t" \
  "s_cmp_eq_u32 vcc_lo, 1\n\t" \
  "s_cbranch_scc1 .Lsf_r12\n\t" \
  "s_cmp_eq_u32 vcc_lo, 2\n\t" \
  "s_cbranch_scc1 .Lsf_r22\n\t" \
  "s_cmp_eq_u32 vcc_lo, 3\n\t" \
  "s_cbranch_scc1 .Lsf_r32\n\t" \
  "s_cmp_eq_u32 vcc_lo, 4\n\t" \
  "s_cbranch_scc1 .Lsf_r42\n\t" \
  "s_cmp_eq_u32 vcc_lo, 5\n\t" \
  "s_cbranch_scc1 .Lsf_r52\n\t" \
  "s_cmp_eq_u32 vcc_lo, 6\n\t" \
  "s_cbranch_scc1 .Lsf_r62\n\t" \
  "s_cmp_eq_u32 vcc_lo, 7\n\t" \
  "s_cbranch_scc1 .Lsf_r72\n\t" \
  ".Lsf_c03:\n\t" \
  "s_mov_b32 vcc_lo, 0\n\t" \
  "s_branch .Lsf_s3\n\t" \
  ".Lsf_c13:\n\t" \
  "s_mov_b32 vcc_lo, 1\n\t" \
  "s_branch .Lsf_s3\n\t" \
  ".Lsf_c23:\n\t" \
  "s_mov_b32 vcc_lo, 2\n\t" \
  "s_branch .Lsf_s3\n\t" \
  ".Lsf_c33:\n\t" \
  "s_mov_b32 vcc_lo, 3\n\t" \
  "s_branch .Lsf_s3\n\t" \
  ".Lsf_c43:\n\t" \
  "s_mov_b32 vcc_lo, 4\n\t" \
  "s_branch .Lsf_s3\n\t" \
  ".Lsf_c53:\n\t" \
  "s_mov_b32 vcc_lo, 5\n\t" \
  "s_branch .Lsf_s3\n\t" \
  ".Lsf_c63:\n\t" \
  "s_mov_b32 vcc_lo, 6\n\t" \
  "s_branch .Lsf_s3\n\t" \
  ".Lsf_c73:\n\t" \
  "s_mov_b32 vcc_lo, 7\n\t" \
  "s_branch .Lsf_s3\n\t" \
  ".Lsf_s3:\n\t" \
  "s_nop 7\n\t" \
  "s_nop 1\n\t" \
  "v_mov_b32 v193, v192\n\t" \
  "s_nop 1\n\t" \
  "v_permlane32_swap_b32 v192, v193\n\t" \
  "s_nop 1\n\t" \
  "v_max_f32 v192, v192, v193\n\t" \
  "s_nop 1\n\t" \
  "v_mov_b32 v193, v192\n\t" \
  "s_nop 1\n\t" \
  "v_permlane16_swap_b32 v192, v193\n\t" \
  "s_nop 1\n\t" \
  "v_max_f32 v192, v192, v193\n\t" \
  "v_max_f32 v192, 0, v192\n\t" \
  "v_sub_f32 v96, v96, v192\n\t" \
  "v_sub_f32 v97, v97, v192\n\t" \
  "v_sub_f32 v98, v98, v192\n\t" \
  "v_sub_f32 v99, v99, v192\n\t" \
  "v_sub_f32 v100, v100, v192\n\t" \
  "v_sub_f32 v101, v101, v192\n\t" \
  "v_sub_f32 v102, v102, v192\n\t" \
  "v_sub_f32 v103, v103, v192\n\t" \
  "v_sub_f32 v104, v104, v192\n\t" \
  "v_sub_f32 v105, v105, v192\n\t" \
  "v_sub_f32 v106, v106, v192\n\t" \
  "v_sub_f32 v107, v107, v192\n\t" \
  "v_sub_f32 v108, v108, v192\n\t" \
  "v_sub_f32 v109, v109, v192\n\t" \
  "v_sub_f32 v110, v110, v192\n\t" \
  "v_sub_f32 v111, v111, v192\n\t" \
  "v_sub_f32 v188, v188, v192\n\t" \
  "v_sub_f32 v189, v189, v192\n\t" \
  "v_sub_f32 v190, v190, v192\n\t" \
  "v_sub_f32 v191, v191, v192\n\t" \
  "v_exp_f32 v199, -v192\n\t" \
  "s_mov_b32 s99, 1\n\t" \
  "s_nop 1\n\t" \
  "s_cmp_eq_u32 vcc_lo, 0\n\t" \
  "s_cbranch_scc1 .Lsf_r03\n\t" \
  "s_cmp_eq_u32 vcc_lo, 1\n\t" \
  "s_cbranch_scc1 .Lsf_r13\n\t" \
  "s_cmp_eq_u32 vcc_lo, 2\n\t" \
  "s_cbranch_scc1 .Lsf_r23\n\t" \
  "s_cmp_eq_u32 vcc_lo, 3\n\t" \
  "s_cbranch_scc1 .Lsf_r33\n\t" \
  "s_cmp_eq_u32 vcc_lo, 4\n\t" \
  "s_cbranch_scc1 .Lsf_r43\n\t" \
  "s_cmp_eq_u32 vcc_lo, 5\n\t" \
  "s_cbranch_scc1 .Lsf_r53\n\t" \
  "s_cmp_eq_u32 vcc_lo, 6\n\t" \
  "s_cbranch_scc1 .Lsf_r63\n\t" \
  "s_cmp_eq_u32 vcc_lo, 7\n\t" \
  "s_cbranch_scc1 .Lsf_r73\n\t" \
  ".Lsf_p0:\n\t" \
  "s_mov_b32 vcc_lo, 0\n\t" \
  "s_branch .Lsf_rs\n\t" \
  ".Lsf_p1:\n\t" \
  "s_mov_b32 vcc_lo, 1\n\t" \
  "s_branch .Lsf_rs\n\t" \
  ".Lsf_p2:\n\t" \
  "s_mov_b32 vcc_lo, 2\n\t" \
  "s_branch .Lsf_rs\n\t" \
  ".Lsf_p3:\n\t" \
  "s_mov_b32 vcc_lo, 3\n\t" \
  "s_branch .Lsf_rs\n\t" \
  ".Lsf_p4:\n\t" \
  "s_mov_b32 vcc_lo, 4\n\t" \
  "s_branch .Lsf_rs\n\t" \
  ".Lsf_p5:\n\t" \
  "s_mov_b32 vcc_lo, 5\n\t" \
  "s_branch .Lsf_rs\n\t" \
  ".Lsf_p6:\n\t" \
  "s_mov_b32 vcc_lo, 6\n\t" \
  "s_branch .Lsf_rs\n\t" \
  ".Lsf_p7:\n\t" \
  "s_mov_b32 vcc_lo, 7\n\t" \
  "s_branch .Lsf_rs\n\t" \
  ".Lsf_rs:\n\t" \
  "s_nop 7\n\t" \
  "s_nop 7\n\t" \
  "v_accvgpr_read_b32 v193, a0\n\t" \
  "v_mul_f32 v193, v196, v193\n\t" \
  "v_accvgpr_write_b32 a0, v193\n\t" \
  "v_accvgpr_read_b32 v193, a1\n\t" \
  "v_mul_f32 v193, v196, v193\n\t" \
  "v_accvgpr_write_b32 a1, v193\n\t" \
  "v_accvgpr_read_b32 v193, a2\n\t" \
  "v_mul_f32 v193, v196, v193\n\t" \
  "v_accvgpr_write_b32 a2, v193\n\t" \
  "v_accvgpr_read_b32 v193, a3\n\t" \
  "v_mul_f32 v193, v196, v193\n\t" \
  "v_accvgpr_write_b32 a3, v193\n\t" \
  "v_accvgpr_read_b32 v193, a4\n\t" \
  "v_mul_f32 v193, v196, v193\n\t" \
  "v_accvgpr_write_b32 a4, v193\n\t" \
  "v_accvgpr_read_b32 v193, a5\n\t" \
  "v_mul_f32 v193, v196, v193\n\t" \
  "v_accvgpr_write_b32 a5, v193\n\t" \
  "v_accvgpr_read_b32 v193, a6\n\t" \
  "v_mul_f32 v193, v196, v193\n\t" \
  "v_accvgpr_write_b32 a6, v193\n\t" \
  "v_accvgpr_read_b32 v193, a7\n\t" \
  "v_mul_f32 v193, v196, v193\n\t" \
  "v_accvgpr_write_b32 a7, v193\n\t" \
  "v_accvgpr_read_b32 v193, a8\n\t" \
  "v_mul_f32 v193, v196, v193\n\t" \
  "v_accvgpr_write_b32 a8, v193\n\t" \
  "v_accvgpr_read_b32 v193, a9\n\t" \
  "v_mul_f32 v193, v196, v193\n\t" \
  "v_accvgpr_write_b32 a9, v193\n\t" \
  "v_accvgpr_read_b32 v193, a10\n\t" \
  "v_mul_f32 v193, v196, v193\n\t" \
  "v_accvgpr_write_b32 a10, v193\n\t" \
  "v_accvgpr_read_b32 v193, a11\n\t" \
  "v_mul_f32 v193, v196, v193\n\t" \
  "v_accvgpr_write_b32 a11, v193\n\t" \
  "v_accvgpr_read_b32 v193, a12\n\t" \
  "v_mul_f32 v193, v196, v193\n\t" \
  "v_accvgpr_write_b32 a12, v193\n\t" \
  "v_accvgpr_read_b32 v193, a13\n\t" \
  "v_mul_f32 v193, v196, v193\n\t" \
  "v_accvgpr_write_b32 a13, v193\n\t" \
  "v_accvgpr_read_b32 v193, a14\n\t" \
  "v_mul_f32 v193, v196, v193\n\t" \
  "v_accvgpr_write_b32 a14, v193\n\t" \
  "v_accvgpr_read_b32 v193, a15\n\t" \
  "v_mul_f32 v193, v196, v193\n\t" \
  "v_accvgpr_write_b32 a15, v193\n\t" \
  "v_accvgpr_read_b32 v193, a16\n\t" \
  "v_mul_f32 v193, v196, v193\n\t" \
  "v_accvgpr_write_b32 a16, v193\n\t" \
  "v_accvgpr_read_b32 v193, a17\n\t" \
  "v_mul_f32 v193, v196, v193\n\t" \
  "v_accvgpr_write_b32 a17, v193\n\t" \
  "v_accvgpr_read_b32 v193, a18\n\t" \
  "v_mul_f32 v193, v196, v193\n\t" \
  "v_accvgpr_write_b32 a18, v193\n\t" \
  "v_accvgpr_read_b32 v193, a19\n\t" \
  "v_mul_f32 v193, v196, v193\n\t" \
  "v_accvgpr_write_b32 a19, v193\n\t" \
  "v_accvgpr_read_b32 v193, a20\n\t" \
  "v_mul_f32 v193, v196, v193\n\t" \
  "v_accvgpr_write_b32 a20, v193\n\t" \
  "v_accvgpr_read_b32 v193, a21\n\t" \
  "v_mul_f32 v193, v196, v193\n\t" \
  "v_accvgpr_write_b32 a21, v193\n\t" \
  "v_accvgpr_read_b32 v193, a22\n\t" \
  "v_mul_f32 v193, v196, v193\n\t" \
  "v_accvgpr_write_b32 a22, v193\n\t" \
  "v_accvgpr_read_b32 v193, a23\n\t" \
  "v_mul_f32 v193, v196, v193\n\t" \
  "v_accvgpr_write_b32 a23, v193\n\t" \
  "v_accvgpr_read_b32 v193, a24\n\t" \
  "v_mul_f32 v193, v196, v193\n\t" \
  "v_accvgpr_write_b32 a24, v193\n\t" \
  "v_accvgpr_read_b32 v193, a25\n\t" \
  "v_mul_f32 v193, v196, v193\n\t" \
  "v_accvgpr_write_b32 a25, v193\n\t" \
  "v_accvgpr_read_b32 v193, a26\n\t" \
  "v_mul_f32 v193, v196, v193\n\t" \
  "v_accvgpr_write_b32 a26, v193\n\t" \
  "v_accvgpr_read_b32 v193, a27\n\t" \
  "v_mul_f32 v193, v196, v193\n\t" \
  "v_accvgpr_write_b32 a27, v193\n\t" \
  "v_accvgpr_read_b32 v193, a28\n\t" \
  "v_mul_f32 v193, v196, v193\n\t" \
  "v_accvgpr_write_b32 a28, v193\n\t" \
  "v_accvgpr_read_b32 v193, a29\n\t" \
  "v_mul_f32 v193, v196, v193\n\t" \
  "v_accvgpr_write_b32 a29, v193\n\t" \
  "v_accvgpr_read_b32 v193, a30\n\t" \
  "v_mul_f32 v193, v196, v193\n\t" \
  "v_accvgpr_write_b32 a30, v193\n\t" \
  "v_accvgpr_read_b32 v193, a31\n\t" \
  "v_mul_f32 v193, v196, v193\n\t" \
  "v_accvgpr_write_b32 a31, v193\n\t" \
  "v_accvgpr_read_b32 v193, a32\n\t" \
  "v_mul_f32 v193, v197, v193\n\t" \
  "v_accvgpr_write_b32 a32, v193\n\t" \
  "v_accvgpr_read_b32 v193, a33\n\t" \
  "v_mul_f32 v193, v197, v193\n\t" \
  "v_accvgpr_write_b32 a33, v193\n\t" \
  "v_accvgpr_read_b32 v193, a34\n\t" \
  "v_mul_f32 v193, v197, v193\n\t" \
  "v_accvgpr_write_b32 a34, v193\n\t" \
  "v_accvgpr_read_b32 v193, a35\n\t" \
  "v_mul_f32 v193, v197, v193\n\t" \
  "v_accvgpr_write_b32 a35, v193\n\t" \
  "v_accvgpr_read_b32 v193, a36\n\t" \
  "v_mul_f32 v193, v197, v193\n\t" \
  "v_accvgpr_write_b32 a36, v193\n\t" \
  "v_accvgpr_read_b32 v193, a37\n\t" \
  "v_mul_f32 v193, v197, v193\n\t" \
  "v_accvgpr_write_b32 a37, v193\n\t" \
  "v_accvgpr_read_b32 v193, a38\n\t" \
  "v_mul_f32 v193, v197, v193\n\t" \
  "v_accvgpr_write_b32 a38, v193\n\t" \
  "v_accvgpr_read_b32 v193, a39\n\t" \
  "v_mul_f32 v193, v197, v193\n\t" \
  "v_accvgpr_write_b32 a39, v193\n\t" \
  "v_accvgpr_read_b32 v193, a40\n\t" \
  "v_mul_f32 v193, v197, v193\n\t" \
  "v_accvgpr_write_b32 a40, v193\n\t" \
  "v_accvgpr_read_b32 v193, a41\n\t" \
  "v_mul_f32 v193, v197, v193\n\t" \
  "v_accvgpr_write_b32 a41, v193\n\t" \
  "v_accvgpr_read_b32 v193, a42\n\t" \
  "v_mul_f32 v193, v197, v193\n\t" \
  "v_accvgpr_write_b32 a42, v193\n\t" \
  "v_accvgpr_read_b32 v193, a43\n\t" \
  "v_mul_f32 v193, v197, v193\n\t" \
  "v_accvgpr_write_b32 a43, v193\n\t" \
  "v_accvgpr_read_b32 v193, a44\n\t" \
  "v_mul_f32 v193, v197, v193\n\t" \
  "v_accvgpr_write_b32 a44, v193\n\t" \
  "v_accvgpr_read_b32 v193, a45\n\t" \
  "v_mul_f32 v193, v197, v193\n\t" \
  "v_accvgpr_write_b32 a45, v193\n\t" \
  "v_accvgpr_read_b32 v193, a46\n\t" \
  "v_mul_f32 v193, v197, v193\n\t" \
  "v_accvgpr_write_b32 a46, v193\n\t" \
  "v_accvgpr_read_b32 v193, a47\n\t" \
  "v_mul_f32 v193, v197, v193\n\t" \
  "v_accvgpr_write_b32 a47, v193\n\t" \
  "v_accvgpr_read_b32 v193, a48\n\t" \
  "v_mul_f32 v193, v197, v193\n\t" \
  "v_accvgpr_write_b32 a48, v193\n\t" \
  "v_accvgpr_read_b32 v193, a49\n\t" \
  "v_mul_f32 v193, v197, v193\n\t" \
  "v_accvgpr_write_b32 a49, v193\n\t" \
  "v_accvgpr_read_b32 v193, a50\n\t" \
  "v_mul_f32 v193, v197, v193\n\t" \
  "v_accvgpr_write_b32 a50, v193\n\t" \
  "v_accvgpr_read_b32 v193, a51\n\t" \
  "v_mul_f32 v193, v197, v193\n\t" \
  "v_accvgpr_write_b32 a51, v193\n\t" \
  "v_accvgpr_read_b32 v193, a52\n\t" \
  "v_mul_f32 v193, v197, v193\n\t" \
  "v_accvgpr_write_b32 a52, v193\n\t" \
  "v_accvgpr_read_b32 v193, a53\n\t" \
  "v_mul_f32 v193, v197, v193\n\t" \
  "v_accvgpr_write_b32 a53, v193\n\t" \
  "v_accvgpr_read_b32 v193, a54\n\t" \
  "v_mul_f32 v193, v197, v193\n\t" \
  "v_accvgpr_write_b32 a54, v193\n\t" \
  "v_accvgpr_read_b32 v193, a55\n\t" \
  "v_mul_f32 v193, v197, v193\n\t" \
  "v_accvgpr_write_b32 a55, v193\n\t" \
  "v_accvgpr_read_b32 v193, a56\n\t" \
  "v_mul_f32 v193, v197, v193\n\t" \
  "v_accvgpr_write_b32 a56, v193\n\t" \
  "v_accvgpr_read_b32 v193, a57\n\t" \
  "v_mul_f32 v193, v197, v193\n\t" \
  "v_accvgpr_write_b32 a57, v193\n\t" \
  "v_accvgpr_read_b32 v193, a58\n\t" \
  "v_mul_f32 v193, v197, v193\n\t" \
  "v_accvgpr_write_b32 a58, v193\n\t" \
  "v_accvgpr_read_b32 v193, a59\n\t" \
  "v_mul_f32 v193, v197, v193\n\t" \
  "v_accvgpr_write_b32 a59, v193\n\t" \
  "v_accvgpr_read_b32 v193, a60\n\t" \
  "v_mul_f32 v193, v197, v193\n\t" \
  "v_accvgpr_write_b32 a60, v193\n\t" \
  "v_accvgpr_read_b32 v193, a61\n\t" \
  "v_mul_f32 v193, v197, v193\n\t" \
  "v_accvgpr_write_b32 a61, v193\n\t" \
  "v_accvgpr_read_b32 v193, a62\n\t" \
  "v_mul_f32 v193, v197, v193\n\t" \
  "v_accvgpr_write_b32 a62, v193\n\t" \
  "v_accvgpr_read_b32 v193, a63\n\t" \
  "v_mul_f32 v193, v197, v193\n\t" \
  "v_accvgpr_write_b32 a63, v193\n\t" \
  "v_accvgpr_read_b32 v193, a64\n\t" \
  "v_mul_f32 v193, v198, v193\n\t" \
  "v_accvgpr_write_b32 a64, v193\n\t" \
  "v_accvgpr_read_b32 v193, a65\n\t" \
  "v_mul_f32 v193, v198, v193\n\t" \
  "v_accvgpr_write_b32 a65, v193\n\t" \
  "v_accvgpr_read_b32 v193, a66\n\t" \
  "v_mul_f32 v193, v198, v193\n\t" \
  "v_accvgpr_write_b32 a66, v193\n\t" \
  "v_accvgpr_read_b32 v193, a67\n\t" \
  "v_mul_f32 v193, v198, v193\n\t" \
  "v_accvgpr_write_b32 a67, v193\n\t" \
  "v_accvgpr_read_b32 v193, a68\n\t" \
  "v_mul_f32 v193, v198, v193\n\t" \
  "v_accvgpr_write_b32 a68, v193\n\t" \
  "v_accvgpr_read_b32 v193, a69\n\t" \
  "v_mul_f32 v193, v198, v193\n\t" \
  "v_accvgpr_write_b32 a69, v193\n\t" \
  "v_accvgpr_read_b32 v193, a70\n\t" \
  "v_mul_f32 v193, v198, v193\n\t" \
  "v_accvgpr_write_b32 a70, v193\n\t" \
  "v_accvgpr_read_b32 v193, a71\n\t" \
  "v_mul_f32 v193, v198, v193\n\t" \
  "v_accvgpr_write_b32 a71, v193\n\t" \
  "v_accvgpr_read_b32 v193, a72\n\t" \
  "v_mul_f32 v193, v198, v193\n\t" \
  "v_accvgpr_write_b32 a72, v193\n\t" \
  "v_accvgpr_read_b32 v193, a73\n\t" \
  "v_mul_f32 v193, v198, v193\n\t" \
  "v_accvgpr_write_b32 a73, v193\n\t" \
  "v_accvgpr_read_b32 v193, a74\n\t" \
  "v_mul_f32 v193, v198, v193\n\t" \
  "v_accvgpr_write_b32 a74, v193\n\t" \
  "v_accvgpr_read_b32 v193, a75\n\t" \
  "v_mul_f32 v193, v198, v193\n\t" \
  "v_accvgpr_write_b32 a75, v193\n\t" \
  "v_accvgpr_read_b32 v193, a76\n\t" \
  "v_mul_f32 v193, v198, v193\n\t" \
  "v_accvgpr_write_b32 a76, v193\n\t" \
  "v_accvgpr_read_b32 v193, a77\n\t" \
  "v_mul_f32 v193, v198, v193\n\t" \
  "v_accvgpr_write_b32 a77, v193\n\t" \
  "v_accvgpr_read_b32 v193, a78\n\t" \
  "v_mul_f32 v193, v198, v193\n\t" \
  "v_accvgpr_write_b32 a78, v193\n\t" \
  "v_accvgpr_read_b32 v193, a79\n\t" \
  "v_mul_f32 v193, v198, v193\n\t" \
  "v_accvgpr_write_b32 a79, v193\n\t" \
  "v_accvgpr_read_b32 v193, a80\n\t" \
  "v_mul_f32 v193, v198, v193\n\t" \
  "v_accvgpr_write_b32 a80, v193\n\t" \
  "v_accvgpr_read_b32 v193, a81\n\t" \
  "v_mul_f32 v193, v198, v193\n\t" \
  "v_accvgpr_write_b32 a81, v193\n\t" \
  "v_accvgpr_read_b32 v193, a82\n\t" \
  "v_mul_f32 v193, v198, v193\n\t" \
  "v_accvgpr_write_b32 a82, v193\n\t" \
  "v_accvgpr_read_b32 v193, a83\n\t" \
  "v_mul_f32 v193, v198, v193\n\t" \
  "v_accvgpr_write_b32 a83, v193\n\t" \
  "v_accvgpr_read_b32 v193, a84\n\t" \
  "v_mul_f32 v193, v198, v193\n\t" \
  "v_accvgpr_write_b32 a84, v193\n\t" \
  "v_accvgpr_read_b32 v193, a85\n\t" \
  "v_mul_f32 v193, v198, v193\n\t" \
  "v_accvgpr_write_b32 a85, v193\n\t" \
  "v_accvgpr_read_b32 v193, a86\n\t" \
  "v_mul_f32 v193, v198, v193\n\t" \
  "v_accvgpr_write_b32 a86, v193\n\t" \
  "v_accvgpr_read_b32 v193, a87\n\t" \
  "v_mul_f32 v193, v198, v193\n\t" \
  "v_accvgpr_write_b32 a87, v193\n\t" \
  "v_accvgpr_read_b32 v193, a88\n\t" \
  "v_mul_f32 v193, v198, v193\n\t" \
  "v_accvgpr_write_b32 a88, v193\n\t" \
  "v_accvgpr_read_b32 v193, a89\n\t" \
  "v_mul_f32 v193, v198, v193\n\t" \
  "v_accvgpr_write_b32 a89, v193\n\t" \
  "v_accvgpr_read_b32 v193, a90\n\t" \
  "v_mul_f32 v193, v198, v193\n\t" \
  "v_accvgpr_write_b32 a90, v193\n\t" \
  "v_accvgpr_read_b32 v193, a91\n\t" \
  "v_mul_f32 v193, v198, v193\n\t" \
  "v_accvgpr_write_b32 a91, v193\n\t" \
  "v_accvgpr_read_b32 v193, a92\n\t" \
  "v_mul_f32 v193, v198, v193\n\t" \
  "v_accvgpr_write_b32 a92, v193\n\t" \
  "v_accvgpr_read_b32 v193, a93\n\t" \
  "v_mul_f32 v193, v198, v193\n\t" \
  "v_accvgpr_write_b32 a93, v193\n\t" \
  "v_accvgpr_read_b32 v193, a94\n\t" \
  "v_mul_f32 v193, v198, v193\n\t" \
  "v_accvgpr_write_b32 a94, v193\n\t" \
  "v_accvgpr_read_b32 v193, a95\n\t" \
  "v_mul_f32 v193, v198, v193\n\t" \
  "v_accvgpr_write_b32 a95, v193\n\t" \
  "v_accvgpr_read_b32 v193, a96\n\t" \
  "v_mul_f32 v193, v199, v193\n\t" \
  "v_accvgpr_write_b32 a96, v193\n\t" \
  "v_accvgpr_read_b32 v193, a97\n\t" \
  "v_mul_f32 v193, v199, v193\n\t" \
  "v_accvgpr_write_b32 a97, v193\n\t" \
  "v_accvgpr_read_b32 v193, a98\n\t" \
  "v_mul_f32 v193, v199, v193\n\t" \
  "v_accvgpr_write_b32 a98, v193\n\t" \
  "v_accvgpr_read_b32 v193, a99\n\t" \
  "v_mul_f32 v193, v199, v193\n\t" \
  "v_accvgpr_write_b32 a99, v193\n\t" \
  "v_accvgpr_read_b32 v193, a100\n\t" \
  "v_mul_f32 v193, v199, v193\n\t" \
  "v_accvgpr_write_b32 a100, v193\n\t" \
  "v_accvgpr_read_b32 v193, a101\n\t" \
  "v_mul_f32 v193, v199, v193\n\t" \
  "v_accvgpr_write_b32 a101, v193\n\t" \
  "v_accvgpr_read_b32 v193, a102\n\t" \
  "v_mul_f32 v193, v199, v193\n\t" \
  "v_accvgpr_write_b32 a102, v193\n\t" \
  "v_accvgpr_read_b32 v193, a103\n\t" \
  "v_mul_f32 v193, v199, v193\n\t" \
  "v_accvgpr_write_b32 a103, v193\n\t" \
  "v_accvgpr_read_b32 v193, a104\n\t" \
  "v_mul_f32 v193, v199, v193\n\t" \
  "v_accvgpr_write_b32 a104, v193\n\t" \
  "v_accvgpr_read_b32 v193, a105\n\t" \
  "v_mul_f32 v193, v199, v193\n\t" \
  "v_accvgpr_write_b32 a105, v193\n\t" \
  "v_accvgpr_read_b32 v193, a106\n\t" \
  "v_mul_f32 v193, v199, v193\n\t" \
  "v_accvgpr_write_b32 a106, v193\n\t" \
  "v_accvgpr_read_b32 v193, a107\n\t" \
  "v_mul_f32 v193, v199, v193\n\t" \
  "v_accvgpr_write_b32 a107, v193\n\t" \
  "v_accvgpr_read_b32 v193, a108\n\t" \
  "v_mul_f32 v193, v199, v193\n\t" \
  "v_accvgpr_write_b32 a108, v193\n\t" \
  "v_accvgpr_read_b32 v193, a109\n\t" \
  "v_mul_f32 v193, v199, v193\n\t" \
  "v_accvgpr_write_b32 a109, v193\n\t" \
  "v_accvgpr_read_b32 v193, a110\n\t" \
  "v_mul_f32 v193, v199, v193\n\t" \
  "v_accvgpr_write_b32 a110, v193\n\t" \
  "v_accvgpr_read_b32 v193, a111\n\t" \
  "v_mul_f32 v193, v199, v193\n\t" \
  "v_accvgpr_write_b32 a111, v193\n\t" \
  "v_accvgpr_read_b32 v193, a112\n\t" \
  "v_mul_f32 v193, v199, v193\n\t" \
  "v_accvgpr_write_b32 a112, v193\n\t" \
  "v_accvgpr_read_b32 v193, a113\n\t" \
  "v_mul_f32 v193, v199, v193\n\t" \
  "v_accvgpr_write_b32 a113, v193\n\t" \
  "v_accvgpr_read_b32 v193, a114\n\t" \
  "v_mul_f32 v193, v199, v193\n\t" \
  "v_accvgpr_write_b32 a114, v193\n\t" \
  "v_accvgpr_read_b32 v193, a115\n\t" \
  "v_mul_f32 v193, v199, v193\n\t" \
  "v_accvgpr_write_b32 a115, v193\n\t" \
  "v_accvgpr_read_b32 v193, a116\n\t" \
  "v_mul_f32 v193, v199, v193\n\t" \
  "v_accvgpr_write_b32 a116, v193\n\t" \
  "v_accvgpr_read_b32 v193, a117\n\t" \
  "v_mul_f32 v193, v199, v193\n\t" \
  "v_accvgpr_write_b32 a117, v193\n\t" \
  "v_accvgpr_read_b32 v193, a118\n\t" \
  "v_mul_f32 v193, v199, v193\n\t" \
  "v_accvgpr_write_b32 a118, v193\n\t" \
  "v_accvgpr_read_b32 v193, a119\n\t" \
  "v_mul_f32 v193, v199, v193\n\t" \
  "v_accvgpr_write_b32 a119, v193\n\t" \
  "v_accvgpr_read_b32 v193, a120\n\t" \
  "v_mul_f32 v193, v199, v193\n\t" \
  "v_accvgpr_write_b32 a120, v193\n\t" \
  "v_accvgpr_read_b32 v193, a121\n\t" \
  "v_mul_f32 v193, v199, v193\n\t" \
  "v_accvgpr_write_b32 a121, v193\n\t" \
  "v_accvgpr_read_b32 v193, a122\n\t" \
  "v_mul_f32 v193, v199, v193\n\t" \
  "v_accvgpr_write_b32 a122, v193\n\t" \
  "v_accvgpr_read_b32 v193, a123\n\t" \
  "v_mul_f32 v193, v199, v193\n\t" \
  "v_accvgpr_write_b32 a123, v193\n\t" \
  "v_accvgpr_read_b32 v193, a124\n\t" \
  "v_mul_f32 v193, v199, v193\n\t" \
  "v_accvgpr_write_b32 a124, v193\n\t" \
  "v_accvgpr_read_b32 v193, a125\n\t" \
  "v_mul_f32 v193, v199, v193\n\t" \
  "v_accvgpr_write_b32 a125, v193\n\t" \
  "v_accvgpr_read_b32 v193, a126\n\t" \
  "v_mul_f32 v193, v199, v193\n\t" \
  "v_accvgpr_write_b32 a126, v193\n\t" \
  "v_accvgpr_read_b32 v193, a127\n\t" \
  "v_mul_f32 v193, v199, v193\n\t" \
  "v_accvgpr_write_b32 a127, v193\n\t" \
  "v_mul_f32 v160, v196, v160\n\t" \
  "v_mul_f32 v161, v196, v161\n\t" \
  "v_mul_f32 v162, v196, v162\n\t" \
  "v_mul_f32 v163, v196, v163\n\t" \
  "v_mov_b32 v196, 1.0\n\t" \
  "v_mul_f32 v164, v197, v164\n\t" \
  "v_mul_f32 v165, v197, v165\n\t" \
  "v_mul_f32 v166, v197, v166\n\t" \
  "v_mul_f32 v167, v197, v167\n\t" \
  "v_mov_b32 v197, 1.0\n\t" \
  "v_mul_f32 v168, v198, v168\n\t" \
  "v_mul_f32 v169, v198, v169\n\t" \
  "v_mul_f32 v170, v198, v170\n\t" \
  "v_mul_f32 v171, v198, v171\n\t" \
  "v_mov_b32 v198, 1.0\n\t" \
  "v_mul_f32 v172, v199, v172\n\t" \
  "v_mul_f32 v173, v199, v173\n\t" \
  "v_mul_f32 v174, v199, v174\n\t" \
  "v_mul_f32 v175, v199, v175\n\t" \
  "v_mov_b32 v199, 1.0\n\t" \
  "s_mov_b32 s99, 0\n\t" \
  "s_nop 1\n\t" \
  "s_cmp_eq_u32 vcc_lo, 0\n\t" \
  "s_cbranch_scc1 .Lsf_q0\n\t" \
  "s_cmp_eq_u32 vcc_lo, 1\n\t" \
  "s_cbranch_scc1 .Lsf_q1\n\t" \
  "s_cmp_eq_u32 vcc_lo, 2\n\t" \
  "s_cbranch_scc1 .Lsf_q2\n\t" \
  "s_cmp_eq_u32 vcc_lo, 3\n\t" \
  "s_cbranch_scc1 .Lsf_q3\n\t" \
  "s_cmp_eq_u32 vcc_lo, 4\n\t" \
  "s_cbranch_scc1 .Lsf_q4\n\t" \
  "s_cmp_eq_u32 vcc_lo, 5\n\t" \
  "s_cbranch_scc1 .Lsf_q5\n\t" \
  "s_cmp_eq_u32 vcc_lo, 6\n\t" \
  "s_cbranch_scc1 .Lsf_q6\n\t" \
  "s_cmp_eq_u32 vcc_lo, 7\n\t" \
  "s_cbranch_scc1 .Lsf_q7\n\t" \
  "s_endpgm\n\t" \
  ".Lsf_v:\n\t" \
  "s_mov_b32 s98, 0\n\t" \
  ".Lsf_v1:\n\t" \
  "s_sleep 4\n\t" \
  "buffer_load_dword v209, v250, s[16:19], 0 offen sc1\n\t" \
  "s_waitcnt vmcnt(0)\n\t" \
  "v_and_b32 v251, 0xbfffbfff, v209\n\t" \
  "v_cmp_ne_u32_e64 s[84:85], s36, v251\n\t" \
  "s_cmp_lg_u64 s[84:85], 0\n\t" \
  "s_cbranch_scc0 .Lsf_w\n\t" \
  "s_add_u32 s98, s98, 1\n\t" \
  "s_cmp_lt_u32 s98, " FQ_CAP "\n\t" \
  "s_cbranch_scc1 .Lsf_v1\n\t" \
  "v_mov_b32 v251, s36\n\t" \
  "buffer_store_dword v251, off, s[16:19], 0 offset:256 sc1\n\t" \
  "s_branch .Lsf_w\n\t" \
  ".subsection 0\n\t" \
  ".Lop_end:\n\t" \
  ""
#define OP_GA0 "=&{v[160:163]}"
#define OP_GA1 "=&{v[164:167]}"
#define OP_CW "=&{s82}"
#define OP_BB "=&{s83}"
#define AO_CLOB "v128", "v129", "v130", "v131", "v132", "v133", "v134", "v135", "v136", "v137", "v138", "v139", "v140", "v141", "v142", "v143", "v144", "v145", "v146", "v147", "v148", "v149", "v150", "v151", "v152", "v153", "v154", "v155", "v156", "v157", "v158", "v159", "v168", "v169", "v170", "v171", "v172", "v173", "v174", "v175", "v208", "v209", "v210", "v211", "v212", "v213", "v214", "v215", "v216", "v217", "v218", "v219", "v234", "v235", "v236", "v237", "v238", "v239", "v242", "v243", "v248", "v249", "v250", "v251", "v252", "v254", "s76", "s77", "s78", "s79", "s80", "s81", "s84", "s85", "s86", "s87", "s88", "s89", "s90", "s91", "s92", "s93", "s94", "s95", "s96", "s97", "s98", "s99", "vcc", "scc", "memory"
#define OP_DRD "s_waitcnt vmcnt(5)\n\t" "ds_read2st64_b64 v[32:35], %1 offset0:64 offset1:65\n\t" "ds_read2st64_b64 v[36:39], %1 offset0:66 offset1:67\n\t" "ds_read2st64_b64 v[40:43], %1 offset0:68 offset1:69\n\t" "ds_read2st64_b64 v[44:47], %1 offset0:70 offset1:71\n\t" "ds_read2st64_b64 v[48:51], %1 offset0:96 offset1:97\n\t" "ds_read2st64_b64 v[52:55], %1 offset0:98 offset1:99\n\t" "ds_read2st64_b64 v[56:59], %1 offset0:100 offset1:101\n\t" "ds_read2st64_b64 v[60:63], %1 offset0:102 offset1:103\n\t" "s_waitcnt lgkmcnt(0)"
#define ST_ASM "s_nop 1\n\t" "v_mfma_f32_16x16x32_bf16 v[80:83], a[176:179], v[160:163], 0\n\t" "v_mfma_f32_16x16x32_bf16 v[80:83], a[180:183], v[164:167], v[80:83]\n\t" "v_mfma_f32_16x16x32_bf16 v[80:83], a[184:187], v[168:171], v[80:83]\n\t" "v_mfma_f32_16x16x32_bf16 v[80:83], a[188:191], v[172:175], v[80:83]\n\t" "v_dot2_f32_bf16 v64, v160, v160, 0\n\t" "v_dot2_f32_bf16 v65, v164, v164, 0\n\t" "v_dot2_f32_bf16 v66, v168, v168, 0\n\t" "v_dot2_f32_bf16 v67, v172, v172, 0\n\t" "v_dot2_f32_bf16 v64, v161, v161, v64\n\t" "v_dot2_f32_bf16 v65, v165, v165, v65\n\t" "v_dot2_f32_bf16 v66, v169, v169, v66\n\t" "v_dot2_f32_bf16 v67, v173, v173, v67\n\t" "v_dot2_f32_bf16 v64, v162, v162, v64\n\t" "v_dot2_f32_bf16 v65, v166, v166, v65\n\t" "v_dot2_f32_bf16 v66, v170, v170, v66\n\t" "v_dot2_f32_bf16 v67, v174, v174, v67\n\t" "v_dot2_f32_bf16 v64, v163, v163, v64\n\t" "v_dot2_f32_bf16 v65, v167, v167, v65\n\t" "v_dot2_f32_bf16 v66, v171, v171, v66\n\t" "v_dot2_f32_bf16 v67, v175, v175, v67\n\t" "s_nop 2\n\t"
static_assert(W_MODB == 2228224 && W_VOTE == 8192);
#define AO_T0
static_assert(W_EPOCH == 2490368 && W_GKEY == 65536 && W_CANDH == 262144 && W_STAT == 524288 && W_YG == 1048576 && W_CANDX == 1966080);
#define MAIN_CLOB "v1", "v2", "v3", "v4", "v5", "v6", "v7", "v8", "v9", "v10", "v11", "v12", "v13", "v14", "v15", "v16", "v17", "v18", "v19", "v20", "v21", "v22", "v23", "v24", "v25", "v26", "v27", "v28", "v29", "v30", "v31", "v32", "v33", "v34", "v35", "v36", "v37", "v38", "v39", "v40", "v41", "v42", "v43", "v44", "v45", "v46", "v47", "v48", "v49", "v50", "v51", "v52", "v53", "v54", "v55", "v56", "v57", "v58", "v59", "v60", "v61", "v62", "v63", "v64", "v65", "v66", "v67", "v68", "v69", "v70", "v71", "v72", "v73", "v74", "v75", "v76", "v77", "v78", "v79", "v80", "v81", "v82", "v83", "v84", "v85", "v86", "v87", "v88", "v89", "v90", "v91", "v92", "v93", "v94", "v95", "v96", "v97", "v98", "v99", "v100", "v101", "v102", "v103", "v104", "v105", "v106", "v107", "v108", "v109", "v110", "v111", "v112", "v113", "v114", "v115", "v116", "v117", "v118", "v119", "v120", "v121", "v122", "v123", "v124", "v125", "v126", "v127", "v128", "v129", "v130", "v131", "v132", "v133", "v134", "v135", "v136", "v137", "v138", "v139", "v140", "v141", "v142", "v143", "v144", "v145", "v146", "v147", "v148", "v149", "v150", "v151", "v152", "v153", "v154", "v155", "v156", "v157", "v158", "v159", "v160", "v161", "v162", "v163", "v164", "v165", "v166", "v167", "v168", "v169", "v170", "v171", "v172", "v173", "v174", "v175", "v176", "v177", "v178", "v179", "v180", "v181", "v182", "v183", "v184", "v185", "v186", "v187", "v188", "v189", "v190", "v191", "v192", "v193", "v194", "v195", "v196", "v197", "v198", "v199", "v200", "v201", "v202", "v203", "v204", "v205", "v206", "v207", "v208", "v209", "v210", "v211", "v212", "v213", "v214", "v215", "v216", "v217", "v218", "v219", "v220", "v221", "v222", "v223", "v224", "v225", "v226", "v227", "v228", "v229", "v230", "v231", "v232", "v233", "v234", "v235", "v236", "v237", "v238", "v239", "v240", "v241", "v242", "v243", "v244", "v245", "v246", "v247", "v248", "v249", "v250", "v251", "v252", "v253", "v254", "s4", "s5", "s6", "s7", "s8", "s9", "s10", "s11", "s12", "s13", "s14", "s15", "s16", "s17", "s18", "s19", "s20", "s21", "s22", "s23", "s24", "s25", "s26", "s27", "s28", "s29", "s30", "s31", "s32", "s33", "s34", "s35", "s36", "s37", "s38", "s39", "s40", "s41", "s42", "s43", "s44", "s45", "s46", "s47", "s48", "s49", "s50", "s51", "s52", "s53", "s54", "s55", "s57", "s58", "s59", "s60", "s61", "s62", "s63", "s64", "s65", "s66", "s67", "s68", "s69", "s70", "s71", "s72", "s73", "s74", "s75", "s76", "s77", "s78", "s79", "s80", "s81", "s82", "s83", "s84", "s85", "s86", "s87", "s88", "s89", "s90", "s91", "s92", "s93", "s94", "s95", "s96", "s97", "s98", "s99", "vcc", "scc", "memory"
#define MAIN_STV
__global__ __launch_bounds__(256, 1) __attribute__((aligned(4096))) void k_fused(Args a) {
  Ctx k;
  STAMP_DECL;
  argp_t ap = (argp_t)__builtin_amdgcn_kernarg_segment_ptr();
  unsigned tid = threadIdx.x; k.bid = blockIdx.x;
  asm volatile(FQ_ASM AT_ASM : MAIN_STV "+{v0}"(tid), "+{s[0:1]}"(ap), "+{s2}"(k.bid), "=&{s3}"(k.wid), "=&{s56}"(k.t0) :: MAIN_CLOB, ACLOB_LO, ACLOB_HI);
  asm volatile(HOP_DEFS);
}

extern "C" void kernel_launch(void* const* d_in, const int* in_sizes, int n_in, void* d_out, int out_size, void* d_ws, size_t ws_size,
                              hipStream_t stream) {
  auto D = [](const void* p, unsigned bytes) { Desc d; d.p = p; d.bytes = bytes; d.w3 = SRD3; return d; };
  const size_t win = (size_t)(SKV - WIN) * DH * 2;
  Args a;
  a.x = D(d_in[0], S * DM * 2); a.w_mod = D(d_in[11], DM * 6 * DM * 2); a.c = D(d_in[10], DM * 2); a.ws = D(d_ws, W_END);
  a.wq = D(d_in[3], DM * QD * 2); a.wo = D(d_in[4], QD * DM * 2); a.wd = D(d_in[9], 0); a.w_r = D(d_in[6], DM * 2);
  a.kc = D((const char*)d_in[1] + win, WIN * DH * 2); a.vc = D((const char*)d_in[2] + win, WIN * DH * 2); a.wg = D(d_in[7], DM * FF * 2); a.wu = D(d_in[8], DM * FF * 2);
  a.out = D(d_out, S * DM * 2);
  (void)hipFuncSetAttribute((const void*)k_fused, hipFuncAttributeMaxDynamicSharedMemorySize, 163840);
  k_fused<<<256, 256, 163840, stream>>>(a);
}
```

```cpp
#include <hip/hip_runtime.h>
#include <hip/hip_bf16.h>
#include <stdint.h>
#include <utility>

typedef unsigned short u16;
using bf16x8 = __attribute__((ext_vector_type(8))) short;
using s16x4  = __attribute__((ext_vector_type(4))) short;
using f32x16 = __attribute__((ext_vector_type(16))) float;
using u32x16 = __attribute__((ext_vector_type(16))) unsigned;
using f32x4  = __attribute__((ext_vector_type(4))) float;
using f32x2  = __attribute__((ext_vector_type(2))) float;
using u32x4  = __attribute__((ext_vector_type(4))) unsigned;
using u32x2  = __attribute__((ext_vector_type(2))) unsigned;
using u32x8 = __attribute__((ext_vector_type(8))) unsigned;
using f32x8 = __attribute__((ext_vector_type(8))) float;
using f32x32 = __attribute__((ext_vector_type(32))) float;
using u32x32 = __attribute__((ext_vector_type(32))) unsigned;
typedef __bf16 bf16x2_t __attribute__((ext_vector_type(2)));
#define DI __device__ __forceinline__
#define NTMASK 0
#define NT_SEL(bit) ((NTMASK >> (bit)) & 1)
#if NT_SEL(0)
#define NTX " nt"
#else
#define NTX ""
#endif
#if NT_SEL(1)
#define NTM " nt"
#else
#define NTM ""
#endif
#if NT_SEL(2)
#define NTQ " nt"
#else
#define NTQ ""
#endif
#if NT_SEL(3)
#define NTO " nt"
#else
#define NTO ""
#endif
#if NT_SEL(4)
#define NTG " nt"
#else
#define NTG ""
#endif
#if NT_SEL(5)
#define NTD " nt"
#else
#define NTD ""
#endif
#if NT_SEL(6)
#define NTT " nt"
#else
#define NTT ""
#endif
#if NT_SEL(7)
#define NTF " nt"
#else
#define NTF ""
#endif

constexpr int S = 2048, DM = 1024, HQ = 32, HKV = 4, DH = 128, SKV = 2048, WIN = 512, FF = 3584;
constexpr int QD = HQ * DH;
constexpr float QSCALE = 0.12751743082f;

DI float bf2f(u16 v) { return __uint_as_float(((unsigned)v) << 16); }
DI u16 f2bf(float x) { unsigned u = __float_as_uint(x); u += 0x7fffu + ((u >> 16) & 1u); return (u16)(u >> 16); }
DI unsigned cvtpk(float lo, float hi) { f32x2 v = {lo, hi}; bf16x2_t b = __builtin_convertvector(v, bf16x2_t); return __builtin_bit_cast(unsigned, b); }
DI float lo16f(unsigned w) { return __uint_as_float(w << 16); }
DI float hi16f(unsigned w) { return __uint_as_float(w & 0xffff0000u); }
DI float rbf(float x) { return lo16f(cvtpk(x, x)); }
DI f32x2 up2(unsigned w) { return f32x2{lo16f(w), hi16f(w)}; }
DI float dot2(unsigned a, unsigned b, float c) { return __builtin_amdgcn_fdot2_f32_bf16(__builtin_bit_cast(bf16x2_t, a), __builtin_bit_cast(bf16x2_t, b), c, false); }
DI unsigned plo(unsigned a, unsigned b) { return __builtin_amdgcn_perm(b, a, 0x05040100u); }
DI unsigned phi(unsigned a, unsigned b) { return __builtin_amdgcn_perm(b, a, 0x07060302u); }
DI unsigned rfl(unsigned x) { return __builtin_amdgcn_readfirstlane(x); }
template <int CTRL> DI float dpp_f(float v) { return __int_as_float(__builtin_amdgcn_update_dpp(0, __float_as_int(v), CTRL, 0xf, 0xf, true)); }
template <int CTRL> DI unsigned dpp_u(unsigned v) { return (unsigned)__builtin_amdgcn_update_dpp(0, (int)v, CTRL, 0xf, 0xf, true); }
DI float fold16(float a, float b) { auto r = __builtin_amdgcn_permlane16_swap(__float_as_uint(a), __float_as_uint(b), false, false); return __uint_as_float(r[0]) + __uint_as_float(r[1]); }
DI float fold32(float a, float b) { auto r = __builtin_amdgcn_permlane32_swap(__float_as_uint(a), __float_as_uint(b), false, false); return __uint_as_float(r[0]) + __uint_as_float(r[1]); }
DI float sum16(float v) { return fold16(v, v); }
DI float sum32(float v) { return fold32(v, v); }
DI float sum_row16(float v) { v += dpp_f<0xB1>(v); v += dpp_f<0x4E>(v); v += dpp_f<0x141>(v); v += dpp_f<0x140>(v); return v; }
DI float wave_sum(float v) { return sum32(sum16(sum_row16(v))); }
DI unsigned wave_maxu(unsigned v) {
  v = max(v, dpp_u<0xB1>(v)); v = max(v, dpp_u<0x4E>(v)); v = max(v, dpp_u<0x141>(v)); v = max(v, dpp_u<0x140>(v));
  { auto r = __builtin_amdgcn_permlane16_swap(v, v, false, false); v = max(r[0], r[1]); }
  { auto r = __builtin_amdgcn_permlane32_swap(v, v, false, false); v = max(r[0], r[1]); }
  return v;
}
#define GAP() __builtin_amdgcn_sched_barrier(0)
template <int N> using iseq = std::make_integer_sequence<int, N>;
template <int... Is> using ilist = std::integer_sequence<int, Is...>;
typedef __amdgpu_buffer_rsrc_t srd_t;
#define SRD3 0x00020000
DI srd_t mk_srd(const void* p, unsigned bytes) { return __builtin_amdgcn_make_buffer_rsrc((void*)p, (short)0, (int)bytes, SRD3); }
DI unsigned lane_id() { unsigned l; asm volatile("v_mbcnt_lo_u32_b32 %0, -1, 0\n\tv_mbcnt_hi_u32_b32 %0, -1, %0" : "=v"(l)); return l; }
struct Desc { const void* p; unsigned bytes, w3; };
struct Args { Desc x, w_mod, c, ws, wq, wo, wd, w_r, kc, vc, wg, wu, out; };
typedef const __attribute__((address_space(4))) Args* argp_t;
DI argp_t args() { argp_t p = (argp_t)__builtin_amdgcn_kernarg_segment_ptr(); asm volatile("" : "+s"(p)); return p; }
#define BARRIER_LGKM() do { asm volatile("s_waitcnt lgkmcnt(0)\n\ts_barrier" ::: "memory"); } while (0)

constexpr unsigned W_EPOCH = 0x260000, W_TMO = 256;
constexpr unsigned W_FLAGH = 0x261000;
constexpr unsigned W_VOTE = 0x2000, W_PROBE = 0x4000;
constexpr unsigned W_GKEY = 0x10000;
constexpr unsigned W_FLAGR = 0x8000;
constexpr unsigned W_FLAGO = 0x20000;
constexpr unsigned W_CANDH = 0x40000;
constexpr unsigned W_STAT = 0x80000;
constexpr unsigned W_YG = 0x100000;
constexpr unsigned W_CANDX = 0x1E0000;
constexpr unsigned W_ST1 = 0x200000;
constexpr unsigned W_MODA = 0x210000;
constexpr unsigned W_MODB = 0x220000;
constexpr unsigned W_SIL = 0x240000;
constexpr unsigned W_HT = 4u << 20;
constexpr unsigned W_OT = 8u << 20;
constexpr unsigned W_XCH = 24u << 20;
constexpr unsigned W_END = 40u << 20;
constexpr unsigned TAGM = 0x40004000u, TAGC = 0x9E3739B9u;
#define SPINCAP 262144
constexpr unsigned kSpinCap = SPINCAP;
#define SL0 1
#define SL1 1
#define SL2 1
#define SL3 1
template <int SL = 1> DI bool spin_fail(unsigned& spins, srd_t ws, unsigned tag) {
  __builtin_amdgcn_s_sleep(SL);
  if (++spins > kSpinCap) { __builtin_amdgcn_raw_buffer_store_b32(tag, ws, W_TMO, 0, 16); return true; }
  return (spins & 255u) == 0u && rfl(__builtin_amdgcn_raw_buffer_load_b32(ws, W_TMO, 0, 16)) == tag;
}
DI u32x4 ld16(srd_t ws, unsigned off) { return __builtin_amdgcn_raw_buffer_load_b128(ws, off, 0, 16); }
DI u32x2 ld8(srd_t ws, unsigned off) { return __builtin_amdgcn_raw_buffer_load_b64(ws, off, 0, 16); }
DI void st16(srd_t ws, unsigned off, u32x4 v) { __builtin_amdgcn_raw_buffer_store_b128(v, ws, off, 0, 16); }
DI void st8(srd_t ws, unsigned off, u32x2 v) { __builtin_amdgcn_raw_buffer_store_b64(v, ws, off, 0, 16); }
DI void st4(srd_t ws, unsigned off, unsigned v) { __builtin_amdgcn_raw_buffer_store_b32(v, ws, off, 0, 16); }
DI void wait_flags(srd_t ws, unsigned off, unsigned tag) {
  const unsigned o = off + (lane_id() & 31u) * 4u;
  for (unsigned spins = 0;;) {
    const unsigned f = __builtin_amdgcn_raw_buffer_load_b32(ws, o, 0, 16);
    if (__all(f == tag)) break;
    if (spin_fail(spins, ws, tag)) break;
  }
  asm volatile("" ::: "memory");
}
template <int OFF, bool DEV = false> DI u32x4 ald16(srd_t s, unsigned vo, unsigned so) {
  u32x4 r;
  if constexpr (DEV) asm volatile("buffer_load_dwordx4 %0, %1, %2, %3 offen offset:%4 sc1" : "=v"(r) : "v"(vo), "s"(s), "s"(so), "i"(OFF) : "memory");
  else               asm volatile("buffer_load_dwordx4 %0, %1, %2, %3 offen offset:%4" NTF : "=v"(r) : "v"(vo), "s"(s), "s"(so), "i"(OFF) : "memory");
  return r;
}
template <int OFF> DI u32x2 ald8(srd_t s, unsigned vo, unsigned so) {
  u32x2 r; asm volatile("buffer_load_dwordx2 %0, %1, %2, %3 offen offset:%4" : "=v"(r) : "v"(vo), "s"(s), "s"(so), "i"(OFF) : "memory"); return r;
}
DI u32x2 ald8d(srd_t s, unsigned vo) { u32x2 r; asm volatile("buffer_load_dwordx2 %0, %1, %2, 0 offen sc1" : "=v"(r) : "v"(vo), "s"(s) : "memory"); return r; }
template <int OFF, bool DEV = false> DI unsigned ald4(srd_t s, unsigned vo, unsigned so) {
  unsigned r;
  if constexpr (DEV) asm volatile("buffer_load_dword %0, %1, %2, %3 offen offset:%4 sc1" : "=v"(r) : "v"(vo), "s"(s), "s"(so), "i"(OFF) : "memory");
  else               asm volatile("buffer_load_dword %0, %1, %2, %3 offen offset:%4" : "=v"(r) : "v"(vo), "s"(s), "s"(so), "i"(OFF) : "memory");
  return r;
}
DI void touch(unsigned& d, const void* base, unsigned vo) { asm volatile("global_load_dword %0, %1, %2" : "+v"(d) : "v"(vo), "s"(base) : "memory"); }
DI void touchq(unsigned& d, const void* base, unsigned vo) { asm volatile("global_load_dword %0, %1, %2" NTQ : "+v"(d) : "v"(vo), "s"(base) : "memory"); }
template <class T> DI T lds_ld(unsigned a) { return *reinterpret_cast<const __attribute__((address_space(3))) T*>(a); }
template <class T> DI void lds_st(unsigned a, T v) { *reinterpret_cast<__attribute__((address_space(3))) T*>(a) = v; }
DI float silu_bf(float v) { return rbf(v * __builtin_amdgcn_rcpf(1.f + __expf(-v))); }
struct Ctx { static constexpr unsigned ldsb = 0; unsigned t0, pm, bid, wid, xcd, j, kvh, hl, rblk, tag, cs, nb; srd_t ws; };
#define TAIL2 1
DI unsigned ffn_j(const Ctx& k) { return TAIL2 ? (k.j < 24 ? k.j : 24 + (k.j & 3)) : min(k.j, 27u); }
DI bool ffn_idle(const Ctx& k) { return TAIL2 ? (k.j >> 2) == 6 + (k.xcd & 1) : k.j >= 28; }
constexpr unsigned UNIT = 16384;
#define STAMPA(I) do { } while (0)
#define STAMPO(I) do { } while (0)
#define STAMP_DECL
#define STAMP(I) do { } while (0)
#define STAMP_FLUSH() do { } while (0)
#define STAMP_ARG
#define STAMP_PASS
#define ACLOB_LO "a0","a1","a2","a3","a4","a5","a6","a7","a8","a9","a10","a11","a12","a13","a14","a15","a16","a17","a18","a19","a20","a21","a22","a23","a24","a25","a26","a27","a28","a29","a30","a31","a32","a33","a34","a35","a36","a37","a38","a39","a40","a41","a42","a43","a44","a45","a46","a47","a48","a49","a50","a51","a52","a53","a54","a55","a56","a57","a58","a59","a60","a61","a62","a63","a64","a65","a66","a67","a68","a69","a70","a71","a72","a73","a74","a75","a76","a77","a78","a79","a80","a81","a82","a83","a84","a85","a86","a87","a88","a89","a90","a91","a92","a93","a94","a95","a96","a97","a98","a99","a100","a101","a102","a103","a104","a105","a106","a107","a108","a109","a110","a111","a112","a113","a114","a115","a116","a117","a118","a119","a120","a121","a122","a123","a124","a125","a126","a127"
#define ACLOB_HI "a128","a129","a130","a131","a132","a133","a134","a135","a136","a137","a138","a139","a140","a141","a142","a143","a144","a145","a146","a147","a148","a149","a150","a151","a152","a153","a154","a155","a156","a157","a158","a159","a160","a161","a162","a163","a164","a165","a166","a167","a168","a169","a170","a171","a172","a173","a174","a175","a176","a177","a178","a179","a180","a181","a182","a183","a184","a185","a186","a187","a188","a189","a190","a191","a192","a193","a194","a195","a196","a197","a198","a199","a200","a201","a202","a203","a204","a205","a206","a207","a208","a209","a210","a211","a212","a213","a214","a215","a216","a217","a218","a219","a220","a221","a222","a223","a224","a225","a226","a227","a228","a229","a230","a231","a232","a233","a234","a235","a236","a237","a238","a239","a240","a241","a242","a243","a244","a245","a246","a247","a248","a249","a250","a251","a252","a253","a254","a255"
#define HOP_DEFS ".set .Lhc00, .Lhs0\n\t.set .Lhs0n, .Lhs4\n\t.set .Lhs4n, .Lhs8\n\t.set .Lhs8n, .Lhr0\n\t.set .Lhc01, .Lhs1\n\t.set .Lhs1n, .Lhs5\n\t.set .Lhs5n, .Lhs9\n\t.set .Lhs9n, .Lhr0\n\t.set .Lhc02, .Lhs2\n\t.set .Lhs2n, .Lhs6\n\t.set .Lhs6n, .Lhs10\n\t.set .Lhs10n, .Lhr0\n\t.set .Lhc03, .Lhs3\n\t.set .Lhs3n, .Lhs7\n\t.set .Lhs7n, .Lhr0\n\t.set .Lhc10, .Lhs11\n\t.set .Lhs11n, .Lhs15\n\t.set .Lhs15n, .Lhr1\n\t.set .Lhc11, .Lhs12\n\t.set .Lhs12n, .Lhs16\n\t.set .Lhs16n, .Lhr1\n\t.set .Lhc12, .Lhs13\n\t.set .Lhs13n, .Lhr1\n\t.set .Lhc13, .Lhs14\n\t.set .Lhs14n, .Lhr1"
#define HOP_PAGE(n) asm volatile("s_branch .Lht" #n "\n\t.p2align 12\n.Lht" #n ":\n\ts_cbranch_execz .Lht" #n "n")
#define PAGE_ALIGN() asm volatile("s_branch .Lpa%=\n\t.p2align 12\n.Lpa%=:" ::: "memory")
#define HOP_FRONT() asm volatile("s_branch .Lhf\n\t.p2align 12\n.Lhf:\n\ts_cbranch_execz .Lhfn")
#define HOP_GO(g, w) asm volatile("s_mov_b64 exec, 0\n\ts_cmp_eq_u32 %0, 0\n\ts_cbranch_scc1 .Lhc" #g "0\n\ts_cmp_eq_u32 %0, 1\n\ts_cbranch_scc1 .Lhc" #g "1\n\ts_cmp_eq_u32 %0, 2\n\ts_cbranch_scc1 .Lhc" #g "2\n\ts_branch .Lhc" #g "3\n.Lhr" #g ":\n\ts_mov_b64 exec, -1" :: "s"(w) : "scc", "memory")
#define FQ_STR2(x) #x
#define FQ_STR(x) FQ_STR2(x)
#define FQ_CAP FQ_STR(SPINCAP)
DI bool tags2(const u32x4& a, unsigned tag) { return (a[1] == tag) & (a[3] == tag); }
DI void bcast4(float v, float (&o)[4]) {
  auto a = __builtin_amdgcn_permlane32_swap(__float_as_uint(v), __float_as_uint(v), false, false);
  auto b = __builtin_amdgcn_permlane16_swap(a[0], a[0], false, false);
  auto c = __builtin_amdgcn_permlane16_swap(a[1], a[1], false, false);
  o[0] = __uint_as_float(b[0]); o[1] = __uint_as_float(b[1]); o[2] = __uint_as_float(c[0]); o[3] = __uint_as_float(c[1]);
}
#define FSTP(x)
#define FSTS(x)
#define FSTA(x)
#define FQ_ASM \
  FSTA("v_mov_b32 v255, 0\n\t") \
  FSTA("s_memrealtime s[94:95]\n\t") \
  FSTA("s_memtime s[92:93]\n\t") \
  FSTA("s_waitcnt lgkmcnt(0)\n\t") \
  FSTA("s_or_b32 s94, s94, 1\n\t") \
  FSTA("v_writelane_b32 v255, s94, 0\n\t") \
  FSTA("v_writelane_b32 v255, s92, 1\n\t") \
  "s_mov_b32 s37, 1\n\t" \
  ".Lfr_again:\n\t" \
  "s_load_dwordx16 s[4:19], s[0:1], 0x0\n\t" \
  "s_load_dwordx16 s[20:35], s[0:1], 0x40\n\t" \
  "s_load_dwordx16 s[60:75], s[0:1], 0x80\n\t" \
  "v_readfirstlane_b32 s3, v0\n\t" \
  "v_and_b32 v254, 63, v0\n\t" \
  "s_lshr_b32 s3, s3, 6\n\t" \
  "s_lshr_b32 s39, s2, 3\n\t" \
  "s_lshl_b32 s54, s3, 5\n\t" \
  "s_add_u32 s54, s54, s39\n\t" \
  "s_lshl_b32 s54, s54, 7\n\t" \
  "v_lshl_add_u32 v48, v254, 14, s54\n\t" \
  "v_min_u32 v48, .Lfz_z-.Lfz_a-4, v48\n\t" \
  "s_getpc_b64 s[96:97]\n\t" \
  ".Lfz_a:\n\t" \
  "global_load_dword v246, v48, s[96:97]\n\t" \
  "s_and_b32 s40, s2, 3\n\t" \
  "v_lshrrev_b32 v52, 2, v0\n\t" \
  "v_lshlrev_b32 v244, 4, v254\n\t" \
  "v_lshlrev_b32 v53, 3, v52\n\t" \
  "v_mul_u32_u24 v52, 49152, v52\n\t" \
  "v_and_or_b32 v52, v244, 48, v52\n\t" \
  "s_lshl_b32 s44, s40, 9\n\t" \
  "s_mul_i32 s45, s40, 3145728\n\t" \
  "s_bfe_u32 s52, s2, 0x10002\n\t" \
  "s_lshl_b32 s52, s52, 5\n\t" \
  "s_add_u32 s52, s52, s39\n\t" \
  "s_lshl_b32 s54, s52, 6\n\t" \
  "s_add_u32 s45, s45, s54\n\t" \
  "s_add_u32 s96, s45, 12288\n\t" \
  "s_add_u32 s97, s45, 24576\n\t" \
  "s_add_u32 s99, s45, 36864\n\t" \
  "v_mov_b32 v1, 2490368\n\t" \
  "s_and_b32 s98, s2, 4\n\t" \
  "s_lshr_b32 s54, s2, 6\n\t" \
  "s_or_b32 s98, s98, s54\n\t" \
  "s_lshl_b32 s41, s98, 19\n\t" \
  "s_lshl_b32 s38, s3, 12\n\t" \
  "s_lshl_b32 s77, s3, 13\n\t" \
  "s_and_b32 s54, s2, 0x38\n\t" \
  "s_lshl_b32 s54, s54, 11\n\t" \
  "s_lshl_b32 s55, s40, 17\n\t" \
  "s_or_b32 s46, s41, s38\n\t" \
  "s_or_b32 s46, s46, s54\n\t" \
  "s_or_b32 s46, s46, s55\n\t" \
  "s_xor_b32 s87, s55, 393216\n\t" \
  "v_lshrrev_b32 v245, 4, v254\n\t" \
  "v_lshrrev_b32 v49, 3, v254\n\t" \
  "v_and_b32 v50, 7, v254\n\t" \
  "v_xor_b32 v51, v50, v245\n\t" \
  "v_lshlrev_b32 v51, 4, v51\n\t" \
  "v_lshl_add_u32 v212, v49, 11, v51\n\t" \
  "v_xor_b32 v213, 64, v212\n\t" \
  "v_add_u32 v213, 15360, v213\n\t" \
  "s_bfe_u32 s42, s2, 0x20004\n\t" \
  "s_lshl_b32 s54, s3, 15\n\t" \
  "s_or_b32 s47, s41, s54\n\t" \
  "s_or_b32 s47, s47, s44\n\t" \
  "s_lshl_b32 s54, s42, 7\n\t" \
  "s_or_b32 s47, s47, s54\n\t" \
  "s_and_b32 s43, s2, 8\n\t" \
  "s_lshl_b32 s54, s43, 15\n\t" \
  "s_or_b32 s57, s47, s87\n\t" \
  "s_xor_b32 s57, s57, s54\n\t" \
  "s_xor_b32 s58, s57, 131072\n\t" \
  "s_lshl_b32 s54, s43, 9\n\t" \
  "s_add_u32 s90, s77, s54\n\t" \
  "s_lshl_b32 s78, s40, 21\n\t" \
  "s_lshl_b32 s50, s39, 14\n\t" \
  "s_lshl2_add_u32 s50, s44, s50\n\t" \
  "s_mul_i32 s54, s3, 0x1080\n\t" \
  "s_and_b32 s54, s54, 0x2080\n\t" \
  "s_or_b32 s50, s50, s54\n\t" \
  "v_lshl_add_u32 v54, v50, 8, s50\n\t" \
  "v_and_b32 v51, 3, v49\n\t" \
  "v_lshl_add_u32 v51, v51, 19, v54\n\t" \
  "s_mov_b32 m0, s90\n\t" \
  "s_waitcnt lgkmcnt(0)\n\t" \
  "buffer_load_dwordx2 v[2:3], v53, s[12:15], s44 offen\n\t" \
  "buffer_load_dwordx4 v[4:7], v52, s[8:11], s45 offen nt\n\t" \
  "buffer_load_dwordx4 v[8:11], v52, s[8:11], s96 offen nt\n\t" \
  "buffer_load_dwordx4 v[12:15], v52, s[8:11], s97 offen nt\n\t" \
  "buffer_load_dwordx4 v[16:19], v52, s[8:11], s99 offen nt\n\t" \
  "s_add_u32 s16, s16, 0x1fffff\n\t" \
  "s_addc_u32 s17, s17, 0\n\t" \
  "s_and_b32 s16, s16, 0xffe00000\n\t" \
  "buffer_load_dword v1, v1, s[16:19], 0 offen sc1\n\t" \
  "s_mov_b32 s96, s46\n\t" \
  "v_mov_b32 v253, v0\n\t" \
  "v_and_b32 v247, 15, v254\n\t" \
  "s_bfe_u32 s43, s2, 0x10003\n\t" \
  "v_and_b32 v48, 3, v254\n\t" \
  "v_lshl_add_u32 v48, v48, 2, v245\n\t" \
  "s_lshl_b32 s54, s3, 8\n\t" \
  "s_add_u32 s55, s54, 147456\n\t" \
  "v_lshl_add_u32 v55, v48, 3, s55\n\t" \
  "v_lshlrev_b32 v56, 3, v247\n\t" \
  "v_add_u32 v56, 147456, v56\n\t" \
  "v_and_b32 v48, 0xf0, v0\n\t" \
  "v_lshl_or_b32 v57, v48, 24, v244\n\t" \
  "s_lshl_b32 s52, s52, 8\n\t" \
  "s_lshl_b32 s55, s40, 14\n\t" \
  "s_add_u32 s52, s52, s55\n\t" \
  "s_add_u32 s52, s52, 2162688\n\t" \
  "v_and_b32 v210, 31, v254\n\t" \
  "v_lshrrev_b32 v48, 5, v254\n\t" \
  "v_lshlrev_b32 v58, 4, v48\n\t" \
  "v_lshl_or_b32 v58, v210, 26, v58\n\t" \
  "s_lshr_b32 s46, s46, 7\n\t" \
  "s_add_u32 s46, s46, 2097152\n\t" \
  "v_cvt_f32_u32 v251, v48\n\t" \
  "v_lshlrev_b32 v248, 4, v210\n\t" \
  "v_lshl_or_b32 v248, v48, 13, v248\n\t" \
  "v_lshlrev_b32 v210, 2, v210\n\t" \
  "v_lshlrev_b32 v249, 4, v247\n\t" \
  "s_lshl_b32 s55, s3, 17\n\t" \
  "s_lshl_b32 s49, s43, 18\n\t" \
  "s_xor_b32 s55, s55, s49\n\t" \
  "buffer_load_dwordx4 v[32:35], v244, s[4:7], s96 offen offset:0\n\t" \
  "buffer_load_dwordx4 v[36:39], v244, s[4:7], s96 offen offset:1024\n\t" \
  "buffer_load_dwordx4 v[40:43], v244, s[4:7], s96 offen offset:2048\n\t" \
  "buffer_load_dwordx4 v[44:47], v244, s[4:7], s96 offen offset:3072\n\t" \
  "s_and_b32 s49, s2, 0x38\n\t" \
  "s_lshl2_add_u32 s55, s44, s55\n\t" \
  "s_lshl_b32 s49, s49, 5\n\t" \
  "s_add_u32 s55, s55, s49\n\t" \
  "v_lshlrev_b32 v48, 5, v245\n\t" \
  "v_xor_b32 v48, v48, v249\n\t" \
  "v_lshl_add_u32 v48, v245, 13, v48\n\t" \
  "v_add_u32 v200, s55, v48\n\t" \
  "v_xor_b32 v48, 0x80, v200\n\t" \
  "v_add_u32 v201, 31744, v200\n\t" \
  "v_add_u32 v202, 63488, v48\n\t" \
  "v_add_u32 v203, 95232, v48\n\t" \
  "s_add_u32 s76, s38, 32768\n\t" \
  "s_lshl_b32 s49, s42, 19\n\t" \
  "s_or_b32 s49, s49, s78\n\t" \
  "s_xor_b32 s80, s49, 1048576\n\t" \
  "s_add_u32 s55, s42, 1\n\t" \
  "s_and_b32 s55, s55, 3\n\t" \
  "s_xor_b32 s55, s55, s42\n\t" \
  "s_lshl_b32 s79, s55, 19\n\t" \
  "s_xor_b32 s79, s79, s49\n\t" \
  "s_xor_b32 s81, s79, 1048576\n\t" \
  "s_lshl_b32 s48, s55, 7\n\t" \
  "s_xor_b32 s48, s48, s47\n\t" \
  "s_xor_b32 s88, s47, 256\n\t" \
  "s_xor_b32 s89, s48, 256\n\t" \
  "v_lshl_add_u32 v48, s43, 2, v245\n\t" \
  "v_bfe_u32 v50, v254, 1, 3\n\t" \
  "v_xor_b32 v48, v48, v50\n\t" \
  "v_lshlrev_b32 v48, 4, v48\n\t" \
  "v_lshl_add_u32 v48, v247, 7, v48\n\t" \
  "v_add_u32 v204, s77, v48\n\t" \
  "v_xor_b32 v205, 64, v204\n\t" \
  "v_add_u32 v206, 98304, v204\n\t" \
  "v_add_u32 v207, 98304, v205\n\t" \
  "v_lshlrev_b32 v208, 5, v245\n\t" \
  "v_add_u32 v208, 148480, v208\n\t" \
  "v_lshlrev_b32 v50, 3, v254\n\t" \
  "s_lshl_b32 s55, s43, 7\n\t" \
  "s_or_b32 s55, s55, 256\n\t" \
  "v_xor_b32 v250, s55, v50\n\t" \
  "s_lshl1_add_u32 s55, s54, 148480\n\t" \
  "v_add_u32 v250, s55, v250\n\t" \
  "v_lshl_add_u32 v214, v245, 10, v249\n\t" \
  "v_lshl_add_u32 v214, s43, 12, v214\n\t" \
  "s_lshr_b32 s55, s2, 2\n\t" \
  "s_xor_b32 s55, s55, s3\n\t" \
  "s_and_b32 s55, s55, 1\n\t" \
  "s_lshl1_add_u32 s55, s3, s55\n\t" \
  "s_lshl_b32 s82, s55, 13\n\t" \
  "s_lshr_b32 s55, s2, 6\n\t" \
  "s_lshl_b32 s55, s55, 20\n\t" \
  "s_add_u32 s82, s82, s55\n\t" \
  "s_add_u32 s82, s82, 4194304\n\t" \
  "s_lshr_b32 s86, s49, 3\n\t" \
  "s_add_u32 s86, s86, s82\n\t" \
  "s_lshr_b32 s55, s87, 9\n\t" \
  "s_add_u32 s86, s86, s55\n\t" \
  "s_lshl2_add_u32 s84, s98, s3\n\t" \
  "s_lshl_b32 s84, s84, 7\n\t" \
  "s_add_u32 s84, s84, 2494464\n\t" \
  "s_bfe_u32 s55, s2, 0x30003\n\t" \
  "s_lshl3_add_u32 s55, s40, s55\n\t" \
  "s_lshl2_add_u32 s85, s55, s84\n\t" \
  "s_lshl_b32 s55, s43, 13\n\t" \
  "s_sub_u32 s83, 4096, s55\n\t" \
  "v_and_b32 v50, 0xf8, v50\n\t" \
  "v_bfe_u32 v48, v254, 2, 2\n\t" \
  "v_lshl_add_u32 v50, v48, 8, v50\n\t" \
  "v_lshl_add_u32 v50, v245, 11, v50\n\t" \
  "v_add_u32 v184, 32768, v50\n\t" \
  "v_xor_b32 v185, 32, v184\n\t" \
  "v_xor_b32 v186, 64, v184\n\t" \
  "v_xor_b32 v187, 96, v184\n\t" \
  "v_xor_b32 v188, 128, v184\n\t" \
  "v_xor_b32 v189, 160, v184\n\t" \
  "v_xor_b32 v190, 192, v184\n\t" \
  "v_xor_b32 v191, 224, v184\n\t" \
  "s_lshl_b32 s51, s98, 12\n\t" \
  "s_add_u32 s51, s51, s54\n\t" \
  "s_add_u32 s51, s51, 2097152\n\t" \
  "s_lshr_b32 s55, s87, 7\n\t" \
  "s_add_u32 s51, s51, s55\n\t" \
  "s_add_u32 s55, s42, s3\n\t" \
  "s_and_b32 s55, s55, 3\n\t" \
  "s_lshl2_add_u32 s55, s40, s55\n\t" \
  "s_lshl_b32 s55, s55, 9\n\t" \
  "s_add_u32 s53, s55, 2162688\n\t" \
  "v_min_u32 v49, 2, v49\n\t" \
  "s_bfe_u32 s55, s2, 0x10002\n\t" \
  "s_lshl2_add_u32 s55, s40, s55\n\t" \
  "s_add_u32 s55, s55, 4\n\t" \
  "v_lshl_add_u32 v49, v49, 1, s55\n\t" \
  "v_lshl_add_u32 v215, v49, 19, v54\n\t" \
  "v_add_u32 v216, 3145728, v215\n\t" \
  "v_and_b32 v215, 0x7fffff, v215\n\t" \
  "v_and_b32 v216, 0x7fffff, v216\n\t" \
  "s_mov_b32 s98, 0xffff0000\n\t" \
  "s_mov_b32 s45, 0x05040100\n\t" \
  "s_mov_b32 s50, 0x07060302\n\t" \
  "s_lshr_b32 s44, s2, 6\n\t" \
  "s_lshl_b32 s44, s44, 9\n\t" \
  "v_lshlrev_b32 v53, 1, v0\n\t" \
  "s_waitcnt vmcnt(9)\n\t" \
  "v_lshlrev_b32 v20, 16, v2\n\t" \
  "v_and_b32 v21, s98, v2\n\t" \
  "v_lshlrev_b32 v22, 16, v3\n\t" \
  "v_and_b32 v23, s98, v3\n\t" \
  "v_mul_f32 v24, 0xbfb8aa3b, v20\n\t" \
  "v_mul_f32 v25, 0xbfb8aa3b, v21\n\t" \
  "v_mul_f32 v26, 0xbfb8aa3b, v22\n\t" \
  "v_mul_f32 v27, 0xbfb8aa3b, v23\n\t" \
  "v_exp_f32 v24, v24\n\t" \
  "v_exp_f32 v25, v25\n\t" \
  "v_exp_f32 v26, v26\n\t" \
  "v_exp_f32 v27, v27\n\t" \
  "v_add_f32 v24, 1.0, v24\n\t" \
  "v_add_f32 v25, 1.0, v25\n\t" \
  "v_add_f32 v26, 1.0, v26\n\t" \
  "v_add_f32 v27, 1.0, v27\n\t" \
  "v_rcp_f32 v24, v24\n\t" \
  "v_rcp_f32 v25, v25\n\t" \
  "v_rcp_f32 v26, v26\n\t" \
  "v_rcp_f32 v27, v27\n\t" \
  "v_mul_f32 v20, v24, v20\n\t" \
  "v_mul_f32 v21, v25, v21\n\t" \
  "v_mul_f32 v22, v26, v22\n\t" \
  "v_mul_f32 v23, v27, v23\n\t" \
  "v_cvt_pk_bf16_f32 v2, v20, v21\n\t" \
  "v_cvt_pk_bf16_f32 v3, v22, v23\n\t" \
  FSTP("s_memrealtime s[94:95]\n\t") \
  FSTP("s_memtime s[92:93]\n\t") \
  FSTP("s_waitcnt lgkmcnt(0)\n\t") \
  FSTP("s_or_b32 s94, s94, 1\n\t") \
  FSTP("v_writelane_b32 v255, s94, 54\n\t") \
  FSTP("v_writelane_b32 v255, s92, 55\n\t") \
  "s_waitcnt vmcnt(5)\n\t" \
  FSTA("s_memrealtime s[94:95]\n\t") \
  FSTA("s_memtime s[92:93]\n\t") \
  FSTA("s_waitcnt lgkmcnt(0)\n\t") \
  FSTA("s_or_b32 s94, s94, 1\n\t") \
  FSTA("v_writelane_b32 v255, s94, 2\n\t") \
  FSTA("v_writelane_b32 v255, s92, 3\n\t") \
  "v_perm_b32 v28, v8, v4, s45\n\t" \
  "v_perm_b32 v29, v9, v5, s45\n\t" \
  "v_perm_b32 v30, v10, v6, s45\n\t" \
  "v_perm_b32 v31, v11, v7, s45\n\t" \
  "v_perm_b32 v4, v8, v4, s50\n\t" \
  "v_perm_b32 v5, v9, v5, s50\n\t" \
  "v_perm_b32 v6, v10, v6, s50\n\t" \
  "v_perm_b32 v7, v11, v7, s50\n\t" \
  "v_dot2_f32_bf16 v20, v28, v2, 0\n\t" \
  "v_dot2_f32_bf16 v21, v29, v2, 0\n\t" \
  "v_dot2_f32_bf16 v22, v30, v2, 0\n\t" \
  "v_dot2_f32_bf16 v23, v31, v2, 0\n\t" \
  "v_dot2_f32_bf16 v24, v4, v2, 0\n\t" \
  "v_dot2_f32_bf16 v25, v5, v2, 0\n\t" \
  "v_dot2_f32_bf16 v26, v6, v2, 0\n\t" \
  "v_dot2_f32_bf16 v27, v7, v2, 0\n\t" \
  "v_perm_b32 v28, v16, v12, s45\n\t" \
  "v_perm_b32 v29, v17, v13, s45\n\t" \
  "v_perm_b32 v30, v18, v14, s45\n\t" \
  "v_perm_b32 v31, v19, v15, s45\n\t" \
  "v_perm_b32 v12, v16, v12, s50\n\t" \
  "v_perm_b32 v13, v17, v13, s50\n\t" \
  "v_perm_b32 v14, v18, v14, s50\n\t" \
  "v_perm_b32 v15, v19, v15, s50\n\t" \
  "v_dot2_f32_bf16 v20, v28, v3, v20\n\t" \
  "v_dot2_f32_bf16 v21, v29, v3, v21\n\t" \
  "v_dot2_f32_bf16 v22, v30, v3, v22\n\t" \
  "v_dot2_f32_bf16 v23, v31, v3, v23\n\t" \
  "v_dot2_f32_bf16 v24, v12, v3, v24\n\t" \
  "v_dot2_f32_bf16 v25, v13, v3, v25\n\t" \
  "v_dot2_f32_bf16 v26, v14, v3, v26\n\t" \
  "v_dot2_f32_bf16 v27, v15, v3, v27\n\t" \
  "v_permlane32_swap_b32 v20, v22\n\t" \
  "v_permlane32_swap_b32 v21, v23\n\t" \
  "v_permlane32_swap_b32 v24, v26\n\t" \
  "v_permlane32_swap_b32 v25, v27\n\t" \
  "v_add_f32 v20, v20, v22\n\t" \
  "v_add_f32 v21, v21, v23\n\t" \
  "v_add_f32 v24, v24, v26\n\t" \
  "v_add_f32 v25, v25, v27\n\t" \
  "v_permlane16_swap_b32 v20, v21\n\t" \
  "s_nop 0\n\t" \
  "v_permlane16_swap_b32 v24, v25\n\t" \
  "v_add_f32 v20, v20, v21\n\t" \
  "v_add_f32 v21, v24, v25\n\t" \
  "s_nop 0\n\t" \
  "v_add_f32_dpp v20, v20, v20 row_ror:8 row_mask:0xf bank_mask:0xf\n\t" \
  "v_add_f32_dpp v21, v21, v21 row_ror:8 row_mask:0xf bank_mask:0xf\n\t" \
  "s_nop 0\n\t" \
  "v_add_f32_dpp v20, v20, v20 row_ror:4 row_mask:0xf bank_mask:0xf\n\t" \
  "v_add_f32_dpp v21, v21, v21 row_ror:4 row_mask:0xf bank_mask:0xf\n\t" \
  "ds_write_b64 v55, v[20:21]\n\t" \
  "buffer_load_dwordx4 v212, s[4:7], s57 offen lds\n\t" \
  "buffer_load_dwordx4 v213, s[4:7], s57 offen offset:1024 lds\n\t" \
  "s_add_u32 m0, s90, 2048\n\t" \
  "s_nop 0\n\t" \
  "buffer_load_dwordx4 v212, s[4:7], s58 offen lds\n\t" \
  "buffer_load_dwordx4 v213, s[4:7], s58 offen offset:1024 lds\n\t" \
  "s_xor_b32 m0, s90, 4096\n\t" \
  "s_xor_b32 s97, s57, 262144\n\t" \
  "buffer_load_dwordx4 v212, s[4:7], s97 offen lds\n\t" \
  "buffer_load_dwordx4 v213, s[4:7], s97 offen offset:1024 lds\n\t" \
  "s_xor_b32 m0, s90, 6144\n\t" \
  "s_xor_b32 s97, s57, 393216\n\t" \
  "buffer_load_dwordx4 v212, s[4:7], s97 offen lds\n\t" \
  "buffer_load_dwordx4 v213, s[4:7], s97 offen offset:1024 lds\n\t" \
  "v_mov_b32 v4, 0\n\t" \
  "v_mov_b32 v5, 0\n\t" \
  "v_mov_b32 v6, 0\n\t" \
  "v_mov_b32 v7, 0\n\t" \
  "s_waitcnt vmcnt(12) lgkmcnt(0)\n\t" \
  "s_barrier\n\t" \
  "ds_read_b64 v[8:9], v56 offset:0\n\t" \
  "ds_read_b64 v[10:11], v56 offset:256\n\t" \
  "ds_read_b64 v[12:13], v56 offset:512\n\t" \
  "ds_read_b64 v[14:15], v56 offset:768\n\t" \
  "v_readfirstlane_b32 s36, v1\n\t" \
  "s_or_b32 s36, s36, 0x40004000\n\t" \
  "s_add_u32 s36, s36, 0x9e3739b9\n\t" \
  "s_and_b32 s36, s36, 0xbfffbfff\n\t" \
  "v_mov_b32 v211, s36\n\t" \
  "v_mov_b32 v17, s36\n\t" \
  "v_mov_b32 v19, s36\n\t" \
  "s_waitcnt lgkmcnt(0)\n\t" \
  "v_add_f32 v8, v8, v10\n\t" \
  "v_add_f32 v12, v12, v14\n\t" \
  "v_add_f32 v9, v9, v11\n\t" \
  "v_add_f32 v13, v13, v15\n\t" \
  "v_add_f32 v16, v8, v12\n\t" \
  "v_add_f32 v18, v9, v13\n\t" \
  "buffer_store_dwordx4 v[16:19], v57, s[16:19], s52 offen sc1\n\t" \
  "s_waitcnt vmcnt(9)\n\t" \
  FSTA("s_memrealtime s[94:95]\n\t") \
  FSTA("s_memtime s[92:93]\n\t") \
  FSTA("s_waitcnt lgkmcnt(0)\n\t") \
  FSTA("s_or_b32 s94, s94, 1\n\t") \
  FSTA("v_writelane_b32 v255, s94, 4\n\t") \
  FSTA("v_writelane_b32 v255, s92, 5\n\t") \
  "v_dot2c_f32_bf16 v4, 0x3f803f80, v32\n\t" \
  "v_dot2c_f32_bf16 v6, v32, v32\n\t" \
  "v_dot2c_f32_bf16 v5, 0x3f803f80, v40\n\t" \
  "v_dot2c_f32_bf16 v7, v40, v40\n\t" \
  "v_dot2c_f32_bf16 v4, 0x3f803f80, v33\n\t" \
  "v_dot2c_f32_bf16 v6, v33, v33\n\t" \
  "v_dot2c_f32_bf16 v5, 0x3f803f80, v41\n\t" \
  "v_dot2c_f32_bf16 v7, v41, v41\n\t" \
  "v_dot2c_f32_bf16 v4, 0x3f803f80, v34\n\t" \
  "v_dot2c_f32_bf16 v6, v34, v34\n\t" \
  "v_dot2c_f32_bf16 v5, 0x3f803f80, v42\n\t" \
  "v_dot2c_f32_bf16 v7, v42, v42\n\t" \
  "v_dot2c_f32_bf16 v4, 0x3f803f80, v35\n\t" \
  "v_dot2c_f32_bf16 v6, v35, v35\n\t" \
  "v_dot2c_f32_bf16 v5, 0x3f803f80, v43\n\t" \
  "v_dot2c_f32_bf16 v7, v43, v43\n\t" \
  "v_dot2c_f32_bf16 v4, 0x3f803f80, v36\n\t" \
  "v_dot2c_f32_bf16 v6, v36, v36\n\t" \
  "v_dot2c_f32_bf16 v5, 0x3f803f80, v44\n\t" \
  "v_dot2c_f32_bf16 v7, v44, v44\n\t" \
  "v_dot2c_f32_bf16 v4, 0x3f803f80, v37\n\t" \
  "v_dot2c_f32_bf16 v6, v37, v37\n\t" \
  "v_dot2c_f32_bf16 v5, 0x3f803f80, v45\n\t" \
  "v_dot2c_f32_bf16 v7, v45, v45\n\t" \
  "v_dot2c_f32_bf16 v4, 0x3f803f80, v38\n\t" \
  "v_dot2c_f32_bf16 v6, v38, v38\n\t" \
  "v_dot2c_f32_bf16 v5, 0x3f803f80, v46\n\t" \
  "v_dot2c_f32_bf16 v7, v46, v46\n\t" \
  "v_dot2c_f32_bf16 v4, 0x3f803f80, v39\n\t" \
  "v_dot2c_f32_bf16 v6, v39, v39\n\t" \
  "v_dot2c_f32_bf16 v5, 0x3f803f80, v47\n\t" \
  "v_dot2c_f32_bf16 v7, v47, v47\n\t" \
  "v_mov_b32 v21, s36\n\t" \
  "v_mov_b32 v23, s36\n\t" \
  "v_permlane32_swap_b32 v4, v5\n\t" \
  "v_permlane32_swap_b32 v6, v7\n\t" \
  "v_add_f32 v4, v4, v5\n\t" \
  "v_add_f32 v6, v6, v7\n\t" \
  "s_nop 1\n\t" \
  "v_permlane16_swap_b32 v4, v6\n\t" \
  "v_add_f32 v4, v4, v6\n\t" \
  "s_nop 1\n\t" \
  "v_add_f32_dpp v4, v4, v4 quad_perm:[1,0,3,2] row_mask:0xf bank_mask:0xf\n\t" \
  "s_nop 1\n\t" \
  "v_add_f32_dpp v4, v4, v4 quad_perm:[2,3,0,1] row_mask:0xf bank_mask:0xf\n\t" \
  "s_nop 1\n\t" \
  "v_add_f32_dpp v4, v4, v4 row_half_mirror row_mask:0xf bank_mask:0xf\n\t" \
  "s_nop 1\n\t" \
  "v_add_f32_dpp v4, v4, v4 row_mirror row_mask:0xf bank_mask:0xf\n\t" \
  "v_mul_f32 v4, 0x3a800000, v4\n\t" \
  "v_mov_b32 v5, v4\n\t" \
  "s_nop 1\n\t" \
  "v_permlane16_swap_b32 v4, v5\n\t" \
  "v_fma_f32 v5, -v4, v4, v5\n\t" \
  "v_max_f32 v5, 0, v5\n\t" \
  "v_add_f32 v5, 0x3727c5ac, v5\n\t" \
  "v_rsq_f32 v20, v5\n\t" \
  "s_nop 0\n\t" \
  "v_mul_f32_e64 v22, v20, -v4\n\t" \
  "buffer_store_dwordx4 v[20:23], v58, s[16:19], s46 offen sc1\n\t" \
  FSTA("s_memrealtime s[94:95]\n\t") \
  FSTA("s_memtime s[92:93]\n\t") \
  FSTA("s_waitcnt lgkmcnt(0)\n\t") \
  FSTA("s_or_b32 s94, s94, 1\n\t") \
  FSTA("v_writelane_b32 v255, s94, 6\n\t") \
  FSTA("v_writelane_b32 v255, s92, 7\n\t") \
  "buffer_load_ushort v218, v53, s[12:15], s44 offen\n\t" \
  "s_add_u32 m0, s76, 0\n\t" \
  "s_nop 0\n\t" \
  "buffer_load_dwordx4 v200, s[20:23], s49 offen offset:0 lds\n\t" \
  "buffer_load_dwordx4 v201, s[20:23], s49 offen offset:1024 lds\n\t" \
  "buffer_load_dwordx4 v202, s[20:23], s49 offen offset:2048 lds\n\t" \
  "buffer_load_dwordx4 v203, s[20:23], s49 offen offset:3072 lds\n\t" \
  "s_add_u32 m0, s77, 49152\n\t" \
  "s_xor_b32 s54, s87, 0\n\t" \
  "s_or_b32 s54, s54, s48\n\t" \
  "buffer_load_dwordx4 v212, s[4:7], s54 offen lds\n\t" \
  "buffer_load_dwordx4 v213, s[4:7], s54 offen offset:1024 lds\n\t" \
  "s_add_u32 m0, s77, 51200\n\t" \
  "s_xor_b32 s54, s87, 131072\n\t" \
  "s_or_b32 s54, s54, s48\n\t" \
  "buffer_load_dwordx4 v212, s[4:7], s54 offen lds\n\t" \
  "buffer_load_dwordx4 v213, s[4:7], s54 offen offset:1024 lds\n\t" \
  "s_add_u32 m0, s77, 53248\n\t" \
  "s_xor_b32 s54, s87, 262144\n\t" \
  "s_or_b32 s54, s54, s48\n\t" \
  "buffer_load_dwordx4 v212, s[4:7], s54 offen lds\n\t" \
  "buffer_load_dwordx4 v213, s[4:7], s54 offen offset:1024 lds\n\t" \
  "s_add_u32 m0, s77, 55296\n\t" \
  "s_xor_b32 s54, s87, 393216\n\t" \
  "s_or_b32 s54, s54, s48\n\t" \
  "buffer_load_dwordx4 v212, s[4:7], s54 offen lds\n\t" \
  "buffer_load_dwordx4 v213, s[4:7], s54 offen offset:1024 lds\n\t" \
  "s_add_u32 m0, s76, 49152\n\t" \
  "s_nop 0\n\t" \
  "buffer_load_dwordx4 v200, s[20:23], s79 offen offset:0 lds\n\t" \
  "buffer_load_dwordx4 v201, s[20:23], s79 offen offset:1024 lds\n\t" \
  "buffer_load_dwordx4 v202, s[20:23], s79 offen offset:2048 lds\n\t" \
  "buffer_load_dwordx4 v203, s[20:23], s79 offen offset:3072 lds\n\t" \
  "v_add_u32 v192, 65536, v184\n\t" \
  "v_add_u32 v193, 65536, v185\n\t" \
  "v_add_u32 v194, 65536, v186\n\t" \
  "v_add_u32 v195, 65536, v187\n\t" \
  "v_add_u32 v196, 65536, v188\n\t" \
  "v_add_u32 v197, 65536, v189\n\t" \
  "v_add_u32 v198, 65536, v190\n\t" \
  "v_add_u32 v199, 65536, v191\n\t" \
  "v_lshrrev_b32 v48, 3, v253\n\t" \
  "s_lshr_b32 s54, s2, 6\n\t" \
  "v_lshl_add_u32 v48, s54, 5, v48\n\t" \
  "v_mul_u32_u24 v48, 98304, v48\n\t" \
  "v_and_b32 v49, 0x70, v244\n\t" \
  "s_bfe_u32 s54, s2, 0x10002\n\t" \
  "s_lshl1_add_u32 s54, s40, s54\n\t" \
  "s_lshl_b32 s54, s54, 10\n\t" \
  "s_and_b32 s55, s2, 0x38\n\t" \
  "s_lshl4_add_u32 s54, s55, s54\n\t" \
  "s_add_u32 s54, s54, 4096\n\t" \
  "v_add3_u32 v209, v48, v49, s54\n\t" \
  "v_lshlrev_b32 v219, 2, v253\n\t" \
  "v_add_u32 v219, 151552, v219\n\t" \
  "s_and_b32 s54, s39, 15\n\t" \
  "s_lshl2_add_u32 s54, s54, s3\n\t" \
  "s_lshl_b32 s54, s54, 11\n\t" \
  "s_lshl_b32 s55, s40, 19\n\t" \
  "s_add_u32 s54, s54, s55\n\t" \
  "v_lshl_add_u32 v217, v247, 7, s54\n\t" \
  "s_mov_b64 exec, 0\n\t" \
  "s_cmp_eq_u32 s3, 0\n\t" \
  "s_cbranch_scc1 .Lhc00\n\t" \
  "s_cmp_eq_u32 s3, 1\n\t" \
  "s_cbranch_scc1 .Lhc01\n\t" \
  "s_cmp_eq_u32 s3, 2\n\t" \
  "s_cbranch_scc1 .Lhc02\n\t" \
  "s_branch .Lhc03\n\t" \
  ".Lhr0:\n\t" \
  "s_mov_b64 exec, -1\n\t" \
  "s_sleep 6\n\t" \
  "s_add_u32 s58, s53, 16384\n\t" \
  "s_add_u32 s90, s53, 32768\n\t" \
  "s_add_u32 s91, s53, 49152\n\t" \
  "s_xor_b32 s96, s51, 0\n\t" \
  "s_xor_b32 s97, s51, 1024\n\t" \
  "s_xor_b32 s99, s51, 2048\n\t" \
  "s_xor_b32 s57, s51, 3072\n\t" \
  "buffer_load_dwordx4 v[128:131], v248, s[16:19], s53 offen sc1\n\t" \
  "buffer_load_dwordx4 v[132:135], v248, s[16:19], s58 offen sc1\n\t" \
  "buffer_load_dwordx4 v[136:139], v248, s[16:19], s90 offen sc1\n\t" \
  "buffer_load_dwordx4 v[140:143], v248, s[16:19], s91 offen sc1\n\t" \
  "buffer_load_dwordx4 v[160:163], v249, s[16:19], s96 offen sc1\n\t" \
  "buffer_load_dwordx4 v[164:167], v249, s[16:19], s97 offen sc1\n\t" \
  "buffer_load_dwordx4 v[168:171], v249, s[16:19], s99 offen sc1\n\t" \
  "buffer_load_dwordx4 v[172:175], v249, s[16:19], s57 offen sc1\n\t" \
  "s_bitcmp1_b32 s2, 7\n\t" \
  "s_cselect_b64 s[12:13], s[64:65], s[60:61]\n\t" \
  FSTP("s_memrealtime s[94:95]\n\t") \
  FSTP("s_memtime s[92:93]\n\t") \
  FSTP("s_waitcnt lgkmcnt(0)\n\t") \
  FSTP("s_or_b32 s94, s94, 1\n\t") \
  FSTP("v_writelane_b32 v255, s94, 56\n\t") \
  FSTP("v_writelane_b32 v255, s92, 57\n\t") \
  "s_waitcnt vmcnt(12)\n\t" \
  "ds_read_b128 v[16:19], v204 offset:0\n\t" \
  "ds_read_b128 v[48:51], v204 offset:2048\n\t" \
  "ds_read_b128 v[80:83], v204 offset:4096\n\t" \
  "ds_read_b128 v[112:115], v204 offset:6144\n\t" \
  "ds_read_b128 v[20:23], v205 offset:0\n\t" \
  "ds_read_b128 v[52:55], v205 offset:2048\n\t" \
  "ds_read_b128 v[84:87], v205 offset:4096\n\t" \
  "ds_read_b128 v[116:119], v205 offset:6144\n\t" \
  "ds_read_b128 v[24:27], v204 offset:49152\n\t" \
  "ds_read_b128 v[56:59], v204 offset:51200\n\t" \
  "ds_read_b128 v[88:91], v204 offset:53248\n\t" \
  "ds_read_b128 v[120:123], v204 offset:55296\n\t" \
  "ds_read_b128 v[28:31], v205 offset:49152\n\t" \
  "ds_read_b128 v[60:63], v205 offset:51200\n\t" \
  "ds_read_b128 v[92:95], v205 offset:53248\n\t" \
  "ds_read_b128 v[124:127], v205 offset:55296\n\t" \
  "v_lshlrev_b32 v218, 16, v218\n\t" \
  "v_mul_f32 v145, 0xbfb8aa3b, v218\n\t" \
  "v_exp_f32 v145, v145\n\t" \
  "s_nop 0\n\t" \
  "v_add_f32 v145, 1.0, v145\n\t" \
  "v_rcp_f32 v145, v145\n\t" \
  "s_nop 0\n\t" \
  "v_mul_f32 v218, v145, v218\n\t" \
  "v_cvt_pk_bf16_f32 v218, v218, v218\n\t" \
  "v_lshlrev_b32 v218, 16, v218\n\t" \
  "s_waitcnt lgkmcnt(12)\n\t" \
  "v_lshlrev_b32 v0, 16, v16\n\t" \
  "v_and_b32 v1, s98, v16\n\t" \
  "v_lshlrev_b32 v2, 16, v17\n\t" \
  "v_and_b32 v3, s98, v17\n\t" \
  "v_lshlrev_b32 v4, 16, v18\n\t" \
  "v_and_b32 v5, s98, v18\n\t" \
  "v_lshlrev_b32 v6, 16, v19\n\t" \
  "v_and_b32 v7, s98, v19\n\t" \
  "v_lshlrev_b32 v32, 16, v48\n\t" \
  "v_and_b32 v33, s98, v48\n\t" \
  "v_lshlrev_b32 v34, 16, v49\n\t" \
  "v_and_b32 v35, s98, v49\n\t" \
  "v_lshlrev_b32 v36, 16, v50\n\t" \
  "v_and_b32 v37, s98, v50\n\t" \
  "v_lshlrev_b32 v38, 16, v51\n\t" \
  "v_and_b32 v39, s98, v51\n\t" \
  "v_lshlrev_b32 v64, 16, v80\n\t" \
  "v_and_b32 v65, s98, v80\n\t" \
  "v_lshlrev_b32 v66, 16, v81\n\t" \
  "v_and_b32 v67, s98, v81\n\t" \
  "v_lshlrev_b32 v68, 16, v82\n\t" \
  "v_and_b32 v69, s98, v82\n\t" \
  "v_lshlrev_b32 v70, 16, v83\n\t" \
  "v_and_b32 v71, s98, v83\n\t" \
  "v_lshlrev_b32 v96, 16, v112\n\t" \
  "v_and_b32 v97, s98, v112\n\t" \
  "v_lshlrev_b32 v98, 16, v113\n\t" \
  "v_and_b32 v99, s98, v113\n\t" \
  "v_lshlrev_b32 v100, 16, v114\n\t" \
  "v_and_b32 v101, s98, v114\n\t" \
  "v_lshlrev_b32 v102, 16, v115\n\t" \
  "v_and_b32 v103, s98, v115\n\t" \
  "s_waitcnt lgkmcnt(8)\n\t" \
  "v_lshlrev_b32 v8, 16, v20\n\t" \
  "v_and_b32 v9, s98, v20\n\t" \
  "v_lshlrev_b32 v10, 16, v21\n\t" \
  "v_and_b32 v11, s98, v21\n\t" \
  "v_lshlrev_b32 v12, 16, v22\n\t" \
  "v_and_b32 v13, s98, v22\n\t" \
  "v_lshlrev_b32 v14, 16, v23\n\t" \
  "v_and_b32 v15, s98, v23\n\t" \
  "v_lshlrev_b32 v40, 16, v52\n\t" \
  "v_and_b32 v41, s98, v52\n\t" \
  "v_lshlrev_b32 v42, 16, v53\n\t" \
  "v_and_b32 v43, s98, v53\n\t" \
  "v_lshlrev_b32 v44, 16, v54\n\t" \
  "v_and_b32 v45, s98, v54\n\t" \
  "v_lshlrev_b32 v46, 16, v55\n\t" \
  "v_and_b32 v47, s98, v55\n\t" \
  "v_lshlrev_b32 v72, 16, v84\n\t" \
  "v_and_b32 v73, s98, v84\n\t" \
  "v_lshlrev_b32 v74, 16, v85\n\t" \
  "v_and_b32 v75, s98, v85\n\t" \
  "v_lshlrev_b32 v76, 16, v86\n\t" \
  "v_and_b32 v77, s98, v86\n\t" \
  "v_lshlrev_b32 v78, 16, v87\n\t" \
  "v_and_b32 v79, s98, v87\n\t" \
  "v_lshlrev_b32 v104, 16, v116\n\t" \
  "v_and_b32 v105, s98, v116\n\t" \
  "v_lshlrev_b32 v106, 16, v117\n\t" \
  "v_and_b32 v107, s98, v117\n\t" \
  "v_lshlrev_b32 v108, 16, v118\n\t" \
  "v_and_b32 v109, s98, v118\n\t" \
  "v_lshlrev_b32 v110, 16, v119\n\t" \
  "v_and_b32 v111, s98, v119\n\t" \
  FSTP("s_memrealtime s[94:95]\n\t") \
  FSTP("s_memtime s[92:93]\n\t") \
  FSTP("s_waitcnt lgkmcnt(0)\n\t") \
  FSTP("s_or_b32 s94, s94, 1\n\t") \
  FSTP("v_writelane_b32 v255, s94, 58\n\t") \
  FSTP("v_writelane_b32 v255, s92, 59\n\t") \
  "s_waitcnt vmcnt(0)\n\t" \
  "v_xor_b32 v144, v211, v129\n\t" \
  "v_bitop3_b32 v144, v144, v131, v211 bitop3:0xf6\n\t" \
  "v_bitop3_b32 v144, v144, v133, v211 bitop3:0xf6\n\t" \
  "v_bitop3_b32 v144, v144, v135, v211 bitop3:0xf6\n\t" \
  "v_bitop3_b32 v144, v144, v137, v211 bitop3:0xf6\n\t" \
  "v_bitop3_b32 v144, v144, v139, v211 bitop3:0xf6\n\t" \
  "v_bitop3_b32 v144, v144, v141, v211 bitop3:0xf6\n\t" \
  "v_bitop3_b32 v144, v144, v143, v211 bitop3:0xf6\n\t" \
  "v_bitop3_b32 v144, v144, v161, v211 bitop3:0xf6\n\t" \
  "v_bitop3_b32 v144, v144, v163, v211 bitop3:0xf6\n\t" \
  "v_bitop3_b32 v144, v144, v165, v211 bitop3:0xf6\n\t" \
  "v_bitop3_b32 v144, v144, v167, v211 bitop3:0xf6\n\t" \
  "v_bitop3_b32 v144, v144, v169, v211 bitop3:0xf6\n\t" \
  "v_bitop3_b32 v144, v144, v171, v211 bitop3:0xf6\n\t" \
  "v_bitop3_b32 v144, v144, v173, v211 bitop3:0xf6\n\t" \
  "v_bitop3_b32 v144, v144, v175, v211 bitop3:0xf6\n\t" \
  "v_cmp_ne_u32 vcc, 0, v144\n\t" \
  "v_add_f32 v146, v128, v132\n\t" \
  "v_add_f32 v148, v136, v140\n\t" \
  "v_add_f32 v147, v130, v134\n\t" \
  "v_add_f32 v149, v138, v142\n\t" \
  "v_add_f32 v146, v146, v148\n\t" \
  "v_add_f32 v147, v147, v149\n\t" \
  "s_cbranch_vccnz .Lfr_s\n\t" \
  ".Lfr_k:\n\t" \
  FSTA("s_memrealtime s[94:95]\n\t") \
  FSTA("s_memtime s[92:93]\n\t") \
  FSTA("s_waitcnt lgkmcnt(0)\n\t") \
  FSTA("s_or_b32 s94, s94, 1\n\t") \
  FSTA("v_writelane_b32 v255, s94, 8\n\t") \
  FSTA("v_writelane_b32 v255, s92, 9\n\t") \
  "v_cvt_pk_bf16_f32 v147, v146, v147\n\t" \
  "v_lshlrev_b32 v146, 16, v147\n\t" \
  "v_and_b32 v147, s98, v147\n\t" \
  "v_add_f32 v146, v146, v251\n\t" \
  "v_add_f32 v147, v147, v251\n\t" \
  "v_cvt_pk_bf16_f32 v147, v146, v147\n\t" \
  "v_lshlrev_b32 v146, 16, v147\n\t" \
  "v_and_b32 v147, s98, v147\n\t" \
  "v_mul_f32 v146, 0x3e0293ee, v146\n\t" \
  "v_mul_f32 v147, 0x3e0293ee, v147\n\t" \
  "ds_write_b64 v250, v[146:147]\n\t" \
  "v_pk_fma_f32 v[0:1], v[0:1], v[160:161], v[162:163] op_sel_hi:[1,0,0]\n\t" \
  "v_pk_fma_f32 v[2:3], v[2:3], v[160:161], v[162:163] op_sel_hi:[1,0,0]\n\t" \
  "v_pk_fma_f32 v[4:5], v[4:5], v[160:161], v[162:163] op_sel_hi:[1,0,0]\n\t" \
  "v_pk_fma_f32 v[6:7], v[6:7], v[160:161], v[162:163] op_sel_hi:[1,0,0]\n\t" \
  "v_pk_fma_f32 v[32:33], v[32:33], v[164:165], v[166:167] op_sel_hi:[1,0,0]\n\t" \
  "v_pk_fma_f32 v[34:35], v[34:35], v[164:165], v[166:167] op_sel_hi:[1,0,0]\n\t" \
  "v_pk_fma_f32 v[36:37], v[36:37], v[164:165], v[166:167] op_sel_hi:[1,0,0]\n\t" \
  "v_pk_fma_f32 v[38:39], v[38:39], v[164:165], v[166:167] op_sel_hi:[1,0,0]\n\t" \
  "v_pk_fma_f32 v[64:65], v[64:65], v[168:169], v[170:171] op_sel_hi:[1,0,0]\n\t" \
  "v_pk_fma_f32 v[66:67], v[66:67], v[168:169], v[170:171] op_sel_hi:[1,0,0]\n\t" \
  "v_pk_fma_f32 v[68:69], v[68:69], v[168:169], v[170:171] op_sel_hi:[1,0,0]\n\t" \
  "v_pk_fma_f32 v[70:71], v[70:71], v[168:169], v[170:171] op_sel_hi:[1,0,0]\n\t" \
  "v_pk_fma_f32 v[96:97], v[96:97], v[172:173], v[174:175] op_sel_hi:[1,0,0]\n\t" \
  "v_pk_fma_f32 v[98:99], v[98:99], v[172:173], v[174:175] op_sel_hi:[1,0,0]\n\t" \
  "v_pk_fma_f32 v[100:101], v[100:101], v[172:173], v[174:175] op_sel_hi:[1,0,0]\n\t" \
  "v_pk_fma_f32 v[102:103], v[102:103], v[172:173], v[174:175] op_sel_hi:[1,0,0]\n\t" \
  FSTP("s_memrealtime s[94:95]\n\t") \
  FSTP("s_memtime s[92:93]\n\t") \
  FSTP("s_waitcnt lgkmcnt(0)\n\t") \
  FSTP("s_or_b32 s94, s94, 1\n\t") \
  FSTP("v_writelane_b32 v255, s94, 24\n\t") \
  FSTP("v_writelane_b32 v255, s92, 25\n\t") \
  "s_waitcnt lgkmcnt(0)\n\t" \
  "s_barrier\n\t" \
  FSTP("s_memrealtime s[94:95]\n\t") \
  FSTP("s_memtime s[92:93]\n\t") \
  FSTP("s_waitcnt lgkmcnt(0)\n\t") \
  FSTP("s_or_b32 s94, s94, 1\n\t") \
  FSTP("v_writelane_b32 v255, s94, 26\n\t") \
  FSTP("v_writelane_b32 v255, s92, 27\n\t") \
  "ds_read_b128 v[128:131], v208 offset:0\n\t" \
  "ds_read_b128 v[132:135], v208 offset:16\n\t" \
  "ds_read_b128 v[136:139], v208 offset:256\n\t" \
  "ds_read_b128 v[140:143], v208 offset:272\n\t" \
  "ds_read_b128 v[144:147], v208 offset:128\n\t" \
  "ds_read_b128 v[148:151], v208 offset:144\n\t" \
  "ds_read_b128 v[152:155], v208 offset:384\n\t" \
  "ds_read_b128 v[156:159], v208 offset:400\n\t" \
  "global_load_dword v246, v215, s[20:21]\n\t" \
  "v_pk_fma_f32 v[8:9], v[8:9], v[160:161], v[162:163] op_sel_hi:[1,0,0]\n\t" \
  "v_pk_fma_f32 v[10:11], v[10:11], v[160:161], v[162:163] op_sel_hi:[1,0,0]\n\t" \
  "v_pk_fma_f32 v[12:13], v[12:13], v[160:161], v[162:163] op_sel_hi:[1,0,0]\n\t" \
  "v_pk_fma_f32 v[14:15], v[14:15], v[160:161], v[162:163] op_sel_hi:[1,0,0]\n\t" \
  "v_pk_fma_f32 v[40:41], v[40:41], v[164:165], v[166:167] op_sel_hi:[1,0,0]\n\t" \
  "v_pk_fma_f32 v[42:43], v[42:43], v[164:165], v[166:167] op_sel_hi:[1,0,0]\n\t" \
  "v_pk_fma_f32 v[44:45], v[44:45], v[164:165], v[166:167] op_sel_hi:[1,0,0]\n\t" \
  "v_pk_fma_f32 v[46:47], v[46:47], v[164:165], v[166:167] op_sel_hi:[1,0,0]\n\t" \
  "v_pk_fma_f32 v[72:73], v[72:73], v[168:169], v[170:171] op_sel_hi:[1,0,0]\n\t" \
  "v_pk_fma_f32 v[74:75], v[74:75], v[168:169], v[170:171] op_sel_hi:[1,0,0]\n\t" \
  "v_pk_fma_f32 v[76:77], v[76:77], v[168:169], v[170:171] op_sel_hi:[1,0,0]\n\t" \
  "v_pk_fma_f32 v[78:79], v[78:79], v[168:169], v[170:171] op_sel_hi:[1,0,0]\n\t" \
  "v_pk_fma_f32 v[104:105], v[104:105], v[172:173], v[174:175] op_sel_hi:[1,0,0]\n\t" \
  "v_pk_fma_f32 v[106:107], v[106:107], v[172:173], v[174:175] op_sel_hi:[1,0,0]\n\t" \
  "v_pk_fma_f32 v[108:109], v[108:109], v[172:173], v[174:175] op_sel_hi:[1,0,0]\n\t" \
  "v_pk_fma_f32 v[110:111], v[110:111], v[172:173], v[174:175] op_sel_hi:[1,0,0]\n\t" \
  "s_xor_b32 s97, s86, 0\n\t" \
  "s_waitcnt lgkmcnt(4)\n\t" \
  "v_pk_fma_f32 v[0:1], v[0:1], v[128:129], v[136:137]\n\t" \
  "v_pk_fma_f32 v[2:3], v[2:3], v[130:131], v[138:139]\n\t" \
  "v_pk_fma_f32 v[4:5], v[4:5], v[132:133], v[140:141]\n\t" \
  "v_pk_fma_f32 v[6:7], v[6:7], v[134:135], v[142:143]\n\t" \
  "v_cvt_pk_bf16_f32 v16, v0, v1\n\t" \
  "v_cvt_pk_bf16_f32 v17, v2, v3\n\t" \
  "v_cvt_pk_bf16_f32 v18, v4, v5\n\t" \
  "v_cvt_pk_bf16_f32 v19, v6, v7\n\t" \
  "buffer_store_dwordx4 v[16:19], v214, s[16:19], s97 offen sc1\n\t" \
  "s_xor_b32 s97, s86, 256\n\t" \
  "v_pk_fma_f32 v[32:33], v[32:33], v[128:129], v[136:137]\n\t" \
  "v_pk_fma_f32 v[34:35], v[34:35], v[130:131], v[138:139]\n\t" \
  "v_pk_fma_f32 v[36:37], v[36:37], v[132:133], v[140:141]\n\t" \
  "v_pk_fma_f32 v[38:39], v[38:39], v[134:135], v[142:143]\n\t" \
  "v_cvt_pk_bf16_f32 v48, v32, v33\n\t" \
  "v_cvt_pk_bf16_f32 v49, v34, v35\n\t" \
  "v_cvt_pk_bf16_f32 v50, v36, v37\n\t" \
  "v_cvt_pk_bf16_f32 v51, v38, v39\n\t" \
  "buffer_store_dwordx4 v[48:51], v214, s[16:19], s97 offen sc1\n\t" \
  "s_xor_b32 s97, s86, 512\n\t" \
  "v_pk_fma_f32 v[64:65], v[64:65], v[128:129], v[136:137]\n\t" \
  "v_pk_fma_f32 v[66:67], v[66:67], v[130:131], v[138:139]\n\t" \
  "v_pk_fma_f32 v[68:69], v[68:69], v[132:133], v[140:141]\n\t" \
  "v_pk_fma_f32 v[70:71], v[70:71], v[134:135], v[142:143]\n\t" \
  "v_cvt_pk_bf16_f32 v80, v64, v65\n\t" \
  "v_cvt_pk_bf16_f32 v81, v66, v67\n\t" \
  "v_cvt_pk_bf16_f32 v82, v68, v69\n\t" \
  "v_cvt_pk_bf16_f32 v83, v70, v71\n\t" \
  ".Lhs0:\n\ts_cbranch_execz .Lhs0n\n\t" \
  "buffer_store_dwordx4 v[80:83], v214, s[16:19], s97 offen sc1\n\t" \
  "s_xor_b32 s97, s86, 768\n\t" \
  "v_pk_fma_f32 v[96:97], v[96:97], v[128:129], v[136:137]\n\t" \
  "v_pk_fma_f32 v[98:99], v[98:99], v[130:131], v[138:139]\n\t" \
  "v_pk_fma_f32 v[100:101], v[100:101], v[132:133], v[140:141]\n\t" \
  "v_pk_fma_f32 v[102:103], v[102:103], v[134:135], v[142:143]\n\t" \
  "v_cvt_pk_bf16_f32 v112, v96, v97\n\t" \
  "v_cvt_pk_bf16_f32 v113, v98, v99\n\t" \
  "v_cvt_pk_bf16_f32 v114, v100, v101\n\t" \
  "v_cvt_pk_bf16_f32 v115, v102, v103\n\t" \
  "buffer_store_dwordx4 v[112:115], v214, s[16:19], s97 offen sc1\n\t" \
  "ds_read_b128 v[128:131], v208 offset:512\n\t" \
  "ds_read_b128 v[132:135], v208 offset:528\n\t" \
  "ds_read_b128 v[136:139], v208 offset:768\n\t" \
  "ds_read_b128 v[140:143], v208 offset:784\n\t" \
  FSTP("s_memrealtime s[94:95]\n\t") \
  FSTP("s_memtime s[92:93]\n\t") \
  "s_add_u32 m0, s77, 98304\n\t" \
  "s_xor_b32 s96, s87, 0\n\t" \
  "s_add_u32 s96, s96, s88\n\t" \
  "s_waitcnt lgkmcnt(4)\n\t" \
  "v_pk_fma_f32 v[8:9], v[8:9], v[144:145], v[152:153]\n\t" \
  "v_pk_fma_f32 v[10:11], v[10:11], v[146:147], v[154:155]\n\t" \
  "v_pk_fma_f32 v[12:13], v[12:13], v[148:149], v[156:157]\n\t" \
  "v_pk_fma_f32 v[14:15], v[14:15], v[150:151], v[158:159]\n\t" \
  "v_cvt_pk_bf16_f32 v20, v8, v9\n\t" \
  "v_cvt_pk_bf16_f32 v21, v10, v11\n\t" \
  "v_cvt_pk_bf16_f32 v22, v12, v13\n\t" \
  "v_cvt_pk_bf16_f32 v23, v14, v15\n\t" \
  "buffer_load_dwordx4 v212, s[4:7], s96 offen lds\n\t" \
  "v_pk_fma_f32 v[40:41], v[40:41], v[144:145], v[152:153]\n\t" \
  "v_pk_fma_f32 v[42:43], v[42:43], v[146:147], v[154:155]\n\t" \
  "v_pk_fma_f32 v[44:45], v[44:45], v[148:149], v[156:157]\n\t" \
  "v_pk_fma_f32 v[46:47], v[46:47], v[150:151], v[158:159]\n\t" \
  "v_cvt_pk_bf16_f32 v52, v40, v41\n\t" \
  "v_cvt_pk_bf16_f32 v53, v42, v43\n\t" \
  "v_cvt_pk_bf16_f32 v54, v44, v45\n\t" \
  "v_cvt_pk_bf16_f32 v55, v46, v47\n\t" \
  "buffer_load_dwordx4 v213, s[4:7], s96 offen offset:1024 lds\n\t" \
  "s_add_u32 m0, s77, 100352\n\t" \
  "s_xor_b32 s96, s87, 131072\n\t" \
  "s_add_u32 s96, s96, s88\n\t" \
  "v_pk_fma_f32 v[72:73], v[72:73], v[144:145], v[152:153]\n\t" \
  "v_pk_fma_f32 v[74:75], v[74:75], v[146:147], v[154:155]\n\t" \
  "v_pk_fma_f32 v[76:77], v[76:77], v[148:149], v[156:157]\n\t" \
  "v_pk_fma_f32 v[78:79], v[78:79], v[150:151], v[158:159]\n\t" \
  "v_cvt_pk_bf16_f32 v84, v72, v73\n\t" \
  "v_cvt_pk_bf16_f32 v85, v74, v75\n\t" \
  "v_cvt_pk_bf16_f32 v86, v76, v77\n\t" \
  "v_cvt_pk_bf16_f32 v87, v78, v79\n\t" \
  "buffer_load_dwordx4 v212, s[4:7], s96 offen lds\n\t" \
  "v_pk_fma_f32 v[104:105], v[104:105], v[144:145], v[152:153]\n\t" \
  "v_pk_fma_f32 v[106:107], v[106:107], v[146:147], v[154:155]\n\t" \
  "v_pk_fma_f32 v[108:109], v[108:109], v[148:149], v[156:157]\n\t" \
  "v_pk_fma_f32 v[110:111], v[110:111], v[150:151], v[158:159]\n\t" \
  "v_cvt_pk_bf16_f32 v116, v104, v105\n\t" \
  "v_cvt_pk_bf16_f32 v117, v106, v107\n\t" \
  "v_cvt_pk_bf16_f32 v118, v108, v109\n\t" \
  "v_cvt_pk_bf16_f32 v119, v110, v111\n\t" \
  "buffer_load_dwordx4 v213, s[4:7], s96 offen offset:1024 lds\n\t" \
  "ds_read_b128 v[144:147], v208 offset:640\n\t" \
  "ds_read_b128 v[148:151], v208 offset:656\n\t" \
  "ds_read_b128 v[152:155], v208 offset:896\n\t" \
  "ds_read_b128 v[156:159], v208 offset:912\n\t" \
  FSTP("v_writelane_b32 v255, s94, 28\n\t") \
  FSTP("v_writelane_b32 v255, s92, 29\n\t") \
  FSTP("s_memrealtime s[94:95]\n\t") \
  FSTP("s_memtime s[92:93]\n\t") \
  "s_add_u32 m0, s77, 102400\n\t" \
  "s_xor_b32 s96, s87, 262144\n\t" \
  "s_add_u32 s96, s96, s88\n\t" \
  "v_lshlrev_b32 v176, 16, v24\n\t" \
  "v_and_b32 v177, s98, v24\n\t" \
  "v_lshlrev_b32 v178, 16, v25\n\t" \
  "v_and_b32 v179, s98, v25\n\t" \
  "v_lshlrev_b32 v180, 16, v26\n\t" \
  "v_and_b32 v181, s98, v26\n\t" \
  "v_lshlrev_b32 v182, 16, v27\n\t" \
  "v_and_b32 v183, s98, v27\n\t" \
  "v_pk_fma_f32 v[176:177], v[176:177], v[160:161], v[162:163] op_sel_hi:[1,0,0]\n\t" \
  "v_pk_fma_f32 v[178:179], v[178:179], v[160:161], v[162:163] op_sel_hi:[1,0,0]\n\t" \
  "v_pk_fma_f32 v[180:181], v[180:181], v[160:161], v[162:163] op_sel_hi:[1,0,0]\n\t" \
  "v_pk_fma_f32 v[182:183], v[182:183], v[160:161], v[162:163] op_sel_hi:[1,0,0]\n\t" \
  "s_waitcnt lgkmcnt(4)\n\t" \
  "v_pk_fma_f32 v[176:177], v[176:177], v[128:129], v[136:137]\n\t" \
  "v_pk_fma_f32 v[178:179], v[178:179], v[130:131], v[138:139]\n\t" \
  "v_pk_fma_f32 v[180:181], v[180:181], v[132:133], v[140:141]\n\t" \
  "v_pk_fma_f32 v[182:183], v[182:183], v[134:135], v[142:143]\n\t" \
  "v_cvt_pk_bf16_f32 v24, v176, v177\n\t" \
  "v_cvt_pk_bf16_f32 v25, v178, v179\n\t" \
  "v_cvt_pk_bf16_f32 v26, v180, v181\n\t" \
  "v_cvt_pk_bf16_f32 v27, v182, v183\n\t" \
  "buffer_load_dwordx4 v212, s[4:7], s96 offen lds\n\t" \
  "v_lshlrev_b32 v176, 16, v56\n\t" \
  "v_and_b32 v177, s98, v56\n\t" \
  "v_lshlrev_b32 v178, 16, v57\n\t" \
  "v_and_b32 v179, s98, v57\n\t" \
  "v_lshlrev_b32 v180, 16, v58\n\t" \
  "v_and_b32 v181, s98, v58\n\t" \
  "v_lshlrev_b32 v182, 16, v59\n\t" \
  "v_and_b32 v183, s98, v59\n\t" \
  "v_pk_fma_f32 v[176:177], v[176:177], v[164:165], v[166:167] op_sel_hi:[1,0,0]\n\t" \
  "v_pk_fma_f32 v[178:179], v[178:179], v[164:165], v[166:167] op_sel_hi:[1,0,0]\n\t" \
  "v_pk_fma_f32 v[180:181], v[180:181], v[164:165], v[166:167] op_sel_hi:[1,0,0]\n\t" \
  "v_pk_fma_f32 v[182:183], v[182:183], v[164:165], v[166:167] op_sel_hi:[1,0,0]\n\t" \
  "v_pk_fma_f32 v[176:177], v[176:177], v[128:129], v[136:137]\n\t" \
  "v_pk_fma_f32 v[178:179], v[178:179], v[130:131], v[138:139]\n\t" \
  "v_pk_fma_f32 v[180:181], v[180:181], v[132:133], v[140:141]\n\t" \
  "v_pk_fma_f32 v[182:183], v[182:183], v[134:135], v[142:143]\n\t" \
  "v_cvt_pk_bf16_f32 v56, v176, v177\n\t" \
  "v_cvt_pk_bf16_f32 v57, v178, v179\n\t" \
  "v_cvt_pk_bf16_f32 v58, v180, v181\n\t" \
  "v_cvt_pk_bf16_f32 v59, v182, v183\n\t" \
  "buffer_load_dwordx4 v213, s[4:7], s96 offen offset:1024 lds\n\t" \
  "s_add_u32 m0, s77, 104448\n\t" \
  "s_xor_b32 s96, s87, 393216\n\t" \
  "s_add_u32 s96, s96, s88\n\t" \
  "v_lshlrev_b32 v176, 16, v88\n\t" \
  "v_and_b32 v177, s98, v88\n\t" \
  "v_lshlrev_b32 v178, 16, v89\n\t" \
  "v_and_b32 v179, s98, v89\n\t" \
  "v_lshlrev_b32 v180, 16, v90\n\t" \
  "v_and_b32 v181, s98, v90\n\t" \
  "v_lshlrev_b32 v182, 16, v91\n\t" \
  "v_and_b32 v183, s98, v91\n\t" \
  "v_pk_fma_f32 v[176:177], v[176:177], v[168:169], v[170:171] op_sel_hi:[1,0,0]\n\t" \
  "v_pk_fma_f32 v[178:179], v[178:179], v[168:169], v[170:171] op_sel_hi:[1,0,0]\n\t" \
  "v_pk_fma_f32 v[180:181], v[180:181], v[168:169], v[170:171] op_sel_hi:[1,0,0]\n\t" \
  "v_pk_fma_f32 v[182:183], v[182:183], v[168:169], v[170:171] op_sel_hi:[1,0,0]\n\t" \
  "v_pk_fma_f32 v[176:177], v[176:177], v[128:129], v[136:137]\n\t" \
  "v_pk_fma_f32 v[178:179], v[178:179], v[130:131], v[138:139]\n\t" \
  "v_pk_fma_f32 v[180:181], v[180:181], v[132:133], v[140:141]\n\t" \
  "v_pk_fma_f32 v[182:183], v[182:183], v[134:135], v[142:143]\n\t" \
  "v_cvt_pk_bf16_f32 v88, v176, v177\n\t" \
  "v_cvt_pk_bf16_f32 v89, v178, v179\n\t" \
  "v_cvt_pk_bf16_f32 v90, v180, v181\n\t" \
  "v_cvt_pk_bf16_f32 v91, v182, v183\n\t" \
  "buffer_load_dwordx4 v212, s[4:7], s96 offen lds\n\t" \
  "v_lshlrev_b32 v176, 16, v120\n\t" \
  "v_and_b32 v177, s98, v120\n\t" \
  "v_lshlrev_b32 v178, 16, v121\n\t" \
  "v_and_b32 v179, s98, v121\n\t" \
  "v_lshlrev_b32 v180, 16, v122\n\t" \
  "v_and_b32 v181, s98, v122\n\t" \
  "v_lshlrev_b32 v182, 16, v123\n\t" \
  "v_and_b32 v183, s98, v123\n\t" \
  "v_pk_fma_f32 v[176:177], v[176:177], v[172:173], v[174:175] op_sel_hi:[1,0,0]\n\t" \
  "v_pk_fma_f32 v[178:179], v[178:179], v[172:173], v[174:175] op_sel_hi:[1,0,0]\n\t" \
  "v_pk_fma_f32 v[180:181], v[180:181], v[172:173], v[174:175] op_sel_hi:[1,0,0]\n\t" \
  "v_pk_fma_f32 v[182:183], v[182:183], v[172:173], v[174:175] op_sel_hi:[1,0,0]\n\t" \
  "v_pk_fma_f32 v[176:177], v[176:177], v[128:129], v[136:137]\n\t" \
  "v_pk_fma_f32 v[178:179], v[178:179], v[130:131], v[138:139]\n\t" \
  "v_pk_fma_f32 v[180:181], v[180:181], v[132:133], v[140:141]\n\t" \
  "v_pk_fma_f32 v[182:183], v[182:183], v[134:135], v[142:143]\n\t" \
  "v_cvt_pk_bf16_f32 v120, v176, v177\n\t" \
  "v_cvt_pk_bf16_f32 v121, v178, v179\n\t" \
  "v_cvt_pk_bf16_f32 v122, v180, v181\n\t" \
  "v_cvt_pk_bf16_f32 v123, v182, v183\n\t" \
  "buffer_load_dwordx4 v213, s[4:7], s96 offen offset:1024 lds\n\t" \
  "ds_read_b128 v[128:131], v208 offset:1024\n\t" \
  "ds_read_b128 v[132:135], v208 offset:1040\n\t" \
  "ds_read_b128 v[136:139], v208 offset:1280\n\t" \
  "ds_read_b128 v[140:143], v208 offset:1296\n\t" \
  FSTP("v_writelane_b32 v255, s94, 30\n\t") \
  FSTP("v_writelane_b32 v255, s92, 31\n\t") \
  FSTP("s_memrealtime s[94:95]\n\t") \
  FSTP("s_memtime s[92:93]\n\t") \
  "s_add_u32 m0, s76, 98304\n\t" \
  "v_lshlrev_b32 v176, 16, v28\n\t" \
  "v_and_b32 v177, s98, v28\n\t" \
  "v_lshlrev_b32 v178, 16, v29\n\t" \
  "v_and_b32 v179, s98, v29\n\t" \
  "v_lshlrev_b32 v180, 16, v30\n\t" \
  "v_and_b32 v181, s98, v30\n\t" \
  "v_lshlrev_b32 v182, 16, v31\n\t" \
  "v_and_b32 v183, s98, v31\n\t" \
  "v_pk_fma_f32 v[176:177], v[176:177], v[160:161], v[162:163] op_sel_hi:[1,0,0]\n\t" \
  "v_pk_fma_f32 v[178:179], v[178:179], v[160:161], v[162:163] op_sel_hi:[1,0,0]\n\t" \
  "v_pk_fma_f32 v[180:181], v[180:181], v[160:161], v[162:163] op_sel_hi:[1,0,0]\n\t" \
  "v_pk_fma_f32 v[182:183], v[182:183], v[160:161], v[162:163] op_sel_hi:[1,0,0]\n\t" \
  "s_waitcnt lgkmcnt(4)\n\t" \
  "v_pk_fma_f32 v[176:177], v[176:177], v[144:145], v[152:153]\n\t" \
  "v_pk_fma_f32 v[178:179], v[178:179], v[146:147], v[154:155]\n\t" \
  "v_pk_fma_f32 v[180:181], v[180:181], v[148:149], v[156:157]\n\t" \
  "v_pk_fma_f32 v[182:183], v[182:183], v[150:151], v[158:159]\n\t" \
  "v_cvt_pk_bf16_f32 v28, v176, v177\n\t" \
  "v_cvt_pk_bf16_f32 v29, v178, v179\n\t" \
  "v_cvt_pk_bf16_f32 v30, v180, v181\n\t" \
  "v_cvt_pk_bf16_f32 v31, v182, v183\n\t" \
  "buffer_load_dwordx4 v200, s[20:23], s80 offen offset:0 lds\n\t" \
  "ds_read_b64_tr_b16 a[128:129], v184 offset:0\n\t" \
  "ds_read_b64_tr_b16 a[130:131], v184 offset:1024\n\t" \
  "ds_read_b64_tr_b16 a[132:133], v185 offset:0\n\t" \
  "ds_read_b64_tr_b16 a[134:135], v185 offset:1024\n\t" \
  "v_lshlrev_b32 v176, 16, v60\n\t" \
  "v_and_b32 v177, s98, v60\n\t" \
  "v_lshlrev_b32 v178, 16, v61\n\t" \
  "v_and_b32 v179, s98, v61\n\t" \
  "v_lshlrev_b32 v180, 16, v62\n\t" \
  "v_and_b32 v181, s98, v62\n\t" \
  "v_lshlrev_b32 v182, 16, v63\n\t" \
  "v_and_b32 v183, s98, v63\n\t" \
  "v_pk_fma_f32 v[176:177], v[176:177], v[164:165], v[166:167] op_sel_hi:[1,0,0]\n\t" \
  "v_pk_fma_f32 v[178:179], v[178:179], v[164:165], v[166:167] op_sel_hi:[1,0,0]\n\t" \
  "v_pk_fma_f32 v[180:181], v[180:181], v[164:165], v[166:167] op_sel_hi:[1,0,0]\n\t" \
  "v_pk_fma_f32 v[182:183], v[182:183], v[164:165], v[166:167] op_sel_hi:[1,0,0]\n\t" \
  "v_pk_fma_f32 v[176:177], v[176:177], v[144:145], v[152:153]\n\t" \
  "v_pk_fma_f32 v[178:179], v[178:179], v[146:147], v[154:155]\n\t" \
  "v_pk_fma_f32 v[180:181], v[180:181], v[148:149], v[156:157]\n\t" \
  "v_pk_fma_f32 v[182:183], v[182:183], v[150:151], v[158:159]\n\t" \
  "v_cvt_pk_bf16_f32 v60, v176, v177\n\t" \
  "v_cvt_pk_bf16_f32 v61, v178, v179\n\t" \
  "v_cvt_pk_bf16_f32 v62, v180, v181\n\t" \
  "v_cvt_pk_bf16_f32 v63, v182, v183\n\t" \
  "buffer_load_dwordx4 v201, s[20:23], s80 offen offset:1024 lds\n\t" \
  "ds_read_b64_tr_b16 a[136:137], v186 offset:0\n\t" \
  "ds_read_b64_tr_b16 a[138:139], v186 offset:1024\n\t" \
  "ds_read_b64_tr_b16 a[140:141], v187 offset:0\n\t" \
  "ds_read_b64_tr_b16 a[142:143], v187 offset:1024\n\t" \
  "v_lshlrev_b32 v176, 16, v92\n\t" \
  "v_and_b32 v177, s98, v92\n\t" \
  "v_lshlrev_b32 v178, 16, v93\n\t" \
  "v_and_b32 v179, s98, v93\n\t" \
  "v_lshlrev_b32 v180, 16, v94\n\t" \
  "v_and_b32 v181, s98, v94\n\t" \
  "v_lshlrev_b32 v182, 16, v95\n\t" \
  "v_and_b32 v183, s98, v95\n\t" \
  "v_pk_fma_f32 v[176:177], v[176:177], v[168:169], v[170:171] op_sel_hi:[1,0,0]\n\t" \
  "v_pk_fma_f32 v[178:179], v[178:179], v[168:169], v[170:171] op_sel_hi:[1,0,0]\n\t" \
  "v_pk_fma_f32 v[180:181], v[180:181], v[168:169], v[170:171] op_sel_hi:[1,0,0]\n\t" \
  "v_pk_fma_f32 v[182:183], v[182:183], v[168:169], v[170:171] op_sel_hi:[1,0,0]\n\t" \
  "v_pk_fma_f32 v[176:177], v[176:177], v[144:145], v[152:153]\n\t" \
  "v_pk_fma_f32 v[178:179], v[178:179], v[146:147], v[154:155]\n\t" \
  "v_pk_fma_f32 v[180:181], v[180:181], v[148:149], v[156:157]\n\t" \
  "v_pk_fma_f32 v[182:183], v[182:183], v[150:151], v[158:159]\n\t" \
  "v_cvt_pk_bf16_f32 v92, v176, v177\n\t" \
  "v_cvt_pk_bf16_f32 v93, v178, v179\n\t" \
  "v_cvt_pk_bf16_f32 v94, v180, v181\n\t" \
  "v_cvt_pk_bf16_f32 v95, v182, v183\n\t" \
  "buffer_load_dwordx4 v202, s[20:23], s80 offen offset:2048 lds\n\t" \
  "ds_read_b64_tr_b16 a[144:145], v188 offset:0\n\t" \
  "ds_read_b64_tr_b16 a[146:147], v188 offset:1024\n\t" \
  "ds_read_b64_tr_b16 a[148:149], v189 offset:0\n\t" \
  "ds_read_b64_tr_b16 a[150:151], v189 offset:1024\n\t" \
  "v_lshlrev_b32 v176, 16, v124\n\t" \
  "v_and_b32 v177, s98, v124\n\t" \
  "v_lshlrev_b32 v178, 16, v125\n\t" \
  "v_and_b32 v179, s98, v125\n\t" \
  "v_lshlrev_b32 v180, 16, v126\n\t" \
  "v_and_b32 v181, s98, v126\n\t" \
  "v_lshlrev_b32 v182, 16, v127\n\t" \
  "v_and_b32 v183, s98, v127\n\t" \
  "v_pk_fma_f32 v[176:177], v[176:177], v[172:173], v[174:175] op_sel_hi:[1,0,0]\n\t" \
  "v_pk_fma_f32 v[178:179], v[178:179], v[172:173], v[174:175] op_sel_hi:[1,0,0]\n\t" \
  "v_pk_fma_f32 v[180:181], v[180:181], v[172:173], v[174:175] op_sel_hi:[1,0,0]\n\t" \
  "v_pk_fma_f32 v[182:183], v[182:183], v[172:173], v[174:175] op_sel_hi:[1,0,0]\n\t" \
  "v_pk_fma_f32 v[176:177], v[176:177], v[144:145], v[152:153]\n\t" \
  "v_pk_fma_f32 v[178:179], v[178:179], v[146:147], v[154:155]\n\t" \
  "v_pk_fma_f32 v[180:181], v[180:181], v[148:149], v[156:157]\n\t" \
  "v_pk_fma_f32 v[182:183], v[182:183], v[150:151], v[158:159]\n\t" \
  "v_cvt_pk_bf16_f32 v124, v176, v177\n\t" \
  "v_cvt_pk_bf16_f32 v125, v178, v179\n\t" \
  "v_cvt_pk_bf16_f32 v126, v180, v181\n\t" \
  "v_cvt_pk_bf16_f32 v127, v182, v183\n\t" \
  "buffer_load_dwordx4 v203, s[20:23], s80 offen offset:3072 lds\n\t" \
  "ds_read_b64_tr_b16 a[152:153], v190 offset:0\n\t" \
  "ds_read_b64_tr_b16 a[154:155], v190 offset:1024\n\t" \
  "ds_read_b64_tr_b16 a[156:157], v191 offset:0\n\t" \
  "ds_read_b64_tr_b16 a[158:159], v191 offset:1024\n\t" \
  "ds_read_b128 v[144:147], v208 offset:1152\n\t" \
  "ds_read_b128 v[148:151], v208 offset:1168\n\t" \
  "ds_read_b128 v[152:155], v208 offset:1408\n\t" \
  "ds_read_b128 v[156:159], v208 offset:1424\n\t" \
  FSTP("v_writelane_b32 v255, s94, 32\n\t") \
  FSTP("v_writelane_b32 v255, s92, 33\n\t") \
  FSTP("s_memrealtime s[94:95]\n\t") \
  FSTP("s_memtime s[92:93]\n\t") \
  "s_waitcnt vmcnt(12)\n\t" \
  "s_mov_b64 exec, 1\n\t" \
  "buffer_store_dword v211, off, s[16:19], s85 sc1\n\t" \
  "s_mov_b64 exec, -1\n\t" \
  FSTP("v_writelane_b32 v255, s94, 34\n\t") \
  FSTP("v_writelane_b32 v255, s92, 35\n\t") \
  FSTP("s_memrealtime s[94:95]\n\t") \
  FSTP("s_memtime s[92:93]\n\t") \
  "s_waitcnt lgkmcnt(4)\n\t" \
  "v_mfma_f32_16x16x32_bf16 v[0:3], a[128:131], v[16:19], 0\n\t" \
  "s_add_u32 m0, s77, 0\n\t" \
  "s_xor_b32 s96, s87, 0\n\t" \
  "s_add_u32 s96, s96, s89\n\t" \
  "v_mfma_f32_16x16x32_bf16 v[32:35], a[128:131], v[48:51], 0\n\t" \
  "buffer_load_dwordx4 v212, s[4:7], s96 offen lds\n\t" \
  "v_mfma_f32_16x16x32_bf16 v[64:67], a[128:131], v[80:83], 0\n\t" \
  "ds_read_b64_tr_b16 a[160:161], v184 offset:8192\n\t" \
  "v_mfma_f32_16x16x32_bf16 v[96:99], a[128:131], v[112:115], 0\n\t" \
  "ds_read_b64_tr_b16 a[162:163], v184 offset:9216\n\t" \
  "v_mfma_f32_16x16x32_bf16 v[100:103], a[132:135], v[112:115], 0\n\t" \
  "buffer_load_dwordx4 v213, s[4:7], s96 offen offset:1024 lds\n\t" \
  "v_mfma_f32_16x16x32_bf16 v[68:71], a[132:135], v[80:83], 0\n\t" \
  "s_add_u32 m0, s77, 2048\n\t" \
  "ds_read_b64_tr_b16 a[164:165], v185 offset:8192\n\t" \
  "s_xor_b32 s96, s87, 131072\n\t" \
  "v_mfma_f32_16x16x32_bf16 v[36:39], a[132:135], v[48:51], 0\n\t" \
  "ds_read_b64_tr_b16 a[166:167], v185 offset:9216\n\t" \
  "s_add_u32 s96, s96, s89\n\t" \
  "v_mfma_f32_16x16x32_bf16 v[4:7], a[132:135], v[16:19], 0\n\t" \
  "ds_read_b64_tr_b16 a[168:169], v186 offset:8192\n\t" \
  "v_mfma_f32_16x16x32_bf16 v[8:11], a[136:139], v[16:19], 0\n\t" \
  "buffer_load_dwordx4 v212, s[4:7], s96 offen lds\n\t" \
  "v_mfma_f32_16x16x32_bf16 v[40:43], a[136:139], v[48:51], 0\n\t" \
  "ds_read_b64_tr_b16 a[170:171], v186 offset:9216\n\t" \
  "v_mfma_f32_16x16x32_bf16 v[72:75], a[136:139], v[80:83], 0\n\t" \
  "ds_read_b64_tr_b16 a[172:173], v187 offset:8192\n\t" \
  "v_mfma_f32_16x16x32_bf16 v[104:107], a[136:139], v[112:115], 0\n\t" \
  "buffer_load_dwordx4 v213, s[4:7], s96 offen offset:1024 lds\n\t" \
  "v_mfma_f32_16x16x32_bf16 v[108:111], a[140:143], v[112:115], 0\n\t" \
  "s_add_u32 m0, s77, 4096\n\t" \
  "ds_read_b64_tr_b16 a[174:175], v187 offset:9216\n\t" \
  "s_xor_b32 s96, s87, 262144\n\t" \
  "v_mfma_f32_16x16x32_bf16 v[76:79], a[140:143], v[80:83], 0\n\t" \
  "ds_read_b64_tr_b16 a[176:177], v188 offset:8192\n\t" \
  "s_add_u32 s96, s96, s89\n\t" \
  "v_mfma_f32_16x16x32_bf16 v[44:47], a[140:143], v[48:51], 0\n\t" \
  "buffer_load_dwordx4 v212, s[4:7], s96 offen lds\n\t" \
  "v_mfma_f32_16x16x32_bf16 v[12:15], a[140:143], v[16:19], 0\n\t" \
  "ds_read_b64_tr_b16 a[178:179], v188 offset:9216\n\t" \
  "v_mfma_f32_16x16x32_bf16 a[64:67], a[144:147], v[16:19], 0\n\t" \
  "ds_read_b64_tr_b16 a[180:181], v189 offset:8192\n\t" \
  "v_mfma_f32_16x16x32_bf16 a[68:71], a[144:147], v[48:51], 0\n\t" \
  "buffer_load_dwordx4 v213, s[4:7], s96 offen offset:1024 lds\n\t" \
  "v_mfma_f32_16x16x32_bf16 a[72:75], a[144:147], v[80:83], 0\n\t" \
  "s_add_u32 m0, s77, 6144\n\t" \
  "ds_read_b64_tr_b16 a[182:183], v189 offset:9216\n\t" \
  "s_xor_b32 s96, s87, 393216\n\t" \
  "v_mfma_f32_16x16x32_bf16 a[76:79], a[144:147], v[112:115], 0\n\t" \
  "ds_read_b64_tr_b16 a[184:185], v190 offset:8192\n\t" \
  "s_add_u32 s96, s96, s89\n\t" \
  "v_mfma_f32_16x16x32_bf16 a[92:95], a[148:151], v[112:115], 0\n\t" \
  "ds_read_b64_tr_b16 a[186:187], v190 offset:9216\n\t" \
  "v_mfma_f32_16x16x32_bf16 a[88:91], a[148:151], v[80:83], 0\n\t" \
  "buffer_load_dwordx4 v212, s[4:7], s96 offen lds\n\t" \
  "v_mfma_f32_16x16x32_bf16 a[84:87], a[148:151], v[48:51], 0\n\t" \
  "ds_read_b64_tr_b16 a[188:189], v191 offset:8192\n\t" \
  "v_mfma_f32_16x16x32_bf16 a[80:83], a[148:151], v[16:19], 0\n\t" \
  "ds_read_b64_tr_b16 a[190:191], v191 offset:9216\n\t" \
  "v_mfma_f32_16x16x32_bf16 a[96:99], a[152:155], v[16:19], 0\n\t" \
  "v_mfma_f32_16x16x32_bf16 a[100:103], a[152:155], v[48:51], 0\n\t" \
  "buffer_load_dwordx4 v213, s[4:7], s96 offen offset:1024 lds\n\t" \
  "v_mfma_f32_16x16x32_bf16 a[104:107], a[152:155], v[80:83], 0\n\t" \
  "v_mfma_f32_16x16x32_bf16 a[108:111], a[152:155], v[112:115], 0\n\t" \
  "v_mfma_f32_16x16x32_bf16 a[124:127], a[156:159], v[112:115], 0\n\t" \
  "v_mfma_f32_16x16x32_bf16 a[120:123], a[156:159], v[80:83], 0\n\t" \
  "s_add_u32 m0, s76, 0\n\t" \
  "v_mfma_f32_16x16x32_bf16 a[116:119], a[156:159], v[48:51], 0\n\t" \
  "s_waitcnt lgkmcnt(0)\n\t" \
  "v_mfma_f32_16x16x32_bf16 a[112:115], a[156:159], v[16:19], 0\n\t" \
  "s_waitcnt vmcnt(13)\n\t" \
  "ds_read_b128 v[16:19], v206 offset:0\n\t" \
  "ds_read_b128 v[48:51], v206 offset:2048\n\t" \
  "s_barrier\n\t" \
  FSTP("v_writelane_b32 v255, s94, 36\n\t") \
  FSTP("v_writelane_b32 v255, s92, 37\n\t") \
  FSTP("s_memrealtime s[94:95]\n\t") \
  FSTP("s_memtime s[92:93]\n\t") \
  "v_mfma_f32_16x16x32_bf16 v[0:3], a[160:163], v[20:23], v[0:3]\n\t" \
  "buffer_load_dwordx4 v200, s[20:23], s81 offen offset:0 lds\n\t" \
  "ds_read_b128 v[80:83], v206 offset:4096\n\t" \
  "ds_read_b128 v[112:115], v206 offset:6144\n\t" \
  "v_mfma_f32_16x16x32_bf16 v[32:35], a[160:163], v[52:55], v[32:35]\n\t" \
  "ds_read_b64_tr_b16 a[128:129], v184 offset:49152\n\t" \
  "v_mfma_f32_16x16x32_bf16 v[64:67], a[160:163], v[84:87], v[64:67]\n\t" \
  "ds_read_b64_tr_b16 a[130:131], v184 offset:50176\n\t" \
  "v_mfma_f32_16x16x32_bf16 v[96:99], a[160:163], v[116:119], v[96:99]\n\t" \
  "buffer_load_dwordx4 v201, s[20:23], s81 offen offset:1024 lds\n\t" \
  "v_mfma_f32_16x16x32_bf16 v[100:103], a[164:167], v[116:119], v[100:103]\n\t" \
  "ds_read_b64_tr_b16 a[132:133], v185 offset:49152\n\t" \
  "v_mfma_f32_16x16x32_bf16 v[68:71], a[164:167], v[84:87], v[68:71]\n\t" \
  "s_waitcnt lgkmcnt(3)\n\t" \
  "v_lshlrev_b32 v176, 16, v16\n\t" \
  "v_and_b32 v177, s98, v16\n\t" \
  "v_mfma_f32_16x16x32_bf16 v[36:39], a[164:167], v[52:55], v[36:39]\n\t" \
  "ds_read_b64_tr_b16 a[134:135], v185 offset:50176\n\t" \
  "v_lshlrev_b32 v178, 16, v17\n\t" \
  "v_mfma_f32_16x16x32_bf16 v[4:7], a[164:167], v[20:23], v[4:7]\n\t" \
  "v_and_b32 v179, s98, v17\n\t" \
  "v_lshlrev_b32 v180, 16, v18\n\t" \
  "v_mfma_f32_16x16x32_bf16 v[8:11], a[168:171], v[20:23], v[8:11]\n\t" \
  "buffer_load_dwordx4 v202, s[20:23], s81 offen offset:2048 lds\n\t" \
  "v_mfma_f32_16x16x32_bf16 v[40:43], a[168:171], v[52:55], v[40:43]\n\t" \
  "ds_read_b64_tr_b16 a[136:137], v186 offset:49152\n\t" \
  "v_and_b32 v181, s98, v18\n\t" \
  "v_mfma_f32_16x16x32_bf16 v[72:75], a[168:171], v[84:87], v[72:75]\n\t" \
  "ds_read_b64_tr_b16 a[138:139], v186 offset:50176\n\t" \
  "v_lshlrev_b32 v182, 16, v19\n\t" \
  "v_mfma_f32_16x16x32_bf16 v[104:107], a[168:171], v[116:119], v[104:107]\n\t" \
  "ds_read_b64_tr_b16 a[140:141], v187 offset:49152\n\t" \
  "v_and_b32 v183, s98, v19\n\t" \
  "v_mfma_f32_16x16x32_bf16 v[108:111], a[172:175], v[116:119], v[108:111]\n\t" \
  "buffer_load_dwordx4 v203, s[20:23], s81 offen offset:3072 lds\n\t" \
  "v_mfma_f32_16x16x32_bf16 v[76:79], a[172:175], v[84:87], v[76:79]\n\t" \
  "s_add_u32 s96, s78, 2097152\n\t" \
  "s_and_b32 s96, s96, 0x780000\n\t" \
  "s_add_u32 m0, s76, 16384\n\t" \
  "v_mfma_f32_16x16x32_bf16 v[44:47], a[172:175], v[52:55], v[44:47]\n\t" \
  "ds_read_b64_tr_b16 a[142:143], v187 offset:50176\n\t" \
  "v_lshlrev_b32 v220, 16, v48\n\t" \
  "v_and_b32 v221, s98, v48\n\t" \
  "v_mfma_f32_16x16x32_bf16 v[12:15], a[172:175], v[20:23], v[12:15]\n\t" \
  "buffer_load_dwordx4 v200, s[20:23], s96 offen offset:0 lds\n\t" \
  "v_mfma_f32_16x16x32_bf16 a[64:67], a[176:179], v[20:23], a[64:67]\n\t" \
  "ds_read_b64_tr_b16 a[144:145], v188 offset:49152\n\t" \
  "v_lshlrev_b32 v222, 16, v49\n\t" \
  "v_mfma_f32_16x16x32_bf16 a[68:71], a[176:179], v[52:55], a[68:71]\n\t" \
  "ds_read_b64_tr_b16 a[146:147], v188 offset:50176\n\t" \
  "v_and_b32 v223, s98, v49\n\t" \
  "v_mfma_f32_16x16x32_bf16 a[72:75], a[176:179], v[84:87], a[72:75]\n\t" \
  "ds_read_b64_tr_b16 a[148:149], v189 offset:49152\n\t" \
  "v_lshlrev_b32 v224, 16, v50\n\t" \
  "v_mfma_f32_16x16x32_bf16 a[76:79], a[176:179], v[116:119], a[76:79]\n\t" \
  "buffer_load_dwordx4 v201, s[20:23], s96 offen offset:1024 lds\n\t" \
  "v_mfma_f32_16x16x32_bf16 a[92:95], a[180:183], v[116:119], a[92:95]\n\t" \
  "ds_read_b64_tr_b16 a[150:151], v189 offset:50176\n\t" \
  "v_and_b32 v225, s98, v50\n\t" \
  "v_mfma_f32_16x16x32_bf16 a[88:91], a[180:183], v[84:87], a[88:91]\n\t" \
  "ds_read_b64_tr_b16 a[152:153], v190 offset:49152\n\t" \
  "v_lshlrev_b32 v226, 16, v51\n\t" \
  "v_mfma_f32_16x16x32_bf16 a[84:87], a[180:183], v[52:55], a[84:87]\n\t" \
  "ds_read_b64_tr_b16 a[154:155], v190 offset:50176\n\t" \
  "v_and_b32 v227, s98, v51\n\t" \
  "v_mfma_f32_16x16x32_bf16 a[80:83], a[180:183], v[20:23], a[80:83]\n\t" \
  "buffer_load_dwordx4 v202, s[20:23], s96 offen offset:2048 lds\n\t" \
  "ds_read_b64_tr_b16 a[156:157], v191 offset:49152\n\t" \
  "ds_read_b64_tr_b16 a[158:159], v191 offset:50176\n\t" \
  "v_lshlrev_b32 v228, 16, v80\n\t" \
  "v_pk_fma_f32 v[176:177], v[176:177], v[160:161], v[162:163] op_sel_hi:[1,0,0]\n\t" \
  "v_pk_fma_f32 v[178:179], v[178:179], v[160:161], v[162:163] op_sel_hi:[1,0,0]\n\t" \
  "v_pk_fma_f32 v[180:181], v[180:181], v[160:161], v[162:163] op_sel_hi:[1,0,0]\n\t" \
  "v_pk_fma_f32 v[182:183], v[182:183], v[160:161], v[162:163] op_sel_hi:[1,0,0]\n\t" \
  "v_pk_fma_f32 v[176:177], v[176:177], v[128:129], v[136:137]\n\t" \
  "v_pk_fma_f32 v[178:179], v[178:179], v[130:131], v[138:139]\n\t" \
  "v_pk_fma_f32 v[180:181], v[180:181], v[132:133], v[140:141]\n\t" \
  "v_pk_fma_f32 v[182:183], v[182:183], v[134:135], v[142:143]\n\t" \
  "v_pk_fma_f32 v[220:221], v[220:221], v[164:165], v[166:167] op_sel_hi:[1,0,0]\n\t" \
  "v_pk_fma_f32 v[222:223], v[222:223], v[164:165], v[166:167] op_sel_hi:[1,0,0]\n\t" \
  "v_pk_fma_f32 v[224:225], v[224:225], v[164:165], v[166:167] op_sel_hi:[1,0,0]\n\t" \
  "v_pk_fma_f32 v[226:227], v[226:227], v[164:165], v[166:167] op_sel_hi:[1,0,0]\n\t" \
  "v_pk_fma_f32 v[220:221], v[220:221], v[128:129], v[136:137]\n\t" \
  "v_pk_fma_f32 v[222:223], v[222:223], v[130:131], v[138:139]\n\t" \
  "v_pk_fma_f32 v[224:225], v[224:225], v[132:133], v[140:141]\n\t" \
  "v_pk_fma_f32 v[226:227], v[226:227], v[134:135], v[142:143]\n\t" \
  "v_cvt_pk_bf16_f32 v16, v176, v177\n\t" \
  "v_mfma_f32_16x16x32_bf16 a[96:99], a[184:187], v[20:23], a[96:99]\n\t" \
  "v_cvt_pk_bf16_f32 v17, v178, v179\n\t" \
  "v_cvt_pk_bf16_f32 v18, v180, v181\n\t" \
  "v_and_b32 v229, s98, v80\n\t" \
  "v_mfma_f32_16x16x32_bf16 a[100:103], a[184:187], v[52:55], a[100:103]\n\t" \
  "v_cvt_pk_bf16_f32 v19, v182, v183\n\t" \
  "v_cvt_pk_bf16_f32 v48, v220, v221\n\t" \
  "v_lshlrev_b32 v230, 16, v81\n\t" \
  "v_and_b32 v231, s98, v81\n\t" \
  "v_mfma_f32_16x16x32_bf16 a[104:107], a[184:187], v[84:87], a[104:107]\n\t" \
  "v_lshlrev_b32 v232, 16, v82\n\t" \
  "v_and_b32 v233, s98, v82\n\t" \
  "v_mfma_f32_16x16x32_bf16 a[108:111], a[184:187], v[116:119], a[108:111]\n\t" \
  "buffer_load_dwordx4 v203, s[20:23], s96 offen offset:3072 lds\n\t" \
  "v_mfma_f32_16x16x32_bf16 a[124:127], a[188:191], v[116:119], a[124:127]\n\t" \
  "v_lshlrev_b32 v234, 16, v83\n\t" \
  "v_and_b32 v235, s98, v83\n\t" \
  "v_mfma_f32_16x16x32_bf16 a[120:123], a[188:191], v[84:87], a[120:123]\n\t" \
  "s_add_u32 s96, s78, 2621440\n\t" \
  "v_lshlrev_b32 v236, 16, v112\n\t" \
  "v_and_b32 v237, s98, v112\n\t" \
  "v_mfma_f32_16x16x32_bf16 a[116:119], a[188:191], v[52:55], a[116:119]\n\t" \
  "s_and_b32 s96, s96, 0x780000\n\t" \
  "s_add_u32 m0, s76, 32768\n\t" \
  "v_lshlrev_b32 v238, 16, v113\n\t" \
  "v_mfma_f32_16x16x32_bf16 a[112:115], a[188:191], v[20:23], a[112:115]\n\t" \
  "s_waitcnt lgkmcnt(0)\n\t" \
  "ds_read_b128 v[20:23], v207 offset:0\n\t" \
  "ds_read_b128 v[52:55], v207 offset:2048\n\t" \
  "ds_read_b128 v[84:87], v207 offset:4096\n\t" \
  "v_and_b32 v239, s98, v113\n\t" \
  FSTP("v_writelane_b32 v255, s94, 38\n\t") \
  FSTP("v_writelane_b32 v255, s92, 39\n\t") \
  FSTP("s_memrealtime s[94:95]\n\t") \
  FSTP("s_memtime s[92:93]\n\t") \
  "v_mfma_f32_16x16x32_bf16 v[0:3], a[128:131], v[24:27], v[0:3]\n\t" \
  "buffer_load_dwordx4 v200, s[20:23], s96 offen offset:0 lds\n\t" \
  "ds_read_b128 v[116:119], v207 offset:6144\n\t" \
  "v_mfma_f32_16x16x32_bf16 v[32:35], a[128:131], v[56:59], v[32:35]\n\t" \
  "v_lshlrev_b32 v240, 16, v114\n\t" \
  "v_and_b32 v241, s98, v114\n\t" \
  "v_mfma_f32_16x16x32_bf16 v[64:67], a[128:131], v[88:91], v[64:67]\n\t" \
  "v_lshlrev_b32 v242, 16, v115\n\t" \
  "v_and_b32 v243, s98, v115\n\t" \
  "v_mfma_f32_16x16x32_bf16 v[96:99], a[128:131], v[120:123], v[96:99]\n\t" \
  "ds_read_b64_tr_b16 a[160:161], v184 offset:57344\n\t" \
  "ds_read_b64_tr_b16 a[162:163], v184 offset:58368\n\t" \
  "v_cvt_pk_bf16_f32 v49, v222, v223\n\t" \
  "v_cvt_pk_bf16_f32 v50, v224, v225\n\t" \
  "v_cvt_pk_bf16_f32 v51, v226, v227\n\t" \
  "v_pk_fma_f32 v[228:229], v[228:229], v[168:169], v[170:171] op_sel_hi:[1,0,0]\n\t" \
  "v_pk_fma_f32 v[230:231], v[230:231], v[168:169], v[170:171] op_sel_hi:[1,0,0]\n\t" \
  "v_pk_fma_f32 v[232:233], v[232:233], v[168:169], v[170:171] op_sel_hi:[1,0,0]\n\t" \
  "v_pk_fma_f32 v[234:235], v[234:235], v[168:169], v[170:171] op_sel_hi:[1,0,0]\n\t" \
  "v_pk_fma_f32 v[228:229], v[228:229], v[128:129], v[136:137]\n\t" \
  "v_pk_fma_f32 v[230:231], v[230:231], v[130:131], v[138:139]\n\t" \
  "v_pk_fma_f32 v[232:233], v[232:233], v[132:133], v[140:141]\n\t" \
  "v_pk_fma_f32 v[234:235], v[234:235], v[134:135], v[142:143]\n\t" \
  "v_pk_fma_f32 v[236:237], v[236:237], v[172:173], v[174:175] op_sel_hi:[1,0,0]\n\t" \
  "v_pk_fma_f32 v[238:239], v[238:239], v[172:173], v[174:175] op_sel_hi:[1,0,0]\n\t" \
  "v_pk_fma_f32 v[240:241], v[240:241], v[172:173], v[174:175] op_sel_hi:[1,0,0]\n\t" \
  "v_pk_fma_f32 v[242:243], v[242:243], v[172:173], v[174:175] op_sel_hi:[1,0,0]\n\t" \
  "v_pk_fma_f32 v[236:237], v[236:237], v[128:129], v[136:137]\n\t" \
  "v_pk_fma_f32 v[238:239], v[238:239], v[130:131], v[138:139]\n\t" \
  "v_pk_fma_f32 v[240:241], v[240:241], v[132:133], v[140:141]\n\t" \
  "v_pk_fma_f32 v[242:243], v[242:243], v[134:135], v[142:143]\n\t" \
  "v_cvt_pk_bf16_f32 v80, v228, v229\n\t" \
  "v_mfma_f32_16x16x32_bf16 v[100:103], a[132:135], v[120:123], v[100:103]\n\t" \
  "ds_read_b64_tr_b16 a[164:165], v185 offset:57344\n\t" \
  "v_cvt_pk_bf16_f32 v81, v230, v231\n\t" \
  "v_mfma_f32_16x16x32_bf16 v[68:71], a[132:135], v[88:91], v[68:71]\n\t" \
  "s_waitcnt lgkmcnt(3)\n\t" \
  "v_lshlrev_b32 v176, 16, v20\n\t" \
  "v_and_b32 v177, s98, v20\n\t" \
  "v_mfma_f32_16x16x32_bf16 v[36:39], a[132:135], v[56:59], v[36:39]\n\t" \
  "buffer_load_dwordx4 v201, s[20:23], s96 offen offset:1024 lds\n\t" \
  "v_mfma_f32_16x16x32_bf16 v[4:7], a[132:135], v[24:27], v[4:7]\n\t" \
  "ds_read_b64_tr_b16 a[166:167], v185 offset:58368\n\t" \
  "v_lshlrev_b32 v178, 16, v21\n\t" \
  "v_mfma_f32_16x16x32_bf16 v[8:11], a[136:139], v[24:27], v[8:11]\n\t" \
  "ds_read_b64_tr_b16 a[168:169], v186 offset:57344\n\t" \
  "v_and_b32 v179, s98, v21\n\t" \
  "v_mfma_f32_16x16x32_bf16 v[40:43], a[136:139], v[56:59], v[40:43]\n\t" \
  "v_lshlrev_b32 v180, 16, v22\n\t" \
  "v_and_b32 v181, s98, v22\n\t" \
  "v_mfma_f32_16x16x32_bf16 v[72:75], a[136:139], v[88:91], v[72:75]\n\t" \
  "ds_read_b64_tr_b16 a[170:171], v186 offset:58368\n\t" \
  "v_lshlrev_b32 v182, 16, v23\n\t" \
  "v_mfma_f32_16x16x32_bf16 v[104:107], a[136:139], v[120:123], v[104:107]\n\t" \
  "ds_read_b64_tr_b16 a[172:173], v187 offset:57344\n\t" \
  "v_and_b32 v183, s98, v23\n\t" \
  "v_mfma_f32_16x16x32_bf16 v[108:111], a[140:143], v[120:123], v[108:111]\n\t" \
  "buffer_load_dwordx4 v202, s[20:23], s96 offen offset:2048 lds\n\t" \
  "v_mfma_f32_16x16x32_bf16 v[76:79], a[140:143], v[88:91], v[76:79]\n\t" \
  "ds_read_b64_tr_b16 a[174:175], v187 offset:58368\n\t" \
  "v_lshlrev_b32 v220, 16, v52\n\t" \
  "v_mfma_f32_16x16x32_bf16 v[44:47], a[140:143], v[56:59], v[44:47]\n\t" \
  "v_and_b32 v221, s98, v52\n\t" \
  "v_lshlrev_b32 v222, 16, v53\n\t" \
  "v_mfma_f32_16x16x32_bf16 v[12:15], a[140:143], v[24:27], v[12:15]\n\t" \
  "ds_read_b64_tr_b16 a[176:177], v188 offset:57344\n\t" \
  "v_and_b32 v223, s98, v53\n\t" \
  "v_mfma_f32_16x16x32_bf16 a[64:67], a[144:147], v[24:27], a[64:67]\n\t" \
  "ds_read_b64_tr_b16 a[178:179], v188 offset:58368\n\t" \
  "v_lshlrev_b32 v224, 16, v54\n\t" \
  "v_mfma_f32_16x16x32_bf16 a[68:71], a[144:147], v[56:59], a[68:71]\n\t" \
  "ds_read_b64_tr_b16 a[180:181], v189 offset:57344\n\t" \
  "v_and_b32 v225, s98, v54\n\t" \
  "v_mfma_f32_16x16x32_bf16 a[72:75], a[144:147], v[88:91], a[72:75]\n\t" \
  "buffer_load_dwordx4 v203, s[20:23], s96 offen offset:3072 lds\n\t" \
  "v_mfma_f32_16x16x32_bf16 a[76:79], a[144:147], v[120:123], a[76:79]\n\t" \
  "ds_read_b64_tr_b16 a[182:183], v189 offset:58368\n\t" \
  "v_cvt_pk_bf16_f32 v82, v232, v233\n\t" \
  "v_mfma_f32_16x16x32_bf16 a[92:95], a[148:151], v[120:123], a[92:95]\n\t" \
  "v_lshlrev_b32 v226, 16, v55\n\t" \
  "v_and_b32 v227, s98, v55\n\t" \
  "v_mfma_f32_16x16x32_bf16 a[88:91], a[148:151], v[88:91], a[88:91]\n\t" \
  "ds_read_b64_tr_b16 a[184:185], v190 offset:57344\n\t" \
  "v_cvt_pk_bf16_f32 v83, v234, v235\n\t" \
  "v_cvt_pk_bf16_f32 v112, v236, v237\n\t" \
  "v_cvt_pk_bf16_f32 v113, v238, v239\n\t" \
  "v_pk_fma_f32 v[176:177], v[176:177], v[160:161], v[162:163] op_sel_hi:[1,0,0]\n\t" \
  "v_pk_fma_f32 v[178:179], v[178:179], v[160:161], v[162:163] op_sel_hi:[1,0,0]\n\t" \
  "v_pk_fma_f32 v[180:181], v[180:181], v[160:161], v[162:163] op_sel_hi:[1,0,0]\n\t" \
  "v_pk_fma_f32 v[182:183], v[182:183], v[160:161], v[162:163] op_sel_hi:[1,0,0]\n\t" \
  "v_pk_fma_f32 v[176:177], v[176:177], v[144:145], v[152:153]\n\t" \
  "v_pk_fma_f32 v[178:179], v[178:179], v[146:147], v[154:155]\n\t" \
  "v_pk_fma_f32 v[180:181], v[180:181], v[148:149], v[156:157]\n\t" \
  "v_pk_fma_f32 v[182:183], v[182:183], v[150:151], v[158:159]\n\t" \
  "v_pk_fma_f32 v[220:221], v[220:221], v[164:165], v[166:167] op_sel_hi:[1,0,0]\n\t" \
  "v_pk_fma_f32 v[222:223], v[222:223], v[164:165], v[166:167] op_sel_hi:[1,0,0]\n\t" \
  "v_pk_fma_f32 v[224:225], v[224:225], v[164:165], v[166:167] op_sel_hi:[1,0,0]\n\t" \
  "v_pk_fma_f32 v[226:227], v[226:227], v[164:165], v[166:167] op_sel_hi:[1,0,0]\n\t" \
  "v_pk_fma_f32 v[220:221], v[220:221], v[144:145], v[152:153]\n\t" \
  "v_pk_fma_f32 v[222:223], v[222:223], v[146:147], v[154:155]\n\t" \
  "v_pk_fma_f32 v[224:225], v[224:225], v[148:149], v[156:157]\n\t" \
  "v_pk_fma_f32 v[226:227], v[226:227], v[150:151], v[158:159]\n\t" \
  "v_cvt_pk_bf16_f32 v20, v176, v177\n\t" \
  "v_mfma_f32_16x16x32_bf16 a[84:87], a[148:151], v[56:59], a[84:87]\n\t" \
  "ds_read_b64_tr_b16 a[186:187], v190 offset:58368\n\t" \
  "v_lshlrev_b32 v228, 16, v84\n\t" \
  "v_mfma_f32_16x16x32_bf16 a[80:83], a[148:151], v[24:27], a[80:83]\n\t" \
  "ds_read_b64_tr_b16 a[188:189], v191 offset:57344\n\t" \
  "v_and_b32 v229, s98, v84\n\t" \
  "v_mfma_f32_16x16x32_bf16 a[96:99], a[152:155], v[24:27], a[96:99]\n\t" \
  "global_load_dword v246, v217, s[12:13]\n\t" \
  "v_mfma_f32_16x16x32_bf16 a[100:103], a[152:155], v[56:59], a[100:103]\n\t" \
  "ds_read_b64_tr_b16 a[190:191], v191 offset:58368\n\t" \
  "v_lshlrev_b32 v230, 16, v85\n\t" \
  "v_mfma_f32_16x16x32_bf16 a[104:107], a[152:155], v[88:91], a[104:107]\n\t" \
  "v_and_b32 v231, s98, v85\n\t" \
  "v_lshlrev_b32 v232, 16, v86\n\t" \
  "v_mfma_f32_16x16x32_bf16 a[108:111], a[152:155], v[120:123], a[108:111]\n\t" \
  ".Lhs1:\n\ts_cbranch_execz .Lhs1n\n\t" \
  "v_and_b32 v233, s98, v86\n\t" \
  "v_lshlrev_b32 v234, 16, v87\n\t" \
  "v_mfma_f32_16x16x32_bf16 a[124:127], a[156:159], v[120:123], a[124:127]\n\t" \
  "v_cvt_pk_bf16_f32 v114, v240, v241\n\t" \
  "v_cvt_pk_bf16_f32 v115, v242, v243\n\t" \
  "v_mfma_f32_16x16x32_bf16 a[120:123], a[156:159], v[88:91], a[120:123]\n\t" \
  "s_add_u32 s96, s78, 3145728\n\t" \
  "v_and_b32 v235, s98, v87\n\t" \
  "v_lshlrev_b32 v236, 16, v116\n\t" \
  "v_mfma_f32_16x16x32_bf16 a[116:119], a[156:159], v[56:59], a[116:119]\n\t" \
  "s_waitcnt vmcnt(22) lgkmcnt(0)\n\t" \
  "s_and_b32 s96, s96, 0x780000\n\t" \
  "s_add_u32 m0, s76, 49152\n\t" \
  "v_mfma_f32_16x16x32_bf16 a[112:115], a[156:159], v[24:27], a[112:115]\n\t" \
  "s_barrier\n\t" \
  FSTP("v_writelane_b32 v255, s94, 40\n\t") \
  FSTP("v_writelane_b32 v255, s92, 41\n\t") \
  FSTP("s_memrealtime s[94:95]\n\t") \
  FSTP("s_memtime s[92:93]\n\t") \
  "v_mfma_f32_16x16x32_bf16 v[0:3], a[160:163], v[28:31], v[0:3]\n\t" \
  "buffer_load_dwordx4 v200, s[20:23], s96 offen offset:0 lds\n\t" \
  "ds_read_b64_tr_b16 a[128:129], v192 offset:32768\n\t" \
  "v_mfma_f32_16x16x32_bf16 v[32:35], a[160:163], v[60:63], v[32:35]\n\t" \
  "s_waitcnt vmcnt(14)\n\t" \
  "ds_read_b128 v[24:27], v204 offset:0\n\t" \
  "ds_read_b64_tr_b16 a[130:131], v192 offset:33792\n\t" \
  "v_and_b32 v237, s98, v116\n\t" \
  "v_mfma_f32_16x16x32_bf16 v[64:67], a[160:163], v[92:95], v[64:67]\n\t" \
  "ds_read_b128 v[56:59], v204 offset:2048\n\t" \
  "ds_read_b64_tr_b16 a[132:133], v193 offset:32768\n\t" \
  "v_cvt_pk_bf16_f32 v21, v178, v179\n\t" \
  "v_mfma_f32_16x16x32_bf16 v[96:99], a[160:163], v[124:127], v[96:99]\n\t" \
  "ds_read_b128 v[88:91], v204 offset:4096\n\t" \
  "v_lshlrev_b32 v238, 16, v117\n\t" \
  "v_mfma_f32_16x16x32_bf16 v[100:103], a[164:167], v[124:127], v[100:103]\n\t" \
  "ds_read_b128 v[120:123], v204 offset:6144\n\t" \
  "v_cvt_pk_bf16_f32 v22, v180, v181\n\t" \
  "v_mfma_f32_16x16x32_bf16 v[68:71], a[164:167], v[92:95], v[68:71]\n\t" \
  "buffer_load_dwordx4 v201, s[20:23], s96 offen offset:1024 lds\n\t" \
  "v_mfma_f32_16x16x32_bf16 v[36:39], a[164:167], v[60:63], v[36:39]\n\t" \
  "ds_read_b64_tr_b16 a[134:135], v193 offset:33792\n\t" \
  "v_and_b32 v239, s98, v117\n\t" \
  "v_mfma_f32_16x16x32_bf16 v[4:7], a[164:167], v[28:31], v[4:7]\n\t" \
  "ds_read_b64_tr_b16 a[136:137], v194 offset:32768\n\t" \
  "v_cvt_pk_bf16_f32 v23, v182, v183\n\t" \
  "v_mfma_f32_16x16x32_bf16 v[8:11], a[168:171], v[28:31], v[8:11]\n\t" \
  "buffer_load_dwordx4 v202, s[20:23], s96 offen offset:2048 lds\n\t" \
  "v_mfma_f32_16x16x32_bf16 v[40:43], a[168:171], v[60:63], v[40:43]\n\t" \
  "ds_read_b64_tr_b16 a[138:139], v194 offset:33792\n\t" \
  "v_lshlrev_b32 v240, 16, v118\n\t" \
  "v_mfma_f32_16x16x32_bf16 v[72:75], a[168:171], v[92:95], v[72:75]\n\t" \
  "ds_read_b64_tr_b16 a[140:141], v195 offset:32768\n\t" \
  "v_and_b32 v241, s98, v118\n\t" \
  "v_mfma_f32_16x16x32_bf16 v[104:107], a[168:171], v[124:127], v[104:107]\n\t" \
  "v_lshlrev_b32 v242, 16, v119\n\t" \
  "v_and_b32 v243, s98, v119\n\t" \
  "v_mfma_f32_16x16x32_bf16 v[108:111], a[172:175], v[124:127], v[108:111]\n\t" \
  "buffer_load_dwordx4 v203, s[20:23], s96 offen offset:3072 lds\n\t" \
  "ds_read_b64_tr_b16 a[142:143], v195 offset:33792\n\t" \
  "ds_read_b128 v[128:131], v208 offset:1536\n\t" \
  "v_cvt_pk_bf16_f32 v52, v220, v221\n\t" \
  "v_cvt_pk_bf16_f32 v53, v222, v223\n\t" \
  "v_pk_fma_f32 v[228:229], v[228:229], v[168:169], v[170:171] op_sel_hi:[1,0,0]\n\t" \
  "v_pk_fma_f32 v[230:231], v[230:231], v[168:169], v[170:171] op_sel_hi:[1,0,0]\n\t" \
  "v_pk_fma_f32 v[232:233], v[232:233], v[168:169], v[170:171] op_sel_hi:[1,0,0]\n\t" \
  "v_pk_fma_f32 v[234:235], v[234:235], v[168:169], v[170:171] op_sel_hi:[1,0,0]\n\t" \
  "v_pk_fma_f32 v[228:229], v[228:229], v[144:145], v[152:153]\n\t" \
  "v_pk_fma_f32 v[230:231], v[230:231], v[146:147], v[154:155]\n\t" \
  "v_pk_fma_f32 v[232:233], v[232:233], v[148:149], v[156:157]\n\t" \
  "v_pk_fma_f32 v[234:235], v[234:235], v[150:151], v[158:159]\n\t" \
  "v_pk_fma_f32 v[236:237], v[236:237], v[172:173], v[174:175] op_sel_hi:[1,0,0]\n\t" \
  "v_pk_fma_f32 v[238:239], v[238:239], v[172:173], v[174:175] op_sel_hi:[1,0,0]\n\t" \
  "v_pk_fma_f32 v[240:241], v[240:241], v[172:173], v[174:175] op_sel_hi:[1,0,0]\n\t" \
  "v_pk_fma_f32 v[242:243], v[242:243], v[172:173], v[174:175] op_sel_hi:[1,0,0]\n\t" \
  "v_pk_fma_f32 v[236:237], v[236:237], v[144:145], v[152:153]\n\t" \
  "v_pk_fma_f32 v[238:239], v[238:239], v[146:147], v[154:155]\n\t" \
  "v_pk_fma_f32 v[240:241], v[240:241], v[148:149], v[156:157]\n\t" \
  "v_pk_fma_f32 v[242:243], v[242:243], v[150:151], v[158:159]\n\t" \
  "v_cvt_pk_bf16_f32 v84, v228, v229\n\t" \
  "v_mfma_f32_16x16x32_bf16 v[76:79], a[172:175], v[92:95], v[76:79]\n\t" \
  "s_add_u32 s96, s78, 3670016\n\t" \
  "s_and_b32 s96, s96, 0x780000\n\t" \
  "s_add_u32 m0, s76, 65536\n\t" \
  "v_mfma_f32_16x16x32_bf16 v[44:47], a[172:175], v[60:63], v[44:47]\n\t" \
  "ds_read_b64_tr_b16 a[144:145], v196 offset:32768\n\t" \
  "v_cvt_pk_bf16_f32 v54, v224, v225\n\t" \
  "v_mfma_f32_16x16x32_bf16 v[12:15], a[172:175], v[28:31], v[12:15]\n\t" \
  "buffer_load_dwordx4 v200, s[20:23], s96 offen offset:0 lds\n\t" \
  "v_mfma_f32_16x16x32_bf16 a[64:67], a[176:179], v[28:31], a[64:67]\n\t" \
  "s_waitcnt lgkmcnt(7)\n\t" \
  "v_lshlrev_b32 v176, 16, v24\n\t" \
  "v_and_b32 v177, s98, v24\n\t" \
  "v_mfma_f32_16x16x32_bf16 a[68:71], a[176:179], v[60:63], a[68:71]\n\t" \
  "ds_read_b64_tr_b16 a[146:147], v196 offset:33792\n\t" \
  "v_lshlrev_b32 v178, 16, v25\n\t" \
  "v_mfma_f32_16x16x32_bf16 a[72:75], a[176:179], v[92:95], a[72:75]\n\t" \
  "ds_read_b64_tr_b16 a[148:149], v197 offset:32768\n\t" \
  "v_and_b32 v179, s98, v25\n\t" \
  "v_mfma_f32_16x16x32_bf16 a[76:79], a[176:179], v[124:127], a[76:79]\n\t" \
  "buffer_load_dwordx4 v201, s[20:23], s96 offen offset:1024 lds\n\t" \
  "v_mfma_f32_16x16x32_bf16 a[92:95], a[180:183], v[124:127], a[92:95]\n\t" \
  "ds_read_b64_tr_b16 a[150:151], v197 offset:33792\n\t" \
  "v_lshlrev_b32 v180, 16, v26\n\t" \
  "v_mfma_f32_16x16x32_bf16 a[88:91], a[180:183], v[92:95], a[88:91]\n\t" \
  "ds_read_b64_tr_b16 a[152:153], v198 offset:32768\n\t" \
  "v_and_b32 v181, s98, v26\n\t" \
  "v_mfma_f32_16x16x32_bf16 a[84:87], a[180:183], v[60:63], a[84:87]\n\t" \
  "ds_read_b64_tr_b16 a[154:155], v198 offset:33792\n\t" \
  "v_lshlrev_b32 v182, 16, v27\n\t" \
  "v_mfma_f32_16x16x32_bf16 a[80:83], a[180:183], v[28:31], a[80:83]\n\t" \
  "buffer_load_dwordx4 v202, s[20:23], s96 offen offset:2048 lds\n\t" \
  "v_mfma_f32_16x16x32_bf16 a[96:99], a[184:187], v[28:31], a[96:99]\n\t" \
  "ds_read_b64_tr_b16 a[156:157], v199 offset:32768\n\t" \
  "v_cvt_pk_bf16_f32 v55, v226, v227\n\t" \
  "v_mfma_f32_16x16x32_bf16 a[100:103], a[184:187], v[60:63], a[100:103]\n\t" \
  "ds_read_b64_tr_b16 a[158:159], v199 offset:33792\n\t" \
  "v_and_b32 v183, s98, v27\n\t" \
  "v_mfma_f32_16x16x32_bf16 a[104:107], a[184:187], v[92:95], a[104:107]\n\t" \
  "v_lshlrev_b32 v220, 16, v56\n\t" \
  "v_and_b32 v221, s98, v56\n\t" \
  "v_mfma_f32_16x16x32_bf16 a[108:111], a[184:187], v[124:127], a[108:111]\n\t" \
  "buffer_load_dwordx4 v203, s[20:23], s96 offen offset:3072 lds\n\t" \
  "v_mfma_f32_16x16x32_bf16 a[124:127], a[188:191], v[124:127], a[124:127]\n\t" \
  "v_lshlrev_b32 v222, 16, v57\n\t" \
  "v_and_b32 v223, s98, v57\n\t" \
  "v_mfma_f32_16x16x32_bf16 a[120:123], a[188:191], v[92:95], a[120:123]\n\t" \
  "v_lshlrev_b32 v224, 16, v58\n\t" \
  "v_cvt_pk_bf16_f32 v85, v230, v231\n\t" \
  "v_mfma_f32_16x16x32_bf16 a[116:119], a[188:191], v[60:63], a[116:119]\n\t" \
  "v_and_b32 v225, s98, v58\n\t" \
  "v_lshlrev_b32 v226, 16, v59\n\t" \
  "v_mfma_f32_16x16x32_bf16 a[112:115], a[188:191], v[28:31], a[112:115]\n\t" \
  "s_waitcnt lgkmcnt(0)\n\t" \
  "v_cvt_pk_bf16_f32 v86, v232, v233\n\t" \
  "v_cvt_pk_bf16_f32 v87, v234, v235\n\t" \
  FSTP("v_writelane_b32 v255, s94, 42\n\t") \
  FSTP("v_writelane_b32 v255, s92, 43\n\t") \
  FSTP("s_memrealtime s[94:95]\n\t") \
  FSTP("s_memtime s[92:93]\n\t") \
  "v_mfma_f32_16x16x32_bf16 v[0:3], a[128:131], v[16:19], v[0:3]\n\t" \
  "buffer_load_dword v252, v210, s[16:19], s84 offen sc1\n\t" \
  "v_mfma_f32_16x16x32_bf16 v[32:35], a[128:131], v[48:51], v[32:35]\n\t" \
  "ds_read_b128 v[28:31], v205 offset:0\n\t" \
  "v_and_b32 v227, s98, v59\n\t" \
  "v_mfma_f32_16x16x32_bf16 v[64:67], a[128:131], v[80:83], v[64:67]\n\t" \
  "s_lshr_b32 s99, s87, 9\n\t" \
  "ds_read_b128 v[60:63], v205 offset:2048\n\t" \
  "ds_read_b64_tr_b16 a[160:161], v192 offset:40960\n\t" \
  "v_lshlrev_b32 v228, 16, v88\n\t" \
  "v_mfma_f32_16x16x32_bf16 v[96:99], a[128:131], v[112:115], v[96:99]\n\t" \
  "ds_read_b128 v[92:95], v205 offset:4096\n\t" \
  "ds_read_b64_tr_b16 a[162:163], v192 offset:41984\n\t" \
  "v_xor_b32 v248, s99, v214\n\t" \
  "v_mfma_f32_16x16x32_bf16 v[100:103], a[132:135], v[112:115], v[100:103]\n\t" \
  "v_xor_b32 v249, 256, v248\n\t" \
  "v_xor_b32 v250, 512, v248\n\t" \
  "v_mfma_f32_16x16x32_bf16 v[68:71], a[132:135], v[80:83], v[68:71]\n\t" \
  "v_xor_b32 v251, 768, v248\n\t" \
  "s_add_u32 s96, s78, 4194304\n\t" \
  "v_mfma_f32_16x16x32_bf16 v[36:39], a[132:135], v[48:51], v[36:39]\n\t" \
  "s_and_b32 s96, s96, 0x780000\n\t" \
  "ds_read_b128 v[124:127], v205 offset:6144\n\t" \
  "ds_read_b64_tr_b16 a[164:165], v193 offset:40960\n\t" \
  "s_add_u32 m0, s76, 81920\n\t" \
  "v_mfma_f32_16x16x32_bf16 v[4:7], a[132:135], v[16:19], v[4:7]\n\t" \
  "buffer_load_dwordx4 v200, s[20:23], s96 offen offset:0 lds\n\t" \
  "v_mfma_f32_16x16x32_bf16 v[8:11], a[136:139], v[16:19], v[8:11]\n\t" \
  "ds_read_b64_tr_b16 a[166:167], v193 offset:41984\n\t" \
  "v_and_b32 v229, s98, v88\n\t" \
  "v_mfma_f32_16x16x32_bf16 v[40:43], a[136:139], v[48:51], v[40:43]\n\t" \
  "ds_read_b64_tr_b16 a[168:169], v194 offset:40960\n\t" \
  "v_lshlrev_b32 v230, 16, v89\n\t" \
  "v_mfma_f32_16x16x32_bf16 v[72:75], a[136:139], v[80:83], v[72:75]\n\t" \
  "buffer_load_dwordx4 v201, s[20:23], s96 offen offset:1024 lds\n\t" \
  "v_mfma_f32_16x16x32_bf16 v[104:107], a[136:139], v[112:115], v[104:107]\n\t" \
  "ds_read_b64_tr_b16 a[170:171], v194 offset:41984\n\t" \
  "v_and_b32 v231, s98, v89\n\t" \
  "v_mfma_f32_16x16x32_bf16 v[108:111], a[140:143], v[112:115], v[108:111]\n\t" \
  "ds_read_b64_tr_b16 a[172:173], v195 offset:40960\n\t" \
  "v_cvt_pk_bf16_f32 v116, v236, v237\n\t" \
  "v_mfma_f32_16x16x32_bf16 v[76:79], a[140:143], v[80:83], v[76:79]\n\t" \
  "ds_read_b64_tr_b16 a[174:175], v195 offset:41984\n\t" \
  "v_lshlrev_b32 v232, 16, v90\n\t" \
  "v_mfma_f32_16x16x32_bf16 v[44:47], a[140:143], v[48:51], v[44:47]\n\t" \
  "ds_read_b64_tr_b16 a[176:177], v196 offset:40960\n\t" \
  "v_cvt_pk_bf16_f32 v117, v238, v239\n\t" \
  "v_mfma_f32_16x16x32_bf16 v[12:15], a[140:143], v[16:19], v[12:15]\n\t" \
  "buffer_load_dwordx4 v202, s[20:23], s96 offen offset:2048 lds\n\t" \
  "v_mfma_f32_16x16x32_bf16 a[64:67], a[144:147], v[16:19], a[64:67]\n\t" \
  "ds_read_b64_tr_b16 a[178:179], v196 offset:41984\n\t" \
  "v_and_b32 v233, s98, v90\n\t" \
  "v_mfma_f32_16x16x32_bf16 a[68:71], a[144:147], v[48:51], a[68:71]\n\t" \
  "ds_read_b64_tr_b16 a[180:181], v197 offset:40960\n\t" \
  "v_cvt_pk_bf16_f32 v118, v240, v241\n\t" \
  "v_mfma_f32_16x16x32_bf16 a[72:75], a[144:147], v[80:83], a[72:75]\n\t" \
  "ds_read_b64_tr_b16 a[182:183], v197 offset:41984\n\t" \
  "v_lshlrev_b32 v234, 16, v91\n\t" \
  "v_mfma_f32_16x16x32_bf16 a[76:79], a[144:147], v[112:115], a[76:79]\n\t" \
  "ds_read_b128 v[132:135], v208 offset:1552\n\t" \
  "v_cvt_pk_bf16_f32 v119, v242, v243\n\t" \
  "v_mfma_f32_16x16x32_bf16 a[92:95], a[148:151], v[112:115], a[92:95]\n\t" \
  "buffer_load_dwordx4 v203, s[20:23], s96 offen offset:3072 lds\n\t" \
  "v_mfma_f32_16x16x32_bf16 a[88:91], a[148:151], v[80:83], a[88:91]\n\t" \
  "ds_read_b64_tr_b16 a[184:185], v198 offset:40960\n\t" \
  "v_and_b32 v235, s98, v91\n\t" \
  "v_mfma_f32_16x16x32_bf16 a[84:87], a[148:151], v[48:51], a[84:87]\n\t" \
  "ds_read_b64_tr_b16 a[186:187], v198 offset:41984\n\t" \
  "v_lshlrev_b32 v236, 16, v120\n\t" \
  "v_mfma_f32_16x16x32_bf16 a[80:83], a[148:151], v[16:19], a[80:83]\n\t" \
  "ds_read_b64_tr_b16 a[188:189], v199 offset:40960\n\t" \
  "v_and_b32 v237, s98, v120\n\t" \
  "v_mfma_f32_16x16x32_bf16 a[96:99], a[152:155], v[16:19], a[96:99]\n\t" \
  "ds_read_b128 v[136:139], v208 offset:1792\n\t" \
  "v_lshlrev_b32 v238, 16, v121\n\t" \
  "v_mfma_f32_16x16x32_bf16 a[100:103], a[152:155], v[48:51], a[100:103]\n\t" \
  "global_load_dword v246, v216, s[20:21]\n\t" \
  "v_mfma_f32_16x16x32_bf16 a[104:107], a[152:155], v[80:83], a[104:107]\n\t" \
  "ds_read_b64_tr_b16 a[190:191], v199 offset:41984\n\t" \
  "v_and_b32 v239, s98, v121\n\t" \
  "v_mfma_f32_16x16x32_bf16 a[108:111], a[152:155], v[112:115], a[108:111]\n\t" \
  "ds_read_b128 v[140:143], v208 offset:1808\n\t" \
  "v_lshlrev_b32 v240, 16, v122\n\t" \
  "v_mfma_f32_16x16x32_bf16 a[124:127], a[156:159], v[112:115], a[124:127]\n\t" \
  "ds_read_b128 v[144:147], v208 offset:1664\n\t" \
  "v_and_b32 v241, s98, v122\n\t" \
  "v_lshlrev_b32 v242, 16, v123\n\t" \
  "v_and_b32 v243, s98, v123\n\t" \
  "s_waitcnt lgkmcnt(1)\n\t" \
  "v_pk_fma_f32 v[176:177], v[176:177], v[160:161], v[162:163] op_sel_hi:[1,0,0]\n\t" \
  "v_pk_fma_f32 v[178:179], v[178:179], v[160:161], v[162:163] op_sel_hi:[1,0,0]\n\t" \
  "v_pk_fma_f32 v[180:181], v[180:181], v[160:161], v[162:163] op_sel_hi:[1,0,0]\n\t" \
  "v_pk_fma_f32 v[182:183], v[182:183], v[160:161], v[162:163] op_sel_hi:[1,0,0]\n\t" \
  "v_pk_fma_f32 v[176:177], v[176:177], v[128:129], v[136:137]\n\t" \
  "v_pk_fma_f32 v[178:179], v[178:179], v[130:131], v[138:139]\n\t" \
  "v_pk_fma_f32 v[180:181], v[180:181], v[132:133], v[140:141]\n\t" \
  "v_pk_fma_f32 v[182:183], v[182:183], v[134:135], v[142:143]\n\t" \
  "v_pk_fma_f32 v[220:221], v[220:221], v[164:165], v[166:167] op_sel_hi:[1,0,0]\n\t" \
  "v_pk_fma_f32 v[222:223], v[222:223], v[164:165], v[166:167] op_sel_hi:[1,0,0]\n\t" \
  "v_pk_fma_f32 v[224:225], v[224:225], v[164:165], v[166:167] op_sel_hi:[1,0,0]\n\t" \
  "v_pk_fma_f32 v[226:227], v[226:227], v[164:165], v[166:167] op_sel_hi:[1,0,0]\n\t" \
  "v_pk_fma_f32 v[220:221], v[220:221], v[128:129], v[136:137]\n\t" \
  "v_pk_fma_f32 v[222:223], v[222:223], v[130:131], v[138:139]\n\t" \
  "v_pk_fma_f32 v[224:225], v[224:225], v[132:133], v[140:141]\n\t" \
  "v_pk_fma_f32 v[226:227], v[226:227], v[134:135], v[142:143]\n\t" \
  "v_cvt_pk_bf16_f32 v24, v176, v177\n\t" \
  "v_mfma_f32_16x16x32_bf16 a[120:123], a[156:159], v[80:83], a[120:123]\n\t" \
  "v_cvt_pk_bf16_f32 v25, v178, v179\n\t" \
  "v_cvt_pk_bf16_f32 v26, v180, v181\n\t" \
  "v_cvt_pk_bf16_f32 v27, v182, v183\n\t" \
  "v_cvt_pk_bf16_f32 v56, v220, v221\n\t" \
  "v_cvt_pk_bf16_f32 v57, v222, v223\n\t" \
  "v_pk_fma_f32 v[228:229], v[228:229], v[168:169], v[170:171] op_sel_hi:[1,0,0]\n\t" \
  "v_pk_fma_f32 v[230:231], v[230:231], v[168:169], v[170:171] op_sel_hi:[1,0,0]\n\t" \
  "v_pk_fma_f32 v[232:233], v[232:233], v[168:169], v[170:171] op_sel_hi:[1,0,0]\n\t" \
  "v_pk_fma_f32 v[234:235], v[234:235], v[168:169], v[170:171] op_sel_hi:[1,0,0]\n\t" \
  "v_pk_fma_f32 v[228:229], v[228:229], v[128:129], v[136:137]\n\t" \
  "v_pk_fma_f32 v[230:231], v[230:231], v[130:131], v[138:139]\n\t" \
  "v_pk_fma_f32 v[232:233], v[232:233], v[132:133], v[140:141]\n\t" \
  "v_pk_fma_f32 v[234:235], v[234:235], v[134:135], v[142:143]\n\t" \
  "v_pk_fma_f32 v[236:237], v[236:237], v[172:173], v[174:175] op_sel_hi:[1,0,0]\n\t" \
  "v_pk_fma_f32 v[238:239], v[238:239], v[172:173], v[174:175] op_sel_hi:[1,0,0]\n\t" \
  "v_pk_fma_f32 v[240:241], v[240:241], v[172:173], v[174:175] op_sel_hi:[1,0,0]\n\t" \
  "v_pk_fma_f32 v[242:243], v[242:243], v[172:173], v[174:175] op_sel_hi:[1,0,0]\n\t" \
  "v_pk_fma_f32 v[236:237], v[236:237], v[128:129], v[136:137]\n\t" \
  "v_pk_fma_f32 v[238:239], v[238:239], v[130:131], v[138:139]\n\t" \
  "v_pk_fma_f32 v[240:241], v[240:241], v[132:133], v[140:141]\n\t" \
  "v_pk_fma_f32 v[242:243], v[242:243], v[134:135], v[142:143]\n\t" \
  "v_cvt_pk_bf16_f32 v88, v228, v229\n\t" \
  "v_mfma_f32_16x16x32_bf16 a[116:119], a[156:159], v[48:51], a[116:119]\n\t" \
  "s_waitcnt vmcnt(23)\n\t" \
  "v_cvt_pk_bf16_f32 v58, v224, v225\n\t" \
  "v_cvt_pk_bf16_f32 v59, v226, v227\n\t" \
  "v_mfma_f32_16x16x32_bf16 a[112:115], a[156:159], v[16:19], a[112:115]\n\t" \
  "s_barrier\n\t" \
  FSTP("v_writelane_b32 v255, s94, 44\n\t") \
  FSTP("v_writelane_b32 v255, s92, 45\n\t") \
  FSTP("s_memrealtime s[94:95]\n\t") \
  FSTP("s_memtime s[92:93]\n\t") \
  "v_mfma_f32_16x16x32_bf16 v[0:3], a[160:163], v[20:23], v[0:3]\n\t" \
  "s_waitcnt vmcnt(5)\n\t" \
  "v_cmp_ne_u32 vcc, v211, v252\n\t" \
  "v_cvt_pk_bf16_f32 v89, v230, v231\n\t" \
  "v_mfma_f32_16x16x32_bf16 v[32:35], a[160:163], v[52:55], v[32:35]\n\t" \
  "ds_read_b64_tr_b16 a[128:129], v184 offset:0\n\t" \
  "v_cvt_pk_bf16_f32 v90, v232, v233\n\t" \
  "v_mfma_f32_16x16x32_bf16 v[64:67], a[160:163], v[84:87], v[64:67]\n\t" \
  "s_cbranch_vccnz .Lfq_s\n\t" \
  ".Lfq_k:\n\t" \
  "v_mfma_f32_16x16x32_bf16 v[96:99], a[160:163], v[116:119], v[96:99]\n\t" \
  "s_add_u32 s97, s78, 2097152\n\t" \
  "s_bfe_u32 s97, s97, 0x140003\n\t" \
  "s_add_u32 s97, s97, s82\n\t" \
  "v_mfma_f32_16x16x32_bf16 v[100:103], a[164:167], v[116:119], v[100:103]\n\t" \
  "buffer_load_dwordx4 a[0:3], v248, s[16:19], s97 offen sc1\n\t" \
  "v_mfma_f32_16x16x32_bf16 v[68:71], a[164:167], v[84:87], v[68:71]\n\t" \
  "buffer_load_dwordx4 a[4:7], v249, s[16:19], s97 offen sc1\n\t" \
  "v_mfma_f32_16x16x32_bf16 v[36:39], a[164:167], v[52:55], v[36:39]\n\t" \
  "ds_read_b64_tr_b16 a[130:131], v184 offset:1024\n\t" \
  "v_cvt_pk_bf16_f32 v91, v234, v235\n\t" \
  "v_mfma_f32_16x16x32_bf16 v[4:7], a[164:167], v[20:23], v[4:7]\n\t" \
  "ds_read_b64_tr_b16 a[132:133], v185 offset:0\n\t" \
  "v_cvt_pk_bf16_f32 v120, v236, v237\n\t" \
  "v_mfma_f32_16x16x32_bf16 v[8:11], a[168:171], v[20:23], v[8:11]\n\t" \
  "buffer_load_dwordx4 a[8:11], v250, s[16:19], s97 offen sc1\n\t" \
  "v_mfma_f32_16x16x32_bf16 v[40:43], a[168:171], v[52:55], v[40:43]\n\t" \
  "ds_read_b64_tr_b16 a[134:135], v185 offset:1024\n\t" \
  "v_cvt_pk_bf16_f32 v121, v238, v239\n\t" \
  "v_mfma_f32_16x16x32_bf16 v[72:75], a[168:171], v[84:87], v[72:75]\n\t" \
  "v_lshlrev_b32 v176, 16, v28\n\t" \
  "v_and_b32 v177, s98, v28\n\t" \
  "v_mfma_f32_16x16x32_bf16 v[104:107], a[168:171], v[116:119], v[104:107]\n\t" \
  "ds_read_b64_tr_b16 a[136:137], v186 offset:0\n\t" \
  "v_lshlrev_b32 v178, 16, v29\n\t" \
  "v_mfma_f32_16x16x32_bf16 v[108:111], a[172:175], v[116:119], v[108:111]\n\t" \
  "buffer_load_dwordx4 a[12:15], v251, s[16:19], s97 offen sc1\n\t" \
  "v_mfma_f32_16x16x32_bf16 v[76:79], a[172:175], v[84:87], v[76:79]\n\t" \
  "s_add_u32 s97, s97, s83\n\t" \
  "ds_read_b128 v[148:151], v208 offset:1680\n\t" \
  "ds_read_b64_tr_b16 a[138:139], v186 offset:1024\n\t" \
  "v_and_b32 v179, s98, v29\n\t" \
  "v_mfma_f32_16x16x32_bf16 v[44:47], a[172:175], v[52:55], v[44:47]\n\t" \
  "ds_read_b128 v[152:155], v208 offset:1920\n\t" \
  "ds_read_b64_tr_b16 a[140:141], v187 offset:0\n\t" \
  "v_lshlrev_b32 v180, 16, v30\n\t" \
  "v_mfma_f32_16x16x32_bf16 v[12:15], a[172:175], v[20:23], v[12:15]\n\t" \
  "buffer_load_dwordx4 a[16:19], v248, s[16:19], s97 offen sc1\n\t" \
  "v_mfma_f32_16x16x32_bf16 a[64:67], a[176:179], v[20:23], a[64:67]\n\t" \
  "ds_read_b64_tr_b16 a[142:143], v187 offset:1024\n\t" \
  "v_and_b32 v181, s98, v30\n\t" \
  "v_mfma_f32_16x16x32_bf16 a[68:71], a[176:179], v[52:55], a[68:71]\n\t" \
  "ds_read_b64_tr_b16 a[144:145], v188 offset:0\n\t" \
  "v_lshlrev_b32 v182, 16, v31\n\t" \
  "v_mfma_f32_16x16x32_bf16 a[72:75], a[176:179], v[84:87], a[72:75]\n\t" \
  "ds_read_b64_tr_b16 a[146:147], v188 offset:1024\n\t" \
  "v_and_b32 v183, s98, v31\n\t" \
  "v_mfma_f32_16x16x32_bf16 a[76:79], a[176:179], v[116:119], a[76:79]\n\t" \
  "buffer_load_dwordx4 a[20:23], v249, s[16:19], s97 offen sc1\n\t" \
  "v_mfma_f32_16x16x32_bf16 a[92:95], a[180:183], v[116:119], a[92:95]\n\t" \
  "ds_read_b64_tr_b16 a[148:149], v189 offset:0\n\t" \
  "v_lshlrev_b32 v220, 16, v60\n\t" \
  "v_mfma_f32_16x16x32_bf16 a[88:91], a[180:183], v[84:87], a[88:91]\n\t" \
  "ds_read_b64_tr_b16 a[150:151], v189 offset:1024\n\t" \
  "v_and_b32 v221, s98, v60\n\t" \
  "v_mfma_f32_16x16x32_bf16 a[84:87], a[180:183], v[52:55], a[84:87]\n\t" \
  "ds_read_b64_tr_b16 a[152:153], v190 offset:0\n\t" \
  "v_lshlrev_b32 v222, 16, v61\n\t" \
  "v_mfma_f32_16x16x32_bf16 a[80:83], a[180:183], v[20:23], a[80:83]\n\t" \
  "buffer_load_dwordx4 a[24:27], v250, s[16:19], s97 offen sc1\n\t" \
  "v_mfma_f32_16x16x32_bf16 a[96:99], a[184:187], v[20:23], a[96:99]\n\t" \
  "ds_read_b64_tr_b16 a[154:155], v190 offset:1024\n\t" \
  "v_cvt_pk_bf16_f32 v122, v240, v241\n\t" \
  "v_mfma_f32_16x16x32_bf16 a[100:103], a[184:187], v[52:55], a[100:103]\n\t" \
  "ds_read_b64_tr_b16 a[156:157], v191 offset:0\n\t" \
  "v_and_b32 v223, s98, v61\n\t" \
  "v_mfma_f32_16x16x32_bf16 a[104:107], a[184:187], v[84:87], a[104:107]\n\t" \
  "ds_read_b64_tr_b16 a[158:159], v191 offset:1024\n\t" \
  "v_cvt_pk_bf16_f32 v123, v242, v243\n\t" \
  "v_mfma_f32_16x16x32_bf16 a[108:111], a[184:187], v[116:119], a[108:111]\n\t" \
  "buffer_load_dwordx4 a[28:31], v251, s[16:19], s97 offen sc1\n\t" \
  "v_mfma_f32_16x16x32_bf16 a[124:127], a[188:191], v[116:119], a[124:127]\n\t" \
  "ds_read_b128 v[156:159], v208 offset:1936\n\t" \
  "v_lshlrev_b32 v224, 16, v62\n\t" \
  "v_mfma_f32_16x16x32_bf16 a[120:123], a[188:191], v[84:87], a[120:123]\n\t" \
  "s_add_u32 s97, s78, 2621440\n\t" \
  "v_and_b32 v225, s98, v62\n\t" \
  "v_lshlrev_b32 v226, 16, v63\n\t" \
  "v_mfma_f32_16x16x32_bf16 a[116:119], a[188:191], v[52:55], a[116:119]\n\t" \
  "s_bfe_u32 s97, s97, 0x140003\n\t" \
  "s_add_u32 s97, s97, s82\n\t" \
  "v_and_b32 v227, s98, v63\n\t" \
  "v_mfma_f32_16x16x32_bf16 a[112:115], a[188:191], v[20:23], a[112:115]\n\t" \
  "s_waitcnt lgkmcnt(1)\n\t" \
  "v_lshlrev_b32 v228, 16, v92\n\t" \
  "v_and_b32 v229, s98, v92\n\t" \
  "v_lshlrev_b32 v230, 16, v93\n\t" \
  "v_and_b32 v231, s98, v93\n\t" \
  "s_waitcnt lgkmcnt(0)\n\t" \
  "v_pk_fma_f32 v[176:177], v[176:177], v[160:161], v[162:163] op_sel_hi:[1,0,0]\n\t" \
  "v_pk_fma_f32 v[178:179], v[178:179], v[160:161], v[162:163] op_sel_hi:[1,0,0]\n\t" \
  "v_pk_fma_f32 v[180:181], v[180:181], v[160:161], v[162:163] op_sel_hi:[1,0,0]\n\t" \
  "v_pk_fma_f32 v[182:183], v[182:183], v[160:161], v[162:163] op_sel_hi:[1,0,0]\n\t" \
  "v_pk_fma_f32 v[176:177], v[176:177], v[144:145], v[152:153]\n\t" \
  "v_pk_fma_f32 v[178:179], v[178:179], v[146:147], v[154:155]\n\t" \
  "v_pk_fma_f32 v[180:181], v[180:181], v[148:149], v[156:157]\n\t" \
  "v_pk_fma_f32 v[182:183], v[182:183], v[150:151], v[158:159]\n\t" \
  "v_pk_fma_f32 v[220:221], v[220:221], v[164:165], v[166:167] op_sel_hi:[1,0,0]\n\t" \
  "v_pk_fma_f32 v[222:223], v[222:223], v[164:165], v[166:167] op_sel_hi:[1,0,0]\n\t" \
  "v_pk_fma_f32 v[224:225], v[224:225], v[164:165], v[166:167] op_sel_hi:[1,0,0]\n\t" \
  "v_pk_fma_f32 v[226:227], v[226:227], v[164:165], v[166:167] op_sel_hi:[1,0,0]\n\t" \
  "v_pk_fma_f32 v[220:221], v[220:221], v[144:145], v[152:153]\n\t" \
  "v_pk_fma_f32 v[222:223], v[222:223], v[146:147], v[154:155]\n\t" \
  "v_pk_fma_f32 v[224:225], v[224:225], v[148:149], v[156:157]\n\t" \
  "v_pk_fma_f32 v[226:227], v[226:227], v[150:151], v[158:159]\n\t" \
  "v_cvt_pk_bf16_f32 v28, v176, v177\n\t" \
  FSTP("v_writelane_b32 v255, s94, 46\n\t") \
  FSTP("v_writelane_b32 v255, s92, 47\n\t") \
  FSTP("s_memrealtime s[94:95]\n\t") \
  FSTP("s_memtime s[92:93]\n\t") \
  "v_mfma_f32_16x16x32_bf16 v[0:3], a[128:131], v[24:27], v[0:3]\n\t" \
  "buffer_load_dwordx4 a[32:35], v248, s[16:19], s97 offen sc1\n\t" \
  "v_mfma_f32_16x16x32_bf16 v[32:35], a[128:131], v[56:59], v[32:35]\n\t" \
  "ds_read_b64_tr_b16 a[160:161], v184 offset:8192\n\t" \
  "v_lshlrev_b32 v232, 16, v94\n\t" \
  "v_mfma_f32_16x16x32_bf16 v[64:67], a[128:131], v[88:91], v[64:67]\n\t" \
  "ds_read_b64_tr_b16 a[162:163], v184 offset:9216\n\t" \
  "v_and_b32 v233, s98, v94\n\t" \
  "v_mfma_f32_16x16x32_bf16 v[96:99], a[128:131], v[120:123], v[96:99]\n\t" \
  "v_lshlrev_b32 v234, 16, v95\n\t" \
  "v_and_b32 v235, s98, v95\n\t" \
  "v_mfma_f32_16x16x32_bf16 v[100:103], a[132:135], v[120:123], v[100:103]\n\t" \
  "buffer_load_dwordx4 a[36:39], v249, s[16:19], s97 offen sc1\n\t" \
  "v_mfma_f32_16x16x32_bf16 v[68:71], a[132:135], v[88:91], v[68:71]\n\t" \
  "ds_read_b64_tr_b16 a[164:165], v185 offset:8192\n\t" \
  "v_lshlrev_b32 v236, 16, v124\n\t" \
  "v_mfma_f32_16x16x32_bf16 v[36:39], a[132:135], v[56:59], v[36:39]\n\t" \
  "ds_read_b64_tr_b16 a[166:167], v185 offset:9216\n\t" \
  "v_and_b32 v237, s98, v124\n\t" \
  "v_mfma_f32_16x16x32_bf16 v[4:7], a[132:135], v[24:27], v[4:7]\n\t" \
  "ds_read_b64_tr_b16 a[168:169], v186 offset:8192\n\t" \
  "v_cvt_pk_bf16_f32 v29, v178, v179\n\t" \
  "v_mfma_f32_16x16x32_bf16 v[8:11], a[136:139], v[24:27], v[8:11]\n\t" \
  "buffer_load_dwordx4 a[40:43], v250, s[16:19], s97 offen sc1\n\t" \
  "v_mfma_f32_16x16x32_bf16 v[40:43], a[136:139], v[56:59], v[40:43]\n\t" \
  "ds_read_b64_tr_b16 a[170:171], v186 offset:9216\n\t" \
  "v_lshlrev_b32 v238, 16, v125\n\t" \
  "v_mfma_f32_16x16x32_bf16 v[72:75], a[136:139], v[88:91], v[72:75]\n\t" \
  "ds_read_b64_tr_b16 a[172:173], v187 offset:8192\n\t" \
  "v_and_b32 v239, s98, v125\n\t" \
  "v_mfma_f32_16x16x32_bf16 v[104:107], a[136:139], v[120:123], v[104:107]\n\t" \
  "v_cvt_pk_bf16_f32 v30, v180, v181\n\t" \
  "v_cvt_pk_bf16_f32 v31, v182, v183\n\t" \
  "v_mfma_f32_16x16x32_bf16 v[108:111], a[140:143], v[120:123], v[108:111]\n\t" \
  "buffer_load_dwordx4 a[44:47], v251, s[16:19], s97 offen sc1\n\t" \
  "v_mfma_f32_16x16x32_bf16 v[76:79], a[140:143], v[88:91], v[76:79]\n\t" \
  "s_add_u32 s97, s97, s83\n\t" \
  "ds_read_b64_tr_b16 a[174:175], v187 offset:9216\n\t" \
  "v_lshlrev_b32 v240, 16, v126\n\t" \
  "v_mfma_f32_16x16x32_bf16 v[44:47], a[140:143], v[56:59], v[44:47]\n\t" \
  "ds_read_b64_tr_b16 a[176:177], v188 offset:8192\n\t" \
  "v_and_b32 v241, s98, v126\n\t" \
  "v_mfma_f32_16x16x32_bf16 v[12:15], a[140:143], v[24:27], v[12:15]\n\t" \
  "buffer_load_dwordx4 a[48:51], v248, s[16:19], s97 offen sc1\n\t" \
  "v_mfma_f32_16x16x32_bf16 a[64:67], a[144:147], v[24:27], a[64:67]\n\t" \
  "ds_read_b64_tr_b16 a[178:179], v188 offset:9216\n\t" \
  "v_cvt_pk_bf16_f32 v60, v220, v221\n\t" \
  "v_mfma_f32_16x16x32_bf16 a[68:71], a[144:147], v[56:59], a[68:71]\n\t" \
  "ds_read_b64_tr_b16 a[180:181], v189 offset:8192\n\t" \
  "v_cvt_pk_bf16_f32 v61, v222, v223\n\t" \
  "v_mfma_f32_16x16x32_bf16 a[72:75], a[144:147], v[88:91], a[72:75]\n\t" \
  "ds_read_b64_tr_b16 a[182:183], v189 offset:9216\n\t" \
  "v_lshlrev_b32 v242, 16, v127\n\t" \
  "v_mfma_f32_16x16x32_bf16 a[76:79], a[144:147], v[120:123], a[76:79]\n\t" \
  "buffer_load_dwordx4 a[52:55], v249, s[16:19], s97 offen sc1\n\t" \
  "v_mfma_f32_16x16x32_bf16 a[92:95], a[148:151], v[120:123], a[92:95]\n\t" \
  "ds_read_b64_tr_b16 a[184:185], v190 offset:8192\n\t" \
  "v_and_b32 v243, s98, v127\n\t" \
  "v_mfma_f32_16x16x32_bf16 a[88:91], a[148:151], v[88:91], a[88:91]\n\t" \
  "buffer_load_dwordx4 a[56:59], v250, s[16:19], s97 offen sc1\n\t" \
  "ds_read_b64_tr_b16 a[186:187], v190 offset:9216\n\t" \
  "ds_read_b64_tr_b16 a[188:189], v191 offset:8192\n\t" \
  "v_cvt_pk_bf16_f32 v62, v224, v225\n\t" \
  "v_cvt_pk_bf16_f32 v63, v226, v227\n\t" \
  "v_pk_fma_f32 v[228:229], v[228:229], v[168:169], v[170:171] op_sel_hi:[1,0,0]\n\t" \
  "v_pk_fma_f32 v[230:231], v[230:231], v[168:169], v[170:171] op_sel_hi:[1,0,0]\n\t" \
  "v_pk_fma_f32 v[232:233], v[232:233], v[168:169], v[170:171] op_sel_hi:[1,0,0]\n\t" \
  "v_pk_fma_f32 v[234:235], v[234:235], v[168:169], v[170:171] op_sel_hi:[1,0,0]\n\t" \
  "v_pk_fma_f32 v[228:229], v[228:229], v[144:145], v[152:153]\n\t" \
  "v_pk_fma_f32 v[230:231], v[230:231], v[146:147], v[154:155]\n\t" \
  "v_pk_fma_f32 v[232:233], v[232:233], v[148:149], v[156:157]\n\t" \
  "v_pk_fma_f32 v[234:235], v[234:235], v[150:151], v[158:159]\n\t" \
  "v_pk_fma_f32 v[236:237], v[236:237], v[172:173], v[174:175] op_sel_hi:[1,0,0]\n\t" \
  "v_pk_fma_f32 v[238:239], v[238:239], v[172:173], v[174:175] op_sel_hi:[1,0,0]\n\t" \
  "v_pk_fma_f32 v[240:241], v[240:241], v[172:173], v[174:175] op_sel_hi:[1,0,0]\n\t" \
  "v_pk_fma_f32 v[242:243], v[242:243], v[172:173], v[174:175] op_sel_hi:[1,0,0]\n\t" \
  "v_pk_fma_f32 v[236:237], v[236:237], v[144:145], v[152:153]\n\t" \
  "v_pk_fma_f32 v[238:239], v[238:239], v[146:147], v[154:155]\n\t" \
  "v_pk_fma_f32 v[240:241], v[240:241], v[148:149], v[156:157]\n\t" \
  "v_pk_fma_f32 v[242:243], v[242:243], v[150:151], v[158:159]\n\t" \
  "v_cvt_pk_bf16_f32 v92, v228, v229\n\t" \
  "v_mfma_f32_16x16x32_bf16 a[84:87], a[148:151], v[56:59], a[84:87]\n\t" \
  "v_cvt_pk_bf16_f32 v93, v230, v231\n\t" \
  "v_cvt_pk_bf16_f32 v94, v232, v233\n\t" \
  "v_mfma_f32_16x16x32_bf16 a[80:83], a[148:151], v[24:27], a[80:83]\n\t" \
  "v_cvt_pk_bf16_f32 v95, v234, v235\n\t" \
  "v_cvt_pk_bf16_f32 v124, v236, v237\n\t" \
  "v_mfma_f32_16x16x32_bf16 a[96:99], a[152:155], v[24:27], a[96:99]\n\t" \
  "v_cvt_pk_bf16_f32 v125, v238, v239\n\t" \
  "v_cvt_pk_bf16_f32 v126, v240, v241\n\t" \
  "v_mfma_f32_16x16x32_bf16 a[100:103], a[152:155], v[56:59], a[100:103]\n\t" \
  "ds_read_b64_tr_b16 a[190:191], v191 offset:9216\n\t" \
  "v_cvt_pk_bf16_f32 v127, v242, v243\n\t" \
  "v_mfma_f32_16x16x32_bf16 a[104:107], a[152:155], v[88:91], a[104:107]\n\t" \
  "buffer_load_dwordx4 a[60:63], v251, s[16:19], s97 offen sc1\n\t" \
  "v_mfma_f32_16x16x32_bf16 a[108:111], a[152:155], v[120:123], a[108:111]\n\t" \
  "v_mfma_f32_16x16x32_bf16 a[124:127], a[156:159], v[120:123], a[124:127]\n\t" \
  "v_mfma_f32_16x16x32_bf16 a[120:123], a[156:159], v[88:91], a[120:123]\n\t" \
  "s_add_u32 s97, s78, 3145728\n\t" \
  "s_bfe_u32 s97, s97, 0x140003\n\t" \
  "s_add_u32 s97, s97, s82\n\t" \
  "v_mfma_f32_16x16x32_bf16 a[116:119], a[156:159], v[56:59], a[116:119]\n\t" \
  "s_waitcnt lgkmcnt(0)\n\t" \
  "v_mfma_f32_16x16x32_bf16 a[112:115], a[156:159], v[24:27], a[112:115]\n\t" \
  "s_barrier\n\t" \
  FSTP("v_writelane_b32 v255, s94, 48\n\t") \
  FSTP("v_writelane_b32 v255, s92, 49\n\t") \
  FSTP("s_memrealtime s[94:95]\n\t") \
  FSTP("s_memtime s[92:93]\n\t") \
  "v_mfma_f32_16x16x32_bf16 v[0:3], a[160:163], v[28:31], v[0:3]\n\t" \
  "buffer_load_dwordx4 a[192:195], v248, s[16:19], s97 offen sc1\n\t" \
  "v_mfma_f32_16x16x32_bf16 v[32:35], a[160:163], v[60:63], v[32:35]\n\t" \
  "ds_read_b64_tr_b16 a[128:129], v184 offset:16384\n\t" \
  "v_mfma_f32_16x16x32_bf16 v[64:67], a[160:163], v[92:95], v[64:67]\n\t" \
  "ds_read_b64_tr_b16 a[130:131], v184 offset:17408\n\t" \
  "v_mfma_f32_16x16x32_bf16 v[96:99], a[160:163], v[124:127], v[96:99]\n\t" \
  "ds_write_b32 v219, v218\n\t" \
  "v_mfma_f32_16x16x32_bf16 v[100:103], a[164:167], v[124:127], v[100:103]\n\t" \
  "buffer_load_dwordx4 a[196:199], v249, s[16:19], s97 offen sc1\n\t" \
  "v_mfma_f32_16x16x32_bf16 v[68:71], a[164:167], v[92:95], v[68:71]\n\t" \
  "ds_read_b64_tr_b16 a[132:133], v185 offset:16384\n\t" \
  "v_and_b32 v219, 0x7fffffe0, v219\n\t" \
  "v_mfma_f32_16x16x32_bf16 v[36:39], a[164:167], v[60:63], v[36:39]\n\t" \
  "ds_read_b64_tr_b16 a[134:135], v185 offset:17408\n\t" \
  "v_mfma_f32_16x16x32_bf16 v[4:7], a[164:167], v[28:31], v[4:7]\n\t" \
  "ds_read_b64_tr_b16 a[136:137], v186 offset:16384\n\t" \
  "v_mfma_f32_16x16x32_bf16 v[8:11], a[168:171], v[28:31], v[8:11]\n\t" \
  "buffer_load_dwordx4 a[200:203], v250, s[16:19], s97 offen sc1\n\t" \
  "v_mfma_f32_16x16x32_bf16 v[40:43], a[168:171], v[60:63], v[40:43]\n\t" \
  "ds_read_b64_tr_b16 a[138:139], v186 offset:17408\n\t" \
  "v_mfma_f32_16x16x32_bf16 v[72:75], a[168:171], v[92:95], v[72:75]\n\t" \
  "ds_read_b64_tr_b16 a[140:141], v187 offset:16384\n\t" \
  "v_mfma_f32_16x16x32_bf16 v[104:107], a[168:171], v[124:127], v[104:107]\n\t" \
  "ds_read_b128 v[168:171], v219 offset:0\n\t" \
  "v_mfma_f32_16x16x32_bf16 v[108:111], a[172:175], v[124:127], v[108:111]\n\t" \
  "buffer_load_dwordx4 a[204:207], v251, s[16:19], s97 offen sc1\n\t" \
  "v_mfma_f32_16x16x32_bf16 v[76:79], a[172:175], v[92:95], v[76:79]\n\t" \
  "ds_read_b64_tr_b16 a[142:143], v187 offset:17408\n\t" \
  "s_add_u32 s97, s97, s83\n\t" \
  "v_mfma_f32_16x16x32_bf16 v[44:47], a[172:175], v[60:63], v[44:47]\n\t" \
  "ds_read_b64_tr_b16 a[144:145], v188 offset:16384\n\t" \
  "v_mfma_f32_16x16x32_bf16 v[12:15], a[172:175], v[28:31], v[12:15]\n\t" \
  "buffer_load_dwordx4 a[208:211], v248, s[16:19], s97 offen sc1\n\t" \
  "v_mfma_f32_16x16x32_bf16 a[64:67], a[176:179], v[28:31], a[64:67]\n\t" \
  "ds_read_b64_tr_b16 a[146:147], v188 offset:17408\n\t" \
  "v_mfma_f32_16x16x32_bf16 a[68:71], a[176:179], v[60:63], a[68:71]\n\t" \
  "ds_read_b64_tr_b16 a[148:149], v189 offset:16384\n\t" \
  "v_mfma_f32_16x16x32_bf16 a[72:75], a[176:179], v[92:95], a[72:75]\n\t" \
  "ds_read_b64_tr_b16 a[150:151], v189 offset:17408\n\t" \
  "v_mfma_f32_16x16x32_bf16 a[76:79], a[176:179], v[124:127], a[76:79]\n\t" \
  "buffer_load_dwordx4 a[212:215], v249, s[16:19], s97 offen sc1\n\t" \
  "v_mfma_f32_16x16x32_bf16 a[92:95], a[180:183], v[124:127], a[92:95]\n\t" \
  "ds_read_b64_tr_b16 a[152:153], v190 offset:16384\n\t" \
  "v_mfma_f32_16x16x32_bf16 a[88:91], a[180:183], v[92:95], a[88:91]\n\t" \
  "ds_read_b128 v[172:175], v219 offset:16\n\t" \
  "v_mfma_f32_16x16x32_bf16 a[84:87], a[180:183], v[60:63], a[84:87]\n\t" \
  "ds_read_b64_tr_b16 a[154:155], v190 offset:17408\n\t" \
  "v_mfma_f32_16x16x32_bf16 a[80:83], a[180:183], v[28:31], a[80:83]\n\t" \
  "buffer_load_dwordx4 a[216:219], v250, s[16:19], s97 offen sc1\n\t" \
  "v_mfma_f32_16x16x32_bf16 a[96:99], a[184:187], v[28:31], a[96:99]\n\t" \
  "ds_read_b64_tr_b16 a[156:157], v191 offset:16384\n\t" \
  "v_mfma_f32_16x16x32_bf16 a[100:103], a[184:187], v[60:63], a[100:103]\n\t" \
  "ds_read_b64_tr_b16 a[158:159], v191 offset:17408\n\t" \
  "v_mfma_f32_16x16x32_bf16 a[104:107], a[184:187], v[92:95], a[104:107]\n\t" \
  "buffer_load_dwordx4 a[220:223], v251, s[16:19], s97 offen sc1\n\t" \
  "v_mfma_f32_16x16x32_bf16 a[108:111], a[184:187], v[124:127], a[108:111]\n\t" \
  "v_mfma_f32_16x16x32_bf16 a[124:127], a[188:191], v[124:127], a[124:127]\n\t" \
  "v_mfma_f32_16x16x32_bf16 a[120:123], a[188:191], v[92:95], a[120:123]\n\t" \
  "s_add_u32 s97, s78, 3670016\n\t" \
  "s_bfe_u32 s97, s97, 0x140003\n\t" \
  "s_add_u32 s97, s97, s82\n\t" \
  "v_mfma_f32_16x16x32_bf16 a[116:119], a[188:191], v[60:63], a[116:119]\n\t" \
  "v_mfma_f32_16x16x32_bf16 a[112:115], a[188:191], v[28:31], a[112:115]\n\t" \
  "s_waitcnt vmcnt(20) lgkmcnt(0)\n\t" \
  FSTP("v_writelane_b32 v255, s94, 50\n\t") \
  FSTP("v_writelane_b32 v255, s92, 51\n\t") \
  FSTP("s_memrealtime s[94:95]\n\t") \
  FSTP("s_memtime s[92:93]\n\t") \
  FSTS("s_memrealtime s[94:95]\n\t") \
  FSTS("s_waitcnt lgkmcnt(0)\n\t") \
  FSTS("v_writelane_b32 v255, s94, 14\n\t") \
  "v_mfma_f32_16x16x32_bf16 a[64:67], a[144:147], a[0:3], a[64:67]\n\t" \
  "buffer_load_dwordx4 a[224:227], v248, s[16:19], s97 offen sc1\n\t" \
  "v_mfma_f32_16x16x32_bf16 a[68:71], a[144:147], a[4:7], a[68:71]\n\t" \
  "s_bfe_u32 s39, s2, 0x30003\n\t" \
  "ds_read_b64_tr_b16 a[160:161], v184 offset:24576\n\t" \
  "s_lshl_b32 s40, s39, 18\n\t" \
  "v_mfma_f32_16x16x32_bf16 a[72:75], a[144:147], a[8:11], a[72:75]\n\t" \
  "s_lshr_b32 s98, s2, 3\n\t" \
  "ds_read_b64_tr_b16 a[162:163], v184 offset:25600\n\t" \
  "v_accvgpr_read_b32 v16, a64\n\t" \
  "v_mfma_f32_16x16x32_bf16 a[76:79], a[144:147], a[12:15], a[76:79]\n\t" \
  "s_lshl_b32 s41, s3, 5\n\t" \
  "v_accvgpr_read_b32 v17, a65\n\t" \
  "v_accvgpr_read_b32 v18, a66\n\t" \
  "v_mfma_f32_16x16x32_bf16 a[92:95], a[148:151], a[12:15], a[92:95]\n\t" \
  ".Lhs2:\n\ts_cbranch_execz .Lhs2n\n\t" \
  "buffer_load_dwordx4 a[228:231], v249, s[16:19], s97 offen sc1\n\t" \
  "v_mfma_f32_16x16x32_bf16 a[88:91], a[148:151], a[8:11], a[88:91]\n\t" \
  "s_add_u32 s41, s41, s98\n\t" \
  "ds_read_b64_tr_b16 a[164:165], v185 offset:24576\n\t" \
  "v_accvgpr_read_b32 v19, a67\n\t" \
  "v_mfma_f32_16x16x32_bf16 a[84:87], a[148:151], a[4:7], a[84:87]\n\t" \
  "s_lshl_b32 s41, s41, 7\n\t" \
  "ds_read_b64_tr_b16 a[166:167], v185 offset:25600\n\t" \
  "v_accvgpr_read_b32 v48, a68\n\t" \
  "v_mfma_f32_16x16x32_bf16 a[80:83], a[148:151], a[0:3], a[80:83]\n\t" \
  "s_and_b32 s12, s2, 3\n\t" \
  "ds_read_b64_tr_b16 a[168:169], v186 offset:24576\n\t" \
  "v_accvgpr_read_b32 v49, a69\n\t" \
  "v_mfma_f32_16x16x32_bf16 a[96:99], a[152:155], a[0:3], a[96:99]\n\t" \
  "buffer_load_dwordx4 a[232:235], v250, s[16:19], s97 offen sc1\n\t" \
  "v_mfma_f32_16x16x32_bf16 a[100:103], a[152:155], a[4:7], a[100:103]\n\t" \
  "s_bfe_u32 s13, s2, 0x10002\n\t" \
  "ds_read_b64_tr_b16 a[170:171], v186 offset:25600\n\t" \
  "v_accvgpr_read_b32 v50, a70\n\t" \
  "v_mfma_f32_16x16x32_bf16 a[104:107], a[152:155], a[8:11], a[104:107]\n\t" \
  "s_lshl1_add_u32 s12, s12, s13\n\t" \
  "ds_read_b64_tr_b16 a[172:173], v187 offset:24576\n\t" \
  "v_accvgpr_read_b32 v51, a71\n\t" \
  "v_mfma_f32_16x16x32_bf16 a[108:111], a[152:155], a[12:15], a[108:111]\n\t" \
  "s_bfe_u32 s59, s2, 0x20003\n\t" \
  "v_accvgpr_read_b32 v80, a72\n\t" \
  "v_accvgpr_read_b32 v81, a73\n\t" \
  "v_mfma_f32_16x16x32_bf16 a[124:127], a[156:159], a[12:15], a[124:127]\n\t" \
  "buffer_load_dwordx4 a[236:239], v251, s[16:19], s97 offen sc1\n\t" \
  "v_mfma_f32_16x16x32_bf16 a[120:123], a[156:159], a[8:11], a[120:123]\n\t" \
  "s_add_u32 s96, s78, 4718592\n\t" \
  "s_and_b32 s96, s96, 0x780000\n\t" \
  "s_add_u32 m0, s76, 98304\n\t" \
  "v_mfma_f32_16x16x32_bf16 a[116:119], a[156:159], a[4:7], a[116:119]\n\t" \
  "s_or_b32 s59, s59, 24\n\t" \
  "ds_read_b64_tr_b16 a[174:175], v187 offset:25600\n\t" \
  "v_accvgpr_read_b32 v82, a74\n\t" \
  "v_mfma_f32_16x16x32_bf16 a[112:115], a[156:159], a[0:3], a[112:115]\n\t" \
  "buffer_load_dwordx4 v200, s[20:23], s96 offen offset:0 lds\n\t" \
  "v_mfma_f32_16x16x32_bf16 v[0:3], a[128:131], a[0:3], v[0:3]\n\t" \
  "ds_read_b64_tr_b16 a[176:177], v188 offset:24576\n\t" \
  "v_accvgpr_read_b32 v83, a75\n\t" \
  "v_mfma_f32_16x16x32_bf16 v[32:35], a[128:131], a[4:7], v[32:35]\n\t" \
  "ds_read_b64_tr_b16 a[178:179], v188 offset:25600\n\t" \
  "v_accvgpr_read_b32 v112, a76\n\t" \
  "v_mfma_f32_16x16x32_bf16 v[64:67], a[128:131], a[8:11], v[64:67]\n\t" \
  "ds_read_b64_tr_b16 a[180:181], v189 offset:24576\n\t" \
  "v_accvgpr_read_b32 v113, a77\n\t" \
  "v_mfma_f32_16x16x32_bf16 v[96:99], a[128:131], a[12:15], v[96:99]\n\t" \
  "buffer_load_dwordx4 v201, s[20:23], s96 offen offset:1024 lds\n\t" \
  "v_mfma_f32_16x16x32_bf16 v[100:103], a[132:135], a[12:15], v[100:103]\n\t" \
  "ds_read_b64_tr_b16 a[182:183], v189 offset:25600\n\t" \
  "v_accvgpr_read_b32 v114, a78\n\t" \
  "v_mfma_f32_16x16x32_bf16 v[68:71], a[132:135], a[8:11], v[68:71]\n\t" \
  "ds_read_b64_tr_b16 a[184:185], v190 offset:24576\n\t" \
  "v_accvgpr_read_b32 v115, a79\n\t" \
  "v_mfma_f32_16x16x32_bf16 v[36:39], a[132:135], a[4:7], v[36:39]\n\t" \
  "ds_read_b64_tr_b16 a[186:187], v190 offset:25600\n\t" \
  "v_accvgpr_read_b32 v116, a92\n\t" \
  "v_mfma_f32_16x16x32_bf16 v[4:7], a[132:135], a[0:3], v[4:7]\n\t" \
  "buffer_load_dwordx4 v202, s[20:23], s96 offen offset:2048 lds\n\t" \
  "v_mfma_f32_16x16x32_bf16 v[8:11], a[136:139], a[0:3], v[8:11]\n\t" \
  "ds_read_b64_tr_b16 a[188:189], v191 offset:24576\n\t" \
  "v_accvgpr_read_b32 v117, a93\n\t" \
  "v_mfma_f32_16x16x32_bf16 v[40:43], a[136:139], a[4:7], v[40:43]\n\t" \
  "ds_read_b64_tr_b16 a[190:191], v191 offset:25600\n\t" \
  "v_accvgpr_read_b32 v118, a94\n\t" \
  "v_mfma_f32_16x16x32_bf16 v[72:75], a[136:139], a[8:11], v[72:75]\n\t" \
  "v_accvgpr_read_b32 v119, a95\n\t" \
  "v_accvgpr_read_b32 v84, a88\n\t" \
  "v_mfma_f32_16x16x32_bf16 v[104:107], a[136:139], a[12:15], v[104:107]\n\t" \
  "buffer_load_dwordx4 v203, s[20:23], s96 offen offset:3072 lds\n\t" \
  "v_mfma_f32_16x16x32_bf16 v[108:111], a[140:143], a[12:15], v[108:111]\n\t" \
  "v_accvgpr_read_b32 v85, a89\n\t" \
  "v_accvgpr_read_b32 v86, a90\n\t" \
  "v_mfma_f32_16x16x32_bf16 v[76:79], a[140:143], a[8:11], v[76:79]\n\t" \
  "s_add_u32 s97, s97, s83\n\t" \
  "v_accvgpr_read_b32 v87, a91\n\t" \
  "v_accvgpr_read_b32 v52, a84\n\t" \
  "v_mfma_f32_16x16x32_bf16 v[44:47], a[140:143], a[4:7], v[44:47]\n\t" \
  "s_waitcnt vmcnt(24) lgkmcnt(0)\n\t" \
  "v_accvgpr_read_b32 v53, a85\n\t" \
  "v_accvgpr_read_b32 v54, a86\n\t" \
  "v_mfma_f32_16x16x32_bf16 v[12:15], a[140:143], a[0:3], v[12:15]\n\t" \
  "s_barrier\n\t" \
  "v_mfma_f32_16x16x32_bf16 a[96:99], a[184:187], a[16:19], a[96:99]\n\t" \
  "buffer_load_dwordx4 a[240:243], v248, s[16:19], s97 offen sc1\n\t" \
  "v_mfma_f32_16x16x32_bf16 a[100:103], a[184:187], a[20:23], a[100:103]\n\t" \
  "ds_read_b64_tr_b16 a[128:129], v184 offset:32768\n\t" \
  "v_accvgpr_read_b32 v55, a87\n\t" \
  "v_mfma_f32_16x16x32_bf16 a[104:107], a[184:187], a[24:27], a[104:107]\n\t" \
  "ds_read_b64_tr_b16 a[130:131], v184 offset:33792\n\t" \
  "v_accvgpr_read_b32 v20, a80\n\t" \
  "v_mfma_f32_16x16x32_bf16 a[108:111], a[184:187], a[28:31], a[108:111]\n\t" \
  "v_accvgpr_read_b32 v21, a81\n\t" \
  "v_accvgpr_read_b32 v22, a82\n\t" \
  "v_mfma_f32_16x16x32_bf16 a[124:127], a[188:191], a[28:31], a[124:127]\n\t" \
  "buffer_load_dwordx4 a[244:247], v249, s[16:19], s97 offen sc1\n\t" \
  "v_mfma_f32_16x16x32_bf16 a[120:123], a[188:191], a[24:27], a[120:123]\n\t" \
  "ds_read_b64_tr_b16 a[132:133], v185 offset:32768\n\t" \
  "v_accvgpr_read_b32 v23, a83\n\t" \
  "v_mfma_f32_16x16x32_bf16 a[116:119], a[188:191], a[20:23], a[116:119]\n\t" \
  "ds_read_b64_tr_b16 a[134:135], v185 offset:33792\n\t" \
  "v_accvgpr_read_b32 v24, a96\n\t" \
  "v_mfma_f32_16x16x32_bf16 a[112:115], a[188:191], a[16:19], a[112:115]\n\t" \
  "ds_read_b64_tr_b16 a[136:137], v186 offset:32768\n\t" \
  "v_accvgpr_read_b32 v25, a97\n\t" \
  "v_mfma_f32_16x16x32_bf16 v[0:3], a[160:163], a[16:19], v[0:3]\n\t" \
  "buffer_load_dwordx4 a[248:251], v250, s[16:19], s97 offen sc1\n\t" \
  "v_mfma_f32_16x16x32_bf16 v[32:35], a[160:163], a[20:23], v[32:35]\n\t" \
  "ds_read_b64_tr_b16 a[138:139], v186 offset:33792\n\t" \
  "v_accvgpr_read_b32 v26, a98\n\t" \
  "v_mfma_f32_16x16x32_bf16 v[64:67], a[160:163], a[24:27], v[64:67]\n\t" \
  "ds_read_b64_tr_b16 a[140:141], v187 offset:32768\n\t" \
  "v_accvgpr_read_b32 v27, a99\n\t" \
  "v_mfma_f32_16x16x32_bf16 v[96:99], a[160:163], a[28:31], v[96:99]\n\t" \
  "v_accvgpr_read_b32 v56, a100\n\t" \
  "v_accvgpr_read_b32 v57, a101\n\t" \
  "v_mfma_f32_16x16x32_bf16 v[100:103], a[164:167], a[28:31], v[100:103]\n\t" \
  "buffer_load_dwordx4 a[252:255], v251, s[16:19], s97 offen sc1\n\t" \
  "v_mfma_f32_16x16x32_bf16 v[68:71], a[164:167], a[24:27], v[68:71]\n\t" \
  "s_add_u32 s97, s78, 4194304\n\t" \
  "s_bfe_u32 s97, s97, 0x140003\n\t" \
  "s_add_u32 s97, s97, s82\n\t" \
  "v_mfma_f32_16x16x32_bf16 v[36:39], a[164:167], a[20:23], v[36:39]\n\t" \
  "ds_read_b64_tr_b16 a[142:143], v187 offset:33792\n\t" \
  "v_accvgpr_read_b32 v58, a102\n\t" \
  "v_mfma_f32_16x16x32_bf16 v[4:7], a[164:167], a[16:19], v[4:7]\n\t" \
  "buffer_load_dwordx4 a[0:3], v248, s[16:19], s97 offen sc1\n\t" \
  "v_mfma_f32_16x16x32_bf16 v[8:11], a[168:171], a[16:19], v[8:11]\n\t" \
  "ds_read_b64_tr_b16 a[144:145], v188 offset:32768\n\t" \
  "v_accvgpr_read_b32 v59, a103\n\t" \
  "v_mfma_f32_16x16x32_bf16 v[40:43], a[168:171], a[20:23], v[40:43]\n\t" \
  "ds_read_b64_tr_b16 a[146:147], v188 offset:33792\n\t" \
  "v_accvgpr_read_b32 v88, a104\n\t" \
  "v_mfma_f32_16x16x32_bf16 v[72:75], a[168:171], a[24:27], v[72:75]\n\t" \
  "ds_read_b64_tr_b16 a[148:149], v189 offset:32768\n\t" \
  "v_accvgpr_read_b32 v89, a105\n\t" \
  "v_mfma_f32_16x16x32_bf16 v[104:107], a[168:171], a[28:31], v[104:107]\n\t" \
  "buffer_load_dwordx4 a[4:7], v249, s[16:19], s97 offen sc1\n\t" \
  "v_mfma_f32_16x16x32_bf16 v[108:111], a[172:175], a[28:31], v[108:111]\n\t" \
  "ds_read_b64_tr_b16 a[150:151], v189 offset:33792\n\t" \
  "v_accvgpr_read_b32 v90, a106\n\t" \
  "v_mfma_f32_16x16x32_bf16 v[76:79], a[172:175], a[24:27], v[76:79]\n\t" \
  "ds_read_b64_tr_b16 a[152:153], v190 offset:32768\n\t" \
  "v_accvgpr_read_b32 v91, a107\n\t" \
  "v_mfma_f32_16x16x32_bf16 v[44:47], a[172:175], a[20:23], v[44:47]\n\t" \
  "ds_read_b64_tr_b16 a[154:155], v190 offset:33792\n\t" \
  "v_accvgpr_read_b32 v120, a108\n\t" \
  "v_mfma_f32_16x16x32_bf16 v[12:15], a[172:175], a[16:19], v[12:15]\n\t" \
  "buffer_load_dwordx4 a[8:11], v250, s[16:19], s97 offen sc1\n\t" \
  "v_mfma_f32_16x16x32_bf16 v[16:19], a[176:179], a[16:19], v[16:19]\n\t" \
  "ds_read_b64_tr_b16 a[156:157], v191 offset:32768\n\t" \
  "v_accvgpr_read_b32 v121, a109\n\t" \
  "v_mfma_f32_16x16x32_bf16 v[48:51], a[176:179], a[20:23], v[48:51]\n\t" \
  "ds_read_b64_tr_b16 a[158:159], v191 offset:33792\n\t" \
  "v_accvgpr_read_b32 v122, a110\n\t" \
  "v_mfma_f32_16x16x32_bf16 v[80:83], a[176:179], a[24:27], v[80:83]\n\t" \
  "v_accvgpr_read_b32 v123, a111\n\t" \
  "v_accvgpr_read_b32 v124, a124\n\t" \
  "v_mfma_f32_16x16x32_bf16 v[112:115], a[176:179], a[28:31], v[112:115]\n\t" \
  "buffer_load_dwordx4 a[12:15], v251, s[16:19], s97 offen sc1\n\t" \
  "v_mfma_f32_16x16x32_bf16 v[116:119], a[180:183], a[28:31], v[116:119]\n\t" \
  "v_mbcnt_lo_u32_b32 v217, -1, 0\n\t" \
  "v_mbcnt_hi_u32_b32 v217, -1, v217\n\t" \
  "v_mfma_f32_16x16x32_bf16 v[84:87], a[180:183], a[24:27], v[84:87]\n\t" \
  "v_cmp_gt_u32 vcc, 16, v217\n\t" \
  "v_lshlrev_b32 v217, 7, v217\n\t" \
  "v_mfma_f32_16x16x32_bf16 v[52:55], a[180:183], a[20:23], v[52:55]\n\t" \
  "v_bfrev_b32 v215, 1\n\t" \
  "v_cndmask_b32 v217, v215, v217, vcc\n\t" \
  "v_mfma_f32_16x16x32_bf16 v[20:23], a[180:183], a[16:19], v[20:23]\n\t" \
  "s_waitcnt vmcnt(28) lgkmcnt(0)\n\t" \
  "v_bfe_u32 v215, v217, 10, 1\n\t" \
  "s_mov_b32 s99, 11264\n\t" \
  "v_mfma_f32_16x16x32_bf16 v[0:3], a[128:131], a[32:35], v[0:3]\n\t" \
  "v_mad_u32_u24 v215, v215, s99, v217\n\t" \
  "s_add_u32 s97, s97, s83\n\t" \
  "v_mfma_f32_16x16x32_bf16 v[32:35], a[128:131], a[36:39], v[32:35]\n\t" \
  "buffer_load_dwordx4 a[16:19], v248, s[16:19], s97 offen sc1\n\t" \
  "v_mfma_f32_16x16x32_bf16 v[64:67], a[128:131], a[40:43], v[64:67]\n\t" \
  "ds_read_b64_tr_b16 a[160:161], v184 offset:40960\n\t" \
  "v_accvgpr_read_b32 v125, a125\n\t" \
  "v_mfma_f32_16x16x32_bf16 v[96:99], a[128:131], a[44:47], v[96:99]\n\t" \
  "ds_read_b64_tr_b16 a[162:163], v184 offset:41984\n\t" \
  "v_accvgpr_read_b32 v126, a126\n\t" \
  "v_mfma_f32_16x16x32_bf16 v[100:103], a[132:135], a[44:47], v[100:103]\n\t" \
  "buffer_load_dwordx4 a[20:23], v249, s[16:19], s97 offen sc1\n\t" \
  "v_mfma_f32_16x16x32_bf16 v[68:71], a[132:135], a[40:43], v[68:71]\n\t" \
  "ds_read_b64_tr_b16 a[164:165], v185 offset:40960\n\t" \
  "v_accvgpr_read_b32 v127, a127\n\t" \
  "v_mfma_f32_16x16x32_bf16 v[36:39], a[132:135], a[36:39], v[36:39]\n\t" \
  "ds_read_b64_tr_b16 a[166:167], v185 offset:41984\n\t" \
  "v_accvgpr_read_b32 v92, a120\n\t" \
  "v_mfma_f32_16x16x32_bf16 v[4:7], a[132:135], a[32:35], v[4:7]\n\t" \
  "ds_read_b64_tr_b16 a[168:169], v186 offset:40960\n\t" \
  "v_accvgpr_read_b32 v93, a121\n\t" \
  "v_mfma_f32_16x16x32_bf16 v[8:11], a[136:139], a[32:35], v[8:11]\n\t" \
  "buffer_load_dwordx4 a[24:27], v250, s[16:19], s97 offen sc1\n\t" \
  "v_mfma_f32_16x16x32_bf16 v[40:43], a[136:139], a[36:39], v[40:43]\n\t" \
  "ds_read_b64_tr_b16 a[170:171], v186 offset:41984\n\t" \
  "v_accvgpr_read_b32 v94, a122\n\t" \
  "v_mfma_f32_16x16x32_bf16 v[72:75], a[136:139], a[40:43], v[72:75]\n\t" \
  "ds_read_b64_tr_b16 a[172:173], v187 offset:40960\n\t" \
  "v_accvgpr_read_b32 v95, a123\n\t" \
  "v_mfma_f32_16x16x32_bf16 v[104:107], a[136:139], a[44:47], v[104:107]\n\t" \
  "v_accvgpr_read_b32 v60, a116\n\t" \
  "v_accvgpr_read_b32 v61, a117\n\t" \
  "v_mfma_f32_16x16x32_bf16 v[108:111], a[140:143], a[44:47], v[108:111]\n\t" \
  "buffer_load_dwordx4 a[28:31], v251, s[16:19], s97 offen sc1\n\t" \
  "v_mfma_f32_16x16x32_bf16 v[76:79], a[140:143], a[40:43], v[76:79]\n\t" \
  "s_add_u32 s96, s78, 5242880\n\t" \
  "s_and_b32 s96, s96, 0x780000\n\t" \
  "s_add_u32 m0, s76, 0\n\t" \
  "v_mfma_f32_16x16x32_bf16 v[44:47], a[140:143], a[36:39], v[44:47]\n\t" \
  "ds_read_b64_tr_b16 a[174:175], v187 offset:41984\n\t" \
  "v_accvgpr_read_b32 v62, a118\n\t" \
  "v_mfma_f32_16x16x32_bf16 v[12:15], a[140:143], a[32:35], v[12:15]\n\t" \
  "buffer_load_dwordx4 v200, s[20:23], s96 offen offset:0 lds\n\t" \
  "v_mfma_f32_16x16x32_bf16 v[16:19], a[144:147], a[32:35], v[16:19]\n\t" \
  "ds_read_b64_tr_b16 a[176:177], v188 offset:40960\n\t" \
  "v_accvgpr_read_b32 v63, a119\n\t" \
  "v_mfma_f32_16x16x32_bf16 v[48:51], a[144:147], a[36:39], v[48:51]\n\t" \
  "ds_read_b64_tr_b16 a[178:179], v188 offset:41984\n\t" \
  "v_accvgpr_read_b32 v28, a112\n\t" \
  "v_mfma_f32_16x16x32_bf16 v[80:83], a[144:147], a[40:43], v[80:83]\n\t" \
  "ds_read_b64_tr_b16 a[180:181], v189 offset:40960\n\t" \
  "v_accvgpr_read_b32 v29, a113\n\t" \
  "v_mfma_f32_16x16x32_bf16 v[112:115], a[144:147], a[44:47], v[112:115]\n\t" \
  "buffer_load_dwordx4 v201, s[20:23], s96 offen offset:1024 lds\n\t" \
  "v_mfma_f32_16x16x32_bf16 v[116:119], a[148:151], a[44:47], v[116:119]\n\t" \
  "ds_read_b64_tr_b16 a[182:183], v189 offset:41984\n\t" \
  "v_accvgpr_read_b32 v30, a114\n\t" \
  "v_mfma_f32_16x16x32_bf16 v[84:87], a[148:151], a[40:43], v[84:87]\n\t" \
  "ds_read_b64_tr_b16 a[184:185], v190 offset:40960\n\t" \
  "v_accvgpr_read_b32 v31, a115\n\t" \
  "v_mfma_f32_16x16x32_bf16 v[52:55], a[148:151], a[36:39], v[52:55]\n\t" \
  "ds_read_b64_tr_b16 a[186:187], v190 offset:41984\n\t" \
  "v_mfma_f32_16x16x32_bf16 v[20:23], a[148:151], a[32:35], v[20:23]\n\t" \
  "buffer_load_dwordx4 v202, s[20:23], s96 offen offset:2048 lds\n\t" \
  "v_mfma_f32_16x16x32_bf16 v[24:27], a[152:155], a[32:35], v[24:27]\n\t" \
  "ds_read_b64_tr_b16 a[188:189], v191 offset:40960\n\t" \
  "v_mfma_f32_16x16x32_bf16 v[56:59], a[152:155], a[36:39], v[56:59]\n\t" \
  "ds_read_b64_tr_b16 a[190:191], v191 offset:41984\n\t" \
  "v_mfma_f32_16x16x32_bf16 v[88:91], a[152:155], a[40:43], v[88:91]\n\t" \
  "s_cmp_lt_u32 s98, 24\n\t" \
  "s_cselect_b32 s59, s98, s59\n\t" \
  "s_mul_i32 s53, s98, 57344\n\t" \
  "v_mfma_f32_16x16x32_bf16 v[120:123], a[152:155], a[44:47], v[120:123]\n\t" \
  "buffer_load_dwordx4 v203, s[20:23], s96 offen offset:3072 lds\n\t" \
  "v_mfma_f32_16x16x32_bf16 v[124:127], a[156:159], a[44:47], v[124:127]\n\t" \
  "s_lshl_b32 s14, s59, 5\n\t" \
  "s_sub_u32 s53, s53, s14\n\t" \
  "s_lshl_b32 s52, s98, 16\n\t" \
  "v_mfma_f32_16x16x32_bf16 v[92:95], a[156:159], a[40:43], v[92:95]\n\t" \
  "s_lshl_b32 s14, s3, 14\n\t" \
  "s_add_u32 s52, s52, s14\n\t" \
  "s_lshl_b32 s14, s13, 13\n\t" \
  "v_mfma_f32_16x16x32_bf16 v[60:63], a[156:159], a[36:39], v[60:63]\n\t" \
  "s_waitcnt vmcnt(32) lgkmcnt(0)\n\t" \
  "s_mov_b32 s99, 0\n\t" \
  "s_add_u32 s52, s52, s14\n\t" \
  "v_mfma_f32_16x16x32_bf16 v[28:31], a[156:159], a[32:35], v[28:31]\n\t" \
  "s_barrier\n\t" \
  "v_mfma_f32_16x16x32_bf16 v[0:3], a[160:163], a[48:51], v[0:3]\n\t" \
  "buffer_load_dwordx4 v[136:139], v209, s[8:11], s99 offen nt\n\t" \
  "v_mfma_f32_16x16x32_bf16 v[32:35], a[160:163], a[52:55], v[32:35]\n\t" \
  "s_add_u32 s52, s52, s78\n\t" \
  "ds_read_b64_tr_b16 a[128:129], v184 offset:49152\n\t" \
  "s_lshl_b32 s51, s3, 15\n\t" \
  "v_mfma_f32_16x16x32_bf16 v[64:67], a[160:163], a[56:59], v[64:67]\n\t" \
  "s_add_u32 s51, s51, s78\n\t" \
  "ds_read_b64_tr_b16 a[130:131], v184 offset:50176\n\t" \
  "s_lshl_b32 s14, s39, 8\n\t" \
  "v_mfma_f32_16x16x32_bf16 v[96:99], a[160:163], a[60:63], v[96:99]\n\t" \
  "s_add_u32 s51, s51, s14\n\t" \
  "ds_read_b64_tr_b16 a[132:133], v185 offset:49152\n\t" \
  "s_lshr_b32 s98, s98, 2\n\t" \
  "v_mfma_f32_16x16x32_bf16 v[100:103], a[164:167], a[60:63], v[100:103]\n\t" \
  "s_add_u32 s13, s13, 6\n\t" \
  "ds_read_b64_tr_b16 a[134:135], v185 offset:50176\n\t" \
  "s_mov_b32 s99, 12288\n\t" \
  "v_mfma_f32_16x16x32_bf16 v[68:71], a[164:167], a[56:59], v[68:71]\n\t" \
  "s_cmp_eq_u32 s98, s13\n\t" \
  "s_cselect_b32 s57, 0x80000000, 0\n\t" \
  "s_cselect_b32 s30, 0, 0x8000\n\t" \
  "v_mfma_f32_16x16x32_bf16 v[36:39], a[164:167], a[52:55], v[36:39]\n\t" \
  "s_mul_i32 s14, s12, 28\n\t" \
  "ds_read_b64_tr_b16 a[136:137], v186 offset:49152\n\t" \
  "s_add_u32 s59, s59, s14\n\t" \
  "v_mfma_f32_16x16x32_bf16 v[4:7], a[164:167], a[48:51], v[4:7]\n\t" \
  "s_lshl_b32 s14, s59, 15\n\t" \
  "s_add_u32 s28, s28, s14\n\t" \
  "s_addc_u32 s29, s29, 0\n\t" \
  "v_mfma_f32_16x16x32_bf16 v[8:11], a[168:171], a[48:51], v[8:11]\n\t" \
  "buffer_load_dwordx4 v[140:143], v209, s[8:11], s99 offen nt\n\t" \
  "v_mfma_f32_16x16x32_bf16 v[40:43], a[168:171], a[52:55], v[40:43]\n\t" \
  "s_mul_i32 s14, s3, 1835008\n\t" \
  "ds_read_b64_tr_b16 a[138:139], v186 offset:50176\n\t" \
  "s_lshl_b32 s59, s59, 5\n\t" \
  "v_mfma_f32_16x16x32_bf16 v[72:75], a[168:171], a[56:59], v[72:75]\n\t" \
  "s_mov_b32 s99, 24576\n\t" \
  "ds_read_b64_tr_b16 a[140:141], v187 offset:49152\n\t" \
  "s_add_u32 s14, s14, s59\n\t" \
  "v_mfma_f32_16x16x32_bf16 v[104:107], a[168:171], a[60:63], v[104:107]\n\t" \
  "s_add_u32 s68, s68, s14\n\t" \
  "s_addc_u32 s69, s69, 0\n\t" \
  "v_mfma_f32_16x16x32_bf16 v[108:111], a[172:175], a[60:63], v[108:111]\n\t" \
  "ds_read_b64_tr_b16 a[142:143], v187 offset:50176\n\t" \
  "v_mfma_f32_16x16x32_bf16 v[76:79], a[172:175], a[56:59], v[76:79]\n\t" \
  "ds_read_b64_tr_b16 a[144:145], v188 offset:49152\n\t" \
  "v_mfma_f32_16x16x32_bf16 v[44:47], a[172:175], a[52:55], v[44:47]\n\t" \
  "s_add_u32 s72, s72, s14\n\t" \
  "s_addc_u32 s73, s73, 0\n\t" \
  "s_sub_u32 s70, s70, s14\n\t" \
  "v_mfma_f32_16x16x32_bf16 v[12:15], a[172:175], a[48:51], v[12:15]\n\t" \
  "buffer_load_dwordx4 v[144:147], v209, s[8:11], s99 offen nt\n\t" \
  "v_mfma_f32_16x16x32_bf16 v[16:19], a[176:179], a[48:51], v[16:19]\n\t" \
  "s_sub_u32 s74, s74, s14\n\t" \
  "ds_read_b64_tr_b16 a[146:147], v188 offset:50176\n\t" \
  "s_lshr_b32 s14, s78, 2\n\t" \
  "v_mfma_f32_16x16x32_bf16 v[48:51], a[176:179], a[52:55], v[48:51]\n\t" \
  "ds_read_b64_tr_b16 a[148:149], v189 offset:49152\n\t" \
  "s_mov_b32 s99, 36864\n\t" \
  "v_mfma_f32_16x16x32_bf16 v[80:83], a[176:179], a[56:59], v[80:83]\n\t" \
  "ds_read_b64_tr_b16 a[150:151], v189 offset:50176\n\t" \
  "v_mfma_f32_16x16x32_bf16 v[112:115], a[176:179], a[60:63], v[112:115]\n\t" \
  "s_add_u32 s60, s60, s14\n\t" \
  "s_addc_u32 s61, s61, 0\n\t" \
  "v_mfma_f32_16x16x32_bf16 v[116:119], a[180:183], a[60:63], v[116:119]\n\t" \
  "ds_read_b64_tr_b16 a[152:153], v190 offset:49152\n\t" \
  "v_mfma_f32_16x16x32_bf16 v[84:87], a[180:183], a[56:59], v[84:87]\n\t" \
  "s_add_u32 s64, s64, s14\n\t" \
  "s_addc_u32 s65, s65, 0\n\t" \
  "s_lshl2_add_u32 s58, s2, s3\n\t" \
  "v_mfma_f32_16x16x32_bf16 v[52:55], a[180:183], a[52:55], v[52:55]\n\t" \
  "s_lshl_b32 s58, s58, 11\n\t" \
  "ds_read_b64_tr_b16 a[154:155], v190 offset:50176\n\t" \
  "s_bfe_u32 s13, s2, 0x10004\n\t" \
  "v_mfma_f32_16x16x32_bf16 v[20:23], a[180:183], a[48:51], v[20:23]\n\t" \
  "buffer_load_dwordx4 v[148:151], v209, s[8:11], s99 offen nt\n\t" \
  "v_mfma_f32_16x16x32_bf16 v[24:27], a[184:187], a[48:51], v[24:27]\n\t" \
  "s_lshl1_add_u32 s12, s12, s13\n\t" \
  "ds_read_b64_tr_b16 a[156:157], v191 offset:49152\n\t" \
  "s_lshl_b32 s13, s13, 13\n\t" \
  "v_mfma_f32_16x16x32_bf16 v[56:59], a[184:187], a[52:55], v[56:59]\n\t" \
  "s_lshl_b32 s14, s12, 7\n\t" \
  "ds_read_b64_tr_b16 a[158:159], v191 offset:50176\n\t" \
  "s_add_u32 s13, s13, s14\n\t" \
  "v_mfma_f32_16x16x32_bf16 v[88:91], a[184:187], a[56:59], v[88:91]\n\t" \
  "s_add_u32 s13, s13, 16384\n\t" \
  "s_lshl_b32 s12, s12, 21\n\t" \
  "s_add_u32 s12, s12, 8388608\n\t" \
  "v_mfma_f32_16x16x32_bf16 v[120:123], a[184:187], a[60:63], v[120:123]\n\t" \
  "s_bitcmp1_b32 s2, 3\n\t" \
  "s_cselect_b32 s91, s12, s13\n\t" \
  "s_lshr_b32 s12, s57, s3\n\t" \
  "v_mfma_f32_16x16x32_bf16 v[124:127], a[188:191], a[60:63], v[124:127]\n\t" \
  "s_cmp_lt_i32 s12, 0\n\t" \
  "s_cselect_b32 s91, s91, 0x80000000\n\t" \
  "s_lshl_b32 s90, s3, 7\n\t" \
  "v_mfma_f32_16x16x32_bf16 v[92:95], a[188:191], a[56:59], v[92:95]\n\t" \
  "s_add_u32 s90, s90, 0x220000\n\t" \
  "s_and_b32 s98, s2, 0xc0\n\t" \
  "s_lshl_b32 s98, s98, 9\n\t" \
  "v_mfma_f32_16x16x32_bf16 v[60:63], a[188:191], a[52:55], v[60:63]\n\t" \
  "s_add_u32 s90, s90, s98\n\t" \
  "s_and_b32 s98, s2, 0x3\n\t" \
  "s_lshl_b32 s98, s98, 13\n\t" \
  "v_mfma_f32_16x16x32_bf16 v[28:31], a[188:191], a[48:51], v[28:31]\n\t" \
  "s_waitcnt vmcnt(32) lgkmcnt(0)\n\t" \
  "s_add_u32 s90, s90, s98\n\t" \
  "s_and_b32 s98, s2, 0x4\n\t" \
  "v_mfma_f32_16x16x32_bf16 v[0:3], a[128:131], a[192:195], v[0:3]\n\t" \
  "s_lshl_b32 s98, s98, 10\n\t" \
  "ds_read_b64_tr_b16 a[160:161], v184 offset:57344\n\t" \
  "s_add_u32 s90, s90, s98\n\t" \
  "v_mfma_f32_16x16x32_bf16 v[32:35], a[128:131], a[196:199], v[32:35]\n\t" \
  "s_and_b32 s98, s2, 0x38\n\t" \
  "ds_read_b64_tr_b16 a[162:163], v184 offset:58368\n\t" \
  "s_lshl_b32 s98, s98, 6\n\t" \
  "v_mfma_f32_16x16x32_bf16 v[64:67], a[128:131], a[200:203], v[64:67]\n\t" \
  "s_add_u32 s90, s90, s98\n\t" \
  "ds_read_b64_tr_b16 a[164:165], v185 offset:57344\n\t" \
  "s_lshr_b32 s98, s2, 2\n\t" \
  "v_mfma_f32_16x16x32_bf16 v[96:99], a[128:131], a[204:207], v[96:99]\n\t" \
  "s_xor_b32 s98, s98, s3\n\t" \
  "ds_read_b64_tr_b16 a[166:167], v185 offset:58368\n\t" \
  "s_lshl_b32 s98, s98, 13\n\t" \
  "v_mfma_f32_16x16x32_bf16 v[100:103], a[132:135], a[204:207], v[100:103]\n\t" \
  "s_and_b32 s42, s98, 0x2000\n\t" \
  "ds_read_b64_tr_b16 a[168:169], v186 offset:57344\n\t" \
  "s_lshr_b32 s98, s2, 6\n\t" \
  "v_mfma_f32_16x16x32_bf16 v[68:71], a[132:135], a[200:203], v[68:71]\n\t" \
  "s_lshl_b32 s98, s98, 22\n\t" \
  "ds_read_b64_tr_b16 a[170:171], v186 offset:58368\n\t" \
  "s_add_u32 s42, s42, s98\n\t" \
  "v_mfma_f32_16x16x32_bf16 v[36:39], a[132:135], a[196:199], v[36:39]\n\t" \
  "s_lshl_b32 s47, s3, 15\n\t" \
  "ds_read_b64_tr_b16 a[172:173], v187 offset:57344\n\t" \
  "s_add_u32 s47, s47, 0x1800000\n\t" \
  "v_mfma_f32_16x16x32_bf16 v[4:7], a[132:135], a[192:195], v[4:7]\n\t" \
  "s_add_u32 s47, s47, s42\n\t" \
  "ds_read_b64_tr_b16 a[174:175], v187 offset:58368\n\t" \
  "s_and_b32 s98, s2, 0x38\n\t" \
  "v_mfma_f32_16x16x32_bf16 v[8:11], a[136:139], a[192:195], v[8:11]\n\t" \
  "s_lshl_b32 s98, s98, 16\n\t" \
  "ds_read_b64_tr_b16 a[176:177], v188 offset:57344\n\t" \
  "s_add_u32 s47, s47, s98\n\t" \
  "v_mfma_f32_16x16x32_bf16 v[40:43], a[136:139], a[196:199], v[40:43]\n\t" \
  "s_add_u32 s46, s42, 0x800000\n\t" \
  "ds_read_b64_tr_b16 a[178:179], v188 offset:58368\n\t" \
  "s_and_b32 s98, s2, 0x3\n\t" \
  "v_mfma_f32_16x16x32_bf16 v[72:75], a[136:139], a[200:203], v[72:75]\n\t" \
  "s_lshl_b32 s98, s98, 20\n\t" \
  "ds_read_b64_tr_b16 a[180:181], v189 offset:57344\n\t" \
  "s_add_u32 s46, s46, s98\n\t" \
  "v_mfma_f32_16x16x32_bf16 v[104:107], a[136:139], a[204:207], v[104:107]\n\t" \
  "s_and_b32 s98, s3, 2\n\t" \
  "ds_read_b64_tr_b16 a[182:183], v189 offset:58368\n\t" \
  "s_lshl_b32 s98, s98, 13\n\t" \
  "v_mfma_f32_16x16x32_bf16 v[108:111], a[140:143], a[204:207], v[108:111]\n\t" \
  "s_add_u32 s46, s46, s98\n\t" \
  "ds_read_b64_tr_b16 a[184:185], v190 offset:57344\n\t" \
  "s_and_b32 s98, s3, 1\n\t" \
  "v_mfma_f32_16x16x32_bf16 v[76:79], a[140:143], a[200:203], v[76:79]\n\t" \
  "s_lshl_b32 s98, s98, 12\n\t" \
  "ds_read_b64_tr_b16 a[186:187], v190 offset:58368\n\t" \
  "s_add_u32 s46, s46, s98\n\t" \
  "v_mfma_f32_16x16x32_bf16 v[44:47], a[140:143], a[196:199], v[44:47]\n\t" \
  "s_lshr_b32 s45, s87, 2\n\t" \
  "ds_read_b64_tr_b16 a[188:189], v191 offset:57344\n\t" \
  "s_add_u32 s45, s45, s46\n\t" \
  "v_mfma_f32_16x16x32_bf16 v[12:15], a[140:143], a[192:195], v[12:15]\n\t" \
  "s_and_b32 s98, s2, 0x38\n\t" \
  "ds_read_b64_tr_b16 a[190:191], v191 offset:58368\n\t" \
  "s_lshl_b32 s98, s98, 14\n\t" \
  "v_mfma_f32_16x16x32_bf16 v[16:19], a[144:147], a[192:195], v[16:19]\n\t" \
  "s_add_u32 s45, s45, s98\n\t" \
  "s_lshr_b32 s98, s2, 1\n\t" \
  "s_xor_b32 s98, s98, s3\n\t" \
  "v_mfma_f32_16x16x32_bf16 v[48:51], a[144:147], a[196:199], v[48:51]\n\t" \
  "s_lshl_b32 s98, s98, 13\n\t" \
  "s_and_b32 s42, s98, 0x2000\n\t" \
  "s_lshr_b32 s98, s2, 5\n\t" \
  "v_mfma_f32_16x16x32_bf16 v[80:83], a[144:147], a[200:203], v[80:83]\n\t" \
  "s_xor_b32 s98, s98, s3\n\t" \
  "s_lshl_b32 s98, s98, 13\n\t" \
  "s_and_b32 s43, s98, 0x2000\n\t" \
  "v_mfma_f32_16x16x32_bf16 v[112:115], a[144:147], a[204:207], v[112:115]\n\t" \
  "s_lshl_b32 s48, s3, 2\n\t" \
  "s_add_u32 s48, s48, 0x8000\n\t" \
  "s_and_b32 s98, s2, 0x80\n\t" \
  "v_mfma_f32_16x16x32_bf16 v[116:119], a[148:151], a[204:207], v[116:119]\n\t" \
  "s_lshl_b32 s98, s98, 7\n\t" \
  "s_add_u32 s48, s48, s98\n\t" \
  "s_and_b32 s98, s2, 0x4\n\t" \
  "v_mfma_f32_16x16x32_bf16 v[84:87], a[148:151], a[200:203], v[84:87]\n\t" \
  "s_lshl_b32 s98, s98, 11\n\t" \
  "s_add_u32 s48, s48, s98\n\t" \
  "s_and_b32 s98, s2, 0x78\n\t" \
  "v_mfma_f32_16x16x32_bf16 v[52:55], a[148:151], a[196:199], v[52:55]\n\t" \
  "s_lshl_b32 s98, s98, 6\n\t" \
  "s_add_u32 s48, s48, s98\n\t" \
  "s_lshl_b32 s49, s3, 7\n\t" \
  "v_mfma_f32_16x16x32_bf16 v[20:23], a[148:151], a[192:195], v[20:23]\n\t" \
  "s_add_u32 s49, s49, 0x20000\n\t" \
  "s_and_b32 s98, s2, 0xc0\n\t" \
  "s_lshl_b32 s98, s98, 9\n\t" \
  "v_mfma_f32_16x16x32_bf16 v[24:27], a[152:155], a[192:195], v[24:27]\n\t" \
  "s_add_u32 s49, s49, s98\n\t" \
  "s_and_b32 s98, s2, 0x2\n\t" \
  "s_lshl_b32 s98, s98, 13\n\t" \
  "v_mfma_f32_16x16x32_bf16 v[56:59], a[152:155], a[196:199], v[56:59]\n\t" \
  "s_add_u32 s49, s49, s98\n\t" \
  "s_and_b32 s98, s2, 0x4\n\t" \
  "s_lshl_b32 s98, s98, 11\n\t" \
  "v_mfma_f32_16x16x32_bf16 v[88:91], a[152:155], a[200:203], v[88:91]\n\t" \
  "s_add_u32 s49, s49, s98\n\t" \
  "s_and_b32 s98, s2, 0x1\n\t" \
  "s_lshl_b32 s98, s98, 12\n\t" \
  "v_mfma_f32_16x16x32_bf16 v[120:123], a[152:155], a[204:207], v[120:123]\n\t" \
  "s_add_u32 s49, s49, s98\n\t" \
  "s_lshl2_add_u32 s49, s39, s49\n\t" \
  "s_lshl_b32 s50, s3, 15\n\t" \
  "v_mfma_f32_16x16x32_bf16 v[124:127], a[156:159], a[204:207], v[124:127]\n\t" \
  "s_and_b32 s98, s2, 0x4\n\t" \
  "s_lshl_b32 s98, s98, 19\n\t" \
  "s_add_u32 s50, s50, s98\n\t" \
  "v_mfma_f32_16x16x32_bf16 v[92:95], a[156:159], a[200:203], v[92:95]\n\t" \
  "s_and_b32 s98, s2, 0xc0\n\t" \
  "s_lshl_b32 s98, s98, 13\n\t" \
  "s_add_u32 s50, s50, s98\n\t" \
  "v_mfma_f32_16x16x32_bf16 v[60:63], a[156:159], a[196:199], v[60:63]\n\t" \
  "s_waitcnt vmcnt(28) lgkmcnt(0)\n\t" \
  "s_add_u32 s97, s78, 4718592\n\t" \
  "s_bfe_u32 s97, s97, 0x140003\n\t" \
  "v_mfma_f32_16x16x32_bf16 v[28:31], a[156:159], a[192:195], v[28:31]\n\t" \
  "s_barrier\n\t" \
  "v_mfma_f32_16x16x32_bf16 v[0:3], a[160:163], a[208:211], v[0:3]\n\t" \
  "ds_read_b64_tr_b16 a[128:129], v192 offset:0\n\t" \
  "ds_read_b64_tr_b16 a[130:131], v192 offset:1024\n\t" \
  "ds_read_b64_tr_b16 a[132:133], v193 offset:0\n\t" \
  "s_add_u32 s97, s97, s82\n\t" \
  "v_mfma_f32_16x16x32_bf16 v[32:35], a[160:163], a[212:215], v[32:35]\n\t" \
  "buffer_load_dwordx4 a[32:35], v248, s[16:19], s97 offen sc1\n\t" \
  "ds_read_b64_tr_b16 a[134:135], v193 offset:1024\n\t" \
  "ds_read_b64_tr_b16 a[136:137], v194 offset:0\n\t" \
  "ds_read_b64_tr_b16 a[138:139], v194 offset:1024\n\t" \
  "v_mfma_f32_16x16x32_bf16 v[64:67], a[160:163], a[216:219], v[64:67]\n\t" \
  "ds_read_b64_tr_b16 a[140:141], v195 offset:0\n\t" \
  "ds_read_b64_tr_b16 a[142:143], v195 offset:1024\n\t" \
  "ds_read_b64_tr_b16 a[144:145], v196 offset:0\n\t" \
  "ds_read_b64_tr_b16 a[146:147], v196 offset:1024\n\t" \
  "v_mfma_f32_16x16x32_bf16 v[96:99], a[160:163], a[220:223], v[96:99]\n\t" \
  "ds_read_b64_tr_b16 a[148:149], v197 offset:0\n\t" \
  "ds_read_b64_tr_b16 a[150:151], v197 offset:1024\n\t" \
  "ds_read_b64_tr_b16 a[152:153], v198 offset:0\n\t" \
  "v_mbcnt_lo_u32_b32 v233, -1, 0\n\t" \
  "v_mfma_f32_16x16x32_bf16 v[100:103], a[164:167], a[220:223], v[100:103]\n\t" \
  "buffer_load_dwordx4 a[36:39], v249, s[16:19], s97 offen sc1\n\t" \
  "ds_read_b64_tr_b16 a[154:155], v198 offset:1024\n\t" \
  "s_and_b32 s98, s2, 0x3\n\t" \
  "v_mfma_f32_16x16x32_bf16 v[68:71], a[164:167], a[216:219], v[68:71]\n\t" \
  "s_lshl_b32 s98, s98, 17\n\t" \
  "ds_read_b64_tr_b16 a[156:157], v199 offset:0\n\t" \
  "v_mbcnt_hi_u32_b32 v233, -1, v233\n\t" \
  "v_mfma_f32_16x16x32_bf16 v[36:39], a[164:167], a[212:215], v[36:39]\n\t" \
  "s_add_u32 s50, s50, s98\n\t" \
  "ds_read_b64_tr_b16 a[158:159], v199 offset:1024\n\t" \
  "v_lshrrev_b32 v129, 4, v233\n\t" \
  "v_mfma_f32_16x16x32_bf16 v[4:7], a[164:167], a[208:211], v[4:7]\n\t" \
  "s_and_b32 s98, s2, 0x38\n\t" \
  "ds_read_b64_tr_b16 a[64:65], v192 offset:8192\n\t" \
  "v_and_b32 v130, 15, v233\n\t" \
  "v_mfma_f32_16x16x32_bf16 v[8:11], a[168:171], a[208:211], v[8:11]\n\t" \
  "buffer_load_dwordx4 a[40:43], v250, s[16:19], s97 offen sc1\n\t" \
  "v_mfma_f32_16x16x32_bf16 v[40:43], a[168:171], a[212:215], v[40:43]\n\t" \
  "s_lshl_b32 s98, s98, 5\n\t" \
  "ds_read_b64_tr_b16 a[66:67], v192 offset:9216\n\t" \
  "v_bfe_u32 v131, v233, 2, 2\n\t" \
  "v_mfma_f32_16x16x32_bf16 v[72:75], a[168:171], a[216:219], v[72:75]\n\t" \
  "s_add_u32 s50, s50, s98\n\t" \
  "ds_read_b64_tr_b16 a[68:69], v193 offset:8192\n\t" \
  "v_and_b32 v132, 3, v233\n\t" \
  "v_mfma_f32_16x16x32_bf16 v[104:107], a[168:171], a[220:223], v[104:107]\n\t" \
  "s_lshr_b32 s98, s2, 2\n\t" \
  "ds_read_b64_tr_b16 a[70:71], v193 offset:9216\n\t" \
  "v_cmp_lt_u32 vcc, 6, v233\n\t" \
  "v_mfma_f32_16x16x32_bf16 v[108:111], a[172:175], a[220:223], v[108:111]\n\t" \
  "buffer_load_dwordx4 a[44:47], v251, s[16:19], s97 offen sc1\n\t" \
  "v_mfma_f32_16x16x32_bf16 v[76:79], a[172:175], a[216:219], v[76:79]\n\t" \
  "s_add_u32 s96, s78, 5767168\n\t" \
  "s_and_b32 s96, s96, 0x780000\n\t" \
  "ds_read_b64_tr_b16 a[72:73], v194 offset:8192\n\t" \
  "s_add_u32 m0, s76, 16384\n\t" \
  "v_mfma_f32_16x16x32_bf16 v[44:47], a[172:175], a[212:215], v[44:47]\n\t" \
  "s_xor_b32 s98, s98, s2\n\t" \
  "ds_read_b64_tr_b16 a[74:75], v194 offset:9216\n\t" \
  "v_mov_b32 v133, 6272\n\t" \
  "v_mfma_f32_16x16x32_bf16 v[12:15], a[172:175], a[208:211], v[12:15]\n\t" \
  "buffer_load_dwordx4 v200, s[20:23], s96 offen offset:0 lds\n\t" \
  "v_mfma_f32_16x16x32_bf16 v[16:19], a[176:179], a[208:211], v[16:19]\n\t" \
  "s_and_b32 s98, s98, 4\n\t" \
  "ds_read_b64_tr_b16 a[76:77], v195 offset:8192\n\t" \
  "v_cndmask_b32 v133, 0, v133, vcc\n\t" \
  "v_mfma_f32_16x16x32_bf16 v[48:51], a[176:179], a[212:215], v[48:51]\n\t" \
  "s_lshl_b32 s44, s98, 4\n\t" \
  "ds_read_b64_tr_b16 a[78:79], v195 offset:9216\n\t" \
  "v_lshl_add_u32 v133, v233, 7, v133\n\t" \
  "v_mfma_f32_16x16x32_bf16 v[80:83], a[176:179], a[216:219], v[80:83]\n\t" \
  "s_and_b32 s98, s2, 0x8\n\t" \
  "ds_read_b64_tr_b16 a[80:81], v196 offset:8192\n\t" \
  "v_cmp_gt_u32 vcc, 14, v233\n\t" \
  "v_mfma_f32_16x16x32_bf16 v[112:115], a[176:179], a[220:223], v[112:115]\n\t" \
  "buffer_load_dwordx4 v201, s[20:23], s96 offen offset:1024 lds\n\t" \
  "v_mfma_f32_16x16x32_bf16 v[116:119], a[180:183], a[220:223], v[116:119]\n\t" \
  "s_lshl_b32 s98, s98, 2\n\t" \
  "ds_read_b64_tr_b16 a[82:83], v196 offset:9216\n\t" \
  "v_bfrev_b32 v247, 1\n\t" \
  "v_mfma_f32_16x16x32_bf16 v[84:87], a[180:183], a[216:219], v[84:87]\n\t" \
  "s_add_u32 s44, s44, s98\n\t" \
  "ds_read_b64_tr_b16 a[84:85], v197 offset:8192\n\t" \
  "v_cndmask_b32 v245, v247, v133, vcc\n\t" \
  "v_mfma_f32_16x16x32_bf16 v[52:55], a[180:183], a[212:215], v[52:55]\n\t" \
  "s_and_b32 s98, s2, 0x3\n\t" \
  "ds_read_b64_tr_b16 a[86:87], v197 offset:9216\n\t" \
  "v_writelane_b32 v247, s91, 0\n\t" \
  "v_mfma_f32_16x16x32_bf16 v[20:23], a[180:183], a[208:211], v[20:23]\n\t" \
  ".Lhs3:\n\ts_cbranch_execz .Lhs3n\n\t" \
  "buffer_load_dwordx4 v202, s[20:23], s96 offen offset:2048 lds\n\t" \
  "v_mfma_f32_16x16x32_bf16 v[24:27], a[184:187], a[208:211], v[24:27]\n\t" \
  "s_lshl_b32 s98, s98, 3\n\t" \
  "ds_read_b64_tr_b16 a[88:89], v198 offset:8192\n\t" \
  "v_lshrrev_b32 v133, 1, v129\n\t" \
  "v_mfma_f32_16x16x32_bf16 v[56:59], a[184:187], a[212:215], v[56:59]\n\t" \
  "s_add_u32 s44, s44, s98\n\t" \
  "ds_read_b64_tr_b16 a[90:91], v198 offset:9216\n\t" \
  "v_and_b32 v134, 1, v129\n\t" \
  "v_mfma_f32_16x16x32_bf16 v[88:91], a[184:187], a[216:219], v[88:91]\n\t" \
  "s_and_b32 s98, s2, 0x4\n\t" \
  "ds_read_b64_tr_b16 a[92:93], v199 offset:8192\n\t" \
  "v_lshl_or_b32 v133, v134, 1, v133\n\t" \
  "v_mfma_f32_16x16x32_bf16 v[120:123], a[184:187], a[220:223], v[120:123]\n\t" \
  "buffer_load_dwordx4 v203, s[20:23], s96 offen offset:3072 lds\n\t" \
  "v_mfma_f32_16x16x32_bf16 v[124:127], a[188:191], a[220:223], v[124:127]\n\t" \
  "s_lshl_b32 s98, s98, 0\n\t" \
  "ds_read_b64_tr_b16 a[94:95], v199 offset:9216\n\t" \
  "v_xor_b32 v133, v133, v130\n\t" \
  "v_mfma_f32_16x16x32_bf16 v[92:95], a[188:191], a[216:219], v[92:95]\n\t" \
  "s_add_u32 s44, s44, s98\n\t" \
  "v_lshlrev_b32 v133, 4, v133\n\t" \
  "v_lshl_add_u32 v133, v130, 8, v133\n\t" \
  "v_mfma_f32_16x16x32_bf16 v[60:63], a[188:191], a[212:215], v[60:63]\n\t" \
  "s_waitcnt vmcnt(24) lgkmcnt(0)\n\t" \
  "s_add_u32 s97, s97, s83\n\t" \
  "v_add_u32 v220, 49152, v133\n\t" \
  "v_mfma_f32_16x16x32_bf16 v[28:31], a[188:191], a[208:211], v[28:31]\n\t" \
  "s_barrier\n\t" \
  "v_mfma_f32_16x16x32_bf16 v[0:3], a[128:131], a[224:227], v[0:3]\n\t" \
  "buffer_load_dwordx4 a[48:51], v248, s[16:19], s97 offen sc1\n\t" \
  "v_mfma_f32_16x16x32_bf16 v[0:3], a[64:67], a[240:243], v[0:3]\n\t" \
  "s_add_u32 s44, s44, 0x2000\n\t" \
  "ds_read_b64_tr_b16 a[160:161], v192 offset:16384\n\t" \
  "v_xor_b32 v221, 64, v220\n\t" \
  "v_mfma_f32_16x16x32_bf16 v[32:35], a[64:67], a[244:247], v[32:35]\n\t" \
  "ds_read_b64_tr_b16 a[162:163], v192 offset:17408\n\t" \
  "v_xor_b32 v222, 128, v220\n\t" \
  "v_mfma_f32_16x16x32_bf16 v[32:35], a[128:131], a[228:231], v[32:35]\n\t" \
  "v_xor_b32 v223, 192, v220\n\t" \
  "v_xor_b32 v133, v130, v129\n\t" \
  "v_mfma_f32_16x16x32_bf16 v[64:67], a[128:131], a[232:235], v[64:67]\n\t" \
  "buffer_load_dwordx4 a[52:55], v249, s[16:19], s97 offen sc1\n\t" \
  "v_mfma_f32_16x16x32_bf16 v[64:67], a[64:67], a[248:251], v[64:67]\n\t" \
  "ds_read_b64_tr_b16 a[164:165], v193 offset:16384\n\t" \
  "v_lshlrev_b32 v133, 4, v133\n\t" \
  "v_mfma_f32_16x16x32_bf16 v[96:99], a[64:67], a[252:255], v[96:99]\n\t" \
  "ds_read_b64_tr_b16 a[166:167], v193 offset:17408\n\t" \
  "v_lshl_add_u32 v224, v129, 8, v133\n\t" \
  "v_mfma_f32_16x16x32_bf16 v[96:99], a[128:131], a[236:239], v[96:99]\n\t" \
  "ds_read_b64_tr_b16 a[168:169], v194 offset:16384\n\t" \
  "v_xor_b32 v225, 64, v224\n\t" \
  "v_mfma_f32_16x16x32_bf16 v[4:7], a[132:135], a[224:227], v[4:7]\n\t" \
  "buffer_load_dwordx4 a[56:59], v250, s[16:19], s97 offen sc1\n\t" \
  "v_mfma_f32_16x16x32_bf16 v[4:7], a[68:71], a[240:243], v[4:7]\n\t" \
  "ds_read_b64_tr_b16 a[170:171], v194 offset:17408\n\t" \
  "v_xor_b32 v226, 128, v224\n\t" \
  "v_mfma_f32_16x16x32_bf16 v[36:39], a[68:71], a[244:247], v[36:39]\n\t" \
  "ds_read_b64_tr_b16 a[172:173], v195 offset:16384\n\t" \
  "v_xor_b32 v227, 192, v224\n\t" \
  "v_mfma_f32_16x16x32_bf16 v[36:39], a[132:135], a[228:231], v[36:39]\n\t" \
  "v_lshlrev_b32 v133, 3, v132\n\t" \
  "v_lshl_add_u32 v133, v129, 10, v133\n\t" \
  "v_mfma_f32_16x16x32_bf16 v[68:71], a[132:135], a[232:235], v[68:71]\n\t" \
  "buffer_load_dwordx4 a[60:63], v251, s[16:19], s97 offen sc1\n\t" \
  "v_mfma_f32_16x16x32_bf16 v[68:71], a[68:71], a[248:251], v[68:71]\n\t" \
  "s_add_u32 s97, s78, 5242880\n\t" \
  "s_bfe_u32 s97, s97, 0x140003\n\t" \
  "s_add_u32 s97, s97, s82\n\t" \
  "v_mfma_f32_16x16x32_bf16 v[100:103], a[68:71], a[252:255], v[100:103]\n\t" \
  "ds_read_b64_tr_b16 a[174:175], v195 offset:17408\n\t" \
  "v_lshl_add_u32 v228, v131, 6, v133\n\t" \
  "v_mfma_f32_16x16x32_bf16 v[100:103], a[132:135], a[236:239], v[100:103]\n\t" \
  "buffer_load_dwordx4 a[192:195], v248, s[16:19], s97 offen sc1\n\t" \
  "v_mfma_f32_16x16x32_bf16 v[8:11], a[136:139], a[224:227], v[8:11]\n\t" \
  "ds_read_b64_tr_b16 a[176:177], v196 offset:16384\n\t" \
  "v_lshl_add_u32 v228, v134, 5, v228\n\t" \
  "v_mfma_f32_16x16x32_bf16 v[8:11], a[72:75], a[240:243], v[8:11]\n\t" \
  "ds_read_b64_tr_b16 a[178:179], v196 offset:17408\n\t" \
  "v_add_u32 v228, 81920, v228\n\t" \
  "v_mfma_f32_16x16x32_bf16 v[40:43], a[72:75], a[244:247], v[40:43]\n\t" \
  "ds_read_b64_tr_b16 a[180:181], v197 offset:16384\n\t" \
  "v_xor_b32 v229, 32, v228\n\t" \
  "v_mfma_f32_16x16x32_bf16 v[40:43], a[136:139], a[228:231], v[40:43]\n\t" \
  "buffer_load_dwordx4 a[196:199], v249, s[16:19], s97 offen sc1\n\t" \
  "v_mfma_f32_16x16x32_bf16 v[72:75], a[136:139], a[232:235], v[72:75]\n\t" \
  "ds_read_b64_tr_b16 a[182:183], v197 offset:17408\n\t" \
  "v_lshl_add_u32 v244, v131, 8, v133\n\t" \
  "v_mfma_f32_16x16x32_bf16 v[72:75], a[72:75], a[248:251], v[72:75]\n\t" \
  "ds_read_b64_tr_b16 a[184:185], v198 offset:16384\n\t" \
  "v_lshl_add_u32 v244, v131, 5, v244\n\t" \
  "v_mfma_f32_16x16x32_bf16 v[104:107], a[72:75], a[252:255], v[104:107]\n\t" \
  "ds_read_b64_tr_b16 a[186:187], v198 offset:17408\n\t" \
  "v_lshl_add_u32 v244, v134, 7, v244\n\t" \
  "v_mfma_f32_16x16x32_bf16 v[104:107], a[136:139], a[236:239], v[104:107]\n\t" \
  "buffer_load_dwordx4 a[200:203], v250, s[16:19], s97 offen sc1\n\t" \
  "v_mfma_f32_16x16x32_bf16 v[12:15], a[140:143], a[224:227], v[12:15]\n\t" \
  "ds_read_b64_tr_b16 a[188:189], v199 offset:16384\n\t" \
  "v_lshlrev_b32 v133, 4, v132\n\t" \
  "v_mfma_f32_16x16x32_bf16 v[12:15], a[76:79], a[240:243], v[12:15]\n\t" \
  "ds_read_b64_tr_b16 a[190:191], v199 offset:17408\n\t" \
  "v_lshl_add_u32 v133, v129, 6, v133\n\t" \
  "v_mfma_f32_16x16x32_bf16 v[44:47], a[76:79], a[244:247], v[44:47]\n\t" \
  "ds_read_b64_tr_b16 a[96:97], v192 offset:24576\n\t" \
  "v_lshl_add_u32 v230, v131, 8, v133\n\t" \
  "v_mfma_f32_16x16x32_bf16 v[44:47], a[140:143], a[228:231], v[44:47]\n\t" \
  "buffer_load_dwordx4 a[204:207], v251, s[16:19], s97 offen sc1\n\t" \
  "v_mfma_f32_16x16x32_bf16 v[76:79], a[140:143], a[232:235], v[76:79]\n\t" \
  "ds_read_b64_tr_b16 a[98:99], v192 offset:25600\n\t" \
  "v_xor_b32 v231, 32, v230\n\t" \
  "v_mfma_f32_16x16x32_bf16 v[76:79], a[76:79], a[248:251], v[76:79]\n\t" \
  "s_add_u32 s97, s97, s83\n\t" \
  "v_lshlrev_b32 v133, 1, v129\n\t" \
  "v_xor_b32 v133, v133, v130\n\t" \
  "v_mfma_f32_16x16x32_bf16 v[108:111], a[76:79], a[252:255], v[108:111]\n\t" \
  "ds_read_b64_tr_b16 a[100:101], v193 offset:24576\n\t" \
  "v_lshlrev_b32 v133, 4, v133\n\t" \
  "v_mfma_f32_16x16x32_bf16 v[108:111], a[140:143], a[236:239], v[108:111]\n\t" \
  "ds_read_b64_tr_b16 a[102:103], v193 offset:25600\n\t" \
  "v_lshl_add_u32 v240, v129, 11, v133\n\t" \
  "v_mfma_f32_16x16x32_bf16 v[16:19], a[144:147], a[224:227], v[16:19]\n\t" \
  "buffer_load_dwordx4 a[208:211], v248, s[16:19], s97 offen sc1\n\t" \
  "v_mfma_f32_16x16x32_bf16 v[16:19], a[80:83], a[240:243], v[16:19]\n\t" \
  "ds_read_b64_tr_b16 a[104:105], v194 offset:24576\n\t" \
  "v_xor_b32 v241, 0x80, v240\n\t" \
  "v_mfma_f32_16x16x32_bf16 v[48:51], a[80:83], a[244:247], v[48:51]\n\t" \
  "s_waitcnt vmcnt(20)\n\t" \
  "v_lshlrev_b32 v128, 16, v136\n\t" \
  "v_lshrrev_b32 v133, 1, v233\n\t" \
  "v_mfma_f32_16x16x32_bf16 v[48:51], a[144:147], a[228:231], v[48:51]\n\t" \
  "ds_read_b64_tr_b16 a[106:107], v194 offset:25600\n\t" \
  "v_mul_u32_u24 v133, 57344, v133\n\t" \
  "v_mfma_f32_16x16x32_bf16 v[80:83], a[144:147], a[232:235], v[80:83]\n\t" \
  "buffer_load_dwordx4 a[212:215], v249, s[16:19], s97 offen sc1\n\t" \
  "v_mfma_f32_16x16x32_bf16 v[80:83], a[80:83], a[248:251], v[80:83]\n\t" \
  "ds_read_b64_tr_b16 a[108:109], v195 offset:24576\n\t" \
  "v_and_b32 v134, 1, v233\n\t" \
  "v_mfma_f32_16x16x32_bf16 v[112:115], a[80:83], a[252:255], v[112:115]\n\t" \
  "ds_read_b64_tr_b16 a[110:111], v195 offset:25600\n\t" \
  "v_lshl_add_u32 v133, v134, 4, v133\n\t" \
  "v_mfma_f32_16x16x32_bf16 v[112:115], a[144:147], a[236:239], v[112:115]\n\t" \
  "ds_read_b64_tr_b16 a[112:113], v196 offset:24576\n\t" \
  "v_or_b32 v232, s57, v133\n\t" \
  "v_mfma_f32_16x16x32_bf16 v[20:23], a[148:151], a[224:227], v[20:23]\n\t" \
  "buffer_load_dwordx4 a[216:219], v250, s[16:19], s97 offen sc1\n\t" \
  "v_mfma_f32_16x16x32_bf16 v[20:23], a[84:87], a[240:243], v[20:23]\n\t" \
  "ds_read_b64_tr_b16 a[114:115], v196 offset:25600\n\t" \
  "v_and_b32 v136, 0xffff0000, v136\n\t" \
  "v_mfma_f32_16x16x32_bf16 v[52:55], a[84:87], a[244:247], v[52:55]\n\t" \
  "v_mul_f32 v176, v128, v168\n\t" \
  "v_mul_f32 v180, v136, v168\n\t" \
  "v_mfma_f32_16x16x32_bf16 v[52:55], a[148:151], a[228:231], v[52:55]\n\t" \
  "ds_read_b64_tr_b16 a[116:117], v197 offset:24576\n\t" \
  "v_lshlrev_b32 v128, 16, v137\n\t" \
  "v_mfma_f32_16x16x32_bf16 v[84:87], a[148:151], a[232:235], v[84:87]\n\t" \
  "buffer_load_dwordx4 a[220:223], v251, s[16:19], s97 offen sc1\n\t" \
  "v_mfma_f32_16x16x32_bf16 v[84:87], a[84:87], a[248:251], v[84:87]\n\t" \
  "s_add_u32 s96, s78, 6291456\n\t" \
  "s_and_b32 s96, s96, 0x780000\n\t" \
  "s_add_u32 m0, s76, 32768\n\t" \
  "v_mfma_f32_16x16x32_bf16 v[116:119], a[84:87], a[252:255], v[116:119]\n\t" \
  "ds_read_b64_tr_b16 a[118:119], v197 offset:25600\n\t" \
  "v_and_b32 v137, 0xffff0000, v137\n\t" \
  "v_mfma_f32_16x16x32_bf16 v[116:119], a[148:151], a[236:239], v[116:119]\n\t" \
  "buffer_load_dwordx4 v200, s[20:23], s96 offen offset:0 lds\n\t" \
  "v_mfma_f32_16x16x32_bf16 v[24:27], a[152:155], a[224:227], v[24:27]\n\t" \
  "ds_read_b64_tr_b16 a[120:121], v198 offset:24576\n\t" \
  "v_mul_f32 v177, v128, v168\n\t" \
  "v_mfma_f32_16x16x32_bf16 v[24:27], a[88:91], a[240:243], v[24:27]\n\t" \
  "ds_read_b64_tr_b16 a[122:123], v198 offset:25600\n\t" \
  "v_mul_f32 v181, v137, v168\n\t" \
  "v_mfma_f32_16x16x32_bf16 v[56:59], a[88:91], a[244:247], v[56:59]\n\t" \
  "ds_read_b64_tr_b16 a[124:125], v199 offset:24576\n\t" \
  "v_lshlrev_b32 v128, 16, v138\n\t" \
  "v_mfma_f32_16x16x32_bf16 v[56:59], a[152:155], a[228:231], v[56:59]\n\t" \
  "buffer_load_dwordx4 v201, s[20:23], s96 offen offset:1024 lds\n\t" \
  "v_mfma_f32_16x16x32_bf16 v[88:91], a[152:155], a[232:235], v[88:91]\n\t" \
  "ds_read_b64_tr_b16 a[126:127], v199 offset:25600\n\t" \
  "v_and_b32 v138, 0xffff0000, v138\n\t" \
  "v_mfma_f32_16x16x32_bf16 v[88:91], a[88:91], a[248:251], v[88:91]\n\t" \
  "v_mul_f32 v178, v128, v168\n\t" \
  "v_mul_f32 v182, v138, v168\n\t" \
  "v_mfma_f32_16x16x32_bf16 v[120:123], a[88:91], a[252:255], v[120:123]\n\t" \
  "v_lshlrev_b32 v128, 16, v139\n\t" \
  "v_and_b32 v139, 0xffff0000, v139\n\t" \
  "v_mfma_f32_16x16x32_bf16 v[120:123], a[152:155], a[236:239], v[120:123]\n\t" \
  "buffer_load_dwordx4 v202, s[20:23], s96 offen offset:2048 lds\n\t" \
  "v_mfma_f32_16x16x32_bf16 v[28:31], a[156:159], a[224:227], v[28:31]\n\t" \
  "v_mul_f32 v179, v128, v168\n\t" \
  "v_mul_f32 v183, v139, v168\n\t" \
  "v_mfma_f32_16x16x32_bf16 v[28:31], a[92:95], a[240:243], v[28:31]\n\t" \
  "s_waitcnt vmcnt(25)\n\t" \
  "v_lshlrev_b32 v128, 16, v140\n\t" \
  "v_and_b32 v140, 0xffff0000, v140\n\t" \
  "v_mfma_f32_16x16x32_bf16 v[60:63], a[92:95], a[244:247], v[60:63]\n\t" \
  "v_fma_f32 v176, v128, v169, v176\n\t" \
  "v_fma_f32 v180, v140, v169, v180\n\t" \
  "v_mfma_f32_16x16x32_bf16 v[60:63], a[156:159], a[228:231], v[60:63]\n\t" \
  "buffer_load_dwordx4 v203, s[20:23], s96 offen offset:3072 lds\n\t" \
  "v_mfma_f32_16x16x32_bf16 v[92:95], a[156:159], a[232:235], v[92:95]\n\t" \
  "v_lshlrev_b32 v128, 16, v141\n\t" \
  "v_and_b32 v141, 0xffff0000, v141\n\t" \
  "v_mfma_f32_16x16x32_bf16 v[92:95], a[92:95], a[248:251], v[92:95]\n\t" \
  "s_add_u32 s97, s78, 5767168\n\t" \
  "v_fma_f32 v177, v128, v169, v177\n\t" \
  "v_fma_f32 v181, v141, v169, v181\n\t" \
  "v_mfma_f32_16x16x32_bf16 v[124:127], a[92:95], a[252:255], v[124:127]\n\t" \
  "s_waitcnt lgkmcnt(0)\n\t" \
  "s_bfe_u32 s97, s97, 0x140003\n\t" \
  "s_add_u32 s97, s97, s82\n\t" \
  "v_mfma_f32_16x16x32_bf16 v[124:127], a[156:159], a[236:239], v[124:127]\n\t" \
  "s_barrier\n\t" \
  "v_mfma_f32_16x16x32_bf16 v[0:3], a[160:163], a[0:3], v[0:3]\n\t" \
  "buffer_load_dwordx4 a[224:227], v248, s[16:19], s97 offen sc1\n\t" \
  "v_mfma_f32_16x16x32_bf16 v[0:3], a[96:99], a[16:19], v[0:3]\n\t" \
  "ds_read_b64_tr_b16 a[128:129], v192 offset:32768\n\t" \
  "v_lshlrev_b32 v128, 16, v142\n\t" \
  "v_mfma_f32_16x16x32_bf16 v[32:35], a[96:99], a[20:23], v[32:35]\n\t" \
  "ds_read_b64_tr_b16 a[130:131], v192 offset:33792\n\t" \
  "v_and_b32 v142, 0xffff0000, v142\n\t" \
  "v_mfma_f32_16x16x32_bf16 v[32:35], a[160:163], a[4:7], v[32:35]\n\t" \
  "v_fma_f32 v178, v128, v169, v178\n\t" \
  "v_fma_f32 v182, v142, v169, v182\n\t" \
  "v_mfma_f32_16x16x32_bf16 v[64:67], a[160:163], a[8:11], v[64:67]\n\t" \
  "buffer_load_dwordx4 a[228:231], v249, s[16:19], s97 offen sc1\n\t" \
  "v_mfma_f32_16x16x32_bf16 v[64:67], a[96:99], a[24:27], v[64:67]\n\t" \
  "ds_read_b64_tr_b16 a[132:133], v193 offset:32768\n\t" \
  "v_lshlrev_b32 v128, 16, v143\n\t" \
  "v_mfma_f32_16x16x32_bf16 v[96:99], a[96:99], a[28:31], v[96:99]\n\t" \
  "ds_read_b64_tr_b16 a[134:135], v193 offset:33792\n\t" \
  "v_and_b32 v143, 0xffff0000, v143\n\t" \
  "v_mfma_f32_16x16x32_bf16 v[96:99], a[160:163], a[12:15], v[96:99]\n\t" \
  "ds_read_b64_tr_b16 a[136:137], v194 offset:32768\n\t" \
  "v_fma_f32 v179, v128, v169, v179\n\t" \
  "v_mfma_f32_16x16x32_bf16 v[4:7], a[164:167], a[0:3], v[4:7]\n\t" \
  "buffer_load_dwordx4 a[232:235], v250, s[16:19], s97 offen sc1\n\t" \
  "v_mfma_f32_16x16x32_bf16 v[4:7], a[100:103], a[16:19], v[4:7]\n\t" \
  "ds_read_b64_tr_b16 a[138:139], v194 offset:33792\n\t" \
  "v_fma_f32 v183, v143, v169, v183\n\t" \
  "v_mfma_f32_16x16x32_bf16 v[36:39], a[100:103], a[20:23], v[36:39]\n\t" \
  "s_waitcnt vmcnt(28)\n\t" \
  "v_lshlrev_b32 v128, 16, v144\n\t" \
  "v_and_b32 v144, 0xffff0000, v144\n\t" \
  "v_mfma_f32_16x16x32_bf16 v[36:39], a[164:167], a[4:7], v[36:39]\n\t" \
  "ds_read_b64_tr_b16 a[140:141], v195 offset:32768\n\t" \
  "v_fma_f32 v176, v128, v170, v176\n\t" \
  "v_mfma_f32_16x16x32_bf16 v[68:71], a[164:167], a[8:11], v[68:71]\n\t" \
  "buffer_load_dwordx4 a[236:239], v251, s[16:19], s97 offen sc1\n\t" \
  "v_mfma_f32_16x16x32_bf16 v[68:71], a[100:103], a[24:27], v[68:71]\n\t" \
  "s_add_u32 s96, s78, 6815744\n\t" \
  "s_and_b32 s96, s96, 0x780000\n\t" \
  "s_add_u32 m0, s76, 65536\n\t" \
  "v_mfma_f32_16x16x32_bf16 v[100:103], a[100:103], a[28:31], v[100:103]\n\t" \
  "ds_read_b64_tr_b16 a[142:143], v195 offset:33792\n\t" \
  "v_fma_f32 v180, v144, v170, v180\n\t" \
  "v_mfma_f32_16x16x32_bf16 v[100:103], a[164:167], a[12:15], v[100:103]\n\t" \
  "buffer_load_dwordx4 v200, s[20:23], s96 offen offset:0 lds\n\t" \
  "v_mfma_f32_16x16x32_bf16 v[8:11], a[168:171], a[0:3], v[8:11]\n\t" \
  "ds_read_b64_tr_b16 a[144:145], v196 offset:32768\n\t" \
  "v_lshlrev_b32 v128, 16, v145\n\t" \
  "v_mfma_f32_16x16x32_bf16 v[8:11], a[104:107], a[16:19], v[8:11]\n\t" \
  "ds_read_b64_tr_b16 a[146:147], v196 offset:33792\n\t" \
  "v_and_b32 v145, 0xffff0000, v145\n\t" \
  "v_mfma_f32_16x16x32_bf16 v[40:43], a[104:107], a[20:23], v[40:43]\n\t" \
  "ds_read_b64_tr_b16 a[148:149], v197 offset:32768\n\t" \
  "v_fma_f32 v177, v128, v170, v177\n\t" \
  "v_mfma_f32_16x16x32_bf16 v[40:43], a[168:171], a[4:7], v[40:43]\n\t" \
  "buffer_load_dwordx4 v201, s[20:23], s96 offen offset:1024 lds\n\t" \
  "v_mfma_f32_16x16x32_bf16 v[72:75], a[168:171], a[8:11], v[72:75]\n\t" \
  "ds_read_b64_tr_b16 a[150:151], v197 offset:33792\n\t" \
  "v_fma_f32 v181, v145, v170, v181\n\t" \
  "v_mfma_f32_16x16x32_bf16 v[72:75], a[104:107], a[24:27], v[72:75]\n\t" \
  "ds_read_b64_tr_b16 a[152:153], v198 offset:32768\n\t" \
  "v_lshlrev_b32 v128, 16, v146\n\t" \
  "v_mfma_f32_16x16x32_bf16 v[104:107], a[104:107], a[28:31], v[104:107]\n\t" \
  "ds_read_b64_tr_b16 a[154:155], v198 offset:33792\n\t" \
  "v_and_b32 v146, 0xffff0000, v146\n\t" \
  "v_mfma_f32_16x16x32_bf16 v[104:107], a[168:171], a[12:15], v[104:107]\n\t" \
  "buffer_load_dwordx4 v202, s[20:23], s96 offen offset:2048 lds\n\t" \
  "v_mfma_f32_16x16x32_bf16 v[12:15], a[172:175], a[0:3], v[12:15]\n\t" \
  "ds_read_b64_tr_b16 a[156:157], v199 offset:32768\n\t" \
  "v_fma_f32 v178, v128, v170, v178\n\t" \
  "v_mfma_f32_16x16x32_bf16 v[12:15], a[108:111], a[16:19], v[12:15]\n\t" \
  "ds_read_b64_tr_b16 a[158:159], v199 offset:33792\n\t" \
  "v_fma_f32 v182, v146, v170, v182\n\t" \
  "v_mfma_f32_16x16x32_bf16 v[44:47], a[108:111], a[20:23], v[44:47]\n\t" \
  "ds_read_b64_tr_b16 a[64:65], v192 offset:40960\n\t" \
  "v_lshlrev_b32 v128, 16, v147\n\t" \
  "v_mfma_f32_16x16x32_bf16 v[44:47], a[172:175], a[4:7], v[44:47]\n\t" \
  "buffer_load_dwordx4 v203, s[20:23], s96 offen offset:3072 lds\n\t" \
  "v_mfma_f32_16x16x32_bf16 v[76:79], a[172:175], a[8:11], v[76:79]\n\t" \
  "ds_read_b64_tr_b16 a[66:67], v192 offset:41984\n\t" \
  "v_and_b32 v147, 0xffff0000, v147\n\t" \
  "v_mfma_f32_16x16x32_bf16 v[76:79], a[108:111], a[24:27], v[76:79]\n\t" \
  "s_add_u32 s97, s97, s83\n\t" \
  "v_fma_f32 v179, v128, v170, v179\n\t" \
  "v_fma_f32 v183, v147, v170, v183\n\t" \
  "v_mfma_f32_16x16x32_bf16 v[108:111], a[108:111], a[28:31], v[108:111]\n\t" \
  "ds_read_b64_tr_b16 a[68:69], v193 offset:40960\n\t" \
  "v_mfma_f32_16x16x32_bf16 v[108:111], a[172:175], a[12:15], v[108:111]\n\t" \
  "s_waitcnt vmcnt(32)\n\t" \
  "v_lshlrev_b32 v128, 16, v148\n\t" \
  "v_and_b32 v148, 0xffff0000, v148\n\t" \
  "v_mfma_f32_16x16x32_bf16 v[16:19], a[176:179], a[0:3], v[16:19]\n\t" \
  "buffer_load_dwordx4 a[240:243], v248, s[16:19], s97 offen sc1\n\t" \
  "v_mfma_f32_16x16x32_bf16 v[16:19], a[112:115], a[16:19], v[16:19]\n\t" \
  "ds_read_b64_tr_b16 a[70:71], v193 offset:41984\n\t" \
  "v_fma_f32 v176, v128, v171, v176\n\t" \
  "v_mfma_f32_16x16x32_bf16 v[48:51], a[112:115], a[20:23], v[48:51]\n\t" \
  "ds_read_b64_tr_b16 a[72:73], v194 offset:40960\n\t" \
  "v_fma_f32 v180, v148, v171, v180\n\t" \
  "v_mfma_f32_16x16x32_bf16 v[48:51], a[176:179], a[4:7], v[48:51]\n\t" \
  "ds_read_b64_tr_b16 a[74:75], v194 offset:41984\n\t" \
  "v_lshlrev_b32 v128, 16, v149\n\t" \
  "v_mfma_f32_16x16x32_bf16 v[80:83], a[176:179], a[8:11], v[80:83]\n\t" \
  "ds_read_b64_tr_b16 a[76:77], v195 offset:40960\n\t" \
  "v_and_b32 v149, 0xffff0000, v149\n\t" \
  "v_mfma_f32_16x16x32_bf16 v[80:83], a[112:115], a[24:27], v[80:83]\n\t" \
  "v_fma_f32 v177, v128, v171, v177\n\t" \
  "v_fma_f32 v181, v149, v171, v181\n\t" \
  "v_mfma_f32_16x16x32_bf16 v[112:115], a[112:115], a[28:31], v[112:115]\n\t" \
  "ds_read_b64_tr_b16 a[78:79], v195 offset:41984\n\t" \
  "v_lshlrev_b32 v128, 16, v150\n\t" \
  "v_mfma_f32_16x16x32_bf16 v[112:115], a[176:179], a[12:15], v[112:115]\n\t" \
  "ds_read_b64_tr_b16 a[80:81], v196 offset:40960\n\t" \
  "v_and_b32 v150, 0xffff0000, v150\n\t" \
  "v_mfma_f32_16x16x32_bf16 v[20:23], a[180:183], a[0:3], v[20:23]\n\t" \
  "buffer_load_dwordx4 a[244:247], v249, s[16:19], s97 offen sc1\n\t" \
  "v_mfma_f32_16x16x32_bf16 v[20:23], a[116:119], a[16:19], v[20:23]\n\t" \
  "ds_read_b64_tr_b16 a[82:83], v196 offset:41984\n\t" \
  "v_fma_f32 v178, v128, v171, v178\n\t" \
  "v_mfma_f32_16x16x32_bf16 v[52:55], a[116:119], a[20:23], v[52:55]\n\t" \
  "v_fma_f32 v182, v150, v171, v182\n\t" \
  "v_lshlrev_b32 v128, 16, v151\n\t" \
  "v_mfma_f32_16x16x32_bf16 v[52:55], a[180:183], a[4:7], v[52:55]\n\t" \
  "ds_read_b64_tr_b16 a[84:85], v197 offset:40960\n\t" \
  "v_and_b32 v151, 0xffff0000, v151\n\t" \
  "v_mfma_f32_16x16x32_bf16 v[84:87], a[180:183], a[8:11], v[84:87]\n\t" \
  "ds_read_b64_tr_b16 a[86:87], v197 offset:41984\n\t" \
  "v_fma_f32 v179, v128, v171, v179\n\t" \
  "v_mfma_f32_16x16x32_bf16 v[84:87], a[116:119], a[24:27], v[84:87]\n\t" \
  "s_cmp_lt_i32 s91, 0\n\t" \
  "s_cselect_b32 s44, s91, s44\n\t" \
  "v_fma_f32 v183, v151, v171, v183\n\t" \
  "v_mfma_f32_16x16x32_bf16 v[116:119], a[116:119], a[28:31], v[116:119]\n\t" \
  "ds_read_b64_tr_b16 a[88:89], v198 offset:40960\n\t" \
  "v_mfma_f32_16x16x32_bf16 v[116:119], a[180:183], a[12:15], v[116:119]\n\t" \
  "buffer_load_dwordx4 a[248:251], v250, s[16:19], s97 offen sc1\n\t" \
  "v_mfma_f32_16x16x32_bf16 v[24:27], a[184:187], a[0:3], v[24:27]\n\t" \
  "ds_read_b64_tr_b16 a[90:91], v198 offset:41984\n\t" \
  "v_mfma_f32_16x16x32_bf16 v[24:27], a[120:123], a[16:19], v[24:27]\n\t" \
  "ds_read_b64_tr_b16 a[92:93], v199 offset:40960\n\t" \
  "v_mfma_f32_16x16x32_bf16 v[56:59], a[120:123], a[20:23], v[56:59]\n\t" \
  "ds_read_b64_tr_b16 a[94:95], v199 offset:41984\n\t" \
  "v_mfma_f32_16x16x32_bf16 v[56:59], a[184:187], a[4:7], v[56:59]\n\t" \
  "v_mfma_f32_16x16x32_bf16 v[88:91], a[184:187], a[8:11], v[88:91]\n\t" \
  "v_mfma_f32_16x16x32_bf16 v[88:91], a[120:123], a[24:27], v[88:91]\n\t" \
  "buffer_load_dwordx4 a[252:255], v251, s[16:19], s97 offen sc1\n\t" \
  "v_mfma_f32_16x16x32_bf16 v[120:123], a[120:123], a[28:31], v[120:123]\n\t" \
  "v_mfma_f32_16x16x32_bf16 v[120:123], a[184:187], a[12:15], v[120:123]\n\t" \
  "v_mfma_f32_16x16x32_bf16 v[28:31], a[188:191], a[0:3], v[28:31]\n\t" \
  "v_mfma_f32_16x16x32_bf16 v[28:31], a[124:127], a[16:19], v[28:31]\n\t" \
  "v_mfma_f32_16x16x32_bf16 v[60:63], a[124:127], a[20:23], v[60:63]\n\t" \
  "v_mfma_f32_16x16x32_bf16 v[60:63], a[188:191], a[4:7], v[60:63]\n\t" \
  "v_mfma_f32_16x16x32_bf16 v[92:95], a[188:191], a[8:11], v[92:95]\n\t" \
  "v_mfma_f32_16x16x32_bf16 v[92:95], a[124:127], a[24:27], v[92:95]\n\t" \
  "s_add_u32 s97, s78, 6291456\n\t" \
  "s_bfe_u32 s97, s97, 0x140003\n\t" \
  "s_add_u32 s97, s97, s82\n\t" \
  "v_mfma_f32_16x16x32_bf16 v[124:127], a[124:127], a[28:31], v[124:127]\n\t" \
  "s_waitcnt vmcnt(24) lgkmcnt(0)\n\t" \
  "v_mfma_f32_16x16x32_bf16 v[124:127], a[188:191], a[12:15], v[124:127]\n\t" \
  "s_barrier\n\t" \
  "v_mfma_f32_16x16x32_bf16 v[0:3], a[128:131], a[32:35], v[0:3]\n\t" \
  "buffer_load_dwordx4 a[0:3], v248, s[16:19], s97 offen sc1\n\t" \
  "v_mfma_f32_16x16x32_bf16 v[0:3], a[64:67], a[48:51], v[0:3]\n\t" \
  "ds_read_b64_tr_b16 a[160:161], v184 offset:0\n\t" \
  "v_mfma_f32_16x16x32_bf16 v[32:35], a[64:67], a[52:55], v[32:35]\n\t" \
  "ds_read_b64_tr_b16 a[162:163], v184 offset:1024\n\t" \
  "v_mfma_f32_16x16x32_bf16 v[32:35], a[128:131], a[36:39], v[32:35]\n\t" \
  "buffer_load_dwordx4 a[4:7], v249, s[16:19], s97 offen sc1\n\t" \
  "v_mfma_f32_16x16x32_bf16 v[64:67], a[128:131], a[40:43], v[64:67]\n\t" \
  "ds_read_b64_tr_b16 a[164:165], v185 offset:0\n\t" \
  "v_mfma_f32_16x16x32_bf16 v[64:67], a[64:67], a[56:59], v[64:67]\n\t" \
  "buffer_load_dwordx4 a[8:11], v250, s[16:19], s97 offen sc1\n\t" \
  "v_mfma_f32_16x16x32_bf16 v[96:99], a[64:67], a[60:63], v[96:99]\n\t" \
  "ds_read_b64_tr_b16 a[166:167], v185 offset:1024\n\t" \
  "v_mfma_f32_16x16x32_bf16 v[96:99], a[128:131], a[44:47], v[96:99]\n\t" \
  "ds_read_b64_tr_b16 a[168:169], v186 offset:0\n\t" \
  "v_mfma_f32_16x16x32_bf16 v[4:7], a[132:135], a[32:35], v[4:7]\n\t" \
  "buffer_load_dwordx4 a[12:15], v251, s[16:19], s97 offen sc1\n\t" \
  "v_mfma_f32_16x16x32_bf16 v[4:7], a[68:71], a[48:51], v[4:7]\n\t" \
  "s_add_u32 s96, s78, 7340032\n\t" \
  "s_and_b32 s96, s96, 0x780000\n\t" \
  "s_add_u32 m0, s76, 81920\n\t" \
  "v_mfma_f32_16x16x32_bf16 v[36:39], a[68:71], a[52:55], v[36:39]\n\t" \
  "buffer_load_dwordx4 v200, s[20:23], s96 offen offset:0 lds\n\t" \
  "v_mfma_f32_16x16x32_bf16 v[36:39], a[132:135], a[36:39], v[36:39]\n\t" \
  "ds_read_b64_tr_b16 a[170:171], v186 offset:1024\n\t" \
  "v_mfma_f32_16x16x32_bf16 v[68:71], a[132:135], a[40:43], v[68:71]\n\t" \
  "ds_read_b64_tr_b16 a[172:173], v187 offset:0\n\t" \
  "v_mfma_f32_16x16x32_bf16 v[68:71], a[68:71], a[56:59], v[68:71]\n\t" \
  "buffer_load_dwordx4 v201, s[20:23], s96 offen offset:1024 lds\n\t" \
  "v_mfma_f32_16x16x32_bf16 v[100:103], a[68:71], a[60:63], v[100:103]\n\t" \
  "ds_read_b64_tr_b16 a[174:175], v187 offset:1024\n\t" \
  "v_mfma_f32_16x16x32_bf16 v[100:103], a[132:135], a[44:47], v[100:103]\n\t" \
  "buffer_load_dwordx4 v202, s[20:23], s96 offen offset:2048 lds\n\t" \
  "v_mfma_f32_16x16x32_bf16 v[8:11], a[136:139], a[32:35], v[8:11]\n\t" \
  "ds_read_b64_tr_b16 a[176:177], v188 offset:0\n\t" \
  "v_mfma_f32_16x16x32_bf16 v[8:11], a[72:75], a[48:51], v[8:11]\n\t" \
  "ds_read_b64_tr_b16 a[178:179], v188 offset:1024\n\t" \
  "v_mfma_f32_16x16x32_bf16 v[40:43], a[72:75], a[52:55], v[40:43]\n\t" \
  "buffer_load_dwordx4 v203, s[20:23], s96 offen offset:3072 lds\n\t" \
  "v_mfma_f32_16x16x32_bf16 v[40:43], a[136:139], a[36:39], v[40:43]\n\t" \
  "ds_read_b64_tr_b16 a[180:181], v189 offset:0\n\t" \
  "s_add_u32 s97, s97, s83\n\t" \
  "v_mfma_f32_16x16x32_bf16 v[72:75], a[136:139], a[40:43], v[72:75]\n\t" \
  "buffer_load_dwordx4 a[16:19], v248, s[16:19], s97 offen sc1\n\t" \
  "v_mfma_f32_16x16x32_bf16 v[72:75], a[72:75], a[56:59], v[72:75]\n\t" \
  "ds_read_b64_tr_b16 a[182:183], v189 offset:1024\n\t" \
  "v_mfma_f32_16x16x32_bf16 v[104:107], a[72:75], a[60:63], v[104:107]\n\t" \
  "ds_read_b64_tr_b16 a[184:185], v190 offset:0\n\t" \
  "v_mfma_f32_16x16x32_bf16 v[104:107], a[136:139], a[44:47], v[104:107]\n\t" \
  "buffer_load_dwordx4 a[20:23], v249, s[16:19], s97 offen sc1\n\t" \
  "v_mfma_f32_16x16x32_bf16 v[12:15], a[140:143], a[32:35], v[12:15]\n\t" \
  "ds_read_b64_tr_b16 a[186:187], v190 offset:1024\n\t" \
  "v_mfma_f32_16x16x32_bf16 v[12:15], a[76:79], a[48:51], v[12:15]\n\t" \
  "buffer_load_dwordx4 a[24:27], v250, s[16:19], s97 offen sc1\n\t" \
  "v_mfma_f32_16x16x32_bf16 v[44:47], a[76:79], a[52:55], v[44:47]\n\t" \
  "ds_read_b64_tr_b16 a[188:189], v191 offset:0\n\t" \
  "v_mfma_f32_16x16x32_bf16 v[44:47], a[140:143], a[36:39], v[44:47]\n\t" \
  "ds_read_b64_tr_b16 a[190:191], v191 offset:1024\n\t" \
  "v_mfma_f32_16x16x32_bf16 v[76:79], a[140:143], a[40:43], v[76:79]\n\t" \
  "buffer_load_dwordx4 a[28:31], v251, s[16:19], s97 offen sc1\n\t" \
  "v_mfma_f32_16x16x32_bf16 v[76:79], a[76:79], a[56:59], v[76:79]\n\t" \
  "ds_read_b64_tr_b16 a[96:97], v184 offset:8192\n\t" \
  "s_mov_b32 s99, 49152\n\t" \
  "v_mfma_f32_16x16x32_bf16 v[108:111], a[76:79], a[60:63], v[108:111]\n\t" \
  "ds_read_b64_tr_b16 a[98:99], v184 offset:9216\n\t" \
  "v_mfma_f32_16x16x32_bf16 v[108:111], a[140:143], a[44:47], v[108:111]\n\t" \
  "ds_read_b64_tr_b16 a[100:101], v185 offset:8192\n\t" \
  "v_mfma_f32_16x16x32_bf16 v[16:19], a[144:147], a[32:35], v[16:19]\n\t" \
  "buffer_load_dwordx4 v[152:155], v209, s[8:11], s99 offen nt\n\t" \
  "v_mfma_f32_16x16x32_bf16 v[16:19], a[80:83], a[48:51], v[16:19]\n\t" \
  "ds_read_b64_tr_b16 a[102:103], v185 offset:9216\n\t" \
  "s_mov_b32 s99, 61440\n\t" \
  "v_mfma_f32_16x16x32_bf16 v[48:51], a[80:83], a[52:55], v[48:51]\n\t" \
  "ds_read_b64_tr_b16 a[104:105], v186 offset:8192\n\t" \
  "v_mfma_f32_16x16x32_bf16 v[48:51], a[144:147], a[36:39], v[48:51]\n\t" \
  "ds_read_b64_tr_b16 a[106:107], v186 offset:9216\n\t" \
  "v_mfma_f32_16x16x32_bf16 v[80:83], a[144:147], a[40:43], v[80:83]\n\t" \
  "ds_read_b64_tr_b16 a[108:109], v187 offset:8192\n\t" \
  "v_mfma_f32_16x16x32_bf16 v[80:83], a[80:83], a[56:59], v[80:83]\n\t" \
  "ds_read_b64_tr_b16 a[110:111], v187 offset:9216\n\t" \
  "v_mfma_f32_16x16x32_bf16 v[112:115], a[80:83], a[60:63], v[112:115]\n\t" \
  "buffer_load_dwordx4 v[156:159], v209, s[8:11], s99 offen nt\n\t" \
  "v_mfma_f32_16x16x32_bf16 v[112:115], a[144:147], a[44:47], v[112:115]\n\t" \
  "ds_read_b64_tr_b16 a[112:113], v188 offset:8192\n\t" \
  "s_mov_b32 s99, 73728\n\t" \
  "v_mfma_f32_16x16x32_bf16 v[20:23], a[148:151], a[32:35], v[20:23]\n\t" \
  "ds_read_b64_tr_b16 a[114:115], v188 offset:9216\n\t" \
  "v_mfma_f32_16x16x32_bf16 v[20:23], a[84:87], a[48:51], v[20:23]\n\t" \
  "ds_read_b64_tr_b16 a[116:117], v189 offset:8192\n\t" \
  "v_mfma_f32_16x16x32_bf16 v[52:55], a[84:87], a[52:55], v[52:55]\n\t" \
  "ds_read_b64_tr_b16 a[118:119], v189 offset:9216\n\t" \
  "v_mfma_f32_16x16x32_bf16 v[52:55], a[148:151], a[36:39], v[52:55]\n\t" \
  "ds_read_b64_tr_b16 a[120:121], v190 offset:8192\n\t" \
  "v_mfma_f32_16x16x32_bf16 v[84:87], a[148:151], a[40:43], v[84:87]\n\t" \
  "ds_read_b64_tr_b16 a[122:123], v190 offset:9216\n\t" \
  "v_mfma_f32_16x16x32_bf16 v[84:87], a[84:87], a[56:59], v[84:87]\n\t" \
  "ds_read_b64_tr_b16 a[124:125], v191 offset:8192\n\t" \
  "v_mfma_f32_16x16x32_bf16 v[116:119], a[84:87], a[60:63], v[116:119]\n\t" \
  "buffer_load_dwordx4 v[160:163], v209, s[8:11], s99 offen nt\n\t" \
  "v_mfma_f32_16x16x32_bf16 v[116:119], a[148:151], a[44:47], v[116:119]\n\t" \
  "ds_read_b64_tr_b16 a[126:127], v191 offset:9216\n\t" \
  "s_mov_b32 s99, 86016\n\t" \
  "v_mfma_f32_16x16x32_bf16 v[24:27], a[152:155], a[32:35], v[24:27]\n\t" \
  "v_mfma_f32_16x16x32_bf16 v[24:27], a[88:91], a[48:51], v[24:27]\n\t" \
  "v_mfma_f32_16x16x32_bf16 v[56:59], a[88:91], a[52:55], v[56:59]\n\t" \
  "v_mfma_f32_16x16x32_bf16 v[56:59], a[152:155], a[36:39], v[56:59]\n\t" \
  "v_mfma_f32_16x16x32_bf16 v[88:91], a[152:155], a[40:43], v[88:91]\n\t" \
  "v_mfma_f32_16x16x32_bf16 v[88:91], a[88:91], a[56:59], v[88:91]\n\t" \
  "buffer_load_dwordx4 v[164:167], v209, s[8:11], s99 offen nt\n\t" \
  "v_mfma_f32_16x16x32_bf16 v[120:123], a[88:91], a[60:63], v[120:123]\n\t" \
  "v_mfma_f32_16x16x32_bf16 v[120:123], a[152:155], a[44:47], v[120:123]\n\t" \
  "v_mfma_f32_16x16x32_bf16 v[28:31], a[156:159], a[32:35], v[28:31]\n\t" \
  "v_mfma_f32_16x16x32_bf16 v[28:31], a[92:95], a[48:51], v[28:31]\n\t" \
  "v_mfma_f32_16x16x32_bf16 v[60:63], a[92:95], a[52:55], v[60:63]\n\t" \
  "v_mfma_f32_16x16x32_bf16 v[60:63], a[156:159], a[36:39], v[60:63]\n\t" \
  "v_mfma_f32_16x16x32_bf16 v[92:95], a[156:159], a[40:43], v[92:95]\n\t" \
  "v_mfma_f32_16x16x32_bf16 v[92:95], a[92:95], a[56:59], v[92:95]\n\t" \
  "v_mfma_f32_16x16x32_bf16 v[124:127], a[92:95], a[60:63], v[124:127]\n\t" \
  "s_waitcnt vmcnt(32) lgkmcnt(0)\n\t" \
  "v_mfma_f32_16x16x32_bf16 v[124:127], a[156:159], a[44:47], v[124:127]\n\t" \
  "s_barrier\n\t" \
  "v_mfma_f32_16x16x32_bf16 v[0:3], a[160:163], a[192:195], v[0:3]\n\t" \
  "ds_read_b64_tr_b16 a[128:129], v184 offset:16384\n\t" \
  "v_mfma_f32_16x16x32_bf16 v[0:3], a[96:99], a[208:211], v[0:3]\n\t" \
  "ds_read_b64_tr_b16 a[130:131], v184 offset:17408\n\t" \
  "v_mfma_f32_16x16x32_bf16 v[32:35], a[96:99], a[212:215], v[32:35]\n\t" \
  "ds_read_b64_tr_b16 a[132:133], v185 offset:16384\n\t" \
  "v_mfma_f32_16x16x32_bf16 v[32:35], a[160:163], a[196:199], v[32:35]\n\t" \
  "ds_read_b64_tr_b16 a[134:135], v185 offset:17408\n\t" \
  "v_mfma_f32_16x16x32_bf16 v[64:67], a[160:163], a[200:203], v[64:67]\n\t" \
  "ds_read_b64_tr_b16 a[136:137], v186 offset:16384\n\t" \
  "v_mfma_f32_16x16x32_bf16 v[64:67], a[96:99], a[216:219], v[64:67]\n\t" \
  "ds_read_b64_tr_b16 a[138:139], v186 offset:17408\n\t" \
  "v_mfma_f32_16x16x32_bf16 v[96:99], a[96:99], a[220:223], v[96:99]\n\t" \
  "ds_read_b64_tr_b16 a[140:141], v187 offset:16384\n\t" \
  "v_mfma_f32_16x16x32_bf16 v[96:99], a[160:163], a[204:207], v[96:99]\n\t" \
  "ds_read_b64_tr_b16 a[142:143], v187 offset:17408\n\t" \
  "v_mfma_f32_16x16x32_bf16 v[4:7], a[164:167], a[192:195], v[4:7]\n\t" \
  "ds_read_b64_tr_b16 a[144:145], v188 offset:16384\n\t" \
  "v_mfma_f32_16x16x32_bf16 v[4:7], a[100:103], a[208:211], v[4:7]\n\t" \
  "ds_read_b64_tr_b16 a[146:147], v188 offset:17408\n\t" \
  "v_mfma_f32_16x16x32_bf16 v[36:39], a[100:103], a[212:215], v[36:39]\n\t" \
  "ds_read_b64_tr_b16 a[148:149], v189 offset:16384\n\t" \
  "v_mfma_f32_16x16x32_bf16 v[36:39], a[164:167], a[196:199], v[36:39]\n\t" \
  "ds_read_b64_tr_b16 a[150:151], v189 offset:17408\n\t" \
  "v_mfma_f32_16x16x32_bf16 v[68:71], a[164:167], a[200:203], v[68:71]\n\t" \
  "ds_read_b64_tr_b16 a[152:153], v190 offset:16384\n\t" \
  "v_mfma_f32_16x16x32_bf16 v[68:71], a[100:103], a[216:219], v[68:71]\n\t" \
  "ds_read_b64_tr_b16 a[154:155], v190 offset:17408\n\t" \
  "v_mfma_f32_16x16x32_bf16 v[100:103], a[100:103], a[220:223], v[100:103]\n\t" \
  "ds_read_b64_tr_b16 a[156:157], v191 offset:16384\n\t" \
  "v_mfma_f32_16x16x32_bf16 v[100:103], a[164:167], a[204:207], v[100:103]\n\t" \
  "ds_read_b64_tr_b16 a[158:159], v191 offset:17408\n\t" \
  "v_mfma_f32_16x16x32_bf16 v[8:11], a[168:171], a[192:195], v[8:11]\n\t" \
  "ds_read_b64_tr_b16 a[64:65], v184 offset:24576\n\t" \
  "v_mfma_f32_16x16x32_bf16 v[8:11], a[104:107], a[208:211], v[8:11]\n\t" \
  "v_mfma_f32_16x16x32_bf16 v[40:43], a[104:107], a[212:215], v[40:43]\n\t" \
  "ds_read_b64_tr_b16 a[66:67], v184 offset:25600\n\t" \
  "v_mfma_f32_16x16x32_bf16 v[40:43], a[168:171], a[196:199], v[40:43]\n\t" \
  "v_mfma_f32_16x16x32_bf16 v[72:75], a[168:171], a[200:203], v[72:75]\n\t" \
  "ds_read_b64_tr_b16 a[68:69], v185 offset:24576\n\t" \
  "v_mfma_f32_16x16x32_bf16 v[72:75], a[104:107], a[216:219], v[72:75]\n\t" \
  "ds_read_b64_tr_b16 a[70:71], v185 offset:25600\n\t" \
  "v_mfma_f32_16x16x32_bf16 v[104:107], a[104:107], a[220:223], v[104:107]\n\t" \
  "v_mfma_f32_16x16x32_bf16 v[104:107], a[168:171], a[204:207], v[104:107]\n\t" \
  "ds_read_b64_tr_b16 a[72:73], v186 offset:24576\n\t" \
  "v_mfma_f32_16x16x32_bf16 v[12:15], a[172:175], a[192:195], v[12:15]\n\t" \
  "v_mfma_f32_16x16x32_bf16 v[12:15], a[108:111], a[208:211], v[12:15]\n\t" \
  "ds_read_b64_tr_b16 a[74:75], v186 offset:25600\n\t" \
  "v_mfma_f32_16x16x32_bf16 v[44:47], a[108:111], a[212:215], v[44:47]\n\t" \
  "ds_read_b64_tr_b16 a[76:77], v187 offset:24576\n\t" \
  "v_mfma_f32_16x16x32_bf16 v[44:47], a[172:175], a[196:199], v[44:47]\n\t" \
  ".Lhs4:\n\ts_cbranch_execz .Lhs4n\n\t" \
  "v_mfma_f32_16x16x32_bf16 v[76:79], a[172:175], a[200:203], v[76:79]\n\t" \
  "ds_read_b64_tr_b16 a[78:79], v187 offset:25600\n\t" \
  "v_mfma_f32_16x16x32_bf16 v[76:79], a[108:111], a[216:219], v[76:79]\n\t" \
  "s_add_u32 s97, s78, 6815744\n\t" \
  "ds_read_b64_tr_b16 a[80:81], v188 offset:24576\n\t" \
  "s_bfe_u32 s97, s97, 0x140003\n\t" \
  "v_mfma_f32_16x16x32_bf16 v[108:111], a[108:111], a[220:223], v[108:111]\n\t" \
  "s_add_u32 s97, s97, s82\n\t" \
  "v_mfma_f32_16x16x32_bf16 v[108:111], a[172:175], a[204:207], v[108:111]\n\t" \
  "buffer_load_dwordx4 a[32:35], v248, s[16:19], s97 offen sc1\n\t" \
  "v_mfma_f32_16x16x32_bf16 v[16:19], a[176:179], a[192:195], v[16:19]\n\t" \
  "ds_read_b64_tr_b16 a[82:83], v188 offset:25600\n\t" \
  "v_mfma_f32_16x16x32_bf16 v[16:19], a[112:115], a[208:211], v[16:19]\n\t" \
  "ds_read_b64_tr_b16 a[84:85], v189 offset:24576\n\t" \
  "v_mfma_f32_16x16x32_bf16 v[48:51], a[112:115], a[212:215], v[48:51]\n\t" \
  "buffer_load_dwordx4 a[36:39], v249, s[16:19], s97 offen sc1\n\t" \
  "v_mfma_f32_16x16x32_bf16 v[48:51], a[176:179], a[196:199], v[48:51]\n\t" \
  "ds_read_b64_tr_b16 a[86:87], v189 offset:25600\n\t" \
  "v_mfma_f32_16x16x32_bf16 v[80:83], a[176:179], a[200:203], v[80:83]\n\t" \
  "ds_read_b64_tr_b16 a[88:89], v190 offset:24576\n\t" \
  "v_mfma_f32_16x16x32_bf16 v[80:83], a[112:115], a[216:219], v[80:83]\n\t" \
  "v_mfma_f32_16x16x32_bf16 v[112:115], a[112:115], a[220:223], v[112:115]\n\t" \
  "buffer_load_dwordx4 a[40:43], v250, s[16:19], s97 offen sc1\n\t" \
  "v_mfma_f32_16x16x32_bf16 v[112:115], a[176:179], a[204:207], v[112:115]\n\t" \
  "ds_read_b64_tr_b16 a[90:91], v190 offset:25600\n\t" \
  "v_mfma_f32_16x16x32_bf16 v[20:23], a[180:183], a[192:195], v[20:23]\n\t" \
  "ds_read_b64_tr_b16 a[92:93], v191 offset:24576\n\t" \
  "v_mfma_f32_16x16x32_bf16 v[20:23], a[116:119], a[208:211], v[20:23]\n\t" \
  "ds_read_b64_tr_b16 a[94:95], v191 offset:25600\n\t" \
  "v_mfma_f32_16x16x32_bf16 v[52:55], a[116:119], a[212:215], v[52:55]\n\t" \
  "buffer_load_dwordx4 a[44:47], v251, s[16:19], s97 offen sc1\n\t" \
  "v_mfma_f32_16x16x32_bf16 v[52:55], a[180:183], a[196:199], v[52:55]\n\t" \
  "s_add_u32 s96, s78, 7864320\n\t" \
  "s_and_b32 s96, s96, 0x780000\n\t" \
  "s_add_u32 m0, s76, 98304\n\t" \
  "v_mfma_f32_16x16x32_bf16 v[84:87], a[180:183], a[200:203], v[84:87]\n\t" \
  "v_mfma_f32_16x16x32_bf16 v[84:87], a[116:119], a[216:219], v[84:87]\n\t" \
  "v_mfma_f32_16x16x32_bf16 v[116:119], a[116:119], a[220:223], v[116:119]\n\t" \
  "buffer_load_dwordx4 v200, s[20:23], s96 offen offset:0 lds\n\t" \
  "v_mfma_f32_16x16x32_bf16 v[116:119], a[180:183], a[204:207], v[116:119]\n\t" \
  "v_mfma_f32_16x16x32_bf16 v[24:27], a[184:187], a[192:195], v[24:27]\n\t" \
  "v_mfma_f32_16x16x32_bf16 v[24:27], a[120:123], a[208:211], v[24:27]\n\t" \
  "buffer_load_dwordx4 v201, s[20:23], s96 offen offset:1024 lds\n\t" \
  "v_mfma_f32_16x16x32_bf16 v[56:59], a[120:123], a[212:215], v[56:59]\n\t" \
  "v_mfma_f32_16x16x32_bf16 v[56:59], a[184:187], a[196:199], v[56:59]\n\t" \
  "v_mfma_f32_16x16x32_bf16 v[88:91], a[184:187], a[200:203], v[88:91]\n\t" \
  "v_mfma_f32_16x16x32_bf16 v[88:91], a[120:123], a[216:219], v[88:91]\n\t" \
  "buffer_load_dwordx4 v202, s[20:23], s96 offen offset:2048 lds\n\t" \
  "v_mfma_f32_16x16x32_bf16 v[120:123], a[120:123], a[220:223], v[120:123]\n\t" \
  "v_mfma_f32_16x16x32_bf16 v[120:123], a[184:187], a[204:207], v[120:123]\n\t" \
  "v_mfma_f32_16x16x32_bf16 v[28:31], a[188:191], a[192:195], v[28:31]\n\t" \
  "v_mfma_f32_16x16x32_bf16 v[28:31], a[124:127], a[208:211], v[28:31]\n\t" \
  "buffer_load_dwordx4 v203, s[20:23], s96 offen offset:3072 lds\n\t" \
  "v_mfma_f32_16x16x32_bf16 v[60:63], a[124:127], a[212:215], v[60:63]\n\t" \
  "v_mfma_f32_16x16x32_bf16 v[60:63], a[188:191], a[196:199], v[60:63]\n\t" \
  "v_mfma_f32_16x16x32_bf16 v[92:95], a[188:191], a[200:203], v[92:95]\n\t" \
  "v_mfma_f32_16x16x32_bf16 v[92:95], a[124:127], a[216:219], v[92:95]\n\t" \
  "s_add_u32 s97, s97, s83\n\t" \
  "v_mfma_f32_16x16x32_bf16 v[124:127], a[124:127], a[220:223], v[124:127]\n\t" \
  "s_waitcnt vmcnt(24) lgkmcnt(0)\n\t" \
  "v_mfma_f32_16x16x32_bf16 v[124:127], a[188:191], a[204:207], v[124:127]\n\t" \
  "s_barrier\n\t" \
  "v_mfma_f32_16x16x32_bf16 v[0:3], a[128:131], a[224:227], v[0:3]\n\t" \
  "buffer_load_dwordx4 a[48:51], v248, s[16:19], s97 offen sc1\n\t" \
  "v_mfma_f32_16x16x32_bf16 v[0:3], a[64:67], a[240:243], v[0:3]\n\t" \
  "ds_read_b64_tr_b16 a[160:161], v184 offset:32768\n\t" \
  "v_mfma_f32_16x16x32_bf16 v[32:35], a[64:67], a[244:247], v[32:35]\n\t" \
  "ds_read_b64_tr_b16 a[162:163], v184 offset:33792\n\t" \
  "v_mfma_f32_16x16x32_bf16 v[32:35], a[128:131], a[228:231], v[32:35]\n\t" \
  "buffer_load_dwordx4 a[52:55], v249, s[16:19], s97 offen sc1\n\t" \
  "v_mfma_f32_16x16x32_bf16 v[64:67], a[128:131], a[232:235], v[64:67]\n\t" \
  "ds_read_b64_tr_b16 a[164:165], v185 offset:32768\n\t" \
  "v_mfma_f32_16x16x32_bf16 v[64:67], a[64:67], a[248:251], v[64:67]\n\t" \
  "ds_read_b64_tr_b16 a[166:167], v185 offset:33792\n\t" \
  "v_mfma_f32_16x16x32_bf16 v[96:99], a[64:67], a[252:255], v[96:99]\n\t" \
  "buffer_load_dwordx4 a[56:59], v250, s[16:19], s97 offen sc1\n\t" \
  "v_mfma_f32_16x16x32_bf16 v[96:99], a[128:131], a[236:239], v[96:99]\n\t" \
  "ds_read_b64_tr_b16 a[168:169], v186 offset:32768\n\t" \
  "v_mfma_f32_16x16x32_bf16 v[4:7], a[132:135], a[224:227], v[4:7]\n\t" \
  "ds_read_b64_tr_b16 a[170:171], v186 offset:33792\n\t" \
  "v_mfma_f32_16x16x32_bf16 v[4:7], a[68:71], a[240:243], v[4:7]\n\t" \
  "ds_read_b64_tr_b16 a[172:173], v187 offset:32768\n\t" \
  "v_mfma_f32_16x16x32_bf16 v[36:39], a[68:71], a[244:247], v[36:39]\n\t" \
  "buffer_load_dwordx4 a[60:63], v251, s[16:19], s97 offen sc1\n\t" \
  "v_mfma_f32_16x16x32_bf16 v[36:39], a[132:135], a[228:231], v[36:39]\n\t" \
  "s_add_u32 s97, s78, 7340032\n\t" \
  "ds_read_b64_tr_b16 a[174:175], v187 offset:33792\n\t" \
  "s_bfe_u32 s97, s97, 0x140003\n\t" \
  "v_mfma_f32_16x16x32_bf16 v[68:71], a[132:135], a[232:235], v[68:71]\n\t" \
  "ds_read_b64_tr_b16 a[176:177], v188 offset:32768\n\t" \
  "s_add_u32 s97, s97, s82\n\t" \
  "v_mfma_f32_16x16x32_bf16 v[68:71], a[68:71], a[248:251], v[68:71]\n\t" \
  "ds_read_b64_tr_b16 a[178:179], v188 offset:33792\n\t" \
  "v_mfma_f32_16x16x32_bf16 v[100:103], a[68:71], a[252:255], v[100:103]\n\t" \
  "buffer_load_dwordx4 a[192:195], v248, s[16:19], s97 offen sc1\n\t" \
  "v_mfma_f32_16x16x32_bf16 v[100:103], a[132:135], a[236:239], v[100:103]\n\t" \
  "ds_read_b64_tr_b16 a[180:181], v189 offset:32768\n\t" \
  "v_mfma_f32_16x16x32_bf16 v[8:11], a[136:139], a[224:227], v[8:11]\n\t" \
  "ds_read_b64_tr_b16 a[182:183], v189 offset:33792\n\t" \
  "v_mfma_f32_16x16x32_bf16 v[8:11], a[72:75], a[240:243], v[8:11]\n\t" \
  "buffer_load_dwordx4 a[196:199], v249, s[16:19], s97 offen sc1\n\t" \
  "v_mfma_f32_16x16x32_bf16 v[40:43], a[72:75], a[244:247], v[40:43]\n\t" \
  "ds_read_b64_tr_b16 a[184:185], v190 offset:32768\n\t" \
  "v_mfma_f32_16x16x32_bf16 v[40:43], a[136:139], a[228:231], v[40:43]\n\t" \
  "ds_read_b64_tr_b16 a[186:187], v190 offset:33792\n\t" \
  "v_mfma_f32_16x16x32_bf16 v[72:75], a[136:139], a[232:235], v[72:75]\n\t" \
  "ds_read_b64_tr_b16 a[188:189], v191 offset:32768\n\t" \
  "v_mfma_f32_16x16x32_bf16 v[72:75], a[72:75], a[248:251], v[72:75]\n\t" \
  "buffer_load_dwordx4 a[200:203], v250, s[16:19], s97 offen sc1\n\t" \
  "v_mfma_f32_16x16x32_bf16 v[104:107], a[72:75], a[252:255], v[104:107]\n\t" \
  "ds_read_b64_tr_b16 a[190:191], v191 offset:33792\n\t" \
  "v_mfma_f32_16x16x32_bf16 v[104:107], a[136:139], a[236:239], v[104:107]\n\t" \
  "ds_read_b64_tr_b16 a[96:97], v184 offset:40960\n\t" \
  "v_mfma_f32_16x16x32_bf16 v[12:15], a[140:143], a[224:227], v[12:15]\n\t" \
  "ds_read_b64_tr_b16 a[98:99], v184 offset:41984\n\t" \
  "v_mfma_f32_16x16x32_bf16 v[12:15], a[76:79], a[240:243], v[12:15]\n\t" \
  "buffer_load_dwordx4 a[204:207], v251, s[16:19], s97 offen sc1\n\t" \
  "v_mfma_f32_16x16x32_bf16 v[44:47], a[76:79], a[244:247], v[44:47]\n\t" \
  "ds_read_b64_tr_b16 a[100:101], v185 offset:40960\n\t" \
  "v_mfma_f32_16x16x32_bf16 v[44:47], a[140:143], a[228:231], v[44:47]\n\t" \
  "ds_read_b64_tr_b16 a[102:103], v185 offset:41984\n\t" \
  "v_mfma_f32_16x16x32_bf16 v[76:79], a[140:143], a[232:235], v[76:79]\n\t" \
  "ds_read_b64_tr_b16 a[104:105], v186 offset:40960\n\t" \
  "v_mfma_f32_16x16x32_bf16 v[76:79], a[76:79], a[248:251], v[76:79]\n\t" \
  "ds_read_b64_tr_b16 a[106:107], v186 offset:41984\n\t" \
  "s_add_u32 s97, s97, s83\n\t" \
  "v_mfma_f32_16x16x32_bf16 v[108:111], a[76:79], a[252:255], v[108:111]\n\t" \
  "ds_read_b64_tr_b16 a[108:109], v187 offset:40960\n\t" \
  "v_mfma_f32_16x16x32_bf16 v[108:111], a[140:143], a[236:239], v[108:111]\n\t" \
  "buffer_load_dwordx4 a[208:211], v248, s[16:19], s97 offen sc1\n\t" \
  "v_mfma_f32_16x16x32_bf16 v[16:19], a[144:147], a[224:227], v[16:19]\n\t" \
  "ds_read_b64_tr_b16 a[110:111], v187 offset:41984\n\t" \
  "v_mfma_f32_16x16x32_bf16 v[16:19], a[80:83], a[240:243], v[16:19]\n\t" \
  "ds_read_b64_tr_b16 a[112:113], v188 offset:40960\n\t" \
  "v_mfma_f32_16x16x32_bf16 v[48:51], a[80:83], a[244:247], v[48:51]\n\t" \
  "s_waitcnt vmcnt(20)\n\t" \
  "v_lshlrev_b32 v128, 16, v152\n\t" \
  "v_and_b32 v152, 0xffff0000, v152\n\t" \
  "v_mfma_f32_16x16x32_bf16 v[48:51], a[144:147], a[228:231], v[48:51]\n\t" \
  "v_fma_f32 v176, v128, v172, v176\n\t" \
  "v_fma_f32 v180, v152, v172, v180\n\t" \
  "v_mfma_f32_16x16x32_bf16 v[80:83], a[144:147], a[232:235], v[80:83]\n\t" \
  "v_lshlrev_b32 v128, 16, v153\n\t" \
  "v_and_b32 v153, 0xffff0000, v153\n\t" \
  "v_mfma_f32_16x16x32_bf16 v[80:83], a[80:83], a[248:251], v[80:83]\n\t" \
  "v_fma_f32 v177, v128, v172, v177\n\t" \
  "v_fma_f32 v181, v153, v172, v181\n\t" \
  "v_mfma_f32_16x16x32_bf16 v[112:115], a[80:83], a[252:255], v[112:115]\n\t" \
  "v_lshlrev_b32 v128, 16, v154\n\t" \
  "v_and_b32 v154, 0xffff0000, v154\n\t" \
  "v_mfma_f32_16x16x32_bf16 v[112:115], a[144:147], a[236:239], v[112:115]\n\t" \
  "v_fma_f32 v178, v128, v172, v178\n\t" \
  "v_fma_f32 v182, v154, v172, v182\n\t" \
  "v_mfma_f32_16x16x32_bf16 v[20:23], a[148:151], a[224:227], v[20:23]\n\t" \
  "buffer_load_dwordx4 a[212:215], v249, s[16:19], s97 offen sc1\n\t" \
  "v_mfma_f32_16x16x32_bf16 v[20:23], a[84:87], a[240:243], v[20:23]\n\t" \
  "ds_read_b64_tr_b16 a[114:115], v188 offset:41984\n\t" \
  "v_lshlrev_b32 v128, 16, v155\n\t" \
  "v_mfma_f32_16x16x32_bf16 v[52:55], a[84:87], a[244:247], v[52:55]\n\t" \
  "v_and_b32 v155, 0xffff0000, v155\n\t" \
  "v_fma_f32 v179, v128, v172, v179\n\t" \
  "v_mfma_f32_16x16x32_bf16 v[52:55], a[148:151], a[228:231], v[52:55]\n\t" \
  "ds_read_b64_tr_b16 a[116:117], v189 offset:40960\n\t" \
  "v_fma_f32 v183, v155, v172, v183\n\t" \
  "v_mfma_f32_16x16x32_bf16 v[84:87], a[148:151], a[232:235], v[84:87]\n\t" \
  "s_waitcnt vmcnt(20)\n\t" \
  "v_lshlrev_b32 v128, 16, v156\n\t" \
  "v_and_b32 v156, 0xffff0000, v156\n\t" \
  "v_mfma_f32_16x16x32_bf16 v[84:87], a[84:87], a[248:251], v[84:87]\n\t" \
  "ds_read_b64_tr_b16 a[118:119], v189 offset:41984\n\t" \
  "v_fma_f32 v176, v128, v173, v176\n\t" \
  "v_mfma_f32_16x16x32_bf16 v[116:119], a[84:87], a[252:255], v[116:119]\n\t" \
  "ds_read_b64_tr_b16 a[120:121], v190 offset:40960\n\t" \
  "v_fma_f32 v180, v156, v173, v180\n\t" \
  "v_mfma_f32_16x16x32_bf16 v[116:119], a[148:151], a[236:239], v[116:119]\n\t" \
  "buffer_load_dwordx4 a[216:219], v250, s[16:19], s97 offen sc1\n\t" \
  "v_mfma_f32_16x16x32_bf16 v[24:27], a[152:155], a[224:227], v[24:27]\n\t" \
  "ds_read_b64_tr_b16 a[122:123], v190 offset:41984\n\t" \
  "v_lshlrev_b32 v128, 16, v157\n\t" \
  "v_mfma_f32_16x16x32_bf16 v[24:27], a[88:91], a[240:243], v[24:27]\n\t" \
  "ds_read_b64_tr_b16 a[124:125], v191 offset:40960\n\t" \
  "v_and_b32 v157, 0xffff0000, v157\n\t" \
  "v_mfma_f32_16x16x32_bf16 v[56:59], a[88:91], a[244:247], v[56:59]\n\t" \
  "v_fma_f32 v177, v128, v173, v177\n\t" \
  "v_fma_f32 v181, v157, v173, v181\n\t" \
  "v_mfma_f32_16x16x32_bf16 v[56:59], a[152:155], a[228:231], v[56:59]\n\t" \
  "ds_read_b64_tr_b16 a[126:127], v191 offset:41984\n\t" \
  "v_lshlrev_b32 v128, 16, v158\n\t" \
  "v_mfma_f32_16x16x32_bf16 v[88:91], a[152:155], a[232:235], v[88:91]\n\t" \
  "v_and_b32 v158, 0xffff0000, v158\n\t" \
  "v_fma_f32 v178, v128, v173, v178\n\t" \
  "v_mfma_f32_16x16x32_bf16 v[88:91], a[88:91], a[248:251], v[88:91]\n\t" \
  "v_fma_f32 v182, v158, v173, v182\n\t" \
  "v_lshlrev_b32 v128, 16, v159\n\t" \
  "v_mfma_f32_16x16x32_bf16 v[120:123], a[88:91], a[252:255], v[120:123]\n\t" \
  "v_and_b32 v159, 0xffff0000, v159\n\t" \
  "v_fma_f32 v179, v128, v173, v179\n\t" \
  "v_mfma_f32_16x16x32_bf16 v[120:123], a[152:155], a[236:239], v[120:123]\n\t" \
  "buffer_load_dwordx4 a[220:223], v251, s[16:19], s97 offen sc1\n\t" \
  "v_mfma_f32_16x16x32_bf16 v[28:31], a[156:159], a[224:227], v[28:31]\n\t" \
  "v_fma_f32 v183, v159, v173, v183\n\t" \
  "v_mfma_f32_16x16x32_bf16 v[28:31], a[92:95], a[240:243], v[28:31]\n\t" \
  "s_waitcnt vmcnt(21)\n\t" \
  "v_lshlrev_b32 v128, 16, v160\n\t" \
  "v_and_b32 v160, 0xffff0000, v160\n\t" \
  "v_mfma_f32_16x16x32_bf16 v[60:63], a[92:95], a[244:247], v[60:63]\n\t" \
  "v_fma_f32 v176, v128, v174, v176\n\t" \
  "v_fma_f32 v180, v160, v174, v180\n\t" \
  "v_mfma_f32_16x16x32_bf16 v[60:63], a[156:159], a[228:231], v[60:63]\n\t" \
  "v_lshlrev_b32 v128, 16, v161\n\t" \
  "v_and_b32 v161, 0xffff0000, v161\n\t" \
  "v_mfma_f32_16x16x32_bf16 v[92:95], a[156:159], a[232:235], v[92:95]\n\t" \
  "v_fma_f32 v177, v128, v174, v177\n\t" \
  "v_fma_f32 v181, v161, v174, v181\n\t" \
  "v_mfma_f32_16x16x32_bf16 v[92:95], a[92:95], a[248:251], v[92:95]\n\t" \
  "s_add_u32 s97, s78, 7864320\n\t" \
  "v_lshlrev_b32 v128, 16, v162\n\t" \
  "v_and_b32 v162, 0xffff0000, v162\n\t" \
  "v_mfma_f32_16x16x32_bf16 v[124:127], a[92:95], a[252:255], v[124:127]\n\t" \
  "s_waitcnt lgkmcnt(0)\n\t" \
  "s_bfe_u32 s97, s97, 0x140003\n\t" \
  "s_add_u32 s97, s97, s82\n\t" \
  "v_mfma_f32_16x16x32_bf16 v[124:127], a[156:159], a[236:239], v[124:127]\n\t" \
  "s_barrier\n\t" \
  "v_mfma_f32_16x16x32_bf16 v[0:3], a[160:163], a[0:3], v[0:3]\n\t" \
  "buffer_load_dwordx4 a[224:227], v248, s[16:19], s97 offen sc1\n\t" \
  "v_mfma_f32_16x16x32_bf16 v[0:3], a[96:99], a[16:19], v[0:3]\n\t" \
  "ds_read_b64_tr_b16 a[128:129], v192 offset:0\n\t" \
  "v_fma_f32 v178, v128, v174, v178\n\t" \
  "v_mfma_f32_16x16x32_bf16 v[32:35], a[96:99], a[20:23], v[32:35]\n\t" \
  "ds_read_b64_tr_b16 a[130:131], v192 offset:1024\n\t" \
  "v_fma_f32 v182, v162, v174, v182\n\t" \
  "v_mfma_f32_16x16x32_bf16 v[32:35], a[160:163], a[4:7], v[32:35]\n\t" \
  "v_lshlrev_b32 v128, 16, v163\n\t" \
  "v_and_b32 v163, 0xffff0000, v163\n\t" \
  "v_mfma_f32_16x16x32_bf16 v[64:67], a[160:163], a[8:11], v[64:67]\n\t" \
  "ds_read_b64_tr_b16 a[132:133], v193 offset:0\n\t" \
  "v_fma_f32 v179, v128, v174, v179\n\t" \
  "v_mfma_f32_16x16x32_bf16 v[64:67], a[96:99], a[24:27], v[64:67]\n\t" \
  "ds_read_b64_tr_b16 a[134:135], v193 offset:1024\n\t" \
  "v_fma_f32 v183, v163, v174, v183\n\t" \
  "v_mfma_f32_16x16x32_bf16 v[96:99], a[96:99], a[28:31], v[96:99]\n\t" \
  "s_waitcnt vmcnt(21)\n\t" \
  "v_lshlrev_b32 v128, 16, v164\n\t" \
  "v_and_b32 v164, 0xffff0000, v164\n\t" \
  "v_mfma_f32_16x16x32_bf16 v[96:99], a[160:163], a[12:15], v[96:99]\n\t" \
  "ds_read_b64_tr_b16 a[136:137], v194 offset:0\n\t" \
  "v_fma_f32 v176, v128, v175, v176\n\t" \
  "v_mfma_f32_16x16x32_bf16 v[4:7], a[164:167], a[0:3], v[4:7]\n\t" \
  "buffer_load_dwordx4 a[228:231], v249, s[16:19], s97 offen sc1\n\t" \
  "v_mfma_f32_16x16x32_bf16 v[4:7], a[100:103], a[16:19], v[4:7]\n\t" \
  "ds_read_b64_tr_b16 a[138:139], v194 offset:1024\n\t" \
  "v_fma_f32 v180, v164, v175, v180\n\t" \
  "v_mfma_f32_16x16x32_bf16 v[36:39], a[100:103], a[20:23], v[36:39]\n\t" \
  "ds_read_b64_tr_b16 a[140:141], v195 offset:0\n\t" \
  "v_lshlrev_b32 v128, 16, v165\n\t" \
  "v_mfma_f32_16x16x32_bf16 v[36:39], a[164:167], a[4:7], v[36:39]\n\t" \
  "v_and_b32 v165, 0xffff0000, v165\n\t" \
  "v_fma_f32 v177, v128, v175, v177\n\t" \
  "v_mfma_f32_16x16x32_bf16 v[68:71], a[164:167], a[8:11], v[68:71]\n\t" \
  "ds_read_b64_tr_b16 a[142:143], v195 offset:1024\n\t" \
  "v_fma_f32 v181, v165, v175, v181\n\t" \
  "v_mfma_f32_16x16x32_bf16 v[68:71], a[100:103], a[24:27], v[68:71]\n\t" \
  "ds_read_b64_tr_b16 a[144:145], v196 offset:0\n\t" \
  "v_lshlrev_b32 v128, 16, v166\n\t" \
  "v_mfma_f32_16x16x32_bf16 v[100:103], a[100:103], a[28:31], v[100:103]\n\t" \
  "v_and_b32 v166, 0xffff0000, v166\n\t" \
  "v_fma_f32 v178, v128, v175, v178\n\t" \
  "v_mfma_f32_16x16x32_bf16 v[100:103], a[164:167], a[12:15], v[100:103]\n\t" \
  "buffer_load_dwordx4 a[232:235], v250, s[16:19], s97 offen sc1\n\t" \
  "v_mfma_f32_16x16x32_bf16 v[8:11], a[168:171], a[0:3], v[8:11]\n\t" \
  "ds_read_b64_tr_b16 a[146:147], v196 offset:1024\n\t" \
  "v_fma_f32 v182, v166, v175, v182\n\t" \
  "v_mfma_f32_16x16x32_bf16 v[8:11], a[104:107], a[16:19], v[8:11]\n\t" \
  "ds_read_b64_tr_b16 a[148:149], v197 offset:0\n\t" \
  "v_lshlrev_b32 v128, 16, v167\n\t" \
  "v_mfma_f32_16x16x32_bf16 v[40:43], a[104:107], a[20:23], v[40:43]\n\t" \
  "ds_read_b64_tr_b16 a[150:151], v197 offset:1024\n\t" \
  "v_and_b32 v167, 0xffff0000, v167\n\t" \
  "v_mfma_f32_16x16x32_bf16 v[40:43], a[168:171], a[4:7], v[40:43]\n\t" \
  "v_fma_f32 v179, v128, v175, v179\n\t" \
  "v_fma_f32 v183, v167, v175, v183\n\t" \
  "v_mfma_f32_16x16x32_bf16 v[72:75], a[168:171], a[8:11], v[72:75]\n\t" \
  "ds_read_b64_tr_b16 a[152:153], v198 offset:0\n\t" \
  "v_and_b32 v138, 7, v233\n\t" \
  "v_mfma_f32_16x16x32_bf16 v[72:75], a[104:107], a[24:27], v[72:75]\n\t" \
  "ds_read_b64_tr_b16 a[154:155], v198 offset:1024\n\t" \
  "v_lshlrev_b32 v138, 5, v138\n\t" \
  "v_mfma_f32_16x16x32_bf16 v[104:107], a[104:107], a[28:31], v[104:107]\n\t" \
  "v_permlane32_swap_b32 v176, v178\n\t" \
  "v_mfma_f32_16x16x32_bf16 v[104:107], a[168:171], a[12:15], v[104:107]\n\t" \
  "buffer_load_dwordx4 a[236:239], v251, s[16:19], s97 offen sc1\n\t" \
  "v_mfma_f32_16x16x32_bf16 v[12:15], a[172:175], a[0:3], v[12:15]\n\t" \
  "v_permlane32_swap_b32 v180, v182\n\t" \
  "v_mfma_f32_16x16x32_bf16 v[12:15], a[108:111], a[16:19], v[12:15]\n\t" \
  "ds_read_b64_tr_b16 a[156:157], v199 offset:0\n\t" \
  "v_mfma_f32_16x16x32_bf16 v[44:47], a[108:111], a[20:23], v[44:47]\n\t" \
  "ds_read_b64_tr_b16 a[158:159], v199 offset:1024\n\t" \
  "v_mfma_f32_16x16x32_bf16 v[44:47], a[172:175], a[4:7], v[44:47]\n\t" \
  "v_permlane32_swap_b32 v177, v179\n\t" \
  "v_mfma_f32_16x16x32_bf16 v[76:79], a[172:175], a[8:11], v[76:79]\n\t" \
  "ds_read_b64_tr_b16 a[64:65], v192 offset:8192\n\t" \
  "v_mfma_f32_16x16x32_bf16 v[76:79], a[108:111], a[24:27], v[76:79]\n\t" \
  "s_add_u32 s97, s97, s83\n\t" \
  "v_permlane32_swap_b32 v181, v183\n\t" \
  "v_mfma_f32_16x16x32_bf16 v[108:111], a[108:111], a[28:31], v[108:111]\n\t" \
  "ds_read_b64_tr_b16 a[66:67], v192 offset:9216\n\t" \
  "v_add_f32 v176, v176, v178\n\t" \
  "v_mfma_f32_16x16x32_bf16 v[108:111], a[172:175], a[12:15], v[108:111]\n\t" \
  "ds_read_b64_tr_b16 a[68:69], v193 offset:8192\n\t" \
  "v_add_f32 v177, v177, v179\n\t" \
  "v_mfma_f32_16x16x32_bf16 v[16:19], a[176:179], a[0:3], v[16:19]\n\t" \
  "buffer_load_dwordx4 a[240:243], v248, s[16:19], s97 offen sc1\n\t" \
  "v_mfma_f32_16x16x32_bf16 v[16:19], a[112:115], a[16:19], v[16:19]\n\t" \
  "ds_read_b64_tr_b16 a[70:71], v193 offset:9216\n\t" \
  "v_add_f32 v180, v180, v182\n\t" \
  "v_mfma_f32_16x16x32_bf16 v[48:51], a[112:115], a[20:23], v[48:51]\n\t" \
  "ds_read_b64_tr_b16 a[72:73], v194 offset:8192\n\t" \
  "v_add_f32 v181, v181, v183\n\t" \
  "v_mfma_f32_16x16x32_bf16 v[48:51], a[176:179], a[4:7], v[48:51]\n\t" \
  "ds_read_b64_tr_b16 a[74:75], v194 offset:9216\n\t" \
  "v_lshrrev_b32 v139, 4, v233\n\t" \
  "v_mfma_f32_16x16x32_bf16 v[80:83], a[176:179], a[8:11], v[80:83]\n\t" \
  "v_permlane16_swap_b32 v176, v177\n\t" \
  "v_mfma_f32_16x16x32_bf16 v[80:83], a[112:115], a[24:27], v[80:83]\n\t" \
  "buffer_load_dwordx4 a[244:247], v249, s[16:19], s97 offen sc1\n\t" \
  "v_mfma_f32_16x16x32_bf16 v[112:115], a[112:115], a[28:31], v[112:115]\n\t" \
  "ds_read_b64_tr_b16 a[76:77], v195 offset:8192\n\t" \
  "v_mfma_f32_16x16x32_bf16 v[112:115], a[176:179], a[12:15], v[112:115]\n\t" \
  "ds_read_b64_tr_b16 a[78:79], v195 offset:9216\n\t" \
  "v_mfma_f32_16x16x32_bf16 v[20:23], a[180:183], a[0:3], v[20:23]\n\t" \
  "ds_read_b64_tr_b16 a[80:81], v196 offset:8192\n\t" \
  "v_mfma_f32_16x16x32_bf16 v[20:23], a[116:119], a[16:19], v[20:23]\n\t" \
  "v_permlane16_swap_b32 v180, v181\n\t" \
  "v_mfma_f32_16x16x32_bf16 v[52:55], a[116:119], a[20:23], v[52:55]\n\t" \
  "buffer_load_dwordx4 a[248:251], v250, s[16:19], s97 offen sc1\n\t" \
  "v_mfma_f32_16x16x32_bf16 v[52:55], a[180:183], a[4:7], v[52:55]\n\t" \
  "ds_read_b64_tr_b16 a[82:83], v196 offset:9216\n\t" \
  "v_add_f32 v176, v176, v177\n\t" \
  "v_mfma_f32_16x16x32_bf16 v[84:87], a[180:183], a[8:11], v[84:87]\n\t" \
  "ds_read_b64_tr_b16 a[84:85], v197 offset:8192\n\t" \
  "v_add_f32 v180, v180, v181\n\t" \
  "v_mfma_f32_16x16x32_bf16 v[84:87], a[116:119], a[24:27], v[84:87]\n\t" \
  "ds_read_b64_tr_b16 a[86:87], v197 offset:9216\n\t" \
  "v_lshl_add_u32 v138, v139, 3, v138\n\t" \
  "v_mfma_f32_16x16x32_bf16 v[116:119], a[116:119], a[28:31], v[116:119]\n\t" \
  "ds_read_b64_tr_b16 a[88:89], v198 offset:8192\n\t" \
  "v_and_b32 v139, 0x7fffff00, v219\n\t" \
  "v_mfma_f32_16x16x32_bf16 v[116:119], a[180:183], a[12:15], v[116:119]\n\t" \
  "buffer_load_dwordx4 a[252:255], v251, s[16:19], s97 offen sc1\n\t" \
  "v_mfma_f32_16x16x32_bf16 v[24:27], a[184:187], a[0:3], v[24:27]\n\t" \
  "ds_read_b64_tr_b16 a[90:91], v198 offset:9216\n\t" \
  "v_add_f32_dpp v136, v176, v176 row_ror:8 row_mask:0xf bank_mask:0xf\n\t" \
  "v_mfma_f32_16x16x32_bf16 v[24:27], a[120:123], a[16:19], v[24:27]\n\t" \
  "ds_read_b64_tr_b16 a[92:93], v199 offset:8192\n\t" \
  "v_add_f32_dpp v137, v180, v180 row_ror:8 row_mask:0xf bank_mask:0xf\n\t" \
  "v_mfma_f32_16x16x32_bf16 v[56:59], a[120:123], a[20:23], v[56:59]\n\t" \
  "ds_read_b64_tr_b16 a[94:95], v199 offset:9216\n\t" \
  "v_add_u32 v138, v139, v138\n\t" \
  "v_mfma_f32_16x16x32_bf16 v[56:59], a[184:187], a[4:7], v[56:59]\n\t" \
  "ds_write_b64 v138, v[136:137] offset:1024\n\t" \
  "v_mfma_f32_16x16x32_bf16 v[88:91], a[184:187], a[8:11], v[88:91]\n\t" \
  "buffer_load_dword v246, v217, s[16:19], s58 offen sc1\n\t" \
  "v_mfma_f32_16x16x32_bf16 v[88:91], a[120:123], a[24:27], v[88:91]\n\t" \
  "s_add_u32 s99, s53, 0\n\t" \
  "v_and_b32 v140, 3, v233\n\t" \
  "v_bfe_u32 v141, v233, 4, 1\n\t" \
  "v_mfma_f32_16x16x32_bf16 v[120:123], a[120:123], a[28:31], v[120:123]\n\t" \
  "v_lshl_add_u32 v140, v141, 2, v140\n\t" \
  "v_bfe_u32 v141, v233, 2, 2\n\t" \
  "v_mfma_f32_16x16x32_bf16 v[120:123], a[184:187], a[12:15], v[120:123]\n\t" \
  "v_lshlrev_b32 v141, 8, v141\n\t" \
  "v_lshl_add_u32 v141, v140, 3, v141\n\t" \
  "v_mfma_f32_16x16x32_bf16 v[28:31], a[188:191], a[0:3], v[28:31]\n\t" \
  "v_lshl_add_u32 v141, s3, 6, v141\n\t" \
  "v_lshlrev_b32 v140, 4, v140\n\t" \
  "v_mfma_f32_16x16x32_bf16 v[28:31], a[124:127], a[16:19], v[28:31]\n\t" \
  "buffer_load_dword v246, v245, s[68:71], s99 offen\n\t" \
  "v_mfma_f32_16x16x32_bf16 v[60:63], a[124:127], a[20:23], v[60:63]\n\t" \
  "v_and_b32 v142, 0x2c, v233\n\t" \
  "v_lshl_or_b32 v140, v142, 26, v140\n\t" \
  "v_mfma_f32_16x16x32_bf16 v[60:63], a[188:191], a[4:7], v[60:63]\n\t" \
  "v_add_u32 v141, 151552, v141\n\t" \
  "v_mov_b32 v145, v211\n\t" \
  "v_mfma_f32_16x16x32_bf16 v[92:95], a[188:191], a[8:11], v[92:95]\n\t" \
  "v_mov_b32 v147, v211\n\t" \
  "v_mfma_f32_16x16x32_bf16 v[92:95], a[124:127], a[24:27], v[92:95]\n\t" \
  "s_add_u32 s99, s53, 14336\n\t" \
  "v_mfma_f32_16x16x32_bf16 v[124:127], a[124:127], a[28:31], v[124:127]\n\t" \
  "s_waitcnt vmcnt(18) lgkmcnt(1)\n\t" \
  "v_mfma_f32_16x16x32_bf16 v[124:127], a[188:191], a[12:15], v[124:127]\n\t" \
  "s_barrier\n\t" \
  "v_mfma_f32_16x16x32_bf16 v[0:3], a[128:131], a[32:35], v[0:3]\n\t" \
  "buffer_load_dword v246, v245, s[68:71], s99 offen\n\t" \
  "v_mfma_f32_16x16x32_bf16 v[0:3], a[64:67], a[48:51], v[0:3]\n\t" \
  "ds_read_b64_tr_b16 a[160:161], v192 offset:16384\n\t" \
  "v_mfma_f32_16x16x32_bf16 v[32:35], a[64:67], a[52:55], v[32:35]\n\t" \
  "ds_read_b64_tr_b16 a[162:163], v192 offset:17408\n\t" \
  "v_mfma_f32_16x16x32_bf16 v[32:35], a[128:131], a[36:39], v[32:35]\n\t" \
  "ds_read_b64_tr_b16 a[164:165], v193 offset:16384\n\t" \
  "v_mfma_f32_16x16x32_bf16 v[64:67], a[128:131], a[40:43], v[64:67]\n\t" \
  "ds_read_b64_tr_b16 a[166:167], v193 offset:17408\n\t" \
  "v_mfma_f32_16x16x32_bf16 v[64:67], a[64:67], a[56:59], v[64:67]\n\t" \
  "ds_read_b64_tr_b16 a[168:169], v194 offset:16384\n\t" \
  "v_mfma_f32_16x16x32_bf16 v[96:99], a[64:67], a[60:63], v[96:99]\n\t" \
  "ds_read_b64_tr_b16 a[170:171], v194 offset:17408\n\t" \
  "v_mfma_f32_16x16x32_bf16 v[96:99], a[128:131], a[44:47], v[96:99]\n\t" \
  "ds_read_b64_tr_b16 a[172:173], v195 offset:16384\n\t" \
  "v_mfma_f32_16x16x32_bf16 v[4:7], a[132:135], a[32:35], v[4:7]\n\t" \
  "ds_read_b64_tr_b16 a[174:175], v195 offset:17408\n\t" \
  "v_mfma_f32_16x16x32_bf16 v[4:7], a[68:71], a[48:51], v[4:7]\n\t" \
  "ds_read_b64_tr_b16 a[176:177], v196 offset:16384\n\t" \
  "v_mfma_f32_16x16x32_bf16 v[36:39], a[68:71], a[52:55], v[36:39]\n\t" \
  "ds_read_b64_tr_b16 a[178:179], v196 offset:17408\n\t" \
  "v_mfma_f32_16x16x32_bf16 v[36:39], a[132:135], a[36:39], v[36:39]\n\t" \
  "ds_read_b64_tr_b16 a[180:181], v197 offset:16384\n\t" \
  "v_mfma_f32_16x16x32_bf16 v[68:71], a[132:135], a[40:43], v[68:71]\n\t" \
  "ds_read_b64_tr_b16 a[182:183], v197 offset:17408\n\t" \
  "v_mfma_f32_16x16x32_bf16 v[68:71], a[68:71], a[56:59], v[68:71]\n\t" \
  "ds_read_b64_tr_b16 a[184:185], v198 offset:16384\n\t" \
  "v_mfma_f32_16x16x32_bf16 v[100:103], a[68:71], a[60:63], v[100:103]\n\t" \
  "ds_read_b64_tr_b16 a[186:187], v198 offset:17408\n\t" \
  "v_mfma_f32_16x16x32_bf16 v[100:103], a[132:135], a[44:47], v[100:103]\n\t" \
  "ds_read_b64_tr_b16 a[188:189], v199 offset:16384\n\t" \
  "v_mfma_f32_16x16x32_bf16 v[8:11], a[136:139], a[32:35], v[8:11]\n\t" \
  "ds_read_b64_tr_b16 a[190:191], v199 offset:17408\n\t" \
  "v_mfma_f32_16x16x32_bf16 v[8:11], a[72:75], a[48:51], v[8:11]\n\t" \
  "ds_read_b64_tr_b16 a[96:97], v192 offset:24576\n\t" \
  "v_mfma_f32_16x16x32_bf16 v[40:43], a[72:75], a[52:55], v[40:43]\n\t" \
  "ds_read_b64_tr_b16 a[98:99], v192 offset:25600\n\t" \
  "v_mfma_f32_16x16x32_bf16 v[40:43], a[136:139], a[36:39], v[40:43]\n\t" \
  "v_mfma_f32_16x16x32_bf16 v[72:75], a[136:139], a[40:43], v[72:75]\n\t" \
  "ds_read_b64_tr_b16 a[100:101], v193 offset:24576\n\t" \
  "v_mfma_f32_16x16x32_bf16 v[72:75], a[72:75], a[56:59], v[72:75]\n\t" \
  "ds_read_b64_tr_b16 a[102:103], v193 offset:25600\n\t" \
  "v_mfma_f32_16x16x32_bf16 v[104:107], a[72:75], a[60:63], v[104:107]\n\t" \
  "v_mfma_f32_16x16x32_bf16 v[104:107], a[136:139], a[44:47], v[104:107]\n\t" \
  "ds_read_b64_tr_b16 a[104:105], v194 offset:24576\n\t" \
  "v_mfma_f32_16x16x32_bf16 v[12:15], a[140:143], a[32:35], v[12:15]\n\t" \
  "v_mfma_f32_16x16x32_bf16 v[12:15], a[76:79], a[48:51], v[12:15]\n\t" \
  "ds_read_b64_tr_b16 a[106:107], v194 offset:25600\n\t" \
  "v_mfma_f32_16x16x32_bf16 v[44:47], a[76:79], a[52:55], v[44:47]\n\t" \
  "ds_read_b64_tr_b16 a[108:109], v195 offset:24576\n\t" \
  "v_mfma_f32_16x16x32_bf16 v[44:47], a[140:143], a[36:39], v[44:47]\n\t" \
  "v_mfma_f32_16x16x32_bf16 v[76:79], a[140:143], a[40:43], v[76:79]\n\t" \
  "ds_read_b64_tr_b16 a[110:111], v195 offset:25600\n\t" \
  "v_mfma_f32_16x16x32_bf16 v[76:79], a[76:79], a[56:59], v[76:79]\n\t" \
  "ds_read_b64_tr_b16 a[112:113], v196 offset:24576\n\t" \
  "s_add_u32 m0, s76, 16384\n\t" \
  "v_mfma_f32_16x16x32_bf16 v[108:111], a[76:79], a[60:63], v[108:111]\n\t" \
  "v_mfma_f32_16x16x32_bf16 v[108:111], a[140:143], a[44:47], v[108:111]\n\t" \
  "buffer_load_dwordx4 v224, s[60:63], s38 offen offset:0 lds\n\t" \
  "v_mfma_f32_16x16x32_bf16 v[16:19], a[144:147], a[32:35], v[16:19]\n\t" \
  "ds_read_b64_tr_b16 a[114:115], v196 offset:25600\n\t" \
  "v_mfma_f32_16x16x32_bf16 v[16:19], a[80:83], a[48:51], v[16:19]\n\t" \
  "ds_read_b64_tr_b16 a[116:117], v197 offset:24576\n\t" \
  "v_mfma_f32_16x16x32_bf16 v[48:51], a[80:83], a[52:55], v[48:51]\n\t" \
  "ds_read_b64_tr_b16 a[118:119], v197 offset:25600\n\t" \
  "v_mfma_f32_16x16x32_bf16 v[48:51], a[144:147], a[36:39], v[48:51]\n\t" \
  "v_mfma_f32_16x16x32_bf16 v[80:83], a[144:147], a[40:43], v[80:83]\n\t" \
  "buffer_load_dwordx4 v225, s[60:63], s38 offen offset:1024 lds\n\t" \
  "v_mfma_f32_16x16x32_bf16 v[80:83], a[80:83], a[56:59], v[80:83]\n\t" \
  "ds_read_b64_tr_b16 a[120:121], v198 offset:24576\n\t" \
  "v_mfma_f32_16x16x32_bf16 v[112:115], a[80:83], a[60:63], v[112:115]\n\t" \
  "ds_read_b64_tr_b16 a[122:123], v198 offset:25600\n\t" \
  "v_mfma_f32_16x16x32_bf16 v[112:115], a[144:147], a[44:47], v[112:115]\n\t" \
  "ds_read_b64_tr_b16 a[124:125], v199 offset:24576\n\t" \
  "v_mfma_f32_16x16x32_bf16 v[20:23], a[148:151], a[32:35], v[20:23]\n\t" \
  "v_mfma_f32_16x16x32_bf16 v[20:23], a[84:87], a[48:51], v[20:23]\n\t" \
  "buffer_load_dwordx4 v226, s[60:63], s38 offen offset:2048 lds\n\t" \
  "v_mfma_f32_16x16x32_bf16 v[52:55], a[84:87], a[52:55], v[52:55]\n\t" \
  "ds_read_b64_tr_b16 a[126:127], v199 offset:25600\n\t" \
  "v_mfma_f32_16x16x32_bf16 v[52:55], a[148:151], a[36:39], v[52:55]\n\t" \
  "v_mfma_f32_16x16x32_bf16 v[84:87], a[148:151], a[40:43], v[84:87]\n\t" \
  "v_mfma_f32_16x16x32_bf16 v[84:87], a[84:87], a[56:59], v[84:87]\n\t" \
  "v_mfma_f32_16x16x32_bf16 v[116:119], a[84:87], a[60:63], v[116:119]\n\t" \
  "buffer_load_dwordx4 v227, s[60:63], s38 offen offset:3072 lds\n\t" \
  "v_mfma_f32_16x16x32_bf16 v[116:119], a[148:151], a[44:47], v[116:119]\n\t" \
  "s_add_u32 m0, s76, 49152\n\t" \
  "v_mfma_f32_16x16x32_bf16 v[24:27], a[152:155], a[32:35], v[24:27]\n\t" \
  "v_mfma_f32_16x16x32_bf16 v[24:27], a[88:91], a[48:51], v[24:27]\n\t" \
  "v_mfma_f32_16x16x32_bf16 v[56:59], a[88:91], a[52:55], v[56:59]\n\t" \
  "v_mfma_f32_16x16x32_bf16 v[56:59], a[152:155], a[36:39], v[56:59]\n\t" \
  "buffer_load_dwordx4 v230, s[64:67], s38 offen offset:0 lds\n\t" \
  "v_mfma_f32_16x16x32_bf16 v[88:91], a[152:155], a[40:43], v[88:91]\n\t" \
  "v_mfma_f32_16x16x32_bf16 v[88:91], a[88:91], a[56:59], v[88:91]\n\t" \
  "v_mfma_f32_16x16x32_bf16 v[120:123], a[88:91], a[60:63], v[120:123]\n\t" \
  "v_mfma_f32_16x16x32_bf16 v[120:123], a[152:155], a[44:47], v[120:123]\n\t" \
  "v_mfma_f32_16x16x32_bf16 v[28:31], a[156:159], a[32:35], v[28:31]\n\t" \
  "buffer_load_dwordx4 v231, s[64:67], s38 offen offset:1024 lds\n\t" \
  "v_mfma_f32_16x16x32_bf16 v[28:31], a[92:95], a[48:51], v[28:31]\n\t" \
  "v_mfma_f32_16x16x32_bf16 v[60:63], a[92:95], a[52:55], v[60:63]\n\t" \
  "v_mfma_f32_16x16x32_bf16 v[60:63], a[156:159], a[36:39], v[60:63]\n\t" \
  "v_mfma_f32_16x16x32_bf16 v[92:95], a[156:159], a[40:43], v[92:95]\n\t" \
  "v_mfma_f32_16x16x32_bf16 v[92:95], a[92:95], a[56:59], v[92:95]\n\t" \
  "v_mfma_f32_16x16x32_bf16 v[124:127], a[92:95], a[60:63], v[124:127]\n\t" \
  "s_waitcnt vmcnt(17) lgkmcnt(0)\n\t" \
  "v_mfma_f32_16x16x32_bf16 v[124:127], a[156:159], a[44:47], v[124:127]\n\t" \
  "s_barrier\n\t" \
  "v_mfma_f32_16x16x32_bf16 v[0:3], a[160:163], a[192:195], v[0:3]\n\t" \
  "buffer_load_dwordx4 v230, s[64:67], s38 offen offset:2048 lds\n\t" \
  "v_mfma_f32_16x16x32_bf16 v[0:3], a[96:99], a[208:211], v[0:3]\n\t" \
  "ds_read_b64_tr_b16 a[128:129], v192 offset:32768\n\t" \
  "v_mfma_f32_16x16x32_bf16 v[32:35], a[96:99], a[212:215], v[32:35]\n\t" \
  "ds_read_b64_tr_b16 a[130:131], v192 offset:33792\n\t" \
  "v_mfma_f32_16x16x32_bf16 v[32:35], a[160:163], a[196:199], v[32:35]\n\t" \
  "ds_read_b64 v[142:143], v141 offset:1024\n\t" \
  "v_mfma_f32_16x16x32_bf16 v[64:67], a[160:163], a[200:203], v[64:67]\n\t" \
  "ds_read_b64_tr_b16 a[132:133], v193 offset:32768\n\t" \
  "v_mfma_f32_16x16x32_bf16 v[64:67], a[96:99], a[216:219], v[64:67]\n\t" \
  "buffer_load_dwordx4 v231, s[64:67], s38 offen offset:3072 lds\n\t" \
  "v_mfma_f32_16x16x32_bf16 v[96:99], a[96:99], a[220:223], v[96:99]\n\t" \
  "s_add_u32 m0, s76, 32768\n\t" \
  "ds_read_b64_tr_b16 a[134:135], v193 offset:33792\n\t" \
  "s_add_u32 s96, s38, 16384\n\t" \
  "v_mfma_f32_16x16x32_bf16 v[96:99], a[160:163], a[204:207], v[96:99]\n\t" \
  "ds_read_b64_tr_b16 a[136:137], v194 offset:32768\n\t" \
  "v_mfma_f32_16x16x32_bf16 v[4:7], a[164:167], a[192:195], v[4:7]\n\t" \
  "buffer_load_dwordx4 v224, s[60:63], s96 offen offset:0 lds\n\t" \
  "v_mfma_f32_16x16x32_bf16 v[4:7], a[100:103], a[208:211], v[4:7]\n\t" \
  "s_waitcnt lgkmcnt(3)\n\t" \
  "v_add_f32_dpp v142, v142, v142 row_ror:4 row_mask:0xf bank_mask:0xf\n\t" \
  "v_add_f32_dpp v143, v143, v143 row_ror:4 row_mask:0xf bank_mask:0xf\n\t" \
  "v_mfma_f32_16x16x32_bf16 v[36:39], a[100:103], a[212:215], v[36:39]\n\t" \
  "ds_read_b64_tr_b16 a[138:139], v194 offset:33792\n\t" \
  "v_add_f32_dpp v144, v142, v142 row_ror:8 row_mask:0xf bank_mask:0xf\n\t" \
  "v_mfma_f32_16x16x32_bf16 v[36:39], a[164:167], a[196:199], v[36:39]\n\t" \
  "ds_read_b64_tr_b16 a[140:141], v195 offset:32768\n\t" \
  "v_add_f32_dpp v146, v143, v143 row_ror:8 row_mask:0xf bank_mask:0xf\n\t" \
  "v_mfma_f32_16x16x32_bf16 v[68:71], a[164:167], a[200:203], v[68:71]\n\t" \
  "buffer_store_dwordx4 v[144:147], v140, s[16:19], s90 offen sc1\n\t" \
  "ds_read_b64_tr_b16 a[142:143], v195 offset:33792\n\t" \
  "ds_read_b64_tr_b16 a[144:145], v196 offset:32768\n\t" \
  "ds_read_b64_tr_b16 a[146:147], v196 offset:33792\n\t" \
  "v_mfma_f32_16x16x32_bf16 v[68:71], a[100:103], a[216:219], v[68:71]\n\t" \
  "buffer_load_dwordx4 v225, s[60:63], s96 offen offset:1024 lds\n\t" \
  "v_mfma_f32_16x16x32_bf16 v[100:103], a[100:103], a[220:223], v[100:103]\n\t" \
  "ds_read_b64_tr_b16 a[148:149], v197 offset:32768\n\t" \
  "v_mfma_f32_16x16x32_bf16 v[100:103], a[164:167], a[204:207], v[100:103]\n\t" \
  ".Lhs5:\n\ts_cbranch_execz .Lhs5n\n\t" \
  "ds_read_b64_tr_b16 a[150:151], v197 offset:33792\n\t" \
  "v_mfma_f32_16x16x32_bf16 v[8:11], a[168:171], a[192:195], v[8:11]\n\t" \
  "buffer_load_dwordx4 v226, s[60:63], s96 offen offset:2048 lds\n\t" \
  "v_mfma_f32_16x16x32_bf16 v[8:11], a[104:107], a[208:211], v[8:11]\n\t" \
  "ds_read_b64_tr_b16 a[152:153], v198 offset:32768\n\t" \
  "v_mfma_f32_16x16x32_bf16 v[40:43], a[104:107], a[212:215], v[40:43]\n\t" \
  "ds_read_b64_tr_b16 a[154:155], v198 offset:33792\n\t" \
  "v_mfma_f32_16x16x32_bf16 v[40:43], a[168:171], a[196:199], v[40:43]\n\t" \
  "ds_read_b64_tr_b16 a[156:157], v199 offset:32768\n\t" \
  "v_mfma_f32_16x16x32_bf16 v[72:75], a[168:171], a[200:203], v[72:75]\n\t" \
  "buffer_load_dwordx4 v227, s[60:63], s96 offen offset:3072 lds\n\t" \
  "v_mfma_f32_16x16x32_bf16 v[72:75], a[104:107], a[216:219], v[72:75]\n\t" \
  "ds_read_b64_tr_b16 a[158:159], v199 offset:33792\n\t" \
  "s_add_u32 s99, s53, 28672\n\t" \
  "v_mfma_f32_16x16x32_bf16 v[104:107], a[104:107], a[220:223], v[104:107]\n\t" \
  "ds_read_b64_tr_b16 a[64:65], v192 offset:40960\n\t" \
  "v_mfma_f32_16x16x32_bf16 v[104:107], a[168:171], a[204:207], v[104:107]\n\t" \
  "ds_read_b64_tr_b16 a[66:67], v192 offset:41984\n\t" \
  "v_mfma_f32_16x16x32_bf16 v[12:15], a[172:175], a[192:195], v[12:15]\n\t" \
  "buffer_load_dword v246, v245, s[68:71], s99 offen\n\t" \
  "v_mfma_f32_16x16x32_bf16 v[12:15], a[108:111], a[208:211], v[12:15]\n\t" \
  "ds_read_b64_tr_b16 a[68:69], v193 offset:40960\n\t" \
  "v_mfma_f32_16x16x32_bf16 v[44:47], a[108:111], a[212:215], v[44:47]\n\t" \
  "ds_read_b64_tr_b16 a[70:71], v193 offset:41984\n\t" \
  "v_mfma_f32_16x16x32_bf16 v[44:47], a[172:175], a[196:199], v[44:47]\n\t" \
  "ds_read_b64_tr_b16 a[72:73], v194 offset:40960\n\t" \
  "v_mfma_f32_16x16x32_bf16 v[76:79], a[172:175], a[200:203], v[76:79]\n\t" \
  "ds_read_b64_tr_b16 a[74:75], v194 offset:41984\n\t" \
  "v_mfma_f32_16x16x32_bf16 v[76:79], a[108:111], a[216:219], v[76:79]\n\t" \
  "ds_read_b64_tr_b16 a[76:77], v195 offset:40960\n\t" \
  "s_add_u32 s99, s53, 43008\n\t" \
  "v_mfma_f32_16x16x32_bf16 v[108:111], a[108:111], a[220:223], v[108:111]\n\t" \
  "ds_read_b64_tr_b16 a[78:79], v195 offset:41984\n\t" \
  "v_mfma_f32_16x16x32_bf16 v[108:111], a[172:175], a[204:207], v[108:111]\n\t" \
  "buffer_load_dword v246, v245, s[68:71], s99 offen\n\t" \
  "v_mfma_f32_16x16x32_bf16 v[16:19], a[176:179], a[192:195], v[16:19]\n\t" \
  "ds_read_b64_tr_b16 a[80:81], v196 offset:40960\n\t" \
  "v_mfma_f32_16x16x32_bf16 v[16:19], a[112:115], a[208:211], v[16:19]\n\t" \
  "ds_read_b64_tr_b16 a[82:83], v196 offset:41984\n\t" \
  "v_mfma_f32_16x16x32_bf16 v[48:51], a[112:115], a[212:215], v[48:51]\n\t" \
  "ds_read_b64_tr_b16 a[84:85], v197 offset:40960\n\t" \
  "v_mfma_f32_16x16x32_bf16 v[48:51], a[176:179], a[196:199], v[48:51]\n\t" \
  "ds_read_b64_tr_b16 a[86:87], v197 offset:41984\n\t" \
  "v_mfma_f32_16x16x32_bf16 v[80:83], a[176:179], a[200:203], v[80:83]\n\t" \
  "ds_read_b64_tr_b16 a[88:89], v198 offset:40960\n\t" \
  "v_mfma_f32_16x16x32_bf16 v[80:83], a[112:115], a[216:219], v[80:83]\n\t" \
  "v_mfma_f32_16x16x32_bf16 v[112:115], a[112:115], a[220:223], v[112:115]\n\t" \
  "ds_read_b64_tr_b16 a[90:91], v198 offset:41984\n\t" \
  "v_mfma_f32_16x16x32_bf16 v[112:115], a[176:179], a[204:207], v[112:115]\n\t" \
  "ds_read_b64_tr_b16 a[92:93], v199 offset:40960\n\t" \
  "v_mfma_f32_16x16x32_bf16 v[20:23], a[180:183], a[192:195], v[20:23]\n\t" \
  "v_mfma_f32_16x16x32_bf16 v[20:23], a[116:119], a[208:211], v[20:23]\n\t" \
  "ds_read_b64_tr_b16 a[94:95], v199 offset:41984\n\t" \
  "v_mfma_f32_16x16x32_bf16 v[52:55], a[116:119], a[212:215], v[52:55]\n\t" \
  "v_mfma_f32_16x16x32_bf16 v[52:55], a[180:183], a[196:199], v[52:55]\n\t" \
  "v_mfma_f32_16x16x32_bf16 v[84:87], a[180:183], a[200:203], v[84:87]\n\t" \
  "v_mfma_f32_16x16x32_bf16 v[84:87], a[116:119], a[216:219], v[84:87]\n\t" \
  "v_mfma_f32_16x16x32_bf16 v[116:119], a[116:119], a[220:223], v[116:119]\n\t" \
  "v_mfma_f32_16x16x32_bf16 v[116:119], a[180:183], a[204:207], v[116:119]\n\t" \
  "v_mfma_f32_16x16x32_bf16 v[24:27], a[184:187], a[192:195], v[24:27]\n\t" \
  "v_mfma_f32_16x16x32_bf16 v[24:27], a[120:123], a[208:211], v[24:27]\n\t" \
  "v_mfma_f32_16x16x32_bf16 v[56:59], a[120:123], a[212:215], v[56:59]\n\t" \
  "v_mfma_f32_16x16x32_bf16 v[56:59], a[184:187], a[196:199], v[56:59]\n\t" \
  "v_mfma_f32_16x16x32_bf16 v[88:91], a[184:187], a[200:203], v[88:91]\n\t" \
  "v_mfma_f32_16x16x32_bf16 v[88:91], a[120:123], a[216:219], v[88:91]\n\t" \
  "v_mfma_f32_16x16x32_bf16 v[120:123], a[120:123], a[220:223], v[120:123]\n\t" \
  "v_mfma_f32_16x16x32_bf16 v[120:123], a[184:187], a[204:207], v[120:123]\n\t" \
  "v_mfma_f32_16x16x32_bf16 v[28:31], a[188:191], a[192:195], v[28:31]\n\t" \
  "v_mfma_f32_16x16x32_bf16 v[28:31], a[124:127], a[208:211], v[28:31]\n\t" \
  "v_mfma_f32_16x16x32_bf16 v[60:63], a[124:127], a[212:215], v[60:63]\n\t" \
  "v_mfma_f32_16x16x32_bf16 v[60:63], a[188:191], a[196:199], v[60:63]\n\t" \
  "v_mfma_f32_16x16x32_bf16 v[92:95], a[188:191], a[200:203], v[92:95]\n\t" \
  "v_mfma_f32_16x16x32_bf16 v[92:95], a[124:127], a[216:219], v[92:95]\n\t" \
  "v_mfma_f32_16x16x32_bf16 v[124:127], a[124:127], a[220:223], v[124:127]\n\t" \
  "v_mfma_f32_16x16x32_bf16 v[124:127], a[188:191], a[204:207], v[124:127]\n\t" \
  "s_waitcnt vmcnt(18) lgkmcnt(0)\n\t" \
  FSTP("s_waitcnt lgkmcnt(0)\n\t") \
  FSTP("v_writelane_b32 v255, s94, 52\n\t") \
  FSTP("v_writelane_b32 v255, s92, 53\n\t") \
  FSTA("s_memrealtime s[94:95]\n\t") \
  FSTA("s_memtime s[92:93]\n\t") \
  FSTA("s_waitcnt lgkmcnt(0)\n\t") \
  FSTA("v_writelane_b32 v255, s94, 16\n\t") \
  FSTA("v_writelane_b32 v255, s92, 17\n\t") \
  ".subsection 1\n\t" \
  ".Lfr_s:\n\t" \
  "s_mov_b32 s54, 0\n\t" \
  ".Lfr_r:\n\t" \
  "s_sleep 1\n\t" \
  "buffer_load_dwordx4 v[128:131], v248, s[16:19], s53 offen sc1\n\t" \
  "buffer_load_dwordx4 v[132:135], v248, s[16:19], s58 offen sc1\n\t" \
  "buffer_load_dwordx4 v[136:139], v248, s[16:19], s90 offen sc1\n\t" \
  "buffer_load_dwordx4 v[140:143], v248, s[16:19], s91 offen sc1\n\t" \
  "buffer_load_dwordx4 v[160:163], v249, s[16:19], s96 offen sc1\n\t" \
  "buffer_load_dwordx4 v[164:167], v249, s[16:19], s97 offen sc1\n\t" \
  "buffer_load_dwordx4 v[168:171], v249, s[16:19], s99 offen sc1\n\t" \
  "buffer_load_dwordx4 v[172:175], v249, s[16:19], s57 offen sc1\n\t" \
  "s_waitcnt vmcnt(0)\n\t" \
  "v_xor_b32 v144, v211, v129\n\t" \
  "v_bitop3_b32 v144, v144, v131, v211 bitop3:0xf6\n\t" \
  "v_bitop3_b32 v144, v144, v133, v211 bitop3:0xf6\n\t" \
  "v_bitop3_b32 v144, v144, v135, v211 bitop3:0xf6\n\t" \
  "v_bitop3_b32 v144, v144, v137, v211 bitop3:0xf6\n\t" \
  "v_bitop3_b32 v144, v144, v139, v211 bitop3:0xf6\n\t" \
  "v_bitop3_b32 v144, v144, v141, v211 bitop3:0xf6\n\t" \
  "v_bitop3_b32 v144, v144, v143, v211 bitop3:0xf6\n\t" \
  "v_bitop3_b32 v144, v144, v161, v211 bitop3:0xf6\n\t" \
  "v_bitop3_b32 v144, v144, v163, v211 bitop3:0xf6\n\t" \
  "v_bitop3_b32 v144, v144, v165, v211 bitop3:0xf6\n\t" \
  "v_bitop3_b32 v144, v144, v167, v211 bitop3:0xf6\n\t" \
  "v_bitop3_b32 v144, v144, v169, v211 bitop3:0xf6\n\t" \
  "v_bitop3_b32 v144, v144, v171, v211 bitop3:0xf6\n\t" \
  "v_bitop3_b32 v144, v144, v173, v211 bitop3:0xf6\n\t" \
  "v_bitop3_b32 v144, v144, v175, v211 bitop3:0xf6\n\t" \
  "v_cmp_ne_u32 vcc, 0, v144\n\t" \
  "v_add_f32 v146, v128, v132\n\t" \
  "v_add_f32 v148, v136, v140\n\t" \
  "v_add_f32 v147, v130, v134\n\t" \
  "v_add_f32 v149, v138, v142\n\t" \
  "v_add_f32 v146, v146, v148\n\t" \
  "v_add_f32 v147, v147, v149\n\t" \
  "s_cbranch_vccz .Lfr_k\n\t" \
  "s_add_u32 s54, s54, 1\n\t" \
  "s_cmp_lt_u32 s54, " FQ_CAP "\n\t" \
  "s_cbranch_scc1 .Lfr_r\n\t" \
  "buffer_store_dword v211, off, s[16:19], 0 offset:256 sc1\n\t" \
  "s_branch .Lfr_k\n\t" \
  ".Lfq_s:\n\t" \
  "s_mov_b32 s97, 0\n\t" \
  ".Lfq_r:\n\t" \
  "buffer_load_dword v252, v210, s[16:19], s84 offen sc1\n\t" \
  "s_waitcnt vmcnt(0)\n\t" \
  "v_cmp_ne_u32 vcc, v211, v252\n\t" \
  "s_cbranch_vccz .Lfq_k\n\t" \
  "s_add_u32 s97, s97, 1\n\t" \
  "s_cmp_lt_u32 s97, " FQ_CAP "\n\t" \
  "s_cbranch_scc1 .Lfq_r\n\t" \
  "buffer_store_dword v211, off, s[16:19], 0 offset:256 sc1\n\t" \
  "s_branch .Lfq_k\n\t" \
  ".subsection 0\n\t"
static_assert(W_VOTE == 8192 && W_FLAGR == 32768 && W_FLAGO == 131072 && W_MODB == 2228224 && W_OT == 8388608 && W_XCH == 25165824 && W_EPOCH == 2490368 && W_FLAGH == 2494464 && W_ST1 == 2097152 && W_MODA == 2162688 && W_HT == 4194304 && W_PROBE == 16384);
#define STS(x)
#define AO_STV
#define STA(x)
#define STP(x)
#define AT_ASM \
  "s_cmp_eq_u32 s37, 0\n\t" \
  "s_cbranch_scc1 .Lsf_go\n\t" \
  "v_mfma_f32_16x16x32_bf16 v[28:31], a[156:159], a[224:227], v[28:31]\n\t" \
  "s_add_u32 s77, s51, 131072\n\t" \
  "v_add_u32 v210, 65536, v228\n\t" \
  "v_add_u32 v248, 14336, v240\n\t" \
  "v_mfma_f32_16x16x32_bf16 v[28:31], a[92:95], a[240:243], v[28:31]\n\t" \
  "v_add_u32 v211, 65536, v229\n\t" \
  "v_mov_b32 v216, 0x3f803f80\n\t" \
  "v_mfma_f32_16x16x32_bf16 v[24:27], a[152:155], a[224:227], v[24:27]\n\t" \
  "v_mov_b32 v217, 0x3f803f80\n\t" \
  "v_mov_b32 v218, 0x3f803f80\n\t" \
  "v_mfma_f32_16x16x32_bf16 v[24:27], a[88:91], a[240:243], v[24:27]\n\t" \
  "v_mov_b32 v219, 0x3f803f80\n\t" \
  "v_and_b32 v208, 1, v233\n\t" \
  "v_mfma_f32_16x16x32_bf16 v[20:23], a[148:151], a[224:227], v[20:23]\n\t" \
  "v_cvt_pk_bf16_f32 v31, v30, v31\n\t" \
  "v_cvt_pk_bf16_f32 v30, v28, v29\n\t" \
  "v_mfma_f32_16x16x32_bf16 v[20:23], a[84:87], a[240:243], v[20:23]\n\t" \
  "v_mul_u32_u24 v208, 0xaaaa, v208\n\t" \
  "v_or_b32 v208, 0x4444, v208\n\t" \
  "v_mfma_f32_16x16x32_bf16 v[16:19], a[144:147], a[224:227], v[16:19]\n\t" \
  "v_cvt_pk_bf16_f32 v28, v24, v25\n\t" \
  "v_cvt_pk_bf16_f32 v29, v26, v27\n\t" \
  "v_mfma_f32_16x16x32_bf16 v[16:19], a[80:83], a[240:243], v[16:19]\n\t" \
  "v_lshl_or_b32 v208, v208, 16, v208\n\t" \
  "v_mov_b32 v160, 0\n\t" \
  "v_mfma_f32_16x16x32_bf16 v[12:15], a[140:143], a[224:227], v[12:15]\n\t" \
  "v_cvt_pk_bf16_f32 v27, v22, v23\n\t" \
  "v_cvt_pk_bf16_f32 v26, v20, v21\n\t" \
  "v_mfma_f32_16x16x32_bf16 v[12:15], a[76:79], a[240:243], v[12:15]\n\t" \
  "v_mov_b32 v161, 0\n\t" \
  "v_mov_b32 v164, 0\n\t" \
  "v_mfma_f32_16x16x32_bf16 v[8:11], a[136:139], a[224:227], v[8:11]\n\t" \
  "s_mov_b32 s54, 0\n\t" \
  "v_mov_b32 v165, 0\n\t" \
  "v_cvt_pk_bf16_f32 v24, v16, v17\n\t" \
  "v_mfma_f32_16x16x32_bf16 v[8:11], a[72:75], a[240:243], v[8:11]\n\t" \
  "v_mov_b32 v168, 0\n\t" \
  "v_mov_b32 v169, 0\n\t" \
  "v_mfma_f32_16x16x32_bf16 v[4:7], a[132:135], a[224:227], v[4:7]\n\t" \
  "v_cvt_pk_bf16_f32 v25, v18, v19\n\t" \
  "v_cvt_pk_bf16_f32 v23, v14, v15\n\t" \
  "v_mfma_f32_16x16x32_bf16 v[4:7], a[68:71], a[240:243], v[4:7]\n\t" \
  "v_mov_b32 v172, 0\n\t" \
  "v_cvt_pk_bf16_f32 v22, v12, v13\n\t" \
  "v_mfma_f32_16x16x32_bf16 v[0:3], a[128:131], a[224:227], v[0:3]\n\t" \
  "s_waitcnt vmcnt(2)\n\t" \
  "v_cvt_pk_bf16_f32 v20, v8, v9\n\t" \
  "v_cvt_pk_bf16_f32 v21, v10, v11\n\t" \
  "v_mfma_f32_16x16x32_bf16 v[0:3], a[64:67], a[240:243], v[0:3]\n\t" \
  "s_barrier\n\t" \
  "v_mfma_f32_16x16x32_bf16 v[60:63], a[156:159], a[228:231], v[60:63]\n\t" \
  "v_cvt_pk_bf16_f32 v19, v6, v7\n\t" \
  "v_cvt_pk_bf16_f32 v18, v4, v5\n\t" \
  "v_mfma_f32_16x16x32_bf16 v[60:63], a[92:95], a[244:247], v[60:63]\n\t" \
  "v_mov_b32 v173, 0\n\t" \
  "v_permlane16_swap_b32 v28, v30\n\t" \
  "v_mfma_f32_16x16x32_bf16 v[56:59], a[152:155], a[228:231], v[56:59]\n\t" \
  "v_cvt_pk_bf16_f32 v16, v0, v1\n\t" \
  "v_cvt_pk_bf16_f32 v17, v2, v3\n\t" \
  "v_mfma_f32_16x16x32_bf16 v[56:59], a[88:91], a[244:247], v[56:59]\n\t" \
  "ds_read_b128 a[0:3], v220 offset:0\n\t" \
  "v_permlane16_swap_b32 v29, v31\n\t" \
  "v_mfma_f32_16x16x32_bf16 v[52:55], a[148:151], a[228:231], v[52:55]\n\t" \
  "v_cvt_pk_bf16_f32 v63, v62, v63\n\t" \
  "v_cvt_pk_bf16_f32 v62, v60, v61\n\t" \
  "v_mfma_f32_16x16x32_bf16 v[52:55], a[84:87], a[244:247], v[52:55]\n\t" \
  "ds_read_b128 a[4:7], v221 offset:0\n\t" \
  "v_permlane16_swap_b32 v24, v26\n\t" \
  "v_mfma_f32_16x16x32_bf16 v[48:51], a[144:147], a[228:231], v[48:51]\n\t" \
  "v_cvt_pk_bf16_f32 v60, v56, v57\n\t" \
  "v_permlane16_swap_b32 v25, v27\n\t" \
  "v_mfma_f32_16x16x32_bf16 v[48:51], a[80:83], a[244:247], v[48:51]\n\t" \
  "ds_read_b128 a[8:11], v222 offset:0\n\t" \
  "v_cvt_pk_bf16_f32 v61, v58, v59\n\t" \
  "v_mfma_f32_16x16x32_bf16 v[44:47], a[140:143], a[228:231], v[44:47]\n\t" \
  "v_cvt_pk_bf16_f32 v59, v54, v55\n\t" \
  "v_permlane16_swap_b32 v20, v22\n\t" \
  "v_mfma_f32_16x16x32_bf16 v[44:47], a[76:79], a[244:247], v[44:47]\n\t" \
  "ds_read_b128 a[12:15], v223 offset:0\n\t" \
  "v_cvt_pk_bf16_f32 v58, v52, v53\n\t" \
  "v_mfma_f32_16x16x32_bf16 v[40:43], a[136:139], a[228:231], v[40:43]\n\t" \
  "v_cvt_pk_bf16_f32 v56, v48, v49\n\t" \
  "v_cvt_pk_bf16_f32 v57, v50, v51\n\t" \
  "v_mfma_f32_16x16x32_bf16 v[40:43], a[72:75], a[244:247], v[40:43]\n\t" \
  "s_add_u32 s99, s53, 0\n\t" \
  "ds_read_b128 a[16:19], v220 offset:4096\n\t" \
  "v_permlane16_swap_b32 v21, v23\n\t" \
  "v_cvt_pk_bf16_f32 v55, v46, v47\n\t" \
  "v_mfma_f32_16x16x32_bf16 v[36:39], a[132:135], a[228:231], v[36:39]\n\t" \
  "ds_read_b128 a[20:23], v221 offset:4096\n\t" \
  "v_cvt_pk_bf16_f32 v54, v44, v45\n\t" \
  "v_mfma_f32_16x16x32_bf16 v[36:39], a[68:71], a[244:247], v[36:39]\n\t" \
  "v_cvt_pk_bf16_f32 v52, v40, v41\n\t" \
  "v_permlane16_swap_b32 v16, v18\n\t" \
  "v_mfma_f32_16x16x32_bf16 v[32:35], a[128:131], a[228:231], v[32:35]\n\t" \
  "buffer_load_dword v246, v245, s[72:75], s99 offen\n\t" \
  "v_mfma_f32_16x16x32_bf16 v[32:35], a[64:67], a[244:247], v[32:35]\n\t" \
  "v_cvt_pk_bf16_f32 v53, v42, v43\n\t" \
  "v_permlane16_swap_b32 v17, v19\n\t" \
  "v_mfma_f32_16x16x32_bf16 v[92:95], a[156:159], a[232:235], v[92:95]\n\t" \
  "ds_read_b128 a[24:27], v222 offset:4096\n\t" \
  "v_cvt_pk_bf16_f32 v51, v38, v39\n\t" \
  "v_mfma_f32_16x16x32_bf16 v[92:95], a[92:95], a[248:251], v[92:95]\n\t" \
  "ds_read_b128 a[28:31], v223 offset:4096\n\t" \
  "v_permlane16_swap_b32 v60, v62\n\t" \
  "v_mfma_f32_16x16x32_bf16 v[88:91], a[152:155], a[232:235], v[88:91]\n\t" \
  "v_cvt_pk_bf16_f32 v50, v36, v37\n\t" \
  "v_cvt_pk_bf16_f32 v48, v32, v33\n\t" \
  "v_mfma_f32_16x16x32_bf16 v[88:91], a[88:91], a[248:251], v[88:91]\n\t" \
  "ds_read_b128 a[32:35], v220 offset:8192\n\t" \
  "v_cvt_pk_bf16_f32 v49, v34, v35\n\t" \
  "v_mfma_f32_16x16x32_bf16 v[84:87], a[148:151], a[232:235], v[84:87]\n\t" \
  "v_cvt_pk_bf16_f32 v95, v94, v95\n\t" \
  "v_cvt_pk_bf16_f32 v94, v92, v93\n\t" \
  "v_mfma_f32_16x16x32_bf16 v[84:87], a[84:87], a[248:251], v[84:87]\n\t" \
  "ds_read_b128 a[36:39], v221 offset:8192\n\t" \
  "v_permlane16_swap_b32 v61, v63\n\t" \
  "v_mfma_f32_16x16x32_bf16 v[80:83], a[144:147], a[232:235], v[80:83]\n\t" \
  "ds_read_b128 a[40:43], v222 offset:8192\n\t" \
  "v_permlane16_swap_b32 v56, v58\n\t" \
  "v_cvt_pk_bf16_f32 v92, v88, v89\n\t" \
  "v_cvt_pk_bf16_f32 v93, v90, v91\n\t" \
  "v_mfma_f32_16x16x32_bf16 v[80:83], a[80:83], a[248:251], v[80:83]\n\t" \
  "v_permlane16_swap_b32 v57, v59\n\t" \
  "v_cvt_pk_bf16_f32 v91, v86, v87\n\t" \
  "v_cvt_pk_bf16_f32 v90, v84, v85\n\t" \
  "v_mfma_f32_16x16x32_bf16 v[76:79], a[140:143], a[232:235], v[76:79]\n\t" \
  "buffer_load_dword v246, v247, s[16:19], 0 offen sc1\n\t" \
  "v_mfma_f32_16x16x32_bf16 v[76:79], a[76:79], a[248:251], v[76:79]\n\t" \
  "v_permlane16_swap_b32 v52, v54\n\t" \
  "v_mfma_f32_16x16x32_bf16 v[72:75], a[136:139], a[232:235], v[72:75]\n\t" \
  "ds_read_b128 a[44:47], v223 offset:8192\n\t" \
  "v_cvt_pk_bf16_f32 v88, v80, v81\n\t" \
  "v_mfma_f32_16x16x32_bf16 v[72:75], a[72:75], a[248:251], v[72:75]\n\t" \
  "ds_read_b128 a[48:51], v220 offset:12288\n\t" \
  "v_cvt_pk_bf16_f32 v89, v82, v83\n\t" \
  "v_mfma_f32_16x16x32_bf16 v[68:71], a[132:135], a[232:235], v[68:71]\n\t" \
  "ds_read_b128 a[52:55], v221 offset:12288\n\t" \
  "v_cvt_pk_bf16_f32 v87, v78, v79\n\t" \
  "v_mfma_f32_16x16x32_bf16 v[68:71], a[68:71], a[248:251], v[68:71]\n\t" \
  "v_cvt_pk_bf16_f32 v86, v76, v77\n\t" \
  "v_permlane16_swap_b32 v53, v55\n\t" \
  "v_mfma_f32_16x16x32_bf16 v[64:67], a[128:131], a[232:235], v[64:67]\n\t" \
  "v_cvt_pk_bf16_f32 v84, v72, v73\n\t" \
  "v_permlane16_swap_b32 v48, v50\n\t" \
  "v_mfma_f32_16x16x32_bf16 v[64:67], a[64:67], a[248:251], v[64:67]\n\t" \
  "ds_read_b128 a[56:59], v222 offset:12288\n\t" \
  "v_cvt_pk_bf16_f32 v85, v74, v75\n\t" \
  "v_mfma_f32_16x16x32_bf16 v[124:127], a[156:159], a[236:239], v[124:127]\n\t" \
  "ds_read_b128 a[60:63], v223 offset:12288\n\t" \
  "v_cvt_pk_bf16_f32 v83, v70, v71\n\t" \
  "v_mfma_f32_16x16x32_bf16 v[124:127], a[92:95], a[252:255], v[124:127]\n\t" \
  "v_cvt_pk_bf16_f32 v82, v68, v69\n\t" \
  "v_permlane16_swap_b32 v49, v51\n\t" \
  "v_mfma_f32_16x16x32_bf16 v[120:123], a[152:155], a[236:239], v[120:123]\n\t" \
  "v_cvt_pk_bf16_f32 v80, v64, v65\n\t" \
  "v_permlane16_swap_b32 v92, v94\n\t" \
  "v_mfma_f32_16x16x32_bf16 v[120:123], a[88:91], a[252:255], v[120:123]\n\t" \
  "v_permlane16_swap_b32 v93, v95\n\t" \
  "v_mfma_f32_16x16x32_bf16 v[116:119], a[148:151], a[236:239], v[116:119]\n\t" \
  "v_cvt_pk_bf16_f32 v81, v66, v67\n\t" \
  "v_cvt_pk_bf16_f32 v127, v126, v127\n\t" \
  "v_mfma_f32_16x16x32_bf16 v[116:119], a[84:87], a[252:255], v[116:119]\n\t" \
  "v_permlane16_swap_b32 v88, v90\n\t" \
  "v_mfma_f32_16x16x32_bf16 v[112:115], a[144:147], a[236:239], v[112:115]\n\t" \
  "v_cvt_pk_bf16_f32 v126, v124, v125\n\t" \
  "v_cvt_pk_bf16_f32 v124, v120, v121\n\t" \
  "v_mfma_f32_16x16x32_bf16 v[112:115], a[80:83], a[252:255], v[112:115]\n\t" \
  "v_cvt_pk_bf16_f32 v125, v122, v123\n\t" \
  "v_permlane16_swap_b32 v89, v91\n\t" \
  "v_mfma_f32_16x16x32_bf16 v[108:111], a[140:143], a[236:239], v[108:111]\n\t" \
  "v_cvt_pk_bf16_f32 v123, v118, v119\n\t" \
  "v_cvt_pk_bf16_f32 v122, v116, v117\n\t" \
  "v_mfma_f32_16x16x32_bf16 v[108:111], a[76:79], a[252:255], v[108:111]\n\t" \
  "v_permlane16_swap_b32 v84, v86\n\t" \
  "v_mfma_f32_16x16x32_bf16 v[104:107], a[136:139], a[236:239], v[104:107]\n\t" \
  "v_permlane16_swap_b32 v85, v87\n\t" \
  "v_mfma_f32_16x16x32_bf16 v[104:107], a[72:75], a[252:255], v[104:107]\n\t" \
  "v_cvt_pk_bf16_f32 v120, v112, v113\n\t" \
  "v_cvt_pk_bf16_f32 v121, v114, v115\n\t" \
  "v_mfma_f32_16x16x32_bf16 v[100:103], a[132:135], a[236:239], v[100:103]\n\t" \
  "s_add_u32 m0, s38, 49152\n\t" \
  "v_permlane16_swap_b32 v80, v82\n\t" \
  "v_mfma_f32_16x16x32_bf16 v[100:103], a[68:71], a[252:255], v[100:103]\n\t" \
  "s_add_u32 s96, s38, 32768\n\t" \
  "v_cvt_pk_bf16_f32 v119, v110, v111\n\t" \
  "v_cvt_pk_bf16_f32 v118, v108, v109\n\t" \
  "v_mfma_f32_16x16x32_bf16 v[96:99], a[128:131], a[236:239], v[96:99]\n\t" \
  "s_waitcnt lgkmcnt(0)\n\t" \
  "v_permlane16_swap_b32 v81, v83\n\t" \
  "v_mfma_f32_16x16x32_bf16 v[96:99], a[64:67], a[252:255], v[96:99]\n\t" \
  "s_barrier\n\t" \
  STS("s_memrealtime s[94:95]\n\t") \
  STS("s_memtime s[92:93]\n\t") \
  "v_mfma_f32_16x16x32_bf16 v[0:3], a[0:3], v[16:19], 0\n\t" \
  "buffer_load_dwordx4 v224, s[60:63], s96 offen offset:0 lds\n\t" \
  "v_mfma_f32_16x16x32_bf16 v[0:3], a[4:7], v[20:23], v[0:3]\n\t" \
  "v_permlane16_swap_b32 v124, v126\n\t" \
  "v_mfma_f32_16x16x32_bf16 v[0:3], a[8:11], v[24:27], v[0:3]\n\t" \
  "ds_read_b128 a[192:195], v220 offset:16384\n\t" \
  "v_cvt_pk_bf16_f32 v116, v104, v105\n\t" \
  "v_mfma_f32_16x16x32_bf16 v[0:3], a[12:15], v[28:31], v[0:3]\n\t" \
  "v_permlane16_swap_b32 v125, v127\n\t" \
  "v_mfma_f32_16x16x32_bf16 v[32:35], a[0:3], v[48:51], 0\n\t" \
  "ds_read_b128 a[196:199], v221 offset:16384\n\t" \
  "v_cvt_pk_bf16_f32 v117, v106, v107\n\t" \
  "v_mfma_f32_16x16x32_bf16 v[32:35], a[4:7], v[52:55], v[32:35]\n\t" \
  "v_cvt_pk_bf16_f32 v115, v102, v103\n\t" \
  "v_cvt_pk_bf16_f32 v114, v100, v101\n\t" \
  "s_nop 0\n\t" \
  "v_max3_f32 v194, v0, v1, v2\n\t" \
  "v_max_f32 v194, v194, v3\n\t" \
  "v_mfma_f32_16x16x32_bf16 v[32:35], a[8:11], v[56:59], v[32:35]\n\t" \
  "v_mov_b32 v195, v194\n\t" \
  "v_cvt_pk_bf16_f32 v112, v96, v97\n\t" \
  "s_nop 0\n\t" \
  "v_permlane32_swap_b32 v194, v195\n\t" \
  "v_mfma_f32_16x16x32_bf16 v[32:35], a[12:15], v[60:63], v[32:35]\n\t" \
  "v_permlane16_swap_b32 v120, v122\n\t" \
  "v_max_f32 v194, v194, v195\n\t" \
  "v_mov_b32 v195, v194\n\t" \
  "s_nop 1\n\t" \
  "v_permlane16_swap_b32 v194, v195\n\t" \
  "v_max_f32 v194, v194, v195\n\t" \
  "v_sub_f32 v176, 0xc2000000, v194\n\t" \
  "v_sub_f32 v177, 0xc2000000, v194\n\t" \
  "v_sub_f32 v178, 0xc2000000, v194\n\t" \
  "v_sub_f32 v179, 0xc2000000, v194\n\t" \
  "s_nop 1\n\t" \
  "v_mfma_f32_16x16x32_bf16 v[4:7], a[16:19], v[16:19], v[176:179]\n\t" \
  "v_add_f32 v0, v0, v176\n\t" \
  "v_add_f32 v1, v1, v176\n\t" \
  "v_add_f32 v2, v2, v176\n\t" \
  "v_add_f32 v3, v3, v176\n\t" \
  "v_mfma_f32_16x16x32_bf16 v[4:7], a[20:23], v[20:23], v[4:7]\n\t" \
  "v_max3_f32 v194, v32, v33, v34\n\t" \
  "v_max_f32 v194, v194, v35\n\t" \
  "v_mov_b32 v195, v194\n\t" \
  "v_exp_f32 v0, v0\n\t" \
  "v_mfma_f32_16x16x32_bf16 v[4:7], a[24:27], v[24:27], v[4:7]\n\t" \
  "buffer_load_dwordx4 v225, s[60:63], s96 offen offset:1024 lds\n\t" \
  "v_permlane32_swap_b32 v194, v195\n\t" \
  "v_max_f32 v194, v194, v195\n\t" \
  "v_mfma_f32_16x16x32_bf16 v[4:7], a[28:31], v[28:31], v[4:7]\n\t" \
  "v_mov_b32 v195, v194\n\t" \
  "v_exp_f32 v1, v1\n\t" \
  "v_exp_f32 v2, v2\n\t" \
  "v_mfma_f32_16x16x32_bf16 v[8:11], a[32:35], v[16:19], v[176:179]\n\t" \
  STS("v_writelane_b32 v255, s94, 18\n\t") \
  STS("v_writelane_b32 v255, s92, 19\n\t") \
  "v_permlane16_swap_b32 v194, v195\n\t" \
  "v_max_f32 v194, v194, v195\n\t" \
  "v_sub_f32 v180, 0xc2000000, v194\n\t" \
  "v_sub_f32 v181, 0xc2000000, v194\n\t" \
  "v_mfma_f32_16x16x32_bf16 v[8:11], a[36:39], v[20:23], v[8:11]\n\t" \
  "v_sub_f32 v182, 0xc2000000, v194\n\t" \
  "v_sub_f32 v183, 0xc2000000, v194\n\t" \
  "v_add_f32 v32, v32, v180\n\t" \
  "v_exp_f32 v3, v3\n\t" \
  "v_mfma_f32_16x16x32_bf16 v[8:11], a[40:43], v[24:27], v[8:11]\n\t" \
  "s_add_u32 s99, s53, 14336\n\t" \
  "v_add_f32 v33, v33, v180\n\t" \
  "v_exp_f32 v4, v4\n\t" \
  "v_exp_f32 v5, v5\n\t" \
  "v_mfma_f32_16x16x32_bf16 v[8:11], a[44:47], v[28:31], v[8:11]\n\t" \
  "v_exp_f32 v6, v6\n\t" \
  "v_exp_f32 v7, v7\n\t" \
  "v_add_f32 v34, v34, v180\n\t" \
  "v_mfma_f32_16x16x32_bf16 v[12:15], a[48:51], v[16:19], v[176:179]\n\t" \
  "buffer_load_dword v246, v245, s[72:75], s99 offen\n\t" \
  "v_mfma_f32_16x16x32_bf16 v[12:15], a[52:55], v[20:23], v[12:15]\n\t" \
  "ds_read_b128 a[200:203], v222 offset:16384\n\t" \
  "v_cvt_pk_bf16_f32 v113, v98, v99\n\t" \
  "v_add_f32 v35, v35, v180\n\t" \
  "v_exp_f32 v8, v8\n\t" \
  "v_mfma_f32_16x16x32_bf16 v[12:15], a[56:59], v[24:27], v[12:15]\n\t" \
  "ds_read_b128 a[204:207], v223 offset:16384\n\t" \
  "v_exp_f32 v9, v9\n\t" \
  "v_exp_f32 v10, v10\n\t" \
  "v_mfma_f32_16x16x32_bf16 v[12:15], a[60:63], v[28:31], v[12:15]\n\t" \
  "buffer_load_dwordx4 v226, s[60:63], s96 offen offset:2048 lds\n\t" \
  "v_exp_f32 v11, v11\n\t" \
  "v_mfma_f32_16x16x32_bf16 v[64:67], a[0:3], v[80:83], 0\n\t" \
  "ds_read_b128 a[208:211], v220 offset:20480\n\t" \
  "v_permlane16_swap_b32 v121, v123\n\t" \
  "v_mfma_f32_16x16x32_bf16 v[64:67], a[4:7], v[84:87], v[64:67]\n\t" \
  "ds_read_b128 a[212:215], v221 offset:20480\n\t" \
  "v_permlane16_swap_b32 v116, v118\n\t" \
  "v_exp_f32 v12, v12\n\t" \
  "v_mfma_f32_16x16x32_bf16 v[64:67], a[8:11], v[88:91], v[64:67]\n\t" \
  "ds_read_b128 a[216:219], v222 offset:20480\n\t" \
  "v_exp_f32 v13, v13\n\t" \
  "v_exp_f32 v14, v14\n\t" \
  "v_mfma_f32_16x16x32_bf16 v[64:67], a[12:15], v[92:95], v[64:67]\n\t" \
  "ds_read_b128 a[220:223], v223 offset:20480\n\t" \
  "v_exp_f32 v15, v15\n\t" \
  "v_exp_f32 v32, v32\n\t" \
  "v_mfma_f32_16x16x32_bf16 v[36:39], a[16:19], v[48:51], v[180:183]\n\t" \
  "v_exp_f32 v33, v33\n\t" \
  "v_exp_f32 v34, v34\n\t" \
  "v_mfma_f32_16x16x32_bf16 v[36:39], a[20:23], v[52:55], v[36:39]\n\t" \
  "s_nop 0\n\t" \
  "v_max3_f32 v194, v64, v65, v66\n\t" \
  "v_max_f32 v194, v194, v67\n\t" \
  "v_mov_b32 v195, v194\n\t" \
  "v_exp_f32 v35, v35\n\t" \
  "v_mfma_f32_16x16x32_bf16 v[36:39], a[24:27], v[56:59], v[36:39]\n\t" \
  "ds_read_b128 a[224:227], v220 offset:24576\n\t" \
  "v_permlane32_swap_b32 v194, v195\n\t" \
  "v_max_f32 v194, v194, v195\n\t" \
  "v_mov_b32 v195, v194\n\t" \
  "v_mfma_f32_16x16x32_bf16 v[36:39], a[28:31], v[60:63], v[36:39]\n\t" \
  "s_nop 0\n\t" \
  "v_permlane16_swap_b32 v194, v195\n\t" \
  "v_max_f32 v194, v194, v195\n\t" \
  "v_sub_f32 v184, 0xc2000000, v194\n\t" \
  "v_sub_f32 v185, 0xc2000000, v194\n\t" \
  "v_mfma_f32_16x16x32_bf16 v[40:43], a[32:35], v[48:51], v[180:183]\n\t" \
  "buffer_load_dwordx4 v227, s[60:63], s96 offen offset:3072 lds\n\t" \
  "v_sub_f32 v186, 0xc2000000, v194\n\t" \
  "v_sub_f32 v187, 0xc2000000, v194\n\t" \
  "v_mfma_f32_16x16x32_bf16 v[40:43], a[36:39], v[52:55], v[40:43]\n\t" \
  "v_add_f32 v64, v64, v184\n\t" \
  "v_permlane16_swap_b32 v117, v119\n\t" \
  "v_exp_f32 v36, v36\n\t" \
  "v_mfma_f32_16x16x32_bf16 v[40:43], a[40:43], v[56:59], v[40:43]\n\t" \
  "v_add_f32 v65, v65, v184\n\t" \
  "v_exp_f32 v37, v37\n\t" \
  "v_exp_f32 v38, v38\n\t" \
  "v_mfma_f32_16x16x32_bf16 v[40:43], a[44:47], v[60:63], v[40:43]\n\t" \
  "v_add_f32 v66, v66, v184\n\t" \
  "v_permlane16_swap_b32 v112, v114\n\t" \
  "v_exp_f32 v39, v39\n\t" \
  "v_mfma_f32_16x16x32_bf16 v[44:47], a[48:51], v[48:51], v[180:183]\n\t" \
  "s_add_u32 m0, s38, 147456\n\t" \
  "v_add_f32 v67, v67, v184\n\t" \
  "v_permlane16_swap_b32 v113, v115\n\t" \
  "v_mfma_f32_16x16x32_bf16 v[44:47], a[52:55], v[52:55], v[44:47]\n\t" \
  "s_add_u32 s96, s38, 16384\n\t" \
  "ds_read_b128 a[228:231], v221 offset:24576\n\t" \
  "v_add_u32 v249, 21504, v241\n\t" \
  "v_exp_f32 v40, v40\n\t" \
  "v_mfma_f32_16x16x32_bf16 v[44:47], a[56:59], v[56:59], v[44:47]\n\t" \
  "ds_read_b128 a[232:235], v222 offset:24576\n\t" \
  "v_exp_f32 v41, v41\n\t" \
  "v_mfma_f32_16x16x32_bf16 v[44:47], a[60:63], v[60:63], v[44:47]\n\t" \
  "v_add_u32 v241, 7168, v241\n\t" \
  "v_exp_f32 v42, v42\n\t" \
  "v_mfma_f32_16x16x32_bf16 v[96:99], a[0:3], v[112:115], 0\n\t" \
  "buffer_load_dwordx4 v230, s[64:67], s96 offen offset:0 lds\n\t" \
  "v_mfma_f32_16x16x32_bf16 v[96:99], a[4:7], v[116:119], v[96:99]\n\t" \
  "s_mov_b32 s89, 0\n\t" \
  "ds_read_b128 a[236:239], v223 offset:24576\n\t" \
  "v_exp_f32 v43, v43\n\t" \
  "v_mfma_f32_16x16x32_bf16 v[96:99], a[8:11], v[120:123], v[96:99]\n\t" \
  "v_cvt_pk_bf16_f32 v128, v0, v1\n\t" \
  "v_exp_f32 v44, v44\n\t" \
  "v_mfma_f32_16x16x32_bf16 v[96:99], a[12:15], v[124:127], v[96:99]\n\t" \
  "ds_read_b128 a[240:243], v220 offset:28672\n\t" \
  "v_exp_f32 v45, v45\n\t" \
  "v_mfma_f32_16x16x32_bf16 v[68:71], a[16:19], v[80:83], v[184:187]\n\t" \
  "v_exp_f32 v46, v46\n\t" \
  "v_mfma_f32_16x16x32_bf16 v[68:71], a[20:23], v[84:87], v[68:71]\n\t" \
  "v_cvt_pk_bf16_f32 v129, v2, v3\n\t" \
  "v_lshlrev_b32 v212, 7, v233\n\t" \
  "s_nop 0\n\t" \
  "v_max3_f32 v194, v96, v97, v98\n\t" \
  "v_max_f32 v194, v194, v99\n\t" \
  "v_mfma_f32_16x16x32_bf16 v[68:71], a[24:27], v[88:91], v[68:71]\n\t" \
  "v_mov_b32 v195, v194\n\t" \
  "v_cmp_gt_u32 vcc, 16, v233\n\t" \
  "s_nop 0\n\t" \
  "v_permlane32_swap_b32 v194, v195\n\t" \
  "v_mfma_f32_16x16x32_bf16 v[68:71], a[28:31], v[92:95], v[68:71]\n\t" \
  "v_exp_f32 v47, v47\n\t" \
  "v_max_f32 v194, v194, v195\n\t" \
  "v_mov_b32 v195, v194\n\t" \
  "v_mfma_f32_16x16x32_bf16 v[72:75], a[32:35], v[80:83], v[184:187]\n\t" \
  "ds_read_b128 a[244:247], v221 offset:28672\n\t" \
  "v_permlane16_swap_b32 v194, v195\n\t" \
  "v_mfma_f32_16x16x32_bf16 v[72:75], a[36:39], v[84:87], v[72:75]\n\t" \
  "v_max_f32 v194, v194, v195\n\t" \
  "v_sub_f32 v188, 0xc2000000, v194\n\t" \
  "v_mfma_f32_16x16x32_bf16 v[72:75], a[40:43], v[88:91], v[72:75]\n\t" \
  "v_sub_f32 v189, 0xc2000000, v194\n\t" \
  "v_sub_f32 v190, 0xc2000000, v194\n\t" \
  "v_mfma_f32_16x16x32_bf16 v[72:75], a[44:47], v[92:95], v[72:75]\n\t" \
  "buffer_load_dwordx4 v231, s[64:67], s96 offen offset:1024 lds\n\t" \
  "v_mfma_f32_16x16x32_bf16 v[76:79], a[48:51], v[80:83], v[184:187]\n\t" \
  "v_sub_f32 v191, 0xc2000000, v194\n\t" \
  "v_add_f32 v96, v96, v188\n\t" \
  "v_mfma_f32_16x16x32_bf16 v[76:79], a[52:55], v[84:87], v[76:79]\n\t" \
  "v_add_f32 v97, v97, v188\n\t" \
  "v_exp_f32 v64, v64\n\t" \
  "v_mfma_f32_16x16x32_bf16 v[76:79], a[56:59], v[88:91], v[76:79]\n\t" \
  "ds_read_b128 a[248:251], v222 offset:28672\n\t" \
  "v_exp_f32 v65, v65\n\t" \
  "v_mfma_f32_16x16x32_bf16 v[76:79], a[60:63], v[92:95], v[76:79]\n\t" \
  "v_add_f32 v98, v98, v188\n\t" \
  "v_add_f32 v99, v99, v188\n\t" \
  "v_mfma_f32_16x16x32_bf16 v[100:103], a[16:19], v[112:115], v[188:191]\n\t" \
  "ds_read_b128 a[252:255], v223 offset:28672\n\t" \
  "v_bfrev_b32 v214, 1\n\t" \
  "v_mfma_f32_16x16x32_bf16 v[100:103], a[20:23], v[116:119], v[100:103]\n\t" \
  "v_cvt_pk_bf16_f32 v130, v4, v5\n\t" \
  "v_exp_f32 v66, v66\n\t" \
  "v_mfma_f32_16x16x32_bf16 v[100:103], a[24:27], v[120:123], v[100:103]\n\t" \
  "buffer_load_dwordx4 v230, s[64:67], s96 offen offset:2048 lds\n\t" \
  "v_mfma_f32_16x16x32_bf16 v[100:103], a[28:31], v[124:127], v[100:103]\n\t" \
  "v_cndmask_b32 v212, v214, v212, vcc\n\t" \
  "v_exp_f32 v67, v67\n\t" \
  "v_mfma_f32_16x16x32_bf16 v[104:107], a[32:35], v[112:115], v[188:191]\n\t" \
  "v_exp_f32 v68, v68\n\t" \
  "v_mfma_f32_16x16x32_bf16 v[104:107], a[36:39], v[116:119], v[104:107]\n\t" \
  "v_cvt_pk_bf16_f32 v131, v6, v7\n\t" \
  "v_cvt_pk_bf16_f32 v132, v8, v9\n\t" \
  "v_mfma_f32_16x16x32_bf16 v[104:107], a[40:43], v[120:123], v[104:107]\n\t" \
  "v_exp_f32 v69, v69\n\t" \
  "v_mfma_f32_16x16x32_bf16 v[104:107], a[44:47], v[124:127], v[104:107]\n\t" \
  "v_cvt_pk_bf16_f32 v133, v10, v11\n\t" \
  "v_cvt_pk_bf16_f32 v134, v12, v13\n\t" \
  "v_mfma_f32_16x16x32_bf16 v[108:111], a[48:51], v[112:115], v[188:191]\n\t" \
  "v_exp_f32 v70, v70\n\t" \
  "v_mfma_f32_16x16x32_bf16 v[108:111], a[52:55], v[116:119], v[108:111]\n\t" \
  "v_exp_f32 v71, v71\n\t" \
  "v_mfma_f32_16x16x32_bf16 v[108:111], a[56:59], v[120:123], v[108:111]\n\t" \
  "v_cvt_pk_bf16_f32 v135, v14, v15\n\t" \
  "v_cvt_pk_bf16_f32 v136, v32, v33\n\t" \
  "v_mfma_f32_16x16x32_bf16 v[108:111], a[60:63], v[124:127], v[108:111]\n\t" \
  "s_waitcnt lgkmcnt(0)\n\t" \
  "buffer_load_dwordx4 v231, s[64:67], s96 offen offset:3072 lds\n\t" \
  STA("s_memrealtime s[94:95]\n\t") \
  STA("s_memtime s[92:93]\n\t") \
  "v_mfma_f32_16x16x32_bf16 v[0:3], a[192:195], v[16:19], v[176:179]\n\t" \
  "s_add_u32 s99, s53, 28672\n\t" \
  "ds_read_b64_tr_b16 a[128:129], v228 offset:0\n\t" \
  "v_cvt_pk_bf16_f32 v137, v34, v35\n\t" \
  "v_mfma_f32_16x16x32_bf16 v[0:3], a[196:199], v[20:23], v[0:3]\n\t" \
  "v_exp_f32 v72, v72\n\t" \
  "v_mfma_f32_16x16x32_bf16 v[0:3], a[200:203], v[24:27], v[0:3]\n\t" \
  "buffer_load_dword v246, v245, s[72:75], s99 offen\n\t" \
  "v_mfma_f32_16x16x32_bf16 v[0:3], a[204:207], v[28:31], v[0:3]\n\t" \
  "ds_read_b64_tr_b16 a[130:131], v228 offset:4096\n\t" \
  "v_cvt_pk_bf16_f32 v138, v36, v37\n\t" \
  "v_mfma_f32_16x16x32_bf16 v[4:7], a[220:223], v[28:31], v[176:179]\n\t" \
  "ds_read_b64_tr_b16 a[132:133], v228 offset:8192\n\t" \
  "v_cvt_pk_bf16_f32 v139, v38, v39\n\t" \
  "v_mfma_f32_16x16x32_bf16 v[4:7], a[216:219], v[24:27], v[4:7]\n\t" \
  "v_exp_f32 v73, v73\n\t" \
  "v_mfma_f32_16x16x32_bf16 v[4:7], a[212:215], v[20:23], v[4:7]\n\t" \
  "ds_read_b64_tr_b16 a[134:135], v228 offset:12288\n\t" \
  "v_cvt_pk_bf16_f32 v140, v40, v41\n\t" \
  "v_mfma_f32_16x16x32_bf16 v[4:7], a[208:211], v[16:19], v[4:7]\n\t" \
  "ds_read_b64_tr_b16 a[136:137], v229 offset:0\n\t" \
  "v_cvt_pk_bf16_f32 v141, v42, v43\n\t" \
  "v_mfma_f32_16x16x32_bf16 v[8:11], a[224:227], v[16:19], v[176:179]\n\t" \
  "v_exp_f32 v74, v74\n\t" \
  "v_mfma_f32_16x16x32_bf16 v[8:11], a[228:231], v[20:23], v[8:11]\n\t" \
  "ds_read_b64_tr_b16 a[138:139], v229 offset:4096\n\t" \
  "v_cvt_pk_bf16_f32 v142, v44, v45\n\t" \
  "v_mfma_f32_16x16x32_bf16 v[8:11], a[232:235], v[24:27], v[8:11]\n\t" \
  "v_exp_f32 v75, v75\n\t" \
  "v_mfma_f32_16x16x32_bf16 v[8:11], a[236:239], v[28:31], v[8:11]\n\t" \
  "ds_read_b64_tr_b16 a[140:141], v229 offset:8192\n\t" \
  "v_lshl_add_u32 v214, v233, 14, s41\n\t" \
  "v_mfma_f32_16x16x32_bf16 v[12:15], a[252:255], v[28:31], v[176:179]\n\t" \
  STA("v_writelane_b32 v255, s94, 26\n\t") \
  STA("v_writelane_b32 v255, s92, 27\n\t") \
  "v_exp_f32 v76, v76\n\t" \
  "v_mfma_f32_16x16x32_bf16 v[12:15], a[248:251], v[24:27], v[12:15]\n\t" \
  "ds_read_b64_tr_b16 a[142:143], v229 offset:12288\n\t" \
  "v_min_u32 v214, .Lfz_z-.Lat_t0-4, v214\n\t" \
  "v_mfma_f32_16x16x32_bf16 v[12:15], a[244:247], v[20:23], v[12:15]\n\t" \
  "s_getpc_b64 s[84:85]\n\t" \
  ".Lat_t0:\n\t" \
  "ds_read_b64_tr_b16 a[144:145], v228 offset:256\n\t" \
  "v_cvt_pk_bf16_f32 v143, v46, v47\n\t" \
  "v_mfma_f32_16x16x32_bf16 v[12:15], a[240:243], v[16:19], v[12:15]\n\t" \
  "global_load_dword v246, v214, s[84:85]\n\t" \
  "v_mfma_f32_16x16x32_bf16 v[32:35], a[192:195], v[48:51], v[180:183]\n\t" \
  "v_exp_f32 v77, v77\n\t" \
  "v_mfma_f32_16x16x32_bf16 v[32:35], a[196:199], v[52:55], v[32:35]\n\t" \
  "ds_read_b64_tr_b16 a[146:147], v228 offset:4352\n\t" \
  "v_cvt_pk_bf16_f32 v144, v64, v65\n\t" \
  "v_mfma_f32_16x16x32_bf16 v[32:35], a[200:203], v[56:59], v[32:35]\n\t" \
  "ds_read_b64_tr_b16 a[148:149], v228 offset:8448\n\t" \
  "v_exp_f32 v78, v78\n\t" \
  "v_mfma_f32_16x16x32_bf16 v[32:35], a[204:207], v[60:63], v[32:35]\n\t" \
  "v_exp_f32 v79, v79\n\t" \
  "v_mfma_f32_16x16x32_bf16 v[36:39], a[220:223], v[60:63], v[180:183]\n\t" \
  "ds_read_b64_tr_b16 a[150:151], v228 offset:12544\n\t" \
  "v_cvt_pk_bf16_f32 v145, v66, v67\n\t" \
  "v_mfma_f32_16x16x32_bf16 v[36:39], a[216:219], v[56:59], v[36:39]\n\t" \
  "v_exp_f32 v96, v96\n\t" \
  "v_mfma_f32_16x16x32_bf16 v[36:39], a[212:215], v[52:55], v[36:39]\n\t" \
  "ds_read_b64_tr_b16 a[152:153], v229 offset:256\n\t" \
  "v_cvt_pk_bf16_f32 v146, v68, v69\n\t" \
  "v_mfma_f32_16x16x32_bf16 v[36:39], a[208:211], v[48:51], v[36:39]\n\t" \
  "ds_read_b64_tr_b16 a[154:155], v229 offset:4352\n\t" \
  "v_exp_f32 v97, v97\n\t" \
  "v_mfma_f32_16x16x32_bf16 v[40:43], a[224:227], v[48:51], v[180:183]\n\t" \
  "v_exp_f32 v98, v98\n\t" \
  "v_mfma_f32_16x16x32_bf16 v[40:43], a[228:231], v[52:55], v[40:43]\n\t" \
  "ds_read_b64_tr_b16 a[156:157], v229 offset:8448\n\t" \
  "v_cvt_pk_bf16_f32 v147, v70, v71\n\t" \
  "v_mfma_f32_16x16x32_bf16 v[40:43], a[232:235], v[56:59], v[40:43]\n\t" \
  "v_exp_f32 v99, v99\n\t" \
  "v_mfma_f32_16x16x32_bf16 v[40:43], a[236:239], v[60:63], v[40:43]\n\t" \
  "ds_read_b64_tr_b16 a[158:159], v229 offset:12544\n\t" \
  "v_cvt_pk_bf16_f32 v148, v72, v73\n\t" \
  "v_mfma_f32_16x16x32_bf16 v[44:47], a[252:255], v[60:63], v[180:183]\n\t" \
  "ds_read_b64_tr_b16 a[160:161], v228 offset:512\n\t" \
  "v_cvt_pk_bf16_f32 v149, v74, v75\n\t" \
  "v_mfma_f32_16x16x32_bf16 v[44:47], a[248:251], v[56:59], v[44:47]\n\t" \
  "v_exp_f32 v100, v100\n\t" \
  "v_mfma_f32_16x16x32_bf16 v[44:47], a[244:247], v[52:55], v[44:47]\n\t" \
  "v_exp_f32 v101, v101\n\t" \
  "v_mfma_f32_16x16x32_bf16 v[44:47], a[240:243], v[48:51], v[44:47]\n\t" \
  "ds_read_b64_tr_b16 a[162:163], v228 offset:4608\n\t" \
  "v_cvt_pk_bf16_f32 v150, v76, v77\n\t" \
  "v_mfma_f32_16x16x32_bf16 v[64:67], a[192:195], v[80:83], v[184:187]\n\t" \
  "ds_read_b64_tr_b16 a[164:165], v228 offset:8704\n\t" \
  "v_cvt_pk_bf16_f32 v151, v78, v79\n\t" \
  "v_mfma_f32_16x16x32_bf16 v[64:67], a[196:199], v[84:87], v[64:67]\n\t" \
  ".Lhs6:\n\ts_cbranch_execz .Lhs6n\n\t" \
  "v_exp_f32 v102, v102\n\t" \
  "v_mfma_f32_16x16x32_bf16 v[64:67], a[200:203], v[88:91], v[64:67]\n\t" \
  "ds_read_b64_tr_b16 a[166:167], v228 offset:12800\n\t" \
  "v_cvt_pk_bf16_f32 v152, v96, v97\n\t" \
  "v_mfma_f32_16x16x32_bf16 v[64:67], a[204:207], v[92:95], v[64:67]\n\t" \
  "ds_read_b64_tr_b16 a[168:169], v229 offset:512\n\t" \
  "v_exp_f32 v103, v103\n\t" \
  "v_mfma_f32_16x16x32_bf16 v[68:71], a[220:223], v[92:95], v[184:187]\n\t" \
  "v_exp_f32 v104, v104\n\t" \
  "v_mfma_f32_16x16x32_bf16 v[68:71], a[216:219], v[88:91], v[68:71]\n\t" \
  "ds_read_b64_tr_b16 a[170:171], v229 offset:4608\n\t" \
  "v_cvt_pk_bf16_f32 v153, v98, v99\n\t" \
  "v_mfma_f32_16x16x32_bf16 v[68:71], a[212:215], v[84:87], v[68:71]\n\t" \
  "v_exp_f32 v105, v105\n\t" \
  "v_mfma_f32_16x16x32_bf16 v[68:71], a[208:211], v[80:83], v[68:71]\n\t" \
  "ds_read_b64_tr_b16 a[172:173], v229 offset:8704\n\t" \
  "v_cvt_pk_bf16_f32 v154, v100, v101\n\t" \
  "v_mfma_f32_16x16x32_bf16 v[72:75], a[224:227], v[80:83], v[184:187]\n\t" \
  "v_exp_f32 v106, v106\n\t" \
  "v_mfma_f32_16x16x32_bf16 v[72:75], a[228:231], v[84:87], v[72:75]\n\t" \
  "ds_read_b64_tr_b16 a[174:175], v229 offset:12800\n\t" \
  "v_cvt_pk_bf16_f32 v155, v102, v103\n\t" \
  "v_mfma_f32_16x16x32_bf16 v[72:75], a[232:235], v[88:91], v[72:75]\n\t" \
  "v_exp_f32 v107, v107\n\t" \
  "v_mfma_f32_16x16x32_bf16 v[72:75], a[236:239], v[92:95], v[72:75]\n\t" \
  "ds_read_b64_tr_b16 a[176:177], v228 offset:768\n\t" \
  "v_cvt_pk_bf16_f32 v156, v104, v105\n\t" \
  "v_mfma_f32_16x16x32_bf16 v[76:79], a[252:255], v[92:95], v[184:187]\n\t" \
  "v_exp_f32 v108, v108\n\t" \
  "v_mfma_f32_16x16x32_bf16 v[76:79], a[248:251], v[88:91], v[76:79]\n\t" \
  "ds_read_b64_tr_b16 a[178:179], v228 offset:4864\n\t" \
  "v_cvt_pk_bf16_f32 v157, v106, v107\n\t" \
  "v_mfma_f32_16x16x32_bf16 v[76:79], a[244:247], v[84:87], v[76:79]\n\t" \
  "v_exp_f32 v109, v109\n\t" \
  "v_mfma_f32_16x16x32_bf16 v[76:79], a[240:243], v[80:83], v[76:79]\n\t" \
  "ds_read_b64_tr_b16 a[180:181], v228 offset:8960\n\t" \
  "v_cvt_pk_bf16_f32 v158, v108, v109\n\t" \
  "v_mfma_f32_16x16x32_bf16 v[96:99], a[192:195], v[112:115], v[188:191]\n\t" \
  "v_exp_f32 v110, v110\n\t" \
  "v_mfma_f32_16x16x32_bf16 v[96:99], a[196:199], v[116:119], v[96:99]\n\t" \
  "s_add_u32 s99, s53, 43008\n\t" \
  "v_exp_f32 v111, v111\n\t" \
  "v_mfma_f32_16x16x32_bf16 v[96:99], a[200:203], v[120:123], v[96:99]\n\t" \
  "ds_read_b64_tr_b16 a[182:183], v228 offset:13056\n\t" \
  "v_cvt_pk_bf16_f32 v159, v110, v111\n\t" \
  "v_mfma_f32_16x16x32_bf16 v[96:99], a[204:207], v[124:127], v[96:99]\n\t" \
  "v_exp_f32 v0, v0\n\t" \
  "v_mfma_f32_16x16x32_bf16 v[100:103], a[220:223], v[124:127], v[188:191]\n\t" \
  "buffer_load_dword v246, v245, s[72:75], s99 offen\n\t" \
  "v_mfma_f32_16x16x32_bf16 v[100:103], a[216:219], v[120:123], v[100:103]\n\t" \
  "ds_read_b64_tr_b16 a[184:185], v229 offset:768\n\t" \
  "v_mfma_f32_16x16x32_bf16 v[100:103], a[212:215], v[116:119], v[100:103]\n\t" \
  "ds_read_b64_tr_b16 a[186:187], v229 offset:4864\n\t" \
  "v_mfma_f32_16x16x32_bf16 v[100:103], a[208:211], v[112:115], v[100:103]\n\t" \
  "v_exp_f32 v1, v1\n\t" \
  "v_mfma_f32_16x16x32_bf16 v[104:107], a[224:227], v[112:115], v[188:191]\n\t" \
  "ds_read_b64_tr_b16 a[188:189], v229 offset:8960\n\t" \
  "v_mfma_f32_16x16x32_bf16 v[104:107], a[228:231], v[116:119], v[104:107]\n\t" \
  "ds_read_b64_tr_b16 a[190:191], v229 offset:13056\n\t" \
  "v_mfma_f32_16x16x32_bf16 v[104:107], a[232:235], v[120:123], v[104:107]\n\t" \
  "v_exp_f32 v2, v2\n\t" \
  "v_mfma_f32_16x16x32_bf16 v[104:107], a[236:239], v[124:127], v[104:107]\n\t" \
  "v_exp_f32 v3, v3\n\t" \
  "v_mfma_f32_16x16x32_bf16 v[108:111], a[252:255], v[124:127], v[188:191]\n\t" \
  "v_exp_f32 v4, v4\n\t" \
  "v_mfma_f32_16x16x32_bf16 v[108:111], a[248:251], v[120:123], v[108:111]\n\t" \
  "v_exp_f32 v5, v5\n\t" \
  "v_mfma_f32_16x16x32_bf16 v[108:111], a[244:247], v[116:119], v[108:111]\n\t" \
  "s_waitcnt vmcnt(3) lgkmcnt(0)\n\t" \
  "v_exp_f32 v6, v6\n\t" \
  "v_mfma_f32_16x16x32_bf16 v[108:111], a[240:243], v[112:115], v[108:111]\n\t" \
  "s_barrier\n\t" \
  STA("s_memrealtime s[94:95]\n\t") \
  STA("s_memtime s[92:93]\n\t") \
  "v_mfma_f32_16x16x32_bf16 a[0:3], a[128:131], v[128:131], 0\n\t" \
  "v_exp_f32 v7, v7\n\t" \
  "v_mfma_f32_16x16x32_bf16 a[0:3], a[132:135], v[132:135], a[0:3]\n\t" \
  "v_exp_f32 v8, v8\n\t" \
  "v_mfma_f32_16x16x32_bf16 a[4:7], a[136:139], v[128:131], 0\n\t" \
  "v_exp_f32 v9, v9\n\t" \
  "v_mfma_f32_16x16x32_bf16 a[4:7], a[140:143], v[132:135], a[4:7]\n\t" \
  "v_exp_f32 v10, v10\n\t" \
  "v_mfma_f32_16x16x32_bf16 a[8:11], a[144:147], v[128:131], 0\n\t" \
  "v_exp_f32 v11, v11\n\t" \
  "v_mfma_f32_16x16x32_bf16 a[8:11], a[148:151], v[132:135], a[8:11]\n\t" \
  "v_exp_f32 v12, v12\n\t" \
  "v_mfma_f32_16x16x32_bf16 a[12:15], a[152:155], v[128:131], 0\n\t" \
  "v_exp_f32 v13, v13\n\t" \
  "v_mfma_f32_16x16x32_bf16 a[12:15], a[156:159], v[132:135], a[12:15]\n\t" \
  "v_exp_f32 v14, v14\n\t" \
  "v_mfma_f32_16x16x32_bf16 a[16:19], a[160:163], v[128:131], 0\n\t" \
  "v_exp_f32 v15, v15\n\t" \
  "v_mfma_f32_16x16x32_bf16 a[16:19], a[164:167], v[132:135], a[16:19]\n\t" \
  "v_exp_f32 v32, v32\n\t" \
  "v_mfma_f32_16x16x32_bf16 a[20:23], a[168:171], v[128:131], 0\n\t" \
  "v_exp_f32 v33, v33\n\t" \
  "v_mfma_f32_16x16x32_bf16 a[20:23], a[172:175], v[132:135], a[20:23]\n\t" \
  "v_exp_f32 v34, v34\n\t" \
  "v_mfma_f32_16x16x32_bf16 a[24:27], a[176:179], v[128:131], 0\n\t" \
  STA("v_writelane_b32 v255, s94, 28\n\t") \
  STA("v_writelane_b32 v255, s92, 29\n\t") \
  "v_exp_f32 v35, v35\n\t" \
  "v_mfma_f32_16x16x32_bf16 a[24:27], a[180:183], v[132:135], a[24:27]\n\t" \
  "v_exp_f32 v36, v36\n\t" \
  "v_mfma_f32_16x16x32_bf16 a[28:31], a[184:187], v[128:131], 0\n\t" \
  "v_exp_f32 v37, v37\n\t" \
  "v_mfma_f32_16x16x32_bf16 a[28:31], a[188:191], v[132:135], a[28:31]\n\t" \
  "v_exp_f32 v38, v38\n\t" \
  "v_smfmac_f32_16x16x64_bf16 v[160:163], v[216:219], v[128:135], v208\n\t" \
  "s_add_u32 m0, s38, 65536\n\t" \
  "ds_read_b128 a[192:195], v220 offset:0\n\t" \
  "v_cvt_pk_bf16_f32 v128, v0, v1\n\t" \
  "v_mfma_f32_16x16x32_bf16 a[32:35], a[128:131], v[136:139], 0\n\t" \
  "s_add_u32 s96, s38, 49152\n\t" \
  "ds_read_b128 a[196:199], v221 offset:0\n\t" \
  "v_cvt_pk_bf16_f32 v129, v2, v3\n\t" \
  "v_mfma_f32_16x16x32_bf16 a[32:35], a[132:135], v[140:143], a[32:35]\n\t" \
  "v_exp_f32 v39, v39\n\t" \
  "v_mfma_f32_16x16x32_bf16 a[36:39], a[136:139], v[136:139], 0\n\t" \
  "v_exp_f32 v40, v40\n\t" \
  "v_mfma_f32_16x16x32_bf16 a[36:39], a[140:143], v[140:143], a[36:39]\n\t" \
  "buffer_load_dwordx4 v224, s[60:63], s96 offen offset:0 lds\n\t" \
  "v_mfma_f32_16x16x32_bf16 a[40:43], a[144:147], v[136:139], 0\n\t" \
  "ds_read_b128 a[200:203], v222 offset:0\n\t" \
  "v_cvt_pk_bf16_f32 v130, v4, v5\n\t" \
  "v_mfma_f32_16x16x32_bf16 a[40:43], a[148:151], v[140:143], a[40:43]\n\t" \
  "v_exp_f32 v41, v41\n\t" \
  "v_mfma_f32_16x16x32_bf16 a[44:47], a[152:155], v[136:139], 0\n\t" \
  "ds_read_b128 a[204:207], v223 offset:0\n\t" \
  "v_cvt_pk_bf16_f32 v131, v6, v7\n\t" \
  "v_mfma_f32_16x16x32_bf16 a[44:47], a[156:159], v[140:143], a[44:47]\n\t" \
  "v_exp_f32 v42, v42\n\t" \
  "v_mfma_f32_16x16x32_bf16 a[48:51], a[160:163], v[136:139], 0\n\t" \
  "ds_read_b128 a[208:211], v220 offset:4096\n\t" \
  "v_cvt_pk_bf16_f32 v132, v8, v9\n\t" \
  "v_mfma_f32_16x16x32_bf16 a[48:51], a[164:167], v[140:143], a[48:51]\n\t" \
  "buffer_load_dwordx4 v225, s[60:63], s96 offen offset:1024 lds\n\t" \
  "v_mfma_f32_16x16x32_bf16 a[52:55], a[168:171], v[136:139], 0\n\t" \
  "v_exp_f32 v43, v43\n\t" \
  "v_mfma_f32_16x16x32_bf16 a[52:55], a[172:175], v[140:143], a[52:55]\n\t" \
  "ds_read_b128 a[212:215], v221 offset:4096\n\t" \
  "v_cvt_pk_bf16_f32 v133, v10, v11\n\t" \
  "v_mfma_f32_16x16x32_bf16 a[56:59], a[176:179], v[136:139], 0\n\t" \
  "v_exp_f32 v44, v44\n\t" \
  "v_mfma_f32_16x16x32_bf16 a[56:59], a[180:183], v[140:143], a[56:59]\n\t" \
  "v_exp_f32 v45, v45\n\t" \
  "v_mfma_f32_16x16x32_bf16 a[60:63], a[184:187], v[136:139], 0\n\t" \
  "buffer_load_dwordx4 v226, s[60:63], s96 offen offset:2048 lds\n\t" \
  "v_mfma_f32_16x16x32_bf16 a[60:63], a[188:191], v[140:143], a[60:63]\n\t" \
  "ds_read_b128 a[216:219], v222 offset:4096\n\t" \
  "v_cvt_pk_bf16_f32 v134, v12, v13\n\t" \
  "v_smfmac_f32_16x16x64_bf16 v[164:167], v[216:219], v[136:143], v208\n\t" \
  "ds_read_b128 a[220:223], v223 offset:4096\n\t" \
  "v_cvt_pk_bf16_f32 v135, v14, v15\n\t" \
  "v_mfma_f32_16x16x32_bf16 a[64:67], a[128:131], v[144:147], 0\n\t" \
  "v_exp_f32 v46, v46\n\t" \
  "v_mfma_f32_16x16x32_bf16 a[64:67], a[132:135], v[148:151], a[64:67]\n\t" \
  "v_exp_f32 v47, v47\n\t" \
  "v_mfma_f32_16x16x32_bf16 a[68:71], a[136:139], v[144:147], 0\n\t" \
  "ds_read_b128 a[224:227], v220 offset:8192\n\t" \
  "v_cvt_pk_bf16_f32 v136, v32, v33\n\t" \
  "v_mfma_f32_16x16x32_bf16 a[68:71], a[140:143], v[148:151], a[68:71]\n\t" \
  "buffer_load_dwordx4 v227, s[60:63], s96 offen offset:3072 lds\n\t" \
  "v_mfma_f32_16x16x32_bf16 a[72:75], a[144:147], v[144:147], 0\n\t" \
  "s_add_u32 m0, s38, 81920\n\t" \
  "v_exp_f32 v64, v64\n\t" \
  "v_mfma_f32_16x16x32_bf16 a[72:75], a[148:151], v[148:151], a[72:75]\n\t" \
  "s_add_u32 s96, s38, 32768\n\t" \
  "ds_read_b128 a[228:231], v221 offset:8192\n\t" \
  "v_cvt_pk_bf16_f32 v137, v34, v35\n\t" \
  "v_mfma_f32_16x16x32_bf16 a[76:79], a[152:155], v[144:147], 0\n\t" \
  "v_exp_f32 v65, v65\n\t" \
  "v_mfma_f32_16x16x32_bf16 a[76:79], a[156:159], v[148:151], a[76:79]\n\t" \
  "v_exp_f32 v66, v66\n\t" \
  "v_mfma_f32_16x16x32_bf16 a[80:83], a[160:163], v[144:147], 0\n\t" \
  "buffer_load_dwordx4 v230, s[64:67], s96 offen offset:0 lds\n\t" \
  "v_mfma_f32_16x16x32_bf16 a[80:83], a[164:167], v[148:151], a[80:83]\n\t" \
  "ds_read_b128 a[232:235], v222 offset:8192\n\t" \
  "v_cvt_pk_bf16_f32 v138, v36, v37\n\t" \
  "v_mfma_f32_16x16x32_bf16 a[84:87], a[168:171], v[144:147], 0\n\t" \
  "v_exp_f32 v67, v67\n\t" \
  "v_mfma_f32_16x16x32_bf16 a[84:87], a[172:175], v[148:151], a[84:87]\n\t" \
  "ds_read_b128 a[236:239], v223 offset:8192\n\t" \
  "v_cvt_pk_bf16_f32 v139, v38, v39\n\t" \
  "v_mfma_f32_16x16x32_bf16 a[88:91], a[176:179], v[144:147], 0\n\t" \
  "v_exp_f32 v68, v68\n\t" \
  "v_mfma_f32_16x16x32_bf16 a[88:91], a[180:183], v[148:151], a[88:91]\n\t" \
  "ds_read_b128 a[240:243], v220 offset:12288\n\t" \
  "v_cvt_pk_bf16_f32 v140, v40, v41\n\t" \
  "v_mfma_f32_16x16x32_bf16 a[92:95], a[184:187], v[144:147], 0\n\t" \
  "buffer_load_dwordx4 v231, s[64:67], s96 offen offset:1024 lds\n\t" \
  "v_mfma_f32_16x16x32_bf16 a[92:95], a[188:191], v[148:151], a[92:95]\n\t" \
  "v_exp_f32 v69, v69\n\t" \
  "v_smfmac_f32_16x16x64_bf16 v[168:171], v[216:219], v[144:151], v208\n\t" \
  "ds_read_b128 a[244:247], v221 offset:12288\n\t" \
  "v_cvt_pk_bf16_f32 v141, v42, v43\n\t" \
  "v_mfma_f32_16x16x32_bf16 a[96:99], a[128:131], v[152:155], 0\n\t" \
  "v_exp_f32 v70, v70\n\t" \
  "v_mfma_f32_16x16x32_bf16 a[96:99], a[132:135], v[156:159], a[96:99]\n\t" \
  "v_exp_f32 v71, v71\n\t" \
  "v_mfma_f32_16x16x32_bf16 a[100:103], a[136:139], v[152:155], 0\n\t" \
  "buffer_load_dwordx4 v230, s[64:67], s96 offen offset:2048 lds\n\t" \
  "v_mfma_f32_16x16x32_bf16 a[100:103], a[140:143], v[156:159], a[100:103]\n\t" \
  "ds_read_b128 a[248:251], v222 offset:12288\n\t" \
  "v_cvt_pk_bf16_f32 v142, v44, v45\n\t" \
  "v_mfma_f32_16x16x32_bf16 a[104:107], a[144:147], v[152:155], 0\n\t" \
  "v_exp_f32 v72, v72\n\t" \
  "v_mfma_f32_16x16x32_bf16 a[104:107], a[148:151], v[156:159], a[104:107]\n\t" \
  "ds_read_b128 a[252:255], v223 offset:12288\n\t" \
  "v_cvt_pk_bf16_f32 v143, v46, v47\n\t" \
  "v_mfma_f32_16x16x32_bf16 a[108:111], a[152:155], v[152:155], 0\n\t" \
  "v_exp_f32 v73, v73\n\t" \
  "v_mfma_f32_16x16x32_bf16 a[108:111], a[156:159], v[156:159], a[108:111]\n\t" \
  "v_exp_f32 v74, v74\n\t" \
  "v_mfma_f32_16x16x32_bf16 a[112:115], a[160:163], v[152:155], 0\n\t" \
  "buffer_load_dwordx4 v231, s[64:67], s96 offen offset:3072 lds\n\t" \
  "v_mfma_f32_16x16x32_bf16 a[112:115], a[164:167], v[156:159], a[112:115]\n\t" \
  "v_exp_f32 v75, v75\n\t" \
  "v_mfma_f32_16x16x32_bf16 a[116:119], a[168:171], v[152:155], 0\n\t" \
  "v_exp_f32 v76, v76\n\t" \
  "v_mfma_f32_16x16x32_bf16 a[116:119], a[172:175], v[156:159], a[116:119]\n\t" \
  "v_exp_f32 v77, v77\n\t" \
  "v_mfma_f32_16x16x32_bf16 a[120:123], a[176:179], v[152:155], 0\n\t" \
  "v_exp_f32 v78, v78\n\t" \
  "v_mfma_f32_16x16x32_bf16 a[120:123], a[180:183], v[156:159], a[120:123]\n\t" \
  "v_exp_f32 v79, v79\n\t" \
  "v_mfma_f32_16x16x32_bf16 a[124:127], a[184:187], v[152:155], 0\n\t" \
  "v_exp_f32 v96, v96\n\t" \
  "v_mfma_f32_16x16x32_bf16 a[124:127], a[188:191], v[156:159], a[124:127]\n\t" \
  "v_cvt_pk_bf16_f32 v144, v64, v65\n\t" \
  "v_cvt_pk_bf16_f32 v145, v66, v67\n\t" \
  "v_smfmac_f32_16x16x64_bf16 v[172:175], v[216:219], v[152:159], v208\n\t" \
  "s_waitcnt lgkmcnt(12)\n\t" \
  "v_cvt_pk_bf16_f32 v146, v68, v69\n\t" \
  STA("s_memrealtime s[94:95]\n\t") \
  STA("s_memtime s[92:93]\n\t") \
  "v_mfma_f32_16x16x32_bf16 v[0:3], a[192:195], v[16:19], v[176:179]\n\t" \
  "ds_read_b64_tr_b16 a[128:129], v210 offset:0\n\t" \
  "v_cvt_pk_bf16_f32 v147, v70, v71\n\t" \
  "v_mfma_f32_16x16x32_bf16 v[0:3], a[196:199], v[20:23], v[0:3]\n\t" \
  "v_exp_f32 v97, v97\n\t" \
  "v_mfma_f32_16x16x32_bf16 v[0:3], a[200:203], v[24:27], v[0:3]\n\t" \
  "s_add_u32 m0, s38, 16384\n\t" \
  "ds_read_b64_tr_b16 a[130:131], v210 offset:4096\n\t" \
  "v_cvt_pk_bf16_f32 v148, v72, v73\n\t" \
  "v_mfma_f32_16x16x32_bf16 v[0:3], a[204:207], v[28:31], v[0:3]\n\t" \
  "s_waitcnt lgkmcnt(10)\n\t" \
  "v_exp_f32 v98, v98\n\t" \
  "v_mfma_f32_16x16x32_bf16 v[4:7], a[220:223], v[28:31], v[176:179]\n\t" \
  "s_mov_b32 s96, 0\n\t" \
  "ds_read_b64_tr_b16 a[132:133], v210 offset:8192\n\t" \
  "v_cvt_pk_bf16_f32 v149, v74, v75\n\t" \
  "v_mfma_f32_16x16x32_bf16 v[4:7], a[216:219], v[24:27], v[4:7]\n\t" \
  "v_exp_f32 v99, v99\n\t" \
  "v_mfma_f32_16x16x32_bf16 v[4:7], a[212:215], v[20:23], v[4:7]\n\t" \
  "buffer_load_dwordx4 v232, s[68:71], s96 offen offset:0 lds\n\t" \
  "v_mfma_f32_16x16x32_bf16 v[4:7], a[208:211], v[16:19], v[4:7]\n\t" \
  "s_waitcnt lgkmcnt(7)\n\t" \
  "ds_read_b64_tr_b16 a[134:135], v210 offset:12288\n\t" \
  "v_cvt_pk_bf16_f32 v150, v76, v77\n\t" \
  "v_mfma_f32_16x16x32_bf16 v[8:11], a[224:227], v[16:19], v[176:179]\n\t" \
  "ds_read_b64_tr_b16 a[136:137], v211 offset:0\n\t" \
  "v_cvt_pk_bf16_f32 v151, v78, v79\n\t" \
  "v_mfma_f32_16x16x32_bf16 v[8:11], a[228:231], v[20:23], v[8:11]\n\t" \
  "ds_read_b64_tr_b16 a[138:139], v211 offset:4096\n\t" \
  "v_cvt_pk_bf16_f32 v152, v96, v97\n\t" \
  "v_mfma_f32_16x16x32_bf16 v[8:11], a[232:235], v[24:27], v[8:11]\n\t" \
  "v_exp_f32 v100, v100\n\t" \
  "v_mfma_f32_16x16x32_bf16 v[8:11], a[236:239], v[28:31], v[8:11]\n\t" \
  "s_waitcnt lgkmcnt(6)\n\t" \
  "ds_read_b64_tr_b16 a[140:141], v211 offset:8192\n\t" \
  "v_cvt_pk_bf16_f32 v153, v98, v99\n\t" \
  "v_mfma_f32_16x16x32_bf16 v[12:15], a[252:255], v[28:31], v[176:179]\n\t" \
  STA("v_writelane_b32 v255, s94, 30\n\t") \
  STA("v_writelane_b32 v255, s92, 31\n\t") \
  "v_exp_f32 v101, v101\n\t" \
  "v_mfma_f32_16x16x32_bf16 v[12:15], a[248:251], v[24:27], v[12:15]\n\t" \
  "ds_read_b64_tr_b16 a[142:143], v211 offset:12288\n\t" \
  "v_cvt_pk_bf16_f32 v154, v100, v101\n\t" \
  "v_mfma_f32_16x16x32_bf16 v[12:15], a[244:247], v[20:23], v[12:15]\n\t" \
  "v_exp_f32 v102, v102\n\t" \
  "v_mfma_f32_16x16x32_bf16 v[12:15], a[240:243], v[16:19], v[12:15]\n\t" \
  "v_exp_f32 v103, v103\n\t" \
  "v_mfma_f32_16x16x32_bf16 v[32:35], a[192:195], v[48:51], v[180:183]\n\t" \
  "s_mov_b32 s96, 6144\n\t" \
  "ds_read_b64_tr_b16 a[144:145], v210 offset:256\n\t" \
  "v_cvt_pk_bf16_f32 v155, v102, v103\n\t" \
  "v_mfma_f32_16x16x32_bf16 v[32:35], a[196:199], v[52:55], v[32:35]\n\t" \
  "v_exp_f32 v104, v104\n\t" \
  "v_mfma_f32_16x16x32_bf16 v[32:35], a[200:203], v[56:59], v[32:35]\n\t" \
  "v_exp_f32 v105, v105\n\t" \
  "v_mfma_f32_16x16x32_bf16 v[32:35], a[204:207], v[60:63], v[32:35]\n\t" \
  "ds_read_b64_tr_b16 a[146:147], v210 offset:4352\n\t" \
  "v_cvt_pk_bf16_f32 v156, v104, v105\n\t" \
  "v_mfma_f32_16x16x32_bf16 v[36:39], a[220:223], v[60:63], v[180:183]\n\t" \
  "buffer_load_dwordx4 v232, s[68:71], s96 offen offset:1024 lds\n\t" \
  "v_mfma_f32_16x16x32_bf16 v[36:39], a[216:219], v[56:59], v[36:39]\n\t" \
  "v_exp_f32 v106, v106\n\t" \
  "v_mfma_f32_16x16x32_bf16 v[36:39], a[212:215], v[52:55], v[36:39]\n\t" \
  "v_exp_f32 v107, v107\n\t" \
  "v_mfma_f32_16x16x32_bf16 v[36:39], a[208:211], v[48:51], v[36:39]\n\t" \
  "ds_read_b64_tr_b16 a[148:149], v210 offset:8448\n\t" \
  "v_cvt_pk_bf16_f32 v157, v106, v107\n\t" \
  "v_mfma_f32_16x16x32_bf16 v[40:43], a[224:227], v[48:51], v[180:183]\n\t" \
  "v_exp_f32 v108, v108\n\t" \
  "v_mfma_f32_16x16x32_bf16 v[40:43], a[228:231], v[52:55], v[40:43]\n\t" \
  "ds_read_b64_tr_b16 a[150:151], v210 offset:12544\n\t" \
  "v_mfma_f32_16x16x32_bf16 v[40:43], a[232:235], v[56:59], v[40:43]\n\t" \
  "ds_read_b64_tr_b16 a[152:153], v211 offset:256\n\t" \
  "v_mfma_f32_16x16x32_bf16 v[40:43], a[236:239], v[60:63], v[40:43]\n\t" \
  "v_exp_f32 v109, v109\n\t" \
  "v_mfma_f32_16x16x32_bf16 v[44:47], a[252:255], v[60:63], v[180:183]\n\t" \
  "ds_read_b64_tr_b16 a[154:155], v211 offset:4352\n\t" \
  "v_cvt_pk_bf16_f32 v158, v108, v109\n\t" \
  "v_mfma_f32_16x16x32_bf16 v[44:47], a[248:251], v[56:59], v[44:47]\n\t" \
  "v_exp_f32 v110, v110\n\t" \
  "v_mfma_f32_16x16x32_bf16 v[44:47], a[244:247], v[52:55], v[44:47]\n\t" \
  "ds_read_b64_tr_b16 a[156:157], v211 offset:8448\n\t" \
  "s_mov_b32 s96, 12288\n\t" \
  "v_mfma_f32_16x16x32_bf16 v[44:47], a[240:243], v[48:51], v[44:47]\n\t" \
  "v_exp_f32 v111, v111\n\t" \
  "v_mfma_f32_16x16x32_bf16 v[64:67], a[192:195], v[80:83], v[184:187]\n\t" \
  "ds_read_b64_tr_b16 a[158:159], v211 offset:12544\n\t" \
  "v_cvt_pk_bf16_f32 v159, v110, v111\n\t" \
  "v_mfma_f32_16x16x32_bf16 v[64:67], a[196:199], v[84:87], v[64:67]\n\t" \
  "ds_read_b64_tr_b16 a[160:161], v210 offset:512\n\t" \
  "v_mfma_f32_16x16x32_bf16 v[64:67], a[200:203], v[88:91], v[64:67]\n\t" \
  "buffer_load_dwordx4 v232, s[68:71], s96 offen offset:2048 lds\n\t" \
  "v_mfma_f32_16x16x32_bf16 v[64:67], a[204:207], v[92:95], v[64:67]\n\t" \
  "ds_read_b64_tr_b16 a[162:163], v210 offset:4608\n\t" \
  "v_mfma_f32_16x16x32_bf16 v[68:71], a[220:223], v[92:95], v[184:187]\n\t" \
  "ds_read_b64_tr_b16 a[164:165], v210 offset:8704\n\t" \
  "v_mfma_f32_16x16x32_bf16 v[68:71], a[216:219], v[88:91], v[68:71]\n\t" \
  "ds_read_b64_tr_b16 a[166:167], v210 offset:12800\n\t" \
  "v_mfma_f32_16x16x32_bf16 v[68:71], a[212:215], v[84:87], v[68:71]\n\t" \
  "v_exp_f32 v0, v0\n\t" \
  "v_mfma_f32_16x16x32_bf16 v[68:71], a[208:211], v[80:83], v[68:71]\n\t" \
  "ds_read_b64_tr_b16 a[168:169], v211 offset:512\n\t" \
  "v_mfma_f32_16x16x32_bf16 v[72:75], a[224:227], v[80:83], v[184:187]\n\t" \
  "v_exp_f32 v1, v1\n\t" \
  "v_mfma_f32_16x16x32_bf16 v[72:75], a[228:231], v[84:87], v[72:75]\n\t" \
  "ds_read_b64_tr_b16 a[170:171], v211 offset:4608\n\t" \
  "v_mfma_f32_16x16x32_bf16 v[72:75], a[232:235], v[88:91], v[72:75]\n\t" \
  "ds_read_b64_tr_b16 a[172:173], v211 offset:8704\n\t" \
  "v_mfma_f32_16x16x32_bf16 v[72:75], a[236:239], v[92:95], v[72:75]\n\t" \
  "v_exp_f32 v2, v2\n\t" \
  "v_mfma_f32_16x16x32_bf16 v[76:79], a[252:255], v[92:95], v[184:187]\n\t" \
  "ds_read_b64_tr_b16 a[174:175], v211 offset:12800\n\t" \
  "s_mov_b32 s96, 18432\n\t" \
  "v_mfma_f32_16x16x32_bf16 v[76:79], a[248:251], v[88:91], v[76:79]\n\t" \
  "ds_read_b64_tr_b16 a[176:177], v210 offset:768\n\t" \
  "v_mfma_f32_16x16x32_bf16 v[76:79], a[244:247], v[84:87], v[76:79]\n\t" \
  "v_exp_f32 v3, v3\n\t" \
  "v_mfma_f32_16x16x32_bf16 v[76:79], a[240:243], v[80:83], v[76:79]\n\t" \
  "ds_read_b64_tr_b16 a[178:179], v210 offset:4864\n\t" \
  "v_mfma_f32_16x16x32_bf16 v[96:99], a[192:195], v[112:115], v[188:191]\n\t" \
  "buffer_load_dwordx4 v232, s[68:71], s96 offen offset:3072 lds\n\t" \
  "v_mfma_f32_16x16x32_bf16 v[96:99], a[196:199], v[116:119], v[96:99]\n\t" \
  "ds_read_b64_tr_b16 a[180:181], v210 offset:8960\n\t" \
  "v_mfma_f32_16x16x32_bf16 v[96:99], a[200:203], v[120:123], v[96:99]\n\t" \
  "ds_read_b64_tr_b16 a[182:183], v210 offset:13056\n\t" \
  "v_mfma_f32_16x16x32_bf16 v[96:99], a[204:207], v[124:127], v[96:99]\n\t" \
  "v_exp_f32 v4, v4\n\t" \
  "v_mfma_f32_16x16x32_bf16 v[100:103], a[220:223], v[124:127], v[188:191]\n\t" \
  "ds_read_b64_tr_b16 a[184:185], v211 offset:768\n\t" \
  "v_mfma_f32_16x16x32_bf16 v[100:103], a[216:219], v[120:123], v[100:103]\n\t" \
  "v_exp_f32 v5, v5\n\t" \
  "v_mfma_f32_16x16x32_bf16 v[100:103], a[212:215], v[116:119], v[100:103]\n\t" \
  "ds_read_b64_tr_b16 a[186:187], v211 offset:4864\n\t" \
  "v_mfma_f32_16x16x32_bf16 v[100:103], a[208:211], v[112:115], v[100:103]\n\t" \
  "ds_read_b64_tr_b16 a[188:189], v211 offset:8960\n\t" \
  "v_mfma_f32_16x16x32_bf16 v[104:107], a[224:227], v[112:115], v[188:191]\n\t" \
  "v_exp_f32 v6, v6\n\t" \
  "v_mfma_f32_16x16x32_bf16 v[104:107], a[228:231], v[116:119], v[104:107]\n\t" \
  "ds_read_b64_tr_b16 a[190:191], v211 offset:13056\n\t" \
  "v_mfma_f32_16x16x32_bf16 v[104:107], a[232:235], v[120:123], v[104:107]\n\t" \
  "v_exp_f32 v7, v7\n\t" \
  "v_mfma_f32_16x16x32_bf16 v[104:107], a[236:239], v[124:127], v[104:107]\n\t" \
  "v_exp_f32 v8, v8\n\t" \
  "v_mfma_f32_16x16x32_bf16 v[108:111], a[252:255], v[124:127], v[188:191]\n\t" \
  "v_exp_f32 v9, v9\n\t" \
  "v_mfma_f32_16x16x32_bf16 v[108:111], a[248:251], v[120:123], v[108:111]\n\t" \
  "v_exp_f32 v10, v10\n\t" \
  "v_mfma_f32_16x16x32_bf16 v[108:111], a[244:247], v[116:119], v[108:111]\n\t" \
  "s_waitcnt vmcnt(4) lgkmcnt(0)\n\t" \
  "v_exp_f32 v11, v11\n\t" \
  "v_mfma_f32_16x16x32_bf16 v[108:111], a[240:243], v[112:115], v[108:111]\n\t" \
  "s_barrier\n\t" \
  STA("s_memrealtime s[94:95]\n\t") \
  STA("s_memtime s[92:93]\n\t") \
  "v_mfma_f32_16x16x32_bf16 a[0:3], a[128:131], v[128:131], a[0:3]\n\t" \
  "buffer_store_dword v233, v247, s[16:19], 0 offen sc1\n\t" \
  "v_mfma_f32_16x16x32_bf16 a[0:3], a[132:135], v[132:135], a[0:3]\n\t" \
  "v_exp_f32 v12, v12\n\t" \
  "v_mfma_f32_16x16x32_bf16 a[4:7], a[136:139], v[128:131], a[4:7]\n\t" \
  "v_exp_f32 v13, v13\n\t" \
  "v_mfma_f32_16x16x32_bf16 a[4:7], a[140:143], v[132:135], a[4:7]\n\t" \
  "v_exp_f32 v14, v14\n\t" \
  "v_mfma_f32_16x16x32_bf16 a[8:11], a[144:147], v[128:131], a[8:11]\n\t" \
  "v_exp_f32 v15, v15\n\t" \
  "v_mfma_f32_16x16x32_bf16 a[8:11], a[148:151], v[132:135], a[8:11]\n\t" \
  "v_exp_f32 v32, v32\n\t" \
  "v_mfma_f32_16x16x32_bf16 a[12:15], a[152:155], v[128:131], a[12:15]\n\t" \
  "v_exp_f32 v33, v33\n\t" \
  "v_mfma_f32_16x16x32_bf16 a[12:15], a[156:159], v[132:135], a[12:15]\n\t" \
  "v_exp_f32 v34, v34\n\t" \
  "v_mfma_f32_16x16x32_bf16 a[16:19], a[160:163], v[128:131], a[16:19]\n\t" \
  "v_exp_f32 v35, v35\n\t" \
  "v_mfma_f32_16x16x32_bf16 a[16:19], a[164:167], v[132:135], a[16:19]\n\t" \
  "v_exp_f32 v36, v36\n\t" \
  "v_mfma_f32_16x16x32_bf16 a[20:23], a[168:171], v[128:131], a[20:23]\n\t" \
  "v_exp_f32 v37, v37\n\t" \
  "v_mfma_f32_16x16x32_bf16 a[20:23], a[172:175], v[132:135], a[20:23]\n\t" \
  "v_exp_f32 v38, v38\n\t" \
  "v_mfma_f32_16x16x32_bf16 a[24:27], a[176:179], v[128:131], a[24:27]\n\t" \
  STA("v_writelane_b32 v255, s94, 32\n\t") \
  STA("v_writelane_b32 v255, s92, 33\n\t") \
  "v_exp_f32 v39, v39\n\t" \
  "v_mfma_f32_16x16x32_bf16 a[24:27], a[180:183], v[132:135], a[24:27]\n\t" \
  "v_exp_f32 v40, v40\n\t" \
  "v_mfma_f32_16x16x32_bf16 a[28:31], a[184:187], v[128:131], a[28:31]\n\t" \
  "v_exp_f32 v41, v41\n\t" \
  "v_mfma_f32_16x16x32_bf16 a[28:31], a[188:191], v[132:135], a[28:31]\n\t" \
  "v_exp_f32 v42, v42\n\t" \
  "v_smfmac_f32_16x16x64_bf16 v[160:163], v[216:219], v[128:135], v208\n\t" \
  "ds_read_b128 a[192:195], v220 offset:16384\n\t" \
  "v_cvt_pk_bf16_f32 v128, v0, v1\n\t" \
  "v_mfma_f32_16x16x32_bf16 a[32:35], a[128:131], v[136:139], a[32:35]\n\t" \
  "ds_read_b128 a[196:199], v221 offset:16384\n\t" \
  "v_cvt_pk_bf16_f32 v129, v2, v3\n\t" \
  "v_mfma_f32_16x16x32_bf16 a[32:35], a[132:135], v[140:143], a[32:35]\n\t" \
  "s_add_u32 m0, s38, 32768\n\t" \
  "v_exp_f32 v43, v43\n\t" \
  "v_mfma_f32_16x16x32_bf16 a[36:39], a[136:139], v[136:139], a[36:39]\n\t" \
  "s_mov_b32 s96, 28672\n\t" \
  "v_exp_f32 v44, v44\n\t" \
  "v_mfma_f32_16x16x32_bf16 a[36:39], a[140:143], v[140:143], a[36:39]\n\t" \
  "ds_read_b128 a[200:203], v222 offset:16384\n\t" \
  "v_cvt_pk_bf16_f32 v130, v4, v5\n\t" \
  "v_mfma_f32_16x16x32_bf16 a[40:43], a[144:147], v[136:139], a[40:43]\n\t" \
  "v_exp_f32 v45, v45\n\t" \
  "v_mfma_f32_16x16x32_bf16 a[40:43], a[148:151], v[140:143], a[40:43]\n\t" \
  "buffer_load_dwordx4 v232, s[68:71], s96 offen offset:0 lds\n\t" \
  "v_mfma_f32_16x16x32_bf16 a[44:47], a[152:155], v[136:139], a[44:47]\n\t" \
  "ds_read_b128 a[204:207], v223 offset:16384\n\t" \
  "v_cvt_pk_bf16_f32 v131, v6, v7\n\t" \
  "v_mfma_f32_16x16x32_bf16 a[44:47], a[156:159], v[140:143], a[44:47]\n\t" \
  "v_exp_f32 v46, v46\n\t" \
  "v_mfma_f32_16x16x32_bf16 a[48:51], a[160:163], v[136:139], a[48:51]\n\t" \
  "ds_read_b128 a[208:211], v220 offset:20480\n\t" \
  "v_cvt_pk_bf16_f32 v132, v8, v9\n\t" \
  "v_mfma_f32_16x16x32_bf16 a[48:51], a[164:167], v[140:143], a[48:51]\n\t" \
  "v_exp_f32 v47, v47\n\t" \
  "v_mfma_f32_16x16x32_bf16 a[52:55], a[168:171], v[136:139], a[52:55]\n\t" \
  "v_exp_f32 v64, v64\n\t" \
  "v_mfma_f32_16x16x32_bf16 a[52:55], a[172:175], v[140:143], a[52:55]\n\t" \
  "ds_read_b128 a[212:215], v221 offset:20480\n\t" \
  "v_cvt_pk_bf16_f32 v133, v10, v11\n\t" \
  "v_mfma_f32_16x16x32_bf16 a[56:59], a[176:179], v[136:139], a[56:59]\n\t" \
  "v_exp_f32 v65, v65\n\t" \
  "v_mfma_f32_16x16x32_bf16 a[56:59], a[180:183], v[140:143], a[56:59]\n\t" \
  "s_mov_b32 s96, 34816\n\t" \
  "v_exp_f32 v66, v66\n\t" \
  "v_mfma_f32_16x16x32_bf16 a[60:63], a[184:187], v[136:139], a[60:63]\n\t" \
  "ds_read_b128 a[216:219], v222 offset:20480\n\t" \
  "v_cvt_pk_bf16_f32 v134, v12, v13\n\t" \
  "v_mfma_f32_16x16x32_bf16 a[60:63], a[188:191], v[140:143], a[60:63]\n\t" \
  "v_exp_f32 v67, v67\n\t" \
  "v_smfmac_f32_16x16x64_bf16 v[164:167], v[216:219], v[136:143], v208\n\t" \
  "ds_read_b128 a[220:223], v223 offset:20480\n\t" \
  "v_cvt_pk_bf16_f32 v135, v14, v15\n\t" \
  "v_mfma_f32_16x16x32_bf16 a[64:67], a[128:131], v[144:147], a[64:67]\n\t" \
  "buffer_load_dwordx4 v232, s[68:71], s96 offen offset:1024 lds\n\t" \
  "v_mfma_f32_16x16x32_bf16 a[64:67], a[132:135], v[148:151], a[64:67]\n\t" \
  "v_exp_f32 v68, v68\n\t" \
  "v_mfma_f32_16x16x32_bf16 a[68:71], a[136:139], v[144:147], a[68:71]\n\t" \
  "ds_read_b128 a[224:227], v220 offset:24576\n\t" \
  "v_cvt_pk_bf16_f32 v136, v32, v33\n\t" \
  "v_mfma_f32_16x16x32_bf16 a[68:71], a[140:143], v[148:151], a[68:71]\n\t" \
  "v_exp_f32 v69, v69\n\t" \
  "v_mfma_f32_16x16x32_bf16 a[72:75], a[144:147], v[144:147], a[72:75]\n\t" \
  "s_add_u32 m0, s38, 49152\n\t" \
  "v_exp_f32 v70, v70\n\t" \
  "v_mfma_f32_16x16x32_bf16 a[72:75], a[148:151], v[148:151], a[72:75]\n\t" \
  "s_add_u32 s96, s38, 65536\n\t" \
  "ds_read_b128 a[228:231], v221 offset:24576\n\t" \
  "v_cvt_pk_bf16_f32 v137, v34, v35\n\t" \
  "v_mfma_f32_16x16x32_bf16 a[76:79], a[152:155], v[144:147], a[76:79]\n\t" \
  "v_exp_f32 v71, v71\n\t" \
  "v_mfma_f32_16x16x32_bf16 a[76:79], a[156:159], v[148:151], a[76:79]\n\t" \
  "v_exp_f32 v72, v72\n\t" \
  "v_mfma_f32_16x16x32_bf16 a[80:83], a[160:163], v[144:147], a[80:83]\n\t" \
  "buffer_load_dwordx4 v224, s[60:63], s96 offen offset:0 lds\n\t" \
  "v_mfma_f32_16x16x32_bf16 a[80:83], a[164:167], v[148:151], a[80:83]\n\t" \
  "ds_read_b128 a[232:235], v222 offset:24576\n\t" \
  "v_cvt_pk_bf16_f32 v138, v36, v37\n\t" \
  "v_mfma_f32_16x16x32_bf16 a[84:87], a[168:171], v[144:147], a[84:87]\n\t" \
  "v_exp_f32 v73, v73\n\t" \
  "v_mfma_f32_16x16x32_bf16 a[84:87], a[172:175], v[148:151], a[84:87]\n\t" \
  "buffer_load_dwordx4 v225, s[60:63], s96 offen offset:1024 lds\n\t" \
  "v_mfma_f32_16x16x32_bf16 a[88:91], a[176:179], v[144:147], a[88:91]\n\t" \
  "ds_read_b128 a[236:239], v223 offset:24576\n\t" \
  "v_cvt_pk_bf16_f32 v139, v38, v39\n\t" \
  "v_mfma_f32_16x16x32_bf16 a[88:91], a[180:183], v[148:151], a[88:91]\n\t" \
  "ds_read_b128 a[240:243], v220 offset:28672\n\t" \
  "v_cvt_pk_bf16_f32 v140, v40, v41\n\t" \
  "v_mfma_f32_16x16x32_bf16 a[92:95], a[184:187], v[144:147], a[92:95]\n\t" \
  "buffer_load_dwordx4 v226, s[60:63], s96 offen offset:2048 lds\n\t" \
  "v_mfma_f32_16x16x32_bf16 a[92:95], a[188:191], v[148:151], a[92:95]\n\t" \
  "v_exp_f32 v74, v74\n\t" \
  "v_smfmac_f32_16x16x64_bf16 v[168:171], v[216:219], v[144:151], v208\n\t" \
  "ds_read_b128 a[244:247], v221 offset:28672\n\t" \
  "v_cvt_pk_bf16_f32 v141, v42, v43\n\t" \
  "v_mfma_f32_16x16x32_bf16 a[96:99], a[128:131], v[152:155], a[96:99]\n\t" \
  "buffer_load_dwordx4 v227, s[60:63], s96 offen offset:3072 lds\n\t" \
  "v_mfma_f32_16x16x32_bf16 a[96:99], a[132:135], v[156:159], a[96:99]\n\t" \
  "s_add_u32 m0, s38, 147456\n\t" \
  "s_add_u32 s96, s38, 49152\n\t" \
  "v_cvt_pk_bf16_f32 v142, v44, v45\n\t" \
  "v_mfma_f32_16x16x32_bf16 a[100:103], a[136:139], v[152:155], a[100:103]\n\t" \
  "buffer_load_dwordx4 v230, s[64:67], s96 offen offset:0 lds\n\t" \
  "v_mfma_f32_16x16x32_bf16 a[100:103], a[140:143], v[156:159], a[100:103]\n\t" \
  "ds_read_b128 a[248:251], v222 offset:28672\n\t" \
  "v_cvt_pk_bf16_f32 v143, v46, v47\n\t" \
  "v_mfma_f32_16x16x32_bf16 a[104:107], a[144:147], v[152:155], a[104:107]\n\t" \
  "v_exp_f32 v75, v75\n\t" \
  "v_mfma_f32_16x16x32_bf16 a[104:107], a[148:151], v[156:159], a[104:107]\n\t" \
  "buffer_load_dwordx4 v231, s[64:67], s96 offen offset:1024 lds\n\t" \
  "v_mfma_f32_16x16x32_bf16 a[108:111], a[152:155], v[152:155], a[108:111]\n\t" \
  "ds_read_b128 a[252:255], v223 offset:28672\n\t" \
  "v_cvt_pk_bf16_f32 v144, v64, v65\n\t" \
  "v_mfma_f32_16x16x32_bf16 a[108:111], a[156:159], v[156:159], a[108:111]\n\t" \
  "v_exp_f32 v76, v76\n\t" \
  "v_mfma_f32_16x16x32_bf16 a[112:115], a[160:163], v[152:155], a[112:115]\n\t" \
  "buffer_load_dwordx4 v230, s[64:67], s96 offen offset:2048 lds\n\t" \
  "v_mfma_f32_16x16x32_bf16 a[112:115], a[164:167], v[156:159], a[112:115]\n\t" \
  "v_exp_f32 v77, v77\n\t" \
  "v_mfma_f32_16x16x32_bf16 a[116:119], a[168:171], v[152:155], a[116:119]\n\t" \
  "v_exp_f32 v78, v78\n\t" \
  "v_mfma_f32_16x16x32_bf16 a[116:119], a[172:175], v[156:159], a[116:119]\n\t" \
  "buffer_load_dwordx4 v231, s[64:67], s96 offen offset:3072 lds\n\t" \
  "v_mfma_f32_16x16x32_bf16 a[120:123], a[176:179], v[152:155], a[120:123]\n\t" \
  "v_exp_f32 v79, v79\n\t" \
  "v_mfma_f32_16x16x32_bf16 a[120:123], a[180:183], v[156:159], a[120:123]\n\t" \
  "v_exp_f32 v96, v96\n\t" \
  "v_mfma_f32_16x16x32_bf16 a[124:127], a[184:187], v[152:155], a[124:127]\n\t" \
  "v_exp_f32 v97, v97\n\t" \
  "v_mfma_f32_16x16x32_bf16 a[124:127], a[188:191], v[156:159], a[124:127]\n\t" \
  "v_cvt_pk_bf16_f32 v145, v66, v67\n\t" \
  "v_cvt_pk_bf16_f32 v146, v68, v69\n\t" \
  "v_smfmac_f32_16x16x64_bf16 v[172:175], v[216:219], v[152:159], v208\n\t" \
  "s_waitcnt lgkmcnt(12)\n\t" \
  "v_cvt_pk_bf16_f32 v147, v70, v71\n\t" \
  STA("s_memrealtime s[94:95]\n\t") \
  STA("s_memtime s[92:93]\n\t") \
  "v_mfma_f32_16x16x32_bf16 v[0:3], a[192:195], v[16:19], v[176:179]\n\t" \
  "ds_read_b64_tr_b16 a[128:129], v228 offset:0\n\t" \
  "v_cvt_pk_bf16_f32 v148, v72, v73\n\t" \
  "v_mfma_f32_16x16x32_bf16 v[0:3], a[196:199], v[20:23], v[0:3]\n\t" \
  "v_exp_f32 v98, v98\n\t" \
  "v_mfma_f32_16x16x32_bf16 v[0:3], a[200:203], v[24:27], v[0:3]\n\t" \
  "s_add_u32 m0, s38, 32768\n\t" \
  "ds_read_b64_tr_b16 a[130:131], v228 offset:4096\n\t" \
  "v_cvt_pk_bf16_f32 v149, v74, v75\n\t" \
  "v_mfma_f32_16x16x32_bf16 v[0:3], a[204:207], v[28:31], v[0:3]\n\t" \
  "s_waitcnt lgkmcnt(10)\n\t" \
  "v_exp_f32 v99, v99\n\t" \
  "v_mfma_f32_16x16x32_bf16 v[4:7], a[220:223], v[28:31], v[176:179]\n\t" \
  "s_mov_b32 s96, 40960\n\t" \
  "ds_read_b64_tr_b16 a[132:133], v228 offset:8192\n\t" \
  "v_cvt_pk_bf16_f32 v150, v76, v77\n\t" \
  "v_mfma_f32_16x16x32_bf16 v[4:7], a[216:219], v[24:27], v[4:7]\n\t" \
  "v_exp_f32 v100, v100\n\t" \
  "v_mfma_f32_16x16x32_bf16 v[4:7], a[212:215], v[20:23], v[4:7]\n\t" \
  "buffer_load_dwordx4 v232, s[68:71], s96 offen offset:2048 lds\n\t" \
  "v_mfma_f32_16x16x32_bf16 v[4:7], a[208:211], v[16:19], v[4:7]\n\t" \
  "s_waitcnt lgkmcnt(7)\n\t" \
  "ds_read_b64_tr_b16 a[134:135], v228 offset:12288\n\t" \
  "v_cvt_pk_bf16_f32 v151, v78, v79\n\t" \
  "v_mfma_f32_16x16x32_bf16 v[8:11], a[224:227], v[16:19], v[176:179]\n\t" \
  "ds_read_b64_tr_b16 a[136:137], v229 offset:0\n\t" \
  "v_cvt_pk_bf16_f32 v152, v96, v97\n\t" \
  "v_mfma_f32_16x16x32_bf16 v[8:11], a[228:231], v[20:23], v[8:11]\n\t" \
  "ds_read_b64_tr_b16 a[138:139], v229 offset:4096\n\t" \
  "v_cvt_pk_bf16_f32 v153, v98, v99\n\t" \
  "v_mfma_f32_16x16x32_bf16 v[8:11], a[232:235], v[24:27], v[8:11]\n\t" \
  "v_exp_f32 v101, v101\n\t" \
  "v_mfma_f32_16x16x32_bf16 v[8:11], a[236:239], v[28:31], v[8:11]\n\t" \
  "s_waitcnt lgkmcnt(6)\n\t" \
  "ds_read_b64_tr_b16 a[140:141], v229 offset:8192\n\t" \
  "v_cvt_pk_bf16_f32 v154, v100, v101\n\t" \
  "v_mfma_f32_16x16x32_bf16 v[12:15], a[252:255], v[28:31], v[176:179]\n\t" \
  STA("v_writelane_b32 v255, s94, 34\n\t") \
  STA("v_writelane_b32 v255, s92, 35\n\t") \
  "v_exp_f32 v102, v102\n\t" \
  "v_mfma_f32_16x16x32_bf16 v[12:15], a[248:251], v[24:27], v[12:15]\n\t" \
  "v_exp_f32 v103, v103\n\t" \
  "v_mfma_f32_16x16x32_bf16 v[12:15], a[244:247], v[20:23], v[12:15]\n\t" \
  "ds_read_b64_tr_b16 a[142:143], v229 offset:12288\n\t" \
  "v_cvt_pk_bf16_f32 v155, v102, v103\n\t" \
  "v_mfma_f32_16x16x32_bf16 v[12:15], a[240:243], v[16:19], v[12:15]\n\t" \
  ".Lhs7:\n\ts_cbranch_execz .Lhs7n\n\t" \
  "v_exp_f32 v104, v104\n\t" \
  "v_mfma_f32_16x16x32_bf16 v[32:35], a[192:195], v[48:51], v[180:183]\n\t" \
  "s_mov_b32 s96, 47104\n\t" \
  "v_exp_f32 v105, v105\n\t" \
  "v_mfma_f32_16x16x32_bf16 v[32:35], a[196:199], v[52:55], v[32:35]\n\t" \
  "ds_read_b64_tr_b16 a[144:145], v228 offset:256\n\t" \
  "v_cvt_pk_bf16_f32 v156, v104, v105\n\t" \
  "v_mfma_f32_16x16x32_bf16 v[32:35], a[200:203], v[56:59], v[32:35]\n\t" \
  "v_exp_f32 v106, v106\n\t" \
  "v_mfma_f32_16x16x32_bf16 v[32:35], a[204:207], v[60:63], v[32:35]\n\t" \
  "v_exp_f32 v107, v107\n\t" \
  "v_mfma_f32_16x16x32_bf16 v[36:39], a[220:223], v[60:63], v[180:183]\n\t" \
  "buffer_load_dwordx4 v232, s[68:71], s96 offen offset:3072 lds\n\t" \
  "v_mfma_f32_16x16x32_bf16 v[36:39], a[216:219], v[56:59], v[36:39]\n\t" \
  "ds_read_b64_tr_b16 a[146:147], v228 offset:4352\n\t" \
  "v_cvt_pk_bf16_f32 v157, v106, v107\n\t" \
  "v_mfma_f32_16x16x32_bf16 v[36:39], a[212:215], v[52:55], v[36:39]\n\t" \
  "v_exp_f32 v108, v108\n\t" \
  "v_mfma_f32_16x16x32_bf16 v[36:39], a[208:211], v[48:51], v[36:39]\n\t" \
  "ds_read_b64_tr_b16 a[148:149], v228 offset:8448\n\t" \
  "v_mfma_f32_16x16x32_bf16 v[40:43], a[224:227], v[48:51], v[180:183]\n\t" \
  "ds_read_b64_tr_b16 a[150:151], v228 offset:12544\n\t" \
  "v_mfma_f32_16x16x32_bf16 v[40:43], a[228:231], v[52:55], v[40:43]\n\t" \
  "v_exp_f32 v109, v109\n\t" \
  "v_mfma_f32_16x16x32_bf16 v[40:43], a[232:235], v[56:59], v[40:43]\n\t" \
  "ds_read_b64_tr_b16 a[152:153], v229 offset:256\n\t" \
  "v_cvt_pk_bf16_f32 v158, v108, v109\n\t" \
  "v_mfma_f32_16x16x32_bf16 v[40:43], a[236:239], v[60:63], v[40:43]\n\t" \
  "v_exp_f32 v110, v110\n\t" \
  "v_mfma_f32_16x16x32_bf16 v[44:47], a[252:255], v[60:63], v[180:183]\n\t" \
  "ds_read_b64_tr_b16 a[154:155], v229 offset:4352\n\t" \
  "v_mfma_f32_16x16x32_bf16 v[44:47], a[248:251], v[56:59], v[44:47]\n\t" \
  "v_exp_f32 v111, v111\n\t" \
  "v_mfma_f32_16x16x32_bf16 v[44:47], a[244:247], v[52:55], v[44:47]\n\t" \
  "s_add_u32 m0, s38, 114688\n\t" \
  "ds_read_b64_tr_b16 a[156:157], v229 offset:8448\n\t" \
  "v_cvt_pk_bf16_f32 v159, v110, v111\n\t" \
  "v_mfma_f32_16x16x32_bf16 v[44:47], a[240:243], v[48:51], v[44:47]\n\t" \
  "ds_read_b64_tr_b16 a[158:159], v229 offset:12544\n\t" \
  "s_mov_b32 s96, 0\n\t" \
  "v_mfma_f32_16x16x32_bf16 v[64:67], a[192:195], v[80:83], v[184:187]\n\t" \
  "ds_read_b64_tr_b16 a[160:161], v228 offset:512\n\t" \
  "v_mfma_f32_16x16x32_bf16 v[64:67], a[196:199], v[84:87], v[64:67]\n\t" \
  "v_exp_f32 v0, v0\n\t" \
  "v_mfma_f32_16x16x32_bf16 v[64:67], a[200:203], v[88:91], v[64:67]\n\t" \
  "buffer_load_dwordx4 v232, s[72:75], s96 offen offset:0 lds\n\t" \
  "v_mfma_f32_16x16x32_bf16 v[64:67], a[204:207], v[92:95], v[64:67]\n\t" \
  "ds_read_b64_tr_b16 a[162:163], v228 offset:4608\n\t" \
  "v_mfma_f32_16x16x32_bf16 v[68:71], a[220:223], v[92:95], v[184:187]\n\t" \
  "ds_read_b64_tr_b16 a[164:165], v228 offset:8704\n\t" \
  "v_mfma_f32_16x16x32_bf16 v[68:71], a[216:219], v[88:91], v[68:71]\n\t" \
  "ds_read_b64_tr_b16 a[166:167], v228 offset:12800\n\t" \
  "v_mfma_f32_16x16x32_bf16 v[68:71], a[212:215], v[84:87], v[68:71]\n\t" \
  "v_exp_f32 v1, v1\n\t" \
  "v_mfma_f32_16x16x32_bf16 v[68:71], a[208:211], v[80:83], v[68:71]\n\t" \
  "ds_read_b64_tr_b16 a[168:169], v229 offset:512\n\t" \
  "v_mfma_f32_16x16x32_bf16 v[72:75], a[224:227], v[80:83], v[184:187]\n\t" \
  "v_exp_f32 v2, v2\n\t" \
  "v_mfma_f32_16x16x32_bf16 v[72:75], a[228:231], v[84:87], v[72:75]\n\t" \
  "ds_read_b64_tr_b16 a[170:171], v229 offset:4608\n\t" \
  "v_mfma_f32_16x16x32_bf16 v[72:75], a[232:235], v[88:91], v[72:75]\n\t" \
  "ds_read_b64_tr_b16 a[172:173], v229 offset:8704\n\t" \
  "v_mfma_f32_16x16x32_bf16 v[72:75], a[236:239], v[92:95], v[72:75]\n\t" \
  "ds_read_b64_tr_b16 a[174:175], v229 offset:12800\n\t" \
  "buffer_load_dword v246, v247, s[16:19], 0 offen sc1\n\t" \
  "v_mfma_f32_16x16x32_bf16 v[76:79], a[252:255], v[92:95], v[184:187]\n\t" \
  "s_mov_b32 s96, 6144\n\t" \
  "v_exp_f32 v3, v3\n\t" \
  "v_mfma_f32_16x16x32_bf16 v[76:79], a[248:251], v[88:91], v[76:79]\n\t" \
  "v_exp_f32 v4, v4\n\t" \
  "v_mfma_f32_16x16x32_bf16 v[76:79], a[244:247], v[84:87], v[76:79]\n\t" \
  "ds_read_b64_tr_b16 a[176:177], v228 offset:768\n\t" \
  "v_mfma_f32_16x16x32_bf16 v[76:79], a[240:243], v[80:83], v[76:79]\n\t" \
  "ds_read_b64_tr_b16 a[178:179], v228 offset:4864\n\t" \
  "v_mfma_f32_16x16x32_bf16 v[96:99], a[192:195], v[112:115], v[188:191]\n\t" \
  "buffer_load_dwordx4 v232, s[72:75], s96 offen offset:1024 lds\n\t" \
  "v_mfma_f32_16x16x32_bf16 v[96:99], a[196:199], v[116:119], v[96:99]\n\t" \
  "ds_read_b64_tr_b16 a[180:181], v228 offset:8960\n\t" \
  "v_mfma_f32_16x16x32_bf16 v[96:99], a[200:203], v[120:123], v[96:99]\n\t" \
  "v_exp_f32 v5, v5\n\t" \
  "v_mfma_f32_16x16x32_bf16 v[96:99], a[204:207], v[124:127], v[96:99]\n\t" \
  "ds_read_b64_tr_b16 a[182:183], v228 offset:13056\n\t" \
  "v_mfma_f32_16x16x32_bf16 v[100:103], a[220:223], v[124:127], v[188:191]\n\t" \
  "ds_read_b64_tr_b16 a[184:185], v229 offset:768\n\t" \
  "v_mfma_f32_16x16x32_bf16 v[100:103], a[216:219], v[120:123], v[100:103]\n\t" \
  "v_exp_f32 v6, v6\n\t" \
  "v_mfma_f32_16x16x32_bf16 v[100:103], a[212:215], v[116:119], v[100:103]\n\t" \
  "ds_read_b64_tr_b16 a[186:187], v229 offset:4864\n\t" \
  "v_mfma_f32_16x16x32_bf16 v[100:103], a[208:211], v[112:115], v[100:103]\n\t" \
  "v_exp_f32 v7, v7\n\t" \
  "v_mfma_f32_16x16x32_bf16 v[104:107], a[224:227], v[112:115], v[188:191]\n\t" \
  "ds_read_b64_tr_b16 a[188:189], v229 offset:8960\n\t" \
  "v_mfma_f32_16x16x32_bf16 v[104:107], a[228:231], v[116:119], v[104:107]\n\t" \
  "ds_read_b64_tr_b16 a[190:191], v229 offset:13056\n\t" \
  "v_mfma_f32_16x16x32_bf16 v[104:107], a[232:235], v[120:123], v[104:107]\n\t" \
  "v_exp_f32 v8, v8\n\t" \
  "v_mfma_f32_16x16x32_bf16 v[104:107], a[236:239], v[124:127], v[104:107]\n\t" \
  "v_exp_f32 v9, v9\n\t" \
  "v_mfma_f32_16x16x32_bf16 v[108:111], a[252:255], v[124:127], v[188:191]\n\t" \
  "v_exp_f32 v10, v10\n\t" \
  "v_mfma_f32_16x16x32_bf16 v[108:111], a[248:251], v[120:123], v[108:111]\n\t" \
  "v_exp_f32 v11, v11\n\t" \
  "v_mfma_f32_16x16x32_bf16 v[108:111], a[244:247], v[116:119], v[108:111]\n\t" \
  "s_waitcnt vmcnt(5) lgkmcnt(0)\n\t" \
  "v_exp_f32 v12, v12\n\t" \
  "v_mfma_f32_16x16x32_bf16 v[108:111], a[240:243], v[112:115], v[108:111]\n\t" \
  "s_barrier\n\t" \
  STA("s_memrealtime s[94:95]\n\t") \
  STA("s_memtime s[92:93]\n\t") \
  "v_mfma_f32_16x16x32_bf16 a[0:3], a[128:131], v[128:131], a[0:3]\n\t" \
  "v_exp_f32 v13, v13\n\t" \
  "v_mfma_f32_16x16x32_bf16 a[0:3], a[132:135], v[132:135], a[0:3]\n\t" \
  "v_exp_f32 v14, v14\n\t" \
  "v_mfma_f32_16x16x32_bf16 a[4:7], a[136:139], v[128:131], a[4:7]\n\t" \
  "v_exp_f32 v15, v15\n\t" \
  "v_mfma_f32_16x16x32_bf16 a[4:7], a[140:143], v[132:135], a[4:7]\n\t" \
  "v_exp_f32 v32, v32\n\t" \
  "v_mfma_f32_16x16x32_bf16 a[8:11], a[144:147], v[128:131], a[8:11]\n\t" \
  "v_exp_f32 v33, v33\n\t" \
  "v_mfma_f32_16x16x32_bf16 a[8:11], a[148:151], v[132:135], a[8:11]\n\t" \
  "v_exp_f32 v34, v34\n\t" \
  "v_mfma_f32_16x16x32_bf16 a[12:15], a[152:155], v[128:131], a[12:15]\n\t" \
  "s_getreg_b32 s98, hwreg(HW_REG_IB_STS, 0, 4)\n\t" \
  "s_cmp_lg_u32 s98, 0\n\t" \
  "s_addc_u32 s54, s54, 0\n\t" \
  "v_mfma_f32_16x16x32_bf16 a[12:15], a[156:159], v[132:135], a[12:15]\n\t" \
  "v_exp_f32 v35, v35\n\t" \
  "v_mfma_f32_16x16x32_bf16 a[16:19], a[160:163], v[128:131], a[16:19]\n\t" \
  "s_getreg_b32 s98, hwreg(HW_REG_IB_STS, 0, 4)\n\t" \
  "s_cmp_lg_u32 s98, 0\n\t" \
  "s_addc_u32 s54, s54, 0\n\t" \
  "v_mfma_f32_16x16x32_bf16 a[16:19], a[164:167], v[132:135], a[16:19]\n\t" \
  "v_exp_f32 v36, v36\n\t" \
  "v_mfma_f32_16x16x32_bf16 a[20:23], a[168:171], v[128:131], a[20:23]\n\t" \
  "s_getreg_b32 s98, hwreg(HW_REG_IB_STS, 0, 4)\n\t" \
  "s_cmp_lg_u32 s98, 0\n\t" \
  "s_addc_u32 s54, s54, 0\n\t" \
  "v_mfma_f32_16x16x32_bf16 a[20:23], a[172:175], v[132:135], a[20:23]\n\t" \
  "v_exp_f32 v37, v37\n\t" \
  "v_mfma_f32_16x16x32_bf16 a[24:27], a[176:179], v[128:131], a[24:27]\n\t" \
  STA("v_writelane_b32 v255, s94, 36\n\t") \
  STA("v_writelane_b32 v255, s92, 37\n\t") \
  "s_getreg_b32 s98, hwreg(HW_REG_IB_STS, 0, 4)\n\t" \
  "s_cmp_lg_u32 s98, 0\n\t" \
  "s_addc_u32 s54, s54, 0\n\t" \
  "v_mfma_f32_16x16x32_bf16 a[24:27], a[180:183], v[132:135], a[24:27]\n\t" \
  "v_exp_f32 v38, v38\n\t" \
  "v_mfma_f32_16x16x32_bf16 a[28:31], a[184:187], v[128:131], a[28:31]\n\t" \
  "v_exp_f32 v39, v39\n\t" \
  "v_mfma_f32_16x16x32_bf16 a[28:31], a[188:191], v[132:135], a[28:31]\n\t" \
  "v_exp_f32 v40, v40\n\t" \
  "v_smfmac_f32_16x16x64_bf16 v[160:163], v[216:219], v[128:135], v208\n\t" \
  "ds_read_b128 a[192:195], v220 offset:0\n\t" \
  "v_cvt_pk_bf16_f32 v128, v0, v1\n\t" \
  "v_mfma_f32_16x16x32_bf16 a[32:35], a[128:131], v[136:139], a[32:35]\n\t" \
  "ds_read_b128 a[196:199], v221 offset:0\n\t" \
  "v_cvt_pk_bf16_f32 v129, v2, v3\n\t" \
  "v_mfma_f32_16x16x32_bf16 a[32:35], a[132:135], v[140:143], a[32:35]\n\t" \
  "s_add_u32 m0, s38, 114688\n\t" \
  "v_exp_f32 v41, v41\n\t" \
  "v_mfma_f32_16x16x32_bf16 a[36:39], a[136:139], v[136:139], a[36:39]\n\t" \
  "s_mov_b32 s96, 12288\n\t" \
  "v_exp_f32 v42, v42\n\t" \
  "v_mfma_f32_16x16x32_bf16 a[36:39], a[140:143], v[140:143], a[36:39]\n\t" \
  "ds_read_b128 a[200:203], v222 offset:0\n\t" \
  "v_cvt_pk_bf16_f32 v130, v4, v5\n\t" \
  "v_mfma_f32_16x16x32_bf16 a[40:43], a[144:147], v[136:139], a[40:43]\n\t" \
  "v_exp_f32 v43, v43\n\t" \
  "v_mfma_f32_16x16x32_bf16 a[40:43], a[148:151], v[140:143], a[40:43]\n\t" \
  "buffer_load_dwordx4 v232, s[72:75], s96 offen offset:2048 lds\n\t" \
  "v_mfma_f32_16x16x32_bf16 a[44:47], a[152:155], v[136:139], a[44:47]\n\t" \
  "ds_read_b128 a[204:207], v223 offset:0\n\t" \
  "v_cvt_pk_bf16_f32 v131, v6, v7\n\t" \
  "v_mfma_f32_16x16x32_bf16 a[44:47], a[156:159], v[140:143], a[44:47]\n\t" \
  "v_exp_f32 v44, v44\n\t" \
  "v_mfma_f32_16x16x32_bf16 a[48:51], a[160:163], v[136:139], a[48:51]\n\t" \
  "ds_read_b128 a[208:211], v220 offset:4096\n\t" \
  "v_cvt_pk_bf16_f32 v132, v8, v9\n\t" \
  "v_mfma_f32_16x16x32_bf16 a[48:51], a[164:167], v[140:143], a[48:51]\n\t" \
  STS("v_writelane_b32 v255, s54, 58\n\t") \
  "s_cmp_ge_u32 s54, 2\n\t" \
  "s_cselect_b32 s98, 0x4000, 0\n\t" \
  "s_or_b32 s98, s98, s36\n\t" \
  "v_mfma_f32_16x16x32_bf16 a[52:55], a[168:171], v[136:139], a[52:55]\n\t" \
  "s_cmp_ge_u32 s54, 3\n\t" \
  "s_cselect_b32 s54, 2.0, 0\n\t" \
  "s_or_b32 s98, s98, s54\n\t" \
  "v_mfma_f32_16x16x32_bf16 a[52:55], a[172:175], v[140:143], a[52:55]\n\t" \
  "ds_read_b128 a[212:215], v221 offset:4096\n\t" \
  "v_mov_b32 v209, s98\n\t" \
  "v_mfma_f32_16x16x32_bf16 a[56:59], a[176:179], v[136:139], a[56:59]\n\t" \
  "v_cvt_pk_bf16_f32 v133, v10, v11\n\t" \
  "v_bfrev_b32 v250, 1\n\t" \
  "v_mfma_f32_16x16x32_bf16 a[56:59], a[180:183], v[140:143], a[56:59]\n\t" \
  "s_mov_b32 s96, 18432\n\t" \
  "v_cvt_pk_bf16_f32 v134, v12, v13\n\t" \
  "v_writelane_b32 v250, s44, 0\n\t" \
  "v_mfma_f32_16x16x32_bf16 a[60:63], a[184:187], v[136:139], a[60:63]\n\t" \
  "ds_read_b128 a[216:219], v222 offset:4096\n\t" \
  "v_cvt_pk_bf16_f32 v135, v14, v15\n\t" \
  "v_mfma_f32_16x16x32_bf16 a[60:63], a[188:191], v[140:143], a[60:63]\n\t" \
  "buffer_store_dword v209, v250, s[16:19], 0 offen sc1\n\t" \
  "ds_read_b128 a[220:223], v223 offset:4096\n\t" \
  "v_smfmac_f32_16x16x64_bf16 v[164:167], v[216:219], v[136:143], v208\n\t" \
  "ds_read_b128 a[224:227], v220 offset:8192\n\t" \
  "v_cvt_pk_bf16_f32 v136, v32, v33\n\t" \
  "v_mfma_f32_16x16x32_bf16 a[64:67], a[128:131], v[144:147], a[64:67]\n\t" \
  "buffer_load_dwordx4 v232, s[72:75], s96 offen offset:3072 lds\n\t" \
  "v_mfma_f32_16x16x32_bf16 a[64:67], a[132:135], v[148:151], a[64:67]\n\t" \
  "v_exp_f32 v45, v45\n\t" \
  "v_mfma_f32_16x16x32_bf16 a[68:71], a[136:139], v[144:147], a[68:71]\n\t" \
  "v_exp_f32 v46, v46\n\t" \
  "v_mfma_f32_16x16x32_bf16 a[68:71], a[140:143], v[148:151], a[68:71]\n\t" \
  "v_exp_f32 v47, v47\n\t" \
  "v_mfma_f32_16x16x32_bf16 a[72:75], a[144:147], v[144:147], a[72:75]\n\t" \
  "s_add_u32 m0, s38, 65536\n\t" \
  "v_exp_f32 v64, v64\n\t" \
  "v_mfma_f32_16x16x32_bf16 a[72:75], a[148:151], v[148:151], a[72:75]\n\t" \
  "s_add_u32 s96, s38, 81920\n\t" \
  "ds_read_b128 a[228:231], v221 offset:8192\n\t" \
  "v_cvt_pk_bf16_f32 v137, v34, v35\n\t" \
  "v_mfma_f32_16x16x32_bf16 a[76:79], a[152:155], v[144:147], a[76:79]\n\t" \
  "v_exp_f32 v65, v65\n\t" \
  "v_mfma_f32_16x16x32_bf16 a[76:79], a[156:159], v[148:151], a[76:79]\n\t" \
  "v_exp_f32 v66, v66\n\t" \
  "v_mfma_f32_16x16x32_bf16 a[80:83], a[160:163], v[144:147], a[80:83]\n\t" \
  "buffer_load_dwordx4 v224, s[60:63], s96 offen offset:0 lds\n\t" \
  "v_mfma_f32_16x16x32_bf16 a[80:83], a[164:167], v[148:151], a[80:83]\n\t" \
  "ds_read_b128 a[232:235], v222 offset:8192\n\t" \
  "v_cvt_pk_bf16_f32 v138, v36, v37\n\t" \
  "v_mfma_f32_16x16x32_bf16 a[84:87], a[168:171], v[144:147], a[84:87]\n\t" \
  "v_exp_f32 v67, v67\n\t" \
  "v_mfma_f32_16x16x32_bf16 a[84:87], a[172:175], v[148:151], a[84:87]\n\t" \
  "buffer_load_dwordx4 v225, s[60:63], s96 offen offset:1024 lds\n\t" \
  "v_mfma_f32_16x16x32_bf16 a[88:91], a[176:179], v[144:147], a[88:91]\n\t" \
  "ds_read_b128 a[236:239], v223 offset:8192\n\t" \
  "v_cvt_pk_bf16_f32 v139, v38, v39\n\t" \
  "v_mfma_f32_16x16x32_bf16 a[88:91], a[180:183], v[148:151], a[88:91]\n\t" \
  "ds_read_b128 a[240:243], v220 offset:12288\n\t" \
  "v_cvt_pk_bf16_f32 v140, v40, v41\n\t" \
  "v_mfma_f32_16x16x32_bf16 a[92:95], a[184:187], v[144:147], a[92:95]\n\t" \
  "buffer_load_dwordx4 v226, s[60:63], s96 offen offset:2048 lds\n\t" \
  "v_mfma_f32_16x16x32_bf16 a[92:95], a[188:191], v[148:151], a[92:95]\n\t" \
  "v_exp_f32 v68, v68\n\t" \
  "v_smfmac_f32_16x16x64_bf16 v[168:171], v[216:219], v[144:151], v208\n\t" \
  "ds_read_b128 a[244:247], v221 offset:12288\n\t" \
  "v_cvt_pk_bf16_f32 v141, v42, v43\n\t" \
  "v_mfma_f32_16x16x32_bf16 a[96:99], a[128:131], v[152:155], a[96:99]\n\t" \
  "buffer_load_dwordx4 v227, s[60:63], s96 offen offset:3072 lds\n\t" \
  "v_mfma_f32_16x16x32_bf16 a[96:99], a[132:135], v[156:159], a[96:99]\n\t" \
  "s_add_u32 m0, s38, 81920\n\t" \
  "s_add_u32 s96, s38, 65536\n\t" \
  "v_cvt_pk_bf16_f32 v142, v44, v45\n\t" \
  "v_mfma_f32_16x16x32_bf16 a[100:103], a[136:139], v[152:155], a[100:103]\n\t" \
  "buffer_load_dwordx4 v230, s[64:67], s96 offen offset:0 lds\n\t" \
  "v_mfma_f32_16x16x32_bf16 a[100:103], a[140:143], v[156:159], a[100:103]\n\t" \
  "ds_read_b128 a[248:251], v222 offset:12288\n\t" \
  "v_cvt_pk_bf16_f32 v143, v46, v47\n\t" \
  "v_mfma_f32_16x16x32_bf16 a[104:107], a[144:147], v[152:155], a[104:107]\n\t" \
  "v_exp_f32 v69, v69\n\t" \
  "v_mfma_f32_16x16x32_bf16 a[104:107], a[148:151], v[156:159], a[104:107]\n\t" \
  "buffer_load_dwordx4 v231, s[64:67], s96 offen offset:1024 lds\n\t" \
  "v_mfma_f32_16x16x32_bf16 a[108:111], a[152:155], v[152:155], a[108:111]\n\t" \
  "ds_read_b128 a[252:255], v223 offset:12288\n\t" \
  "v_cvt_pk_bf16_f32 v144, v64, v65\n\t" \
  "v_mfma_f32_16x16x32_bf16 a[108:111], a[156:159], v[156:159], a[108:111]\n\t" \
  "v_exp_f32 v70, v70\n\t" \
  "v_mfma_f32_16x16x32_bf16 a[112:115], a[160:163], v[152:155], a[112:115]\n\t" \
  "buffer_load_dwordx4 v230, s[64:67], s96 offen offset:2048 lds\n\t" \
  "v_mfma_f32_16x16x32_bf16 a[112:115], a[164:167], v[156:159], a[112:115]\n\t" \
  "v_exp_f32 v71, v71\n\t" \
  "v_mfma_f32_16x16x32_bf16 a[116:119], a[168:171], v[152:155], a[116:119]\n\t" \
  "v_exp_f32 v72, v72\n\t" \
  "v_mfma_f32_16x16x32_bf16 a[116:119], a[172:175], v[156:159], a[116:119]\n\t" \
  "buffer_load_dwordx4 v231, s[64:67], s96 offen offset:3072 lds\n\t" \
  "v_mfma_f32_16x16x32_bf16 a[120:123], a[176:179], v[152:155], a[120:123]\n\t" \
  "v_exp_f32 v73, v73\n\t" \
  "v_mfma_f32_16x16x32_bf16 a[120:123], a[180:183], v[156:159], a[120:123]\n\t" \
  "v_exp_f32 v74, v74\n\t" \
  "v_mfma_f32_16x16x32_bf16 a[124:127], a[184:187], v[152:155], a[124:127]\n\t" \
  "v_exp_f32 v75, v75\n\t" \
  "v_mfma_f32_16x16x32_bf16 a[124:127], a[188:191], v[156:159], a[124:127]\n\t" \
  "v_exp_f32 v76, v76\n\t" \
  "v_smfmac_f32_16x16x64_bf16 v[172:175], v[216:219], v[152:159], v208\n\t" \
  "s_waitcnt lgkmcnt(12)\n\t" \
  "v_cvt_pk_bf16_f32 v145, v66, v67\n\t" \
  STA("s_memrealtime s[94:95]\n\t") \
  STA("s_memtime s[92:93]\n\t") \
  "v_mfma_f32_16x16x32_bf16 v[0:3], a[192:195], v[16:19], v[176:179]\n\t" \
  "s_lshr_b32 s78, s38, 6\n\t" \
  "ds_read_b64_tr_b16 a[128:129], v210 offset:0\n\t" \
  "v_cvt_pk_bf16_f32 v146, v68, v69\n\t" \
  "v_mfma_f32_16x16x32_bf16 v[0:3], a[196:199], v[20:23], v[0:3]\n\t" \
  "v_exp_f32 v77, v77\n\t" \
  "v_mfma_f32_16x16x32_bf16 v[0:3], a[200:203], v[24:27], v[0:3]\n\t" \
  "s_add_u32 m0, s38, 131072\n\t" \
  "ds_read_b64_tr_b16 a[130:131], v210 offset:4096\n\t" \
  "v_cvt_pk_bf16_f32 v147, v70, v71\n\t" \
  "v_mfma_f32_16x16x32_bf16 v[0:3], a[204:207], v[28:31], v[0:3]\n\t" \
  "s_waitcnt lgkmcnt(10)\n\t" \
  "v_exp_f32 v78, v78\n\t" \
  "v_mfma_f32_16x16x32_bf16 v[4:7], a[220:223], v[28:31], v[176:179]\n\t" \
  "s_mov_b32 s96, 28672\n\t" \
  "ds_read_b64_tr_b16 a[132:133], v210 offset:8192\n\t" \
  "v_cvt_pk_bf16_f32 v148, v72, v73\n\t" \
  "v_mfma_f32_16x16x32_bf16 v[4:7], a[216:219], v[24:27], v[4:7]\n\t" \
  "v_exp_f32 v79, v79\n\t" \
  "v_mfma_f32_16x16x32_bf16 v[4:7], a[212:215], v[20:23], v[4:7]\n\t" \
  "buffer_load_dwordx4 v232, s[72:75], s96 offen offset:0 lds\n\t" \
  "v_mfma_f32_16x16x32_bf16 v[4:7], a[208:211], v[16:19], v[4:7]\n\t" \
  "s_waitcnt lgkmcnt(7)\n\t" \
  "ds_read_b64_tr_b16 a[134:135], v210 offset:12288\n\t" \
  "v_cvt_pk_bf16_f32 v149, v74, v75\n\t" \
  "v_mfma_f32_16x16x32_bf16 v[8:11], a[224:227], v[16:19], v[176:179]\n\t" \
  "ds_read_b64_tr_b16 a[136:137], v211 offset:0\n\t" \
  "v_cvt_pk_bf16_f32 v150, v76, v77\n\t" \
  "v_mfma_f32_16x16x32_bf16 v[8:11], a[228:231], v[20:23], v[8:11]\n\t" \
  "ds_read_b64_tr_b16 a[138:139], v211 offset:4096\n\t" \
  "v_cvt_pk_bf16_f32 v151, v78, v79\n\t" \
  "v_mfma_f32_16x16x32_bf16 v[8:11], a[232:235], v[24:27], v[8:11]\n\t" \
  "v_exp_f32 v96, v96\n\t" \
  "v_mfma_f32_16x16x32_bf16 v[8:11], a[236:239], v[28:31], v[8:11]\n\t" \
  "s_waitcnt lgkmcnt(6)\n\t" \
  "v_exp_f32 v97, v97\n\t" \
  "v_mfma_f32_16x16x32_bf16 v[12:15], a[252:255], v[28:31], v[176:179]\n\t" \
  STA("v_writelane_b32 v255, s94, 38\n\t") \
  STA("v_writelane_b32 v255, s92, 39\n\t") \
  "ds_read_b64_tr_b16 a[140:141], v211 offset:8192\n\t" \
  "v_cvt_pk_bf16_f32 v152, v96, v97\n\t" \
  "v_mfma_f32_16x16x32_bf16 v[12:15], a[248:251], v[24:27], v[12:15]\n\t" \
  "v_exp_f32 v98, v98\n\t" \
  "v_mfma_f32_16x16x32_bf16 v[12:15], a[244:247], v[20:23], v[12:15]\n\t" \
  "ds_read_b64_tr_b16 a[142:143], v211 offset:12288\n\t" \
  "v_xor_b32 v234, s78, v244\n\t" \
  "v_mfma_f32_16x16x32_bf16 v[12:15], a[240:243], v[16:19], v[12:15]\n\t" \
  "v_exp_f32 v99, v99\n\t" \
  "v_mfma_f32_16x16x32_bf16 v[32:35], a[192:195], v[48:51], v[180:183]\n\t" \
  "s_mov_b32 s96, 34816\n\t" \
  "ds_read_b64_tr_b16 a[144:145], v210 offset:256\n\t" \
  "v_cvt_pk_bf16_f32 v153, v98, v99\n\t" \
  "v_mfma_f32_16x16x32_bf16 v[32:35], a[196:199], v[52:55], v[32:35]\n\t" \
  "ds_read_b64_tr_b16 a[146:147], v210 offset:4352\n\t" \
  "v_xor_b32 v235, 32, v234\n\t" \
  "v_mfma_f32_16x16x32_bf16 v[32:35], a[200:203], v[56:59], v[32:35]\n\t" \
  "v_exp_f32 v100, v100\n\t" \
  "v_mfma_f32_16x16x32_bf16 v[32:35], a[204:207], v[60:63], v[32:35]\n\t" \
  "ds_read_b64_tr_b16 a[148:149], v210 offset:8448\n\t" \
  "v_add_u32 v236, 65536, v234\n\t" \
  "v_mfma_f32_16x16x32_bf16 v[36:39], a[220:223], v[60:63], v[180:183]\n\t" \
  "buffer_load_dwordx4 v232, s[72:75], s96 offen offset:1024 lds\n\t" \
  "v_mfma_f32_16x16x32_bf16 v[36:39], a[216:219], v[56:59], v[36:39]\n\t" \
  "ds_read_b64_tr_b16 a[150:151], v210 offset:12544\n\t" \
  "v_add_u32 v237, 65536, v235\n\t" \
  "v_mfma_f32_16x16x32_bf16 v[36:39], a[212:215], v[52:55], v[36:39]\n\t" \
  "v_exp_f32 v101, v101\n\t" \
  "v_mfma_f32_16x16x32_bf16 v[36:39], a[208:211], v[48:51], v[36:39]\n\t" \
  "ds_read_b64_tr_b16 a[152:153], v211 offset:256\n\t" \
  "v_cvt_pk_bf16_f32 v154, v100, v101\n\t" \
  "v_mfma_f32_16x16x32_bf16 v[40:43], a[224:227], v[48:51], v[180:183]\n\t" \
  "v_exp_f32 v102, v102\n\t" \
  "v_mfma_f32_16x16x32_bf16 v[40:43], a[228:231], v[52:55], v[40:43]\n\t" \
  "ds_read_b64_tr_b16 a[154:155], v211 offset:4352\n\t" \
  "v_add_u32 v238, 131072, v234\n\t" \
  "v_mfma_f32_16x16x32_bf16 v[40:43], a[232:235], v[56:59], v[40:43]\n\t" \
  "v_exp_f32 v103, v103\n\t" \
  "v_mfma_f32_16x16x32_bf16 v[40:43], a[236:239], v[60:63], v[40:43]\n\t" \
  "ds_read_b64_tr_b16 a[156:157], v211 offset:8448\n\t" \
  "v_cvt_pk_bf16_f32 v155, v102, v103\n\t" \
  "v_mfma_f32_16x16x32_bf16 v[44:47], a[252:255], v[60:63], v[180:183]\n\t" \
  "v_exp_f32 v104, v104\n\t" \
  "v_mfma_f32_16x16x32_bf16 v[44:47], a[248:251], v[56:59], v[44:47]\n\t" \
  "ds_read_b64_tr_b16 a[158:159], v211 offset:12544\n\t" \
  "v_add_u32 v239, 131072, v235\n\t" \
  "v_mfma_f32_16x16x32_bf16 v[44:47], a[244:247], v[52:55], v[44:47]\n\t" \
  "s_mov_b32 s96, 40960\n\t" \
  "v_exp_f32 v105, v105\n\t" \
  "v_mfma_f32_16x16x32_bf16 v[44:47], a[240:243], v[48:51], v[44:47]\n\t" \
  "ds_read_b64_tr_b16 a[160:161], v210 offset:512\n\t" \
  "v_cvt_pk_bf16_f32 v156, v104, v105\n\t" \
  "v_mfma_f32_16x16x32_bf16 v[64:67], a[192:195], v[80:83], v[184:187]\n\t" \
  "ds_read_b64_tr_b16 a[162:163], v210 offset:4608\n\t" \
  "v_lshlrev_b32 v242, 4, v233\n\t" \
  "v_mfma_f32_16x16x32_bf16 v[64:67], a[196:199], v[84:87], v[64:67]\n\t" \
  "v_exp_f32 v106, v106\n\t" \
  "v_mfma_f32_16x16x32_bf16 v[64:67], a[200:203], v[88:91], v[64:67]\n\t" \
  "buffer_load_dwordx4 v232, s[72:75], s96 offen offset:2048 lds\n\t" \
  "v_mfma_f32_16x16x32_bf16 v[64:67], a[204:207], v[92:95], v[64:67]\n\t" \
  "ds_read_b64_tr_b16 a[164:165], v210 offset:8704\n\t" \
  "v_add_u32 v213, s38, v242\n\t" \
  "v_mfma_f32_16x16x32_bf16 v[68:71], a[220:223], v[92:95], v[184:187]\n\t" \
  "ds_read_b64_tr_b16 a[166:167], v210 offset:12800\n\t" \
  "v_add_u32 v215, 98304, v213\n\t" \
  "v_mfma_f32_16x16x32_bf16 v[68:71], a[216:219], v[88:91], v[68:71]\n\t" \
  "v_exp_f32 v107, v107\n\t" \
  "v_mfma_f32_16x16x32_bf16 v[68:71], a[212:215], v[84:87], v[68:71]\n\t" \
  "ds_read_b64_tr_b16 a[168:169], v211 offset:512\n\t" \
  "v_cvt_pk_bf16_f32 v157, v106, v107\n\t" \
  "v_mfma_f32_16x16x32_bf16 v[68:71], a[208:211], v[80:83], v[68:71]\n\t" \
  "v_exp_f32 v108, v108\n\t" \
  "v_mfma_f32_16x16x32_bf16 v[72:75], a[224:227], v[80:83], v[184:187]\n\t" \
  "ds_read_b64_tr_b16 a[170:171], v211 offset:4608\n\t" \
  "v_mov_b32 v243, s36\n\t" \
  "v_mfma_f32_16x16x32_bf16 v[72:75], a[228:231], v[84:87], v[72:75]\n\t" \
  "v_exp_f32 v109, v109\n\t" \
  "v_mfma_f32_16x16x32_bf16 v[72:75], a[232:235], v[88:91], v[72:75]\n\t" \
  "ds_read_b64_tr_b16 a[172:173], v211 offset:8704\n\t" \
  "v_cvt_pk_bf16_f32 v158, v108, v109\n\t" \
  "v_mfma_f32_16x16x32_bf16 v[72:75], a[236:239], v[92:95], v[72:75]\n\t" \
  "ds_read_b64_tr_b16 a[174:175], v211 offset:12800\n\t" \
  "v_lshlrev_b32 v254, 9, v233\n\t" \
  "v_mfma_f32_16x16x32_bf16 v[76:79], a[252:255], v[92:95], v[184:187]\n\t" \
  "s_mov_b32 s96, 47104\n\t" \
  "v_exp_f32 v110, v110\n\t" \
  "v_mfma_f32_16x16x32_bf16 v[76:79], a[248:251], v[88:91], v[76:79]\n\t" \
  "ds_read_b64_tr_b16 a[176:177], v210 offset:768\n\t" \
  "v_and_b32 v244, 0x38, v233\n\t" \
  "v_mfma_f32_16x16x32_bf16 v[76:79], a[244:247], v[84:87], v[76:79]\n\t" \
  "v_exp_f32 v111, v111\n\t" \
  "v_mfma_f32_16x16x32_bf16 v[76:79], a[240:243], v[80:83], v[76:79]\n\t" \
  "ds_read_b64_tr_b16 a[178:179], v210 offset:4864\n\t" \
  "v_cvt_pk_bf16_f32 v159, v110, v111\n\t" \
  "v_mfma_f32_16x16x32_bf16 v[96:99], a[192:195], v[112:115], v[188:191]\n\t" \
  "buffer_load_dwordx4 v232, s[72:75], s96 offen offset:3072 lds\n\t" \
  "v_mfma_f32_16x16x32_bf16 v[96:99], a[196:199], v[116:119], v[96:99]\n\t" \
  "ds_read_b64_tr_b16 a[180:181], v210 offset:8960\n\t" \
  "v_lshl_or_b32 v254, v244, 26, v254\n\t" \
  "v_mfma_f32_16x16x32_bf16 v[96:99], a[200:203], v[120:123], v[96:99]\n\t" \
  "s_brev_b32 s78, 1\n\t" \
  "ds_read_b64_tr_b16 a[182:183], v210 offset:13056\n\t" \
  "v_mov_b32 v244, s78\n\t" \
  "v_mfma_f32_16x16x32_bf16 v[96:99], a[204:207], v[124:127], v[96:99]\n\t" \
  "s_add_u32 s99, s52, 0\n\t" \
  "ds_read_b64_tr_b16 a[184:185], v211 offset:768\n\t" \
  "v_cmp_eq_u32 vcc, s39, v233\n\t" \
  "v_mfma_f32_16x16x32_bf16 v[100:103], a[220:223], v[124:127], v[188:191]\n\t" \
  "ds_read_b64_tr_b16 a[186:187], v211 offset:4864\n\t" \
  "v_add_u32 v245, s39, v233\n\t" \
  "v_mfma_f32_16x16x32_bf16 v[100:103], a[216:219], v[120:123], v[100:103]\n\t" \
  "ds_read_b64_tr_b16 a[188:189], v211 offset:8960\n\t" \
  "v_bfi_b32 v245, 7, v245, v233\n\t" \
  "v_mfma_f32_16x16x32_bf16 v[100:103], a[212:215], v[116:119], v[100:103]\n\t" \
  "buffer_load_dword v246, v212, s[24:27], s99 offen\n\t" \
  "v_mfma_f32_16x16x32_bf16 v[100:103], a[208:211], v[112:115], v[100:103]\n\t" \
  "ds_read_b64_tr_b16 a[190:191], v211 offset:13056\n\t" \
  "v_cndmask_b32 v254, v254, v244, vcc\n\t" \
  "v_mfma_f32_16x16x32_bf16 v[104:107], a[224:227], v[112:115], v[188:191]\n\t" \
  "v_exp_f32 v0, v0\n\t" \
  "v_mfma_f32_16x16x32_bf16 v[104:107], a[228:231], v[116:119], v[104:107]\n\t" \
  "v_exp_f32 v1, v1\n\t" \
  "v_mfma_f32_16x16x32_bf16 v[104:107], a[232:235], v[120:123], v[104:107]\n\t" \
  "v_exp_f32 v2, v2\n\t" \
  "v_mfma_f32_16x16x32_bf16 v[104:107], a[236:239], v[124:127], v[104:107]\n\t" \
  "v_exp_f32 v3, v3\n\t" \
  "v_mfma_f32_16x16x32_bf16 v[108:111], a[252:255], v[124:127], v[188:191]\n\t" \
  "v_exp_f32 v4, v4\n\t" \
  "v_mfma_f32_16x16x32_bf16 v[108:111], a[248:251], v[120:123], v[108:111]\n\t" \
  "v_exp_f32 v5, v5\n\t" \
  "v_mfma_f32_16x16x32_bf16 v[108:111], a[244:247], v[116:119], v[108:111]\n\t" \
  "s_waitcnt vmcnt(5) lgkmcnt(0)\n\t" \
  "v_exp_f32 v6, v6\n\t" \
  "v_mfma_f32_16x16x32_bf16 v[108:111], a[240:243], v[112:115], v[108:111]\n\t" \
  "s_barrier\n\t" \
  STA("s_memrealtime s[94:95]\n\t") \
  STA("s_memtime s[92:93]\n\t") \
  "v_mfma_f32_16x16x32_bf16 a[0:3], a[128:131], v[128:131], a[0:3]\n\t" \
  "v_exp_f32 v7, v7\n\t" \
  "v_mfma_f32_16x16x32_bf16 a[0:3], a[132:135], v[132:135], a[0:3]\n\t" \
  "v_exp_f32 v8, v8\n\t" \
  "v_mfma_f32_16x16x32_bf16 a[4:7], a[136:139], v[128:131], a[4:7]\n\t" \
  "v_exp_f32 v9, v9\n\t" \
  "v_mfma_f32_16x16x32_bf16 a[4:7], a[140:143], v[132:135], a[4:7]\n\t" \
  "v_exp_f32 v10, v10\n\t" \
  "v_mfma_f32_16x16x32_bf16 a[8:11], a[144:147], v[128:131], a[8:11]\n\t" \
  "v_exp_f32 v11, v11\n\t" \
  "v_mfma_f32_16x16x32_bf16 a[8:11], a[148:151], v[132:135], a[8:11]\n\t" \
  "v_exp_f32 v12, v12\n\t" \
  "v_mfma_f32_16x16x32_bf16 a[12:15], a[152:155], v[128:131], a[12:15]\n\t" \
  "v_exp_f32 v13, v13\n\t" \
  "v_mfma_f32_16x16x32_bf16 a[12:15], a[156:159], v[132:135], a[12:15]\n\t" \
  "v_exp_f32 v14, v14\n\t" \
  "v_mfma_f32_16x16x32_bf16 a[16:19], a[160:163], v[128:131], a[16:19]\n\t" \
  "v_exp_f32 v15, v15\n\t" \
  "v_mfma_f32_16x16x32_bf16 a[16:19], a[164:167], v[132:135], a[16:19]\n\t" \
  "v_exp_f32 v32, v32\n\t" \
  "v_mfma_f32_16x16x32_bf16 a[20:23], a[168:171], v[128:131], a[20:23]\n\t" \
  "v_exp_f32 v33, v33\n\t" \
  "v_mfma_f32_16x16x32_bf16 a[20:23], a[172:175], v[132:135], a[20:23]\n\t" \
  "v_exp_f32 v34, v34\n\t" \
  "v_mfma_f32_16x16x32_bf16 a[24:27], a[176:179], v[128:131], a[24:27]\n\t" \
  STA("v_writelane_b32 v255, s94, 40\n\t") \
  STA("v_writelane_b32 v255, s92, 41\n\t") \
  "v_exp_f32 v35, v35\n\t" \
  "v_mfma_f32_16x16x32_bf16 a[24:27], a[180:183], v[132:135], a[24:27]\n\t" \
  "v_exp_f32 v36, v36\n\t" \
  "v_mfma_f32_16x16x32_bf16 a[28:31], a[184:187], v[128:131], a[28:31]\n\t" \
  "v_exp_f32 v37, v37\n\t" \
  "v_mfma_f32_16x16x32_bf16 a[28:31], a[188:191], v[132:135], a[28:31]\n\t" \
  "v_exp_f32 v38, v38\n\t" \
  "v_smfmac_f32_16x16x64_bf16 v[160:163], v[216:219], v[128:135], v208\n\t" \
  "s_add_u32 m0, s38, 49152\n\t" \
  "ds_read_b128 a[192:195], v220 offset:16384\n\t" \
  "v_cvt_pk_bf16_f32 v128, v0, v1\n\t" \
  "v_mfma_f32_16x16x32_bf16 a[32:35], a[128:131], v[136:139], a[32:35]\n\t" \
  "s_add_u32 s96, s38, 98304\n\t" \
  "ds_read_b128 a[196:199], v221 offset:16384\n\t" \
  "v_cvt_pk_bf16_f32 v129, v2, v3\n\t" \
  "v_mfma_f32_16x16x32_bf16 a[32:35], a[132:135], v[140:143], a[32:35]\n\t" \
  "v_exp_f32 v39, v39\n\t" \
  "v_mfma_f32_16x16x32_bf16 a[36:39], a[136:139], v[136:139], a[36:39]\n\t" \
  "v_exp_f32 v40, v40\n\t" \
  "v_mfma_f32_16x16x32_bf16 a[36:39], a[140:143], v[140:143], a[36:39]\n\t" \
  "buffer_load_dwordx4 v224, s[60:63], s96 offen offset:0 lds\n\t" \
  "v_mfma_f32_16x16x32_bf16 a[40:43], a[144:147], v[136:139], a[40:43]\n\t" \
  "ds_read_b128 a[200:203], v222 offset:16384\n\t" \
  "v_cvt_pk_bf16_f32 v130, v4, v5\n\t" \
  "v_mfma_f32_16x16x32_bf16 a[40:43], a[148:151], v[140:143], a[40:43]\n\t" \
  "v_exp_f32 v41, v41\n\t" \
  "v_mfma_f32_16x16x32_bf16 a[44:47], a[152:155], v[136:139], a[44:47]\n\t" \
  "ds_read_b128 a[204:207], v223 offset:16384\n\t" \
  "v_cvt_pk_bf16_f32 v131, v6, v7\n\t" \
  "v_mfma_f32_16x16x32_bf16 a[44:47], a[156:159], v[140:143], a[44:47]\n\t" \
  "v_exp_f32 v42, v42\n\t" \
  "v_mfma_f32_16x16x32_bf16 a[48:51], a[160:163], v[136:139], a[48:51]\n\t" \
  "ds_read_b128 a[208:211], v220 offset:20480\n\t" \
  "v_cvt_pk_bf16_f32 v132, v8, v9\n\t" \
  "v_mfma_f32_16x16x32_bf16 a[48:51], a[164:167], v[140:143], a[48:51]\n\t" \
  "buffer_load_dwordx4 v225, s[60:63], s96 offen offset:1024 lds\n\t" \
  "v_mfma_f32_16x16x32_bf16 a[52:55], a[168:171], v[136:139], a[52:55]\n\t" \
  "v_exp_f32 v43, v43\n\t" \
  "v_mfma_f32_16x16x32_bf16 a[52:55], a[172:175], v[140:143], a[52:55]\n\t" \
  "ds_read_b128 a[212:215], v221 offset:20480\n\t" \
  "v_cvt_pk_bf16_f32 v133, v10, v11\n\t" \
  "v_mfma_f32_16x16x32_bf16 a[56:59], a[176:179], v[136:139], a[56:59]\n\t" \
  "v_exp_f32 v44, v44\n\t" \
  "v_mfma_f32_16x16x32_bf16 a[56:59], a[180:183], v[140:143], a[56:59]\n\t" \
  "v_exp_f32 v45, v45\n\t" \
  "v_mfma_f32_16x16x32_bf16 a[60:63], a[184:187], v[136:139], a[60:63]\n\t" \
  "buffer_load_dwordx4 v226, s[60:63], s96 offen offset:2048 lds\n\t" \
  "v_mfma_f32_16x16x32_bf16 a[60:63], a[188:191], v[140:143], a[60:63]\n\t" \
  "ds_read_b128 a[216:219], v222 offset:20480\n\t" \
  "v_cvt_pk_bf16_f32 v134, v12, v13\n\t" \
  "v_smfmac_f32_16x16x64_bf16 v[164:167], v[216:219], v[136:143], v208\n\t" \
  "ds_read_b128 a[220:223], v223 offset:20480\n\t" \
  "v_cvt_pk_bf16_f32 v135, v14, v15\n\t" \
  "v_mfma_f32_16x16x32_bf16 a[64:67], a[128:131], v[144:147], a[64:67]\n\t" \
  "v_exp_f32 v46, v46\n\t" \
  "v_mfma_f32_16x16x32_bf16 a[64:67], a[132:135], v[148:151], a[64:67]\n\t" \
  "v_exp_f32 v47, v47\n\t" \
  "v_mfma_f32_16x16x32_bf16 a[68:71], a[136:139], v[144:147], a[68:71]\n\t" \
  "ds_read_b128 a[224:227], v220 offset:24576\n\t" \
  "v_cvt_pk_bf16_f32 v136, v32, v33\n\t" \
  "v_mfma_f32_16x16x32_bf16 a[68:71], a[140:143], v[148:151], a[68:71]\n\t" \
  "buffer_load_dwordx4 v227, s[60:63], s96 offen offset:3072 lds\n\t" \
  "v_mfma_f32_16x16x32_bf16 a[72:75], a[144:147], v[144:147], a[72:75]\n\t" \
  "s_add_u32 m0, s38, 147456\n\t" \
  "v_exp_f32 v64, v64\n\t" \
  "v_mfma_f32_16x16x32_bf16 a[72:75], a[148:151], v[148:151], a[72:75]\n\t" \
  "s_add_u32 s96, s38, 81920\n\t" \
  "ds_read_b128 a[228:231], v221 offset:24576\n\t" \
  "v_cvt_pk_bf16_f32 v137, v34, v35\n\t" \
  "v_mfma_f32_16x16x32_bf16 a[76:79], a[152:155], v[144:147], a[76:79]\n\t" \
  "v_exp_f32 v65, v65\n\t" \
  "v_mfma_f32_16x16x32_bf16 a[76:79], a[156:159], v[148:151], a[76:79]\n\t" \
  "v_exp_f32 v66, v66\n\t" \
  "v_mfma_f32_16x16x32_bf16 a[80:83], a[160:163], v[144:147], a[80:83]\n\t" \
  "buffer_load_dwordx4 v230, s[64:67], s96 offen offset:0 lds\n\t" \
  "v_mfma_f32_16x16x32_bf16 a[80:83], a[164:167], v[148:151], a[80:83]\n\t" \
  "ds_read_b128 a[232:235], v222 offset:24576\n\t" \
  "v_cvt_pk_bf16_f32 v138, v36, v37\n\t" \
  "v_mfma_f32_16x16x32_bf16 a[84:87], a[168:171], v[144:147], a[84:87]\n\t" \
  "v_exp_f32 v67, v67\n\t" \
  "v_mfma_f32_16x16x32_bf16 a[84:87], a[172:175], v[148:151], a[84:87]\n\t" \
  "ds_read_b128 a[236:239], v223 offset:24576\n\t" \
  "v_cvt_pk_bf16_f32 v139, v38, v39\n\t" \
  "v_mfma_f32_16x16x32_bf16 a[88:91], a[176:179], v[144:147], a[88:91]\n\t" \
  "v_exp_f32 v68, v68\n\t" \
  "v_mfma_f32_16x16x32_bf16 a[88:91], a[180:183], v[148:151], a[88:91]\n\t" \
  "ds_read_b128 a[240:243], v220 offset:28672\n\t" \
  "v_cvt_pk_bf16_f32 v140, v40, v41\n\t" \
  "v_mfma_f32_16x16x32_bf16 a[92:95], a[184:187], v[144:147], a[92:95]\n\t" \
  "buffer_load_dwordx4 v231, s[64:67], s96 offen offset:1024 lds\n\t" \
  "v_mfma_f32_16x16x32_bf16 a[92:95], a[188:191], v[148:151], a[92:95]\n\t" \
  "v_exp_f32 v69, v69\n\t" \
  "v_smfmac_f32_16x16x64_bf16 v[168:171], v[216:219], v[144:151], v208\n\t" \
  "ds_read_b128 a[244:247], v221 offset:28672\n\t" \
  "v_cvt_pk_bf16_f32 v141, v42, v43\n\t" \
  "v_mfma_f32_16x16x32_bf16 a[96:99], a[128:131], v[152:155], a[96:99]\n\t" \
  "v_exp_f32 v70, v70\n\t" \
  "v_mfma_f32_16x16x32_bf16 a[96:99], a[132:135], v[156:159], a[96:99]\n\t" \
  "v_exp_f32 v71, v71\n\t" \
  "v_mfma_f32_16x16x32_bf16 a[100:103], a[136:139], v[152:155], a[100:103]\n\t" \
  "buffer_load_dwordx4 v230, s[64:67], s96 offen offset:2048 lds\n\t" \
  "v_mfma_f32_16x16x32_bf16 a[100:103], a[140:143], v[156:159], a[100:103]\n\t" \
  "ds_read_b128 a[248:251], v222 offset:28672\n\t" \
  "v_cvt_pk_bf16_f32 v142, v44, v45\n\t" \
  "v_mfma_f32_16x16x32_bf16 a[104:107], a[144:147], v[152:155], a[104:107]\n\t" \
  "v_exp_f32 v72, v72\n\t" \
  "v_mfma_f32_16x16x32_bf16 a[104:107], a[148:151], v[156:159], a[104:107]\n\t" \
  "ds_read_b128 a[252:255], v223 offset:28672\n\t" \
  "v_cvt_pk_bf16_f32 v143, v46, v47\n\t" \
  "v_mfma_f32_16x16x32_bf16 a[108:111], a[152:155], v[152:155], a[108:111]\n\t" \
  ".Lhs8:\n\ts_cbranch_execz .Lhs8n\n\t" \
  "v_exp_f32 v73, v73\n\t" \
  "v_mfma_f32_16x16x32_bf16 a[108:111], a[156:159], v[156:159], a[108:111]\n\t" \
  "v_exp_f32 v74, v74\n\t" \
  "v_mfma_f32_16x16x32_bf16 a[112:115], a[160:163], v[152:155], a[112:115]\n\t" \
  "buffer_load_dwordx4 v231, s[64:67], s96 offen offset:3072 lds\n\t" \
  "v_mfma_f32_16x16x32_bf16 a[112:115], a[164:167], v[156:159], a[112:115]\n\t" \
  "v_exp_f32 v75, v75\n\t" \
  "v_mfma_f32_16x16x32_bf16 a[116:119], a[168:171], v[152:155], a[116:119]\n\t" \
  "v_exp_f32 v76, v76\n\t" \
  "v_mfma_f32_16x16x32_bf16 a[116:119], a[172:175], v[156:159], a[116:119]\n\t" \
  "v_exp_f32 v77, v77\n\t" \
  "v_mfma_f32_16x16x32_bf16 a[120:123], a[176:179], v[152:155], a[120:123]\n\t" \
  "s_add_u32 s99, s52, 2048\n\t" \
  "v_exp_f32 v78, v78\n\t" \
  "v_mfma_f32_16x16x32_bf16 a[120:123], a[180:183], v[156:159], a[120:123]\n\t" \
  "v_exp_f32 v79, v79\n\t" \
  "v_mfma_f32_16x16x32_bf16 a[124:127], a[184:187], v[152:155], a[124:127]\n\t" \
  "v_exp_f32 v96, v96\n\t" \
  "v_mfma_f32_16x16x32_bf16 a[124:127], a[188:191], v[156:159], a[124:127]\n\t" \
  "buffer_load_dword v246, v212, s[24:27], s99 offen\n\t" \
  "v_smfmac_f32_16x16x64_bf16 v[172:175], v[216:219], v[152:159], v208\n\t" \
  "s_waitcnt lgkmcnt(12)\n\t" \
  "v_cvt_pk_bf16_f32 v144, v64, v65\n\t" \
  STA("s_memrealtime s[94:95]\n\t") \
  STA("s_memtime s[92:93]\n\t") \
  "v_mfma_f32_16x16x32_bf16 v[0:3], a[192:195], v[16:19], v[176:179]\n\t" \
  "ds_read_b64_tr_b16 a[128:129], v228 offset:0\n\t" \
  "v_cvt_pk_bf16_f32 v145, v66, v67\n\t" \
  "v_mfma_f32_16x16x32_bf16 v[0:3], a[196:199], v[20:23], v[0:3]\n\t" \
  "v_exp_f32 v97, v97\n\t" \
  "v_mfma_f32_16x16x32_bf16 v[0:3], a[200:203], v[24:27], v[0:3]\n\t" \
  "ds_read_b64_tr_b16 a[130:131], v228 offset:4096\n\t" \
  "v_cvt_pk_bf16_f32 v146, v68, v69\n\t" \
  "v_mfma_f32_16x16x32_bf16 v[0:3], a[204:207], v[28:31], v[0:3]\n\t" \
  "s_waitcnt lgkmcnt(10)\n\t" \
  "v_exp_f32 v98, v98\n\t" \
  "v_mfma_f32_16x16x32_bf16 v[4:7], a[220:223], v[28:31], v[176:179]\n\t" \
  "ds_read_b64_tr_b16 a[132:133], v228 offset:8192\n\t" \
  "v_cvt_pk_bf16_f32 v147, v70, v71\n\t" \
  "v_mfma_f32_16x16x32_bf16 v[4:7], a[216:219], v[24:27], v[4:7]\n\t" \
  "v_exp_f32 v99, v99\n\t" \
  "v_mfma_f32_16x16x32_bf16 v[4:7], a[212:215], v[20:23], v[4:7]\n\t" \
  "ds_read_b64_tr_b16 a[134:135], v228 offset:12288\n\t" \
  "v_cvt_pk_bf16_f32 v148, v72, v73\n\t" \
  "v_mfma_f32_16x16x32_bf16 v[4:7], a[208:211], v[16:19], v[4:7]\n\t" \
  "s_waitcnt lgkmcnt(8)\n\t" \
  "ds_read_b64_tr_b16 a[136:137], v229 offset:0\n\t" \
  "v_cvt_pk_bf16_f32 v149, v74, v75\n\t" \
  "v_mfma_f32_16x16x32_bf16 v[8:11], a[224:227], v[16:19], v[176:179]\n\t" \
  "v_exp_f32 v100, v100\n\t" \
  "v_mfma_f32_16x16x32_bf16 v[8:11], a[228:231], v[20:23], v[8:11]\n\t" \
  "s_add_u32 s99, s52, 4096\n\t" \
  "ds_read_b64_tr_b16 a[138:139], v229 offset:4096\n\t" \
  "v_cvt_pk_bf16_f32 v150, v76, v77\n\t" \
  "v_mfma_f32_16x16x32_bf16 v[8:11], a[232:235], v[24:27], v[8:11]\n\t" \
  "v_exp_f32 v101, v101\n\t" \
  "v_mfma_f32_16x16x32_bf16 v[8:11], a[236:239], v[28:31], v[8:11]\n\t" \
  "s_waitcnt lgkmcnt(6)\n\t" \
  "ds_read_b64_tr_b16 a[140:141], v229 offset:8192\n\t" \
  "v_cvt_pk_bf16_f32 v151, v78, v79\n\t" \
  "v_mfma_f32_16x16x32_bf16 v[12:15], a[252:255], v[28:31], v[176:179]\n\t" \
  STA("v_writelane_b32 v255, s94, 42\n\t") \
  STA("v_writelane_b32 v255, s92, 43\n\t") \
  "buffer_load_dword v246, v212, s[24:27], s99 offen\n\t" \
  "v_mfma_f32_16x16x32_bf16 v[12:15], a[248:251], v[24:27], v[12:15]\n\t" \
  "ds_read_b64_tr_b16 a[142:143], v229 offset:12288\n\t" \
  "v_cvt_pk_bf16_f32 v152, v96, v97\n\t" \
  "v_mfma_f32_16x16x32_bf16 v[12:15], a[244:247], v[20:23], v[12:15]\n\t" \
  "ds_read_b64_tr_b16 a[144:145], v228 offset:256\n\t" \
  "v_cvt_pk_bf16_f32 v153, v98, v99\n\t" \
  "v_mfma_f32_16x16x32_bf16 v[12:15], a[240:243], v[16:19], v[12:15]\n\t" \
  "v_exp_f32 v102, v102\n\t" \
  "v_mfma_f32_16x16x32_bf16 v[32:35], a[192:195], v[48:51], v[180:183]\n\t" \
  "ds_read_b64_tr_b16 a[146:147], v228 offset:4352\n\t" \
  "v_cvt_pk_bf16_f32 v154, v100, v101\n\t" \
  "v_mfma_f32_16x16x32_bf16 v[32:35], a[196:199], v[52:55], v[32:35]\n\t" \
  "v_exp_f32 v103, v103\n\t" \
  "v_mfma_f32_16x16x32_bf16 v[32:35], a[200:203], v[56:59], v[32:35]\n\t" \
  "ds_read_b64_tr_b16 a[148:149], v228 offset:8448\n\t" \
  "v_cvt_pk_bf16_f32 v155, v102, v103\n\t" \
  "v_mfma_f32_16x16x32_bf16 v[32:35], a[204:207], v[60:63], v[32:35]\n\t" \
  "v_exp_f32 v104, v104\n\t" \
  "v_mfma_f32_16x16x32_bf16 v[36:39], a[220:223], v[60:63], v[180:183]\n\t" \
  "ds_read_b64_tr_b16 a[150:151], v228 offset:12544\n\t" \
  "v_and_b32 v245, 31, v245\n\t" \
  "v_mfma_f32_16x16x32_bf16 v[36:39], a[216:219], v[56:59], v[36:39]\n\t" \
  "v_exp_f32 v105, v105\n\t" \
  "v_mfma_f32_16x16x32_bf16 v[36:39], a[212:215], v[52:55], v[36:39]\n\t" \
  "ds_read_b64_tr_b16 a[152:153], v229 offset:256\n\t" \
  "v_cvt_pk_bf16_f32 v156, v104, v105\n\t" \
  "v_mfma_f32_16x16x32_bf16 v[36:39], a[208:211], v[48:51], v[36:39]\n\t" \
  "v_exp_f32 v106, v106\n\t" \
  "v_mfma_f32_16x16x32_bf16 v[40:43], a[224:227], v[48:51], v[180:183]\n\t" \
  "ds_read_b64_tr_b16 a[154:155], v229 offset:4352\n\t" \
  "v_lshlrev_b32 v245, 2, v245\n\t" \
  "v_mfma_f32_16x16x32_bf16 v[40:43], a[228:231], v[52:55], v[40:43]\n\t" \
  "v_exp_f32 v107, v107\n\t" \
  "v_mfma_f32_16x16x32_bf16 v[40:43], a[232:235], v[56:59], v[40:43]\n\t" \
  "ds_read_b64_tr_b16 a[156:157], v229 offset:8448\n\t" \
  "v_cvt_pk_bf16_f32 v157, v106, v107\n\t" \
  "v_mfma_f32_16x16x32_bf16 v[40:43], a[236:239], v[60:63], v[40:43]\n\t" \
  "ds_read_b64_tr_b16 a[158:159], v229 offset:12544\n\t" \
  "v_writelane_b32 v244, 0, 0\n\t" \
  "v_mfma_f32_16x16x32_bf16 v[44:47], a[252:255], v[60:63], v[180:183]\n\t" \
  "v_exp_f32 v108, v108\n\t" \
  "v_mfma_f32_16x16x32_bf16 v[44:47], a[248:251], v[56:59], v[44:47]\n\t" \
  "v_exp_f32 v109, v109\n\t" \
  "v_mfma_f32_16x16x32_bf16 v[44:47], a[244:247], v[52:55], v[44:47]\n\t" \
  "ds_read_b64_tr_b16 a[160:161], v228 offset:512\n\t" \
  "v_cvt_pk_bf16_f32 v158, v108, v109\n\t" \
  "v_mfma_f32_16x16x32_bf16 v[44:47], a[240:243], v[48:51], v[44:47]\n\t" \
  "v_exp_f32 v110, v110\n\t" \
  "v_mfma_f32_16x16x32_bf16 v[64:67], a[192:195], v[80:83], v[184:187]\n\t" \
  "v_exp_f32 v111, v111\n\t" \
  "v_mfma_f32_16x16x32_bf16 v[64:67], a[196:199], v[84:87], v[64:67]\n\t" \
  "ds_read_b64_tr_b16 a[162:163], v228 offset:4608\n\t" \
  "v_cvt_pk_bf16_f32 v159, v110, v111\n\t" \
  "v_mfma_f32_16x16x32_bf16 v[64:67], a[200:203], v[88:91], v[64:67]\n\t" \
  "v_exp_f32 v0, v0\n\t" \
  "v_mfma_f32_16x16x32_bf16 v[64:67], a[204:207], v[92:95], v[64:67]\n\t" \
  "ds_read_b64_tr_b16 a[164:165], v228 offset:8704\n\t" \
  "v_mfma_f32_16x16x32_bf16 v[68:71], a[220:223], v[92:95], v[184:187]\n\t" \
  "v_exp_f32 v1, v1\n\t" \
  "v_mfma_f32_16x16x32_bf16 v[68:71], a[216:219], v[88:91], v[68:71]\n\t" \
  "ds_read_b64_tr_b16 a[166:167], v228 offset:12800\n\t" \
  "v_mfma_f32_16x16x32_bf16 v[68:71], a[212:215], v[84:87], v[68:71]\n\t" \
  "ds_read_b64_tr_b16 a[168:169], v229 offset:512\n\t" \
  "v_mfma_f32_16x16x32_bf16 v[68:71], a[208:211], v[80:83], v[68:71]\n\t" \
  "v_exp_f32 v2, v2\n\t" \
  "v_mfma_f32_16x16x32_bf16 v[72:75], a[224:227], v[80:83], v[184:187]\n\t" \
  "ds_read_b64_tr_b16 a[170:171], v229 offset:4608\n\t" \
  "v_mfma_f32_16x16x32_bf16 v[72:75], a[228:231], v[84:87], v[72:75]\n\t" \
  "v_exp_f32 v3, v3\n\t" \
  "v_mfma_f32_16x16x32_bf16 v[72:75], a[232:235], v[88:91], v[72:75]\n\t" \
  "ds_read_b64_tr_b16 a[172:173], v229 offset:8704\n\t" \
  "v_mfma_f32_16x16x32_bf16 v[72:75], a[236:239], v[92:95], v[72:75]\n\t" \
  "ds_read_b64_tr_b16 a[174:175], v229 offset:12800\n\t" \
  "v_mfma_f32_16x16x32_bf16 v[76:79], a[252:255], v[92:95], v[184:187]\n\t" \
  "v_exp_f32 v4, v4\n\t" \
  "v_mfma_f32_16x16x32_bf16 v[76:79], a[248:251], v[88:91], v[76:79]\n\t" \
  "ds_read_b64_tr_b16 a[176:177], v228 offset:768\n\t" \
  "v_mfma_f32_16x16x32_bf16 v[76:79], a[244:247], v[84:87], v[76:79]\n\t" \
  "v_exp_f32 v5, v5\n\t" \
  "v_mfma_f32_16x16x32_bf16 v[76:79], a[240:243], v[80:83], v[76:79]\n\t" \
  "ds_read_b64_tr_b16 a[178:179], v228 offset:4864\n\t" \
  "v_mfma_f32_16x16x32_bf16 v[96:99], a[192:195], v[112:115], v[188:191]\n\t" \
  "ds_read_b64_tr_b16 a[180:181], v228 offset:8960\n\t" \
  "v_mfma_f32_16x16x32_bf16 v[96:99], a[196:199], v[116:119], v[96:99]\n\t" \
  "v_exp_f32 v6, v6\n\t" \
  "v_mfma_f32_16x16x32_bf16 v[96:99], a[200:203], v[120:123], v[96:99]\n\t" \
  "ds_read_b64_tr_b16 a[182:183], v228 offset:13056\n\t" \
  "v_mfma_f32_16x16x32_bf16 v[96:99], a[204:207], v[124:127], v[96:99]\n\t" \
  "ds_read_b64_tr_b16 a[184:185], v229 offset:768\n\t" \
  "v_mfma_f32_16x16x32_bf16 v[100:103], a[220:223], v[124:127], v[188:191]\n\t" \
  "v_exp_f32 v7, v7\n\t" \
  "v_mfma_f32_16x16x32_bf16 v[100:103], a[216:219], v[120:123], v[100:103]\n\t" \
  "ds_read_b64_tr_b16 a[186:187], v229 offset:4864\n\t" \
  "v_mfma_f32_16x16x32_bf16 v[100:103], a[212:215], v[116:119], v[100:103]\n\t" \
  "v_exp_f32 v8, v8\n\t" \
  "v_mfma_f32_16x16x32_bf16 v[100:103], a[208:211], v[112:115], v[100:103]\n\t" \
  "ds_read_b64_tr_b16 a[188:189], v229 offset:8960\n\t" \
  "v_mfma_f32_16x16x32_bf16 v[104:107], a[224:227], v[112:115], v[188:191]\n\t" \
  "ds_read_b64_tr_b16 a[190:191], v229 offset:13056\n\t" \
  "v_mfma_f32_16x16x32_bf16 v[104:107], a[228:231], v[116:119], v[104:107]\n\t" \
  "v_exp_f32 v9, v9\n\t" \
  "v_mfma_f32_16x16x32_bf16 v[104:107], a[232:235], v[120:123], v[104:107]\n\t" \
  "v_exp_f32 v10, v10\n\t" \
  "v_mfma_f32_16x16x32_bf16 v[104:107], a[236:239], v[124:127], v[104:107]\n\t" \
  "v_exp_f32 v11, v11\n\t" \
  "v_mfma_f32_16x16x32_bf16 v[108:111], a[252:255], v[124:127], v[188:191]\n\t" \
  "v_exp_f32 v12, v12\n\t" \
  "v_mfma_f32_16x16x32_bf16 v[108:111], a[248:251], v[120:123], v[108:111]\n\t" \
  "v_exp_f32 v13, v13\n\t" \
  "v_mfma_f32_16x16x32_bf16 v[108:111], a[244:247], v[116:119], v[108:111]\n\t" \
  "s_waitcnt vmcnt(2) lgkmcnt(0)\n\t" \
  "v_exp_f32 v14, v14\n\t" \
  "v_mfma_f32_16x16x32_bf16 v[108:111], a[240:243], v[112:115], v[108:111]\n\t" \
  "s_barrier\n\t" \
  STA("s_memrealtime s[94:95]\n\t") \
  STA("s_memtime s[92:93]\n\t") \
  "v_mfma_f32_16x16x32_bf16 a[0:3], a[128:131], v[128:131], a[0:3]\n\t" \
  "v_exp_f32 v15, v15\n\t" \
  "v_mfma_f32_16x16x32_bf16 a[0:3], a[132:135], v[132:135], a[0:3]\n\t" \
  "v_exp_f32 v32, v32\n\t" \
  "v_mfma_f32_16x16x32_bf16 a[4:7], a[136:139], v[128:131], a[4:7]\n\t" \
  "v_exp_f32 v33, v33\n\t" \
  "v_mfma_f32_16x16x32_bf16 a[4:7], a[140:143], v[132:135], a[4:7]\n\t" \
  "v_exp_f32 v34, v34\n\t" \
  "v_mfma_f32_16x16x32_bf16 a[8:11], a[144:147], v[128:131], a[8:11]\n\t" \
  "v_exp_f32 v35, v35\n\t" \
  "v_mfma_f32_16x16x32_bf16 a[8:11], a[148:151], v[132:135], a[8:11]\n\t" \
  "v_exp_f32 v36, v36\n\t" \
  "v_mfma_f32_16x16x32_bf16 a[12:15], a[152:155], v[128:131], a[12:15]\n\t" \
  "v_exp_f32 v37, v37\n\t" \
  "v_mfma_f32_16x16x32_bf16 a[12:15], a[156:159], v[132:135], a[12:15]\n\t" \
  "v_exp_f32 v38, v38\n\t" \
  "v_mfma_f32_16x16x32_bf16 a[16:19], a[160:163], v[128:131], a[16:19]\n\t" \
  "v_exp_f32 v39, v39\n\t" \
  "v_mfma_f32_16x16x32_bf16 a[16:19], a[164:167], v[132:135], a[16:19]\n\t" \
  "v_exp_f32 v40, v40\n\t" \
  "v_mfma_f32_16x16x32_bf16 a[20:23], a[168:171], v[128:131], a[20:23]\n\t" \
  "v_exp_f32 v41, v41\n\t" \
  "v_mfma_f32_16x16x32_bf16 a[20:23], a[172:175], v[132:135], a[20:23]\n\t" \
  "v_exp_f32 v42, v42\n\t" \
  "v_mfma_f32_16x16x32_bf16 a[24:27], a[176:179], v[128:131], a[24:27]\n\t" \
  STA("v_writelane_b32 v255, s94, 44\n\t") \
  STA("v_writelane_b32 v255, s92, 45\n\t") \
  "v_exp_f32 v43, v43\n\t" \
  "v_mfma_f32_16x16x32_bf16 a[24:27], a[180:183], v[132:135], a[24:27]\n\t" \
  "v_exp_f32 v44, v44\n\t" \
  "v_mfma_f32_16x16x32_bf16 a[28:31], a[184:187], v[128:131], a[28:31]\n\t" \
  "v_exp_f32 v45, v45\n\t" \
  "v_mfma_f32_16x16x32_bf16 a[28:31], a[188:191], v[132:135], a[28:31]\n\t" \
  "v_exp_f32 v46, v46\n\t" \
  "v_smfmac_f32_16x16x64_bf16 v[160:163], v[216:219], v[128:135], v208\n\t" \
  "s_add_u32 m0, s38, 65536\n\t" \
  "ds_read_b128 a[192:195], v220 offset:0\n\t" \
  "v_cvt_pk_bf16_f32 v128, v0, v1\n\t" \
  "v_mfma_f32_16x16x32_bf16 a[32:35], a[128:131], v[136:139], a[32:35]\n\t" \
  "s_add_u32 s96, s38, 114688\n\t" \
  "ds_read_b128 a[196:199], v221 offset:0\n\t" \
  "v_cvt_pk_bf16_f32 v129, v2, v3\n\t" \
  "v_mfma_f32_16x16x32_bf16 a[32:35], a[132:135], v[140:143], a[32:35]\n\t" \
  "v_exp_f32 v47, v47\n\t" \
  "v_mfma_f32_16x16x32_bf16 a[36:39], a[136:139], v[136:139], a[36:39]\n\t" \
  "v_exp_f32 v64, v64\n\t" \
  "v_mfma_f32_16x16x32_bf16 a[36:39], a[140:143], v[140:143], a[36:39]\n\t" \
  "buffer_load_dwordx4 v224, s[60:63], s96 offen offset:0 lds\n\t" \
  "v_mfma_f32_16x16x32_bf16 a[40:43], a[144:147], v[136:139], a[40:43]\n\t" \
  "ds_read_b128 a[200:203], v222 offset:0\n\t" \
  "v_cvt_pk_bf16_f32 v130, v4, v5\n\t" \
  "v_mfma_f32_16x16x32_bf16 a[40:43], a[148:151], v[140:143], a[40:43]\n\t" \
  "v_exp_f32 v65, v65\n\t" \
  "v_mfma_f32_16x16x32_bf16 a[44:47], a[152:155], v[136:139], a[44:47]\n\t" \
  "ds_read_b128 a[204:207], v223 offset:0\n\t" \
  "v_cvt_pk_bf16_f32 v131, v6, v7\n\t" \
  "v_mfma_f32_16x16x32_bf16 a[44:47], a[156:159], v[140:143], a[44:47]\n\t" \
  "v_exp_f32 v66, v66\n\t" \
  "v_mfma_f32_16x16x32_bf16 a[48:51], a[160:163], v[136:139], a[48:51]\n\t" \
  "ds_read_b128 a[208:211], v220 offset:4096\n\t" \
  "v_cvt_pk_bf16_f32 v132, v8, v9\n\t" \
  "v_mfma_f32_16x16x32_bf16 a[48:51], a[164:167], v[140:143], a[48:51]\n\t" \
  "buffer_load_dwordx4 v225, s[60:63], s96 offen offset:1024 lds\n\t" \
  "v_mfma_f32_16x16x32_bf16 a[52:55], a[168:171], v[136:139], a[52:55]\n\t" \
  "v_exp_f32 v67, v67\n\t" \
  "v_mfma_f32_16x16x32_bf16 a[52:55], a[172:175], v[140:143], a[52:55]\n\t" \
  "ds_read_b128 a[212:215], v221 offset:4096\n\t" \
  "v_cvt_pk_bf16_f32 v133, v10, v11\n\t" \
  "v_mfma_f32_16x16x32_bf16 a[56:59], a[176:179], v[136:139], a[56:59]\n\t" \
  "v_exp_f32 v68, v68\n\t" \
  "v_mfma_f32_16x16x32_bf16 a[56:59], a[180:183], v[140:143], a[56:59]\n\t" \
  "v_exp_f32 v69, v69\n\t" \
  "v_mfma_f32_16x16x32_bf16 a[60:63], a[184:187], v[136:139], a[60:63]\n\t" \
  "buffer_load_dwordx4 v226, s[60:63], s96 offen offset:2048 lds\n\t" \
  "v_mfma_f32_16x16x32_bf16 a[60:63], a[188:191], v[140:143], a[60:63]\n\t" \
  "ds_read_b128 a[216:219], v222 offset:4096\n\t" \
  "v_cvt_pk_bf16_f32 v134, v12, v13\n\t" \
  "v_smfmac_f32_16x16x64_bf16 v[164:167], v[216:219], v[136:143], v208\n\t" \
  "ds_read_b128 v[192:195], v215 offset:32768\n\t" \
  "v_cvt_pk_bf16_f32 v135, v14, v15\n\t" \
  "v_mfma_f32_16x16x32_bf16 a[64:67], a[128:131], v[144:147], a[64:67]\n\t" \
  "ds_read_b128 a[220:223], v223 offset:4096\n\t" \
  "v_cvt_pk_bf16_f32 v136, v32, v33\n\t" \
  "v_mfma_f32_16x16x32_bf16 a[64:67], a[132:135], v[148:151], a[64:67]\n\t" \
  "v_exp_f32 v70, v70\n\t" \
  "v_mfma_f32_16x16x32_bf16 a[68:71], a[136:139], v[144:147], a[68:71]\n\t" \
  "ds_read_b128 a[224:227], v220 offset:8192\n\t" \
  "v_cvt_pk_bf16_f32 v137, v34, v35\n\t" \
  "v_mfma_f32_16x16x32_bf16 a[68:71], a[140:143], v[148:151], a[68:71]\n\t" \
  "buffer_load_dwordx4 v227, s[60:63], s96 offen offset:3072 lds\n\t" \
  "v_mfma_f32_16x16x32_bf16 a[72:75], a[144:147], v[144:147], a[72:75]\n\t" \
  "s_add_u32 m0, s38, 81920\n\t" \
  "v_exp_f32 v71, v71\n\t" \
  "v_mfma_f32_16x16x32_bf16 a[72:75], a[148:151], v[148:151], a[72:75]\n\t" \
  "s_add_u32 s96, s38, 98304\n\t" \
  "ds_read_b128 a[228:231], v221 offset:8192\n\t" \
  "v_cvt_pk_bf16_f32 v138, v36, v37\n\t" \
  "v_mfma_f32_16x16x32_bf16 a[76:79], a[152:155], v[144:147], a[76:79]\n\t" \
  "v_exp_f32 v72, v72\n\t" \
  "v_mfma_f32_16x16x32_bf16 a[76:79], a[156:159], v[148:151], a[76:79]\n\t" \
  "v_exp_f32 v73, v73\n\t" \
  "v_mfma_f32_16x16x32_bf16 a[80:83], a[160:163], v[144:147], a[80:83]\n\t" \
  "buffer_load_dwordx4 v230, s[64:67], s96 offen offset:0 lds\n\t" \
  "v_mfma_f32_16x16x32_bf16 a[80:83], a[164:167], v[148:151], a[80:83]\n\t" \
  "ds_read_b128 a[232:235], v222 offset:8192\n\t" \
  "v_cvt_pk_bf16_f32 v139, v38, v39\n\t" \
  "v_mfma_f32_16x16x32_bf16 a[84:87], a[168:171], v[144:147], a[84:87]\n\t" \
  "v_exp_f32 v74, v74\n\t" \
  "v_mfma_f32_16x16x32_bf16 a[84:87], a[172:175], v[148:151], a[84:87]\n\t" \
  "ds_read_b128 a[236:239], v223 offset:8192\n\t" \
  "v_cvt_pk_bf16_f32 v140, v40, v41\n\t" \
  "v_mfma_f32_16x16x32_bf16 a[88:91], a[176:179], v[144:147], a[88:91]\n\t" \
  "v_exp_f32 v75, v75\n\t" \
  "v_mfma_f32_16x16x32_bf16 a[88:91], a[180:183], v[148:151], a[88:91]\n\t" \
  "ds_read_b128 a[240:243], v220 offset:12288\n\t" \
  "v_cvt_pk_bf16_f32 v141, v42, v43\n\t" \
  "v_mfma_f32_16x16x32_bf16 a[92:95], a[184:187], v[144:147], a[92:95]\n\t" \
  "buffer_load_dwordx4 v231, s[64:67], s96 offen offset:1024 lds\n\t" \
  "v_mfma_f32_16x16x32_bf16 a[92:95], a[188:191], v[148:151], a[92:95]\n\t" \
  "v_exp_f32 v76, v76\n\t" \
  "v_smfmac_f32_16x16x64_bf16 v[168:171], v[216:219], v[144:151], v208\n\t" \
  "ds_read_b128 a[244:247], v221 offset:12288\n\t" \
  "v_cvt_pk_bf16_f32 v142, v44, v45\n\t" \
  "v_mfma_f32_16x16x32_bf16 a[96:99], a[128:131], v[152:155], a[96:99]\n\t" \
  "v_exp_f32 v77, v77\n\t" \
  "v_mfma_f32_16x16x32_bf16 a[96:99], a[132:135], v[156:159], a[96:99]\n\t" \
  "v_exp_f32 v78, v78\n\t" \
  "v_mfma_f32_16x16x32_bf16 a[100:103], a[136:139], v[152:155], a[100:103]\n\t" \
  "buffer_load_dwordx4 v230, s[64:67], s96 offen offset:2048 lds\n\t" \
  "v_mfma_f32_16x16x32_bf16 a[100:103], a[140:143], v[156:159], a[100:103]\n\t" \
  "ds_read_b128 a[248:251], v222 offset:12288\n\t" \
  "v_cvt_pk_bf16_f32 v143, v46, v47\n\t" \
  "v_mfma_f32_16x16x32_bf16 a[104:107], a[144:147], v[152:155], a[104:107]\n\t" \
  "v_exp_f32 v79, v79\n\t" \
  "v_mfma_f32_16x16x32_bf16 a[104:107], a[148:151], v[156:159], a[104:107]\n\t" \
  "ds_read_b128 a[252:255], v223 offset:12288\n\t" \
  "v_cvt_pk_bf16_f32 v144, v64, v65\n\t" \
  "v_mfma_f32_16x16x32_bf16 a[108:111], a[152:155], v[152:155], a[108:111]\n\t" \
  "v_exp_f32 v96, v96\n\t" \
  "v_mfma_f32_16x16x32_bf16 a[108:111], a[156:159], v[156:159], a[108:111]\n\t" \
  "v_exp_f32 v97, v97\n\t" \
  "v_mfma_f32_16x16x32_bf16 a[112:115], a[160:163], v[152:155], a[112:115]\n\t" \
  "buffer_load_dwordx4 v231, s[64:67], s96 offen offset:3072 lds\n\t" \
  "v_mfma_f32_16x16x32_bf16 a[112:115], a[164:167], v[156:159], a[112:115]\n\t" \
  "v_cvt_pk_bf16_f32 v145, v66, v67\n\t" \
  "v_cvt_pk_bf16_f32 v146, v68, v69\n\t" \
  "v_mfma_f32_16x16x32_bf16 a[116:119], a[168:171], v[152:155], a[116:119]\n\t" \
  "v_exp_f32 v98, v98\n\t" \
  "v_mfma_f32_16x16x32_bf16 a[116:119], a[172:175], v[156:159], a[116:119]\n\t" \
  "v_exp_f32 v99, v99\n\t" \
  "v_mfma_f32_16x16x32_bf16 a[120:123], a[176:179], v[152:155], a[120:123]\n\t" \
  "s_add_u32 s99, s52, 6144\n\t" \
  "v_cvt_pk_bf16_f32 v147, v70, v71\n\t" \
  "v_cvt_pk_bf16_f32 v148, v72, v73\n\t" \
  "v_mfma_f32_16x16x32_bf16 a[120:123], a[180:183], v[156:159], a[120:123]\n\t" \
  "v_exp_f32 v100, v100\n\t" \
  "v_mfma_f32_16x16x32_bf16 a[124:127], a[184:187], v[152:155], a[124:127]\n\t" \
  "v_exp_f32 v101, v101\n\t" \
  "v_mfma_f32_16x16x32_bf16 a[124:127], a[188:191], v[156:159], a[124:127]\n\t" \
  "buffer_load_dword v246, v212, s[24:27], s99 offen\n\t" \
  "v_smfmac_f32_16x16x64_bf16 v[172:175], v[216:219], v[152:159], v208\n\t" \
  "s_waitcnt lgkmcnt(13)\n\t" \
  "ds_read_b128 v[196:199], v215 offset:33792\n\t" \
  "v_cvt_pk_bf16_f32 v149, v74, v75\n\t" \
  STA("s_memrealtime s[94:95]\n\t") \
  STA("s_memtime s[92:93]\n\t") \
  "v_mfma_f32_16x16x32_bf16 v[0:3], a[192:195], v[16:19], v[176:179]\n\t" \
  "ds_read_b64_tr_b16 a[128:129], v210 offset:0\n\t" \
  "v_cvt_pk_bf16_f32 v150, v76, v77\n\t" \
  "v_mfma_f32_16x16x32_bf16 v[0:3], a[196:199], v[20:23], v[0:3]\n\t" \
  "v_exp_f32 v102, v102\n\t" \
  "v_mfma_f32_16x16x32_bf16 v[0:3], a[200:203], v[24:27], v[0:3]\n\t" \
  "ds_read_b64_tr_b16 a[130:131], v210 offset:4096\n\t" \
  "v_cvt_pk_bf16_f32 v151, v78, v79\n\t" \
  "v_mfma_f32_16x16x32_bf16 v[0:3], a[204:207], v[28:31], v[0:3]\n\t" \
  "s_waitcnt lgkmcnt(11)\n\t" \
  "v_exp_f32 v103, v103\n\t" \
  "v_mfma_f32_16x16x32_bf16 v[4:7], a[220:223], v[28:31], v[176:179]\n\t" \
  "ds_read_b64_tr_b16 a[132:133], v210 offset:8192\n\t" \
  "v_lshrrev_b32 v250, 1, v233\n\t" \
  "v_mfma_f32_16x16x32_bf16 v[4:7], a[216:219], v[24:27], v[4:7]\n\t" \
  "v_lshl_add_u32 v214, v233, 14, s41\n\t" \
  "v_lshlrev_b32 v250, 2, v250\n\t" \
  "v_mfma_f32_16x16x32_bf16 v[4:7], a[212:215], v[20:23], v[4:7]\n\t" \
  "ds_read_b64_tr_b16 a[134:135], v210 offset:12288\n\t" \
  "v_add_u32 v250, 8192, v250\n\t" \
  "v_mfma_f32_16x16x32_bf16 v[4:7], a[208:211], v[16:19], v[4:7]\n\t" \
  "s_waitcnt lgkmcnt(9)\n\t" \
  "ds_read_b64_tr_b16 a[136:137], v211 offset:0\n\t" \
  "v_min_u32 v214, .Lfz_z-.Lat_t1-4, v214\n\t" \
  "v_mfma_f32_16x16x32_bf16 v[8:11], a[224:227], v[16:19], v[176:179]\n\t" \
  "buffer_load_dword v209, v250, s[16:19], 0 offen sc1\n\t" \
  "v_mfma_f32_16x16x32_bf16 v[8:11], a[228:231], v[20:23], v[8:11]\n\t" \
  "s_getpc_b64 s[84:85]\n\t" \
  ".Lat_t1:\n\t" \
  "ds_read_b64_tr_b16 a[138:139], v211 offset:4096\n\t" \
  "v_cvt_pk_bf16_f32 v152, v96, v97\n\t" \
  "v_mfma_f32_16x16x32_bf16 v[8:11], a[232:235], v[24:27], v[8:11]\n\t" \
  "global_load_dword v246, v214, s[84:85]\n\t" \
  "v_mfma_f32_16x16x32_bf16 v[8:11], a[236:239], v[28:31], v[8:11]\n\t" \
  "s_waitcnt lgkmcnt(7)\n\t" \
  "ds_read_b64_tr_b16 a[140:141], v211 offset:8192\n\t" \
  "v_cvt_pk_bf16_f32 v153, v98, v99\n\t" \
  "v_mfma_f32_16x16x32_bf16 v[12:15], a[252:255], v[28:31], v[176:179]\n\t" \
  STA("v_writelane_b32 v255, s94, 46\n\t") \
  STA("v_writelane_b32 v255, s92, 47\n\t") \
  "v_exp_f32 v104, v104\n\t" \
  "v_mfma_f32_16x16x32_bf16 v[12:15], a[248:251], v[24:27], v[12:15]\n\t" \
  "ds_read_b64_tr_b16 a[142:143], v211 offset:12288\n\t" \
  "v_cvt_pk_bf16_f32 v154, v100, v101\n\t" \
  "v_mfma_f32_16x16x32_bf16 v[12:15], a[244:247], v[20:23], v[12:15]\n\t" \
  "ds_read_b64_tr_b16 a[144:145], v210 offset:256\n\t" \
  "v_cvt_pk_bf16_f32 v155, v102, v103\n\t" \
  "v_mfma_f32_16x16x32_bf16 v[12:15], a[240:243], v[16:19], v[12:15]\n\t" \
  "v_exp_f32 v105, v105\n\t" \
  "v_mfma_f32_16x16x32_bf16 v[32:35], a[192:195], v[48:51], v[180:183]\n\t" \
  "ds_read_b64_tr_b16 a[146:147], v210 offset:4352\n\t" \
  "v_cvt_pk_bf16_f32 v156, v104, v105\n\t" \
  "v_mfma_f32_16x16x32_bf16 v[32:35], a[196:199], v[52:55], v[32:35]\n\t" \
  "v_exp_f32 v106, v106\n\t" \
  "v_mfma_f32_16x16x32_bf16 v[32:35], a[200:203], v[56:59], v[32:35]\n\t" \
  "v_exp_f32 v107, v107\n\t" \
  "v_mfma_f32_16x16x32_bf16 v[32:35], a[204:207], v[60:63], v[32:35]\n\t" \
  "ds_read_b64_tr_b16 a[148:149], v210 offset:8448\n\t" \
  "v_cvt_pk_bf16_f32 v157, v106, v107\n\t" \
  "v_mfma_f32_16x16x32_bf16 v[36:39], a[220:223], v[60:63], v[180:183]\n\t" \
  "v_exp_f32 v108, v108\n\t" \
  "v_mfma_f32_16x16x32_bf16 v[36:39], a[216:219], v[56:59], v[36:39]\n\t" \
  "v_exp_f32 v109, v109\n\t" \
  "v_mfma_f32_16x16x32_bf16 v[36:39], a[212:215], v[52:55], v[36:39]\n\t" \
  "ds_read_b64_tr_b16 a[150:151], v210 offset:12544\n\t" \
  "v_cvt_pk_bf16_f32 v158, v108, v109\n\t" \
  "v_mfma_f32_16x16x32_bf16 v[36:39], a[208:211], v[48:51], v[36:39]\n\t" \
  "v_exp_f32 v110, v110\n\t" \
  "v_mfma_f32_16x16x32_bf16 v[40:43], a[224:227], v[48:51], v[180:183]\n\t" \
  "v_exp_f32 v111, v111\n\t" \
  "v_mfma_f32_16x16x32_bf16 v[40:43], a[228:231], v[52:55], v[40:43]\n\t" \
  "ds_read_b64_tr_b16 a[152:153], v211 offset:256\n\t" \
  "v_cvt_pk_bf16_f32 v159, v110, v111\n\t" \
  "v_mfma_f32_16x16x32_bf16 v[40:43], a[232:235], v[56:59], v[40:43]\n\t" \
  "v_exp_f32 v0, v0\n\t" \
  "v_mfma_f32_16x16x32_bf16 v[40:43], a[236:239], v[60:63], v[40:43]\n\t" \
  "v_exp_f32 v1, v1\n\t" \
  "v_mfma_f32_16x16x32_bf16 v[44:47], a[252:255], v[60:63], v[180:183]\n\t" \
  "v_exp_f32 v2, v2\n\t" \
  "v_mfma_f32_16x16x32_bf16 v[44:47], a[248:251], v[56:59], v[44:47]\n\t" \
  "v_exp_f32 v3, v3\n\t" \
  "v_mfma_f32_16x16x32_bf16 v[44:47], a[244:247], v[52:55], v[44:47]\n\t" \
  "ds_read_b64_tr_b16 a[154:155], v211 offset:4352\n\t" \
  "v_mfma_f32_16x16x32_bf16 v[44:47], a[240:243], v[48:51], v[44:47]\n\t" \
  "ds_read_b64_tr_b16 a[156:157], v211 offset:8448\n\t" \
  "v_mfma_f32_16x16x32_bf16 v[64:67], a[192:195], v[80:83], v[184:187]\n\t" \
  "v_exp_f32 v4, v4\n\t" \
  "v_mfma_f32_16x16x32_bf16 v[64:67], a[196:199], v[84:87], v[64:67]\n\t" \
  "v_exp_f32 v5, v5\n\t" \
  "v_mfma_f32_16x16x32_bf16 v[64:67], a[200:203], v[88:91], v[64:67]\n\t" \
  "ds_read_b64_tr_b16 a[158:159], v211 offset:12544\n\t" \
  "v_mfma_f32_16x16x32_bf16 v[64:67], a[204:207], v[92:95], v[64:67]\n\t" \
  "ds_read_b64_tr_b16 a[160:161], v210 offset:512\n\t" \
  "v_mfma_f32_16x16x32_bf16 v[68:71], a[220:223], v[92:95], v[184:187]\n\t" \
  "v_exp_f32 v6, v6\n\t" \
  "v_mfma_f32_16x16x32_bf16 v[68:71], a[216:219], v[88:91], v[68:71]\n\t" \
  "ds_read_b64_tr_b16 a[162:163], v210 offset:4608\n\t" \
  "v_mfma_f32_16x16x32_bf16 v[68:71], a[212:215], v[84:87], v[68:71]\n\t" \
  "v_exp_f32 v7, v7\n\t" \
  "v_mfma_f32_16x16x32_bf16 v[68:71], a[208:211], v[80:83], v[68:71]\n\t" \
  "ds_read_b64_tr_b16 a[164:165], v210 offset:8704\n\t" \
  "v_mfma_f32_16x16x32_bf16 v[72:75], a[224:227], v[80:83], v[184:187]\n\t" \
  "v_exp_f32 v8, v8\n\t" \
  "v_mfma_f32_16x16x32_bf16 v[72:75], a[228:231], v[84:87], v[72:75]\n\t" \
  "ds_read_b64_tr_b16 a[166:167], v210 offset:12800\n\t" \
  "v_mfma_f32_16x16x32_bf16 v[72:75], a[232:235], v[88:91], v[72:75]\n\t" \
  "v_exp_f32 v9, v9\n\t" \
  "v_mfma_f32_16x16x32_bf16 v[72:75], a[236:239], v[92:95], v[72:75]\n\t" \
  "ds_read_b64_tr_b16 a[168:169], v211 offset:512\n\t" \
  "v_mfma_f32_16x16x32_bf16 v[76:79], a[252:255], v[92:95], v[184:187]\n\t" \
  "v_exp_f32 v10, v10\n\t" \
  "v_mfma_f32_16x16x32_bf16 v[76:79], a[248:251], v[88:91], v[76:79]\n\t" \
  "ds_read_b64_tr_b16 a[170:171], v211 offset:4608\n\t" \
  "v_mfma_f32_16x16x32_bf16 v[76:79], a[244:247], v[84:87], v[76:79]\n\t" \
  "ds_read_b64_tr_b16 a[172:173], v211 offset:8704\n\t" \
  "v_mfma_f32_16x16x32_bf16 v[76:79], a[240:243], v[80:83], v[76:79]\n\t" \
  "v_exp_f32 v11, v11\n\t" \
  "v_mfma_f32_16x16x32_bf16 v[96:99], a[192:195], v[112:115], v[188:191]\n\t" \
  "ds_read_b64_tr_b16 a[174:175], v211 offset:12800\n\t" \
  "v_mfma_f32_16x16x32_bf16 v[96:99], a[196:199], v[116:119], v[96:99]\n\t" \
  "v_exp_f32 v12, v12\n\t" \
  "v_mfma_f32_16x16x32_bf16 v[96:99], a[200:203], v[120:123], v[96:99]\n\t" \
  "ds_read_b64_tr_b16 a[176:177], v210 offset:768\n\t" \
  "v_mfma_f32_16x16x32_bf16 v[96:99], a[204:207], v[124:127], v[96:99]\n\t" \
  "v_exp_f32 v13, v13\n\t" \
  "v_mfma_f32_16x16x32_bf16 v[100:103], a[220:223], v[124:127], v[188:191]\n\t" \
  "ds_read_b64_tr_b16 a[178:179], v210 offset:4864\n\t" \
  "v_mfma_f32_16x16x32_bf16 v[100:103], a[216:219], v[120:123], v[100:103]\n\t" \
  "v_exp_f32 v14, v14\n\t" \
  "v_mfma_f32_16x16x32_bf16 v[100:103], a[212:215], v[116:119], v[100:103]\n\t" \
  "ds_read_b64_tr_b16 a[180:181], v210 offset:8960\n\t" \
  "v_mfma_f32_16x16x32_bf16 v[100:103], a[208:211], v[112:115], v[100:103]\n\t" \
  "v_exp_f32 v15, v15\n\t" \
  "v_mfma_f32_16x16x32_bf16 v[104:107], a[224:227], v[112:115], v[188:191]\n\t" \
  "ds_read_b64_tr_b16 a[182:183], v210 offset:13056\n\t" \
  "v_mfma_f32_16x16x32_bf16 v[104:107], a[228:231], v[116:119], v[104:107]\n\t" \
  "ds_read_b64_tr_b16 a[184:185], v211 offset:768\n\t" \
  "v_mfma_f32_16x16x32_bf16 v[104:107], a[232:235], v[120:123], v[104:107]\n\t" \
  "v_exp_f32 v32, v32\n\t" \
  "v_mfma_f32_16x16x32_bf16 v[104:107], a[236:239], v[124:127], v[104:107]\n\t" \
  "ds_read_b64_tr_b16 a[186:187], v211 offset:4864\n\t" \
  "v_mfma_f32_16x16x32_bf16 v[108:111], a[252:255], v[124:127], v[188:191]\n\t" \
  "ds_read_b64_tr_b16 a[188:189], v211 offset:8960\n\t" \
  "v_exp_f32 v33, v33\n\t" \
  "v_mfma_f32_16x16x32_bf16 v[108:111], a[248:251], v[120:123], v[108:111]\n\t" \
  "ds_read_b64_tr_b16 a[190:191], v211 offset:13056\n\t" \
  "v_exp_f32 v34, v34\n\t" \
  "v_mfma_f32_16x16x32_bf16 v[108:111], a[244:247], v[116:119], v[108:111]\n\t" \
  "s_waitcnt vmcnt(3) lgkmcnt(0)\n\t" \
  "v_exp_f32 v35, v35\n\t" \
  "v_mfma_f32_16x16x32_bf16 v[108:111], a[240:243], v[112:115], v[108:111]\n\t" \
  "s_barrier\n\t" \
  STA("s_memrealtime s[94:95]\n\t") \
  STA("s_memtime s[92:93]\n\t") \
  "v_mfma_f32_16x16x32_bf16 a[0:3], a[128:131], v[128:131], a[0:3]\n\t" \
  "v_exp_f32 v36, v36\n\t" \
  "v_mfma_f32_16x16x32_bf16 a[0:3], a[132:135], v[132:135], a[0:3]\n\t" \
  "v_exp_f32 v37, v37\n\t" \
  "v_mfma_f32_16x16x32_bf16 a[4:7], a[136:139], v[128:131], a[4:7]\n\t" \
  "v_exp_f32 v38, v38\n\t" \
  "v_mfma_f32_16x16x32_bf16 a[4:7], a[140:143], v[132:135], a[4:7]\n\t" \
  "v_exp_f32 v39, v39\n\t" \
  "v_mfma_f32_16x16x32_bf16 a[8:11], a[144:147], v[128:131], a[8:11]\n\t" \
  "v_exp_f32 v40, v40\n\t" \
  "v_mfma_f32_16x16x32_bf16 a[8:11], a[148:151], v[132:135], a[8:11]\n\t" \
  "v_exp_f32 v41, v41\n\t" \
  "v_mfma_f32_16x16x32_bf16 a[12:15], a[152:155], v[128:131], a[12:15]\n\t" \
  "v_exp_f32 v42, v42\n\t" \
  "v_mfma_f32_16x16x32_bf16 a[12:15], a[156:159], v[132:135], a[12:15]\n\t" \
  "v_exp_f32 v43, v43\n\t" \
  "v_mfma_f32_16x16x32_bf16 a[16:19], a[160:163], v[128:131], a[16:19]\n\t" \
  "v_exp_f32 v44, v44\n\t" \
  "v_mfma_f32_16x16x32_bf16 a[16:19], a[164:167], v[132:135], a[16:19]\n\t" \
  "v_exp_f32 v45, v45\n\t" \
  "v_mfma_f32_16x16x32_bf16 a[20:23], a[168:171], v[128:131], a[20:23]\n\t" \
  "v_exp_f32 v46, v46\n\t" \
  "v_mfma_f32_16x16x32_bf16 a[20:23], a[172:175], v[132:135], a[20:23]\n\t" \
  "v_exp_f32 v47, v47\n\t" \
  "v_mfma_f32_16x16x32_bf16 a[24:27], a[176:179], v[128:131], a[24:27]\n\t" \
  STA("v_writelane_b32 v255, s94, 48\n\t") \
  STA("v_writelane_b32 v255, s92, 49\n\t") \
  "v_exp_f32 v64, v64\n\t" \
  "v_mfma_f32_16x16x32_bf16 a[24:27], a[180:183], v[132:135], a[24:27]\n\t" \
  "v_exp_f32 v65, v65\n\t" \
  "v_mfma_f32_16x16x32_bf16 a[28:31], a[184:187], v[128:131], a[28:31]\n\t" \
  "ds_read_b128 a[192:195], v220 offset:16384\n\t" \
  "v_exp_f32 v66, v66\n\t" \
  "v_mfma_f32_16x16x32_bf16 a[28:31], a[188:191], v[132:135], a[28:31]\n\t" \
  "v_exp_f32 v67, v67\n\t" \
  "v_smfmac_f32_16x16x64_bf16 v[160:163], v[216:219], v[128:135], v208\n\t" \
  "s_add_u32 m0, s38, 147456\n\t" \
  "ds_read_b128 v[200:203], v215 offset:34816\n\t" \
  "v_exp_f32 v68, v68\n\t" \
  "v_mfma_f32_16x16x32_bf16 a[32:35], a[128:131], v[136:139], a[32:35]\n\t" \
  "s_add_u32 s96, s38, 114688\n\t" \
  "ds_read_b128 a[196:199], v221 offset:16384\n\t" \
  "v_exp_f32 v69, v69\n\t" \
  "v_mfma_f32_16x16x32_bf16 a[32:35], a[132:135], v[140:143], a[32:35]\n\t" \
  "v_exp_f32 v70, v70\n\t" \
  "v_mfma_f32_16x16x32_bf16 a[36:39], a[136:139], v[136:139], a[36:39]\n\t" \
  "v_exp_f32 v71, v71\n\t" \
  "v_mfma_f32_16x16x32_bf16 a[36:39], a[140:143], v[140:143], a[36:39]\n\t" \
  "buffer_load_dwordx4 v230, s[64:67], s96 offen offset:0 lds\n\t" \
  "v_mfma_f32_16x16x32_bf16 a[40:43], a[144:147], v[136:139], a[40:43]\n\t" \
  "ds_read_b128 a[200:203], v222 offset:16384\n\t" \
  "v_exp_f32 v72, v72\n\t" \
  "v_mfma_f32_16x16x32_bf16 a[40:43], a[148:151], v[140:143], a[40:43]\n\t" \
  "v_exp_f32 v73, v73\n\t" \
  "v_mfma_f32_16x16x32_bf16 a[44:47], a[152:155], v[136:139], a[44:47]\n\t" \
  "v_exp_f32 v74, v74\n\t" \
  "v_mfma_f32_16x16x32_bf16 a[44:47], a[156:159], v[140:143], a[44:47]\n\t" \
  "buffer_load_dwordx4 v231, s[64:67], s96 offen offset:1024 lds\n\t" \
  "v_mfma_f32_16x16x32_bf16 a[48:51], a[160:163], v[136:139], a[48:51]\n\t" \
  "ds_read_b128 a[204:207], v223 offset:16384\n\t" \
  "v_exp_f32 v75, v75\n\t" \
  "v_mfma_f32_16x16x32_bf16 a[48:51], a[164:167], v[140:143], a[48:51]\n\t" \
  "v_exp_f32 v76, v76\n\t" \
  "v_mfma_f32_16x16x32_bf16 a[52:55], a[168:171], v[136:139], a[52:55]\n\t" \
  "v_exp_f32 v77, v77\n\t" \
  "v_mfma_f32_16x16x32_bf16 a[52:55], a[172:175], v[140:143], a[52:55]\n\t" \
  "buffer_load_dwordx4 v230, s[64:67], s96 offen offset:2048 lds\n\t" \
  "v_mfma_f32_16x16x32_bf16 a[56:59], a[176:179], v[136:139], a[56:59]\n\t" \
  "ds_read_b128 a[208:211], v220 offset:20480\n\t" \
  "v_cvt_pk_bf16_f32 v128, v0, v1\n\t" \
  "v_mfma_f32_16x16x32_bf16 a[56:59], a[180:183], v[140:143], a[56:59]\n\t" \
  "v_exp_f32 v78, v78\n\t" \
  "v_mfma_f32_16x16x32_bf16 a[60:63], a[184:187], v[136:139], a[60:63]\n\t" \
  "buffer_load_dwordx4 v231, s[64:67], s96 offen offset:3072 lds\n\t" \
  "v_mfma_f32_16x16x32_bf16 a[60:63], a[188:191], v[140:143], a[60:63]\n\t" \
  "s_add_u32 s96, s40, 0\n\t" \
  "ds_read_b128 a[212:215], v221 offset:20480\n\t" \
  "v_cvt_pk_bf16_f32 v129, v2, v3\n\t" \
  "v_smfmac_f32_16x16x64_bf16 v[164:167], v[216:219], v[136:143], v208\n\t" \
  "s_and_b32 s96, s96, 0x1c0000\n\t" \
  "s_add_u32 s96, s96, s51\n\t" \
  "s_add_u32 m0, s38, 0\n\t" \
  "v_mfma_f32_16x16x32_bf16 a[64:67], a[128:131], v[144:147], a[64:67]\n\t" \
  "ds_read_b128 a[216:219], v222 offset:20480\n\t" \
  "v_exp_f32 v79, v79\n\t" \
  "v_mfma_f32_16x16x32_bf16 a[64:67], a[132:135], v[148:151], a[64:67]\n\t" \
  "buffer_load_dwordx4 v240, s[24:27], s96 offen offset:0 lds\n\t" \
  "v_mfma_f32_16x16x32_bf16 a[68:71], a[136:139], v[144:147], a[68:71]\n\t" \
  "ds_read_b128 a[220:223], v223 offset:20480\n\t" \
  "v_exp_f32 v96, v96\n\t" \
  "v_mfma_f32_16x16x32_bf16 a[68:71], a[140:143], v[148:151], a[68:71]\n\t" \
  "ds_read_b128 a[224:227], v220 offset:24576\n\t" \
  "v_exp_f32 v97, v97\n\t" \
  "v_mfma_f32_16x16x32_bf16 a[72:75], a[144:147], v[144:147], a[72:75]\n\t" \
  "v_cvt_pk_bf16_f32 v130, v4, v5\n\t" \
  "v_exp_f32 v98, v98\n\t" \
  "v_mfma_f32_16x16x32_bf16 a[72:75], a[148:151], v[148:151], a[72:75]\n\t" \
  "buffer_load_dwordx4 v241, s[24:27], s96 offen offset:1024 lds\n\t" \
  "v_mfma_f32_16x16x32_bf16 a[76:79], a[152:155], v[144:147], a[76:79]\n\t" \
  "v_exp_f32 v99, v99\n\t" \
  "v_mfma_f32_16x16x32_bf16 a[76:79], a[156:159], v[148:151], a[76:79]\n\t" \
  "s_waitcnt vmcnt(7)\n\t" \
  "v_and_b32 v251, 0xbfffbfff, v209\n\t" \
  "v_cmp_ne_u32_e64 s[84:85], s36, v251\n\t" \
  "v_mfma_f32_16x16x32_bf16 a[80:83], a[160:163], v[144:147], a[80:83]\n\t" \
  "buffer_load_dwordx4 v248, s[24:27], s96 offen offset:2048 lds\n\t" \
  "v_mfma_f32_16x16x32_bf16 a[80:83], a[164:167], v[148:151], a[80:83]\n\t" \
  ".Lhs9:\n\ts_cbranch_execz .Lhs9n\n\t" \
  "ds_read_b128 a[228:231], v221 offset:24576\n\t" \
  "v_exp_f32 v100, v100\n\t" \
  "v_mfma_f32_16x16x32_bf16 a[84:87], a[168:171], v[144:147], a[84:87]\n\t" \
  "s_cmp_lg_u64 s[84:85], 0\n\t" \
  "s_cbranch_scc1 .Lat_v\n\t" \
  ".Lat_w:\n\t" \
  "v_mfma_f32_16x16x32_bf16 a[84:87], a[172:175], v[148:151], a[84:87]\n\t" \
  "v_and_b32 v251, 1, v233\n\t" \
  "v_lshlrev_b32 v251, 4, v251\n\t" \
  "v_mfma_f32_16x16x32_bf16 a[88:91], a[176:179], v[144:147], a[88:91]\n\t" \
  "buffer_load_dwordx4 v249, s[24:27], s96 offen offset:3072 lds\n\t" \
  "v_mfma_f32_16x16x32_bf16 a[88:91], a[180:183], v[148:151], a[88:91]\n\t" \
  "s_add_u32 s96, s40, 262144\n\t" \
  "v_lshrrev_b32 v251, v251, v209\n\t" \
  "v_and_b32 v251, 0x4000, v251\n\t" \
  "v_mfma_f32_16x16x32_bf16 a[92:95], a[184:187], v[144:147], a[92:95]\n\t" \
  "s_and_b32 s96, s96, 0x1c0000\n\t" \
  "s_add_u32 s96, s96, s51\n\t" \
  "s_add_u32 m0, s38, 49152\n\t" \
  "v_mfma_f32_16x16x32_bf16 a[92:95], a[188:191], v[148:151], a[92:95]\n\t" \
  "ds_read_b128 a[232:235], v222 offset:24576\n\t" \
  "v_cmp_ne_u32_e64 s[84:85], 0, v251\n\t" \
  STS("s_nop 4\n\t") \
  STS("v_writelane_b32 v255, s84, 60\n\t") \
  STS("v_writelane_b32 v255, s85, 61\n\t") \
  "v_smfmac_f32_16x16x64_bf16 v[168:171], v[216:219], v[144:151], v208\n\t" \
  "buffer_load_dwordx4 v240, s[24:27], s96 offen offset:0 lds\n\t" \
  "v_mfma_f32_16x16x32_bf16 a[96:99], a[128:131], v[152:155], a[96:99]\n\t" \
  "s_bcnt1_i32_b32 s98, s84\n\t" \
  "ds_read_b128 a[236:239], v223 offset:24576\n\t" \
  "s_bcnt1_i32_b32 s54, s85\n\t" \
  "v_mfma_f32_16x16x32_bf16 a[96:99], a[132:135], v[156:159], a[96:99]\n\t" \
  "s_cmp_gt_u32 s98, s54\n\t" \
  "s_cselect_b32 s55, 0x2000, 0\n\t" \
  "s_cselect_b32 s98, s84, s85\n\t" \
  "v_mfma_f32_16x16x32_bf16 a[100:103], a[136:139], v[152:155], a[100:103]\n\t" \
  "buffer_load_dwordx4 v241, s[24:27], s96 offen offset:1024 lds\n\t" \
  "v_mfma_f32_16x16x32_bf16 a[100:103], a[140:143], v[156:159], a[100:103]\n\t" \
  "s_cmp_lg_u32 s55, 0\n\t" \
  "s_cselect_b32 s84, s85, s84\n\t" \
  "s_lshr_b32 s85, s98, 16\n\t" \
  "v_mfma_f32_16x16x32_bf16 a[104:107], a[144:147], v[152:155], a[104:107]\n\t" \
  "s_bcnt1_i32_b32 s85, s85\n\t" \
  "s_lshr_b32 s84, s84, 16\n\t" \
  "s_bcnt1_i32_b32 s84, s84\n\t" \
  "v_mfma_f32_16x16x32_bf16 a[104:107], a[148:151], v[156:159], a[104:107]\n\t" \
  "s_add_u32 s84, s84, 7\n\t" \
  "ds_read_b128 a[240:243], v220 offset:28672\n\t" \
  "v_exp_f32 v101, v101\n\t" \
  "v_mfma_f32_16x16x32_bf16 a[108:111], a[152:155], v[152:155], a[108:111]\n\t" \
  "buffer_load_dwordx4 v248, s[24:27], s96 offen offset:2048 lds\n\t" \
  "v_cvt_pk_bf16_f32 v131, v6, v7\n\t" \
  "v_mfma_f32_16x16x32_bf16 a[108:111], a[156:159], v[156:159], a[108:111]\n\t" \
  "s_cmp_ge_u32 s85, s84\n\t" \
  "s_cselect_b32 s54, -1, 0\n\t" \
  "s_xor_b32 s98, s42, s55\n\t" \
  "v_mfma_f32_16x16x32_bf16 a[112:115], a[160:163], v[152:155], a[112:115]\n\t" \
  "s_and_b32 s98, s98, s54\n\t" \
  "ds_read_b128 a[244:247], v221 offset:28672\n\t" \
  "s_xor_b32 s45, s45, s98\n\t" \
  "v_cvt_pk_bf16_f32 v132, v8, v9\n\t" \
  "v_mfma_f32_16x16x32_bf16 a[112:115], a[164:167], v[156:159], a[112:115]\n\t" \
  "s_xor_b32 s46, s46, s98\n\t" \
  STS("v_writelane_b32 v255, s54, 57\n\t") \
  STS("v_writelane_b32 v255, s55, 59\n\t") \
  "s_xor_b32 s98, s43, s55\n\t" \
  "s_and_b32 s98, s98, s54\n\t" \
  "v_mfma_f32_16x16x32_bf16 a[116:119], a[168:171], v[152:155], a[116:119]\n\t" \
  "buffer_load_dwordx4 v249, s[24:27], s96 offen offset:3072 lds\n\t" \
  "v_exp_f32 v102, v102\n\t" \
  "v_mfma_f32_16x16x32_bf16 a[116:119], a[172:175], v[156:159], a[116:119]\n\t" \
  "s_xor_b32 s47, s47, s98\n\t" \
  "s_xor_b32 s48, s48, s55\n\t" \
  "ds_read_b128 a[248:251], v222 offset:28672\n\t" \
  "v_cvt_pk_bf16_f32 v133, v10, v11\n\t" \
  "v_mfma_f32_16x16x32_bf16 a[120:123], a[176:179], v[152:155], a[120:123]\n\t" \
  "s_xor_b32 s49, s49, s55\n\t" \
  "ds_read_b128 a[252:255], v223 offset:28672\n\t" \
  "v_cvt_pk_bf16_f32 v134, v12, v13\n\t" \
  "v_exp_f32 v103, v103\n\t" \
  "v_mfma_f32_16x16x32_bf16 a[120:123], a[180:183], v[156:159], a[120:123]\n\t" \
  "s_xor_b32 s98, s45, 0\n\t" \
  "v_cvt_pk_bf16_f32 v135, v14, v15\n\t" \
  "v_exp_f32 v104, v104\n\t" \
  "v_mfma_f32_16x16x32_bf16 a[124:127], a[184:187], v[152:155], a[124:127]\n\t" \
  "s_lshl_b32 s64, s39, 17\n\t" \
  "v_cvt_pk_bf16_f32 v136, v32, v33\n\t" \
  "v_exp_f32 v105, v105\n\t" \
  "v_mfma_f32_16x16x32_bf16 a[124:127], a[188:191], v[156:159], a[124:127]\n\t" \
  "s_bfe_u32 s70, s46, 0x20014\n\t" \
  "v_cvt_pk_bf16_f32 v137, v34, v35\n\t" \
  "v_exp_f32 v106, v106\n\t" \
  "v_smfmac_f32_16x16x64_bf16 v[172:175], v[216:219], v[152:159], v208\n\t" \
  "s_waitcnt lgkmcnt(12)\n\t" \
  "ds_read_b128 v[204:207], v215 offset:35840\n\t" \
  "v_exp_f32 v107, v107\n\t" \
  STA("s_memrealtime s[94:95]\n\t") \
  STA("s_memtime s[92:93]\n\t") \
  "v_mfma_f32_16x16x32_bf16 v[0:3], a[192:195], v[16:19], v[176:179]\n\t" \
  "ds_read_b64_tr_b16 a[128:129], v228 offset:0\n\t" \
  "ds_read_b64_tr_b16 a[130:131], v228 offset:4096\n\t" \
  "ds_read_b64_tr_b16 a[132:133], v228 offset:8192\n\t" \
  "ds_read_b64_tr_b16 a[134:135], v228 offset:12288\n\t" \
  "v_mfma_f32_16x16x32_bf16 v[0:3], a[196:199], v[20:23], v[0:3]\n\t" \
  "ds_read_b64_tr_b16 a[136:137], v229 offset:0\n\t" \
  "ds_read_b64_tr_b16 a[138:139], v229 offset:4096\n\t" \
  "ds_read_b64_tr_b16 a[140:141], v229 offset:8192\n\t" \
  "v_exp_f32 v108, v108\n\t" \
  "v_mfma_f32_16x16x32_bf16 v[0:3], a[200:203], v[24:27], v[0:3]\n\t" \
  "s_lshl_b32 s70, s70, 14\n\t" \
  "ds_read_b64_tr_b16 a[142:143], v229 offset:12288\n\t" \
  "v_exp_f32 v109, v109\n\t" \
  "v_mfma_f32_16x16x32_bf16 v[0:3], a[204:207], v[28:31], v[0:3]\n\t" \
  "s_waitcnt lgkmcnt(15)\n\t" \
  "ds_read_b64_tr_b16 a[144:145], v228 offset:256\n\t" \
  "v_exp_f32 v110, v110\n\t" \
  "v_cvt_pk_bf16_f32 v138, v36, v37\n\t" \
  "v_mfma_f32_16x16x32_bf16 v[4:7], a[220:223], v[28:31], v[176:179]\n\t" \
  "s_xor_b32 s67, s70, 49152\n\t" \
  "v_cvt_pk_bf16_f32 v139, v38, v39\n\t" \
  "v_cvt_pk_bf16_f32 v140, v40, v41\n\t" \
  "v_mfma_f32_16x16x32_bf16 v[4:7], a[216:219], v[24:27], v[4:7]\n\t" \
  "s_lshl1_add_u32 s60, s67, s46\n\t" \
  "ds_read_b64_tr_b16 a[146:147], v228 offset:4352\n\t" \
  "v_exp_f32 v111, v111\n\t" \
  "v_mfma_f32_16x16x32_bf16 v[4:7], a[212:215], v[20:23], v[4:7]\n\t" \
  "s_xor_b32 s68, s70, 32768\n\t" \
  "ds_read_b64_tr_b16 a[148:149], v228 offset:8448\n\t" \
  "v_cvt_pk_bf16_f32 v141, v42, v43\n\t" \
  "v_mfma_f32_16x16x32_bf16 v[4:7], a[208:211], v[16:19], v[4:7]\n\t" \
  "s_waitcnt lgkmcnt(15)\n\t" \
  "ds_read_b64_tr_b16 a[150:151], v228 offset:12544\n\t" \
  "v_exp_f32 v0, v0\n\t" \
  "v_mfma_f32_16x16x32_bf16 v[8:11], a[224:227], v[16:19], v[176:179]\n\t" \
  "s_lshl1_add_u32 s61, s68, s46\n\t" \
  "v_cvt_pk_bf16_f32 v142, v44, v45\n\t" \
  "v_cvt_pk_bf16_f32 v143, v46, v47\n\t" \
  "v_mfma_f32_16x16x32_bf16 v[8:11], a[228:231], v[20:23], v[8:11]\n\t" \
  "s_xor_b32 s69, s70, 16384\n\t" \
  "v_exp_f32 v1, v1\n\t" \
  "v_mfma_f32_16x16x32_bf16 v[8:11], a[232:235], v[24:27], v[8:11]\n\t" \
  "s_lshl1_add_u32 s62, s69, s46\n\t" \
  "ds_read_b64_tr_b16 a[152:153], v229 offset:256\n\t" \
  "v_cvt_pk_bf16_f32 v144, v64, v65\n\t" \
  "v_mfma_f32_16x16x32_bf16 v[8:11], a[236:239], v[28:31], v[8:11]\n\t" \
  "s_waitcnt lgkmcnt(14)\n\t" \
  "v_exp_f32 v2, v2\n\t" \
  "v_mfma_f32_16x16x32_bf16 v[12:15], a[252:255], v[28:31], v[176:179]\n\t" \
  STA("v_writelane_b32 v255, s94, 50\n\t") \
  STA("v_writelane_b32 v255, s92, 51\n\t") \
  "s_lshl1_add_u32 s63, s70, s46\n\t" \
  "v_exp_f32 v3, v3\n\t" \
  "v_mfma_f32_16x16x32_bf16 v[12:15], a[248:251], v[24:27], v[12:15]\n\t" \
  "s_barrier\n\t" \
  "v_mfma_f32_16x16x32_bf16 v[12:15], a[244:247], v[20:23], v[12:15]\n\t" \
  "s_mul_i32 s65, s39, 508\n\t" \
  "ds_read_b64_tr_b16 a[154:155], v229 offset:4352\n\t" \
  "v_cvt_pk_bf16_f32 v145, v66, v67\n\t" \
  "v_mfma_f32_16x16x32_bf16 v[12:15], a[240:243], v[16:19], v[12:15]\n\t" \
  "s_add_u32 s65, s65, s49\n\t" \
  "ds_read_b64_tr_b16 a[156:157], v229 offset:8448\n\t" \
  "v_cvt_pk_bf16_f32 v146, v68, v69\n\t" \
  "v_mfma_f32_16x16x32_bf16 v[32:35], a[192:195], v[48:51], v[180:183]\n\t" \
  "s_lshl_b32 s80, s39, 2\n\t" \
  "v_exp_f32 v4, v4\n\t" \
  "v_mfma_f32_16x16x32_bf16 v[32:35], a[196:199], v[52:55], v[32:35]\n\t" \
  "s_add_u32 s80, s80, s3\n\t" \
  "ds_read_b64_tr_b16 a[158:159], v229 offset:12544\n\t" \
  "v_cvt_pk_bf16_f32 v147, v70, v71\n\t" \
  "v_mfma_f32_16x16x32_bf16 v[32:35], a[200:203], v[56:59], v[32:35]\n\t" \
  "s_add_u32 s96, s40, 0\n\t" \
  "s_and_b32 s96, s96, 0x1c0000\n\t" \
  "s_add_u32 s96, s96, s77\n\t" \
  "v_mfma_f32_16x16x32_bf16 v[32:35], a[204:207], v[60:63], v[32:35]\n\t" \
  "s_add_u32 m0, s38, 98304\n\t" \
  "ds_read_b64_tr_b16 a[160:161], v228 offset:512\n\t" \
  "v_cvt_pk_bf16_f32 v148, v72, v73\n\t" \
  "v_mfma_f32_16x16x32_bf16 v[36:39], a[220:223], v[60:63], v[180:183]\n\t" \
  "buffer_load_dwordx4 v240, s[24:27], s96 offen offset:0 lds\n\t" \
  "v_mfma_f32_16x16x32_bf16 v[36:39], a[216:219], v[56:59], v[36:39]\n\t" \
  "s_lshl_b32 s80, s80, 6\n\t" \
  "v_exp_f32 v5, v5\n\t" \
  "v_mfma_f32_16x16x32_bf16 v[36:39], a[212:215], v[52:55], v[36:39]\n\t" \
  "s_add_u32 s76, s38, 147456\n\t" \
  "ds_read_b64_tr_b16 a[162:163], v228 offset:4608\n\t" \
  "v_cvt_pk_bf16_f32 v149, v74, v75\n\t" \
  "v_mfma_f32_16x16x32_bf16 v[36:39], a[208:211], v[48:51], v[36:39]\n\t" \
  "s_lshl_b32 s75, s80, 2\n\t" \
  "ds_read_b64_tr_b16 a[164:165], v228 offset:8704\n\t" \
  "v_exp_f32 v6, v6\n\t" \
  "v_mfma_f32_16x16x32_bf16 v[40:43], a[224:227], v[48:51], v[180:183]\n\t" \
  "buffer_load_dwordx4 v241, s[24:27], s96 offen offset:1024 lds\n\t" \
  "v_mfma_f32_16x16x32_bf16 v[40:43], a[228:231], v[52:55], v[40:43]\n\t" \
  "s_add_u32 s75, s75, 2228224\n\t" \
  "v_exp_f32 v7, v7\n\t" \
  "v_mfma_f32_16x16x32_bf16 v[40:43], a[232:235], v[56:59], v[40:43]\n\t" \
  "s_lshr_b32 s71, s67, 7\n\t" \
  "v_cvt_pk_bf16_f32 v150, v76, v77\n\t" \
  "v_cvt_pk_bf16_f32 v151, v78, v79\n\t" \
  "v_mfma_f32_16x16x32_bf16 v[40:43], a[236:239], v[60:63], v[40:43]\n\t" \
  "buffer_load_dwordx4 v248, s[24:27], s96 offen offset:2048 lds\n\t" \
  "v_mfma_f32_16x16x32_bf16 v[44:47], a[252:255], v[60:63], v[180:183]\n\t" \
  "s_add_u32 s71, s71, s48\n\t" \
  "v_exp_f32 v8, v8\n\t" \
  "v_mfma_f32_16x16x32_bf16 v[44:47], a[248:251], v[56:59], v[44:47]\n\t" \
  "s_lshr_b32 s72, s68, 7\n\t" \
  "ds_read_b64_tr_b16 a[166:167], v228 offset:12800\n\t" \
  "v_cvt_pk_bf16_f32 v152, v96, v97\n\t" \
  "v_mfma_f32_16x16x32_bf16 v[44:47], a[244:247], v[52:55], v[44:47]\n\t" \
  "buffer_load_dwordx4 v249, s[24:27], s96 offen offset:3072 lds\n\t" \
  "v_mfma_f32_16x16x32_bf16 v[44:47], a[240:243], v[48:51], v[44:47]\n\t" \
  "s_add_u32 s72, s72, s48\n\t" \
  "ds_read_b64_tr_b16 a[168:169], v229 offset:512\n\t" \
  "v_cvt_pk_bf16_f32 v153, v98, v99\n\t" \
  "v_mfma_f32_16x16x32_bf16 v[64:67], a[192:195], v[80:83], v[184:187]\n\t" \
  "s_add_u32 s96, s40, 524288\n\t" \
  "s_and_b32 s96, s96, 0x1c0000\n\t" \
  "s_add_u32 s96, s96, s51\n\t" \
  "v_mfma_f32_16x16x32_bf16 v[64:67], a[196:199], v[84:87], v[64:67]\n\t" \
  "s_add_u32 m0, s38, 131072\n\t" \
  "v_cvt_pk_bf16_f32 v154, v100, v101\n\t" \
  "v_exp_f32 v9, v9\n\t" \
  "v_mfma_f32_16x16x32_bf16 v[64:67], a[200:203], v[88:91], v[64:67]\n\t" \
  "s_waitcnt lgkmcnt(15)\n\t" \
  "buffer_load_dwordx4 v240, s[24:27], s96 offen offset:0 lds\n\t" \
  "v_mfma_f32_16x16x32_bf16 v[64:67], a[204:207], v[92:95], v[64:67]\n\t" \
  "s_lshr_b32 s66, s69, 7\n\t" \
  "ds_read_b64_tr_b16 a[170:171], v229 offset:4608\n\t" \
  "v_cvt_pk_bf16_f32 v155, v102, v103\n\t" \
  "v_mfma_f32_16x16x32_bf16 v[68:71], a[220:223], v[92:95], v[184:187]\n\t" \
  "s_add_u32 s66, s66, s48\n\t" \
  "v_exp_f32 v10, v10\n\t" \
  "v_mfma_f32_16x16x32_bf16 v[68:71], a[216:219], v[88:91], v[68:71]\n\t" \
  "buffer_load_dwordx4 v241, s[24:27], s96 offen offset:1024 lds\n\t" \
  "v_mfma_f32_16x16x32_bf16 v[68:71], a[212:215], v[84:87], v[68:71]\n\t" \
  "s_lshr_b32 s73, s70, 7\n\t" \
  "ds_read_b64_tr_b16 a[172:173], v229 offset:8704\n\t" \
  "v_cvt_pk_bf16_f32 v156, v104, v105\n\t" \
  "v_mfma_f32_16x16x32_bf16 v[68:71], a[208:211], v[80:83], v[68:71]\n\t" \
  "s_add_u32 s73, s73, s48\n\t" \
  "ds_read_b64_tr_b16 a[174:175], v229 offset:12800\n\t" \
  "v_exp_f32 v11, v11\n\t" \
  "v_mfma_f32_16x16x32_bf16 v[72:75], a[224:227], v[80:83], v[184:187]\n\t" \
  "buffer_load_dwordx4 v248, s[24:27], s96 offen offset:2048 lds\n\t" \
  "v_mfma_f32_16x16x32_bf16 v[72:75], a[228:231], v[84:87], v[72:75]\n\t" \
  "s_lshl3_add_u32 s67, s67, s47\n\t" \
  "v_exp_f32 v12, v12\n\t" \
  "v_mfma_f32_16x16x32_bf16 v[72:75], a[232:235], v[88:91], v[72:75]\n\t" \
  "s_lshl3_add_u32 s68, s68, s47\n\t" \
  "ds_read_b64_tr_b16 a[176:177], v228 offset:768\n\t" \
  "v_cvt_pk_bf16_f32 v157, v106, v107\n\t" \
  "v_mfma_f32_16x16x32_bf16 v[72:75], a[236:239], v[92:95], v[72:75]\n\t" \
  "s_add_u32 s68, s68, 4096\n\t" \
  "v_exp_f32 v13, v13\n\t" \
  "v_mfma_f32_16x16x32_bf16 v[76:79], a[252:255], v[92:95], v[184:187]\n\t" \
  "buffer_load_dwordx4 v249, s[24:27], s96 offen offset:3072 lds\n\t" \
  "v_mfma_f32_16x16x32_bf16 v[76:79], a[248:251], v[88:91], v[76:79]\n\t" \
  "s_add_u32 s96, s40, 262144\n\t" \
  "s_and_b32 s96, s96, 0x1c0000\n\t" \
  "s_add_u32 s96, s96, s77\n\t" \
  "v_mfma_f32_16x16x32_bf16 v[76:79], a[244:247], v[84:87], v[76:79]\n\t" \
  "s_add_u32 m0, s38, 65536\n\t" \
  "ds_read_b64_tr_b16 a[178:179], v228 offset:4864\n\t" \
  "v_cvt_pk_bf16_f32 v158, v108, v109\n\t" \
  "v_mfma_f32_16x16x32_bf16 v[76:79], a[240:243], v[80:83], v[76:79]\n\t" \
  "buffer_load_dwordx4 v240, s[24:27], s96 offen offset:0 lds\n\t" \
  "v_mfma_f32_16x16x32_bf16 v[96:99], a[192:195], v[112:115], v[188:191]\n\t" \
  "s_lshl3_add_u32 s69, s69, s47\n\t" \
  "ds_read_b64_tr_b16 a[180:181], v228 offset:8960\n\t" \
  "v_exp_f32 v14, v14\n\t" \
  "v_mfma_f32_16x16x32_bf16 v[96:99], a[196:199], v[116:119], v[96:99]\n\t" \
  "s_add_u32 s69, s69, 16384\n\t" \
  "ds_read_b64_tr_b16 a[182:183], v228 offset:13056\n\t" \
  "v_cvt_pk_bf16_f32 v159, v110, v111\n\t" \
  "v_mfma_f32_16x16x32_bf16 v[96:99], a[200:203], v[120:123], v[96:99]\n\t" \
  "buffer_load_dwordx4 v241, s[24:27], s96 offen offset:1024 lds\n\t" \
  "v_mfma_f32_16x16x32_bf16 v[96:99], a[204:207], v[124:127], v[96:99]\n\t" \
  "s_lshl3_add_u32 s70, s70, s47\n\t" \
  "ds_read_b64_tr_b16 a[184:185], v229 offset:768\n\t" \
  "v_exp_f32 v15, v15\n\t" \
  "v_mfma_f32_16x16x32_bf16 v[100:103], a[220:223], v[124:127], v[188:191]\n\t" \
  "v_exp_f32 v32, v32\n\t" \
  "v_mfma_f32_16x16x32_bf16 v[100:103], a[216:219], v[120:123], v[100:103]\n\t" \
  "v_exp_f32 v33, v33\n\t" \
  "v_mfma_f32_16x16x32_bf16 v[100:103], a[212:215], v[116:119], v[100:103]\n\t" \
  "buffer_load_dwordx4 v248, s[24:27], s96 offen offset:2048 lds\n\t" \
  "v_mfma_f32_16x16x32_bf16 v[100:103], a[208:211], v[112:115], v[100:103]\n\t" \
  "ds_read_b64_tr_b16 a[186:187], v229 offset:4864\n\t" \
  "v_cvt_pk_bf16_f32 v0, v0, v1\n\t" \
  "v_mfma_f32_16x16x32_bf16 v[104:107], a[224:227], v[112:115], v[188:191]\n\t" \
  "ds_read_b64_tr_b16 a[188:189], v229 offset:8960\n\t" \
  "v_exp_f32 v34, v34\n\t" \
  "v_mfma_f32_16x16x32_bf16 v[104:107], a[228:231], v[116:119], v[104:107]\n\t" \
  "buffer_load_dwordx4 v249, s[24:27], s96 offen offset:3072 lds\n\t" \
  "v_mfma_f32_16x16x32_bf16 v[104:107], a[232:235], v[120:123], v[104:107]\n\t" \
  "s_add_u32 s96, s40, 524288\n\t" \
  "ds_read_b64_tr_b16 a[190:191], v229 offset:13056\n\t" \
  "v_exp_f32 v35, v35\n\t" \
  "v_mfma_f32_16x16x32_bf16 v[104:107], a[236:239], v[124:127], v[104:107]\n\t" \
  "s_and_b32 s96, s96, 0x1c0000\n\t" \
  "v_exp_f32 v36, v36\n\t" \
  "v_mfma_f32_16x16x32_bf16 v[108:111], a[252:255], v[124:127], v[188:191]\n\t" \
  "s_add_u32 s96, s96, s77\n\t" \
  "v_exp_f32 v37, v37\n\t" \
  "v_mfma_f32_16x16x32_bf16 v[108:111], a[248:251], v[120:123], v[108:111]\n\t" \
  "s_add_u32 m0, s38, 81920\n\t" \
  "v_exp_f32 v38, v38\n\t" \
  "v_mfma_f32_16x16x32_bf16 v[108:111], a[244:247], v[116:119], v[108:111]\n\t" \
  "s_waitcnt vmcnt(8) lgkmcnt(0)\n\t" \
  "v_exp_f32 v39, v39\n\t" \
  "v_mfma_f32_16x16x32_bf16 v[108:111], a[240:243], v[112:115], v[108:111]\n\t" \
  "s_barrier\n\t" \
  STS("s_memrealtime s[94:95]\n\t") \
  STS("s_memtime s[92:93]\n\t") \
  "v_mfma_f32_16x16x32_bf16 a[0:3], a[128:131], v[128:131], a[0:3]\n\t" \
  "buffer_load_dwordx4 v240, s[24:27], s96 offen offset:0 lds\n\t" \
  "ds_read_b64_tr_b16 v[16:17], v210 offset:0\n\t" \
  "ds_read_b128 v[176:179], v215 offset:16384\n\t" \
  "ds_read_b128 v[180:183], v215 offset:17408\n\t" \
  "v_mfma_f32_16x16x32_bf16 a[0:3], a[132:135], v[132:135], a[0:3]\n\t" \
  "ds_read_b64_tr_b16 v[18:19], v210 offset:4096\n\t" \
  "ds_read_b64_tr_b16 v[20:21], v210 offset:8192\n\t" \
  "ds_read_b64_tr_b16 v[22:23], v210 offset:12288\n\t" \
  "ds_read_b64_tr_b16 v[24:25], v211 offset:0\n\t" \
  "v_mfma_f32_16x16x32_bf16 a[4:7], a[136:139], v[128:131], a[4:7]\n\t" \
  "ds_read_b64_tr_b16 v[26:27], v211 offset:4096\n\t" \
  "ds_read_b64_tr_b16 v[28:29], v211 offset:8192\n\t" \
  "ds_read_b64_tr_b16 v[30:31], v211 offset:12288\n\t" \
  "ds_read_b128 v[184:187], v215 offset:18432\n\t" \
  "v_mfma_f32_16x16x32_bf16 a[4:7], a[140:143], v[132:135], a[4:7]\n\t" \
  "buffer_load_dwordx4 v241, s[24:27], s96 offen offset:1024 lds\n\t" \
  "ds_read_b64_tr_b16 v[48:49], v210 offset:256\n\t" \
  "ds_read_b64_tr_b16 v[50:51], v210 offset:4352\n\t" \
  "ds_read_b64_tr_b16 v[52:53], v210 offset:8448\n\t" \
  "v_mfma_f32_16x16x32_bf16 a[8:11], a[144:147], v[128:131], a[8:11]\n\t" \
  "ds_read_b128 v[188:191], v215 offset:19456\n\t" \
  "ds_read_b64_tr_b16 v[54:55], v210 offset:12544\n\t" \
  "ds_read_b64_tr_b16 v[56:57], v211 offset:256\n\t" \
  "v_cvt_pk_bf16_f32 v1, v2, v3\n\t" \
  "v_mfma_f32_16x16x32_bf16 a[8:11], a[148:151], v[132:135], a[8:11]\n\t" \
  "ds_read_b64_tr_b16 v[58:59], v211 offset:4352\n\t" \
  "v_cvt_pk_bf16_f32 v2, v4, v5\n\t" \
  "v_mfma_f32_16x16x32_bf16 a[12:15], a[152:155], v[128:131], a[12:15]\n\t" \
  "buffer_load_dwordx4 v248, s[24:27], s96 offen offset:2048 lds\n\t" \
  "v_mfma_f32_16x16x32_bf16 a[12:15], a[156:159], v[132:135], a[12:15]\n\t" \
  "ds_read_b64_tr_b16 v[60:61], v211 offset:8448\n\t" \
  "v_exp_f32 v40, v40\n\t" \
  "v_mfma_f32_16x16x32_bf16 a[16:19], a[160:163], v[128:131], a[16:19]\n\t" \
  "v_exp_f32 v41, v41\n\t" \
  "v_mfma_f32_16x16x32_bf16 a[16:19], a[164:167], v[132:135], a[16:19]\n\t" \
  "buffer_load_dwordx4 v249, s[24:27], s96 offen offset:3072 lds\n\t" \
  "v_mfma_f32_16x16x32_bf16 a[20:23], a[168:171], v[128:131], a[20:23]\n\t" \
  "s_add_u32 s96, s40, 786432\n\t" \
  "ds_read_b64_tr_b16 v[62:63], v211 offset:12544\n\t" \
  "v_cvt_pk_bf16_f32 v3, v6, v7\n\t" \
  "v_mfma_f32_16x16x32_bf16 a[20:23], a[172:175], v[132:135], a[20:23]\n\t" \
  "s_and_b32 s96, s96, 0x1c0000\n\t" \
  "ds_read_b64_tr_b16 v[80:81], v210 offset:512\n\t" \
  "v_cvt_pk_bf16_f32 v4, v8, v9\n\t" \
  "v_mfma_f32_16x16x32_bf16 a[24:27], a[176:179], v[128:131], a[24:27]\n\t" \
  STS("v_writelane_b32 v255, s94, 20\n\t") \
  STS("v_writelane_b32 v255, s92, 21\n\t") \
  "s_add_u32 s96, s96, s51\n\t" \
  "v_cvt_pk_bf16_f32 v5, v10, v11\n\t" \
  "s_add_u32 m0, s38, 114688\n\t" \
  "v_mfma_f32_16x16x32_bf16 a[24:27], a[180:183], v[132:135], a[24:27]\n\t" \
  "v_exp_f32 v42, v42\n\t" \
  "v_mfma_f32_16x16x32_bf16 a[28:31], a[184:187], v[128:131], a[28:31]\n\t" \
  "s_waitcnt lgkmcnt(6)\n\t" \
  "buffer_load_dwordx4 v240, s[24:27], s96 offen offset:0 lds\n\t" \
  "v_mfma_f32_16x16x32_bf16 a[28:31], a[188:191], v[132:135], a[28:31]\n\t" \
  "ds_read_b64_tr_b16 v[82:83], v210 offset:4608\n\t" \
  "v_cvt_pk_bf16_f32 v6, v12, v13\n\t" \
  "v_smfmac_f32_16x16x64_bf16 v[160:163], v[216:219], v[128:135], v208\n\t" \
  "ds_read_b128 a[224:227], v213 offset:16384\n\t" \
  "v_cvt_pk_bf16_f32 v7, v14, v15\n\t" \
  "v_mfma_f32_16x16x32_bf16 a[32:35], a[128:131], v[136:139], a[32:35]\n\t" \
  "buffer_load_dwordx4 v241, s[24:27], s96 offen offset:1024 lds\n\t" \
  "v_mfma_f32_16x16x32_bf16 a[32:35], a[132:135], v[140:143], a[32:35]\n\t" \
  "v_exp_f32 v43, v43\n\t" \
  "v_mfma_f32_16x16x32_bf16 a[36:39], a[136:139], v[136:139], a[36:39]\n\t" \
  "v_exp_f32 v44, v44\n\t" \
  "v_mfma_f32_16x16x32_bf16 a[36:39], a[140:143], v[140:143], a[36:39]\n\t" \
  "buffer_load_dwordx4 v248, s[24:27], s96 offen offset:2048 lds\n\t" \
  "v_mfma_f32_16x16x32_bf16 a[40:43], a[144:147], v[136:139], a[40:43]\n\t" \
  "v_exp_f32 v45, v45\n\t" \
  "v_mfma_f32_16x16x32_bf16 a[40:43], a[148:151], v[140:143], a[40:43]\n\t" \
  "ds_read_b64_tr_b16 v[84:85], v210 offset:8704\n\t" \
  "v_cvt_pk_bf16_f32 v32, v32, v33\n\t" \
  "v_mfma_f32_16x16x32_bf16 a[44:47], a[152:155], v[136:139], a[44:47]\n\t" \
  "buffer_load_dwordx4 v249, s[24:27], s96 offen offset:3072 lds\n\t" \
  "v_mfma_f32_16x16x32_bf16 a[44:47], a[156:159], v[140:143], a[44:47]\n\t" \
  "ds_read_b64_tr_b16 v[86:87], v210 offset:12800\n\t" \
  "v_cvt_pk_bf16_f32 v33, v34, v35\n\t" \
  "v_mfma_f32_16x16x32_bf16 a[48:51], a[160:163], v[136:139], a[48:51]\n\t" \
  "ds_read_b64_tr_b16 v[88:89], v211 offset:512\n\t" \
  "v_exp_f32 v46, v46\n\t" \
  "v_mfma_f32_16x16x32_bf16 a[48:51], a[164:167], v[140:143], a[48:51]\n\t" \
  "ds_read_b64_tr_b16 v[90:91], v211 offset:4608\n\t" \
  "v_exp_f32 v47, v47\n\t" \
  "v_mfma_f32_16x16x32_bf16 a[52:55], a[168:171], v[136:139], a[52:55]\n\t" \
  "ds_read_b64_tr_b16 v[92:93], v211 offset:8704\n\t" \
  "v_cvt_pk_bf16_f32 v34, v36, v37\n\t" \
  "v_mfma_f32_16x16x32_bf16 a[52:55], a[172:175], v[140:143], a[52:55]\n\t" \
  "ds_read_b64_tr_b16 v[94:95], v211 offset:12800\n\t" \
  "v_cvt_pk_bf16_f32 v35, v38, v39\n\t" \
  "v_mfma_f32_16x16x32_bf16 a[56:59], a[176:179], v[136:139], a[56:59]\n\t" \
  "ds_read_b64_tr_b16 v[112:113], v210 offset:768\n\t" \
  "v_exp_f32 v64, v64\n\t" \
  "v_mfma_f32_16x16x32_bf16 a[56:59], a[180:183], v[140:143], a[56:59]\n\t" \
  "ds_read_b64_tr_b16 v[114:115], v210 offset:4864\n\t" \
  "v_cvt_pk_bf16_f32 v36, v40, v41\n\t" \
  "v_mfma_f32_16x16x32_bf16 a[60:63], a[184:187], v[136:139], a[60:63]\n\t" \
  "ds_read_b64_tr_b16 v[116:117], v210 offset:8960\n\t" \
  "v_exp_f32 v65, v65\n\t" \
  "v_mfma_f32_16x16x32_bf16 a[60:63], a[188:191], v[140:143], a[60:63]\n\t" \
  "ds_read_b64_tr_b16 v[118:119], v210 offset:13056\n\t" \
  "v_add_u32 v230, 0x10000, v242\n\t" \
  "v_smfmac_f32_16x16x64_bf16 v[164:167], v[216:219], v[136:143], v208\n\t" \
  "ds_read_b64_tr_b16 a[192:193], v234 offset:0\n\t" \
  "v_cvt_pk_bf16_f32 v37, v42, v43\n\t" \
  "v_mfma_f32_16x16x32_bf16 a[0:3], v[16:19], v[0:3], a[0:3]\n\t" \
  "ds_read_b64_tr_b16 v[120:121], v211 offset:768\n\t" \
  "v_exp_f32 v66, v66\n\t" \
  "v_mfma_f32_16x16x32_bf16 a[0:3], v[20:23], v[4:7], a[0:3]\n\t" \
  "ds_read_b64_tr_b16 v[122:123], v211 offset:4864\n\t" \
  "v_exp_f32 v67, v67\n\t" \
  "v_smfmac_f32_16x16x64_bf16 v[160:163], v[216:219], v[0:7], v208\n\t" \
  "ds_read_b64_tr_b16 v[124:125], v211 offset:8960\n\t" \
  "v_add_u32 v232, 0x20000, v242\n\t" \
  "v_mfma_f32_16x16x32_bf16 a[4:7], v[24:27], v[0:3], a[4:7]\n\t" \
  "v_exp_f32 v68, v68\n\t" \
  "v_mfma_f32_16x16x32_bf16 a[4:7], v[28:31], v[4:7], a[4:7]\n\t" \
  "s_waitcnt lgkmcnt(15)\n\t" \
  "ds_read_b64_tr_b16 v[126:127], v211 offset:13056\n\t" \
  "v_cvt_pk_bf16_f32 v38, v44, v45\n\t" \
  "v_mfma_f32_16x16x32_bf16 a[8:11], v[48:51], v[0:3], a[8:11]\n\t" \
  "v_add_f32 v160, v160, v161\n\t" \
  "v_exp_f32 v69, v69\n\t" \
  "v_mfma_f32_16x16x32_bf16 a[8:11], v[52:55], v[4:7], a[8:11]\n\t" \
  "v_rcp_f32 v224, v160\n\t" \
  "v_mfma_f32_16x16x32_bf16 a[12:15], v[56:59], v[0:3], a[12:15]\n\t" \
  "v_accvgpr_read_b32 v128, a0\n\t" \
  "v_accvgpr_read_b32 v129, a1\n\t" \
  "v_accvgpr_read_b32 v130, a2\n\t" \
  "v_accvgpr_read_b32 v131, a3\n\t" \
  "v_accvgpr_read_b32 v132, a4\n\t" \
  "v_accvgpr_read_b32 v133, a5\n\t" \
  "v_accvgpr_read_b32 v134, a6\n\t" \
  "v_accvgpr_read_b32 v135, a7\n\t" \
  "v_pk_mul_f32 v[128:129], v[128:129], v[224:225] op_sel_hi:[1,0]\n\t" \
  "v_pk_mul_f32 v[130:131], v[130:131], v[224:225] op_sel_hi:[1,0]\n\t" \
  "v_pk_mul_f32 v[132:133], v[132:133], v[224:225] op_sel_hi:[1,0]\n\t" \
  "v_pk_mul_f32 v[134:135], v[134:135], v[224:225] op_sel_hi:[1,0]\n\t" \
  "v_cvt_pk_bf16_f32 v128, v128, v129\n\t" \
  "v_cvt_pk_bf16_f32 v129, v130, v131\n\t" \
  "v_cvt_pk_bf16_f32 v130, v132, v133\n\t" \
  "v_cvt_pk_bf16_f32 v131, v134, v135\n\t" \
  "buffer_store_dwordx4 v[128:131], v242, s[16:19], s98 offen offset:0 sc1\n\t" \
  "v_mfma_f32_16x16x32_bf16 a[12:15], v[60:63], v[4:7], a[12:15]\n\t" \
  "s_waitcnt lgkmcnt(9)\n\t" \
  "v_cmp_lt_f32 vcc, 0x5f800000, v160\n\t" \
  "v_exp_f32 v70, v70\n\t" \
  "v_mfma_f32_16x16x32_bf16 a[16:19], v[80:83], v[0:3], a[16:19]\n\t" \
  "s_cmp_lg_u64 vcc, 0\n\t" \
  "s_cselect_b32 s89, s78, s89\n\t" \
  "v_or_b32 v254, s89, v254\n\t" \
  "v_mfma_f32_16x16x32_bf16 a[16:19], v[84:87], v[4:7], a[16:19]\n\t" \
  "ds_read_b64_tr_b16 a[194:195], v234 offset:4096\n\t" \
  "v_cvt_pk_bf16_f32 v39, v46, v47\n\t" \
  "v_mfma_f32_16x16x32_bf16 a[20:23], v[88:91], v[0:3], a[20:23]\n\t" \
  "v_accvgpr_read_b32 v136, a8\n\t" \
  "v_accvgpr_read_b32 v137, a9\n\t" \
  "v_accvgpr_read_b32 v138, a10\n\t" \
  "v_accvgpr_read_b32 v139, a11\n\t" \
  "v_accvgpr_read_b32 v140, a12\n\t" \
  "v_accvgpr_read_b32 v141, a13\n\t" \
  "v_accvgpr_read_b32 v142, a14\n\t" \
  "v_accvgpr_read_b32 v143, a15\n\t" \
  "v_pk_mul_f32 v[136:137], v[136:137], v[224:225] op_sel_hi:[1,0]\n\t" \
  "v_pk_mul_f32 v[138:139], v[138:139], v[224:225] op_sel_hi:[1,0]\n\t" \
  "v_pk_mul_f32 v[140:141], v[140:141], v[224:225] op_sel_hi:[1,0]\n\t" \
  "v_pk_mul_f32 v[142:143], v[142:143], v[224:225] op_sel_hi:[1,0]\n\t" \
  "v_cvt_pk_bf16_f32 v136, v136, v137\n\t" \
  "v_cvt_pk_bf16_f32 v137, v138, v139\n\t" \
  "v_cvt_pk_bf16_f32 v138, v140, v141\n\t" \
  "v_cvt_pk_bf16_f32 v139, v142, v143\n\t" \
  "buffer_store_dwordx4 v[136:139], v242, s[16:19], s98 offen offset:1024 sc1\n\t" \
  "v_mfma_f32_16x16x32_bf16 a[20:23], v[92:95], v[4:7], a[20:23]\n\t" \
  "s_waitcnt lgkmcnt(1)\n\t" \
  "ds_read_b64_tr_b16 a[196:197], v235 offset:0\n\t" \
  "v_exp_f32 v71, v71\n\t" \
  "v_mfma_f32_16x16x32_bf16 a[24:27], v[112:115], v[0:3], a[24:27]\n\t" \
  "s_add_u32 s96, s40, 786432\n\t" \
  "ds_read_b64_tr_b16 a[198:199], v235 offset:4096\n\t" \
  "v_exp_f32 v72, v72\n\t" \
  "v_mfma_f32_16x16x32_bf16 a[24:27], v[116:119], v[4:7], a[24:27]\n\t" \
  "s_and_b32 s96, s96, 0x1c0000\n\t" \
  "ds_read_b64_tr_b16 a[200:201], v234 offset:8192\n\t" \
  "v_cvt_pk_bf16_f32 v64, v64, v65\n\t" \
  "v_mfma_f32_16x16x32_bf16 a[28:31], v[120:123], v[0:3], a[28:31]\n\t" \
  "s_add_u32 s96, s96, s77\n\t" \
  "v_exp_f32 v73, v73\n\t" \
  "v_mfma_f32_16x16x32_bf16 a[28:31], v[124:127], v[4:7], a[28:31]\n\t" \
  "s_add_u32 m0, s38, 147456\n\t" \
  "v_exp_f32 v74, v74\n\t" \
  "v_mfma_f32_16x16x32_bf16 a[64:67], a[128:131], v[144:147], a[64:67]\n\t" \
  "s_barrier\n\t" \
  "v_mfma_f32_16x16x32_bf16 a[64:67], a[132:135], v[148:151], a[64:67]\n\t" \
  "v_exp_f32 v75, v75\n\t" \
  "v_mfma_f32_16x16x32_bf16 a[68:71], a[136:139], v[144:147], a[68:71]\n\t" \
  "v_accvgpr_read_b32 v0, a16\n\t" \
  "v_accvgpr_read_b32 v1, a17\n\t" \
  "v_accvgpr_read_b32 v2, a18\n\t" \
  "v_accvgpr_read_b32 v3, a19\n\t" \
  "v_accvgpr_read_b32 v4, a20\n\t" \
  "v_accvgpr_read_b32 v5, a21\n\t" \
  "v_accvgpr_read_b32 v6, a22\n\t" \
  "v_accvgpr_read_b32 v7, a23\n\t" \
  "v_pk_mul_f32 v[0:1], v[0:1], v[224:225] op_sel_hi:[1,0]\n\t" \
  "v_pk_mul_f32 v[2:3], v[2:3], v[224:225] op_sel_hi:[1,0]\n\t" \
  "v_pk_mul_f32 v[4:5], v[4:5], v[224:225] op_sel_hi:[1,0]\n\t" \
  "v_pk_mul_f32 v[6:7], v[6:7], v[224:225] op_sel_hi:[1,0]\n\t" \
  "v_cvt_pk_bf16_f32 v0, v0, v1\n\t" \
  "v_cvt_pk_bf16_f32 v1, v2, v3\n\t" \
  "v_cvt_pk_bf16_f32 v2, v4, v5\n\t" \
  "v_cvt_pk_bf16_f32 v3, v6, v7\n\t" \
  "buffer_store_dwordx4 v[0:3], v242, s[16:19], s98 offen offset:2048 sc1\n\t" \
  "v_mfma_f32_16x16x32_bf16 a[68:71], a[140:143], v[148:151], a[68:71]\n\t" \
  "v_accvgpr_read_b32 v8, a24\n\t" \
  "v_accvgpr_read_b32 v9, a25\n\t" \
  "v_accvgpr_read_b32 v10, a26\n\t" \
  "v_accvgpr_read_b32 v11, a27\n\t" \
  "v_accvgpr_read_b32 v12, a28\n\t" \
  "v_accvgpr_read_b32 v13, a29\n\t" \
  "v_accvgpr_read_b32 v14, a30\n\t" \
  "v_accvgpr_read_b32 v15, a31\n\t" \
  "v_pk_mul_f32 v[8:9], v[8:9], v[224:225] op_sel_hi:[1,0]\n\t" \
  "v_pk_mul_f32 v[10:11], v[10:11], v[224:225] op_sel_hi:[1,0]\n\t" \
  "v_pk_mul_f32 v[12:13], v[12:13], v[224:225] op_sel_hi:[1,0]\n\t" \
  "v_pk_mul_f32 v[14:15], v[14:15], v[224:225] op_sel_hi:[1,0]\n\t" \
  "v_cvt_pk_bf16_f32 v8, v8, v9\n\t" \
  "v_cvt_pk_bf16_f32 v9, v10, v11\n\t" \
  "v_cvt_pk_bf16_f32 v10, v12, v13\n\t" \
  "v_cvt_pk_bf16_f32 v11, v14, v15\n\t" \
  "buffer_store_dwordx4 v[8:11], v242, s[16:19], s98 offen offset:3072 sc1\n\t" \
  "v_mfma_f32_16x16x32_bf16 a[72:75], a[144:147], v[144:147], a[72:75]\n\t" \
  "buffer_load_dwordx4 v240, s[24:27], s96 offen offset:0 lds\n\t" \
  "v_mfma_f32_16x16x32_bf16 a[72:75], a[148:151], v[148:151], a[72:75]\n\t" \
  "s_xor_b32 s98, s45, 32768\n\t" \
  "ds_read_b64_tr_b16 a[202:203], v234 offset:12288\n\t" \
  "v_exp_f32 v76, v76\n\t" \
  "v_mfma_f32_16x16x32_bf16 a[76:79], a[152:155], v[144:147], a[76:79]\n\t" \
  "ds_read_b128 a[228:231], v213 offset:17408\n\t" \
  "v_exp_f32 v77, v77\n\t" \
  "v_mfma_f32_16x16x32_bf16 a[76:79], a[156:159], v[148:151], a[76:79]\n\t" \
  "ds_read_b128 a[232:235], v213 offset:18432\n\t" \
  "v_cvt_pk_bf16_f32 v65, v66, v67\n\t" \
  "v_mfma_f32_16x16x32_bf16 a[80:83], a[160:163], v[144:147], a[80:83]\n\t" \
  "ds_read_b64_tr_b16 a[204:205], v235 offset:8192\n\t" \
  "v_cvt_pk_bf16_f32 v66, v68, v69\n\t" \
  "v_mfma_f32_16x16x32_bf16 a[80:83], a[164:167], v[148:151], a[80:83]\n\t" \
  "ds_read_b64_tr_b16 a[206:207], v235 offset:12288\n\t" \
  "v_cvt_pk_bf16_f32 v67, v70, v71\n\t" \
  "v_mfma_f32_16x16x32_bf16 a[84:87], a[168:171], v[144:147], a[84:87]\n\t" \
  "buffer_load_dwordx4 v241, s[24:27], s96 offen offset:1024 lds\n\t" \
  "v_mfma_f32_16x16x32_bf16 a[84:87], a[172:175], v[148:151], a[84:87]\n\t" \
  "v_exp_f32 v78, v78\n\t" \
  "v_mfma_f32_16x16x32_bf16 a[88:91], a[176:179], v[144:147], a[88:91]\n\t" \
  "ds_read_b128 a[236:239], v213 offset:19456\n\t" \
  "v_cvt_pk_bf16_f32 v68, v72, v73\n\t" \
  "v_mfma_f32_16x16x32_bf16 a[88:91], a[180:183], v[148:151], a[88:91]\n\t" \
  "buffer_load_dwordx4 v248, s[24:27], s96 offen offset:2048 lds\n\t" \
  "ds_write_b128 v213, v[128:131] offset:16384\n\t" \
  "v_mfma_f32_16x16x32_bf16 a[92:95], a[184:187], v[144:147], a[92:95]\n\t" \
  "ds_write_b128 v213, v[136:139] offset:17408\n\t" \
  "v_mfma_f32_16x16x32_bf16 a[92:95], a[188:191], v[148:151], a[92:95]\n\t" \
  "ds_write_b128 v213, v[0:3] offset:18432\n\t" \
  "v_smfmac_f32_16x16x64_bf16 v[168:171], v[216:219], v[144:151], v208\n\t" \
  "ds_write_b128 v213, v[8:11] offset:19456\n\t" \
  "v_mfma_f32_16x16x32_bf16 a[32:35], v[16:19], v[32:35], a[32:35]\n\t" \
  "buffer_load_dwordx4 v249, s[24:27], s96 offen offset:3072 lds\n\t" \
  "v_mfma_f32_16x16x32_bf16 a[32:35], v[20:23], v[36:39], a[32:35]\n\t" \
  "ds_read_b64_tr_b16 a[208:209], v236 offset:32768\n\t" \
  "v_exp_f32 v79, v79\n\t" \
  "v_smfmac_f32_16x16x64_bf16 v[164:167], v[216:219], v[32:39], v208\n\t" \
  "ds_read_b64_tr_b16 a[210:211], v236 offset:36864\n\t" \
  "v_exp_f32 v96, v96\n\t" \
  "v_mfma_f32_16x16x32_bf16 a[36:39], v[24:27], v[32:35], a[36:39]\n\t" \
  "ds_read_b64_tr_b16 a[212:213], v237 offset:32768\n\t" \
  "v_cvt_pk_bf16_f32 v69, v74, v75\n\t" \
  "v_mfma_f32_16x16x32_bf16 a[36:39], v[28:31], v[36:39], a[36:39]\n\t" \
  "ds_read_b64_tr_b16 a[214:215], v237 offset:36864\n\t" \
  "v_cvt_pk_bf16_f32 v70, v76, v77\n\t" \
  "v_add_f32 v164, v164, v165\n\t" \
  "v_rcp_f32 v226, v164\n\t" \
  "v_mfma_f32_16x16x32_bf16 a[40:43], v[48:51], v[32:35], a[40:43]\n\t" \
  ".Lhs10:\n\ts_cbranch_execz .Lhs10n\n\t" \
  "v_cmp_lt_f32 vcc, 0x5f800000, v164\n\t" \
  "v_accvgpr_read_b32 v0, a32\n\t" \
  "v_accvgpr_read_b32 v1, a33\n\t" \
  "v_accvgpr_read_b32 v2, a34\n\t" \
  "v_mfma_f32_16x16x32_bf16 a[40:43], v[52:55], v[36:39], a[40:43]\n\t" \
  "s_cmp_lg_u64 vcc, 0\n\t" \
  "s_cselect_b32 s89, s78, s89\n\t" \
  "v_or_b32 v254, s89, v254\n\t" \
  "v_accvgpr_read_b32 v3, a35\n\t" \
  "v_accvgpr_read_b32 v4, a36\n\t" \
  "v_mfma_f32_16x16x32_bf16 a[44:47], v[56:59], v[32:35], a[44:47]\n\t" \
  "ds_read_b64_tr_b16 a[216:217], v236 offset:40960\n\t" \
  "v_exp_f32 v97, v97\n\t" \
  "v_accvgpr_read_b32 v5, a37\n\t" \
  "v_accvgpr_read_b32 v6, a38\n\t" \
  "v_mfma_f32_16x16x32_bf16 a[44:47], v[60:63], v[36:39], a[44:47]\n\t" \
  "v_accvgpr_read_b32 v7, a39\n\t" \
  "v_mul_f32 v0, v0, v226\n\t" \
  "v_mfma_f32_16x16x32_bf16 a[48:51], v[80:83], v[32:35], a[48:51]\n\t" \
  "v_mul_f32 v1, v1, v226\n\t" \
  "v_mul_f32 v2, v2, v226\n\t" \
  "v_mfma_f32_16x16x32_bf16 a[48:51], v[84:87], v[36:39], a[48:51]\n\t" \
  "v_mul_f32 v3, v3, v226\n\t" \
  "v_exp_f32 v98, v98\n\t" \
  "v_mfma_f32_16x16x32_bf16 a[52:55], v[88:91], v[32:35], a[52:55]\n\t" \
  "v_mul_f32 v4, v4, v226\n\t" \
  "v_exp_f32 v99, v99\n\t" \
  "v_mfma_f32_16x16x32_bf16 a[52:55], v[92:95], v[36:39], a[52:55]\n\t" \
  "v_mul_f32 v5, v5, v226\n\t" \
  "v_mul_f32 v6, v6, v226\n\t" \
  "v_mfma_f32_16x16x32_bf16 a[56:59], v[112:115], v[32:35], a[56:59]\n\t" \
  "v_mul_f32 v7, v7, v226\n\t" \
  "v_exp_f32 v100, v100\n\t" \
  "v_mfma_f32_16x16x32_bf16 a[56:59], v[116:119], v[36:39], a[56:59]\n\t" \
  "ds_read_b64_tr_b16 a[218:219], v236 offset:45056\n\t" \
  "v_cvt_pk_bf16_f32 v71, v78, v79\n\t" \
  "v_mfma_f32_16x16x32_bf16 a[60:63], v[120:123], v[32:35], a[60:63]\n\t" \
  "v_exp_f32 v101, v101\n\t" \
  "v_mfma_f32_16x16x32_bf16 a[60:63], v[124:127], v[36:39], a[60:63]\n\t" \
  "v_cvt_pk_bf16_f32 v32, v0, v1\n\t" \
  "v_cvt_pk_bf16_f32 v33, v2, v3\n\t" \
  "v_cvt_pk_bf16_f32 v34, v4, v5\n\t" \
  "v_cvt_pk_bf16_f32 v35, v6, v7\n\t" \
  "v_mfma_f32_16x16x32_bf16 a[96:99], a[128:131], v[152:155], a[96:99]\n\t" \
  "v_accvgpr_read_b32 v8, a40\n\t" \
  "v_accvgpr_read_b32 v9, a41\n\t" \
  "v_accvgpr_read_b32 v10, a42\n\t" \
  "v_accvgpr_read_b32 v11, a43\n\t" \
  "v_mfma_f32_16x16x32_bf16 a[96:99], a[132:135], v[156:159], a[96:99]\n\t" \
  "s_waitcnt vmcnt(8) lgkmcnt(11)\n\t" \
  "v_accvgpr_read_b32 v12, a44\n\t" \
  "v_accvgpr_read_b32 v13, a45\n\t" \
  "v_accvgpr_read_b32 v14, a46\n\t" \
  "v_accvgpr_read_b32 v15, a47\n\t" \
  "v_mfma_f32_16x16x32_bf16 a[100:103], a[136:139], v[152:155], a[100:103]\n\t" \
  "v_mul_f32 v8, v8, v226\n\t" \
  "v_mul_f32 v9, v9, v226\n\t" \
  "s_barrier\n\t" \
  "v_mfma_f32_16x16x32_bf16 a[100:103], a[140:143], v[156:159], a[100:103]\n\t" \
  "buffer_store_dwordx4 v[32:35], v242, s[16:19], s98 offen offset:0 sc1\n\t" \
  "v_mul_f32 v10, v10, v226\n\t" \
  "v_mul_f32 v11, v11, v226\n\t" \
  "v_mul_f32 v12, v12, v226\n\t" \
  "v_mfma_f32_16x16x32_bf16 a[104:107], a[144:147], v[152:155], a[104:107]\n\t" \
  "v_mul_f32 v13, v13, v226\n\t" \
  "v_mul_f32 v14, v14, v226\n\t" \
  "v_mul_f32 v15, v15, v226\n\t" \
  "v_cvt_pk_bf16_f32 v36, v8, v9\n\t" \
  "v_mfma_f32_16x16x32_bf16 a[104:107], a[148:151], v[156:159], a[104:107]\n\t" \
  "v_cvt_pk_bf16_f32 v37, v10, v11\n\t" \
  "v_cvt_pk_bf16_f32 v38, v12, v13\n\t" \
  "v_cvt_pk_bf16_f32 v39, v14, v15\n\t" \
  "v_accvgpr_read_b32 v0, a48\n\t" \
  "v_mfma_f32_16x16x32_bf16 a[108:111], a[152:155], v[152:155], a[108:111]\n\t" \
  "v_accvgpr_read_b32 v1, a49\n\t" \
  "v_accvgpr_read_b32 v2, a50\n\t" \
  "v_accvgpr_read_b32 v3, a51\n\t" \
  "v_accvgpr_read_b32 v4, a52\n\t" \
  "v_mfma_f32_16x16x32_bf16 a[108:111], a[156:159], v[156:159], a[108:111]\n\t" \
  "v_accvgpr_read_b32 v5, a53\n\t" \
  "v_accvgpr_read_b32 v6, a54\n\t" \
  "v_accvgpr_read_b32 v7, a55\n\t" \
  "v_exp_f32 v102, v102\n\t" \
  "v_mfma_f32_16x16x32_bf16 a[112:115], a[160:163], v[152:155], a[112:115]\n\t" \
  "v_mul_f32 v0, v0, v226\n\t" \
  "v_mul_f32 v1, v1, v226\n\t" \
  "v_mul_f32 v2, v2, v226\n\t" \
  "v_exp_f32 v103, v103\n\t" \
  "v_mfma_f32_16x16x32_bf16 a[112:115], a[164:167], v[156:159], a[112:115]\n\t" \
  "v_mul_f32 v3, v3, v226\n\t" \
  "v_mul_f32 v4, v4, v226\n\t" \
  "v_mul_f32 v5, v5, v226\n\t" \
  "v_mul_f32 v6, v6, v226\n\t" \
  "v_mfma_f32_16x16x32_bf16 a[116:119], a[168:171], v[152:155], a[116:119]\n\t" \
  "v_mul_f32 v7, v7, v226\n\t" \
  "v_cvt_pk_bf16_f32 v40, v0, v1\n\t" \
  "v_cvt_pk_bf16_f32 v41, v2, v3\n\t" \
  "v_exp_f32 v104, v104\n\t" \
  "v_mfma_f32_16x16x32_bf16 a[116:119], a[172:175], v[156:159], a[116:119]\n\t" \
  "v_cvt_pk_bf16_f32 v96, v96, v97\n\t" \
  "v_cvt_pk_bf16_f32 v42, v4, v5\n\t" \
  "v_cvt_pk_bf16_f32 v43, v6, v7\n\t" \
  "v_accvgpr_read_b32 v8, a56\n\t" \
  "v_mfma_f32_16x16x32_bf16 a[120:123], a[176:179], v[152:155], a[120:123]\n\t" \
  "buffer_store_dwordx4 v[36:39], v242, s[16:19], s98 offen offset:1024 sc1\n\t" \
  "v_accvgpr_read_b32 v9, a57\n\t" \
  "v_accvgpr_read_b32 v10, a58\n\t" \
  "v_accvgpr_read_b32 v11, a59\n\t" \
  "v_mfma_f32_16x16x32_bf16 a[120:123], a[180:183], v[156:159], a[120:123]\n\t" \
  "v_accvgpr_read_b32 v12, a60\n\t" \
  "v_accvgpr_read_b32 v13, a61\n\t" \
  "v_accvgpr_read_b32 v14, a62\n\t" \
  "v_exp_f32 v105, v105\n\t" \
  "v_mfma_f32_16x16x32_bf16 a[124:127], a[184:187], v[152:155], a[124:127]\n\t" \
  "v_cvt_pk_bf16_f32 v97, v98, v99\n\t" \
  "v_accvgpr_read_b32 v15, a63\n\t" \
  "v_mul_f32 v8, v8, v226\n\t" \
  "v_mul_f32 v9, v9, v226\n\t" \
  "v_mfma_f32_16x16x32_bf16 a[124:127], a[188:191], v[156:159], a[124:127]\n\t" \
  "v_mul_f32 v10, v10, v226\n\t" \
  "v_mul_f32 v11, v11, v226\n\t" \
  "v_mul_f32 v12, v12, v226\n\t" \
  "v_exp_f32 v106, v106\n\t" \
  "v_smfmac_f32_16x16x64_bf16 v[172:175], v[216:219], v[152:159], v208\n\t" \
  "ds_read_b64_tr_b16 a[220:221], v237 offset:40960\n\t" \
  "v_cvt_pk_bf16_f32 v98, v100, v101\n\t" \
  "v_mul_f32 v13, v13, v226\n\t" \
  "v_mul_f32 v14, v14, v226\n\t" \
  "v_mfma_f32_16x16x32_bf16 a[64:67], v[16:19], v[64:67], a[64:67]\n\t" \
  "s_waitcnt vmcnt(6)\n\t" \
  "buffer_store_dword v243, v254, s[16:19], s49 offen offset:0 sc1\n\t" \
  "ds_read_b64_tr_b16 a[222:223], v237 offset:45056\n\t" \
  "v_mfma_f32_16x16x32_bf16 a[64:67], v[20:23], v[68:71], a[64:67]\n\t" \
  "v_cvt_pk_bf16_f32 v99, v102, v103\n\t" \
  "v_mul_f32 v15, v15, v226\n\t" \
  "v_exp_f32 v107, v107\n\t" \
  "v_smfmac_f32_16x16x64_bf16 v[168:171], v[216:219], v[64:71], v208\n\t" \
  "ds_read_b64_tr_b16 a[128:129], v234 offset:49152\n\t" \
  "v_exp_f32 v108, v108\n\t" \
  "v_mfma_f32_16x16x32_bf16 a[68:71], v[24:27], v[64:67], a[68:71]\n\t" \
  "v_cvt_pk_bf16_f32 v100, v104, v105\n\t" \
  "v_exp_f32 v109, v109\n\t" \
  "v_mfma_f32_16x16x32_bf16 a[68:71], v[28:31], v[68:71], a[68:71]\n\t" \
  "buffer_store_dwordx4 v[40:43], v242, s[16:19], s98 offen offset:2048 sc1\n\t" \
  "v_cvt_pk_bf16_f32 v44, v8, v9\n\t" \
  "v_add_f32 v168, v168, v169\n\t" \
  "v_rcp_f32 v220, v168\n\t" \
  "v_mfma_f32_16x16x32_bf16 a[72:75], v[48:51], v[64:67], a[72:75]\n\t" \
  "v_cmp_lt_f32 vcc, 0x5f800000, v168\n\t" \
  "v_exp_f32 v110, v110\n\t" \
  "v_mfma_f32_16x16x32_bf16 a[72:75], v[52:55], v[68:71], a[72:75]\n\t" \
  "s_cmp_lg_u64 vcc, 0\n\t" \
  "s_cselect_b32 s89, s78, s89\n\t" \
  "v_or_b32 v254, s89, v254\n\t" \
  "v_mfma_f32_16x16x32_bf16 a[76:79], v[56:59], v[64:67], a[76:79]\n\t" \
  "v_cvt_pk_bf16_f32 v101, v106, v107\n\t" \
  "v_cvt_pk_bf16_f32 v45, v10, v11\n\t" \
  "v_mfma_f32_16x16x32_bf16 a[76:79], v[60:63], v[68:71], a[76:79]\n\t" \
  "ds_read_b64_tr_b16 a[130:131], v234 offset:53248\n\t" \
  "v_cvt_pk_bf16_f32 v46, v12, v13\n\t" \
  "v_mfma_f32_16x16x32_bf16 a[80:83], v[80:83], v[64:67], a[80:83]\n\t" \
  "v_cvt_pk_bf16_f32 v102, v108, v109\n\t" \
  "v_cvt_pk_bf16_f32 v47, v14, v15\n\t" \
  "v_mfma_f32_16x16x32_bf16 a[80:83], v[84:87], v[68:71], a[80:83]\n\t" \
  "ds_read_b64_tr_b16 a[132:133], v235 offset:49152\n\t" \
  "v_exp_f32 v111, v111\n\t" \
  "v_mfma_f32_16x16x32_bf16 a[84:87], v[88:91], v[64:67], a[84:87]\n\t" \
  "ds_read_b128 a[240:243], v213 offset:32768\n\t" \
  "v_cvt_pk_bf16_f32 v103, v110, v111\n\t" \
  "v_mfma_f32_16x16x32_bf16 a[84:87], v[92:95], v[68:71], a[84:87]\n\t" \
  "s_add_u32 s96, s40, 1048576\n\t" \
  "ds_read_b128 a[244:247], v213 offset:33792\n\t" \
  "s_and_b32 s96, s96, 0x1c0000\n\t" \
  "v_mfma_f32_16x16x32_bf16 a[88:91], v[112:115], v[64:67], a[88:91]\n\t" \
  "buffer_store_dwordx4 v[44:47], v242, s[16:19], s98 offen offset:3072 sc1\n\t" \
  "ds_read_b128 a[248:251], v213 offset:34816\n\t" \
  "ds_read_b128 a[252:255], v213 offset:35840\n\t" \
  "ds_read_b64_tr_b16 a[134:135], v235 offset:53248\n\t" \
  "v_mfma_f32_16x16x32_bf16 a[88:91], v[116:119], v[68:71], a[88:91]\n\t" \
  "ds_write_b128 v213, v[32:35] offset:32768\n\t" \
  "v_mfma_f32_16x16x32_bf16 a[92:95], v[120:123], v[64:67], a[92:95]\n\t" \
  "ds_write_b128 v213, v[36:39] offset:33792\n\t" \
  "v_mfma_f32_16x16x32_bf16 a[92:95], v[124:127], v[68:71], a[92:95]\n\t" \
  "s_waitcnt vmcnt(5) lgkmcnt(10)\n\t" \
  "s_add_u32 s96, s96, s51\n\t" \
  "s_add_u32 m0, s38, 98304\n\t" \
  "v_accvgpr_read_b32 v64, a64\n\t" \
  "v_accvgpr_read_b32 v65, a65\n\t" \
  "v_mfma_f32_16x16x32_bf16 a[96:99], v[16:19], v[96:99], a[96:99]\n\t" \
  "v_accvgpr_read_b32 v66, a66\n\t" \
  "v_accvgpr_read_b32 v67, a67\n\t" \
  "s_barrier\n\t" \
  "v_mfma_f32_16x16x32_bf16 a[96:99], v[20:23], v[100:103], a[96:99]\n\t" \
  "buffer_load_dwordx4 v240, s[24:27], s96 offen offset:0 lds\n\t" \
  "v_accvgpr_read_b32 v68, a68\n\t" \
  "v_accvgpr_read_b32 v69, a69\n\t" \
  "v_smfmac_f32_16x16x64_bf16 v[172:175], v[216:219], v[96:103], v208\n\t" \
  "s_xor_b32 s98, s45, 65536\n\t" \
  "ds_read_b128 v[128:131], v242 offset:16384\n\t" \
  "v_accvgpr_read_b32 v70, a70\n\t" \
  "v_accvgpr_read_b32 v71, a71\n\t" \
  "v_mfma_f32_16x16x32_bf16 a[100:103], v[24:27], v[96:99], a[100:103]\n\t" \
  "ds_read_b128 v[132:135], v242 offset:17408\n\t" \
  "v_mul_f32 v64, v64, v220\n\t" \
  "v_mfma_f32_16x16x32_bf16 a[100:103], v[28:31], v[100:103], a[100:103]\n\t" \
  "v_add_f32 v172, v172, v173\n\t" \
  "v_rcp_f32 v222, v172\n\t" \
  "v_mfma_f32_16x16x32_bf16 a[104:107], v[48:51], v[96:99], a[104:107]\n\t" \
  "buffer_load_dwordx4 v241, s[24:27], s96 offen offset:1024 lds\n\t" \
  "v_mfma_f32_16x16x32_bf16 a[104:107], v[52:55], v[100:103], a[104:107]\n\t" \
  "ds_read_b128 v[136:139], v242 offset:18432\n\t" \
  "v_cmp_lt_f32 vcc, 0x5f800000, v172\n\t" \
  "v_mfma_f32_16x16x32_bf16 a[108:111], v[56:59], v[96:99], a[108:111]\n\t" \
  "s_cmp_lg_u64 vcc, 0\n\t" \
  "s_cselect_b32 s89, s78, s89\n\t" \
  "v_or_b32 v254, s89, v254\n\t" \
  "v_mfma_f32_16x16x32_bf16 a[108:111], v[60:63], v[100:103], a[108:111]\n\t" \
  "buffer_load_dwordx4 v248, s[24:27], s96 offen offset:2048 lds\n\t" \
  "v_mul_f32 v65, v65, v220\n\t" \
  "v_mfma_f32_16x16x32_bf16 a[112:115], v[80:83], v[96:99], a[112:115]\n\t" \
  "s_cmp_lg_u32 s89, 0\n\t" \
  "s_cbranch_scc1 .Lat_x\n\t" \
  ".Lat_y:\n\t" \
  "v_mfma_f32_16x16x32_bf16 a[112:115], v[84:87], v[100:103], a[112:115]\n\t" \
  "ds_write_b128 v213, v[40:43] offset:34816\n\t" \
  "v_mfma_f32_16x16x32_bf16 a[116:119], v[88:91], v[96:99], a[116:119]\n\t" \
  "buffer_load_dwordx4 v249, s[24:27], s96 offen offset:3072 lds\n\t" \
  "ds_read_b128 v[140:143], v242 offset:19456\n\t" \
  "v_mul_f32 v66, v66, v220\n\t" \
  "v_mfma_f32_16x16x32_bf16 a[116:119], v[92:95], v[100:103], a[116:119]\n\t" \
  "ds_read_b64_tr_b16 a[136:137], v234 offset:57344\n\t" \
  "ds_read_b128 v[144:147], v242 offset:20480\n\t" \
  "v_mul_f32 v67, v67, v220\n\t" \
  "v_mfma_f32_16x16x32_bf16 a[120:123], v[112:115], v[96:99], a[120:123]\n\t" \
  "ds_read_b128 v[148:151], v242 offset:21504\n\t" \
  "v_mul_f32 v68, v68, v220\n\t" \
  "v_mfma_f32_16x16x32_bf16 a[120:123], v[116:119], v[100:103], a[120:123]\n\t" \
  "ds_write_b128 v213, v[44:47] offset:35840\n\t" \
  "v_mfma_f32_16x16x32_bf16 a[124:127], v[120:123], v[96:99], a[124:127]\n\t" \
  "ds_read_b64_tr_b16 a[138:139], v234 offset:61440\n\t" \
  "ds_read_b128 v[152:155], v242 offset:22528\n\t" \
  "v_mul_f32 v69, v69, v220\n\t" \
  "v_mul_f32 v70, v70, v220\n\t" \
  "v_mfma_f32_16x16x32_bf16 a[124:127], v[124:127], v[100:103], a[124:127]\n\t" \
  "s_waitcnt lgkmcnt(6)\n\t" \
  "ds_read_b64_tr_b16 a[140:141], v235 offset:57344\n\t" \
  "ds_read_b128 v[156:159], v242 offset:23552\n\t" \
  "v_mul_f32 v71, v71, v220\n\t" \
  ".Lat_join:\n\t" \
  STS("s_memrealtime s[94:95]\n\t") \
  STS("s_memtime s[92:93]\n\t") \
  "v_mfma_f32_16x16x32_bf16 v[0:3], a[192:195], v[128:131], 0\n\t" \
  "v_cvt_pk_bf16_f32 v112, v64, v65\n\t" \
  "v_cvt_pk_bf16_f32 v113, v66, v67\n\t" \
  "v_cvt_pk_bf16_f32 v114, v68, v69\n\t" \
  "v_cvt_pk_bf16_f32 v115, v70, v71\n\t" \
  "v_mfma_f32_16x16x32_bf16 v[0:3], a[200:203], v[132:135], v[0:3]\n\t" \
  "buffer_load_dword v246, v245, s[16:19], s65 offen sc1\n\t" \
  "v_accvgpr_read_b32 v72, a72\n\t" \
  "v_accvgpr_read_b32 v73, a73\n\t" \
  "v_accvgpr_read_b32 v74, a74\n\t" \
  "v_mfma_f32_16x16x32_bf16 v[0:3], a[208:211], v[136:139], v[0:3]\n\t" \
  "v_accvgpr_read_b32 v75, a75\n\t" \
  "v_accvgpr_read_b32 v76, a76\n\t" \
  "v_accvgpr_read_b32 v77, a77\n\t" \
  "v_accvgpr_read_b32 v78, a78\n\t" \
  "v_mfma_f32_16x16x32_bf16 v[0:3], a[216:219], v[140:143], v[0:3]\n\t" \
  "ds_read_b128 v[160:163], v242 offset:24576\n\t" \
  "v_accvgpr_read_b32 v79, a79\n\t" \
  "v_mul_f32 v72, v72, v220\n\t" \
  "v_mul_f32 v73, v73, v220\n\t" \
  "v_mfma_f32_16x16x32_bf16 v[16:19], a[220:223], v[140:143], 0\n\t" \
  "ds_read_b128 v[164:167], v242 offset:25600\n\t" \
  "v_mul_f32 v74, v74, v220\n\t" \
  "v_mul_f32 v75, v75, v220\n\t" \
  "v_mul_f32 v76, v76, v220\n\t" \
  "v_mfma_f32_16x16x32_bf16 v[16:19], a[212:215], v[136:139], v[16:19]\n\t" \
  "ds_read_b128 v[168:171], v242 offset:26624\n\t" \
  "v_mul_f32 v77, v77, v220\n\t" \
  "v_mul_f32 v78, v78, v220\n\t" \
  "v_mul_f32 v79, v79, v220\n\t" \
  "v_mfma_f32_16x16x32_bf16 v[16:19], a[204:207], v[132:135], v[16:19]\n\t" \
  "ds_read_b128 v[172:175], v242 offset:27648\n\t" \
  "v_cvt_pk_bf16_f32 v116, v72, v73\n\t" \
  "v_cvt_pk_bf16_f32 v117, v74, v75\n\t" \
  "v_cvt_pk_bf16_f32 v118, v76, v77\n\t" \
  "v_mfma_f32_16x16x32_bf16 v[16:19], a[196:199], v[128:131], v[16:19]\n\t" \
  "s_waitcnt lgkmcnt(4)\n\t" \
  "ds_read_b64_tr_b16 a[142:143], v235 offset:61440\n\t" \
  "v_cvt_pk_bf16_f32 v119, v78, v79\n\t" \
  "v_accvgpr_read_b32 v64, a80\n\t" \
  "v_accvgpr_read_b32 v65, a81\n\t" \
  "v_mfma_f32_16x16x32_bf16 v[4:7], a[192:195], v[144:147], 0\n\t" \
  "buffer_store_dwordx4 v[112:115], v242, s[16:19], s98 offen offset:0 sc1\n\t" \
  "v_accvgpr_read_b32 v66, a82\n\t" \
  "ds_read_b128 v[128:131], v242 offset:28672\n\t" \
  "v_accvgpr_read_b32 v67, a83\n\t" \
  "v_mfma_f32_16x16x32_bf16 v[4:7], a[200:203], v[148:151], v[4:7]\n\t" \
  "v_accvgpr_read_b32 v68, a84\n\t" \
  "v_accvgpr_read_b32 v69, a85\n\t" \
  "v_accvgpr_read_b32 v70, a86\n\t" \
  "v_accvgpr_read_b32 v71, a87\n\t" \
  "v_mfma_f32_16x16x32_bf16 v[4:7], a[208:211], v[152:155], v[4:7]\n\t" \
  "ds_read_b64_tr_b16 a[144:145], v236 offset:0\n\t" \
  "ds_read_b128 v[132:135], v242 offset:29696\n\t" \
  "v_mul_f32 v64, v64, v220\n\t" \
  "v_mul_f32 v65, v65, v220\n\t" \
  "v_mfma_f32_16x16x32_bf16 v[4:7], a[216:219], v[156:159], v[4:7]\n\t" \
  "v_mul_f32 v66, v66, v220\n\t" \
  "v_mul_f32 v67, v67, v220\n\t" \
  "v_mul_f32 v68, v68, v220\n\t" \
  "v_mul_f32 v69, v69, v220\n\t" \
  "v_mfma_f32_16x16x32_bf16 v[20:23], a[220:223], v[156:159], 0\n\t" \
  STS("v_writelane_b32 v255, s94, 22\n\t") \
  STS("v_writelane_b32 v255, s92, 23\n\t") \
  "ds_read_b64_tr_b16 a[146:147], v236 offset:4096\n\t" \
  "ds_read_b128 v[136:139], v242 offset:30720\n\t" \
  "v_mul_f32 v70, v70, v220\n\t" \
  "v_mul_f32 v71, v71, v220\n\t" \
  "v_mfma_f32_16x16x32_bf16 v[20:23], a[212:215], v[152:155], v[20:23]\n\t" \
  "v_cvt_pk_bf16_f32 v120, v64, v65\n\t" \
  "v_cvt_pk_bf16_f32 v121, v66, v67\n\t" \
  "v_cvt_pk_bf16_f32 v122, v68, v69\n\t" \
  "v_cvt_pk_bf16_f32 v123, v70, v71\n\t" \
  "v_mfma_f32_16x16x32_bf16 v[20:23], a[204:207], v[148:151], v[20:23]\n\t" \
  "s_waitcnt lgkmcnt(5)\n\t" \
  "ds_read_b64_tr_b16 a[148:149], v237 offset:0\n\t" \
  "ds_read_b128 v[140:143], v242 offset:31744\n\t" \
  "v_accvgpr_read_b32 v72, a88\n\t" \
  "v_accvgpr_read_b32 v73, a89\n\t" \
  "v_mfma_f32_16x16x32_bf16 v[20:23], a[196:199], v[144:147], v[20:23]\n\t" \
  "v_accvgpr_read_b32 v74, a90\n\t" \
  "v_accvgpr_read_b32 v75, a91\n\t" \
  "s_barrier\n\t" \
  "v_mfma_f32_16x16x32_bf16 v[8:11], a[192:195], v[160:163], 0\n\t" \
  "ds_read_b128 v[144:147], v242 offset:32768\n\t" \
  "v_accvgpr_read_b32 v76, a92\n\t" \
  "v_accvgpr_read_b32 v77, a93\n\t" \
  "v_accvgpr_read_b32 v78, a94\n\t" \
  "v_mfma_f32_16x16x32_bf16 v[8:11], a[200:203], v[164:167], v[8:11]\n\t" \
  "ds_read_b32 v209, v213 offset:0\n\t" \
  "ds_read_b64_tr_b16 a[150:151], v237 offset:4096\n\t" \
  "ds_read_b128 v[148:151], v242 offset:33792\n\t" \
  "v_accvgpr_read_b32 v79, a95\n\t" \
  "v_mfma_f32_16x16x32_bf16 v[8:11], a[208:211], v[168:171], v[8:11]\n\t" \
  "buffer_store_dwordx4 v[116:119], v242, s[16:19], s98 offen offset:1024 sc1\n\t" \
  "ds_read_b64_tr_b16 a[152:153], v236 offset:8192\n\t" \
  "ds_read_b128 v[152:155], v242 offset:34816\n\t" \
  "ds_read_b64_tr_b16 a[154:155], v236 offset:12288\n\t" \
  "v_mfma_f32_16x16x32_bf16 v[8:11], a[216:219], v[172:175], v[8:11]\n\t" \
  "ds_read_b64_tr_b16 a[156:157], v237 offset:8192\n\t" \
  "v_mul_f32 v72, v72, v220\n\t" \
  "v_mfma_f32_16x16x32_bf16 v[24:27], a[220:223], v[172:175], 0\n\t" \
  "v_mul_f32 v73, v73, v220\n\t" \
  "v_mul_f32 v74, v74, v220\n\t" \
  "v_mfma_f32_16x16x32_bf16 v[24:27], a[212:215], v[168:171], v[24:27]\n\t" \
  "ds_read_b128 v[156:159], v242 offset:35840\n\t" \
  "v_mul_f32 v75, v75, v220\n\t" \
  "v_mfma_f32_16x16x32_bf16 v[24:27], a[204:207], v[164:167], v[24:27]\n\t" \
  "s_waitcnt lgkmcnt(7)\n\t" \
  "v_cmp_eq_u32 vcc, s36, v209\n\t" \
  "v_mul_f32 v76, v76, v220\n\t" \
  "v_mfma_f32_16x16x32_bf16 v[24:27], a[196:199], v[160:163], v[24:27]\n\t" \
  "s_and_b32 vcc_lo, vcc_lo, s37\n\t" \
  "s_cbranch_scc1 .Lat_rd\n\t" \
  "v_mfma_f32_16x16x32_bf16 v[12:15], a[192:195], v[128:131], 0\n\t" \
  "s_waitcnt vmcnt(7)\n\t" \
  "buffer_store_dword v243, v254, s[16:19], s49 offen offset:32 sc1\n\t" \
  "ds_read_b128 v[160:163], v242 offset:36864\n\t" \
  "v_mfma_f32_16x16x32_bf16 v[12:15], a[200:203], v[132:135], v[12:15]\n\t" \
  "ds_write_b128 v213, v[112:115] offset:0\n\t" \
  "v_mfma_f32_16x16x32_bf16 v[12:15], a[208:211], v[136:139], v[12:15]\n\t" \
  "ds_read_b64_tr_b16 a[158:159], v237 offset:12288\n\t" \
  "ds_read_b128 v[164:167], v242 offset:37888\n\t" \
  "v_mul_f32 v77, v77, v220\n\t" \
  "v_mfma_f32_16x16x32_bf16 v[12:15], a[216:219], v[140:143], v[12:15]\n\t" \
  "ds_read_b64_tr_b16 a[160:161], v238 offset:0\n\t" \
  "ds_read_b128 v[168:171], v242 offset:38912\n\t" \
  "v_mul_f32 v78, v78, v220\n\t" \
  "v_mfma_f32_16x16x32_bf16 v[28:31], a[220:223], v[140:143], 0\n\t" \
  "ds_write_b128 v213, v[116:119] offset:1024\n\t" \
  "v_mfma_f32_16x16x32_bf16 v[28:31], a[212:215], v[136:139], v[28:31]\n\t" \
  "ds_write_b128 v213, v[120:123] offset:2048\n\t" \
  "v_mfma_f32_16x16x32_bf16 v[28:31], a[204:207], v[132:135], v[28:31]\n\t" \
  "s_add_u32 s97, s64, 131072\n\t" \
  "ds_read_b64_tr_b16 a[162:163], v238 offset:4096\n\t" \
  "ds_read_b128 v[172:175], v242 offset:39936\n\t" \
  "v_mul_f32 v79, v79, v220\n\t" \
  "v_mfma_f32_16x16x32_bf16 v[28:31], a[196:199], v[128:131], v[28:31]\n\t" \
  "s_waitcnt lgkmcnt(10)\n\t" \
  "s_waitcnt vmcnt(3)\n\t" \
  "v_cmp_ne_u32 vcc, s36, v246\n\t" \
  "v_cvt_pk_bf16_f32 v124, v72, v73\n\t" \
  STP("s_memrealtime s[94:95]\n\t") \
  STP("s_memtime s[92:93]\n\t") \
  "v_mfma_f32_16x16x32_bf16 v[32:35], a[192:195], v[144:147], 0\n\t" \
  "s_and_b32 s97, s97, 0xe0000\n\t" \
  "s_add_u32 s97, s97, s60\n\t" \
  "s_add_u32 m0, s38, 49152\n\t" \
  "v_mfma_f32_16x16x32_bf16 v[32:35], a[200:203], v[148:151], v[32:35]\n\t" \
  "s_and_b32 vcc_lo, vcc_lo, 0xe\n\t" \
  "s_cbranch_scc1 .Lat_s0\n\t" \
  ".Lat_k0:\n\t" \
  "ds_read_b128 v[128:131], v242 offset:40960\n\t" \
  "v_cvt_pk_bf16_f32 v125, v74, v75\n\t" \
  "v_mfma_f32_16x16x32_bf16 v[32:35], a[208:211], v[152:155], v[32:35]\n\t" \
  "buffer_load_dwordx4 v242, s[16:19], s97 offen offset:0 sc1 lds\n\t" \
  "v_mfma_f32_16x16x32_bf16 v[32:35], a[216:219], v[156:159], v[32:35]\n\t" \
  "ds_read_b64_tr_b16 a[164:165], v239 offset:0\n\t" \
  "ds_read_b128 v[132:135], v242 offset:41984\n\t" \
  "v_cvt_pk_bf16_f32 v126, v76, v77\n\t" \
  "v_mfma_f32_16x16x32_bf16 v[48:51], a[220:223], v[156:159], 0\n\t" \
  "ds_read_b128 v[136:139], v242 offset:43008\n\t" \
  "v_cvt_pk_bf16_f32 v127, v78, v79\n\t" \
  "v_mfma_f32_16x16x32_bf16 v[48:51], a[212:215], v[152:155], v[48:51]\n\t" \
  "buffer_load_dwordx4 v242, s[16:19], s97 offen offset:1024 sc1 lds\n\t" \
  "ds_write_b128 v213, v[124:127] offset:3072\n\t" \
  "v_mfma_f32_16x16x32_bf16 v[48:51], a[204:207], v[148:151], v[48:51]\n\t" \
  "ds_read_b64_tr_b16 a[166:167], v239 offset:4096\n\t" \
  "v_mfma_f32_16x16x32_bf16 v[48:51], a[196:199], v[144:147], v[48:51]\n\t" \
  "s_waitcnt lgkmcnt(6)\n\t" \
  "ds_read_b64_tr_b16 a[168:169], v238 offset:8192\n\t" \
  "ds_read_b128 v[140:143], v242 offset:44032\n\t" \
  "v_mfma_f32_16x16x32_bf16 v[36:39], a[192:195], v[160:163], 0\n\t" \
  "buffer_load_dwordx4 v242, s[16:19], s97 offen offset:2048 sc1 lds\n\t" \
  "v_mfma_f32_16x16x32_bf16 v[36:39], a[200:203], v[164:167], v[36:39]\n\t" \
  "ds_read_b64_tr_b16 a[170:171], v238 offset:12288\n\t" \
  "ds_read_b128 v[144:147], v242 offset:45056\n\t" \
  "v_mfma_f32_16x16x32_bf16 v[36:39], a[208:211], v[168:171], v[36:39]\n\t" \
  "ds_read_b64_tr_b16 a[172:173], v239 offset:8192\n\t" \
  "ds_read_b128 v[148:151], v242 offset:46080\n\t" \
  "v_mfma_f32_16x16x32_bf16 v[36:39], a[216:219], v[172:175], v[36:39]\n\t" \
  "buffer_load_dwordx4 v242, s[16:19], s97 offen offset:3072 sc1 lds\n\t" \
  "v_mfma_f32_16x16x32_bf16 v[52:55], a[220:223], v[172:175], 0\n\t" \
  STP("v_writelane_b32 v255, s94, 24\n\t") \
  STP("v_writelane_b32 v255, s92, 25\n\t") \
  "ds_read_b128 v[152:155], v242 offset:47104\n\t" \
  "v_mfma_f32_16x16x32_bf16 v[52:55], a[212:215], v[168:171], v[52:55]\n\t" \
  "ds_read_b64_tr_b16 a[174:175], v239 offset:12288\n\t" \
  "v_mfma_f32_16x16x32_bf16 v[52:55], a[204:207], v[164:167], v[52:55]\n\t" \
  "s_waitcnt lgkmcnt(6)\n\t" \
  "buffer_store_dwordx4 v[120:123], v242, s[16:19], s98 offen offset:2048 sc1\n\t" \
  "ds_read_b64_tr_b16 a[176:177], v236 offset:16384\n\t" \
  "ds_read_b64_tr_b16 a[178:179], v236 offset:20480\n\t" \
  "ds_read_b128 v[156:159], v242 offset:48128\n\t" \
  "v_mfma_f32_16x16x32_bf16 v[52:55], a[196:199], v[160:163], v[52:55]\n\t" \
  "s_barrier\n\t" \
  "v_mfma_f32_16x16x32_bf16 v[40:43], a[192:195], v[128:131], 0\n\t" \
  "ds_read_b128 v[160:163], v242 offset:0\n\t" \
  "v_mfma_f32_16x16x32_bf16 v[40:43], a[200:203], v[132:135], v[40:43]\n\t" \
  "ds_read_b64_tr_b16 a[180:181], v237 offset:16384\n\t" \
  "v_mfma_f32_16x16x32_bf16 v[40:43], a[208:211], v[136:139], v[40:43]\n\t" \
  "ds_read_b128 v[164:167], v242 offset:1024\n\t" \
  "v_mfma_f32_16x16x32_bf16 v[40:43], a[216:219], v[140:143], v[40:43]\n\t" \
  "ds_read_b64_tr_b16 a[182:183], v237 offset:20480\n\t" \
  "v_mfma_f32_16x16x32_bf16 v[56:59], a[220:223], v[140:143], 0\n\t" \
  "ds_read_b128 v[168:171], v242 offset:2048\n\t" \
  "v_mfma_f32_16x16x32_bf16 v[56:59], a[212:215], v[136:139], v[56:59]\n\t" \
  "ds_read_b128 v[172:175], v242 offset:3072\n\t" \
  "v_mfma_f32_16x16x32_bf16 v[56:59], a[204:207], v[132:135], v[56:59]\n\t" \
  "ds_read_b64_tr_b16 a[184:185], v236 offset:24576\n\t" \
  "v_mfma_f32_16x16x32_bf16 v[56:59], a[196:199], v[128:131], v[56:59]\n\t" \
  "s_waitcnt lgkmcnt(7)\n\t" \
  "ds_read_b64_tr_b16 a[186:187], v236 offset:28672\n\t" \
  "v_mfma_f32_16x16x32_bf16 v[44:47], a[192:195], v[144:147], 0\n\t" \
  "ds_read_b128 v[128:131], v242 offset:4096\n\t" \
  "v_mfma_f32_16x16x32_bf16 v[44:47], a[200:203], v[148:151], v[44:47]\n\t" \
  "ds_read_b128 v[132:135], v242 offset:5120\n\t" \
  "v_mfma_f32_16x16x32_bf16 v[44:47], a[208:211], v[152:155], v[44:47]\n\t" \
  "ds_read_b128 v[136:139], v242 offset:6144\n\t" \
  "v_mfma_f32_16x16x32_bf16 v[44:47], a[216:219], v[156:159], v[44:47]\n\t" \
  "ds_read_b64_tr_b16 a[188:189], v237 offset:24576\n\t" \
  "v_mfma_f32_16x16x32_bf16 v[60:63], a[220:223], v[156:159], 0\n\t" \
  "ds_read_b128 v[140:143], v242 offset:7168\n\t" \
  "v_mfma_f32_16x16x32_bf16 v[60:63], a[212:215], v[152:155], v[60:63]\n\t" \
  "ds_read_b64_tr_b16 a[190:191], v237 offset:28672\n\t" \
  "v_mfma_f32_16x16x32_bf16 v[60:63], a[204:207], v[148:151], v[60:63]\n\t" \
  "ds_read_b64_tr_b16 a[0:1], v236 offset:49152\n\t" \
  "v_mfma_f32_16x16x32_bf16 v[60:63], a[196:199], v[144:147], v[60:63]\n\t" \
  "s_waitcnt lgkmcnt(9)\n\t" \
  "ds_read_b64_tr_b16 a[2:3], v236 offset:53248\n\t" \
  STP("s_memrealtime s[94:95]\n\t") \
  STP("s_memtime s[92:93]\n\t") \
  "v_mfma_f32_16x16x32_bf16 v[64:67], a[192:195], v[160:163], 0\n\t" \
  "ds_read_b128 v[144:147], v242 offset:8192\n\t" \
  "v_mfma_f32_16x16x32_bf16 v[64:67], a[200:203], v[164:167], v[64:67]\n\t" \
  "s_add_u32 s97, s64, 262144\n\t" \
  "ds_read_b128 v[148:151], v242 offset:9216\n\t" \
  "s_and_b32 s97, s97, 0xe0000\n\t" \
  "v_mfma_f32_16x16x32_bf16 v[64:67], a[208:211], v[168:171], v[64:67]\n\t" \
  "s_add_u32 s97, s97, s60\n\t" \
  "ds_read_b128 v[152:155], v242 offset:10240\n\t" \
  "s_add_u32 m0, s38, 65536\n\t" \
  "v_mfma_f32_16x16x32_bf16 v[64:67], a[216:219], v[172:175], v[64:67]\n\t" \
  "ds_read_b128 v[156:159], v242 offset:11264\n\t" \
  "v_mfma_f32_16x16x32_bf16 v[80:83], a[220:223], v[172:175], 0\n\t" \
  "ds_read_b64_tr_b16 a[4:5], v237 offset:49152\n\t" \
  "v_mfma_f32_16x16x32_bf16 v[80:83], a[212:215], v[168:171], v[80:83]\n\t" \
  "buffer_load_dword v246, v245, s[16:19], s65 offen sc1\n\t" \
  "v_mfma_f32_16x16x32_bf16 v[80:83], a[204:207], v[164:167], v[80:83]\n\t" \
  "ds_read_b64_tr_b16 a[6:7], v237 offset:53248\n\t" \
  "v_mfma_f32_16x16x32_bf16 v[80:83], a[196:199], v[160:163], v[80:83]\n\t" \
  "s_waitcnt lgkmcnt(9)\n\t" \
  "buffer_load_dwordx4 v242, s[16:19], s97 offen offset:0 sc1 lds\n\t" \
  "v_mfma_f32_16x16x32_bf16 v[68:71], a[192:195], v[128:131], 0\n\t" \
  "s_mov_b64 exec, 0\n\t" \
  "s_cmp_eq_u32 s3, 0\n\t" \
  "s_cbranch_scc1 .Lhc10\n\t" \
  "s_cmp_eq_u32 s3, 1\n\t" \
  "s_cbranch_scc1 .Lhc11\n\t" \
  "s_cmp_eq_u32 s3, 2\n\t" \
  "s_cbranch_scc1 .Lhc12\n\t" \
  "s_branch .Lhc13\n\t" \
  ".Lhr1:\n\t" \
  "s_mov_b64 exec, -1\n\t" \
  "v_mfma_f32_16x16x32_bf16 v[68:71], a[200:203], v[132:135], v[68:71]\n\t" \
  "ds_read_b128 v[160:163], v242 offset:12288\n\t" \
  "v_mfma_f32_16x16x32_bf16 v[68:71], a[208:211], v[136:139], v[68:71]\n\t" \
  "buffer_load_dwordx4 v242, s[16:19], s97 offen offset:1024 sc1 lds\n\t" \
  "v_mfma_f32_16x16x32_bf16 v[68:71], a[216:219], v[140:143], v[68:71]\n\t" \
  "ds_read_b64_tr_b16 a[8:9], v236 offset:57344\n\t" \
  "ds_read_b128 v[164:167], v242 offset:13312\n\t" \
  "v_mfma_f32_16x16x32_bf16 v[84:87], a[220:223], v[140:143], 0\n\t" \
  STP("v_writelane_b32 v255, s94, 26\n\t") \
  STP("v_writelane_b32 v255, s92, 27\n\t") \
  "ds_read_b128 v[168:171], v242 offset:14336\n\t" \
  "v_mfma_f32_16x16x32_bf16 v[84:87], a[212:215], v[136:139], v[84:87]\n\t" \
  "buffer_load_dwordx4 v242, s[16:19], s97 offen offset:2048 sc1 lds\n\t" \
  "v_mfma_f32_16x16x32_bf16 v[84:87], a[204:207], v[132:135], v[84:87]\n\t" \
  "s_waitcnt vmcnt(5) lgkmcnt(6)\n\t" \
  "ds_read_b128 v[172:175], v242 offset:15360\n\t" \
  "v_mfma_f32_16x16x32_bf16 v[84:87], a[196:199], v[128:131], v[84:87]\n\t" \
  "s_barrier\n\t" \
  "v_mfma_f32_16x16x32_bf16 v[72:75], a[192:195], v[144:147], 0\n\t" \
  "buffer_load_dwordx4 v242, s[16:19], s97 offen offset:3072 sc1 lds\n\t" \
  "v_mfma_f32_16x16x32_bf16 v[72:75], a[200:203], v[148:151], v[72:75]\n\t" \
  "s_add_u32 s97, s64, 393216\n\t" \
  "s_and_b32 s97, s97, 0xe0000\n\t" \
  "s_add_u32 s97, s97, s60\n\t" \
  "v_mfma_f32_16x16x32_bf16 v[72:75], a[208:211], v[152:155], v[72:75]\n\t" \
  "s_add_u32 m0, s38, 81920\n\t" \
  "ds_read_b64_tr_b16 a[10:11], v236 offset:61440\n\t" \
  "ds_read_b128 v[128:131], v242 offset:49152\n\t" \
  "v_mfma_f32_16x16x32_bf16 v[72:75], a[216:219], v[156:159], v[72:75]\n\t" \
  "buffer_load_dwordx4 v242, s[16:19], s97 offen offset:0 sc1 lds\n\t" \
  "v_mfma_f32_16x16x32_bf16 v[88:91], a[220:223], v[156:159], 0\n\t" \
  "ds_read_b64_tr_b16 a[12:13], v237 offset:57344\n\t" \
  "ds_read_b128 v[132:135], v242 offset:50176\n\t" \
  "v_mfma_f32_16x16x32_bf16 v[88:91], a[212:215], v[152:155], v[88:91]\n\t" \
  "ds_read_b64_tr_b16 a[14:15], v237 offset:61440\n\t" \
  "ds_read_b128 v[136:139], v242 offset:51200\n\t" \
  "v_mfma_f32_16x16x32_bf16 v[88:91], a[204:207], v[148:151], v[88:91]\n\t" \
  "buffer_load_dwordx4 v242, s[16:19], s97 offen offset:1024 sc1 lds\n\t" \
  "v_mfma_f32_16x16x32_bf16 v[88:91], a[196:199], v[144:147], v[88:91]\n\t" \
  "s_waitcnt lgkmcnt(6)\n\t" \
  "ds_read_b64_tr_b16 a[16:17], v238 offset:16384\n\t" \
  "ds_read_b128 v[140:143], v242 offset:52224\n\t" \
  "v_mfma_f32_16x16x32_bf16 v[76:79], a[192:195], v[160:163], 0\n\t" \
  "ds_read_b128 v[144:147], v242 offset:53248\n\t" \
  "v_mfma_f32_16x16x32_bf16 v[76:79], a[200:203], v[164:167], v[76:79]\n\t" \
  "buffer_load_dwordx4 v242, s[16:19], s97 offen offset:2048 sc1 lds\n\t" \
  "v_mfma_f32_16x16x32_bf16 v[76:79], a[208:211], v[168:171], v[76:79]\n\t" \
  "ds_read_b128 v[148:151], v242 offset:54272\n\t" \
  "v_mfma_f32_16x16x32_bf16 v[76:79], a[216:219], v[172:175], v[76:79]\n\t" \
  "ds_read_b128 v[152:155], v242 offset:55296\n\t" \
  "v_mfma_f32_16x16x32_bf16 v[92:95], a[220:223], v[172:175], 0\n\t" \
  "buffer_load_dwordx4 v242, s[16:19], s97 offen offset:3072 sc1 lds\n\t" \
  "v_mfma_f32_16x16x32_bf16 v[92:95], a[212:215], v[168:171], v[92:95]\n\t" \
  "s_add_u32 s96, s40, 1048576\n\t" \
  "s_and_b32 s96, s96, 0x1c0000\n\t" \
  "s_add_u32 s96, s96, s77\n\t" \
  "v_mfma_f32_16x16x32_bf16 v[92:95], a[204:207], v[164:167], v[92:95]\n\t" \
  "s_add_u32 m0, s38, 131072\n\t" \
  "ds_read_b64_tr_b16 a[18:19], v238 offset:20480\n\t" \
  "ds_read_b128 v[156:159], v242 offset:56320\n\t" \
  "v_mfma_f32_16x16x32_bf16 v[92:95], a[196:199], v[160:163], v[92:95]\n\t" \
  "s_waitcnt lgkmcnt(5)\n\t" \
  "buffer_load_dwordx4 v240, s[24:27], s96 offen offset:0 lds\n\t" \
  STP("s_memrealtime s[94:95]\n\t") \
  STP("s_memtime s[92:93]\n\t") \
  "v_mfma_f32_16x16x32_bf16 v[0:3], a[128:131], v[128:131], v[0:3]\n\t" \
  "ds_read_b64_tr_b16 a[20:21], v239 offset:16384\n\t" \
  "ds_read_b128 v[160:163], v242 offset:57344\n\t" \
  "v_accvgpr_read_b32 v96, a96\n\t" \
  "v_mfma_f32_16x16x32_bf16 v[0:3], a[136:139], v[132:135], v[0:3]\n\t" \
  "ds_read_b64_tr_b16 a[22:23], v239 offset:20480\n\t" \
  "ds_read_b128 v[164:167], v242 offset:58368\n\t" \
  "v_accvgpr_read_b32 v97, a97\n\t" \
  "v_mfma_f32_16x16x32_bf16 v[0:3], a[144:147], v[136:139], v[0:3]\n\t" \
  "buffer_load_dwordx4 v241, s[24:27], s96 offen offset:1024 lds\n\t" \
  "v_mfma_f32_16x16x32_bf16 v[0:3], a[152:155], v[140:143], v[0:3]\n\t" \
  "ds_read_b64_tr_b16 a[24:25], v238 offset:24576\n\t" \
  "ds_read_b128 v[168:171], v242 offset:59392\n\t" \
  "v_accvgpr_read_b32 v98, a98\n\t" \
  "v_mfma_f32_16x16x32_bf16 v[16:19], a[156:159], v[140:143], v[16:19]\n\t" \
  "s_waitcnt vmcnt(10)\n\t" \
  "v_cmp_ne_u32 vcc, s36, v246\n\t" \
  "v_accvgpr_read_b32 v99, a99\n\t" \
  "v_mfma_f32_16x16x32_bf16 v[16:19], a[148:151], v[136:139], v[16:19]\n\t" \
  ".Lhs11:\n\ts_cbranch_execz .Lhs11n\n\t" \
  "buffer_load_dwordx4 v248, s[24:27], s96 offen offset:2048 lds\n\t" \
  "v_mfma_f32_16x16x32_bf16 v[16:19], a[140:143], v[132:135], v[16:19]\n\t" \
  "s_and_b32 vcc_lo, vcc_lo, 0xfefe\n\t" \
  "s_cbranch_scc1 .Lat_s1\n\t" \
  ".Lat_k1:\n\t" \
  "v_mfma_f32_16x16x32_bf16 v[16:19], a[132:135], v[128:131], v[16:19]\n\t" \
  "s_waitcnt lgkmcnt(6)\n\t" \
  "ds_read_b64_tr_b16 a[26:27], v238 offset:28672\n\t" \
  "ds_read_b128 v[172:175], v242 offset:60416\n\t" \
  "v_accvgpr_read_b32 v100, a100\n\t" \
  "v_mfma_f32_16x16x32_bf16 v[4:7], a[128:131], v[144:147], v[4:7]\n\t" \
  "buffer_load_dwordx4 v249, s[24:27], s96 offen offset:3072 lds\n\t" \
  "v_mfma_f32_16x16x32_bf16 v[4:7], a[136:139], v[148:151], v[4:7]\n\t" \
  "s_add_u32 s97, s64, 131072\n\t" \
  "s_and_b32 s97, s97, 0xe0000\n\t" \
  "ds_read_b128 v[128:131], v242 offset:61440\n\t" \
  "s_add_u32 s97, s97, s61\n\t" \
  "v_mfma_f32_16x16x32_bf16 v[4:7], a[144:147], v[152:155], v[4:7]\n\t" \
  "s_add_u32 m0, s38, 32768\n\t" \
  "ds_read_b64_tr_b16 a[28:29], v239 offset:24576\n\t" \
  "ds_read_b128 v[132:135], v242 offset:62464\n\t" \
  "v_accvgpr_read_b32 v101, a101\n\t" \
  "v_mfma_f32_16x16x32_bf16 v[4:7], a[152:155], v[156:159], v[4:7]\n\t" \
  "buffer_load_dwordx4 v242, s[16:19], s97 offen offset:0 sc1 lds\n\t" \
  "v_mfma_f32_16x16x32_bf16 v[20:23], a[156:159], v[156:159], v[20:23]\n\t" \
  STP("v_writelane_b32 v255, s94, 28\n\t") \
  STP("v_writelane_b32 v255, s92, 29\n\t") \
  "ds_read_b128 v[136:139], v242 offset:63488\n\t" \
  "v_accvgpr_read_b32 v102, a102\n\t" \
  "v_mfma_f32_16x16x32_bf16 v[20:23], a[148:151], v[152:155], v[20:23]\n\t" \
  "ds_read_b64_tr_b16 a[30:31], v239 offset:28672\n\t" \
  "v_accvgpr_read_b32 v103, a103\n\t" \
  "v_mfma_f32_16x16x32_bf16 v[20:23], a[140:143], v[148:151], v[20:23]\n\t" \
  "s_waitcnt vmcnt(9) lgkmcnt(5)\n\t" \
  "buffer_load_dwordx4 v242, s[16:19], s97 offen offset:1024 sc1 lds\n\t" \
  "v_mfma_f32_16x16x32_bf16 v[20:23], a[132:135], v[144:147], v[20:23]\n\t" \
  "s_barrier\n\t" \
  "v_mfma_f32_16x16x32_bf16 v[8:11], a[128:131], v[160:163], v[8:11]\n\t" \
  "ds_read_b128 v[140:143], v242 offset:64512\n\t" \
  "v_mul_f32 v96, v96, v222\n\t" \
  "v_mfma_f32_16x16x32_bf16 v[8:11], a[136:139], v[164:167], v[8:11]\n\t" \
  "buffer_load_dwordx4 v242, s[16:19], s97 offen offset:2048 sc1 lds\n\t" \
  "v_mfma_f32_16x16x32_bf16 v[8:11], a[144:147], v[168:171], v[8:11]\n\t" \
  "ds_read_b128 v[144:147], v230 offset:0\n\t" \
  "v_mul_f32 v97, v97, v222\n\t" \
  "v_mfma_f32_16x16x32_bf16 v[8:11], a[152:155], v[172:175], v[8:11]\n\t" \
  "ds_read_b128 v[148:151], v230 offset:1024\n\t" \
  "v_mul_f32 v98, v98, v222\n\t" \
  "v_mfma_f32_16x16x32_bf16 v[24:27], a[156:159], v[172:175], v[24:27]\n\t" \
  "buffer_load_dwordx4 v242, s[16:19], s97 offen offset:3072 sc1 lds\n\t" \
  "v_mfma_f32_16x16x32_bf16 v[24:27], a[148:151], v[168:171], v[24:27]\n\t" \
  "ds_read_b128 v[152:155], v230 offset:2048\n\t" \
  "v_mul_f32 v99, v99, v222\n\t" \
  "v_mfma_f32_16x16x32_bf16 v[24:27], a[140:143], v[164:167], v[24:27]\n\t" \
  "ds_read_b64_tr_b16 a[32:33], v236 offset:32768\n\t" \
  "v_mul_f32 v100, v100, v222\n\t" \
  "v_mfma_f32_16x16x32_bf16 v[24:27], a[132:135], v[160:163], v[24:27]\n\t" \
  "s_waitcnt lgkmcnt(4)\n\t" \
  "buffer_store_dwordx4 v[124:127], v242, s[16:19], s98 offen offset:3072 sc1\n\t" \
  "ds_read_b64_tr_b16 a[34:35], v236 offset:36864\n\t" \
  "ds_read_b64_tr_b16 a[36:37], v237 offset:32768\n\t" \
  "ds_read_b128 v[156:159], v230 offset:3072\n\t" \
  "v_mfma_f32_16x16x32_bf16 v[12:15], a[128:131], v[128:131], v[12:15]\n\t" \
  "s_xor_b32 s98, s45, 98304\n\t" \
  "ds_read_b128 v[160:163], v230 offset:4096\n\t" \
  "v_mul_f32 v101, v101, v222\n\t" \
  "v_mfma_f32_16x16x32_bf16 v[12:15], a[136:139], v[132:135], v[12:15]\n\t" \
  "ds_read_b64_tr_b16 a[38:39], v237 offset:36864\n\t" \
  "v_mul_f32 v102, v102, v222\n\t" \
  "v_mfma_f32_16x16x32_bf16 v[12:15], a[144:147], v[136:139], v[12:15]\n\t" \
  "s_add_u32 s97, s64, 524288\n\t" \
  "ds_read_b128 v[164:167], v230 offset:5120\n\t" \
  "v_mul_f32 v103, v103, v222\n\t" \
  "v_mfma_f32_16x16x32_bf16 v[12:15], a[152:155], v[140:143], v[12:15]\n\t" \
  "s_and_b32 s97, s97, 0xe0000\n\t" \
  "ds_read_b64_tr_b16 a[40:41], v236 offset:40960\n\t" \
  "v_cvt_pk_bf16_f32 v96, v96, v97\n\t" \
  "v_mfma_f32_16x16x32_bf16 v[28:31], a[156:159], v[140:143], v[28:31]\n\t" \
  "s_add_u32 s97, s97, s60\n\t" \
  "ds_read_b128 v[168:171], v230 offset:6144\n\t" \
  "v_cvt_pk_bf16_f32 v97, v98, v99\n\t" \
  "v_mfma_f32_16x16x32_bf16 v[28:31], a[148:151], v[136:139], v[28:31]\n\t" \
  "s_add_u32 m0, s38, 0\n\t" \
  "ds_read_b64_tr_b16 a[42:43], v236 offset:45056\n\t" \
  "v_cvt_pk_bf16_f32 v98, v100, v101\n\t" \
  "v_mfma_f32_16x16x32_bf16 v[28:31], a[140:143], v[132:135], v[28:31]\n\t" \
  "ds_read_b128 v[172:175], v230 offset:7168\n\t" \
  "v_cvt_pk_bf16_f32 v99, v102, v103\n\t" \
  "v_mfma_f32_16x16x32_bf16 v[28:31], a[132:135], v[128:131], v[28:31]\n\t" \
  "s_waitcnt lgkmcnt(7)\n\t" \
  "ds_write_b128 v213, v[96:99] offset:16384\n\t" \
  STP("s_memrealtime s[94:95]\n\t") \
  STP("s_memtime s[92:93]\n\t") \
  "v_mfma_f32_16x16x32_bf16 v[0:3], a[160:163], v[144:147], v[0:3]\n\t" \
  "buffer_load_dwordx4 v242, s[16:19], s97 offen offset:0 sc1 lds\n\t" \
  "v_mfma_f32_16x16x32_bf16 v[0:3], a[168:171], v[148:151], v[0:3]\n\t" \
  "ds_read_b128 v[128:131], v230 offset:8192\n\t" \
  "v_accvgpr_read_b32 v104, a104\n\t" \
  "v_mfma_f32_16x16x32_bf16 v[0:3], a[176:179], v[152:155], v[0:3]\n\t" \
  "ds_read_b64_tr_b16 a[44:45], v237 offset:40960\n\t" \
  "v_accvgpr_read_b32 v105, a105\n\t" \
  "v_mfma_f32_16x16x32_bf16 v[0:3], a[184:187], v[156:159], v[0:3]\n\t" \
  "buffer_load_dwordx4 v242, s[16:19], s97 offen offset:1024 sc1 lds\n\t" \
  "v_mfma_f32_16x16x32_bf16 v[16:19], a[188:191], v[156:159], v[16:19]\n\t" \
  "ds_read_b128 v[132:135], v230 offset:9216\n\t" \
  "v_accvgpr_read_b32 v106, a106\n\t" \
  "v_mfma_f32_16x16x32_bf16 v[16:19], a[180:183], v[152:155], v[16:19]\n\t" \
  "ds_read_b128 v[136:139], v230 offset:10240\n\t" \
  "v_accvgpr_read_b32 v107, a107\n\t" \
  "v_mfma_f32_16x16x32_bf16 v[16:19], a[172:175], v[148:151], v[16:19]\n\t" \
  "ds_read_b64_tr_b16 a[46:47], v237 offset:45056\n\t" \
  "v_accvgpr_read_b32 v108, a108\n\t" \
  "v_mfma_f32_16x16x32_bf16 v[16:19], a[164:167], v[144:147], v[16:19]\n\t" \
  "s_waitcnt lgkmcnt(6)\n\t" \
  "ds_read_b128 v[140:143], v230 offset:11264\n\t" \
  "v_accvgpr_read_b32 v109, a109\n\t" \
  "v_mfma_f32_16x16x32_bf16 v[4:7], a[160:163], v[160:163], v[4:7]\n\t" \
  "ds_read_b128 v[144:147], v230 offset:12288\n\t" \
  "v_accvgpr_read_b32 v110, a110\n\t" \
  "v_mfma_f32_16x16x32_bf16 v[4:7], a[168:171], v[164:167], v[4:7]\n\t" \
  "v_accvgpr_read_b32 v111, a111\n\t" \
  "v_mul_f32 v104, v104, v222\n\t" \
  "v_mfma_f32_16x16x32_bf16 v[4:7], a[176:179], v[168:171], v[4:7]\n\t" \
  "ds_read_b128 v[148:151], v230 offset:13312\n\t" \
  "v_mul_f32 v105, v105, v222\n\t" \
  "v_mfma_f32_16x16x32_bf16 v[4:7], a[184:187], v[172:175], v[4:7]\n\t" \
  "buffer_load_dwordx4 v242, s[16:19], s97 offen offset:2048 sc1 lds\n\t" \
  "v_mfma_f32_16x16x32_bf16 v[20:23], a[188:191], v[172:175], v[20:23]\n\t" \
  STP("v_writelane_b32 v255, s94, 30\n\t") \
  STP("v_writelane_b32 v255, s92, 31\n\t") \
  "ds_read_b128 v[152:155], v230 offset:14336\n\t" \
  "v_mul_f32 v106, v106, v222\n\t" \
  "v_mfma_f32_16x16x32_bf16 v[20:23], a[180:183], v[168:171], v[20:23]\n\t" \
  "v_mul_f32 v107, v107, v222\n\t" \
  "v_mul_f32 v108, v108, v222\n\t" \
  "v_mfma_f32_16x16x32_bf16 v[20:23], a[172:175], v[164:167], v[20:23]\n\t" \
  "s_waitcnt vmcnt(12) lgkmcnt(3)\n\t" \
  "buffer_load_dwordx4 v242, s[16:19], s97 offen offset:3072 sc1 lds\n\t" \
  "v_mfma_f32_16x16x32_bf16 v[20:23], a[164:167], v[160:163], v[20:23]\n\t" \
  "s_barrier\n\t" \
  "v_mfma_f32_16x16x32_bf16 v[8:11], a[160:163], v[128:131], v[8:11]\n\t" \
  "ds_read_b128 v[156:159], v230 offset:15360\n\t" \
  "v_mul_f32 v109, v109, v222\n\t" \
  "v_mfma_f32_16x16x32_bf16 v[8:11], a[168:171], v[132:135], v[8:11]\n\t" \
  "buffer_store_dwordx4 v[96:99], v242, s[16:19], s98 offen offset:0 sc1\n\t" \
  "v_mul_f32 v110, v110, v222\n\t" \
  "ds_read_b128 v[160:163], v230 offset:16384\n\t" \
  "v_mul_f32 v111, v111, v222\n\t" \
  "v_mfma_f32_16x16x32_bf16 v[8:11], a[176:179], v[136:139], v[8:11]\n\t" \
  "s_add_u32 s96, s40, 1310720\n\t" \
  "ds_read_b128 v[164:167], v230 offset:17408\n\t" \
  "v_cvt_pk_bf16_f32 v104, v104, v105\n\t" \
  "v_mfma_f32_16x16x32_bf16 v[8:11], a[184:187], v[140:143], v[8:11]\n\t" \
  "s_and_b32 s96, s96, 0x1c0000\n\t" \
  "v_cvt_pk_bf16_f32 v105, v106, v107\n\t" \
  "v_cvt_pk_bf16_f32 v106, v108, v109\n\t" \
  "v_mfma_f32_16x16x32_bf16 v[24:27], a[188:191], v[140:143], v[24:27]\n\t" \
  "s_add_u32 s96, s96, s51\n\t" \
  "ds_read_b128 v[168:171], v230 offset:18432\n\t" \
  "v_cvt_pk_bf16_f32 v107, v110, v111\n\t" \
  "v_mfma_f32_16x16x32_bf16 v[24:27], a[180:183], v[136:139], v[24:27]\n\t" \
  "ds_write_b128 v213, v[104:107] offset:17408\n\t" \
  "v_mfma_f32_16x16x32_bf16 v[24:27], a[172:175], v[132:135], v[24:27]\n\t" \
  "s_add_u32 m0, s38, 147456\n\t" \
  "ds_read_b128 v[172:175], v230 offset:19456\n\t" \
  "v_accvgpr_read_b32 v96, a112\n\t" \
  "v_mfma_f32_16x16x32_bf16 v[24:27], a[164:167], v[128:131], v[24:27]\n\t" \
  "s_waitcnt lgkmcnt(5)\n\t" \
  "v_accvgpr_read_b32 v97, a113\n\t" \
  "v_accvgpr_read_b32 v98, a114\n\t" \
  "v_mfma_f32_16x16x32_bf16 v[12:15], a[160:163], v[144:147], v[12:15]\n\t" \
  "buffer_load_dwordx4 v240, s[24:27], s96 offen offset:0 lds\n\t" \
  "v_mfma_f32_16x16x32_bf16 v[12:15], a[168:171], v[148:151], v[12:15]\n\t" \
  "ds_read_b128 v[128:131], v230 offset:20480\n\t" \
  "v_accvgpr_read_b32 v99, a115\n\t" \
  "v_mfma_f32_16x16x32_bf16 v[12:15], a[176:179], v[152:155], v[12:15]\n\t" \
  "ds_read_b128 v[132:135], v230 offset:21504\n\t" \
  "v_accvgpr_read_b32 v100, a116\n\t" \
  "v_mfma_f32_16x16x32_bf16 v[12:15], a[184:187], v[156:159], v[12:15]\n\t" \
  "v_accvgpr_read_b32 v101, a117\n\t" \
  "v_accvgpr_read_b32 v102, a118\n\t" \
  "v_mfma_f32_16x16x32_bf16 v[28:31], a[188:191], v[156:159], v[28:31]\n\t" \
  "buffer_load_dwordx4 v241, s[24:27], s96 offen offset:1024 lds\n\t" \
  "v_mfma_f32_16x16x32_bf16 v[28:31], a[180:183], v[152:155], v[28:31]\n\t" \
  "ds_read_b128 v[136:139], v230 offset:22528\n\t" \
  "v_accvgpr_read_b32 v103, a119\n\t" \
  "v_mfma_f32_16x16x32_bf16 v[28:31], a[172:175], v[148:151], v[28:31]\n\t" \
  "ds_read_b128 v[140:143], v230 offset:23552\n\t" \
  "v_mul_f32 v96, v96, v222\n\t" \
  "v_mfma_f32_16x16x32_bf16 v[28:31], a[164:167], v[144:147], v[28:31]\n\t" \
  "s_waitcnt lgkmcnt(4)\n\t" \
  "buffer_load_dwordx4 v248, s[24:27], s96 offen offset:2048 lds\n\t" \
  "v_mfma_f32_16x16x32_bf16 v[0:3], a[0:3], v[160:163], v[0:3]\n\t" \
  "ds_read_b128 v[144:147], v230 offset:24576\n\t" \
  "v_mul_f32 v97, v97, v222\n\t" \
  "v_mfma_f32_16x16x32_bf16 v[0:3], a[8:11], v[164:167], v[0:3]\n\t" \
  "v_mul_f32 v98, v98, v222\n\t" \
  "v_mul_f32 v99, v99, v222\n\t" \
  "v_mfma_f32_16x16x32_bf16 v[0:3], a[16:19], v[168:171], v[0:3]\n\t" \
  "buffer_load_dwordx4 v249, s[24:27], s96 offen offset:3072 lds\n\t" \
  "v_mfma_f32_16x16x32_bf16 v[0:3], a[24:27], v[172:175], v[0:3]\n\t" \
  "s_add_u32 s97, s64, 262144\n\t" \
  "s_and_b32 s97, s97, 0xe0000\n\t" \
  "s_add_u32 s97, s97, s61\n\t" \
  "v_mfma_f32_16x16x32_bf16 v[16:19], a[28:31], v[172:175], v[16:19]\n\t" \
  "s_add_u32 m0, s38, 49152\n\t" \
  "ds_read_b128 v[148:151], v230 offset:25600\n\t" \
  "v_mul_f32 v100, v100, v222\n\t" \
  "v_mfma_f32_16x16x32_bf16 v[16:19], a[20:23], v[168:171], v[16:19]\n\t" \
  "buffer_load_dwordx4 v242, s[16:19], s97 offen offset:0 sc1 lds\n\t" \
  "v_mfma_f32_16x16x32_bf16 v[16:19], a[12:15], v[164:167], v[16:19]\n\t" \
  "ds_read_b128 v[152:155], v230 offset:26624\n\t" \
  "v_mul_f32 v101, v101, v222\n\t" \
  "v_mfma_f32_16x16x32_bf16 v[16:19], a[4:7], v[160:163], v[16:19]\n\t" \
  "s_waitcnt lgkmcnt(3)\n\t" \
  "ds_read_b128 v[156:159], v230 offset:27648\n\t" \
  "v_mul_f32 v102, v102, v222\n\t" \
  "v_mfma_f32_16x16x32_bf16 v[4:7], a[0:3], v[128:131], v[4:7]\n\t" \
  "ds_read_b128 v[160:163], v230 offset:28672\n\t" \
  "v_mul_f32 v103, v103, v222\n\t" \
  "v_mfma_f32_16x16x32_bf16 v[4:7], a[8:11], v[132:135], v[4:7]\n\t" \
  "v_cvt_pk_bf16_f32 v96, v96, v97\n\t" \
  "v_cvt_pk_bf16_f32 v97, v98, v99\n\t" \
  "v_mfma_f32_16x16x32_bf16 v[4:7], a[16:19], v[136:139], v[4:7]\n\t" \
  "ds_read_b128 v[164:167], v230 offset:29696\n\t" \
  "v_cvt_pk_bf16_f32 v98, v100, v101\n\t" \
  "v_mfma_f32_16x16x32_bf16 v[4:7], a[24:27], v[140:143], v[4:7]\n\t" \
  "buffer_load_dwordx4 v242, s[16:19], s97 offen offset:1024 sc1 lds\n\t" \
  "v_mfma_f32_16x16x32_bf16 v[20:23], a[28:31], v[140:143], v[20:23]\n\t" \
  "ds_read_b128 v[168:171], v230 offset:30720\n\t" \
  "v_cvt_pk_bf16_f32 v99, v102, v103\n\t" \
  "v_mfma_f32_16x16x32_bf16 v[20:23], a[20:23], v[136:139], v[20:23]\n\t" \
  "ds_write_b128 v213, v[96:99] offset:18432\n\t" \
  "v_mfma_f32_16x16x32_bf16 v[20:23], a[12:15], v[132:135], v[20:23]\n\t" \
  "s_waitcnt vmcnt(12) lgkmcnt(4)\n\t" \
  "buffer_load_dwordx4 v242, s[16:19], s97 offen offset:2048 sc1 lds\n\t" \
  "v_mfma_f32_16x16x32_bf16 v[20:23], a[4:7], v[128:131], v[20:23]\n\t" \
  "s_barrier\n\t" \
  "v_mfma_f32_16x16x32_bf16 v[8:11], a[0:3], v[144:147], v[8:11]\n\t" \
  "ds_read_b128 v[172:175], v230 offset:31744\n\t" \
  "v_mfma_f32_16x16x32_bf16 v[8:11], a[8:11], v[148:151], v[8:11]\n\t" \
  "buffer_load_dwordx4 v242, s[16:19], s97 offen offset:3072 sc1 lds\n\t" \
  "v_mfma_f32_16x16x32_bf16 v[8:11], a[16:19], v[152:155], v[8:11]\n\t" \
  "ds_read_b128 v[128:131], v242 offset:32768\n\t" \
  "v_mfma_f32_16x16x32_bf16 v[8:11], a[24:27], v[156:159], v[8:11]\n\t" \
  "ds_read_b128 v[132:135], v242 offset:33792\n\t" \
  "v_mfma_f32_16x16x32_bf16 v[24:27], a[28:31], v[156:159], v[24:27]\n\t" \
  "buffer_store_dwordx4 v[104:107], v242, s[16:19], s98 offen offset:1024 sc1\n\t" \
  "ds_read_b64_tr_b16 a[48:49], v238 offset:0\n\t" \
  "ds_read_b64_tr_b16 a[50:51], v238 offset:4096\n\t" \
  "ds_read_b128 v[136:139], v242 offset:34816\n\t" \
  "v_mfma_f32_16x16x32_bf16 v[24:27], a[20:23], v[152:155], v[24:27]\n\t" \
  "v_accvgpr_read_b32 v104, a120\n\t" \
  "v_accvgpr_read_b32 v105, a121\n\t" \
  "v_mfma_f32_16x16x32_bf16 v[24:27], a[12:15], v[148:151], v[24:27]\n\t" \
  "ds_read_b128 v[140:143], v242 offset:35840\n\t" \
  "v_accvgpr_read_b32 v106, a122\n\t" \
  "v_mfma_f32_16x16x32_bf16 v[24:27], a[4:7], v[144:147], v[24:27]\n\t" \
  "s_waitcnt lgkmcnt(6)\n\t" \
  "ds_read_b64_tr_b16 a[52:53], v239 offset:0\n\t" \
  "v_accvgpr_read_b32 v107, a123\n\t" \
  "v_mfma_f32_16x16x32_bf16 v[12:15], a[0:3], v[160:163], v[12:15]\n\t" \
  "s_add_u32 s96, s40, 1310720\n\t" \
  "ds_read_b128 v[144:147], v242 offset:36864\n\t" \
  "v_accvgpr_read_b32 v108, a124\n\t" \
  "v_mfma_f32_16x16x32_bf16 v[12:15], a[8:11], v[164:167], v[12:15]\n\t" \
  "s_and_b32 s96, s96, 0x1c0000\n\t" \
  "ds_read_b64_tr_b16 a[54:55], v239 offset:4096\n\t" \
  "v_accvgpr_read_b32 v109, a125\n\t" \
  "v_mfma_f32_16x16x32_bf16 v[12:15], a[16:19], v[168:171], v[12:15]\n\t" \
  "s_add_u32 s96, s96, s77\n\t" \
  "ds_read_b128 v[148:151], v242 offset:37888\n\t" \
  "v_accvgpr_read_b32 v110, a126\n\t" \
  "v_mfma_f32_16x16x32_bf16 v[12:15], a[24:27], v[172:175], v[12:15]\n\t" \
  "s_add_u32 m0, s38, 65536\n\t" \
  "ds_read_b64_tr_b16 a[56:57], v238 offset:8192\n\t" \
  "v_accvgpr_read_b32 v111, a127\n\t" \
  "v_mfma_f32_16x16x32_bf16 v[28:31], a[28:31], v[172:175], v[28:31]\n\t" \
  "ds_read_b128 v[152:155], v242 offset:38912\n\t" \
  "v_mul_f32 v104, v104, v222\n\t" \
  "v_mfma_f32_16x16x32_bf16 v[28:31], a[20:23], v[168:171], v[28:31]\n\t" \
  "buffer_load_dwordx4 v240, s[24:27], s96 offen offset:0 lds\n\t" \
  "v_mfma_f32_16x16x32_bf16 v[28:31], a[12:15], v[164:167], v[28:31]\n\t" \
  "ds_read_b128 v[156:159], v242 offset:39936\n\t" \
  "v_mul_f32 v105, v105, v222\n\t" \
  "v_mfma_f32_16x16x32_bf16 v[28:31], a[4:7], v[160:163], v[28:31]\n\t" \
  "s_waitcnt lgkmcnt(7)\n\t" \
  "ds_read_b64_tr_b16 a[58:59], v238 offset:12288\n\t" \
  "v_mul_f32 v106, v106, v222\n\t" \
  "v_mfma_f32_16x16x32_bf16 v[32:35], a[128:131], v[128:131], v[32:35]\n\t" \
  "ds_read_b128 v[160:163], v242 offset:40960\n\t" \
  "v_mul_f32 v107, v107, v222\n\t" \
  "v_mfma_f32_16x16x32_bf16 v[32:35], a[136:139], v[132:135], v[32:35]\n\t" \
  "ds_read_b64_tr_b16 a[60:61], v239 offset:8192\n\t" \
  "v_mul_f32 v108, v108, v222\n\t" \
  "v_mfma_f32_16x16x32_bf16 v[32:35], a[144:147], v[136:139], v[32:35]\n\t" \
  "ds_read_b128 v[164:167], v242 offset:41984\n\t" \
  "v_mul_f32 v109, v109, v222\n\t" \
  "v_mfma_f32_16x16x32_bf16 v[32:35], a[152:155], v[140:143], v[32:35]\n\t" \
  "buffer_load_dwordx4 v241, s[24:27], s96 offen offset:1024 lds\n\t" \
  "v_mfma_f32_16x16x32_bf16 v[48:51], a[156:159], v[140:143], v[48:51]\n\t" \
  "ds_read_b128 v[168:171], v242 offset:43008\n\t" \
  "v_mul_f32 v110, v110, v222\n\t" \
  "v_mfma_f32_16x16x32_bf16 v[48:51], a[148:151], v[136:139], v[48:51]\n\t" \
  "ds_read_b64_tr_b16 a[62:63], v239 offset:12288\n\t" \
  "v_mul_f32 v111, v111, v222\n\t" \
  "v_mfma_f32_16x16x32_bf16 v[48:51], a[140:143], v[132:135], v[48:51]\n\t" \
  "ds_read_b128 v[172:175], v242 offset:44032\n\t" \
  "v_cvt_pk_bf16_f32 v104, v104, v105\n\t" \
  "v_mfma_f32_16x16x32_bf16 v[48:51], a[132:135], v[128:131], v[48:51]\n\t" \
  "s_waitcnt lgkmcnt(7)\n\t" \
  "v_cvt_pk_bf16_f32 v105, v106, v107\n\t" \
  "v_cvt_pk_bf16_f32 v106, v108, v109\n\t" \
  "v_mfma_f32_16x16x32_bf16 v[36:39], a[128:131], v[144:147], v[36:39]\n\t" \
  "ds_read_b128 v[128:131], v242 offset:45056\n\t" \
  "v_cvt_pk_bf16_f32 v107, v110, v111\n\t" \
  "v_mfma_f32_16x16x32_bf16 v[36:39], a[136:139], v[148:151], v[36:39]\n\t" \
  "ds_write_b128 v213, v[104:107] offset:19456\n\t" \
  "v_mfma_f32_16x16x32_bf16 v[36:39], a[144:147], v[152:155], v[36:39]\n\t" \
  "ds_read_b128 v[132:135], v242 offset:46080\n\t" \
  "v_mfma_f32_16x16x32_bf16 v[36:39], a[152:155], v[156:159], v[36:39]\n\t" \
  "ds_read_b128 v[136:139], v242 offset:47104\n\t" \
  "v_mfma_f32_16x16x32_bf16 v[52:55], a[156:159], v[156:159], v[52:55]\n\t" \
  "ds_read_b128 v[140:143], v242 offset:48128\n\t" \
  "v_mfma_f32_16x16x32_bf16 v[52:55], a[148:151], v[152:155], v[52:55]\n\t" \
  "v_mfma_f32_16x16x32_bf16 v[52:55], a[140:143], v[148:151], v[52:55]\n\t" \
  "s_waitcnt vmcnt(12) lgkmcnt(5)\n\t" \
  "buffer_load_dwordx4 v248, s[24:27], s96 offen offset:2048 lds\n\t" \
  "v_mfma_f32_16x16x32_bf16 v[52:55], a[132:135], v[144:147], v[52:55]\n\t" \
  "s_barrier\n\t" \
  "v_mfma_f32_16x16x32_bf16 v[40:43], a[128:131], v[160:163], v[40:43]\n\t" \
  "ds_read_b128 v[144:147], v242 offset:0\n\t" \
  "v_mfma_f32_16x16x32_bf16 v[40:43], a[136:139], v[164:167], v[40:43]\n\t" \
  "buffer_load_dwordx4 v249, s[24:27], s96 offen offset:3072 lds\n\t" \
  "v_mfma_f32_16x16x32_bf16 v[40:43], a[144:147], v[168:171], v[40:43]\n\t" \
  "s_add_u32 s96, s40, 1572864\n\t" \
  "ds_read_b128 v[148:151], v242 offset:1024\n\t" \
  "s_and_b32 s96, s96, 0x1c0000\n\t" \
  "v_mfma_f32_16x16x32_bf16 v[40:43], a[152:155], v[172:175], v[40:43]\n\t" \
  "s_add_u32 s96, s96, s51\n\t" \
  "ds_read_b128 v[152:155], v242 offset:2048\n\t" \
  "s_add_u32 m0, s38, 81920\n\t" \
  "v_mfma_f32_16x16x32_bf16 v[56:59], a[156:159], v[172:175], v[56:59]\n\t" \
  "buffer_store_dwordx4 v[96:99], v242, s[16:19], s98 offen offset:2048 sc1\n\t" \
  "ds_read_b128 v[156:159], v242 offset:3072\n\t" \
  "v_mfma_f32_16x16x32_bf16 v[56:59], a[148:151], v[168:171], v[56:59]\n\t" \
  "v_mfma_f32_16x16x32_bf16 v[56:59], a[140:143], v[164:167], v[56:59]\n\t" \
  "v_mfma_f32_16x16x32_bf16 v[56:59], a[132:135], v[160:163], v[56:59]\n\t" \
  "s_waitcnt lgkmcnt(4)\n\t" \
  "buffer_load_dwordx4 v240, s[24:27], s96 offen offset:0 lds\n\t" \
  "v_mfma_f32_16x16x32_bf16 v[44:47], a[128:131], v[128:131], v[44:47]\n\t" \
  "ds_read_b128 v[160:163], v242 offset:4096\n\t" \
  "v_mfma_f32_16x16x32_bf16 v[44:47], a[136:139], v[132:135], v[44:47]\n\t" \
  "ds_read_b128 v[164:167], v242 offset:5120\n\t" \
  "v_mfma_f32_16x16x32_bf16 v[44:47], a[144:147], v[136:139], v[44:47]\n\t" \
  "buffer_load_dwordx4 v241, s[24:27], s96 offen offset:1024 lds\n\t" \
  "v_mfma_f32_16x16x32_bf16 v[44:47], a[152:155], v[140:143], v[44:47]\n\t" \
  "ds_read_b128 v[168:171], v242 offset:6144\n\t" \
  "v_mfma_f32_16x16x32_bf16 v[60:63], a[156:159], v[140:143], v[60:63]\n\t" \
  "ds_read_b128 v[172:175], v242 offset:7168\n\t" \
  "v_mfma_f32_16x16x32_bf16 v[60:63], a[148:151], v[136:139], v[60:63]\n\t" \
  "buffer_load_dwordx4 v248, s[24:27], s96 offen offset:2048 lds\n\t" \
  "v_mfma_f32_16x16x32_bf16 v[60:63], a[140:143], v[132:135], v[60:63]\n\t" \
  "v_mfma_f32_16x16x32_bf16 v[60:63], a[132:135], v[128:131], v[60:63]\n\t" \
  "s_waitcnt lgkmcnt(4)\n\t" \
  "v_mfma_f32_16x16x32_bf16 v[0:3], a[32:35], v[144:147], v[0:3]\n\t" \
  "buffer_load_dwordx4 v249, s[24:27], s96 offen offset:3072 lds\n\t" \
  "v_mfma_f32_16x16x32_bf16 v[0:3], a[40:43], v[148:151], v[0:3]\n\t" \
  "ds_read_b128 v[128:131], v242 offset:8192\n\t" \
  "v_mfma_f32_16x16x32_bf16 v[0:3], a[48:51], v[152:155], v[0:3]\n\t" \
  "ds_read_b128 v[132:135], v242 offset:9216\n\t" \
  "v_mfma_f32_16x16x32_bf16 v[0:3], a[56:59], v[156:159], v[0:3]\n\t" \
  "buffer_store_dwordx4 v[104:107], v242, s[16:19], s98 offen offset:3072 sc1\n\t" \
  "ds_read_b128 v[136:139], v242 offset:10240\n\t" \
  "ds_read_b128 v[140:143], v242 offset:11264\n\t" \
  "v_mfma_f32_16x16x32_bf16 v[16:19], a[60:63], v[156:159], v[16:19]\n\t" \
  "v_mfma_f32_16x16x32_bf16 v[16:19], a[52:55], v[152:155], v[16:19]\n\t" \
  "v_mfma_f32_16x16x32_bf16 v[16:19], a[44:47], v[148:151], v[16:19]\n\t" \
  "v_mfma_f32_16x16x32_bf16 v[16:19], a[36:39], v[144:147], v[16:19]\n\t" \
  "s_waitcnt lgkmcnt(4)\n\t" \
  "v_mfma_f32_16x16x32_bf16 v[4:7], a[32:35], v[160:163], v[4:7]\n\t" \
  "ds_read_b128 v[144:147], v242 offset:12288\n\t" \
  "v_mfma_f32_16x16x32_bf16 v[4:7], a[40:43], v[164:167], v[4:7]\n\t" \
  "ds_read_b128 v[148:151], v242 offset:13312\n\t" \
  "v_mfma_f32_16x16x32_bf16 v[4:7], a[48:51], v[168:171], v[4:7]\n\t" \
  "s_add_u32 s97, s64, 655360\n\t" \
  "ds_read_b128 v[152:155], v242 offset:14336\n\t" \
  "s_and_b32 s97, s97, 0xe0000\n\t" \
  "v_mfma_f32_16x16x32_bf16 v[4:7], a[56:59], v[172:175], v[4:7]\n\t" \
  "s_add_u32 s97, s97, s60\n\t" \
  "ds_read_b128 v[156:159], v242 offset:15360\n\t" \
  "s_add_u32 m0, s38, 131072\n\t" \
  "v_mfma_f32_16x16x32_bf16 v[20:23], a[60:63], v[172:175], v[20:23]\n\t" \
  "v_mfma_f32_16x16x32_bf16 v[20:23], a[52:55], v[168:171], v[20:23]\n\t" \
  "v_mfma_f32_16x16x32_bf16 v[20:23], a[44:47], v[164:167], v[20:23]\n\t" \
  "s_waitcnt vmcnt(11) lgkmcnt(4)\n\t" \
  "v_mfma_f32_16x16x32_bf16 v[20:23], a[36:39], v[160:163], v[20:23]\n\t" \
  "s_barrier\n\t" \
  "v_mfma_f32_16x16x32_bf16 v[8:11], a[32:35], v[128:131], v[8:11]\n\t" \
  "buffer_load_dwordx4 v242, s[16:19], s97 offen offset:0 sc1 lds\n\t" \
  "v_mfma_f32_16x16x32_bf16 v[8:11], a[40:43], v[132:135], v[8:11]\n\t" \
  "ds_read_b128 v[160:163], v242 offset:49152\n\t" \
  "v_mfma_f32_16x16x32_bf16 v[8:11], a[48:51], v[136:139], v[8:11]\n\t" \
  "ds_read_b128 v[164:167], v242 offset:50176\n\t" \
  "v_mfma_f32_16x16x32_bf16 v[8:11], a[56:59], v[140:143], v[8:11]\n\t" \
  "buffer_load_dwordx4 v242, s[16:19], s97 offen offset:1024 sc1 lds\n\t" \
  "v_mfma_f32_16x16x32_bf16 v[24:27], a[60:63], v[140:143], v[24:27]\n\t" \
  "ds_read_b128 v[168:171], v242 offset:51200\n\t" \
  "v_mfma_f32_16x16x32_bf16 v[24:27], a[52:55], v[136:139], v[24:27]\n\t" \
  "ds_read_b64_tr_b16 a[64:65], v238 offset:16384\n\t" \
  "v_mfma_f32_16x16x32_bf16 v[24:27], a[44:47], v[132:135], v[24:27]\n\t" \
  "buffer_load_dwordx4 v242, s[16:19], s97 offen offset:2048 sc1 lds\n\t" \
  "v_mfma_f32_16x16x32_bf16 v[24:27], a[36:39], v[128:131], v[24:27]\n\t" \
  "s_waitcnt lgkmcnt(4)\n\t" \
  "ds_read_b64_tr_b16 a[66:67], v238 offset:20480\n\t" \
  "ds_read_b128 v[172:175], v242 offset:52224\n\t" \
  "v_mfma_f32_16x16x32_bf16 v[12:15], a[32:35], v[144:147], v[12:15]\n\t" \
  "ds_read_b128 v[128:131], v242 offset:53248\n\t" \
  "v_mfma_f32_16x16x32_bf16 v[12:15], a[40:43], v[148:151], v[12:15]\n\t" \
  "ds_read_b64_tr_b16 a[68:69], v239 offset:16384\n\t" \
  "v_mfma_f32_16x16x32_bf16 v[12:15], a[48:51], v[152:155], v[12:15]\n\t" \
  "buffer_load_dwordx4 v242, s[16:19], s97 offen offset:3072 sc1 lds\n\t" \
  "v_mfma_f32_16x16x32_bf16 v[12:15], a[56:59], v[156:159], v[12:15]\n\t" \
  "s_add_u32 s96, s40, 1572864\n\t" \
  "s_and_b32 s96, s96, 0x1c0000\n\t" \
  "s_add_u32 s96, s96, s77\n\t" \
  "v_mfma_f32_16x16x32_bf16 v[28:31], a[60:63], v[156:159], v[28:31]\n\t" \
  "s_add_u32 m0, s38, 32768\n\t" \
  "ds_read_b128 v[132:135], v242 offset:54272\n\t" \
  "v_mfma_f32_16x16x32_bf16 v[28:31], a[52:55], v[152:155], v[28:31]\n\t" \
  "buffer_load_dwordx4 v240, s[24:27], s96 offen offset:0 lds\n\t" \
  "v_mfma_f32_16x16x32_bf16 v[28:31], a[44:47], v[148:151], v[28:31]\n\t" \
  "ds_read_b128 v[136:139], v242 offset:55296\n\t" \
  "v_mfma_f32_16x16x32_bf16 v[28:31], a[36:39], v[144:147], v[28:31]\n\t" \
  "s_waitcnt lgkmcnt(4)\n\t" \
  "ds_read_b128 v[140:143], v242 offset:56320\n\t" \
  "v_mfma_f32_16x16x32_bf16 v[32:35], a[160:163], v[160:163], v[32:35]\n\t" \
  "ds_read_b64_tr_b16 a[70:71], v239 offset:20480\n\t" \
  "v_mfma_f32_16x16x32_bf16 v[32:35], a[168:171], v[164:167], v[32:35]\n\t" \
  "buffer_load_dwordx4 v241, s[24:27], s96 offen offset:1024 lds\n\t" \
  "v_mfma_f32_16x16x32_bf16 v[32:35], a[176:179], v[168:171], v[32:35]\n\t" \
  "ds_read_b128 v[144:147], v242 offset:57344\n\t" \
  "v_mfma_f32_16x16x32_bf16 v[32:35], a[184:187], v[172:175], v[32:35]\n\t" \
  "ds_read_b64_tr_b16 a[72:73], v238 offset:24576\n\t" \
  "v_mfma_f32_16x16x32_bf16 v[48:51], a[188:191], v[172:175], v[48:51]\n\t" \
  "ds_read_b128 v[148:151], v242 offset:58368\n\t" \
  "v_mfma_f32_16x16x32_bf16 v[48:51], a[180:183], v[168:171], v[48:51]\n\t" \
  "ds_read_b128 v[152:155], v242 offset:59392\n\t" \
  "v_mfma_f32_16x16x32_bf16 v[48:51], a[172:175], v[164:167], v[48:51]\n\t" \
  "ds_read_b128 v[156:159], v242 offset:60416\n\t" \
  "ds_read_b64_tr_b16 a[74:75], v238 offset:28672\n\t" \
  "v_mfma_f32_16x16x32_bf16 v[48:51], a[164:167], v[160:163], v[48:51]\n\t" \
  "s_waitcnt lgkmcnt(7)\n\t" \
  "ds_read_b64_tr_b16 a[76:77], v239 offset:24576\n\t" \
  "v_mfma_f32_16x16x32_bf16 v[36:39], a[160:163], v[128:131], v[36:39]\n\t" \
  "ds_read_b64_tr_b16 a[78:79], v239 offset:28672\n\t" \
  "v_mfma_f32_16x16x32_bf16 v[36:39], a[168:171], v[132:135], v[36:39]\n\t" \
  "ds_read_b128 v[160:163], v242 offset:61440\n\t" \
  "v_mfma_f32_16x16x32_bf16 v[36:39], a[176:179], v[136:139], v[36:39]\n\t" \
  "ds_read_b128 v[164:167], v242 offset:62464\n\t" \
  "v_mfma_f32_16x16x32_bf16 v[36:39], a[184:187], v[140:143], v[36:39]\n\t" \
  "ds_read_b128 v[168:171], v242 offset:63488\n\t" \
  "v_mfma_f32_16x16x32_bf16 v[52:55], a[188:191], v[140:143], v[52:55]\n\t" \
  "ds_read_b128 v[172:175], v242 offset:64512\n\t" \
  "v_mfma_f32_16x16x32_bf16 v[52:55], a[180:183], v[136:139], v[52:55]\n\t" \
  "buffer_load_dwordx4 v248, s[24:27], s96 offen offset:2048 lds\n\t" \
  "v_mfma_f32_16x16x32_bf16 v[52:55], a[172:175], v[132:135], v[52:55]\n\t" \
  "s_waitcnt vmcnt(13) lgkmcnt(4)\n\t" \
  "v_mfma_f32_16x16x32_bf16 v[52:55], a[164:167], v[128:131], v[52:55]\n\t" \
  "s_barrier\n\t" \
  "v_mfma_f32_16x16x32_bf16 v[40:43], a[160:163], v[144:147], v[40:43]\n\t" \
  "buffer_load_dwordx4 v249, s[24:27], s96 offen offset:3072 lds\n\t" \
  "v_mfma_f32_16x16x32_bf16 v[40:43], a[168:171], v[148:151], v[40:43]\n\t" \
  "s_add_u32 s97, s64, 393216\n\t" \
  "ds_read_b128 v[128:131], v242 offset:16384\n\t" \
  "s_and_b32 s97, s97, 0xe0000\n\t" \
  "v_mfma_f32_16x16x32_bf16 v[40:43], a[176:179], v[152:155], v[40:43]\n\t" \
  "s_add_u32 s97, s97, s61\n\t" \
  "ds_read_b128 v[132:135], v242 offset:17408\n\t" \
  "s_add_u32 m0, s38, 0\n\t" \
  "v_mfma_f32_16x16x32_bf16 v[40:43], a[184:187], v[156:159], v[40:43]\n\t" \
  "ds_read_b64_tr_b16 a[80:81], v236 offset:0\n\t" \
  "v_mfma_f32_16x16x32_bf16 v[56:59], a[188:191], v[156:159], v[56:59]\n\t" \
  "buffer_load_dwordx4 v242, s[16:19], s97 offen offset:0 sc1 lds\n\t" \
  "v_mfma_f32_16x16x32_bf16 v[56:59], a[180:183], v[152:155], v[56:59]\n\t" \
  "ds_read_b64_tr_b16 a[82:83], v236 offset:4096\n\t" \
  "ds_read_b128 v[136:139], v242 offset:18432\n\t" \
  "v_mfma_f32_16x16x32_bf16 v[56:59], a[172:175], v[148:151], v[56:59]\n\t" \
  "ds_read_b64_tr_b16 a[84:85], v237 offset:0\n\t" \
  "ds_read_b128 v[140:143], v242 offset:19456\n\t" \
  "v_mfma_f32_16x16x32_bf16 v[56:59], a[164:167], v[144:147], v[56:59]\n\t" \
  "s_waitcnt lgkmcnt(7)\n\t" \
  "buffer_load_dwordx4 v242, s[16:19], s97 offen offset:1024 sc1 lds\n\t" \
  "v_mfma_f32_16x16x32_bf16 v[44:47], a[160:163], v[160:163], v[44:47]\n\t" \
  "ds_read_b128 v[144:147], v242 offset:20480\n\t" \
  "v_mfma_f32_16x16x32_bf16 v[44:47], a[168:171], v[164:167], v[44:47]\n\t" \
  "ds_read_b128 v[148:151], v242 offset:21504\n\t" \
  "v_mfma_f32_16x16x32_bf16 v[44:47], a[176:179], v[168:171], v[44:47]\n\t" \
  "ds_read_b64_tr_b16 a[86:87], v237 offset:4096\n\t" \
  "v_mfma_f32_16x16x32_bf16 v[44:47], a[184:187], v[172:175], v[44:47]\n\t" \
  "ds_read_b128 v[152:155], v242 offset:22528\n\t" \
  "v_mfma_f32_16x16x32_bf16 v[60:63], a[188:191], v[172:175], v[60:63]\n\t" \
  "buffer_load_dwordx4 v242, s[16:19], s97 offen offset:2048 sc1 lds\n\t" \
  "v_mfma_f32_16x16x32_bf16 v[60:63], a[180:183], v[168:171], v[60:63]\n\t" \
  "ds_read_b64_tr_b16 a[88:89], v236 offset:8192\n\t" \
  "v_mfma_f32_16x16x32_bf16 v[60:63], a[172:175], v[164:167], v[60:63]\n\t" \
  "ds_read_b128 v[156:159], v242 offset:23552\n\t" \
  "v_mfma_f32_16x16x32_bf16 v[60:63], a[164:167], v[160:163], v[60:63]\n\t" \
  "s_waitcnt lgkmcnt(6)\n\t" \
  "buffer_load_dwordx4 v242, s[16:19], s97 offen offset:3072 sc1 lds\n\t" \
  "v_mfma_f32_16x16x32_bf16 v[96:99], a[192:195], v[128:131], 0\n\t" \
  "s_add_u32 s96, s40, 1835008\n\t" \
  "ds_read_b128 v[160:163], v242 offset:24576\n\t" \
  "s_and_b32 s96, s96, 0x1c0000\n\t" \
  "v_mfma_f32_16x16x32_bf16 v[96:99], a[200:203], v[132:135], v[96:99]\n\t" \
  "s_add_u32 s96, s96, s51\n\t" \
  "ds_read_b64_tr_b16 a[90:91], v236 offset:12288\n\t" \
  "s_add_u32 m0, s38, 147456\n\t" \
  "v_mfma_f32_16x16x32_bf16 v[96:99], a[208:211], v[136:139], v[96:99]\n\t" \
  "s_waitcnt vmcnt(12)\n\t" \
  "buffer_store_dword v243, v254, s[16:19], s49 offen offset:64 sc1\n\t" \
  "ds_read_b128 v[164:167], v242 offset:25600\n\t" \
  "v_mfma_f32_16x16x32_bf16 v[96:99], a[216:219], v[140:143], v[96:99]\n\t" \
  "ds_read_b64_tr_b16 a[92:93], v237 offset:8192\n\t" \
  "v_mfma_f32_16x16x32_bf16 v[112:115], a[220:223], v[140:143], 0\n\t" \
  "buffer_load_dwordx4 v240, s[24:27], s96 offen offset:0 lds\n\t" \
  "v_mfma_f32_16x16x32_bf16 v[112:115], a[212:215], v[136:139], v[112:115]\n\t" \
  "ds_read_b128 v[168:171], v242 offset:26624\n\t" \
  "v_mfma_f32_16x16x32_bf16 v[112:115], a[204:207], v[132:135], v[112:115]\n\t" \
  "ds_read_b128 v[172:175], v242 offset:27648\n\t" \
  "v_mfma_f32_16x16x32_bf16 v[112:115], a[196:199], v[128:131], v[112:115]\n\t" \
  "s_waitcnt lgkmcnt(6)\n\t" \
  "buffer_load_dwordx4 v241, s[24:27], s96 offen offset:1024 lds\n\t" \
  "v_mfma_f32_16x16x32_bf16 v[100:103], a[192:195], v[144:147], 0\n\t" \
  "ds_read_b64_tr_b16 a[94:95], v237 offset:12288\n\t" \
  "v_mfma_f32_16x16x32_bf16 v[100:103], a[200:203], v[148:151], v[100:103]\n\t" \
  "ds_read_b128 v[128:131], v242 offset:28672\n\t" \
  "v_mfma_f32_16x16x32_bf16 v[100:103], a[208:211], v[152:155], v[100:103]\n\t" \
  "ds_read_b128 v[132:135], v242 offset:29696\n\t" \
  "v_mfma_f32_16x16x32_bf16 v[100:103], a[216:219], v[156:159], v[100:103]\n\t" \
  "ds_read_b128 v[136:139], v242 offset:30720\n\t" \
  "v_mfma_f32_16x16x32_bf16 v[116:119], a[220:223], v[156:159], 0\n\t" \
  "ds_read_b128 v[140:143], v242 offset:31744\n\t" \
  "v_mfma_f32_16x16x32_bf16 v[116:119], a[212:215], v[152:155], v[116:119]\n\t" \
  "v_mfma_f32_16x16x32_bf16 v[116:119], a[204:207], v[148:151], v[116:119]\n\t" \
  "s_waitcnt vmcnt(11) lgkmcnt(4)\n\t" \
  "v_mfma_f32_16x16x32_bf16 v[116:119], a[196:199], v[144:147], v[116:119]\n\t" \
  "s_barrier\n\t" \
  "v_mfma_f32_16x16x32_bf16 v[104:107], a[192:195], v[160:163], 0\n\t" \
  "ds_read_b128 v[144:147], v232 offset:0\n\t" \
  "v_mfma_f32_16x16x32_bf16 v[104:107], a[200:203], v[164:167], v[104:107]\n\t" \
  "ds_read_b64_tr_b16 a[96:97], v236 offset:16384\n\t" \
  "v_mfma_f32_16x16x32_bf16 v[104:107], a[208:211], v[168:171], v[104:107]\n\t" \
  "ds_read_b128 v[148:151], v232 offset:1024\n\t" \
  "v_mfma_f32_16x16x32_bf16 v[104:107], a[216:219], v[172:175], v[104:107]\n\t" \
  "ds_read_b128 v[152:155], v232 offset:2048\n\t" \
  "v_mfma_f32_16x16x32_bf16 v[120:123], a[220:223], v[172:175], 0\n\t" \
  ".Lhs12:\n\ts_cbranch_execz .Lhs12n\n\t" \
  "ds_read_b64_tr_b16 a[98:99], v236 offset:20480\n\t" \
  "v_mfma_f32_16x16x32_bf16 v[120:123], a[212:215], v[168:171], v[120:123]\n\t" \
  "ds_read_b128 v[156:159], v232 offset:3072\n\t" \
  "v_mfma_f32_16x16x32_bf16 v[120:123], a[204:207], v[164:167], v[120:123]\n\t" \
  "buffer_load_dwordx4 v248, s[24:27], s96 offen offset:2048 lds\n\t" \
  "v_mfma_f32_16x16x32_bf16 v[120:123], a[196:199], v[160:163], v[120:123]\n\t" \
  "s_waitcnt lgkmcnt(6)\n\t" \
  "ds_read_b64_tr_b16 a[100:101], v237 offset:16384\n\t" \
  "v_mfma_f32_16x16x32_bf16 v[108:111], a[192:195], v[128:131], 0\n\t" \
  "ds_read_b128 v[160:163], v232 offset:4096\n\t" \
  "v_mfma_f32_16x16x32_bf16 v[108:111], a[200:203], v[132:135], v[108:111]\n\t" \
  "ds_read_b64_tr_b16 a[102:103], v237 offset:20480\n\t" \
  "v_mfma_f32_16x16x32_bf16 v[108:111], a[208:211], v[136:139], v[108:111]\n\t" \
  "buffer_load_dwordx4 v249, s[24:27], s96 offen offset:3072 lds\n\t" \
  "v_mfma_f32_16x16x32_bf16 v[108:111], a[216:219], v[140:143], v[108:111]\n\t" \
  "s_add_u32 s97, s64, 786432\n\t" \
  "s_and_b32 s97, s97, 0xe0000\n\t" \
  "s_add_u32 s97, s97, s60\n\t" \
  "v_mfma_f32_16x16x32_bf16 v[124:127], a[220:223], v[140:143], 0\n\t" \
  "s_add_u32 m0, s38, 65536\n\t" \
  "ds_read_b64_tr_b16 a[104:105], v236 offset:24576\n\t" \
  "ds_read_b128 v[164:167], v232 offset:5120\n\t" \
  "v_mfma_f32_16x16x32_bf16 v[124:127], a[212:215], v[136:139], v[124:127]\n\t" \
  "buffer_load_dwordx4 v242, s[16:19], s97 offen offset:0 sc1 lds\n\t" \
  "v_mfma_f32_16x16x32_bf16 v[124:127], a[204:207], v[132:135], v[124:127]\n\t" \
  "ds_read_b64_tr_b16 a[106:107], v236 offset:28672\n\t" \
  "ds_read_b128 v[168:171], v232 offset:6144\n\t" \
  "v_mfma_f32_16x16x32_bf16 v[124:127], a[196:199], v[128:131], v[124:127]\n\t" \
  "s_waitcnt lgkmcnt(7)\n\t" \
  "ds_read_b128 v[172:175], v232 offset:7168\n\t" \
  "v_mfma_f32_16x16x32_bf16 v[0:3], a[64:67], v[144:147], v[0:3]\n\t" \
  "ds_read_b128 v[128:131], v232 offset:8192\n\t" \
  "v_lshrrev_b32 v252, 5, v233\n\t" \
  "v_mfma_f32_16x16x32_bf16 v[0:3], a[72:75], v[148:151], v[0:3]\n\t" \
  "buffer_load_dwordx4 v242, s[16:19], s97 offen offset:1024 sc1 lds\n\t" \
  "v_mfma_f32_16x16x32_bf16 v[0:3], a[80:83], v[152:155], v[0:3]\n\t" \
  "ds_read_b128 v[132:135], v232 offset:9216\n\t" \
  "v_mul_u32_u24 v252, 1536, v252\n\t" \
  "v_mfma_f32_16x16x32_bf16 v[0:3], a[88:91], v[156:159], v[0:3]\n\t" \
  "ds_read_b64_tr_b16 a[108:109], v237 offset:24576\n\t" \
  "v_lshl_add_u32 v252, s3, 9, v252\n\t" \
  "v_mfma_f32_16x16x32_bf16 v[16:19], a[92:95], v[156:159], v[16:19]\n\t" \
  "buffer_load_dwordx4 v242, s[16:19], s97 offen offset:2048 sc1 lds\n\t" \
  "v_mfma_f32_16x16x32_bf16 v[16:19], a[84:87], v[152:155], v[16:19]\n\t" \
  "ds_read_b128 v[136:139], v232 offset:10240\n\t" \
  "v_add_u32 v252, v252, v242\n\t" \
  "v_mfma_f32_16x16x32_bf16 v[16:19], a[76:79], v[148:151], v[16:19]\n\t" \
  "ds_read_b128 v[140:143], v232 offset:11264\n\t" \
  "v_lshrrev_b32 v247, 1, v242\n\t" \
  "v_mfma_f32_16x16x32_bf16 v[16:19], a[68:71], v[144:147], v[16:19]\n\t" \
  "s_waitcnt lgkmcnt(5)\n\t" \
  "buffer_load_dwordx4 v242, s[16:19], s97 offen offset:3072 sc1 lds\n\t" \
  "v_mfma_f32_16x16x32_bf16 v[4:7], a[64:67], v[160:163], v[4:7]\n\t" \
  "s_add_u32 s96, s40, 1835008\n\t" \
  "s_and_b32 s96, s96, 0x1c0000\n\t" \
  "s_add_u32 s96, s96, s77\n\t" \
  "v_mfma_f32_16x16x32_bf16 v[4:7], a[72:75], v[164:167], v[4:7]\n\t" \
  "s_add_u32 m0, s38, 49152\n\t" \
  "ds_read_b128 v[144:147], v232 offset:12288\n\t" \
  "v_add_u32 v247, s38, v247\n\t" \
  "v_mfma_f32_16x16x32_bf16 v[4:7], a[80:83], v[168:171], v[4:7]\n\t" \
  "buffer_load_dwordx4 v240, s[24:27], s96 offen offset:0 lds\n\t" \
  "v_mfma_f32_16x16x32_bf16 v[4:7], a[88:91], v[172:175], v[4:7]\n\t" \
  "ds_read_b128 v[148:151], v232 offset:13312\n\t" \
  "ds_read_b64_tr_b16 a[110:111], v237 offset:28672\n\t" \
  "v_mfma_f32_16x16x32_bf16 v[20:23], a[92:95], v[172:175], v[20:23]\n\t" \
  "ds_read_b128 v[152:155], v232 offset:14336\n\t" \
  "v_mfma_f32_16x16x32_bf16 v[20:23], a[84:87], v[168:171], v[20:23]\n\t" \
  "buffer_load_dwordx4 v241, s[24:27], s96 offen offset:1024 lds\n\t" \
  "v_mfma_f32_16x16x32_bf16 v[20:23], a[76:79], v[164:167], v[20:23]\n\t" \
  "s_waitcnt vmcnt(11) lgkmcnt(1)\n\t" \
  "ds_read_b128 v[156:159], v232 offset:15360\n\t" \
  "v_mfma_f32_16x16x32_bf16 v[20:23], a[68:71], v[160:163], v[20:23]\n\t" \
  "s_barrier\n\t" \
  "v_mfma_f32_16x16x32_bf16 v[8:11], a[64:67], v[128:131], v[8:11]\n\t" \
  "buffer_load_dwordx4 v248, s[24:27], s96 offen offset:2048 lds\n\t" \
  "v_mfma_f32_16x16x32_bf16 v[8:11], a[72:75], v[132:135], v[8:11]\n\t" \
  "ds_read_b128 v[160:163], v242 offset:0\n\t" \
  "v_mfma_f32_16x16x32_bf16 v[8:11], a[80:83], v[136:139], v[8:11]\n\t" \
  "ds_read_b128 v[164:167], v242 offset:1024\n\t" \
  "v_mfma_f32_16x16x32_bf16 v[8:11], a[88:91], v[140:143], v[8:11]\n\t" \
  "ds_read_b64_tr_b16 a[112:113], v234 offset:32768\n\t" \
  "v_mfma_f32_16x16x32_bf16 v[24:27], a[92:95], v[140:143], v[24:27]\n\t" \
  "ds_read_b128 v[168:171], v242 offset:2048\n\t" \
  "v_mfma_f32_16x16x32_bf16 v[24:27], a[84:87], v[136:139], v[24:27]\n\t" \
  "buffer_load_dwordx4 v249, s[24:27], s96 offen offset:3072 lds\n\t" \
  "v_mfma_f32_16x16x32_bf16 v[24:27], a[76:79], v[132:135], v[24:27]\n\t" \
  "s_add_u32 s97, s64, 524288\n\t" \
  "s_and_b32 s97, s97, 0xe0000\n\t" \
  "s_add_u32 s97, s97, s61\n\t" \
  "v_mfma_f32_16x16x32_bf16 v[24:27], a[68:71], v[128:131], v[24:27]\n\t" \
  "s_waitcnt lgkmcnt(4)\n\t" \
  "ds_read_b128 v[172:175], v242 offset:3072\n\t" \
  "s_add_u32 m0, s38, 81920\n\t" \
  "v_mfma_f32_16x16x32_bf16 v[12:15], a[64:67], v[144:147], v[12:15]\n\t" \
  "buffer_load_dwordx4 v242, s[16:19], s97 offen offset:0 sc1 lds\n\t" \
  "v_mfma_f32_16x16x32_bf16 v[12:15], a[72:75], v[148:151], v[12:15]\n\t" \
  "ds_read_b128 v[128:131], v242 offset:4096\n\t" \
  "v_mfma_f32_16x16x32_bf16 v[12:15], a[80:83], v[152:155], v[12:15]\n\t" \
  "ds_read_b64_tr_b16 a[114:115], v234 offset:36864\n\t" \
  "v_mfma_f32_16x16x32_bf16 v[12:15], a[88:91], v[156:159], v[12:15]\n\t" \
  "ds_read_b128 v[132:135], v242 offset:5120\n\t" \
  "v_mfma_f32_16x16x32_bf16 v[28:31], a[92:95], v[156:159], v[28:31]\n\t" \
  "ds_read_b64_tr_b16 a[116:117], v235 offset:32768\n\t" \
  "v_mfma_f32_16x16x32_bf16 v[28:31], a[84:87], v[152:155], v[28:31]\n\t" \
  "ds_read_b128 v[136:139], v242 offset:6144\n\t" \
  "v_mfma_f32_16x16x32_bf16 v[28:31], a[76:79], v[148:151], v[28:31]\n\t" \
  "ds_read_b128 v[140:143], v242 offset:7168\n\t" \
  "v_mfma_f32_16x16x32_bf16 v[28:31], a[68:71], v[144:147], v[28:31]\n\t" \
  "s_waitcnt lgkmcnt(6)\n\t" \
  "ds_read_b64_tr_b16 a[118:119], v235 offset:36864\n\t" \
  STP("s_memrealtime s[94:95]\n\t") \
  STP("s_memtime s[92:93]\n\t") \
  "v_mfma_f32_16x16x32_bf16 v[32:35], a[0:3], v[160:163], v[32:35]\n\t" \
  "ds_read_b128 v[144:147], v242 offset:8192\n\t" \
  "v_mfma_f32_16x16x32_bf16 v[32:35], a[8:11], v[164:167], v[32:35]\n\t" \
  "ds_read_b64_tr_b16 a[120:121], v234 offset:40960\n\t" \
  "v_mfma_f32_16x16x32_bf16 v[32:35], a[16:19], v[168:171], v[32:35]\n\t" \
  "ds_read_b128 v[148:151], v242 offset:9216\n\t" \
  "v_mfma_f32_16x16x32_bf16 v[32:35], a[24:27], v[172:175], v[32:35]\n\t" \
  "ds_read_b64_tr_b16 a[122:123], v234 offset:45056\n\t" \
  "v_mfma_f32_16x16x32_bf16 v[48:51], a[28:31], v[172:175], v[48:51]\n\t" \
  "ds_read_b128 v[152:155], v242 offset:10240\n\t" \
  "v_mfma_f32_16x16x32_bf16 v[48:51], a[20:23], v[168:171], v[48:51]\n\t" \
  "ds_read_b64_tr_b16 a[124:125], v235 offset:40960\n\t" \
  "v_mfma_f32_16x16x32_bf16 v[48:51], a[12:15], v[164:167], v[48:51]\n\t" \
  "buffer_load_dwordx4 v242, s[16:19], s97 offen offset:1024 sc1 lds\n\t" \
  "v_mfma_f32_16x16x32_bf16 v[48:51], a[4:7], v[160:163], v[48:51]\n\t" \
  "s_waitcnt lgkmcnt(7)\n\t" \
  "ds_read_b128 v[156:159], v242 offset:11264\n\t" \
  "v_mfma_f32_16x16x32_bf16 v[36:39], a[0:3], v[128:131], v[36:39]\n\t" \
  "ds_read_b64_tr_b16 a[126:127], v235 offset:45056\n\t" \
  "v_mfma_f32_16x16x32_bf16 v[36:39], a[8:11], v[132:135], v[36:39]\n\t" \
  "ds_read_b128 v[160:163], v242 offset:12288\n\t" \
  "v_mfma_f32_16x16x32_bf16 v[36:39], a[16:19], v[136:139], v[36:39]\n\t" \
  "buffer_load_dwordx4 v242, s[16:19], s97 offen offset:2048 sc1 lds\n\t" \
  "v_mfma_f32_16x16x32_bf16 v[36:39], a[24:27], v[140:143], v[36:39]\n\t" \
  "ds_read_b128 v[164:167], v242 offset:13312\n\t" \
  "v_mfma_f32_16x16x32_bf16 v[52:55], a[28:31], v[140:143], v[52:55]\n\t" \
  STP("v_writelane_b32 v255, s94, 32\n\t") \
  STP("v_writelane_b32 v255, s92, 33\n\t") \
  "ds_read_b128 v[168:171], v242 offset:14336\n\t" \
  "v_mfma_f32_16x16x32_bf16 v[52:55], a[20:23], v[136:139], v[52:55]\n\t" \
  "buffer_load_dwordx4 v242, s[16:19], s97 offen offset:3072 sc1 lds\n\t" \
  "v_mfma_f32_16x16x32_bf16 v[52:55], a[12:15], v[132:135], v[52:55]\n\t" \
  "s_waitcnt vmcnt(8) lgkmcnt(3)\n\t" \
  "s_add_u32 s97, s64, 917504\n\t" \
  "s_and_b32 s97, s97, 0xe0000\n\t" \
  "v_mfma_f32_16x16x32_bf16 v[52:55], a[4:7], v[128:131], v[52:55]\n\t" \
  "s_barrier\n\t" \
  "v_mfma_f32_16x16x32_bf16 v[40:43], a[0:3], v[144:147], v[40:43]\n\t" \
  "s_add_u32 s97, s97, s60\n\t" \
  "ds_read_b128 v[172:175], v242 offset:15360\n\t" \
  "s_add_u32 m0, s38, 131072\n\t" \
  "v_mfma_f32_16x16x32_bf16 v[40:43], a[8:11], v[148:151], v[40:43]\n\t" \
  "buffer_load_dwordx4 v242, s[16:19], s97 offen offset:0 sc1 lds\n\t" \
  "v_mfma_f32_16x16x32_bf16 v[40:43], a[16:19], v[152:155], v[40:43]\n\t" \
  "ds_read_b128 v[128:131], v230 offset:0\n\t" \
  "v_mfma_f32_16x16x32_bf16 v[40:43], a[24:27], v[156:159], v[40:43]\n\t" \
  "buffer_load_dwordx4 v242, s[16:19], s97 offen offset:1024 sc1 lds\n\t" \
  "v_mfma_f32_16x16x32_bf16 v[56:59], a[28:31], v[156:159], v[56:59]\n\t" \
  "ds_read_b128 v[132:135], v230 offset:1024\n\t" \
  "v_mfma_f32_16x16x32_bf16 v[56:59], a[20:23], v[152:155], v[56:59]\n\t" \
  "ds_read_b128 v[136:139], v230 offset:2048\n\t" \
  "v_mfma_f32_16x16x32_bf16 v[56:59], a[12:15], v[148:151], v[56:59]\n\t" \
  "buffer_load_dwordx4 v242, s[16:19], s97 offen offset:2048 sc1 lds\n\t" \
  "v_mfma_f32_16x16x32_bf16 v[56:59], a[4:7], v[144:147], v[56:59]\n\t" \
  "s_waitcnt lgkmcnt(3)\n\t" \
  "ds_read_b128 v[140:143], v230 offset:3072\n\t" \
  "v_mfma_f32_16x16x32_bf16 v[44:47], a[0:3], v[160:163], v[44:47]\n\t" \
  "ds_read_b128 v[144:147], v230 offset:4096\n\t" \
  "v_mfma_f32_16x16x32_bf16 v[44:47], a[8:11], v[164:167], v[44:47]\n\t" \
  "ds_read_b128 v[148:151], v230 offset:5120\n\t" \
  "v_mfma_f32_16x16x32_bf16 v[44:47], a[16:19], v[168:171], v[44:47]\n\t" \
  "ds_read_b128 v[152:155], v230 offset:6144\n\t" \
  "v_mfma_f32_16x16x32_bf16 v[44:47], a[24:27], v[172:175], v[44:47]\n\t" \
  "ds_read_b128 v[156:159], v230 offset:7168\n\t" \
  "v_mfma_f32_16x16x32_bf16 v[60:63], a[28:31], v[172:175], v[60:63]\n\t" \
  "buffer_load_dwordx4 v242, s[16:19], s97 offen offset:3072 sc1 lds\n\t" \
  "v_mfma_f32_16x16x32_bf16 v[60:63], a[20:23], v[168:171], v[60:63]\n\t" \
  "s_add_u32 s97, s64, 655360\n\t" \
  "s_and_b32 s97, s97, 0xe0000\n\t" \
  "s_add_u32 s97, s97, s61\n\t" \
  "v_mfma_f32_16x16x32_bf16 v[60:63], a[12:15], v[164:167], v[60:63]\n\t" \
  "s_add_u32 m0, s38, 16384\n\t" \
  "v_mfma_f32_16x16x32_bf16 v[60:63], a[4:7], v[160:163], v[60:63]\n\t" \
  "s_waitcnt lgkmcnt(4)\n\t" \
  "buffer_load_dword v246, v245, s[16:19], s65 offen sc1\n\t" \
  "v_mfma_f32_16x16x32_bf16 v[0:3], a[96:99], v[128:131], v[0:3]\n\t" \
  "ds_read_b128 v[160:163], v230 offset:8192\n\t" \
  "v_mfma_f32_16x16x32_bf16 v[0:3], a[104:107], v[132:135], v[0:3]\n\t" \
  "ds_read_b128 v[164:167], v230 offset:9216\n\t" \
  "v_mfma_f32_16x16x32_bf16 v[0:3], a[112:115], v[136:139], v[0:3]\n\t" \
  "ds_read_b128 v[168:171], v230 offset:10240\n\t" \
  "v_mfma_f32_16x16x32_bf16 v[0:3], a[120:123], v[140:143], v[0:3]\n\t" \
  "buffer_load_dwordx4 v242, s[16:19], s97 offen offset:0 sc1 lds\n\t" \
  "v_mfma_f32_16x16x32_bf16 v[16:19], a[124:127], v[140:143], v[16:19]\n\t" \
  "ds_read_b128 v[172:175], v230 offset:11264\n\t" \
  "v_mfma_f32_16x16x32_bf16 v[16:19], a[116:119], v[136:139], v[16:19]\n\t" \
  "v_mfma_f32_16x16x32_bf16 v[16:19], a[108:111], v[132:135], v[16:19]\n\t" \
  "v_mfma_f32_16x16x32_bf16 v[16:19], a[100:103], v[128:131], v[16:19]\n\t" \
  "s_waitcnt lgkmcnt(4)\n\t" \
  "buffer_load_dwordx4 v242, s[16:19], s97 offen offset:1024 sc1 lds\n\t" \
  "v_mfma_f32_16x16x32_bf16 v[4:7], a[96:99], v[144:147], v[4:7]\n\t" \
  "ds_read_b128 v[128:131], v230 offset:12288\n\t" \
  "v_mfma_f32_16x16x32_bf16 v[4:7], a[104:107], v[148:151], v[4:7]\n\t" \
  "ds_read_b128 v[132:135], v230 offset:13312\n\t" \
  "v_mfma_f32_16x16x32_bf16 v[4:7], a[112:115], v[152:155], v[4:7]\n\t" \
  "buffer_load_dwordx4 v242, s[16:19], s97 offen offset:2048 sc1 lds\n\t" \
  "v_mfma_f32_16x16x32_bf16 v[4:7], a[120:123], v[156:159], v[4:7]\n\t" \
  "ds_read_b128 v[136:139], v230 offset:14336\n\t" \
  "v_mfma_f32_16x16x32_bf16 v[20:23], a[124:127], v[156:159], v[20:23]\n\t" \
  "ds_read_b128 v[140:143], v230 offset:15360\n\t" \
  "v_mfma_f32_16x16x32_bf16 v[20:23], a[116:119], v[152:155], v[20:23]\n\t" \
  "buffer_load_dwordx4 v242, s[16:19], s97 offen offset:3072 sc1 lds\n\t" \
  "v_mfma_f32_16x16x32_bf16 v[20:23], a[108:111], v[148:151], v[20:23]\n\t" \
  "s_waitcnt vmcnt(9) lgkmcnt(4)\n\t" \
  "s_add_u32 s97, s64, 786432\n\t" \
  "s_and_b32 s97, s97, 0xe0000\n\t" \
  "v_mfma_f32_16x16x32_bf16 v[20:23], a[100:103], v[144:147], v[20:23]\n\t" \
  "s_barrier\n\t" \
  "v_mfma_f32_16x16x32_bf16 v[8:11], a[96:99], v[160:163], v[8:11]\n\t" \
  "s_add_u32 s97, s97, s61\n\t" \
  "ds_read_b128 v[144:147], v230 offset:16384\n\t" \
  "ds_read_b128 v[148:151], v230 offset:17408\n\t" \
  "s_add_u32 m0, s38, 0\n\t" \
  "v_mfma_f32_16x16x32_bf16 v[8:11], a[104:107], v[164:167], v[8:11]\n\t" \
  "buffer_load_dwordx4 v242, s[16:19], s97 offen offset:0 sc1 lds\n\t" \
  "ds_read_b64_tr_b16 a[192:193], v238 offset:16384\n\t" \
  "v_mfma_f32_16x16x32_bf16 v[8:11], a[112:115], v[168:171], v[8:11]\n\t" \
  "ds_read_b64_tr_b16 a[194:195], v238 offset:20480\n\t" \
  "ds_read_b128 v[152:155], v230 offset:18432\n\t" \
  "v_mfma_f32_16x16x32_bf16 v[8:11], a[120:123], v[172:175], v[8:11]\n\t" \
  "buffer_load_dwordx4 v242, s[16:19], s97 offen offset:1024 sc1 lds\n\t" \
  "v_mfma_f32_16x16x32_bf16 v[24:27], a[124:127], v[172:175], v[24:27]\n\t" \
  "ds_read_b128 v[156:159], v230 offset:19456\n\t" \
  "v_mfma_f32_16x16x32_bf16 v[24:27], a[116:119], v[168:171], v[24:27]\n\t" \
  "ds_read_b64_tr_b16 a[196:197], v239 offset:16384\n\t" \
  "v_mfma_f32_16x16x32_bf16 v[24:27], a[108:111], v[164:167], v[24:27]\n\t" \
  "buffer_load_dwordx4 v242, s[16:19], s97 offen offset:2048 sc1 lds\n\t" \
  "v_mfma_f32_16x16x32_bf16 v[24:27], a[100:103], v[160:163], v[24:27]\n\t" \
  "s_waitcnt lgkmcnt(7)\n\t" \
  "ds_read_b64_tr_b16 a[198:199], v239 offset:20480\n\t" \
  "v_mfma_f32_16x16x32_bf16 v[12:15], a[96:99], v[128:131], v[12:15]\n\t" \
  "ds_read_b128 v[160:163], v230 offset:20480\n\t" \
  "ds_read_b128 v[164:167], v230 offset:21504\n\t" \
  "v_mfma_f32_16x16x32_bf16 v[12:15], a[104:107], v[132:135], v[12:15]\n\t" \
  "buffer_load_dwordx4 v242, s[16:19], s97 offen offset:3072 sc1 lds\n\t" \
  "v_mfma_f32_16x16x32_bf16 v[12:15], a[112:115], v[136:139], v[12:15]\n\t" \
  "s_add_u32 s97, s64, 917504\n\t" \
  "s_and_b32 s97, s97, 0xe0000\n\t" \
  "ds_read_b128 v[168:171], v230 offset:22528\n\t" \
  "s_add_u32 s97, s97, s61\n\t" \
  "v_mfma_f32_16x16x32_bf16 v[12:15], a[120:123], v[140:143], v[12:15]\n\t" \
  "s_add_u32 m0, s38, 32768\n\t" \
  "ds_read_b128 v[172:175], v230 offset:23552\n\t" \
  "ds_read_b64_tr_b16 a[200:201], v238 offset:24576\n\t" \
  "v_mfma_f32_16x16x32_bf16 v[28:31], a[124:127], v[140:143], v[28:31]\n\t" \
  "buffer_load_dwordx4 v242, s[16:19], s97 offen offset:0 sc1 lds\n\t" \
  "v_mfma_f32_16x16x32_bf16 v[28:31], a[116:119], v[136:139], v[28:31]\n\t" \
  "ds_read_b64_tr_b16 a[202:203], v238 offset:28672\n\t" \
  "v_mfma_f32_16x16x32_bf16 v[28:31], a[108:111], v[132:135], v[28:31]\n\t" \
  "ds_read_b64_tr_b16 a[204:205], v239 offset:24576\n\t" \
  "v_mfma_f32_16x16x32_bf16 v[28:31], a[100:103], v[128:131], v[28:31]\n\t" \
  "s_waitcnt lgkmcnt(9)\n\t" \
  "buffer_load_dwordx4 v242, s[16:19], s97 offen offset:1024 sc1 lds\n\t" \
  "v_mfma_f32_16x16x32_bf16 v[32:35], a[32:35], v[144:147], v[32:35]\n\t" \
  "ds_read_b128 v[128:131], v230 offset:24576\n\t" \
  "ds_read_b128 v[132:135], v230 offset:25600\n\t" \
  "v_mfma_f32_16x16x32_bf16 v[32:35], a[40:43], v[148:151], v[32:35]\n\t" \
  "ds_read_b64_tr_b16 a[206:207], v239 offset:28672\n\t" \
  "ds_read_b128 v[136:139], v230 offset:26624\n\t" \
  "v_mfma_f32_16x16x32_bf16 v[32:35], a[48:51], v[152:155], v[32:35]\n\t" \
  "buffer_load_dwordx4 v242, s[16:19], s97 offen offset:2048 sc1 lds\n\t" \
  "v_mfma_f32_16x16x32_bf16 v[32:35], a[56:59], v[156:159], v[32:35]\n\t" \
  "s_waitcnt vmcnt(11)\n\t" \
  "v_cmp_ne_u32 vcc, s36, v246\n\t" \
  "v_mfma_f32_16x16x32_bf16 v[48:51], a[60:63], v[156:159], v[48:51]\n\t" \
  "s_and_b32 vcc_lo, vcc_lo, 0xfe0000\n\t" \
  "s_cbranch_scc1 .Lat_s2\n\t" \
  ".Lat_k2:\n\t" \
  "v_mfma_f32_16x16x32_bf16 v[48:51], a[52:55], v[152:155], v[48:51]\n\t" \
  "buffer_load_dwordx4 v242, s[16:19], s97 offen offset:3072 sc1 lds\n\t" \
  "v_mfma_f32_16x16x32_bf16 v[48:51], a[44:47], v[148:151], v[48:51]\n\t" \
  "s_add_u32 s97, s64, 131072\n\t" \
  "s_and_b32 s97, s97, 0xe0000\n\t" \
  "ds_read_b128 v[140:143], v230 offset:27648\n\t" \
  "s_add_u32 s97, s97, s62\n\t" \
  "v_mfma_f32_16x16x32_bf16 v[48:51], a[36:39], v[144:147], v[48:51]\n\t" \
  "s_waitcnt lgkmcnt(8)\n\t" \
  "ds_read_b64_tr_b16 a[208:209], v234 offset:49152\n\t" \
  "s_add_u32 m0, s38, 98304\n\t" \
  "v_mfma_f32_16x16x32_bf16 v[36:39], a[32:35], v[160:163], v[36:39]\n\t" \
  "buffer_load_dwordx4 v242, s[16:19], s97 offen offset:0 sc1 lds\n\t" \
  "ds_read_b128 v[144:147], v230 offset:28672\n\t" \
  "ds_read_b128 v[148:151], v230 offset:29696\n\t" \
  "v_mfma_f32_16x16x32_bf16 v[36:39], a[40:43], v[164:167], v[36:39]\n\t" \
  "ds_read_b64_tr_b16 a[210:211], v234 offset:53248\n\t" \
  "ds_read_b128 v[152:155], v230 offset:30720\n\t" \
  "v_mfma_f32_16x16x32_bf16 v[36:39], a[48:51], v[168:171], v[36:39]\n\t" \
  "ds_read_b128 v[156:159], v230 offset:31744\n\t" \
  "v_mfma_f32_16x16x32_bf16 v[36:39], a[56:59], v[172:175], v[36:39]\n\t" \
  "buffer_load_dwordx4 v242, s[16:19], s97 offen offset:1024 sc1 lds\n\t" \
  "v_mfma_f32_16x16x32_bf16 v[52:55], a[60:63], v[172:175], v[52:55]\n\t" \
  "ds_read_b64_tr_b16 a[212:213], v235 offset:49152\n\t" \
  "v_mfma_f32_16x16x32_bf16 v[52:55], a[52:55], v[168:171], v[52:55]\n\t" \
  "ds_read_b64_tr_b16 a[214:215], v235 offset:53248\n\t" \
  "v_mfma_f32_16x16x32_bf16 v[52:55], a[44:47], v[164:167], v[52:55]\n\t" \
  "s_waitcnt vmcnt(10) lgkmcnt(8)\n\t" \
  "buffer_load_dwordx4 v242, s[16:19], s97 offen offset:2048 sc1 lds\n\t" \
  "v_mfma_f32_16x16x32_bf16 v[52:55], a[36:39], v[160:163], v[52:55]\n\t" \
  "s_barrier\n\t" \
  "v_mfma_f32_16x16x32_bf16 v[40:43], a[32:35], v[128:131], v[40:43]\n\t" \
  "ds_read_b128 v[160:163], v232 offset:0\n\t" \
  "v_mfma_f32_16x16x32_bf16 v[40:43], a[40:43], v[132:135], v[40:43]\n\t" \
  "ds_read_b64_tr_b16 a[216:217], v234 offset:57344\n\t" \
  "v_mfma_f32_16x16x32_bf16 v[40:43], a[48:51], v[136:139], v[40:43]\n\t" \
  "buffer_load_dwordx4 v242, s[16:19], s97 offen offset:3072 sc1 lds\n\t" \
  "v_mfma_f32_16x16x32_bf16 v[40:43], a[56:59], v[140:143], v[40:43]\n\t" \
  "s_add_u32 s97, s64, 262144\n\t" \
  "ds_read_b64_tr_b16 a[218:219], v234 offset:61440\n\t" \
  "ds_read_b128 v[164:167], v232 offset:1024\n\t" \
  "s_and_b32 s97, s97, 0xe0000\n\t" \
  "v_mfma_f32_16x16x32_bf16 v[56:59], a[60:63], v[140:143], v[56:59]\n\t" \
  "s_add_u32 s97, s97, s62\n\t" \
  "ds_read_b128 v[168:171], v232 offset:2048\n\t" \
  "s_add_u32 m0, s38, 65536\n\t" \
  "v_mfma_f32_16x16x32_bf16 v[56:59], a[52:55], v[136:139], v[56:59]\n\t" \
  "ds_read_b128 v[172:175], v232 offset:3072\n\t" \
  "v_mfma_f32_16x16x32_bf16 v[56:59], a[44:47], v[132:135], v[56:59]\n\t" \
  "buffer_load_dwordx4 v242, s[16:19], s97 offen offset:0 sc1 lds\n\t" \
  "v_mfma_f32_16x16x32_bf16 v[56:59], a[36:39], v[128:131], v[56:59]\n\t" \
  "s_waitcnt lgkmcnt(8)\n\t" \
  "ds_read_b64_tr_b16 a[220:221], v235 offset:57344\n\t" \
  "v_mfma_f32_16x16x32_bf16 v[44:47], a[32:35], v[144:147], v[44:47]\n\t" \
  "ds_read_b128 v[128:131], v232 offset:4096\n\t" \
  "v_mfma_f32_16x16x32_bf16 v[44:47], a[40:43], v[148:151], v[44:47]\n\t" \
  "buffer_load_dwordx4 v242, s[16:19], s97 offen offset:1024 sc1 lds\n\t" \
  "v_mfma_f32_16x16x32_bf16 v[44:47], a[48:51], v[152:155], v[44:47]\n\t" \
  "ds_read_b64_tr_b16 a[222:223], v235 offset:61440\n\t" \
  "v_mfma_f32_16x16x32_bf16 v[44:47], a[56:59], v[156:159], v[44:47]\n\t" \
  "ds_read_b128 v[132:135], v232 offset:5120\n\t" \
  "v_mfma_f32_16x16x32_bf16 v[60:63], a[60:63], v[156:159], v[60:63]\n\t" \
  "ds_read_b128 v[136:139], v232 offset:6144\n\t" \
  "v_mfma_f32_16x16x32_bf16 v[60:63], a[52:55], v[152:155], v[60:63]\n\t" \
  "buffer_load_dwordx4 v242, s[16:19], s97 offen offset:2048 sc1 lds\n\t" \
  "v_mfma_f32_16x16x32_bf16 v[60:63], a[44:47], v[148:151], v[60:63]\n\t" \
  "ds_read_b128 v[140:143], v232 offset:7168\n\t" \
  "v_mfma_f32_16x16x32_bf16 v[60:63], a[36:39], v[144:147], v[60:63]\n\t" \
  "s_waitcnt lgkmcnt(3)\n\t" \
  "v_mfma_f32_16x16x32_bf16 v[0:3], a[192:195], v[160:163], v[0:3]\n\t" \
  "buffer_load_dwordx4 v242, s[16:19], s97 offen offset:3072 sc1 lds\n\t" \
  "v_mfma_f32_16x16x32_bf16 v[0:3], a[200:203], v[164:167], v[0:3]\n\t" \
  "s_add_u32 s97, s64, 393216\n\t" \
  "ds_read_b128 v[144:147], v232 offset:8192\n\t" \
  "s_and_b32 s97, s97, 0xe0000\n\t" \
  "v_mfma_f32_16x16x32_bf16 v[0:3], a[208:211], v[168:171], v[0:3]\n\t" \
  "s_add_u32 s97, s97, s62\n\t" \
  "ds_read_b128 v[148:151], v232 offset:9216\n\t" \
  "s_add_u32 m0, s38, 114688\n\t" \
  "v_mfma_f32_16x16x32_bf16 v[0:3], a[216:219], v[172:175], v[0:3]\n\t" \
  "ds_read_b128 v[152:155], v232 offset:10240\n\t" \
  "v_mfma_f32_16x16x32_bf16 v[16:19], a[220:223], v[172:175], v[16:19]\n\t" \
  "buffer_load_dwordx4 v242, s[16:19], s97 offen offset:0 sc1 lds\n\t" \
  "v_mfma_f32_16x16x32_bf16 v[16:19], a[212:215], v[168:171], v[16:19]\n\t" \
  "ds_read_b128 v[156:159], v232 offset:11264\n\t" \
  "v_mfma_f32_16x16x32_bf16 v[16:19], a[204:207], v[164:167], v[16:19]\n\t" \
  "v_mfma_f32_16x16x32_bf16 v[16:19], a[196:199], v[160:163], v[16:19]\n\t" \
  "s_waitcnt lgkmcnt(4)\n\t" \
  "buffer_load_dwordx4 v242, s[16:19], s97 offen offset:1024 sc1 lds\n\t" \
  "v_mfma_f32_16x16x32_bf16 v[4:7], a[192:195], v[128:131], v[4:7]\n\t" \
  "ds_read_b128 v[160:163], v232 offset:12288\n\t" \
  "v_cvt_pk_bf16_f32 v208, v0, v1\n\t" \
  "v_mfma_f32_16x16x32_bf16 v[4:7], a[200:203], v[132:135], v[4:7]\n\t" \
  "ds_read_b128 v[164:167], v232 offset:13312\n\t" \
  "v_cvt_pk_bf16_f32 v209, v2, v3\n\t" \
  "v_mfma_f32_16x16x32_bf16 v[4:7], a[208:211], v[136:139], v[4:7]\n\t" \
  "v_cvt_pk_bf16_f32 v210, v16, v17\n\t" \
  "v_cvt_pk_bf16_f32 v211, v18, v19\n\t" \
  "v_mfma_f32_16x16x32_bf16 v[4:7], a[216:219], v[140:143], v[4:7]\n\t" \
  "buffer_load_dwordx4 v242, s[16:19], s97 offen offset:2048 sc1 lds\n\t" \
  "v_mfma_f32_16x16x32_bf16 v[20:23], a[220:223], v[140:143], v[20:23]\n\t" \
  "ds_read_b128 v[168:171], v232 offset:14336\n\t" \
  "v_mfma_f32_16x16x32_bf16 v[20:23], a[212:215], v[136:139], v[20:23]\n\t" \
  "ds_read_b128 v[172:175], v232 offset:15360\n\t" \
  "v_mfma_f32_16x16x32_bf16 v[20:23], a[204:207], v[132:135], v[20:23]\n\t" \
  "v_mfma_f32_16x16x32_bf16 v[20:23], a[196:199], v[128:131], v[20:23]\n\t" \
  "s_waitcnt lgkmcnt(4)\n\t" \
  "v_cvt_pk_bf16_f32 v212, v4, v5\n\t" \
  "v_cvt_pk_bf16_f32 v213, v6, v7\n\t" \
  "v_mfma_f32_16x16x32_bf16 v[8:11], a[192:195], v[144:147], v[8:11]\n\t" \
  "ds_read_b128 v[128:131], v242 offset:16384\n\t" \
  "v_mfma_f32_16x16x32_bf16 v[8:11], a[200:203], v[148:151], v[8:11]\n\t" \
  "ds_read_b128 v[132:135], v242 offset:17408\n\t" \
  "v_mfma_f32_16x16x32_bf16 v[8:11], a[208:211], v[152:155], v[8:11]\n\t" \
  "v_cvt_pk_bf16_f32 v214, v20, v21\n\t" \
  "v_cvt_pk_bf16_f32 v215, v22, v23\n\t" \
  "v_mfma_f32_16x16x32_bf16 v[8:11], a[216:219], v[156:159], v[8:11]\n\t" \
  "ds_read_b128 v[136:139], v242 offset:18432\n\t" \
  "v_mfma_f32_16x16x32_bf16 v[24:27], a[220:223], v[156:159], v[24:27]\n\t" \
  "ds_read_b128 v[140:143], v242 offset:19456\n\t" \
  "v_mfma_f32_16x16x32_bf16 v[24:27], a[212:215], v[152:155], v[24:27]\n\t" \
  "v_mfma_f32_16x16x32_bf16 v[24:27], a[204:207], v[148:151], v[24:27]\n\t" \
  "v_mfma_f32_16x16x32_bf16 v[24:27], a[196:199], v[144:147], v[24:27]\n\t" \
  "s_waitcnt lgkmcnt(4)\n\t" \
  "v_mfma_f32_16x16x32_bf16 v[12:15], a[192:195], v[160:163], v[12:15]\n\t" \
  "ds_read_b128 v[144:147], v242 offset:20480\n\t" \
  "v_cvt_pk_bf16_f32 v216, v8, v9\n\t" \
  "v_mfma_f32_16x16x32_bf16 v[12:15], a[200:203], v[164:167], v[12:15]\n\t" \
  "ds_read_b128 v[148:151], v242 offset:21504\n\t" \
  "v_cvt_pk_bf16_f32 v217, v10, v11\n\t" \
  "v_mfma_f32_16x16x32_bf16 v[12:15], a[208:211], v[168:171], v[12:15]\n\t" \
  "v_cvt_pk_bf16_f32 v218, v24, v25\n\t" \
  "v_cvt_pk_bf16_f32 v219, v26, v27\n\t" \
  "v_mfma_f32_16x16x32_bf16 v[12:15], a[216:219], v[172:175], v[12:15]\n\t" \
  "buffer_store_dwordx4 v[208:211], v242, s[16:19], s67 offen offset:0 sc1\n\t" \
  "ds_read_b128 v[152:155], v242 offset:22528\n\t" \
  "ds_read_b128 v[156:159], v242 offset:23552\n\t" \
  "v_mfma_f32_16x16x32_bf16 v[28:31], a[220:223], v[172:175], v[28:31]\n\t" \
  "v_mfma_f32_16x16x32_bf16 v[28:31], a[212:215], v[168:171], v[28:31]\n\t" \
  "v_mfma_f32_16x16x32_bf16 v[28:31], a[204:207], v[164:167], v[28:31]\n\t" \
  "v_mfma_f32_16x16x32_bf16 v[28:31], a[196:199], v[160:163], v[28:31]\n\t" \
  "s_waitcnt lgkmcnt(4)\n\t" \
  "v_cvt_pk_bf16_f32 v220, v12, v13\n\t" \
  "v_cvt_pk_bf16_f32 v221, v14, v15\n\t" \
  "v_mfma_f32_16x16x32_bf16 v[32:35], a[64:67], v[128:131], v[32:35]\n\t" \
  "buffer_store_dwordx4 v[212:215], v242, s[16:19], s67 offen offset:1024 sc1\n\t" \
  "ds_read_b128 v[160:163], v242 offset:24576\n\t" \
  "ds_read_b128 v[164:167], v242 offset:25600\n\t" \
  "ds_read_b128 v[168:171], v242 offset:26624\n\t" \
  "v_mfma_f32_16x16x32_bf16 v[32:35], a[72:75], v[132:135], v[32:35]\n\t" \
  "v_cvt_pk_bf16_f32 v222, v28, v29\n\t" \
  "v_cvt_pk_bf16_f32 v223, v30, v31\n\t" \
  "v_mfma_f32_16x16x32_bf16 v[32:35], a[80:83], v[136:139], v[32:35]\n\t" \
  "ds_read_b128 v[172:175], v242 offset:27648\n\t" \
  "v_mfma_f32_16x16x32_bf16 v[32:35], a[88:91], v[140:143], v[32:35]\n\t" \
  "v_mfma_f32_16x16x32_bf16 v[48:51], a[92:95], v[140:143], v[48:51]\n\t" \
  "v_mfma_f32_16x16x32_bf16 v[48:51], a[84:87], v[136:139], v[48:51]\n\t" \
  "v_mfma_f32_16x16x32_bf16 v[48:51], a[76:79], v[132:135], v[48:51]\n\t" \
  "v_mfma_f32_16x16x32_bf16 v[48:51], a[68:71], v[128:131], v[48:51]\n\t" \
  "s_waitcnt lgkmcnt(4)\n\t" \
  "v_mfma_f32_16x16x32_bf16 v[36:39], a[64:67], v[144:147], v[36:39]\n\t" \
  "ds_read_b128 v[128:131], v242 offset:28672\n\t" \
  "v_mfma_f32_16x16x32_bf16 v[36:39], a[72:75], v[148:151], v[36:39]\n\t" \
  "ds_read_b128 v[132:135], v242 offset:29696\n\t" \
  "v_mfma_f32_16x16x32_bf16 v[36:39], a[80:83], v[152:155], v[36:39]\n\t" \
  "buffer_store_dwordx4 v[216:219], v242, s[16:19], s67 offen offset:2048 sc1\n\t" \
  "ds_read_b128 v[136:139], v242 offset:30720\n\t" \
  "ds_read_b128 v[140:143], v242 offset:31744\n\t" \
  "v_mfma_f32_16x16x32_bf16 v[36:39], a[88:91], v[156:159], v[36:39]\n\t" \
  "v_mfma_f32_16x16x32_bf16 v[52:55], a[92:95], v[156:159], v[52:55]\n\t" \
  "v_mfma_f32_16x16x32_bf16 v[52:55], a[84:87], v[152:155], v[52:55]\n\t" \
  "v_mfma_f32_16x16x32_bf16 v[52:55], a[76:79], v[148:151], v[52:55]\n\t" \
  "s_waitcnt vmcnt(14) lgkmcnt(4)\n\t" \
  "v_mfma_f32_16x16x32_bf16 v[52:55], a[68:71], v[144:147], v[52:55]\n\t" \
  "s_barrier\n\t" \
  "v_mfma_f32_16x16x32_bf16 v[40:43], a[64:67], v[160:163], v[40:43]\n\t" \
  "ds_read_b128 v[144:147], v242 offset:0\n\t" \
  "v_mfma_f32_16x16x32_bf16 v[40:43], a[72:75], v[164:167], v[40:43]\n\t" \
  "ds_read_b128 v[148:151], v242 offset:1024\n\t" \
  "v_mfma_f32_16x16x32_bf16 v[40:43], a[80:83], v[168:171], v[40:43]\n\t" \
  "ds_read_b128 v[152:155], v242 offset:2048\n\t" \
  "v_mfma_f32_16x16x32_bf16 v[40:43], a[88:91], v[172:175], v[40:43]\n\t" \
  "ds_read_b128 v[156:159], v242 offset:3072\n\t" \
  "v_mfma_f32_16x16x32_bf16 v[56:59], a[92:95], v[172:175], v[56:59]\n\t" \
  "v_mfma_f32_16x16x32_bf16 v[56:59], a[84:87], v[168:171], v[56:59]\n\t" \
  "v_mfma_f32_16x16x32_bf16 v[56:59], a[76:79], v[164:167], v[56:59]\n\t" \
  "v_mfma_f32_16x16x32_bf16 v[56:59], a[68:71], v[160:163], v[56:59]\n\t" \
  "s_waitcnt lgkmcnt(4)\n\t" \
  "buffer_store_dwordx4 v[220:223], v242, s[16:19], s67 offen offset:3072 sc1\n\t" \
  "v_mfma_f32_16x16x32_bf16 v[44:47], a[64:67], v[128:131], v[44:47]\n\t" \
  "ds_read_b128 v[160:163], v242 offset:4096\n\t" \
  "v_mfma_f32_16x16x32_bf16 v[44:47], a[72:75], v[132:135], v[44:47]\n\t" \
  "ds_read_b128 v[164:167], v242 offset:5120\n\t" \
  "v_mfma_f32_16x16x32_bf16 v[44:47], a[80:83], v[136:139], v[44:47]\n\t" \
  "ds_read_b128 v[168:171], v242 offset:6144\n\t" \
  "v_mfma_f32_16x16x32_bf16 v[44:47], a[88:91], v[140:143], v[44:47]\n\t" \
  "ds_read_b128 v[172:175], v242 offset:7168\n\t" \
  "v_mfma_f32_16x16x32_bf16 v[60:63], a[92:95], v[140:143], v[60:63]\n\t" \
  "v_mfma_f32_16x16x32_bf16 v[60:63], a[84:87], v[136:139], v[60:63]\n\t" \
  "v_mfma_f32_16x16x32_bf16 v[60:63], a[76:79], v[132:135], v[60:63]\n\t" \
  "v_mfma_f32_16x16x32_bf16 v[60:63], a[68:71], v[128:131], v[60:63]\n\t" \
  "s_waitcnt lgkmcnt(4)\n\t" \
  "v_mfma_f32_16x16x32_bf16 v[32:35], a[96:99], v[144:147], v[32:35]\n\t" \
  "ds_read_b128 v[128:131], v242 offset:8192\n\t" \
  "v_mfma_f32_16x16x32_bf16 v[32:35], a[104:107], v[148:151], v[32:35]\n\t" \
  "ds_read_b128 v[132:135], v242 offset:9216\n\t" \
  "v_mfma_f32_16x16x32_bf16 v[32:35], a[112:115], v[152:155], v[32:35]\n\t" \
  "ds_read_b128 v[136:139], v242 offset:10240\n\t" \
  "v_mfma_f32_16x16x32_bf16 v[32:35], a[120:123], v[156:159], v[32:35]\n\t" \
  "buffer_load_dwordx4 v242, s[16:19], s97 offen offset:3072 sc1 lds\n\t" \
  "v_mfma_f32_16x16x32_bf16 v[48:51], a[124:127], v[156:159], v[48:51]\n\t" \
  "s_add_u32 s97, s64, 524288\n\t" \
  "ds_read_b128 v[140:143], v242 offset:11264\n\t" \
  "s_and_b32 s97, s97, 0xe0000\n\t" \
  "v_mfma_f32_16x16x32_bf16 v[48:51], a[116:119], v[152:155], v[48:51]\n\t" \
  "s_add_u32 s97, s97, s62\n\t" \
  "s_add_u32 m0, s38, 81920\n\t" \
  "v_mfma_f32_16x16x32_bf16 v[48:51], a[108:111], v[148:151], v[48:51]\n\t" \
  "v_mfma_f32_16x16x32_bf16 v[48:51], a[100:103], v[144:147], v[48:51]\n\t" \
  "s_waitcnt lgkmcnt(4)\n\t" \
  "buffer_load_dwordx4 v242, s[16:19], s97 offen offset:0 sc1 lds\n\t" \
  "v_mfma_f32_16x16x32_bf16 v[36:39], a[96:99], v[160:163], v[36:39]\n\t" \
  "ds_read_b128 v[144:147], v242 offset:12288\n\t" \
  "v_mfma_f32_16x16x32_bf16 v[36:39], a[104:107], v[164:167], v[36:39]\n\t" \
  "ds_read_b128 v[148:151], v242 offset:13312\n\t" \
  "v_mfma_f32_16x16x32_bf16 v[36:39], a[112:115], v[168:171], v[36:39]\n\t" \
  "ds_read_b128 v[152:155], v242 offset:14336\n\t" \
  "v_mfma_f32_16x16x32_bf16 v[36:39], a[120:123], v[172:175], v[36:39]\n\t" \
  "buffer_load_dwordx4 v242, s[16:19], s97 offen offset:1024 sc1 lds\n\t" \
  "v_mfma_f32_16x16x32_bf16 v[52:55], a[124:127], v[172:175], v[52:55]\n\t" \
  "ds_read_b128 v[156:159], v242 offset:15360\n\t" \
  "v_mfma_f32_16x16x32_bf16 v[52:55], a[116:119], v[168:171], v[52:55]\n\t" \
  "v_mfma_f32_16x16x32_bf16 v[52:55], a[108:111], v[164:167], v[52:55]\n\t" \
  "buffer_load_dwordx4 v242, s[16:19], s97 offen offset:2048 sc1 lds\n\t" \
  "v_mfma_f32_16x16x32_bf16 v[52:55], a[100:103], v[160:163], v[52:55]\n\t" \
  "s_waitcnt lgkmcnt(4)\n\t" \
  "v_mfma_f32_16x16x32_bf16 v[40:43], a[96:99], v[128:131], v[40:43]\n\t" \
  "ds_read_b128 v[160:163], v242 offset:32768\n\t" \
  "v_mfma_f32_16x16x32_bf16 v[40:43], a[104:107], v[132:135], v[40:43]\n\t" \
  "ds_read_b128 v[164:167], v242 offset:33792\n\t" \
  "v_mfma_f32_16x16x32_bf16 v[40:43], a[112:115], v[136:139], v[40:43]\n\t" \
  "buffer_load_dwordx4 v242, s[16:19], s97 offen offset:3072 sc1 lds\n\t" \
  "v_mfma_f32_16x16x32_bf16 v[40:43], a[120:123], v[140:143], v[40:43]\n\t" \
  "s_add_u32 s97, s64, 655360\n\t" \
  "s_and_b32 s97, s97, 0xe0000\n\t" \
  "s_add_u32 s97, s97, s62\n\t" \
  "v_mfma_f32_16x16x32_bf16 v[56:59], a[124:127], v[140:143], v[56:59]\n\t" \
  "s_add_u32 m0, s38, 131072\n\t" \
  "ds_read_b128 v[168:171], v242 offset:34816\n\t" \
  "v_mfma_f32_16x16x32_bf16 v[56:59], a[116:119], v[136:139], v[56:59]\n\t" \
  "buffer_load_dwordx4 v242, s[16:19], s97 offen offset:0 sc1 lds\n\t" \
  "v_mfma_f32_16x16x32_bf16 v[56:59], a[108:111], v[132:135], v[56:59]\n\t" \
  "ds_read_b128 v[172:175], v242 offset:35840\n\t" \
  "v_mfma_f32_16x16x32_bf16 v[56:59], a[100:103], v[128:131], v[56:59]\n\t" \
  "s_waitcnt lgkmcnt(4)\n\t" \
  "v_mfma_f32_16x16x32_bf16 v[44:47], a[96:99], v[144:147], v[44:47]\n\t" \
  "ds_read_b128 v[128:131], v242 offset:36864\n\t" \
  "v_mfma_f32_16x16x32_bf16 v[44:47], a[104:107], v[148:151], v[44:47]\n\t" \
  "buffer_load_dwordx4 v242, s[16:19], s97 offen offset:1024 sc1 lds\n\t" \
  "v_mfma_f32_16x16x32_bf16 v[44:47], a[112:115], v[152:155], v[44:47]\n\t" \
  "ds_read_b128 v[132:135], v242 offset:37888\n\t" \
  "v_mfma_f32_16x16x32_bf16 v[44:47], a[120:123], v[156:159], v[44:47]\n\t" \
  "ds_read_b128 v[136:139], v242 offset:38912\n\t" \
  "v_mfma_f32_16x16x32_bf16 v[60:63], a[124:127], v[156:159], v[60:63]\n\t" \
  "buffer_load_dwordx4 v242, s[16:19], s97 offen offset:2048 sc1 lds\n\t" \
  "v_mfma_f32_16x16x32_bf16 v[60:63], a[116:119], v[152:155], v[60:63]\n\t" \
  "ds_read_b128 v[140:143], v242 offset:39936\n\t" \
  "v_mfma_f32_16x16x32_bf16 v[60:63], a[108:111], v[148:151], v[60:63]\n\t" \
  ".Lhs13:\n\ts_cbranch_execz .Lhs13n\n\t" \
  "v_mfma_f32_16x16x32_bf16 v[60:63], a[100:103], v[144:147], v[60:63]\n\t" \
  "s_waitcnt lgkmcnt(4)\n\t" \
  "v_mfma_f32_16x16x32_bf16 v[32:35], a[192:195], v[160:163], v[32:35]\n\t" \
  "buffer_load_dwordx4 v242, s[16:19], s97 offen offset:3072 sc1 lds\n\t" \
  "v_mfma_f32_16x16x32_bf16 v[32:35], a[200:203], v[164:167], v[32:35]\n\t" \
  "s_add_u32 s97, s64, 786432\n\t" \
  "ds_read_b128 v[144:147], v242 offset:40960\n\t" \
  "s_and_b32 s97, s97, 0xe0000\n\t" \
  "v_mfma_f32_16x16x32_bf16 v[32:35], a[208:211], v[168:171], v[32:35]\n\t" \
  "s_add_u32 s97, s97, s62\n\t" \
  "ds_read_b128 v[148:151], v242 offset:41984\n\t" \
  "s_add_u32 m0, s38, 49152\n\t" \
  "v_mfma_f32_16x16x32_bf16 v[32:35], a[216:219], v[172:175], v[32:35]\n\t" \
  "ds_read_b128 v[152:155], v242 offset:43008\n\t" \
  "v_mfma_f32_16x16x32_bf16 v[48:51], a[220:223], v[172:175], v[48:51]\n\t" \
  "ds_read_b128 v[156:159], v242 offset:44032\n\t" \
  "v_mfma_f32_16x16x32_bf16 v[48:51], a[212:215], v[168:171], v[48:51]\n\t" \
  "v_mfma_f32_16x16x32_bf16 v[48:51], a[204:207], v[164:167], v[48:51]\n\t" \
  "buffer_load_dwordx4 v242, s[16:19], s97 offen offset:0 sc1 lds\n\t" \
  "v_mfma_f32_16x16x32_bf16 v[48:51], a[196:199], v[160:163], v[48:51]\n\t" \
  "s_waitcnt lgkmcnt(4)\n\t" \
  "v_cvt_pk_bf16_f32 v0, v32, v33\n\t" \
  "v_cvt_pk_bf16_f32 v1, v34, v35\n\t" \
  "v_mfma_f32_16x16x32_bf16 v[36:39], a[192:195], v[128:131], v[36:39]\n\t" \
  "ds_read_b128 v[160:163], v242 offset:45056\n\t" \
  "v_mfma_f32_16x16x32_bf16 v[36:39], a[200:203], v[132:135], v[36:39]\n\t" \
  "ds_read_b128 v[164:167], v242 offset:46080\n\t" \
  "v_mfma_f32_16x16x32_bf16 v[36:39], a[208:211], v[136:139], v[36:39]\n\t" \
  "v_cvt_pk_bf16_f32 v2, v48, v49\n\t" \
  "v_cvt_pk_bf16_f32 v3, v50, v51\n\t" \
  "v_mfma_f32_16x16x32_bf16 v[36:39], a[216:219], v[140:143], v[36:39]\n\t" \
  "ds_read_b128 v[168:171], v242 offset:47104\n\t" \
  "v_mfma_f32_16x16x32_bf16 v[52:55], a[220:223], v[140:143], v[52:55]\n\t" \
  "ds_read_b128 v[172:175], v242 offset:48128\n\t" \
  "v_mfma_f32_16x16x32_bf16 v[52:55], a[212:215], v[136:139], v[52:55]\n\t" \
  "v_mfma_f32_16x16x32_bf16 v[52:55], a[204:207], v[132:135], v[52:55]\n\t" \
  "s_waitcnt vmcnt(17) lgkmcnt(4)\n\t" \
  "v_mfma_f32_16x16x32_bf16 v[52:55], a[196:199], v[128:131], v[52:55]\n\t" \
  "s_barrier\n\t" \
  "v_mfma_f32_16x16x32_bf16 v[40:43], a[192:195], v[144:147], v[40:43]\n\t" \
  "ds_read_b128 v[128:131], v230 offset:32768\n\t" \
  "v_cvt_pk_bf16_f32 v4, v36, v37\n\t" \
  "v_mfma_f32_16x16x32_bf16 v[40:43], a[200:203], v[148:151], v[40:43]\n\t" \
  "v_cvt_pk_bf16_f32 v5, v38, v39\n\t" \
  "v_and_b32 v235, 15, v233\n\t" \
  "v_mfma_f32_16x16x32_bf16 v[40:43], a[208:211], v[152:155], v[40:43]\n\t" \
  "v_cvt_pk_bf16_f32 v6, v52, v53\n\t" \
  "v_cvt_pk_bf16_f32 v7, v54, v55\n\t" \
  "v_mfma_f32_16x16x32_bf16 v[40:43], a[216:219], v[156:159], v[40:43]\n\t" \
  "buffer_store_dwordx4 v[0:3], v242, s[16:19], s68 offen offset:0 sc1\n\t" \
  "ds_read_b128 v[132:135], v230 offset:33792\n\t" \
  "v_lshrrev_b32 v236, 4, v233\n\t" \
  "v_lshlrev_b32 v234, 13, v236\n\t" \
  "v_mfma_f32_16x16x32_bf16 v[56:59], a[220:223], v[156:159], v[56:59]\n\t" \
  "ds_read_b128 v[136:139], v230 offset:34816\n\t" \
  "v_lshl_add_u32 v234, v235, 4, v234\n\t" \
  "v_mfma_f32_16x16x32_bf16 v[56:59], a[212:215], v[152:155], v[56:59]\n\t" \
  "buffer_load_dwordx4 v242, s[16:19], s97 offen offset:1024 sc1 lds\n\t" \
  "v_mfma_f32_16x16x32_bf16 v[56:59], a[204:207], v[148:151], v[56:59]\n\t" \
  "ds_read_b128 v[140:143], v230 offset:35840\n\t" \
  "v_lshlrev_b32 v240, 7, v236\n\t" \
  "v_mfma_f32_16x16x32_bf16 v[56:59], a[196:199], v[144:147], v[56:59]\n\t" \
  "s_waitcnt lgkmcnt(4)\n\t" \
  "v_cvt_pk_bf16_f32 v8, v40, v41\n\t" \
  "v_cvt_pk_bf16_f32 v9, v42, v43\n\t" \
  "v_mfma_f32_16x16x32_bf16 v[44:47], a[192:195], v[160:163], v[44:47]\n\t" \
  "s_waitcnt vmcnt(12)\n\t" \
  "buffer_store_dword v243, v244, s[16:19], s71 offen offset:0 sc1\n\t" \
  "ds_read_b128 v[144:147], v230 offset:36864\n\t" \
  "v_mfma_f32_16x16x32_bf16 v[44:47], a[200:203], v[164:167], v[44:47]\n\t" \
  "v_cvt_pk_bf16_f32 v10, v56, v57\n\t" \
  "v_cvt_pk_bf16_f32 v11, v58, v59\n\t" \
  "v_mfma_f32_16x16x32_bf16 v[44:47], a[208:211], v[168:171], v[44:47]\n\t" \
  "buffer_store_dwordx4 v[4:7], v242, s[16:19], s68 offen offset:1024 sc1\n\t" \
  "ds_read_b128 v[148:151], v230 offset:37888\n\t" \
  "v_lshl_add_u32 v240, v235, 3, v240\n\t" \
  "v_add_u32 v240, s76, v240\n\t" \
  "v_mfma_f32_16x16x32_bf16 v[44:47], a[216:219], v[172:175], v[44:47]\n\t" \
  "ds_read_b128 v[152:155], v230 offset:38912\n\t" \
  "v_lshlrev_b32 v235, 2, v235\n\t" \
  "v_mfma_f32_16x16x32_bf16 v[60:63], a[220:223], v[172:175], v[60:63]\n\t" \
  "ds_read_b128 v[156:159], v230 offset:39936\n\t" \
  "v_mfma_f32_16x16x32_bf16 v[60:63], a[212:215], v[168:171], v[60:63]\n\t" \
  "v_mfma_f32_16x16x32_bf16 v[60:63], a[204:207], v[164:167], v[60:63]\n\t" \
  "v_mfma_f32_16x16x32_bf16 v[60:63], a[196:199], v[160:163], v[60:63]\n\t" \
  "s_waitcnt lgkmcnt(4)\n\t" \
  "v_cvt_pk_bf16_f32 v12, v44, v45\n\t" \
  "v_cvt_pk_bf16_f32 v13, v46, v47\n\t" \
  STP("s_memrealtime s[94:95]\n\t") \
  STP("s_memtime s[92:93]\n\t") \
  "v_mfma_f32_16x16x32_bf16 v[64:67], a[128:131], v[128:131], v[64:67]\n\t" \
  "ds_read_b128 v[160:163], v230 offset:40960\n\t" \
  "v_mfma_f32_16x16x32_bf16 v[64:67], a[136:139], v[132:135], v[64:67]\n\t" \
  "ds_read_b128 v[164:167], v230 offset:41984\n\t" \
  "v_mfma_f32_16x16x32_bf16 v[64:67], a[144:147], v[136:139], v[64:67]\n\t" \
  "v_cvt_pk_bf16_f32 v14, v60, v61\n\t" \
  "v_cvt_pk_bf16_f32 v15, v62, v63\n\t" \
  "v_mfma_f32_16x16x32_bf16 v[64:67], a[152:155], v[140:143], v[64:67]\n\t" \
  "ds_read_b128 v[168:171], v230 offset:43008\n\t" \
  "s_add_u32 s78, s75, 0\n\t" \
  "v_mfma_f32_16x16x32_bf16 v[80:83], a[156:159], v[140:143], v[80:83]\n\t" \
  "ds_read_b128 v[172:175], v230 offset:44032\n\t" \
  "v_mfma_f32_16x16x32_bf16 v[80:83], a[148:151], v[136:139], v[80:83]\n\t" \
  "v_mfma_f32_16x16x32_bf16 v[80:83], a[140:143], v[132:135], v[80:83]\n\t" \
  "v_mfma_f32_16x16x32_bf16 v[80:83], a[132:135], v[128:131], v[80:83]\n\t" \
  "s_waitcnt lgkmcnt(4)\n\t" \
  "buffer_load_dwordx4 v[208:211], v234, s[16:19], s78 offen sc1\n\t" \
  "v_mfma_f32_16x16x32_bf16 v[68:71], a[128:131], v[144:147], v[68:71]\n\t" \
  "ds_read_b128 v[128:131], v230 offset:45056\n\t" \
  "s_add_u32 s78, s75, 32768\n\t" \
  "v_mfma_f32_16x16x32_bf16 v[68:71], a[136:139], v[148:151], v[68:71]\n\t" \
  "ds_read_b128 v[132:135], v230 offset:46080\n\t" \
  "v_mfma_f32_16x16x32_bf16 v[68:71], a[144:147], v[152:155], v[68:71]\n\t" \
  "ds_read_b128 v[136:139], v230 offset:47104\n\t" \
  "v_mfma_f32_16x16x32_bf16 v[68:71], a[152:155], v[156:159], v[68:71]\n\t" \
  "ds_read_b128 v[140:143], v230 offset:48128\n\t" \
  "v_mfma_f32_16x16x32_bf16 v[84:87], a[156:159], v[156:159], v[84:87]\n\t" \
  STP("v_writelane_b32 v255, s94, 34\n\t") \
  STP("v_writelane_b32 v255, s92, 35\n\t") \
  "buffer_load_dwordx4 v[212:215], v234, s[16:19], s78 offen sc1\n\t" \
  "v_mfma_f32_16x16x32_bf16 v[84:87], a[148:151], v[152:155], v[84:87]\n\t" \
  "s_add_u32 s78, s75, 65536\n\t" \
  "v_mfma_f32_16x16x32_bf16 v[84:87], a[140:143], v[148:151], v[84:87]\n\t" \
  "v_mfma_f32_16x16x32_bf16 v[84:87], a[132:135], v[144:147], v[84:87]\n\t" \
  "s_waitcnt lgkmcnt(4)\n\t" \
  "v_mfma_f32_16x16x32_bf16 v[72:75], a[128:131], v[160:163], v[72:75]\n\t" \
  "ds_read_b128 v[144:147], v230 offset:0\n\t" \
  "v_mfma_f32_16x16x32_bf16 v[72:75], a[136:139], v[164:167], v[72:75]\n\t" \
  "buffer_load_dwordx4 v[216:219], v234, s[16:19], s78 offen sc1\n\t" \
  "v_mfma_f32_16x16x32_bf16 v[72:75], a[144:147], v[168:171], v[72:75]\n\t" \
  "ds_read_b128 v[148:151], v230 offset:1024\n\t" \
  "s_add_u32 s78, s75, 98304\n\t" \
  "v_mfma_f32_16x16x32_bf16 v[72:75], a[152:155], v[172:175], v[72:75]\n\t" \
  "ds_read_b128 v[152:155], v230 offset:2048\n\t" \
  "v_mfma_f32_16x16x32_bf16 v[88:91], a[156:159], v[172:175], v[88:91]\n\t" \
  "ds_read_b128 v[156:159], v230 offset:3072\n\t" \
  "v_mfma_f32_16x16x32_bf16 v[88:91], a[148:151], v[168:171], v[88:91]\n\t" \
  "v_mfma_f32_16x16x32_bf16 v[88:91], a[140:143], v[164:167], v[88:91]\n\t" \
  "buffer_load_dwordx4 v[220:223], v234, s[16:19], s78 offen sc1\n\t" \
  "v_mfma_f32_16x16x32_bf16 v[88:91], a[132:135], v[160:163], v[88:91]\n\t" \
  "s_waitcnt lgkmcnt(4)\n\t" \
  "buffer_load_dword v224, v235, s[32:35], s80 offen\n\t" \
  "v_mfma_f32_16x16x32_bf16 v[76:79], a[128:131], v[128:131], v[76:79]\n\t" \
  "ds_read_b128 v[160:163], v230 offset:4096\n\t" \
  "v_lshrrev_b32 v250, 4, v233\n\t" \
  "v_mfma_f32_16x16x32_bf16 v[76:79], a[136:139], v[132:135], v[76:79]\n\t" \
  "v_and_b32 v238, 15, v233\n\t" \
  "v_xor_b32 v26, v238, v250\n\t" \
  "v_mfma_f32_16x16x32_bf16 v[76:79], a[144:147], v[136:139], v[76:79]\n\t" \
  "ds_read_b128 v[164:167], v230 offset:5120\n\t" \
  "v_lshlrev_b32 v26, 4, v26\n\t" \
  "v_mfma_f32_16x16x32_bf16 v[76:79], a[152:155], v[140:143], v[76:79]\n\t" \
  "v_lshl_add_u32 v26, v250, 11, v26\n\t" \
  "v_xor_b32 v27, 64, v26\n\t" \
  "v_mfma_f32_16x16x32_bf16 v[92:95], a[156:159], v[140:143], v[92:95]\n\t" \
  "ds_read_b128 v[168:171], v230 offset:6144\n\t" \
  "v_xor_b32 v28, 0x80, v26\n\t" \
  "v_mfma_f32_16x16x32_bf16 v[92:95], a[148:151], v[136:139], v[92:95]\n\t" \
  "v_xor_b32 v29, 0xc0, v26\n\t" \
  "v_lshrrev_b32 v30, 5, v233\n\t" \
  "v_mfma_f32_16x16x32_bf16 v[92:95], a[140:143], v[132:135], v[92:95]\n\t" \
  "ds_read_b128 v[172:175], v230 offset:7168\n\t" \
  "v_lshl_add_u32 v30, s3, 2, v30\n\t" \
  "v_mfma_f32_16x16x32_bf16 v[92:95], a[132:135], v[128:131], v[92:95]\n\t" \
  "s_waitcnt lgkmcnt(4)\n\t" \
  "v_xor_b32 v30, v30, v238\n\t" \
  "v_lshlrev_b32 v30, 4, v30\n\t" \
  "v_mfma_f32_16x16x32_bf16 v[64:67], a[160:163], v[144:147], v[64:67]\n\t" \
  "buffer_store_dwordx4 v[8:11], v242, s[16:19], s68 offen offset:2048 sc1\n\t" \
  "ds_read_b128 v[128:131], v230 offset:8192\n\t" \
  "v_lshl_add_u32 v30, v238, 8, v30\n\t" \
  "v_and_b32 v250, 1, v250\n\t" \
  "v_mfma_f32_16x16x32_bf16 v[64:67], a[168:171], v[148:151], v[64:67]\n\t" \
  "v_lshl_add_u32 v30, v250, 3, v30\n\t" \
  "v_add_u32 v238, 0, v30\n\t" \
  "v_mfma_f32_16x16x32_bf16 v[64:67], a[176:179], v[152:155], v[64:67]\n\t" \
  "ds_read_b128 v[132:135], v230 offset:9216\n\t" \
  "v_xor_b32 v239, 32, v238\n\t" \
  "v_mfma_f32_16x16x32_bf16 v[64:67], a[184:187], v[156:159], v[64:67]\n\t" \
  "v_bfe_u32 v237, v233, 1, 2\n\t" \
  "v_bfe_u32 v16, v233, 3, 1\n\t" \
  "v_mfma_f32_16x16x32_bf16 v[80:83], a[188:191], v[156:159], v[80:83]\n\t" \
  "ds_read_b128 v[136:139], v230 offset:10240\n\t" \
  "v_lshlrev_b32 v237, 4, v237\n\t" \
  "v_mfma_f32_16x16x32_bf16 v[80:83], a[180:183], v[152:155], v[80:83]\n\t" \
  "v_lshl_add_u32 v237, v16, 3, v237\n\t" \
  "v_and_b32 v16, 1, v233\n\t" \
  "v_mfma_f32_16x16x32_bf16 v[80:83], a[172:175], v[148:151], v[80:83]\n\t" \
  "ds_read_b128 v[140:143], v230 offset:11264\n\t" \
  "v_lshl_add_u32 v237, v16, 2, v237\n\t" \
  "v_mfma_f32_16x16x32_bf16 v[80:83], a[164:167], v[144:147], v[80:83]\n\t" \
  "s_waitcnt lgkmcnt(4)\n\t" \
  "v_add_u32 v237, s76, v237\n\t" \
  "v_cmp_eq_u32 vcc, 2, v236\n\t" \
  "v_mfma_f32_16x16x32_bf16 v[68:71], a[160:163], v[160:163], v[68:71]\n\t" \
  "ds_read_b128 v[144:147], v230 offset:12288\n\t" \
  "v_mov_b32 v16, 0\n\t" \
  "v_mfma_f32_16x16x32_bf16 v[68:71], a[168:171], v[164:167], v[68:71]\n\t" \
  "v_mov_b32 v17, 0\n\t" \
  "v_mov_b32 v18, 0\n\t" \
  "v_mfma_f32_16x16x32_bf16 v[68:71], a[176:179], v[168:171], v[68:71]\n\t" \
  "ds_read_b128 v[148:151], v230 offset:13312\n\t" \
  "v_mov_b32 v19, 0\n\t" \
  "v_mfma_f32_16x16x32_bf16 v[68:71], a[184:187], v[172:175], v[68:71]\n\t" \
  "v_cndmask_b32 v236, 0, 1.0, vcc\n\t" \
  "v_mov_b32 v20, 0x3f803f80\n\t" \
  "v_mfma_f32_16x16x32_bf16 v[84:87], a[188:191], v[172:175], v[84:87]\n\t" \
  "ds_read_b128 v[152:155], v230 offset:14336\n\t" \
  "v_add_u32 v21, s76, v242\n\t" \
  "v_mfma_f32_16x16x32_bf16 v[84:87], a[180:183], v[168:171], v[84:87]\n\t" \
  "buffer_load_dwordx4 v242, s[16:19], s97 offen offset:2048 sc1 lds\n\t" \
  "v_mfma_f32_16x16x32_bf16 v[84:87], a[172:175], v[164:167], v[84:87]\n\t" \
  "s_waitcnt vmcnt(16) lgkmcnt(3)\n\t" \
  "ds_write_b128 v21, v[16:19] offset:0\n\t" \
  "v_mfma_f32_16x16x32_bf16 v[84:87], a[164:167], v[160:163], v[84:87]\n\t" \
  "ds_write_b128 v21, v[16:19] offset:1024\n\t" \
  "s_barrier\n\t" \
  "v_mfma_f32_16x16x32_bf16 v[72:75], a[160:163], v[128:131], v[72:75]\n\t" \
  "buffer_load_dwordx4 v242, s[16:19], s97 offen offset:3072 sc1 lds\n\t" \
  "v_mfma_f32_16x16x32_bf16 v[72:75], a[168:171], v[132:135], v[72:75]\n\t" \
  "ds_read_b128 v[156:159], v230 offset:15360\n\t" \
  "v_mfma_f32_16x16x32_bf16 v[72:75], a[176:179], v[136:139], v[72:75]\n\t" \
  "ds_read_b128 v[160:163], v230 offset:49152\n\t" \
  "v_mfma_f32_16x16x32_bf16 v[72:75], a[184:187], v[140:143], v[72:75]\n\t" \
  "ds_write_b128 v21, v[16:19] offset:1536\n\t" \
  "v_mfma_f32_16x16x32_bf16 v[88:91], a[188:191], v[140:143], v[88:91]\n\t" \
  "buffer_store_dwordx4 v[12:15], v242, s[16:19], s68 offen offset:3072 sc1\n\t" \
  "ds_read_b128 v[164:167], v230 offset:50176\n\t" \
  "ds_read_b128 v[168:171], v230 offset:51200\n\t" \
  "ds_read_b128 v[172:175], v230 offset:52224\n\t" \
  "v_mfma_f32_16x16x32_bf16 v[88:91], a[180:183], v[136:139], v[88:91]\n\t" \
  "s_mov_b64 exec, 0xffff\n\t" \
  "ds_write_b32 v237, v20 offset:960\n\t" \
  "s_mov_b64 exec, -1\n\t" \
  "v_mfma_f32_16x16x32_bf16 v[88:91], a[172:175], v[132:135], v[88:91]\n\t" \
  "v_mfma_f32_16x16x32_bf16 v[88:91], a[164:167], v[128:131], v[88:91]\n\t" \
  "s_waitcnt lgkmcnt(6)\n\t" \
  "v_mfma_f32_16x16x32_bf16 v[76:79], a[160:163], v[144:147], v[76:79]\n\t" \
  "ds_read_b128 v[128:131], v230 offset:53248\n\t" \
  "v_mfma_f32_16x16x32_bf16 v[76:79], a[168:171], v[148:151], v[76:79]\n\t" \
  "ds_read_b128 v[132:135], v230 offset:54272\n\t" \
  "v_mfma_f32_16x16x32_bf16 v[76:79], a[176:179], v[152:155], v[76:79]\n\t" \
  "s_add_u32 m0, s38, 0\n\t" \
  "ds_read_b128 v[136:139], v230 offset:55296\n\t" \
  "s_add_u32 s96, s50, 0\n\t" \
  "v_mfma_f32_16x16x32_bf16 v[76:79], a[184:187], v[156:159], v[76:79]\n\t" \
  "ds_read_b128 v[140:143], v230 offset:56320\n\t" \
  "v_mfma_f32_16x16x32_bf16 v[92:95], a[188:191], v[156:159], v[92:95]\n\t" \
  "v_mfma_f32_16x16x32_bf16 v[92:95], a[180:183], v[152:155], v[92:95]\n\t" \
  "v_mfma_f32_16x16x32_bf16 v[92:95], a[172:175], v[148:151], v[92:95]\n\t" \
  "v_mfma_f32_16x16x32_bf16 v[92:95], a[164:167], v[144:147], v[92:95]\n\t" \
  "s_waitcnt lgkmcnt(5)\n\t" \
  "v_mfma_f32_16x16x32_bf16 v[64:67], a[0:3], v[160:163], v[64:67]\n\t" \
  "buffer_load_dwordx4 v26, s[4:7], s96 offen offset:0 lds\n\t" \
  "v_mfma_f32_16x16x32_bf16 v[64:67], a[8:11], v[164:167], v[64:67]\n\t" \
  "s_add_u32 s96, s50, 7168\n\t" \
  "ds_read_b128 v[144:147], v230 offset:57344\n\t" \
  "v_mfma_f32_16x16x32_bf16 v[64:67], a[16:19], v[168:171], v[64:67]\n\t" \
  "ds_read_b128 v[148:151], v230 offset:58368\n\t" \
  "v_mfma_f32_16x16x32_bf16 v[64:67], a[24:27], v[172:175], v[64:67]\n\t" \
  "ds_read_b128 v[152:155], v230 offset:59392\n\t" \
  "v_mfma_f32_16x16x32_bf16 v[80:83], a[28:31], v[172:175], v[80:83]\n\t" \
  "ds_read_b128 v[156:159], v230 offset:60416\n\t" \
  "v_mfma_f32_16x16x32_bf16 v[80:83], a[20:23], v[168:171], v[80:83]\n\t" \
  "buffer_load_dwordx4 v27, s[4:7], s96 offen offset:1024 lds\n\t" \
  "v_mfma_f32_16x16x32_bf16 v[80:83], a[12:15], v[164:167], v[80:83]\n\t" \
  "s_add_u32 s96, s50, 14336\n\t" \
  "v_mfma_f32_16x16x32_bf16 v[80:83], a[4:7], v[160:163], v[80:83]\n\t" \
  "s_waitcnt lgkmcnt(4)\n\t" \
  "v_mfma_f32_16x16x32_bf16 v[68:71], a[0:3], v[128:131], v[68:71]\n\t" \
  "ds_read_b128 v[160:163], v230 offset:61440\n\t" \
  "v_mfma_f32_16x16x32_bf16 v[68:71], a[8:11], v[132:135], v[68:71]\n\t" \
  "ds_read_b128 v[164:167], v230 offset:62464\n\t" \
  "v_mfma_f32_16x16x32_bf16 v[68:71], a[16:19], v[136:139], v[68:71]\n\t" \
  "buffer_load_dwordx4 v28, s[4:7], s96 offen offset:2048 lds\n\t" \
  "v_mfma_f32_16x16x32_bf16 v[68:71], a[24:27], v[140:143], v[68:71]\n\t" \
  "s_add_u32 s96, s50, 21504\n\t" \
  "ds_read_b128 v[168:171], v230 offset:63488\n\t" \
  "v_mfma_f32_16x16x32_bf16 v[84:87], a[28:31], v[140:143], v[84:87]\n\t" \
  "ds_read_b128 v[172:175], v230 offset:64512\n\t" \
  "v_mfma_f32_16x16x32_bf16 v[84:87], a[20:23], v[136:139], v[84:87]\n\t" \
  "v_mfma_f32_16x16x32_bf16 v[84:87], a[12:15], v[132:135], v[84:87]\n\t" \
  "v_mfma_f32_16x16x32_bf16 v[84:87], a[4:7], v[128:131], v[84:87]\n\t" \
  "s_waitcnt lgkmcnt(4)\n\t" \
  "buffer_load_dwordx4 v29, s[4:7], s96 offen offset:3072 lds\n\t" \
  "v_mfma_f32_16x16x32_bf16 v[72:75], a[0:3], v[144:147], v[72:75]\n\t" \
  "s_mov_b32 s40, 0x05040100\n\t" \
  "ds_read_b128 v[128:131], v230 offset:16384\n\t" \
  "s_mov_b32 s41, 0x07060302\n\t" \
  "v_mfma_f32_16x16x32_bf16 v[72:75], a[8:11], v[148:151], v[72:75]\n\t" \
  "s_load_dwordx4 s[4:7], s[0:1], 0xc0\n\t" \
  "ds_read_b128 v[132:135], v230 offset:17408\n\t" \
  "s_and_b32 s42, s2, 4\n\t" \
  "v_mfma_f32_16x16x32_bf16 v[72:75], a[16:19], v[152:155], v[72:75]\n\t" \
  "s_add_u32 s97, s64, 917504\n\t" \
  "s_and_b32 s97, s97, 0xe0000\n\t" \
  "s_add_u32 s97, s97, s62\n\t" \
  "v_mfma_f32_16x16x32_bf16 v[72:75], a[24:27], v[156:159], v[72:75]\n\t" \
  "s_add_u32 m0, s38, 16384\n\t" \
  "ds_read_b128 v[136:139], v230 offset:18432\n\t" \
  "s_lshl_b32 s9, s42, 11\n\t" \
  "v_mfma_f32_16x16x32_bf16 v[88:91], a[28:31], v[156:159], v[88:91]\n\t" \
  "buffer_load_dwordx4 v242, s[16:19], s97 offen offset:0 sc1 lds\n\t" \
  "v_mfma_f32_16x16x32_bf16 v[88:91], a[20:23], v[152:155], v[88:91]\n\t" \
  "s_xor_b32 s9, s9, s55\n\t" \
  "ds_read_b128 v[140:143], v230 offset:19456\n\t" \
  "s_lshr_b32 s43, s2, 6\n\t" \
  "v_mfma_f32_16x16x32_bf16 v[88:91], a[12:15], v[148:151], v[88:91]\n\t" \
  "s_add_u32 s42, s42, s43\n\t" \
  "s_and_b32 s43, s2, 3\n\t" \
  "s_lshl2_add_u32 s8, s42, s43\n\t" \
  "v_mfma_f32_16x16x32_bf16 v[88:91], a[4:7], v[144:147], v[88:91]\n\t" \
  "s_waitcnt lgkmcnt(4)\n\t" \
  "s_and_b32 s11, s8, 15\n\t" \
  "s_lshl_b32 s11, s11, 15\n\t" \
  "v_mfma_f32_16x16x32_bf16 v[76:79], a[0:3], v[160:163], v[76:79]\n\t" \
  "buffer_load_dwordx4 v242, s[16:19], s97 offen offset:1024 sc1 lds\n\t" \
  "v_mfma_f32_16x16x32_bf16 v[76:79], a[8:11], v[164:167], v[76:79]\n\t" \
  "s_add_u32 s11, s11, s9\n\t" \
  "ds_read_b128 v[144:147], v230 offset:20480\n\t" \
  "s_add_u32 s11, s11, 524288\n\t" \
  "v_mfma_f32_16x16x32_bf16 v[76:79], a[16:19], v[168:171], v[76:79]\n\t" \
  "s_waitcnt vmcnt(10)\n\t" \
  "v_xor_b32 v246, v243, v209\n\t" \
  "v_bitop3_b32 v246, v246, v211, v243 bitop3:0xf6\n\t" \
  "v_mfma_f32_16x16x32_bf16 v[76:79], a[24:27], v[172:175], v[76:79]\n\t" \
  "s_and_b32 s42, s2, 0x20\n\t" \
  "v_bitop3_b32 v246, v246, v213, v243 bitop3:0xf6\n\t" \
  "v_bitop3_b32 v246, v246, v215, v243 bitop3:0xf6\n\t" \
  "v_mfma_f32_16x16x32_bf16 v[92:95], a[28:31], v[172:175], v[92:95]\n\t" \
  "s_lshl_b32 s42, s42, 9\n\t" \
  "v_bitop3_b32 v246, v246, v217, v243 bitop3:0xf6\n\t" \
  "v_bitop3_b32 v246, v246, v219, v243 bitop3:0xf6\n\t" \
  "v_mfma_f32_16x16x32_bf16 v[92:95], a[20:23], v[168:171], v[92:95]\n\t" \
  "s_add_u32 s10, s11, s42\n\t" \
  "v_bitop3_b32 v246, v246, v221, v243 bitop3:0xf6\n\t" \
  "v_bitop3_b32 v246, v246, v223, v243 bitop3:0xf6\n\t" \
  "v_mfma_f32_16x16x32_bf16 v[92:95], a[12:15], v[164:167], v[92:95]\n\t" \
  "buffer_load_dwordx4 v242, s[16:19], s97 offen offset:2048 sc1 lds\n\t" \
  "v_mfma_f32_16x16x32_bf16 v[92:95], a[4:7], v[160:163], v[92:95]\n\t" \
  "s_waitcnt lgkmcnt(1)\n\t" \
  "ds_read_b128 v[148:151], v230 offset:21504\n\t" \
  "v_cmp_ne_u32 vcc, 0, v246\n\t" \
  "v_mfma_f32_16x16x32_bf16 v[64:67], a[32:35], v[128:131], v[64:67]\n\t" \
  "buffer_load_dwordx4 v242, s[16:19], s97 offen offset:3072 sc1 lds\n\t" \
  "ds_read_b128 v[152:155], v230 offset:22528\n\t" \
  "s_and_b32 s42, s2, 0x18\n\t" \
  "v_mfma_f32_16x16x32_bf16 v[64:67], a[40:43], v[132:135], v[64:67]\n\t" \
  "s_add_u32 s97, s64, 131072\n\t" \
  "s_and_b32 s97, s97, 0xe0000\n\t" \
  "s_add_u32 s97, s97, s63\n\t" \
  "v_mfma_f32_16x16x32_bf16 v[64:67], a[48:51], v[136:139], v[64:67]\n\t" \
  "s_cbranch_vccnz .Lat_s3\n\t" \
  ".Lat_k3:\n\t" \
  "ds_read_b128 v[156:159], v230 offset:23552\n\t" \
  "s_add_u32 m0, s38, 32768\n\t" \
  "v_mfma_f32_16x16x32_bf16 v[64:67], a[56:59], v[140:143], v[64:67]\n\t" \
  "buffer_load_dwordx4 v242, s[16:19], s97 offen offset:0 sc1 lds\n\t" \
  "v_mfma_f32_16x16x32_bf16 v[80:83], a[60:63], v[140:143], v[80:83]\n\t" \
  "s_lshl_b32 s42, s42, 8\n\t" \
  "ds_read_b128 v[160:163], v230 offset:24576\n\t" \
  "v_add_f32 v208, v208, v212\n\t" \
  "v_mfma_f32_16x16x32_bf16 v[80:83], a[52:55], v[136:139], v[80:83]\n\t" \
  "s_add_u32 s10, s10, s42\n\t" \
  "ds_read_b128 v[164:167], v230 offset:25600\n\t" \
  "v_add_f32 v216, v216, v220\n\t" \
  "v_mfma_f32_16x16x32_bf16 v[80:83], a[44:47], v[132:135], v[80:83]\n\t" \
  "s_mul_i32 s42, s3, 0x3000\n\t" \
  "ds_read_b128 v[168:171], v230 offset:26624\n\t" \
  "v_add_f32 v210, v210, v214\n\t" \
  "v_mfma_f32_16x16x32_bf16 v[80:83], a[36:39], v[128:131], v[80:83]\n\t" \
  "s_waitcnt lgkmcnt(3)\n\t" \
  "ds_read_b128 v[172:175], v230 offset:27648\n\t" \
  "v_add_f32 v218, v218, v222\n\t" \
  "v_mfma_f32_16x16x32_bf16 v[68:71], a[32:35], v[144:147], v[68:71]\n\t" \
  "buffer_load_dwordx4 v242, s[16:19], s97 offen offset:1024 sc1 lds\n\t" \
  "v_mfma_f32_16x16x32_bf16 v[68:71], a[40:43], v[148:151], v[68:71]\n\t" \
  "s_and_b32 s42, s42, 0x1000\n\t" \
  "ds_read_b128 v[128:131], v230 offset:28672\n\t" \
  "v_add_f32 v208, v208, v216\n\t" \
  "v_mfma_f32_16x16x32_bf16 v[68:71], a[48:51], v[152:155], v[68:71]\n\t" \
  "s_add_u32 s11, s11, s42\n\t" \
  "ds_read_b128 v[132:135], v230 offset:29696\n\t" \
  "v_add_f32 v210, v210, v218\n\t" \
  "v_mfma_f32_16x16x32_bf16 v[68:71], a[56:59], v[156:159], v[68:71]\n\t" \
  "s_and_b32 s42, s3, 2\n\t" \
  "v_lshlrev_b32 v19, 16, v224\n\t" \
  "v_and_b32 v20, 0xffff0000, v224\n\t" \
  "v_mfma_f32_16x16x32_bf16 v[84:87], a[60:63], v[156:159], v[84:87]\n\t" \
  "s_lshl_b32 s42, s42, 13\n\t" \
  "ds_read_b128 v[136:139], v230 offset:30720\n\t" \
  "v_cvt_pk_bf16_f32 v18, v208, v210\n\t" \
  "v_mfma_f32_16x16x32_bf16 v[84:87], a[52:55], v[152:155], v[84:87]\n\t" \
  "s_add_u32 s11, s11, s42\n\t" \
  "v_lshlrev_b32 v16, 16, v18\n\t" \
  "v_and_b32 v17, 0xffff0000, v18\n\t" \
  "v_mfma_f32_16x16x32_bf16 v[84:87], a[44:47], v[148:151], v[84:87]\n\t" \
  "s_waitcnt vmcnt(11) lgkmcnt(3)\n\t" \
  "ds_read_b128 v[140:143], v230 offset:31744\n\t" \
  "v_add_f32 v16, v16, v236\n\t" \
  "v_mfma_f32_16x16x32_bf16 v[84:87], a[36:39], v[144:147], v[84:87]\n\t" \
  "s_barrier\n\t" \
  "v_mfma_f32_16x16x32_bf16 v[72:75], a[32:35], v[160:163], v[72:75]\n\t" \
  "buffer_load_dwordx4 v242, s[16:19], s97 offen offset:2048 sc1 lds\n\t" \
  "v_mfma_f32_16x16x32_bf16 v[72:75], a[40:43], v[164:167], v[72:75]\n\t" \
  "s_add_u32 s12, s11, 2048\n\t" \
  "ds_read_b128 v[144:147], v232 offset:0\n\t" \
  "v_add_f32 v17, v17, v236\n\t" \
  "v_mfma_f32_16x16x32_bf16 v[72:75], a[48:51], v[168:171], v[72:75]\n\t" \
  "s_sub_u32 s39, s10, s11\n\t" \
  "ds_read_b128 v[148:151], v232 offset:1024\n\t" \
  "v_cvt_pk_bf16_f32 v18, v16, v17\n\t" \
  "v_mfma_f32_16x16x32_bf16 v[72:75], a[56:59], v[172:175], v[72:75]\n\t" \
  "v_lshlrev_b32 v16, 16, v18\n\t" \
  "v_and_b32 v17, 0xffff0000, v18\n\t" \
  "v_mfma_f32_16x16x32_bf16 v[88:91], a[60:63], v[172:175], v[88:91]\n\t" \
  "ds_read_b128 v[152:155], v232 offset:2048\n\t" \
  "v_mfma_f32_16x16x32_bf16 v[88:91], a[52:55], v[168:171], v[88:91]\n\t" \
  "s_waitcnt vmcnt(11)\n\t" \
  "buffer_store_dword v243, v244, s[16:19], s72 offen offset:16 sc1\n\t" \
  "ds_read_b128 v[156:159], v232 offset:3072\n\t" \
  "v_mfma_f32_16x16x32_bf16 v[88:91], a[44:47], v[164:167], v[88:91]\n\t" \
  "ds_write_b64 v240, v[16:17] offset:2560\n\t" \
  "v_mfma_f32_16x16x32_bf16 v[88:91], a[36:39], v[160:163], v[88:91]\n\t" \
  "s_waitcnt lgkmcnt(5)\n\t" \
  "v_mul_f32 v22, v16, v19\n\t" \
  "v_mul_f32 v23, v17, v20\n\t" \
  "v_mfma_f32_16x16x32_bf16 v[76:79], a[32:35], v[128:131], v[76:79]\n\t" \
  "ds_read_b128 v[160:163], v232 offset:4096\n\t" \
  "v_cvt_pk_bf16_f32 v24, v22, v23\n\t" \
  "v_mfma_f32_16x16x32_bf16 v[76:79], a[40:43], v[132:135], v[76:79]\n\t" \
  "v_add_f32 v18, v22, v23\n\t" \
  "v_lshlrev_b32 v19, 16, v24\n\t" \
  "v_mfma_f32_16x16x32_bf16 v[76:79], a[48:51], v[136:139], v[76:79]\n\t" \
  "ds_read_b128 v[164:167], v232 offset:5120\n\t" \
  "v_and_b32 v20, 0xffff0000, v24\n\t" \
  "v_mfma_f32_16x16x32_bf16 v[76:79], a[56:59], v[140:143], v[76:79]\n\t" \
  "v_add_f32_dpp v18, v18, v18 row_ror:8 row_mask:0xf bank_mask:0xf\n\t" \
  "v_sub_f32 v22, v22, v19\n\t" \
  "v_mfma_f32_16x16x32_bf16 v[92:95], a[60:63], v[140:143], v[92:95]\n\t" \
  "ds_read_b128 v[168:171], v232 offset:6144\n\t" \
  "v_sub_f32 v23, v23, v20\n\t" \
  "v_mfma_f32_16x16x32_bf16 v[92:95], a[52:55], v[136:139], v[92:95]\n\t" \
  "v_add_f32_dpp v18, v18, v18 row_ror:4 row_mask:0xf bank_mask:0xf\n\t" \
  "v_cvt_pk_bf16_f32 v25, v22, v23\n\t" \
  "v_mfma_f32_16x16x32_bf16 v[92:95], a[44:47], v[132:135], v[92:95]\n\t" \
  "s_mov_b32 s84, 0\n\t" \
  "ds_read_b128 v[172:175], v232 offset:7168\n\t" \
  "v_add_f32_dpp v18, v18, v18 row_ror:2 row_mask:0xf bank_mask:0xf\n\t" \
  "v_mfma_f32_16x16x32_bf16 v[92:95], a[36:39], v[128:131], v[92:95]\n\t" \
  "s_waitcnt lgkmcnt(5)\n\t" \
  "buffer_load_dwordx4 v242, s[16:19], s97 offen offset:3072 sc1 lds\n\t" \
  "v_mfma_f32_16x16x32_bf16 v[64:67], a[64:67], v[144:147], v[64:67]\n\t" \
  "s_mov_b32 s85, 0xffff\n\t" \
  "ds_read_b128 v[128:131], v232 offset:8192\n\t" \
  "v_add_f32_dpp v18, v18, v18 row_ror:1 row_mask:0xf bank_mask:0xf\n\t" \
  "v_mfma_f32_16x16x32_bf16 v[64:67], a[72:75], v[148:151], v[64:67]\n\t" \
  "s_mov_b64 exec, s[84:85]\n\t" \
  "ds_write_b32 v237, v24 offset:1040\n\t" \
  "ds_write_b32 v237, v25 offset:1120\n\t" \
  "s_mov_b64 exec, -1\n\t" \
  "v_mfma_f32_16x16x32_bf16 v[64:67], a[80:83], v[152:155], v[64:67]\n\t" \
  "s_add_u32 s97, s64, 262144\n\t" \
  "s_and_b32 s97, s97, 0xe0000\n\t" \
  "s_add_u32 s97, s97, s63\n\t" \
  "v_mfma_f32_16x16x32_bf16 v[64:67], a[88:91], v[156:159], v[64:67]\n\t" \
  "s_add_u32 m0, s38, 65536\n\t" \
  "ds_read_b128 v[132:135], v232 offset:9216\n\t" \
  "v_readlane_b32 s82, v18, 32\n\t" \
  "v_mfma_f32_16x16x32_bf16 v[80:83], a[92:95], v[156:159], v[80:83]\n\t" \
  "buffer_load_dwordx4 v242, s[16:19], s97 offen offset:0 sc1 lds\n\t" \
  "v_mfma_f32_16x16x32_bf16 v[80:83], a[84:87], v[152:155], v[80:83]\n\t" \
  "ds_read_b128 v[136:139], v232 offset:10240\n\t" \
  "v_readlane_b32 s83, v18, 16\n\t" \
  "v_mfma_f32_16x16x32_bf16 v[80:83], a[76:79], v[148:151], v[80:83]\n\t" \
  "ds_read_b128 v[140:143], v232 offset:11264\n\t" \
  "v_and_b32 v235, 16, v242\n\t" \
  "v_mfma_f32_16x16x32_bf16 v[80:83], a[68:71], v[144:147], v[80:83]\n\t" \
  "s_waitcnt lgkmcnt(6)\n\t" \
  "s_cmp_eq_u32 s39, 0\n\t" \
  "s_cselect_b32 s11, s12, s11\n\t" \
  "v_mfma_f32_16x16x32_bf16 v[68:71], a[64:67], v[160:163], v[68:71]\n\t" \
  "ds_read_b128 v[144:147], v232 offset:12288\n\t" \
  "v_mfma_f32_16x16x32_bf16 v[68:71], a[72:75], v[164:167], v[68:71]\n\t" \
  "buffer_load_dwordx4 v242, s[16:19], s97 offen offset:1024 sc1 lds\n\t" \
  "v_mfma_f32_16x16x32_bf16 v[68:71], a[80:83], v[168:171], v[68:71]\n\t" \
  "ds_read_b128 v[148:151], v232 offset:13312\n\t" \
  "v_mfma_f32_16x16x32_bf16 v[68:71], a[88:91], v[172:175], v[68:71]\n\t" \
  "s_cmp_eq_u32 s39, 2048\n\t" \
  "s_cselect_b32 s12, s11, s12\n\t" \
  "s_lshl_b32 s42, s3, 10\n\t" \
  "v_mfma_f32_16x16x32_bf16 v[84:87], a[92:95], v[172:175], v[84:87]\n\t" \
  "buffer_load_dwordx4 v242, s[16:19], s97 offen offset:2048 sc1 lds\n\t" \
  "v_mfma_f32_16x16x32_bf16 v[84:87], a[84:87], v[168:171], v[84:87]\n\t" \
  "s_add_u32 s10, s10, s42\n\t" \
  "ds_read_b128 v[152:155], v232 offset:14336\n\t" \
  "s_lshr_b32 s42, s2, 1\n\t" \
  "v_mfma_f32_16x16x32_bf16 v[84:87], a[76:79], v[164:167], v[84:87]\n\t" \
  "s_and_b32 s42, s42, 12\n\t" \
  "ds_read_b128 v[156:159], v232 offset:15360\n\t" \
  "s_or_b32 s42, s42, s43\n\t" \
  "v_mfma_f32_16x16x32_bf16 v[84:87], a[68:71], v[160:163], v[84:87]\n\t" \
  "s_waitcnt lgkmcnt(4)\n\t" \
  "buffer_load_dwordx4 v242, s[16:19], s97 offen offset:3072 sc1 lds\n\t" \
  "v_mfma_f32_16x16x32_bf16 v[72:75], a[64:67], v[128:131], v[72:75]\n\t" \
  "s_add_u32 s97, s64, 393216\n\t" \
  "s_and_b32 s97, s97, 0xe0000\n\t" \
  "s_add_u32 s97, s97, s63\n\t" \
  "v_mfma_f32_16x16x32_bf16 v[72:75], a[72:75], v[132:135], v[72:75]\n\t" \
  "s_add_u32 m0, s38, 98304\n\t" \
  "ds_read_b128 v[160:163], v242 offset:49152\n\t" \
  "s_lshl_b32 s13, s42, 8\n\t" \
  "v_mfma_f32_16x16x32_bf16 v[72:75], a[80:83], v[136:139], v[72:75]\n\t" \
  "buffer_load_dwordx4 v242, s[16:19], s97 offen offset:0 sc1 lds\n\t" \
  "v_mfma_f32_16x16x32_bf16 v[72:75], a[88:91], v[140:143], v[72:75]\n\t" \
  "s_add_u32 s13, s13, s9\n\t" \
  "ds_read_b128 v[164:167], v242 offset:50176\n\t" \
  "s_add_u32 s13, s13, 65536\n\t" \
  "v_mfma_f32_16x16x32_bf16 v[88:91], a[92:95], v[140:143], v[88:91]\n\t" \
  "s_bfe_u32 s44, s2, 0x30003\n\t" \
  "ds_read_b128 v[168:171], v242 offset:51200\n\t" \
  "s_lshl_b32 s42, s3, 13\n\t" \
  "v_mfma_f32_16x16x32_bf16 v[88:91], a[84:87], v[136:139], v[88:91]\n\t" \
  "buffer_load_dwordx4 v242, s[16:19], s97 offen offset:1024 sc1 lds\n\t" \
  "v_mfma_f32_16x16x32_bf16 v[88:91], a[76:79], v[132:135], v[88:91]\n\t" \
  "s_and_b32 s42, s42, 0x2000\n\t" \
  "ds_read_b128 v[172:175], v242 offset:52224\n\t" \
  "s_xor_b32 s14, s42, s55\n\t" \
  "v_mfma_f32_16x16x32_bf16 v[88:91], a[68:71], v[128:131], v[88:91]\n\t" \
  "s_waitcnt lgkmcnt(4)\n\t" \
  "s_lshr_b32 s42, s3, 1\n\t" \
  "s_lshl1_add_u32 s42, s44, s42\n\t" \
  "v_mfma_f32_16x16x32_bf16 v[76:79], a[64:67], v[144:147], v[76:79]\n\t" \
  "s_lshl_b32 s42, s42, 8\n\t" \
  "ds_read_b128 v[128:131], v242 offset:53248\n\t" \
  "s_add_u32 s14, s14, s42\n\t" \
  "v_mfma_f32_16x16x32_bf16 v[76:79], a[72:75], v[148:151], v[76:79]\n\t" \
  "s_lshl3_add_u32 s14, s8, s14\n\t" \
  "ds_read_b128 v[132:135], v242 offset:54272\n\t" \
  "s_add_u32 s14, s14, 65536\n\t" \
  "v_mfma_f32_16x16x32_bf16 v[76:79], a[80:83], v[152:155], v[76:79]\n\t" \
  "s_add_u32 s15, s9, 262144\n\t" \
  "ds_read_b128 v[136:139], v242 offset:55296\n\t" \
  "s_lshl_b32 s42, s8, 13\n\t" \
  "v_mfma_f32_16x16x32_bf16 v[76:79], a[88:91], v[156:159], v[76:79]\n\t" \
  "s_and_b32 s42, s42, 0x3c000\n\t" \
  "ds_read_b128 v[140:143], v242 offset:56320\n\t" \
  "s_add_u32 s20, s15, s42\n\t" \
  "v_mfma_f32_16x16x32_bf16 v[92:95], a[92:95], v[156:159], v[92:95]\n\t" \
  "buffer_load_dwordx4 v242, s[16:19], s97 offen offset:2048 sc1 lds\n\t" \
  "v_mfma_f32_16x16x32_bf16 v[92:95], a[84:87], v[152:155], v[92:95]\n\t" \
  "s_and_b32 s42, s8, 1\n\t" \
  "s_lshl_b32 s42, s42, 12\n\t" \
  "s_add_u32 s20, s20, s42\n\t" \
  "v_mfma_f32_16x16x32_bf16 v[92:95], a[76:79], v[148:151], v[92:95]\n\t" \
  "s_lshl2_add_u32 s42, s44, s3\n\t" \
  "s_lshl_b32 s42, s42, 7\n\t" \
  "s_add_u32 s20, s20, s42\n\t" \
  "v_mfma_f32_16x16x32_bf16 v[92:95], a[68:71], v[144:147], v[92:95]\n\t" \
  "s_waitcnt lgkmcnt(4)\n\t" \
  "s_and_b32 s21, s42, 0x7ff\n\t" \
  "s_lshl_b32 s22, s8, 17\n\t" \
  "v_mfma_f32_16x16x32_bf16 v[64:67], a[96:99], v[160:163], v[64:67]\n\t" \
  "s_lshr_b32 s42, s42, 1\n\t" \
  "ds_read_b128 v[144:147], v242 offset:57344\n\t" \
  "s_add_u32 s22, s22, s42\n\t" \
  "v_mfma_f32_16x16x32_bf16 v[64:67], a[104:107], v[164:167], v[64:67]\n\t" \
  "buffer_load_dwordx4 v242, s[16:19], s97 offen offset:3072 sc1 lds\n\t" \
  "v_mfma_f32_16x16x32_bf16 v[64:67], a[112:115], v[168:171], v[64:67]\n\t" \
  "s_and_b32 s42, s2, 0x20\n\t" \
  "ds_read_b128 v[148:151], v242 offset:58368\n\t" \
  "s_lshl_b32 s42, s42, 8\n\t" \
  "v_mfma_f32_16x16x32_bf16 v[64:67], a[120:123], v[172:175], v[64:67]\n\t" \
  "s_xor_b32 s42, s42, s55\n\t" \
  "ds_read_b128 v[152:155], v242 offset:59392\n\t" \
  "s_add_u32 s21, s21, s42\n\t" \
  "v_mfma_f32_16x16x32_bf16 v[80:83], a[124:127], v[172:175], v[80:83]\n\t" \
  "s_add_u32 s97, s64, 524288\n\t" \
  "s_and_b32 s97, s97, 0xe0000\n\t" \
  "s_add_u32 s97, s97, s63\n\t" \
  "v_mfma_f32_16x16x32_bf16 v[80:83], a[116:119], v[168:171], v[80:83]\n\t" \
  "s_add_u32 m0, s38, 114688\n\t" \
  "ds_read_b128 v[156:159], v242 offset:60416\n\t" \
  "s_lshl_b32 s42, s43, 11\n\t" \
  "v_mfma_f32_16x16x32_bf16 v[80:83], a[108:111], v[164:167], v[80:83]\n\t" \
  ".Lhs14:\n\ts_cbranch_execz .Lhs14n\n\t" \
  "buffer_load_dwordx4 v242, s[16:19], s97 offen offset:0 sc1 lds\n\t" \
  "v_mfma_f32_16x16x32_bf16 v[80:83], a[100:103], v[160:163], v[80:83]\n\t" \
  "s_waitcnt lgkmcnt(4)\n\t" \
  "s_add_u32 s21, s21, s42\n\t" \
  "s_lshr_b32 s42, s8, 2\n\t" \
  "v_mfma_f32_16x16x32_bf16 v[68:71], a[96:99], v[128:131], v[68:71]\n\t" \
  "s_lshl_b32 s42, s42, 14\n\t" \
  "ds_read_b128 v[160:163], v242 offset:61440\n\t" \
  "s_add_u32 s21, s21, s42\n\t" \
  "v_mfma_f32_16x16x32_bf16 v[68:71], a[104:107], v[132:135], v[68:71]\n\t" \
  "buffer_load_dwordx4 v242, s[16:19], s97 offen offset:1024 sc1 lds\n\t" \
  "v_mfma_f32_16x16x32_bf16 v[68:71], a[112:115], v[136:139], v[68:71]\n\t" \
  "s_add_u32 s21, s21, 1966080\n\t" \
  "ds_read_b128 v[164:167], v242 offset:62464\n\t" \
  "s_lshl_b32 s42, s59, 7\n\t" \
  "v_mfma_f32_16x16x32_bf16 v[68:71], a[120:123], v[140:143], v[68:71]\n\t" \
  "s_and_b32 s23, s42, 0xffffc000\n\t" \
  "ds_read_b128 v[168:171], v242 offset:63488\n\t" \
  "s_lshl_b32 s42, s59, 6\n\t" \
  "v_mfma_f32_16x16x32_bf16 v[84:87], a[124:127], v[140:143], v[84:87]\n\t" \
  "s_and_b32 s42, s42, 0x1800\n\t" \
  "ds_read_b128 v[172:175], v242 offset:64512\n\t" \
  "s_add_u32 s23, s23, s42\n\t" \
  "v_mfma_f32_16x16x32_bf16 v[84:87], a[116:119], v[136:139], v[84:87]\n\t" \
  "s_mul_i32 s42, s3, 0x1400\n\t" \
  "s_and_b32 s42, s42, 0x2400\n\t" \
  "s_xor_b32 s42, s42, s55\n\t" \
  "v_mfma_f32_16x16x32_bf16 v[84:87], a[108:111], v[132:135], v[84:87]\n\t" \
  "s_waitcnt vmcnt(10) lgkmcnt(4)\n\t" \
  "buffer_load_dwordx4 v242, s[16:19], s97 offen offset:2048 sc1 lds\n\t" \
  "v_mfma_f32_16x16x32_bf16 v[84:87], a[100:103], v[128:131], v[84:87]\n\t" \
  "s_barrier\n\t" \
  "v_mfma_f32_16x16x32_bf16 v[72:75], a[96:99], v[144:147], v[72:75]\n\t" \
  "s_add_u32 s23, s23, s42\n\t" \
  "ds_read_b128 v[128:131], v242 offset:16384\n\t" \
  "s_add_u32 s23, s23, 1048576\n\t" \
  "v_mfma_f32_16x16x32_bf16 v[72:75], a[104:107], v[148:151], v[72:75]\n\t" \
  "buffer_load_dwordx4 v242, s[16:19], s97 offen offset:3072 sc1 lds\n\t" \
  "v_mfma_f32_16x16x32_bf16 v[72:75], a[112:115], v[152:155], v[72:75]\n\t" \
  "s_add_u32 s97, s64, 655360\n\t" \
  "s_and_b32 s97, s97, 0xe0000\n\t" \
  "s_add_u32 s97, s97, s63\n\t" \
  "v_mfma_f32_16x16x32_bf16 v[72:75], a[120:123], v[156:159], v[72:75]\n\t" \
  "s_add_u32 m0, s38, 81920\n\t" \
  "ds_read_b128 v[132:135], v242 offset:17408\n\t" \
  "v_mfma_f32_16x16x32_bf16 v[88:91], a[124:127], v[156:159], v[88:91]\n\t" \
  "buffer_load_dwordx4 v242, s[16:19], s97 offen offset:0 sc1 lds\n\t" \
  "v_mfma_f32_16x16x32_bf16 v[88:91], a[116:119], v[152:155], v[88:91]\n\t" \
  "ds_read_b128 v[136:139], v242 offset:18432\n\t" \
  "v_mfma_f32_16x16x32_bf16 v[88:91], a[108:111], v[148:151], v[88:91]\n\t" \
  "ds_read_b128 v[140:143], v242 offset:19456\n\t" \
  "v_mfma_f32_16x16x32_bf16 v[88:91], a[100:103], v[144:147], v[88:91]\n\t" \
  "s_waitcnt lgkmcnt(4)\n\t" \
  "buffer_load_dwordx4 v242, s[16:19], s97 offen offset:1024 sc1 lds\n\t" \
  "v_mfma_f32_16x16x32_bf16 v[76:79], a[96:99], v[160:163], v[76:79]\n\t" \
  "ds_read_b128 v[144:147], v242 offset:20480\n\t" \
  "v_mfma_f32_16x16x32_bf16 v[76:79], a[104:107], v[164:167], v[76:79]\n\t" \
  "ds_read_b128 v[148:151], v242 offset:21504\n\t" \
  "v_mfma_f32_16x16x32_bf16 v[76:79], a[112:115], v[168:171], v[76:79]\n\t" \
  "buffer_load_dwordx4 v242, s[16:19], s97 offen offset:2048 sc1 lds\n\t" \
  "v_mfma_f32_16x16x32_bf16 v[76:79], a[120:123], v[172:175], v[76:79]\n\t" \
  "ds_read_b128 v[152:155], v242 offset:22528\n\t" \
  "v_mfma_f32_16x16x32_bf16 v[92:95], a[124:127], v[172:175], v[92:95]\n\t" \
  "ds_read_b128 v[156:159], v242 offset:23552\n\t" \
  "v_mfma_f32_16x16x32_bf16 v[92:95], a[116:119], v[168:171], v[92:95]\n\t" \
  "buffer_load_dwordx4 v242, s[16:19], s97 offen offset:3072 sc1 lds\n\t" \
  "v_mfma_f32_16x16x32_bf16 v[92:95], a[108:111], v[164:167], v[92:95]\n\t" \
  "s_add_u32 s97, s64, 786432\n\t" \
  "s_and_b32 s97, s97, 0xe0000\n\t" \
  "s_add_u32 s97, s97, s63\n\t" \
  "v_mfma_f32_16x16x32_bf16 v[92:95], a[100:103], v[160:163], v[92:95]\n\t" \
  "s_waitcnt lgkmcnt(4)\n\t" \
  "s_add_u32 m0, s38, 131072\n\t" \
  "v_mfma_f32_16x16x32_bf16 v[64:67], a[192:195], v[128:131], v[64:67]\n\t" \
  "buffer_load_dwordx4 v242, s[16:19], s97 offen offset:0 sc1 lds\n\t" \
  "v_mfma_f32_16x16x32_bf16 v[64:67], a[200:203], v[132:135], v[64:67]\n\t" \
  "ds_read_b128 v[160:163], v242 offset:24576\n\t" \
  "v_mfma_f32_16x16x32_bf16 v[64:67], a[208:211], v[136:139], v[64:67]\n\t" \
  "ds_read_b128 v[164:167], v242 offset:25600\n\t" \
  "v_mfma_f32_16x16x32_bf16 v[64:67], a[216:219], v[140:143], v[64:67]\n\t" \
  "buffer_load_dwordx4 v242, s[16:19], s97 offen offset:1024 sc1 lds\n\t" \
  "v_mfma_f32_16x16x32_bf16 v[80:83], a[220:223], v[140:143], v[80:83]\n\t" \
  "ds_read_b128 v[168:171], v242 offset:26624\n\t" \
  "v_mfma_f32_16x16x32_bf16 v[80:83], a[212:215], v[136:139], v[80:83]\n\t" \
  "ds_read_b128 v[172:175], v242 offset:27648\n\t" \
  "v_mfma_f32_16x16x32_bf16 v[80:83], a[204:207], v[132:135], v[80:83]\n\t" \
  "buffer_load_dwordx4 v242, s[16:19], s97 offen offset:2048 sc1 lds\n\t" \
  "v_mfma_f32_16x16x32_bf16 v[80:83], a[196:199], v[128:131], v[80:83]\n\t" \
  "s_waitcnt lgkmcnt(4)\n\t" \
  "v_cvt_pk_bf16_f32 v0, v64, v65\n\t" \
  "v_cvt_pk_bf16_f32 v1, v66, v67\n\t" \
  "v_mfma_f32_16x16x32_bf16 v[68:71], a[192:195], v[144:147], v[68:71]\n\t" \
  "buffer_load_dword v246, v235, s[16:19], s73 offen sc1\n\t" \
  "v_mfma_f32_16x16x32_bf16 v[68:71], a[200:203], v[148:151], v[68:71]\n\t" \
  "buffer_load_dwordx4 v242, s[16:19], s97 offen offset:3072 sc1 lds\n\t" \
  "v_mfma_f32_16x16x32_bf16 v[68:71], a[208:211], v[152:155], v[68:71]\n\t" \
  "ds_read_b128 v[128:131], v242 offset:28672\n\t" \
  "v_cvt_pk_bf16_f32 v2, v80, v81\n\t" \
  "v_mfma_f32_16x16x32_bf16 v[68:71], a[216:219], v[156:159], v[68:71]\n\t" \
  "ds_read_b128 v[132:135], v242 offset:29696\n\t" \
  "v_cvt_pk_bf16_f32 v3, v82, v83\n\t" \
  "v_mfma_f32_16x16x32_bf16 v[84:87], a[220:223], v[156:159], v[84:87]\n\t" \
  "ds_read_b128 v[136:139], v242 offset:30720\n\t" \
  "v_mov_b32 v250, 0x3f80\n\t" \
  "v_mfma_f32_16x16x32_bf16 v[84:87], a[212:215], v[152:155], v[84:87]\n\t" \
  "ds_read_b128 v[140:143], v242 offset:31744\n\t" \
  "v_lshlrev_b32 v250, v235, v250\n\t" \
  "v_mfma_f32_16x16x32_bf16 v[84:87], a[204:207], v[148:151], v[84:87]\n\t" \
  "s_mov_b32 s84, 0x300003\n\t" \
  "s_mov_b32 s85, 0x30000300\n\t" \
  "s_mov_b32 s86, 0xc0000c\n\t" \
  "v_mfma_f32_16x16x32_bf16 v[84:87], a[196:199], v[144:147], v[84:87]\n\t" \
  "s_waitcnt lgkmcnt(4)\n\t" \
  "v_cvt_pk_bf16_f32 v4, v68, v69\n\t" \
  "v_cvt_pk_bf16_f32 v5, v70, v71\n\t" \
  "v_mfma_f32_16x16x32_bf16 v[72:75], a[192:195], v[160:163], v[72:75]\n\t" \
  "buffer_store_dwordx4 v[0:3], v242, s[16:19], s69 offen offset:0 sc1\n\t" \
  "ds_read_b128 v[144:147], v242 offset:32768\n\t" \
  "ds_read_b128 v[148:151], v242 offset:33792\n\t" \
  "ds_read_b128 v[152:155], v242 offset:34816\n\t" \
  "v_mfma_f32_16x16x32_bf16 v[72:75], a[200:203], v[164:167], v[72:75]\n\t" \
  "s_mov_b32 s87, 0xc0000c00\n\t" \
  "v_cvt_pk_bf16_f32 v6, v84, v85\n\t" \
  "v_cvt_pk_bf16_f32 v7, v86, v87\n\t" \
  "v_mfma_f32_16x16x32_bf16 v[72:75], a[208:211], v[168:171], v[72:75]\n\t" \
  "v_cndmask_b32 v248, 0, v250, s[84:85]\n\t" \
  "v_cndmask_b32 v249, 0, v250, s[86:87]\n\t" \
  "v_mfma_f32_16x16x32_bf16 v[72:75], a[216:219], v[172:175], v[72:75]\n\t" \
  "v_and_b32 v236, 15, v233\n\t" \
  "v_lshrrev_b32 v250, 4, v233\n\t" \
  "v_mfma_f32_16x16x32_bf16 v[88:91], a[220:223], v[172:175], v[88:91]\n\t" \
  "v_mul_u32_u24 v236, 80, v236\n\t" \
  "v_lshlrev_b32 v237, 4, v250\n\t" \
  "v_mfma_f32_16x16x32_bf16 v[88:91], a[212:215], v[168:171], v[88:91]\n\t" \
  "v_add_u32 v237, s76, v237\n\t" \
  "v_add_u32 v236, v237, v236\n\t" \
  "v_mfma_f32_16x16x32_bf16 v[88:91], a[204:207], v[164:167], v[88:91]\n\t" \
  "s_add_u32 s78, s70, 4096\n\t" \
  "ds_read_b128 v[156:159], v242 offset:35840\n\t" \
  "s_add_u32 s79, s70, 16384\n\t" \
  "v_mfma_f32_16x16x32_bf16 v[88:91], a[196:199], v[160:163], v[88:91]\n\t" \
  "s_waitcnt lgkmcnt(4)\n\t" \
  "v_cvt_pk_bf16_f32 v8, v72, v73\n\t" \
  "v_cvt_pk_bf16_f32 v9, v74, v75\n\t" \
  "v_mfma_f32_16x16x32_bf16 v[76:79], a[192:195], v[128:131], v[76:79]\n\t" \
  "buffer_store_dwordx4 v[4:7], v242, s[16:19], s69 offen offset:1024 sc1\n\t" \
  "ds_read_b128 v[160:163], v242 offset:36864\n\t" \
  "ds_read_b128 v[164:167], v242 offset:37888\n\t" \
  "ds_read_b128 v[168:171], v242 offset:38912\n\t" \
  "v_mfma_f32_16x16x32_bf16 v[76:79], a[200:203], v[132:135], v[76:79]\n\t" \
  "v_cvt_pk_bf16_f32 v10, v88, v89\n\t" \
  "v_cvt_pk_bf16_f32 v11, v90, v91\n\t" \
  "v_mfma_f32_16x16x32_bf16 v[76:79], a[208:211], v[136:139], v[76:79]\n\t" \
  "ds_read_b128 v[172:175], v242 offset:39936\n\t" \
  "v_mfma_f32_16x16x32_bf16 v[76:79], a[216:219], v[140:143], v[76:79]\n\t" \
  "v_mfma_f32_16x16x32_bf16 v[92:95], a[220:223], v[140:143], v[92:95]\n\t" \
  "v_mfma_f32_16x16x32_bf16 v[92:95], a[212:215], v[136:139], v[92:95]\n\t" \
  "v_mfma_f32_16x16x32_bf16 v[92:95], a[204:207], v[132:135], v[92:95]\n\t" \
  "v_mfma_f32_16x16x32_bf16 v[92:95], a[196:199], v[128:131], v[92:95]\n\t" \
  "s_waitcnt lgkmcnt(4)\n\t" \
  STP("s_memrealtime s[94:95]\n\t") \
  STP("s_memtime s[92:93]\n\t") \
  "v_mfma_f32_16x16x32_bf16 v[96:99], a[128:131], v[144:147], v[96:99]\n\t" \
  "buffer_store_dwordx4 v[8:11], v242, s[16:19], s69 offen offset:2048 sc1\n\t" \
  "ds_read_b128 v[128:131], v242 offset:40960\n\t" \
  "v_cvt_pk_bf16_f32 v12, v76, v77\n\t" \
  "v_cvt_pk_bf16_f32 v13, v78, v79\n\t" \
  "v_mfma_f32_16x16x32_bf16 v[96:99], a[136:139], v[148:151], v[96:99]\n\t" \
  "ds_read_b64 v[208:209], v238 offset:0\n\t" \
  "v_cvt_pk_bf16_f32 v14, v92, v93\n\t" \
  "v_mfma_f32_16x16x32_bf16 v[96:99], a[144:147], v[152:155], v[96:99]\n\t" \
  "ds_read_b128 v[132:135], v242 offset:41984\n\t" \
  "v_cvt_pk_bf16_f32 v15, v94, v95\n\t" \
  "v_mfma_f32_16x16x32_bf16 v[96:99], a[152:155], v[156:159], v[96:99]\n\t" \
  "ds_read_b64 v[210:211], v238 offset:4096\n\t" \
  "v_mfma_f32_16x16x32_bf16 v[112:115], a[156:159], v[156:159], v[112:115]\n\t" \
  "ds_read_b128 v[136:139], v242 offset:43008\n\t" \
  "v_mfma_f32_16x16x32_bf16 v[112:115], a[148:151], v[152:155], v[112:115]\n\t" \
  "ds_read_b64 v[212:213], v238 offset:8192\n\t" \
  "v_mfma_f32_16x16x32_bf16 v[112:115], a[140:143], v[148:151], v[112:115]\n\t" \
  "ds_read_b128 v[140:143], v242 offset:44032\n\t" \
  "v_mfma_f32_16x16x32_bf16 v[112:115], a[132:135], v[144:147], v[112:115]\n\t" \
  "s_waitcnt lgkmcnt(7)\n\t" \
  "ds_read_b64 v[214:215], v238 offset:12288\n\t" \
  "v_mfma_f32_16x16x32_bf16 v[100:103], a[128:131], v[160:163], v[100:103]\n\t" \
  "buffer_store_dwordx4 v[12:15], v242, s[16:19], s69 offen offset:3072 sc1\n\t" \
  "ds_read_b128 v[144:147], v242 offset:45056\n\t" \
  "ds_read_b128 v[148:151], v242 offset:46080\n\t" \
  "ds_read_b64 v[216:217], v239 offset:0\n\t" \
  "v_mfma_f32_16x16x32_bf16 v[100:103], a[136:139], v[164:167], v[100:103]\n\t" \
  "ds_read_b128 v[152:155], v242 offset:47104\n\t" \
  "v_mfma_f32_16x16x32_bf16 v[100:103], a[144:147], v[168:171], v[100:103]\n\t" \
  "ds_read_b64 v[218:219], v239 offset:4096\n\t" \
  "v_mfma_f32_16x16x32_bf16 v[100:103], a[152:155], v[172:175], v[100:103]\n\t" \
  "ds_read_b128 v[156:159], v242 offset:48128\n\t" \
  "v_mfma_f32_16x16x32_bf16 v[116:119], a[156:159], v[172:175], v[116:119]\n\t" \
  STP("v_writelane_b32 v255, s94, 36\n\t") \
  STP("v_writelane_b32 v255, s92, 37\n\t") \
  "ds_read_b64 v[220:221], v239 offset:8192\n\t" \
  "v_mfma_f32_16x16x32_bf16 v[116:119], a[148:151], v[168:171], v[116:119]\n\t" \
  "ds_read_b64 v[222:223], v239 offset:12288\n\t" \
  "v_mfma_f32_16x16x32_bf16 v[116:119], a[140:143], v[164:167], v[116:119]\n\t" \
  "s_waitcnt vmcnt(17) lgkmcnt(9)\n\t" \
  "v_lshl_add_u32 v224, s3, 10, v242\n\t" \
  "v_and_b32 v225, 31, v233\n\t" \
  "v_mfma_f32_16x16x32_bf16 v[116:119], a[132:135], v[160:163], v[116:119]\n\t" \
  "s_barrier\n\t" \
  "v_mfma_f32_16x16x32_bf16 v[104:107], a[128:131], v[128:131], v[104:107]\n\t" \
  "ds_read_b128 v[160:163], v230 offset:0\n\t" \
  "v_lshlrev_b32 v245, 2, v225\n\t" \
  "v_mfma_f32_16x16x32_bf16 v[104:107], a[136:139], v[132:135], v[104:107]\n\t" \
  "s_waitcnt lgkmcnt(1)\n\t" \
  "v_lshlrev_b32 v0, 16, v208\n\t" \
  "v_lshlrev_b32 v225, 3, v225\n\t" \
  "v_mfma_f32_16x16x32_bf16 v[104:107], a[144:147], v[136:139], v[104:107]\n\t" \
  "ds_read_b128 v[164:167], v230 offset:1024\n\t" \
  "v_and_b32 v226, 0xffffffe0, v224\n\t" \
  "v_mfma_f32_16x16x32_bf16 v[104:107], a[152:155], v[140:143], v[104:107]\n\t" \
  "v_lshl_add_u32 v227, s8, 6, v233\n\t" \
  "v_sub_u32 v227, 0x7ff, v227\n\t" \
  "v_mfma_f32_16x16x32_bf16 v[120:123], a[156:159], v[140:143], v[120:123]\n\t" \
  "s_add_u32 s97, s64, 917504\n\t" \
  "ds_read_b128 v[168:171], v230 offset:2048\n\t" \
  "v_and_b32 v228, 0x30, v233\n\t" \
  "v_mfma_f32_16x16x32_bf16 v[120:123], a[148:151], v[136:139], v[120:123]\n\t" \
  "s_and_b32 s97, s97, 0xe0000\n\t" \
  "v_and_b32 v239, 15, v233\n\t" \
  "v_and_b32 v241, 16, v233\n\t" \
  "v_mfma_f32_16x16x32_bf16 v[120:123], a[140:143], v[132:135], v[120:123]\n\t" \
  "s_add_u32 s97, s97, s63\n\t" \
  "ds_read_b128 v[172:175], v230 offset:3072\n\t" \
  "v_lshlrev_b32 v231, 11, v239\n\t" \
  "v_mfma_f32_16x16x32_bf16 v[120:123], a[132:135], v[128:131], v[120:123]\n\t" \
  "s_add_u32 m0, s38, 16384\n\t" \
  "v_lshl_add_u32 v231, v241, 1, v231\n\t" \
  "v_lshrrev_b32 v239, 5, v233\n\t" \
  "v_mfma_f32_16x16x32_bf16 v[108:111], a[128:131], v[144:147], v[108:111]\n\t" \
  "ds_read_b128 v[128:131], v230 offset:4096\n\t" \
  "v_lshl_add_u32 v231, v239, 4, v231\n\t" \
  "v_mfma_f32_16x16x32_bf16 v[108:111], a[136:139], v[148:151], v[108:111]\n\t" \
  "v_lshl_add_u32 v253, v239, 6, v241\n\t" \
  "v_and_b32 v239, 1, v233\n\t" \
  "v_mfma_f32_16x16x32_bf16 v[108:111], a[144:147], v[152:155], v[108:111]\n\t" \
  "buffer_load_dwordx4 v242, s[16:19], s97 offen offset:0 sc1 lds\n\t" \
  "v_mfma_f32_16x16x32_bf16 v[108:111], a[152:155], v[156:159], v[108:111]\n\t" \
  "ds_read_b128 v[132:135], v230 offset:5120\n\t" \
  "v_lshl_add_u32 v253, v239, 5, v253\n\t" \
  "v_mfma_f32_16x16x32_bf16 v[124:127], a[156:159], v[156:159], v[124:127]\n\t" \
  "ds_read_b128 v[136:139], v230 offset:6144\n\t" \
  "v_lshl_add_u32 v253, s3, 7, v253\n\t" \
  "v_mfma_f32_16x16x32_bf16 v[124:127], a[148:151], v[152:155], v[124:127]\n\t" \
  "v_and_b32 v239, 0x800, v224\n\t" \
  "v_lshl_or_b32 v240, v239, 20, v242\n\t" \
  "v_mfma_f32_16x16x32_bf16 v[124:127], a[140:143], v[148:151], v[124:127]\n\t" \
  "buffer_load_dwordx4 v242, s[16:19], s97 offen offset:1024 sc1 lds\n\t" \
  "v_mfma_f32_16x16x32_bf16 v[124:127], a[132:135], v[144:147], v[124:127]\n\t" \
  "s_waitcnt lgkmcnt(3)\n\t" \
  "ds_read_b128 v[140:143], v230 offset:7168\n\t" \
  "v_mov_b32 v238, s82\n\t" \
  "v_mfma_f32_16x16x32_bf16 v[96:99], a[160:163], v[160:163], v[96:99]\n\t" \
  "ds_read_b128 v[144:147], v230 offset:8192\n\t" \
  "v_writelane_b32 v238, s83, 1\n\t" \
  "v_mfma_f32_16x16x32_bf16 v[96:99], a[168:171], v[164:167], v[96:99]\n\t" \
  "buffer_load_dwordx4 v242, s[16:19], s97 offen offset:2048 sc1 lds\n\t" \
  "v_mfma_f32_16x16x32_bf16 v[96:99], a[176:179], v[168:171], v[96:99]\n\t" \
  "ds_read_b128 v[148:151], v230 offset:9216\n\t" \
  "v_perm_b32 v239, v180, v176, s40\n\t" \
  "v_mfma_f32_16x16x32_bf16 v[96:99], a[184:187], v[172:175], v[96:99]\n\t" \
  "v_perm_b32 v180, v180, v176, s41\n\t" \
  "v_mov_b32 v176, v239\n\t" \
  "v_mfma_f32_16x16x32_bf16 v[112:115], a[188:191], v[172:175], v[112:115]\n\t" \
  "ds_read_b128 v[152:155], v230 offset:10240\n\t" \
  "v_perm_b32 v239, v181, v177, s40\n\t" \
  "v_mfma_f32_16x16x32_bf16 v[112:115], a[180:183], v[168:171], v[112:115]\n\t" \
  "buffer_load_dwordx4 v242, s[16:19], s97 offen offset:3072 sc1 lds\n\t" \
  "v_mfma_f32_16x16x32_bf16 v[112:115], a[172:175], v[164:167], v[112:115]\n\t" \
  "ds_read_b128 v[156:159], v230 offset:11264\n\t" \
  "v_perm_b32 v181, v181, v177, s41\n\t" \
  "v_mfma_f32_16x16x32_bf16 v[112:115], a[164:167], v[160:163], v[112:115]\n\t" \
  "s_waitcnt lgkmcnt(4)\n\t" \
  "v_mov_b32 v177, v239\n\t" \
  "v_perm_b32 v239, v182, v178, s40\n\t" \
  "v_mfma_f32_16x16x32_bf16 v[100:103], a[160:163], v[128:131], v[100:103]\n\t" \
  "ds_read_b128 v[160:163], v230 offset:12288\n\t" \
  "v_perm_b32 v182, v182, v178, s41\n\t" \
  "v_mfma_f32_16x16x32_bf16 v[100:103], a[168:171], v[132:135], v[100:103]\n\t" \
  "v_mov_b32 v178, v239\n\t" \
  "v_perm_b32 v239, v183, v179, s40\n\t" \
  "v_mfma_f32_16x16x32_bf16 v[100:103], a[176:179], v[136:139], v[100:103]\n\t" \
  "ds_read_b128 v[164:167], v230 offset:13312\n\t" \
  "v_perm_b32 v183, v183, v179, s41\n\t" \
  "v_mfma_f32_16x16x32_bf16 v[100:103], a[184:187], v[140:143], v[100:103]\n\t" \
  "v_mov_b32 v179, v239\n\t" \
  "v_perm_b32 v239, v188, v184, s40\n\t" \
  "v_mfma_f32_16x16x32_bf16 v[116:119], a[188:191], v[140:143], v[116:119]\n\t" \
  "ds_read_b128 v[168:171], v230 offset:14336\n\t" \
  "v_perm_b32 v188, v188, v184, s41\n\t" \
  "v_mfma_f32_16x16x32_bf16 v[116:119], a[180:183], v[136:139], v[116:119]\n\t" \
  "v_mov_b32 v184, v239\n\t" \
  "v_perm_b32 v239, v189, v185, s40\n\t" \
  "v_mfma_f32_16x16x32_bf16 v[116:119], a[172:175], v[132:135], v[116:119]\n\t" \
  "ds_read_b128 v[172:175], v230 offset:15360\n\t" \
  "v_perm_b32 v189, v189, v185, s41\n\t" \
  "v_mfma_f32_16x16x32_bf16 v[116:119], a[164:167], v[128:131], v[116:119]\n\t" \
  "s_waitcnt lgkmcnt(4)\n\t" \
  "v_and_b32 v1, 0xffff0000, v208\n\t" \
  "v_mov_b32 v185, v239\n\t" \
  "v_mfma_f32_16x16x32_bf16 v[104:107], a[160:163], v[144:147], v[104:107]\n\t" \
  "s_waitcnt vmcnt(9)\n\t" \
  "v_cmp_ne_u32 vcc, s36, v246\n\t" \
  "v_perm_b32 v239, v190, v186, s40\n\t" \
  "v_mfma_f32_16x16x32_bf16 v[104:107], a[168:171], v[148:151], v[104:107]\n\t" \
  "ds_read_b128 v[128:131], v230 offset:32768\n\t" \
  "v_lshlrev_b32 v2, 16, v209\n\t" \
  "v_mfma_f32_16x16x32_bf16 v[104:107], a[176:179], v[152:155], v[104:107]\n\t" \
  "s_cbranch_vccnz .Lat_s4\n\t" \
  ".Lat_k4:\n\t" \
  "v_mfma_f32_16x16x32_bf16 v[104:107], a[184:187], v[156:159], v[104:107]\n\t" \
  "buffer_load_dwordx4 v[32:35], v242, s[16:19], s70 offen offset:0 sc1\n\t" \
  "v_mfma_f32_16x16x32_bf16 v[120:123], a[188:191], v[156:159], v[120:123]\n\t" \
  "ds_read_b128 v[132:135], v230 offset:33792\n\t" \
  "v_perm_b32 v190, v190, v186, s41\n\t" \
  "v_mfma_f32_16x16x32_bf16 v[120:123], a[180:183], v[152:155], v[120:123]\n\t" \
  "ds_read_b128 v[136:139], v230 offset:34816\n\t" \
  "v_and_b32 v3, 0xffff0000, v209\n\t" \
  "v_mfma_f32_16x16x32_bf16 v[120:123], a[172:175], v[148:151], v[120:123]\n\t" \
  "ds_read_b128 v[140:143], v230 offset:35840\n\t" \
  "v_lshlrev_b32 v4, 16, v210\n\t" \
  "v_mfma_f32_16x16x32_bf16 v[120:123], a[164:167], v[144:147], v[120:123]\n\t" \
  "s_waitcnt lgkmcnt(4)\n\t" \
  "v_mov_b32 v186, v239\n\t" \
  "v_perm_b32 v239, v191, v187, s40\n\t" \
  "v_mfma_f32_16x16x32_bf16 v[108:111], a[160:163], v[160:163], v[108:111]\n\t" \
  "ds_read_b128 v[144:147], v230 offset:36864\n\t" \
  "v_and_b32 v5, 0xffff0000, v210\n\t" \
  "v_mfma_f32_16x16x32_bf16 v[108:111], a[168:171], v[164:167], v[108:111]\n\t" \
  "buffer_load_dwordx4 v[36:39], v242, s[16:19], s70 offen offset:1024 sc1\n\t" \
  "v_mfma_f32_16x16x32_bf16 v[108:111], a[176:179], v[168:171], v[108:111]\n\t" \
  "ds_read_b128 v[148:151], v230 offset:37888\n\t" \
  "v_lshlrev_b32 v6, 16, v211\n\t" \
  "v_mfma_f32_16x16x32_bf16 v[108:111], a[184:187], v[172:175], v[108:111]\n\t" \
  "v_perm_b32 v191, v191, v187, s41\n\t" \
  "v_mov_b32 v187, v239\n\t" \
  "v_mfma_f32_16x16x32_bf16 v[124:127], a[188:191], v[172:175], v[124:127]\n\t" \
  "ds_read_b128 v[152:155], v230 offset:38912\n\t" \
  "v_and_b32 v7, 0xffff0000, v211\n\t" \
  "v_mfma_f32_16x16x32_bf16 v[124:127], a[180:183], v[168:171], v[124:127]\n\t" \
  "v_lshlrev_b32 v8, 16, v212\n\t" \
  "v_perm_b32 v239, v196, v192, s40\n\t" \
  "v_mfma_f32_16x16x32_bf16 v[124:127], a[172:175], v[164:167], v[124:127]\n\t" \
  "ds_read_b128 v[156:159], v230 offset:39936\n\t" \
  "v_perm_b32 v196, v196, v192, s41\n\t" \
  "v_mfma_f32_16x16x32_bf16 v[124:127], a[164:167], v[160:163], v[124:127]\n\t" \
  "s_waitcnt lgkmcnt(4)\n\t" \
  "buffer_load_dwordx4 v[40:43], v242, s[16:19], s70 offen offset:2048 sc1\n\t" \
  "v_mfma_f32_16x16x32_bf16 v[96:99], a[0:3], v[128:131], v[96:99]\n\t" \
  "ds_read_b128 v[160:163], v230 offset:40960\n\t" \
  "v_and_b32 v9, 0xffff0000, v212\n\t" \
  "v_mfma_f32_16x16x32_bf16 v[96:99], a[8:11], v[132:135], v[96:99]\n\t" \
  "v_lshlrev_b32 v10, 16, v213\n\t" \
  "v_mov_b32 v192, v239\n\t" \
  "v_mfma_f32_16x16x32_bf16 v[96:99], a[16:19], v[136:139], v[96:99]\n\t" \
  "ds_read_b128 v[164:167], v230 offset:41984\n\t" \
  "v_perm_b32 v239, v197, v193, s40\n\t" \
  "v_mfma_f32_16x16x32_bf16 v[96:99], a[24:27], v[140:143], v[96:99]\n\t" \
  "v_and_b32 v11, 0xffff0000, v213\n\t" \
  "v_perm_b32 v197, v197, v193, s41\n\t" \
  "v_mfma_f32_16x16x32_bf16 v[112:115], a[28:31], v[140:143], v[112:115]\n\t" \
  "ds_read_b128 v[168:171], v230 offset:43008\n\t" \
  "v_lshlrev_b32 v12, 16, v214\n\t" \
  "v_mfma_f32_16x16x32_bf16 v[112:115], a[20:23], v[136:139], v[112:115]\n\t" \
  "buffer_load_dwordx4 v[44:47], v242, s[16:19], s70 offen offset:3072 sc1\n\t" \
  "v_mfma_f32_16x16x32_bf16 v[112:115], a[12:15], v[132:135], v[112:115]\n\t" \
  "ds_read_b128 v[172:175], v230 offset:44032\n\t" \
  "v_mov_b32 v193, v239\n\t" \
  "v_mfma_f32_16x16x32_bf16 v[112:115], a[4:7], v[128:131], v[112:115]\n\t" \
  "s_waitcnt lgkmcnt(4)\n\t" \
  "v_and_b32 v13, 0xffff0000, v214\n\t" \
  "v_perm_b32 v239, v198, v194, s40\n\t" \
  "v_mfma_f32_16x16x32_bf16 v[100:103], a[0:3], v[144:147], v[100:103]\n\t" \
  "ds_read_b128 v[128:131], v230 offset:45056\n\t" \
  "v_lshlrev_b32 v14, 16, v215\n\t" \
  "v_mfma_f32_16x16x32_bf16 v[100:103], a[8:11], v[148:151], v[100:103]\n\t" \
  "v_and_b32 v15, 0xffff0000, v215\n\t" \
  "v_perm_b32 v198, v198, v194, s41\n\t" \
  "v_mfma_f32_16x16x32_bf16 v[100:103], a[16:19], v[152:155], v[100:103]\n\t" \
  "ds_read_b128 v[132:135], v230 offset:46080\n\t" \
  "v_mov_b32 v194, v239\n\t" \
  "v_mfma_f32_16x16x32_bf16 v[100:103], a[24:27], v[156:159], v[100:103]\n\t" \
  "buffer_load_dwordx4 v[48:51], v242, s[16:19], s78 offen offset:0 sc1\n\t" \
  "v_mfma_f32_16x16x32_bf16 v[116:119], a[28:31], v[156:159], v[116:119]\n\t" \
  "ds_read_b128 v[136:139], v230 offset:47104\n\t" \
  "v_lshlrev_b32 v16, 16, v216\n\t" \
  "v_mfma_f32_16x16x32_bf16 v[116:119], a[20:23], v[152:155], v[116:119]\n\t" \
  "v_and_b32 v17, 0xffff0000, v216\n\t" \
  "v_perm_b32 v239, v199, v195, s40\n\t" \
  "v_mfma_f32_16x16x32_bf16 v[116:119], a[12:15], v[148:151], v[116:119]\n\t" \
  "s_waitcnt lgkmcnt(3)\n\t" \
  "ds_read_b128 v[140:143], v230 offset:48128\n\t" \
  "v_lshlrev_b32 v18, 16, v217\n\t" \
  "v_mfma_f32_16x16x32_bf16 v[116:119], a[4:7], v[144:147], v[116:119]\n\t" \
  "s_barrier\n\t" \
  "v_mfma_f32_16x16x32_bf16 v[104:107], a[0:3], v[160:163], v[104:107]\n\t" \
  "s_waitcnt vmcnt(9)\n\t" \
  "buffer_store_dword v243, v244, s[16:19], s66 offen offset:32 sc1\n\t" \
  "ds_read_b128 v[144:147], v230 offset:49152\n\t" \
  "v_mfma_f32_16x16x32_bf16 v[104:107], a[8:11], v[164:167], v[104:107]\n\t" \
  "v_perm_b32 v199, v199, v195, s41\n\t" \
  "v_mov_b32 v195, v239\n\t" \
  "v_mfma_f32_16x16x32_bf16 v[104:107], a[16:19], v[168:171], v[104:107]\n\t" \
  "buffer_load_dwordx4 v[52:55], v242, s[16:19], s78 offen offset:1024 sc1\n\t" \
  "v_mfma_f32_16x16x32_bf16 v[104:107], a[24:27], v[172:175], v[104:107]\n\t" \
  "ds_read_b128 v[148:151], v230 offset:50176\n\t" \
  "v_and_b32 v19, 0xffff0000, v217\n\t" \
  "v_mfma_f32_16x16x32_bf16 v[120:123], a[28:31], v[172:175], v[120:123]\n\t" \
  "ds_read_b128 v[152:155], v230 offset:51200\n\t" \
  "v_lshlrev_b32 v20, 16, v218\n\t" \
  "v_mfma_f32_16x16x32_bf16 v[120:123], a[20:23], v[168:171], v[120:123]\n\t" \
  "v_and_b32 v21, 0xffff0000, v218\n\t" \
  "v_perm_b32 v239, v204, v200, s40\n\t" \
  "v_mfma_f32_16x16x32_bf16 v[120:123], a[12:15], v[164:167], v[120:123]\n\t" \
  "ds_read_b128 v[156:159], v230 offset:52224\n\t" \
  "v_perm_b32 v204, v204, v200, s41\n\t" \
  "v_mfma_f32_16x16x32_bf16 v[120:123], a[4:7], v[160:163], v[120:123]\n\t" \
  "s_waitcnt lgkmcnt(4)\n\t" \
  "buffer_load_dwordx4 v[56:59], v242, s[16:19], s78 offen offset:2048 sc1\n\t" \
  "v_mfma_f32_16x16x32_bf16 v[108:111], a[0:3], v[128:131], v[108:111]\n\t" \
  "ds_read_b128 v[160:163], v230 offset:53248\n\t" \
  "v_lshlrev_b32 v22, 16, v219\n\t" \
  "v_mfma_f32_16x16x32_bf16 v[108:111], a[8:11], v[132:135], v[108:111]\n\t" \
  "v_and_b32 v23, 0xffff0000, v219\n\t" \
  "v_mov_b32 v200, v239\n\t" \
  "v_mfma_f32_16x16x32_bf16 v[108:111], a[16:19], v[136:139], v[108:111]\n\t" \
  "ds_read_b128 v[164:167], v230 offset:54272\n\t" \
  "v_perm_b32 v239, v205, v201, s40\n\t" \
  "v_mfma_f32_16x16x32_bf16 v[108:111], a[24:27], v[140:143], v[108:111]\n\t" \
  "v_lshlrev_b32 v24, 16, v220\n\t" \
  "v_perm_b32 v205, v205, v201, s41\n\t" \
  "v_mfma_f32_16x16x32_bf16 v[124:127], a[28:31], v[140:143], v[124:127]\n\t" \
  "ds_read_b128 v[168:171], v230 offset:55296\n\t" \
  "v_and_b32 v25, 0xffff0000, v220\n\t" \
  "v_mfma_f32_16x16x32_bf16 v[124:127], a[20:23], v[136:139], v[124:127]\n\t" \
  "buffer_load_dwordx4 v[60:63], v242, s[16:19], s78 offen offset:3072 sc1\n\t" \
  "v_mfma_f32_16x16x32_bf16 v[124:127], a[12:15], v[132:135], v[124:127]\n\t" \
  "ds_read_b128 v[172:175], v230 offset:56320\n\t" \
  "v_accvgpr_write_b32 a128, 0\n\t" \
  "v_mfma_f32_16x16x32_bf16 v[124:127], a[4:7], v[128:131], v[124:127]\n\t" \
  "s_waitcnt lgkmcnt(4)\n\t" \
  "v_accvgpr_write_b32 a129, 0\n\t" \
  "v_accvgpr_write_b32 a130, v248\n\t" \
  "v_mfma_f32_16x16x32_bf16 v[96:99], a[32:35], v[144:147], v[96:99]\n\t" \
  "ds_read_b128 v[128:131], v230 offset:57344\n\t" \
  "v_accvgpr_write_b32 a131, v249\n\t" \
  "v_mfma_f32_16x16x32_bf16 v[96:99], a[40:43], v[148:151], v[96:99]\n\t" \
  "v_accvgpr_write_b32 a132, 0\n\t" \
  "v_accvgpr_write_b32 a133, 0\n\t" \
  "v_mfma_f32_16x16x32_bf16 v[96:99], a[48:51], v[152:155], v[96:99]\n\t" \
  "ds_read_b128 v[132:135], v230 offset:58368\n\t" \
  "v_mov_b32 v201, v239\n\t" \
  "v_mfma_f32_16x16x32_bf16 v[96:99], a[56:59], v[156:159], v[96:99]\n\t" \
  "ds_read_b128 a[176:179], v236 offset:960\n\t" \
  "v_lshlrev_b32 v26, 16, v221\n\t" \
  "v_mfma_f32_16x16x32_bf16 v[112:115], a[60:63], v[156:159], v[112:115]\n\t" \
  "ds_read_b128 v[136:139], v230 offset:59392\n\t" \
  "v_perm_b32 v239, v206, v202, s40\n\t" \
  "v_mfma_f32_16x16x32_bf16 v[112:115], a[52:55], v[152:155], v[112:115]\n\t" \
  "ds_read_b128 a[180:183], v236 offset:640\n\t" \
  "v_and_b32 v27, 0xffff0000, v221\n\t" \
  "v_mfma_f32_16x16x32_bf16 v[112:115], a[44:47], v[148:151], v[112:115]\n\t" \
  "ds_read_b128 v[140:143], v230 offset:60416\n\t" \
  "v_perm_b32 v206, v206, v202, s41\n\t" \
  "v_mfma_f32_16x16x32_bf16 v[112:115], a[36:39], v[144:147], v[112:115]\n\t" \
  "s_waitcnt lgkmcnt(6)\n\t" \
  "ds_read_b128 a[184:187], v236 offset:320\n\t" \
  "v_lshlrev_b32 v28, 16, v222\n\t" \
  "v_mfma_f32_16x16x32_bf16 v[100:103], a[32:35], v[160:163], v[100:103]\n\t" \
  "ds_read_b128 v[144:147], v230 offset:61440\n\t" \
  "v_mov_b32 v202, v239\n\t" \
  "v_mfma_f32_16x16x32_bf16 v[100:103], a[40:43], v[164:167], v[100:103]\n\t" \
  "ds_read_b128 a[188:191], v236 offset:0\n\t" \
  "v_and_b32 v29, 0xffff0000, v222\n\t" \
  "v_mfma_f32_16x16x32_bf16 v[100:103], a[48:51], v[168:171], v[100:103]\n\t" \
  "ds_read_b128 v[148:151], v230 offset:62464\n\t" \
  "v_perm_b32 v239, v207, v203, s40\n\t" \
  "v_mfma_f32_16x16x32_bf16 v[100:103], a[56:59], v[172:175], v[100:103]\n\t" \
  "v_lshlrev_b32 v30, 16, v223\n\t" \
  "v_perm_b32 v207, v207, v203, s41\n\t" \
  "v_mfma_f32_16x16x32_bf16 v[116:119], a[60:63], v[172:175], v[116:119]\n\t" \
  "ds_read_b128 v[152:155], v230 offset:63488\n\t" \
  "v_and_b32 v31, 0xffff0000, v223\n\t" \
  "v_mfma_f32_16x16x32_bf16 v[116:119], a[52:55], v[168:171], v[116:119]\n\t" \
  "v_mov_b32 v203, v239\n\t" \
  "v_accvgpr_read_b32 v239, a224\n\t" \
  "v_mfma_f32_16x16x32_bf16 v[116:119], a[44:47], v[164:167], v[116:119]\n\t" \
  "ds_read_b128 v[156:159], v230 offset:64512\n\t" \
  "v_accvgpr_read_b32 v84, a228\n\t" \
  "v_mfma_f32_16x16x32_bf16 v[116:119], a[36:39], v[160:163], v[116:119]\n\t" \
  "s_waitcnt lgkmcnt(6)\n\t" \
  "v_perm_b32 v80, v84, v239, s40\n\t" \
  "v_perm_b32 v84, v84, v239, s41\n\t" \
  "v_mfma_f32_16x16x32_bf16 v[104:107], a[32:35], v[128:131], v[104:107]\n\t" \
  "ds_read_b128 v[160:163], v230 offset:16384\n\t" \
  "v_accvgpr_read_b32 v239, a225\n\t" \
  "v_mfma_f32_16x16x32_bf16 v[104:107], a[40:43], v[132:135], v[104:107]\n\t" \
  "v_accvgpr_read_b32 v85, a229\n\t" \
  "v_perm_b32 v81, v85, v239, s40\n\t" \
  "v_mfma_f32_16x16x32_bf16 v[104:107], a[48:51], v[136:139], v[104:107]\n\t" \
  "ds_read_b128 v[164:167], v230 offset:17408\n\t" \
  "v_perm_b32 v85, v85, v239, s41\n\t" \
  "v_mfma_f32_16x16x32_bf16 v[104:107], a[56:59], v[140:143], v[104:107]\n\t" \
  "v_accvgpr_read_b32 v239, a226\n\t" \
  "v_accvgpr_read_b32 v86, a230\n\t" \
  "v_mfma_f32_16x16x32_bf16 v[120:123], a[60:63], v[140:143], v[120:123]\n\t" \
  "ds_read_b128 v[168:171], v230 offset:18432\n\t" \
  "v_perm_b32 v82, v86, v239, s40\n\t" \
  "v_mfma_f32_16x16x32_bf16 v[120:123], a[52:55], v[136:139], v[120:123]\n\t" \
  "v_perm_b32 v86, v86, v239, s41\n\t" \
  "v_accvgpr_read_b32 v239, a227\n\t" \
  "v_mfma_f32_16x16x32_bf16 v[120:123], a[44:47], v[132:135], v[120:123]\n\t" \
  "ds_read_b128 v[172:175], v230 offset:19456\n\t" \
  "v_accvgpr_read_b32 v87, a231\n\t" \
  "v_mfma_f32_16x16x32_bf16 v[120:123], a[36:39], v[128:131], v[120:123]\n\t" \
  "s_waitcnt lgkmcnt(4)\n\t" \
  "v_perm_b32 v83, v87, v239, s40\n\t" \
  "v_perm_b32 v87, v87, v239, s41\n\t" \
  "v_mfma_f32_16x16x32_bf16 v[108:111], a[32:35], v[144:147], v[108:111]\n\t" \
  "ds_read_b128 v[128:131], v230 offset:20480\n\t" \
  "v_accvgpr_read_b32 v239, a232\n\t" \
  "v_mfma_f32_16x16x32_bf16 v[108:111], a[40:43], v[148:151], v[108:111]\n\t" \
  "v_accvgpr_read_b32 v92, a236\n\t" \
  "v_perm_b32 v88, v92, v239, s40\n\t" \
  "v_mfma_f32_16x16x32_bf16 v[108:111], a[48:51], v[152:155], v[108:111]\n\t" \
  "ds_read_b128 v[132:135], v230 offset:21504\n\t" \
  "v_perm_b32 v92, v92, v239, s41\n\t" \
  "v_mfma_f32_16x16x32_bf16 v[108:111], a[56:59], v[156:159], v[108:111]\n\t" \
  "v_accvgpr_read_b32 v239, a233\n\t" \
  "v_accvgpr_read_b32 v93, a237\n\t" \
  "v_mfma_f32_16x16x32_bf16 v[124:127], a[60:63], v[156:159], v[124:127]\n\t" \
  "buffer_load_dword v246, off, s[16:19], s73 offset:32 sc1\n\t" \
  "v_mfma_f32_16x16x32_bf16 v[124:127], a[52:55], v[152:155], v[124:127]\n\t" \
  "ds_read_b128 v[136:139], v230 offset:22528\n\t" \
  "v_perm_b32 v89, v93, v239, s40\n\t" \
  "v_mfma_f32_16x16x32_bf16 v[124:127], a[44:47], v[148:151], v[124:127]\n\t" \
  "ds_read_b128 v[140:143], v230 offset:23552\n\t" \
  "v_perm_b32 v93, v93, v239, s41\n\t" \
  "v_mfma_f32_16x16x32_bf16 v[124:127], a[36:39], v[144:147], v[124:127]\n\t" \
  "s_waitcnt lgkmcnt(4)\n\t" \
  "v_accvgpr_read_b32 v239, a234\n\t" \
  "v_accvgpr_read_b32 v94, a238\n\t" \
  "v_mfma_f32_16x16x32_bf16 v[96:99], a[64:67], v[160:163], v[96:99]\n\t" \
  "ds_read_b128 v[144:147], v230 offset:24576\n\t" \
  "v_perm_b32 v90, v94, v239, s40\n\t" \
  "v_mfma_f32_16x16x32_bf16 v[96:99], a[72:75], v[164:167], v[96:99]\n\t" \
  "v_perm_b32 v94, v94, v239, s41\n\t" \
  "v_accvgpr_read_b32 v239, a235\n\t" \
  "v_mfma_f32_16x16x32_bf16 v[96:99], a[80:83], v[168:171], v[96:99]\n\t" \
  "ds_read_b128 v[148:151], v230 offset:25600\n\t" \
  "v_accvgpr_read_b32 v95, a239\n\t" \
  "v_mfma_f32_16x16x32_bf16 v[96:99], a[88:91], v[172:175], v[96:99]\n\t" \
  "v_perm_b32 v91, v95, v239, s40\n\t" \
  "v_perm_b32 v95, v95, v239, s41\n\t" \
  "v_mfma_f32_16x16x32_bf16 v[112:115], a[92:95], v[172:175], v[112:115]\n\t" \
  ".Lhs15:\n\ts_cbranch_execz .Lhs15n\n\t" \
  "ds_read_b128 v[152:155], v230 offset:26624\n\t" \
  "v_accvgpr_read_b32 v239, a240\n\t" \
  "v_mfma_f32_16x16x32_bf16 v[112:115], a[84:87], v[168:171], v[112:115]\n\t" \
  "v_accvgpr_read_b32 v212, a244\n\t" \
  "v_perm_b32 v208, v212, v239, s40\n\t" \
  "v_mfma_f32_16x16x32_bf16 v[112:115], a[76:79], v[164:167], v[112:115]\n\t" \
  "ds_read_b128 v[156:159], v230 offset:27648\n\t" \
  "v_perm_b32 v212, v212, v239, s41\n\t" \
  "v_mfma_f32_16x16x32_bf16 v[112:115], a[68:71], v[160:163], v[112:115]\n\t" \
  "s_waitcnt lgkmcnt(4)\n\t" \
  "v_accvgpr_read_b32 v239, a241\n\t" \
  "v_accvgpr_read_b32 v213, a245\n\t" \
  "v_mfma_f32_16x16x32_bf16 v[100:103], a[64:67], v[128:131], v[100:103]\n\t" \
  "ds_read_b128 v[160:163], v230 offset:28672\n\t" \
  "v_perm_b32 v209, v213, v239, s40\n\t" \
  "v_mfma_f32_16x16x32_bf16 v[100:103], a[72:75], v[132:135], v[100:103]\n\t" \
  "v_perm_b32 v213, v213, v239, s41\n\t" \
  "v_accvgpr_read_b32 v239, a242\n\t" \
  "v_mfma_f32_16x16x32_bf16 v[100:103], a[80:83], v[136:139], v[100:103]\n\t" \
  "ds_read_b128 v[164:167], v230 offset:29696\n\t" \
  "v_accvgpr_read_b32 v214, a246\n\t" \
  "v_mfma_f32_16x16x32_bf16 v[100:103], a[88:91], v[140:143], v[100:103]\n\t" \
  "v_perm_b32 v210, v214, v239, s40\n\t" \
  "v_perm_b32 v214, v214, v239, s41\n\t" \
  "v_mfma_f32_16x16x32_bf16 v[116:119], a[92:95], v[140:143], v[116:119]\n\t" \
  "ds_read_b128 v[168:171], v230 offset:30720\n\t" \
  "v_accvgpr_read_b32 v239, a243\n\t" \
  "v_mfma_f32_16x16x32_bf16 v[116:119], a[84:87], v[136:139], v[116:119]\n\t" \
  "v_accvgpr_read_b32 v215, a247\n\t" \
  "v_perm_b32 v211, v215, v239, s40\n\t" \
  "v_mfma_f32_16x16x32_bf16 v[116:119], a[76:79], v[132:135], v[116:119]\n\t" \
  "s_waitcnt vmcnt(10) lgkmcnt(3)\n\t" \
  "ds_read_b128 v[172:175], v230 offset:31744\n\t" \
  "v_perm_b32 v215, v215, v239, s41\n\t" \
  "v_mfma_f32_16x16x32_bf16 v[116:119], a[68:71], v[128:131], v[116:119]\n\t" \
  "s_barrier\n\t" \
  "v_mfma_f32_16x16x32_bf16 v[104:107], a[64:67], v[144:147], v[104:107]\n\t" \
  "ds_read_b128 v[128:131], v232 offset:0\n\t" \
  "v_accvgpr_read_b32 v239, a248\n\t" \
  "v_mfma_f32_16x16x32_bf16 v[104:107], a[72:75], v[148:151], v[104:107]\n\t" \
  "v_accvgpr_read_b32 v220, a252\n\t" \
  "v_perm_b32 v216, v220, v239, s40\n\t" \
  "v_mfma_f32_16x16x32_bf16 v[104:107], a[80:83], v[152:155], v[104:107]\n\t" \
  "ds_read_b128 v[132:135], v232 offset:1024\n\t" \
  "v_perm_b32 v220, v220, v239, s41\n\t" \
  "v_mfma_f32_16x16x32_bf16 v[104:107], a[88:91], v[156:159], v[104:107]\n\t" \
  "v_accvgpr_read_b32 v239, a249\n\t" \
  "v_accvgpr_read_b32 v221, a253\n\t" \
  "v_mfma_f32_16x16x32_bf16 v[120:123], a[92:95], v[156:159], v[120:123]\n\t" \
  "ds_read_b128 v[136:139], v232 offset:2048\n\t" \
  "v_perm_b32 v217, v221, v239, s40\n\t" \
  "v_mfma_f32_16x16x32_bf16 v[120:123], a[84:87], v[152:155], v[120:123]\n\t" \
  "v_perm_b32 v221, v221, v239, s41\n\t" \
  "v_accvgpr_read_b32 v239, a250\n\t" \
  "v_mfma_f32_16x16x32_bf16 v[120:123], a[76:79], v[148:151], v[120:123]\n\t" \
  "ds_read_b128 v[140:143], v232 offset:3072\n\t" \
  "v_accvgpr_read_b32 v222, a254\n\t" \
  "v_mfma_f32_16x16x32_bf16 v[120:123], a[68:71], v[144:147], v[120:123]\n\t" \
  "s_waitcnt lgkmcnt(4)\n\t" \
  "v_perm_b32 v218, v222, v239, s40\n\t" \
  "v_perm_b32 v222, v222, v239, s41\n\t" \
  "v_mfma_f32_16x16x32_bf16 v[108:111], a[64:67], v[160:163], v[108:111]\n\t" \
  "ds_read_b128 v[144:147], v232 offset:4096\n\t" \
  "v_accvgpr_read_b32 v239, a251\n\t" \
  "v_mfma_f32_16x16x32_bf16 v[108:111], a[72:75], v[164:167], v[108:111]\n\t" \
  "v_accvgpr_read_b32 v223, a255\n\t" \
  "v_perm_b32 v219, v223, v239, s40\n\t" \
  "v_mfma_f32_16x16x32_bf16 v[108:111], a[80:83], v[168:171], v[108:111]\n\t" \
  "ds_read_b128 v[148:151], v232 offset:5120\n\t" \
  "v_perm_b32 v223, v223, v239, s41\n\t" \
  "v_mfma_f32_16x16x32_bf16 v[108:111], a[88:91], v[172:175], v[108:111]\n\t" \
  "buffer_load_dword v251, off, s[16:19], s73 offset:32 sc1\n\t" \
  "v_mfma_f32_16x16x32_bf16 v[124:127], a[92:95], v[172:175], v[124:127]\n\t" \
  "ds_read_b128 v[152:155], v232 offset:6144\n\t" \
  "v_mfma_f32_16x16x32_bf16 v[124:127], a[84:87], v[168:171], v[124:127]\n\t" \
  "ds_read_b128 v[156:159], v232 offset:7168\n\t" \
  "v_mfma_f32_16x16x32_bf16 v[124:127], a[76:79], v[164:167], v[124:127]\n\t" \
  "v_mfma_f32_16x16x32_bf16 v[124:127], a[68:71], v[160:163], v[124:127]\n\t" \
  "s_waitcnt lgkmcnt(4)\n\t" \
  "v_mfma_f32_16x16x32_bf16 v[96:99], a[96:99], v[128:131], v[96:99]\n\t" \
  "s_waitcnt vmcnt(1)\n\t" \
  "v_cmp_ne_u32 vcc, s36, v246\n\t" \
  "v_mfma_f32_16x16x32_bf16 v[96:99], a[104:107], v[132:135], v[96:99]\n\t" \
  "ds_read_b128 v[160:163], v232 offset:8192\n\t" \
  "v_mfma_f32_16x16x32_bf16 v[96:99], a[112:115], v[136:139], v[96:99]\n\t" \
  "ds_read_b128 v[164:167], v232 offset:9216\n\t" \
  "s_mov_b32 s88, vcc_lo\n\t" \
  "s_cbranch_vccnz .Lat_n2\n\t" \
  "buffer_load_dwordx4 v[64:67], v242, s[16:19], s79 offen offset:0 sc1\n\t" \
  "buffer_load_dwordx4 v[68:71], v242, s[16:19], s79 offen offset:1024 sc1\n\t" \
  "buffer_load_dwordx4 v[72:75], v242, s[16:19], s79 offen offset:2048 sc1\n\t" \
  "buffer_load_dwordx4 v[76:79], v242, s[16:19], s79 offen offset:3072 sc1\n\t" \
  ".Lat_n2:\n\t" \
  "v_mfma_f32_16x16x32_bf16 v[96:99], a[120:123], v[140:143], v[96:99]\n\t" \
  "ds_read_b128 v[168:171], v232 offset:10240\n\t" \
  "v_mfma_f32_16x16x32_bf16 v[112:115], a[124:127], v[140:143], v[112:115]\n\t" \
  "ds_read_b128 v[172:175], v232 offset:11264\n\t" \
  "v_mfma_f32_16x16x32_bf16 v[112:115], a[116:119], v[136:139], v[112:115]\n\t" \
  "v_mfma_f32_16x16x32_bf16 v[112:115], a[108:111], v[132:135], v[112:115]\n\t" \
  "v_mfma_f32_16x16x32_bf16 v[112:115], a[100:103], v[128:131], v[112:115]\n\t" \
  "s_waitcnt lgkmcnt(4)\n\t" \
  "v_mfma_f32_16x16x32_bf16 v[100:103], a[96:99], v[144:147], v[100:103]\n\t" \
  "ds_read_b128 v[128:131], v232 offset:12288\n\t" \
  "v_mfma_f32_16x16x32_bf16 v[100:103], a[104:107], v[148:151], v[100:103]\n\t" \
  "ds_read_b128 v[132:135], v232 offset:13312\n\t" \
  "v_mfma_f32_16x16x32_bf16 v[100:103], a[112:115], v[152:155], v[100:103]\n\t" \
  "ds_read_b128 v[136:139], v232 offset:14336\n\t" \
  "v_mfma_f32_16x16x32_bf16 v[100:103], a[120:123], v[156:159], v[100:103]\n\t" \
  "ds_read_b128 v[140:143], v232 offset:15360\n\t" \
  "v_mfma_f32_16x16x32_bf16 v[116:119], a[124:127], v[156:159], v[116:119]\n\t" \
  "v_mfma_f32_16x16x32_bf16 v[116:119], a[116:119], v[152:155], v[116:119]\n\t" \
  "v_mfma_f32_16x16x32_bf16 v[116:119], a[108:111], v[148:151], v[116:119]\n\t" \
  "v_mfma_f32_16x16x32_bf16 v[116:119], a[100:103], v[144:147], v[116:119]\n\t" \
  "s_waitcnt lgkmcnt(4)\n\t" \
  "v_mfma_f32_16x16x32_bf16 v[104:107], a[96:99], v[160:163], v[104:107]\n\t" \
  "ds_read_b128 v[144:147], v242 offset:16384\n\t" \
  "v_mfma_f32_16x16x32_bf16 v[104:107], a[104:107], v[164:167], v[104:107]\n\t" \
  "ds_read_b128 v[148:151], v242 offset:17408\n\t" \
  "v_mfma_f32_16x16x32_bf16 v[104:107], a[112:115], v[168:171], v[104:107]\n\t" \
  "ds_read_b128 v[152:155], v242 offset:18432\n\t" \
  "v_mfma_f32_16x16x32_bf16 v[104:107], a[120:123], v[172:175], v[104:107]\n\t" \
  "ds_read_b128 v[156:159], v242 offset:19456\n\t" \
  "v_mfma_f32_16x16x32_bf16 v[120:123], a[124:127], v[172:175], v[120:123]\n\t" \
  "v_mfma_f32_16x16x32_bf16 v[120:123], a[116:119], v[168:171], v[120:123]\n\t" \
  "v_mfma_f32_16x16x32_bf16 v[120:123], a[108:111], v[164:167], v[120:123]\n\t" \
  "v_mfma_f32_16x16x32_bf16 v[120:123], a[100:103], v[160:163], v[120:123]\n\t" \
  "s_waitcnt lgkmcnt(4)\n\t" \
  "v_mfma_f32_16x16x32_bf16 v[108:111], a[96:99], v[128:131], v[108:111]\n\t" \
  "ds_read_b128 v[160:163], v242 offset:20480\n\t" \
  "v_mfma_f32_16x16x32_bf16 v[108:111], a[104:107], v[132:135], v[108:111]\n\t" \
  "ds_read_b128 v[164:167], v242 offset:21504\n\t" \
  "v_mfma_f32_16x16x32_bf16 v[108:111], a[112:115], v[136:139], v[108:111]\n\t" \
  "ds_read_b128 v[168:171], v242 offset:22528\n\t" \
  "v_mfma_f32_16x16x32_bf16 v[108:111], a[120:123], v[140:143], v[108:111]\n\t" \
  "ds_read_b128 v[172:175], v242 offset:23552\n\t" \
  "v_mfma_f32_16x16x32_bf16 v[124:127], a[124:127], v[140:143], v[124:127]\n\t" \
  "v_mfma_f32_16x16x32_bf16 v[124:127], a[116:119], v[136:139], v[124:127]\n\t" \
  "v_mfma_f32_16x16x32_bf16 v[124:127], a[108:111], v[132:135], v[124:127]\n\t" \
  "v_mfma_f32_16x16x32_bf16 v[124:127], a[100:103], v[128:131], v[124:127]\n\t" \
  "v_mfma_f32_16x16x32_bf16 v[96:99], a[130:133], v[32:35], v[96:99]\n\t" \
  "v_mfma_f32_16x16x32_bf16 v[100:103], a[130:133], v[36:39], v[100:103]\n\t" \
  "v_mfma_f32_16x16x32_bf16 v[104:107], a[130:133], v[40:43], v[104:107]\n\t" \
  "v_mfma_f32_16x16x32_bf16 v[108:111], a[130:133], v[44:47], v[108:111]\n\t" \
  "v_mfma_f32_16x16x32_bf16 v[112:115], a[128:131], v[32:35], v[112:115]\n\t" \
  "v_mfma_f32_16x16x32_bf16 v[116:119], a[128:131], v[36:39], v[116:119]\n\t" \
  "v_mfma_f32_16x16x32_bf16 v[120:123], a[128:131], v[40:43], v[120:123]\n\t" \
  "v_mfma_f32_16x16x32_bf16 v[124:127], a[128:131], v[44:47], v[124:127]\n\t" \
  "s_waitcnt lgkmcnt(4)\n\t" \
  "v_mfma_f32_16x16x32_bf16 v[96:99], a[192:195], v[144:147], v[96:99]\n\t" \
  "s_cmp_lg_u32 s88, 0\n\t" \
  "s_cbranch_scc1 .Lat_m3\n\t" \
  ".Lat_n3:\n\t" \
  "v_mfma_f32_16x16x32_bf16 v[96:99], a[200:203], v[148:151], v[96:99]\n\t" \
  "s_add_u32 m0, s38, 32768\n\t" \
  "ds_read_b128 v[128:131], v242 offset:24576\n\t" \
  "s_mov_b32 s96, 0\n\t" \
  "v_mfma_f32_16x16x32_bf16 v[96:99], a[208:211], v[152:155], v[96:99]\n\t" \
  "buffer_load_dwordx4 v252, s[28:31], s96 offen offset:0 nt lds\n\t" \
  "v_mfma_f32_16x16x32_bf16 v[96:99], a[216:219], v[156:159], v[96:99]\n\t" \
  "ds_read_b128 v[132:135], v242 offset:25600\n\t" \
  "s_mov_b32 s96, 3072\n\t" \
  "v_mfma_f32_16x16x32_bf16 v[112:115], a[220:223], v[156:159], v[112:115]\n\t" \
  "buffer_load_dwordx4 v252, s[28:31], s96 offen offset:1024 nt lds\n\t" \
  "v_mfma_f32_16x16x32_bf16 v[112:115], a[212:215], v[152:155], v[112:115]\n\t" \
  "ds_read_b128 v[136:139], v242 offset:26624\n\t" \
  "s_mov_b32 s96, 6144\n\t" \
  "v_mfma_f32_16x16x32_bf16 v[112:115], a[204:207], v[148:151], v[112:115]\n\t" \
  "buffer_load_dwordx4 v252, s[28:31], s96 offen offset:2048 nt lds\n\t" \
  "v_mfma_f32_16x16x32_bf16 v[112:115], a[196:199], v[144:147], v[112:115]\n\t" \
  "ds_read_b128 v[140:143], v242 offset:27648\n\t" \
  "s_mov_b32 s96, 9216\n\t" \
  "v_mfma_f32_16x16x32_bf16 v[96:99], a[130:133], v[48:51], v[96:99]\n\t" \
  "buffer_load_dwordx4 v252, s[28:31], s96 offen offset:3072 nt lds\n\t" \
  "v_mfma_f32_16x16x32_bf16 v[100:103], a[130:133], v[52:55], v[100:103]\n\t" \
  "s_add_u32 m0, s38, 49152\n\t" \
  "s_mov_b32 s96, 16384\n\t" \
  "v_mfma_f32_16x16x32_bf16 v[104:107], a[130:133], v[56:59], v[104:107]\n\t" \
  "buffer_load_dwordx4 v252, s[28:31], s96 offen offset:0 nt lds\n\t" \
  "v_mfma_f32_16x16x32_bf16 v[108:111], a[130:133], v[60:63], v[108:111]\n\t" \
  "s_mov_b32 s96, 19456\n\t" \
  "v_mfma_f32_16x16x32_bf16 v[112:115], a[128:131], v[48:51], v[112:115]\n\t" \
  "buffer_load_dwordx4 v252, s[28:31], s96 offen offset:1024 nt lds\n\t" \
  "v_mfma_f32_16x16x32_bf16 v[116:119], a[128:131], v[52:55], v[116:119]\n\t" \
  "s_mov_b32 s96, 22528\n\t" \
  "v_mfma_f32_16x16x32_bf16 v[120:123], a[128:131], v[56:59], v[120:123]\n\t" \
  "buffer_load_dwordx4 v252, s[28:31], s96 offen offset:2048 nt lds\n\t" \
  "v_mfma_f32_16x16x32_bf16 v[124:127], a[128:131], v[60:63], v[124:127]\n\t" \
  "s_waitcnt lgkmcnt(4)\n\t" \
  "s_mov_b32 s96, 25600\n\t" \
  "v_mfma_f32_16x16x32_bf16 v[100:103], a[192:195], v[160:163], v[100:103]\n\t" \
  "buffer_load_dwordx4 v252, s[28:31], s96 offen offset:3072 nt lds\n\t" \
  "v_mfma_f32_16x16x32_bf16 v[100:103], a[200:203], v[164:167], v[100:103]\n\t" \
  "ds_read_b128 v[144:147], v242 offset:28672\n\t" \
  "v_mfma_f32_16x16x32_bf16 v[100:103], a[208:211], v[168:171], v[100:103]\n\t" \
  "ds_read_b128 v[148:151], v242 offset:29696\n\t" \
  "v_mfma_f32_16x16x32_bf16 v[100:103], a[216:219], v[172:175], v[100:103]\n\t" \
  "ds_read_b128 v[152:155], v242 offset:30720\n\t" \
  "v_mfma_f32_16x16x32_bf16 v[116:119], a[220:223], v[172:175], v[116:119]\n\t" \
  "ds_read_b128 v[156:159], v242 offset:31744\n\t" \
  "v_mfma_f32_16x16x32_bf16 v[116:119], a[212:215], v[168:171], v[116:119]\n\t" \
  "v_mfma_f32_16x16x32_bf16 v[116:119], a[204:207], v[164:167], v[116:119]\n\t" \
  "v_mfma_f32_16x16x32_bf16 v[116:119], a[196:199], v[160:163], v[116:119]\n\t" \
  "s_waitcnt lgkmcnt(4)\n\t" \
  "v_mfma_f32_16x16x32_bf16 v[104:107], a[192:195], v[128:131], v[104:107]\n\t" \
  "ds_read_b128 v[160:163], v237 offset:2560\n\t" \
  "v_mfma_f32_16x16x32_bf16 v[104:107], a[200:203], v[132:135], v[104:107]\n\t" \
  "ds_read_b128 v[164:167], v237 offset:2624\n\t" \
  "v_mfma_f32_16x16x32_bf16 v[104:107], a[208:211], v[136:139], v[104:107]\n\t" \
  "v_mfma_f32_16x16x32_bf16 v[104:107], a[216:219], v[140:143], v[104:107]\n\t" \
  "v_mfma_f32_16x16x32_bf16 v[120:123], a[220:223], v[140:143], v[120:123]\n\t" \
  "v_mfma_f32_16x16x32_bf16 v[120:123], a[212:215], v[136:139], v[120:123]\n\t" \
  "v_mfma_f32_16x16x32_bf16 v[120:123], a[204:207], v[132:135], v[120:123]\n\t" \
  "v_mfma_f32_16x16x32_bf16 v[120:123], a[196:199], v[128:131], v[120:123]\n\t" \
  "s_waitcnt lgkmcnt(2)\n\t" \
  "v_mfma_f32_16x16x32_bf16 v[108:111], a[192:195], v[144:147], v[108:111]\n\t" \
  "v_mfma_f32_16x16x32_bf16 v[108:111], a[200:203], v[148:151], v[108:111]\n\t" \
  "v_mfma_f32_16x16x32_bf16 v[108:111], a[208:211], v[152:155], v[108:111]\n\t" \
  "v_mfma_f32_16x16x32_bf16 v[108:111], a[216:219], v[156:159], v[108:111]\n\t" \
  "v_mfma_f32_16x16x32_bf16 v[124:127], a[220:223], v[156:159], v[124:127]\n\t" \
  "v_mfma_f32_16x16x32_bf16 v[124:127], a[212:215], v[152:155], v[124:127]\n\t" \
  "v_mfma_f32_16x16x32_bf16 v[124:127], a[204:207], v[148:151], v[124:127]\n\t" \
  "v_mfma_f32_16x16x32_bf16 v[124:127], a[196:199], v[144:147], v[124:127]\n\t" \
  STP("s_memrealtime s[94:95]\n\t") \
  STP("s_memtime s[92:93]\n\t") \
  "s_cmp_lg_u32 s88, 0\n\t" \
  "s_cbranch_scc1 .Lat_s99\n\t" \
  ".Lat_d2:\n\t" \
  "s_waitcnt vmcnt(8)\n\t" \
  "v_mfma_f32_16x16x32_bf16 v[96:99], a[130:133], v[64:67], v[96:99]\n\t" \
  "v_mfma_f32_16x16x32_bf16 v[100:103], a[130:133], v[68:71], v[100:103]\n\t" \
  "v_mfma_f32_16x16x32_bf16 v[104:107], a[130:133], v[72:75], v[104:107]\n\t" \
  "v_mfma_f32_16x16x32_bf16 v[108:111], a[130:133], v[76:79], v[108:111]\n\t" \
  "v_mfma_f32_16x16x32_bf16 v[112:115], a[128:131], v[64:67], v[112:115]\n\t" \
  "v_mfma_f32_16x16x32_bf16 v[116:119], a[128:131], v[68:71], v[116:119]\n\t" \
  "v_mfma_f32_16x16x32_bf16 v[120:123], a[128:131], v[72:75], v[120:123]\n\t" \
  "v_mfma_f32_16x16x32_bf16 v[124:127], a[128:131], v[76:79], v[124:127]\n\t" \
  "s_waitcnt lgkmcnt(0)\n\t" \
  STP("v_writelane_b32 v255, s94, 38\n\t") \
  STP("v_writelane_b32 v255, s92, 39\n\t") \
  "v_pk_fma_f32 v[0:1], v[160:161], v[96:97], v[0:1]\n\t" \
  "v_pk_fma_f32 v[4:5], v[160:161], v[100:101], v[4:5]\n\t" \
  "v_pk_fma_f32 v[8:9], v[160:161], v[104:105], v[8:9]\n\t" \
  "v_pk_fma_f32 v[12:13], v[160:161], v[108:109], v[12:13]\n\t" \
  "v_pk_fma_f32 v[2:3], v[162:163], v[98:99], v[2:3]\n\t" \
  "v_pk_fma_f32 v[6:7], v[162:163], v[102:103], v[6:7]\n\t" \
  "v_pk_fma_f32 v[10:11], v[162:163], v[106:107], v[10:11]\n\t" \
  "v_pk_fma_f32 v[14:15], v[162:163], v[110:111], v[14:15]\n\t" \
  "v_pk_fma_f32 v[16:17], v[164:165], v[112:113], v[16:17]\n\t" \
  "v_pk_fma_f32 v[20:21], v[164:165], v[116:117], v[20:21]\n\t" \
  "v_pk_fma_f32 v[24:25], v[164:165], v[120:121], v[24:25]\n\t" \
  "v_pk_fma_f32 v[28:29], v[164:165], v[124:125], v[28:29]\n\t" \
  "v_pk_fma_f32 v[18:19], v[166:167], v[114:115], v[18:19]\n\t" \
  "v_pk_fma_f32 v[22:23], v[166:167], v[118:119], v[22:23]\n\t" \
  "v_pk_fma_f32 v[26:27], v[166:167], v[122:123], v[26:27]\n\t" \
  "v_pk_fma_f32 v[30:31], v[166:167], v[126:127], v[30:31]\n\t" \
  "v_cvt_pk_bf16_f32 v128, v0, v1\n\t" \
  "v_cvt_pk_bf16_f32 v132, v4, v5\n\t" \
  "v_cvt_pk_bf16_f32 v136, v8, v9\n\t" \
  "v_cvt_pk_bf16_f32 v140, v12, v13\n\t" \
  "v_cvt_pk_bf16_f32 v129, v2, v3\n\t" \
  "v_cvt_pk_bf16_f32 v133, v6, v7\n\t" \
  "v_cvt_pk_bf16_f32 v137, v10, v11\n\t" \
  "v_cvt_pk_bf16_f32 v141, v14, v15\n\t" \
  "v_cvt_pk_bf16_f32 v130, v16, v17\n\t" \
  "v_cvt_pk_bf16_f32 v134, v20, v21\n\t" \
  "v_cvt_pk_bf16_f32 v138, v24, v25\n\t" \
  "v_cvt_pk_bf16_f32 v142, v28, v29\n\t" \
  "v_cvt_pk_bf16_f32 v131, v18, v19\n\t" \
  "v_cvt_pk_bf16_f32 v135, v22, v23\n\t" \
  "v_cvt_pk_bf16_f32 v139, v26, v27\n\t" \
  "v_cvt_pk_bf16_f32 v143, v30, v31\n\t" \
  "v_dot2_f32_bf16 v64, v128, v128, 0\n\t" \
  "v_dot2_f32_bf16 v65, v132, v132, 0\n\t" \
  "v_dot2_f32_bf16 v66, v136, v136, 0\n\t" \
  "v_dot2_f32_bf16 v67, v140, v140, 0\n\t" \
  "v_dot2_f32_bf16 v64, v129, v129, v64\n\t" \
  "v_dot2_f32_bf16 v65, v133, v133, v65\n\t" \
  "v_dot2_f32_bf16 v66, v137, v137, v66\n\t" \
  "v_dot2_f32_bf16 v67, v141, v141, v67\n\t" \
  "v_dot2_f32_bf16 v64, v130, v130, v64\n\t" \
  "v_dot2_f32_bf16 v65, v134, v134, v65\n\t" \
  "v_dot2_f32_bf16 v66, v138, v138, v66\n\t" \
  "v_dot2_f32_bf16 v67, v142, v142, v67\n\t" \
  "v_dot2_f32_bf16 v64, v131, v131, v64\n\t" \
  "v_dot2_f32_bf16 v65, v135, v135, v65\n\t" \
  "v_dot2_f32_bf16 v66, v139, v139, v66\n\t" \
  "v_dot2_f32_bf16 v67, v143, v143, v67\n\t" \
  "v_mfma_f32_16x16x32_bf16 v[68:71], a[176:179], v[128:131], 0\n\t" \
  "v_mfma_f32_16x16x32_bf16 v[68:71], a[180:183], v[132:135], v[68:71]\n\t" \
  "v_permlane32_swap_b32 v64, v66\n\t" \
  "v_mfma_f32_16x16x32_bf16 v[68:71], a[184:187], v[136:139], v[68:71]\n\t" \
  "v_permlane32_swap_b32 v65, v67\n\t" \
  "v_mfma_f32_16x16x32_bf16 v[68:71], a[188:191], v[140:143], v[68:71]\n\t" \
  "v_add_f32 v64, v64, v66\n\t" \
  "v_add_f32 v65, v65, v67\n\t" \
  "v_mov_b32 v73, s36\n\t" \
  "v_mov_b32 v75, s36\n\t" \
  "v_permlane16_swap_b32 v64, v65\n\t" \
  "s_mov_b32 s81, 0\n\t" \
  "s_nop 1\n\t" \
  "v_add_f32 v70, v69, v70\n\t" \
  "v_add_f32 v69, v64, v65\n\t" \
  "v_mov_b32 v71, v238\n\t" \
  "ds_write_b128 v224, v[68:71]\n\t" \
  "s_waitcnt lgkmcnt(0)\n\t" \
  "s_barrier\n\t" \
  "ds_read_b128 v[96:99], v242 offset:0\n\t" \
  "ds_read_b128 v[100:103], v242 offset:1024\n\t" \
  "ds_read_b128 v[104:107], v242 offset:2048\n\t" \
  "ds_read_b128 v[108:111], v242 offset:3072\n\t" \
  "s_cmp_eq_u32 s3, 0\n\t" \
  "s_cselect_b64 vcc, -1, 0\n\t" \
  "s_waitcnt lgkmcnt(2)\n\t" \
  "v_pk_add_f32 v[96:97], v[96:97], v[100:101]\n\t" \
  "v_pk_add_f32 v[98:99], v[98:99], v[102:103]\n\t" \
  "s_waitcnt lgkmcnt(0)\n\t" \
  "v_pk_add_f32 v[104:105], v[104:105], v[108:109]\n\t" \
  "v_pk_add_f32 v[106:107], v[106:107], v[110:111]\n\t" \
  "v_pk_add_f32 v[64:65], v[96:97], v[104:105]\n\t" \
  "v_pk_add_f32 v[66:67], v[98:99], v[106:107]\n\t" \
  "v_cndmask_b32 v72, v66, v64, vcc\n\t" \
  "v_cndmask_b32 v74, v67, v65, vcc\n\t" \
  "buffer_store_dwordx4 v[72:75], v240, s[16:19], s10 offen sc1\n\t" \
  STP("s_memrealtime s[94:95]\n\t") \
  STP("s_memtime s[92:93]\n\t") \
  STP("s_waitcnt lgkmcnt(0)\n\t") \
  STP("v_writelane_b32 v255, s94, 40\n\t") \
  STP("v_writelane_b32 v255, s92, 41\n\t") \
  "buffer_load_dwordx4 v[96:99], v242, s[16:19], s11 offen offset:0 sc1\n\t" \
  "buffer_load_dwordx4 v[100:103], v242, s[16:19], s11 offen offset:1024 sc1\n\t" \
  "buffer_load_dwordx4 v[104:107], v242, s[16:19], s12 offen offset:0 sc1\n\t" \
  "buffer_load_dwordx4 v[108:111], v242, s[16:19], s12 offen offset:1024 sc1\n\t" \
  "ds_read_b128 v[144:147], v237 offset:2688\n\t" \
  "ds_read_b128 v[148:151], v237 offset:2752\n\t" \
  "ds_read_b128 v[152:155], v237 offset:2816\n\t" \
  "ds_read_b128 v[156:159], v237 offset:2880\n\t" \
  "ds_read_b128 v[168:171], v237 offset:2944\n\t" \
  "ds_read_b128 v[172:175], v237 offset:3008\n\t" \
  "v_lshlrev_b32 v0, 16, v128\n\t" \
  "v_and_b32 v1, 0xffff0000, v128\n\t" \
  "v_lshlrev_b32 v2, 16, v129\n\t" \
  "v_and_b32 v3, 0xffff0000, v129\n\t" \
  "v_lshlrev_b32 v4, 16, v130\n\t" \
  "v_and_b32 v5, 0xffff0000, v130\n\t" \
  "v_lshlrev_b32 v6, 16, v131\n\t" \
  "v_and_b32 v7, 0xffff0000, v131\n\t" \
  "v_lshlrev_b32 v8, 16, v132\n\t" \
  "v_and_b32 v9, 0xffff0000, v132\n\t" \
  "v_lshlrev_b32 v10, 16, v133\n\t" \
  "v_and_b32 v11, 0xffff0000, v133\n\t" \
  "v_lshlrev_b32 v12, 16, v134\n\t" \
  "v_and_b32 v13, 0xffff0000, v134\n\t" \
  "v_lshlrev_b32 v14, 16, v135\n\t" \
  "v_and_b32 v15, 0xffff0000, v135\n\t" \
  "v_lshlrev_b32 v16, 16, v136\n\t" \
  "v_and_b32 v17, 0xffff0000, v136\n\t" \
  "v_lshlrev_b32 v18, 16, v137\n\t" \
  "v_and_b32 v19, 0xffff0000, v137\n\t" \
  "v_lshlrev_b32 v20, 16, v138\n\t" \
  "v_and_b32 v21, 0xffff0000, v138\n\t" \
  "v_lshlrev_b32 v22, 16, v139\n\t" \
  "v_and_b32 v23, 0xffff0000, v139\n\t" \
  "v_lshlrev_b32 v24, 16, v140\n\t" \
  "v_and_b32 v25, 0xffff0000, v140\n\t" \
  "v_lshlrev_b32 v26, 16, v141\n\t" \
  "v_and_b32 v27, 0xffff0000, v141\n\t" \
  "v_lshlrev_b32 v28, 16, v142\n\t" \
  "v_and_b32 v29, 0xffff0000, v142\n\t" \
  "v_lshlrev_b32 v30, 16, v143\n\t" \
  "v_and_b32 v31, 0xffff0000, v143\n\t" \
  "s_waitcnt vmcnt(5)\n\t" \
  "ds_read2st64_b64 v[32:35], v247 offset0:64 offset1:65\n\t" \
  "ds_read2st64_b64 v[36:39], v247 offset0:66 offset1:67\n\t" \
  "ds_read2st64_b64 v[40:43], v247 offset0:68 offset1:69\n\t" \
  "ds_read2st64_b64 v[44:47], v247 offset0:70 offset1:71\n\t" \
  "ds_read2st64_b64 v[48:51], v247 offset0:96 offset1:97\n\t" \
  "ds_read2st64_b64 v[52:55], v247 offset0:98 offset1:99\n\t" \
  "ds_read2st64_b64 v[56:59], v247 offset0:100 offset1:101\n\t" \
  "ds_read2st64_b64 v[60:63], v247 offset0:102 offset1:103\n\t" \
  "s_waitcnt lgkmcnt(0)\n\t" \
  "v_perm_b32 v112, v34, v32, s40\n\t" \
  "v_perm_b32 v34, v34, v32, s41\n\t" \
  "v_perm_b32 v113, v35, v33, s40\n\t" \
  "v_perm_b32 v35, v35, v33, s41\n\t" \
  "v_perm_b32 v114, v38, v36, s40\n\t" \
  "v_perm_b32 v38, v38, v36, s41\n\t" \
  "v_perm_b32 v115, v39, v37, s40\n\t" \
  "v_perm_b32 v39, v39, v37, s41\n\t" \
  "v_perm_b32 v116, v42, v40, s40\n\t" \
  "v_perm_b32 v42, v42, v40, s41\n\t" \
  "v_perm_b32 v117, v43, v41, s40\n\t" \
  "v_perm_b32 v43, v43, v41, s41\n\t" \
  "v_perm_b32 v118, v46, v44, s40\n\t" \
  "v_perm_b32 v46, v46, v44, s41\n\t" \
  "v_perm_b32 v119, v47, v45, s40\n\t" \
  "v_perm_b32 v47, v47, v45, s41\n\t" \
  "v_perm_b32 v120, v50, v48, s40\n\t" \
  "v_perm_b32 v50, v50, v48, s41\n\t" \
  "v_perm_b32 v121, v51, v49, s40\n\t" \
  "v_perm_b32 v51, v51, v49, s41\n\t" \
  "v_perm_b32 v122, v54, v52, s40\n\t" \
  "v_perm_b32 v54, v54, v52, s41\n\t" \
  "v_perm_b32 v123, v55, v53, s40\n\t" \
  "v_perm_b32 v55, v55, v53, s41\n\t" \
  "v_perm_b32 v124, v58, v56, s40\n\t" \
  "v_perm_b32 v58, v58, v56, s41\n\t" \
  "v_perm_b32 v125, v59, v57, s40\n\t" \
  "v_perm_b32 v59, v59, v57, s41\n\t" \
  "v_perm_b32 v126, v62, v60, s40\n\t" \
  "v_perm_b32 v62, v62, v60, s41\n\t" \
  "v_perm_b32 v127, v63, v61, s40\n\t" \
  "v_perm_b32 v63, v63, v61, s41\n\t" \
  "s_cmp_lt_u32 s39, 4096\n\t" \
  "s_cselect_b64 s[52:53], -1, 0\n\t" \
  "s_branch .Ltl_w1\n\t" \
  ".Ltl_r1:\n\t" \
  "s_sleep 1\n\t" \
  "buffer_load_dwordx4 v[96:99], v242, s[16:19], s11 offen offset:0 sc1\n\t" \
  "buffer_load_dwordx4 v[100:103], v242, s[16:19], s11 offen offset:1024 sc1\n\t" \
  "buffer_load_dwordx4 v[104:107], v242, s[16:19], s12 offen offset:0 sc1\n\t" \
  "buffer_load_dwordx4 v[108:111], v242, s[16:19], s12 offen offset:1024 sc1\n\t" \
  ".Ltl_w1:\n\t" \
  "s_waitcnt vmcnt(0)\n\t" \
  "v_xor_b32 v72, s36, v97\n\t" \
  "v_bitop3_b32 v72, v72, v99, s36 bitop3:0xf6\n\t" \
  "v_bitop3_b32 v72, v72, v101, s36 bitop3:0xf6\n\t" \
  "v_bitop3_b32 v72, v72, v103, s36 bitop3:0xf6\n\t" \
  "v_bitop3_b32 v72, v72, v105, s36 bitop3:0xf6\n\t" \
  "v_bitop3_b32 v72, v72, v107, s36 bitop3:0xf6\n\t" \
  "v_bitop3_b32 v72, v72, v109, s36 bitop3:0xf6\n\t" \
  "v_bitop3_b32 v72, v72, v111, s36 bitop3:0xf6\n\t" \
  "v_cmp_ne_u32 vcc, 0, v72\n\t" \
  "v_cndmask_b32 v96, v96, v64, s[52:53]\n\t" \
  "v_cndmask_b32 v98, v98, v65, s[52:53]\n\t" \
  "v_cndmask_b32 v100, v100, v66, s[52:53]\n\t" \
  "v_cndmask_b32 v102, v102, v67, s[52:53]\n\t" \
  "v_add_f32 v68, v96, v104\n\t" \
  "v_add_f32 v69, v98, v106\n\t" \
  "v_add_f32 v70, v100, v108\n\t" \
  "v_add_f32 v71, v102, v110\n\t" \
  "s_cbranch_vccz .Ltl_k1\n\t" \
  "s_add_u32 s81, s81, 1\n\t" \
  "s_cmp_lt_u32 s81, " FQ_CAP "\n\t" \
  "s_cbranch_scc1 .Ltl_r1\n\t" \
  "buffer_store_dword v243, off, s[16:19], 0 offset:256 sc1\n\t" \
  ".Ltl_k1:\n\t" \
  "ds_write_b128 v224, v[68:71] offset:4096\n\t" \
  "s_waitcnt lgkmcnt(0)\n\t" \
  "s_barrier\n\t" \
  "ds_read_b128 v[96:99], v242 offset:4096\n\t" \
  "ds_read_b128 v[100:103], v242 offset:5120\n\t" \
  "ds_read_b128 v[104:107], v242 offset:6144\n\t" \
  "ds_read_b128 v[108:111], v242 offset:7168\n\t" \
  "buffer_load_dwordx2 v[248:249], v225, s[16:19], s13 offen sc1\n\t" \
  STP("s_memrealtime s[94:95]\n\t") \
  STP("s_memtime s[92:93]\n\t") \
  STP("s_waitcnt lgkmcnt(0)\n\t") \
  STP("v_writelane_b32 v255, s94, 42\n\t") \
  STP("v_writelane_b32 v255, s92, 43\n\t") \
  "s_waitcnt lgkmcnt(2)\n\t" \
  "v_pk_add_f32 v[96:97], v[96:97], v[100:101]\n\t" \
  "v_pk_add_f32 v[98:99], v[98:99], v[102:103]\n\t" \
  "s_waitcnt lgkmcnt(0)\n\t" \
  "v_pk_add_f32 v[104:105], v[104:105], v[108:109]\n\t" \
  "v_pk_add_f32 v[106:107], v[106:107], v[110:111]\n\t" \
  "v_pk_add_f32 v[64:65], v[96:97], v[104:105]\n\t" \
  "v_pk_add_f32 v[66:67], v[98:99], v[106:107]\n\t" \
  "v_mul_f32 v64, 0x3a800000, v64\n\t" \
  "v_mul_f32 v65, 0x3a800000, v65\n\t" \
  "v_readlane_b32 s42, v67, 0\n\t" \
  "v_readlane_b32 s43, v67, 1\n\t" \
  "v_fma_f32 v65, -v64, v64, v65\n\t" \
  "v_max_f32 v65, 0, v65\n\t" \
  "v_add_f32 v65, 0x3727c5ac, v65\n\t" \
  "v_rsq_f32 v68, v65\n\t" \
  "v_fma_f32 v66, -v64, s42, v66\n\t" \
  "v_fma_f32 v66, v68, v66, s43\n\t" \
  "v_cvt_pk_bf16_f32 v66, v66, v66\n\t" \
  "v_ashrrev_i32 v67, 31, v66\n\t" \
  "v_or_b32 v67, 0x80000000, v67\n\t" \
  "v_xor_b32 v66, v66, v67\n\t" \
  "v_and_b32 v66, 0xffff0000, v66\n\t" \
  "v_or_b32 v66, v66, v227\n\t" \
  "s_nop 1\n\t" \
  "v_max_u32_dpp v66, v66, v66 quad_perm:[1,0,3,2] row_mask:0xf bank_mask:0xf\n\t" \
  "v_mul_f32_e64 v70, v68, -v64\n\t" \
  "v_mov_b32 v65, s36\n\t" \
  "v_max_u32_dpp v66, v66, v66 quad_perm:[2,3,0,1] row_mask:0xf bank_mask:0xf\n\t" \
  "v_mov_b32 v97, s36\n\t" \
  "v_mov_b32 v99, s36\n\t" \
  "v_max_u32_dpp v66, v66, v66 row_half_mirror row_mask:0xf bank_mask:0xf\n\t" \
  "v_mov_b32 v101, s36\n\t" \
  "v_mov_b32 v103, s36\n\t" \
  "v_max_u32_dpp v66, v66, v66 row_mirror row_mask:0xf bank_mask:0xf\n\t" \
  "v_mov_b32 v105, s36\n\t" \
  "v_mov_b32 v107, s36\n\t" \
  "v_readlane_b32 s46, v66, 0\n\t" \
  "v_readlane_b32 s42, v66, 16\n\t" \
  "v_readlane_b32 s43, v66, 32\n\t" \
  "v_readlane_b32 s44, v66, 48\n\t" \
  "s_max_u32 s46, s46, s42\n\t" \
  "s_max_u32 s43, s43, s44\n\t" \
  "s_max_u32 s46, s46, s43\n\t" \
  "v_mov_b32 v64, s46\n\t" \
  "buffer_store_dwordx2 v[64:65], v244, s[16:19], s14 offen sc1\n\t" \
  STP("s_memrealtime s[94:95]\n\t") \
  STP("s_memtime s[92:93]\n\t") \
  STP("s_waitcnt lgkmcnt(0)\n\t") \
  STP("v_writelane_b32 v255, s94, 44\n\t") \
  STP("v_writelane_b32 v255, s92, 45\n\t") \
  "s_not_b32 s48, s46\n\t" \
  "s_and_b32 s48, s48, 63\n\t" \
  "s_and_b32 s44, s48, 15\n\t" \
  "s_lshl_b32 s50, 0x10001, s44\n\t" \
  "s_mov_b32 s51, s50\n\t" \
  "v_readlane_b32 s52, v68, s48\n\t" \
  "v_readlane_b32 s54, v70, s48\n\t" \
  "s_lshr_b32 s48, s48, 4\n\t" \
  "s_lshl_b32 s44, s48, 2\n\t" \
  "v_mov_b32 v109, s36\n\t" \
  "v_mov_b32 v111, s36\n\t" \
  "s_set_gpr_idx_on s44, gpr_idx(SRC0)\n\t" \
  "v_mov_b32 v104, v128\n\t" \
  "v_mov_b32 v106, v129\n\t" \
  "v_mov_b32 v108, v130\n\t" \
  "v_mov_b32 v110, v131\n\t" \
  "s_set_gpr_idx_off\n\t" \
  "v_mov_b32 v66, s54\n\t" \
  "v_lshlrev_b32 v160, 16, v104\n\t" \
  "v_and_b32 v161, 0xffff0000, v104\n\t" \
  "v_lshlrev_b32 v162, 16, v106\n\t" \
  "v_and_b32 v163, 0xffff0000, v106\n\t" \
  "v_lshlrev_b32 v164, 16, v108\n\t" \
  "v_and_b32 v165, 0xffff0000, v108\n\t" \
  "v_lshlrev_b32 v166, 16, v110\n\t" \
  "v_and_b32 v167, 0xffff0000, v110\n\t" \
  "v_fma_f32 v160, v160, s52, v66\n\t" \
  "v_fma_f32 v161, v161, s52, v66\n\t" \
  "v_fma_f32 v162, v162, s52, v66\n\t" \
  "v_fma_f32 v163, v163, s52, v66\n\t" \
  "v_fma_f32 v164, v164, s52, v66\n\t" \
  "v_fma_f32 v165, v165, s52, v66\n\t" \
  "v_fma_f32 v166, v166, s52, v66\n\t" \
  "v_fma_f32 v167, v167, s52, v66\n\t" \
  "v_pk_fma_f32 v[160:161], v[160:161], v[152:153], v[144:145]\n\t" \
  "v_pk_fma_f32 v[162:163], v[162:163], v[154:155], v[146:147]\n\t" \
  "v_pk_fma_f32 v[164:165], v[164:165], v[156:157], v[148:149]\n\t" \
  "v_pk_fma_f32 v[166:167], v[166:167], v[158:159], v[150:151]\n\t" \
  "v_cvt_pk_bf16_f32 v96, v160, v161\n\t" \
  "v_cvt_pk_bf16_f32 v98, v162, v163\n\t" \
  "v_cvt_pk_bf16_f32 v100, v164, v165\n\t" \
  "v_cvt_pk_bf16_f32 v102, v166, v167\n\t" \
  "s_waitcnt vmcnt(1)\n\t" \
  "v_cmp_eq_u32 vcc, s36, v249\n\t" \
  "v_mov_b32 v69, v68\n\t" \
  "v_mov_b32 v71, v70\n\t" \
  "v_cndmask_b32 v64, 0, v248, vcc\n\t" \
  "s_nop 1\n\t" \
  "v_max_u32_dpp v64, v64, v64 quad_perm:[1,0,3,2] row_mask:0xf bank_mask:0xf\n\t" \
  "v_permlane32_swap_b32 v68, v69\n\t" \
  "v_permlane32_swap_b32 v70, v71\n\t" \
  "v_max_u32_dpp v64, v64, v64 quad_perm:[2,3,0,1] row_mask:0xf bank_mask:0xf\n\t" \
  "v_mov_b32 v160, v68\n\t" \
  "v_mov_b32 v161, v69\n\t" \
  "v_max_u32_dpp v64, v64, v64 row_half_mirror row_mask:0xf bank_mask:0xf\n\t" \
  "v_mov_b32 v162, v70\n\t" \
  "v_mov_b32 v163, v71\n\t" \
  "v_max_u32_dpp v64, v64, v64 row_mirror row_mask:0xf bank_mask:0xf\n\t" \
  "s_nop 0\n\t" \
  "v_readlane_b32 s45, v64, 0\n\t" \
  "v_readlane_b32 s42, v64, 16\n\t" \
  "s_max_u32 s45, s45, s42\n\t" \
  "s_cmp_ge_u32 s46, s45\n\t" \
  "s_cselect_b32 s49, 1, 0\n\t" \
  "s_max_u32 s45, s45, s46\n\t" \
  "s_not_b32 s42, s45\n\t" \
  "s_bfe_u32 s47, s42, 0x50006\n\t" \
  "s_lshl_b32 s43, s42, 7\n\t" \
  "s_and_b32 s43, s43, 0x3c000\n\t" \
  "s_lshl_b32 s42, s42, 6\n\t" \
  "s_and_b32 s42, s42, 0x1000\n\t" \
  "s_add_u32 s42, s42, s43\n\t" \
  "s_add_u32 s42, s42, s15\n\t" \
  "buffer_load_dwordx2 v[248:249], v225, s[16:19], s13 offen sc1\n\t" \
  "buffer_load_dwordx4 v[72:75], v226, s[16:19], s42 offen offset:0 sc1\n\t" \
  "buffer_load_dwordx4 v[76:79], v226, s[16:19], s42 offen offset:16 sc1\n\t" \
  "s_cmp_eq_u32 s49, 0\n\t" \
  "s_cbranch_scc1 .Ltl_nc\n\t" \
  "s_xor_b32 s43, s20, 8192\n\t" \
  "s_mov_b64 exec, s[50:51]\n\t" \
  "buffer_store_dwordx4 v[96:99], v228, s[16:19], s20 offen offset:0 sc1\n\t" \
  "buffer_store_dwordx4 v[100:103], v228, s[16:19], s20 offen offset:64 sc1\n\t" \
  "buffer_store_dwordx4 v[96:99], v228, s[16:19], s43 offen offset:0 sc1\n\t" \
  "buffer_store_dwordx4 v[100:103], v228, s[16:19], s43 offen offset:64 sc1\n\t" \
  "buffer_store_dwordx4 v[104:107], v228, s[16:19], s21 offen offset:0 sc1\n\t" \
  "buffer_store_dwordx4 v[108:111], v228, s[16:19], s21 offen offset:64 sc1\n\t" \
  "s_mov_b64 exec, -1\n\t" \
  ".Ltl_nc:\n\t" \
  "v_permlane16_swap_b32 v68, v160\n\t" \
  "v_permlane16_swap_b32 v69, v161\n\t" \
  "v_permlane16_swap_b32 v70, v162\n\t" \
  "v_permlane16_swap_b32 v71, v163\n\t" \
  "v_pk_fma_f32 v[96:97], v[0:1], v[68:69], v[70:71] op_sel:[0,0,0] op_sel_hi:[1,0,0]\n\t" \
  "v_pk_fma_f32 v[98:99], v[2:3], v[68:69], v[70:71] op_sel:[0,0,0] op_sel_hi:[1,0,0]\n\t" \
  "v_pk_fma_f32 v[100:101], v[4:5], v[68:69], v[70:71] op_sel:[0,0,0] op_sel_hi:[1,0,0]\n\t" \
  "v_pk_fma_f32 v[102:103], v[6:7], v[68:69], v[70:71] op_sel:[0,0,0] op_sel_hi:[1,0,0]\n\t" \
  "v_pk_fma_f32 v[96:97], v[96:97], v[152:153], v[144:145]\n\t" \
  "v_pk_fma_f32 v[98:99], v[98:99], v[154:155], v[146:147]\n\t" \
  ".Lhs16:\n\ts_cbranch_execz .Lhs16n\n\t" \
  "v_pk_fma_f32 v[100:101], v[100:101], v[156:157], v[148:149]\n\t" \
  "v_pk_fma_f32 v[102:103], v[102:103], v[158:159], v[150:151]\n\t" \
  "v_cvt_pk_bf16_f32 v97, v96, v97\n\t" \
  "v_cvt_pk_bf16_f32 v99, v98, v99\n\t" \
  "v_cvt_pk_bf16_f32 v101, v100, v101\n\t" \
  "v_cvt_pk_bf16_f32 v103, v102, v103\n\t" \
  "v_lshlrev_b32 v96, 16, v97\n\t" \
  "v_and_b32 v97, 0xffff0000, v97\n\t" \
  "v_lshlrev_b32 v98, 16, v99\n\t" \
  "v_and_b32 v99, 0xffff0000, v99\n\t" \
  "v_lshlrev_b32 v100, 16, v101\n\t" \
  "v_and_b32 v101, 0xffff0000, v101\n\t" \
  "v_lshlrev_b32 v102, 16, v103\n\t" \
  "v_and_b32 v103, 0xffff0000, v103\n\t" \
  "v_pk_fma_f32 v[96:97], v[168:169], v[96:97], v[0:1]\n\t" \
  "v_pk_fma_f32 v[98:99], v[170:171], v[98:99], v[2:3]\n\t" \
  "v_pk_fma_f32 v[100:101], v[172:173], v[100:101], v[4:5]\n\t" \
  "v_pk_fma_f32 v[102:103], v[174:175], v[102:103], v[6:7]\n\t" \
  "v_cvt_pk_bf16_f32 v128, v96, v97\n\t" \
  "v_cvt_pk_bf16_f32 v129, v98, v99\n\t" \
  "v_cvt_pk_bf16_f32 v130, v100, v101\n\t" \
  "v_cvt_pk_bf16_f32 v131, v102, v103\n\t" \
  "v_pk_fma_f32 v[104:105], v[8:9], v[160:161], v[162:163] op_sel:[0,0,0] op_sel_hi:[1,0,0]\n\t" \
  "v_pk_fma_f32 v[106:107], v[10:11], v[160:161], v[162:163] op_sel:[0,0,0] op_sel_hi:[1,0,0]\n\t" \
  "v_pk_fma_f32 v[108:109], v[12:13], v[160:161], v[162:163] op_sel:[0,0,0] op_sel_hi:[1,0,0]\n\t" \
  "v_pk_fma_f32 v[110:111], v[14:15], v[160:161], v[162:163] op_sel:[0,0,0] op_sel_hi:[1,0,0]\n\t" \
  "v_pk_fma_f32 v[104:105], v[104:105], v[152:153], v[144:145]\n\t" \
  "v_pk_fma_f32 v[106:107], v[106:107], v[154:155], v[146:147]\n\t" \
  "v_pk_fma_f32 v[108:109], v[108:109], v[156:157], v[148:149]\n\t" \
  "v_pk_fma_f32 v[110:111], v[110:111], v[158:159], v[150:151]\n\t" \
  "v_cvt_pk_bf16_f32 v105, v104, v105\n\t" \
  "v_cvt_pk_bf16_f32 v107, v106, v107\n\t" \
  "v_cvt_pk_bf16_f32 v109, v108, v109\n\t" \
  "v_cvt_pk_bf16_f32 v111, v110, v111\n\t" \
  "v_lshlrev_b32 v104, 16, v105\n\t" \
  "v_and_b32 v105, 0xffff0000, v105\n\t" \
  "v_lshlrev_b32 v106, 16, v107\n\t" \
  "v_and_b32 v107, 0xffff0000, v107\n\t" \
  "v_lshlrev_b32 v108, 16, v109\n\t" \
  "v_and_b32 v109, 0xffff0000, v109\n\t" \
  "v_lshlrev_b32 v110, 16, v111\n\t" \
  "v_and_b32 v111, 0xffff0000, v111\n\t" \
  "v_pk_fma_f32 v[104:105], v[168:169], v[104:105], v[8:9]\n\t" \
  "v_pk_fma_f32 v[106:107], v[170:171], v[106:107], v[10:11]\n\t" \
  "v_pk_fma_f32 v[108:109], v[172:173], v[108:109], v[12:13]\n\t" \
  "v_pk_fma_f32 v[110:111], v[174:175], v[110:111], v[14:15]\n\t" \
  "v_cvt_pk_bf16_f32 v132, v104, v105\n\t" \
  "v_cvt_pk_bf16_f32 v133, v106, v107\n\t" \
  "v_cvt_pk_bf16_f32 v134, v108, v109\n\t" \
  "v_cvt_pk_bf16_f32 v135, v110, v111\n\t" \
  "v_pk_fma_f32 v[96:97], v[16:17], v[68:69], v[70:71] op_sel:[0,1,1] op_sel_hi:[1,1,1]\n\t" \
  "v_pk_fma_f32 v[98:99], v[18:19], v[68:69], v[70:71] op_sel:[0,1,1] op_sel_hi:[1,1,1]\n\t" \
  "v_pk_fma_f32 v[100:101], v[20:21], v[68:69], v[70:71] op_sel:[0,1,1] op_sel_hi:[1,1,1]\n\t" \
  "v_pk_fma_f32 v[102:103], v[22:23], v[68:69], v[70:71] op_sel:[0,1,1] op_sel_hi:[1,1,1]\n\t" \
  "v_pk_fma_f32 v[96:97], v[96:97], v[152:153], v[144:145]\n\t" \
  "v_pk_fma_f32 v[98:99], v[98:99], v[154:155], v[146:147]\n\t" \
  "v_pk_fma_f32 v[100:101], v[100:101], v[156:157], v[148:149]\n\t" \
  "v_pk_fma_f32 v[102:103], v[102:103], v[158:159], v[150:151]\n\t" \
  "v_cvt_pk_bf16_f32 v97, v96, v97\n\t" \
  "v_cvt_pk_bf16_f32 v99, v98, v99\n\t" \
  "v_cvt_pk_bf16_f32 v101, v100, v101\n\t" \
  "v_cvt_pk_bf16_f32 v103, v102, v103\n\t" \
  "v_lshlrev_b32 v96, 16, v97\n\t" \
  "v_and_b32 v97, 0xffff0000, v97\n\t" \
  "v_lshlrev_b32 v98, 16, v99\n\t" \
  "v_and_b32 v99, 0xffff0000, v99\n\t" \
  "v_lshlrev_b32 v100, 16, v101\n\t" \
  "v_and_b32 v101, 0xffff0000, v101\n\t" \
  "v_lshlrev_b32 v102, 16, v103\n\t" \
  "v_and_b32 v103, 0xffff0000, v103\n\t" \
  "v_pk_fma_f32 v[96:97], v[168:169], v[96:97], v[16:17]\n\t" \
  "v_pk_fma_f32 v[98:99], v[170:171], v[98:99], v[18:19]\n\t" \
  "v_pk_fma_f32 v[100:101], v[172:173], v[100:101], v[20:21]\n\t" \
  "v_pk_fma_f32 v[102:103], v[174:175], v[102:103], v[22:23]\n\t" \
  "v_cvt_pk_bf16_f32 v136, v96, v97\n\t" \
  "v_cvt_pk_bf16_f32 v137, v98, v99\n\t" \
  "v_cvt_pk_bf16_f32 v138, v100, v101\n\t" \
  "v_cvt_pk_bf16_f32 v139, v102, v103\n\t" \
  "v_pk_fma_f32 v[104:105], v[24:25], v[160:161], v[162:163] op_sel:[0,1,1] op_sel_hi:[1,1,1]\n\t" \
  "v_pk_fma_f32 v[106:107], v[26:27], v[160:161], v[162:163] op_sel:[0,1,1] op_sel_hi:[1,1,1]\n\t" \
  "v_pk_fma_f32 v[108:109], v[28:29], v[160:161], v[162:163] op_sel:[0,1,1] op_sel_hi:[1,1,1]\n\t" \
  "v_pk_fma_f32 v[110:111], v[30:31], v[160:161], v[162:163] op_sel:[0,1,1] op_sel_hi:[1,1,1]\n\t" \
  "v_pk_fma_f32 v[104:105], v[104:105], v[152:153], v[144:145]\n\t" \
  "v_pk_fma_f32 v[106:107], v[106:107], v[154:155], v[146:147]\n\t" \
  "v_pk_fma_f32 v[108:109], v[108:109], v[156:157], v[148:149]\n\t" \
  "v_pk_fma_f32 v[110:111], v[110:111], v[158:159], v[150:151]\n\t" \
  "v_cvt_pk_bf16_f32 v105, v104, v105\n\t" \
  "v_cvt_pk_bf16_f32 v107, v106, v107\n\t" \
  "v_cvt_pk_bf16_f32 v109, v108, v109\n\t" \
  "v_cvt_pk_bf16_f32 v111, v110, v111\n\t" \
  "v_lshlrev_b32 v104, 16, v105\n\t" \
  "v_and_b32 v105, 0xffff0000, v105\n\t" \
  "v_lshlrev_b32 v106, 16, v107\n\t" \
  "v_and_b32 v107, 0xffff0000, v107\n\t" \
  "v_lshlrev_b32 v108, 16, v109\n\t" \
  "v_and_b32 v109, 0xffff0000, v109\n\t" \
  "v_lshlrev_b32 v110, 16, v111\n\t" \
  "v_and_b32 v111, 0xffff0000, v111\n\t" \
  "v_pk_fma_f32 v[104:105], v[168:169], v[104:105], v[24:25]\n\t" \
  "v_pk_fma_f32 v[106:107], v[170:171], v[106:107], v[26:27]\n\t" \
  "v_pk_fma_f32 v[108:109], v[172:173], v[108:109], v[28:29]\n\t" \
  "v_pk_fma_f32 v[110:111], v[174:175], v[110:111], v[30:31]\n\t" \
  "v_cvt_pk_bf16_f32 v140, v104, v105\n\t" \
  "v_cvt_pk_bf16_f32 v141, v106, v107\n\t" \
  "v_cvt_pk_bf16_f32 v142, v108, v109\n\t" \
  "v_cvt_pk_bf16_f32 v143, v110, v111\n\t" \
  "v_permlane16_swap_b32 v128, v130\n\t" \
  "v_permlane16_swap_b32 v129, v131\n\t" \
  "v_permlane16_swap_b32 v132, v134\n\t" \
  "v_permlane16_swap_b32 v133, v135\n\t" \
  "v_permlane16_swap_b32 v136, v138\n\t" \
  "v_permlane16_swap_b32 v137, v139\n\t" \
  "v_permlane16_swap_b32 v140, v142\n\t" \
  "v_permlane16_swap_b32 v141, v143\n\t" \
  STP("s_memrealtime s[94:95]\n\t") \
  STP("s_memtime s[92:93]\n\t") \
  STP("s_waitcnt lgkmcnt(0)\n\t") \
  STP("v_writelane_b32 v255, s94, 46\n\t") \
  STP("v_writelane_b32 v255, s92, 47\n\t") \
  "s_mov_b32 s98, 0\n\t" \
  "s_mov_b32 s81, 0\n\t" \
  "s_lshl_b32 s99, 1, s8\n\t" \
  "s_cmp_eq_u32 s49, 0\n\t" \
  "s_cbranch_scc1 .Ltl_w2\n\t" \
  "s_waitcnt vmcnt(6)\n\t" \
  "s_branch .Ltl_t2\n\t" \
  ".Ltl_s2:\n\t" \
  "s_bcnt1_i32_b32 s54, s54\n\t" \
  "s_cmp_ge_u32 s54, 24\n\t" \
  "s_cselect_b32 s54, 1, s98\n\t" \
  "s_cmp_lg_u32 s54, 0\n\t" \
  "s_cbranch_scc1 .Ltl_q2\n\t" \
  "s_mov_b32 s98, 1\n\t" \
  "s_mov_b64 s[96:97], -1\n\t" \
  "s_cmp_eq_u32 s49, 0\n\t" \
  "s_cselect_b64 s[52:53], 0, s[50:51]\n\t" \
  "s_not_b64 s[52:53], s[52:53]\n\t" \
  "s_cmp_eq_u32 s48, 0\n\t" \
  "s_cselect_b64 exec, s[52:53], s[96:97]\n\t" \
  "s_add_u32 s44, s22, 0\n\t" \
  "buffer_store_dwordx4 v[128:131], v231, s[4:7], s44 offen sc0 sc1\n\t" \
  "s_cmp_eq_u32 s48, 1\n\t" \
  "s_cselect_b64 exec, s[52:53], s[96:97]\n\t" \
  "s_add_u32 s44, s22, 32768\n\t" \
  "buffer_store_dwordx4 v[132:135], v231, s[4:7], s44 offen sc0 sc1\n\t" \
  "s_cmp_eq_u32 s48, 2\n\t" \
  "s_cselect_b64 exec, s[52:53], s[96:97]\n\t" \
  "s_add_u32 s44, s22, 65536\n\t" \
  "buffer_store_dwordx4 v[136:139], v231, s[4:7], s44 offen sc0 sc1\n\t" \
  "s_cmp_eq_u32 s48, 3\n\t" \
  "s_cselect_b64 exec, s[52:53], s[96:97]\n\t" \
  "s_add_u32 s44, s22, 98304\n\t" \
  "buffer_store_dwordx4 v[140:143], v231, s[4:7], s44 offen sc0 sc1\n\t" \
  "s_mov_b64 exec, -1\n\t" \
  ".Ltl_q2:\n\t" \
  "s_add_u32 s81, s81, 1\n\t" \
  "s_cmp_lt_u32 s81, " FQ_CAP "\n\t" \
  "s_cbranch_scc0 .Ltl_g2\n\t" \
  "s_sleep 1\n\t" \
  "s_not_b32 s42, s45\n\t" \
  "s_bfe_u32 s47, s42, 0x50006\n\t" \
  "s_lshl_b32 s43, s42, 7\n\t" \
  "s_and_b32 s43, s43, 0x3c000\n\t" \
  "s_lshl_b32 s42, s42, 6\n\t" \
  "s_and_b32 s42, s42, 0x1000\n\t" \
  "s_add_u32 s42, s42, s43\n\t" \
  "s_add_u32 s42, s42, s15\n\t" \
  "buffer_load_dwordx2 v[248:249], v225, s[16:19], s13 offen sc1\n\t" \
  "buffer_load_dwordx4 v[72:75], v226, s[16:19], s42 offen offset:0 sc1\n\t" \
  "buffer_load_dwordx4 v[76:79], v226, s[16:19], s42 offen offset:16 sc1\n\t" \
  ".Ltl_w2:\n\t" \
  "s_waitcnt vmcnt(0)\n\t" \
  ".Ltl_t2:\n\t" \
  "v_cmp_eq_u32 vcc, s36, v249\n\t" \
  "v_xor_b32 v65, s36, v73\n\t" \
  "v_bitop3_b32 v65, v65, v75, s36 bitop3:0xf6\n\t" \
  "v_cndmask_b32 v64, 0, v248, vcc\n\t" \
  "s_or_b32 s54, vcc_lo, s99\n\t" \
  "s_nop 0\n\t" \
  "v_max_u32_dpp v64, v64, v64 quad_perm:[1,0,3,2] row_mask:0xf bank_mask:0xf\n\t" \
  "v_bitop3_b32 v65, v65, v77, s36 bitop3:0xf6\n\t" \
  "v_bitop3_b32 v65, v65, v79, s36 bitop3:0xf6\n\t" \
  "v_max_u32_dpp v64, v64, v64 quad_perm:[2,3,0,1] row_mask:0xf bank_mask:0xf\n\t" \
  "v_cmp_ne_u32 vcc, 0, v65\n\t" \
  "s_nop 0\n\t" \
  "v_max_u32_dpp v64, v64, v64 row_half_mirror row_mask:0xf bank_mask:0xf\n\t" \
  "s_nop 1\n\t" \
  "v_max_u32_dpp v64, v64, v64 row_mirror row_mask:0xf bank_mask:0xf\n\t" \
  "s_nop 0\n\t" \
  "v_readlane_b32 s45, v64, 0\n\t" \
  "v_readlane_b32 s42, v64, 16\n\t" \
  "s_max_u32 s45, s45, s42\n\t" \
  "s_max_u32 s45, s45, s46\n\t" \
  "s_not_b32 s42, s45\n\t" \
  "s_bfe_u32 s42, s42, 0x50006\n\t" \
  "s_xor_b32 s42, s42, s47\n\t" \
  "s_orn2_b32 s42, s42, s54\n\t" \
  "s_or_b32 s42, s42, vcc_lo\n\t" \
  "s_or_b32 s42, s42, vcc_hi\n\t" \
  "s_cbranch_scc1 .Ltl_s2\n\t" \
  ".Ltl_k2:\n\t" \
  STP("s_memrealtime s[94:95]\n\t") \
  STP("s_memtime s[92:93]\n\t") \
  STP("s_waitcnt lgkmcnt(0)\n\t") \
  STP("v_writelane_b32 v255, s94, 48\n\t") \
  STP("v_writelane_b32 v255, s92, 49\n\t") \
  "s_cmp_lt_i32 s57, 0\n\t" \
  "s_cbranch_scc1 .Ltl_nf\n\t" \
  "v_dot2_f32_bf16 v96, v80, v72, 0\n\t" \
  "v_dot2_f32_bf16 v97, v84, v72, 0\n\t" \
  "v_dot2_f32_bf16 v98, v81, v72, 0\n\t" \
  "v_dot2_f32_bf16 v99, v85, v72, 0\n\t" \
  "v_dot2_f32_bf16 v100, v82, v72, 0\n\t" \
  "v_dot2_f32_bf16 v101, v86, v72, 0\n\t" \
  "v_dot2_f32_bf16 v102, v83, v72, 0\n\t" \
  "v_dot2_f32_bf16 v103, v87, v72, 0\n\t" \
  "v_dot2_f32_bf16 v104, v176, v72, 0\n\t" \
  "v_dot2_f32_bf16 v105, v180, v72, 0\n\t" \
  "v_dot2_f32_bf16 v106, v177, v72, 0\n\t" \
  "v_dot2_f32_bf16 v107, v181, v72, 0\n\t" \
  "v_dot2_f32_bf16 v108, v178, v72, 0\n\t" \
  "v_dot2_f32_bf16 v109, v182, v72, 0\n\t" \
  "v_dot2_f32_bf16 v110, v179, v72, 0\n\t" \
  "v_dot2_f32_bf16 v111, v183, v72, 0\n\t" \
  "v_dot2_f32_bf16 v96, v88, v74, v96\n\t" \
  "v_dot2_f32_bf16 v97, v92, v74, v97\n\t" \
  "v_dot2_f32_bf16 v98, v89, v74, v98\n\t" \
  "v_dot2_f32_bf16 v99, v93, v74, v99\n\t" \
  "v_dot2_f32_bf16 v100, v90, v74, v100\n\t" \
  "v_dot2_f32_bf16 v101, v94, v74, v101\n\t" \
  "v_dot2_f32_bf16 v102, v91, v74, v102\n\t" \
  "v_dot2_f32_bf16 v103, v95, v74, v103\n\t" \
  "v_dot2_f32_bf16 v104, v184, v74, v104\n\t" \
  "v_dot2_f32_bf16 v105, v188, v74, v105\n\t" \
  "v_dot2_f32_bf16 v106, v185, v74, v106\n\t" \
  "v_dot2_f32_bf16 v107, v189, v74, v107\n\t" \
  "v_dot2_f32_bf16 v108, v186, v74, v108\n\t" \
  "v_dot2_f32_bf16 v109, v190, v74, v109\n\t" \
  "v_dot2_f32_bf16 v110, v187, v74, v110\n\t" \
  "v_dot2_f32_bf16 v111, v191, v74, v111\n\t" \
  "v_dot2_f32_bf16 v96, v208, v76, v96\n\t" \
  "v_dot2_f32_bf16 v97, v212, v76, v97\n\t" \
  "v_dot2_f32_bf16 v98, v209, v76, v98\n\t" \
  "v_dot2_f32_bf16 v99, v213, v76, v99\n\t" \
  "v_dot2_f32_bf16 v100, v210, v76, v100\n\t" \
  "v_dot2_f32_bf16 v101, v214, v76, v101\n\t" \
  "v_dot2_f32_bf16 v102, v211, v76, v102\n\t" \
  "v_dot2_f32_bf16 v103, v215, v76, v103\n\t" \
  "v_dot2_f32_bf16 v104, v192, v76, v104\n\t" \
  "v_dot2_f32_bf16 v105, v196, v76, v105\n\t" \
  "v_dot2_f32_bf16 v106, v193, v76, v106\n\t" \
  "v_dot2_f32_bf16 v107, v197, v76, v107\n\t" \
  "v_dot2_f32_bf16 v108, v194, v76, v108\n\t" \
  "v_dot2_f32_bf16 v109, v198, v76, v109\n\t" \
  "v_dot2_f32_bf16 v110, v195, v76, v110\n\t" \
  "v_dot2_f32_bf16 v111, v199, v76, v111\n\t" \
  "v_dot2_f32_bf16 v96, v216, v78, v96\n\t" \
  "v_dot2_f32_bf16 v97, v220, v78, v97\n\t" \
  "v_dot2_f32_bf16 v98, v217, v78, v98\n\t" \
  "v_dot2_f32_bf16 v99, v221, v78, v99\n\t" \
  "v_dot2_f32_bf16 v100, v218, v78, v100\n\t" \
  "v_dot2_f32_bf16 v101, v222, v78, v101\n\t" \
  "v_dot2_f32_bf16 v102, v219, v78, v102\n\t" \
  "v_dot2_f32_bf16 v103, v223, v78, v103\n\t" \
  "v_dot2_f32_bf16 v104, v200, v78, v104\n\t" \
  "v_dot2_f32_bf16 v105, v204, v78, v105\n\t" \
  "v_dot2_f32_bf16 v106, v201, v78, v106\n\t" \
  "v_dot2_f32_bf16 v107, v205, v78, v107\n\t" \
  "v_dot2_f32_bf16 v108, v202, v78, v108\n\t" \
  "v_dot2_f32_bf16 v109, v206, v78, v109\n\t" \
  "v_dot2_f32_bf16 v110, v203, v78, v110\n\t" \
  "v_dot2_f32_bf16 v111, v207, v78, v111\n\t" \
  "v_permlane32_swap_b32 v96, v104\n\t" \
  "v_permlane32_swap_b32 v97, v105\n\t" \
  "v_permlane32_swap_b32 v98, v106\n\t" \
  "v_permlane32_swap_b32 v99, v107\n\t" \
  "v_permlane32_swap_b32 v100, v108\n\t" \
  "v_permlane32_swap_b32 v101, v109\n\t" \
  "v_permlane32_swap_b32 v102, v110\n\t" \
  "v_permlane32_swap_b32 v103, v111\n\t" \
  "v_add_f32 v96, v96, v104\n\t" \
  "v_add_f32 v97, v97, v105\n\t" \
  "v_add_f32 v98, v98, v106\n\t" \
  "v_add_f32 v99, v99, v107\n\t" \
  "v_add_f32 v100, v100, v108\n\t" \
  "v_add_f32 v101, v101, v109\n\t" \
  "v_add_f32 v102, v102, v110\n\t" \
  "v_add_f32 v103, v103, v111\n\t" \
  "v_permlane16_swap_b32 v96, v100\n\t" \
  "v_permlane16_swap_b32 v97, v101\n\t" \
  "v_permlane16_swap_b32 v98, v102\n\t" \
  "v_permlane16_swap_b32 v99, v103\n\t" \
  "v_add_f32 v96, v96, v100\n\t" \
  "v_add_f32 v97, v97, v101\n\t" \
  "v_add_f32 v98, v98, v102\n\t" \
  "v_add_f32 v99, v99, v103\n\t" \
  "v_add_f32_dpp v96, v96, v96 row_ror:8 row_mask:0xf bank_mask:0xf\n\t" \
  "v_add_f32_dpp v97, v97, v97 row_ror:8 row_mask:0xf bank_mask:0xf\n\t" \
  "v_add_f32_dpp v98, v98, v98 row_ror:8 row_mask:0xf bank_mask:0xf\n\t" \
  "v_add_f32_dpp v99, v99, v99 row_ror:8 row_mask:0xf bank_mask:0xf\n\t" \
  "v_add_f32_dpp v96, v96, v96 row_ror:4 row_mask:0xf bank_mask:0xf\n\t" \
  "v_add_f32_dpp v97, v97, v97 row_ror:4 row_mask:0xf bank_mask:0xf\n\t" \
  "v_add_f32_dpp v98, v98, v98 row_ror:4 row_mask:0xf bank_mask:0xf\n\t" \
  "v_add_f32_dpp v99, v99, v99 row_ror:4 row_mask:0xf bank_mask:0xf\n\t" \
  "v_add_f32_dpp v96, v96, v96 row_ror:2 row_mask:0xf bank_mask:0xf\n\t" \
  "v_add_f32_dpp v97, v97, v97 row_ror:2 row_mask:0xf bank_mask:0xf\n\t" \
  "v_add_f32_dpp v98, v98, v98 row_ror:2 row_mask:0xf bank_mask:0xf\n\t" \
  "v_add_f32_dpp v99, v99, v99 row_ror:2 row_mask:0xf bank_mask:0xf\n\t" \
  "s_mov_b32 exec_lo, 0x00030003\n\t" \
  "s_mov_b32 exec_hi, 0x00030003\n\t" \
  "ds_write_b128 v253, v[96:99] offset:8192\n\t" \
  "s_mov_b64 exec, -1\n\t" \
  "v_mov_b32 v101, s36\n\t" \
  "v_mov_b32 v103, s36\n\t" \
  "s_waitcnt lgkmcnt(0)\n\t" \
  "s_barrier\n\t" \
  "v_add_u32 v104, 8192, v245\n\t" \
  "ds_read2_b32 v[96:97], v104 offset1:64\n\t" \
  "ds_read2_b32 v[98:99], v104 offset0:32 offset1:96\n\t" \
  "s_waitcnt lgkmcnt(0)\n\t" \
  "v_pk_add_f32 v[96:97], v[96:97], v[98:99]\n\t" \
  "v_add_f32 v96, v96, v97\n\t" \
  "v_cvt_pk_bf16_f32 v96, v96, v96\n\t" \
  "v_lshlrev_b32 v96, 16, v96\n\t" \
  "v_mov_b32 v97, v96\n\t" \
  "s_nop 1\n\t" \
  "v_permlane16_swap_b32 v96, v97\n\t" \
  "v_mul_f32 v98, 0xbfb8aa3b, v96\n\t" \
  "v_exp_f32 v98, v98\n\t" \
  "s_nop 0\n\t" \
  "v_add_f32 v98, 1.0, v98\n\t" \
  "v_rcp_f32 v98, v98\n\t" \
  "s_nop 0\n\t" \
  "v_mul_f32 v96, v98, v96\n\t" \
  "v_cvt_pk_bf16_f32 v96, v96, v96\n\t" \
  "v_lshlrev_b32 v96, 16, v96\n\t" \
  "v_mul_f32 v96, v97, v96\n\t" \
  "s_nop 1\n\t" \
  "v_mov_b32_dpp v97, v96 quad_perm:[1,0,3,2] row_mask:0xf bank_mask:0xf\n\t" \
  "v_cvt_pk_bf16_f32 v96, v96, v97\n\t" \
  "s_nop 0\n\t" \
  "v_readlane_b32 s60, v96, 0\n\t" \
  "v_readlane_b32 s61, v96, 2\n\t" \
  "v_readlane_b32 s62, v96, 4\n\t" \
  "v_readlane_b32 s63, v96, 6\n\t" \
  "v_readlane_b32 s64, v96, 8\n\t" \
  "v_readlane_b32 s65, v96, 10\n\t" \
  "v_readlane_b32 s66, v96, 12\n\t" \
  "v_readlane_b32 s67, v96, 14\n\t" \
  "v_dot2_f32_bf16 v104, v112, s60, 0\n\t" \
  "v_dot2_f32_bf16 v105, v34, s60, 0\n\t" \
  "v_dot2_f32_bf16 v106, v113, s60, 0\n\t" \
  "v_dot2_f32_bf16 v107, v35, s60, 0\n\t" \
  "v_dot2_f32_bf16 v104, v114, s61, v104\n\t" \
  "v_dot2_f32_bf16 v105, v38, s61, v105\n\t" \
  "v_dot2_f32_bf16 v106, v115, s61, v106\n\t" \
  "v_dot2_f32_bf16 v107, v39, s61, v107\n\t" \
  "v_dot2_f32_bf16 v104, v116, s62, v104\n\t" \
  "v_dot2_f32_bf16 v105, v42, s62, v105\n\t" \
  "v_dot2_f32_bf16 v106, v117, s62, v106\n\t" \
  "v_dot2_f32_bf16 v107, v43, s62, v107\n\t" \
  "v_dot2_f32_bf16 v104, v118, s63, v104\n\t" \
  "v_dot2_f32_bf16 v105, v46, s63, v105\n\t" \
  "v_dot2_f32_bf16 v106, v119, s63, v106\n\t" \
  "v_dot2_f32_bf16 v107, v47, s63, v107\n\t" \
  "v_dot2_f32_bf16 v104, v120, s64, v104\n\t" \
  "v_dot2_f32_bf16 v105, v50, s64, v105\n\t" \
  "v_dot2_f32_bf16 v106, v121, s64, v106\n\t" \
  "v_dot2_f32_bf16 v107, v51, s64, v107\n\t" \
  "v_dot2_f32_bf16 v104, v122, s65, v104\n\t" \
  "v_dot2_f32_bf16 v105, v54, s65, v105\n\t" \
  "v_dot2_f32_bf16 v106, v123, s65, v106\n\t" \
  "v_dot2_f32_bf16 v107, v55, s65, v107\n\t" \
  "v_dot2_f32_bf16 v104, v124, s66, v104\n\t" \
  "v_dot2_f32_bf16 v105, v58, s66, v105\n\t" \
  "v_dot2_f32_bf16 v106, v125, s66, v106\n\t" \
  "v_dot2_f32_bf16 v107, v59, s66, v107\n\t" \
  "v_dot2_f32_bf16 v104, v126, s67, v104\n\t" \
  "v_dot2_f32_bf16 v105, v62, s67, v105\n\t" \
  "v_dot2_f32_bf16 v106, v127, s67, v106\n\t" \
  "v_dot2_f32_bf16 v107, v63, s67, v107\n\t" \
  "s_nop 0\n\t" \
  "v_cvt_pk_bf16_f32 v100, v104, v105\n\t" \
  "s_nop 0\n\t" \
  "v_cvt_pk_bf16_f32 v102, v106, v107\n\t" \
  "buffer_store_dwordx4 v[100:103], v242, s[16:19], s23 offen sc1\n\t" \
  ".Ltl_nf:\n\t" \
  STP("s_memrealtime s[94:95]\n\t") \
  STP("s_memtime s[92:93]\n\t") \
  STP("s_waitcnt lgkmcnt(0)\n\t") \
  STP("v_writelane_b32 v255, s94, 50\n\t") \
  STP("v_writelane_b32 v255, s92, 51\n\t") \
  "s_cmp_eq_u32 s47, s8\n\t" \
  "s_cselect_b64 s[52:53], s[50:51], 0\n\t" \
  "s_cselect_b32 s54, 0, s49\n\t" \
  "s_not_b64 s[52:53], s[52:53]\n\t" \
  "s_mov_b64 s[96:97], -1\n\t" \
  "s_cmp_eq_u32 s98, 0\n\t" \
  "s_cbranch_scc1 .Ltl_so\n\t" \
  "s_cmp_eq_u32 s54, 0\n\t" \
  "s_cbranch_scc1 .Ltl_ns\n\t" \
  "s_mov_b64 s[52:53], s[50:51]\n\t" \
  "s_mov_b64 s[96:97], 0\n\t" \
  ".Ltl_so:\n\t" \
  "s_cmp_eq_u32 s48, 0\n\t" \
  "s_cselect_b64 exec, s[52:53], s[96:97]\n\t" \
  "s_add_u32 s44, s22, 0\n\t" \
  "buffer_store_dwordx4 v[128:131], v231, s[4:7], s44 offen sc0 sc1\n\t" \
  "s_cmp_eq_u32 s48, 1\n\t" \
  "s_cselect_b64 exec, s[52:53], s[96:97]\n\t" \
  "s_add_u32 s44, s22, 32768\n\t" \
  "buffer_store_dwordx4 v[132:135], v231, s[4:7], s44 offen sc0 sc1\n\t" \
  "s_cmp_eq_u32 s48, 2\n\t" \
  "s_cselect_b64 exec, s[52:53], s[96:97]\n\t" \
  "s_add_u32 s44, s22, 65536\n\t" \
  "buffer_store_dwordx4 v[136:139], v231, s[4:7], s44 offen sc0 sc1\n\t" \
  "s_cmp_eq_u32 s48, 3\n\t" \
  "s_cselect_b64 exec, s[52:53], s[96:97]\n\t" \
  "s_add_u32 s44, s22, 98304\n\t" \
  "buffer_store_dwordx4 v[140:143], v231, s[4:7], s44 offen sc0 sc1\n\t" \
  "s_mov_b64 exec, -1\n\t" \
  ".Ltl_ns:\n\t" \
  "s_or_b32 s42, s2, s3\n\t" \
  "s_cbranch_scc1 .Ltl_ne\n\t" \
  "s_mov_b32 s42, 2490368\n\t" \
  "buffer_store_dword v243, v244, s[16:19], s42 offen sc1\n\t" \
  ".Ltl_ne:\n\t" \
  "s_cmp_lt_i32 s57, 0\n\t" \
  "s_cbranch_scc0 .Ltl_end\n\t" \
  "s_and_b32 s44, s2, 3\n\t" \
  "v_lshrrev_b32 v4, 4, v224\n\t" \
  "v_lshrrev_b32 v0, 5, v4\n\t" \
  "v_lshlrev_b32 v0, 14, v0\n\t" \
  "v_bfe_u32 v5, v4, 3, 2\n\t" \
  "v_lshl_add_u32 v0, v5, 11, v0\n\t" \
  "v_and_b32 v5, 0x70, v242\n\t" \
  "v_add_u32 v0, v0, v5\n\t" \
  "v_and_b32 v1, 15, v233\n\t" \
  "v_lshlrev_b32 v1, 3, v1\n\t" \
  "s_lshl_b32 s42, s44, 12\n\t" \
  "s_add_u32 s42, s42, 150400\n\t" \
  "v_add_u32 v6, s42, v1\n\t" \
  "ds_read_b64 v[6:7], v6\n\t" \
  "v_lshrrev_b32 v3, 2, v228\n\t" \
  "v_add_u32 v3, v3, v5\n\t" \
  "v_lshl_add_u32 v3, s3, 7, v3\n\t" \
  "s_and_b32 s42, s2, 0x38\n\t" \
  "s_lshl_b32 s42, s42, 6\n\t" \
  "s_lshl_b32 s44, s44, 7\n\t" \
  "s_add_u32 s44, s44, s42\n\t" \
  "s_and_b32 s60, s44, 0x7ff\n\t" \
  "s_add_u32 s61, s60, s9\n\t" \
  "s_add_u32 s62, s61, 1048576\n\t" \
  "s_add_u32 s63, s62, 131072\n\t" \
  "s_add_u32 s64, s62, 262144\n\t" \
  "s_add_u32 s65, s62, 393216\n\t" \
  "s_add_u32 s66, s62, 524288\n\t" \
  "s_add_u32 s67, s62, 655360\n\t" \
  "s_add_u32 s68, s62, 786432\n\t" \
  "s_not_b32 s42, s45\n\t" \
  "s_bfe_u32 s47, s42, 0x50006\n\t" \
  "s_lshl_b32 s43, s42, 7\n\t" \
  "s_and_b32 s43, s43, 0x3c000\n\t" \
  "s_lshl_b32 s42, s42, 6\n\t" \
  "s_and_b32 s42, s42, 0x1000\n\t" \
  "s_add_u32 s42, s42, s43\n\t" \
  "s_add_u32 s42, s42, s15\n\t" \
  "s_add_u32 s69, s42, s44\n\t" \
  "s_lshl_b32 s42, s47, 12\n\t" \
  "s_and_b32 s42, s42, 0x1c000\n\t" \
  "s_add_u32 s70, s61, s42\n\t" \
  "s_and_b32 s42, s47, 3\n\t" \
  "s_lshl_b32 s42, s42, 11\n\t" \
  "s_add_u32 s70, s70, s42\n\t" \
  "s_add_u32 s70, s70, 1966080\n\t" \
  "s_not_b32 s42, s45\n\t" \
  "s_and_b32 s42, s42, 0x7ff\n\t" \
  "s_lshl_b32 s42, s42, 11\n\t" \
  "s_lshr_b32 s71, s44, 1\n\t" \
  "s_add_u32 s71, s71, s42\n\t" \
  "s_ashr_i32 s42, s45, 31\n\t" \
  "s_orn2_b32 s42, 0x80000000, s42\n\t" \
  "s_xor_b32 s42, s42, s45\n\t" \
  "s_and_b32 s42, s42, 0xffff0000\n\t" \
  "v_mov_b32 v2, s42\n\t" \
  "v_mul_f32 v2, 0xbfb8aa3b, v2\n\t" \
  "v_exp_f32 v2, v2\n\t" \
  "s_nop 0\n\t" \
  "v_add_f32 v2, 1.0, v2\n\t" \
  "v_rcp_f32 v2, v2\n\t" \
  "s_nop 0\n\t" \
  "v_cvt_pk_bf16_f32 v2, v2, v2\n\t" \
  "v_lshlrev_b32 v2, 16, v2\n\t" \
  "s_mov_b32 s81, 0\n\t" \
  "s_sleep 8\n\t" \
  ".Ltl_r3:\n\t" \
  "s_sleep 1\n\t" \
  "buffer_load_dwordx4 v[12:15], v0, s[16:19], s62 offen sc1\n\t" \
  "buffer_load_dwordx4 v[16:19], v0, s[16:19], s63 offen sc1\n\t" \
  "buffer_load_dwordx4 v[20:23], v0, s[16:19], s64 offen sc1\n\t" \
  "buffer_load_dwordx4 v[24:27], v0, s[16:19], s65 offen sc1\n\t" \
  "buffer_load_dwordx4 v[28:31], v0, s[16:19], s66 offen sc1\n\t" \
  "buffer_load_dwordx4 v[32:35], v0, s[16:19], s67 offen sc1\n\t" \
  "buffer_load_dwordx4 v[36:39], v0, s[16:19], s68 offen sc1\n\t" \
  "buffer_load_dwordx2 v[8:9], v1, s[16:19], s69 offen sc1\n\t" \
  "buffer_load_dwordx2 v[10:11], v1, s[16:19], s70 offen sc1\n\t" \
  "s_waitcnt vmcnt(0) lgkmcnt(0)\n\t" \
  "v_xor_b32 v4, s36, v9\n\t" \
  "v_bitop3_b32 v4, v4, v11, s36 bitop3:0xf6\n\t" \
  "v_bitop3_b32 v4, v4, v13, s36 bitop3:0xf6\n\t" \
  "v_bitop3_b32 v4, v4, v15, s36 bitop3:0xf6\n\t" \
  "v_bitop3_b32 v4, v4, v17, s36 bitop3:0xf6\n\t" \
  "v_bitop3_b32 v4, v4, v19, s36 bitop3:0xf6\n\t" \
  "v_bitop3_b32 v4, v4, v21, s36 bitop3:0xf6\n\t" \
  "v_bitop3_b32 v4, v4, v23, s36 bitop3:0xf6\n\t" \
  "v_bitop3_b32 v4, v4, v25, s36 bitop3:0xf6\n\t" \
  "v_bitop3_b32 v4, v4, v27, s36 bitop3:0xf6\n\t" \
  "v_bitop3_b32 v4, v4, v29, s36 bitop3:0xf6\n\t" \
  "v_bitop3_b32 v4, v4, v31, s36 bitop3:0xf6\n\t" \
  "v_bitop3_b32 v4, v4, v33, s36 bitop3:0xf6\n\t" \
  "v_bitop3_b32 v4, v4, v35, s36 bitop3:0xf6\n\t" \
  "v_bitop3_b32 v4, v4, v37, s36 bitop3:0xf6\n\t" \
  "v_bitop3_b32 v4, v4, v39, s36 bitop3:0xf6\n\t" \
  "v_cmp_ne_u32 vcc, 0, v4\n\t" \
  "v_lshlrev_b32 v40, 16, v12\n\t" \
  "v_and_b32 v41, 0xffff0000, v12\n\t" \
  "v_lshlrev_b32 v42, 16, v14\n\t" \
  "v_and_b32 v43, 0xffff0000, v14\n\t" \
  "v_lshlrev_b32 v44, 16, v16\n\t" \
  "v_and_b32 v45, 0xffff0000, v16\n\t" \
  "v_lshlrev_b32 v46, 16, v18\n\t" \
  "v_and_b32 v47, 0xffff0000, v18\n\t" \
  "s_cbranch_vccz .Ltl_k3\n\t" \
  "s_add_u32 s81, s81, 1\n\t" \
  "s_cmp_lt_u32 s81, " FQ_CAP "\n\t" \
  "s_cbranch_scc1 .Ltl_r3\n\t" \
  "buffer_store_dword v243, off, s[16:19], 0 offset:256 sc1\n\t" \
  ".Ltl_k3:\n\t" \
  "v_pk_add_f32 v[40:41], v[40:41], v[44:45]\n\t" \
  "v_pk_add_f32 v[42:43], v[42:43], v[46:47]\n\t" \
  "v_lshlrev_b32 v44, 16, v20\n\t" \
  "v_and_b32 v45, 0xffff0000, v20\n\t" \
  "v_lshlrev_b32 v46, 16, v22\n\t" \
  "v_and_b32 v47, 0xffff0000, v22\n\t" \
  "v_pk_add_f32 v[40:41], v[40:41], v[44:45]\n\t" \
  "v_pk_add_f32 v[42:43], v[42:43], v[46:47]\n\t" \
  "v_lshlrev_b32 v44, 16, v24\n\t" \
  "v_and_b32 v45, 0xffff0000, v24\n\t" \
  "v_lshlrev_b32 v46, 16, v26\n\t" \
  "v_and_b32 v47, 0xffff0000, v26\n\t" \
  "v_pk_add_f32 v[40:41], v[40:41], v[44:45]\n\t" \
  "v_pk_add_f32 v[42:43], v[42:43], v[46:47]\n\t" \
  "v_lshlrev_b32 v44, 16, v28\n\t" \
  "v_and_b32 v45, 0xffff0000, v28\n\t" \
  "v_lshlrev_b32 v46, 16, v30\n\t" \
  "v_and_b32 v47, 0xffff0000, v30\n\t" \
  "v_pk_add_f32 v[40:41], v[40:41], v[44:45]\n\t" \
  "v_pk_add_f32 v[42:43], v[42:43], v[46:47]\n\t" \
  "v_lshlrev_b32 v44, 16, v32\n\t" \
  "v_and_b32 v45, 0xffff0000, v32\n\t" \
  "v_lshlrev_b32 v46, 16, v34\n\t" \
  "v_and_b32 v47, 0xffff0000, v34\n\t" \
  "v_pk_add_f32 v[40:41], v[40:41], v[44:45]\n\t" \
  "v_pk_add_f32 v[42:43], v[42:43], v[46:47]\n\t" \
  "v_lshlrev_b32 v44, 16, v36\n\t" \
  "v_and_b32 v45, 0xffff0000, v36\n\t" \
  "v_lshlrev_b32 v46, 16, v38\n\t" \
  "v_and_b32 v47, 0xffff0000, v38\n\t" \
  "v_pk_add_f32 v[40:41], v[40:41], v[44:45]\n\t" \
  "v_pk_add_f32 v[42:43], v[42:43], v[46:47]\n\t" \
  "s_nop 1\n\t" \
  "v_permlane32_swap_b32 v40, v42\n\t" \
  "v_permlane32_swap_b32 v41, v43\n\t" \
  "v_add_f32 v40, v40, v42\n\t" \
  "v_add_f32 v41, v41, v43\n\t" \
  "s_nop 1\n\t" \
  "v_permlane16_swap_b32 v40, v41\n\t" \
  "v_add_f32 v40, v40, v41\n\t" \
  "s_nop 1\n\t" \
  "v_add_f32_dpp v40, v40, v40 row_ror:8 row_mask:0xf bank_mask:0xf\n\t" \
  "ds_write_b32 v3, v40 offset:12288\n\t" \
  "v_lshlrev_b32 v44, 16, v8\n\t" \
  "v_and_b32 v45, 0xffff0000, v8\n\t" \
  "v_lshlrev_b32 v46, 16, v10\n\t" \
  "v_and_b32 v47, 0xffff0000, v10\n\t" \
  "s_waitcnt lgkmcnt(0)\n\t" \
  "s_barrier\n\t" \
  "s_cmp_lg_u32 s3, 0\n\t" \
  "s_cbranch_scc1 .Ltl_end\n\t" \
  "v_add_u32 v4, 12288, v1\n\t" \
  "ds_read2_b64 v[12:15], v4 offset1:16\n\t" \
  "ds_read2_b64 v[16:19], v4 offset0:32 offset1:48\n\t" \
  "v_lshrrev_b32 v1, 1, v1\n\t" \
  "s_waitcnt lgkmcnt(0)\n\t" \
  "v_pk_add_f32 v[12:13], v[12:13], v[14:15]\n\t" \
  "v_pk_add_f32 v[16:17], v[16:17], v[18:19]\n\t" \
  "v_pk_add_f32 v[12:13], v[12:13], v[16:17]\n\t" \
  "v_cvt_pk_bf16_f32 v13, v12, v13\n\t" \
  "v_lshlrev_b32 v12, 16, v13\n\t" \
  "v_and_b32 v13, 0xffff0000, v13\n\t" \
  "v_mul_f32 v12, v12, v2\n\t" \
  "v_mul_f32 v13, v13, v2\n\t" \
  "v_cvt_pk_bf16_f32 v13, v12, v13\n\t" \
  "v_lshlrev_b32 v12, 16, v13\n\t" \
  "v_and_b32 v13, 0xffff0000, v13\n\t" \
  "v_pk_add_f32 v[12:13], v[12:13], v[44:45]\n\t" \
  "v_cvt_pk_bf16_f32 v13, v12, v13\n\t" \
  "v_lshlrev_b32 v12, 16, v13\n\t" \
  "v_and_b32 v13, 0xffff0000, v13\n\t" \
  "v_pk_mul_f32 v[12:13], v[12:13], v[6:7]\n\t" \
  "v_cvt_pk_bf16_f32 v13, v12, v13\n\t" \
  "v_lshlrev_b32 v12, 16, v13\n\t" \
  "v_and_b32 v13, 0xffff0000, v13\n\t" \
  "v_pk_add_f32 v[12:13], v[12:13], v[46:47]\n\t" \
  "v_cvt_pk_bf16_f32 v12, v12, v13\n\t" \
  "s_mov_b64 exec, 0xffff\n\t" \
  "buffer_store_dword v12, v1, s[4:7], s71 offen sc0 sc1\n\t" \
  "s_mov_b64 exec, -1\n\t" \
  ".Ltl_end:\n\t" \
  STP("s_memrealtime s[94:95]\n\t") \
  STP("s_memtime s[92:93]\n\t") \
  STP("s_waitcnt lgkmcnt(0)\n\t") \
  STP("v_writelane_b32 v255, s94, 52\n\t") \
  STP("v_writelane_b32 v255, s92, 53\n\t") \
  ".subsection 1\n\t" \
  ".Lat_x:\n\t" \
  "v_lshlrev_b32 v209, 12, v233\n\t" \
  "v_add_u32 v209, 0, v209\n\t" \
  "s_mov_b64 exec, 15\n\t" \
  "ds_write_b32 v209, v243\n\t" \
  "s_mov_b64 exec, -1\n\t" \
  "s_waitcnt lgkmcnt(0)\n\t" \
  "s_branch .Lat_y\n\t" \
  ".Lat_rd:\n\t" \
  "s_waitcnt vmcnt(0) lgkmcnt(0)\n\t" \
  "s_barrier\n\t" \
  "s_nop 7\n\t" \
  "v_lshl_add_u32 v0, s3, 6, v233\n\t" \
  "s_mov_b32 s37, 0\n\t" \
  "s_branch .Lfr_again\n\t" \
  ".Lat_s0:\n\t" \
  "s_mov_b32 s81, 0\n\t" \
  ".Lat_r0:\n\t" \
  "s_sleep 4\n\t" \
  "buffer_load_dword v246, v245, s[16:19], s65 offen sc1\n\t" \
  "s_waitcnt vmcnt(0)\n\t" \
  "v_cmp_ne_u32 vcc, s36, v246\n\t" \
  "s_and_b32 vcc_lo, vcc_lo, 0xe\n\t" \
  "s_cbranch_scc0 .Lat_k0\n\t" \
  "s_add_u32 s81, s81, 1\n\t" \
  "s_cmp_lt_u32 s81, " FQ_CAP "\n\t" \
  "s_cbranch_scc1 .Lat_r0\n\t" \
  "buffer_store_dword v243, off, s[16:19], 0 offset:256 sc1\n\t" \
  "s_branch .Lat_k0\n\t" \
  ".Lat_s1:\n\t" \
  "s_mov_b32 s81, 0\n\t" \
  ".Lat_r1:\n\t" \
  "s_sleep 4\n\t" \
  "buffer_load_dword v246, v245, s[16:19], s65 offen sc1\n\t" \
  "s_waitcnt vmcnt(0)\n\t" \
  "v_cmp_ne_u32 vcc, s36, v246\n\t" \
  "s_and_b32 vcc_lo, vcc_lo, 0xfefe\n\t" \
  "s_cbranch_scc0 .Lat_k1\n\t" \
  "s_add_u32 s81, s81, 1\n\t" \
  "s_cmp_lt_u32 s81, " FQ_CAP "\n\t" \
  "s_cbranch_scc1 .Lat_r1\n\t" \
  "buffer_store_dword v243, off, s[16:19], 0 offset:256 sc1\n\t" \
  "s_branch .Lat_k1\n\t" \
  ".Lat_s2:\n\t" \
  "s_mov_b32 s81, 0\n\t" \
  ".Lat_r2:\n\t" \
  "s_sleep 4\n\t" \
  "buffer_load_dword v246, v245, s[16:19], s65 offen sc1\n\t" \
  "s_waitcnt vmcnt(0)\n\t" \
  "v_cmp_ne_u32 vcc, s36, v246\n\t" \
  "s_and_b32 vcc_lo, vcc_lo, 0xfe0000\n\t" \
  "s_cbranch_scc0 .Lat_k2\n\t" \
  "s_add_u32 s81, s81, 1\n\t" \
  "s_cmp_lt_u32 s81, " FQ_CAP "\n\t" \
  "s_cbranch_scc1 .Lat_r2\n\t" \
  "buffer_store_dword v243, off, s[16:19], 0 offset:256 sc1\n\t" \
  "s_branch .Lat_k2\n\t" \
  ".Lat_s3:\n\t" \
  "s_mov_b32 s81, 0\n\t" \
  ".Lat_r3:\n\t" \
  "s_sleep 4\n\t" \
  "s_add_u32 s78, s75, 0\n\t" \
  "buffer_load_dwordx4 v[208:211], v234, s[16:19], s78 offen sc1\n\t" \
  "s_add_u32 s78, s75, 32768\n\t" \
  "buffer_load_dwordx4 v[212:215], v234, s[16:19], s78 offen sc1\n\t" \
  "s_add_u32 s78, s75, 65536\n\t" \
  "buffer_load_dwordx4 v[216:219], v234, s[16:19], s78 offen sc1\n\t" \
  "s_add_u32 s78, s75, 98304\n\t" \
  "buffer_load_dwordx4 v[220:223], v234, s[16:19], s78 offen sc1\n\t" \
  "s_waitcnt vmcnt(0)\n\t" \
  "v_xor_b32 v246, v243, v209\n\t" \
  "v_bitop3_b32 v246, v246, v211, v243 bitop3:0xf6\n\t" \
  "v_bitop3_b32 v246, v246, v213, v243 bitop3:0xf6\n\t" \
  "v_bitop3_b32 v246, v246, v215, v243 bitop3:0xf6\n\t" \
  "v_bitop3_b32 v246, v246, v217, v243 bitop3:0xf6\n\t" \
  "v_bitop3_b32 v246, v246, v219, v243 bitop3:0xf6\n\t" \
  "v_bitop3_b32 v246, v246, v221, v243 bitop3:0xf6\n\t" \
  "v_bitop3_b32 v246, v246, v223, v243 bitop3:0xf6\n\t" \
  "v_cmp_ne_u32 vcc, 0, v246\n\t" \
  "s_cbranch_vccz .Lat_k3\n\t" \
  "s_add_u32 s81, s81, 1\n\t" \
  "s_cmp_lt_u32 s81, " FQ_CAP "\n\t" \
  "s_cbranch_scc1 .Lat_r3\n\t" \
  "buffer_store_dword v243, off, s[16:19], 0 offset:256 sc1\n\t" \
  "s_branch .Lat_k3\n\t" \
  ".Lat_s4:\n\t" \
  "s_mov_b32 s81, 0\n\t" \
  ".Lat_r4:\n\t" \
  "s_sleep 4\n\t" \
  "buffer_load_dword v246, v235, s[16:19], s73 offen sc1\n\t" \
  "s_waitcnt vmcnt(0)\n\t" \
  "v_cmp_ne_u32 vcc, s36, v246\n\t" \
  "s_cbranch_vccz .Lat_k4\n\t" \
  "s_add_u32 s81, s81, 1\n\t" \
  "s_cmp_lt_u32 s81, " FQ_CAP "\n\t" \
  "s_cbranch_scc1 .Lat_r4\n\t" \
  "buffer_store_dword v243, off, s[16:19], 0 offset:256 sc1\n\t" \
  "s_branch .Lat_k4\n\t" \
  ".Lat_m3:\n\t" \
  "s_waitcnt vmcnt(0)\n\t" \
  "v_cmp_ne_u32 vcc, s36, v251\n\t" \
  "s_mov_b32 s88, vcc_lo\n\t" \
  "s_cbranch_vccnz .Lat_n3\n\t" \
  "buffer_load_dwordx4 v[64:67], v242, s[16:19], s79 offen offset:0 sc1\n\t" \
  "buffer_load_dwordx4 v[68:71], v242, s[16:19], s79 offen offset:1024 sc1\n\t" \
  "buffer_load_dwordx4 v[72:75], v242, s[16:19], s79 offen offset:2048 sc1\n\t" \
  "buffer_load_dwordx4 v[76:79], v242, s[16:19], s79 offen offset:3072 sc1\n\t" \
  "s_branch .Lat_n3\n\t" \
  ".Lat_v:\n\t" \
  "s_mov_b32 s98, 0\n\t" \
  ".Lat_v1:\n\t" \
  "s_sleep 4\n\t" \
  "buffer_load_dword v209, v250, s[16:19], 0 offen sc1\n\t" \
  "s_waitcnt vmcnt(0)\n\t" \
  "v_and_b32 v251, 0xbfffbfff, v209\n\t" \
  "v_cmp_ne_u32_e64 s[84:85], s36, v251\n\t" \
  "s_cmp_lg_u64 s[84:85], 0\n\t" \
  "s_cbranch_scc0 .Lat_w\n\t" \
  "s_add_u32 s98, s98, 1\n\t" \
  "s_cmp_lt_u32 s98, " FQ_CAP "\n\t" \
  "s_cbranch_scc1 .Lat_v1\n\t" \
  "v_mov_b32 v251, s36\n\t" \
  "buffer_store_dword v251, off, s[16:19], 0 offset:256 sc1\n\t" \
  "s_branch .Lat_w\n\t" \
  ".Lat_s99:\n\t" \
  "s_mov_b32 s81, 0\n\t" \
  ".Lat_r99:\n\t" \
  "buffer_load_dword v246, off, s[16:19], s73 offset:32 sc1\n\t" \
  "s_waitcnt vmcnt(0)\n\t" \
  "v_cmp_ne_u32 vcc, s36, v246\n\t" \
  "s_cbranch_vccz .Lat_k99\n\t" \
  "s_add_u32 s81, s81, 1\n\t" \
  "s_cmp_lt_u32 s81, " FQ_CAP "\n\t" \
  "s_cbranch_scc1 .Lat_r99\n\t" \
  "buffer_store_dword v243, off, s[16:19], 0 offset:256 sc1\n\t" \
  ".Lat_k99:\n\t" \
  "buffer_load_dwordx4 v[64:67], v242, s[16:19], s79 offen offset:0 sc1\n\t" \
  "buffer_load_dwordx4 v[68:71], v242, s[16:19], s79 offen offset:1024 sc1\n\t" \
  "buffer_load_dwordx4 v[72:75], v242, s[16:19], s79 offen offset:2048 sc1\n\t" \
  "buffer_load_dwordx4 v[76:79], v242, s[16:19], s79 offen offset:3072 sc1\n\t" \
  "s_waitcnt vmcnt(0)\n\t" \
  "s_branch .Lat_d2\n\t" \
  ".Ltl_g2:\n\t" \
  "buffer_store_dword v243, off, s[16:19], 0 offset:256 sc1\n\t" \
  "s_branch .Ltl_k2\n\t" \
  "s_endpgm\n\t" \
  ".Lfz_z:\n\t" \
  ".fill 128, 4, 0xBF800000\n\t" \
  ".subsection 2\n\t" \
  ".Lsf_go:\n\t" \
  "v_mfma_f32_16x16x32_bf16 v[28:31], a[156:159], a[224:227], v[28:31]\n\t" \
  "v_mov_b32 v196, 1.0\n\t" \
  "v_mov_b32 v197, 1.0\n\t" \
  "v_mfma_f32_16x16x32_bf16 v[28:31], a[92:95], a[240:243], v[28:31]\n\t" \
  "v_mov_b32 v198, 1.0\n\t" \
  "v_mov_b32 v199, 1.0\n\t" \
  "v_mfma_f32_16x16x32_bf16 v[24:27], a[152:155], a[224:227], v[24:27]\n\t" \
  "s_mov_b32 s99, 0\n\t" \
  "v_add_u32 v210, 65536, v228\n\t" \
  "v_add_u32 v211, 65536, v229\n\t" \
  "v_mfma_f32_16x16x32_bf16 v[24:27], a[88:91], a[240:243], v[24:27]\n\t" \
  "v_mov_b32 v216, 0x3f803f80\n\t" \
  "v_cvt_pk_bf16_f32 v31, v30, v31\n\t" \
  "v_mfma_f32_16x16x32_bf16 v[20:23], a[148:151], a[224:227], v[20:23]\n\t" \
  "v_mov_b32 v217, 0x3f803f80\n\t" \
  "v_cvt_pk_bf16_f32 v30, v28, v29\n\t" \
  "v_mfma_f32_16x16x32_bf16 v[20:23], a[84:87], a[240:243], v[20:23]\n\t" \
  "v_mov_b32 v218, 0x3f803f80\n\t" \
  "v_mov_b32 v219, 0x3f803f80\n\t" \
  "v_mfma_f32_16x16x32_bf16 v[16:19], a[144:147], a[224:227], v[16:19]\n\t" \
  "v_cvt_pk_bf16_f32 v28, v24, v25\n\t" \
  "v_cvt_pk_bf16_f32 v29, v26, v27\n\t" \
  "v_mfma_f32_16x16x32_bf16 v[16:19], a[80:83], a[240:243], v[16:19]\n\t" \
  "v_and_b32 v208, 1, v233\n\t" \
  "v_mul_u32_u24 v208, 0xaaaa, v208\n\t" \
  "v_mfma_f32_16x16x32_bf16 v[12:15], a[140:143], a[224:227], v[12:15]\n\t" \
  "v_cvt_pk_bf16_f32 v27, v22, v23\n\t" \
  "v_cvt_pk_bf16_f32 v26, v20, v21\n\t" \
  "v_mfma_f32_16x16x32_bf16 v[12:15], a[76:79], a[240:243], v[12:15]\n\t" \
  "v_or_b32 v208, 0x4444, v208\n\t" \
  "v_lshl_or_b32 v208, v208, 16, v208\n\t" \
  "v_mfma_f32_16x16x32_bf16 v[8:11], a[136:139], a[224:227], v[8:11]\n\t" \
  "v_mov_b32 v160, 0\n\t" \
  "v_mov_b32 v161, 0\n\t" \
  "v_mfma_f32_16x16x32_bf16 v[8:11], a[72:75], a[240:243], v[8:11]\n\t" \
  "v_mov_b32 v164, 0\n\t" \
  "v_mov_b32 v165, 0\n\t" \
  "v_mfma_f32_16x16x32_bf16 v[4:7], a[132:135], a[224:227], v[4:7]\n\t" \
  "v_mov_b32 v168, 0\n\t" \
  "v_cvt_pk_bf16_f32 v24, v16, v17\n\t" \
  "v_mfma_f32_16x16x32_bf16 v[4:7], a[68:71], a[240:243], v[4:7]\n\t" \
  "v_cvt_pk_bf16_f32 v25, v18, v19\n\t" \
  "v_cvt_pk_bf16_f32 v23, v14, v15\n\t" \
  "v_mfma_f32_16x16x32_bf16 v[0:3], a[128:131], a[224:227], v[0:3]\n\t" \
  "s_waitcnt vmcnt(2)\n\t" \
  "v_cvt_pk_bf16_f32 v22, v12, v13\n\t" \
  "v_cvt_pk_bf16_f32 v20, v8, v9\n\t" \
  "v_mfma_f32_16x16x32_bf16 v[0:3], a[64:67], a[240:243], v[0:3]\n\t" \
  "s_barrier\n\t" \
  "v_mfma_f32_16x16x32_bf16 v[60:63], a[156:159], a[228:231], v[60:63]\n\t" \
  "v_cvt_pk_bf16_f32 v21, v10, v11\n\t" \
  "v_cvt_pk_bf16_f32 v19, v6, v7\n\t" \
  "v_mfma_f32_16x16x32_bf16 v[60:63], a[92:95], a[244:247], v[60:63]\n\t" \
  "v_mov_b32 v169, 0\n\t" \
  "v_cvt_pk_bf16_f32 v18, v4, v5\n\t" \
  "v_mfma_f32_16x16x32_bf16 v[56:59], a[152:155], a[228:231], v[56:59]\n\t" \
  "v_cvt_pk_bf16_f32 v16, v0, v1\n\t" \
  "v_cvt_pk_bf16_f32 v17, v2, v3\n\t" \
  "v_mfma_f32_16x16x32_bf16 v[56:59], a[88:91], a[244:247], v[56:59]\n\t" \
  "ds_read_b128 a[0:3], v220 offset:0\n\t" \
  "v_mov_b32 v172, 0\n\t" \
  "v_mfma_f32_16x16x32_bf16 v[52:55], a[148:151], a[228:231], v[52:55]\n\t" \
  "v_cvt_pk_bf16_f32 v63, v62, v63\n\t" \
  "v_cvt_pk_bf16_f32 v62, v60, v61\n\t" \
  "v_mfma_f32_16x16x32_bf16 v[52:55], a[84:87], a[244:247], v[52:55]\n\t" \
  "ds_read_b128 a[4:7], v221 offset:0\n\t" \
  "v_permlane16_swap_b32 v28, v30\n\t" \
  "v_mfma_f32_16x16x32_bf16 v[48:51], a[144:147], a[228:231], v[48:51]\n\t" \
  "v_cvt_pk_bf16_f32 v60, v56, v57\n\t" \
  "v_permlane16_swap_b32 v29, v31\n\t" \
  "v_mfma_f32_16x16x32_bf16 v[48:51], a[80:83], a[244:247], v[48:51]\n\t" \
  "ds_read_b128 a[8:11], v222 offset:0\n\t" \
  "v_mov_b32 v173, 0\n\t" \
  "v_cvt_pk_bf16_f32 v61, v58, v59\n\t" \
  "v_mfma_f32_16x16x32_bf16 v[44:47], a[140:143], a[228:231], v[44:47]\n\t" \
  "v_cvt_pk_bf16_f32 v59, v54, v55\n\t" \
  "v_cvt_pk_bf16_f32 v58, v52, v53\n\t" \
  "v_mfma_f32_16x16x32_bf16 v[44:47], a[76:79], a[244:247], v[44:47]\n\t" \
  "ds_read_b128 a[12:15], v223 offset:0\n\t" \
  "v_permlane16_swap_b32 v24, v26\n\t" \
  "v_mfma_f32_16x16x32_bf16 v[40:43], a[136:139], a[228:231], v[40:43]\n\t" \
  "v_cvt_pk_bf16_f32 v56, v48, v49\n\t" \
  "v_permlane16_swap_b32 v25, v27\n\t" \
  "v_mfma_f32_16x16x32_bf16 v[40:43], a[72:75], a[244:247], v[40:43]\n\t" \
  "ds_read_b128 a[16:19], v220 offset:4096\n\t" \
  "v_cvt_pk_bf16_f32 v57, v50, v51\n\t" \
  "v_mfma_f32_16x16x32_bf16 v[36:39], a[132:135], a[228:231], v[36:39]\n\t" \
  "v_cvt_pk_bf16_f32 v55, v46, v47\n\t" \
  "v_cvt_pk_bf16_f32 v54, v44, v45\n\t" \
  "v_mfma_f32_16x16x32_bf16 v[36:39], a[68:71], a[244:247], v[36:39]\n\t" \
  "ds_read_b128 a[20:23], v221 offset:4096\n\t" \
  "v_permlane16_swap_b32 v20, v22\n\t" \
  "v_mfma_f32_16x16x32_bf16 v[32:35], a[128:131], a[228:231], v[32:35]\n\t" \
  "v_cvt_pk_bf16_f32 v52, v40, v41\n\t" \
  "v_permlane16_swap_b32 v21, v23\n\t" \
  "v_mfma_f32_16x16x32_bf16 v[32:35], a[64:67], a[244:247], v[32:35]\n\t" \
  "ds_read_b128 a[24:27], v222 offset:4096\n\t" \
  "v_cvt_pk_bf16_f32 v53, v42, v43\n\t" \
  "v_mfma_f32_16x16x32_bf16 v[92:95], a[156:159], a[232:235], v[92:95]\n\t" \
  "v_cvt_pk_bf16_f32 v51, v38, v39\n\t" \
  "v_cvt_pk_bf16_f32 v50, v36, v37\n\t" \
  "v_mfma_f32_16x16x32_bf16 v[92:95], a[92:95], a[248:251], v[92:95]\n\t" \
  "ds_read_b128 a[28:31], v223 offset:4096\n\t" \
  "v_permlane16_swap_b32 v16, v18\n\t" \
  "v_mfma_f32_16x16x32_bf16 v[88:91], a[152:155], a[232:235], v[88:91]\n\t" \
  "v_cvt_pk_bf16_f32 v48, v32, v33\n\t" \
  "v_permlane16_swap_b32 v17, v19\n\t" \
  "v_mfma_f32_16x16x32_bf16 v[88:91], a[88:91], a[248:251], v[88:91]\n\t" \
  "ds_read_b128 a[32:35], v220 offset:8192\n\t" \
  "v_cvt_pk_bf16_f32 v49, v34, v35\n\t" \
  "v_mfma_f32_16x16x32_bf16 v[84:87], a[148:151], a[232:235], v[84:87]\n\t" \
  "v_cvt_pk_bf16_f32 v95, v94, v95\n\t" \
  "v_cvt_pk_bf16_f32 v94, v92, v93\n\t" \
  "v_mfma_f32_16x16x32_bf16 v[84:87], a[84:87], a[248:251], v[84:87]\n\t" \
  "ds_read_b128 a[36:39], v221 offset:8192\n\t" \
  "v_permlane16_swap_b32 v60, v62\n\t" \
  "v_mfma_f32_16x16x32_bf16 v[80:83], a[144:147], a[232:235], v[80:83]\n\t" \
  "v_cvt_pk_bf16_f32 v92, v88, v89\n\t" \
  "v_permlane16_swap_b32 v61, v63\n\t" \
  "v_mfma_f32_16x16x32_bf16 v[80:83], a[80:83], a[248:251], v[80:83]\n\t" \
  "ds_read_b128 a[40:43], v222 offset:8192\n\t" \
  "v_cvt_pk_bf16_f32 v93, v90, v91\n\t" \
  "v_mfma_f32_16x16x32_bf16 v[76:79], a[140:143], a[232:235], v[76:79]\n\t" \
  "v_cvt_pk_bf16_f32 v91, v86, v87\n\t" \
  "v_cvt_pk_bf16_f32 v90, v84, v85\n\t" \
  "v_mfma_f32_16x16x32_bf16 v[76:79], a[76:79], a[248:251], v[76:79]\n\t" \
  "ds_read_b128 a[44:47], v223 offset:8192\n\t" \
  "v_permlane16_swap_b32 v56, v58\n\t" \
  "v_mfma_f32_16x16x32_bf16 v[72:75], a[136:139], a[232:235], v[72:75]\n\t" \
  "v_cvt_pk_bf16_f32 v88, v80, v81\n\t" \
  "v_permlane16_swap_b32 v57, v59\n\t" \
  "v_mfma_f32_16x16x32_bf16 v[72:75], a[72:75], a[248:251], v[72:75]\n\t" \
  "ds_read_b128 a[48:51], v220 offset:12288\n\t" \
  "v_cvt_pk_bf16_f32 v89, v82, v83\n\t" \
  "v_mfma_f32_16x16x32_bf16 v[68:71], a[132:135], a[232:235], v[68:71]\n\t" \
  "ds_read_b128 a[52:55], v221 offset:12288\n\t" \
  "v_cvt_pk_bf16_f32 v87, v78, v79\n\t" \
  "v_mfma_f32_16x16x32_bf16 v[68:71], a[68:71], a[248:251], v[68:71]\n\t" \
  "v_cvt_pk_bf16_f32 v86, v76, v77\n\t" \
  "v_permlane16_swap_b32 v52, v54\n\t" \
  "v_mfma_f32_16x16x32_bf16 v[64:67], a[128:131], a[232:235], v[64:67]\n\t" \
  "ds_read_b128 a[56:59], v222 offset:12288\n\t" \
  "v_permlane16_swap_b32 v53, v55\n\t" \
  "v_mfma_f32_16x16x32_bf16 v[64:67], a[64:67], a[248:251], v[64:67]\n\t" \
  "v_cvt_pk_bf16_f32 v84, v72, v73\n\t" \
  "v_cvt_pk_bf16_f32 v85, v74, v75\n\t" \
  "v_mfma_f32_16x16x32_bf16 v[124:127], a[156:159], a[236:239], v[124:127]\n\t" \
  "ds_read_b128 a[60:63], v223 offset:12288\n\t" \
  "v_cvt_pk_bf16_f32 v83, v70, v71\n\t" \
  "v_mfma_f32_16x16x32_bf16 v[124:127], a[92:95], a[252:255], v[124:127]\n\t" \
  "v_cvt_pk_bf16_f32 v82, v68, v69\n\t" \
  "v_permlane16_swap_b32 v48, v50\n\t" \
  "v_mfma_f32_16x16x32_bf16 v[120:123], a[152:155], a[236:239], v[120:123]\n\t" \
  "v_cvt_pk_bf16_f32 v80, v64, v65\n\t" \
  "v_permlane16_swap_b32 v49, v51\n\t" \
  "v_mfma_f32_16x16x32_bf16 v[120:123], a[88:91], a[252:255], v[120:123]\n\t" \
  "v_permlane16_swap_b32 v92, v94\n\t" \
  "v_mfma_f32_16x16x32_bf16 v[116:119], a[148:151], a[236:239], v[116:119]\n\t" \
  "v_cvt_pk_bf16_f32 v81, v66, v67\n\t" \
  "v_cvt_pk_bf16_f32 v127, v126, v127\n\t" \
  "v_mfma_f32_16x16x32_bf16 v[116:119], a[84:87], a[252:255], v[116:119]\n\t" \
  "v_permlane16_swap_b32 v93, v95\n\t" \
  "v_mfma_f32_16x16x32_bf16 v[112:115], a[144:147], a[236:239], v[112:115]\n\t" \
  "v_cvt_pk_bf16_f32 v126, v124, v125\n\t" \
  "v_cvt_pk_bf16_f32 v124, v120, v121\n\t" \
  "v_mfma_f32_16x16x32_bf16 v[112:115], a[80:83], a[252:255], v[112:115]\n\t" \
  "v_cvt_pk_bf16_f32 v125, v122, v123\n\t" \
  "v_permlane16_swap_b32 v88, v90\n\t" \
  "v_mfma_f32_16x16x32_bf16 v[108:111], a[140:143], a[236:239], v[108:111]\n\t" \
  "v_permlane16_swap_b32 v89, v91\n\t" \
  "v_mfma_f32_16x16x32_bf16 v[108:111], a[76:79], a[252:255], v[108:111]\n\t" \
  "v_cvt_pk_bf16_f32 v123, v118, v119\n\t" \
  "v_cvt_pk_bf16_f32 v122, v116, v117\n\t" \
  "v_mfma_f32_16x16x32_bf16 v[104:107], a[136:139], a[236:239], v[104:107]\n\t" \
  "v_permlane16_swap_b32 v84, v86\n\t" \
  "v_mfma_f32_16x16x32_bf16 v[104:107], a[72:75], a[252:255], v[104:107]\n\t" \
  "v_cvt_pk_bf16_f32 v120, v112, v113\n\t" \
  "v_cvt_pk_bf16_f32 v121, v114, v115\n\t" \
  "v_mfma_f32_16x16x32_bf16 v[100:103], a[132:135], a[236:239], v[100:103]\n\t" \
  "s_add_u32 m0, s38, 49152\n\t" \
  "v_permlane16_swap_b32 v85, v87\n\t" \
  "v_mfma_f32_16x16x32_bf16 v[100:103], a[68:71], a[252:255], v[100:103]\n\t" \
  "s_add_u32 s96, s38, 32768\n\t" \
  "v_permlane16_swap_b32 v80, v82\n\t" \
  "v_mfma_f32_16x16x32_bf16 v[96:99], a[128:131], a[236:239], v[96:99]\n\t" \
  "s_waitcnt lgkmcnt(0)\n\t" \
  "v_permlane16_swap_b32 v81, v83\n\t" \
  "v_mfma_f32_16x16x32_bf16 v[96:99], a[64:67], a[252:255], v[96:99]\n\t" \
  "s_barrier\n\t" \
  "v_mfma_f32_16x16x32_bf16 v[0:3], a[0:3], v[16:19], 0\n\t" \
  "buffer_load_dwordx4 v224, s[60:63], s96 offen offset:0 lds\n\t" \
  "v_mfma_f32_16x16x32_bf16 v[0:3], a[4:7], v[20:23], v[0:3]\n\t" \
  "v_cvt_pk_bf16_f32 v119, v110, v111\n\t" \
  "v_cvt_pk_bf16_f32 v118, v108, v109\n\t" \
  "v_mfma_f32_16x16x32_bf16 v[0:3], a[8:11], v[24:27], v[0:3]\n\t" \
  "v_permlane16_swap_b32 v124, v126\n\t" \
  "v_mfma_f32_16x16x32_bf16 v[0:3], a[12:15], v[28:31], v[0:3]\n\t" \
  "ds_read_b128 a[192:195], v220 offset:16384\n\t" \
  "v_cvt_pk_bf16_f32 v116, v104, v105\n\t" \
  "v_mfma_f32_16x16x32_bf16 v[32:35], a[0:3], v[48:51], 0\n\t" \
  "v_permlane16_swap_b32 v125, v127\n\t" \
  "v_mfma_f32_16x16x32_bf16 v[32:35], a[4:7], v[52:55], v[32:35]\n\t" \
  "ds_read_b128 a[196:199], v221 offset:16384\n\t" \
  "v_cvt_pk_bf16_f32 v117, v106, v107\n\t" \
  "s_nop 0\n\t" \
  "v_max3_f32 v194, v0, v1, v2\n\t" \
  "v_max_f32 v194, v194, v3\n\t" \
  "v_mfma_f32_16x16x32_bf16 v[32:35], a[8:11], v[56:59], v[32:35]\n\t" \
  "v_mov_b32 v195, v194\n\t" \
  "v_cvt_pk_bf16_f32 v115, v102, v103\n\t" \
  "s_nop 0\n\t" \
  "v_permlane32_swap_b32 v194, v195\n\t" \
  "v_mfma_f32_16x16x32_bf16 v[32:35], a[12:15], v[60:63], v[32:35]\n\t" \
  "v_permlane16_swap_b32 v120, v122\n\t" \
  "v_max_f32 v194, v194, v195\n\t" \
  "v_mov_b32 v195, v194\n\t" \
  "s_nop 1\n\t" \
  "v_permlane16_swap_b32 v194, v195\n\t" \
  "v_max_f32 v194, v194, v195\n\t" \
  "v_sub_f32 v176, 0xc2000000, v194\n\t" \
  "v_sub_f32 v177, 0xc2000000, v194\n\t" \
  "v_sub_f32 v178, 0xc2000000, v194\n\t" \
  "v_sub_f32 v179, 0xc2000000, v194\n\t" \
  "s_nop 1\n\t" \
  "v_mfma_f32_16x16x32_bf16 v[4:7], a[16:19], v[16:19], v[176:179]\n\t" \
  "v_add_f32 v0, v0, v176\n\t" \
  "v_add_f32 v1, v1, v176\n\t" \
  "v_add_f32 v2, v2, v176\n\t" \
  "v_add_f32 v3, v3, v176\n\t" \
  "v_mfma_f32_16x16x32_bf16 v[4:7], a[20:23], v[20:23], v[4:7]\n\t" \
  "buffer_load_dwordx4 v225, s[60:63], s96 offen offset:1024 lds\n\t" \
  "v_max3_f32 v194, v32, v33, v34\n\t" \
  "v_max_f32 v194, v194, v35\n\t" \
  "v_mov_b32 v195, v194\n\t" \
  "v_mfma_f32_16x16x32_bf16 v[4:7], a[24:27], v[24:27], v[4:7]\n\t" \
  "ds_read_b128 a[200:203], v222 offset:16384\n\t" \
  "v_permlane32_swap_b32 v194, v195\n\t" \
  "v_max_f32 v194, v194, v195\n\t" \
  "v_mov_b32 v195, v194\n\t" \
  "v_mfma_f32_16x16x32_bf16 v[4:7], a[28:31], v[28:31], v[4:7]\n\t" \
  "v_permlane16_swap_b32 v121, v123\n\t" \
  "v_permlane16_swap_b32 v194, v195\n\t" \
  "v_mfma_f32_16x16x32_bf16 v[8:11], a[32:35], v[16:19], v[176:179]\n\t" \
  "v_max_f32 v194, v194, v195\n\t" \
  "v_sub_f32 v180, 0xc2000000, v194\n\t" \
  "v_mfma_f32_16x16x32_bf16 v[8:11], a[36:39], v[20:23], v[8:11]\n\t" \
  "v_sub_f32 v181, 0xc2000000, v194\n\t" \
  "v_sub_f32 v182, 0xc2000000, v194\n\t" \
  "v_mfma_f32_16x16x32_bf16 v[8:11], a[40:43], v[24:27], v[8:11]\n\t" \
  "v_sub_f32 v183, 0xc2000000, v194\n\t" \
  "v_max3_f32 v192, v4, v5, v6\n\t" \
  "v_mfma_f32_16x16x32_bf16 v[8:11], a[44:47], v[28:31], v[8:11]\n\t" \
  "v_add_f32 v32, v32, v180\n\t" \
  "v_add_f32 v33, v33, v180\n\t" \
  "v_mfma_f32_16x16x32_bf16 v[12:15], a[48:51], v[16:19], v[176:179]\n\t" \
  "v_add_f32 v34, v34, v180\n\t" \
  "v_add_f32 v35, v35, v180\n\t" \
  "v_mfma_f32_16x16x32_bf16 v[12:15], a[52:55], v[20:23], v[12:15]\n\t" \
  "ds_read_b128 a[204:207], v223 offset:16384\n\t" \
  "v_permlane16_swap_b32 v116, v118\n\t" \
  "v_max3_f32 v203, v7, v8, v9\n\t" \
  "v_max3_f32 v192, v192, v10, v11\n\t" \
  "v_mfma_f32_16x16x32_bf16 v[12:15], a[56:59], v[24:27], v[12:15]\n\t" \
  "buffer_load_dwordx4 v226, s[60:63], s96 offen offset:2048 lds\n\t" \
  "v_mfma_f32_16x16x32_bf16 v[12:15], a[60:63], v[28:31], v[12:15]\n\t" \
  "ds_read_b128 a[208:211], v220 offset:20480\n\t" \
  "v_permlane16_swap_b32 v117, v119\n\t" \
  "v_cvt_pk_bf16_f32 v114, v100, v101\n\t" \
  "v_mfma_f32_16x16x32_bf16 v[64:67], a[0:3], v[80:83], 0\n\t" \
  "s_nop 3\n\t" \
  "v_max3_f32 v203, v203, v12, v13\n\t" \
  "v_mfma_f32_16x16x32_bf16 v[64:67], a[4:7], v[84:87], v[64:67]\n\t" \
  "ds_read_b128 a[212:215], v221 offset:20480\n\t" \
  "v_max3_f32 v192, v192, v14, v15\n\t" \
  "v_max_f32 v192, v192, v203\n\t" \
  "v_cmp_lt_f32 vcc, 0x42800000, v192\n\t" \
  "v_mfma_f32_16x16x32_bf16 v[64:67], a[8:11], v[88:91], v[64:67]\n\t" \
  "v_cvt_pk_bf16_f32 v112, v96, v97\n\t" \
  "v_cvt_pk_bf16_f32 v113, v98, v99\n\t" \
  "v_mfma_f32_16x16x32_bf16 v[64:67], a[12:15], v[92:95], v[64:67]\n\t" \
  "s_cbranch_vccnz .Lsf_c00\n\t" \
  ".Lsf_r00:\n\t" \
  "v_mfma_f32_16x16x32_bf16 v[36:39], a[16:19], v[48:51], v[180:183]\n\t" \
  "ds_read_b128 a[216:219], v222 offset:20480\n\t" \
  "v_exp_f32 v0, v0\n\t" \
  "v_exp_f32 v1, v1\n\t" \
  "v_mfma_f32_16x16x32_bf16 v[36:39], a[20:23], v[52:55], v[36:39]\n\t" \
  "ds_read_b128 a[220:223], v223 offset:20480\n\t" \
  "s_nop 0\n\t" \
  "v_max3_f32 v194, v64, v65, v66\n\t" \
  "v_max_f32 v194, v194, v67\n\t" \
  "v_mov_b32 v195, v194\n\t" \
  "v_mfma_f32_16x16x32_bf16 v[36:39], a[24:27], v[56:59], v[36:39]\n\t" \
  "ds_read_b128 a[224:227], v220 offset:24576\n\t" \
  "v_permlane32_swap_b32 v194, v195\n\t" \
  "v_max_f32 v194, v194, v195\n\t" \
  "v_mov_b32 v195, v194\n\t" \
  "v_mfma_f32_16x16x32_bf16 v[36:39], a[28:31], v[60:63], v[36:39]\n\t" \
  "s_nop 0\n\t" \
  "v_permlane16_swap_b32 v194, v195\n\t" \
  "v_max_f32 v194, v194, v195\n\t" \
  "v_sub_f32 v184, 0xc2000000, v194\n\t" \
  "v_sub_f32 v185, 0xc2000000, v194\n\t" \
  "v_mfma_f32_16x16x32_bf16 v[40:43], a[32:35], v[48:51], v[180:183]\n\t" \
  "buffer_load_dwordx4 v227, s[60:63], s96 offen offset:3072 lds\n\t" \
  "v_sub_f32 v186, 0xc2000000, v194\n\t" \
  "v_max3_f32 v192, v36, v37, v38\n\t" \
  "v_sub_f32 v187, 0xc2000000, v194\n\t" \
  "v_mfma_f32_16x16x32_bf16 v[40:43], a[36:39], v[52:55], v[40:43]\n\t" \
  "v_add_f32 v64, v64, v184\n\t" \
  "v_exp_f32 v2, v2\n\t" \
  "v_exp_f32 v3, v3\n\t" \
  "v_mfma_f32_16x16x32_bf16 v[40:43], a[40:43], v[56:59], v[40:43]\n\t" \
  "v_add_f32 v65, v65, v184\n\t" \
  "v_permlane16_swap_b32 v112, v114\n\t" \
  "v_exp_f32 v4, v4\n\t" \
  "v_mfma_f32_16x16x32_bf16 v[40:43], a[44:47], v[60:63], v[40:43]\n\t" \
  "v_add_f32 v66, v66, v184\n\t" \
  "v_exp_f32 v5, v5\n\t" \
  "v_exp_f32 v6, v6\n\t" \
  "v_mfma_f32_16x16x32_bf16 v[44:47], a[48:51], v[48:51], v[180:183]\n\t" \
  "s_add_u32 m0, s38, 147456\n\t" \
  "v_add_f32 v67, v67, v184\n\t" \
  "v_exp_f32 v7, v7\n\t" \
  "v_exp_f32 v8, v8\n\t" \
  "v_mfma_f32_16x16x32_bf16 v[44:47], a[52:55], v[52:55], v[44:47]\n\t" \
  "ds_read_b128 a[228:231], v221 offset:24576\n\t" \
  "v_max3_f32 v203, v39, v40, v41\n\t" \
  "v_max3_f32 v192, v192, v42, v43\n\t" \
  "v_exp_f32 v9, v9\n\t" \
  "v_mfma_f32_16x16x32_bf16 v[44:47], a[56:59], v[56:59], v[44:47]\n\t" \
  "s_add_u32 s96, s38, 16384\n\t" \
  "ds_read_b128 a[232:235], v222 offset:24576\n\t" \
  "v_permlane16_swap_b32 v113, v115\n\t" \
  "v_exp_f32 v10, v10\n\t" \
  "v_mfma_f32_16x16x32_bf16 v[44:47], a[60:63], v[60:63], v[44:47]\n\t" \
  "v_cvt_pk_bf16_f32 v128, v0, v1\n\t" \
  "v_exp_f32 v11, v11\n\t" \
  "v_exp_f32 v12, v12\n\t" \
  "v_mfma_f32_16x16x32_bf16 v[96:99], a[0:3], v[112:115], 0\n\t" \
  "ds_read_b128 a[236:239], v223 offset:24576\n\t" \
  "v_exp_f32 v13, v13\n\t" \
  "v_exp_f32 v14, v14\n\t" \
  "v_mfma_f32_16x16x32_bf16 v[96:99], a[4:7], v[116:119], v[96:99]\n\t" \
  "buffer_load_dwordx4 v230, s[64:67], s96 offen offset:0 lds\n\t" \
  "v_max3_f32 v203, v203, v44, v45\n\t" \
  "v_max3_f32 v192, v192, v46, v47\n\t" \
  "v_max_f32 v192, v192, v203\n\t" \
  "v_mfma_f32_16x16x32_bf16 v[96:99], a[8:11], v[120:123], v[96:99]\n\t" \
  "v_cvt_pk_bf16_f32 v129, v2, v3\n\t" \
  "v_cvt_pk_bf16_f32 v130, v4, v5\n\t" \
  "v_cmp_lt_f32 vcc, 0x42800000, v192\n\t" \
  "v_exp_f32 v15, v15\n\t" \
  "v_mfma_f32_16x16x32_bf16 v[96:99], a[12:15], v[124:127], v[96:99]\n\t" \
  "ds_read_b128 a[240:243], v220 offset:28672\n\t" \
  "s_cbranch_vccnz .Lsf_c01\n\t" \
  ".Lsf_r01:\n\t" \
  "v_mfma_f32_16x16x32_bf16 v[68:71], a[16:19], v[80:83], v[184:187]\n\t" \
  "v_exp_f32 v32, v32\n\t" \
  "v_exp_f32 v33, v33\n\t" \
  "v_mfma_f32_16x16x32_bf16 v[68:71], a[20:23], v[84:87], v[68:71]\n\t" \
  "v_cvt_pk_bf16_f32 v131, v6, v7\n\t" \
  "s_nop 0\n\t" \
  "v_max3_f32 v194, v96, v97, v98\n\t" \
  "v_max_f32 v194, v194, v99\n\t" \
  "v_mov_b32 v195, v194\n\t" \
  "v_mfma_f32_16x16x32_bf16 v[68:71], a[24:27], v[88:91], v[68:71]\n\t" \
  "s_nop 0\n\t" \
  "v_permlane32_swap_b32 v194, v195\n\t" \
  "v_max_f32 v194, v194, v195\n\t" \
  "v_mov_b32 v195, v194\n\t" \
  "v_mfma_f32_16x16x32_bf16 v[68:71], a[28:31], v[92:95], v[68:71]\n\t" \
  "ds_read_b128 a[244:247], v221 offset:28672\n\t" \
  "v_permlane16_swap_b32 v194, v195\n\t" \
  "v_max_f32 v194, v194, v195\n\t" \
  "v_sub_f32 v188, 0xc2000000, v194\n\t" \
  "v_mfma_f32_16x16x32_bf16 v[72:75], a[32:35], v[80:83], v[184:187]\n\t" \
  "v_sub_f32 v189, 0xc2000000, v194\n\t" \
  "v_sub_f32 v190, 0xc2000000, v194\n\t" \
  "v_exp_f32 v34, v34\n\t" \
  "v_max3_f32 v192, v68, v69, v70\n\t" \
  "v_mfma_f32_16x16x32_bf16 v[72:75], a[36:39], v[84:87], v[72:75]\n\t" \
  "v_sub_f32 v191, 0xc2000000, v194\n\t" \
  "v_exp_f32 v35, v35\n\t" \
  "v_exp_f32 v36, v36\n\t" \
  "v_mfma_f32_16x16x32_bf16 v[72:75], a[40:43], v[88:91], v[72:75]\n\t" \
  "buffer_load_dwordx4 v231, s[64:67], s96 offen offset:1024 lds\n\t" \
  "v_exp_f32 v37, v37\n\t" \
  "v_mfma_f32_16x16x32_bf16 v[72:75], a[44:47], v[92:95], v[72:75]\n\t" \
  "v_add_f32 v96, v96, v188\n\t" \
  "v_exp_f32 v38, v38\n\t" \
  "v_exp_f32 v39, v39\n\t" \
  "v_mfma_f32_16x16x32_bf16 v[76:79], a[48:51], v[80:83], v[184:187]\n\t" \
  "v_add_f32 v97, v97, v188\n\t" \
  "v_exp_f32 v40, v40\n\t" \
  "v_exp_f32 v41, v41\n\t" \
  "v_mfma_f32_16x16x32_bf16 v[76:79], a[52:55], v[84:87], v[76:79]\n\t" \
  "v_add_f32 v98, v98, v188\n\t" \
  "v_max3_f32 v203, v71, v72, v73\n\t" \
  "v_max3_f32 v192, v192, v74, v75\n\t" \
  "v_exp_f32 v42, v42\n\t" \
  "v_mfma_f32_16x16x32_bf16 v[76:79], a[56:59], v[88:91], v[76:79]\n\t" \
  "ds_read_b128 a[248:251], v222 offset:28672\n\t" \
  "v_exp_f32 v43, v43\n\t" \
  "v_exp_f32 v44, v44\n\t" \
  "v_mfma_f32_16x16x32_bf16 v[76:79], a[60:63], v[92:95], v[76:79]\n\t" \
  "v_add_f32 v99, v99, v188\n\t" \
  "v_exp_f32 v45, v45\n\t" \
  "v_exp_f32 v46, v46\n\t" \
  "v_mfma_f32_16x16x32_bf16 v[100:103], a[16:19], v[112:115], v[188:191]\n\t" \
  "ds_read_b128 a[252:255], v223 offset:28672\n\t" \
  "v_cvt_pk_bf16_f32 v132, v8, v9\n\t" \
  "v_exp_f32 v47, v47\n\t" \
  "v_mfma_f32_16x16x32_bf16 v[100:103], a[20:23], v[116:119], v[100:103]\n\t" \
  "v_max3_f32 v203, v203, v76, v77\n\t" \
  "v_max3_f32 v192, v192, v78, v79\n\t" \
  "v_max_f32 v192, v192, v203\n\t" \
  "v_cmp_lt_f32 vcc, 0x42800000, v192\n\t" \
  "v_mfma_f32_16x16x32_bf16 v[100:103], a[24:27], v[120:123], v[100:103]\n\t" \
  "buffer_load_dwordx4 v230, s[64:67], s96 offen offset:2048 lds\n\t" \
  "v_mfma_f32_16x16x32_bf16 v[100:103], a[28:31], v[124:127], v[100:103]\n\t" \
  "s_cbranch_vccnz .Lsf_c02\n\t" \
  ".Lsf_r02:\n\t" \
  "v_mfma_f32_16x16x32_bf16 v[104:107], a[32:35], v[112:115], v[188:191]\n\t" \
  "v_cvt_pk_bf16_f32 v133, v10, v11\n\t" \
  "v_exp_f32 v64, v64\n\t" \
  "v_exp_f32 v65, v65\n\t" \
  "v_mfma_f32_16x16x32_bf16 v[104:107], a[36:39], v[116:119], v[104:107]\n\t" \
  "v_cvt_pk_bf16_f32 v134, v12, v13\n\t" \
  "v_cvt_pk_bf16_f32 v135, v14, v15\n\t" \
  "v_exp_f32 v66, v66\n\t" \
  "v_mfma_f32_16x16x32_bf16 v[104:107], a[40:43], v[120:123], v[104:107]\n\t" \
  "v_cvt_pk_bf16_f32 v136, v32, v33\n\t" \
  "v_exp_f32 v67, v67\n\t" \
  "v_max3_f32 v192, v100, v101, v102\n\t" \
  "v_mfma_f32_16x16x32_bf16 v[104:107], a[44:47], v[124:127], v[104:107]\n\t" \
  "v_cvt_pk_bf16_f32 v137, v34, v35\n\t" \
  "v_exp_f32 v68, v68\n\t" \
  "v_mfma_f32_16x16x32_bf16 v[108:111], a[48:51], v[112:115], v[188:191]\n\t" \
  "v_exp_f32 v69, v69\n\t" \
  "v_mfma_f32_16x16x32_bf16 v[108:111], a[52:55], v[116:119], v[108:111]\n\t" \
  "v_cvt_pk_bf16_f32 v138, v36, v37\n\t" \
  "v_exp_f32 v70, v70\n\t" \
  "v_mfma_f32_16x16x32_bf16 v[108:111], a[56:59], v[120:123], v[108:111]\n\t" \
  "v_exp_f32 v71, v71\n\t" \
  "v_max3_f32 v203, v103, v104, v105\n\t" \
  "v_max3_f32 v192, v192, v106, v107\n\t" \
  "v_mfma_f32_16x16x32_bf16 v[108:111], a[60:63], v[124:127], v[108:111]\n\t" \
  "s_waitcnt lgkmcnt(0)\n\t" \
  "buffer_load_dwordx4 v231, s[64:67], s96 offen offset:3072 lds\n\t" \
  "v_mfma_f32_16x16x32_bf16 v[0:3], a[192:195], v[16:19], v[176:179]\n\t" \
  "ds_read_b64_tr_b16 a[128:129], v228 offset:0\n\t" \
  "v_cvt_pk_bf16_f32 v139, v38, v39\n\t" \
  "v_mfma_f32_16x16x32_bf16 v[0:3], a[196:199], v[20:23], v[0:3]\n\t" \
  "v_exp_f32 v72, v72\n\t" \
  "v_mfma_f32_16x16x32_bf16 v[0:3], a[200:203], v[24:27], v[0:3]\n\t" \
  "v_max3_f32 v203, v203, v108, v109\n\t" \
  "v_max3_f32 v192, v192, v110, v111\n\t" \
  "v_mfma_f32_16x16x32_bf16 v[0:3], a[204:207], v[28:31], v[0:3]\n\t" \
  "v_max_f32 v192, v192, v203\n\t" \
  "ds_read_b64_tr_b16 a[130:131], v228 offset:4096\n\t" \
  "v_cmp_lt_f32 vcc, 0x42800000, v192\n\t" \
  "v_mfma_f32_16x16x32_bf16 v[4:7], a[220:223], v[28:31], v[176:179]\n\t" \
  "ds_read_b64_tr_b16 a[132:133], v228 offset:8192\n\t" \
  "v_exp_f32 v73, v73\n\t" \
  "v_mfma_f32_16x16x32_bf16 v[4:7], a[216:219], v[24:27], v[4:7]\n\t" \
  "s_cbranch_vccnz .Lsf_c03\n\t" \
  ".Lsf_r03:\n\t" \
  "v_mfma_f32_16x16x32_bf16 v[4:7], a[212:215], v[20:23], v[4:7]\n\t" \
  "s_cmp_lg_u32 s99, 0\n\t" \
  "s_cbranch_scc1 .Lsf_p0\n\t" \
  ".Lsf_q0:\n\t" \
  "v_mfma_f32_16x16x32_bf16 v[4:7], a[208:211], v[16:19], v[4:7]\n\t" \
  "ds_read_b64_tr_b16 a[134:135], v228 offset:12288\n\t" \
  "v_max3_f32 v192, v0, v1, v2\n\t" \
  "v_mfma_f32_16x16x32_bf16 v[8:11], a[224:227], v[16:19], v[176:179]\n\t" \
  "ds_read_b64_tr_b16 a[136:137], v229 offset:0\n\t" \
  "v_exp_f32 v74, v74\n\t" \
  "v_mfma_f32_16x16x32_bf16 v[8:11], a[228:231], v[20:23], v[8:11]\n\t" \
  "ds_read_b64_tr_b16 a[138:139], v229 offset:4096\n\t" \
  "v_cvt_pk_bf16_f32 v140, v40, v41\n\t" \
  "v_mfma_f32_16x16x32_bf16 v[8:11], a[232:235], v[24:27], v[8:11]\n\t" \
  "v_max3_f32 v203, v3, v4, v5\n\t" \
  "v_max3_f32 v192, v192, v6, v7\n\t" \
  "v_mfma_f32_16x16x32_bf16 v[8:11], a[236:239], v[28:31], v[8:11]\n\t" \
  "ds_read_b64_tr_b16 a[140:141], v229 offset:8192\n\t" \
  "v_exp_f32 v75, v75\n\t" \
  "v_mfma_f32_16x16x32_bf16 v[12:15], a[252:255], v[28:31], v[176:179]\n\t" \
  "v_exp_f32 v76, v76\n\t" \
  "v_mfma_f32_16x16x32_bf16 v[12:15], a[248:251], v[24:27], v[12:15]\n\t" \
  "ds_read_b64_tr_b16 a[142:143], v229 offset:12288\n\t" \
  "v_cvt_pk_bf16_f32 v141, v42, v43\n\t" \
  "v_mfma_f32_16x16x32_bf16 v[12:15], a[244:247], v[20:23], v[12:15]\n\t" \
  "v_max3_f32 v203, v203, v8, v9\n\t" \
  "v_max3_f32 v192, v192, v10, v11\n\t" \
  "v_mfma_f32_16x16x32_bf16 v[12:15], a[240:243], v[16:19], v[12:15]\n\t" \
  "ds_read_b64_tr_b16 a[144:145], v228 offset:256\n\t" \
  "v_exp_f32 v77, v77\n\t" \
  "v_mfma_f32_16x16x32_bf16 v[32:35], a[192:195], v[48:51], v[180:183]\n\t" \
  "ds_read_b64_tr_b16 a[146:147], v228 offset:4352\n\t" \
  "v_cvt_pk_bf16_f32 v142, v44, v45\n\t" \
  "v_mfma_f32_16x16x32_bf16 v[32:35], a[196:199], v[52:55], v[32:35]\n\t" \
  "v_cvt_pk_bf16_f32 v143, v46, v47\n\t" \
  "v_exp_f32 v78, v78\n\t" \
  "v_mfma_f32_16x16x32_bf16 v[32:35], a[200:203], v[56:59], v[32:35]\n\t" \
  "v_max3_f32 v203, v203, v12, v13\n\t" \
  "v_max3_f32 v192, v192, v14, v15\n\t" \
  "v_mfma_f32_16x16x32_bf16 v[32:35], a[204:207], v[60:63], v[32:35]\n\t" \
  "v_max_f32 v192, v192, v203\n\t" \
  "v_cmp_lt_f32 vcc, 0x42800000, v192\n\t" \
  "v_mfma_f32_16x16x32_bf16 v[36:39], a[220:223], v[60:63], v[180:183]\n\t" \
  "ds_read_b64_tr_b16 a[148:149], v228 offset:8448\n\t" \
  "v_cvt_pk_bf16_f32 v144, v64, v65\n\t" \
  "v_mfma_f32_16x16x32_bf16 v[36:39], a[216:219], v[56:59], v[36:39]\n\t" \
  "s_cbranch_vccnz .Lsf_c10\n\t" \
  ".Lsf_r10:\n\t" \
  "v_mfma_f32_16x16x32_bf16 v[36:39], a[212:215], v[52:55], v[36:39]\n\t" \
  "v_max3_f32 v192, v32, v33, v34\n\t" \
  "v_exp_f32 v79, v79\n\t" \
  "v_mfma_f32_16x16x32_bf16 v[36:39], a[208:211], v[48:51], v[36:39]\n\t" \
  "ds_read_b64_tr_b16 a[150:151], v228 offset:12544\n\t" \
  "v_exp_f32 v96, v96\n\t" \
  "v_mfma_f32_16x16x32_bf16 v[40:43], a[224:227], v[48:51], v[180:183]\n\t" \
  "ds_read_b64_tr_b16 a[152:153], v229 offset:256\n\t" \
  "v_exp_f32 v97, v97\n\t" \
  "v_mfma_f32_16x16x32_bf16 v[40:43], a[228:231], v[52:55], v[40:43]\n\t" \
  "ds_read_b64_tr_b16 a[154:155], v229 offset:4352\n\t" \
  "v_cvt_pk_bf16_f32 v145, v66, v67\n\t" \
  "v_mfma_f32_16x16x32_bf16 v[40:43], a[232:235], v[56:59], v[40:43]\n\t" \
  "v_max3_f32 v203, v35, v36, v37\n\t" \
  "v_max3_f32 v192, v192, v38, v39\n\t" \
  "v_mfma_f32_16x16x32_bf16 v[40:43], a[236:239], v[60:63], v[40:43]\n\t" \
  "ds_read_b64_tr_b16 a[156:157], v229 offset:8448\n\t" \
  "v_exp_f32 v98, v98\n\t" \
  "v_mfma_f32_16x16x32_bf16 v[44:47], a[252:255], v[60:63], v[180:183]\n\t" \
  "ds_read_b64_tr_b16 a[158:159], v229 offset:12544\n\t" \
  "v_exp_f32 v99, v99\n\t" \
  "v_mfma_f32_16x16x32_bf16 v[44:47], a[248:251], v[56:59], v[44:47]\n\t" \
  "ds_read_b64_tr_b16 a[160:161], v228 offset:512\n\t" \
  "v_cvt_pk_bf16_f32 v146, v68, v69\n\t" \
  "v_mfma_f32_16x16x32_bf16 v[44:47], a[244:247], v[52:55], v[44:47]\n\t" \
  "v_max3_f32 v203, v203, v40, v41\n\t" \
  "v_max3_f32 v192, v192, v42, v43\n\t" \
  "v_mfma_f32_16x16x32_bf16 v[44:47], a[240:243], v[48:51], v[44:47]\n\t" \
  "v_cvt_pk_bf16_f32 v147, v70, v71\n\t" \
  "v_exp_f32 v100, v100\n\t" \
  "v_mfma_f32_16x16x32_bf16 v[64:67], a[192:195], v[80:83], v[184:187]\n\t" \
  "ds_read_b64_tr_b16 a[162:163], v228 offset:4608\n\t" \
  "v_exp_f32 v101, v101\n\t" \
  "v_mfma_f32_16x16x32_bf16 v[64:67], a[196:199], v[84:87], v[64:67]\n\t" \
  "ds_read_b64_tr_b16 a[164:165], v228 offset:8704\n\t" \
  "v_cvt_pk_bf16_f32 v148, v72, v73\n\t" \
  "v_mfma_f32_16x16x32_bf16 v[64:67], a[200:203], v[88:91], v[64:67]\n\t" \
  "v_max3_f32 v203, v203, v44, v45\n\t" \
  "v_max3_f32 v192, v192, v46, v47\n\t" \
  "v_mfma_f32_16x16x32_bf16 v[64:67], a[204:207], v[92:95], v[64:67]\n\t" \
  "v_max_f32 v192, v192, v203\n\t" \
  "v_cmp_lt_f32 vcc, 0x42800000, v192\n\t" \
  "v_mfma_f32_16x16x32_bf16 v[68:71], a[220:223], v[92:95], v[184:187]\n\t" \
  "ds_read_b64_tr_b16 a[166:167], v228 offset:12800\n\t" \
  "v_exp_f32 v102, v102\n\t" \
  "v_mfma_f32_16x16x32_bf16 v[68:71], a[216:219], v[88:91], v[68:71]\n\t" \
  "s_cbranch_vccnz .Lsf_c11\n\t" \
  ".Lsf_r11:\n\t" \
  "v_mfma_f32_16x16x32_bf16 v[68:71], a[212:215], v[84:87], v[68:71]\n\t" \
  "v_max3_f32 v192, v64, v65, v66\n\t" \
  "v_exp_f32 v103, v103\n\t" \
  "v_mfma_f32_16x16x32_bf16 v[68:71], a[208:211], v[80:83], v[68:71]\n\t" \
  "ds_read_b64_tr_b16 a[168:169], v229 offset:512\n\t" \
  "v_exp_f32 v104, v104\n\t" \
  "v_cvt_pk_bf16_f32 v149, v74, v75\n\t" \
  "v_mfma_f32_16x16x32_bf16 v[72:75], a[224:227], v[80:83], v[184:187]\n\t" \
  "ds_read_b64_tr_b16 a[170:171], v229 offset:4608\n\t" \
  "v_exp_f32 v105, v105\n\t" \
  "v_mfma_f32_16x16x32_bf16 v[72:75], a[228:231], v[84:87], v[72:75]\n\t" \
  "ds_read_b64_tr_b16 a[172:173], v229 offset:8704\n\t" \
  "v_max3_f32 v203, v67, v68, v69\n\t" \
  "v_mfma_f32_16x16x32_bf16 v[72:75], a[232:235], v[88:91], v[72:75]\n\t" \
  "v_max3_f32 v192, v192, v70, v71\n\t" \
  "v_exp_f32 v106, v106\n\t" \
  "v_mfma_f32_16x16x32_bf16 v[72:75], a[236:239], v[92:95], v[72:75]\n\t" \
  "ds_read_b64_tr_b16 a[174:175], v229 offset:12800\n\t" \
  "v_cvt_pk_bf16_f32 v150, v76, v77\n\t" \
  "v_cvt_pk_bf16_f32 v151, v78, v79\n\t" \
  "v_mfma_f32_16x16x32_bf16 v[76:79], a[252:255], v[92:95], v[184:187]\n\t" \
  "ds_read_b64_tr_b16 a[176:177], v228 offset:768\n\t" \
  "v_exp_f32 v107, v107\n\t" \
  "v_mfma_f32_16x16x32_bf16 v[76:79], a[248:251], v[88:91], v[76:79]\n\t" \
  "v_cvt_pk_bf16_f32 v152, v96, v97\n\t" \
  "v_max3_f32 v203, v203, v72, v73\n\t" \
  "v_mfma_f32_16x16x32_bf16 v[76:79], a[244:247], v[84:87], v[76:79]\n\t" \
  "v_max3_f32 v192, v192, v74, v75\n\t" \
  "v_exp_f32 v108, v108\n\t" \
  "v_mfma_f32_16x16x32_bf16 v[76:79], a[240:243], v[80:83], v[76:79]\n\t" \
  "ds_read_b64_tr_b16 a[178:179], v228 offset:4864\n\t" \
  "v_cvt_pk_bf16_f32 v153, v98, v99\n\t" \
  "v_mfma_f32_16x16x32_bf16 v[96:99], a[192:195], v[112:115], v[188:191]\n\t" \
  "ds_read_b64_tr_b16 a[180:181], v228 offset:8960\n\t" \
  "v_exp_f32 v109, v109\n\t" \
  "v_mfma_f32_16x16x32_bf16 v[96:99], a[196:199], v[116:119], v[96:99]\n\t" \
  "v_cvt_pk_bf16_f32 v154, v100, v101\n\t" \
  "v_cvt_pk_bf16_f32 v155, v102, v103\n\t" \
  "v_mfma_f32_16x16x32_bf16 v[96:99], a[200:203], v[120:123], v[96:99]\n\t" \
  "v_max3_f32 v203, v203, v76, v77\n\t" \
  "v_max3_f32 v192, v192, v78, v79\n\t" \
  "v_mfma_f32_16x16x32_bf16 v[96:99], a[204:207], v[124:127], v[96:99]\n\t" \
  "v_max_f32 v192, v192, v203\n\t" \
  "v_cmp_lt_f32 vcc, 0x42800000, v192\n\t" \
  "v_mfma_f32_16x16x32_bf16 v[100:103], a[220:223], v[124:127], v[188:191]\n\t" \
  "ds_read_b64_tr_b16 a[182:183], v228 offset:13056\n\t" \
  "v_exp_f32 v110, v110\n\t" \
  "v_mfma_f32_16x16x32_bf16 v[100:103], a[216:219], v[120:123], v[100:103]\n\t" \
  "s_cbranch_vccnz .Lsf_c12\n\t" \
  ".Lsf_r12:\n\t" \
  "v_mfma_f32_16x16x32_bf16 v[100:103], a[212:215], v[116:119], v[100:103]\n\t" \
  "v_max3_f32 v192, v96, v97, v98\n\t" \
  "v_exp_f32 v111, v111\n\t" \
  "v_cvt_pk_bf16_f32 v156, v104, v105\n\t" \
  "v_mfma_f32_16x16x32_bf16 v[100:103], a[208:211], v[112:115], v[100:103]\n\t" \
  "ds_read_b64_tr_b16 a[184:185], v229 offset:768\n\t" \
  "v_cvt_pk_bf16_f32 v157, v106, v107\n\t" \
  "v_mfma_f32_16x16x32_bf16 v[104:107], a[224:227], v[112:115], v[188:191]\n\t" \
  "ds_read_b64_tr_b16 a[186:187], v229 offset:4864\n\t" \
  "v_cvt_pk_bf16_f32 v158, v108, v109\n\t" \
  "v_mfma_f32_16x16x32_bf16 v[104:107], a[228:231], v[116:119], v[104:107]\n\t" \
  "ds_read_b64_tr_b16 a[188:189], v229 offset:8960\n\t" \
  "v_cvt_pk_bf16_f32 v159, v110, v111\n\t" \
  "v_mfma_f32_16x16x32_bf16 v[104:107], a[232:235], v[120:123], v[104:107]\n\t" \
  "v_max3_f32 v203, v99, v100, v101\n\t" \
  "v_max3_f32 v192, v192, v102, v103\n\t" \
  "v_mfma_f32_16x16x32_bf16 v[104:107], a[236:239], v[124:127], v[104:107]\n\t" \
  "ds_read_b64_tr_b16 a[190:191], v229 offset:13056\n\t" \
  "v_mfma_f32_16x16x32_bf16 v[108:111], a[252:255], v[124:127], v[188:191]\n\t" \
  "v_exp_f32 v0, v0\n\t" \
  "v_mfma_f32_16x16x32_bf16 v[108:111], a[248:251], v[120:123], v[108:111]\n\t" \
  "v_exp_f32 v1, v1\n\t" \
  "v_mfma_f32_16x16x32_bf16 v[108:111], a[244:247], v[116:119], v[108:111]\n\t" \
  "s_waitcnt vmcnt(0) lgkmcnt(0)\n\t" \
  "v_exp_f32 v2, v2\n\t" \
  "v_mfma_f32_16x16x32_bf16 v[108:111], a[240:243], v[112:115], v[108:111]\n\t" \
  "s_barrier\n\t" \
  "v_mfma_f32_16x16x32_bf16 a[0:3], a[128:131], v[128:131], 0\n\t" \
  "v_max3_f32 v203, v203, v104, v105\n\t" \
  "v_max3_f32 v192, v192, v106, v107\n\t" \
  "v_mfma_f32_16x16x32_bf16 a[0:3], a[132:135], v[132:135], a[0:3]\n\t" \
  "v_exp_f32 v3, v3\n\t" \
  "v_mfma_f32_16x16x32_bf16 a[4:7], a[136:139], v[128:131], 0\n\t" \
  "v_exp_f32 v4, v4\n\t" \
  "v_mfma_f32_16x16x32_bf16 a[4:7], a[140:143], v[132:135], a[4:7]\n\t" \
  "v_max3_f32 v203, v203, v108, v109\n\t" \
  "v_max3_f32 v192, v192, v110, v111\n\t" \
  "v_mfma_f32_16x16x32_bf16 a[8:11], a[144:147], v[128:131], 0\n\t" \
  "v_max_f32 v192, v192, v203\n\t" \
  "v_cmp_lt_f32 vcc, 0x42800000, v192\n\t" \
  "v_mfma_f32_16x16x32_bf16 a[8:11], a[148:151], v[132:135], a[8:11]\n\t" \
  "v_exp_f32 v5, v5\n\t" \
  "v_mfma_f32_16x16x32_bf16 a[12:15], a[152:155], v[128:131], 0\n\t" \
  "s_cbranch_vccnz .Lsf_c13\n\t" \
  ".Lsf_r13:\n\t" \
  "v_mfma_f32_16x16x32_bf16 a[12:15], a[156:159], v[132:135], a[12:15]\n\t" \
  "s_add_u32 m0, s38, 65536\n\t" \
  "v_exp_f32 v6, v6\n\t" \
  "v_mfma_f32_16x16x32_bf16 a[16:19], a[160:163], v[128:131], 0\n\t" \
  "s_add_u32 s96, s38, 49152\n\t" \
  "v_exp_f32 v7, v7\n\t" \
  "v_mfma_f32_16x16x32_bf16 a[16:19], a[164:167], v[132:135], a[16:19]\n\t" \
  "v_exp_f32 v8, v8\n\t" \
  "v_mfma_f32_16x16x32_bf16 a[20:23], a[168:171], v[128:131], 0\n\t" \
  "buffer_load_dwordx4 v224, s[60:63], s96 offen offset:0 lds\n\t" \
  "v_mfma_f32_16x16x32_bf16 a[20:23], a[172:175], v[132:135], a[20:23]\n\t" \
  "v_exp_f32 v9, v9\n\t" \
  "v_mfma_f32_16x16x32_bf16 a[24:27], a[176:179], v[128:131], 0\n\t" \
  "v_exp_f32 v10, v10\n\t" \
  "v_mfma_f32_16x16x32_bf16 a[24:27], a[180:183], v[132:135], a[24:27]\n\t" \
  "v_exp_f32 v11, v11\n\t" \
  "v_mfma_f32_16x16x32_bf16 a[28:31], a[184:187], v[128:131], 0\n\t" \
  "v_exp_f32 v12, v12\n\t" \
  "v_mfma_f32_16x16x32_bf16 a[28:31], a[188:191], v[132:135], a[28:31]\n\t" \
  "v_exp_f32 v13, v13\n\t" \
  "v_smfmac_f32_16x16x64_bf16 v[160:163], v[216:219], v[128:135], v208\n\t" \
  "ds_read_b128 a[192:195], v220 offset:0\n\t" \
  "v_cvt_pk_bf16_f32 v128, v0, v1\n\t" \
  "v_mfma_f32_16x16x32_bf16 a[32:35], a[128:131], v[136:139], 0\n\t" \
  "buffer_load_dwordx4 v225, s[60:63], s96 offen offset:1024 lds\n\t" \
  "v_mfma_f32_16x16x32_bf16 a[32:35], a[132:135], v[140:143], a[32:35]\n\t" \
  "v_exp_f32 v14, v14\n\t" \
  "v_mfma_f32_16x16x32_bf16 a[36:39], a[136:139], v[136:139], 0\n\t" \
  "v_exp_f32 v15, v15\n\t" \
  "v_mfma_f32_16x16x32_bf16 a[36:39], a[140:143], v[140:143], a[36:39]\n\t" \
  "ds_read_b128 a[196:199], v221 offset:0\n\t" \
  "v_cvt_pk_bf16_f32 v129, v2, v3\n\t" \
  "v_mfma_f32_16x16x32_bf16 a[40:43], a[144:147], v[136:139], 0\n\t" \
  "v_exp_f32 v32, v32\n\t" \
  "v_mfma_f32_16x16x32_bf16 a[40:43], a[148:151], v[140:143], a[40:43]\n\t" \
  "v_exp_f32 v33, v33\n\t" \
  "v_mfma_f32_16x16x32_bf16 a[44:47], a[152:155], v[136:139], 0\n\t" \
  "buffer_load_dwordx4 v226, s[60:63], s96 offen offset:2048 lds\n\t" \
  "v_mfma_f32_16x16x32_bf16 a[44:47], a[156:159], v[140:143], a[44:47]\n\t" \
  "ds_read_b128 a[200:203], v222 offset:0\n\t" \
  "v_cvt_pk_bf16_f32 v130, v4, v5\n\t" \
  "v_mfma_f32_16x16x32_bf16 a[48:51], a[160:163], v[136:139], 0\n\t" \
  "v_exp_f32 v34, v34\n\t" \
  "v_mfma_f32_16x16x32_bf16 a[48:51], a[164:167], v[140:143], a[48:51]\n\t" \
  "ds_read_b128 a[204:207], v223 offset:0\n\t" \
  "v_cvt_pk_bf16_f32 v131, v6, v7\n\t" \
  "v_mfma_f32_16x16x32_bf16 a[52:55], a[168:171], v[136:139], 0\n\t" \
  "v_exp_f32 v35, v35\n\t" \
  "v_mfma_f32_16x16x32_bf16 a[52:55], a[172:175], v[140:143], a[52:55]\n\t" \
  "ds_read_b128 a[208:211], v220 offset:4096\n\t" \
  "v_cvt_pk_bf16_f32 v132, v8, v9\n\t" \
  "v_mfma_f32_16x16x32_bf16 a[56:59], a[176:179], v[136:139], 0\n\t" \
  "buffer_load_dwordx4 v227, s[60:63], s96 offen offset:3072 lds\n\t" \
  "v_mfma_f32_16x16x32_bf16 a[56:59], a[180:183], v[140:143], a[56:59]\n\t" \
  "v_exp_f32 v36, v36\n\t" \
  "v_mfma_f32_16x16x32_bf16 a[60:63], a[184:187], v[136:139], 0\n\t" \
  "s_add_u32 m0, s38, 81920\n\t" \
  "ds_read_b128 a[212:215], v221 offset:4096\n\t" \
  "v_cvt_pk_bf16_f32 v133, v10, v11\n\t" \
  "v_mfma_f32_16x16x32_bf16 a[60:63], a[188:191], v[140:143], a[60:63]\n\t" \
  "s_add_u32 s96, s38, 32768\n\t" \
  "v_exp_f32 v37, v37\n\t" \
  "v_smfmac_f32_16x16x64_bf16 v[164:167], v[216:219], v[136:143], v208\n\t" \
  "ds_read_b128 a[216:219], v222 offset:4096\n\t" \
  "v_cvt_pk_bf16_f32 v134, v12, v13\n\t" \
  "v_mfma_f32_16x16x32_bf16 a[64:67], a[128:131], v[144:147], 0\n\t" \
  "v_exp_f32 v38, v38\n\t" \
  "v_mfma_f32_16x16x32_bf16 a[64:67], a[132:135], v[148:151], a[64:67]\n\t" \
  "buffer_load_dwordx4 v230, s[64:67], s96 offen offset:0 lds\n\t" \
  "v_mfma_f32_16x16x32_bf16 a[68:71], a[136:139], v[144:147], 0\n\t" \
  "ds_read_b128 a[220:223], v223 offset:4096\n\t" \
  "v_cvt_pk_bf16_f32 v135, v14, v15\n\t" \
  "v_mfma_f32_16x16x32_bf16 a[68:71], a[140:143], v[148:151], a[68:71]\n\t" \
  "v_exp_f32 v39, v39\n\t" \
  "v_mfma_f32_16x16x32_bf16 a[72:75], a[144:147], v[144:147], 0\n\t" \
  "v_exp_f32 v40, v40\n\t" \
  "v_mfma_f32_16x16x32_bf16 a[72:75], a[148:151], v[148:151], a[72:75]\n\t" \
  "ds_read_b128 a[224:227], v220 offset:8192\n\t" \
  "v_cvt_pk_bf16_f32 v136, v32, v33\n\t" \
  "v_mfma_f32_16x16x32_bf16 a[76:79], a[152:155], v[144:147], 0\n\t" \
  "ds_read_b128 a[228:231], v221 offset:8192\n\t" \
  "v_cvt_pk_bf16_f32 v137, v34, v35\n\t" \
  "v_mfma_f32_16x16x32_bf16 a[76:79], a[156:159], v[148:151], a[76:79]\n\t" \
  "v_exp_f32 v41, v41\n\t" \
  "v_mfma_f32_16x16x32_bf16 a[80:83], a[160:163], v[144:147], 0\n\t" \
  "buffer_load_dwordx4 v231, s[64:67], s96 offen offset:1024 lds\n\t" \
  "v_mfma_f32_16x16x32_bf16 a[80:83], a[164:167], v[148:151], a[80:83]\n\t" \
  "v_exp_f32 v42, v42\n\t" \
  "v_mfma_f32_16x16x32_bf16 a[84:87], a[168:171], v[144:147], 0\n\t" \
  "ds_read_b128 a[232:235], v222 offset:8192\n\t" \
  "v_cvt_pk_bf16_f32 v138, v36, v37\n\t" \
  "v_mfma_f32_16x16x32_bf16 a[84:87], a[172:175], v[148:151], a[84:87]\n\t" \
  "v_exp_f32 v43, v43\n\t" \
  "v_mfma_f32_16x16x32_bf16 a[88:91], a[176:179], v[144:147], 0\n\t" \
  "ds_read_b128 a[236:239], v223 offset:8192\n\t" \
  "v_cvt_pk_bf16_f32 v139, v38, v39\n\t" \
  "v_mfma_f32_16x16x32_bf16 a[88:91], a[180:183], v[148:151], a[88:91]\n\t" \
  "v_exp_f32 v44, v44\n\t" \
  "v_mfma_f32_16x16x32_bf16 a[92:95], a[184:187], v[144:147], 0\n\t" \
  "buffer_load_dwordx4 v230, s[64:67], s96 offen offset:2048 lds\n\t" \
  "v_mfma_f32_16x16x32_bf16 a[92:95], a[188:191], v[148:151], a[92:95]\n\t" \
  "ds_read_b128 a[240:243], v220 offset:12288\n\t" \
  "v_cvt_pk_bf16_f32 v140, v40, v41\n\t" \
  "v_smfmac_f32_16x16x64_bf16 v[168:171], v[216:219], v[144:151], v208\n\t" \
  "ds_read_b128 a[244:247], v221 offset:12288\n\t" \
  "v_cvt_pk_bf16_f32 v141, v42, v43\n\t" \
  "v_mfma_f32_16x16x32_bf16 a[96:99], a[128:131], v[152:155], 0\n\t" \
  "v_exp_f32 v45, v45\n\t" \
  "v_mfma_f32_16x16x32_bf16 a[96:99], a[132:135], v[156:159], a[96:99]\n\t" \
  "v_exp_f32 v46, v46\n\t" \
  "v_mfma_f32_16x16x32_bf16 a[100:103], a[136:139], v[152:155], 0\n\t" \
  "v_exp_f32 v47, v47\n\t" \
  "v_mfma_f32_16x16x32_bf16 a[100:103], a[140:143], v[156:159], a[100:103]\n\t" \
  "buffer_load_dwordx4 v231, s[64:67], s96 offen offset:3072 lds\n\t" \
  "v_mfma_f32_16x16x32_bf16 a[104:107], a[144:147], v[152:155], 0\n\t" \
  "ds_read_b128 a[248:251], v222 offset:12288\n\t" \
  "v_cvt_pk_bf16_f32 v142, v44, v45\n\t" \
  "v_mfma_f32_16x16x32_bf16 a[104:107], a[148:151], v[156:159], a[104:107]\n\t" \
  "ds_read_b128 a[252:255], v223 offset:12288\n\t" \
  "v_cvt_pk_bf16_f32 v143, v46, v47\n\t" \
  "v_mfma_f32_16x16x32_bf16 a[108:111], a[152:155], v[152:155], 0\n\t" \
  "v_exp_f32 v64, v64\n\t" \
  "v_mfma_f32_16x16x32_bf16 a[108:111], a[156:159], v[156:159], a[108:111]\n\t" \
  "v_exp_f32 v65, v65\n\t" \
  "v_mfma_f32_16x16x32_bf16 a[112:115], a[160:163], v[152:155], 0\n\t" \
  "v_exp_f32 v66, v66\n\t" \
  "v_mfma_f32_16x16x32_bf16 a[112:115], a[164:167], v[156:159], a[112:115]\n\t" \
  "v_exp_f32 v67, v67\n\t" \
  "v_mfma_f32_16x16x32_bf16 a[116:119], a[168:171], v[152:155], 0\n\t" \
  "v_exp_f32 v68, v68\n\t" \
  "v_mfma_f32_16x16x32_bf16 a[116:119], a[172:175], v[156:159], a[116:119]\n\t" \
  "v_exp_f32 v69, v69\n\t" \
  "v_mfma_f32_16x16x32_bf16 a[120:123], a[176:179], v[152:155], 0\n\t" \
  "v_exp_f32 v70, v70\n\t" \
  "v_mfma_f32_16x16x32_bf16 a[120:123], a[180:183], v[156:159], a[120:123]\n\t" \
  "v_exp_f32 v71, v71\n\t" \
  "v_mfma_f32_16x16x32_bf16 a[124:127], a[184:187], v[152:155], 0\n\t" \
  "v_exp_f32 v72, v72\n\t" \
  "v_mfma_f32_16x16x32_bf16 a[124:127], a[188:191], v[156:159], a[124:127]\n\t" \
  "v_exp_f32 v73, v73\n\t" \
  "v_smfmac_f32_16x16x64_bf16 v[172:175], v[216:219], v[152:159], v208\n\t" \
  "s_waitcnt lgkmcnt(12)\n\t" \
  "s_cmp_lg_u32 s99, 0\n\t" \
  "s_cbranch_scc1 .Lsf_p1\n\t" \
  ".Lsf_q1:\n\t" \
  "v_mfma_f32_16x16x32_bf16 v[0:3], a[192:195], v[16:19], v[176:179]\n\t" \
  "ds_read_b64_tr_b16 a[128:129], v210 offset:0\n\t" \
  "v_cvt_pk_bf16_f32 v144, v64, v65\n\t" \
  "v_mfma_f32_16x16x32_bf16 v[0:3], a[196:199], v[20:23], v[0:3]\n\t" \
  "v_exp_f32 v74, v74\n\t" \
  "v_mfma_f32_16x16x32_bf16 v[0:3], a[200:203], v[24:27], v[0:3]\n\t" \
  "ds_read_b64_tr_b16 a[130:131], v210 offset:4096\n\t" \
  "v_cvt_pk_bf16_f32 v145, v66, v67\n\t" \
  "v_mfma_f32_16x16x32_bf16 v[0:3], a[204:207], v[28:31], v[0:3]\n\t" \
  "s_waitcnt lgkmcnt(10)\n\t" \
  "v_exp_f32 v75, v75\n\t" \
  "v_mfma_f32_16x16x32_bf16 v[4:7], a[220:223], v[28:31], v[176:179]\n\t" \
  "ds_read_b64_tr_b16 a[132:133], v210 offset:8192\n\t" \
  "v_cvt_pk_bf16_f32 v146, v68, v69\n\t" \
  "v_mfma_f32_16x16x32_bf16 v[4:7], a[216:219], v[24:27], v[4:7]\n\t" \
  "v_exp_f32 v76, v76\n\t" \
  "v_mfma_f32_16x16x32_bf16 v[4:7], a[212:215], v[20:23], v[4:7]\n\t" \
  "ds_read_b64_tr_b16 a[134:135], v210 offset:12288\n\t" \
  "v_max3_f32 v192, v0, v1, v2\n\t" \
  "v_mfma_f32_16x16x32_bf16 v[4:7], a[208:211], v[16:19], v[4:7]\n\t" \
  "s_waitcnt lgkmcnt(8)\n\t" \
  "ds_read_b64_tr_b16 a[136:137], v211 offset:0\n\t" \
  "v_cvt_pk_bf16_f32 v147, v70, v71\n\t" \
  "v_mfma_f32_16x16x32_bf16 v[8:11], a[224:227], v[16:19], v[176:179]\n\t" \
  "v_exp_f32 v77, v77\n\t" \
  "v_mfma_f32_16x16x32_bf16 v[8:11], a[228:231], v[20:23], v[8:11]\n\t" \
  "ds_read_b64_tr_b16 a[138:139], v211 offset:4096\n\t" \
  "v_cvt_pk_bf16_f32 v148, v72, v73\n\t" \
  "v_mfma_f32_16x16x32_bf16 v[8:11], a[232:235], v[24:27], v[8:11]\n\t" \
  "v_max3_f32 v203, v3, v4, v5\n\t" \
  "v_max3_f32 v192, v192, v6, v7\n\t" \
  "v_mfma_f32_16x16x32_bf16 v[8:11], a[236:239], v[28:31], v[8:11]\n\t" \
  "s_waitcnt lgkmcnt(6)\n\t" \
  "ds_read_b64_tr_b16 a[140:141], v211 offset:8192\n\t" \
  "v_cvt_pk_bf16_f32 v149, v74, v75\n\t" \
  "v_mfma_f32_16x16x32_bf16 v[12:15], a[252:255], v[28:31], v[176:179]\n\t" \
  "v_exp_f32 v78, v78\n\t" \
  "v_mfma_f32_16x16x32_bf16 v[12:15], a[248:251], v[24:27], v[12:15]\n\t" \
  "ds_read_b64_tr_b16 a[142:143], v211 offset:12288\n\t" \
  "v_cvt_pk_bf16_f32 v150, v76, v77\n\t" \
  "v_mfma_f32_16x16x32_bf16 v[12:15], a[244:247], v[20:23], v[12:15]\n\t" \
  "v_max3_f32 v203, v203, v8, v9\n\t" \
  "v_max3_f32 v192, v192, v10, v11\n\t" \
  "v_mfma_f32_16x16x32_bf16 v[12:15], a[240:243], v[16:19], v[12:15]\n\t" \
  "v_exp_f32 v79, v79\n\t" \
  "v_mfma_f32_16x16x32_bf16 v[32:35], a[192:195], v[48:51], v[180:183]\n\t" \
  "ds_read_b64_tr_b16 a[144:145], v210 offset:256\n\t" \
  "v_cvt_pk_bf16_f32 v151, v78, v79\n\t" \
  "v_mfma_f32_16x16x32_bf16 v[32:35], a[196:199], v[52:55], v[32:35]\n\t" \
  "v_exp_f32 v96, v96\n\t" \
  "v_mfma_f32_16x16x32_bf16 v[32:35], a[200:203], v[56:59], v[32:35]\n\t" \
  "ds_read_b64_tr_b16 a[146:147], v210 offset:4352\n\t" \
  "v_max3_f32 v203, v203, v12, v13\n\t" \
  "v_mfma_f32_16x16x32_bf16 v[32:35], a[204:207], v[60:63], v[32:35]\n\t" \
  "v_max3_f32 v192, v192, v14, v15\n\t" \
  "v_max_f32 v192, v192, v203\n\t" \
  "v_mfma_f32_16x16x32_bf16 v[36:39], a[220:223], v[60:63], v[180:183]\n\t" \
  "ds_read_b64_tr_b16 a[148:149], v210 offset:8448\n\t" \
  "v_cmp_lt_f32 vcc, 0x42800000, v192\n\t" \
  "v_mfma_f32_16x16x32_bf16 v[36:39], a[216:219], v[56:59], v[36:39]\n\t" \
  "v_exp_f32 v97, v97\n\t" \
  "v_mfma_f32_16x16x32_bf16 v[36:39], a[212:215], v[52:55], v[36:39]\n\t" \
  "s_cbranch_vccnz .Lsf_c20\n\t" \
  ".Lsf_r20:\n\t" \
  "v_mfma_f32_16x16x32_bf16 v[36:39], a[208:211], v[48:51], v[36:39]\n\t" \
  "ds_read_b64_tr_b16 a[150:151], v210 offset:12544\n\t" \
  "v_max3_f32 v192, v32, v33, v34\n\t" \
  "v_mfma_f32_16x16x32_bf16 v[40:43], a[224:227], v[48:51], v[180:183]\n\t" \
  "ds_read_b64_tr_b16 a[152:153], v211 offset:256\n\t" \
  "v_cvt_pk_bf16_f32 v152, v96, v97\n\t" \
  "v_mfma_f32_16x16x32_bf16 v[40:43], a[228:231], v[52:55], v[40:43]\n\t" \
  "v_exp_f32 v98, v98\n\t" \
  "v_mfma_f32_16x16x32_bf16 v[40:43], a[232:235], v[56:59], v[40:43]\n\t" \
  "v_max3_f32 v203, v35, v36, v37\n\t" \
  "v_max3_f32 v192, v192, v38, v39\n\t" \
  "v_mfma_f32_16x16x32_bf16 v[40:43], a[236:239], v[60:63], v[40:43]\n\t" \
  "v_exp_f32 v99, v99\n\t" \
  "v_mfma_f32_16x16x32_bf16 v[44:47], a[252:255], v[60:63], v[180:183]\n\t" \
  "ds_read_b64_tr_b16 a[154:155], v211 offset:4352\n\t" \
  "v_cvt_pk_bf16_f32 v153, v98, v99\n\t" \
  "v_mfma_f32_16x16x32_bf16 v[44:47], a[248:251], v[56:59], v[44:47]\n\t" \
  "v_exp_f32 v100, v100\n\t" \
  "v_mfma_f32_16x16x32_bf16 v[44:47], a[244:247], v[52:55], v[44:47]\n\t" \
  "ds_read_b64_tr_b16 a[156:157], v211 offset:8448\n\t" \
  "v_max3_f32 v203, v203, v40, v41\n\t" \
  "v_mfma_f32_16x16x32_bf16 v[44:47], a[240:243], v[48:51], v[44:47]\n\t" \
  "ds_read_b64_tr_b16 a[158:159], v211 offset:12544\n\t" \
  "v_max3_f32 v192, v192, v42, v43\n\t" \
  "v_mfma_f32_16x16x32_bf16 v[64:67], a[192:195], v[80:83], v[184:187]\n\t" \
  "v_exp_f32 v101, v101\n\t" \
  "v_mfma_f32_16x16x32_bf16 v[64:67], a[196:199], v[84:87], v[64:67]\n\t" \
  "ds_read_b64_tr_b16 a[160:161], v210 offset:512\n\t" \
  "v_cvt_pk_bf16_f32 v154, v100, v101\n\t" \
  "v_mfma_f32_16x16x32_bf16 v[64:67], a[200:203], v[88:91], v[64:67]\n\t" \
  "v_max3_f32 v203, v203, v44, v45\n\t" \
  "v_max3_f32 v192, v192, v46, v47\n\t" \
  "v_mfma_f32_16x16x32_bf16 v[64:67], a[204:207], v[92:95], v[64:67]\n\t" \
  "v_max_f32 v192, v192, v203\n\t" \
  "v_cmp_lt_f32 vcc, 0x42800000, v192\n\t" \
  "v_mfma_f32_16x16x32_bf16 v[68:71], a[220:223], v[92:95], v[184:187]\n\t" \
  "v_exp_f32 v102, v102\n\t" \
  "v_mfma_f32_16x16x32_bf16 v[68:71], a[216:219], v[88:91], v[68:71]\n\t" \
  "s_cbranch_vccnz .Lsf_c21\n\t" \
  ".Lsf_r21:\n\t" \
  "v_mfma_f32_16x16x32_bf16 v[68:71], a[212:215], v[84:87], v[68:71]\n\t" \
  "ds_read_b64_tr_b16 a[162:163], v210 offset:4608\n\t" \
  "v_max3_f32 v192, v64, v65, v66\n\t" \
  "v_mfma_f32_16x16x32_bf16 v[68:71], a[208:211], v[80:83], v[68:71]\n\t" \
  "ds_read_b64_tr_b16 a[164:165], v210 offset:8704\n\t" \
  "v_exp_f32 v103, v103\n\t" \
  "v_mfma_f32_16x16x32_bf16 v[72:75], a[224:227], v[80:83], v[184:187]\n\t" \
  "ds_read_b64_tr_b16 a[166:167], v210 offset:12800\n\t" \
  "v_exp_f32 v104, v104\n\t" \
  "v_mfma_f32_16x16x32_bf16 v[72:75], a[228:231], v[84:87], v[72:75]\n\t" \
  "ds_read_b64_tr_b16 a[168:169], v211 offset:512\n\t" \
  "v_exp_f32 v105, v105\n\t" \
  "v_mfma_f32_16x16x32_bf16 v[72:75], a[232:235], v[88:91], v[72:75]\n\t" \
  "v_max3_f32 v203, v67, v68, v69\n\t" \
  "v_max3_f32 v192, v192, v70, v71\n\t" \
  "v_mfma_f32_16x16x32_bf16 v[72:75], a[236:239], v[92:95], v[72:75]\n\t" \
  "ds_read_b64_tr_b16 a[170:171], v211 offset:4608\n\t" \
  "v_exp_f32 v106, v106\n\t" \
  "v_mfma_f32_16x16x32_bf16 v[76:79], a[252:255], v[92:95], v[184:187]\n\t" \
  "ds_read_b64_tr_b16 a[172:173], v211 offset:8704\n\t" \
  "v_exp_f32 v107, v107\n\t" \
  "v_mfma_f32_16x16x32_bf16 v[76:79], a[248:251], v[88:91], v[76:79]\n\t" \
  "ds_read_b64_tr_b16 a[174:175], v211 offset:12800\n\t" \
  "v_cvt_pk_bf16_f32 v155, v102, v103\n\t" \
  "v_mfma_f32_16x16x32_bf16 v[76:79], a[244:247], v[84:87], v[76:79]\n\t" \
  "v_max3_f32 v203, v203, v72, v73\n\t" \
  "v_max3_f32 v192, v192, v74, v75\n\t" \
  "v_mfma_f32_16x16x32_bf16 v[76:79], a[240:243], v[80:83], v[76:79]\n\t" \
  "ds_read_b64_tr_b16 a[176:177], v210 offset:768\n\t" \
  "v_exp_f32 v108, v108\n\t" \
  "v_mfma_f32_16x16x32_bf16 v[96:99], a[192:195], v[112:115], v[188:191]\n\t" \
  "ds_read_b64_tr_b16 a[178:179], v210 offset:4864\n\t" \
  "v_exp_f32 v109, v109\n\t" \
  "v_mfma_f32_16x16x32_bf16 v[96:99], a[196:199], v[116:119], v[96:99]\n\t" \
  "ds_read_b64_tr_b16 a[180:181], v210 offset:8960\n\t" \
  "v_cvt_pk_bf16_f32 v156, v104, v105\n\t" \
  "v_mfma_f32_16x16x32_bf16 v[96:99], a[200:203], v[120:123], v[96:99]\n\t" \
  "v_max3_f32 v203, v203, v76, v77\n\t" \
  "v_max3_f32 v192, v192, v78, v79\n\t" \
  "v_mfma_f32_16x16x32_bf16 v[96:99], a[204:207], v[124:127], v[96:99]\n\t" \
  "v_max_f32 v192, v192, v203\n\t" \
  "v_cmp_lt_f32 vcc, 0x42800000, v192\n\t" \
  "v_mfma_f32_16x16x32_bf16 v[100:103], a[220:223], v[124:127], v[188:191]\n\t" \
  "ds_read_b64_tr_b16 a[182:183], v210 offset:13056\n\t" \
  "v_exp_f32 v110, v110\n\t" \
  "v_mfma_f32_16x16x32_bf16 v[100:103], a[216:219], v[120:123], v[100:103]\n\t" \
  "s_cbranch_vccnz .Lsf_c22\n\t" \
  ".Lsf_r22:\n\t" \
  "v_mfma_f32_16x16x32_bf16 v[100:103], a[212:215], v[116:119], v[100:103]\n\t" \
  "v_max3_f32 v192, v96, v97, v98\n\t" \
  "v_exp_f32 v111, v111\n\t" \
  "v_mfma_f32_16x16x32_bf16 v[100:103], a[208:211], v[112:115], v[100:103]\n\t" \
  "ds_read_b64_tr_b16 a[184:185], v211 offset:768\n\t" \
  "v_cvt_pk_bf16_f32 v157, v106, v107\n\t" \
  "v_mfma_f32_16x16x32_bf16 v[104:107], a[224:227], v[112:115], v[188:191]\n\t" \
  "ds_read_b64_tr_b16 a[186:187], v211 offset:4864\n\t" \
  "v_cvt_pk_bf16_f32 v158, v108, v109\n\t" \
  "v_mfma_f32_16x16x32_bf16 v[104:107], a[228:231], v[116:119], v[104:107]\n\t" \
  "ds_read_b64_tr_b16 a[188:189], v211 offset:8960\n\t" \
  "v_cvt_pk_bf16_f32 v159, v110, v111\n\t" \
  "v_mfma_f32_16x16x32_bf16 v[104:107], a[232:235], v[120:123], v[104:107]\n\t" \
  "v_max3_f32 v203, v99, v100, v101\n\t" \
  "v_max3_f32 v192, v192, v102, v103\n\t" \
  "v_mfma_f32_16x16x32_bf16 v[104:107], a[236:239], v[124:127], v[104:107]\n\t" \
  "ds_read_b64_tr_b16 a[190:191], v211 offset:13056\n\t" \
  "v_mfma_f32_16x16x32_bf16 v[108:111], a[252:255], v[124:127], v[188:191]\n\t" \
  "v_exp_f32 v0, v0\n\t" \
  "v_mfma_f32_16x16x32_bf16 v[108:111], a[248:251], v[120:123], v[108:111]\n\t" \
  "v_exp_f32 v1, v1\n\t" \
  "v_mfma_f32_16x16x32_bf16 v[108:111], a[244:247], v[116:119], v[108:111]\n\t" \
  "s_waitcnt vmcnt(0) lgkmcnt(0)\n\t" \
  "v_exp_f32 v2, v2\n\t" \
  "v_mfma_f32_16x16x32_bf16 v[108:111], a[240:243], v[112:115], v[108:111]\n\t" \
  "s_barrier\n\t" \
  "v_mfma_f32_16x16x32_bf16 a[0:3], a[128:131], v[128:131], a[0:3]\n\t" \
  "v_max3_f32 v203, v203, v104, v105\n\t" \
  "v_max3_f32 v192, v192, v106, v107\n\t" \
  "v_mfma_f32_16x16x32_bf16 a[0:3], a[132:135], v[132:135], a[0:3]\n\t" \
  "v_exp_f32 v3, v3\n\t" \
  "v_mfma_f32_16x16x32_bf16 a[4:7], a[136:139], v[128:131], a[4:7]\n\t" \
  "v_exp_f32 v4, v4\n\t" \
  "v_mfma_f32_16x16x32_bf16 a[4:7], a[140:143], v[132:135], a[4:7]\n\t" \
  "v_max3_f32 v203, v203, v108, v109\n\t" \
  "v_max3_f32 v192, v192, v110, v111\n\t" \
  "v_mfma_f32_16x16x32_bf16 a[8:11], a[144:147], v[128:131], a[8:11]\n\t" \
  "v_max_f32 v192, v192, v203\n\t" \
  "v_cmp_lt_f32 vcc, 0x42800000, v192\n\t" \
  "v_mfma_f32_16x16x32_bf16 a[8:11], a[148:151], v[132:135], a[8:11]\n\t" \
  "v_exp_f32 v5, v5\n\t" \
  "v_mfma_f32_16x16x32_bf16 a[12:15], a[152:155], v[128:131], a[12:15]\n\t" \
  "s_cbranch_vccnz .Lsf_c23\n\t" \
  ".Lsf_r23:\n\t" \
  "v_mfma_f32_16x16x32_bf16 a[12:15], a[156:159], v[132:135], a[12:15]\n\t" \
  "s_add_u32 m0, s38, 49152\n\t" \
  "v_exp_f32 v6, v6\n\t" \
  "v_mfma_f32_16x16x32_bf16 a[16:19], a[160:163], v[128:131], a[16:19]\n\t" \
  "s_add_u32 s96, s38, 65536\n\t" \
  "v_exp_f32 v7, v7\n\t" \
  "v_mfma_f32_16x16x32_bf16 a[16:19], a[164:167], v[132:135], a[16:19]\n\t" \
  "v_exp_f32 v8, v8\n\t" \
  "v_mfma_f32_16x16x32_bf16 a[20:23], a[168:171], v[128:131], a[20:23]\n\t" \
  "buffer_load_dwordx4 v224, s[60:63], s96 offen offset:0 lds\n\t" \
  "v_mfma_f32_16x16x32_bf16 a[20:23], a[172:175], v[132:135], a[20:23]\n\t" \
  "v_exp_f32 v9, v9\n\t" \
  "v_mfma_f32_16x16x32_bf16 a[24:27], a[176:179], v[128:131], a[24:27]\n\t" \
  "v_exp_f32 v10, v10\n\t" \
  "v_mfma_f32_16x16x32_bf16 a[24:27], a[180:183], v[132:135], a[24:27]\n\t" \
  "v_exp_f32 v11, v11\n\t" \
  "v_mfma_f32_16x16x32_bf16 a[28:31], a[184:187], v[128:131], a[28:31]\n\t" \
  "v_exp_f32 v12, v12\n\t" \
  "v_mfma_f32_16x16x32_bf16 a[28:31], a[188:191], v[132:135], a[28:31]\n\t" \
  "v_exp_f32 v13, v13\n\t" \
  "v_smfmac_f32_16x16x64_bf16 v[160:163], v[216:219], v[128:135], v208\n\t" \
  "ds_read_b128 a[192:195], v220 offset:16384\n\t" \
  "v_cvt_pk_bf16_f32 v128, v0, v1\n\t" \
  "v_mfma_f32_16x16x32_bf16 a[32:35], a[128:131], v[136:139], a[32:35]\n\t" \
  "buffer_load_dwordx4 v225, s[60:63], s96 offen offset:1024 lds\n\t" \
  "v_mfma_f32_16x16x32_bf16 a[32:35], a[132:135], v[140:143], a[32:35]\n\t" \
  "v_exp_f32 v14, v14\n\t" \
  "v_mfma_f32_16x16x32_bf16 a[36:39], a[136:139], v[136:139], a[36:39]\n\t" \
  "v_exp_f32 v15, v15\n\t" \
  "v_mfma_f32_16x16x32_bf16 a[36:39], a[140:143], v[140:143], a[36:39]\n\t" \
  "ds_read_b128 a[196:199], v221 offset:16384\n\t" \
  "v_cvt_pk_bf16_f32 v129, v2, v3\n\t" \
  "v_mfma_f32_16x16x32_bf16 a[40:43], a[144:147], v[136:139], a[40:43]\n\t" \
  "v_exp_f32 v32, v32\n\t" \
  "v_mfma_f32_16x16x32_bf16 a[40:43], a[148:151], v[140:143], a[40:43]\n\t" \
  "v_exp_f32 v33, v33\n\t" \
  "v_mfma_f32_16x16x32_bf16 a[44:47], a[152:155], v[136:139], a[44:47]\n\t" \
  "buffer_load_dwordx4 v226, s[60:63], s96 offen offset:2048 lds\n\t" \
  "v_mfma_f32_16x16x32_bf16 a[44:47], a[156:159], v[140:143], a[44:47]\n\t" \
  "ds_read_b128 a[200:203], v222 offset:16384\n\t" \
  "v_cvt_pk_bf16_f32 v130, v4, v5\n\t" \
  "v_mfma_f32_16x16x32_bf16 a[48:51], a[160:163], v[136:139], a[48:51]\n\t" \
  "v_exp_f32 v34, v34\n\t" \
  "v_mfma_f32_16x16x32_bf16 a[48:51], a[164:167], v[140:143], a[48:51]\n\t" \
  "ds_read_b128 a[204:207], v223 offset:16384\n\t" \
  "v_cvt_pk_bf16_f32 v131, v6, v7\n\t" \
  "v_mfma_f32_16x16x32_bf16 a[52:55], a[168:171], v[136:139], a[52:55]\n\t" \
  "v_exp_f32 v35, v35\n\t" \
  "v_mfma_f32_16x16x32_bf16 a[52:55], a[172:175], v[140:143], a[52:55]\n\t" \
  "ds_read_b128 a[208:211], v220 offset:20480\n\t" \
  "v_cvt_pk_bf16_f32 v132, v8, v9\n\t" \
  "v_mfma_f32_16x16x32_bf16 a[56:59], a[176:179], v[136:139], a[56:59]\n\t" \
  "buffer_load_dwordx4 v227, s[60:63], s96 offen offset:3072 lds\n\t" \
  "v_mfma_f32_16x16x32_bf16 a[56:59], a[180:183], v[140:143], a[56:59]\n\t" \
  "v_exp_f32 v36, v36\n\t" \
  "v_mfma_f32_16x16x32_bf16 a[60:63], a[184:187], v[136:139], a[60:63]\n\t" \
  "s_add_u32 m0, s38, 147456\n\t" \
  "ds_read_b128 a[212:215], v221 offset:20480\n\t" \
  "v_cvt_pk_bf16_f32 v133, v10, v11\n\t" \
  "v_mfma_f32_16x16x32_bf16 a[60:63], a[188:191], v[140:143], a[60:63]\n\t" \
  "s_add_u32 s96, s38, 49152\n\t" \
  "v_exp_f32 v37, v37\n\t" \
  "v_smfmac_f32_16x16x64_bf16 v[164:167], v[216:219], v[136:143], v208\n\t" \
  "ds_read_b128 a[216:219], v222 offset:20480\n\t" \
  "v_cvt_pk_bf16_f32 v134, v12, v13\n\t" \
  "v_mfma_f32_16x16x32_bf16 a[64:67], a[128:131], v[144:147], a[64:67]\n\t" \
  "v_exp_f32 v38, v38\n\t" \
  "v_mfma_f32_16x16x32_bf16 a[64:67], a[132:135], v[148:151], a[64:67]\n\t" \
  "buffer_load_dwordx4 v230, s[64:67], s96 offen offset:0 lds\n\t" \
  "v_mfma_f32_16x16x32_bf16 a[68:71], a[136:139], v[144:147], a[68:71]\n\t" \
  "ds_read_b128 a[220:223], v223 offset:20480\n\t" \
  "v_cvt_pk_bf16_f32 v135, v14, v15\n\t" \
  "v_mfma_f32_16x16x32_bf16 a[68:71], a[140:143], v[148:151], a[68:71]\n\t" \
  "v_exp_f32 v39, v39\n\t" \
  "v_mfma_f32_16x16x32_bf16 a[72:75], a[144:147], v[144:147], a[72:75]\n\t" \
  "v_exp_f32 v40, v40\n\t" \
  "v_mfma_f32_16x16x32_bf16 a[72:75], a[148:151], v[148:151], a[72:75]\n\t" \
  "ds_read_b128 a[224:227], v220 offset:24576\n\t" \
  "v_cvt_pk_bf16_f32 v136, v32, v33\n\t" \
  "v_mfma_f32_16x16x32_bf16 a[76:79], a[152:155], v[144:147], a[76:79]\n\t" \
  "ds_read_b128 a[228:231], v221 offset:24576\n\t" \
  "v_cvt_pk_bf16_f32 v137, v34, v35\n\t" \
  "v_mfma_f32_16x16x32_bf16 a[76:79], a[156:159], v[148:151], a[76:79]\n\t" \
  "v_exp_f32 v41, v41\n\t" \
  "v_mfma_f32_16x16x32_bf16 a[80:83], a[160:163], v[144:147], a[80:83]\n\t" \
  "buffer_load_dwordx4 v231, s[64:67], s96 offen offset:1024 lds\n\t" \
  "v_mfma_f32_16x16x32_bf16 a[80:83], a[164:167], v[148:151], a[80:83]\n\t" \
  "v_exp_f32 v42, v42\n\t" \
  "v_mfma_f32_16x16x32_bf16 a[84:87], a[168:171], v[144:147], a[84:87]\n\t" \
  "ds_read_b128 a[232:235], v222 offset:24576\n\t" \
  "v_cvt_pk_bf16_f32 v138, v36, v37\n\t" \
  "v_mfma_f32_16x16x32_bf16 a[84:87], a[172:175], v[148:151], a[84:87]\n\t" \
  "v_exp_f32 v43, v43\n\t" \
  "v_mfma_f32_16x16x32_bf16 a[88:91], a[176:179], v[144:147], a[88:91]\n\t" \
  "ds_read_b128 a[236:239], v223 offset:24576\n\t" \
  "v_cvt_pk_bf16_f32 v139, v38, v39\n\t" \
  "v_mfma_f32_16x16x32_bf16 a[88:91], a[180:183], v[148:151], a[88:91]\n\t" \
  "v_exp_f32 v44, v44\n\t" \
  "v_mfma_f32_16x16x32_bf16 a[92:95], a[184:187], v[144:147], a[92:95]\n\t" \
  "buffer_load_dwordx4 v230, s[64:67], s96 offen offset:2048 lds\n\t" \
  "v_mfma_f32_16x16x32_bf16 a[92:95], a[188:191], v[148:151], a[92:95]\n\t" \
  "ds_read_b128 a[240:243], v220 offset:28672\n\t" \
  "v_cvt_pk_bf16_f32 v140, v40, v41\n\t" \
  "v_smfmac_f32_16x16x64_bf16 v[168:171], v[216:219], v[144:151], v208\n\t" \
  "ds_read_b128 a[244:247], v221 offset:28672\n\t" \
  "v_cvt_pk_bf16_f32 v141, v42, v43\n\t" \
  "v_mfma_f32_16x16x32_bf16 a[96:99], a[128:131], v[152:155], a[96:99]\n\t" \
  "v_exp_f32 v45, v45\n\t" \
  "v_mfma_f32_16x16x32_bf16 a[96:99], a[132:135], v[156:159], a[96:99]\n\t" \
  "v_exp_f32 v46, v46\n\t" \
  "v_mfma_f32_16x16x32_bf16 a[100:103], a[136:139], v[152:155], a[100:103]\n\t" \
  "v_exp_f32 v47, v47\n\t" \
  "v_mfma_f32_16x16x32_bf16 a[100:103], a[140:143], v[156:159], a[100:103]\n\t" \
  "buffer_load_dwordx4 v231, s[64:67], s96 offen offset:3072 lds\n\t" \
  "v_mfma_f32_16x16x32_bf16 a[104:107], a[144:147], v[152:155], a[104:107]\n\t" \
  "ds_read_b128 a[248:251], v222 offset:28672\n\t" \
  "v_cvt_pk_bf16_f32 v142, v44, v45\n\t" \
  "v_mfma_f32_16x16x32_bf16 a[104:107], a[148:151], v[156:159], a[104:107]\n\t" \
  "ds_read_b128 a[252:255], v223 offset:28672\n\t" \
  "v_cvt_pk_bf16_f32 v143, v46, v47\n\t" \
  "v_mfma_f32_16x16x32_bf16 a[108:111], a[152:155], v[152:155], a[108:111]\n\t" \
  "v_exp_f32 v64, v64\n\t" \
  "v_mfma_f32_16x16x32_bf16 a[108:111], a[156:159], v[156:159], a[108:111]\n\t" \
  "v_exp_f32 v65, v65\n\t" \
  "v_mfma_f32_16x16x32_bf16 a[112:115], a[160:163], v[152:155], a[112:115]\n\t" \
  "v_exp_f32 v66, v66\n\t" \
  "v_mfma_f32_16x16x32_bf16 a[112:115], a[164:167], v[156:159], a[112:115]\n\t" \
  "v_exp_f32 v67, v67\n\t" \
  "v_mfma_f32_16x16x32_bf16 a[116:119], a[168:171], v[152:155], a[116:119]\n\t" \
  "v_exp_f32 v68, v68\n\t" \
  "v_mfma_f32_16x16x32_bf16 a[116:119], a[172:175], v[156:159], a[116:119]\n\t" \
  "v_exp_f32 v69, v69\n\t" \
  "v_mfma_f32_16x16x32_bf16 a[120:123], a[176:179], v[152:155], a[120:123]\n\t" \
  "v_exp_f32 v70, v70\n\t" \
  "v_mfma_f32_16x16x32_bf16 a[120:123], a[180:183], v[156:159], a[120:123]\n\t" \
  "v_exp_f32 v71, v71\n\t" \
  "v_mfma_f32_16x16x32_bf16 a[124:127], a[184:187], v[152:155], a[124:127]\n\t" \
  "v_exp_f32 v72, v72\n\t" \
  "v_mfma_f32_16x16x32_bf16 a[124:127], a[188:191], v[156:159], a[124:127]\n\t" \
  "v_exp_f32 v73, v73\n\t" \
  "v_smfmac_f32_16x16x64_bf16 v[172:175], v[216:219], v[152:159], v208\n\t" \
  "s_waitcnt lgkmcnt(12)\n\t" \
  "s_cmp_lg_u32 s99, 0\n\t" \
  "s_cbranch_scc1 .Lsf_p2\n\t" \
  ".Lsf_q2:\n\t" \
  "v_mfma_f32_16x16x32_bf16 v[0:3], a[192:195], v[16:19], v[176:179]\n\t" \
  "ds_read_b64_tr_b16 a[128:129], v228 offset:0\n\t" \
  "v_cvt_pk_bf16_f32 v144, v64, v65\n\t" \
  "v_mfma_f32_16x16x32_bf16 v[0:3], a[196:199], v[20:23], v[0:3]\n\t" \
  "v_exp_f32 v74, v74\n\t" \
  "v_mfma_f32_16x16x32_bf16 v[0:3], a[200:203], v[24:27], v[0:3]\n\t" \
  "ds_read_b64_tr_b16 a[130:131], v228 offset:4096\n\t" \
  "v_cvt_pk_bf16_f32 v145, v66, v67\n\t" \
  "v_mfma_f32_16x16x32_bf16 v[0:3], a[204:207], v[28:31], v[0:3]\n\t" \
  "s_waitcnt lgkmcnt(10)\n\t" \
  "v_exp_f32 v75, v75\n\t" \
  "v_mfma_f32_16x16x32_bf16 v[4:7], a[220:223], v[28:31], v[176:179]\n\t" \
  "ds_read_b64_tr_b16 a[132:133], v228 offset:8192\n\t" \
  "v_cvt_pk_bf16_f32 v146, v68, v69\n\t" \
  "v_mfma_f32_16x16x32_bf16 v[4:7], a[216:219], v[24:27], v[4:7]\n\t" \
  "v_exp_f32 v76, v76\n\t" \
  "v_mfma_f32_16x16x32_bf16 v[4:7], a[212:215], v[20:23], v[4:7]\n\t" \
  "ds_read_b64_tr_b16 a[134:135], v228 offset:12288\n\t" \
  "v_max3_f32 v192, v0, v1, v2\n\t" \
  "v_mfma_f32_16x16x32_bf16 v[4:7], a[208:211], v[16:19], v[4:7]\n\t" \
  "s_waitcnt lgkmcnt(8)\n\t" \
  "ds_read_b64_tr_b16 a[136:137], v229 offset:0\n\t" \
  "v_cvt_pk_bf16_f32 v147, v70, v71\n\t" \
  "v_mfma_f32_16x16x32_bf16 v[8:11], a[224:227], v[16:19], v[176:179]\n\t" \
  "v_exp_f32 v77, v77\n\t" \
  "v_mfma_f32_16x16x32_bf16 v[8:11], a[228:231], v[20:23], v[8:11]\n\t" \
  "ds_read_b64_tr_b16 a[138:139], v229 offset:4096\n\t" \
  "v_cvt_pk_bf16_f32 v148, v72, v73\n\t" \
  "v_mfma_f32_16x16x32_bf16 v[8:11], a[232:235], v[24:27], v[8:11]\n\t" \
  "v_max3_f32 v203, v3, v4, v5\n\t" \
  "v_max3_f32 v192, v192, v6, v7\n\t" \
  "v_mfma_f32_16x16x32_bf16 v[8:11], a[236:239], v[28:31], v[8:11]\n\t" \
  "s_waitcnt lgkmcnt(6)\n\t" \
  "ds_read_b64_tr_b16 a[140:141], v229 offset:8192\n\t" \
  "v_cvt_pk_bf16_f32 v149, v74, v75\n\t" \
  "v_mfma_f32_16x16x32_bf16 v[12:15], a[252:255], v[28:31], v[176:179]\n\t" \
  "v_exp_f32 v78, v78\n\t" \
  "v_mfma_f32_16x16x32_bf16 v[12:15], a[248:251], v[24:27], v[12:15]\n\t" \
  "ds_read_b64_tr_b16 a[142:143], v229 offset:12288\n\t" \
  "v_cvt_pk_bf16_f32 v150, v76, v77\n\t" \
  "v_mfma_f32_16x16x32_bf16 v[12:15], a[244:247], v[20:23], v[12:15]\n\t" \
  "v_max3_f32 v203, v203, v8, v9\n\t" \
  "v_max3_f32 v192, v192, v10, v11\n\t" \
  "v_mfma_f32_16x16x32_bf16 v[12:15], a[240:243], v[16:19], v[12:15]\n\t" \
  "v_exp_f32 v79, v79\n\t" \
  "v_mfma_f32_16x16x32_bf16 v[32:35], a[192:195], v[48:51], v[180:183]\n\t" \
  "ds_read_b64_tr_b16 a[144:145], v228 offset:256\n\t" \
  "v_cvt_pk_bf16_f32 v151, v78, v79\n\t" \
  "v_mfma_f32_16x16x32_bf16 v[32:35], a[196:199], v[52:55], v[32:35]\n\t" \
  "v_exp_f32 v96, v96\n\t" \
  "v_mfma_f32_16x16x32_bf16 v[32:35], a[200:203], v[56:59], v[32:35]\n\t" \
  "ds_read_b64_tr_b16 a[146:147], v228 offset:4352\n\t" \
  "v_max3_f32 v203, v203, v12, v13\n\t" \
  "v_mfma_f32_16x16x32_bf16 v[32:35], a[204:207], v[60:63], v[32:35]\n\t" \
  "v_max3_f32 v192, v192, v14, v15\n\t" \
  "v_max_f32 v192, v192, v203\n\t" \
  "v_mfma_f32_16x16x32_bf16 v[36:39], a[220:223], v[60:63], v[180:183]\n\t" \
  "ds_read_b64_tr_b16 a[148:149], v228 offset:8448\n\t" \
  "v_cmp_lt_f32 vcc, 0x42800000, v192\n\t" \
  "v_mfma_f32_16x16x32_bf16 v[36:39], a[216:219], v[56:59], v[36:39]\n\t" \
  "v_exp_f32 v97, v97\n\t" \
  "v_mfma_f32_16x16x32_bf16 v[36:39], a[212:215], v[52:55], v[36:39]\n\t" \
  "s_cbranch_vccnz .Lsf_c30\n\t" \
  ".Lsf_r30:\n\t" \
  "v_mfma_f32_16x16x32_bf16 v[36:39], a[208:211], v[48:51], v[36:39]\n\t" \
  "ds_read_b64_tr_b16 a[150:151], v228 offset:12544\n\t" \
  "v_max3_f32 v192, v32, v33, v34\n\t" \
  "v_mfma_f32_16x16x32_bf16 v[40:43], a[224:227], v[48:51], v[180:183]\n\t" \
  "ds_read_b64_tr_b16 a[152:153], v229 offset:256\n\t" \
  "v_cvt_pk_bf16_f32 v152, v96, v97\n\t" \
  "v_mfma_f32_16x16x32_bf16 v[40:43], a[228:231], v[52:55], v[40:43]\n\t" \
  "v_exp_f32 v98, v98\n\t" \
  "v_mfma_f32_16x16x32_bf16 v[40:43], a[232:235], v[56:59], v[40:43]\n\t" \
  "v_max3_f32 v203, v35, v36, v37\n\t" \
  "v_max3_f32 v192, v192, v38, v39\n\t" \
  "v_mfma_f32_16x16x32_bf16 v[40:43], a[236:239], v[60:63], v[40:43]\n\t" \
  "v_exp_f32 v99, v99\n\t" \
  "v_mfma_f32_16x16x32_bf16 v[44:47], a[252:255], v[60:63], v[180:183]\n\t" \
  "ds_read_b64_tr_b16 a[154:155], v229 offset:4352\n\t" \
  "v_cvt_pk_bf16_f32 v153, v98, v99\n\t" \
  "v_mfma_f32_16x16x32_bf16 v[44:47], a[248:251], v[56:59], v[44:47]\n\t" \
  "v_exp_f32 v100, v100\n\t" \
  "v_mfma_f32_16x16x32_bf16 v[44:47], a[244:247], v[52:55], v[44:47]\n\t" \
  "ds_read_b64_tr_b16 a[156:157], v229 offset:8448\n\t" \
  "v_max3_f32 v203, v203, v40, v41\n\t" \
  "v_mfma_f32_16x16x32_bf16 v[44:47], a[240:243], v[48:51], v[44:47]\n\t" \
  "ds_read_b64_tr_b16 a[158:159], v229 offset:12544\n\t" \
  "v_max3_f32 v192, v192, v42, v43\n\t" \
  "v_mfma_f32_16x16x32_bf16 v[64:67], a[192:195], v[80:83], v[184:187]\n\t" \
  "v_exp_f32 v101, v101\n\t" \
  "v_mfma_f32_16x16x32_bf16 v[64:67], a[196:199], v[84:87], v[64:67]\n\t" \
  "ds_read_b64_tr_b16 a[160:161], v228 offset:512\n\t" \
  "v_cvt_pk_bf16_f32 v154, v100, v101\n\t" \
  "v_mfma_f32_16x16x32_bf16 v[64:67], a[200:203], v[88:91], v[64:67]\n\t" \
  "v_max3_f32 v203, v203, v44, v45\n\t" \
  "v_max3_f32 v192, v192, v46, v47\n\t" \
  "v_mfma_f32_16x16x32_bf16 v[64:67], a[204:207], v[92:95], v[64:67]\n\t" \
  "v_max_f32 v192, v192, v203\n\t" \
  "v_cmp_lt_f32 vcc, 0x42800000, v192\n\t" \
  "v_mfma_f32_16x16x32_bf16 v[68:71], a[220:223], v[92:95], v[184:187]\n\t" \
  "v_exp_f32 v102, v102\n\t" \
  "v_mfma_f32_16x16x32_bf16 v[68:71], a[216:219], v[88:91], v[68:71]\n\t" \
  "s_cbranch_vccnz .Lsf_c31\n\t" \
  ".Lsf_r31:\n\t" \
  "v_mfma_f32_16x16x32_bf16 v[68:71], a[212:215], v[84:87], v[68:71]\n\t" \
  "ds_read_b64_tr_b16 a[162:163], v228 offset:4608\n\t" \
  "v_max3_f32 v192, v64, v65, v66\n\t" \
  "v_mfma_f32_16x16x32_bf16 v[68:71], a[208:211], v[80:83], v[68:71]\n\t" \
  "ds_read_b64_tr_b16 a[164:165], v228 offset:8704\n\t" \
  "v_exp_f32 v103, v103\n\t" \
  "v_mfma_f32_16x16x32_bf16 v[72:75], a[224:227], v[80:83], v[184:187]\n\t" \
  "ds_read_b64_tr_b16 a[166:167], v228 offset:12800\n\t" \
  "v_exp_f32 v104, v104\n\t" \
  "v_mfma_f32_16x16x32_bf16 v[72:75], a[228:231], v[84:87], v[72:75]\n\t" \
  "ds_read_b64_tr_b16 a[168:169], v229 offset:512\n\t" \
  "v_exp_f32 v105, v105\n\t" \
  "v_mfma_f32_16x16x32_bf16 v[72:75], a[232:235], v[88:91], v[72:75]\n\t" \
  "v_max3_f32 v203, v67, v68, v69\n\t" \
  "v_max3_f32 v192, v192, v70, v71\n\t" \
  "v_mfma_f32_16x16x32_bf16 v[72:75], a[236:239], v[92:95], v[72:75]\n\t" \
  "ds_read_b64_tr_b16 a[170:171], v229 offset:4608\n\t" \
  "v_exp_f32 v106, v106\n\t" \
  "v_mfma_f32_16x16x32_bf16 v[76:79], a[252:255], v[92:95], v[184:187]\n\t" \
  "ds_read_b64_tr_b16 a[172:173], v229 offset:8704\n\t" \
  "v_exp_f32 v107, v107\n\t" \
  "v_mfma_f32_16x16x32_bf16 v[76:79], a[248:251], v[88:91], v[76:79]\n\t" \
  "ds_read_b64_tr_b16 a[174:175], v229 offset:12800\n\t" \
  "v_cvt_pk_bf16_f32 v155, v102, v103\n\t" \
  "v_mfma_f32_16x16x32_bf16 v[76:79], a[244:247], v[84:87], v[76:79]\n\t" \
  "v_max3_f32 v203, v203, v72, v73\n\t" \
  "v_max3_f32 v192, v192, v74, v75\n\t" \
  "v_mfma_f32_16x16x32_bf16 v[76:79], a[240:243], v[80:83], v[76:79]\n\t" \
  "ds_read_b64_tr_b16 a[176:177], v228 offset:768\n\t" \
  "v_exp_f32 v108, v108\n\t" \
  "v_mfma_f32_16x16x32_bf16 v[96:99], a[192:195], v[112:115], v[188:191]\n\t" \
  "ds_read_b64_tr_b16 a[178:179], v228 offset:4864\n\t" \
  "v_exp_f32 v109, v109\n\t" \
  "v_mfma_f32_16x16x32_bf16 v[96:99], a[196:199], v[116:119], v[96:99]\n\t" \
  "ds_read_b64_tr_b16 a[180:181], v228 offset:8960\n\t" \
  "v_cvt_pk_bf16_f32 v156, v104, v105\n\t" \
  "v_mfma_f32_16x16x32_bf16 v[96:99], a[200:203], v[120:123], v[96:99]\n\t" \
  "v_max3_f32 v203, v203, v76, v77\n\t" \
  "v_max3_f32 v192, v192, v78, v79\n\t" \
  "v_mfma_f32_16x16x32_bf16 v[96:99], a[204:207], v[124:127], v[96:99]\n\t" \
  "v_max_f32 v192, v192, v203\n\t" \
  "v_cmp_lt_f32 vcc, 0x42800000, v192\n\t" \
  "v_mfma_f32_16x16x32_bf16 v[100:103], a[220:223], v[124:127], v[188:191]\n\t" \
  "ds_read_b64_tr_b16 a[182:183], v228 offset:13056\n\t" \
  "v_exp_f32 v110, v110\n\t" \
  "v_mfma_f32_16x16x32_bf16 v[100:103], a[216:219], v[120:123], v[100:103]\n\t" \
  "s_cbranch_vccnz .Lsf_c32\n\t" \
  ".Lsf_r32:\n\t" \
  "v_mfma_f32_16x16x32_bf16 v[100:103], a[212:215], v[116:119], v[100:103]\n\t" \
  "v_max3_f32 v192, v96, v97, v98\n\t" \
  "v_exp_f32 v111, v111\n\t" \
  "v_mfma_f32_16x16x32_bf16 v[100:103], a[208:211], v[112:115], v[100:103]\n\t" \
  "ds_read_b64_tr_b16 a[184:185], v229 offset:768\n\t" \
  "v_cvt_pk_bf16_f32 v157, v106, v107\n\t" \
  "v_mfma_f32_16x16x32_bf16 v[104:107], a[224:227], v[112:115], v[188:191]\n\t" \
  "ds_read_b64_tr_b16 a[186:187], v229 offset:4864\n\t" \
  "v_cvt_pk_bf16_f32 v158, v108, v109\n\t" \
  "v_mfma_f32_16x16x32_bf16 v[104:107], a[228:231], v[116:119], v[104:107]\n\t" \
  "ds_read_b64_tr_b16 a[188:189], v229 offset:8960\n\t" \
  "v_cvt_pk_bf16_f32 v159, v110, v111\n\t" \
  "v_mfma_f32_16x16x32_bf16 v[104:107], a[232:235], v[120:123], v[104:107]\n\t" \
  "v_max3_f32 v203, v99, v100, v101\n\t" \
  "v_max3_f32 v192, v192, v102, v103\n\t" \
  "v_mfma_f32_16x16x32_bf16 v[104:107], a[236:239], v[124:127], v[104:107]\n\t" \
  "ds_read_b64_tr_b16 a[190:191], v229 offset:13056\n\t" \
  "v_mfma_f32_16x16x32_bf16 v[108:111], a[252:255], v[124:127], v[188:191]\n\t" \
  "v_exp_f32 v0, v0\n\t" \
  "v_mfma_f32_16x16x32_bf16 v[108:111], a[248:251], v[120:123], v[108:111]\n\t" \
  "v_exp_f32 v1, v1\n\t" \
  "v_mfma_f32_16x16x32_bf16 v[108:111], a[244:247], v[116:119], v[108:111]\n\t" \
  "s_waitcnt vmcnt(0) lgkmcnt(0)\n\t" \
  "v_exp_f32 v2, v2\n\t" \
  "v_mfma_f32_16x16x32_bf16 v[108:111], a[240:243], v[112:115], v[108:111]\n\t" \
  "s_barrier\n\t" \
  "v_mfma_f32_16x16x32_bf16 a[0:3], a[128:131], v[128:131], a[0:3]\n\t" \
  "v_max3_f32 v203, v203, v104, v105\n\t" \
  "v_max3_f32 v192, v192, v106, v107\n\t" \
  "v_mfma_f32_16x16x32_bf16 a[0:3], a[132:135], v[132:135], a[0:3]\n\t" \
  "v_exp_f32 v3, v3\n\t" \
  "v_mfma_f32_16x16x32_bf16 a[4:7], a[136:139], v[128:131], a[4:7]\n\t" \
  "v_exp_f32 v4, v4\n\t" \
  "v_mfma_f32_16x16x32_bf16 a[4:7], a[140:143], v[132:135], a[4:7]\n\t" \
  "v_max3_f32 v203, v203, v108, v109\n\t" \
  "v_max3_f32 v192, v192, v110, v111\n\t" \
  "v_mfma_f32_16x16x32_bf16 a[8:11], a[144:147], v[128:131], a[8:11]\n\t" \
  "v_max_f32 v192, v192, v203\n\t" \
  "v_cmp_lt_f32 vcc, 0x42800000, v192\n\t" \
  "v_mfma_f32_16x16x32_bf16 a[8:11], a[148:151], v[132:135], a[8:11]\n\t" \
  "v_exp_f32 v5, v5\n\t" \
  "v_mfma_f32_16x16x32_bf16 a[12:15], a[152:155], v[128:131], a[12:15]\n\t" \
  "s_cbranch_vccnz .Lsf_c33\n\t" \
  ".Lsf_r33:\n\t" \
  "v_mfma_f32_16x16x32_bf16 a[12:15], a[156:159], v[132:135], a[12:15]\n\t" \
  "s_add_u32 m0, s38, 65536\n\t" \
  "v_exp_f32 v6, v6\n\t" \
  "v_mfma_f32_16x16x32_bf16 a[16:19], a[160:163], v[128:131], a[16:19]\n\t" \
  "s_add_u32 s96, s38, 81920\n\t" \
  "v_exp_f32 v7, v7\n\t" \
  "v_mfma_f32_16x16x32_bf16 a[16:19], a[164:167], v[132:135], a[16:19]\n\t" \
  "v_exp_f32 v8, v8\n\t" \
  "v_mfma_f32_16x16x32_bf16 a[20:23], a[168:171], v[128:131], a[20:23]\n\t" \
  "buffer_load_dwordx4 v224, s[60:63], s96 offen offset:0 lds\n\t" \
  "v_mfma_f32_16x16x32_bf16 a[20:23], a[172:175], v[132:135], a[20:23]\n\t" \
  "v_exp_f32 v9, v9\n\t" \
  "v_mfma_f32_16x16x32_bf16 a[24:27], a[176:179], v[128:131], a[24:27]\n\t" \
  "v_exp_f32 v10, v10\n\t" \
  "v_mfma_f32_16x16x32_bf16 a[24:27], a[180:183], v[132:135], a[24:27]\n\t" \
  "v_exp_f32 v11, v11\n\t" \
  "v_mfma_f32_16x16x32_bf16 a[28:31], a[184:187], v[128:131], a[28:31]\n\t" \
  "v_exp_f32 v12, v12\n\t" \
  "v_mfma_f32_16x16x32_bf16 a[28:31], a[188:191], v[132:135], a[28:31]\n\t" \
  "v_exp_f32 v13, v13\n\t" \
  "v_smfmac_f32_16x16x64_bf16 v[160:163], v[216:219], v[128:135], v208\n\t" \
  "ds_read_b128 a[192:195], v220 offset:0\n\t" \
  "v_cvt_pk_bf16_f32 v128, v0, v1\n\t" \
  "v_mfma_f32_16x16x32_bf16 a[32:35], a[128:131], v[136:139], a[32:35]\n\t" \
  "buffer_load_dwordx4 v225, s[60:63], s96 offen offset:1024 lds\n\t" \
  "v_mfma_f32_16x16x32_bf16 a[32:35], a[132:135], v[140:143], a[32:35]\n\t" \
  "v_exp_f32 v14, v14\n\t" \
  "v_mfma_f32_16x16x32_bf16 a[36:39], a[136:139], v[136:139], a[36:39]\n\t" \
  "v_exp_f32 v15, v15\n\t" \
  "v_mfma_f32_16x16x32_bf16 a[36:39], a[140:143], v[140:143], a[36:39]\n\t" \
  "ds_read_b128 a[196:199], v221 offset:0\n\t" \
  "v_cvt_pk_bf16_f32 v129, v2, v3\n\t" \
  "v_mfma_f32_16x16x32_bf16 a[40:43], a[144:147], v[136:139], a[40:43]\n\t" \
  "v_exp_f32 v32, v32\n\t" \
  "v_mfma_f32_16x16x32_bf16 a[40:43], a[148:151], v[140:143], a[40:43]\n\t" \
  "v_exp_f32 v33, v33\n\t" \
  "v_mfma_f32_16x16x32_bf16 a[44:47], a[152:155], v[136:139], a[44:47]\n\t" \
  "buffer_load_dwordx4 v226, s[60:63], s96 offen offset:2048 lds\n\t" \
  "v_mfma_f32_16x16x32_bf16 a[44:47], a[156:159], v[140:143], a[44:47]\n\t" \
  "ds_read_b128 a[200:203], v222 offset:0\n\t" \
  "v_cvt_pk_bf16_f32 v130, v4, v5\n\t" \
  "v_mfma_f32_16x16x32_bf16 a[48:51], a[160:163], v[136:139], a[48:51]\n\t" \
  "v_exp_f32 v34, v34\n\t" \
  "v_mfma_f32_16x16x32_bf16 a[48:51], a[164:167], v[140:143], a[48:51]\n\t" \
  "ds_read_b128 a[204:207], v223 offset:0\n\t" \
  "v_cvt_pk_bf16_f32 v131, v6, v7\n\t" \
  "v_mfma_f32_16x16x32_bf16 a[52:55], a[168:171], v[136:139], a[52:55]\n\t" \
  "v_exp_f32 v35, v35\n\t" \
  "v_mfma_f32_16x16x32_bf16 a[52:55], a[172:175], v[140:143], a[52:55]\n\t" \
  "ds_read_b128 a[208:211], v220 offset:4096\n\t" \
  "v_cvt_pk_bf16_f32 v132, v8, v9\n\t" \
  "v_mfma_f32_16x16x32_bf16 a[56:59], a[176:179], v[136:139], a[56:59]\n\t" \
  "buffer_load_dwordx4 v227, s[60:63], s96 offen offset:3072 lds\n\t" \
  "v_mfma_f32_16x16x32_bf16 a[56:59], a[180:183], v[140:143], a[56:59]\n\t" \
  "v_exp_f32 v36, v36\n\t" \
  "v_mfma_f32_16x16x32_bf16 a[60:63], a[184:187], v[136:139], a[60:63]\n\t" \
  "s_add_u32 m0, s38, 81920\n\t" \
  "ds_read_b128 a[212:215], v221 offset:4096\n\t" \
  "v_cvt_pk_bf16_f32 v133, v10, v11\n\t" \
  "v_mfma_f32_16x16x32_bf16 a[60:63], a[188:191], v[140:143], a[60:63]\n\t" \
  "s_add_u32 s96, s38, 65536\n\t" \
  "v_exp_f32 v37, v37\n\t" \
  "v_smfmac_f32_16x16x64_bf16 v[164:167], v[216:219], v[136:143], v208\n\t" \
  "ds_read_b128 a[216:219], v222 offset:4096\n\t" \
  "v_cvt_pk_bf16_f32 v134, v12, v13\n\t" \
  "v_mfma_f32_16x16x32_bf16 a[64:67], a[128:131], v[144:147], a[64:67]\n\t" \
  "v_exp_f32 v38, v38\n\t" \
  "v_mfma_f32_16x16x32_bf16 a[64:67], a[132:135], v[148:151], a[64:67]\n\t" \
  "buffer_load_dwordx4 v230, s[64:67], s96 offen offset:0 lds\n\t" \
  "v_mfma_f32_16x16x32_bf16 a[68:71], a[136:139], v[144:147], a[68:71]\n\t" \
  "ds_read_b128 a[220:223], v223 offset:4096\n\t" \
  "v_cvt_pk_bf16_f32 v135, v14, v15\n\t" \
  "v_mfma_f32_16x16x32_bf16 a[68:71], a[140:143], v[148:151], a[68:71]\n\t" \
  "v_exp_f32 v39, v39\n\t" \
  "v_mfma_f32_16x16x32_bf16 a[72:75], a[144:147], v[144:147], a[72:75]\n\t" \
  "v_exp_f32 v40, v40\n\t" \
  "v_mfma_f32_16x16x32_bf16 a[72:75], a[148:151], v[148:151], a[72:75]\n\t" \
  "ds_read_b128 a[224:227], v220 offset:8192\n\t" \
  "v_cvt_pk_bf16_f32 v136, v32, v33\n\t" \
  "v_mfma_f32_16x16x32_bf16 a[76:79], a[152:155], v[144:147], a[76:79]\n\t" \
  "ds_read_b128 a[228:231], v221 offset:8192\n\t" \
  "v_cvt_pk_bf16_f32 v137, v34, v35\n\t" \
  "v_mfma_f32_16x16x32_bf16 a[76:79], a[156:159], v[148:151], a[76:79]\n\t" \
  "v_exp_f32 v41, v41\n\t" \
  "v_mfma_f32_16x16x32_bf16 a[80:83], a[160:163], v[144:147], a[80:83]\n\t" \
  "buffer_load_dwordx4 v231, s[64:67], s96 offen offset:1024 lds\n\t" \
  "v_mfma_f32_16x16x32_bf16 a[80:83], a[164:167], v[148:151], a[80:83]\n\t" \
  "v_exp_f32 v42, v42\n\t" \
  "v_mfma_f32_16x16x32_bf16 a[84:87], a[168:171], v[144:147], a[84:87]\n\t" \
  "ds_read_b128 a[232:235], v222 offset:8192\n\t" \
  "v_cvt_pk_bf16_f32 v138, v36, v37\n\t" \
  "v_mfma_f32_16x16x32_bf16 a[84:87], a[172:175], v[148:151], a[84:87]\n\t" \
  "v_exp_f32 v43, v43\n\t" \
  "v_mfma_f32_16x16x32_bf16 a[88:91], a[176:179], v[144:147], a[88:91]\n\t" \
  "ds_read_b128 a[236:239], v223 offset:8192\n\t" \
  "v_cvt_pk_bf16_f32 v139, v38, v39\n\t" \
  "v_mfma_f32_16x16x32_bf16 a[88:91], a[180:183], v[148:151], a[88:91]\n\t" \
  "v_exp_f32 v44, v44\n\t" \
  "v_mfma_f32_16x16x32_bf16 a[92:95], a[184:187], v[144:147], a[92:95]\n\t" \
  "buffer_load_dwordx4 v230, s[64:67], s96 offen offset:2048 lds\n\t" \
  "v_mfma_f32_16x16x32_bf16 a[92:95], a[188:191], v[148:151], a[92:95]\n\t" \
  "ds_read_b128 a[240:243], v220 offset:12288\n\t" \
  "v_cvt_pk_bf16_f32 v140, v40, v41\n\t" \
  "v_smfmac_f32_16x16x64_bf16 v[168:171], v[216:219], v[144:151], v208\n\t" \
  "ds_read_b128 a[244:247], v221 offset:12288\n\t" \
  "v_cvt_pk_bf16_f32 v141, v42, v43\n\t" \
  "v_mfma_f32_16x16x32_bf16 a[96:99], a[128:131], v[152:155], a[96:99]\n\t" \
  "v_exp_f32 v45, v45\n\t" \
  "v_mfma_f32_16x16x32_bf16 a[96:99], a[132:135], v[156:159], a[96:99]\n\t" \
  "v_exp_f32 v46, v46\n\t" \
  "v_mfma_f32_16x16x32_bf16 a[100:103], a[136:139], v[152:155], a[100:103]\n\t" \
  "v_exp_f32 v47, v47\n\t" \
  "v_mfma_f32_16x16x32_bf16 a[100:103], a[140:143], v[156:159], a[100:103]\n\t" \
  "buffer_load_dwordx4 v231, s[64:67], s96 offen offset:3072 lds\n\t" \
  "v_mfma_f32_16x16x32_bf16 a[104:107], a[144:147], v[152:155], a[104:107]\n\t" \
  "ds_read_b128 a[248:251], v222 offset:12288\n\t" \
  "v_cvt_pk_bf16_f32 v142, v44, v45\n\t" \
  "v_mfma_f32_16x16x32_bf16 a[104:107], a[148:151], v[156:159], a[104:107]\n\t" \
  "ds_read_b128 a[252:255], v223 offset:12288\n\t" \
  "v_cvt_pk_bf16_f32 v143, v46, v47\n\t" \
  "v_mfma_f32_16x16x32_bf16 a[108:111], a[152:155], v[152:155], a[108:111]\n\t" \
  "v_exp_f32 v64, v64\n\t" \
  "v_mfma_f32_16x16x32_bf16 a[108:111], a[156:159], v[156:159], a[108:111]\n\t" \
  "v_exp_f32 v65, v65\n\t" \
  "v_mfma_f32_16x16x32_bf16 a[112:115], a[160:163], v[152:155], a[112:115]\n\t" \
  "v_exp_f32 v66, v66\n\t" \
  "v_mfma_f32_16x16x32_bf16 a[112:115], a[164:167], v[156:159], a[112:115]\n\t" \
  "v_exp_f32 v67, v67\n\t" \
  "v_mfma_f32_16x16x32_bf16 a[116:119], a[168:171], v[152:155], a[116:119]\n\t" \
  "v_exp_f32 v68, v68\n\t" \
  "v_mfma_f32_16x16x32_bf16 a[116:119], a[172:175], v[156:159], a[116:119]\n\t" \
  "v_exp_f32 v69, v69\n\t" \
  "v_mfma_f32_16x16x32_bf16 a[120:123], a[176:179], v[152:155], a[120:123]\n\t" \
  "v_exp_f32 v70, v70\n\t" \
  "v_mfma_f32_16x16x32_bf16 a[120:123], a[180:183], v[156:159], a[120:123]\n\t" \
  "v_exp_f32 v71, v71\n\t" \
  "v_mfma_f32_16x16x32_bf16 a[124:127], a[184:187], v[152:155], a[124:127]\n\t" \
  "v_exp_f32 v72, v72\n\t" \
  "v_mfma_f32_16x16x32_bf16 a[124:127], a[188:191], v[156:159], a[124:127]\n\t" \
  "v_exp_f32 v73, v73\n\t" \
  "v_smfmac_f32_16x16x64_bf16 v[172:175], v[216:219], v[152:159], v208\n\t" \
  "s_waitcnt lgkmcnt(12)\n\t" \
  "s_cmp_lg_u32 s99, 0\n\t" \
  "s_cbranch_scc1 .Lsf_p3\n\t" \
  ".Lsf_q3:\n\t" \
  "v_mfma_f32_16x16x32_bf16 v[0:3], a[192:195], v[16:19], v[176:179]\n\t" \
  "ds_read_b64_tr_b16 a[128:129], v210 offset:0\n\t" \
  "v_cvt_pk_bf16_f32 v144, v64, v65\n\t" \
  "v_mfma_f32_16x16x32_bf16 v[0:3], a[196:199], v[20:23], v[0:3]\n\t" \
  "v_exp_f32 v74, v74\n\t" \
  "v_mfma_f32_16x16x32_bf16 v[0:3], a[200:203], v[24:27], v[0:3]\n\t" \
  "ds_read_b64_tr_b16 a[130:131], v210 offset:4096\n\t" \
  "v_cvt_pk_bf16_f32 v145, v66, v67\n\t" \
  "v_mfma_f32_16x16x32_bf16 v[0:3], a[204:207], v[28:31], v[0:3]\n\t" \
  "s_waitcnt lgkmcnt(10)\n\t" \
  "v_exp_f32 v75, v75\n\t" \
  "v_mfma_f32_16x16x32_bf16 v[4:7], a[220:223], v[28:31], v[176:179]\n\t" \
  "ds_read_b64_tr_b16 a[132:133], v210 offset:8192\n\t" \
  "v_cvt_pk_bf16_f32 v146, v68, v69\n\t" \
  "v_mfma_f32_16x16x32_bf16 v[4:7], a[216:219], v[24:27], v[4:7]\n\t" \
  "v_exp_f32 v76, v76\n\t" \
  "v_mfma_f32_16x16x32_bf16 v[4:7], a[212:215], v[20:23], v[4:7]\n\t" \
  "ds_read_b64_tr_b16 a[134:135], v210 offset:12288\n\t" \
  "v_max3_f32 v192, v0, v1, v2\n\t" \
  "v_mfma_f32_16x16x32_bf16 v[4:7], a[208:211], v[16:19], v[4:7]\n\t" \
  "s_waitcnt lgkmcnt(8)\n\t" \
  "ds_read_b64_tr_b16 a[136:137], v211 offset:0\n\t" \
  "v_cvt_pk_bf16_f32 v147, v70, v71\n\t" \
  "v_mfma_f32_16x16x32_bf16 v[8:11], a[224:227], v[16:19], v[176:179]\n\t" \
  "v_exp_f32 v77, v77\n\t" \
  "v_mfma_f32_16x16x32_bf16 v[8:11], a[228:231], v[20:23], v[8:11]\n\t" \
  "ds_read_b64_tr_b16 a[138:139], v211 offset:4096\n\t" \
  "v_cvt_pk_bf16_f32 v148, v72, v73\n\t" \
  "v_mfma_f32_16x16x32_bf16 v[8:11], a[232:235], v[24:27], v[8:11]\n\t" \
  "v_max3_f32 v203, v3, v4, v5\n\t" \
  "v_max3_f32 v192, v192, v6, v7\n\t" \
  "v_mfma_f32_16x16x32_bf16 v[8:11], a[236:239], v[28:31], v[8:11]\n\t" \
  "s_waitcnt lgkmcnt(6)\n\t" \
  "ds_read_b64_tr_b16 a[140:141], v211 offset:8192\n\t" \
  "v_cvt_pk_bf16_f32 v149, v74, v75\n\t" \
  "v_mfma_f32_16x16x32_bf16 v[12:15], a[252:255], v[28:31], v[176:179]\n\t" \
  "v_exp_f32 v78, v78\n\t" \
  "v_mfma_f32_16x16x32_bf16 v[12:15], a[248:251], v[24:27], v[12:15]\n\t" \
  "ds_read_b64_tr_b16 a[142:143], v211 offset:12288\n\t" \
  "v_cvt_pk_bf16_f32 v150, v76, v77\n\t" \
  "v_mfma_f32_16x16x32_bf16 v[12:15], a[244:247], v[20:23], v[12:15]\n\t" \
  "v_max3_f32 v203, v203, v8, v9\n\t" \
  "v_max3_f32 v192, v192, v10, v11\n\t" \
  "v_mfma_f32_16x16x32_bf16 v[12:15], a[240:243], v[16:19], v[12:15]\n\t" \
  "v_exp_f32 v79, v79\n\t" \
  "v_mfma_f32_16x16x32_bf16 v[32:35], a[192:195], v[48:51], v[180:183]\n\t" \
  "ds_read_b64_tr_b16 a[144:145], v210 offset:256\n\t" \
  "v_cvt_pk_bf16_f32 v151, v78, v79\n\t" \
  "v_mfma_f32_16x16x32_bf16 v[32:35], a[196:199], v[52:55], v[32:35]\n\t" \
  "v_exp_f32 v96, v96\n\t" \
  "v_mfma_f32_16x16x32_bf16 v[32:35], a[200:203], v[56:59], v[32:35]\n\t" \
  "ds_read_b64_tr_b16 a[146:147], v210 offset:4352\n\t" \
  "v_max3_f32 v203, v203, v12, v13\n\t" \
  "v_mfma_f32_16x16x32_bf16 v[32:35], a[204:207], v[60:63], v[32:35]\n\t" \
  "v_max3_f32 v192, v192, v14, v15\n\t" \
  "v_max_f32 v192, v192, v203\n\t" \
  "v_mfma_f32_16x16x32_bf16 v[36:39], a[220:223], v[60:63], v[180:183]\n\t" \
  "ds_read_b64_tr_b16 a[148:149], v210 offset:8448\n\t" \
  "v_cmp_lt_f32 vcc, 0x42800000, v192\n\t" \
  "v_mfma_f32_16x16x32_bf16 v[36:39], a[216:219], v[56:59], v[36:39]\n\t" \
  "v_exp_f32 v97, v97\n\t" \
  "v_mfma_f32_16x16x32_bf16 v[36:39], a[212:215], v[52:55], v[36:39]\n\t" \
  "s_cbranch_vccnz .Lsf_c40\n\t" \
  ".Lsf_r40:\n\t" \
  "v_mfma_f32_16x16x32_bf16 v[36:39], a[208:211], v[48:51], v[36:39]\n\t" \
  "ds_read_b64_tr_b16 a[150:151], v210 offset:12544\n\t" \
  "v_max3_f32 v192, v32, v33, v34\n\t" \
  "v_mfma_f32_16x16x32_bf16 v[40:43], a[224:227], v[48:51], v[180:183]\n\t" \
  "ds_read_b64_tr_b16 a[152:153], v211 offset:256\n\t" \
  "v_cvt_pk_bf16_f32 v152, v96, v97\n\t" \
  "v_mfma_f32_16x16x32_bf16 v[40:43], a[228:231], v[52:55], v[40:43]\n\t" \
  "v_exp_f32 v98, v98\n\t" \
  "v_mfma_f32_16x16x32_bf16 v[40:43], a[232:235], v[56:59], v[40:43]\n\t" \
  "v_max3_f32 v203, v35, v36, v37\n\t" \
  "v_max3_f32 v192, v192, v38, v39\n\t" \
  "v_mfma_f32_16x16x32_bf16 v[40:43], a[236:239], v[60:63], v[40:43]\n\t" \
  "v_exp_f32 v99, v99\n\t" \
  "v_mfma_f32_16x16x32_bf16 v[44:47], a[252:255], v[60:63], v[180:183]\n\t" \
  "ds_read_b64_tr_b16 a[154:155], v211 offset:4352\n\t" \
  "v_cvt_pk_bf16_f32 v153, v98, v99\n\t" \
  "v_mfma_f32_16x16x32_bf16 v[44:47], a[248:251], v[56:59], v[44:47]\n\t" \
  "v_exp_f32 v100, v100\n\t" \
  "v_mfma_f32_16x16x32_bf16 v[44:47], a[244:247], v[52:55], v[44:47]\n\t" \
  "ds_read_b64_tr_b16 a[156:157], v211 offset:8448\n\t" \
  "v_max3_f32 v203, v203, v40, v41\n\t" \
  "v_mfma_f32_16x16x32_bf16 v[44:47], a[240:243], v[48:51], v[44:47]\n\t" \
  "ds_read_b64_tr_b16 a[158:159], v211 offset:12544\n\t" \
  "v_max3_f32 v192, v192, v42, v43\n\t" \
  "v_mfma_f32_16x16x32_bf16 v[64:67], a[192:195], v[80:83], v[184:187]\n\t" \
  "v_exp_f32 v101, v101\n\t" \
  "v_mfma_f32_16x16x32_bf16 v[64:67], a[196:199], v[84:87], v[64:67]\n\t" \
  "ds_read_b64_tr_b16 a[160:161], v210 offset:512\n\t" \
  "v_cvt_pk_bf16_f32 v154, v100, v101\n\t" \
  "v_mfma_f32_16x16x32_bf16 v[64:67], a[200:203], v[88:91], v[64:67]\n\t" \
  "v_max3_f32 v203, v203, v44, v45\n\t" \
  "v_max3_f32 v192, v192, v46, v47\n\t" \
  "v_mfma_f32_16x16x32_bf16 v[64:67], a[204:207], v[92:95], v[64:67]\n\t" \
  "v_max_f32 v192, v192, v203\n\t" \
  "v_cmp_lt_f32 vcc, 0x42800000, v192\n\t" \
  "v_mfma_f32_16x16x32_bf16 v[68:71], a[220:223], v[92:95], v[184:187]\n\t" \
  "v_exp_f32 v102, v102\n\t" \
  "v_mfma_f32_16x16x32_bf16 v[68:71], a[216:219], v[88:91], v[68:71]\n\t" \
  "s_cbranch_vccnz .Lsf_c41\n\t" \
  ".Lsf_r41:\n\t" \
  "v_mfma_f32_16x16x32_bf16 v[68:71], a[212:215], v[84:87], v[68:71]\n\t" \
  "ds_read_b64_tr_b16 a[162:163], v210 offset:4608\n\t" \
  "v_max3_f32 v192, v64, v65, v66\n\t" \
  "v_mfma_f32_16x16x32_bf16 v[68:71], a[208:211], v[80:83], v[68:71]\n\t" \
  "ds_read_b64_tr_b16 a[164:165], v210 offset:8704\n\t" \
  "v_exp_f32 v103, v103\n\t" \
  "v_mfma_f32_16x16x32_bf16 v[72:75], a[224:227], v[80:83], v[184:187]\n\t" \
  "ds_read_b64_tr_b16 a[166:167], v210 offset:12800\n\t" \
  "v_exp_f32 v104, v104\n\t" \
  "v_mfma_f32_16x16x32_bf16 v[72:75], a[228:231], v[84:87], v[72:75]\n\t" \
  "ds_read_b64_tr_b16 a[168:169], v211 offset:512\n\t" \
  "v_exp_f32 v105, v105\n\t" \
  "v_mfma_f32_16x16x32_bf16 v[72:75], a[232:235], v[88:91], v[72:75]\n\t" \
  "v_max3_f32 v203, v67, v68, v69\n\t" \
  "v_max3_f32 v192, v192, v70, v71\n\t" \
  "v_mfma_f32_16x16x32_bf16 v[72:75], a[236:239], v[92:95], v[72:75]\n\t" \
  "ds_read_b64_tr_b16 a[170:171], v211 offset:4608\n\t" \
  "v_exp_f32 v106, v106\n\t" \
  "v_mfma_f32_16x16x32_bf16 v[76:79], a[252:255], v[92:95], v[184:187]\n\t" \
  "ds_read_b64_tr_b16 a[172:173], v211 offset:8704\n\t" \
  "v_exp_f32 v107, v107\n\t" \
  "v_mfma_f32_16x16x32_bf16 v[76:79], a[248:251], v[88:91], v[76:79]\n\t" \
  "ds_read_b64_tr_b16 a[174:175], v211 offset:12800\n\t" \
  "v_cvt_pk_bf16_f32 v155, v102, v103\n\t" \
  "v_mfma_f32_16x16x32_bf16 v[76:79], a[244:247], v[84:87], v[76:79]\n\t" \
  "v_max3_f32 v203, v203, v72, v73\n\t" \
  "v_max3_f32 v192, v192, v74, v75\n\t" \
  "v_mfma_f32_16x16x32_bf16 v[76:79], a[240:243], v[80:83], v[76:79]\n\t" \
  "ds_read_b64_tr_b16 a[176:177], v210 offset:768\n\t" \
  "v_exp_f32 v108, v108\n\t" \
  "v_mfma_f32_16x16x32_bf16 v[96:99], a[192:195], v[112:115], v[188:191]\n\t" \
  "ds_read_b64_tr_b16 a[178:179], v210 offset:4864\n\t" \
  "v_exp_f32 v109, v109\n\t" \
  "v_mfma_f32_16x16x32_bf16 v[96:99], a[196:199], v[116:119], v[96:99]\n\t" \
  "ds_read_b64_tr_b16 a[180:181], v210 offset:8960\n\t" \
  "v_cvt_pk_bf16_f32 v156, v104, v105\n\t" \
  "v_mfma_f32_16x16x32_bf16 v[96:99], a[200:203], v[120:123], v[96:99]\n\t" \
  "v_max3_f32 v203, v203, v76, v77\n\t" \
  "v_max3_f32 v192, v192, v78, v79\n\t" \
  "v_mfma_f32_16x16x32_bf16 v[96:99], a[204:207], v[124:127], v[96:99]\n\t" \
  "v_max_f32 v192, v192, v203\n\t" \
  "v_cmp_lt_f32 vcc, 0x42800000, v192\n\t" \
  "v_mfma_f32_16x16x32_bf16 v[100:103], a[220:223], v[124:127], v[188:191]\n\t" \
  "ds_read_b64_tr_b16 a[182:183], v210 offset:13056\n\t" \
  "v_exp_f32 v110, v110\n\t" \
  "v_mfma_f32_16x16x32_bf16 v[100:103], a[216:219], v[120:123], v[100:103]\n\t" \
  "s_cbranch_vccnz .Lsf_c42\n\t" \
  ".Lsf_r42:\n\t" \
  "v_mfma_f32_16x16x32_bf16 v[100:103], a[212:215], v[116:119], v[100:103]\n\t" \
  "v_max3_f32 v192, v96, v97, v98\n\t" \
  "v_exp_f32 v111, v111\n\t" \
  "v_mfma_f32_16x16x32_bf16 v[100:103], a[208:211], v[112:115], v[100:103]\n\t" \
  "ds_read_b64_tr_b16 a[184:185], v211 offset:768\n\t" \
  "v_cvt_pk_bf16_f32 v157, v106, v107\n\t" \
  "v_mfma_f32_16x16x32_bf16 v[104:107], a[224:227], v[112:115], v[188:191]\n\t" \
  "ds_read_b64_tr_b16 a[186:187], v211 offset:4864\n\t" \
  "v_cvt_pk_bf16_f32 v158, v108, v109\n\t" \
  "v_mfma_f32_16x16x32_bf16 v[104:107], a[228:231], v[116:119], v[104:107]\n\t" \
  "ds_read_b64_tr_b16 a[188:189], v211 offset:8960\n\t" \
  "v_cvt_pk_bf16_f32 v159, v110, v111\n\t" \
  "v_mfma_f32_16x16x32_bf16 v[104:107], a[232:235], v[120:123], v[104:107]\n\t" \
  "v_max3_f32 v203, v99, v100, v101\n\t" \
  "v_max3_f32 v192, v192, v102, v103\n\t" \
  "v_mfma_f32_16x16x32_bf16 v[104:107], a[236:239], v[124:127], v[104:107]\n\t" \
  "ds_read_b64_tr_b16 a[190:191], v211 offset:13056\n\t" \
  "v_mfma_f32_16x16x32_bf16 v[108:111], a[252:255], v[124:127], v[188:191]\n\t" \
  "v_exp_f32 v0, v0\n\t" \
  "v_mfma_f32_16x16x32_bf16 v[108:111], a[248:251], v[120:123], v[108:111]\n\t" \
  "v_exp_f32 v1, v1\n\t" \
  "v_mfma_f32_16x16x32_bf16 v[108:111], a[244:247], v[116:119], v[108:111]\n\t" \
  "s_waitcnt vmcnt(0) lgkmcnt(0)\n\t" \
  "v_exp_f32 v2, v2\n\t" \
  "v_mfma_f32_16x16x32_bf16 v[108:111], a[240:243], v[112:115], v[108:111]\n\t" \
  "s_barrier\n\t" \
  "v_mfma_f32_16x16x32_bf16 a[0:3], a[128:131], v[128:131], a[0:3]\n\t" \
  "v_max3_f32 v203, v203, v104, v105\n\t" \
  "v_max3_f32 v192, v192, v106, v107\n\t" \
  "v_mfma_f32_16x16x32_bf16 a[0:3], a[132:135], v[132:135], a[0:3]\n\t" \
  "v_exp_f32 v3, v3\n\t" \
  "v_mfma_f32_16x16x32_bf16 a[4:7], a[136:139], v[128:131], a[4:7]\n\t" \
  "v_exp_f32 v4, v4\n\t" \
  "v_mfma_f32_16x16x32_bf16 a[4:7], a[140:143], v[132:135], a[4:7]\n\t" \
  "v_max3_f32 v203, v203, v108, v109\n\t" \
  "v_max3_f32 v192, v192, v110, v111\n\t" \
  "v_mfma_f32_16x16x32_bf16 a[8:11], a[144:147], v[128:131], a[8:11]\n\t" \
  "v_max_f32 v192, v192, v203\n\t" \
  "v_cmp_lt_f32 vcc, 0x42800000, v192\n\t" \
  "v_mfma_f32_16x16x32_bf16 a[8:11], a[148:151], v[132:135], a[8:11]\n\t" \
  "v_exp_f32 v5, v5\n\t" \
  "v_mfma_f32_16x16x32_bf16 a[12:15], a[152:155], v[128:131], a[12:15]\n\t" \
  "s_cbranch_vccnz .Lsf_c43\n\t" \
  ".Lsf_r43:\n\t" \
  "v_mfma_f32_16x16x32_bf16 a[12:15], a[156:159], v[132:135], a[12:15]\n\t" \
  "s_add_u32 m0, s38, 49152\n\t" \
  "v_exp_f32 v6, v6\n\t" \
  "v_mfma_f32_16x16x32_bf16 a[16:19], a[160:163], v[128:131], a[16:19]\n\t" \
  "s_add_u32 s96, s38, 98304\n\t" \
  "v_exp_f32 v7, v7\n\t" \
  "v_mfma_f32_16x16x32_bf16 a[16:19], a[164:167], v[132:135], a[16:19]\n\t" \
  "v_exp_f32 v8, v8\n\t" \
  "v_mfma_f32_16x16x32_bf16 a[20:23], a[168:171], v[128:131], a[20:23]\n\t" \
  "buffer_load_dwordx4 v224, s[60:63], s96 offen offset:0 lds\n\t" \
  "v_mfma_f32_16x16x32_bf16 a[20:23], a[172:175], v[132:135], a[20:23]\n\t" \
  "v_exp_f32 v9, v9\n\t" \
  "v_mfma_f32_16x16x32_bf16 a[24:27], a[176:179], v[128:131], a[24:27]\n\t" \
  "v_exp_f32 v10, v10\n\t" \
  "v_mfma_f32_16x16x32_bf16 a[24:27], a[180:183], v[132:135], a[24:27]\n\t" \
  "v_exp_f32 v11, v11\n\t" \
  "v_mfma_f32_16x16x32_bf16 a[28:31], a[184:187], v[128:131], a[28:31]\n\t" \
  "v_exp_f32 v12, v12\n\t" \
  "v_mfma_f32_16x16x32_bf16 a[28:31], a[188:191], v[132:135], a[28:31]\n\t" \
  "v_exp_f32 v13, v13\n\t" \
  "v_smfmac_f32_16x16x64_bf16 v[160:163], v[216:219], v[128:135], v208\n\t" \
  "ds_read_b128 a[192:195], v220 offset:16384\n\t" \
  "v_cvt_pk_bf16_f32 v128, v0, v1\n\t" \
  "v_mfma_f32_16x16x32_bf16 a[32:35], a[128:131], v[136:139], a[32:35]\n\t" \
  "buffer_load_dwordx4 v225, s[60:63], s96 offen offset:1024 lds\n\t" \
  "v_mfma_f32_16x16x32_bf16 a[32:35], a[132:135], v[140:143], a[32:35]\n\t" \
  "v_exp_f32 v14, v14\n\t" \
  "v_mfma_f32_16x16x32_bf16 a[36:39], a[136:139], v[136:139], a[36:39]\n\t" \
  "v_exp_f32 v15, v15\n\t" \
  "v_mfma_f32_16x16x32_bf16 a[36:39], a[140:143], v[140:143], a[36:39]\n\t" \
  "ds_read_b128 a[196:199], v221 offset:16384\n\t" \
  "v_cvt_pk_bf16_f32 v129, v2, v3\n\t" \
  "v_mfma_f32_16x16x32_bf16 a[40:43], a[144:147], v[136:139], a[40:43]\n\t" \
  "v_exp_f32 v32, v32\n\t" \
  "v_mfma_f32_16x16x32_bf16 a[40:43], a[148:151], v[140:143], a[40:43]\n\t" \
  "v_exp_f32 v33, v33\n\t" \
  "v_mfma_f32_16x16x32_bf16 a[44:47], a[152:155], v[136:139], a[44:47]\n\t" \
  "buffer_load_dwordx4 v226, s[60:63], s96 offen offset:2048 lds\n\t" \
  "v_mfma_f32_16x16x32_bf16 a[44:47], a[156:159], v[140:143], a[44:47]\n\t" \
  "ds_read_b128 a[200:203], v222 offset:16384\n\t" \
  "v_cvt_pk_bf16_f32 v130, v4, v5\n\t" \
  "v_mfma_f32_16x16x32_bf16 a[48:51], a[160:163], v[136:139], a[48:51]\n\t" \
  "v_exp_f32 v34, v34\n\t" \
  "v_mfma_f32_16x16x32_bf16 a[48:51], a[164:167], v[140:143], a[48:51]\n\t" \
  "ds_read_b128 a[204:207], v223 offset:16384\n\t" \
  "v_cvt_pk_bf16_f32 v131, v6, v7\n\t" \
  "v_mfma_f32_16x16x32_bf16 a[52:55], a[168:171], v[136:139], a[52:55]\n\t" \
  "v_exp_f32 v35, v35\n\t" \
  "v_mfma_f32_16x16x32_bf16 a[52:55], a[172:175], v[140:143], a[52:55]\n\t" \
  "ds_read_b128 a[208:211], v220 offset:20480\n\t" \
  "v_cvt_pk_bf16_f32 v132, v8, v9\n\t" \
  "v_mfma_f32_16x16x32_bf16 a[56:59], a[176:179], v[136:139], a[56:59]\n\t" \
  "buffer_load_dwordx4 v227, s[60:63], s96 offen offset:3072 lds\n\t" \
  "v_mfma_f32_16x16x32_bf16 a[56:59], a[180:183], v[140:143], a[56:59]\n\t" \
  "v_exp_f32 v36, v36\n\t" \
  "v_mfma_f32_16x16x32_bf16 a[60:63], a[184:187], v[136:139], a[60:63]\n\t" \
  "s_add_u32 m0, s38, 147456\n\t" \
  "ds_read_b128 a[212:215], v221 offset:20480\n\t" \
  "v_cvt_pk_bf16_f32 v133, v10, v11\n\t" \
  "v_mfma_f32_16x16x32_bf16 a[60:63], a[188:191], v[140:143], a[60:63]\n\t" \
  "s_add_u32 s96, s38, 81920\n\t" \
  "v_exp_f32 v37, v37\n\t" \
  "v_smfmac_f32_16x16x64_bf16 v[164:167], v[216:219], v[136:143], v208\n\t" \
  "ds_read_b128 a[216:219], v222 offset:20480\n\t" \
  "v_cvt_pk_bf16_f32 v134, v12, v13\n\t" \
  "v_mfma_f32_16x16x32_bf16 a[64:67], a[128:131], v[144:147], a[64:67]\n\t" \
  "v_exp_f32 v38, v38\n\t" \
  "v_mfma_f32_16x16x32_bf16 a[64:67], a[132:135], v[148:151], a[64:67]\n\t" \
  "buffer_load_dwordx4 v230, s[64:67], s96 offen offset:0 lds\n\t" \
  "v_mfma_f32_16x16x32_bf16 a[68:71], a[136:139], v[144:147], a[68:71]\n\t" \
  "ds_read_b128 a[220:223], v223 offset:20480\n\t" \
  "v_cvt_pk_bf16_f32 v135, v14, v15\n\t" \
  "v_mfma_f32_16x16x32_bf16 a[68:71], a[140:143], v[148:151], a[68:71]\n\t" \
  "v_exp_f32 v39, v39\n\t" \
  "v_mfma_f32_16x16x32_bf16 a[72:75], a[144:147], v[144:147], a[72:75]\n\t" \
  "v_exp_f32 v40, v40\n\t" \
  "v_mfma_f32_16x16x32_bf16 a[72:75], a[148:151], v[148:151], a[72:75]\n\t" \
  "ds_read_b128 a[224:227], v220 offset:24576\n\t" \
  "v_cvt_pk_bf16_f32 v136, v32, v33\n\t" \
  "v_mfma_f32_16x16x32_bf16 a[76:79], a[152:155], v[144:147], a[76:79]\n\t" \
  "ds_read_b128 a[228:231], v221 offset:24576\n\t" \
  "v_cvt_pk_bf16_f32 v137, v34, v35\n\t" \
  "v_mfma_f32_16x16x32_bf16 a[76:79], a[156:159], v[148:151], a[76:79]\n\t" \
  "v_exp_f32 v41, v41\n\t" \
  "v_mfma_f32_16x16x32_bf16 a[80:83], a[160:163], v[144:147], a[80:83]\n\t" \
  "buffer_load_dwordx4 v231, s[64:67], s96 offen offset:1024 lds\n\t" \
  "v_mfma_f32_16x16x32_bf16 a[80:83], a[164:167], v[148:151], a[80:83]\n\t" \
  "v_exp_f32 v42, v42\n\t" \
  "v_mfma_f32_16x16x32_bf16 a[84:87], a[168:171], v[144:147], a[84:87]\n\t" \
  "ds_read_b128 a[232:235], v222 offset:24576\n\t" \
  "v_cvt_pk_bf16_f32 v138, v36, v37\n\t" \
  "v_mfma_f32_16x16x32_bf16 a[84:87], a[172:175], v[148:151], a[84:87]\n\t" \
  "v_exp_f32 v43, v43\n\t" \
  "v_mfma_f32_16x16x32_bf16 a[88:91], a[176:179], v[144:147], a[88:91]\n\t" \
  "ds_read_b128 a[236:239], v223 offset:24576\n\t" \
  "v_cvt_pk_bf16_f32 v139, v38, v39\n\t" \
  "v_mfma_f32_16x16x32_bf16 a[88:91], a[180:183], v[148:151], a[88:91]\n\t" \
  "v_exp_f32 v44, v44\n\t" \
  "v_mfma_f32_16x16x32_bf16 a[92:95], a[184:187], v[144:147], a[92:95]\n\t" \
  "buffer_load_dwordx4 v230, s[64:67], s96 offen offset:2048 lds\n\t" \
  "v_mfma_f32_16x16x32_bf16 a[92:95], a[188:191], v[148:151], a[92:95]\n\t" \
  "ds_read_b128 a[240:243], v220 offset:28672\n\t" \
  "v_cvt_pk_bf16_f32 v140, v40, v41\n\t" \
  "v_smfmac_f32_16x16x64_bf16 v[168:171], v[216:219], v[144:151], v208\n\t" \
  "ds_read_b128 a[244:247], v221 offset:28672\n\t" \
  "v_cvt_pk_bf16_f32 v141, v42, v43\n\t" \
  "v_mfma_f32_16x16x32_bf16 a[96:99], a[128:131], v[152:155], a[96:99]\n\t" \
  "v_exp_f32 v45, v45\n\t" \
  "v_mfma_f32_16x16x32_bf16 a[96:99], a[132:135], v[156:159], a[96:99]\n\t" \
  "v_exp_f32 v46, v46\n\t" \
  "v_mfma_f32_16x16x32_bf16 a[100:103], a[136:139], v[152:155], a[100:103]\n\t" \
  "v_exp_f32 v47, v47\n\t" \
  "v_mfma_f32_16x16x32_bf16 a[100:103], a[140:143], v[156:159], a[100:103]\n\t" \
  "buffer_load_dwordx4 v231, s[64:67], s96 offen offset:3072 lds\n\t" \
  "v_mfma_f32_16x16x32_bf16 a[104:107], a[144:147], v[152:155], a[104:107]\n\t" \
  "ds_read_b128 a[248:251], v222 offset:28672\n\t" \
  "v_cvt_pk_bf16_f32 v142, v44, v45\n\t" \
  "v_mfma_f32_16x16x32_bf16 a[104:107], a[148:151], v[156:159], a[104:107]\n\t" \
  "ds_read_b128 a[252:255], v223 offset:28672\n\t" \
  "v_cvt_pk_bf16_f32 v143, v46, v47\n\t" \
  "v_mfma_f32_16x16x32_bf16 a[108:111], a[152:155], v[152:155], a[108:111]\n\t" \
  "v_exp_f32 v64, v64\n\t" \
  "v_mfma_f32_16x16x32_bf16 a[108:111], a[156:159], v[156:159], a[108:111]\n\t" \
  "v_exp_f32 v65, v65\n\t" \
  "v_mfma_f32_16x16x32_bf16 a[112:115], a[160:163], v[152:155], a[112:115]\n\t" \
  "v_exp_f32 v66, v66\n\t" \
  "v_mfma_f32_16x16x32_bf16 a[112:115], a[164:167], v[156:159], a[112:115]\n\t" \
  "v_exp_f32 v67, v67\n\t" \
  "v_mfma_f32_16x16x32_bf16 a[116:119], a[168:171], v[152:155], a[116:119]\n\t" \
  "v_exp_f32 v68, v68\n\t" \
  "v_mfma_f32_16x16x32_bf16 a[116:119], a[172:175], v[156:159], a[116:119]\n\t" \
  "v_exp_f32 v69, v69\n\t" \
  "v_mfma_f32_16x16x32_bf16 a[120:123], a[176:179], v[152:155], a[120:123]\n\t" \
  "v_exp_f32 v70, v70\n\t" \
  "v_mfma_f32_16x16x32_bf16 a[120:123], a[180:183], v[156:159], a[120:123]\n\t" \
  "v_exp_f32 v71, v71\n\t" \
  "v_mfma_f32_16x16x32_bf16 a[124:127], a[184:187], v[152:155], a[124:127]\n\t" \
  "v_exp_f32 v72, v72\n\t" \
  "v_mfma_f32_16x16x32_bf16 a[124:127], a[188:191], v[156:159], a[124:127]\n\t" \
  "v_exp_f32 v73, v73\n\t" \
  "v_smfmac_f32_16x16x64_bf16 v[172:175], v[216:219], v[152:159], v208\n\t" \
  "s_waitcnt lgkmcnt(12)\n\t" \
  "s_cmp_lg_u32 s99, 0\n\t" \
  "s_cbranch_scc1 .Lsf_p4\n\t" \
  ".Lsf_q4:\n\t" \
  "v_mfma_f32_16x16x32_bf16 v[0:3], a[192:195], v[16:19], v[176:179]\n\t" \
  "ds_read_b64_tr_b16 a[128:129], v228 offset:0\n\t" \
  "v_cvt_pk_bf16_f32 v144, v64, v65\n\t" \
  "v_mfma_f32_16x16x32_bf16 v[0:3], a[196:199], v[20:23], v[0:3]\n\t" \
  "v_exp_f32 v74, v74\n\t" \
  "v_mfma_f32_16x16x32_bf16 v[0:3], a[200:203], v[24:27], v[0:3]\n\t" \
  "ds_read_b64_tr_b16 a[130:131], v228 offset:4096\n\t" \
  "v_cvt_pk_bf16_f32 v145, v66, v67\n\t" \
  "v_mfma_f32_16x16x32_bf16 v[0:3], a[204:207], v[28:31], v[0:3]\n\t" \
  "s_waitcnt lgkmcnt(10)\n\t" \
  "v_exp_f32 v75, v75\n\t" \
  "v_mfma_f32_16x16x32_bf16 v[4:7], a[220:223], v[28:31], v[176:179]\n\t" \
  "ds_read_b64_tr_b16 a[132:133], v228 offset:8192\n\t" \
  "v_cvt_pk_bf16_f32 v146, v68, v69\n\t" \
  "v_mfma_f32_16x16x32_bf16 v[4:7], a[216:219], v[24:27], v[4:7]\n\t" \
  "v_exp_f32 v76, v76\n\t" \
  "v_mfma_f32_16x16x32_bf16 v[4:7], a[212:215], v[20:23], v[4:7]\n\t" \
  "ds_read_b64_tr_b16 a[134:135], v228 offset:12288\n\t" \
  "v_max3_f32 v192, v0, v1, v2\n\t" \
  "v_mfma_f32_16x16x32_bf16 v[4:7], a[208:211], v[16:19], v[4:7]\n\t" \
  "s_waitcnt lgkmcnt(8)\n\t" \
  "ds_read_b64_tr_b16 a[136:137], v229 offset:0\n\t" \
  "v_cvt_pk_bf16_f32 v147, v70, v71\n\t" \
  "v_mfma_f32_16x16x32_bf16 v[8:11], a[224:227], v[16:19], v[176:179]\n\t" \
  "v_exp_f32 v77, v77\n\t" \
  "v_mfma_f32_16x16x32_bf16 v[8:11], a[228:231], v[20:23], v[8:11]\n\t" \
  "ds_read_b64_tr_b16 a[138:139], v229 offset:4096\n\t" \
  "v_cvt_pk_bf16_f32 v148, v72, v73\n\t" \
  "v_mfma_f32_16x16x32_bf16 v[8:11], a[232:235], v[24:27], v[8:11]\n\t" \
  "v_max3_f32 v203, v3, v4, v5\n\t" \
  "v_max3_f32 v192, v192, v6, v7\n\t" \
  "v_mfma_f32_16x16x32_bf16 v[8:11], a[236:239], v[28:31], v[8:11]\n\t" \
  "s_waitcnt lgkmcnt(6)\n\t" \
  "ds_read_b64_tr_b16 a[140:141], v229 offset:8192\n\t" \
  "v_cvt_pk_bf16_f32 v149, v74, v75\n\t" \
  "v_mfma_f32_16x16x32_bf16 v[12:15], a[252:255], v[28:31], v[176:179]\n\t" \
  "v_exp_f32 v78, v78\n\t" \
  "v_mfma_f32_16x16x32_bf16 v[12:15], a[248:251], v[24:27], v[12:15]\n\t" \
  "ds_read_b64_tr_b16 a[142:143], v229 offset:12288\n\t" \
  "v_cvt_pk_bf16_f32 v150, v76, v77\n\t" \
  "v_mfma_f32_16x16x32_bf16 v[12:15], a[244:247], v[20:23], v[12:15]\n\t" \
  "v_max3_f32 v203, v203, v8, v9\n\t" \
  "v_max3_f32 v192, v192, v10, v11\n\t" \
  "v_mfma_f32_16x16x32_bf16 v[12:15], a[240:243], v[16:19], v[12:15]\n\t" \
  "v_exp_f32 v79, v79\n\t" \
  "v_mfma_f32_16x16x32_bf16 v[32:35], a[192:195], v[48:51], v[180:183]\n\t" \
  "ds_read_b64_tr_b16 a[144:145], v228 offset:256\n\t" \
  "v_cvt_pk_bf16_f32 v151, v78, v79\n\t" \
  "v_mfma_f32_16x16x32_bf16 v[32:35], a[196:199], v[52:55], v[32:35]\n\t" \
  "v_exp_f32 v96, v96\n\t" \
  "v_mfma_f32_16x16x32_bf16 v[32:35], a[200:203], v[56:59], v[32:35]\n\t" \
  "ds_read_b64_tr_b16 a[146:147], v228 offset:4352\n\t" \
  "v_max3_f32 v203, v203, v12, v13\n\t" \
  "v_mfma_f32_16x16x32_bf16 v[32:35], a[204:207], v[60:63], v[32:35]\n\t" \
  "v_max3_f32 v192, v192, v14, v15\n\t" \
  "v_max_f32 v192, v192, v203\n\t" \
  "v_mfma_f32_16x16x32_bf16 v[36:39], a[220:223], v[60:63], v[180:183]\n\t" \
  "ds_read_b64_tr_b16 a[148:149], v228 offset:8448\n\t" \
  "v_cmp_lt_f32 vcc, 0x42800000, v192\n\t" \
  "v_mfma_f32_16x16x32_bf16 v[36:39], a[216:219], v[56:59], v[36:39]\n\t" \
  "v_exp_f32 v97, v97\n\t" \
  "v_mfma_f32_16x16x32_bf16 v[36:39], a[212:215], v[52:55], v[36:39]\n\t" \
  "s_cbranch_vccnz .Lsf_c50\n\t" \
  ".Lsf_r50:\n\t" \
  "v_mfma_f32_16x16x32_bf16 v[36:39], a[208:211], v[48:51], v[36:39]\n\t" \
  "ds_read_b64_tr_b16 a[150:151], v228 offset:12544\n\t" \
  "v_max3_f32 v192, v32, v33, v34\n\t" \
  "v_mfma_f32_16x16x32_bf16 v[40:43], a[224:227], v[48:51], v[180:183]\n\t" \
  "ds_read_b64_tr_b16 a[152:153], v229 offset:256\n\t" \
  "v_cvt_pk_bf16_f32 v152, v96, v97\n\t" \
  "v_mfma_f32_16x16x32_bf16 v[40:43], a[228:231], v[52:55], v[40:43]\n\t" \
  "v_exp_f32 v98, v98\n\t" \
  "v_mfma_f32_16x16x32_bf16 v[40:43], a[232:235], v[56:59], v[40:43]\n\t" \
  "v_max3_f32 v203, v35, v36, v37\n\t" \
  "v_max3_f32 v192, v192, v38, v39\n\t" \
  "v_mfma_f32_16x16x32_bf16 v[40:43], a[236:239], v[60:63], v[40:43]\n\t" \
  "v_exp_f32 v99, v99\n\t" \
  "v_mfma_f32_16x16x32_bf16 v[44:47], a[252:255], v[60:63], v[180:183]\n\t" \
  "ds_read_b64_tr_b16 a[154:155], v229 offset:4352\n\t" \
  "v_cvt_pk_bf16_f32 v153, v98, v99\n\t" \
  "v_mfma_f32_16x16x32_bf16 v[44:47], a[248:251], v[56:59], v[44:47]\n\t" \
  "v_exp_f32 v100, v100\n\t" \
  "v_mfma_f32_16x16x32_bf16 v[44:47], a[244:247], v[52:55], v[44:47]\n\t" \
  "ds_read_b64_tr_b16 a[156:157], v229 offset:8448\n\t" \
  "v_max3_f32 v203, v203, v40, v41\n\t" \
  "v_mfma_f32_16x16x32_bf16 v[44:47], a[240:243], v[48:51], v[44:47]\n\t" \
  "ds_read_b64_tr_b16 a[158:159], v229 offset:12544\n\t" \
  "v_max3_f32 v192, v192, v42, v43\n\t" \
  "v_mfma_f32_16x16x32_bf16 v[64:67], a[192:195], v[80:83], v[184:187]\n\t" \
  "v_exp_f32 v101, v101\n\t" \
  "v_mfma_f32_16x16x32_bf16 v[64:67], a[196:199], v[84:87], v[64:67]\n\t" \
  "ds_read_b64_tr_b16 a[160:161], v228 offset:512\n\t" \
  "v_cvt_pk_bf16_f32 v154, v100, v101\n\t" \
  "v_mfma_f32_16x16x32_bf16 v[64:67], a[200:203], v[88:91], v[64:67]\n\t" \
  "v_max3_f32 v203, v203, v44, v45\n\t" \
  "v_max3_f32 v192, v192, v46, v47\n\t" \
  "v_mfma_f32_16x16x32_bf16 v[64:67], a[204:207], v[92:95], v[64:67]\n\t" \
  "v_max_f32 v192, v192, v203\n\t" \
  "v_cmp_lt_f32 vcc, 0x42800000, v192\n\t" \
  "v_mfma_f32_16x16x32_bf16 v[68:71], a[220:223], v[92:95], v[184:187]\n\t" \
  "v_exp_f32 v102, v102\n\t" \
  "v_mfma_f32_16x16x32_bf16 v[68:71], a[216:219], v[88:91], v[68:71]\n\t" \
  "s_cbranch_vccnz .Lsf_c51\n\t" \
  ".Lsf_r51:\n\t" \
  "v_mfma_f32_16x16x32_bf16 v[68:71], a[212:215], v[84:87], v[68:71]\n\t" \
  "ds_read_b64_tr_b16 a[162:163], v228 offset:4608\n\t" \
  "v_max3_f32 v192, v64, v65, v66\n\t" \
  "v_mfma_f32_16x16x32_bf16 v[68:71], a[208:211], v[80:83], v[68:71]\n\t" \
  "ds_read_b64_tr_b16 a[164:165], v228 offset:8704\n\t" \
  "v_exp_f32 v103, v103\n\t" \
  "v_mfma_f32_16x16x32_bf16 v[72:75], a[224:227], v[80:83], v[184:187]\n\t" \
  "ds_read_b64_tr_b16 a[166:167], v228 offset:12800\n\t" \
  "v_exp_f32 v104, v104\n\t" \
  "v_mfma_f32_16x16x32_bf16 v[72:75], a[228:231], v[84:87], v[72:75]\n\t" \
  "ds_read_b64_tr_b16 a[168:169], v229 offset:512\n\t" \
  "v_exp_f32 v105, v105\n\t" \
  "v_mfma_f32_16x16x32_bf16 v[72:75], a[232:235], v[88:91], v[72:75]\n\t" \
  "v_max3_f32 v203, v67, v68, v69\n\t" \
  "v_max3_f32 v192, v192, v70, v71\n\t" \
  "v_mfma_f32_16x16x32_bf16 v[72:75], a[236:239], v[92:95], v[72:75]\n\t" \
  "ds_read_b64_tr_b16 a[170:171], v229 offset:4608\n\t" \
  "v_exp_f32 v106, v106\n\t" \
  "v_mfma_f32_16x16x32_bf16 v[76:79], a[252:255], v[92:95], v[184:187]\n\t" \
  "ds_read_b64_tr_b16 a[172:173], v229 offset:8704\n\t" \
  "v_exp_f32 v107, v107\n\t" \
  "v_mfma_f32_16x16x32_bf16 v[76:79], a[248:251], v[88:91], v[76:79]\n\t" \
  "ds_read_b64_tr_b16 a[174:175], v229 offset:12800\n\t" \
  "v_cvt_pk_bf16_f32 v155, v102, v103\n\t" \
  "v_mfma_f32_16x16x32_bf16 v[76:79], a[244:247], v[84:87], v[76:79]\n\t" \
  "v_max3_f32 v203, v203, v72, v73\n\t" \
  "v_max3_f32 v192, v192, v74, v75\n\t" \
  "v_mfma_f32_16x16x32_bf16 v[76:79], a[240:243], v[80:83], v[76:79]\n\t" \
  "ds_read_b64_tr_b16 a[176:177], v228 offset:768\n\t" \
  "v_exp_f32 v108, v108\n\t" \
  "v_mfma_f32_16x16x32_bf16 v[96:99], a[192:195], v[112:115], v[188:191]\n\t" \
  "ds_read_b64_tr_b16 a[178:179], v228 offset:4864\n\t" \
  "v_exp_f32 v109, v109\n\t" \
  "v_mfma_f32_16x16x32_bf16 v[96:99], a[196:199], v[116:119], v[96:99]\n\t" \
  "ds_read_b64_tr_b16 a[180:181], v228 offset:8960\n\t" \
  "v_cvt_pk_bf16_f32 v156, v104, v105\n\t" \
  "v_mfma_f32_16x16x32_bf16 v[96:99], a[200:203], v[120:123], v[96:99]\n\t" \
  "v_max3_f32 v203, v203, v76, v77\n\t" \
  "v_max3_f32 v192, v192, v78, v79\n\t" \
  "v_mfma_f32_16x16x32_bf16 v[96:99], a[204:207], v[124:127], v[96:99]\n\t" \
  "v_max_f32 v192, v192, v203\n\t" \
  "v_cmp_lt_f32 vcc, 0x42800000, v192\n\t" \
  "v_mfma_f32_16x16x32_bf16 v[100:103], a[220:223], v[124:127], v[188:191]\n\t" \
  "ds_read_b64_tr_b16 a[182:183], v228 offset:13056\n\t" \
  "v_exp_f32 v110, v110\n\t" \
  "v_mfma_f32_16x16x32_bf16 v[100:103], a[216:219], v[120:123], v[100:103]\n\t" \
  "s_cbranch_vccnz .Lsf_c52\n\t" \
  ".Lsf_r52:\n\t" \
  "v_mfma_f32_16x16x32_bf16 v[100:103], a[212:215], v[116:119], v[100:103]\n\t" \
  "v_max3_f32 v192, v96, v97, v98\n\t" \
  "v_exp_f32 v111, v111\n\t" \
  "v_mfma_f32_16x16x32_bf16 v[100:103], a[208:211], v[112:115], v[100:103]\n\t" \
  "ds_read_b64_tr_b16 a[184:185], v229 offset:768\n\t" \
  "v_cvt_pk_bf16_f32 v157, v106, v107\n\t" \
  "v_mfma_f32_16x16x32_bf16 v[104:107], a[224:227], v[112:115], v[188:191]\n\t" \
  "ds_read_b64_tr_b16 a[186:187], v229 offset:4864\n\t" \
  "v_cvt_pk_bf16_f32 v158, v108, v109\n\t" \
  "v_mfma_f32_16x16x32_bf16 v[104:107], a[228:231], v[116:119], v[104:107]\n\t" \
  "ds_read_b64_tr_b16 a[188:189], v229 offset:8960\n\t" \
  "v_cvt_pk_bf16_f32 v159, v110, v111\n\t" \
  "v_mfma_f32_16x16x32_bf16 v[104:107], a[232:235], v[120:123], v[104:107]\n\t" \
  "v_max3_f32 v203, v99, v100, v101\n\t" \
  "v_max3_f32 v192, v192, v102, v103\n\t" \
  "v_mfma_f32_16x16x32_bf16 v[104:107], a[236:239], v[124:127], v[104:107]\n\t" \
  "ds_read_b64_tr_b16 a[190:191], v229 offset:13056\n\t" \
  "v_mfma_f32_16x16x32_bf16 v[108:111], a[252:255], v[124:127], v[188:191]\n\t" \
  "v_exp_f32 v0, v0\n\t" \
  "v_mfma_f32_16x16x32_bf16 v[108:111], a[248:251], v[120:123], v[108:111]\n\t" \
  "v_exp_f32 v1, v1\n\t" \
  "v_mfma_f32_16x16x32_bf16 v[108:111], a[244:247], v[116:119], v[108:111]\n\t" \
  "s_waitcnt vmcnt(0) lgkmcnt(0)\n\t" \
  "v_exp_f32 v2, v2\n\t" \
  "v_mfma_f32_16x16x32_bf16 v[108:111], a[240:243], v[112:115], v[108:111]\n\t" \
  "s_barrier\n\t" \
  "v_mfma_f32_16x16x32_bf16 a[0:3], a[128:131], v[128:131], a[0:3]\n\t" \
  "v_max3_f32 v203, v203, v104, v105\n\t" \
  "v_max3_f32 v192, v192, v106, v107\n\t" \
  "v_mfma_f32_16x16x32_bf16 a[0:3], a[132:135], v[132:135], a[0:3]\n\t" \
  "v_exp_f32 v3, v3\n\t" \
  "v_mfma_f32_16x16x32_bf16 a[4:7], a[136:139], v[128:131], a[4:7]\n\t" \
  "v_exp_f32 v4, v4\n\t" \
  "v_mfma_f32_16x16x32_bf16 a[4:7], a[140:143], v[132:135], a[4:7]\n\t" \
  "v_max3_f32 v203, v203, v108, v109\n\t" \
  "v_max3_f32 v192, v192, v110, v111\n\t" \
  "v_mfma_f32_16x16x32_bf16 a[8:11], a[144:147], v[128:131], a[8:11]\n\t" \
  "v_max_f32 v192, v192, v203\n\t" \
  "v_cmp_lt_f32 vcc, 0x42800000, v192\n\t" \
  "v_mfma_f32_16x16x32_bf16 a[8:11], a[148:151], v[132:135], a[8:11]\n\t" \
  "v_exp_f32 v5, v5\n\t" \
  "v_mfma_f32_16x16x32_bf16 a[12:15], a[152:155], v[128:131], a[12:15]\n\t" \
  "s_cbranch_vccnz .Lsf_c53\n\t" \
  ".Lsf_r53:\n\t" \
  "v_mfma_f32_16x16x32_bf16 a[12:15], a[156:159], v[132:135], a[12:15]\n\t" \
  "s_add_u32 m0, s38, 65536\n\t" \
  "v_exp_f32 v6, v6\n\t" \
  "v_mfma_f32_16x16x32_bf16 a[16:19], a[160:163], v[128:131], a[16:19]\n\t" \
  "s_add_u32 s96, s38, 114688\n\t" \
  "v_exp_f32 v7, v7\n\t" \
  "v_mfma_f32_16x16x32_bf16 a[16:19], a[164:167], v[132:135], a[16:19]\n\t" \
  "v_exp_f32 v8, v8\n\t" \
  "v_mfma_f32_16x16x32_bf16 a[20:23], a[168:171], v[128:131], a[20:23]\n\t" \
  "buffer_load_dwordx4 v224, s[60:63], s96 offen offset:0 lds\n\t" \
  "v_mfma_f32_16x16x32_bf16 a[20:23], a[172:175], v[132:135], a[20:23]\n\t" \
  "v_exp_f32 v9, v9\n\t" \
  "v_mfma_f32_16x16x32_bf16 a[24:27], a[176:179], v[128:131], a[24:27]\n\t" \
  "v_exp_f32 v10, v10\n\t" \
  "v_mfma_f32_16x16x32_bf16 a[24:27], a[180:183], v[132:135], a[24:27]\n\t" \
  "v_exp_f32 v11, v11\n\t" \
  "v_mfma_f32_16x16x32_bf16 a[28:31], a[184:187], v[128:131], a[28:31]\n\t" \
  "v_exp_f32 v12, v12\n\t" \
  "v_mfma_f32_16x16x32_bf16 a[28:31], a[188:191], v[132:135], a[28:31]\n\t" \
  "v_exp_f32 v13, v13\n\t" \
  "v_smfmac_f32_16x16x64_bf16 v[160:163], v[216:219], v[128:135], v208\n\t" \
  "ds_read_b128 a[192:195], v220 offset:0\n\t" \
  "v_cvt_pk_bf16_f32 v128, v0, v1\n\t" \
  "v_mfma_f32_16x16x32_bf16 a[32:35], a[128:131], v[136:139], a[32:35]\n\t" \
  "buffer_load_dwordx4 v225, s[60:63], s96 offen offset:1024 lds\n\t" \
  "v_mfma_f32_16x16x32_bf16 a[32:35], a[132:135], v[140:143], a[32:35]\n\t" \
  "v_exp_f32 v14, v14\n\t" \
  "v_mfma_f32_16x16x32_bf16 a[36:39], a[136:139], v[136:139], a[36:39]\n\t" \
  "ds_read_b128 a[196:199], v221 offset:0\n\t" \
  "v_cvt_pk_bf16_f32 v129, v2, v3\n\t" \
  "v_mfma_f32_16x16x32_bf16 a[36:39], a[140:143], v[140:143], a[36:39]\n\t" \
  "v_exp_f32 v15, v15\n\t" \
  "v_mfma_f32_16x16x32_bf16 a[40:43], a[144:147], v[136:139], a[40:43]\n\t" \
  "v_exp_f32 v32, v32\n\t" \
  "v_mfma_f32_16x16x32_bf16 a[40:43], a[148:151], v[140:143], a[40:43]\n\t" \
  "ds_read_b128 a[200:203], v222 offset:0\n\t" \
  "v_cvt_pk_bf16_f32 v130, v4, v5\n\t" \
  "v_mfma_f32_16x16x32_bf16 a[44:47], a[152:155], v[136:139], a[44:47]\n\t" \
  "buffer_load_dwordx4 v226, s[60:63], s96 offen offset:2048 lds\n\t" \
  "v_mfma_f32_16x16x32_bf16 a[44:47], a[156:159], v[140:143], a[44:47]\n\t" \
  "ds_read_b128 a[204:207], v223 offset:0\n\t" \
  "v_cvt_pk_bf16_f32 v131, v6, v7\n\t" \
  "v_mfma_f32_16x16x32_bf16 a[48:51], a[160:163], v[136:139], a[48:51]\n\t" \
  "v_exp_f32 v33, v33\n\t" \
  "v_mfma_f32_16x16x32_bf16 a[48:51], a[164:167], v[140:143], a[48:51]\n\t" \
  "v_exp_f32 v34, v34\n\t" \
  "v_mfma_f32_16x16x32_bf16 a[52:55], a[168:171], v[136:139], a[52:55]\n\t" \
  "ds_read_b128 a[208:211], v220 offset:4096\n\t" \
  "v_cvt_pk_bf16_f32 v132, v8, v9\n\t" \
  "v_mfma_f32_16x16x32_bf16 a[52:55], a[172:175], v[140:143], a[52:55]\n\t" \
  "v_exp_f32 v35, v35\n\t" \
  "v_mfma_f32_16x16x32_bf16 a[56:59], a[176:179], v[136:139], a[56:59]\n\t" \
  "buffer_load_dwordx4 v227, s[60:63], s96 offen offset:3072 lds\n\t" \
  "v_mfma_f32_16x16x32_bf16 a[56:59], a[180:183], v[140:143], a[56:59]\n\t" \
  "ds_read_b128 a[212:215], v221 offset:4096\n\t" \
  "v_cvt_pk_bf16_f32 v133, v10, v11\n\t" \
  "v_mfma_f32_16x16x32_bf16 a[60:63], a[184:187], v[136:139], a[60:63]\n\t" \
  "s_add_u32 m0, s38, 81920\n\t" \
  "v_exp_f32 v36, v36\n\t" \
  "v_mfma_f32_16x16x32_bf16 a[60:63], a[188:191], v[140:143], a[60:63]\n\t" \
  "s_add_u32 s96, s38, 98304\n\t" \
  "ds_read_b128 a[216:219], v222 offset:4096\n\t" \
  "v_cvt_pk_bf16_f32 v134, v12, v13\n\t" \
  "v_smfmac_f32_16x16x64_bf16 v[164:167], v[216:219], v[136:143], v208\n\t" \
  "ds_read_b128 a[220:223], v223 offset:4096\n\t" \
  "v_cvt_pk_bf16_f32 v135, v14, v15\n\t" \
  "v_mfma_f32_16x16x32_bf16 a[64:67], a[128:131], v[144:147], a[64:67]\n\t" \
  "v_exp_f32 v37, v37\n\t" \
  "v_mfma_f32_16x16x32_bf16 a[64:67], a[132:135], v[148:151], a[64:67]\n\t" \
  "buffer_load_dwordx4 v230, s[64:67], s96 offen offset:0 lds\n\t" \
  "v_mfma_f32_16x16x32_bf16 a[68:71], a[136:139], v[144:147], a[68:71]\n\t" \
  "v_exp_f32 v38, v38\n\t" \
  "v_mfma_f32_16x16x32_bf16 a[68:71], a[140:143], v[148:151], a[68:71]\n\t" \
  "ds_read_b128 a[224:227], v220 offset:8192\n\t" \
  "v_cvt_pk_bf16_f32 v136, v32, v33\n\t" \
  "v_mfma_f32_16x16x32_bf16 a[72:75], a[144:147], v[144:147], a[72:75]\n\t" \
  "v_exp_f32 v39, v39\n\t" \
  "v_mfma_f32_16x16x32_bf16 a[72:75], a[148:151], v[148:151], a[72:75]\n\t" \
  "ds_read_b128 a[228:231], v221 offset:8192\n\t" \
  "v_cvt_pk_bf16_f32 v137, v34, v35\n\t" \
  "v_mfma_f32_16x16x32_bf16 a[76:79], a[152:155], v[144:147], a[76:79]\n\t" \
  "v_exp_f32 v40, v40\n\t" \
  "v_mfma_f32_16x16x32_bf16 a[76:79], a[156:159], v[148:151], a[76:79]\n\t" \
  "v_exp_f32 v41, v41\n\t" \
  "v_mfma_f32_16x16x32_bf16 a[80:83], a[160:163], v[144:147], a[80:83]\n\t" \
  "buffer_load_dwordx4 v231, s[64:67], s96 offen offset:1024 lds\n\t" \
  "v_mfma_f32_16x16x32_bf16 a[80:83], a[164:167], v[148:151], a[80:83]\n\t" \
  "ds_read_b128 a[232:235], v222 offset:8192\n\t" \
  "v_cvt_pk_bf16_f32 v138, v36, v37\n\t" \
  "v_mfma_f32_16x16x32_bf16 a[84:87], a[168:171], v[144:147], a[84:87]\n\t" \
  "v_exp_f32 v42, v42\n\t" \
  "v_mfma_f32_16x16x32_bf16 a[84:87], a[172:175], v[148:151], a[84:87]\n\t" \
  "ds_read_b128 a[236:239], v223 offset:8192\n\t" \
  "v_cvt_pk_bf16_f32 v139, v38, v39\n\t" \
  "v_mfma_f32_16x16x32_bf16 a[88:91], a[176:179], v[144:147], a[88:91]\n\t" \
  "v_exp_f32 v43, v43\n\t" \
  "v_mfma_f32_16x16x32_bf16 a[88:91], a[180:183], v[148:151], a[88:91]\n\t" \
  "ds_read_b128 a[240:243], v220 offset:12288\n\t" \
  "v_cvt_pk_bf16_f32 v140, v40, v41\n\t" \
  "v_mfma_f32_16x16x32_bf16 a[92:95], a[184:187], v[144:147], a[92:95]\n\t" \
  "buffer_load_dwordx4 v230, s[64:67], s96 offen offset:2048 lds\n\t" \
  "v_mfma_f32_16x16x32_bf16 a[92:95], a[188:191], v[148:151], a[92:95]\n\t" \
  "v_exp_f32 v44, v44\n\t" \
  "v_smfmac_f32_16x16x64_bf16 v[168:171], v[216:219], v[144:151], v208\n\t" \
  "ds_read_b128 a[244:247], v221 offset:12288\n\t" \
  "v_cvt_pk_bf16_f32 v141, v42, v43\n\t" \
  "v_mfma_f32_16x16x32_bf16 a[96:99], a[128:131], v[152:155], a[96:99]\n\t" \
  "v_exp_f32 v45, v45\n\t" \
  "v_mfma_f32_16x16x32_bf16 a[96:99], a[132:135], v[156:159], a[96:99]\n\t" \
  "v_exp_f32 v46, v46\n\t" \
  "v_mfma_f32_16x16x32_bf16 a[100:103], a[136:139], v[152:155], a[100:103]\n\t" \
  "ds_read_b128 a[248:251], v222 offset:12288\n\t" \
  "v_cvt_pk_bf16_f32 v142, v44, v45\n\t" \
  "v_mfma_f32_16x16x32_bf16 a[100:103], a[140:143], v[156:159], a[100:103]\n\t" \
  "buffer_load_dwordx4 v231, s[64:67], s96 offen offset:3072 lds\n\t" \
  "v_mfma_f32_16x16x32_bf16 a[104:107], a[144:147], v[152:155], a[104:107]\n\t" \
  "v_exp_f32 v47, v47\n\t" \
  "v_mfma_f32_16x16x32_bf16 a[104:107], a[148:151], v[156:159], a[104:107]\n\t" \
  "ds_read_b128 a[252:255], v223 offset:12288\n\t" \
  "v_cvt_pk_bf16_f32 v143, v46, v47\n\t" \
  "v_mfma_f32_16x16x32_bf16 a[108:111], a[152:155], v[152:155], a[108:111]\n\t" \
  "v_exp_f32 v64, v64\n\t" \
  "v_mfma_f32_16x16x32_bf16 a[108:111], a[156:159], v[156:159], a[108:111]\n\t" \
  "v_exp_f32 v65, v65\n\t" \
  "v_mfma_f32_16x16x32_bf16 a[112:115], a[160:163], v[152:155], a[112:115]\n\t" \
  "v_exp_f32 v66, v66\n\t" \
  "v_mfma_f32_16x16x32_bf16 a[112:115], a[164:167], v[156:159], a[112:115]\n\t" \
  "v_exp_f32 v67, v67\n\t" \
  "v_mfma_f32_16x16x32_bf16 a[116:119], a[168:171], v[152:155], a[116:119]\n\t" \
  "v_exp_f32 v68, v68\n\t" \
  "v_mfma_f32_16x16x32_bf16 a[116:119], a[172:175], v[156:159], a[116:119]\n\t" \
  "v_exp_f32 v69, v69\n\t" \
  "v_mfma_f32_16x16x32_bf16 a[120:123], a[176:179], v[152:155], a[120:123]\n\t" \
  "v_exp_f32 v70, v70\n\t" \
  "v_mfma_f32_16x16x32_bf16 a[120:123], a[180:183], v[156:159], a[120:123]\n\t" \
  "v_exp_f32 v71, v71\n\t" \
  "v_mfma_f32_16x16x32_bf16 a[124:127], a[184:187], v[152:155], a[124:127]\n\t" \
  "v_exp_f32 v72, v72\n\t" \
  "v_mfma_f32_16x16x32_bf16 a[124:127], a[188:191], v[156:159], a[124:127]\n\t" \
  "v_exp_f32 v73, v73\n\t" \
  "v_smfmac_f32_16x16x64_bf16 v[172:175], v[216:219], v[152:159], v208\n\t" \
  "s_waitcnt lgkmcnt(12)\n\t" \
  "s_cmp_lg_u32 s99, 0\n\t" \
  "s_cbranch_scc1 .Lsf_p5\n\t" \
  ".Lsf_q5:\n\t" \
  "v_mfma_f32_16x16x32_bf16 v[0:3], a[192:195], v[16:19], v[176:179]\n\t" \
  "ds_read_b64_tr_b16 a[128:129], v210 offset:0\n\t" \
  "v_cvt_pk_bf16_f32 v144, v64, v65\n\t" \
  "v_mfma_f32_16x16x32_bf16 v[0:3], a[196:199], v[20:23], v[0:3]\n\t" \
  "v_exp_f32 v74, v74\n\t" \
  "v_mfma_f32_16x16x32_bf16 v[0:3], a[200:203], v[24:27], v[0:3]\n\t" \
  "ds_read_b64_tr_b16 a[130:131], v210 offset:4096\n\t" \
  "v_cvt_pk_bf16_f32 v145, v66, v67\n\t" \
  "v_mfma_f32_16x16x32_bf16 v[0:3], a[204:207], v[28:31], v[0:3]\n\t" \
  "s_waitcnt lgkmcnt(10)\n\t" \
  "v_exp_f32 v75, v75\n\t" \
  "v_mfma_f32_16x16x32_bf16 v[4:7], a[220:223], v[28:31], v[176:179]\n\t" \
  "ds_read_b64_tr_b16 a[132:133], v210 offset:8192\n\t" \
  "v_cvt_pk_bf16_f32 v146, v68, v69\n\t" \
  "v_mfma_f32_16x16x32_bf16 v[4:7], a[216:219], v[24:27], v[4:7]\n\t" \
  "v_exp_f32 v76, v76\n\t" \
  "v_mfma_f32_16x16x32_bf16 v[4:7], a[212:215], v[20:23], v[4:7]\n\t" \
  "ds_read_b64_tr_b16 a[134:135], v210 offset:12288\n\t" \
  "v_max3_f32 v192, v0, v1, v2\n\t" \
  "v_mfma_f32_16x16x32_bf16 v[4:7], a[208:211], v[16:19], v[4:7]\n\t" \
  "s_waitcnt lgkmcnt(8)\n\t" \
  "ds_read_b64_tr_b16 a[136:137], v211 offset:0\n\t" \
  "v_cvt_pk_bf16_f32 v147, v70, v71\n\t" \
  "v_mfma_f32_16x16x32_bf16 v[8:11], a[224:227], v[16:19], v[176:179]\n\t" \
  "v_exp_f32 v77, v77\n\t" \
  "v_mfma_f32_16x16x32_bf16 v[8:11], a[228:231], v[20:23], v[8:11]\n\t" \
  "ds_read_b64_tr_b16 a[138:139], v211 offset:4096\n\t" \
  "v_cvt_pk_bf16_f32 v148, v72, v73\n\t" \
  "v_mfma_f32_16x16x32_bf16 v[8:11], a[232:235], v[24:27], v[8:11]\n\t" \
  "v_max3_f32 v203, v3, v4, v5\n\t" \
  "v_max3_f32 v192, v192, v6, v7\n\t" \
  "v_mfma_f32_16x16x32_bf16 v[8:11], a[236:239], v[28:31], v[8:11]\n\t" \
  "s_waitcnt lgkmcnt(6)\n\t" \
  "ds_read_b64_tr_b16 a[140:141], v211 offset:8192\n\t" \
  "v_cvt_pk_bf16_f32 v149, v74, v75\n\t" \
  "v_mfma_f32_16x16x32_bf16 v[12:15], a[252:255], v[28:31], v[176:179]\n\t" \
  "v_exp_f32 v78, v78\n\t" \
  "v_mfma_f32_16x16x32_bf16 v[12:15], a[248:251], v[24:27], v[12:15]\n\t" \
  "ds_read_b64_tr_b16 a[142:143], v211 offset:12288\n\t" \
  "v_cvt_pk_bf16_f32 v150, v76, v77\n\t" \
  "v_mfma_f32_16x16x32_bf16 v[12:15], a[244:247], v[20:23], v[12:15]\n\t" \
  "v_max3_f32 v203, v203, v8, v9\n\t" \
  "v_max3_f32 v192, v192, v10, v11\n\t" \
  "v_mfma_f32_16x16x32_bf16 v[12:15], a[240:243], v[16:19], v[12:15]\n\t" \
  "v_exp_f32 v79, v79\n\t" \
  "v_mfma_f32_16x16x32_bf16 v[32:35], a[192:195], v[48:51], v[180:183]\n\t" \
  "ds_read_b64_tr_b16 a[144:145], v210 offset:256\n\t" \
  "v_cvt_pk_bf16_f32 v151, v78, v79\n\t" \
  "v_mfma_f32_16x16x32_bf16 v[32:35], a[196:199], v[52:55], v[32:35]\n\t" \
  "v_exp_f32 v96, v96\n\t" \
  "v_mfma_f32_16x16x32_bf16 v[32:35], a[200:203], v[56:59], v[32:35]\n\t" \
  "ds_read_b64_tr_b16 a[146:147], v210 offset:4352\n\t" \
  "v_max3_f32 v203, v203, v12, v13\n\t" \
  "v_mfma_f32_16x16x32_bf16 v[32:35], a[204:207], v[60:63], v[32:35]\n\t" \
  "v_max3_f32 v192, v192, v14, v15\n\t" \
  "v_max_f32 v192, v192, v203\n\t" \
  "v_mfma_f32_16x16x32_bf16 v[36:39], a[220:223], v[60:63], v[180:183]\n\t" \
  "ds_read_b64_tr_b16 a[148:149], v210 offset:8448\n\t" \
  "v_cmp_lt_f32 vcc, 0x42800000, v192\n\t" \
  "v_mfma_f32_16x16x32_bf16 v[36:39], a[216:219], v[56:59], v[36:39]\n\t" \
  "v_exp_f32 v97, v97\n\t" \
  "v_mfma_f32_16x16x32_bf16 v[36:39], a[212:215], v[52:55], v[36:39]\n\t" \
  "s_cbranch_vccnz .Lsf_c60\n\t" \
  ".Lsf_r60:\n\t" \
  "v_mfma_f32_16x16x32_bf16 v[36:39], a[208:211], v[48:51], v[36:39]\n\t" \
  "ds_read_b64_tr_b16 a[150:151], v210 offset:12544\n\t" \
  "v_max3_f32 v192, v32, v33, v34\n\t" \
  "v_mfma_f32_16x16x32_bf16 v[40:43], a[224:227], v[48:51], v[180:183]\n\t" \
  "ds_read_b64_tr_b16 a[152:153], v211 offset:256\n\t" \
  "v_cvt_pk_bf16_f32 v152, v96, v97\n\t" \
  "v_mfma_f32_16x16x32_bf16 v[40:43], a[228:231], v[52:55], v[40:43]\n\t" \
  "v_exp_f32 v98, v98\n\t" \
  "v_mfma_f32_16x16x32_bf16 v[40:43], a[232:235], v[56:59], v[40:43]\n\t" \
  "v_max3_f32 v203, v35, v36, v37\n\t" \
  "v_max3_f32 v192, v192, v38, v39\n\t" \
  "v_mfma_f32_16x16x32_bf16 v[40:43], a[236:239], v[60:63], v[40:43]\n\t" \
  "v_exp_f32 v99, v99\n\t" \
  "v_mfma_f32_16x16x32_bf16 v[44:47], a[252:255], v[60:63], v[180:183]\n\t" \
  "ds_read_b64_tr_b16 a[154:155], v211 offset:4352\n\t" \
  "v_cvt_pk_bf16_f32 v153, v98, v99\n\t" \
  "v_mfma_f32_16x16x32_bf16 v[44:47], a[248:251], v[56:59], v[44:47]\n\t" \
  "v_exp_f32 v100, v100\n\t" \
  "v_mfma_f32_16x16x32_bf16 v[44:47], a[244:247], v[52:55], v[44:47]\n\t" \
  "ds_read_b64_tr_b16 a[156:157], v211 offset:8448\n\t" \
  "v_max3_f32 v203, v203, v40, v41\n\t" \
  "v_mfma_f32_16x16x32_bf16 v[44:47], a[240:243], v[48:51], v[44:47]\n\t" \
  "ds_read_b64_tr_b16 a[158:159], v211 offset:12544\n\t" \
  "v_max3_f32 v192, v192, v42, v43\n\t" \
  "v_mfma_f32_16x16x32_bf16 v[64:67], a[192:195], v[80:83], v[184:187]\n\t" \
  "v_exp_f32 v101, v101\n\t" \
  "v_mfma_f32_16x16x32_bf16 v[64:67], a[196:199], v[84:87], v[64:67]\n\t" \
  "ds_read_b64_tr_b16 a[160:161], v210 offset:512\n\t" \
  "v_cvt_pk_bf16_f32 v154, v100, v101\n\t" \
  "v_mfma_f32_16x16x32_bf16 v[64:67], a[200:203], v[88:91], v[64:67]\n\t" \
  "v_max3_f32 v203, v203, v44, v45\n\t" \
  "v_max3_f32 v192, v192, v46, v47\n\t" \
  "v_mfma_f32_16x16x32_bf16 v[64:67], a[204:207], v[92:95], v[64:67]\n\t" \
  "v_max_f32 v192, v192, v203\n\t" \
  "v_cmp_lt_f32 vcc, 0x42800000, v192\n\t" \
  "v_mfma_f32_16x16x32_bf16 v[68:71], a[220:223], v[92:95], v[184:187]\n\t" \
  "v_exp_f32 v102, v102\n\t" \
  "v_mfma_f32_16x16x32_bf16 v[68:71], a[216:219], v[88:91], v[68:71]\n\t" \
  "s_cbranch_vccnz .Lsf_c61\n\t" \
  ".Lsf_r61:\n\t" \
  "v_mfma_f32_16x16x32_bf16 v[68:71], a[212:215], v[84:87], v[68:71]\n\t" \
  "ds_read_b64_tr_b16 a[162:163], v210 offset:4608\n\t" \
  "v_max3_f32 v192, v64, v65, v66\n\t" \
  "v_mfma_f32_16x16x32_bf16 v[68:71], a[208:211], v[80:83], v[68:71]\n\t" \
  "ds_read_b64_tr_b16 a[164:165], v210 offset:8704\n\t" \
  "v_exp_f32 v103, v103\n\t" \
  "v_mfma_f32_16x16x32_bf16 v[72:75], a[224:227], v[80:83], v[184:187]\n\t" \
  "ds_read_b64_tr_b16 a[166:167], v210 offset:12800\n\t" \
  "v_exp_f32 v104, v104\n\t" \
  "v_mfma_f32_16x16x32_bf16 v[72:75], a[228:231], v[84:87], v[72:75]\n\t" \
  "ds_read_b64_tr_b16 a[168:169], v211 offset:512\n\t" \
  "v_exp_f32 v105, v105\n\t" \
  "v_mfma_f32_16x16x32_bf16 v[72:75], a[232:235], v[88:91], v[72:75]\n\t" \
  "v_max3_f32 v203, v67, v68, v69\n\t" \
  "v_max3_f32 v192, v192, v70, v71\n\t" \
  "v_mfma_f32_16x16x32_bf16 v[72:75], a[236:239], v[92:95], v[72:75]\n\t" \
  "ds_read_b64_tr_b16 a[170:171], v211 offset:4608\n\t" \
  "v_exp_f32 v106, v106\n\t" \
  "v_mfma_f32_16x16x32_bf16 v[76:79], a[252:255], v[92:95], v[184:187]\n\t" \
  "ds_read_b64_tr_b16 a[172:173], v211 offset:8704\n\t" \
  "v_exp_f32 v107, v107\n\t" \
  "v_mfma_f32_16x16x32_bf16 v[76:79], a[248:251], v[88:91], v[76:79]\n\t" \
  "ds_read_b64_tr_b16 a[174:175], v211 offset:12800\n\t" \
  "v_cvt_pk_bf16_f32 v155, v102, v103\n\t" \
  "v_mfma_f32_16x16x32_bf16 v[76:79], a[244:247], v[84:87], v[76:79]\n\t" \
  "v_max3_f32 v203, v203, v72, v73\n\t" \
  "v_max3_f32 v192, v192, v74, v75\n\t" \
  "v_mfma_f32_16x16x32_bf16 v[76:79], a[240:243], v[80:83], v[76:79]\n\t" \
  "ds_read_b64_tr_b16 a[176:177], v210 offset:768\n\t" \
  "v_exp_f32 v108, v108\n\t" \
  "v_mfma_f32_16x16x32_bf16 v[96:99], a[192:195], v[112:115], v[188:191]\n\t" \
  "ds_read_b64_tr_b16 a[178:179], v210 offset:4864\n\t" \
  "v_exp_f32 v109, v109\n\t" \
  "v_mfma_f32_16x16x32_bf16 v[96:99], a[196:199], v[116:119], v[96:99]\n\t" \
  "ds_read_b64_tr_b16 a[180:181], v210 offset:8960\n\t" \
  "v_cvt_pk_bf16_f32 v156, v104, v105\n\t" \
  "v_mfma_f32_16x16x32_bf16 v[96:99], a[200:203], v[120:123], v[96:99]\n\t" \
  "v_max3_f32 v203, v203, v76, v77\n\t" \
  "v_max3_f32 v192, v192, v78, v79\n\t" \
  "v_mfma_f32_16x16x32_bf16 v[96:99], a[204:207], v[124:127], v[96:99]\n\t" \
  "v_max_f32 v192, v192, v203\n\t" \
  "v_cmp_lt_f32 vcc, 0x42800000, v192\n\t" \
  "v_mfma_f32_16x16x32_bf16 v[100:103], a[220:223], v[124:127], v[188:191]\n\t" \
  "ds_read_b64_tr_b16 a[182:183], v210 offset:13056\n\t" \
  "v_exp_f32 v110, v110\n\t" \
  "v_mfma_f32_16x16x32_bf16 v[100:103], a[216:219], v[120:123], v[100:103]\n\t" \
  "s_cbranch_vccnz .Lsf_c62\n\t" \
  ".Lsf_r62:\n\t" \
  "v_mfma_f32_16x16x32_bf16 v[100:103], a[212:215], v[116:119], v[100:103]\n\t" \
  "v_max3_f32 v192, v96, v97, v98\n\t" \
  "v_exp_f32 v111, v111\n\t" \
  "v_mfma_f32_16x16x32_bf16 v[100:103], a[208:211], v[112:115], v[100:103]\n\t" \
  "ds_read_b64_tr_b16 a[184:185], v211 offset:768\n\t" \
  "v_cvt_pk_bf16_f32 v157, v106, v107\n\t" \
  "v_mfma_f32_16x16x32_bf16 v[104:107], a[224:227], v[112:115], v[188:191]\n\t" \
  "ds_read_b64_tr_b16 a[186:187], v211 offset:4864\n\t" \
  "v_cvt_pk_bf16_f32 v158, v108, v109\n\t" \
  "v_mfma_f32_16x16x32_bf16 v[104:107], a[228:231], v[116:119], v[104:107]\n\t" \
  "ds_read_b64_tr_b16 a[188:189], v211 offset:8960\n\t" \
  "v_cvt_pk_bf16_f32 v159, v110, v111\n\t" \
  "v_mfma_f32_16x16x32_bf16 v[104:107], a[232:235], v[120:123], v[104:107]\n\t" \
  "v_max3_f32 v203, v99, v100, v101\n\t" \
  "v_max3_f32 v192, v192, v102, v103\n\t" \
  "v_mfma_f32_16x16x32_bf16 v[104:107], a[236:239], v[124:127], v[104:107]\n\t" \
  "ds_read_b64_tr_b16 a[190:191], v211 offset:13056\n\t" \
  "v_mfma_f32_16x16x32_bf16 v[108:111], a[252:255], v[124:127], v[188:191]\n\t" \
  "v_exp_f32 v0, v0\n\t" \
  "v_mfma_f32_16x16x32_bf16 v[108:111], a[248:251], v[120:123], v[108:111]\n\t" \
  "v_exp_f32 v1, v1\n\t" \
  "v_mfma_f32_16x16x32_bf16 v[108:111], a[244:247], v[116:119], v[108:111]\n\t" \
  "s_waitcnt vmcnt(0) lgkmcnt(0)\n\t" \
  "v_exp_f32 v2, v2\n\t" \
  "v_mfma_f32_16x16x32_bf16 v[108:111], a[240:243], v[112:115], v[108:111]\n\t" \
  "s_barrier\n\t" \
  "v_mfma_f32_16x16x32_bf16 a[0:3], a[128:131], v[128:131], a[0:3]\n\t" \
  "v_max3_f32 v203, v203, v104, v105\n\t" \
  "v_max3_f32 v192, v192, v106, v107\n\t" \
  "v_mfma_f32_16x16x32_bf16 a[0:3], a[132:135], v[132:135], a[0:3]\n\t" \
  "v_exp_f32 v3, v3\n\t" \
  "v_mfma_f32_16x16x32_bf16 a[4:7], a[136:139], v[128:131], a[4:7]\n\t" \
  "v_exp_f32 v4, v4\n\t" \
  "v_mfma_f32_16x16x32_bf16 a[4:7], a[140:143], v[132:135], a[4:7]\n\t" \
  "v_max3_f32 v203, v203, v108, v109\n\t" \
  "v_max3_f32 v192, v192, v110, v111\n\t" \
  "v_mfma_f32_16x16x32_bf16 a[8:11], a[144:147], v[128:131], a[8:11]\n\t" \
  "v_max_f32 v192, v192, v203\n\t" \
  "v_cmp_lt_f32 vcc, 0x42800000, v192\n\t" \
  "v_mfma_f32_16x16x32_bf16 a[8:11], a[148:151], v[132:135], a[8:11]\n\t" \
  "v_exp_f32 v5, v5\n\t" \
  "v_mfma_f32_16x16x32_bf16 a[12:15], a[152:155], v[128:131], a[12:15]\n\t" \
  "s_cbranch_vccnz .Lsf_c63\n\t" \
  ".Lsf_r63:\n\t" \
  "v_mfma_f32_16x16x32_bf16 a[12:15], a[156:159], v[132:135], a[12:15]\n\t" \
  "s_add_u32 m0, s38, 147456\n\t" \
  "v_exp_f32 v6, v6\n\t" \
  "v_mfma_f32_16x16x32_bf16 a[16:19], a[160:163], v[128:131], a[16:19]\n\t" \
  "s_add_u32 s96, s38, 114688\n\t" \
  "v_exp_f32 v7, v7\n\t" \
  "v_mfma_f32_16x16x32_bf16 a[16:19], a[164:167], v[132:135], a[16:19]\n\t" \
  "v_exp_f32 v8, v8\n\t" \
  "v_mfma_f32_16x16x32_bf16 a[20:23], a[168:171], v[128:131], a[20:23]\n\t" \
  "buffer_load_dwordx4 v230, s[64:67], s96 offen offset:0 lds\n\t" \
  "v_mfma_f32_16x16x32_bf16 a[20:23], a[172:175], v[132:135], a[20:23]\n\t" \
  "v_exp_f32 v9, v9\n\t" \
  "v_mfma_f32_16x16x32_bf16 a[24:27], a[176:179], v[128:131], a[24:27]\n\t" \
  "v_exp_f32 v10, v10\n\t" \
  "v_mfma_f32_16x16x32_bf16 a[24:27], a[180:183], v[132:135], a[24:27]\n\t" \
  "v_exp_f32 v11, v11\n\t" \
  "v_mfma_f32_16x16x32_bf16 a[28:31], a[184:187], v[128:131], a[28:31]\n\t" \
  "v_exp_f32 v12, v12\n\t" \
  "v_mfma_f32_16x16x32_bf16 a[28:31], a[188:191], v[132:135], a[28:31]\n\t" \
  "v_exp_f32 v13, v13\n\t" \
  "v_smfmac_f32_16x16x64_bf16 v[160:163], v[216:219], v[128:135], v208\n\t" \
  "ds_read_b128 a[192:195], v220 offset:16384\n\t" \
  "v_cvt_pk_bf16_f32 v128, v0, v1\n\t" \
  "v_mfma_f32_16x16x32_bf16 a[32:35], a[128:131], v[136:139], a[32:35]\n\t" \
  "v_exp_f32 v14, v14\n\t" \
  "v_mfma_f32_16x16x32_bf16 a[32:35], a[132:135], v[140:143], a[32:35]\n\t" \
  "v_exp_f32 v15, v15\n\t" \
  "v_mfma_f32_16x16x32_bf16 a[36:39], a[136:139], v[136:139], a[36:39]\n\t" \
  "ds_read_b128 a[196:199], v221 offset:16384\n\t" \
  "v_cvt_pk_bf16_f32 v129, v2, v3\n\t" \
  "v_mfma_f32_16x16x32_bf16 a[36:39], a[140:143], v[140:143], a[36:39]\n\t" \
  "v_exp_f32 v32, v32\n\t" \
  "v_mfma_f32_16x16x32_bf16 a[40:43], a[144:147], v[136:139], a[40:43]\n\t" \
  "ds_read_b128 a[200:203], v222 offset:16384\n\t" \
  "v_cvt_pk_bf16_f32 v130, v4, v5\n\t" \
  "v_mfma_f32_16x16x32_bf16 a[40:43], a[148:151], v[140:143], a[40:43]\n\t" \
  "v_exp_f32 v33, v33\n\t" \
  "v_mfma_f32_16x16x32_bf16 a[44:47], a[152:155], v[136:139], a[44:47]\n\t" \
  "buffer_load_dwordx4 v231, s[64:67], s96 offen offset:1024 lds\n\t" \
  "v_mfma_f32_16x16x32_bf16 a[44:47], a[156:159], v[140:143], a[44:47]\n\t" \
  "ds_read_b128 a[204:207], v223 offset:16384\n\t" \
  "v_cvt_pk_bf16_f32 v131, v6, v7\n\t" \
  "v_mfma_f32_16x16x32_bf16 a[48:51], a[160:163], v[136:139], a[48:51]\n\t" \
  "v_exp_f32 v34, v34\n\t" \
  "v_mfma_f32_16x16x32_bf16 a[48:51], a[164:167], v[140:143], a[48:51]\n\t" \
  "v_exp_f32 v35, v35\n\t" \
  "v_mfma_f32_16x16x32_bf16 a[52:55], a[168:171], v[136:139], a[52:55]\n\t" \
  "ds_read_b128 a[208:211], v220 offset:20480\n\t" \
  "v_cvt_pk_bf16_f32 v132, v8, v9\n\t" \
  "v_mfma_f32_16x16x32_bf16 a[52:55], a[172:175], v[140:143], a[52:55]\n\t" \
  "v_exp_f32 v36, v36\n\t" \
  "v_mfma_f32_16x16x32_bf16 a[56:59], a[176:179], v[136:139], a[56:59]\n\t" \
  "ds_read_b128 a[212:215], v221 offset:20480\n\t" \
  "v_cvt_pk_bf16_f32 v133, v10, v11\n\t" \
  "v_mfma_f32_16x16x32_bf16 a[56:59], a[180:183], v[140:143], a[56:59]\n\t" \
  "v_exp_f32 v37, v37\n\t" \
  "v_mfma_f32_16x16x32_bf16 a[60:63], a[184:187], v[136:139], a[60:63]\n\t" \
  "ds_read_b128 a[216:219], v222 offset:20480\n\t" \
  "v_cvt_pk_bf16_f32 v134, v12, v13\n\t" \
  "v_mfma_f32_16x16x32_bf16 a[60:63], a[188:191], v[140:143], a[60:63]\n\t" \
  "v_exp_f32 v38, v38\n\t" \
  "v_smfmac_f32_16x16x64_bf16 v[164:167], v[216:219], v[136:143], v208\n\t" \
  "ds_read_b128 a[220:223], v223 offset:20480\n\t" \
  "v_cvt_pk_bf16_f32 v135, v14, v15\n\t" \
  "v_mfma_f32_16x16x32_bf16 a[64:67], a[128:131], v[144:147], a[64:67]\n\t" \
  "v_exp_f32 v39, v39\n\t" \
  "v_mfma_f32_16x16x32_bf16 a[64:67], a[132:135], v[148:151], a[64:67]\n\t" \
  "buffer_load_dwordx4 v230, s[64:67], s96 offen offset:2048 lds\n\t" \
  "v_mfma_f32_16x16x32_bf16 a[68:71], a[136:139], v[144:147], a[68:71]\n\t" \
  "ds_read_b128 a[224:227], v220 offset:24576\n\t" \
  "v_cvt_pk_bf16_f32 v136, v32, v33\n\t" \
  "v_mfma_f32_16x16x32_bf16 a[68:71], a[140:143], v[148:151], a[68:71]\n\t" \
  "v_exp_f32 v40, v40\n\t" \
  "v_mfma_f32_16x16x32_bf16 a[72:75], a[144:147], v[144:147], a[72:75]\n\t" \
  "v_exp_f32 v41, v41\n\t" \
  "v_mfma_f32_16x16x32_bf16 a[72:75], a[148:151], v[148:151], a[72:75]\n\t" \
  "ds_read_b128 a[228:231], v221 offset:24576\n\t" \
  "v_cvt_pk_bf16_f32 v137, v34, v35\n\t" \
  "v_mfma_f32_16x16x32_bf16 a[76:79], a[152:155], v[144:147], a[76:79]\n\t" \
  "v_exp_f32 v42, v42\n\t" \
  "v_mfma_f32_16x16x32_bf16 a[76:79], a[156:159], v[148:151], a[76:79]\n\t" \
  "v_exp_f32 v43, v43\n\t" \
  "v_mfma_f32_16x16x32_bf16 a[80:83], a[160:163], v[144:147], a[80:83]\n\t" \
  "ds_read_b128 a[232:235], v222 offset:24576\n\t" \
  "v_cvt_pk_bf16_f32 v138, v36, v37\n\t" \
  "v_mfma_f32_16x16x32_bf16 a[80:83], a[164:167], v[148:151], a[80:83]\n\t" \
  "v_exp_f32 v44, v44\n\t" \
  "v_mfma_f32_16x16x32_bf16 a[84:87], a[168:171], v[144:147], a[84:87]\n\t" \
  "v_exp_f32 v45, v45\n\t" \
  "v_mfma_f32_16x16x32_bf16 a[84:87], a[172:175], v[148:151], a[84:87]\n\t" \
  "ds_read_b128 a[236:239], v223 offset:24576\n\t" \
  "v_cvt_pk_bf16_f32 v139, v38, v39\n\t" \
  "v_mfma_f32_16x16x32_bf16 a[88:91], a[176:179], v[144:147], a[88:91]\n\t" \
  "v_exp_f32 v46, v46\n\t" \
  "v_mfma_f32_16x16x32_bf16 a[88:91], a[180:183], v[148:151], a[88:91]\n\t" \
  "ds_read_b128 a[240:243], v220 offset:28672\n\t" \
  "v_cvt_pk_bf16_f32 v140, v40, v41\n\t" \
  "v_mfma_f32_16x16x32_bf16 a[92:95], a[184:187], v[144:147], a[92:95]\n\t" \
  "buffer_load_dwordx4 v231, s[64:67], s96 offen offset:3072 lds\n\t" \
  "v_mfma_f32_16x16x32_bf16 a[92:95], a[188:191], v[148:151], a[92:95]\n\t" \
  "v_exp_f32 v47, v47\n\t" \
  "v_smfmac_f32_16x16x64_bf16 v[168:171], v[216:219], v[144:151], v208\n\t" \
  "ds_read_b128 a[244:247], v221 offset:28672\n\t" \
  "v_cvt_pk_bf16_f32 v141, v42, v43\n\t" \
  "v_mfma_f32_16x16x32_bf16 a[96:99], a[128:131], v[152:155], a[96:99]\n\t" \
  "v_exp_f32 v64, v64\n\t" \
  "v_mfma_f32_16x16x32_bf16 a[96:99], a[132:135], v[156:159], a[96:99]\n\t" \
  "v_exp_f32 v65, v65\n\t" \
  "v_mfma_f32_16x16x32_bf16 a[100:103], a[136:139], v[152:155], a[100:103]\n\t" \
  "ds_read_b128 a[248:251], v222 offset:28672\n\t" \
  "v_cvt_pk_bf16_f32 v142, v44, v45\n\t" \
  "v_mfma_f32_16x16x32_bf16 a[100:103], a[140:143], v[156:159], a[100:103]\n\t" \
  "v_exp_f32 v66, v66\n\t" \
  "v_mfma_f32_16x16x32_bf16 a[104:107], a[144:147], v[152:155], a[104:107]\n\t" \
  "v_exp_f32 v67, v67\n\t" \
  "v_mfma_f32_16x16x32_bf16 a[104:107], a[148:151], v[156:159], a[104:107]\n\t" \
  "ds_read_b128 a[252:255], v223 offset:28672\n\t" \
  "v_cvt_pk_bf16_f32 v143, v46, v47\n\t" \
  "v_mfma_f32_16x16x32_bf16 a[108:111], a[152:155], v[152:155], a[108:111]\n\t" \
  "v_exp_f32 v68, v68\n\t" \
  "v_mfma_f32_16x16x32_bf16 a[108:111], a[156:159], v[156:159], a[108:111]\n\t" \
  "v_exp_f32 v69, v69\n\t" \
  "v_mfma_f32_16x16x32_bf16 a[112:115], a[160:163], v[152:155], a[112:115]\n\t" \
  "v_exp_f32 v70, v70\n\t" \
  "v_mfma_f32_16x16x32_bf16 a[112:115], a[164:167], v[156:159], a[112:115]\n\t" \
  "v_exp_f32 v71, v71\n\t" \
  "v_mfma_f32_16x16x32_bf16 a[116:119], a[168:171], v[152:155], a[116:119]\n\t" \
  "v_exp_f32 v72, v72\n\t" \
  "v_mfma_f32_16x16x32_bf16 a[116:119], a[172:175], v[156:159], a[116:119]\n\t" \
  "v_exp_f32 v73, v73\n\t" \
  "v_mfma_f32_16x16x32_bf16 a[120:123], a[176:179], v[152:155], a[120:123]\n\t" \
  "v_exp_f32 v74, v74\n\t" \
  "v_mfma_f32_16x16x32_bf16 a[120:123], a[180:183], v[156:159], a[120:123]\n\t" \
  "v_exp_f32 v75, v75\n\t" \
  "v_mfma_f32_16x16x32_bf16 a[124:127], a[184:187], v[152:155], a[124:127]\n\t" \
  "v_exp_f32 v76, v76\n\t" \
  "v_mfma_f32_16x16x32_bf16 a[124:127], a[188:191], v[156:159], a[124:127]\n\t" \
  "v_exp_f32 v77, v77\n\t" \
  "v_smfmac_f32_16x16x64_bf16 v[172:175], v[216:219], v[152:159], v208\n\t" \
  "s_waitcnt lgkmcnt(12)\n\t" \
  "s_cmp_lg_u32 s99, 0\n\t" \
  "s_cbranch_scc1 .Lsf_p6\n\t" \
  ".Lsf_q6:\n\t" \
  "v_mfma_f32_16x16x32_bf16 v[0:3], a[192:195], v[16:19], v[176:179]\n\t" \
  "ds_read_b64_tr_b16 a[128:129], v228 offset:0\n\t" \
  "v_cvt_pk_bf16_f32 v144, v64, v65\n\t" \
  "v_mfma_f32_16x16x32_bf16 v[0:3], a[196:199], v[20:23], v[0:3]\n\t" \
  "v_exp_f32 v78, v78\n\t" \
  "v_mfma_f32_16x16x32_bf16 v[0:3], a[200:203], v[24:27], v[0:3]\n\t" \
  "ds_read_b64_tr_b16 a[130:131], v228 offset:4096\n\t" \
  "v_cvt_pk_bf16_f32 v145, v66, v67\n\t" \
  "v_mfma_f32_16x16x32_bf16 v[0:3], a[204:207], v[28:31], v[0:3]\n\t" \
  "s_waitcnt lgkmcnt(10)\n\t" \
  "v_exp_f32 v79, v79\n\t" \
  "v_mfma_f32_16x16x32_bf16 v[4:7], a[220:223], v[28:31], v[176:179]\n\t" \
  "ds_read_b64_tr_b16 a[132:133], v228 offset:8192\n\t" \
  "v_cvt_pk_bf16_f32 v146, v68, v69\n\t" \
  "v_mfma_f32_16x16x32_bf16 v[4:7], a[216:219], v[24:27], v[4:7]\n\t" \
  "v_exp_f32 v96, v96\n\t" \
  "v_mfma_f32_16x16x32_bf16 v[4:7], a[212:215], v[20:23], v[4:7]\n\t" \
  "ds_read_b64_tr_b16 a[134:135], v228 offset:12288\n\t" \
  "v_max3_f32 v192, v0, v1, v2\n\t" \
  "v_mfma_f32_16x16x32_bf16 v[4:7], a[208:211], v[16:19], v[4:7]\n\t" \
  "s_waitcnt lgkmcnt(8)\n\t" \
  "ds_read_b64_tr_b16 a[136:137], v229 offset:0\n\t" \
  "v_cvt_pk_bf16_f32 v147, v70, v71\n\t" \
  "v_mfma_f32_16x16x32_bf16 v[8:11], a[224:227], v[16:19], v[176:179]\n\t" \
  "v_exp_f32 v97, v97\n\t" \
  "v_mfma_f32_16x16x32_bf16 v[8:11], a[228:231], v[20:23], v[8:11]\n\t" \
  "ds_read_b64_tr_b16 a[138:139], v229 offset:4096\n\t" \
  "v_cvt_pk_bf16_f32 v148, v72, v73\n\t" \
  "v_mfma_f32_16x16x32_bf16 v[8:11], a[232:235], v[24:27], v[8:11]\n\t" \
  "v_max3_f32 v203, v3, v4, v5\n\t" \
  "v_max3_f32 v192, v192, v6, v7\n\t" \
  "v_mfma_f32_16x16x32_bf16 v[8:11], a[236:239], v[28:31], v[8:11]\n\t" \
  "s_waitcnt lgkmcnt(6)\n\t" \
  "ds_read_b64_tr_b16 a[140:141], v229 offset:8192\n\t" \
  "v_cvt_pk_bf16_f32 v149, v74, v75\n\t" \
  "v_mfma_f32_16x16x32_bf16 v[12:15], a[252:255], v[28:31], v[176:179]\n\t" \
  "v_exp_f32 v98, v98\n\t" \
  "v_mfma_f32_16x16x32_bf16 v[12:15], a[248:251], v[24:27], v[12:15]\n\t" \
  "ds_read_b64_tr_b16 a[142:143], v229 offset:12288\n\t" \
  "v_cvt_pk_bf16_f32 v150, v76, v77\n\t" \
  "v_mfma_f32_16x16x32_bf16 v[12:15], a[244:247], v[20:23], v[12:15]\n\t" \
  "v_max3_f32 v203, v203, v8, v9\n\t" \
  "v_max3_f32 v192, v192, v10, v11\n\t" \
  "v_mfma_f32_16x16x32_bf16 v[12:15], a[240:243], v[16:19], v[12:15]\n\t" \
  "ds_read_b64_tr_b16 a[144:145], v228 offset:256\n\t" \
  "v_cvt_pk_bf16_f32 v151, v78, v79\n\t" \
  "v_mfma_f32_16x16x32_bf16 v[32:35], a[192:195], v[48:51], v[180:183]\n\t" \
  "ds_read_b64_tr_b16 a[146:147], v228 offset:4352\n\t" \
  "v_cvt_pk_bf16_f32 v152, v96, v97\n\t" \
  "v_mfma_f32_16x16x32_bf16 v[32:35], a[196:199], v[52:55], v[32:35]\n\t" \
  "v_exp_f32 v99, v99\n\t" \
  "v_mfma_f32_16x16x32_bf16 v[32:35], a[200:203], v[56:59], v[32:35]\n\t" \
  "v_max3_f32 v203, v203, v12, v13\n\t" \
  "v_max3_f32 v192, v192, v14, v15\n\t" \
  "v_mfma_f32_16x16x32_bf16 v[32:35], a[204:207], v[60:63], v[32:35]\n\t" \
  "v_max_f32 v192, v192, v203\n\t" \
  "v_cmp_lt_f32 vcc, 0x42800000, v192\n\t" \
  "v_mfma_f32_16x16x32_bf16 v[36:39], a[220:223], v[60:63], v[180:183]\n\t" \
  "ds_read_b64_tr_b16 a[148:149], v228 offset:8448\n\t" \
  "v_cvt_pk_bf16_f32 v153, v98, v99\n\t" \
  "v_mfma_f32_16x16x32_bf16 v[36:39], a[216:219], v[56:59], v[36:39]\n\t" \
  "s_cbranch_vccnz .Lsf_c70\n\t" \
  ".Lsf_r70:\n\t" \
  "v_mfma_f32_16x16x32_bf16 v[36:39], a[212:215], v[52:55], v[36:39]\n\t" \
  "ds_read_b64_tr_b16 a[150:151], v228 offset:12544\n\t" \
  "v_max3_f32 v192, v32, v33, v34\n\t" \
  "v_mfma_f32_16x16x32_bf16 v[36:39], a[208:211], v[48:51], v[36:39]\n\t" \
  "v_exp_f32 v100, v100\n\t" \
  "v_mfma_f32_16x16x32_bf16 v[40:43], a[224:227], v[48:51], v[180:183]\n\t" \
  "v_exp_f32 v101, v101\n\t" \
  "v_mfma_f32_16x16x32_bf16 v[40:43], a[228:231], v[52:55], v[40:43]\n\t" \
  "ds_read_b64_tr_b16 a[152:153], v229 offset:256\n\t" \
  "v_cvt_pk_bf16_f32 v154, v100, v101\n\t" \
  "v_mfma_f32_16x16x32_bf16 v[40:43], a[232:235], v[56:59], v[40:43]\n\t" \
  "ds_read_b64_tr_b16 a[154:155], v229 offset:4352\n\t" \
  "v_max3_f32 v203, v35, v36, v37\n\t" \
  "v_mfma_f32_16x16x32_bf16 v[40:43], a[236:239], v[60:63], v[40:43]\n\t" \
  "ds_read_b64_tr_b16 a[156:157], v229 offset:8448\n\t" \
  "v_max3_f32 v192, v192, v38, v39\n\t" \
  "v_mfma_f32_16x16x32_bf16 v[44:47], a[252:255], v[60:63], v[180:183]\n\t" \
  "v_exp_f32 v102, v102\n\t" \
  "v_mfma_f32_16x16x32_bf16 v[44:47], a[248:251], v[56:59], v[44:47]\n\t" \
  "v_exp_f32 v103, v103\n\t" \
  "v_mfma_f32_16x16x32_bf16 v[44:47], a[244:247], v[52:55], v[44:47]\n\t" \
  "ds_read_b64_tr_b16 a[158:159], v229 offset:12544\n\t" \
  "v_max3_f32 v203, v203, v40, v41\n\t" \
  "v_mfma_f32_16x16x32_bf16 v[44:47], a[240:243], v[48:51], v[44:47]\n\t" \
  "ds_read_b64_tr_b16 a[160:161], v228 offset:512\n\t" \
  "v_max3_f32 v192, v192, v42, v43\n\t" \
  "v_mfma_f32_16x16x32_bf16 v[64:67], a[192:195], v[80:83], v[184:187]\n\t" \
  "ds_read_b64_tr_b16 a[162:163], v228 offset:4608\n\t" \
  "v_cvt_pk_bf16_f32 v155, v102, v103\n\t" \
  "v_mfma_f32_16x16x32_bf16 v[64:67], a[196:199], v[84:87], v[64:67]\n\t" \
  "v_exp_f32 v104, v104\n\t" \
  "v_mfma_f32_16x16x32_bf16 v[64:67], a[200:203], v[88:91], v[64:67]\n\t" \
  "v_max3_f32 v203, v203, v44, v45\n\t" \
  "v_max3_f32 v192, v192, v46, v47\n\t" \
  "v_mfma_f32_16x16x32_bf16 v[64:67], a[204:207], v[92:95], v[64:67]\n\t" \
  "v_max_f32 v192, v192, v203\n\t" \
  "v_cmp_lt_f32 vcc, 0x42800000, v192\n\t" \
  "v_mfma_f32_16x16x32_bf16 v[68:71], a[220:223], v[92:95], v[184:187]\n\t" \
  "v_exp_f32 v105, v105\n\t" \
  "v_mfma_f32_16x16x32_bf16 v[68:71], a[216:219], v[88:91], v[68:71]\n\t" \
  "s_cbranch_vccnz .Lsf_c71\n\t" \
  ".Lsf_r71:\n\t" \
  "v_mfma_f32_16x16x32_bf16 v[68:71], a[212:215], v[84:87], v[68:71]\n\t" \
  "ds_read_b64_tr_b16 a[164:165], v228 offset:8704\n\t" \
  "v_max3_f32 v192, v64, v65, v66\n\t" \
  "v_mfma_f32_16x16x32_bf16 v[68:71], a[208:211], v[80:83], v[68:71]\n\t" \
  "ds_read_b64_tr_b16 a[166:167], v228 offset:12800\n\t" \
  "v_cvt_pk_bf16_f32 v156, v104, v105\n\t" \
  "v_mfma_f32_16x16x32_bf16 v[72:75], a[224:227], v[80:83], v[184:187]\n\t" \
  "v_exp_f32 v106, v106\n\t" \
  "v_mfma_f32_16x16x32_bf16 v[72:75], a[228:231], v[84:87], v[72:75]\n\t" \
  "v_exp_f32 v107, v107\n\t" \
  "v_mfma_f32_16x16x32_bf16 v[72:75], a[232:235], v[88:91], v[72:75]\n\t" \
  "ds_read_b64_tr_b16 a[168:169], v229 offset:512\n\t" \
  "v_max3_f32 v203, v67, v68, v69\n\t" \
  "v_mfma_f32_16x16x32_bf16 v[72:75], a[236:239], v[92:95], v[72:75]\n\t" \
  "ds_read_b64_tr_b16 a[170:171], v229 offset:4608\n\t" \
  "v_max3_f32 v192, v192, v70, v71\n\t" \
  "v_mfma_f32_16x16x32_bf16 v[76:79], a[252:255], v[92:95], v[184:187]\n\t" \
  "ds_read_b64_tr_b16 a[172:173], v229 offset:8704\n\t" \
  "v_cvt_pk_bf16_f32 v157, v106, v107\n\t" \
  "v_mfma_f32_16x16x32_bf16 v[76:79], a[248:251], v[88:91], v[76:79]\n\t" \
  "v_exp_f32 v108, v108\n\t" \
  "v_mfma_f32_16x16x32_bf16 v[76:79], a[244:247], v[84:87], v[76:79]\n\t" \
  "v_max3_f32 v203, v203, v72, v73\n\t" \
  "v_max3_f32 v192, v192, v74, v75\n\t" \
  "v_mfma_f32_16x16x32_bf16 v[76:79], a[240:243], v[80:83], v[76:79]\n\t" \
  "v_exp_f32 v109, v109\n\t" \
  "v_mfma_f32_16x16x32_bf16 v[96:99], a[192:195], v[112:115], v[188:191]\n\t" \
  "ds_read_b64_tr_b16 a[174:175], v229 offset:12800\n\t" \
  "v_cvt_pk_bf16_f32 v158, v108, v109\n\t" \
  "v_mfma_f32_16x16x32_bf16 v[96:99], a[196:199], v[116:119], v[96:99]\n\t" \
  "v_exp_f32 v110, v110\n\t" \
  "v_mfma_f32_16x16x32_bf16 v[96:99], a[200:203], v[120:123], v[96:99]\n\t" \
  "ds_read_b64_tr_b16 a[176:177], v228 offset:768\n\t" \
  "v_max3_f32 v203, v203, v76, v77\n\t" \
  "v_mfma_f32_16x16x32_bf16 v[96:99], a[204:207], v[124:127], v[96:99]\n\t" \
  "v_max3_f32 v192, v192, v78, v79\n\t" \
  "v_max_f32 v192, v192, v203\n\t" \
  "v_mfma_f32_16x16x32_bf16 v[100:103], a[220:223], v[124:127], v[188:191]\n\t" \
  "ds_read_b64_tr_b16 a[178:179], v228 offset:4864\n\t" \
  "v_cmp_lt_f32 vcc, 0x42800000, v192\n\t" \
  "v_mfma_f32_16x16x32_bf16 v[100:103], a[216:219], v[120:123], v[100:103]\n\t" \
  "ds_read_b64_tr_b16 a[180:181], v228 offset:8960\n\t" \
  "v_exp_f32 v111, v111\n\t" \
  "v_mfma_f32_16x16x32_bf16 v[100:103], a[212:215], v[116:119], v[100:103]\n\t" \
  "s_cbranch_vccnz .Lsf_c72\n\t" \
  ".Lsf_r72:\n\t" \
  "v_mfma_f32_16x16x32_bf16 v[100:103], a[208:211], v[112:115], v[100:103]\n\t" \
  "ds_read_b64_tr_b16 a[182:183], v228 offset:13056\n\t" \
  "v_max3_f32 v192, v96, v97, v98\n\t" \
  "v_mfma_f32_16x16x32_bf16 v[104:107], a[224:227], v[112:115], v[188:191]\n\t" \
  "ds_read_b64_tr_b16 a[184:185], v229 offset:768\n\t" \
  "v_cvt_pk_bf16_f32 v159, v110, v111\n\t" \
  "v_mfma_f32_16x16x32_bf16 v[104:107], a[228:231], v[116:119], v[104:107]\n\t" \
  "ds_read_b64_tr_b16 a[186:187], v229 offset:4864\n\t" \
  "v_mfma_f32_16x16x32_bf16 v[104:107], a[232:235], v[120:123], v[104:107]\n\t" \
  "ds_read_b64_tr_b16 a[188:189], v229 offset:8960\n\t" \
  "v_max3_f32 v203, v99, v100, v101\n\t" \
  "v_mfma_f32_16x16x32_bf16 v[104:107], a[236:239], v[124:127], v[104:107]\n\t" \
  "ds_read_b64_tr_b16 a[190:191], v229 offset:13056\n\t" \
  "v_max3_f32 v192, v192, v102, v103\n\t" \
  "v_mfma_f32_16x16x32_bf16 v[108:111], a[252:255], v[124:127], v[188:191]\n\t" \
  "v_exp_f32 v0, v0\n\t" \
  "v_mfma_f32_16x16x32_bf16 v[108:111], a[248:251], v[120:123], v[108:111]\n\t" \
  "v_exp_f32 v1, v1\n\t" \
  "v_mfma_f32_16x16x32_bf16 v[108:111], a[244:247], v[116:119], v[108:111]\n\t" \
  "s_waitcnt vmcnt(0) lgkmcnt(0)\n\t" \
  "v_max3_f32 v203, v203, v104, v105\n\t" \
  "v_max3_f32 v192, v192, v106, v107\n\t" \
  "v_mfma_f32_16x16x32_bf16 v[108:111], a[240:243], v[112:115], v[108:111]\n\t" \
  "s_barrier\n\t" \
  "v_mfma_f32_16x16x32_bf16 a[0:3], a[128:131], v[128:131], a[0:3]\n\t" \
  "v_exp_f32 v2, v2\n\t" \
  "v_mfma_f32_16x16x32_bf16 a[0:3], a[132:135], v[132:135], a[0:3]\n\t" \
  "ds_read_b64_tr_b16 v[16:17], v210 offset:0\n\t" \
  "v_cvt_pk_bf16_f32 v0, v0, v1\n\t" \
  "v_mfma_f32_16x16x32_bf16 a[4:7], a[136:139], v[128:131], a[4:7]\n\t" \
  "ds_read_b64_tr_b16 v[18:19], v210 offset:4096\n\t" \
  "v_max3_f32 v203, v203, v108, v109\n\t" \
  "v_mfma_f32_16x16x32_bf16 a[4:7], a[140:143], v[132:135], a[4:7]\n\t" \
  "v_max3_f32 v192, v192, v110, v111\n\t" \
  "v_max_f32 v192, v192, v203\n\t" \
  "v_mfma_f32_16x16x32_bf16 a[8:11], a[144:147], v[128:131], a[8:11]\n\t" \
  "ds_read_b64_tr_b16 v[20:21], v210 offset:8192\n\t" \
  "v_cmp_lt_f32 vcc, 0x42800000, v192\n\t" \
  "v_mfma_f32_16x16x32_bf16 a[8:11], a[148:151], v[132:135], a[8:11]\n\t" \
  "v_exp_f32 v3, v3\n\t" \
  "v_mfma_f32_16x16x32_bf16 a[12:15], a[152:155], v[128:131], a[12:15]\n\t" \
  "s_cbranch_vccnz .Lsf_c73\n\t" \
  ".Lsf_r73:\n\t" \
  "v_mfma_f32_16x16x32_bf16 a[12:15], a[156:159], v[132:135], a[12:15]\n\t" \
  "ds_read_b64_tr_b16 v[22:23], v210 offset:12288\n\t" \
  "v_cvt_pk_bf16_f32 v1, v2, v3\n\t" \
  "v_mfma_f32_16x16x32_bf16 a[16:19], a[160:163], v[128:131], a[16:19]\n\t" \
  "v_exp_f32 v4, v4\n\t" \
  "v_mfma_f32_16x16x32_bf16 a[16:19], a[164:167], v[132:135], a[16:19]\n\t" \
  "v_exp_f32 v5, v5\n\t" \
  "v_mfma_f32_16x16x32_bf16 a[20:23], a[168:171], v[128:131], a[20:23]\n\t" \
  "ds_read_b64_tr_b16 v[24:25], v211 offset:0\n\t" \
  "v_cvt_pk_bf16_f32 v2, v4, v5\n\t" \
  "v_mfma_f32_16x16x32_bf16 a[20:23], a[172:175], v[132:135], a[20:23]\n\t" \
  "v_exp_f32 v6, v6\n\t" \
  "v_mfma_f32_16x16x32_bf16 a[24:27], a[176:179], v[128:131], a[24:27]\n\t" \
  "v_exp_f32 v7, v7\n\t" \
  "v_mfma_f32_16x16x32_bf16 a[24:27], a[180:183], v[132:135], a[24:27]\n\t" \
  "ds_read_b64_tr_b16 v[26:27], v211 offset:4096\n\t" \
  "v_cvt_pk_bf16_f32 v3, v6, v7\n\t" \
  "v_mfma_f32_16x16x32_bf16 a[28:31], a[184:187], v[128:131], a[28:31]\n\t" \
  "v_exp_f32 v8, v8\n\t" \
  "v_mfma_f32_16x16x32_bf16 a[28:31], a[188:191], v[132:135], a[28:31]\n\t" \
  "ds_read_b64_tr_b16 v[28:29], v211 offset:8192\n\t" \
  "v_smfmac_f32_16x16x64_bf16 v[160:163], v[216:219], v[128:135], v208\n\t" \
  "ds_read_b64_tr_b16 v[30:31], v211 offset:12288\n\t" \
  "v_mfma_f32_16x16x32_bf16 a[32:35], a[128:131], v[136:139], a[32:35]\n\t" \
  "ds_read_b64_tr_b16 v[48:49], v210 offset:256\n\t" \
  "v_mfma_f32_16x16x32_bf16 a[32:35], a[132:135], v[140:143], a[32:35]\n\t" \
  "ds_read_b64_tr_b16 v[50:51], v210 offset:4352\n\t" \
  "v_mfma_f32_16x16x32_bf16 a[36:39], a[136:139], v[136:139], a[36:39]\n\t" \
  "v_exp_f32 v9, v9\n\t" \
  "v_mfma_f32_16x16x32_bf16 a[36:39], a[140:143], v[140:143], a[36:39]\n\t" \
  "ds_read_b64_tr_b16 v[52:53], v210 offset:8448\n\t" \
  "v_cvt_pk_bf16_f32 v4, v8, v9\n\t" \
  "v_mfma_f32_16x16x32_bf16 a[40:43], a[144:147], v[136:139], a[40:43]\n\t" \
  "v_exp_f32 v10, v10\n\t" \
  "v_mfma_f32_16x16x32_bf16 a[40:43], a[148:151], v[140:143], a[40:43]\n\t" \
  "ds_read_b64_tr_b16 v[54:55], v210 offset:12544\n\t" \
  "v_mfma_f32_16x16x32_bf16 a[44:47], a[152:155], v[136:139], a[44:47]\n\t" \
  "v_exp_f32 v11, v11\n\t" \
  "v_mfma_f32_16x16x32_bf16 a[44:47], a[156:159], v[140:143], a[44:47]\n\t" \
  "ds_read_b64_tr_b16 v[56:57], v211 offset:256\n\t" \
  "v_cvt_pk_bf16_f32 v5, v10, v11\n\t" \
  "v_mfma_f32_16x16x32_bf16 a[48:51], a[160:163], v[136:139], a[48:51]\n\t" \
  "v_exp_f32 v12, v12\n\t" \
  "v_mfma_f32_16x16x32_bf16 a[48:51], a[164:167], v[140:143], a[48:51]\n\t" \
  "v_exp_f32 v13, v13\n\t" \
  "v_mfma_f32_16x16x32_bf16 a[52:55], a[168:171], v[136:139], a[52:55]\n\t" \
  "ds_read_b64_tr_b16 v[58:59], v211 offset:4352\n\t" \
  "v_cvt_pk_bf16_f32 v6, v12, v13\n\t" \
  "v_mfma_f32_16x16x32_bf16 a[52:55], a[172:175], v[140:143], a[52:55]\n\t" \
  "ds_read_b64_tr_b16 v[60:61], v211 offset:8448\n\t" \
  "v_mfma_f32_16x16x32_bf16 a[56:59], a[176:179], v[136:139], a[56:59]\n\t" \
  "v_exp_f32 v14, v14\n\t" \
  "v_mfma_f32_16x16x32_bf16 a[56:59], a[180:183], v[140:143], a[56:59]\n\t" \
  "v_exp_f32 v15, v15\n\t" \
  "v_mfma_f32_16x16x32_bf16 a[60:63], a[184:187], v[136:139], a[60:63]\n\t" \
  "ds_read_b64_tr_b16 v[62:63], v211 offset:12544\n\t" \
  "v_cvt_pk_bf16_f32 v7, v14, v15\n\t" \
  "v_mfma_f32_16x16x32_bf16 a[60:63], a[188:191], v[140:143], a[60:63]\n\t" \
  "v_exp_f32 v32, v32\n\t" \
  "v_smfmac_f32_16x16x64_bf16 v[164:167], v[216:219], v[136:143], v208\n\t" \
  "ds_read_b64_tr_b16 v[80:81], v210 offset:512\n\t" \
  "v_mfma_f32_16x16x32_bf16 a[64:67], a[128:131], v[144:147], a[64:67]\n\t" \
  "v_exp_f32 v33, v33\n\t" \
  "v_mfma_f32_16x16x32_bf16 a[64:67], a[132:135], v[148:151], a[64:67]\n\t" \
  "ds_read_b64_tr_b16 v[82:83], v210 offset:4608\n\t" \
  "v_cvt_pk_bf16_f32 v32, v32, v33\n\t" \
  "v_mfma_f32_16x16x32_bf16 a[68:71], a[136:139], v[144:147], a[68:71]\n\t" \
  "v_exp_f32 v34, v34\n\t" \
  "v_mfma_f32_16x16x32_bf16 a[68:71], a[140:143], v[148:151], a[68:71]\n\t" \
  "ds_read_b64_tr_b16 v[84:85], v210 offset:8704\n\t" \
  "v_mfma_f32_16x16x32_bf16 a[72:75], a[144:147], v[144:147], a[72:75]\n\t" \
  "v_exp_f32 v35, v35\n\t" \
  "v_mfma_f32_16x16x32_bf16 a[72:75], a[148:151], v[148:151], a[72:75]\n\t" \
  "ds_read_b64_tr_b16 v[86:87], v210 offset:12800\n\t" \
  "v_cvt_pk_bf16_f32 v33, v34, v35\n\t" \
  "v_mfma_f32_16x16x32_bf16 a[76:79], a[152:155], v[144:147], a[76:79]\n\t" \
  "v_exp_f32 v36, v36\n\t" \
  "v_mfma_f32_16x16x32_bf16 a[76:79], a[156:159], v[148:151], a[76:79]\n\t" \
  "ds_read_b64_tr_b16 v[88:89], v211 offset:512\n\t" \
  "v_mfma_f32_16x16x32_bf16 a[80:83], a[160:163], v[144:147], a[80:83]\n\t" \
  "v_exp_f32 v37, v37\n\t" \
  "v_mfma_f32_16x16x32_bf16 a[80:83], a[164:167], v[148:151], a[80:83]\n\t" \
  "ds_read_b64_tr_b16 v[90:91], v211 offset:4608\n\t" \
  "v_cvt_pk_bf16_f32 v34, v36, v37\n\t" \
  "v_mfma_f32_16x16x32_bf16 a[84:87], a[168:171], v[144:147], a[84:87]\n\t" \
  "v_exp_f32 v38, v38\n\t" \
  "v_mfma_f32_16x16x32_bf16 a[84:87], a[172:175], v[148:151], a[84:87]\n\t" \
  "v_exp_f32 v39, v39\n\t" \
  "v_mfma_f32_16x16x32_bf16 a[88:91], a[176:179], v[144:147], a[88:91]\n\t" \
  "ds_read_b64_tr_b16 v[92:93], v211 offset:8704\n\t" \
  "v_cvt_pk_bf16_f32 v35, v38, v39\n\t" \
  "v_mfma_f32_16x16x32_bf16 a[88:91], a[180:183], v[148:151], a[88:91]\n\t" \
  "v_exp_f32 v40, v40\n\t" \
  "v_mfma_f32_16x16x32_bf16 a[92:95], a[184:187], v[144:147], a[92:95]\n\t" \
  "ds_read_b64_tr_b16 v[94:95], v211 offset:12800\n\t" \
  "v_mfma_f32_16x16x32_bf16 a[92:95], a[188:191], v[148:151], a[92:95]\n\t" \
  "v_exp_f32 v41, v41\n\t" \
  "v_smfmac_f32_16x16x64_bf16 v[168:171], v[216:219], v[144:151], v208\n\t" \
  "ds_read_b64_tr_b16 v[112:113], v210 offset:768\n\t" \
  "v_cvt_pk_bf16_f32 v36, v40, v41\n\t" \
  "v_mfma_f32_16x16x32_bf16 a[96:99], a[128:131], v[152:155], a[96:99]\n\t" \
  "ds_read_b64_tr_b16 v[114:115], v210 offset:4864\n\t" \
  "v_mfma_f32_16x16x32_bf16 a[96:99], a[132:135], v[156:159], a[96:99]\n\t" \
  "v_exp_f32 v42, v42\n\t" \
  "v_mfma_f32_16x16x32_bf16 a[100:103], a[136:139], v[152:155], a[100:103]\n\t" \
  "ds_read_b64_tr_b16 v[116:117], v210 offset:8960\n\t" \
  "v_mfma_f32_16x16x32_bf16 a[100:103], a[140:143], v[156:159], a[100:103]\n\t" \
  "v_exp_f32 v43, v43\n\t" \
  "v_mfma_f32_16x16x32_bf16 a[104:107], a[144:147], v[152:155], a[104:107]\n\t" \
  "ds_read_b64_tr_b16 v[118:119], v210 offset:13056\n\t" \
  "v_cvt_pk_bf16_f32 v37, v42, v43\n\t" \
  "v_mfma_f32_16x16x32_bf16 a[104:107], a[148:151], v[156:159], a[104:107]\n\t" \
  "v_exp_f32 v44, v44\n\t" \
  "v_mfma_f32_16x16x32_bf16 a[108:111], a[152:155], v[152:155], a[108:111]\n\t" \
  "ds_read_b64_tr_b16 v[120:121], v211 offset:768\n\t" \
  "v_mfma_f32_16x16x32_bf16 a[108:111], a[156:159], v[156:159], a[108:111]\n\t" \
  "v_exp_f32 v45, v45\n\t" \
  "v_mfma_f32_16x16x32_bf16 a[112:115], a[160:163], v[152:155], a[112:115]\n\t" \
  "ds_read_b64_tr_b16 v[122:123], v211 offset:4864\n\t" \
  "v_cvt_pk_bf16_f32 v38, v44, v45\n\t" \
  "v_mfma_f32_16x16x32_bf16 a[112:115], a[164:167], v[156:159], a[112:115]\n\t" \
  "v_exp_f32 v46, v46\n\t" \
  "v_mfma_f32_16x16x32_bf16 a[116:119], a[168:171], v[152:155], a[116:119]\n\t" \
  "v_exp_f32 v47, v47\n\t" \
  "v_mfma_f32_16x16x32_bf16 a[116:119], a[172:175], v[156:159], a[116:119]\n\t" \
  "ds_read_b64_tr_b16 v[124:125], v211 offset:8960\n\t" \
  "v_cvt_pk_bf16_f32 v39, v46, v47\n\t" \
  "v_mfma_f32_16x16x32_bf16 a[120:123], a[176:179], v[152:155], a[120:123]\n\t" \
  "v_exp_f32 v64, v64\n\t" \
  "v_mfma_f32_16x16x32_bf16 a[120:123], a[180:183], v[156:159], a[120:123]\n\t" \
  "ds_read_b64_tr_b16 v[126:127], v211 offset:13056\n\t" \
  "v_mfma_f32_16x16x32_bf16 a[124:127], a[184:187], v[152:155], a[124:127]\n\t" \
  "v_exp_f32 v65, v65\n\t" \
  "v_mfma_f32_16x16x32_bf16 a[124:127], a[188:191], v[156:159], a[124:127]\n\t" \
  "v_exp_f32 v66, v66\n\t" \
  "v_smfmac_f32_16x16x64_bf16 v[172:175], v[216:219], v[152:159], v208\n\t" \
  "s_waitcnt lgkmcnt(15)\n\t" \
  "s_cmp_lg_u32 s99, 0\n\t" \
  "s_cbranch_scc1 .Lsf_p7\n\t" \
  ".Lsf_q7:\n\t" \
  "v_mfma_f32_16x16x32_bf16 a[0:3], v[16:19], v[0:3], a[0:3]\n\t" \
  "v_exp_f32 v67, v67\n\t" \
  "v_mfma_f32_16x16x32_bf16 a[0:3], v[20:23], v[4:7], a[0:3]\n\t" \
  "v_exp_f32 v68, v68\n\t" \
  "v_smfmac_f32_16x16x64_bf16 v[160:163], v[216:219], v[0:7], v208\n\t" \
  "v_cvt_pk_bf16_f32 v64, v64, v65\n\t" \
  "v_mfma_f32_16x16x32_bf16 a[4:7], v[24:27], v[0:3], a[4:7]\n\t" \
  "v_exp_f32 v69, v69\n\t" \
  "v_mfma_f32_16x16x32_bf16 a[4:7], v[28:31], v[4:7], a[4:7]\n\t" \
  "v_exp_f32 v70, v70\n\t" \
  "v_mfma_f32_16x16x32_bf16 a[8:11], v[48:51], v[0:3], a[8:11]\n\t" \
  "v_exp_f32 v71, v71\n\t" \
  "v_mfma_f32_16x16x32_bf16 a[8:11], v[52:55], v[4:7], a[8:11]\n\t" \
  "v_exp_f32 v72, v72\n\t" \
  "v_mfma_f32_16x16x32_bf16 a[12:15], v[56:59], v[0:3], a[12:15]\n\t" \
  "v_exp_f32 v73, v73\n\t" \
  "v_mfma_f32_16x16x32_bf16 a[12:15], v[60:63], v[4:7], a[12:15]\n\t" \
  "s_waitcnt lgkmcnt(8)\n\t" \
  "v_exp_f32 v74, v74\n\t" \
  "v_mfma_f32_16x16x32_bf16 a[16:19], v[80:83], v[0:3], a[16:19]\n\t" \
  "v_exp_f32 v75, v75\n\t" \
  "v_mfma_f32_16x16x32_bf16 a[16:19], v[84:87], v[4:7], a[16:19]\n\t" \
  "v_cvt_pk_bf16_f32 v65, v66, v67\n\t" \
  "v_cvt_pk_bf16_f32 v66, v68, v69\n\t" \
  "v_mfma_f32_16x16x32_bf16 a[20:23], v[88:91], v[0:3], a[20:23]\n\t" \
  "v_exp_f32 v76, v76\n\t" \
  "v_mfma_f32_16x16x32_bf16 a[20:23], v[92:95], v[4:7], a[20:23]\n\t" \
  "s_waitcnt lgkmcnt(0)\n\t" \
  "v_exp_f32 v77, v77\n\t" \
  "v_mfma_f32_16x16x32_bf16 a[24:27], v[112:115], v[0:3], a[24:27]\n\t" \
  "v_cvt_pk_bf16_f32 v67, v70, v71\n\t" \
  "v_cvt_pk_bf16_f32 v68, v72, v73\n\t" \
  "v_mfma_f32_16x16x32_bf16 a[24:27], v[116:119], v[4:7], a[24:27]\n\t" \
  "v_exp_f32 v78, v78\n\t" \
  "v_mfma_f32_16x16x32_bf16 a[28:31], v[120:123], v[0:3], a[28:31]\n\t" \
  "v_exp_f32 v79, v79\n\t" \
  "v_mfma_f32_16x16x32_bf16 a[28:31], v[124:127], v[4:7], a[28:31]\n\t" \
  "v_cvt_pk_bf16_f32 v69, v74, v75\n\t" \
  "v_cvt_pk_bf16_f32 v70, v76, v77\n\t" \
  "v_mfma_f32_16x16x32_bf16 a[32:35], v[16:19], v[32:35], a[32:35]\n\t" \
  "v_exp_f32 v96, v96\n\t" \
  "v_mfma_f32_16x16x32_bf16 a[32:35], v[20:23], v[36:39], a[32:35]\n\t" \
  "v_exp_f32 v97, v97\n\t" \
  "v_smfmac_f32_16x16x64_bf16 v[164:167], v[216:219], v[32:39], v208\n\t" \
  "v_cvt_pk_bf16_f32 v71, v78, v79\n\t" \
  "v_mfma_f32_16x16x32_bf16 a[36:39], v[24:27], v[32:35], a[36:39]\n\t" \
  "v_exp_f32 v98, v98\n\t" \
  "v_mfma_f32_16x16x32_bf16 a[36:39], v[28:31], v[36:39], a[36:39]\n\t" \
  "v_exp_f32 v99, v99\n\t" \
  "v_mfma_f32_16x16x32_bf16 a[40:43], v[48:51], v[32:35], a[40:43]\n\t" \
  "v_exp_f32 v100, v100\n\t" \
  "v_mfma_f32_16x16x32_bf16 a[40:43], v[52:55], v[36:39], a[40:43]\n\t" \
  "v_exp_f32 v101, v101\n\t" \
  "v_mfma_f32_16x16x32_bf16 a[44:47], v[56:59], v[32:35], a[44:47]\n\t" \
  "v_exp_f32 v102, v102\n\t" \
  "v_mfma_f32_16x16x32_bf16 a[44:47], v[60:63], v[36:39], a[44:47]\n\t" \
  "v_exp_f32 v103, v103\n\t" \
  "v_mfma_f32_16x16x32_bf16 a[48:51], v[80:83], v[32:35], a[48:51]\n\t" \
  "v_exp_f32 v104, v104\n\t" \
  "v_mfma_f32_16x16x32_bf16 a[48:51], v[84:87], v[36:39], a[48:51]\n\t" \
  "v_exp_f32 v105, v105\n\t" \
  "v_mfma_f32_16x16x32_bf16 a[52:55], v[88:91], v[32:35], a[52:55]\n\t" \
  "v_cvt_pk_bf16_f32 v96, v96, v97\n\t" \
  "v_cvt_pk_bf16_f32 v97, v98, v99\n\t" \
  "v_mfma_f32_16x16x32_bf16 a[52:55], v[92:95], v[36:39], a[52:55]\n\t" \
  "v_exp_f32 v106, v106\n\t" \
  "v_mfma_f32_16x16x32_bf16 a[56:59], v[112:115], v[32:35], a[56:59]\n\t" \
  "v_exp_f32 v107, v107\n\t" \
  "v_mfma_f32_16x16x32_bf16 a[56:59], v[116:119], v[36:39], a[56:59]\n\t" \
  "v_cvt_pk_bf16_f32 v98, v100, v101\n\t" \
  "v_cvt_pk_bf16_f32 v99, v102, v103\n\t" \
  "v_mfma_f32_16x16x32_bf16 a[60:63], v[120:123], v[32:35], a[60:63]\n\t" \
  "v_exp_f32 v108, v108\n\t" \
  "v_mfma_f32_16x16x32_bf16 a[60:63], v[124:127], v[36:39], a[60:63]\n\t" \
  "v_exp_f32 v109, v109\n\t" \
  "v_mfma_f32_16x16x32_bf16 a[64:67], v[16:19], v[64:67], a[64:67]\n\t" \
  "v_cvt_pk_bf16_f32 v100, v104, v105\n\t" \
  "v_cvt_pk_bf16_f32 v101, v106, v107\n\t" \
  "v_mfma_f32_16x16x32_bf16 a[64:67], v[20:23], v[68:71], a[64:67]\n\t" \
  "v_exp_f32 v110, v110\n\t" \
  "v_smfmac_f32_16x16x64_bf16 v[168:171], v[216:219], v[64:71], v208\n\t" \
  "v_cvt_pk_bf16_f32 v102, v108, v109\n\t" \
  "v_mfma_f32_16x16x32_bf16 a[68:71], v[24:27], v[64:67], a[68:71]\n\t" \
  "v_exp_f32 v111, v111\n\t" \
  "v_mfma_f32_16x16x32_bf16 a[68:71], v[28:31], v[68:71], a[68:71]\n\t" \
  "v_cvt_pk_bf16_f32 v103, v110, v111\n\t" \
  "v_mfma_f32_16x16x32_bf16 a[72:75], v[48:51], v[64:67], a[72:75]\n\t" \
  "v_mfma_f32_16x16x32_bf16 a[72:75], v[52:55], v[68:71], a[72:75]\n\t" \
  "v_mfma_f32_16x16x32_bf16 a[76:79], v[56:59], v[64:67], a[76:79]\n\t" \
  "v_mfma_f32_16x16x32_bf16 a[76:79], v[60:63], v[68:71], a[76:79]\n\t" \
  "v_mfma_f32_16x16x32_bf16 a[80:83], v[80:83], v[64:67], a[80:83]\n\t" \
  "v_mfma_f32_16x16x32_bf16 a[80:83], v[84:87], v[68:71], a[80:83]\n\t" \
  "v_mfma_f32_16x16x32_bf16 a[84:87], v[88:91], v[64:67], a[84:87]\n\t" \
  "v_mfma_f32_16x16x32_bf16 a[84:87], v[92:95], v[68:71], a[84:87]\n\t" \
  "v_mfma_f32_16x16x32_bf16 a[88:91], v[112:115], v[64:67], a[88:91]\n\t" \
  "v_mfma_f32_16x16x32_bf16 a[88:91], v[116:119], v[68:71], a[88:91]\n\t" \
  "v_mfma_f32_16x16x32_bf16 a[92:95], v[120:123], v[64:67], a[92:95]\n\t" \
  "v_mfma_f32_16x16x32_bf16 a[92:95], v[124:127], v[68:71], a[92:95]\n\t" \
  "v_mfma_f32_16x16x32_bf16 a[96:99], v[16:19], v[96:99], a[96:99]\n\t" \
  "v_mfma_f32_16x16x32_bf16 a[96:99], v[20:23], v[100:103], a[96:99]\n\t" \
  "v_smfmac_f32_16x16x64_bf16 v[172:175], v[216:219], v[96:103], v208\n\t" \
  "v_mfma_f32_16x16x32_bf16 a[100:103], v[24:27], v[96:99], a[100:103]\n\t" \
  "v_mfma_f32_16x16x32_bf16 a[100:103], v[28:31], v[100:103], a[100:103]\n\t" \
  "v_mfma_f32_16x16x32_bf16 a[104:107], v[48:51], v[96:99], a[104:107]\n\t" \
  "v_mfma_f32_16x16x32_bf16 a[104:107], v[52:55], v[100:103], a[104:107]\n\t" \
  "v_mfma_f32_16x16x32_bf16 a[108:111], v[56:59], v[96:99], a[108:111]\n\t" \
  "v_mfma_f32_16x16x32_bf16 a[108:111], v[60:63], v[100:103], a[108:111]\n\t" \
  "v_mfma_f32_16x16x32_bf16 a[112:115], v[80:83], v[96:99], a[112:115]\n\t" \
  "v_mfma_f32_16x16x32_bf16 a[112:115], v[84:87], v[100:103], a[112:115]\n\t" \
  "v_mfma_f32_16x16x32_bf16 a[116:119], v[88:91], v[96:99], a[116:119]\n\t" \
  "v_mfma_f32_16x16x32_bf16 a[116:119], v[92:95], v[100:103], a[116:119]\n\t" \
  "v_mfma_f32_16x16x32_bf16 a[120:123], v[112:115], v[96:99], a[120:123]\n\t" \
  "v_mfma_f32_16x16x32_bf16 a[120:123], v[116:119], v[100:103], a[120:123]\n\t" \
  "v_mfma_f32_16x16x32_bf16 a[124:127], v[120:123], v[96:99], a[124:127]\n\t" \
  "v_mfma_f32_16x16x32_bf16 a[124:127], v[124:127], v[100:103], a[124:127]\n\t" \
  "s_nop 7\n\t" \
  "s_waitcnt vmcnt(0)\n\t" \
  "s_barrier\n\t" \
  "s_add_u32 s77, s51, 131072\n\t" \
  "v_add_u32 v248, 14336, v240\n\t" \
  "s_waitcnt vmcnt(0) lgkmcnt(0)\n\t" \
  "s_barrier\n\t" \
  "s_waitcnt vmcnt(0) lgkmcnt(0)\n\t" \
  "s_barrier\n\t" \
  "v_add_u32 v249, 21504, v241\n\t" \
  "v_add_u32 v241, 7168, v241\n\t" \
  "s_mov_b32 s89, 0\n\t" \
  "v_lshlrev_b32 v212, 7, v233\n\t" \
  "v_cmp_gt_u32 vcc, 16, v233\n\t" \
  "v_bfrev_b32 v214, 1\n\t" \
  "s_nop 0\n\t" \
  "v_cndmask_b32 v212, v214, v212, vcc\n\t" \
  "s_waitcnt vmcnt(0) lgkmcnt(0)\n\t" \
  "s_barrier\n\t" \
  "s_add_u32 m0, s38, 16384\n\t" \
  "s_mov_b32 s96, 0\n\t" \
  "buffer_load_dwordx4 v232, s[68:71], s96 offen offset:0 lds\n\t" \
  "s_mov_b32 s96, 6144\n\t" \
  "buffer_load_dwordx4 v232, s[68:71], s96 offen offset:1024 lds\n\t" \
  "s_mov_b32 s96, 12288\n\t" \
  "buffer_load_dwordx4 v232, s[68:71], s96 offen offset:2048 lds\n\t" \
  "s_mov_b32 s96, 18432\n\t" \
  "buffer_load_dwordx4 v232, s[68:71], s96 offen offset:3072 lds\n\t" \
  "s_waitcnt vmcnt(0) lgkmcnt(0)\n\t" \
  "s_barrier\n\t" \
  "s_add_u32 m0, s38, 32768\n\t" \
  "s_mov_b32 s96, 28672\n\t" \
  "buffer_load_dwordx4 v232, s[68:71], s96 offen offset:0 lds\n\t" \
  "s_mov_b32 s96, 34816\n\t" \
  "buffer_load_dwordx4 v232, s[68:71], s96 offen offset:1024 lds\n\t" \
  "s_add_u32 m0, s38, 32768\n\t" \
  "s_mov_b32 s96, 40960\n\t" \
  "buffer_load_dwordx4 v232, s[68:71], s96 offen offset:2048 lds\n\t" \
  "s_mov_b32 s96, 47104\n\t" \
  "buffer_load_dwordx4 v232, s[68:71], s96 offen offset:3072 lds\n\t" \
  "s_add_u32 m0, s38, 114688\n\t" \
  "s_mov_b32 s96, 0\n\t" \
  "buffer_load_dwordx4 v232, s[72:75], s96 offen offset:0 lds\n\t" \
  "s_mov_b32 s96, 6144\n\t" \
  "buffer_load_dwordx4 v232, s[72:75], s96 offen offset:1024 lds\n\t" \
  "s_waitcnt vmcnt(0) lgkmcnt(0)\n\t" \
  "s_barrier\n\t" \
  "s_add_u32 m0, s38, 114688\n\t" \
  "s_mov_b32 s96, 12288\n\t" \
  "buffer_load_dwordx4 v232, s[72:75], s96 offen offset:2048 lds\n\t" \
  "s_mov_b32 s96, 18432\n\t" \
  "buffer_load_dwordx4 v232, s[72:75], s96 offen offset:3072 lds\n\t" \
  "s_lshr_b32 s78, s38, 6\n\t" \
  "s_add_u32 m0, s38, 131072\n\t" \
  "s_mov_b32 s96, 28672\n\t" \
  "buffer_load_dwordx4 v232, s[72:75], s96 offen offset:0 lds\n\t" \
  "v_xor_b32 v234, s78, v244\n\t" \
  "s_mov_b32 s96, 34816\n\t" \
  "v_xor_b32 v235, 32, v234\n\t" \
  "v_add_u32 v236, 65536, v234\n\t" \
  "buffer_load_dwordx4 v232, s[72:75], s96 offen offset:1024 lds\n\t" \
  "v_add_u32 v237, 65536, v235\n\t" \
  "v_add_u32 v238, 131072, v234\n\t" \
  "v_add_u32 v239, 131072, v235\n\t" \
  "s_mov_b32 s96, 40960\n\t" \
  "v_lshlrev_b32 v242, 4, v233\n\t" \
  "buffer_load_dwordx4 v232, s[72:75], s96 offen offset:2048 lds\n\t" \
  "v_add_u32 v213, s38, v242\n\t" \
  "v_add_u32 v215, 98304, v213\n\t" \
  "v_mov_b32 v243, s36\n\t" \
  "v_lshlrev_b32 v254, 9, v233\n\t" \
  "s_mov_b32 s96, 47104\n\t" \
  "v_and_b32 v244, 0x38, v233\n\t" \
  "buffer_load_dwordx4 v232, s[72:75], s96 offen offset:3072 lds\n\t" \
  "v_lshl_or_b32 v254, v244, 26, v254\n\t" \
  "s_brev_b32 s78, 1\n\t" \
  "v_mov_b32 v244, s78\n\t" \
  "v_cmp_eq_u32 vcc, s39, v233\n\t" \
  "v_add_u32 v245, s39, v233\n\t" \
  "v_bfi_b32 v245, 7, v245, v233\n\t" \
  "v_cndmask_b32 v254, v254, v244, vcc\n\t" \
  "s_waitcnt vmcnt(0) lgkmcnt(0)\n\t" \
  "s_barrier\n\t" \
  "v_and_b32 v245, 31, v245\n\t" \
  "v_lshlrev_b32 v245, 2, v245\n\t" \
  "v_writelane_b32 v244, 0, 0\n\t" \
  "s_waitcnt vmcnt(0) lgkmcnt(0)\n\t" \
  "s_barrier\n\t" \
  "ds_read_b128 v[192:195], v215 offset:32768\n\t" \
  "ds_read_b128 v[196:199], v215 offset:33792\n\t" \
  "v_lshrrev_b32 v250, 1, v233\n\t" \
  "v_lshlrev_b32 v250, 2, v250\n\t" \
  "v_add_u32 v250, 8192, v250\n\t" \
  "buffer_load_dword v209, v250, s[16:19], 0 offen sc1\n\t" \
  "s_waitcnt vmcnt(0) lgkmcnt(0)\n\t" \
  "s_barrier\n\t" \
  "ds_read_b128 v[200:203], v215 offset:34816\n\t" \
  "s_add_u32 s96, s40, 0\n\t" \
  "s_and_b32 s96, s96, 0x1c0000\n\t" \
  "s_add_u32 s96, s96, s51\n\t" \
  "s_add_u32 m0, s38, 0\n\t" \
  "s_nop 0\n\t" \
  "buffer_load_dwordx4 v240, s[24:27], s96 offen offset:0 lds\n\t" \
  "buffer_load_dwordx4 v241, s[24:27], s96 offen offset:1024 lds\n\t" \
  "s_waitcnt vmcnt(0) lgkmcnt(0)\n\t" \
  "v_and_b32 v251, 0xbfffbfff, v209\n\t" \
  "v_cmp_ne_u32_e64 s[84:85], s36, v251\n\t" \
  "buffer_load_dwordx4 v248, s[24:27], s96 offen offset:2048 lds\n\t" \
  "s_cmp_lg_u64 s[84:85], 0\n\t" \
  "s_cbranch_scc1 .Lsf_v\n\t" \
  ".Lsf_w:\n\t" \
  "v_and_b32 v251, 1, v233\n\t" \
  "v_lshlrev_b32 v251, 4, v251\n\t" \
  "buffer_load_dwordx4 v249, s[24:27], s96 offen offset:3072 lds\n\t" \
  "s_add_u32 s96, s40, 262144\n\t" \
  "v_lshrrev_b32 v251, v251, v209\n\t" \
  "v_and_b32 v251, 0x4000, v251\n\t" \
  "s_and_b32 s96, s96, 0x1c0000\n\t" \
  "s_add_u32 s96, s96, s51\n\t" \
  "s_add_u32 m0, s38, 49152\n\t" \
  "v_cmp_ne_u32_e64 s[84:85], 0, v251\n\t" \
  "buffer_load_dwordx4 v240, s[24:27], s96 offen offset:0 lds\n\t" \
  "s_bcnt1_i32_b32 s98, s84\n\t" \
  "s_bcnt1_i32_b32 s54, s85\n\t" \
  "s_cmp_gt_u32 s98, s54\n\t" \
  "s_cselect_b32 s55, 0x2000, 0\n\t" \
  "s_cselect_b32 s98, s84, s85\n\t" \
  "buffer_load_dwordx4 v241, s[24:27], s96 offen offset:1024 lds\n\t" \
  "s_cmp_lg_u32 s55, 0\n\t" \
  "s_cselect_b32 s84, s85, s84\n\t" \
  "s_lshr_b32 s85, s98, 16\n\t" \
  "s_bcnt1_i32_b32 s85, s85\n\t" \
  "s_lshr_b32 s84, s84, 16\n\t" \
  "s_bcnt1_i32_b32 s84, s84\n\t" \
  "s_add_u32 s84, s84, 7\n\t" \
  "buffer_load_dwordx4 v248, s[24:27], s96 offen offset:2048 lds\n\t" \
  "s_cmp_ge_u32 s85, s84\n\t" \
  "s_cselect_b32 s54, -1, 0\n\t" \
  "s_xor_b32 s98, s42, s55\n\t" \
  "s_and_b32 s98, s98, s54\n\t" \
  "s_xor_b32 s45, s45, s98\n\t" \
  "s_xor_b32 s46, s46, s98\n\t" \
  "s_xor_b32 s98, s43, s55\n\t" \
  "s_and_b32 s98, s98, s54\n\t" \
  "buffer_load_dwordx4 v249, s[24:27], s96 offen offset:3072 lds\n\t" \
  "s_xor_b32 s47, s47, s98\n\t" \
  "s_xor_b32 s48, s48, s55\n\t" \
  "s_xor_b32 s49, s49, s55\n\t" \
  "s_xor_b32 s98, s45, 0\n\t" \
  "s_lshl_b32 s64, s39, 17\n\t" \
  "s_bfe_u32 s70, s46, 0x20014\n\t" \
  "ds_read_b128 v[204:207], v215 offset:35840\n\t" \
  "s_lshl_b32 s70, s70, 14\n\t" \
  "s_xor_b32 s67, s70, 49152\n\t" \
  "s_lshl1_add_u32 s60, s67, s46\n\t" \
  "s_xor_b32 s68, s70, 32768\n\t" \
  "s_lshl1_add_u32 s61, s68, s46\n\t" \
  "s_xor_b32 s69, s70, 16384\n\t" \
  "s_lshl1_add_u32 s62, s69, s46\n\t" \
  "s_lshl1_add_u32 s63, s70, s46\n\t" \
  "s_waitcnt vmcnt(0) lgkmcnt(0)\n\t" \
  "s_barrier\n\t" \
  "s_mul_i32 s65, s39, 508\n\t" \
  "s_add_u32 s65, s65, s49\n\t" \
  "s_lshl_b32 s80, s39, 2\n\t" \
  "s_add_u32 s80, s80, s3\n\t" \
  "s_add_u32 s96, s40, 0\n\t" \
  "s_and_b32 s96, s96, 0x1c0000\n\t" \
  "s_add_u32 s96, s96, s77\n\t" \
  "s_add_u32 m0, s38, 98304\n\t" \
  "s_nop 0\n\t" \
  "buffer_load_dwordx4 v240, s[24:27], s96 offen offset:0 lds\n\t" \
  "s_lshl_b32 s80, s80, 6\n\t" \
  "s_add_u32 s76, s38, 147456\n\t" \
  "s_lshl_b32 s75, s80, 2\n\t" \
  "buffer_load_dwordx4 v241, s[24:27], s96 offen offset:1024 lds\n\t" \
  "s_add_u32 s75, s75, 2228224\n\t" \
  "s_lshr_b32 s71, s67, 7\n\t" \
  "buffer_load_dwordx4 v248, s[24:27], s96 offen offset:2048 lds\n\t" \
  "s_add_u32 s71, s71, s48\n\t" \
  "s_lshr_b32 s72, s68, 7\n\t" \
  "buffer_load_dwordx4 v249, s[24:27], s96 offen offset:3072 lds\n\t" \
  "s_add_u32 s72, s72, s48\n\t" \
  "s_add_u32 s96, s40, 524288\n\t" \
  "s_and_b32 s96, s96, 0x1c0000\n\t" \
  "s_add_u32 s96, s96, s51\n\t" \
  "s_add_u32 m0, s38, 131072\n\t" \
  "s_waitcnt vmcnt(0) lgkmcnt(0)\n\t" \
  "buffer_load_dwordx4 v240, s[24:27], s96 offen offset:0 lds\n\t" \
  "s_lshr_b32 s66, s69, 7\n\t" \
  "s_add_u32 s66, s66, s48\n\t" \
  "buffer_load_dwordx4 v241, s[24:27], s96 offen offset:1024 lds\n\t" \
  "s_lshr_b32 s73, s70, 7\n\t" \
  "s_add_u32 s73, s73, s48\n\t" \
  "buffer_load_dwordx4 v248, s[24:27], s96 offen offset:2048 lds\n\t" \
  "s_lshl3_add_u32 s67, s67, s47\n\t" \
  "s_lshl3_add_u32 s68, s68, s47\n\t" \
  "s_add_u32 s68, s68, 4096\n\t" \
  "buffer_load_dwordx4 v249, s[24:27], s96 offen offset:3072 lds\n\t" \
  "s_add_u32 s96, s40, 262144\n\t" \
  "s_and_b32 s96, s96, 0x1c0000\n\t" \
  "s_add_u32 s96, s96, s77\n\t" \
  "s_add_u32 m0, s38, 65536\n\t" \
  "s_nop 0\n\t" \
  "buffer_load_dwordx4 v240, s[24:27], s96 offen offset:0 lds\n\t" \
  "s_lshl3_add_u32 s69, s69, s47\n\t" \
  "s_add_u32 s69, s69, 16384\n\t" \
  "buffer_load_dwordx4 v241, s[24:27], s96 offen offset:1024 lds\n\t" \
  "s_lshl3_add_u32 s70, s70, s47\n\t" \
  "buffer_load_dwordx4 v248, s[24:27], s96 offen offset:2048 lds\n\t" \
  "buffer_load_dwordx4 v249, s[24:27], s96 offen offset:3072 lds\n\t" \
  "s_add_u32 s96, s40, 524288\n\t" \
  "s_and_b32 s96, s96, 0x1c0000\n\t" \
  "s_add_u32 s96, s96, s77\n\t" \
  "s_add_u32 m0, s38, 81920\n\t" \
  "s_waitcnt vmcnt(0) lgkmcnt(0)\n\t" \
  "s_barrier\n\t" \
  "buffer_load_dwordx4 v240, s[24:27], s96 offen offset:0 lds\n\t" \
  "ds_read_b128 v[176:179], v215 offset:16384\n\t" \
  "ds_read_b128 v[180:183], v215 offset:17408\n\t" \
  "ds_read_b128 v[184:187], v215 offset:18432\n\t" \
  "buffer_load_dwordx4 v241, s[24:27], s96 offen offset:1024 lds\n\t" \
  "ds_read_b128 v[188:191], v215 offset:19456\n\t" \
  "buffer_load_dwordx4 v248, s[24:27], s96 offen offset:2048 lds\n\t" \
  "buffer_load_dwordx4 v249, s[24:27], s96 offen offset:3072 lds\n\t" \
  "s_add_u32 s96, s40, 786432\n\t" \
  "s_and_b32 s96, s96, 0x1c0000\n\t" \
  "s_add_u32 s96, s96, s51\n\t" \
  "s_add_u32 m0, s38, 114688\n\t" \
  "s_waitcnt vmcnt(0) lgkmcnt(0)\n\t" \
  "buffer_load_dwordx4 v240, s[24:27], s96 offen offset:0 lds\n\t" \
  "ds_read_b128 a[224:227], v213 offset:16384\n\t" \
  "buffer_load_dwordx4 v241, s[24:27], s96 offen offset:1024 lds\n\t" \
  "buffer_load_dwordx4 v248, s[24:27], s96 offen offset:2048 lds\n\t" \
  "buffer_load_dwordx4 v249, s[24:27], s96 offen offset:3072 lds\n\t" \
  "v_add_u32 v230, 0x10000, v242\n\t" \
  "ds_read_b64_tr_b16 a[192:193], v234 offset:0\n\t" \
  "v_add_u32 v232, 0x20000, v242\n\t" \
  "v_add_f32 v160, v160, v161\n\t" \
  "v_rcp_f32 v224, v160\n\t" \
  "v_accvgpr_read_b32 v128, a0\n\t" \
  "v_accvgpr_read_b32 v129, a1\n\t" \
  "v_accvgpr_read_b32 v130, a2\n\t" \
  "v_accvgpr_read_b32 v131, a3\n\t" \
  "v_accvgpr_read_b32 v132, a4\n\t" \
  "v_accvgpr_read_b32 v133, a5\n\t" \
  "v_accvgpr_read_b32 v134, a6\n\t" \
  "v_accvgpr_read_b32 v135, a7\n\t" \
  "v_pk_mul_f32 v[128:129], v[128:129], v[224:225] op_sel_hi:[1,0]\n\t" \
  "v_pk_mul_f32 v[130:131], v[130:131], v[224:225] op_sel_hi:[1,0]\n\t" \
  "v_pk_mul_f32 v[132:133], v[132:133], v[224:225] op_sel_hi:[1,0]\n\t" \
  "v_pk_mul_f32 v[134:135], v[134:135], v[224:225] op_sel_hi:[1,0]\n\t" \
  "v_cvt_pk_bf16_f32 v128, v128, v129\n\t" \
  "v_cvt_pk_bf16_f32 v129, v130, v131\n\t" \
  "v_cvt_pk_bf16_f32 v130, v132, v133\n\t" \
  "v_cvt_pk_bf16_f32 v131, v134, v135\n\t" \
  "buffer_store_dwordx4 v[128:131], v242, s[16:19], s98 offen offset:0 sc1\n\t" \
  "ds_read_b64_tr_b16 a[194:195], v234 offset:4096\n\t" \
  "v_accvgpr_read_b32 v136, a8\n\t" \
  "v_accvgpr_read_b32 v137, a9\n\t" \
  "v_accvgpr_read_b32 v138, a10\n\t" \
  "v_accvgpr_read_b32 v139, a11\n\t" \
  "v_accvgpr_read_b32 v140, a12\n\t" \
  "v_accvgpr_read_b32 v141, a13\n\t" \
  "v_accvgpr_read_b32 v142, a14\n\t" \
  "v_accvgpr_read_b32 v143, a15\n\t" \
  "v_pk_mul_f32 v[136:137], v[136:137], v[224:225] op_sel_hi:[1,0]\n\t" \
  "v_pk_mul_f32 v[138:139], v[138:139], v[224:225] op_sel_hi:[1,0]\n\t" \
  "v_pk_mul_f32 v[140:141], v[140:141], v[224:225] op_sel_hi:[1,0]\n\t" \
  "v_pk_mul_f32 v[142:143], v[142:143], v[224:225] op_sel_hi:[1,0]\n\t" \
  "v_cvt_pk_bf16_f32 v136, v136, v137\n\t" \
  "v_cvt_pk_bf16_f32 v137, v138, v139\n\t" \
  "v_cvt_pk_bf16_f32 v138, v140, v141\n\t" \
  "v_cvt_pk_bf16_f32 v139, v142, v143\n\t" \
  "buffer_store_dwordx4 v[136:139], v242, s[16:19], s98 offen offset:1024 sc1\n\t" \
  "ds_read_b64_tr_b16 a[196:197], v235 offset:0\n\t" \
  "s_add_u32 s96, s40, 786432\n\t" \
  "ds_read_b64_tr_b16 a[198:199], v235 offset:4096\n\t" \
  "s_and_b32 s96, s96, 0x1c0000\n\t" \
  "ds_read_b64_tr_b16 a[200:201], v234 offset:8192\n\t" \
  "s_add_u32 s96, s96, s77\n\t" \
  "s_add_u32 m0, s38, 147456\n\t" \
  "s_waitcnt vmcnt(0) lgkmcnt(0)\n\t" \
  "s_barrier\n\t" \
  "v_accvgpr_read_b32 v0, a16\n\t" \
  "v_accvgpr_read_b32 v1, a17\n\t" \
  "v_accvgpr_read_b32 v2, a18\n\t" \
  "v_accvgpr_read_b32 v3, a19\n\t" \
  "v_accvgpr_read_b32 v4, a20\n\t" \
  "v_accvgpr_read_b32 v5, a21\n\t" \
  "v_accvgpr_read_b32 v6, a22\n\t" \
  "v_accvgpr_read_b32 v7, a23\n\t" \
  "v_pk_mul_f32 v[0:1], v[0:1], v[224:225] op_sel_hi:[1,0]\n\t" \
  "v_pk_mul_f32 v[2:3], v[2:3], v[224:225] op_sel_hi:[1,0]\n\t" \
  "v_pk_mul_f32 v[4:5], v[4:5], v[224:225] op_sel_hi:[1,0]\n\t" \
  "v_pk_mul_f32 v[6:7], v[6:7], v[224:225] op_sel_hi:[1,0]\n\t" \
  "v_cvt_pk_bf16_f32 v0, v0, v1\n\t" \
  "v_cvt_pk_bf16_f32 v1, v2, v3\n\t" \
  "v_cvt_pk_bf16_f32 v2, v4, v5\n\t" \
  "v_cvt_pk_bf16_f32 v3, v6, v7\n\t" \
  "buffer_store_dwordx4 v[0:3], v242, s[16:19], s98 offen offset:2048 sc1\n\t" \
  "v_accvgpr_read_b32 v8, a24\n\t" \
  "v_accvgpr_read_b32 v9, a25\n\t" \
  "v_accvgpr_read_b32 v10, a26\n\t" \
  "v_accvgpr_read_b32 v11, a27\n\t" \
  "v_accvgpr_read_b32 v12, a28\n\t" \
  "v_accvgpr_read_b32 v13, a29\n\t" \
  "v_accvgpr_read_b32 v14, a30\n\t" \
  "v_accvgpr_read_b32 v15, a31\n\t" \
  "v_pk_mul_f32 v[8:9], v[8:9], v[224:225] op_sel_hi:[1,0]\n\t" \
  "v_pk_mul_f32 v[10:11], v[10:11], v[224:225] op_sel_hi:[1,0]\n\t" \
  "v_pk_mul_f32 v[12:13], v[12:13], v[224:225] op_sel_hi:[1,0]\n\t" \
  "v_pk_mul_f32 v[14:15], v[14:15], v[224:225] op_sel_hi:[1,0]\n\t" \
  "v_cvt_pk_bf16_f32 v8, v8, v9\n\t" \
  "v_cvt_pk_bf16_f32 v9, v10, v11\n\t" \
  "v_cvt_pk_bf16_f32 v10, v12, v13\n\t" \
  "v_cvt_pk_bf16_f32 v11, v14, v15\n\t" \
  "buffer_store_dwordx4 v[8:11], v242, s[16:19], s98 offen offset:3072 sc1\n\t" \
  "buffer_load_dwordx4 v240, s[24:27], s96 offen offset:0 lds\n\t" \
  "s_xor_b32 s98, s45, 32768\n\t" \
  "ds_read_b64_tr_b16 a[202:203], v234 offset:12288\n\t" \
  "ds_read_b128 a[228:231], v213 offset:17408\n\t" \
  "ds_read_b128 a[232:235], v213 offset:18432\n\t" \
  "ds_read_b64_tr_b16 a[204:205], v235 offset:8192\n\t" \
  "ds_read_b64_tr_b16 a[206:207], v235 offset:12288\n\t" \
  "buffer_load_dwordx4 v241, s[24:27], s96 offen offset:1024 lds\n\t" \
  "ds_read_b128 a[236:239], v213 offset:19456\n\t" \
  "buffer_load_dwordx4 v248, s[24:27], s96 offen offset:2048 lds\n\t" \
  "ds_write_b128 v213, v[128:131] offset:16384\n\t" \
  "ds_write_b128 v213, v[136:139] offset:17408\n\t" \
  "ds_write_b128 v213, v[0:3] offset:18432\n\t" \
  "ds_write_b128 v213, v[8:11] offset:19456\n\t" \
  "buffer_load_dwordx4 v249, s[24:27], s96 offen offset:3072 lds\n\t" \
  "ds_read_b64_tr_b16 a[208:209], v236 offset:32768\n\t" \
  "ds_read_b64_tr_b16 a[210:211], v236 offset:36864\n\t" \
  "ds_read_b64_tr_b16 a[212:213], v237 offset:32768\n\t" \
  "ds_read_b64_tr_b16 a[214:215], v237 offset:36864\n\t" \
  "v_add_f32 v164, v164, v165\n\t" \
  "v_rcp_f32 v226, v164\n\t" \
  "v_accvgpr_read_b32 v0, a32\n\t" \
  "v_accvgpr_read_b32 v1, a33\n\t" \
  "v_accvgpr_read_b32 v2, a34\n\t" \
  "v_accvgpr_read_b32 v3, a35\n\t" \
  "v_accvgpr_read_b32 v4, a36\n\t" \
  "ds_read_b64_tr_b16 a[216:217], v236 offset:40960\n\t" \
  "v_accvgpr_read_b32 v5, a37\n\t" \
  "v_accvgpr_read_b32 v6, a38\n\t" \
  "v_accvgpr_read_b32 v7, a39\n\t" \
  "v_mul_f32 v0, v0, v226\n\t" \
  "v_mul_f32 v1, v1, v226\n\t" \
  "v_mul_f32 v2, v2, v226\n\t" \
  "v_mul_f32 v3, v3, v226\n\t" \
  "v_mul_f32 v4, v4, v226\n\t" \
  "v_mul_f32 v5, v5, v226\n\t" \
  "v_mul_f32 v6, v6, v226\n\t" \
  "v_mul_f32 v7, v7, v226\n\t" \
  "ds_read_b64_tr_b16 a[218:219], v236 offset:45056\n\t" \
  "v_cvt_pk_bf16_f32 v32, v0, v1\n\t" \
  "v_cvt_pk_bf16_f32 v33, v2, v3\n\t" \
  "v_cvt_pk_bf16_f32 v34, v4, v5\n\t" \
  "v_cvt_pk_bf16_f32 v35, v6, v7\n\t" \
  "v_accvgpr_read_b32 v8, a40\n\t" \
  "v_accvgpr_read_b32 v9, a41\n\t" \
  "v_accvgpr_read_b32 v10, a42\n\t" \
  "v_accvgpr_read_b32 v11, a43\n\t" \
  "v_accvgpr_read_b32 v12, a44\n\t" \
  "v_accvgpr_read_b32 v13, a45\n\t" \
  "v_accvgpr_read_b32 v14, a46\n\t" \
  "v_accvgpr_read_b32 v15, a47\n\t" \
  "v_mul_f32 v8, v8, v226\n\t" \
  "v_mul_f32 v9, v9, v226\n\t" \
  "s_waitcnt vmcnt(0) lgkmcnt(0)\n\t" \
  "s_barrier\n\t" \
  "buffer_store_dwordx4 v[32:35], v242, s[16:19], s98 offen offset:0 sc1\n\t" \
  "v_mul_f32 v10, v10, v226\n\t" \
  "v_mul_f32 v11, v11, v226\n\t" \
  "v_mul_f32 v12, v12, v226\n\t" \
  "v_mul_f32 v13, v13, v226\n\t" \
  "v_mul_f32 v14, v14, v226\n\t" \
  "v_mul_f32 v15, v15, v226\n\t" \
  "v_cvt_pk_bf16_f32 v36, v8, v9\n\t" \
  "v_cvt_pk_bf16_f32 v37, v10, v11\n\t" \
  "v_cvt_pk_bf16_f32 v38, v12, v13\n\t" \
  "v_cvt_pk_bf16_f32 v39, v14, v15\n\t" \
  "v_accvgpr_read_b32 v0, a48\n\t" \
  "v_accvgpr_read_b32 v1, a49\n\t" \
  "v_accvgpr_read_b32 v2, a50\n\t" \
  "v_accvgpr_read_b32 v3, a51\n\t" \
  "v_accvgpr_read_b32 v4, a52\n\t" \
  "v_accvgpr_read_b32 v5, a53\n\t" \
  "v_accvgpr_read_b32 v6, a54\n\t" \
  "v_accvgpr_read_b32 v7, a55\n\t" \
  "v_mul_f32 v0, v0, v226\n\t" \
  "v_mul_f32 v1, v1, v226\n\t" \
  "v_mul_f32 v2, v2, v226\n\t" \
  "v_mul_f32 v3, v3, v226\n\t" \
  "v_mul_f32 v4, v4, v226\n\t" \
  "v_mul_f32 v5, v5, v226\n\t" \
  "v_mul_f32 v6, v6, v226\n\t" \
  "v_mul_f32 v7, v7, v226\n\t" \
  "v_cvt_pk_bf16_f32 v40, v0, v1\n\t" \
  "v_cvt_pk_bf16_f32 v41, v2, v3\n\t" \
  "v_cvt_pk_bf16_f32 v42, v4, v5\n\t" \
  "v_cvt_pk_bf16_f32 v43, v6, v7\n\t" \
  "v_accvgpr_read_b32 v8, a56\n\t" \
  "buffer_store_dwordx4 v[36:39], v242, s[16:19], s98 offen offset:1024 sc1\n\t" \
  "v_accvgpr_read_b32 v9, a57\n\t" \
  "v_accvgpr_read_b32 v10, a58\n\t" \
  "v_accvgpr_read_b32 v11, a59\n\t" \
  "v_accvgpr_read_b32 v12, a60\n\t" \
  "v_accvgpr_read_b32 v13, a61\n\t" \
  "v_accvgpr_read_b32 v14, a62\n\t" \
  "v_accvgpr_read_b32 v15, a63\n\t" \
  "v_mul_f32 v8, v8, v226\n\t" \
  "v_mul_f32 v9, v9, v226\n\t" \
  "v_mul_f32 v10, v10, v226\n\t" \
  "v_mul_f32 v11, v11, v226\n\t" \
  "v_mul_f32 v12, v12, v226\n\t" \
  "ds_read_b64_tr_b16 a[220:221], v237 offset:40960\n\t" \
  "v_mul_f32 v13, v13, v226\n\t" \
  "v_mul_f32 v14, v14, v226\n\t" \
  "s_waitcnt vmcnt(0) lgkmcnt(0)\n\t" \
  "buffer_store_dword v243, v254, s[16:19], s49 offen offset:0 sc1\n\t" \
  "ds_read_b64_tr_b16 a[222:223], v237 offset:45056\n\t" \
  "v_mul_f32 v15, v15, v226\n\t" \
  "ds_read_b64_tr_b16 a[128:129], v234 offset:49152\n\t" \
  "buffer_store_dwordx4 v[40:43], v242, s[16:19], s98 offen offset:2048 sc1\n\t" \
  "v_cvt_pk_bf16_f32 v44, v8, v9\n\t" \
  "v_add_f32 v168, v168, v169\n\t" \
  "v_rcp_f32 v220, v168\n\t" \
  "v_cvt_pk_bf16_f32 v45, v10, v11\n\t" \
  "ds_read_b64_tr_b16 a[130:131], v234 offset:53248\n\t" \
  "v_cvt_pk_bf16_f32 v46, v12, v13\n\t" \
  "v_cvt_pk_bf16_f32 v47, v14, v15\n\t" \
  "ds_read_b64_tr_b16 a[132:133], v235 offset:49152\n\t" \
  "ds_read_b128 a[240:243], v213 offset:32768\n\t" \
  "s_add_u32 s96, s40, 1048576\n\t" \
  "ds_read_b128 a[244:247], v213 offset:33792\n\t" \
  "s_and_b32 s96, s96, 0x1c0000\n\t" \
  "buffer_store_dwordx4 v[44:47], v242, s[16:19], s98 offen offset:3072 sc1\n\t" \
  "ds_read_b128 a[248:251], v213 offset:34816\n\t" \
  "ds_read_b128 a[252:255], v213 offset:35840\n\t" \
  "ds_read_b64_tr_b16 a[134:135], v235 offset:53248\n\t" \
  "ds_write_b128 v213, v[32:35] offset:32768\n\t" \
  "ds_write_b128 v213, v[36:39] offset:33792\n\t" \
  "s_add_u32 s96, s96, s51\n\t" \
  "s_add_u32 m0, s38, 98304\n\t" \
  "v_accvgpr_read_b32 v64, a64\n\t" \
  "v_accvgpr_read_b32 v65, a65\n\t" \
  "v_accvgpr_read_b32 v66, a66\n\t" \
  "v_accvgpr_read_b32 v67, a67\n\t" \
  "s_waitcnt vmcnt(0) lgkmcnt(0)\n\t" \
  "s_barrier\n\t" \
  "buffer_load_dwordx4 v240, s[24:27], s96 offen offset:0 lds\n\t" \
  "v_accvgpr_read_b32 v68, a68\n\t" \
  "v_accvgpr_read_b32 v69, a69\n\t" \
  "s_xor_b32 s98, s45, 65536\n\t" \
  "ds_read_b128 v[128:131], v242 offset:16384\n\t" \
  "v_accvgpr_read_b32 v70, a70\n\t" \
  "v_accvgpr_read_b32 v71, a71\n\t" \
  "ds_read_b128 v[132:135], v242 offset:17408\n\t" \
  "v_mul_f32 v64, v64, v220\n\t" \
  "v_add_f32 v172, v172, v173\n\t" \
  "v_rcp_f32 v222, v172\n\t" \
  "buffer_load_dwordx4 v241, s[24:27], s96 offen offset:1024 lds\n\t" \
  "ds_read_b128 v[136:139], v242 offset:18432\n\t" \
  "buffer_load_dwordx4 v248, s[24:27], s96 offen offset:2048 lds\n\t" \
  "v_mul_f32 v65, v65, v220\n\t" \
  "ds_write_b128 v213, v[40:43] offset:34816\n\t" \
  "buffer_load_dwordx4 v249, s[24:27], s96 offen offset:3072 lds\n\t" \
  "ds_read_b128 v[140:143], v242 offset:19456\n\t" \
  "v_mul_f32 v66, v66, v220\n\t" \
  "ds_read_b64_tr_b16 a[136:137], v234 offset:57344\n\t" \
  "ds_read_b128 v[144:147], v242 offset:20480\n\t" \
  "v_mul_f32 v67, v67, v220\n\t" \
  "ds_read_b128 v[148:151], v242 offset:21504\n\t" \
  "v_mul_f32 v68, v68, v220\n\t" \
  "ds_write_b128 v213, v[44:47] offset:35840\n\t" \
  "ds_read_b64_tr_b16 a[138:139], v234 offset:61440\n\t" \
  "ds_read_b128 v[152:155], v242 offset:22528\n\t" \
  "v_mul_f32 v69, v69, v220\n\t" \
  "v_mul_f32 v70, v70, v220\n\t" \
  "ds_read_b64_tr_b16 a[140:141], v235 offset:57344\n\t" \
  "ds_read_b128 v[156:159], v242 offset:23552\n\t" \
  "v_mul_f32 v71, v71, v220\n\t" \
  "s_waitcnt vmcnt(0) lgkmcnt(0)\n\t" \
  "s_nop 1\n\t" \
  "s_branch .Lat_join\n\t" \
  ".Lsf_c00:\n\t" \
  "s_mov_b32 vcc_lo, 0\n\t" \
  "s_branch .Lsf_s0\n\t" \
  ".Lsf_c10:\n\t" \
  "s_mov_b32 vcc_lo, 1\n\t" \
  "s_branch .Lsf_s0\n\t" \
  ".Lsf_c20:\n\t" \
  "s_mov_b32 vcc_lo, 2\n\t" \
  "s_branch .Lsf_s0\n\t" \
  ".Lsf_c30:\n\t" \
  "s_mov_b32 vcc_lo, 3\n\t" \
  "s_branch .Lsf_s0\n\t" \
  ".Lsf_c40:\n\t" \
  "s_mov_b32 vcc_lo, 4\n\t" \
  "s_branch .Lsf_s0\n\t" \
  ".Lsf_c50:\n\t" \
  "s_mov_b32 vcc_lo, 5\n\t" \
  "s_branch .Lsf_s0\n\t" \
  ".Lsf_c60:\n\t" \
  "s_mov_b32 vcc_lo, 6\n\t" \
  "s_branch .Lsf_s0\n\t" \
  ".Lsf_c70:\n\t" \
  "s_mov_b32 vcc_lo, 7\n\t" \
  "s_branch .Lsf_s0\n\t" \
  ".Lsf_s0:\n\t" \
  "s_nop 7\n\t" \
  "s_nop 1\n\t" \
  "v_mov_b32 v193, v192\n\t" \
  "s_nop 1\n\t" \
  "v_permlane32_swap_b32 v192, v193\n\t" \
  "s_nop 1\n\t" \
  "v_max_f32 v192, v192, v193\n\t" \
  "s_nop 1\n\t" \
  "v_mov_b32 v193, v192\n\t" \
  "s_nop 1\n\t" \
  "v_permlane16_swap_b32 v192, v193\n\t" \
  "s_nop 1\n\t" \
  "v_max_f32 v192, v192, v193\n\t" \
  "v_max_f32 v192, 0, v192\n\t" \
  "v_sub_f32 v0, v0, v192\n\t" \
  "v_sub_f32 v1, v1, v192\n\t" \
  "v_sub_f32 v2, v2, v192\n\t" \
  "v_sub_f32 v3, v3, v192\n\t" \
  "v_sub_f32 v4, v4, v192\n\t" \
  "v_sub_f32 v5, v5, v192\n\t" \
  "v_sub_f32 v6, v6, v192\n\t" \
  "v_sub_f32 v7, v7, v192\n\t" \
  "v_sub_f32 v8, v8, v192\n\t" \
  "v_sub_f32 v9, v9, v192\n\t" \
  "v_sub_f32 v10, v10, v192\n\t" \
  "v_sub_f32 v11, v11, v192\n\t" \
  "v_sub_f32 v12, v12, v192\n\t" \
  "v_sub_f32 v13, v13, v192\n\t" \
  "v_sub_f32 v14, v14, v192\n\t" \
  "v_sub_f32 v15, v15, v192\n\t" \
  "v_sub_f32 v176, v176, v192\n\t" \
  "v_sub_f32 v177, v177, v192\n\t" \
  "v_sub_f32 v178, v178, v192\n\t" \
  "v_sub_f32 v179, v179, v192\n\t" \
  "v_exp_f32 v196, -v192\n\t" \
  "s_mov_b32 s99, 1\n\t" \
  "s_nop 1\n\t" \
  "s_cmp_eq_u32 vcc_lo, 0\n\t" \
  "s_cbranch_scc1 .Lsf_r00\n\t" \
  "s_cmp_eq_u32 vcc_lo, 1\n\t" \
  "s_cbranch_scc1 .Lsf_r10\n\t" \
  "s_cmp_eq_u32 vcc_lo, 2\n\t" \
  "s_cbranch_scc1 .Lsf_r20\n\t" \
  "s_cmp_eq_u32 vcc_lo, 3\n\t" \
  "s_cbranch_scc1 .Lsf_r30\n\t" \
  "s_cmp_eq_u32 vcc_lo, 4\n\t" \
  "s_cbranch_scc1 .Lsf_r40\n\t" \
  "s_cmp_eq_u32 vcc_lo, 5\n\t" \
  "s_cbranch_scc1 .Lsf_r50\n\t" \
  "s_cmp_eq_u32 vcc_lo, 6\n\t" \
  "s_cbranch_scc1 .Lsf_r60\n\t" \
  "s_cmp_eq_u32 vcc_lo, 7\n\t" \
  "s_cbranch_scc1 .Lsf_r70\n\t" \
  ".Lsf_c01:\n\t" \
  "s_mov_b32 vcc_lo, 0\n\t" \
  "s_branch .Lsf_s1\n\t" \
  ".Lsf_c11:\n\t" \
  "s_mov_b32 vcc_lo, 1\n\t" \
  "s_branch .Lsf_s1\n\t" \
  ".Lsf_c21:\n\t" \
  "s_mov_b32 vcc_lo, 2\n\t" \
  "s_branch .Lsf_s1\n\t" \
  ".Lsf_c31:\n\t" \
  "s_mov_b32 vcc_lo, 3\n\t" \
  "s_branch .Lsf_s1\n\t" \
  ".Lsf_c41:\n\t" \
  "s_mov_b32 vcc_lo, 4\n\t" \
  "s_branch .Lsf_s1\n\t" \
  ".Lsf_c51:\n\t" \
  "s_mov_b32 vcc_lo, 5\n\t" \
  "s_branch .Lsf_s1\n\t" \
  ".Lsf_c61:\n\t" \
  "s_mov_b32 vcc_lo, 6\n\t" \
  "s_branch .Lsf_s1\n\t" \
  ".Lsf_c71:\n\t" \
  "s_mov_b32 vcc_lo, 7\n\t" \
  "s_branch .Lsf_s1\n\t" \
  ".Lsf_s1:\n\t" \
  "s_nop 7\n\t" \
  "s_nop 1\n\t" \
  "v_mov_b32 v193, v192\n\t" \
  "s_nop 1\n\t" \
  "v_permlane32_swap_b32 v192, v193\n\t" \
  "s_nop 1\n\t" \
  "v_max_f32 v192, v192, v193\n\t" \
  "s_nop 1\n\t" \
  "v_mov_b32 v193, v192\n\t" \
  "s_nop 1\n\t" \
  "v_permlane16_swap_b32 v192, v193\n\t" \
  "s_nop 1\n\t" \
  "v_max_f32 v192, v192, v193\n\t" \
  "v_max_f32 v192, 0, v192\n\t" \
  "v_sub_f32 v32, v32, v192\n\t" \
  "v_sub_f32 v33, v33, v192\n\t" \
  "v_sub_f32 v34, v34, v192\n\t" \
  "v_sub_f32 v35, v35, v192\n\t" \
  "v_sub_f32 v36, v36, v192\n\t" \
  "v_sub_f32 v37, v37, v192\n\t" \
  "v_sub_f32 v38, v38, v192\n\t" \
  "v_sub_f32 v39, v39, v192\n\t" \
  "v_sub_f32 v40, v40, v192\n\t" \
  "v_sub_f32 v41, v41, v192\n\t" \
  "v_sub_f32 v42, v42, v192\n\t" \
  "v_sub_f32 v43, v43, v192\n\t" \
  "v_sub_f32 v44, v44, v192\n\t" \
  "v_sub_f32 v45, v45, v192\n\t" \
  "v_sub_f32 v46, v46, v192\n\t" \
  "v_sub_f32 v47, v47, v192\n\t" \
  "v_sub_f32 v180, v180, v192\n\t" \
  "v_sub_f32 v181, v181, v192\n\t" \
  "v_sub_f32 v182, v182, v192\n\t" \
  "v_sub_f32 v183, v183, v192\n\t" \
  "v_exp_f32 v197, -v192\n\t" \
  "s_mov_b32 s99, 1\n\t" \
  "s_nop 1\n\t" \
  "s_cmp_eq_u32 vcc_lo, 0\n\t" \
  "s_cbranch_scc1 .Lsf_r01\n\t" \
  "s_cmp_eq_u32 vcc_lo, 1\n\t" \
  "s_cbranch_scc1 .Lsf_r11\n\t" \
  "s_cmp_eq_u32 vcc_lo, 2\n\t" \
  "s_cbranch_scc1 .Lsf_r21\n\t" \
  "s_cmp_eq_u32 vcc_lo, 3\n\t" \
  "s_cbranch_scc1 .Lsf_r31\n\t" \
  "s_cmp_eq_u32 vcc_lo, 4\n\t" \
  "s_cbranch_scc1 .Lsf_r41\n\t" \
  "s_cmp_eq_u32 vcc_lo, 5\n\t" \
  "s_cbranch_scc1 .Lsf_r51\n\t" \
  "s_cmp_eq_u32 vcc_lo, 6\n\t" \
  "s_cbranch_scc1 .Lsf_r61\n\t" \
  "s_cmp_eq_u32 vcc_lo, 7\n\t" \
  "s_cbranch_scc1 .Lsf_r71\n\t" \
  ".Lsf_c02:\n\t" \
  "s_mov_b32 vcc_lo, 0\n\t" \
  "s_branch .Lsf_s2\n\t" \
  ".Lsf_c12:\n\t" \
  "s_mov_b32 vcc_lo, 1\n\t" \
  "s_branch .Lsf_s2\n\t" \
  ".Lsf_c22:\n\t" \
  "s_mov_b32 vcc_lo, 2\n\t" \
  "s_branch .Lsf_s2\n\t" \
  ".Lsf_c32:\n\t" \
  "s_mov_b32 vcc_lo, 3\n\t" \
  "s_branch .Lsf_s2\n\t" \
  ".Lsf_c42:\n\t" \
  "s_mov_b32 vcc_lo, 4\n\t" \
  "s_branch .Lsf_s2\n\t" \
  ".Lsf_c52:\n\t" \
  "s_mov_b32 vcc_lo, 5\n\t" \
  "s_branch .Lsf_s2\n\t" \
  ".Lsf_c62:\n\t" \
  "s_mov_b32 vcc_lo, 6\n\t" \
  "s_branch .Lsf_s2\n\t" \
  ".Lsf_c72:\n\t" \
  "s_mov_b32 vcc_lo, 7\n\t" \
  "s_branch .Lsf_s2\n\t" \
  ".Lsf_s2:\n\t" \
  "s_nop 7\n\t" \
  "s_nop 1\n\t" \
  "v_mov_b32 v193, v192\n\t" \
  "s_nop 1\n\t" \
  "v_permlane32_swap_b32 v192, v193\n\t" \
  "s_nop 1\n\t" \
  "v_max_f32 v192, v192, v193\n\t" \
  "s_nop 1\n\t" \
  "v_mov_b32 v193, v192\n\t" \
  "s_nop 1\n\t" \
  "v_permlane16_swap_b32 v192, v193\n\t" \
  "s_nop 1\n\t" \
  "v_max_f32 v192, v192, v193\n\t" \
  "v_max_f32 v192, 0, v192\n\t" \
  "v_sub_f32 v64, v64, v192\n\t" \
  "v_sub_f32 v65, v65, v192\n\t" \
  "v_sub_f32 v66, v66, v192\n\t" \
  "v_sub_f32 v67, v67, v192\n\t" \
  "v_sub_f32 v68, v68, v192\n\t" \
  "v_sub_f32 v69, v69, v192\n\t" \
  "v_sub_f32 v70, v70, v192\n\t" \
  "v_sub_f32 v71, v71, v192\n\t" \
  "v_sub_f32 v72, v72, v192\n\t" \
  "v_sub_f32 v73, v73, v192\n\t" \
  "v_sub_f32 v74, v74, v192\n\t" \
  "v_sub_f32 v75, v75, v192\n\t" \
  "v_sub_f32 v76, v76, v192\n\t" \
  "v_sub_f32 v77, v77, v192\n\t" \
  "v_sub_f32 v78, v78, v192\n\t" \
  "v_sub_f32 v79, v79, v192\n\t" \
  "v_sub_f32 v184, v184, v192\n\t" \
  "v_sub_f32 v185, v185, v192\n\t" \
  "v_sub_f32 v186, v186, v192\n\t" \
  "v_sub_f32 v187, v187, v192\n\t" \
  "v_exp_f32 v198, -v192\n\t" \
  "s_mov_b32 s99, 1\n\t" \
  "s_nop 1\n\t" \
  "s_cmp_eq_u32 vcc_lo, 0\n\t" \
  "s_cbranch_scc1 .Lsf_r02\n\t" \
  "s_cmp_eq_u32 vcc_lo, 1\n\t" \
  "s_cbranch_scc1 .Lsf_r12\n\t" \
  "s_cmp_eq_u32 vcc_lo, 2\n\t" \
  "s_cbranch_scc1 .Lsf_r22\n\t" \
  "s_cmp_eq_u32 vcc_lo, 3\n\t" \
  "s_cbranch_scc1 .Lsf_r32\n\t" \
  "s_cmp_eq_u32 vcc_lo, 4\n\t" \
  "s_cbranch_scc1 .Lsf_r42\n\t" \
  "s_cmp_eq_u32 vcc_lo, 5\n\t" \
  "s_cbranch_scc1 .Lsf_r52\n\t" \
  "s_cmp_eq_u32 vcc_lo, 6\n\t" \
  "s_cbranch_scc1 .Lsf_r62\n\t" \
  "s_cmp_eq_u32 vcc_lo, 7\n\t" \
  "s_cbranch_scc1 .Lsf_r72\n\t" \
  ".Lsf_c03:\n\t" \
  "s_mov_b32 vcc_lo, 0\n\t" \
  "s_branch .Lsf_s3\n\t" \
  ".Lsf_c13:\n\t" \
  "s_mov_b32 vcc_lo, 1\n\t" \
  "s_branch .Lsf_s3\n\t" \
  ".Lsf_c23:\n\t" \
  "s_mov_b32 vcc_lo, 2\n\t" \
  "s_branch .Lsf_s3\n\t" \
  ".Lsf_c33:\n\t" \
  "s_mov_b32 vcc_lo, 3\n\t" \
  "s_branch .Lsf_s3\n\t" \
  ".Lsf_c43:\n\t" \
  "s_mov_b32 vcc_lo, 4\n\t" \
  "s_branch .Lsf_s3\n\t" \
  ".Lsf_c53:\n\t" \
  "s_mov_b32 vcc_lo, 5\n\t" \
  "s_branch .Lsf_s3\n\t" \
  ".Lsf_c63:\n\t" \
  "s_mov_b32 vcc_lo, 6\n\t" \
  "s_branch .Lsf_s3\n\t" \
  ".Lsf_c73:\n\t" \
  "s_mov_b32 vcc_lo, 7\n\t" \
  "s_branch .Lsf_s3\n\t" \
  ".Lsf_s3:\n\t" \
  "s_nop 7\n\t" \
  "s_nop 1\n\t" \
  "v_mov_b32 v193, v192\n\t" \
  "s_nop 1\n\t" \
  "v_permlane32_swap_b32 v192, v193\n\t" \
  "s_nop 1\n\t" \
  "v_max_f32 v192, v192, v193\n\t" \
  "s_nop 1\n\t" \
  "v_mov_b32 v193, v192\n\t" \
  "s_nop 1\n\t" \
  "v_permlane16_swap_b32 v192, v193\n\t" \
  "s_nop 1\n\t" \
  "v_max_f32 v192, v192, v193\n\t" \
  "v_max_f32 v192, 0, v192\n\t" \
  "v_sub_f32 v96, v96, v192\n\t" \
  "v_sub_f32 v97, v97, v192\n\t" \
  "v_sub_f32 v98, v98, v192\n\t" \
  "v_sub_f32 v99, v99, v192\n\t" \
  "v_sub_f32 v100, v100, v192\n\t" \
  "v_sub_f32 v101, v101, v192\n\t" \
  "v_sub_f32 v102, v102, v192\n\t" \
  "v_sub_f32 v103, v103, v192\n\t" \
  "v_sub_f32 v104, v104, v192\n\t" \
  "v_sub_f32 v105, v105, v192\n\t" \
  "v_sub_f32 v106, v106, v192\n\t" \
  "v_sub_f32 v107, v107, v192\n\t" \
  "v_sub_f32 v108, v108, v192\n\t" \
  "v_sub_f32 v109, v109, v192\n\t" \
  "v_sub_f32 v110, v110, v192\n\t" \
  "v_sub_f32 v111, v111, v192\n\t" \
  "v_sub_f32 v188, v188, v192\n\t" \
  "v_sub_f32 v189, v189, v192\n\t" \
  "v_sub_f32 v190, v190, v192\n\t" \
  "v_sub_f32 v191, v191, v192\n\t" \
  "v_exp_f32 v199, -v192\n\t" \
  "s_mov_b32 s99, 1\n\t" \
  "s_nop 1\n\t" \
  "s_cmp_eq_u32 vcc_lo, 0\n\t" \
  "s_cbranch_scc1 .Lsf_r03\n\t" \
  "s_cmp_eq_u32 vcc_lo, 1\n\t" \
  "s_cbranch_scc1 .Lsf_r13\n\t" \
  "s_cmp_eq_u32 vcc_lo, 2\n\t" \
  "s_cbranch_scc1 .Lsf_r23\n\t" \
  "s_cmp_eq_u32 vcc_lo, 3\n\t" \
  "s_cbranch_scc1 .Lsf_r33\n\t" \
  "s_cmp_eq_u32 vcc_lo, 4\n\t" \
  "s_cbranch_scc1 .Lsf_r43\n\t" \
  "s_cmp_eq_u32 vcc_lo, 5\n\t" \
  "s_cbranch_scc1 .Lsf_r53\n\t" \
  "s_cmp_eq_u32 vcc_lo, 6\n\t" \
  "s_cbranch_scc1 .Lsf_r63\n\t" \
  "s_cmp_eq_u32 vcc_lo, 7\n\t" \
  "s_cbranch_scc1 .Lsf_r73\n\t" \
  ".Lsf_p0:\n\t" \
  "s_mov_b32 vcc_lo, 0\n\t" \
  "s_branch .Lsf_rs\n\t" \
  ".Lsf_p1:\n\t" \
  "s_mov_b32 vcc_lo, 1\n\t" \
  "s_branch .Lsf_rs\n\t" \
  ".Lsf_p2:\n\t" \
  "s_mov_b32 vcc_lo, 2\n\t" \
  "s_branch .Lsf_rs\n\t" \
  ".Lsf_p3:\n\t" \
  "s_mov_b32 vcc_lo, 3\n\t" \
  "s_branch .Lsf_rs\n\t" \
  ".Lsf_p4:\n\t" \
  "s_mov_b32 vcc_lo, 4\n\t" \
  "s_branch .Lsf_rs\n\t" \
  ".Lsf_p5:\n\t" \
  "s_mov_b32 vcc_lo, 5\n\t" \
  "s_branch .Lsf_rs\n\t" \
  ".Lsf_p6:\n\t" \
  "s_mov_b32 vcc_lo, 6\n\t" \
  "s_branch .Lsf_rs\n\t" \
  ".Lsf_p7:\n\t" \
  "s_mov_b32 vcc_lo, 7\n\t" \
  "s_branch .Lsf_rs\n\t" \
  ".Lsf_rs:\n\t" \
  "s_nop 7\n\t" \
  "s_nop 7\n\t" \
  "v_accvgpr_read_b32 v193, a0\n\t" \
  "v_mul_f32 v193, v196, v193\n\t" \
  "v_accvgpr_write_b32 a0, v193\n\t" \
  "v_accvgpr_read_b32 v193, a1\n\t" \
  "v_mul_f32 v193, v196, v193\n\t" \
  "v_accvgpr_write_b32 a1, v193\n\t" \
  "v_accvgpr_read_b32 v193, a2\n\t" \
  "v_mul_f32 v193, v196, v193\n\t" \
  "v_accvgpr_write_b32 a2, v193\n\t" \
  "v_accvgpr_read_b32 v193, a3\n\t" \
  "v_mul_f32 v193, v196, v193\n\t" \
  "v_accvgpr_write_b32 a3, v193\n\t" \
  "v_accvgpr_read_b32 v193, a4\n\t" \
  "v_mul_f32 v193, v196, v193\n\t" \
  "v_accvgpr_write_b32 a4, v193\n\t" \
  "v_accvgpr_read_b32 v193, a5\n\t" \
  "v_mul_f32 v193, v196, v193\n\t" \
  "v_accvgpr_write_b32 a5, v193\n\t" \
  "v_accvgpr_read_b32 v193, a6\n\t" \
  "v_mul_f32 v193, v196, v193\n\t" \
  "v_accvgpr_write_b32 a6, v193\n\t" \
  "v_accvgpr_read_b32 v193, a7\n\t" \
  "v_mul_f32 v193, v196, v193\n\t" \
  "v_accvgpr_write_b32 a7, v193\n\t" \
  "v_accvgpr_read_b32 v193, a8\n\t" \
  "v_mul_f32 v193, v196, v193\n\t" \
  "v_accvgpr_write_b32 a8, v193\n\t" \
  "v_accvgpr_read_b32 v193, a9\n\t" \
  "v_mul_f32 v193, v196, v193\n\t" \
  "v_accvgpr_write_b32 a9, v193\n\t" \
  "v_accvgpr_read_b32 v193, a10\n\t" \
  "v_mul_f32 v193, v196, v193\n\t" \
  "v_accvgpr_write_b32 a10, v193\n\t" \
  "v_accvgpr_read_b32 v193, a11\n\t" \
  "v_mul_f32 v193, v196, v193\n\t" \
  "v_accvgpr_write_b32 a11, v193\n\t" \
  "v_accvgpr_read_b32 v193, a12\n\t" \
  "v_mul_f32 v193, v196, v193\n\t" \
  "v_accvgpr_write_b32 a12, v193\n\t" \
  "v_accvgpr_read_b32 v193, a13\n\t" \
  "v_mul_f32 v193, v196, v193\n\t" \
  "v_accvgpr_write_b32 a13, v193\n\t" \
  "v_accvgpr_read_b32 v193, a14\n\t" \
  "v_mul_f32 v193, v196, v193\n\t" \
  "v_accvgpr_write_b32 a14, v193\n\t" \
  "v_accvgpr_read_b32 v193, a15\n\t" \
  "v_mul_f32 v193, v196, v193\n\t" \
  "v_accvgpr_write_b32 a15, v193\n\t" \
  "v_accvgpr_read_b32 v193, a16\n\t" \
  "v_mul_f32 v193, v196, v193\n\t" \
  "v_accvgpr_write_b32 a16, v193\n\t" \
  "v_accvgpr_read_b32 v193, a17\n\t" \
  "v_mul_f32 v193, v196, v193\n\t" \
  "v_accvgpr_write_b32 a17, v193\n\t" \
  "v_accvgpr_read_b32 v193, a18\n\t" \
  "v_mul_f32 v193, v196, v193\n\t" \
  "v_accvgpr_write_b32 a18, v193\n\t" \
  "v_accvgpr_read_b32 v193, a19\n\t" \
  "v_mul_f32 v193, v196, v193\n\t" \
  "v_accvgpr_write_b32 a19, v193\n\t" \
  "v_accvgpr_read_b32 v193, a20\n\t" \
  "v_mul_f32 v193, v196, v193\n\t" \
  "v_accvgpr_write_b32 a20, v193\n\t" \
  "v_accvgpr_read_b32 v193, a21\n\t" \
  "v_mul_f32 v193, v196, v193\n\t" \
  "v_accvgpr_write_b32 a21, v193\n\t" \
  "v_accvgpr_read_b32 v193, a22\n\t" \
  "v_mul_f32 v193, v196, v193\n\t" \
  "v_accvgpr_write_b32 a22, v193\n\t" \
  "v_accvgpr_read_b32 v193, a23\n\t" \
  "v_mul_f32 v193, v196, v193\n\t" \
  "v_accvgpr_write_b32 a23, v193\n\t" \
  "v_accvgpr_read_b32 v193, a24\n\t" \
  "v_mul_f32 v193, v196, v193\n\t" \
  "v_accvgpr_write_b32 a24, v193\n\t" \
  "v_accvgpr_read_b32 v193, a25\n\t" \
  "v_mul_f32 v193, v196, v193\n\t" \
  "v_accvgpr_write_b32 a25, v193\n\t" \
  "v_accvgpr_read_b32 v193, a26\n\t" \
  "v_mul_f32 v193, v196, v193\n\t" \
  "v_accvgpr_write_b32 a26, v193\n\t" \
  "v_accvgpr_read_b32 v193, a27\n\t" \
  "v_mul_f32 v193, v196, v193\n\t" \
  "v_accvgpr_write_b32 a27, v193\n\t" \
  "v_accvgpr_read_b32 v193, a28\n\t" \
  "v_mul_f32 v193, v196, v193\n\t" \
  "v_accvgpr_write_b32 a28, v193\n\t" \
  "v_accvgpr_read_b32 v193, a29\n\t" \
  "v_mul_f32 v193, v196, v193\n\t" \
  "v_accvgpr_write_b32 a29, v193\n\t" \
  "v_accvgpr_read_b32 v193, a30\n\t" \
  "v_mul_f32 v193, v196, v193\n\t" \
  "v_accvgpr_write_b32 a30, v193\n\t" \
  "v_accvgpr_read_b32 v193, a31\n\t" \
  "v_mul_f32 v193, v196, v193\n\t" \
  "v_accvgpr_write_b32 a31, v193\n\t" \
  "v_accvgpr_read_b32 v193, a32\n\t" \
  "v_mul_f32 v193, v197, v193\n\t" \
  "v_accvgpr_write_b32 a32, v193\n\t" \
  "v_accvgpr_read_b32 v193, a33\n\t" \
  "v_mul_f32 v193, v197, v193\n\t" \
  "v_accvgpr_write_b32 a33, v193\n\t" \
  "v_accvgpr_read_b32 v193, a34\n\t" \
  "v_mul_f32 v193, v197, v193\n\t" \
  "v_accvgpr_write_b32 a34, v193\n\t" \
  "v_accvgpr_read_b32 v193, a35\n\t" \
  "v_mul_f32 v193, v197, v193\n\t" \
  "v_accvgpr_write_b32 a35, v193\n\t" \
  "v_accvgpr_read_b32 v193, a36\n\t" \
  "v_mul_f32 v193, v197, v193\n\t" \
  "v_accvgpr_write_b32 a36, v193\n\t" \
  "v_accvgpr_read_b32 v193, a37\n\t" \
  "v_mul_f32 v193, v197, v193\n\t" \
  "v_accvgpr_write_b32 a37, v193\n\t" \
  "v_accvgpr_read_b32 v193, a38\n\t" \
  "v_mul_f32 v193, v197, v193\n\t" \
  "v_accvgpr_write_b32 a38, v193\n\t" \
  "v_accvgpr_read_b32 v193, a39\n\t" \
  "v_mul_f32 v193, v197, v193\n\t" \
  "v_accvgpr_write_b32 a39, v193\n\t" \
  "v_accvgpr_read_b32 v193, a40\n\t" \
  "v_mul_f32 v193, v197, v193\n\t" \
  "v_accvgpr_write_b32 a40, v193\n\t" \
  "v_accvgpr_read_b32 v193, a41\n\t" \
  "v_mul_f32 v193, v197, v193\n\t" \
  "v_accvgpr_write_b32 a41, v193\n\t" \
  "v_accvgpr_read_b32 v193, a42\n\t" \
  "v_mul_f32 v193, v197, v193\n\t" \
  "v_accvgpr_write_b32 a42, v193\n\t" \
  "v_accvgpr_read_b32 v193, a43\n\t" \
  "v_mul_f32 v193, v197, v193\n\t" \
  "v_accvgpr_write_b32 a43, v193\n\t" \
  "v_accvgpr_read_b32 v193, a44\n\t" \
  "v_mul_f32 v193, v197, v193\n\t" \
  "v_accvgpr_write_b32 a44, v193\n\t" \
  "v_accvgpr_read_b32 v193, a45\n\t" \
  "v_mul_f32 v193, v197, v193\n\t" \
  "v_accvgpr_write_b32 a45, v193\n\t" \
  "v_accvgpr_read_b32 v193, a46\n\t" \
  "v_mul_f32 v193, v197, v193\n\t" \
  "v_accvgpr_write_b32 a46, v193\n\t" \
  "v_accvgpr_read_b32 v193, a47\n\t" \
  "v_mul_f32 v193, v197, v193\n\t" \
  "v_accvgpr_write_b32 a47, v193\n\t" \
  "v_accvgpr_read_b32 v193, a48\n\t" \
  "v_mul_f32 v193, v197, v193\n\t" \
  "v_accvgpr_write_b32 a48, v193\n\t" \
  "v_accvgpr_read_b32 v193, a49\n\t" \
  "v_mul_f32 v193, v197, v193\n\t" \
  "v_accvgpr_write_b32 a49, v193\n\t" \
  "v_accvgpr_read_b32 v193, a50\n\t" \
  "v_mul_f32 v193, v197, v193\n\t" \
  "v_accvgpr_write_b32 a50, v193\n\t" \
  "v_accvgpr_read_b32 v193, a51\n\t" \
  "v_mul_f32 v193, v197, v193\n\t" \
  "v_accvgpr_write_b32 a51, v193\n\t" \
  "v_accvgpr_read_b32 v193, a52\n\t" \
  "v_mul_f32 v193, v197, v193\n\t" \
  "v_accvgpr_write_b32 a52, v193\n\t" \
  "v_accvgpr_read_b32 v193, a53\n\t" \
  "v_mul_f32 v193, v197, v193\n\t" \
  "v_accvgpr_write_b32 a53, v193\n\t" \
  "v_accvgpr_read_b32 v193, a54\n\t" \
  "v_mul_f32 v193, v197, v193\n\t" \
  "v_accvgpr_write_b32 a54, v193\n\t" \
  "v_accvgpr_read_b32 v193, a55\n\t" \
  "v_mul_f32 v193, v197, v193\n\t" \
  "v_accvgpr_write_b32 a55, v193\n\t" \
  "v_accvgpr_read_b32 v193, a56\n\t" \
  "v_mul_f32 v193, v197, v193\n\t" \
  "v_accvgpr_write_b32 a56, v193\n\t" \
  "v_accvgpr_read_b32 v193, a57\n\t" \
  "v_mul_f32 v193, v197, v193\n\t" \
  "v_accvgpr_write_b32 a57, v193\n\t" \
  "v_accvgpr_read_b32 v193, a58\n\t" \
  "v_mul_f32 v193, v197, v193\n\t" \
  "v_accvgpr_write_b32 a58, v193\n\t" \
  "v_accvgpr_read_b32 v193, a59\n\t" \
  "v_mul_f32 v193, v197, v193\n\t" \
  "v_accvgpr_write_b32 a59, v193\n\t" \
  "v_accvgpr_read_b32 v193, a60\n\t" \
  "v_mul_f32 v193, v197, v193\n\t" \
  "v_accvgpr_write_b32 a60, v193\n\t" \
  "v_accvgpr_read_b32 v193, a61\n\t" \
  "v_mul_f32 v193, v197, v193\n\t" \
  "v_accvgpr_write_b32 a61, v193\n\t" \
  "v_accvgpr_read_b32 v193, a62\n\t" \
  "v_mul_f32 v193, v197, v193\n\t" \
  "v_accvgpr_write_b32 a62, v193\n\t" \
  "v_accvgpr_read_b32 v193, a63\n\t" \
  "v_mul_f32 v193, v197, v193\n\t" \
  "v_accvgpr_write_b32 a63, v193\n\t" \
  "v_accvgpr_read_b32 v193, a64\n\t" \
  "v_mul_f32 v193, v198, v193\n\t" \
  "v_accvgpr_write_b32 a64, v193\n\t" \
  "v_accvgpr_read_b32 v193, a65\n\t" \
  "v_mul_f32 v193, v198, v193\n\t" \
  "v_accvgpr_write_b32 a65, v193\n\t" \
  "v_accvgpr_read_b32 v193, a66\n\t" \
  "v_mul_f32 v193, v198, v193\n\t" \
  "v_accvgpr_write_b32 a66, v193\n\t" \
  "v_accvgpr_read_b32 v193, a67\n\t" \
  "v_mul_f32 v193, v198, v193\n\t" \
  "v_accvgpr_write_b32 a67, v193\n\t" \
  "v_accvgpr_read_b32 v193, a68\n\t" \
  "v_mul_f32 v193, v198, v193\n\t" \
  "v_accvgpr_write_b32 a68, v193\n\t" \
  "v_accvgpr_read_b32 v193, a69\n\t" \
  "v_mul_f32 v193, v198, v193\n\t" \
  "v_accvgpr_write_b32 a69, v193\n\t" \
  "v_accvgpr_read_b32 v193, a70\n\t" \
  "v_mul_f32 v193, v198, v193\n\t" \
  "v_accvgpr_write_b32 a70, v193\n\t" \
  "v_accvgpr_read_b32 v193, a71\n\t" \
  "v_mul_f32 v193, v198, v193\n\t" \
  "v_accvgpr_write_b32 a71, v193\n\t" \
  "v_accvgpr_read_b32 v193, a72\n\t" \
  "v_mul_f32 v193, v198, v193\n\t" \
  "v_accvgpr_write_b32 a72, v193\n\t" \
  "v_accvgpr_read_b32 v193, a73\n\t" \
  "v_mul_f32 v193, v198, v193\n\t" \
  "v_accvgpr_write_b32 a73, v193\n\t" \
  "v_accvgpr_read_b32 v193, a74\n\t" \
  "v_mul_f32 v193, v198, v193\n\t" \
  "v_accvgpr_write_b32 a74, v193\n\t" \
  "v_accvgpr_read_b32 v193, a75\n\t" \
  "v_mul_f32 v193, v198, v193\n\t" \
  "v_accvgpr_write_b32 a75, v193\n\t" \
  "v_accvgpr_read_b32 v193, a76\n\t" \
  "v_mul_f32 v193, v198, v193\n\t" \
  "v_accvgpr_write_b32 a76, v193\n\t" \
  "v_accvgpr_read_b32 v193, a77\n\t" \
  "v_mul_f32 v193, v198, v193\n\t" \
  "v_accvgpr_write_b32 a77, v193\n\t" \
  "v_accvgpr_read_b32 v193, a78\n\t" \
  "v_mul_f32 v193, v198, v193\n\t" \
  "v_accvgpr_write_b32 a78, v193\n\t" \
  "v_accvgpr_read_b32 v193, a79\n\t" \
  "v_mul_f32 v193, v198, v193\n\t" \
  "v_accvgpr_write_b32 a79, v193\n\t" \
  "v_accvgpr_read_b32 v193, a80\n\t" \
  "v_mul_f32 v193, v198, v193\n\t" \
  "v_accvgpr_write_b32 a80, v193\n\t" \
  "v_accvgpr_read_b32 v193, a81\n\t" \
  "v_mul_f32 v193, v198, v193\n\t" \
  "v_accvgpr_write_b32 a81, v193\n\t" \
  "v_accvgpr_read_b32 v193, a82\n\t" \
  "v_mul_f32 v193, v198, v193\n\t" \
  "v_accvgpr_write_b32 a82, v193\n\t" \
  "v_accvgpr_read_b32 v193, a83\n\t" \
  "v_mul_f32 v193, v198, v193\n\t" \
  "v_accvgpr_write_b32 a83, v193\n\t" \
  "v_accvgpr_read_b32 v193, a84\n\t" \
  "v_mul_f32 v193, v198, v193\n\t" \
  "v_accvgpr_write_b32 a84, v193\n\t" \
  "v_accvgpr_read_b32 v193, a85\n\t" \
  "v_mul_f32 v193, v198, v193\n\t" \
  "v_accvgpr_write_b32 a85, v193\n\t" \
  "v_accvgpr_read_b32 v193, a86\n\t" \
  "v_mul_f32 v193, v198, v193\n\t" \
  "v_accvgpr_write_b32 a86, v193\n\t" \
  "v_accvgpr_read_b32 v193, a87\n\t" \
  "v_mul_f32 v193, v198, v193\n\t" \
  "v_accvgpr_write_b32 a87, v193\n\t" \
  "v_accvgpr_read_b32 v193, a88\n\t" \
  "v_mul_f32 v193, v198, v193\n\t" \
  "v_accvgpr_write_b32 a88, v193\n\t" \
  "v_accvgpr_read_b32 v193, a89\n\t" \
  "v_mul_f32 v193, v198, v193\n\t" \
  "v_accvgpr_write_b32 a89, v193\n\t" \
  "v_accvgpr_read_b32 v193, a90\n\t" \
  "v_mul_f32 v193, v198, v193\n\t" \
  "v_accvgpr_write_b32 a90, v193\n\t" \
  "v_accvgpr_read_b32 v193, a91\n\t" \
  "v_mul_f32 v193, v198, v193\n\t" \
  "v_accvgpr_write_b32 a91, v193\n\t" \
  "v_accvgpr_read_b32 v193, a92\n\t" \
  "v_mul_f32 v193, v198, v193\n\t" \
  "v_accvgpr_write_b32 a92, v193\n\t" \
  "v_accvgpr_read_b32 v193, a93\n\t" \
  "v_mul_f32 v193, v198, v193\n\t" \
  "v_accvgpr_write_b32 a93, v193\n\t" \
  "v_accvgpr_read_b32 v193, a94\n\t" \
  "v_mul_f32 v193, v198, v193\n\t" \
  "v_accvgpr_write_b32 a94, v193\n\t" \
  "v_accvgpr_read_b32 v193, a95\n\t" \
  "v_mul_f32 v193, v198, v193\n\t" \
  "v_accvgpr_write_b32 a95, v193\n\t" \
  "v_accvgpr_read_b32 v193, a96\n\t" \
  "v_mul_f32 v193, v199, v193\n\t" \
  "v_accvgpr_write_b32 a96, v193\n\t" \
  "v_accvgpr_read_b32 v193, a97\n\t" \
  "v_mul_f32 v193, v199, v193\n\t" \
  "v_accvgpr_write_b32 a97, v193\n\t" \
  "v_accvgpr_read_b32 v193, a98\n\t" \
  "v_mul_f32 v193, v199, v193\n\t" \
  "v_accvgpr_write_b32 a98, v193\n\t" \
  "v_accvgpr_read_b32 v193, a99\n\t" \
  "v_mul_f32 v193, v199, v193\n\t" \
  "v_accvgpr_write_b32 a99, v193\n\t" \
  "v_accvgpr_read_b32 v193, a100\n\t" \
  "v_mul_f32 v193, v199, v193\n\t" \
  "v_accvgpr_write_b32 a100, v193\n\t" \
  "v_accvgpr_read_b32 v193, a101\n\t" \
  "v_mul_f32 v193, v199, v193\n\t" \
  "v_accvgpr_write_b32 a101, v193\n\t" \
  "v_accvgpr_read_b32 v193, a102\n\t" \
  "v_mul_f32 v193, v199, v193\n\t" \
  "v_accvgpr_write_b32 a102, v193\n\t" \
  "v_accvgpr_read_b32 v193, a103\n\t" \
  "v_mul_f32 v193, v199, v193\n\t" \
  "v_accvgpr_write_b32 a103, v193\n\t" \
  "v_accvgpr_read_b32 v193, a104\n\t" \
  "v_mul_f32 v193, v199, v193\n\t" \
  "v_accvgpr_write_b32 a104, v193\n\t" \
  "v_accvgpr_read_b32 v193, a105\n\t" \
  "v_mul_f32 v193, v199, v193\n\t" \
  "v_accvgpr_write_b32 a105, v193\n\t" \
  "v_accvgpr_read_b32 v193, a106\n\t" \
  "v_mul_f32 v193, v199, v193\n\t" \
  "v_accvgpr_write_b32 a106, v193\n\t" \
  "v_accvgpr_read_b32 v193, a107\n\t" \
  "v_mul_f32 v193, v199, v193\n\t" \
  "v_accvgpr_write_b32 a107, v193\n\t" \
  "v_accvgpr_read_b32 v193, a108\n\t" \
  "v_mul_f32 v193, v199, v193\n\t" \
  "v_accvgpr_write_b32 a108, v193\n\t" \
  "v_accvgpr_read_b32 v193, a109\n\t" \
  "v_mul_f32 v193, v199, v193\n\t" \
  "v_accvgpr_write_b32 a109, v193\n\t" \
  "v_accvgpr_read_b32 v193, a110\n\t" \
  "v_mul_f32 v193, v199, v193\n\t" \
  "v_accvgpr_write_b32 a110, v193\n\t" \
  "v_accvgpr_read_b32 v193, a111\n\t" \
  "v_mul_f32 v193, v199, v193\n\t" \
  "v_accvgpr_write_b32 a111, v193\n\t" \
  "v_accvgpr_read_b32 v193, a112\n\t" \
  "v_mul_f32 v193, v199, v193\n\t" \
  "v_accvgpr_write_b32 a112, v193\n\t" \
  "v_accvgpr_read_b32 v193, a113\n\t" \
  "v_mul_f32 v193, v199, v193\n\t" \
  "v_accvgpr_write_b32 a113, v193\n\t" \
  "v_accvgpr_read_b32 v193, a114\n\t" \
  "v_mul_f32 v193, v199, v193\n\t" \
  "v_accvgpr_write_b32 a114, v193\n\t" \
  "v_accvgpr_read_b32 v193, a115\n\t" \
  "v_mul_f32 v193, v199, v193\n\t" \
  "v_accvgpr_write_b32 a115, v193\n\t" \
  "v_accvgpr_read_b32 v193, a116\n\t" \
  "v_mul_f32 v193, v199, v193\n\t" \
  "v_accvgpr_write_b32 a116, v193\n\t" \
  "v_accvgpr_read_b32 v193, a117\n\t" \
  "v_mul_f32 v193, v199, v193\n\t" \
  "v_accvgpr_write_b32 a117, v193\n\t" \
  "v_accvgpr_read_b32 v193, a118\n\t" \
  "v_mul_f32 v193, v199, v193\n\t" \
  "v_accvgpr_write_b32 a118, v193\n\t" \
  "v_accvgpr_read_b32 v193, a119\n\t" \
  "v_mul_f32 v193, v199, v193\n\t" \
  "v_accvgpr_write_b32 a119, v193\n\t" \
  "v_accvgpr_read_b32 v193, a120\n\t" \
  "v_mul_f32 v193, v199, v193\n\t" \
  "v_accvgpr_write_b32 a120, v193\n\t" \
  "v_accvgpr_read_b32 v193, a121\n\t" \
  "v_mul_f32 v193, v199, v193\n\t" \
  "v_accvgpr_write_b32 a121, v193\n\t" \
  "v_accvgpr_read_b32 v193, a122\n\t" \
  "v_mul_f32 v193, v199, v193\n\t" \
  "v_accvgpr_write_b32 a122, v193\n\t" \
  "v_accvgpr_read_b32 v193, a123\n\t" \
  "v_mul_f32 v193, v199, v193\n\t" \
  "v_accvgpr_write_b32 a123, v193\n\t" \
  "v_accvgpr_read_b32 v193, a124\n\t" \
  "v_mul_f32 v193, v199, v193\n\t" \
  "v_accvgpr_write_b32 a124, v193\n\t" \
  "v_accvgpr_read_b32 v193, a125\n\t" \
  "v_mul_f32 v193, v199, v193\n\t" \
  "v_accvgpr_write_b32 a125, v193\n\t" \
  "v_accvgpr_read_b32 v193, a126\n\t" \
  "v_mul_f32 v193, v199, v193\n\t" \
  "v_accvgpr_write_b32 a126, v193\n\t" \
  "v_accvgpr_read_b32 v193, a127\n\t" \
  "v_mul_f32 v193, v199, v193\n\t" \
  "v_accvgpr_write_b32 a127, v193\n\t" \
  "v_mul_f32 v160, v196, v160\n\t" \
  "v_mul_f32 v161, v196, v161\n\t" \
  "v_mul_f32 v162, v196, v162\n\t" \
  "v_mul_f32 v163, v196, v163\n\t" \
  "v_mov_b32 v196, 1.0\n\t" \
  "v_mul_f32 v164, v197, v164\n\t" \
  "v_mul_f32 v165, v197, v165\n\t" \
  "v_mul_f32 v166, v197, v166\n\t" \
  "v_mul_f32 v167, v197, v167\n\t" \
  "v_mov_b32 v197, 1.0\n\t" \
  "v_mul_f32 v168, v198, v168\n\t" \
  "v_mul_f32 v169, v198, v169\n\t" \
  "v_mul_f32 v170, v198, v170\n\t" \
  "v_mul_f32 v171, v198, v171\n\t" \
  "v_mov_b32 v198, 1.0\n\t" \
  "v_mul_f32 v172, v199, v172\n\t" \
  "v_mul_f32 v173, v199, v173\n\t" \
  "v_mul_f32 v174, v199, v174\n\t" \
  "v_mul_f32 v175, v199, v175\n\t" \
  "v_mov_b32 v199, 1.0\n\t" \
  "s_mov_b32 s99, 0\n\t" \
  "s_nop 1\n\t" \
  "s_cmp_eq_u32 vcc_lo, 0\n\t" \
  "s_cbranch_scc1 .Lsf_q0\n\t" \
  "s_cmp_eq_u32 vcc_lo, 1\n\t" \
  "s_cbranch_scc1 .Lsf_q1\n\t" \
  "s_cmp_eq_u32 vcc_lo, 2\n\t" \
  "s_cbranch_scc1 .Lsf_q2\n\t" \
  "s_cmp_eq_u32 vcc_lo, 3\n\t" \
  "s_cbranch_scc1 .Lsf_q3\n\t" \
  "s_cmp_eq_u32 vcc_lo, 4\n\t" \
  "s_cbranch_scc1 .Lsf_q4\n\t" \
  "s_cmp_eq_u32 vcc_lo, 5\n\t" \
  "s_cbranch_scc1 .Lsf_q5\n\t" \
  "s_cmp_eq_u32 vcc_lo, 6\n\t" \
  "s_cbranch_scc1 .Lsf_q6\n\t" \
  "s_cmp_eq_u32 vcc_lo, 7\n\t" \
  "s_cbranch_scc1 .Lsf_q7\n\t" \
  "s_endpgm\n\t" \
  ".Lsf_v:\n\t" \
  "s_mov_b32 s98, 0\n\t" \
  ".Lsf_v1:\n\t" \
  "s_sleep 4\n\t" \
  "buffer_load_dword v209, v250, s[16:19], 0 offen sc1\n\t" \
  "s_waitcnt vmcnt(0)\n\t" \
  "v_and_b32 v251, 0xbfffbfff, v209\n\t" \
  "v_cmp_ne_u32_e64 s[84:85], s36, v251\n\t" \
  "s_cmp_lg_u64 s[84:85], 0\n\t" \
  "s_cbranch_scc0 .Lsf_w\n\t" \
  "s_add_u32 s98, s98, 1\n\t" \
  "s_cmp_lt_u32 s98, " FQ_CAP "\n\t" \
  "s_cbranch_scc1 .Lsf_v1\n\t" \
  "v_mov_b32 v251, s36\n\t" \
  "buffer_store_dword v251, off, s[16:19], 0 offset:256 sc1\n\t" \
  "s_branch .Lsf_w\n\t" \
  ".subsection 0\n\t" \
  ".Lop_end:\n\t" \
  ""
#define OP_GA0 "=&{v[160:163]}"
#define OP_GA1 "=&{v[164:167]}"
#define OP_CW "=&{s82}"
#define OP_BB "=&{s83}"
#define AO_CLOB "v128", "v129", "v130", "v131", "v132", "v133", "v134", "v135", "v136", "v137", "v138", "v139", "v140", "v141", "v142", "v143", "v144", "v145", "v146", "v147", "v148", "v149", "v150", "v151", "v152", "v153", "v154", "v155", "v156", "v157", "v158", "v159", "v168", "v169", "v170", "v171", "v172", "v173", "v174", "v175", "v208", "v209", "v210", "v211", "v212", "v213", "v214", "v215", "v216", "v217", "v218", "v219", "v234", "v235", "v236", "v237", "v238", "v239", "v242", "v243", "v248", "v249", "v250", "v251", "v252", "v254", "s76", "s77", "s78", "s79", "s80", "s81", "s84", "s85", "s86", "s87", "s88", "s89", "s90", "s91", "s92", "s93", "s94", "s95", "s96", "s97", "s98", "s99", "vcc", "scc", "memory"
#define OP_DRD "s_waitcnt vmcnt(5)\n\t" "ds_read2st64_b64 v[32:35], %1 offset0:64 offset1:65\n\t" "ds_read2st64_b64 v[36:39], %1 offset0:66 offset1:67\n\t" "ds_read2st64_b64 v[40:43], %1 offset0:68 offset1:69\n\t" "ds_read2st64_b64 v[44:47], %1 offset0:70 offset1:71\n\t" "ds_read2st64_b64 v[48:51], %1 offset0:96 offset1:97\n\t" "ds_read2st64_b64 v[52:55], %1 offset0:98 offset1:99\n\t" "ds_read2st64_b64 v[56:59], %1 offset0:100 offset1:101\n\t" "ds_read2st64_b64 v[60:63], %1 offset0:102 offset1:103\n\t" "s_waitcnt lgkmcnt(0)"
#define ST_ASM "s_nop 1\n\t" "v_mfma_f32_16x16x32_bf16 v[80:83], a[176:179], v[160:163], 0\n\t" "v_mfma_f32_16x16x32_bf16 v[80:83], a[180:183], v[164:167], v[80:83]\n\t" "v_mfma_f32_16x16x32_bf16 v[80:83], a[184:187], v[168:171], v[80:83]\n\t" "v_mfma_f32_16x16x32_bf16 v[80:83], a[188:191], v[172:175], v[80:83]\n\t" "v_dot2_f32_bf16 v64, v160, v160, 0\n\t" "v_dot2_f32_bf16 v65, v164, v164, 0\n\t" "v_dot2_f32_bf16 v66, v168, v168, 0\n\t" "v_dot2_f32_bf16 v67, v172, v172, 0\n\t" "v_dot2_f32_bf16 v64, v161, v161, v64\n\t" "v_dot2_f32_bf16 v65, v165, v165, v65\n\t" "v_dot2_f32_bf16 v66, v169, v169, v66\n\t" "v_dot2_f32_bf16 v67, v173, v173, v67\n\t" "v_dot2_f32_bf16 v64, v162, v162, v64\n\t" "v_dot2_f32_bf16 v65, v166, v166, v65\n\t" "v_dot2_f32_bf16 v66, v170, v170, v66\n\t" "v_dot2_f32_bf16 v67, v174, v174, v67\n\t" "v_dot2_f32_bf16 v64, v163, v163, v64\n\t" "v_dot2_f32_bf16 v65, v167, v167, v65\n\t" "v_dot2_f32_bf16 v66, v171, v171, v66\n\t" "v_dot2_f32_bf16 v67, v175, v175, v67\n\t" "s_nop 2\n\t"
static_assert(W_MODB == 2228224 && W_VOTE == 8192);
#define AO_T0
static_assert(W_EPOCH == 2490368 && W_GKEY == 65536 && W_CANDH == 262144 && W_STAT == 524288 && W_YG == 1048576 && W_CANDX == 1966080);
#define MAIN_CLOB "v1", "v2", "v3", "v4", "v5", "v6", "v7", "v8", "v9", "v10", "v11", "v12", "v13", "v14", "v15", "v16", "v17", "v18", "v19", "v20", "v21", "v22", "v23", "v24", "v25", "v26", "v27", "v28", "v29", "v30", "v31", "v32", "v33", "v34", "v35", "v36", "v37", "v38", "v39", "v40", "v41", "v42", "v43", "v44", "v45", "v46", "v47", "v48", "v49", "v50", "v51", "v52", "v53", "v54", "v55", "v56", "v57", "v58", "v59", "v60", "v61", "v62", "v63", "v64", "v65", "v66", "v67", "v68", "v69", "v70", "v71", "v72", "v73", "v74", "v75", "v76", "v77", "v78", "v79", "v80", "v81", "v82", "v83", "v84", "v85", "v86", "v87", "v88", "v89", "v90", "v91", "v92", "v93", "v94", "v95", "v96", "v97", "v98", "v99", "v100", "v101", "v102", "v103", "v104", "v105", "v106", "v107", "v108", "v109", "v110", "v111", "v112", "v113", "v114", "v115", "v116", "v117", "v118", "v119", "v120", "v121", "v122", "v123", "v124", "v125", "v126", "v127", "v128", "v129", "v130", "v131", "v132", "v133", "v134", "v135", "v136", "v137", "v138", "v139", "v140", "v141", "v142", "v143", "v144", "v145", "v146", "v147", "v148", "v149", "v150", "v151", "v152", "v153", "v154", "v155", "v156", "v157", "v158", "v159", "v160", "v161", "v162", "v163", "v164", "v165", "v166", "v167", "v168", "v169", "v170", "v171", "v172", "v173", "v174", "v175", "v176", "v177", "v178", "v179", "v180", "v181", "v182", "v183", "v184", "v185", "v186", "v187", "v188", "v189", "v190", "v191", "v192", "v193", "v194", "v195", "v196", "v197", "v198", "v199", "v200", "v201", "v202", "v203", "v204", "v205", "v206", "v207", "v208", "v209", "v210", "v211", "v212", "v213", "v214", "v215", "v216", "v217", "v218", "v219", "v220", "v221", "v222", "v223", "v224", "v225", "v226", "v227", "v228", "v229", "v230", "v231", "v232", "v233", "v234", "v235", "v236", "v237", "v238", "v239", "v240", "v241", "v242", "v243", "v244", "v245", "v246", "v247", "v248", "v249", "v250", "v251", "v252", "v253", "v254", "s4", "s5", "s6", "s7", "s8", "s9", "s10", "s11", "s12", "s13", "s14", "s15", "s16", "s17", "s18", "s19", "s20", "s21", "s22", "s23", "s24", "s25", "s26", "s27", "s28", "s29", "s30", "s31", "s32", "s33", "s34", "s35", "s36", "s37", "s38", "s39", "s40", "s41", "s42", "s43", "s44", "s45", "s46", "s47", "s48", "s49", "s50", "s51", "s52", "s53", "s54", "s55", "s57", "s58", "s59", "s60", "s61", "s62", "s63", "s64", "s65", "s66", "s67", "s68", "s69", "s70", "s71", "s72", "s73", "s74", "s75", "s76", "s77", "s78", "s79", "s80", "s81", "s82", "s83", "s84", "s85", "s86", "s87", "s88", "s89", "s90", "s91", "s92", "s93", "s94", "s95", "s96", "s97", "s98", "s99", "vcc", "scc", "memory"
#define MAIN_STV
__global__ __launch_bounds__(256, 1) __attribute__((aligned(4096))) void k_fused(Args a) {
  Ctx k;
  STAMP_DECL;
  argp_t ap = (argp_t)__builtin_amdgcn_kernarg_segment_ptr();
  unsigned tid = threadIdx.x; k.bid = blockIdx.x;
  asm volatile(FQ_ASM AT_ASM : MAIN_STV "+{v0}"(tid), "+{s[0:1]}"(ap), "+{s2}"(k.bid), "=&{s3}"(k.wid), "=&{s56}"(k.t0) :: MAIN_CLOB, ACLOB_LO, ACLOB_HI);
  asm volatile(HOP_DEFS);
}

extern "C" void kernel_launch(void* const* d_in, const int* in_sizes, int n_in, void* d_out, int out_size, void* d_ws, size_t ws_size,
                              hipStream_t stream) {
  auto D = [](const void* p, unsigned bytes) { Desc d; d.p = p; d.bytes = bytes; d.w3 = SRD3; return d; };
  const size_t win = (size_t)(SKV - WIN) * DH * 2;
  Args a;
  a.x = D(d_in[0], S * DM * 2); a.w_mod = D(d_in[11], DM * 6 * DM * 2); a.c = D(d_in[10], DM * 2); a.ws = D(d_ws, W_END);
  a.wq = D(d_in[3], DM * QD * 2); a.wo = D(d_in[4], QD * DM * 2); a.wd = D(d_in[9], 0); a.w_r = D(d_in[6], DM * 2);
  a.kc = D((const char*)d_in[1] + win, WIN * DH * 2); a.vc = D((const char*)d_in[2] + win, WIN * DH * 2); a.wg = D(d_in[7], DM * FF * 2); a.wu = D(d_in[8], DM * FF * 2);
  a.out = D(d_out, S * DM * 2);
  (void)hipFuncSetAttribute((const void*)k_fused, hipFuncAttributeMaxDynamicSharedMemorySize, 163840);
  k_fused<<<256, 256, 163840, stream>>>(a);
}
```

```cpp
#include <hip/hip_runtime.h>
#include <hip/hip_bf16.h>
#include <stdint.h>
#include <utility>

typedef unsigned short u16;
using bf16x8 = __attribute__((ext_vector_type(8))) short;
using s16x4  = __attribute__((ext_vector_type(4))) short;
using f32x16 = __attribute__((ext_vector_type(16))) float;
using u32x16 = __attribute__((ext_vector_type(16))) unsigned;
using f32x4  = __attribute__((ext_vector_type(4))) float;
using f32x2  = __attribute__((ext_vector_type(2))) float;
using u32x4  = __attribute__((ext_vector_type(4))) unsigned;
using u32x2  = __attribute__((ext_vector_type(2))) unsigned;
using u32x8 = __attribute__((ext_vector_type(8))) unsigned;
using f32x8 = __attribute__((ext_vector_type(8))) float;
using f32x32 = __attribute__((ext_vector_type(32))) float;
using u32x32 = __attribute__((ext_vector_type(32))) unsigned;
typedef __bf16 bf16x2_t __attribute__((ext_vector_type(2)));
#define DI __device__ __forceinline__
#define NTMASK 0
#define NT_SEL(bit) ((NTMASK >> (bit)) & 1)
#if NT_SEL(0)
#define NTX " nt"
#else
#define NTX ""
#endif
#if NT_SEL(1)
#define NTM " nt"
#else
#define NTM ""
#endif
#if NT_SEL(2)
#define NTQ " nt"
#else
#define NTQ ""
#endif
#if NT_SEL(3)
#define NTO " nt"
#else
#define NTO ""
#endif
#if NT_SEL(4)
#define NTG " nt"
#else
#define NTG ""
#endif
#if NT_SEL(5)
#define NTD " nt"
#else
#define NTD ""
#endif
#if NT_SEL(6)
#define NTT " nt"
#else
#define NTT ""
#endif
#if NT_SEL(7)
#define NTF " nt"
#else
#define NTF ""
#endif

constexpr int S = 2048, DM = 1024, HQ = 32, HKV = 4, DH = 128, SKV = 2048, WIN = 512, FF = 3584;
constexpr int QD = HQ * DH;
constexpr float QSCALE = 0.12751743082f;

DI float bf2f(u16 v) { return __uint_as_float(((unsigned)v) << 16); }
DI u16 f2bf(float x) { unsigned u = __float_as_uint(x); u += 0x7fffu + ((u >> 16) & 1u); return (u16)(u >> 16); }
DI unsigned cvtpk(float lo, float hi) { f32x2 v = {lo, hi}; bf16x2_t b = __builtin_convertvector(v, bf16x2_t); return __builtin_bit_cast(unsigned, b); }
DI float lo16f(unsigned w) { return __uint_as_float(w << 16); }
DI float hi16f(unsigned w) { return __uint_as_float(w & 0xffff0000u); }
DI float rbf(float x) { return lo16f(cvtpk(x, x)); }
DI f32x2 up2(unsigned w) { return f32x2{lo16f(w), hi16f(w)}; }
DI float dot2(unsigned a, unsigned b, float c) { return __builtin_amdgcn_fdot2_f32_bf16(__builtin_bit_cast(bf16x2_t, a), __builtin_bit_cast(bf16x2_t, b), c, false); }
DI unsigned plo(unsigned a, unsigned b) { return __builtin_amdgcn_perm(b, a, 0x05040100u); }
DI unsigned phi(unsigned a, unsigned b) { return __builtin_amdgcn_perm(b, a, 0x07060302u); }
DI unsigned rfl(unsigned x) { return __builtin_amdgcn_readfirstlane(x); }
template <int CTRL> DI float dpp_f(float v) { return __int_as_float(__builtin_amdgcn_update_dpp(0, __float_as_int(v), CTRL, 0xf, 0xf, true)); }
template <int CTRL> DI unsigned dpp_u(unsigned v) { return (unsigned)__builtin_amdgcn_update_dpp(0, (int)v, CTRL, 0xf, 0xf, true); }
DI float fold16(float a, float b) { auto r = __builtin_amdgcn_permlane16_swap(__float_as_uint(a), __float_as_uint(b), false, false); return __uint_as_float(r[0]) + __uint_as_float(r[1]); }
DI float fold32(float a, float b) { auto r = __builtin_amdgcn_permlane32_swap(__float_as_uint(a), __float_as_uint(b), false, false); return __uint_as_float(r[0]) + __uint_as_float(r[1]); }
DI float sum16(float v) { return fold16(v, v); }
DI float sum32(float v) { return fold32(v, v); }
DI float sum_row16(float v) { v += dpp_f<0xB1>(v); v += dpp_f<0x4E>(v); v += dpp_f<0x141>(v); v += dpp_f<0x140>(v); return v; }
DI float wave_sum(float v) { return sum32(sum16(sum_row16(v))); }
DI unsigned wave_maxu(unsigned v) {
  v = max(v, dpp_u<0xB1>(v)); v = max(v, dpp_u<0x4E>(v)); v = max(v, dpp_u<0x141>(v)); v = max(v, dpp_u<0x140>(v));
  { auto r = __builtin_amdgcn_permlane16_swap(v, v, false, false); v = max(r[0], r[1]); }
  { auto r = __builtin_amdgcn_permlane32_swap(v, v, false, false); v = max(r[0], r[1]); }
  return v;
}
#define GAP() __builtin_amdgcn_sched_barrier(0)
template <int N> using iseq = std::make_integer_sequence<int, N>;
template <int... Is> using ilist = std::integer_sequence<int, Is...>;
typedef __amdgpu_buffer_rsrc_t srd_t;
#define SRD3 0x00020000
DI srd_t mk_srd(const void* p, unsigned bytes) { return __builtin_amdgcn_make_buffer_rsrc((void*)p, (short)0, (int)bytes, SRD3); }
DI unsigned lane_id() { unsigned l; asm volatile("v_mbcnt_lo_u32_b32 %0, -1, 0\n\tv_mbcnt_hi_u32_b32 %0, -1, %0" : "=v"(l)); return l; }
struct Desc { const void* p; unsigned bytes, w3; };
struct Args { Desc x, w_mod, c, ws, wq, wo, wd, w_r, kc, vc, wg, wu, out; };
typedef const __attribute__((address_space(4))) Args* argp_t;
DI argp_t args() { argp_t p = (argp_t)__builtin_amdgcn_kernarg_segment_ptr(); asm volatile("" : "+s"(p)); return p; }
#define BARRIER_LGKM() do { asm volatile("s_waitcnt lgkmcnt(0)\n\ts_barrier" ::: "memory"); } while (0)

constexpr unsigned W_EPOCH = 0x260000, W_TMO = 256;
constexpr unsigned W_FLAGH = 0x261000;
constexpr unsigned W_VOTE = 0x2000, W_PROBE = 0x4000;
constexpr unsigned W_GKEY = 0x10000;
constexpr unsigned W_FLAGR = 0x8000;
constexpr unsigned W_FLAGO = 0x20000;
constexpr unsigned W_CANDH = 0x40000;
constexpr unsigned W_STAT = 0x80000;
constexpr unsigned W_YG = 0x100000;
constexpr unsigned W_CANDX = 0x1E0000;
constexpr unsigned W_ST1 = 0x200000;
constexpr unsigned W_MODA = 0x210000;
constexpr unsigned W_MODB = 0x220000;
constexpr unsigned W_SIL = 0x240000;
constexpr unsigned W_HT = 4u << 20;
constexpr unsigned W_OT = 8u << 20;
constexpr unsigned W_XCH = 24u << 20;
constexpr unsigned W_END = 40u << 20;
constexpr unsigned TAGM = 0x40004000u, TAGC = 0x9E3739B9u;
#define SPINCAP 262144
constexpr unsigned kSpinCap = SPINCAP;
#define SL0 1
#define SL1 1
#define SL2 1
#define SL3 1
template <int SL = 1> DI bool spin_fail(unsigned& spins, srd_t ws, unsigned tag) {
  __builtin_amdgcn_s_sleep(SL);
  if (++spins > kSpinCap) { __builtin_amdgcn_raw_buffer_store_b32(tag, ws, W_TMO, 0, 16); return true; }
  return (spins & 255u) == 0u && rfl(__builtin_amdgcn_raw_buffer_load_b32(ws, W_TMO, 0, 16)) == tag;
}
DI u32x4 ld16(srd_t ws, unsigned off) { return __builtin_amdgcn_raw_buffer_load_b128(ws, off, 0, 16); }
DI u32x2 ld8(srd_t ws, unsigned off) { return __builtin_amdgcn_raw_buffer_load_b64(ws, off, 0, 16); }
DI void st16(srd_t ws, unsigned off, u32x4 v) { __builtin_amdgcn_raw_buffer_store_b128(v, ws, off, 0, 16); }
DI void st8(srd_t ws, unsigned off, u32x2 v) { __builtin_amdgcn_raw_buffer_store_b64(v, ws, off, 0, 16); }
DI void st4(srd_t ws, unsigned off, unsigned v) { __builtin_amdgcn_raw_buffer_store_b32(v, ws, off, 0, 16); }
DI void wait_flags(srd_t ws, unsigned off, unsigned tag) {
  const unsigned o = off + (lane_id() & 31u) * 4u;
  for (unsigned spins = 0;;) {
    const unsigned f = __builtin_amdgcn_raw_buffer_load_b32(ws, o, 0, 16);
    if (__all(f == tag)) break;
    if (spin_fail(spins, ws, tag)) break;
  }
  asm volatile("" ::: "memory");
}
template <int OFF, bool DEV = false> DI u32x4 ald16(srd_t s, unsigned vo, unsigned so) {
  u32x4 r;
  if constexpr (DEV) asm volatile("buffer_load_dwordx4 %0, %1, %2, %3 offen offset:%4 sc1" : "=v"(r) : "v"(vo), "s"(s), "s"(so), "i"(OFF) : "memory");
  else               asm volatile("buffer_load_dwordx4 %0, %1, %2, %3 offen offset:%4" NTF : "=v"(r) : "v"(vo), "s"(s), "s"(so), "i"(OFF) : "memory");
  return r;
}
template <int OFF> DI u32x2 ald8(srd_t s, unsigned vo, unsigned so) {
  u32x2 r; asm volatile("buffer_load_dwordx2 %0, %1, %2, %3 offen offset:%4" : "=v"(r) : "v"(vo), "s"(s), "s"(so), "i"(OFF) : "memory"); return r;
}
DI u32x2 ald8d(srd_t s, unsigned vo) { u32x2 r; asm volatile("buffer_load_dwordx2 %0, %1, %2, 0 offen sc1" : "=v"(r) : "v"(vo), "s"(s) : "memory"); return r; }
template <int OFF, bool DEV = false> DI unsigned ald4(srd_t s, unsigned vo, unsigned so) {
  unsigned r;
  if constexpr (DEV) asm volatile("buffer_load_dword %0, %1, %2, %3 offen offset:%4 sc1" : "=v"(r) : "v"(vo), "s"(s), "s"(so), "i"(OFF) : "memory");
  else               asm volatile("buffer_load_dword %0, %1, %2, %3 offen offset:%4" : "=v"(r) : "v"(vo), "s"(s), "s"(so), "i"(OFF) : "memory");
  return r;
}
DI void touch(unsigned& d, const void* base, unsigned vo) { asm volatile("global_load_dword %0, %1, %2" : "+v"(d) : "v"(vo), "s"(base) : "memory"); }
DI void touchq(unsigned& d, const void* base, unsigned vo) { asm volatile("global_load_dword %0, %1, %2" NTQ : "+v"(d) : "v"(vo), "s"(base) : "memory"); }
template <class T> DI T lds_ld(unsigned a) { return *reinterpret_cast<const __attribute__((address_space(3))) T*>(a); }
template <class T> DI void lds_st(unsigned a, T v) { *reinterpret_cast<__attribute__((address_space(3))) T*>(a) = v; }
DI float silu_bf(float v) { return rbf(v * __builtin_amdgcn_rcpf(1.f + __expf(-v))); }
struct Ctx { static constexpr unsigned ldsb = 0; unsigned t0, pm, bid, wid, xcd, j, kvh, hl, rblk, tag, cs, nb; srd_t ws; };
#define TAIL2 1
DI unsigned ffn_j(const Ctx& k) { return TAIL2 ? (k.j < 24 ? k.j : 24 + (k.j & 3)) : min(k.j, 27u); }
DI bool ffn_idle(const Ctx& k) { return TAIL2 ? (k.j >> 2) == 6 + (k.xcd & 1) : k.j >= 28; }
constexpr unsigned UNIT = 16384;
#define STAMPA(I) do { } while (0)
#define STAMPO(I) do { } while (0)
#define STAMP_DECL
#define STAMP(I) do { } while (0)
#define STAMP_FLUSH() do { } while (0)
#define STAMP_ARG
#define STAMP_PASS
#define ACLOB_LO "a0","a1","a2","a3","a4","a5","a6","a7","a8","a9","a10","a11","a12","a13","a14","a15","a16","a17","a18","a19","a20","a21","a22","a23","a24","a25","a26","a27","a28","a29","a30","a31","a32","a33","a34","a35","a36","a37","a38","a39","a40","a41","a42","a43","a44","a45","a46","a47","a48","a49","a50","a51","a52","a53","a54","a55","a56","a57","a58","a59","a60","a61","a62","a63","a64","a65","a66","a67","a68","a69","a70","a71","a72","a73","a74","a75","a76","a77","a78","a79","a80","a81","a82","a83","a84","a85","a86","a87","a88","a89","a90","a91","a92","a93","a94","a95","a96","a97","a98","a99","a100","a101","a102","a103","a104","a105","a106","a107","a108","a109","a110","a111","a112","a113","a114","a115","a116","a117","a118","a119","a120","a121","a122","a123","a124","a125","a126","a127"
#define ACLOB_HI "a128","a129","a130","a131","a132","a133","a134","a135","a136","a137","a138","a139","a140","a141","a142","a143","a144","a145","a146","a147","a148","a149","a150","a151","a152","a153","a154","a155","a156","a157","a158","a159","a160","a161","a162","a163","a164","a165","a166","a167","a168","a169","a170","a171","a172","a173","a174","a175","a176","a177","a178","a179","a180","a181","a182","a183","a184","a185","a186","a187","a188","a189","a190","a191","a192","a193","a194","a195","a196","a197","a198","a199","a200","a201","a202","a203","a204","a205","a206","a207","a208","a209","a210","a211","a212","a213","a214","a215","a216","a217","a218","a219","a220","a221","a222","a223","a224","a225","a226","a227","a228","a229","a230","a231","a232","a233","a234","a235","a236","a237","a238","a239","a240","a241","a242","a243","a244","a245","a246","a247","a248","a249","a250","a251","a252","a253","a254","a255"
#define HOP_DEFS ".set .Lhc00, .Lhs0\n\t.set .Lhs0n, .Lhs4\n\t.set .Lhs4n, .Lhs8\n\t.set .Lhs8n, .Lhr0\n\t.set .Lhc01, .Lhs1\n\t.set .Lhs1n, .Lhs5\n\t.set .Lhs5n, .Lhs9\n\t.set .Lhs9n, .Lhr0\n\t.set .Lhc02, .Lhs2\n\t.set .Lhs2n, .Lhs6\n\t.set .Lhs6n, .Lhs10\n\t.set .Lhs10n, .Lhr0\n\t.set .Lhc03, .Lhs3\n\t.set .Lhs3n, .Lhs7\n\t.set .Lhs7n, .Lhr0\n\t.set .Lhc10, .Lhs11\n\t.set .Lhs11n, .Lhs15\n\t.set .Lhs15n, .Lhr1\n\t.set .Lhc11, .Lhs12\n\t.set .Lhs12n, .Lhs16\n\t.set .Lhs16n, .Lhr1\n\t.set .Lhc12, .Lhs13\n\t.set .Lhs13n, .Lhr1\n\t.set .Lhc13, .Lhs14\n\t.set .Lhs14n, .Lhr1"
#define HOP_PAGE(n) asm volatile("s_branch .Lht" #n "\n\t.p2align 12\n.Lht" #n ":\n\ts_cbranch_execz .Lht" #n "n")
#define PAGE_ALIGN() asm volatile("s_branch .Lpa%=\n\t.p2align 12\n.Lpa%=:" ::: "memory")
#define HOP_FRONT() asm volatile("s_branch .Lhf\n\t.p2align 12\n.Lhf:\n\ts_cbranch_execz .Lhfn")
#define HOP_GO(g, w) asm volatile("s_mov_b64 exec, 0\n\ts_cmp_eq_u32 %0, 0\n\ts_cbranch_scc1 .Lhc" #g "0\n\ts_cmp_eq_u32 %0, 1\n\ts_cbranch_scc1 .Lhc" #g "1\n\ts_cmp_eq_u32 %0, 2\n\ts_cbranch_scc1 .Lhc" #g "2\n\ts_branch .Lhc" #g "3\n.Lhr" #g ":\n\ts_mov_b64 exec, -1" :: "s"(w) : "scc", "memory")
#define FQ_STR2(x) #x
#define FQ_STR(x) FQ_STR2(x)
#define FQ_CAP FQ_STR(SPINCAP)
DI bool tags2(const u32x4& a, unsigned tag) { return (a[1] == tag) & (a[3] == tag); }
DI void bcast4(float v, float (&o)[4]) {
  auto a = __builtin_amdgcn_permlane32_swap(__float_as_uint(v), __float_as_uint(v), false, false);
  auto b = __builtin_amdgcn_permlane16_swap(a[0], a[0], false, false);
  auto c = __builtin_amdgcn_permlane16_swap(a[1], a[1], false, false);
  o[0] = __uint_as_float(b[0]); o[1] = __uint_as_float(b[1]); o[2] = __uint_as_float(c[0]); o[3] = __uint_as_float(c[1]);
}
#define FSTP(x)
#define FSTS(x)
#define FSTA(x)
#define FQ_ASM \
  FSTA("v_mov_b32 v255, 0\n\t") \
  FSTA("s_memrealtime s[94:95]\n\t") \
  FSTA("s_memtime s[92:93]\n\t") \
  FSTA("s_waitcnt lgkmcnt(0)\n\t") \
  FSTA("s_or_b32 s94, s94, 1\n\t") \
  FSTA("v_writelane_b32 v255, s94, 0\n\t") \
  FSTA("v_writelane_b32 v255, s92, 1\n\t") \
  "s_mov_b32 s37, 1\n\t" \
  ".Lfr_again:\n\t" \
  "s_load_dwordx16 s[4:19], s[0:1], 0x0\n\t" \
  "s_load_dwordx16 s[20:35], s[0:1], 0x40\n\t" \
  "s_load_dwordx16 s[60:75], s[0:1], 0x80\n\t" \
  "v_readfirstlane_b32 s3, v0\n\t" \
  "v_and_b32 v254, 63, v0\n\t" \
  "s_lshr_b32 s3, s3, 6\n\t" \
  "s_lshr_b32 s39, s2, 3\n\t" \
  "s_lshl_b32 s54, s3, 5\n\t" \
  "s_add_u32 s54, s54, s39\n\t" \
  "s_lshl_b32 s54, s54, 7\n\t" \
  "v_lshl_add_u32 v48, v254, 14, s54\n\t" \
  "v_min_u32 v48, .Lfz_z-.Lfz_a-4, v48\n\t" \
  "s_getpc_b64 s[96:97]\n\t" \
  ".Lfz_a:\n\t" \
  "global_load_dword v246, v48, s[96:97]\n\t" \
  "s_and_b32 s40, s2, 3\n\t" \
  "v_lshrrev_b32 v52, 2, v0\n\t" \
  "v_lshlrev_b32 v244, 4, v254\n\t" \
  "v_lshlrev_b32 v53, 3, v52\n\t" \
  "v_mul_u32_u24 v52, 49152, v52\n\t" \
  "v_and_or_b32 v52, v244, 48, v52\n\t" \
  "s_lshl_b32 s44, s40, 9\n\t" \
  "s_mul_i32 s45, s40, 3145728\n\t" \
  "s_bfe_u32 s52, s2, 0x10002\n\t" \
  "s_lshl_b32 s52, s52, 5\n\t" \
  "s_add_u32 s52, s52, s39\n\t" \
  "s_lshl_b32 s54, s52, 6\n\t" \
  "s_add_u32 s45, s45, s54\n\t" \
  "s_add_u32 s96, s45, 12288\n\t" \
  "s_add_u32 s97, s45, 24576\n\t" \
  "s_add_u32 s99, s45, 36864\n\t" \
  "v_mov_b32 v1, 2490368\n\t" \
  "s_and_b32 s98, s2, 4\n\t" \
  "s_lshr_b32 s54, s2, 6\n\t" \
  "s_or_b32 s98, s98, s54\n\t" \
  "s_lshl_b32 s41, s98, 19\n\t" \
  "s_lshl_b32 s38, s3, 12\n\t" \
  "s_lshl_b32 s77, s3, 13\n\t" \
  "s_and_b32 s54, s2, 0x38\n\t" \
  "s_lshl_b32 s54, s54, 11\n\t" \
  "s_lshl_b32 s55, s40, 17\n\t" \
  "s_or_b32 s46, s41, s38\n\t" \
  "s_or_b32 s46, s46, s54\n\t" \
  "s_or_b32 s46, s46, s55\n\t" \
  "s_xor_b32 s87, s55, 393216\n\t" \
  "v_lshrrev_b32 v245, 4, v254\n\t" \
  "v_lshrrev_b32 v49, 3, v254\n\t" \
  "v_and_b32 v50, 7, v254\n\t" \
  "v_xor_b32 v51, v50, v245\n\t" \
  "v_lshlrev_b32 v51, 4, v51\n\t" \
  "v_lshl_add_u32 v212, v49, 11, v51\n\t" \
  "v_xor_b32 v213, 64, v212\n\t" \
  "v_add_u32 v213, 15360, v213\n\t" \
  "s_bfe_u32 s42, s2, 0x20004\n\t" \
  "s_lshl_b32 s54, s3, 15\n\t" \
  "s_or_b32 s47, s41, s54\n\t" \
  "s_or_b32 s47, s47, s44\n\t" \
  "s_lshl_b32 s54, s42, 7\n\t" \
  "s_or_b32 s47, s47, s54\n\t" \
  "s_and_b32 s43, s2, 8\n\t" \
  "s_lshl_b32 s54, s43, 15\n\t" \
  "s_or_b32 s57, s47, s87\n\t" \
  "s_xor_b32 s57, s57, s54\n\t" \
  "s_xor_b32 s58, s57, 131072\n\t" \
  "s_lshl_b32 s54, s43, 9\n\t" \
  "s_add_u32 s90, s77, s54\n\t" \
  "s_lshl_b32 s78, s40, 21\n\t" \
  "s_lshl_b32 s50, s39, 14\n\t" \
  "s_lshl2_add_u32 s50, s44, s50\n\t" \
  "s_mul_i32 s54, s3, 0x1080\n\t" \
  "s_and_b32 s54, s54, 0x2080\n\t" \
  "s_or_b32 s50, s50, s54\n\t" \
  "v_lshl_add_u32 v54, v50, 8, s50\n\t" \
  "v_and_b32 v51, 3, v49\n\t" \
  "v_lshl_add_u32 v51, v51, 19, v54\n\t" \
  "s_mov_b32 m0, s90\n\t" \
  "s_waitcnt lgkmcnt(0)\n\t" \
  "buffer_load_dwordx2 v[2:3], v53, s[12:15], s44 offen\n\t" \
  "buffer_load_dwordx4 v[4:7], v52, s[8:11], s45 offen nt\n\t" \
  "buffer_load_dwordx4 v[8:11], v52, s[8:11], s96 offen nt\n\t" \
  "buffer_load_dwordx4 v[12:15], v52, s[8:11], s97 offen nt\n\t" \
  "buffer_load_dwordx4 v[16:19], v52, s[8:11], s99 offen nt\n\t" \
  "s_add_u32 s16, s16, 0x1fffff\n\t" \
  "s_addc_u32 s17, s17, 0\n\t" \
  "s_and_b32 s16, s16, 0xffe00000\n\t" \
  "buffer_load_dword v1, v1, s[16:19], 0 offen sc1\n\t" \
  "s_mov_b32 s96, s46\n\t" \
  "v_mov_b32 v253, v0\n\t" \
  "v_and_b32 v247, 15, v254\n\t" \
  "s_bfe_u32 s43, s2, 0x10003\n\t" \
  "v_and_b32 v48, 3, v254\n\t" \
  "v_lshl_add_u32 v48, v48, 2, v245\n\t" \
  "s_lshl_b32 s54, s3, 8\n\t" \
  "s_add_u32 s55, s54, 147456\n\t" \
  "v_lshl_add_u32 v55, v48, 3, s55\n\t" \
  "v_lshlrev_b32 v56, 3, v247\n\t" \
  "v_add_u32 v56, 147456, v56\n\t" \
  "v_and_b32 v48, 0xf0, v0\n\t" \
  "v_lshl_or_b32 v57, v48, 24, v244\n\t" \
  "s_lshl_b32 s52, s52, 8\n\t" \
  "s_lshl_b32 s55, s40, 14\n\t" \
  "s_add_u32 s52, s52, s55\n\t" \
  "s_add_u32 s52, s52, 2162688\n\t" \
  "v_and_b32 v210, 31, v254\n\t" \
  "v_lshrrev_b32 v48, 5, v254\n\t" \
  "v_lshlrev_b32 v58, 4, v48\n\t" \
  "v_lshl_or_b32 v58, v210, 26, v58\n\t" \
  "s_lshr_b32 s46, s46, 7\n\t" \
  "s_add_u32 s46, s46, 2097152\n\t" \
  "v_cvt_f32_u32 v251, v48\n\t" \
  "v_lshlrev_b32 v248, 4, v210\n\t" \
  "v_lshl_or_b32 v248, v48, 13, v248\n\t" \
  "v_lshlrev_b32 v210, 2, v210\n\t" \
  "v_lshlrev_b32 v249, 4, v247\n\t" \
  "s_lshl_b32 s55, s3, 17\n\t" \
  "s_lshl_b32 s49, s43, 18\n\t" \
  "s_xor_b32 s55, s55, s49\n\t" \
  "buffer_load_dwordx4 v[32:35], v244, s[4:7], s96 offen offset:0\n\t" \
  "buffer_load_dwordx4 v[36:39], v244, s[4:7], s96 offen offset:1024\n\t" \
  "buffer_load_dwordx4 v[40:43], v244, s[4:7], s96 offen offset:2048\n\t" \
  "buffer_load_dwordx4 v[44:47], v244, s[4:7], s96 offen offset:3072\n\t" \
  "s_and_b32 s49, s2, 0x38\n\t" \
  "s_lshl2_add_u32 s55, s44, s55\n\t" \
  "s_lshl_b32 s49, s49, 5\n\t" \
  "s_add_u32 s55, s55, s49\n\t" \
  "v_lshlrev_b32 v48, 5, v245\n\t" \
  "v_xor_b32 v48, v48, v249\n\t" \
  "v_lshl_add_u32 v48, v245, 13, v48\n\t" \
  "v_add_u32 v200, s55, v48\n\t" \
  "v_xor_b32 v48, 0x80, v200\n\t" \
  "v_add_u32 v201, 31744, v200\n\t" \
  "v_add_u32 v202, 63488, v48\n\t" \
  "v_add_u32 v203, 95232, v48\n\t" \
  "s_add_u32 s76, s38, 32768\n\t" \
  "s_lshl_b32 s49, s42, 19\n\t" \
  "s_or_b32 s49, s49, s78\n\t" \
  "s_xor_b32 s80, s49, 1048576\n\t" \
  "s_add_u32 s55, s42, 1\n\t" \
  "s_and_b32 s55, s55, 3\n\t" \
  "s_xor_b32 s55, s55, s42\n\t" \
  "s_lshl_b32 s79, s55, 19\n\t" \
  "s_xor_b32 s79, s79, s49\n\t" \
  "s_xor_b32 s81, s79, 1048576\n\t" \
  "s_lshl_b32 s48, s55, 7\n\t" \
  "s_xor_b32 s48, s48, s47\n\t" \
  "s_xor_b32 s88, s47, 256\n\t" \
  "s_xor_b32 s89, s48, 256\n\t" \
  "v_lshl_add_u32 v48, s43, 2, v245\n\t" \
  "v_bfe_u32 v50, v254, 1, 3\n\t" \
  "v_xor_b32 v48, v48, v50\n\t" \
  "v_lshlrev_b32 v48, 4, v48\n\t" \
  "v_lshl_add_u32 v48, v247, 7, v48\n\t" \
  "v_add_u32 v204, s77, v48\n\t" \
  "v_xor_b32 v205, 64, v204\n\t" \
  "v_add_u32 v206, 98304, v204\n\t" \
  "v_add_u32 v207, 98304, v205\n\t" \
  "v_lshlrev_b32 v208, 5, v245\n\t" \
  "v_add_u32 v208, 148480, v208\n\t" \
  "v_lshlrev_b32 v50, 3, v254\n\t" \
  "s_lshl_b32 s55, s43, 7\n\t" \
  "s_or_b32 s55, s55, 256\n\t" \
  "v_xor_b32 v250, s55, v50\n\t" \
  "s_lshl1_add_u32 s55, s54, 148480\n\t" \
  "v_add_u32 v250, s55, v250\n\t" \
  "v_lshl_add_u32 v214, v245, 10, v249\n\t" \
  "v_lshl_add_u32 v214, s43, 12, v214\n\t" \
  "s_lshr_b32 s55, s2, 2\n\t" \
  "s_xor_b32 s55, s55, s3\n\t" \
  "s_and_b32 s55, s55, 1\n\t" \
  "s_lshl1_add_u32 s55, s3, s55\n\t" \
  "s_lshl_b32 s82, s55, 13\n\t" \
  "s_lshr_b32 s55, s2, 6\n\t" \
  "s_lshl_b32 s55, s55, 20\n\t" \
  "s_add_u32 s82, s82, s55\n\t" \
  "s_add_u32 s82, s82, 4194304\n\t" \
  "s_lshr_b32 s86, s49, 3\n\t" \
  "s_add_u32 s86, s86, s82\n\t" \
  "s_lshr_b32 s55, s87, 9\n\t" \
  "s_add_u32 s86, s86, s55\n\t" \
  "s_lshl2_add_u32 s84, s98, s3\n\t" \
  "s_lshl_b32 s84, s84, 7\n\t" \
  "s_add_u32 s84, s84, 2494464\n\t" \
  "s_bfe_u32 s55, s2, 0x30003\n\t" \
  "s_lshl3_add_u32 s55, s40, s55\n\t" \
  "s_lshl2_add_u32 s85, s55, s84\n\t" \
  "s_lshl_b32 s55, s43, 13\n\t" \
  "s_sub_u32 s83, 4096, s55\n\t" \
  "v_and_b32 v50, 0xf8, v50\n\t" \
  "v_bfe_u32 v48, v254, 2, 2\n\t" \
  "v_lshl_add_u32 v50, v48, 8, v50\n\t" \
  "v_lshl_add_u32 v50, v245, 11, v50\n\t" \
  "v_add_u32 v184, 32768, v50\n\t" \
  "v_xor_b32 v185, 32, v184\n\t" \
  "v_xor_b32 v186, 64, v184\n\t" \
  "v_xor_b32 v187, 96, v184\n\t" \
  "v_xor_b32 v188, 128, v184\n\t" \
  "v_xor_b32 v189, 160, v184\n\t" \
  "v_xor_b32 v190, 192, v184\n\t" \
  "v_xor_b32 v191, 224, v184\n\t" \
  "s_lshl_b32 s51, s98, 12\n\t" \
  "s_add_u32 s51, s51, s54\n\t" \
  "s_add_u32 s51, s51, 2097152\n\t" \
  "s_lshr_b32 s55, s87, 7\n\t" \
  "s_add_u32 s51, s51, s55\n\t" \
  "s_add_u32 s55, s42, s3\n\t" \
  "s_and_b32 s55, s55, 3\n\t" \
  "s_lshl2_add_u32 s55, s40, s55\n\t" \
  "s_lshl_b32 s55, s55, 9\n\t" \
  "s_add_u32 s53, s55, 2162688\n\t" \
  "v_min_u32 v49, 2, v49\n\t" \
  "s_bfe_u32 s55, s2, 0x10002\n\t" \
  "s_lshl2_add_u32 s55, s40, s55\n\t" \
  "s_add_u32 s55, s55, 4\n\t" \
  "v_lshl_add_u32 v49, v49, 1, s55\n\t" \
  "v_lshl_add_u32 v215, v49, 19, v54\n\t" \
  "v_add_u32 v216, 3145728, v215\n\t" \
  "v_and_b32 v215, 0x7fffff, v215\n\t" \
  "v_and_b32 v216, 0x7fffff, v216\n\t" \
  "s_mov_b32 s98, 0xffff0000\n\t" \
  "s_mov_b32 s45, 0x05040100\n\t" \
  "s_mov_b32 s50, 0x07060302\n\t" \
  "s_lshr_b32 s44, s2, 6\n\t" \
  "s_lshl_b32 s44, s44, 9\n\t" \
  "v_lshlrev_b32 v53, 1, v0\n\t" \
  "s_waitcnt vmcnt(9)\n\t" \
  "v_lshlrev_b32 v20, 16, v2\n\t" \
  "v_and_b32 v21, s98, v2\n\t" \
  "v_lshlrev_b32 v22, 16, v3\n\t" \
  "v_and_b32 v23, s98, v3\n\t" \
  "v_mul_f32 v24, 0xbfb8aa3b, v20\n\t" \
  "v_mul_f32 v25, 0xbfb8aa3b, v21\n\t" \
  "v_mul_f32 v26, 0xbfb8aa3b, v22\n\t" \
  "v_mul_f32 v27, 0xbfb8aa3b, v23\n\t" \
  "v_exp_f32 v24, v24\n\t" \
  "v_exp_f32 v25, v25\n\t" \
  "v_exp_f32 v26, v26\n\t" \
  "v_exp_f32 v27, v27\n\t" \
  "v_add_f32 v24, 1.0, v24\n\t" \
  "v_add_f32 v25, 1.0, v25\n\t" \
  "v_add_f32 v26, 1.0, v26\n\t" \
  "v_add_f32 v27, 1.0, v27\n\t" \
  "v_rcp_f32 v24, v24\n\t" \
  "v_rcp_f32 v25, v25\n\t" \
  "v_rcp_f32 v26, v26\n\t" \
  "v_rcp_f32 v27, v27\n\t" \
  "v_mul_f32 v20, v24, v20\n\t" \
  "v_mul_f32 v21, v25, v21\n\t" \
  "v_mul_f32 v22, v26, v22\n\t" \
  "v_mul_f32 v23, v27, v23\n\t" \
  "v_cvt_pk_bf16_f32 v2, v20, v21\n\t" \
  "v_cvt_pk_bf16_f32 v3, v22, v23\n\t" \
  FSTP("s_memrealtime s[94:95]\n\t") \
  FSTP("s_memtime s[92:93]\n\t") \
  FSTP("s_waitcnt lgkmcnt(0)\n\t") \
  FSTP("s_or_b32 s94, s94, 1\n\t") \
  FSTP("v_writelane_b32 v255, s94, 54\n\t") \
  FSTP("v_writelane_b32 v255, s92, 55\n\t") \
  "s_waitcnt vmcnt(5)\n\t" \
  FSTA("s_memrealtime s[94:95]\n\t") \
  FSTA("s_memtime s[92:93]\n\t") \
  FSTA("s_waitcnt lgkmcnt(0)\n\t") \
  FSTA("s_or_b32 s94, s94, 1\n\t") \
  FSTA("v_writelane_b32 v255, s94, 2\n\t") \
  FSTA("v_writelane_b32 v255, s92, 3\n\t") \
  "v_perm_b32 v28, v8, v4, s45\n\t" \
  "v_perm_b32 v29, v9, v5, s45\n\t" \
  "v_perm_b32 v30, v10, v6, s45\n\t" \
  "v_perm_b32 v31, v11, v7, s45\n\t" \
  "v_perm_b32 v4, v8, v4, s50\n\t" \
  "v_perm_b32 v5, v9, v5, s50\n\t" \
  "v_perm_b32 v6, v10, v6, s50\n\t" \
  "v_perm_b32 v7, v11, v7, s50\n\t" \
  "v_dot2_f32_bf16 v20, v28, v2, 0\n\t" \
  "v_dot2_f32_bf16 v21, v29, v2, 0\n\t" \
  "v_dot2_f32_bf16 v22, v30, v2, 0\n\t" \
  "v_dot2_f32_bf16 v23, v31, v2, 0\n\t" \
  "v_dot2_f32_bf16 v24, v4, v2, 0\n\t" \
  "v_dot2_f32_bf16 v25, v5, v2, 0\n\t" \
  "v_dot2_f32_bf16 v26, v6, v2, 0\n\t" \
  "v_dot2_f32_bf16 v27, v7, v2, 0\n\t" \
  "v_perm_b32 v28, v16, v12, s45\n\t" \
  "v_perm_b32 v29, v17, v13, s45\n\t" \
  "v_perm_b32 v30, v18, v14, s45\n\t" \
  "v_perm_b32 v31, v19, v15, s45\n\t" \
  "v_perm_b32 v12, v16, v12, s50\n\t" \
  "v_perm_b32 v13, v17, v13, s50\n\t" \
  "v_perm_b32 v14, v18, v14, s50\n\t" \
  "v_perm_b32 v15, v19, v15, s50\n\t" \
  "v_dot2_f32_bf16 v20, v28, v3, v20\n\t" \
  "v_dot2_f32_bf16 v21, v29, v3, v21\n\t" \
  "v_dot2_f32_bf16 v22, v30, v3, v22\n\t" \
  "v_dot2_f32_bf16 v23, v31, v3, v23\n\t" \
  "v_dot2_f32_bf16 v24, v12, v3, v24\n\t" \
  "v_dot2_f32_bf16 v25, v13, v3, v25\n\t" \
  "v_dot2_f32_bf16 v26, v14, v3, v26\n\t" \
  "v_dot2_f32_bf16 v27, v15, v3, v27\n\t" \
  "v_permlane32_swap_b32 v20, v22\n\t" \
  "v_permlane32_swap_b32 v21, v23\n\t" \
  "v_permlane32_swap_b32 v24, v26\n\t" \
  "v_permlane32_swap_b32 v25, v27\n\t" \
  "v_add_f32 v20, v20, v22\n\t" \
  "v_add_f32 v21, v21, v23\n\t" \
  "v_add_f32 v24, v24, v26\n\t" \
  "v_add_f32 v25, v25, v27\n\t" \
  "v_permlane16_swap_b32 v20, v21\n\t" \
  "s_nop 0\n\t" \
  "v_permlane16_swap_b32 v24, v25\n\t" \
  "v_add_f32 v20, v20, v21\n\t" \
  "v_add_f32 v21, v24, v25\n\t" \
  "s_nop 0\n\t" \
  "v_add_f32_dpp v20, v20, v20 row_ror:8 row_mask:0xf bank_mask:0xf\n\t" \
  "v_add_f32_dpp v21, v21, v21 row_ror:8 row_mask:0xf bank_mask:0xf\n\t" \
  "s_nop 0\n\t" \
  "v_add_f32_dpp v20, v20, v20 row_ror:4 row_mask:0xf bank_mask:0xf\n\t" \
  "v_add_f32_dpp v21, v21, v21 row_ror:4 row_mask:0xf bank_mask:0xf\n\t" \
  "ds_write_b64 v55, v[20:21]\n\t" \
  "buffer_load_dwordx4 v212, s[4:7], s57 offen lds\n\t" \
  "buffer_load_dwordx4 v213, s[4:7], s57 offen offset:1024 lds\n\t" \
  "s_add_u32 m0, s90, 2048\n\t" \
  "s_nop 0\n\t" \
  "buffer_load_dwordx4 v212, s[4:7], s58 offen lds\n\t" \
  "buffer_load_dwordx4 v213, s[4:7], s58 offen offset:1024 lds\n\t" \
  "s_xor_b32 m0, s90, 4096\n\t" \
  "s_xor_b32 s97, s57, 262144\n\t" \
  "buffer_load_dwordx4 v212, s[4:7], s97 offen lds\n\t" \
  "buffer_load_dwordx4 v213, s[4:7], s97 offen offset:1024 lds\n\t" \
  "s_xor_b32 m0, s90, 6144\n\t" \
  "s_xor_b32 s97, s57, 393216\n\t" \
  "buffer_load_dwordx4 v212, s[4:7], s97 offen lds\n\t" \
  "buffer_load_dwordx4 v213, s[4:7], s97 offen offset:1024 lds\n\t" \
  "v_mov_b32 v4, 0\n\t" \
  "v_mov_b32 v5, 0\n\t" \
  "v_mov_b32 v6, 0\n\t" \
  "v_mov_b32 v7, 0\n\t" \
  "s_waitcnt vmcnt(12) lgkmcnt(0)\n\t" \
  "s_barrier\n\t" \
  "ds_read_b64 v[8:9], v56 offset:0\n\t" \
  "ds_read_b64 v[10:11], v56 offset:256\n\t" \
  "ds_read_b64 v[12:13], v56 offset:512\n\t" \
  "ds_read_b64 v[14:15], v56 offset:768\n\t" \
  "v_readfirstlane_b32 s36, v1\n\t" \
  "s_or_b32 s36, s36, 0x40004000\n\t" \
  "s_add_u32 s36, s36, 0x9e3739b9\n\t" \
  "s_and_b32 s36, s36, 0xbfffbfff\n\t" \
  "v_mov_b32 v211, s36\n\t" \
  "v_mov_b32 v17, s36\n\t" \
  "v_mov_b32 v19, s36\n\t" \
  "s_waitcnt lgkmcnt(0)\n\t" \
  "v_add_f32 v8, v8, v10\n\t" \
  "v_add_f32 v12, v12, v14\n\t" \
  "v_add_f32 v9, v9, v11\n\t" \
  "v_add_f32 v13, v13, v15\n\t" \
  "v_add_f32 v16, v8, v12\n\t" \
  "v_add_f32 v18, v9, v13\n\t" \
  "buffer_store_dwordx4 v[16:19], v57, s[16:19], s52 offen sc1\n\t" \
  "s_waitcnt vmcnt(9)\n\t" \
  FSTA("s_memrealtime s[94:95]\n\t") \
  FSTA("s_memtime s[92:93]\n\t") \
  FSTA("s_waitcnt lgkmcnt(0)\n\t") \
  FSTA("s_or_b32 s94, s94, 1\n\t") \
  FSTA("v_writelane_b32 v255, s94, 4\n\t") \
  FSTA("v_writelane_b32 v255, s92, 5\n\t") \
  "v_dot2c_f32_bf16 v4, 0x3f803f80, v32\n\t" \
  "v_dot2c_f32_bf16 v6, v32, v32\n\t" \
  "v_dot2c_f32_bf16 v5, 0x3f803f80, v40\n\t" \
  "v_dot2c_f32_bf16 v7, v40, v40\n\t" \
  "v_dot2c_f32_bf16 v4, 0x3f803f80, v33\n\t" \
  "v_dot2c_f32_bf16 v6, v33, v33\n\t" \
  "v_dot2c_f32_bf16 v5, 0x3f803f80, v41\n\t" \
  "v_dot2c_f32_bf16 v7, v41, v41\n\t" \
  "v_dot2c_f32_bf16 v4, 0x3f803f80, v34\n\t" \
  "v_dot2c_f32_bf16 v6, v34, v34\n\t" \
  "v_dot2c_f32_bf16 v5, 0x3f803f80, v42\n\t" \
  "v_dot2c_f32_bf16 v7, v42, v42\n\t" \
  "v_dot2c_f32_bf16 v4, 0x3f803f80, v35\n\t" \
  "v_dot2c_f32_bf16 v6, v35, v35\n\t" \
  "v_dot2c_f32_bf16 v5, 0x3f803f80, v43\n\t" \
  "v_dot2c_f32_bf16 v7, v43, v43\n\t" \
  "v_dot2c_f32_bf16 v4, 0x3f803f80, v36\n\t" \
  "v_dot2c_f32_bf16 v6, v36, v36\n\t" \
  "v_dot2c_f32_bf16 v5, 0x3f803f80, v44\n\t" \
  "v_dot2c_f32_bf16 v7, v44, v44\n\t" \
  "v_dot2c_f32_bf16 v4, 0x3f803f80, v37\n\t" \
  "v_dot2c_f32_bf16 v6, v37, v37\n\t" \
  "v_dot2c_f32_bf16 v5, 0x3f803f80, v45\n\t" \
  "v_dot2c_f32_bf16 v7, v45, v45\n\t" \
  "v_dot2c_f32_bf16 v4, 0x3f803f80, v38\n\t" \
  "v_dot2c_f32_bf16 v6, v38, v38\n\t" \
  "v_dot2c_f32_bf16 v5, 0x3f803f80, v46\n\t" \
  "v_dot2c_f32_bf16 v7, v46, v46\n\t" \
  "v_dot2c_f32_bf16 v4, 0x3f803f80, v39\n\t" \
  "v_dot2c_f32_bf16 v6, v39, v39\n\t" \
  "v_dot2c_f32_bf16 v5, 0x3f803f80, v47\n\t" \
  "v_dot2c_f32_bf16 v7, v47, v47\n\t" \
  "v_mov_b32 v21, s36\n\t" \
  "v_mov_b32 v23, s36\n\t" \
  "v_permlane32_swap_b32 v4, v5\n\t" \
  "v_permlane32_swap_b32 v6, v7\n\t" \
  "v_add_f32 v4, v4, v5\n\t" \
  "v_add_f32 v6, v6, v7\n\t" \
  "s_nop 1\n\t" \
  "v_permlane16_swap_b32 v4, v6\n\t" \
  "v_add_f32 v4, v4, v6\n\t" \
  "s_nop 1\n\t" \
  "v_add_f32_dpp v4, v4, v4 quad_perm:[1,0,3,2] row_mask:0xf bank_mask:0xf\n\t" \
  "s_nop 1\n\t" \
  "v_add_f32_dpp v4, v4, v4 quad_perm:[2,3,0,1] row_mask:0xf bank_mask:0xf\n\t" \
  "s_nop 1\n\t" \
  "v_add_f32_dpp v4, v4, v4 row_half_mirror row_mask:0xf bank_mask:0xf\n\t" \
  "s_nop 1\n\t" \
  "v_add_f32_dpp v4, v4, v4 row_mirror row_mask:0xf bank_mask:0xf\n\t" \
  "v_mul_f32 v4, 0x3a800000, v4\n\t" \
  "v_mov_b32 v5, v4\n\t" \
  "s_nop 1\n\t" \
  "v_permlane16_swap_b32 v4, v5\n\t" \
  "v_fma_f32 v5, -v4, v4, v5\n\t" \
  "v_max_f32 v5, 0, v5\n\t" \
  "v_add_f32 v5, 0x3727c5ac, v5\n\t" \
  "v_rsq_f32 v20, v5\n\t" \
  "s_nop 0\n\t" \
  "v_mul_f32_e64 v22, v20, -v4\n\t" \
  "buffer_store_dwordx4 v[20:23], v58, s[16:19], s46 offen sc1\n\t" \
  FSTA("s_memrealtime s[94:95]\n\t") \
  FSTA("s_memtime s[92:93]\n\t") \
  FSTA("s_waitcnt lgkmcnt(0)\n\t") \
  FSTA("s_or_b32 s94, s94, 1\n\t") \
  FSTA("v_writelane_b32 v255, s94, 6\n\t") \
  FSTA("v_writelane_b32 v255, s92, 7\n\t") \
  "buffer_load_ushort v218, v53, s[12:15], s44 offen\n\t" \
  "s_add_u32 m0, s76, 0\n\t" \
  "s_nop 0\n\t" \
  "buffer_load_dwordx4 v200, s[20:23], s49 offen offset:0 lds\n\t" \
  "buffer_load_dwordx4 v201, s[20:23], s49 offen offset:1024 lds\n\t" \
  "buffer_load_dwordx4 v202, s[20:23], s49 offen offset:2048 lds\n\t" \
  "buffer_load_dwordx4 v203, s[20:23], s49 offen offset:3072 lds\n\t" \
  "s_add_u32 m0, s77, 49152\n\t" \
  "s_xor_b32 s54, s87, 0\n\t" \
  "s_or_b32 s54, s54, s48\n\t" \
  "buffer_load_dwordx4 v212, s[4:7], s54 offen lds\n\t" \
  "buffer_load_dwordx4 v213, s[4:7], s54 offen offset:1024 lds\n\t" \
  "s_add_u32 m0, s77, 51200\n\t" \
  "s_xor_b32 s54, s87, 131072\n\t" \
  "s_or_b32 s54, s54, s48\n\t" \
  "buffer_load_dwordx4 v212, s[4:7], s54 offen lds\n\t" \
  "buffer_load_dwordx4 v213, s[4:7], s54 offen offset:1024 lds\n\t" \
  "s_add_u32 m0, s77, 53248\n\t" \
  "s_xor_b32 s54, s87, 262144\n\t" \
  "s_or_b32 s54, s54, s48\n\t" \
  "buffer_load_dwordx4 v212, s[4:7], s54 offen lds\n\t" \
  "buffer_load_dwordx4 v213, s[4:7], s54 offen offset:1024 lds\n\t" \
  "s_add_u32 m0, s77, 55296\n\t" \
  "s_xor_b32 s54, s87, 393216\n\t" \
  "s_or_b32 s54, s54, s48\n\t" \
  "buffer_load_dwordx4 v212, s[4:7], s54 offen lds\n\t" \
  "buffer_load_dwordx4 v213, s[4:7], s54 offen offset:1024 lds\n\t" \
  "s_add_u32 m0, s76, 49152\n\t" \
  "s_nop 0\n\t" \
  "buffer_load_dwordx4 v200, s[20:23], s79 offen offset:0 lds\n\t" \
  "buffer_load_dwordx4 v201, s[20:23], s79 offen offset:1024 lds\n\t" \
  "buffer_load_dwordx4 v202, s[20:23], s79 offen offset:2048 lds\n\t" \
  "buffer_load_dwordx4 v203, s[20:23], s79 offen offset:3072 lds\n\t" \
  "v_add_u32 v192, 65536, v184\n\t" \
  "v_add_u32 v193, 65536, v185\n\t" \
  "v_add_u32 v194, 65536, v186\n\t" \
  "v_add_u32 v195, 65536, v187\n\t" \
  "v_add_u32 v196, 65536, v188\n\t" \
  "v_add_u32 v197, 65536, v189\n\t" \
  "v_add_u32 v198, 65536, v190\n\t" \
  "v_add_u32 v199, 65536, v191\n\t" \
  "v_lshrrev_b32 v48, 3, v253\n\t" \
  "s_lshr_b32 s54, s2, 6\n\t" \
  "v_lshl_add_u32 v48, s54, 5, v48\n\t" \
  "v_mul_u32_u24 v48, 98304, v48\n\t" \
  "v_and_b32 v49, 0x70, v244\n\t" \
  "s_bfe_u32 s54, s2, 0x10002\n\t" \
  "s_lshl1_add_u32 s54, s40, s54\n\t" \
  "s_lshl_b32 s54, s54, 10\n\t" \
  "s_and_b32 s55, s2, 0x38\n\t" \
  "s_lshl4_add_u32 s54, s55, s54\n\t" \
  "s_add_u32 s54, s54, 4096\n\t" \
  "v_add3_u32 v209, v48, v49, s54\n\t" \
  "v_lshlrev_b32 v219, 2, v253\n\t" \
  "v_add_u32 v219, 151552, v219\n\t" \
  "s_and_b32 s54, s39, 15\n\t" \
  "s_lshl2_add_u32 s54, s54, s3\n\t" \
  "s_lshl_b32 s54, s54, 11\n\t" \
  "s_lshl_b32 s55, s40, 19\n\t" \
  "s_add_u32 s54, s54, s55\n\t" \
  "v_lshl_add_u32 v217, v247, 7, s54\n\t" \
  "s_mov_b64 exec, 0\n\t" \
  "s_cmp_eq_u32 s3, 0\n\t" \
  "s_cbranch_scc1 .Lhc00\n\t" \
  "s_cmp_eq_u32 s3, 1\n\t" \
  "s_cbranch_scc1 .Lhc01\n\t" \
  "s_cmp_eq_u32 s3, 2\n\t" \
  "s_cbranch_scc1 .Lhc02\n\t" \
  "s_branch .Lhc03\n\t" \
  ".Lhr0:\n\t" \
  "s_mov_b64 exec, -1\n\t" \
  "s_sleep 6\n\t" \
  "s_add_u32 s58, s53, 16384\n\t" \
  "s_add_u32 s90, s53, 32768\n\t" \
  "s_add_u32 s91, s53, 49152\n\t" \
  "s_xor_b32 s96, s51, 0\n\t" \
  "s_xor_b32 s97, s51, 1024\n\t" \
  "s_xor_b32 s99, s51, 2048\n\t" \
  "s_xor_b32 s57, s51, 3072\n\t" \
  "buffer_load_dwordx4 v[128:131], v248, s[16:19], s53 offen sc1\n\t" \
  "buffer_load_dwordx4 v[132:135], v248, s[16:19], s58 offen sc1\n\t" \
  "buffer_load_dwordx4 v[136:139], v248, s[16:19], s90 offen sc1\n\t" \
  "buffer_load_dwordx4 v[140:143], v248, s[16:19], s91 offen sc1\n\t" \
  "buffer_load_dwordx4 v[160:163], v249, s[16:19], s96 offen sc1\n\t" \
  "buffer_load_dwordx4 v[164:167], v249, s[16:19], s97 offen sc1\n\t" \
  "buffer_load_dwordx4 v[168:171], v249, s[16:19], s99 offen sc1\n\t" \
  "buffer_load_dwordx4 v[172:175], v249, s[16:19], s57 offen sc1\n\t" \
  "s_bitcmp1_b32 s2, 7\n\t" \
  "s_cselect_b64 s[12:13], s[64:65], s[60:61]\n\t" \
  FSTP("s_memrealtime s[94:95]\n\t") \
  FSTP("s_memtime s[92:93]\n\t") \
  FSTP("s_waitcnt lgkmcnt(0)\n\t") \
  FSTP("s_or_b32 s94, s94, 1\n\t") \
  FSTP("v_writelane_b32 v255, s94, 56\n\t") \
  FSTP("v_writelane_b32 v255, s92, 57\n\t") \
  "s_waitcnt vmcnt(12)\n\t" \
  "ds_read_b128 v[16:19], v204 offset:0\n\t" \
  "ds_read_b128 v[48:51], v204 offset:2048\n\t" \
  "ds_read_b128 v[80:83], v204 offset:4096\n\t" \
  "ds_read_b128 v[112:115], v204 offset:6144\n\t" \
  "ds_read_b128 v[20:23], v205 offset:0\n\t" \
  "ds_read_b128 v[52:55], v205 offset:2048\n\t" \
  "ds_read_b128 v[84:87], v205 offset:4096\n\t" \
  "ds_read_b128 v[116:119], v205 offset:6144\n\t" \
  "ds_read_b128 v[24:27], v204 offset:49152\n\t" \
  "ds_read_b128 v[56:59], v204 offset:51200\n\t" \
  "ds_read_b128 v[88:91], v204 offset:53248\n\t" \
  "ds_read_b128 v[120:123], v204 offset:55296\n\t" \
  "ds_read_b128 v[28:31], v205 offset:49152\n\t" \
  "ds_read_b128 v[60:63], v205 offset:51200\n\t" \
  "ds_read_b128 v[92:95], v205 offset:53248\n\t" \
  "ds_read_b128 v[124:127], v205 offset:55296\n\t" \
  "v_lshlrev_b32 v218, 16, v218\n\t" \
  "v_mul_f32 v145, 0xbfb8aa3b, v218\n\t" \
  "v_exp_f32 v145, v145\n\t" \
  "s_nop 0\n\t" \
  "v_add_f32 v145, 1.0, v145\n\t" \
  "v_rcp_f32 v145, v145\n\t" \
  "s_nop 0\n\t" \
  "v_mul_f32 v218, v145, v218\n\t" \
  "v_cvt_pk_bf16_f32 v218, v218, v218\n\t" \
  "v_lshlrev_b32 v218, 16, v218\n\t" \
  "s_waitcnt lgkmcnt(12)\n\t" \
  "v_lshlrev_b32 v0, 16, v16\n\t" \
  "v_and_b32 v1, s98, v16\n\t" \
  "v_lshlrev_b32 v2, 16, v17\n\t" \
  "v_and_b32 v3, s98, v17\n\t" \
  "v_lshlrev_b32 v4, 16, v18\n\t" \
  "v_and_b32 v5, s98, v18\n\t" \
  "v_lshlrev_b32 v6, 16, v19\n\t" \
  "v_and_b32 v7, s98, v19\n\t" \
  "v_lshlrev_b32 v32, 16, v48\n\t" \
  "v_and_b32 v33, s98, v48\n\t" \
  "v_lshlrev_b32 v34, 16, v49\n\t" \
  "v_and_b32 v35, s98, v49\n\t" \
  "v_lshlrev_b32 v36, 16, v50\n\t" \
  "v_and_b32 v37, s98, v50\n\t" \
  "v_lshlrev_b32 v38, 16, v51\n\t" \
  "v_and_b32 v39, s98, v51\n\t" \
  "v_lshlrev_b32 v64, 16, v80\n\t" \
  "v_and_b32 v65, s98, v80\n\t" \
  "v_lshlrev_b32 v66, 16, v81\n\t" \
  "v_and_b32 v67, s98, v81\n\t" \
  "v_lshlrev_b32 v68, 16, v82\n\t" \
  "v_and_b32 v69, s98, v82\n\t" \
  "v_lshlrev_b32 v70, 16, v83\n\t" \
  "v_and_b32 v71, s98, v83\n\t" \
  "v_lshlrev_b32 v96, 16, v112\n\t" \
  "v_and_b32 v97, s98, v112\n\t" \
  "v_lshlrev_b32 v98, 16, v113\n\t" \
  "v_and_b32 v99, s98, v113\n\t" \
  "v_lshlrev_b32 v100, 16, v114\n\t" \
  "v_and_b32 v101, s98, v114\n\t" \
  "v_lshlrev_b32 v102, 16, v115\n\t" \
  "v_and_b32 v103, s98, v115\n\t" \
  "s_waitcnt lgkmcnt(8)\n\t" \
  "v_lshlrev_b32 v8, 16, v20\n\t" \
  "v_and_b32 v9, s98, v20\n\t" \
  "v_lshlrev_b32 v10, 16, v21\n\t" \
  "v_and_b32 v11, s98, v21\n\t" \
  "v_lshlrev_b32 v12, 16, v22\n\t" \
  "v_and_b32 v13, s98, v22\n\t" \
  "v_lshlrev_b32 v14, 16, v23\n\t" \
  "v_and_b32 v15, s98, v23\n\t" \
  "v_lshlrev_b32 v40, 16, v52\n\t" \
  "v_and_b32 v41, s98, v52\n\t" \
  "v_lshlrev_b32 v42, 16, v53\n\t" \
  "v_and_b32 v43, s98, v53\n\t" \
  "v_lshlrev_b32 v44, 16, v54\n\t" \
  "v_and_b32 v45, s98, v54\n\t" \
  "v_lshlrev_b32 v46, 16, v55\n\t" \
  "v_and_b32 v47, s98, v55\n\t" \
  "v_lshlrev_b32 v72, 16, v84\n\t" \
  "v_and_b32 v73, s98, v84\n\t" \
  "v_lshlrev_b32 v74, 16, v85\n\t" \
  "v_and_b32 v75, s98, v85\n\t" \
  "v_lshlrev_b32 v76, 16, v86\n\t" \
  "v_and_b32 v77, s98, v86\n\t" \
  "v_lshlrev_b32 v78, 16, v87\n\t" \
  "v_and_b32 v79, s98, v87\n\t" \
  "v_lshlrev_b32 v104, 16, v116\n\t" \
  "v_and_b32 v105, s98, v116\n\t" \
  "v_lshlrev_b32 v106, 16, v117\n\t" \
  "v_and_b32 v107, s98, v117\n\t" \
  "v_lshlrev_b32 v108, 16, v118\n\t" \
  "v_and_b32 v109, s98, v118\n\t" \
  "v_lshlrev_b32 v110, 16, v119\n\t" \
  "v_and_b32 v111, s98, v119\n\t" \
  FSTP("s_memrealtime s[94:95]\n\t") \
  FSTP("s_memtime s[92:93]\n\t") \
  FSTP("s_waitcnt lgkmcnt(0)\n\t") \
  FSTP("s_or_b32 s94, s94, 1\n\t") \
  FSTP("v_writelane_b32 v255, s94, 58\n\t") \
  FSTP("v_writelane_b32 v255, s92, 59\n\t") \
  "s_waitcnt vmcnt(0)\n\t" \
  "v_xor_b32 v144, v211, v129\n\t" \
  "v_bitop3_b32 v144, v144, v131, v211 bitop3:0xf6\n\t" \
  "v_bitop3_b32 v144, v144, v133, v211 bitop3:0xf6\n\t" \
  "v_bitop3_b32 v144, v144, v135, v211 bitop3:0xf6\n\t" \
  "v_bitop3_b32 v144, v144, v137, v211 bitop3:0xf6\n\t" \
  "v_bitop3_b32 v144, v144, v139, v211 bitop3:0xf6\n\t" \
  "v_bitop3_b32 v144, v144, v141, v211 bitop3:0xf6\n\t" \
  "v_bitop3_b32 v144, v144, v143, v211 bitop3:0xf6\n\t" \
  "v_bitop3_b32 v144, v144, v161, v211 bitop3:0xf6\n\t" \
  "v_bitop3_b32 v144, v144, v163, v211 bitop3:0xf6\n\t" \
  "v_bitop3_b32 v144, v144, v165, v211 bitop3:0xf6\n\t" \
  "v_bitop3_b32 v144, v144, v167, v211 bitop3:0xf6\n\t" \
  "v_bitop3_b32 v144, v144, v169, v211 bitop3:0xf6\n\t" \
  "v_bitop3_b32 v144, v144, v171, v211 bitop3:0xf6\n\t" \
  "v_bitop3_b32 v144, v144, v173, v211 bitop3:0xf6\n\t" \
  "v_bitop3_b32 v144, v144, v175, v211 bitop3:0xf6\n\t" \
  "v_cmp_ne_u32 vcc, 0, v144\n\t" \
  "v_add_f32 v146, v128, v132\n\t" \
  "v_add_f32 v148, v136, v140\n\t" \
  "v_add_f32 v147, v130, v134\n\t" \
  "v_add_f32 v149, v138, v142\n\t" \
  "v_add_f32 v146, v146, v148\n\t" \
  "v_add_f32 v147, v147, v149\n\t" \
  "s_cbranch_vccnz .Lfr_s\n\t" \
  ".Lfr_k:\n\t" \
  FSTA("s_memrealtime s[94:95]\n\t") \
  FSTA("s_memtime s[92:93]\n\t") \
  FSTA("s_waitcnt lgkmcnt(0)\n\t") \
  FSTA("s_or_b32 s94, s94, 1\n\t") \
  FSTA("v_writelane_b32 v255, s94, 8\n\t") \
  FSTA("v_writelane_b32 v255, s92, 9\n\t") \
  "v_cvt_pk_bf16_f32 v147, v146, v147\n\t" \
  "v_lshlrev_b32 v146, 16, v147\n\t" \
  "v_and_b32 v147, s98, v147\n\t" \
  "v_add_f32 v146, v146, v251\n\t" \
  "v_add_f32 v147, v147, v251\n\t" \
  "v_cvt_pk_bf16_f32 v147, v146, v147\n\t" \
  "v_lshlrev_b32 v146, 16, v147\n\t" \
  "v_and_b32 v147, s98, v147\n\t" \
  "v_mul_f32 v146, 0x3e0293ee, v146\n\t" \
  "v_mul_f32 v147, 0x3e0293ee, v147\n\t" \
  "ds_write_b64 v250, v[146:147]\n\t" \
  "v_pk_fma_f32 v[0:1], v[0:1], v[160:161], v[162:163] op_sel_hi:[1,0,0]\n\t" \
  "v_pk_fma_f32 v[2:3], v[2:3], v[160:161], v[162:163] op_sel_hi:[1,0,0]\n\t" \
  "v_pk_fma_f32 v[4:5], v[4:5], v[160:161], v[162:163] op_sel_hi:[1,0,0]\n\t" \
  "v_pk_fma_f32 v[6:7], v[6:7], v[160:161], v[162:163] op_sel_hi:[1,0,0]\n\t" \
  "v_pk_fma_f32 v[32:33], v[32:33], v[164:165], v[166:167] op_sel_hi:[1,0,0]\n\t" \
  "v_pk_fma_f32 v[34:35], v[34:35], v[164:165], v[166:167] op_sel_hi:[1,0,0]\n\t" \
  "v_pk_fma_f32 v[36:37], v[36:37], v[164:165], v[166:167] op_sel_hi:[1,0,0]\n\t" \
  "v_pk_fma_f32 v[38:39], v[38:39], v[164:165], v[166:167] op_sel_hi:[1,0,0]\n\t" \
  "v_pk_fma_f32 v[64:65], v[64:65], v[168:169], v[170:171] op_sel_hi:[1,0,0]\n\t" \
  "v_pk_fma_f32 v[66:67], v[66:67], v[168:169], v[170:171] op_sel_hi:[1,0,0]\n\t" \
  "v_pk_fma_f32 v[68:69], v[68:69], v[168:169], v[170:171] op_sel_hi:[1,0,0]\n\t" \
  "v_pk_fma_f32 v[70:71], v[70:71], v[168:169], v[170:171] op_sel_hi:[1,0,0]\n\t" \
  "v_pk_fma_f32 v[96:97], v[96:97], v[172:173], v[174:175] op_sel_hi:[1,0,0]\n\t" \
  "v_pk_fma_f32 v[98:99], v[98:99], v[172:173], v[174:175] op_sel_hi:[1,0,0]\n\t" \
  "v_pk_fma_f32 v[100:101], v[100:101], v[172:173], v[174:175] op_sel_hi:[1,0,0]\n\t" \
  "v_pk_fma_f32 v[102:103], v[102:103], v[172:173], v[174:175] op_sel_hi:[1,0,0]\n\t" \
  FSTP("s_memrealtime s[94:95]\n\t") \
  FSTP("s_memtime s[92:93]\n\t") \
  FSTP("s_waitcnt lgkmcnt(0)\n\t") \
  FSTP("s_or_b32 s94, s94, 1\n\t") \
  FSTP("v_writelane_b32 v255, s94, 24\n\t") \
  FSTP("v_writelane_b32 v255, s92, 25\n\t") \
  "s_waitcnt lgkmcnt(0)\n\t" \
  "s_barrier\n\t" \
  FSTP("s_memrealtime s[94:95]\n\t") \
  FSTP("s_memtime s[92:93]\n\t") \
  FSTP("s_waitcnt lgkmcnt(0)\n\t") \
  FSTP("s_or_b32 s94, s94, 1\n\t") \
  FSTP("v_writelane_b32 v255, s94, 26\n\t") \
  FSTP("v_writelane_b32 v255, s92, 27\n\t") \
  "ds_read_b128 v[128:131], v208 offset:0\n\t" \
  "ds_read_b128 v[132:135], v208 offset:16\n\t" \
  "ds_read_b128 v[136:139], v208 offset:256\n\t" \
  "ds_read_b128 v[140:143], v208 offset:272\n\t" \
  "ds_read_b128 v[144:147], v208 offset:128\n\t" \
  "ds_read_b128 v[148:151], v208 offset:144\n\t" \
  "ds_read_b128 v[152:155], v208 offset:384\n\t" \
  "ds_read_b128 v[156:159], v208 offset:400\n\t" \
  "global_load_dword v246, v215, s[20:21]\n\t" \
  "v_pk_fma_f32 v[8:9], v[8:9], v[160:161], v[162:163] op_sel_hi:[1,0,0]\n\t" \
  "v_pk_fma_f32 v[10:11], v[10:11], v[160:161], v[162:163] op_sel_hi:[1,0,0]\n\t" \
  "v_pk_fma_f32 v[12:13], v[12:13], v[160:161], v[162:163] op_sel_hi:[1,0,0]\n\t" \
  "v_pk_fma_f32 v[14:15], v[14:15], v[160:161], v[162:163] op_sel_hi:[1,0,0]\n\t" \
  "v_pk_fma_f32 v[40:41], v[40:41], v[164:165], v[166:167] op_sel_hi:[1,0,0]\n\t" \
  "v_pk_fma_f32 v[42:43], v[42:43], v[164:165], v[166:167] op_sel_hi:[1,0,0]\n\t" \
  "v_pk_fma_f32 v[44:45], v[44:45], v[164:165], v[166:167] op_sel_hi:[1,0,0]\n\t" \
  "v_pk_fma_f32 v[46:47], v[46:47], v[164:165], v[166:167] op_sel_hi:[1,0,0]\n\t" \
  "v_pk_fma_f32 v[72:73], v[72:73], v[168:169], v[170:171] op_sel_hi:[1,0,0]\n\t" \
  "v_pk_fma_f32 v[74:75], v[74:75], v[168:169], v[170:171] op_sel_hi:[1,0,0]\n\t" \
  "v_pk_fma_f32 v[76:77], v[76:77], v[168:169], v[170:171] op_sel_hi:[1,0,0]\n\t" \
  "v_pk_fma_f32 v[78:79], v[78:79], v[168:169], v[170:171] op_sel_hi:[1,0,0]\n\t" \
  "v_pk_fma_f32 v[104:105], v[104:105], v[172:173], v[174:175] op_sel_hi:[1,0,0]\n\t" \
  "v_pk_fma_f32 v[106:107], v[106:107], v[172:173], v[174:175] op_sel_hi:[1,0,0]\n\t" \
  "v_pk_fma_f32 v[108:109], v[108:109], v[172:173], v[174:175] op_sel_hi:[1,0,0]\n\t" \
  "v_pk_fma_f32 v[110:111], v[110:111], v[172:173], v[174:175] op_sel_hi:[1,0,0]\n\t" \
  "s_xor_b32 s97, s86, 0\n\t" \
  "s_waitcnt lgkmcnt(4)\n\t" \
  "v_pk_fma_f32 v[0:1], v[0:1], v[128:129], v[136:137]\n\t" \
  "v_pk_fma_f32 v[2:3], v[2:3], v[130:131], v[138:139]\n\t" \
  "v_pk_fma_f32 v[4:5], v[4:5], v[132:133], v[140:141]\n\t" \
  "v_pk_fma_f32 v[6:7], v[6:7], v[134:135], v[142:143]\n\t" \
  "v_cvt_pk_bf16_f32 v16, v0, v1\n\t" \
  "v_cvt_pk_bf16_f32 v17, v2, v3\n\t" \
  "v_cvt_pk_bf16_f32 v18, v4, v5\n\t" \
  "v_cvt_pk_bf16_f32 v19, v6, v7\n\t" \
  "buffer_store_dwordx4 v[16:19], v214, s[16:19], s97 offen sc1\n\t" \
  "s_xor_b32 s97, s86, 256\n\t" \
  "v_pk_fma_f32 v[32:33], v[32:33], v[128:129], v[136:137]\n\t" \
  "v_pk_fma_f32 v[34:35], v[34:35], v[130:131], v[138:139]\n\t" \
  "v_pk_fma_f32 v[36:37], v[36:37], v[132:133], v[140:141]\n\t" \
  "v_pk_fma_f32 v[38:39], v[38:39], v[134:135], v[142:143]\n\t" \
  "v_cvt_pk_bf16_f32 v48, v32, v33\n\t" \
  "v_cvt_pk_bf16_f32 v49, v34, v35\n\t" \
  "v_cvt_pk_bf16_f32 v50, v36, v37\n\t" \
  "v_cvt_pk_bf16_f32 v51, v38, v39\n\t" \
  "buffer_store_dwordx4 v[48:51], v214, s[16:19], s97 offen sc1\n\t" \
  "s_xor_b32 s97, s86, 512\n\t" \
  "v_pk_fma_f32 v[64:65], v[64:65], v[128:129], v[136:137]\n\t" \
  "v_pk_fma_f32 v[66:67], v[66:67], v[130:131], v[138:139]\n\t" \
  "v_pk_fma_f32 v[68:69], v[68:69], v[132:133], v[140:141]\n\t" \
  "v_pk_fma_f32 v[70:71], v[70:71], v[134:135], v[142:143]\n\t" \
  "v_cvt_pk_bf16_f32 v80, v64, v65\n\t" \
  "v_cvt_pk_bf16_f32 v81, v66, v67\n\t" \
  "v_cvt_pk_bf16_f32 v82, v68, v69\n\t" \
  "v_cvt_pk_bf16_f32 v83, v70, v71\n\t" \
  ".Lhs0:\n\ts_cbranch_execz .Lhs0n\n\t" \
  "buffer_store_dwordx4 v[80:83], v214, s[16:19], s97 offen sc1\n\t" \
  "s_xor_b32 s97, s86, 768\n\t" \
  "v_pk_fma_f32 v[96:97], v[96:97], v[128:129], v[136:137]\n\t" \
  "v_pk_fma_f32 v[98:99], v[98:99], v[130:131], v[138:139]\n\t" \
  "v_pk_fma_f32 v[100:101], v[100:101], v[132:133], v[140:141]\n\t" \
  "v_pk_fma_f32 v[102:103], v[102:103], v[134:135], v[142:143]\n\t" \
  "v_cvt_pk_bf16_f32 v112, v96, v97\n\t" \
  "v_cvt_pk_bf16_f32 v113, v98, v99\n\t" \
  "v_cvt_pk_bf16_f32 v114, v100, v101\n\t" \
  "v_cvt_pk_bf16_f32 v115, v102, v103\n\t" \
  "buffer_store_dwordx4 v[112:115], v214, s[16:19], s97 offen sc1\n\t" \
  "ds_read_b128 v[128:131], v208 offset:512\n\t" \
  "ds_read_b128 v[132:135], v208 offset:528\n\t" \
  "ds_read_b128 v[136:139], v208 offset:768\n\t" \
  "ds_read_b128 v[140:143], v208 offset:784\n\t" \
  FSTP("s_memrealtime s[94:95]\n\t") \
  FSTP("s_memtime s[92:93]\n\t") \
  "s_add_u32 m0, s77, 98304\n\t" \
  "s_xor_b32 s96, s87, 0\n\t" \
  "s_add_u32 s96, s96, s88\n\t" \
  "s_waitcnt lgkmcnt(4)\n\t" \
  "v_pk_fma_f32 v[8:9], v[8:9], v[144:145], v[152:153]\n\t" \
  "v_pk_fma_f32 v[10:11], v[10:11], v[146:147], v[154:155]\n\t" \
  "v_pk_fma_f32 v[12:13], v[12:13], v[148:149], v[156:157]\n\t" \
  "v_pk_fma_f32 v[14:15], v[14:15], v[150:151], v[158:159]\n\t" \
  "v_cvt_pk_bf16_f32 v20, v8, v9\n\t" \
  "v_cvt_pk_bf16_f32 v21, v10, v11\n\t" \
  "v_cvt_pk_bf16_f32 v22, v12, v13\n\t" \
  "v_cvt_pk_bf16_f32 v23, v14, v15\n\t" \
  "buffer_load_dwordx4 v212, s[4:7], s96 offen lds\n\t" \
  "v_pk_fma_f32 v[40:41], v[40:41], v[144:145], v[152:153]\n\t" \
  "v_pk_fma_f32 v[42:43], v[42:43], v[146:147], v[154:155]\n\t" \
  "v_pk_fma_f32 v[44:45], v[44:45], v[148:149], v[156:157]\n\t" \
  "v_pk_fma_f32 v[46:47], v[46:47], v[150:151], v[158:159]\n\t" \
  "v_cvt_pk_bf16_f32 v52, v40, v41\n\t" \
  "v_cvt_pk_bf16_f32 v53, v42, v43\n\t" \
  "v_cvt_pk_bf16_f32 v54, v44, v45\n\t" \
  "v_cvt_pk_bf16_f32 v55, v46, v47\n\t" \
  "buffer_load_dwordx4 v213, s[4:7], s96 offen offset:1024 lds\n\t" \
  "s_add_u32 m0, s77, 100352\n\t" \
  "s_xor_b32 s96, s87, 131072\n\t" \
  "s_add_u32 s96, s96, s88\n\t" \
  "v_pk_fma_f32 v[72:73], v[72:73], v[144:145], v[152:153]\n\t" \
  "v_pk_fma_f32 v[74:75], v[74:75], v[146:147], v[154:155]\n\t" \
  "v_pk_fma_f32 v[76:77], v[76:77], v[148:149], v[156:157]\n\t" \
  "v_pk_fma_f32 v[78:79], v[78:79], v[150:151], v[158:159]\n\t" \
  "v_cvt_pk_bf16_f32 v84, v72, v73\n\t" \
  "v_cvt_pk_bf16_f32 v85, v74, v75\n\t" \
  "v_cvt_pk_bf16_f32 v86, v76, v77\n\t" \
  "v_cvt_pk_bf16_f32 v87, v78, v79\n\t" \
  "buffer_load_dwordx4 v212, s[4:7], s96 offen lds\n\t" \
  "v_pk_fma_f32 v[104:105], v[104:105], v[144:145], v[152:153]\n\t" \
  "v_pk_fma_f32 v[106:107], v[106:107], v[146:147], v[154:155]\n\t" \
  "v_pk_fma_f32 v[108:109], v[108:109], v[148:149], v[156:157]\n\t" \
  "v_pk_fma_f32 v[110:111], v[110:111], v[150:151], v[158:159]\n\t" \
  "v_cvt_pk_bf16_f32 v116, v104, v105\n\t" \
  "v_cvt_pk_bf16_f32 v117, v106, v107\n\t" \
  "v_cvt_pk_bf16_f32 v118, v108, v109\n\t" \
  "v_cvt_pk_bf16_f32 v119, v110, v111\n\t" \
  "buffer_load_dwordx4 v213, s[4:7], s96 offen offset:1024 lds\n\t" \
  "ds_read_b128 v[144:147], v208 offset:640\n\t" \
  "ds_read_b128 v[148:151], v208 offset:656\n\t" \
  "ds_read_b128 v[152:155], v208 offset:896\n\t" \
  "ds_read_b128 v[156:159], v208 offset:912\n\t" \
  FSTP("v_writelane_b32 v255, s94, 28\n\t") \
  FSTP("v_writelane_b32 v255, s92, 29\n\t") \
  FSTP("s_memrealtime s[94:95]\n\t") \
  FSTP("s_memtime s[92:93]\n\t") \
  "s_add_u32 m0, s77, 102400\n\t" \
  "s_xor_b32 s96, s87, 262144\n\t" \
  "s_add_u32 s96, s96, s88\n\t" \
  "v_lshlrev_b32 v176, 16, v24\n\t" \
  "v_and_b32 v177, s98, v24\n\t" \
  "v_lshlrev_b32 v178, 16, v25\n\t" \
  "v_and_b32 v179, s98, v25\n\t" \
  "v_lshlrev_b32 v180, 16, v26\n\t" \
  "v_and_b32 v181, s98, v26\n\t" \
  "v_lshlrev_b32 v182, 16, v27\n\t" \
  "v_and_b32 v183, s98, v27\n\t" \
  "v_pk_fma_f32 v[176:177], v[176:177], v[160:161], v[162:163] op_sel_hi:[1,0,0]\n\t" \
  "v_pk_fma_f32 v[178:179], v[178:179], v[160:161], v[162:163] op_sel_hi:[1,0,0]\n\t" \
  "v_pk_fma_f32 v[180:181], v[180:181], v[160:161], v[162:163] op_sel_hi:[1,0,0]\n\t" \
  "v_pk_fma_f32 v[182:183], v[182:183], v[160:161], v[162:163] op_sel_hi:[1,0,0]\n\t" \
  "s_waitcnt lgkmcnt(4)\n\t" \
  "v_pk_fma_f32 v[176:177], v[176:177], v[128:129], v[136:137]\n\t" \
  "v_pk_fma_f32 v[178:179], v[178:179], v[130:131], v[138:139]\n\t" \
  "v_pk_fma_f32 v[180:181], v[180:181], v[132:133], v[140:141]\n\t" \
  "v_pk_fma_f32 v[182:183], v[182:183], v[134:135], v[142:143]\n\t" \
  "v_cvt_pk_bf16_f32 v24, v176, v177\n\t" \
  "v_cvt_pk_bf16_f32 v25, v178, v179\n\t" \
  "v_cvt_pk_bf16_f32 v26, v180, v181\n\t" \
  "v_cvt_pk_bf16_f32 v27, v182, v183\n\t" \
  "buffer_load_dwordx4 v212, s[4:7], s96 offen lds\n\t" \
  "v_lshlrev_b32 v176, 16, v56\n\t" \
  "v_and_b32 v177, s98, v56\n\t" \
  "v_lshlrev_b32 v178, 16, v57\n\t" \
  "v_and_b32 v179, s98, v57\n\t" \
  "v_lshlrev_b32 v180, 16, v58\n\t" \
  "v_and_b32 v181, s98, v58\n\t" \
  "v_lshlrev_b32 v182, 16, v59\n\t" \
  "v_and_b32 v183, s98, v59\n\t" \
  "v_pk_fma_f32 v[176:177], v[176:177], v[164:165], v[166:167] op_sel_hi:[1,0,0]\n\t" \
  "v_pk_fma_f32 v[178:179], v[178:179], v[164:165], v[166:167] op_sel_hi:[1,0,0]\n\t" \
  "v_pk_fma_f32 v[180:181], v[180:181], v[164:165], v[166:167] op_sel_hi:[1,0,0]\n\t" \
  "v_pk_fma_f32 v[182:183], v[182:183], v[164:165], v[166:167] op_sel_hi:[1,0,0]\n\t" \
  "v_pk_fma_f32 v[176:177], v[176:177], v[128:129], v[136:137]\n\t" \
  "v_pk_fma_f32 v[178:179], v[178:179], v[130:131], v[138:139]\n\t" \
  "v_pk_fma_f32 v[180:181], v[180:181], v[132:133], v[140:141]\n\t" \
  "v_pk_fma_f32 v[182:183], v[182:183], v[134:135], v[142:143]\n\t" \
  "v_cvt_pk_bf16_f32 v56, v176, v177\n\t" \
  "v_cvt_pk_bf16_f32 v57, v178, v179\n\t" \
  "v_cvt_pk_bf16_f32 v58, v180, v181\n\t" \
  "v_cvt_pk_bf16_f32 v59, v182, v183\n\t" \
  "buffer_load_dwordx4 v213, s[4:7], s96 offen offset:1024 lds\n\t" \
  "s_add_u32 m0, s77, 104448\n\t" \
  "s_xor_b32 s96, s87, 393216\n\t" \
  "s_add_u32 s96, s96, s88\n\t" \
  "v_lshlrev_b32 v176, 16, v88\n\t" \
  "v_and_b32 v177, s98, v88\n\t" \
  "v_lshlrev_b32 v178, 16, v89\n\t" \
  "v_and_b32 v179, s98, v89\n\t" \
  "v_lshlrev_b32 v180, 16, v90\n\t" \
  "v_and_b32 v181, s98, v90\n\t" \
  "v_lshlrev_b32 v182, 16, v91\n\t" \
  "v_and_b32 v183, s98, v91\n\t" \
  "v_pk_fma_f32 v[176:177], v[176:177], v[168:169], v[170:171] op_sel_hi:[1,0,0]\n\t" \
  "v_pk_fma_f32 v[178:179], v[178:179], v[168:169], v[170:171] op_sel_hi:[1,0,0]\n\t" \
  "v_pk_fma_f32 v[180:181], v[180:181], v[168:169], v[170:171] op_sel_hi:[1,0,0]\n\t" \
  "v_pk_fma_f32 v[182:183], v[182:183], v[168:169], v[170:171] op_sel_hi:[1,0,0]\n\t" \
  "v_pk_fma_f32 v[176:177], v[176:177], v[128:129], v[136:137]\n\t" \
  "v_pk_fma_f32 v[178:179], v[178:179], v[130:131], v[138:139]\n\t" \
  "v_pk_fma_f32 v[180:181], v[180:181], v[132:133], v[140:141]\n\t" \
  "v_pk_fma_f32 v[182:183], v[182:183], v[134:135], v[142:143]\n\t" \
  "v_cvt_pk_bf16_f32 v88, v176, v177\n\t" \
  "v_cvt_pk_bf16_f32 v89, v178, v179\n\t" \
  "v_cvt_pk_bf16_f32 v90, v180, v181\n\t" \
  "v_cvt_pk_bf16_f32 v91, v182, v183\n\t" \
  "buffer_load_dwordx4 v212, s[4:7], s96 offen lds\n\t" \
  "v_lshlrev_b32 v176, 16, v120\n\t" \
  "v_and_b32 v177, s98, v120\n\t" \
  "v_lshlrev_b32 v178, 16, v121\n\t" \
  "v_and_b32 v179, s98, v121\n\t" \
  "v_lshlrev_b32 v180, 16, v122\n\t" \
  "v_and_b32 v181, s98, v122\n\t" \
  "v_lshlrev_b32 v182, 16, v123\n\t" \
  "v_and_b32 v183, s98, v123\n\t" \
  "v_pk_fma_f32 v[176:177], v[176:177], v[172:173], v[174:175] op_sel_hi:[1,0,0]\n\t" \
  "v_pk_fma_f32 v[178:179], v[178:179], v[172:173], v[174:175] op_sel_hi:[1,0,0]\n\t" \
  "v_pk_fma_f32 v[180:181], v[180:181], v[172:173], v[174:175] op_sel_hi:[1,0,0]\n\t" \
  "v_pk_fma_f32 v[182:183], v[182:183], v[172:173], v[174:175] op_sel_hi:[1,0,0]\n\t" \
  "v_pk_fma_f32 v[176:177], v[176:177], v[128:129], v[136:137]\n\t" \
  "v_pk_fma_f32 v[178:179], v[178:179], v[130:131], v[138:139]\n\t" \
  "v_pk_fma_f32 v[180:181], v[180:181], v[132:133], v[140:141]\n\t" \
  "v_pk_fma_f32 v[182:183], v[182:183], v[134:135], v[142:143]\n\t" \
  "v_cvt_pk_bf16_f32 v120, v176, v177\n\t" \
  "v_cvt_pk_bf16_f32 v121, v178, v179\n\t" \
  "v_cvt_pk_bf16_f32 v122, v180, v181\n\t" \
  "v_cvt_pk_bf16_f32 v123, v182, v183\n\t" \
  "buffer_load_dwordx4 v213, s[4:7], s96 offen offset:1024 lds\n\t" \
  "ds_read_b128 v[128:131], v208 offset:1024\n\t" \
  "ds_read_b128 v[132:135], v208 offset:1040\n\t" \
  "ds_read_b128 v[136:139], v208 offset:1280\n\t" \
  "ds_read_b128 v[140:143], v208 offset:1296\n\t" \
  FSTP("v_writelane_b32 v255, s94, 30\n\t") \
  FSTP("v_writelane_b32 v255, s92, 31\n\t") \
  FSTP("s_memrealtime s[94:95]\n\t") \
  FSTP("s_memtime s[92:93]\n\t") \
  "s_add_u32 m0, s76, 98304\n\t" \
  "v_lshlrev_b32 v176, 16, v28\n\t" \
  "v_and_b32 v177, s98, v28\n\t" \
  "v_lshlrev_b32 v178, 16, v29\n\t" \
  "v_and_b32 v179, s98, v29\n\t" \
  "v_lshlrev_b32 v180, 16, v30\n\t" \
  "v_and_b32 v181, s98, v30\n\t" \
  "v_lshlrev_b32 v182, 16, v31\n\t" \
  "v_and_b32 v183, s98, v31\n\t" \
  "v_pk_fma_f32 v[176:177], v[176:177], v[160:161], v[162:163] op_sel_hi:[1,0,0]\n\t" \
  "v_pk_fma_f32 v[178:179], v[178:179], v[160:161], v[162:163] op_sel_hi:[1,0,0]\n\t" \
  "v_pk_fma_f32 v[180:181], v[180:181], v[160:161], v[162:163] op_sel_hi:[1,0,0]\n\t" \
  "v_pk_fma_f32 v[182:183], v[182:183], v[160:161], v[162:163] op_sel_hi:[1,0,0]\n\t" \
  "s_waitcnt lgkmcnt(4)\n\t" \
  "v_pk_fma_f32 v[176:177], v[176:177], v[144:145], v[152:153]\n\t" \
  "v_pk_fma_f32 v[178:179], v[178:179], v[146:147], v[154:155]\n\t" \
  "v_pk_fma_f32 v[180:181], v[180:181], v[148:149], v[156:157]\n\t" \
  "v_pk_fma_f32 v[182:183], v[182:183], v[150:151], v[158:159]\n\t" \
  "v_cvt_pk_bf16_f32 v28, v176, v177\n\t" \
  "v_cvt_pk_bf16_f32 v29, v178, v179\n\t" \
  "v_cvt_pk_bf16_f32 v30, v180, v181\n\t" \
  "v_cvt_pk_bf16_f32 v31, v182, v183\n\t" \
  "buffer_load_dwordx4 v200, s[20:23], s80 offen offset:0 lds\n\t" \
  "ds_read_b64_tr_b16 a[128:129], v184 offset:0\n\t" \
  "ds_read_b64_tr_b16 a[130:131], v184 offset:1024\n\t" \
  "ds_read_b64_tr_b16 a[132:133], v185 offset:0\n\t" \
  "ds_read_b64_tr_b16 a[134:135], v185 offset:1024\n\t" \
  "v_lshlrev_b32 v176, 16, v60\n\t" \
  "v_and_b32 v177, s98, v60\n\t" \
  "v_lshlrev_b32 v178, 16, v61\n\t" \
  "v_and_b32 v179, s98, v61\n\t" \
  "v_lshlrev_b32 v180, 16, v62\n\t" \
  "v_and_b32 v181, s98, v62\n\t" \
  "v_lshlrev_b32 v182, 16, v63\n\t" \
  "v_and_b32 v183, s98, v63\n\t" \
  "v_pk_fma_f32 v[176:177], v[176:177], v[164:165], v[166:167] op_sel_hi:[1,0,0]\n\t" \
  "v_pk_fma_f32 v[178:179], v[178:179], v[164:165], v[166:167] op_sel_hi:[1,0,0]\n\t" \
  "v_pk_fma_f32 v[180:181], v[180:181], v[164:165], v[166:167] op_sel_hi:[1,0,0]\n\t" \
  "v_pk_fma_f32 v[182:183], v[182:183], v[164:165], v[166:167] op_sel_hi:[1,0,0]\n\t" \
  "v_pk_fma_f32 v[176:177], v[176:177], v[144:145], v[152:153]\n\t" \
  "v_pk_fma_f32 v[178:179], v[178:179], v[146:147], v[154:155]\n\t" \
  "v_pk_fma_f32 v[180:181], v[180:181], v[148:149], v[156:157]\n\t" \
  "v_pk_fma_f32 v[182:183], v[182:183], v[150:151], v[158:159]\n\t" \
  "v_cvt_pk_bf16_f32 v60, v176, v177\n\t" \
  "v_cvt_pk_bf16_f32 v61, v178, v179\n\t" \
  "v_cvt_pk_bf16_f32 v62, v180, v181\n\t" \
  "v_cvt_pk_bf16_f32 v63, v182, v183\n\t" \
  "buffer_load_dwordx4 v201, s[20:23], s80 offen offset:1024 lds\n\t" \
  "ds_read_b64_tr_b16 a[136:137], v186 offset:0\n\t" \
  "ds_read_b64_tr_b16 a[138:139], v186 offset:1024\n\t" \
  "ds_read_b64_tr_b16 a[140:141], v187 offset:0\n\t" \
  "ds_read_b64_tr_b16 a[142:143], v187 offset:1024\n\t" \
  "v_lshlrev_b32 v176, 16, v92\n\t" \
  "v_and_b32 v177, s98, v92\n\t" \
  "v_lshlrev_b32 v178, 16, v93\n\t" \
  "v_and_b32 v179, s98, v93\n\t" \
  "v_lshlrev_b32 v180, 16, v94\n\t" \
  "v_and_b32 v181, s98, v94\n\t" \
  "v_lshlrev_b32 v182, 16, v95\n\t" \
  "v_and_b32 v183, s98, v95\n\t" \
  "v_pk_fma_f32 v[176:177], v[176:177], v[168:169], v[170:171] op_sel_hi:[1,0,0]\n\t" \
  "v_pk_fma_f32 v[178:179], v[178:179], v[168:169], v[170:171] op_sel_hi:[1,0,0]\n\t" \
  "v_pk_fma_f32 v[180:181], v[180:181], v[168:169], v[170:171] op_sel_hi:[1,0,0]\n\t" \
  "v_pk_fma_f32 v[182:183], v[182:183], v[168:169], v[170:171] op_sel_hi:[1,0,0]\n\t" \
  "v_pk_fma_f32 v[176:177], v[176:177], v[144:145], v[152:153]\n\t" \
  "v_pk_fma_f32 v[178:179], v[178:179], v[146:147], v[154:155]\n\t" \
  "v_pk_fma_f32 v[180:181], v[180:181], v[148:149], v[156:157]\n\t" \
  "v_pk_fma_f32 v[182:183], v[182:183], v[150:151], v[158:159]\n\t" \
  "v_cvt_pk_bf16_f32 v92, v176, v177\n\t" \
  "v_cvt_pk_bf16_f32 v93, v178, v179\n\t" \
  "v_cvt_pk_bf16_f32 v94, v180, v181\n\t" \
  "v_cvt_pk_bf16_f32 v95, v182, v183\n\t" \
  "buffer_load_dwordx4 v202, s[20:23], s80 offen offset:2048 lds\n\t" \
  "ds_read_b64_tr_b16 a[144:145], v188 offset:0\n\t" \
  "ds_read_b64_tr_b16 a[146:147], v188 offset:1024\n\t" \
  "ds_read_b64_tr_b16 a[148:149], v189 offset:0\n\t" \
  "ds_read_b64_tr_b16 a[150:151], v189 offset:1024\n\t" \
  "v_lshlrev_b32 v176, 16, v124\n\t" \
  "v_and_b32 v177, s98, v124\n\t" \
  "v_lshlrev_b32 v178, 16, v125\n\t" \
  "v_and_b32 v179, s98, v125\n\t" \
  "v_lshlrev_b32 v180, 16, v126\n\t" \
  "v_and_b32 v181, s98, v126\n\t" \
  "v_lshlrev_b32 v182, 16, v127\n\t" \
  "v_and_b32 v183, s98, v127\n\t" \
  "v_pk_fma_f32 v[176:177], v[176:177], v[172:173], v[174:175] op_sel_hi:[1,0,0]\n\t" \
  "v_pk_fma_f32 v[178:179], v[178:179], v[172:173], v[174:175] op_sel_hi:[1,0,0]\n\t" \
  "v_pk_fma_f32 v[180:181], v[180:181], v[172:173], v[174:175] op_sel_hi:[1,0,0]\n\t" \
  "v_pk_fma_f32 v[182:183], v[182:183], v[172:173], v[174:175] op_sel_hi:[1,0,0]\n\t" \
  "v_pk_fma_f32 v[176:177], v[176:177], v[144:145], v[152:153]\n\t" \
  "v_pk_fma_f32 v[178:179], v[178:179], v[146:147], v[154:155]\n\t" \
  "v_pk_fma_f32 v[180:181], v[180:181], v[148:149], v[156:157]\n\t" \
  "v_pk_fma_f32 v[182:183], v[182:183], v[150:151], v[158:159]\n\t" \
  "v_cvt_pk_bf16_f32 v124, v176, v177\n\t" \
  "v_cvt_pk_bf16_f32 v125, v178, v179\n\t" \
  "v_cvt_pk_bf16_f32 v126, v180, v181\n\t" \
  "v_cvt_pk_bf16_f32 v127, v182, v183\n\t" \
  "buffer_load_dwordx4 v203, s[20:23], s80 offen offset:3072 lds\n\t" \
  "ds_read_b64_tr_b16 a[152:153], v190 offset:0\n\t" \
  "ds_read_b64_tr_b16 a[154:155], v190 offset:1024\n\t" \
  "ds_read_b64_tr_b16 a[156:157], v191 offset:0\n\t" \
  "ds_read_b64_tr_b16 a[158:159], v191 offset:1024\n\t" \
  "ds_read_b128 v[144:147], v208 offset:1152\n\t" \
  "ds_read_b128 v[148:151], v208 offset:1168\n\t" \
  "ds_read_b128 v[152:155], v208 offset:1408\n\t" \
  "ds_read_b128 v[156:159], v208 offset:1424\n\t" \
  FSTP("v_writelane_b32 v255, s94, 32\n\t") \
  FSTP("v_writelane_b32 v255, s92, 33\n\t") \
  FSTP("s_memrealtime s[94:95]\n\t") \
  FSTP("s_memtime s[92:93]\n\t") \
  "s_waitcnt vmcnt(12)\n\t" \
  "s_mov_b64 exec, 1\n\t" \
  "buffer_store_dword v211, off, s[16:19], s85 sc1\n\t" \
  "s_mov_b64 exec, -1\n\t" \
  FSTP("v_writelane_b32 v255, s94, 34\n\t") \
  FSTP("v_writelane_b32 v255, s92, 35\n\t") \
  FSTP("s_memrealtime s[94:95]\n\t") \
  FSTP("s_memtime s[92:93]\n\t") \
  "s_waitcnt lgkmcnt(4)\n\t" \
  "v_mfma_f32_16x16x32_bf16 v[0:3], a[128:131], v[16:19], 0\n\t" \
  "s_add_u32 m0, s77, 0\n\t" \
  "s_xor_b32 s96, s87, 0\n\t" \
  "s_add_u32 s96, s96, s89\n\t" \
  "v_mfma_f32_16x16x32_bf16 v[32:35], a[128:131], v[48:51], 0\n\t" \
  "buffer_load_dwordx4 v212, s[4:7], s96 offen lds\n\t" \
  "v_mfma_f32_16x16x32_bf16 v[64:67], a[128:131], v[80:83], 0\n\t" \
  "ds_read_b64_tr_b16 a[160:161], v184 offset:8192\n\t" \
  "v_mfma_f32_16x16x32_bf16 v[96:99], a[128:131], v[112:115], 0\n\t" \
  "ds_read_b64_tr_b16 a[162:163], v184 offset:9216\n\t" \
  "v_mfma_f32_16x16x32_bf16 v[100:103], a[132:135], v[112:115], 0\n\t" \
  "buffer_load_dwordx4 v213, s[4:7], s96 offen offset:1024 lds\n\t" \
  "v_mfma_f32_16x16x32_bf16 v[68:71], a[132:135], v[80:83], 0\n\t" \
  "s_add_u32 m0, s77, 2048\n\t" \
  "ds_read_b64_tr_b16 a[164:165], v185 offset:8192\n\t" \
  "s_xor_b32 s96, s87, 131072\n\t" \
  "v_mfma_f32_16x16x32_bf16 v[36:39], a[132:135], v[48:51], 0\n\t" \
  "ds_read_b64_tr_b16 a[166:167], v185 offset:9216\n\t" \
  "s_add_u32 s96, s96, s89\n\t" \
  "v_mfma_f32_16x16x32_bf16 v[4:7], a[132:135], v[16:19], 0\n\t" \
  "ds_read_b64_tr_b16 a[168:169], v186 offset:8192\n\t" \
  "v_mfma_f32_16x16x32_bf16 v[8:11], a[136:139], v[16:19], 0\n\t" \
  "buffer_load_dwordx4 v212, s[4:7], s96 offen lds\n\t" \
  "v_mfma_f32_16x16x32_bf16 v[40:43], a[136:139], v[48:51], 0\n\t" \
  "ds_read_b64_tr_b16 a[170:171], v186 offset:9216\n\t" \
  "v_mfma_f32_16x16x32_bf16 v[72:75], a[136:139], v[80:83], 0\n\t" \
  "ds_read_b64_tr_b16 a[172:173], v187 offset:8192\n\t" \
  "v_mfma_f32_16x16x32_bf16 v[104:107], a[136:139], v[112:115], 0\n\t" \
  "buffer_load_dwordx4 v213, s[4:7], s96 offen offset:1024 lds\n\t" \
  "v_mfma_f32_16x16x32_bf16 v[108:111], a[140:143], v[112:115], 0\n\t" \
  "s_add_u32 m0, s77, 4096\n\t" \
  "ds_read_b64_tr_b16 a[174:175], v187 offset:9216\n\t" \
  "s_xor_b32 s96, s87, 262144\n\t" \
  "v_mfma_f32_16x16x32_bf16 v[76:79], a[140:143], v[80:83], 0\n\t" \
  "ds_read_b64_tr_b16 a[176:177], v188 offset:8192\n\t" \
  "s_add_u32 s96, s96, s89\n\t" \
  "v_mfma_f32_16x16x32_bf16 v[44:47], a[140:143], v[48:51], 0\n\t" \
  "buffer_load_dwordx4 v212, s[4:7], s96 offen lds\n\t" \
  "v_mfma_f32_16x16x32_bf16 v[12:15], a[140:143], v[16:19], 0\n\t" \
  "ds_read_b64_tr_b16 a[178:179], v188 offset:9216\n\t" \
  "v_mfma_f32_16x16x32_bf16 a[64:67], a[144:147], v[16:19], 0\n\t" \
  "ds_read_b64_tr_b16 a[180:181], v189 offset:8192\n\t" \
  "v_mfma_f32_16x16x32_bf16 a[68:71], a[144:147], v[48:51], 0\n\t" \
  "buffer_load_dwordx4 v213, s[4:7], s96 offen offset:1024 lds\n\t" \
  "v_mfma_f32_16x16x32_bf16 a[72:75], a[144:147], v[80:83], 0\n\t" \
  "s_add_u32 m0, s77, 6144\n\t" \
  "ds_read_b64_tr_b16 a[182:183], v189 offset:9216\n\t" \
  "s_xor_b32 s96, s87, 393216\n\t" \
  "v_mfma_f32_16x16x32_bf16 a[76:79], a[144:147], v[112:115], 0\n\t" \
  "ds_read_b64_tr_b16 a[184:185], v190 offset:8192\n\t" \
  "s_add_u32 s96, s96, s89\n\t" \
  "v_mfma_f32_16x16x32_bf16 a[92:95], a[148:151], v[112:115], 0\n\t" \
  "ds_read_b64_tr_b16 a[186:187], v190 offset:9216\n\t" \
  "v_mfma_f32_16x16x32_bf16 a[88:91], a[148:151], v[80:83], 0\n\t" \
  "buffer_load_dwordx4 v212, s[4:7], s96 offen lds\n\t" \
  "v_mfma_f32_16x16x32_bf16 a[84:87], a[148:151], v[48:51], 0\n\t" \
  "ds_read_b64_tr_b16 a[188:189], v191 offset:8192\n\t" \
  "v_mfma_f32_16x16x32_bf16 a[80:83], a[148:151], v[16:19], 0\n\t" \
  "ds_read_b64_tr_b16 a[190:191], v191 offset:9216\n\t" \
  "v_mfma_f32_16x16x32_bf16 a[96:99], a[152:155], v[16:19], 0\n\t" \
  "v_mfma_f32_16x16x32_bf16 a[100:103], a[152:155], v[48:51], 0\n\t" \
  "buffer_load_dwordx4 v213, s[4:7], s96 offen offset:1024 lds\n\t" \
  "v_mfma_f32_16x16x32_bf16 a[104:107], a[152:155], v[80:83], 0\n\t" \
  "v_mfma_f32_16x16x32_bf16 a[108:111], a[152:155], v[112:115], 0\n\t" \
  "v_mfma_f32_16x16x32_bf16 a[124:127], a[156:159], v[112:115], 0\n\t" \
  "v_mfma_f32_16x16x32_bf16 a[120:123], a[156:159], v[80:83], 0\n\t" \
  "s_add_u32 m0, s76, 0\n\t" \
  "v_mfma_f32_16x16x32_bf16 a[116:119], a[156:159], v[48:51], 0\n\t" \
  "s_waitcnt lgkmcnt(0)\n\t" \
  "v_mfma_f32_16x16x32_bf16 a[112:115], a[156:159], v[16:19], 0\n\t" \
  "s_waitcnt vmcnt(13)\n\t" \
  "ds_read_b128 v[16:19], v206 offset:0\n\t" \
  "ds_read_b128 v[48:51], v206 offset:2048\n\t" \
  "s_barrier\n\t" \
  FSTP("v_writelane_b32 v255, s94, 36\n\t") \
  FSTP("v_writelane_b32 v255, s92, 37\n\t") \
  FSTP("s_memrealtime s[94:95]\n\t") \
  FSTP("s_memtime s[92:93]\n\t") \
  "v_mfma_f32_16x16x32_bf16 v[0:3], a[160:163], v[20:23], v[0:3]\n\t" \
  "buffer_load_dwordx4 v200, s[20:23], s81 offen offset:0 lds\n\t" \
  "ds_read_b128 v[80:83], v206 offset:4096\n\t" \
  "ds_read_b128 v[112:115], v206 offset:6144\n\t" \
  "v_mfma_f32_16x16x32_bf16 v[32:35], a[160:163], v[52:55], v[32:35]\n\t" \
  "ds_read_b64_tr_b16 a[128:129], v184 offset:49152\n\t" \
  "v_mfma_f32_16x16x32_bf16 v[64:67], a[160:163], v[84:87], v[64:67]\n\t" \
  "ds_read_b64_tr_b16 a[130:131], v184 offset:50176\n\t" \
  "v_mfma_f32_16x16x32_bf16 v[96:99], a[160:163], v[116:119], v[96:99]\n\t" \
  "buffer_load_dwordx4 v201, s[20:23], s81 offen offset:1024 lds\n\t" \
  "v_mfma_f32_16x16x32_bf16 v[100:103], a[164:167], v[116:119], v[100:103]\n\t" \
  "ds_read_b64_tr_b16 a[132:133], v185 offset:49152\n\t" \
  "v_mfma_f32_16x16x32_bf16 v[68:71], a[164:167], v[84:87], v[68:71]\n\t" \
  "s_waitcnt lgkmcnt(3)\n\t" \
  "v_lshlrev_b32 v176, 16, v16\n\t" \
  "v_and_b32 v177, s98, v16\n\t" \
  "v_mfma_f32_16x16x32_bf16 v[36:39], a[164:167], v[52:55], v[36:39]\n\t" \
  "ds_read_b64_tr_b16 a[134:135], v185 offset:50176\n\t" \
  "v_lshlrev_b32 v178, 16, v17\n\t" \
  "v_mfma_f32_16x16x32_bf16 v[4:7], a[164:167], v[20:23], v[4:7]\n\t" \
  "v_and_b32 v179, s98, v17\n\t" \
  "v_lshlrev_b32 v180, 16, v18\n\t" \
  "v_mfma_f32_16x16x32_bf16 v[8:11], a[168:171], v[20:23], v[8:11]\n\t" \
  "buffer_load_dwordx4 v202, s[20:23], s81 offen offset:2048 lds\n\t" \
  "v_mfma_f32_16x16x32_bf16 v[40:43], a[168:171], v[52:55], v[40:43]\n\t" \
  "ds_read_b64_tr_b16 a[136:137], v186 offset:49152\n\t" \
  "v_and_b32 v181, s98, v18\n\t" \
  "v_mfma_f32_16x16x32_bf16 v[72:75], a[168:171], v[84:87], v[72:75]\n\t" \
  "ds_read_b64_tr_b16 a[138:139], v186 offset:50176\n\t" \
  "v_lshlrev_b32 v182, 16, v19\n\t" \
  "v_mfma_f32_16x16x32_bf16 v[104:107], a[168:171], v[116:119], v[104:107]\n\t" \
  "ds_read_b64_tr_b16 a[140:141], v187 offset:49152\n\t" \
  "v_and_b32 v183, s98, v19\n\t" \
  "v_mfma_f32_16x16x32_bf16 v[108:111], a[172:175], v[116:119], v[108:111]\n\t" \
  "buffer_load_dwordx4 v203, s[20:23], s81 offen offset:3072 lds\n\t" \
  "v_mfma_f32_16x16x32_bf16 v[76:79], a[172:175], v[84:87], v[76:79]\n\t" \
  "s_add_u32 s96, s78, 2097152\n\t" \
  "s_and_b32 s96, s96, 0x780000\n\t" \
  "s_add_u32 m0, s76, 16384\n\t" \
  "v_mfma_f32_16x16x32_bf16 v[44:47], a[172:175], v[52:55], v[44:47]\n\t" \
  "ds_read_b64_tr_b16 a[142:143], v187 offset:50176\n\t" \
  "v_lshlrev_b32 v220, 16, v48\n\t" \
  "v_and_b32 v221, s98, v48\n\t" \
  "v_mfma_f32_16x16x32_bf16 v[12:15], a[172:175], v[20:23], v[12:15]\n\t" \
  "buffer_load_dwordx4 v200, s[20:23], s96 offen offset:0 lds\n\t" \
  "v_mfma_f32_16x16x32_bf16 a[64:67], a[176:179], v[20:23], a[64:67]\n\t" \
  "ds_read_b64_tr_b16 a[144:145], v188 offset:49152\n\t" \
  "v_lshlrev_b32 v222, 16, v49\n\t" \
  "v_mfma_f32_16x16x32_bf16 a[68:71], a[176:179], v[52:55], a[68:71]\n\t" \
  "ds_read_b64_tr_b16 a[146:147], v188 offset:50176\n\t" \
  "v_and_b32 v223, s98, v49\n\t" \
  "v_mfma_f32_16x16x32_bf16 a[72:75], a[176:179], v[84:87], a[72:75]\n\t" \
  "ds_read_b64_tr_b16 a[148:149], v189 offset:49152\n\t" \
  "v_lshlrev_b32 v224, 16, v50\n\t" \
  "v_mfma_f32_16x16x32_bf16 a[76:79], a[176:179], v[116:119], a[76:79]\n\t" \
  "buffer_load_dwordx4 v201, s[20:23], s96 offen offset:1024 lds\n\t" \
  "v_mfma_f32_16x16x32_bf16 a[92:95], a[180:183], v[116:119], a[92:95]\n\t" \
  "ds_read_b64_tr_b16 a[150:151], v189 offset:50176\n\t" \
  "v_and_b32 v225, s98, v50\n\t" \
  "v_mfma_f32_16x16x32_bf16 a[88:91], a[180:183], v[84:87], a[88:91]\n\t" \
  "ds_read_b64_tr_b16 a[152:153], v190 offset:49152\n\t" \
  "v_lshlrev_b32 v226, 16, v51\n\t" \
  "v_mfma_f32_16x16x32_bf16 a[84:87], a[180:183], v[52:55], a[84:87]\n\t" \
  "ds_read_b64_tr_b16 a[154:155], v190 offset:50176\n\t" \
  "v_and_b32 v227, s98, v51\n\t" \
  "v_mfma_f32_16x16x32_bf16 a[80:83], a[180:183], v[20:23], a[80:83]\n\t" \
  "buffer_load_dwordx4 v202, s[20:23], s96 offen offset:2048 lds\n\t" \
  "ds_read_b64_tr_b16 a[156:157], v191 offset:49152\n\t" \
  "ds_read_b64_tr_b16 a[158:159], v191 offset:50176\n\t" \
  "v_lshlrev_b32 v228, 16, v80\n\t" \
  "v_pk_fma_f32 v[176:177], v[176:177], v[160:161], v[162:163] op_sel_hi:[1,0,0]\n\t" \
  "v_pk_fma_f32 v[178:179], v[178:179], v[160:161], v[162:163] op_sel_hi:[1,0,0]\n\t" \
  "v_pk_fma_f32 v[180:181], v[180:181], v[160:161], v[162:163] op_sel_hi:[1,0,0]\n\t" \
  "v_pk_fma_f32 v[182:183], v[182:183], v[160:161], v[162:163] op_sel_hi:[1,0,0]\n\t" \
  "v_pk_fma_f32 v[176:177], v[176:177], v[128:129], v[136:137]\n\t" \
  "v_pk_fma_f32 v[178:179], v[178:179], v[130:131], v[138:139]\n\t" \
  "v_pk_fma_f32 v[180:181], v[180:181], v[132:133], v[140:141]\n\t" \
  "v_pk_fma_f32 v[182:183], v[182:183], v[134:135], v[142:143]\n\t" \
  "v_pk_fma_f32 v[220:221], v[220:221], v[164:165], v[166:167] op_sel_hi:[1,0,0]\n\t" \
  "v_pk_fma_f32 v[222:223], v[222:223], v[164:165], v[166:167] op_sel_hi:[1,0,0]\n\t" \
  "v_pk_fma_f32 v[224:225], v[224:225], v[164:165], v[166:167] op_sel_hi:[1,0,0]\n\t" \
  "v_pk_fma_f32 v[226:227], v[226:227], v[164:165], v[166:167] op_sel_hi:[1,0,0]\n\t" \
  "v_pk_fma_f32 v[220:221], v[220:221], v[128:129], v[136:137]\n\t" \
  "v_pk_fma_f32 v[222:223], v[222:223], v[130:131], v[138:139]\n\t" \
  "v_pk_fma_f32 v[224:225], v[224:225], v[132:133], v[140:141]\n\t" \
  "v_pk_fma_f32 v[226:227], v[226:227], v[134:135], v[142:143]\n\t" \
  "v_cvt_pk_bf16_f32 v16, v176, v177\n\t" \
  "v_mfma_f32_16x16x32_bf16 a[96:99], a[184:187], v[20:23], a[96:99]\n\t" \
  "v_cvt_pk_bf16_f32 v17, v178, v179\n\t" \
  "v_cvt_pk_bf16_f32 v18, v180, v181\n\t" \
  "v_and_b32 v229, s98, v80\n\t" \
  "v_mfma_f32_16x16x32_bf16 a[100:103], a[184:187], v[52:55], a[100:103]\n\t" \
  "v_cvt_pk_bf16_f32 v19, v182, v183\n\t" \
  "v_cvt_pk_bf16_f32 v48, v220, v221\n\t" \
  "v_lshlrev_b32 v230, 16, v81\n\t" \
  "v_and_b32 v231, s98, v81\n\t" \
  "v_mfma_f32_16x16x32_bf16 a[104:107], a[184:187], v[84:87], a[104:107]\n\t" \
  "v_lshlrev_b32 v232, 16, v82\n\t" \
  "v_and_b32 v233, s98, v82\n\t" \
  "v_mfma_f32_16x16x32_bf16 a[108:111], a[184:187], v[116:119], a[108:111]\n\t" \
  "buffer_load_dwordx4 v203, s[20:23], s96 offen offset:3072 lds\n\t" \
  "v_mfma_f32_16x16x32_bf16 a[124:127], a[188:191], v[116:119], a[124:127]\n\t" \
  "v_lshlrev_b32 v234, 16, v83\n\t" \
  "v_and_b32 v235, s98, v83\n\t" \
  "v_mfma_f32_16x16x32_bf16 a[120:123], a[188:191], v[84:87], a[120:123]\n\t" \
  "s_add_u32 s96, s78, 2621440\n\t" \
  "v_lshlrev_b32 v236, 16, v112\n\t" \
  "v_and_b32 v237, s98, v112\n\t" \
  "v_mfma_f32_16x16x32_bf16 a[116:119], a[188:191], v[52:55], a[116:119]\n\t" \
  "s_and_b32 s96, s96, 0x780000\n\t" \
  "s_add_u32 m0, s76, 32768\n\t" \
  "v_lshlrev_b32 v238, 16, v113\n\t" \
  "v_mfma_f32_16x16x32_bf16 a[112:115], a[188:191], v[20:23], a[112:115]\n\t" \
  "s_waitcnt lgkmcnt(0)\n\t" \
  "ds_read_b128 v[20:23], v207 offset:0\n\t" \
  "ds_read_b128 v[52:55], v207 offset:2048\n\t" \
  "ds_read_b128 v[84:87], v207 offset:4096\n\t" \
  "v_and_b32 v239, s98, v113\n\t" \
  FSTP("v_writelane_b32 v255, s94, 38\n\t") \
  FSTP("v_writelane_b32 v255, s92, 39\n\t") \
  FSTP("s_memrealtime s[94:95]\n\t") \
  FSTP("s_memtime s[92:93]\n\t") \
  "v_mfma_f32_16x16x32_bf16 v[0:3], a[128:131], v[24:27], v[0:3]\n\t" \
  "buffer_load_dwordx4 v200, s[20:23], s96 offen offset:0 lds\n\t" \
  "ds_read_b128 v[116:119], v207 offset:6144\n\t" \
  "v_mfma_f32_16x16x32_bf16 v[32:35], a[128:131], v[56:59], v[32:35]\n\t" \
  "v_lshlrev_b32 v240, 16, v114\n\t" \
  "v_and_b32 v241, s98, v114\n\t" \
  "v_mfma_f32_16x16x32_bf16 v[64:67], a[128:131], v[88:91], v[64:67]\n\t" \
  "v_lshlrev_b32 v242, 16, v115\n\t" \
  "v_and_b32 v243, s98, v115\n\t" \
  "v_mfma_f32_16x16x32_bf16 v[96:99], a[128:131], v[120:123], v[96:99]\n\t" \
  "ds_read_b64_tr_b16 a[160:161], v184 offset:57344\n\t" \
  "ds_read_b64_tr_b16 a[162:163], v184 offset:58368\n\t" \
  "v_cvt_pk_bf16_f32 v49, v222, v223\n\t" \
  "v_cvt_pk_bf16_f32 v50, v224, v225\n\t" \
  "v_cvt_pk_bf16_f32 v51, v226, v227\n\t" \
  "v_pk_fma_f32 v[228:229], v[228:229], v[168:169], v[170:171] op_sel_hi:[1,0,0]\n\t" \
  "v_pk_fma_f32 v[230:231], v[230:231], v[168:169], v[170:171] op_sel_hi:[1,0,0]\n\t" \
  "v_pk_fma_f32 v[232:233], v[232:233], v[168:169], v[170:171] op_sel_hi:[1,0,0]\n\t" \
  "v_pk_fma_f32 v[234:235], v[234:235], v[168:169], v[170:171] op_sel_hi:[1,0,0]\n\t" \
  "v_pk_fma_f32 v[228:229], v[228:229], v[128:129], v[136:137]\n\t" \
  "v_pk_fma_f32 v[230:231], v[230:231], v[130:131], v[138:139]\n\t" \
  "v_pk_fma_f32 v[232:233], v[232:233], v[132:133], v[140:141]\n\t" \
  "v_pk_fma_f32 v[234:235], v[234:235], v[134:135], v[142:143]\n\t" \
  "v_pk_fma_f32 v[236:237], v[236:237], v[172:173], v[174:175] op_sel_hi:[1,0,0]\n\t" \
  "v_pk_fma_f32 v[238:239], v[238:239], v[172:173], v[174:175] op_sel_hi:[1,0,0]\n\t" \
  "v_pk_fma_f32 v[240:241], v[240:241], v[172:173], v[174:175] op_sel_hi:[1,0,0]\n\t" \
  "v_pk_fma_f32 v[242:243], v[242:243], v[172:173], v[174:175] op_sel_hi:[1,0,0]\n\t" \
  "v_pk_fma_f32 v[236:237], v[236:237], v[128:129], v[136:137]\n\t" \
  "v_pk_fma_f32 v[238:239], v[238:239], v[130:131], v[138:139]\n\t" \
  "v_pk_fma_f32 v[240:241], v[240:241], v[132:133], v[140:141]\n\t" \
  "v_pk_fma_f32 v[242:243], v[242:243], v[134:135], v[142:143]\n\t" \
  "v_cvt_pk_bf16_f32 v80, v228, v229\n\t" \
  "v_mfma_f32_16x16x32_bf16 v[100:103], a[132:135], v[120:123], v[100:103]\n\t" \
  "ds_read_b64_tr_b16 a[164:165], v185 offset:57344\n\t" \
  "v_cvt_pk_bf16_f32 v81, v230, v231\n\t" \
  "v_mfma_f32_16x16x32_bf16 v[68:71], a[132:135], v[88:91], v[68:71]\n\t" \
  "s_waitcnt lgkmcnt(3)\n\t" \
  "v_lshlrev_b32 v176, 16, v20\n\t" \
  "v_and_b32 v177, s98, v20\n\t" \
  "v_mfma_f32_16x16x32_bf16 v[36:39], a[132:135], v[56:59], v[36:39]\n\t" \
  "buffer_load_dwordx4 v201, s[20:23], s96 offen offset:1024 lds\n\t" \
  "v_mfma_f32_16x16x32_bf16 v[4:7], a[132:135], v[24:27], v[4:7]\n\t" \
  "ds_read_b64_tr_b16 a[166:167], v185 offset:58368\n\t" \
  "v_lshlrev_b32 v178, 16, v21\n\t" \
  "v_mfma_f32_16x16x32_bf16 v[8:11], a[136:139], v[24:27], v[8:11]\n\t" \
  "ds_read_b64_tr_b16 a[168:169], v186 offset:57344\n\t" \
  "v_and_b32 v179, s98, v21\n\t" \
  "v_mfma_f32_16x16x32_bf16 v[40:43], a[136:139], v[56:59], v[40:43]\n\t" \
  "v_lshlrev_b32 v180, 16, v22\n\t" \
  "v_and_b32 v181, s98, v22\n\t" \
  "v_mfma_f32_16x16x32_bf16 v[72:75], a[136:139], v[88:91], v[72:75]\n\t" \
  "ds_read_b64_tr_b16 a[170:171], v186 offset:58368\n\t" \
  "v_lshlrev_b32 v182, 16, v23\n\t" \
  "v_mfma_f32_16x16x32_bf16 v[104:107], a[136:139], v[120:123], v[104:107]\n\t" \
  "ds_read_b64_tr_b16 a[172:173], v187 offset:57344\n\t" \
  "v_and_b32 v183, s98, v23\n\t" \
  "v_mfma_f32_16x16x32_bf16 v[108:111], a[140:143], v[120:123], v[108:111]\n\t" \
  "buffer_load_dwordx4 v202, s[20:23], s96 offen offset:2048 lds\n\t" \
  "v_mfma_f32_16x16x32_bf16 v[76:79], a[140:143], v[88:91], v[76:79]\n\t" \
  "ds_read_b64_tr_b16 a[174:175], v187 offset:58368\n\t" \
  "v_lshlrev_b32 v220, 16, v52\n\t" \
  "v_mfma_f32_16x16x32_bf16 v[44:47], a[140:143], v[56:59], v[44:47]\n\t" \
  "v_and_b32 v221, s98, v52\n\t" \
  "v_lshlrev_b32 v222, 16, v53\n\t" \
  "v_mfma_f32_16x16x32_bf16 v[12:15], a[140:143], v[24:27], v[12:15]\n\t" \
  "ds_read_b64_tr_b16 a[176:177], v188 offset:57344\n\t" \
  "v_and_b32 v223, s98, v53\n\t" \
  "v_mfma_f32_16x16x32_bf16 a[64:67], a[144:147], v[24:27], a[64:67]\n\t" \
  "ds_read_b64_tr_b16 a[178:179], v188 offset:58368\n\t" \
  "v_lshlrev_b32 v224, 16, v54\n\t" \
  "v_mfma_f32_16x16x32_bf16 a[68:71], a[144:147], v[56:59], a[68:71]\n\t" \
  "ds_read_b64_tr_b16 a[180:181], v189 offset:57344\n\t" \
  "v_and_b32 v225, s98, v54\n\t" \
  "v_mfma_f32_16x16x32_bf16 a[72:75], a[144:147], v[88:91], a[72:75]\n\t" \
  "buffer_load_dwordx4 v203, s[20:23], s96 offen offset:3072 lds\n\t" \
  "v_mfma_f32_16x16x32_bf16 a[76:79], a[144:147], v[120:123], a[76:79]\n\t" \
  "ds_read_b64_tr_b16 a[182:183], v189 offset:58368\n\t" \
  "v_cvt_pk_bf16_f32 v82, v232, v233\n\t" \
  "v_mfma_f32_16x16x32_bf16 a[92:95], a[148:151], v[120:123], a[92:95]\n\t" \
  "v_lshlrev_b32 v226, 16, v55\n\t" \
  "v_and_b32 v227, s98, v55\n\t" \
  "v_mfma_f32_16x16x32_bf16 a[88:91], a[148:151], v[88:91], a[88:91]\n\t" \
  "ds_read_b64_tr_b16 a[184:185], v190 offset:57344\n\t" \
  "v_cvt_pk_bf16_f32 v83, v234, v235\n\t" \
  "v_cvt_pk_bf16_f32 v112, v236, v237\n\t" \
  "v_cvt_pk_bf16_f32 v113, v238, v239\n\t" \
  "v_pk_fma_f32 v[176:177], v[176:177], v[160:161], v[162:163] op_sel_hi:[1,0,0]\n\t" \
  "v_pk_fma_f32 v[178:179], v[178:179], v[160:161], v[162:163] op_sel_hi:[1,0,0]\n\t" \
  "v_pk_fma_f32 v[180:181], v[180:181], v[160:161], v[162:163] op_sel_hi:[1,0,0]\n\t" \
  "v_pk_fma_f32 v[182:183], v[182:183], v[160:161], v[162:163] op_sel_hi:[1,0,0]\n\t" \
  "v_pk_fma_f32 v[176:177], v[176:177], v[144:145], v[152:153]\n\t" \
  "v_pk_fma_f32 v[178:179], v[178:179], v[146:147], v[154:155]\n\t" \
  "v_pk_fma_f32 v[180:181], v[180:181], v[148:149], v[156:157]\n\t" \
  "v_pk_fma_f32 v[182:183], v[182:183], v[150:151], v[158:159]\n\t" \
  "v_pk_fma_f32 v[220:221], v[220:221], v[164:165], v[166:167] op_sel_hi:[1,0,0]\n\t" \
  "v_pk_fma_f32 v[222:223], v[222:223], v[164:165], v[166:167] op_sel_hi:[1,0,0]\n\t" \
  "v_pk_fma_f32 v[224:225], v[224:225], v[164:165], v[166:167] op_sel_hi:[1,0,0]\n\t" \
  "v_pk_fma_f32 v[226:227], v[226:227], v[164:165], v[166:167] op_sel_hi:[1,0,0]\n\t" \
  "v_pk_fma_f32 v[220:221], v[220:221], v[144:145], v[152:153]\n\t" \
  "v_pk_fma_f32 v[222:223], v[222:223], v[146:147], v[154:155]\n\t" \
  "v_pk_fma_f32 v[224:225], v[224:225], v[148:149], v[156:157]\n\t" \
  "v_pk_fma_f32 v[226:227], v[226:227], v[150:151], v[158:159]\n\t" \
  "v_cvt_pk_bf16_f32 v20, v176, v177\n\t" \
  "v_mfma_f32_16x16x32_bf16 a[84:87], a[148:151], v[56:59], a[84:87]\n\t" \
  "ds_read_b64_tr_b16 a[186:187], v190 offset:58368\n\t" \
  "v_lshlrev_b32 v228, 16, v84\n\t" \
  "v_mfma_f32_16x16x32_bf16 a[80:83], a[148:151], v[24:27], a[80:83]\n\t" \
  "ds_read_b64_tr_b16 a[188:189], v191 offset:57344\n\t" \
  "v_and_b32 v229, s98, v84\n\t" \
  "v_mfma_f32_16x16x32_bf16 a[96:99], a[152:155], v[24:27], a[96:99]\n\t" \
  "global_load_dword v246, v217, s[12:13]\n\t" \
  "v_mfma_f32_16x16x32_bf16 a[100:103], a[152:155], v[56:59], a[100:103]\n\t" \
  "ds_read_b64_tr_b16 a[190:191], v191 offset:58368\n\t" \
  "v_lshlrev_b32 v230, 16, v85\n\t" \
  "v_mfma_f32_16x16x32_bf16 a[104:107], a[152:155], v[88:91], a[104:107]\n\t" \
  "v_and_b32 v231, s98, v85\n\t" \
  "v_lshlrev_b32 v232, 16, v86\n\t" \
  "v_mfma_f32_16x16x32_bf16 a[108:111], a[152:155], v[120:123], a[108:111]\n\t" \
  ".Lhs1:\n\ts_cbranch_execz .Lhs1n\n\t" \
  "v_and_b32 v233, s98, v86\n\t" \
  "v_lshlrev_b32 v234, 16, v87\n\t" \
  "v_mfma_f32_16x16x32_bf16 a[124:127], a[156:159], v[120:123], a[124:127]\n\t" \
  "v_cvt_pk_bf16_f32 v114, v240, v241\n\t" \
  "v_cvt_pk_bf16_f32 v115, v242, v243\n\t" \
  "v_mfma_f32_16x16x32_bf16 a[120:123], a[156:159], v[88:91], a[120:123]\n\t" \
  "s_add_u32 s96, s78, 3145728\n\t" \
  "v_and_b32 v235, s98, v87\n\t" \
  "v_lshlrev_b32 v236, 16, v116\n\t" \
  "v_mfma_f32_16x16x32_bf16 a[116:119], a[156:159], v[56:59], a[116:119]\n\t" \
  "s_waitcnt vmcnt(22) lgkmcnt(0)\n\t" \
  "s_and_b32 s96, s96, 0x780000\n\t" \
  "s_add_u32 m0, s76, 49152\n\t" \
  "v_mfma_f32_16x16x32_bf16 a[112:115], a[156:159], v[24:27], a[112:115]\n\t" \
  "s_barrier\n\t" \
  FSTP("v_writelane_b32 v255, s94, 40\n\t") \
  FSTP("v_writelane_b32 v255, s92, 41\n\t") \
  FSTP("s_memrealtime s[94:95]\n\t") \
  FSTP("s_memtime s[92:93]\n\t") \
  "v_mfma_f32_16x16x32_bf16 v[0:3], a[160:163], v[28:31], v[0:3]\n\t" \
  "buffer_load_dwordx4 v200, s[20:23], s96 offen offset:0 lds\n\t" \
  "ds_read_b64_tr_b16 a[128:129], v192 offset:32768\n\t" \
  "v_mfma_f32_16x16x32_bf16 v[32:35], a[160:163], v[60:63], v[32:35]\n\t" \
  "s_waitcnt vmcnt(14)\n\t" \
  "ds_read_b128 v[24:27], v204 offset:0\n\t" \
  "ds_read_b64_tr_b16 a[130:131], v192 offset:33792\n\t" \
  "v_and_b32 v237, s98, v116\n\t" \
  "v_mfma_f32_16x16x32_bf16 v[64:67], a[160:163], v[92:95], v[64:67]\n\t" \
  "ds_read_b128 v[56:59], v204 offset:2048\n\t" \
  "ds_read_b64_tr_b16 a[132:133], v193 offset:32768\n\t" \
  "v_cvt_pk_bf16_f32 v21, v178, v179\n\t" \
  "v_mfma_f32_16x16x32_bf16 v[96:99], a[160:163], v[124:127], v[96:99]\n\t" \
  "ds_read_b128 v[88:91], v204 offset:4096\n\t" \
  "v_lshlrev_b32 v238, 16, v117\n\t" \
  "v_mfma_f32_16x16x32_bf16 v[100:103], a[164:167], v[124:127], v[100:103]\n\t" \
  "ds_read_b128 v[120:123], v204 offset:6144\n\t" \
  "v_cvt_pk_bf16_f32 v22, v180, v181\n\t" \
  "v_mfma_f32_16x16x32_bf16 v[68:71], a[164:167], v[92:95], v[68:71]\n\t" \
  "buffer_load_dwordx4 v201, s[20:23], s96 offen offset:1024 lds\n\t" \
  "v_mfma_f32_16x16x32_bf16 v[36:39], a[164:167], v[60:63], v[36:39]\n\t" \
  "ds_read_b64_tr_b16 a[134:135], v193 offset:33792\n\t" \
  "v_and_b32 v239, s98, v117\n\t" \
  "v_mfma_f32_16x16x32_bf16 v[4:7], a[164:167], v[28:31], v[4:7]\n\t" \
  "ds_read_b64_tr_b16 a[136:137], v194 offset:32768\n\t" \
  "v_cvt_pk_bf16_f32 v23, v182, v183\n\t" \
  "v_mfma_f32_16x16x32_bf16 v[8:11], a[168:171], v[28:31], v[8:11]\n\t" \
  "buffer_load_dwordx4 v202, s[20:23], s96 offen offset:2048 lds\n\t" \
  "v_mfma_f32_16x16x32_bf16 v[40:43], a[168:171], v[60:63], v[40:43]\n\t" \
  "ds_read_b64_tr_b16 a[138:139], v194 offset:33792\n\t" \
  "v_lshlrev_b32 v240, 16, v118\n\t" \
  "v_mfma_f32_16x16x32_bf16 v[72:75], a[168:171], v[92:95], v[72:75]\n\t" \
  "ds_read_b64_tr_b16 a[140:141], v195 offset:32768\n\t" \
  "v_and_b32 v241, s98, v118\n\t" \
  "v_mfma_f32_16x16x32_bf16 v[104:107], a[168:171], v[124:127], v[104:107]\n\t" \
  "v_lshlrev_b32 v242, 16, v119\n\t" \
  "v_and_b32 v243, s98, v119\n\t" \
  "v_mfma_f32_16x16x32_bf16 v[108:111], a[172:175], v[124:127], v[108:111]\n\t" \
  "buffer_load_dwordx4 v203, s[20:23], s96 offen offset:3072 lds\n\t" \
  "ds_read_b64_tr_b16 a[142:143], v195 offset:33792\n\t" \
  "ds_read_b128 v[128:131], v208 offset:1536\n\t" \
  "v_cvt_pk_bf16_f32 v52, v220, v221\n\t" \
  "v_cvt_pk_bf16_f32 v53, v222, v223\n\t" \
  "v_pk_fma_f32 v[228:229], v[228:229], v[168:169], v[170:171] op_sel_hi:[1,0,0]\n\t" \
  "v_pk_fma_f32 v[230:231], v[230:231], v[168:169], v[170:171] op_sel_hi:[1,0,0]\n\t" \
  "v_pk_fma_f32 v[232:233], v[232:233], v[168:169], v[170:171] op_sel_hi:[1,0,0]\n\t" \
  "v_pk_fma_f32 v[234:235], v[234:235], v[168:169], v[170:171] op_sel_hi:[1,0,0]\n\t" \
  "v_pk_fma_f32 v[228:229], v[228:229], v[144:145], v[152:153]\n\t" \
  "v_pk_fma_f32 v[230:231], v[230:231], v[146:147], v[154:155]\n\t" \
  "v_pk_fma_f32 v[232:233], v[232:233], v[148:149], v[156:157]\n\t" \
  "v_pk_fma_f32 v[234:235], v[234:235], v[150:151], v[158:159]\n\t" \
  "v_pk_fma_f32 v[236:237], v[236:237], v[172:173], v[174:175] op_sel_hi:[1,0,0]\n\t" \
  "v_pk_fma_f32 v[238:239], v[238:239], v[172:173], v[174:175] op_sel_hi:[1,0,0]\n\t" \
  "v_pk_fma_f32 v[240:241], v[240:241], v[172:173], v[174:175] op_sel_hi:[1,0,0]\n\t" \
  "v_pk_fma_f32 v[242:243], v[242:243], v[172:173], v[174:175] op_sel_hi:[1,0,0]\n\t" \
  "v_pk_fma_f32 v[236:237], v[236:237], v[144:145], v[152:153]\n\t" \
  "v_pk_fma_f32 v[238:239], v[238:239], v[146:147], v[154:155]\n\t" \
  "v_pk_fma_f32 v[240:241], v[240:241], v[148:149], v[156:157]\n\t" \
  "v_pk_fma_f32 v[242:243], v[242:243], v[150:151], v[158:159]\n\t" \
  "v_cvt_pk_bf16_f32 v84, v228, v229\n\t" \
  "v_mfma_f32_16x16x32_bf16 v[76:79], a[172:175], v[92:95], v[76:79]\n\t" \
  "s_add_u32 s96, s78, 3670016\n\t" \
  "s_and_b32 s96, s96, 0x780000\n\t" \
  "s_add_u32 m0, s76, 65536\n\t" \
  "v_mfma_f32_16x16x32_bf16 v[44:47], a[172:175], v[60:63], v[44:47]\n\t" \
  "ds_read_b64_tr_b16 a[144:145], v196 offset:32768\n\t" \
  "v_cvt_pk_bf16_f32 v54, v224, v225\n\t" \
  "v_mfma_f32_16x16x32_bf16 v[12:15], a[172:175], v[28:31], v[12:15]\n\t" \
  "buffer_load_dwordx4 v200, s[20:23], s96 offen offset:0 lds\n\t" \
  "v_mfma_f32_16x16x32_bf16 a[64:67], a[176:179], v[28:31], a[64:67]\n\t" \
  "s_waitcnt lgkmcnt(7)\n\t" \
  "v_lshlrev_b32 v176, 16, v24\n\t" \
  "v_and_b32 v177, s98, v24\n\t" \
  "v_mfma_f32_16x16x32_bf16 a[68:71], a[176:179], v[60:63], a[68:71]\n\t" \
  "ds_read_b64_tr_b16 a[146:147], v196 offset:33792\n\t" \
  "v_lshlrev_b32 v178, 16, v25\n\t" \
  "v_mfma_f32_16x16x32_bf16 a[72:75], a[176:179], v[92:95], a[72:75]\n\t" \
  "ds_read_b64_tr_b16 a[148:149], v197 offset:32768\n\t" \
  "v_and_b32 v179, s98, v25\n\t" \
  "v_mfma_f32_16x16x32_bf16 a[76:79], a[176:179], v[124:127], a[76:79]\n\t" \
  "buffer_load_dwordx4 v201, s[20:23], s96 offen offset:1024 lds\n\t" \
  "v_mfma_f32_16x16x32_bf16 a[92:95], a[180:183], v[124:127], a[92:95]\n\t" \
  "ds_read_b64_tr_b16 a[150:151], v197 offset:33792\n\t" \
  "v_lshlrev_b32 v180, 16, v26\n\t" \
  "v_mfma_f32_16x16x32_bf16 a[88:91], a[180:183], v[92:95], a[88:91]\n\t" \
  "ds_read_b64_tr_b16 a[152:153], v198 offset:32768\n\t" \
  "v_and_b32 v181, s98, v26\n\t" \
  "v_mfma_f32_16x16x32_bf16 a[84:87], a[180:183], v[60:63], a[84:87]\n\t" \
  "ds_read_b64_tr_b16 a[154:155], v198 offset:33792\n\t" \
  "v_lshlrev_b32 v182, 16, v27\n\t" \
  "v_mfma_f32_16x16x32_bf16 a[80:83], a[180:183], v[28:31], a[80:83]\n\t" \
  "buffer_load_dwordx4 v202, s[20:23], s96 offen offset:2048 lds\n\t" \
  "v_mfma_f32_16x16x32_bf16 a[96:99], a[184:187], v[28:31], a[96:99]\n\t" \
  "ds_read_b64_tr_b16 a[156:157], v199 offset:32768\n\t" \
  "v_cvt_pk_bf16_f32 v55, v226, v227\n\t" \
  "v_mfma_f32_16x16x32_bf16 a[100:103], a[184:187], v[60:63], a[100:103]\n\t" \
  "ds_read_b64_tr_b16 a[158:159], v199 offset:33792\n\t" \
  "v_and_b32 v183, s98, v27\n\t" \
  "v_mfma_f32_16x16x32_bf16 a[104:107], a[184:187], v[92:95], a[104:107]\n\t" \
  "v_lshlrev_b32 v220, 16, v56\n\t" \
  "v_and_b32 v221, s98, v56\n\t" \
  "v_mfma_f32_16x16x32_bf16 a[108:111], a[184:187], v[124:127], a[108:111]\n\t" \
  "buffer_load_dwordx4 v203, s[20:23], s96 offen offset:3072 lds\n\t" \
  "v_mfma_f32_16x16x32_bf16 a[124:127], a[188:191], v[124:127], a[124:127]\n\t" \
  "v_lshlrev_b32 v222, 16, v57\n\t" \
  "v_and_b32 v223, s98, v57\n\t" \
  "v_mfma_f32_16x16x32_bf16 a[120:123], a[188:191], v[92:95], a[120:123]\n\t" \
  "v_lshlrev_b32 v224, 16, v58\n\t" \
  "v_cvt_pk_bf16_f32 v85, v230, v231\n\t" \
  "v_mfma_f32_16x16x32_bf16 a[116:119], a[188:191], v[60:63], a[116:119]\n\t" \
  "v_and_b32 v225, s98, v58\n\t" \
  "v_lshlrev_b32 v226, 16, v59\n\t" \
  "v_mfma_f32_16x16x32_bf16 a[112:115], a[188:191], v[28:31], a[112:115]\n\t" \
  "s_waitcnt lgkmcnt(0)\n\t" \
  "v_cvt_pk_bf16_f32 v86, v232, v233\n\t" \
  "v_cvt_pk_bf16_f32 v87, v234, v235\n\t" \
  FSTP("v_writelane_b32 v255, s94, 42\n\t") \
  FSTP("v_writelane_b32 v255, s92, 43\n\t") \
  FSTP("s_memrealtime s[94:95]\n\t") \
  FSTP("s_memtime s[92:93]\n\t") \
  "v_mfma_f32_16x16x32_bf16 v[0:3], a[128:131], v[16:19], v[0:3]\n\t" \
  "buffer_load_dword v252, v210, s[16:19], s84 offen sc1\n\t" \
  "v_mfma_f32_16x16x32_bf16 v[32:35], a[128:131], v[48:51], v[32:35]\n\t" \
  "ds_read_b128 v[28:31], v205 offset:0\n\t" \
  "v_and_b32 v227, s98, v59\n\t" \
  "v_mfma_f32_16x16x32_bf16 v[64:67], a[128:131], v[80:83], v[64:67]\n\t" \
  "s_lshr_b32 s99, s87, 9\n\t" \
  "ds_read_b128 v[60:63], v205 offset:2048\n\t" \
  "ds_read_b64_tr_b16 a[160:161], v192 offset:40960\n\t" \
  "v_lshlrev_b32 v228, 16, v88\n\t" \
  "v_mfma_f32_16x16x32_bf16 v[96:99], a[128:131], v[112:115], v[96:99]\n\t" \
  "ds_read_b128 v[92:95], v205 offset:4096\n\t" \
  "ds_read_b64_tr_b16 a[162:163], v192 offset:41984\n\t" \
  "v_xor_b32 v248, s99, v214\n\t" \
  "v_mfma_f32_16x16x32_bf16 v[100:103], a[132:135], v[112:115], v[100:103]\n\t" \
  "v_xor_b32 v249, 256, v248\n\t" \
  "v_xor_b32 v250, 512, v248\n\t" \
  "v_mfma_f32_16x16x32_bf16 v[68:71], a[132:135], v[80:83], v[68:71]\n\t" \
  "v_xor_b32 v251, 768, v248\n\t" \
  "s_add_u32 s96, s78, 4194304\n\t" \
  "v_mfma_f32_16x16x32_bf16 v[36:39], a[132:135], v[48:51], v[36:39]\n\t" \
  "s_and_b32 s96, s96, 0x780000\n\t" \
  "ds_read_b128 v[124:127], v205 offset:6144\n\t" \
  "ds_read_b64_tr_b16 a[164:165], v193 offset:40960\n\t" \
  "s_add_u32 m0, s76, 81920\n\t" \
  "v_mfma_f32_16x16x32_bf16 v[4:7], a[132:135], v[16:19], v[4:7]\n\t" \
  "buffer_load_dwordx4 v200, s[20:23], s96 offen offset:0 lds\n\t" \
  "v_mfma_f32_16x16x32_bf16 v[8:11], a[136:139], v[16:19], v[8:11]\n\t" \
  "ds_read_b64_tr_b16 a[166:167], v193 offset:41984\n\t" \
  "v_and_b32 v229, s98, v88\n\t" \
  "v_mfma_f32_16x16x32_bf16 v[40:43], a[136:139], v[48:51], v[40:43]\n\t" \
  "ds_read_b64_tr_b16 a[168:169], v194 offset:40960\n\t" \
  "v_lshlrev_b32 v230, 16, v89\n\t" \
  "v_mfma_f32_16x16x32_bf16 v[72:75], a[136:139], v[80:83], v[72:75]\n\t" \
  "buffer_load_dwordx4 v201, s[20:23], s96 offen offset:1024 lds\n\t" \
  "v_mfma_f32_16x16x32_bf16 v[104:107], a[136:139], v[112:115], v[104:107]\n\t" \
  "ds_read_b64_tr_b16 a[170:171], v194 offset:41984\n\t" \
  "v_and_b32 v231, s98, v89\n\t" \
  "v_mfma_f32_16x16x32_bf16 v[108:111], a[140:143], v[112:115], v[108:111]\n\t" \
  "ds_read_b64_tr_b16 a[172:173], v195 offset:40960\n\t" \
  "v_cvt_pk_bf16_f32 v116, v236, v237\n\t" \
  "v_mfma_f32_16x16x32_bf16 v[76:79], a[140:143], v[80:83], v[76:79]\n\t" \
  "ds_read_b64_tr_b16 a[174:175], v195 offset:41984\n\t" \
  "v_lshlrev_b32 v232, 16, v90\n\t" \
  "v_mfma_f32_16x16x32_bf16 v[44:47], a[140:143], v[48:51], v[44:47]\n\t" \
  "ds_read_b64_tr_b16 a[176:177], v196 offset:40960\n\t" \
  "v_cvt_pk_bf16_f32 v117, v238, v239\n\t" \
  "v_mfma_f32_16x16x32_bf16 v[12:15], a[140:143], v[16:19], v[12:15]\n\t" \
  "buffer_load_dwordx4 v202, s[20:23], s96 offen offset:2048 lds\n\t" \
  "v_mfma_f32_16x16x32_bf16 a[64:67], a[144:147], v[16:19], a[64:67]\n\t" \
  "ds_read_b64_tr_b16 a[178:179], v196 offset:41984\n\t" \
  "v_and_b32 v233, s98, v90\n\t" \
  "v_mfma_f32_16x16x32_bf16 a[68:71], a[144:147], v[48:51], a[68:71]\n\t" \
  "ds_read_b64_tr_b16 a[180:181], v197 offset:40960\n\t" \
  "v_cvt_pk_bf16_f32 v118, v240, v241\n\t" \
  "v_mfma_f32_16x16x32_bf16 a[72:75], a[144:147], v[80:83], a[72:75]\n\t" \
  "ds_read_b64_tr_b16 a[182:183], v197 offset:41984\n\t" \
  "v_lshlrev_b32 v234, 16, v91\n\t" \
  "v_mfma_f32_16x16x32_bf16 a[76:79], a[144:147], v[112:115], a[76:79]\n\t" \
  "ds_read_b128 v[132:135], v208 offset:1552\n\t" \
  "v_cvt_pk_bf16_f32 v119, v242, v243\n\t" \
  "v_mfma_f32_16x16x32_bf16 a[92:95], a[148:151], v[112:115], a[92:95]\n\t" \
  "buffer_load_dwordx4 v203, s[20:23], s96 offen offset:3072 lds\n\t" \
  "v_mfma_f32_16x16x32_bf16 a[88:91], a[148:151], v[80:83], a[88:91]\n\t" \
  "ds_read_b64_tr_b16 a[184:185], v198 offset:40960\n\t" \
  "v_and_b32 v235, s98, v91\n\t" \
  "v_mfma_f32_16x16x32_bf16 a[84:87], a[148:151], v[48:51], a[84:87]\n\t" \
  "ds_read_b64_tr_b16 a[186:187], v198 offset:41984\n\t" \
  "v_lshlrev_b32 v236, 16, v120\n\t" \
  "v_mfma_f32_16x16x32_bf16 a[80:83], a[148:151], v[16:19], a[80:83]\n\t" \
  "ds_read_b64_tr_b16 a[188:189], v199 offset:40960\n\t" \
  "v_and_b32 v237, s98, v120\n\t" \
  "v_mfma_f32_16x16x32_bf16 a[96:99], a[152:155], v[16:19], a[96:99]\n\t" \
  "ds_read_b128 v[136:139], v208 offset:1792\n\t" \
  "v_lshlrev_b32 v238, 16, v121\n\t" \
  "v_mfma_f32_16x16x32_bf16 a[100:103], a[152:155], v[48:51], a[100:103]\n\t" \
  "global_load_dword v246, v216, s[20:21]\n\t" \
  "v_mfma_f32_16x16x32_bf16 a[104:107], a[152:155], v[80:83], a[104:107]\n\t" \
  "ds_read_b64_tr_b16 a[190:191], v199 offset:41984\n\t" \
  "v_and_b32 v239, s98, v121\n\t" \
  "v_mfma_f32_16x16x32_bf16 a[108:111], a[152:155], v[112:115], a[108:111]\n\t" \
  "ds_read_b128 v[140:143], v208 offset:1808\n\t" \
  "v_lshlrev_b32 v240, 16, v122\n\t" \
  "v_mfma_f32_16x16x32_bf16 a[124:127], a[156:159], v[112:115], a[124:127]\n\t" \
  "ds_read_b128 v[144:147], v208 offset:1664\n\t" \
  "v_and_b32 v241, s98, v122\n\t" \
  "v_lshlrev_b32 v242, 16, v123\n\t" \
  "v_and_b32 v243, s98, v123\n\t" \
  "s_waitcnt lgkmcnt(1)\n\t" \
  "v_pk_fma_f32 v[176:177], v[176:177], v[160:161], v[162:163] op_sel_hi:[1,0,0]\n\t" \
  "v_pk_fma_f32 v[178:179], v[178:179], v[160:161], v[162:163] op_sel_hi:[1,0,0]\n\t" \
  "v_pk_fma_f32 v[180:181], v[180:181], v[160:161], v[162:163] op_sel_hi:[1,0,0]\n\t" \
  "v_pk_fma_f32 v[182:183], v[182:183], v[160:161], v[162:163] op_sel_hi:[1,0,0]\n\t" \
  "v_pk_fma_f32 v[176:177], v[176:177], v[128:129], v[136:137]\n\t" \
  "v_pk_fma_f32 v[178:179], v[178:179], v[130:131], v[138:139]\n\t" \
  "v_pk_fma_f32 v[180:181], v[180:181], v[132:133], v[140:141]\n\t" \
  "v_pk_fma_f32 v[182:183], v[182:183], v[134:135], v[142:143]\n\t" \
  "v_pk_fma_f32 v[220:221], v[220:221], v[164:165], v[166:167] op_sel_hi:[1,0,0]\n\t" \
  "v_pk_fma_f32 v[222:223], v[222:223], v[164:165], v[166:167] op_sel_hi:[1,0,0]\n\t" \
  "v_pk_fma_f32 v[224:225], v[224:225], v[164:165], v[166:167] op_sel_hi:[1,0,0]\n\t" \
  "v_pk_fma_f32 v[226:227], v[226:227], v[164:165], v[166:167] op_sel_hi:[1,0,0]\n\t" \
  "v_pk_fma_f32 v[220:221], v[220:221], v[128:129], v[136:137]\n\t" \
  "v_pk_fma_f32 v[222:223], v[222:223], v[130:131], v[138:139]\n\t" \
  "v_pk_fma_f32 v[224:225], v[224:225], v[132:133], v[140:141]\n\t" \
  "v_pk_fma_f32 v[226:227], v[226:227], v[134:135], v[142:143]\n\t" \
  "v_cvt_pk_bf16_f32 v24, v176, v177\n\t" \
  "v_mfma_f32_16x16x32_bf16 a[120:123], a[156:159], v[80:83], a[120:123]\n\t" \
  "v_cvt_pk_bf16_f32 v25, v178, v179\n\t" \
  "v_cvt_pk_bf16_f32 v26, v180, v181\n\t" \
  "v_cvt_pk_bf16_f32 v27, v182, v183\n\t" \
  "v_cvt_pk_bf16_f32 v56, v220, v221\n\t" \
  "v_cvt_pk_bf16_f32 v57, v222, v223\n\t" \
  "v_pk_fma_f32 v[228:229], v[228:229], v[168:169], v[170:171] op_sel_hi:[1,0,0]\n\t" \
  "v_pk_fma_f32 v[230:231], v[230:231], v[168:169], v[170:171] op_sel_hi:[1,0,0]\n\t" \
  "v_pk_fma_f32 v[232:233], v[232:233], v[168:169], v[170:171] op_sel_hi:[1,0,0]\n\t" \
  "v_pk_fma_f32 v[234:235], v[234:235], v[168:169], v[170:171] op_sel_hi:[1,0,0]\n\t" \
  "v_pk_fma_f32 v[228:229], v[228:229], v[128:129], v[136:137]\n\t" \
  "v_pk_fma_f32 v[230:231], v[230:231], v[130:131], v[138:139]\n\t" \
  "v_pk_fma_f32 v[232:233], v[232:233], v[132:133], v[140:141]\n\t" \
  "v_pk_fma_f32 v[234:235], v[234:235], v[134:135], v[142:143]\n\t" \
  "v_pk_fma_f32 v[236:237], v[236:237], v[172:173], v[174:175] op_sel_hi:[1,0,0]\n\t" \
  "v_pk_fma_f32 v[238:239], v[238:239], v[172:173], v[174:175] op_sel_hi:[1,0,0]\n\t" \
  "v_pk_fma_f32 v[240:241], v[240:241], v[172:173], v[174:175] op_sel_hi:[1,0,0]\n\t" \
  "v_pk_fma_f32 v[242:243], v[242:243], v[172:173], v[174:175] op_sel_hi:[1,0,0]\n\t" \
  "v_pk_fma_f32 v[236:237], v[236:237], v[128:129], v[136:137]\n\t" \
  "v_pk_fma_f32 v[238:239], v[238:239], v[130:131], v[138:139]\n\t" \
  "v_pk_fma_f32 v[240:241], v[240:241], v[132:133], v[140:141]\n\t" \
  "v_pk_fma_f32 v[242:243], v[242:243], v[134:135], v[142:143]\n\t" \
  "v_cvt_pk_bf16_f32 v88, v228, v229\n\t" \
  "v_mfma_f32_16x16x32_bf16 a[116:119], a[156:159], v[48:51], a[116:119]\n\t" \
  "s_waitcnt vmcnt(23)\n\t" \
  "v_cvt_pk_bf16_f32 v58, v224, v225\n\t" \
  "v_cvt_pk_bf16_f32 v59, v226, v227\n\t" \
  "v_mfma_f32_16x16x32_bf16 a[112:115], a[156:159], v[16:19], a[112:115]\n\t" \
  "s_barrier\n\t" \
  FSTP("v_writelane_b32 v255, s94, 44\n\t") \
  FSTP("v_writelane_b32 v255, s92, 45\n\t") \
  FSTP("s_memrealtime s[94:95]\n\t") \
  FSTP("s_memtime s[92:93]\n\t") \
  "v_mfma_f32_16x16x32_bf16 v[0:3], a[160:163], v[20:23], v[0:3]\n\t" \
  "s_waitcnt vmcnt(5)\n\t" \
  "v_cmp_ne_u32 vcc, v211, v252\n\t" \
  "v_cvt_pk_bf16_f32 v89, v230, v231\n\t" \
  "v_mfma_f32_16x16x32_bf16 v[32:35], a[160:163], v[52:55], v[32:35]\n\t" \
  "ds_read_b64_tr_b16 a[128:129], v184 offset:0\n\t" \
  "v_cvt_pk_bf16_f32 v90, v232, v233\n\t" \
  "v_mfma_f32_16x16x32_bf16 v[64:67], a[160:163], v[84:87], v[64:67]\n\t" \
  "s_cbranch_vccnz .Lfq_s\n\t" \
  ".Lfq_k:\n\t" \
  "v_mfma_f32_16x16x32_bf16 v[96:99], a[160:163], v[116:119], v[96:99]\n\t" \
  "s_add_u32 s97, s78, 2097152\n\t" \
  "s_bfe_u32 s97, s97, 0x140003\n\t" \
  "s_add_u32 s97, s97, s82\n\t" \
  "v_mfma_f32_16x16x32_bf16 v[100:103], a[164:167], v[116:119], v[100:103]\n\t" \
  "buffer_load_dwordx4 a[0:3], v248, s[16:19], s97 offen sc1\n\t" \
  "v_mfma_f32_16x16x32_bf16 v[68:71], a[164:167], v[84:87], v[68:71]\n\t" \
  "buffer_load_dwordx4 a[4:7], v249, s[16:19], s97 offen sc1\n\t" \
  "v_mfma_f32_16x16x32_bf16 v[36:39], a[164:167], v[52:55], v[36:39]\n\t" \
  "ds_read_b64_tr_b16 a[130:131], v184 offset:1024\n\t" \
  "v_cvt_pk_bf16_f32 v91, v234, v235\n\t" \
  "v_mfma_f32_16x16x32_bf16 v[4:7], a[164:167], v[20:23], v[4:7]\n\t" \
  "ds_read_b64_tr_b16 a[132:133], v185 offset:0\n\t" \
  "v_cvt_pk_bf16_f32 v120, v236, v237\n\t" \
  "v_mfma_f32_16x16x32_bf16 v[8:11], a[168:171], v[20:23], v[8:11]\n\t" \
  "buffer_load_dwordx4 a[8:11], v250, s[16:19], s97 offen sc1\n\t" \
  "v_mfma_f32_16x16x32_bf16 v[40:43], a[168:171], v[52:55], v[40:43]\n\t" \
  "ds_read_b64_tr_b16 a[134:135], v185 offset:1024\n\t" \
  "v_cvt_pk_bf16_f32 v121, v238, v239\n\t" \
  "v_mfma_f32_16x16x32_bf16 v[72:75], a[168:171], v[84:87], v[72:75]\n\t" \
  "v_lshlrev_b32 v176, 16, v28\n\t" \
  "v_and_b32 v177, s98, v28\n\t" \
  "v_mfma_f32_16x16x32_bf16 v[104:107], a[168:171], v[116:119], v[104:107]\n\t" \
  "ds_read_b64_tr_b16 a[136:137], v186 offset:0\n\t" \
  "v_lshlrev_b32 v178, 16, v29\n\t" \
  "v_mfma_f32_16x16x32_bf16 v[108:111], a[172:175], v[116:119], v[108:111]\n\t" \
  "buffer_load_dwordx4 a[12:15], v251, s[16:19], s97 offen sc1\n\t" \
  "v_mfma_f32_16x16x32_bf16 v[76:79], a[172:175], v[84:87], v[76:79]\n\t" \
  "s_add_u32 s97, s97, s83\n\t" \
  "ds_read_b128 v[148:151], v208 offset:1680\n\t" \
  "ds_read_b64_tr_b16 a[138:139], v186 offset:1024\n\t" \
  "v_and_b32 v179, s98, v29\n\t" \
  "v_mfma_f32_16x16x32_bf16 v[44:47], a[172:175], v[52:55], v[44:47]\n\t" \
  "ds_read_b128 v[152:155], v208 offset:1920\n\t" \
  "ds_read_b64_tr_b16 a[140:141], v187 offset:0\n\t" \
  "v_lshlrev_b32 v180, 16, v30\n\t" \
  "v_mfma_f32_16x16x32_bf16 v[12:15], a[172:175], v[20:23], v[12:15]\n\t" \
  "buffer_load_dwordx4 a[16:19], v248, s[16:19], s97 offen sc1\n\t" \
  "v_mfma_f32_16x16x32_bf16 a[64:67], a[176:179], v[20:23], a[64:67]\n\t" \
  "ds_read_b64_tr_b16 a[142:143], v187 offset:1024\n\t" \
  "v_and_b32 v181, s98, v30\n\t" \
  "v_mfma_f32_16x16x32_bf16 a[68:71], a[176:179], v[52:55], a[68:71]\n\t" \
  "ds_read_b64_tr_b16 a[144:145], v188 offset:0\n\t" \
  "v_lshlrev_b32 v182, 16, v31\n\t" \
  "v_mfma_f32_16x16x32_bf16 a[72:75], a[176:179], v[84:87], a[72:75]\n\t" \
  "ds_read_b64_tr_b16 a[146:147], v188 offset:1024\n\t" \
  "v_and_b32 v183, s98, v31\n\t" \
  "v_mfma_f32_16x16x32_bf16 a[76:79], a[176:179], v[116:119], a[76:79]\n\t" \
  "buffer_load_dwordx4 a[20:23], v249, s[16:19], s97 offen sc1\n\t" \
  "v_mfma_f32_16x16x32_bf16 a[92:95], a[180:183], v[116:119], a[92:95]\n\t" \
  "ds_read_b64_tr_b16 a[148:149], v189 offset:0\n\t" \
  "v_lshlrev_b32 v220, 16, v60\n\t" \
  "v_mfma_f32_16x16x32_bf16 a[88:91], a[180:183], v[84:87], a[88:91]\n\t" \
  "ds_read_b64_tr_b16 a[150:151], v189 offset:1024\n\t" \
  "v_and_b32 v221, s98, v60\n\t" \
  "v_mfma_f32_16x16x32_bf16 a[84:87], a[180:183], v[52:55], a[84:87]\n\t" \
  "ds_read_b64_tr_b16 a[152:153], v190 offset:0\n\t" \
  "v_lshlrev_b32 v222, 16, v61\n\t" \
  "v_mfma_f32_16x16x32_bf16 a[80:83], a[180:183], v[20:23], a[80:83]\n\t" \
  "buffer_load_dwordx4 a[24:27], v250, s[16:19], s97 offen sc1\n\t" \
  "v_mfma_f32_16x16x32_bf16 a[96:99], a[184:187], v[20:23], a[96:99]\n\t" \
  "ds_read_b64_tr_b16 a[154:155], v190 offset:1024\n\t" \
  "v_cvt_pk_bf16_f32 v122, v240, v241\n\t" \
  "v_mfma_f32_16x16x32_bf16 a[100:103], a[184:187], v[52:55], a[100:103]\n\t" \
  "ds_read_b64_tr_b16 a[156:157], v191 offset:0\n\t" \
  "v_and_b32 v223, s98, v61\n\t" \
  "v_mfma_f32_16x16x32_bf16 a[104:107], a[184:187], v[84:87], a[104:107]\n\t" \
  "ds_read_b64_tr_b16 a[158:159], v191 offset:1024\n\t" \
  "v_cvt_pk_bf16_f32 v123, v242, v243\n\t" \
  "v_mfma_f32_16x16x32_bf16 a[108:111], a[184:187], v[116:119], a[108:111]\n\t" \
  "buffer_load_dwordx4 a[28:31], v251, s[16:19], s97 offen sc1\n\t" \
  "v_mfma_f32_16x16x32_bf16 a[124:127], a[188:191], v[116:119], a[124:127]\n\t" \
  "ds_read_b128 v[156:159], v208 offset:1936\n\t" \
  "v_lshlrev_b32 v224, 16, v62\n\t" \
  "v_mfma_f32_16x16x32_bf16 a[120:123], a[188:191], v[84:87], a[120:123]\n\t" \
  "s_add_u32 s97, s78, 2621440\n\t" \
  "v_and_b32 v225, s98, v62\n\t" \
  "v_lshlrev_b32 v226, 16, v63\n\t" \
  "v_mfma_f32_16x16x32_bf16 a[116:119], a[188:191], v[52:55], a[116:119]\n\t" \
  "s_bfe_u32 s97, s97, 0x140003\n\t" \
  "s_add_u32 s97, s97, s82\n\t" \
  "v_and_b32 v227, s98, v63\n\t" \
  "v_mfma_f32_16x16x32_bf16 a[112:115], a[188:191], v[20:23], a[112:115]\n\t" \
  "s_waitcnt lgkmcnt(1)\n\t" \
  "v_lshlrev_b32 v228, 16, v92\n\t" \
  "v_and_b32 v229, s98, v92\n\t" \
  "v_lshlrev_b32 v230, 16, v93\n\t" \
  "v_and_b32 v231, s98, v93\n\t" \
  "s_waitcnt lgkmcnt(0)\n\t" \
  "v_pk_fma_f32 v[176:177], v[176:177], v[160:161], v[162:163] op_sel_hi:[1,0,0]\n\t" \
  "v_pk_fma_f32 v[178:179], v[178:179], v[160:161], v[162:163] op_sel_hi:[1,0,0]\n\t" \
  "v_pk_fma_f32 v[180:181], v[180:181], v[160:161], v[162:163] op_sel_hi:[1,0,0]\n\t" \
  "v_pk_fma_f32 v[182:183], v[182:183], v[160:161], v[162:163] op_sel_hi:[1,0,0]\n\t" \
  "v_pk_fma_f32 v[176:177], v[176:177], v[144:145], v[152:153]\n\t" \
  "v_pk_fma_f32 v[178:179], v[178:179], v[146:147], v[154:155]\n\t" \
  "v_pk_fma_f32 v[180:181], v[180:181], v[148:149], v[156:157]\n\t" \
  "v_pk_fma_f32 v[182:183], v[182:183], v[150:151], v[158:159]\n\t" \
  "v_pk_fma_f32 v[220:221], v[220:221], v[164:165], v[166:167] op_sel_hi:[1,0,0]\n\t" \
  "v_pk_fma_f32 v[222:223], v[222:223], v[164:165], v[166:167] op_sel_hi:[1,0,0]\n\t" \
  "v_pk_fma_f32 v[224:225], v[224:225], v[164:165], v[166:167] op_sel_hi:[1,0,0]\n\t" \
  "v_pk_fma_f32 v[226:227], v[226:227], v[164:165], v[166:167] op_sel_hi:[1,0,0]\n\t" \
  "v_pk_fma_f32 v[220:221], v[220:221], v[144:145], v[152:153]\n\t" \
  "v_pk_fma_f32 v[222:223], v[222:223], v[146:147], v[154:155]\n\t" \
  "v_pk_fma_f32 v[224:225], v[224:225], v[148:149], v[156:157]\n\t" \
  "v_pk_fma_f32 v[226:227], v[226:227], v[150:151], v[158:159]\n\t" \
  "v_cvt_pk_bf16_f32 v28, v176, v177\n\t" \
  FSTP("v_writelane_b32 v255, s94, 46\n\t") \
  FSTP("v_writelane_b32 v255, s92, 47\n\t") \
  FSTP("s_memrealtime s[94:95]\n\t") \
  FSTP("s_memtime s[92:93]\n\t") \
  "v_mfma_f32_16x16x32_bf16 v[0:3], a[128:131], v[24:27], v[0:3]\n\t" \
  "buffer_load_dwordx4 a[32:35], v248, s[16:19], s97 offen sc1\n\t" \
  "v_mfma_f32_16x16x32_bf16 v[32:35], a[128:131], v[56:59], v[32:35]\n\t" \
  "ds_read_b64_tr_b16 a[160:161], v184 offset:8192\n\t" \
  "v_lshlrev_b32 v232, 16, v94\n\t" \
  "v_mfma_f32_16x16x32_bf16 v[64:67], a[128:131], v[88:91], v[64:67]\n\t" \
  "ds_read_b64_tr_b16 a[162:163], v184 offset:9216\n\t" \
  "v_and_b32 v233, s98, v94\n\t" \
  "v_mfma_f32_16x16x32_bf16 v[96:99], a[128:131], v[120:123], v[96:99]\n\t" \
  "v_lshlrev_b32 v234, 16, v95\n\t" \
  "v_and_b32 v235, s98, v95\n\t" \
  "v_mfma_f32_16x16x32_bf16 v[100:103], a[132:135], v[120:123], v[100:103]\n\t" \
  "buffer_load_dwordx4 a[36:39], v249, s[16:19], s97 offen sc1\n\t" \
  "v_mfma_f32_16x16x32_bf16 v[68:71], a[132:135], v[88:91], v[68:71]\n\t" \
  "ds_read_b64_tr_b16 a[164:165], v185 offset:8192\n\t" \
  "v_lshlrev_b32 v236, 16, v124\n\t" \
  "v_mfma_f32_16x16x32_bf16 v[36:39], a[132:135], v[56:59], v[36:39]\n\t" \
  "ds_read_b64_tr_b16 a[166:167], v185 offset:9216\n\t" \
  "v_and_b32 v237, s98, v124\n\t" \
  "v_mfma_f32_16x16x32_bf16 v[4:7], a[132:135], v[24:27], v[4:7]\n\t" \
  "ds_read_b64_tr_b16 a[168:169], v186 offset:8192\n\t" \
  "v_cvt_pk_bf16_f32 v29, v178, v179\n\t" \
  "v_mfma_f32_16x16x32_bf16 v[8:11], a[136:139], v[24:27], v[8:11]\n\t" \
  "buffer_load_dwordx4 a[40:43], v250, s[16:19], s97 offen sc1\n\t" \
  "v_mfma_f32_16x16x32_bf16 v[40:43], a[136:139], v[56:59], v[40:43]\n\t" \
  "ds_read_b64_tr_b16 a[170:171], v186 offset:9216\n\t" \
  "v_lshlrev_b32 v238, 16, v125\n\t" \
  "v_mfma_f32_16x16x32_bf16 v[72:75], a[136:139], v[88:91], v[72:75]\n\t" \
  "ds_read_b64_tr_b16 a[172:173], v187 offset:8192\n\t" \
  "v_and_b32 v239, s98, v125\n\t" \
  "v_mfma_f32_16x16x32_bf16 v[104:107], a[136:139], v[120:123], v[104:107]\n\t" \
  "v_cvt_pk_bf16_f32 v30, v180, v181\n\t" \
  "v_cvt_pk_bf16_f32 v31, v182, v183\n\t" \
  "v_mfma_f32_16x16x32_bf16 v[108:111], a[140:143], v[120:123], v[108:111]\n\t" \
  "buffer_load_dwordx4 a[44:47], v251, s[16:19], s97 offen sc1\n\t" \
  "v_mfma_f32_16x16x32_bf16 v[76:79], a[140:143], v[88:91], v[76:79]\n\t" \
  "s_add_u32 s97, s97, s83\n\t" \
  "ds_read_b64_tr_b16 a[174:175], v187 offset:9216\n\t" \
  "v_lshlrev_b32 v240, 16, v126\n\t" \
  "v_mfma_f32_16x16x32_bf16 v[44:47], a[140:143], v[56:59], v[44:47]\n\t" \
  "ds_read_b64_tr_b16 a[176:177], v188 offset:8192\n\t" \
  "v_and_b32 v241, s98, v126\n\t" \
  "v_mfma_f32_16x16x32_bf16 v[12:15], a[140:143], v[24:27], v[12:15]\n\t" \
  "buffer_load_dwordx4 a[48:51], v248, s[16:19], s97 offen sc1\n\t" \
  "v_mfma_f32_16x16x32_bf16 a[64:67], a[144:147], v[24:27], a[64:67]\n\t" \
  "ds_read_b64_tr_b16 a[178:179], v188 offset:9216\n\t" \
  "v_cvt_pk_bf16_f32 v60, v220, v221\n\t" \
  "v_mfma_f32_16x16x32_bf16 a[68:71], a[144:147], v[56:59], a[68:71]\n\t" \
  "ds_read_b64_tr_b16 a[180:181], v189 offset:8192\n\t" \
  "v_cvt_pk_bf16_f32 v61, v222, v223\n\t" \
  "v_mfma_f32_16x16x32_bf16 a[72:75], a[144:147], v[88:91], a[72:75]\n\t" \
  "ds_read_b64_tr_b16 a[182:183], v189 offset:9216\n\t" \
  "v_lshlrev_b32 v242, 16, v127\n\t" \
  "v_mfma_f32_16x16x32_bf16 a[76:79], a[144:147], v[120:123], a[76:79]\n\t" \
  "buffer_load_dwordx4 a[52:55], v249, s[16:19], s97 offen sc1\n\t" \
  "v_mfma_f32_16x16x32_bf16 a[92:95], a[148:151], v[120:123], a[92:95]\n\t" \
  "ds_read_b64_tr_b16 a[184:185], v190 offset:8192\n\t" \
  "v_and_b32 v243, s98, v127\n\t" \
  "v_mfma_f32_16x16x32_bf16 a[88:91], a[148:151], v[88:91], a[88:91]\n\t" \
  "buffer_load_dwordx4 a[56:59], v250, s[16:19], s97 offen sc1\n\t" \
  "ds_read_b64_tr_b16 a[186:187], v190 offset:9216\n\t" \
  "ds_read_b64_tr_b16 a[188:189], v191 offset:8192\n\t" \
  "v_cvt_pk_bf16_f32 v62, v224, v225\n\t" \
  "v_cvt_pk_bf16_f32 v63, v226, v227\n\t" \
  "v_pk_fma_f32 v[228:229], v[228:229], v[168:169], v[170:171] op_sel_hi:[1,0,0]\n\t" \
  "v_pk_fma_f32 v[230:231], v[230:231], v[168:169], v[170:171] op_sel_hi:[1,0,0]\n\t" \
  "v_pk_fma_f32 v[232:233], v[232:233], v[168:169], v[170:171] op_sel_hi:[1,0,0]\n\t" \
  "v_pk_fma_f32 v[234:235], v[234:235], v[168:169], v[170:171] op_sel_hi:[1,0,0]\n\t" \
  "v_pk_fma_f32 v[228:229], v[228:229], v[144:145], v[152:153]\n\t" \
  "v_pk_fma_f32 v[230:231], v[230:231], v[146:147], v[154:155]\n\t" \
  "v_pk_fma_f32 v[232:233], v[232:233], v[148:149], v[156:157]\n\t" \
  "v_pk_fma_f32 v[234:235], v[234:235], v[150:151], v[158:159]\n\t" \
  "v_pk_fma_f32 v[236:237], v[236:237], v[172:173], v[174:175] op_sel_hi:[1,0,0]\n\t" \
  "v_pk_fma_f32 v[238:239], v[238:239], v[172:173], v[174:175] op_sel_hi:[1,0,0]\n\t" \
  "v_pk_fma_f32 v[240:241], v[240:241], v[172:173], v[174:175] op_sel_hi:[1,0,0]\n\t" \
  "v_pk_fma_f32 v[242:243], v[242:243], v[172:173], v[174:175] op_sel_hi:[1,0,0]\n\t" \
  "v_pk_fma_f32 v[236:237], v[236:237], v[144:145], v[152:153]\n\t" \
  "v_pk_fma_f32 v[238:239], v[238:239], v[146:147], v[154:155]\n\t" \
  "v_pk_fma_f32 v[240:241], v[240:241], v[148:149], v[156:157]\n\t" \
  "v_pk_fma_f32 v[242:243], v[242:243], v[150:151], v[158:159]\n\t" \
  "v_cvt_pk_bf16_f32 v92, v228, v229\n\t" \
  "v_mfma_f32_16x16x32_bf16 a[84:87], a[148:151], v[56:59], a[84:87]\n\t" \
  "v_cvt_pk_bf16_f32 v93, v230, v231\n\t" \
  "v_cvt_pk_bf16_f32 v94, v232, v233\n\t" \
  "v_mfma_f32_16x16x32_bf16 a[80:83], a[148:151], v[24:27], a[80:83]\n\t" \
  "v_cvt_pk_bf16_f32 v95, v234, v235\n\t" \
  "v_cvt_pk_bf16_f32 v124, v236, v237\n\t" \
  "v_mfma_f32_16x16x32_bf16 a[96:99], a[152:155], v[24:27], a[96:99]\n\t" \
  "v_cvt_pk_bf16_f32 v125, v238, v239\n\t" \
  "v_cvt_pk_bf16_f32 v126, v240, v241\n\t" \
  "v_mfma_f32_16x16x32_bf16 a[100:103], a[152:155], v[56:59], a[100:103]\n\t" \
  "ds_read_b64_tr_b16 a[190:191], v191 offset:9216\n\t" \
  "v_cvt_pk_bf16_f32 v127, v242, v243\n\t" \
  "v_mfma_f32_16x16x32_bf16 a[104:107], a[152:155], v[88:91], a[104:107]\n\t" \
  "buffer_load_dwordx4 a[60:63], v251, s[16:19], s97 offen sc1\n\t" \
  "v_mfma_f32_16x16x32_bf16 a[108:111], a[152:155], v[120:123], a[108:111]\n\t" \
  "v_mfma_f32_16x16x32_bf16 a[124:127], a[156:159], v[120:123], a[124:127]\n\t" \
  "v_mfma_f32_16x16x32_bf16 a[120:123], a[156:159], v[88:91], a[120:123]\n\t" \
  "s_add_u32 s97, s78, 3145728\n\t" \
  "s_bfe_u32 s97, s97, 0x140003\n\t" \
  "s_add_u32 s97, s97, s82\n\t" \
  "v_mfma_f32_16x16x32_bf16 a[116:119], a[156:159], v[56:59], a[116:119]\n\t" \
  "s_waitcnt lgkmcnt(0)\n\t" \
  "v_mfma_f32_16x16x32_bf16 a[112:115], a[156:159], v[24:27], a[112:115]\n\t" \
  "s_barrier\n\t" \
  FSTP("v_writelane_b32 v255, s94, 48\n\t") \
  FSTP("v_writelane_b32 v255, s92, 49\n\t") \
  FSTP("s_memrealtime s[94:95]\n\t") \
  FSTP("s_memtime s[92:93]\n\t") \
  "v_mfma_f32_16x16x32_bf16 v[0:3], a[160:163], v[28:31], v[0:3]\n\t" \
  "buffer_load_dwordx4 a[192:195], v248, s[16:19], s97 offen sc1\n\t" \
  "v_mfma_f32_16x16x32_bf16 v[32:35], a[160:163], v[60:63], v[32:35]\n\t" \
  "ds_read_b64_tr_b16 a[128:129], v184 offset:16384\n\t" \
  "v_mfma_f32_16x16x32_bf16 v[64:67], a[160:163], v[92:95], v[64:67]\n\t" \
  "ds_read_b64_tr_b16 a[130:131], v184 offset:17408\n\t" \
  "v_mfma_f32_16x16x32_bf16 v[96:99], a[160:163], v[124:127], v[96:99]\n\t" \
  "ds_write_b32 v219, v218\n\t" \
  "v_mfma_f32_16x16x32_bf16 v[100:103], a[164:167], v[124:127], v[100:103]\n\t" \
  "buffer_load_dwordx4 a[196:199], v249, s[16:19], s97 offen sc1\n\t" \
  "v_mfma_f32_16x16x32_bf16 v[68:71], a[164:167], v[92:95], v[68:71]\n\t" \
  "ds_read_b64_tr_b16 a[132:133], v185 offset:16384\n\t" \
  "v_and_b32 v219, 0x7fffffe0, v219\n\t" \
  "v_mfma_f32_16x16x32_bf16 v[36:39], a[164:167], v[60:63], v[36:39]\n\t" \
  "ds_read_b64_tr_b16 a[134:135], v185 offset:17408\n\t" \
  "v_mfma_f32_16x16x32_bf16 v[4:7], a[164:167], v[28:31], v[4:7]\n\t" \
  "ds_read_b64_tr_b16 a[136:137], v186 offset:16384\n\t" \
  "v_mfma_f32_16x16x32_bf16 v[8:11], a[168:171], v[28:31], v[8:11]\n\t" \
  "buffer_load_dwordx4 a[200:203], v250, s[16:19], s97 offen sc1\n\t" \
  "v_mfma_f32_16x16x32_bf16 v[40:43], a[168:171], v[60:63], v[40:43]\n\t" \
  "ds_read_b64_tr_b16 a[138:139], v186 offset:17408\n\t" \
  "v_mfma_f32_16x16x32_bf16 v[72:75], a[168:171], v[92:95], v[72:75]\n\t" \
  "ds_read_b64_tr_b16 a[140:141], v187 offset:16384\n\t" \
  "v_mfma_f32_16x16x32_bf16 v[104:107], a[168:171], v[124:127], v[104:107]\n\t" \
  "ds_read_b128 v[168:171], v219 offset:0\n\t" \
  "v_mfma_f32_16x16x32_bf16 v[108:111], a[172:175], v[124:127], v[108:111]\n\t" \
  "buffer_load_dwordx4 a[204:207], v251, s[16:19], s97 offen sc1\n\t" \
  "v_mfma_f32_16x16x32_bf16 v[76:79], a[172:175], v[92:95], v[76:79]\n\t" \
  "ds_read_b64_tr_b16 a[142:143], v187 offset:17408\n\t" \
  "s_add_u32 s97, s97, s83\n\t" \
  "v_mfma_f32_16x16x32_bf16 v[44:47], a[172:175], v[60:63], v[44:47]\n\t" \
  "ds_read_b64_tr_b16 a[144:145], v188 offset:16384\n\t" \
  "v_mfma_f32_16x16x32_bf16 v[12:15], a[172:175], v[28:31], v[12:15]\n\t" \
  "buffer_load_dwordx4 a[208:211], v248, s[16:19], s97 offen sc1\n\t" \
  "v_mfma_f32_16x16x32_bf16 a[64:67], a[176:179], v[28:31], a[64:67]\n\t" \
  "ds_read_b64_tr_b16 a[146:147], v188 offset:17408\n\t" \
  "v_mfma_f32_16x16x32_bf16 a[68:71], a[176:179], v[60:63], a[68:71]\n\t" \
  "ds_read_b64_tr_b16 a[148:149], v189 offset:16384\n\t" \
  "v_mfma_f32_16x16x32_bf16 a[72:75], a[176:179], v[92:95], a[72:75]\n\t" \
  "ds_read_b64_tr_b16 a[150:151], v189 offset:17408\n\t" \
  "v_mfma_f32_16x16x32_bf16 a[76:79], a[176:179], v[124:127], a[76:79]\n\t" \
  "buffer_load_dwordx4 a[212:215], v249, s[16:19], s97 offen sc1\n\t" \
  "v_mfma_f32_16x16x32_bf16 a[92:95], a[180:183], v[124:127], a[92:95]\n\t" \
  "ds_read_b64_tr_b16 a[152:153], v190 offset:16384\n\t" \
  "v_mfma_f32_16x16x32_bf16 a[88:91], a[180:183], v[92:95], a[88:91]\n\t" \
  "ds_read_b128 v[172:175], v219 offset:16\n\t" \
  "v_mfma_f32_16x16x32_bf16 a[84:87], a[180:183], v[60:63], a[84:87]\n\t" \
  "ds_read_b64_tr_b16 a[154:155], v190 offset:17408\n\t" \
  "v_mfma_f32_16x16x32_bf16 a[80:83], a[180:183], v[28:31], a[80:83]\n\t" \
  "buffer_load_dwordx4 a[216:219], v250, s[16:19], s97 offen sc1\n\t" \
  "v_mfma_f32_16x16x32_bf16 a[96:99], a[184:187], v[28:31], a[96:99]\n\t" \
  "ds_read_b64_tr_b16 a[156:157], v191 offset:16384\n\t" \
  "v_mfma_f32_16x16x32_bf16 a[100:103], a[184:187], v[60:63], a[100:103]\n\t" \
  "ds_read_b64_tr_b16 a[158:159], v191 offset:17408\n\t" \
  "v_mfma_f32_16x16x32_bf16 a[104:107], a[184:187], v[92:95], a[104:107]\n\t" \
  "buffer_load_dwordx4 a[220:223], v251, s[16:19], s97 offen sc1\n\t" \
  "v_mfma_f32_16x16x32_bf16 a[108:111], a[184:187], v[124:127], a[108:111]\n\t" \
  "v_mfma_f32_16x16x32_bf16 a[124:127], a[188:191], v[124:127], a[124:127]\n\t" \
  "v_mfma_f32_16x16x32_bf16 a[120:123], a[188:191], v[92:95], a[120:123]\n\t" \
  "s_add_u32 s97, s78, 3670016\n\t" \
  "s_bfe_u32 s97, s97, 0x140003\n\t" \
  "s_add_u32 s97, s97, s82\n\t" \
  "v_mfma_f32_16x16x32_bf16 a[116:119], a[188:191], v[60:63], a[116:119]\n\t" \
  "v_mfma_f32_16x16x32_bf16 a[112:115], a[188:191], v[28:31], a[112:115]\n\t" \
  "s_waitcnt vmcnt(20) lgkmcnt(0)\n\t" \
  FSTP("v_writelane_b32 v255, s94, 50\n\t") \
  FSTP("v_writelane_b32 v255, s92, 51\n\t") \
  FSTP("s_memrealtime s[94:95]\n\t") \
  FSTP("s_memtime s[92:93]\n\t") \
  FSTS("s_memrealtime s[94:95]\n\t") \
  FSTS("s_waitcnt lgkmcnt(0)\n\t") \
  FSTS("v_writelane_b32 v255, s94, 14\n\t") \
  "v_mfma_f32_16x16x32_bf16 a[64:67], a[144:147], a[0:3], a[64:67]\n\t" \
  "buffer_load_dwordx4 a[224:227], v248, s[16:19], s97 offen sc1\n\t" \
  "v_mfma_f32_16x16x32_bf16 a[68:71], a[144:147], a[4:7], a[68:71]\n\t" \
  "s_bfe_u32 s39, s2, 0x30003\n\t" \
  "ds_read_b64_tr_b16 a[160:161], v184 offset:24576\n\t" \
  "s_lshl_b32 s40, s39, 18\n\t" \
  "v_mfma_f32_16x16x32_bf16 a[72:75], a[144:147], a[8:11], a[72:75]\n\t" \
  "s_lshr_b32 s98, s2, 3\n\t" \
  "ds_read_b64_tr_b16 a[162:163], v184 offset:25600\n\t" \
  "v_accvgpr_read_b32 v16, a64\n\t" \
  "v_mfma_f32_16x16x32_bf16 a[76:79], a[144:147], a[12:15], a[76:79]\n\t" \
  "s_lshl_b32 s41, s3, 5\n\t" \
  "v_accvgpr_read_b32 v17, a65\n\t" \
  "v_accvgpr_read_b32 v18, a66\n\t" \
  "v_mfma_f32_16x16x32_bf16 a[92:95], a[148:151], a[12:15], a[92:95]\n\t" \
  ".Lhs2:\n\ts_cbranch_execz .Lhs2n\n\t" \
  "buffer_load_dwordx4 a[228:231], v249, s[16:19], s97 offen sc1\n\t" \
  "v_mfma_f32_16x16x32_bf16 a[88:91], a[148:151], a[8:11], a[88:91]\n\t" \
  "s_add_u32 s41, s41, s98\n\t" \
  "ds_read_b64_tr_b16 a[164:165], v185 offset:24576\n\t" \
  "v_accvgpr_read_b32 v19, a67\n\t" \
  "v_mfma_f32_16x16x32_bf16 a[84:87], a[148:151], a[4:7], a[84:87]\n\t" \
  "s_lshl_b32 s41, s41, 7\n\t" \
  "ds_read_b64_tr_b16 a[166:167], v185 offset:25600\n\t" \
  "v_accvgpr_read_b32 v48, a68\n\t" \
  "v_mfma_f32_16x16x32_bf16 a[80:83], a[148:151], a[0:3], a[80:83]\n\t" \
  "s_and_b32 s12, s2, 3\n\t" \
  "ds_read_b64_tr_b16 a[168:169], v186 offset:24576\n\t" \
  "v_accvgpr_read_b32 v49, a69\n\t" \
  "v_mfma_f32_16x16x32_bf16 a[96:99], a[152:155], a[0:3], a[96:99]\n\t" \
  "buffer_load_dwordx4 a[232:235], v250, s[16:19], s97 offen sc1\n\t" \
  "v_mfma_f32_16x16x32_bf16 a[100:103], a[152:155], a[4:7], a[100:103]\n\t" \
  "s_bfe_u32 s13, s2, 0x10002\n\t" \
  "ds_read_b64_tr_b16 a[170:171], v186 offset:25600\n\t" \
  "v_accvgpr_read_b32 v50, a70\n\t" \
  "v_mfma_f32_16x16x32_bf16 a[104:107], a[152:155], a[8:11], a[104:107]\n\t" \
  "s_lshl1_add_u32 s12, s12, s13\n\t" \
  "ds_read_b64_tr_b16 a[172:173], v187 offset:24576\n\t" \
  "v_accvgpr_read_b32 v51, a71\n\t" \
  "v_mfma_f32_16x16x32_bf16 a[108:111], a[152:155], a[12:15], a[108:111]\n\t" \
  "s_bfe_u32 s59, s2, 0x20003\n\t" \
  "v_accvgpr_read_b32 v80, a72\n\t" \
  "v_accvgpr_read_b32 v81, a73\n\t" \
  "v_mfma_f32_16x16x32_bf16 a[124:127], a[156:159], a[12:15], a[124:127]\n\t" \
  "buffer_load_dwordx4 a[236:239], v251, s[16:19], s97 offen sc1\n\t" \
  "v_mfma_f32_16x16x32_bf16 a[120:123], a[156:159], a[8:11], a[120:123]\n\t" \
  "s_add_u32 s96, s78, 4718592\n\t" \
  "s_and_b32 s96, s96, 0x780000\n\t" \
  "s_add_u32 m0, s76, 98304\n\t" \
  "v_mfma_f32_16x16x32_bf16 a[116:119], a[156:159], a[4:7], a[116:119]\n\t" \
  "s_or_b32 s59, s59, 24\n\t" \
  "ds_read_b64_tr_b16 a[174:175], v187 offset:25600\n\t" \
  "v_accvgpr_read_b32 v82, a74\n\t" \
  "v_mfma_f32_16x16x32_bf16 a[112:115], a[156:159], a[0:3], a[112:115]\n\t" \
  "buffer_load_dwordx4 v200, s[20:23], s96 offen offset:0 lds\n\t" \
  "v_mfma_f32_16x16x32_bf16 v[0:3], a[128:131], a[0:3], v[0:3]\n\t" \
  "ds_read_b64_tr_b16 a[176:177], v188 offset:24576\n\t" \
  "v_accvgpr_read_b32 v83, a75\n\t" \
  "v_mfma_f32_16x16x32_bf16 v[32:35], a[128:131], a[4:7], v[32:35]\n\t" \
  "ds_read_b64_tr_b16 a[178:179], v188 offset:25600\n\t" \
  "v_accvgpr_read_b32 v112, a76\n\t" \
  "v_mfma_f32_16x16x32_bf16 v[64:67], a[128:131], a[8:11], v[64:67]\n\t" \
  "ds_read_b64_tr_b16 a[180:181], v189 offset:24576\n\t" \
  "v_accvgpr_read_b32 v113, a77\n\t" \
  "v_mfma_f32_16x16x32_bf16 v[96:99], a[128:131], a[12:15], v[96:99]\n\t" \
  "buffer_load_dwordx4 v201, s[20:23], s96 offen offset:1024 lds\n\t" \
  "v_mfma_f32_16x16x32_bf16 v[100:103], a[132:135], a[12:15], v[100:103]\n\t" \
  "ds_read_b64_tr_b16 a[182:183], v189 offset:25600\n\t" \
  "v_accvgpr_read_b32 v114, a78\n\t" \
  "v_mfma_f32_16x16x32_bf16 v[68:71], a[132:135], a[8:11], v[68:71]\n\t" \
  "ds_read_b64_tr_b16 a[184:185], v190 offset:24576\n\t" \
  "v_accvgpr_read_b32 v115, a79\n\t" \
  "v_mfma_f32_16x16x32_bf16 v[36:39], a[132:135], a[4:7], v[36:39]\n\t" \
  "ds_read_b64_tr_b16 a[186:187], v190 offset:25600\n\t" \
  "v_accvgpr_read_b32 v116, a92\n\t" \
  "v_mfma_f32_16x16x32_bf16 v[4:7], a[132:135], a[0:3], v[4:7]\n\t" \
  "buffer_load_dwordx4 v202, s[20:23], s96 offen offset:2048 lds\n\t" \
  "v_mfma_f32_16x16x32_bf16 v[8:11], a[136:139], a[0:3], v[8:11]\n\t" \
  "ds_read_b64_tr_b16 a[188:189], v191 offset:24576\n\t" \
  "v_accvgpr_read_b32 v117, a93\n\t" \
  "v_mfma_f32_16x16x32_bf16 v[40:43], a[136:139], a[4:7], v[40:43]\n\t" \
  "ds_read_b64_tr_b16 a[190:191], v191 offset:25600\n\t" \
  "v_accvgpr_read_b32 v118, a94\n\t" \
  "v_mfma_f32_16x16x32_bf16 v[72:75], a[136:139], a[8:11], v[72:75]\n\t" \
  "v_accvgpr_read_b32 v119, a95\n\t" \
  "v_accvgpr_read_b32 v84, a88\n\t" \
  "v_mfma_f32_16x16x32_bf16 v[104:107], a[136:139], a[12:15], v[104:107]\n\t" \
  "buffer_load_dwordx4 v203, s[20:23], s96 offen offset:3072 lds\n\t" \
  "v_mfma_f32_16x16x32_bf16 v[108:111], a[140:143], a[12:15], v[108:111]\n\t" \
  "v_accvgpr_read_b32 v85, a89\n\t" \
  "v_accvgpr_read_b32 v86, a90\n\t" \
  "v_mfma_f32_16x16x32_bf16 v[76:79], a[140:143], a[8:11], v[76:79]\n\t" \
  "s_add_u32 s97, s97, s83\n\t" \
  "v_accvgpr_read_b32 v87, a91\n\t" \
  "v_accvgpr_read_b32 v52, a84\n\t" \
  "v_mfma_f32_16x16x32_bf16 v[44:47], a[140:143], a[4:7], v[44:47]\n\t" \
  "s_waitcnt vmcnt(24) lgkmcnt(0)\n\t" \
  "v_accvgpr_read_b32 v53, a85\n\t" \
  "v_accvgpr_read_b32 v54, a86\n\t" \
  "v_mfma_f32_16x16x32_bf16 v[12:15], a[140:143], a[0:3], v[12:15]\n\t" \
  "s_barrier\n\t" \
  "v_mfma_f32_16x16x32_bf16 a[96:99], a[184:187], a[16:19], a[96:99]\n\t" \
  "buffer_load_dwordx4 a[240:243], v248, s[16:19], s97 offen sc1\n\t" \
  "v_mfma_f32_16x16x32_bf16 a[100:103], a[184:187], a[20:23], a[100:103]\n\t" \
  "ds_read_b64_tr_b16 a[128:129], v184 offset:32768\n\t" \
  "v_accvgpr_read_b32 v55, a87\n\t" \
  "v_mfma_f32_16x16x32_bf16 a[104:107], a[184:187], a[24:27], a[104:107]\n\t" \
  "ds_read_b64_tr_b16 a[130:131], v184 offset:33792\n\t" \
  "v_accvgpr_read_b32 v20, a80\n\t" \
  "v_mfma_f32_16x16x32_bf16 a[108:111], a[184:187], a[28:31], a[108:111]\n\t" \
  "v_accvgpr_read_b32 v21, a81\n\t" \
  "v_accvgpr_read_b32 v22, a82\n\t" \
  "v_mfma_f32_16x16x32_bf16 a[124:127], a[188:191], a[28:31], a[124:127]\n\t" \
  "buffer_load_dwordx4 a[244:247], v249, s[16:19], s97 offen sc1\n\t" \
  "v_mfma_f32_16x16x32_bf16 a[120:123], a[188:191], a[24:27], a[120:123]\n\t" \
  "ds_read_b64_tr_b16 a[132:133], v185 offset:32768\n\t" \
  "v_accvgpr_read_b32 v23, a83\n\t" \
  "v_mfma_f32_16x16x32_bf16 a[116:119], a[188:191], a[20:23], a[116:119]\n\t" \
  "ds_read_b64_tr_b16 a[134:135], v185 offset:33792\n\t" \
  "v_accvgpr_read_b32 v24, a96\n\t" \
  "v_mfma_f32_16x16x32_bf16 a[112:115], a[188:191], a[16:19], a[112:115]\n\t" \
  "ds_read_b64_tr_b16 a[136:137], v186 offset:32768\n\t" \
  "v_accvgpr_read_b32 v25, a97\n\t" \
  "v_mfma_f32_16x16x32_bf16 v[0:3], a[160:163], a[16:19], v[0:3]\n\t" \
  "buffer_load_dwordx4 a[248:251], v250, s[16:19], s97 offen sc1\n\t" \
  "v_mfma_f32_16x16x32_bf16 v[32:35], a[160:163], a[20:23], v[32:35]\n\t" \
  "ds_read_b64_tr_b16 a[138:139], v186 offset:33792\n\t" \
  "v_accvgpr_read_b32 v26, a98\n\t" \
  "v_mfma_f32_16x16x32_bf16 v[64:67], a[160:163], a[24:27], v[64:67]\n\t" \
  "ds_read_b64_tr_b16 a[140:141], v187 offset:32768\n\t" \
  "v_accvgpr_read_b32 v27, a99\n\t" \
  "v_mfma_f32_16x16x32_bf16 v[96:99], a[160:163], a[28:31], v[96:99]\n\t" \
  "v_accvgpr_read_b32 v56, a100\n\t" \
  "v_accvgpr_read_b32 v57, a101\n\t" \
  "v_mfma_f32_16x16x32_bf16 v[100:103], a[164:167], a[28:31], v[100:103]\n\t" \
  "buffer_load_dwordx4 a[252:255], v251, s[16:19], s97 offen sc1\n\t" \
  "v_mfma_f32_16x16x32_bf16 v[68:71], a[164:167], a[24:27], v[68:71]\n\t" \
  "s_add_u32 s97, s78, 4194304\n\t" \
  "s_bfe_u32 s97, s97, 0x140003\n\t" \
  "s_add_u32 s97, s97, s82\n\t" \
  "v_mfma_f32_16x16x32_bf16 v[36:39], a[164:167], a[20:23], v[36:39]\n\t" \
  "ds_read_b64_tr_b16 a[142:143], v187 offset:33792\n\t" \
  "v_accvgpr_read_b32 v58, a102\n\t" \
  "v_mfma_f32_16x16x32_bf16 v[4:7], a[164:167], a[16:19], v[4:7]\n\t" \
  "buffer_load_dwordx4 a[0:3], v248, s[16:19], s97 offen sc1\n\t" \
  "v_mfma_f32_16x16x32_bf16 v[8:11], a[168:171], a[16:19], v[8:11]\n\t" \
  "ds_read_b64_tr_b16 a[144:145], v188 offset:32768\n\t" \
  "v_accvgpr_read_b32 v59, a103\n\t" \
  "v_mfma_f32_16x16x32_bf16 v[40:43], a[168:171], a[20:23], v[40:43]\n\t" \
  "ds_read_b64_tr_b16 a[146:147], v188 offset:33792\n\t" \
  "v_accvgpr_read_b32 v88, a104\n\t" \
  "v_mfma_f32_16x16x32_bf16 v[72:75], a[168:171], a[24:27], v[72:75]\n\t" \
  "ds_read_b64_tr_b16 a[148:149], v189 offset:32768\n\t" \
  "v_accvgpr_read_b32 v89, a105\n\t" \
  "v_mfma_f32_16x16x32_bf16 v[104:107], a[168:171], a[28:31], v[104:107]\n\t" \
  "buffer_load_dwordx4 a[4:7], v249, s[16:19], s97 offen sc1\n\t" \
  "v_mfma_f32_16x16x32_bf16 v[108:111], a[172:175], a[28:31], v[108:111]\n\t" \
  "ds_read_b64_tr_b16 a[150:151], v189 offset:33792\n\t" \
  "v_accvgpr_read_b32 v90, a106\n\t" \
  "v_mfma_f32_16x16x32_bf16 v[76:79], a[172:175], a[24:27], v[76:79]\n\t" \
  "ds_read_b64_tr_b16 a[152:153], v190 offset:32768\n\t" \
  "v_accvgpr_read_b32 v91, a107\n\t" \
  "v_mfma_f32_16x16x32_bf16 v[44:47], a[172:175], a[20:23], v[44:47]\n\t" \
  "ds_read_b64_tr_b16 a[154:155], v190 offset:33792\n\t" \
  "v_accvgpr_read_b32 v120, a108\n\t" \
  "v_mfma_f32_16x16x32_bf16 v[12:15], a[172:175], a[16:19], v[12:15]\n\t" \
  "buffer_load_dwordx4 a[8:11], v250, s[16:19], s97 offen sc1\n\t" \
  "v_mfma_f32_16x16x32_bf16 v[16:19], a[176:179], a[16:19], v[16:19]\n\t" \
  "ds_read_b64_tr_b16 a[156:157], v191 offset:32768\n\t" \
  "v_accvgpr_read_b32 v121, a109\n\t" \
  "v_mfma_f32_16x16x32_bf16 v[48:51], a[176:179], a[20:23], v[48:51]\n\t" \
  "ds_read_b64_tr_b16 a[158:159], v191 offset:33792\n\t" \
  "v_accvgpr_read_b32 v122, a110\n\t" \
  "v_mfma_f32_16x16x32_bf16 v[80:83], a[176:179], a[24:27], v[80:83]\n\t" \
  "v_accvgpr_read_b32 v123, a111\n\t" \
  "v_accvgpr_read_b32 v124, a124\n\t" \
  "v_mfma_f32_16x16x32_bf16 v[112:115], a[176:179], a[28:31], v[112:115]\n\t" \
  "buffer_load_dwordx4 a[12:15], v251, s[16:19], s97 offen sc1\n\t" \
  "v_mfma_f32_16x16x32_bf16 v[116:119], a[180:183], a[28:31], v[116:119]\n\t" \
  "v_mbcnt_lo_u32_b32 v217, -1, 0\n\t" \
  "v_mbcnt_hi_u32_b32 v217, -1, v217\n\t" \
  "v_mfma_f32_16x16x32_bf16 v[84:87], a[180:183], a[24:27], v[84:87]\n\t" \
  "v_cmp_gt_u32 vcc, 16, v217\n\t" \
  "v_lshlrev_b32 v217, 7, v217\n\t" \
  "v_mfma_f32_16x16x32_bf16 v[52:55], a[180:183], a[20:23], v[52:55]\n\t" \
  "v_bfrev_b32 v215, 1\n\t" \
  "v_cndmask_b32 v217, v215, v217, vcc\n\t" \
  "v_mfma_f32_16x16x32_bf16 v[20:23], a[180:183], a[16:19], v[20:23]\n\t" \
  "s_waitcnt vmcnt(28) lgkmcnt(0)\n\t" \
  "v_bfe_u32 v215, v217, 10, 1\n\t" \
  "s_mov_b32 s99, 11264\n\t" \
  "v_mfma_f32_16x16x32_bf16 v[0:3], a[128:131], a[32:35], v[0:3]\n\t" \
  "v_mad_u32_u24 v215, v215, s99, v217\n\t" \
  "s_add_u32 s97, s97, s83\n\t" \
  "v_mfma_f32_16x16x32_bf16 v[32:35], a[128:131], a[36:39], v[32:35]\n\t" \
  "buffer_load_dwordx4 a[16:19], v248, s[16:19], s97 offen sc1\n\t" \
  "v_mfma_f32_16x16x32_bf16 v[64:67], a[128:131], a[40:43], v[64:67]\n\t" \
  "ds_read_b64_tr_b16 a[160:161], v184 offset:40960\n\t" \
  "v_accvgpr_read_b32 v125, a125\n\t" \
  "v_mfma_f32_16x16x32_bf16 v[96:99], a[128:131], a[44:47], v[96:99]\n\t" \
  "ds_read_b64_tr_b16 a[162:163], v184 offset:41984\n\t" \
  "v_accvgpr_read_b32 v126, a126\n\t" \
  "v_mfma_f32_16x16x32_bf16 v[100:103], a[132:135], a[44:47], v[100:103]\n\t" \
  "buffer_load_dwordx4 a[20:23], v249, s[16:19], s97 offen sc1\n\t" \
  "v_mfma_f32_16x16x32_bf16 v[68:71], a[132:135], a[40:43], v[68:71]\n\t" \
  "ds_read_b64_tr_b16 a[164:165], v185 offset:40960\n\t" \
  "v_accvgpr_read_b32 v127, a127\n\t" \
  "v_mfma_f32_16x16x32_bf16 v[36:39], a[132:135], a[36:39], v[36:39]\n\t" \
  "ds_read_b64_tr_b16 a[166:167], v185 offset:41984\n\t" \
  "v_accvgpr_read_b32 v92, a120\n\t" \
  "v_mfma_f32_16x16x32_bf16 v[4:7], a[132:135], a[32:35], v[4:7]\n\t" \
  "ds_read_b64_tr_b16 a[168:169], v186 offset:40960\n\t" \
  "v_accvgpr_read_b32 v93, a121\n\t" \
  "v_mfma_f32_16x16x32_bf16 v[8:11], a[136:139], a[32:35], v[8:11]\n\t" \
  "buffer_load_dwordx4 a[24:27], v250, s[16:19], s97 offen sc1\n\t" \
  "v_mfma_f32_16x16x32_bf16 v[40:43], a[136:139], a[36:39], v[40:43]\n\t" \
  "ds_read_b64_tr_b16 a[170:171], v186 offset:41984\n\t" \
  "v_accvgpr_read_b32 v94, a122\n\t" \
  "v_mfma_f32_16x16x32_bf16 v[72:75], a[136:139], a[40:43], v[72:75]\n\t" \
  "ds_read_b64_tr_b16 a[172:173], v187 offset:40960\n\t" \
  "v_accvgpr_read_b32 v95, a123\n\t" \
  "v_mfma_f32_16x16x32_bf16 v[104:107], a[136:139], a[44:47], v[104:107]\n\t" \
  "v_accvgpr_read_b32 v60, a116\n\t" \
  "v_accvgpr_read_b32 v61, a117\n\t" \
  "v_mfma_f32_16x16x32_bf16 v[108:111], a[140:143], a[44:47], v[108:111]\n\t" \
  "buffer_load_dwordx4 a[28:31], v251, s[16:19], s97 offen sc1\n\t" \
  "v_mfma_f32_16x16x32_bf16 v[76:79], a[140:143], a[40:43], v[76:79]\n\t" \
  "s_add_u32 s96, s78, 5242880\n\t" \
  "s_and_b32 s96, s96, 0x780000\n\t" \
  "s_add_u32 m0, s76, 0\n\t" \
  "v_mfma_f32_16x16x32_bf16 v[44:47], a[140:143], a[36:39], v[44:47]\n\t" \
  "ds_read_b64_tr_b16 a[174:175], v187 offset:41984\n\t" \
  "v_accvgpr_read_b32 v62, a118\n\t" \
  "v_mfma_f32_16x16x32_bf16 v[12:15], a[140:143], a[32:35], v[12:15]\n\t" \
  "buffer_load_dwordx4 v200, s[20:23], s96 offen offset:0 lds\n\t" \
  "v_mfma_f32_16x16x32_bf16 v[16:19], a[144:147], a[32:35], v[16:19]\n\t" \
  "ds_read_b64_tr_b16 a[176:177], v188 offset:40960\n\t" \
  "v_accvgpr_read_b32 v63, a119\n\t" \
  "v_mfma_f32_16x16x32_bf16 v[48:51], a[144:147], a[36:39], v[48:51]\n\t" \
  "ds_read_b64_tr_b16 a[178:179], v188 offset:41984\n\t" \
  "v_accvgpr_read_b32 v28, a112\n\t" \
  "v_mfma_f32_16x16x32_bf16 v[80:83], a[144:147], a[40:43], v[80:83]\n\t" \
  "ds_read_b64_tr_b16 a[180:181], v189 offset:40960\n\t" \
  "v_accvgpr_read_b32 v29, a113\n\t" \
  "v_mfma_f32_16x16x32_bf16 v[112:115], a[144:147], a[44:47], v[112:115]\n\t" \
  "buffer_load_dwordx4 v201, s[20:23], s96 offen offset:1024 lds\n\t" \
  "v_mfma_f32_16x16x32_bf16 v[116:119], a[148:151], a[44:47], v[116:119]\n\t" \
  "ds_read_b64_tr_b16 a[182:183], v189 offset:41984\n\t" \
  "v_accvgpr_read_b32 v30, a114\n\t" \
  "v_mfma_f32_16x16x32_bf16 v[84:87], a[148:151], a[40:43], v[84:87]\n\t" \
  "ds_read_b64_tr_b16 a[184:185], v190 offset:40960\n\t" \
  "v_accvgpr_read_b32 v31, a115\n\t" \
  "v_mfma_f32_16x16x32_bf16 v[52:55], a[148:151], a[36:39], v[52:55]\n\t" \
  "ds_read_b64_tr_b16 a[186:187], v190 offset:41984\n\t" \
  "v_mfma_f32_16x16x32_bf16 v[20:23], a[148:151], a[32:35], v[20:23]\n\t" \
  "buffer_load_dwordx4 v202, s[20:23], s96 offen offset:2048 lds\n\t" \
  "v_mfma_f32_16x16x32_bf16 v[24:27], a[152:155], a[32:35], v[24:27]\n\t" \
  "ds_read_b64_tr_b16 a[188:189], v191 offset:40960\n\t" \
  "v_mfma_f32_16x16x32_bf16 v[56:59], a[152:155], a[36:39], v[56:59]\n\t" \
  "ds_read_b64_tr_b16 a[190:191], v191 offset:41984\n\t" \
  "v_mfma_f32_16x16x32_bf16 v[88:91], a[152:155], a[40:43], v[88:91]\n\t" \
  "s_cmp_lt_u32 s98, 24\n\t" \
  "s_cselect_b32 s59, s98, s59\n\t" \
  "s_mul_i32 s53, s98, 57344\n\t" \
  "v_mfma_f32_16x16x32_bf16 v[120:123], a[152:155], a[44:47], v[120:123]\n\t" \
  "buffer_load_dwordx4 v203, s[20:23], s96 offen offset:3072 lds\n\t" \
  "v_mfma_f32_16x16x32_bf16 v[124:127], a[156:159], a[44:47], v[124:127]\n\t" \
  "s_lshl_b32 s14, s59, 5\n\t" \
  "s_sub_u32 s53, s53, s14\n\t" \
  "s_lshl_b32 s52, s98, 16\n\t" \
  "v_mfma_f32_16x16x32_bf16 v[92:95], a[156:159], a[40:43], v[92:95]\n\t" \
  "s_lshl_b32 s14, s3, 14\n\t" \
  "s_add_u32 s52, s52, s14\n\t" \
  "s_lshl_b32 s14, s13, 13\n\t" \
  "v_mfma_f32_16x16x32_bf16 v[60:63], a[156:159], a[36:39], v[60:63]\n\t" \
  "s_waitcnt vmcnt(32) lgkmcnt(0)\n\t" \
  "s_mov_b32 s99, 0\n\t" \
  "s_add_u32 s52, s52, s14\n\t" \
  "v_mfma_f32_16x16x32_bf16 v[28:31], a[156:159], a[32:35], v[28:31]\n\t" \
  "s_barrier\n\t" \
  "v_mfma_f32_16x16x32_bf16 v[0:3], a[160:163], a[48:51], v[0:3]\n\t" \
  "buffer_load_dwordx4 v[136:139], v209, s[8:11], s99 offen nt\n\t" \
  "v_mfma_f32_16x16x32_bf16 v[32:35], a[160:163], a[52:55], v[32:35]\n\t" \
  "s_add_u32 s52, s52, s78\n\t" \
  "ds_read_b64_tr_b16 a[128:129], v184 offset:49152\n\t" \
  "s_lshl_b32 s51, s3, 15\n\t" \
  "v_mfma_f32_16x16x32_bf16 v[64:67], a[160:163], a[56:59], v[64:67]\n\t" \
  "s_add_u32 s51, s51, s78\n\t" \
  "ds_read_b64_tr_b16 a[130:131], v184 offset:50176\n\t" \
  "s_lshl_b32 s14, s39, 8\n\t" \
  "v_mfma_f32_16x16x32_bf16 v[96:99], a[160:163], a[60:63], v[96:99]\n\t" \
  "s_add_u32 s51, s51, s14\n\t" \
  "ds_read_b64_tr_b16 a[132:133], v185 offset:49152\n\t" \
  "s_lshr_b32 s98, s98, 2\n\t" \
  "v_mfma_f32_16x16x32_bf16 v[100:103], a[164:167], a[60:63], v[100:103]\n\t" \
  "s_mov_b32 s99, 12288\n\t" \
  "ds_read_b64_tr_b16 a[134:135], v185 offset:50176\n\t" \
  "s_add_u32 s13, s13, 6\n\t" \
  "v_mfma_f32_16x16x32_bf16 v[68:71], a[164:167], a[56:59], v[68:71]\n\t" \
  "s_cmp_eq_u32 s98, s13\n\t" \
  "s_cselect_b32 s57, 0x80000000, 0\n\t" \
  "s_cselect_b32 s30, 0, 0x8000\n\t" \
  "v_mfma_f32_16x16x32_bf16 v[36:39], a[164:167], a[52:55], v[36:39]\n\t" \
  "buffer_load_dwordx4 v[140:143], v209, s[8:11], s99 offen nt\n\t" \
  "v_mfma_f32_16x16x32_bf16 v[4:7], a[164:167], a[48:51], v[4:7]\n\t" \
  "s_mul_i32 s14, s12, 28\n\t" \
  "ds_read_b64_tr_b16 a[136:137], v186 offset:49152\n\t" \
  "s_add_u32 s59, s59, s14\n\t" \
  "v_mfma_f32_16x16x32_bf16 v[8:11], a[168:171], a[48:51], v[8:11]\n\t" \
  "s_lshl_b32 s14, s59, 15\n\t" \
  "s_add_u32 s28, s28, s14\n\t" \
  "s_addc_u32 s29, s29, 0\n\t" \
  "v_mfma_f32_16x16x32_bf16 v[40:43], a[168:171], a[52:55], v[40:43]\n\t" \
  "s_mul_i32 s14, s3, 1835008\n\t" \
  "ds_read_b64_tr_b16 a[138:139], v186 offset:50176\n\t" \
  "s_lshl_b32 s59, s59, 5\n\t" \
  "v_mfma_f32_16x16x32_bf16 v[72:75], a[168:171], a[56:59], v[72:75]\n\t" \
  "s_mov_b32 s99, 24576\n\t" \
  "ds_read_b64_tr_b16 a[140:141], v187 offset:49152\n\t" \
  "s_add_u32 s14, s14, s59\n\t" \
  "v_mfma_f32_16x16x32_bf16 v[104:107], a[168:171], a[60:63], v[104:107]\n\t" \
  "s_add_u32 s68, s68, s14\n\t" \
  "s_addc_u32 s69, s69, 0\n\t" \
  "v_mfma_f32_16x16x32_bf16 v[108:111], a[172:175], a[60:63], v[108:111]\n\t" \
  "buffer_load_dwordx4 v[144:147], v209, s[8:11], s99 offen nt\n\t" \
  "v_mfma_f32_16x16x32_bf16 v[76:79], a[172:175], a[56:59], v[76:79]\n\t" \
  "ds_read_b64_tr_b16 a[142:143], v187 offset:50176\n\t" \
  "s_mov_b32 s99, 36864\n\t" \
  "v_mfma_f32_16x16x32_bf16 v[44:47], a[172:175], a[52:55], v[44:47]\n\t" \
  "ds_read_b64_tr_b16 a[144:145], v188 offset:49152\n\t" \
  "v_mfma_f32_16x16x32_bf16 v[12:15], a[172:175], a[48:51], v[12:15]\n\t" \
  "ds_read_b64_tr_b16 a[146:147], v188 offset:50176\n\t" \
  "v_mfma_f32_16x16x32_bf16 v[16:19], a[176:179], a[48:51], v[16:19]\n\t" \
  "s_add_u32 s72, s72, s14\n\t" \
  "s_addc_u32 s73, s73, 0\n\t" \
  "s_sub_u32 s70, s70, s14\n\t" \
  "v_mfma_f32_16x16x32_bf16 v[48:51], a[176:179], a[52:55], v[48:51]\n\t" \
  "s_sub_u32 s74, s74, s14\n\t" \
  "ds_read_b64_tr_b16 a[148:149], v189 offset:49152\n\t" \
  "s_lshr_b32 s14, s78, 2\n\t" \
  "v_mfma_f32_16x16x32_bf16 v[80:83], a[176:179], a[56:59], v[80:83]\n\t" \
  "buffer_load_dwordx4 v[148:151], v209, s[8:11], s99 offen nt\n\t" \
  "v_mfma_f32_16x16x32_bf16 v[112:115], a[176:179], a[60:63], v[112:115]\n\t" \
  "ds_read_b64_tr_b16 a[150:151], v189 offset:50176\n\t" \
  "v_mfma_f32_16x16x32_bf16 v[116:119], a[180:183], a[60:63], v[116:119]\n\t" \
  "ds_read_b64_tr_b16 a[152:153], v190 offset:49152\n\t" \
  "v_mfma_f32_16x16x32_bf16 v[84:87], a[180:183], a[56:59], v[84:87]\n\t" \
  "s_add_u32 s60, s60, s14\n\t" \
  "s_addc_u32 s61, s61, 0\n\t" \
  "v_mfma_f32_16x16x32_bf16 v[52:55], a[180:183], a[52:55], v[52:55]\n\t" \
  "ds_read_b64_tr_b16 a[154:155], v190 offset:50176\n\t" \
  "v_mfma_f32_16x16x32_bf16 v[20:23], a[180:183], a[48:51], v[20:23]\n\t" \
  "ds_read_b64_tr_b16 a[156:157], v191 offset:49152\n\t" \
  "v_mfma_f32_16x16x32_bf16 v[24:27], a[184:187], a[48:51], v[24:27]\n\t" \
  "s_add_u32 s64, s64, s14\n\t" \
  "s_addc_u32 s65, s65, 0\n\t" \
  "s_lshl2_add_u32 s58, s2, s3\n\t" \
  "v_mfma_f32_16x16x32_bf16 v[56:59], a[184:187], a[52:55], v[56:59]\n\t" \
  "s_lshl_b32 s58, s58, 11\n\t" \
  "ds_read_b64_tr_b16 a[158:159], v191 offset:50176\n\t" \
  "s_bfe_u32 s13, s2, 0x10004\n\t" \
  "v_mfma_f32_16x16x32_bf16 v[88:91], a[184:187], a[56:59], v[88:91]\n\t" \
  "s_lshl1_add_u32 s12, s12, s13\n\t" \
  "s_lshl_b32 s13, s13, 13\n\t" \
  "s_lshl_b32 s14, s12, 7\n\t" \
  "v_mfma_f32_16x16x32_bf16 v[120:123], a[184:187], a[60:63], v[120:123]\n\t" \
  "s_add_u32 s13, s13, s14\n\t" \
  "s_add_u32 s13, s13, 16384\n\t" \
  "s_lshl_b32 s12, s12, 21\n\t" \
  "v_mfma_f32_16x16x32_bf16 v[124:127], a[188:191], a[60:63], v[124:127]\n\t" \
  "s_add_u32 s12, s12, 8388608\n\t" \
  "s_bitcmp1_b32 s2, 3\n\t" \
  "s_cselect_b32 s91, s12, s13\n\t" \
  "v_mfma_f32_16x16x32_bf16 v[92:95], a[188:191], a[56:59], v[92:95]\n\t" \
  "s_lshr_b32 s12, s57, s3\n\t" \
  "s_cmp_lt_i32 s12, 0\n\t" \
  "s_cselect_b32 s91, s91, 0x80000000\n\t" \
  "v_mfma_f32_16x16x32_bf16 v[60:63], a[188:191], a[52:55], v[60:63]\n\t" \
  "buffer_load_dword v246, v217, s[16:19], s58 offen sc1\n\t" \
  "v_mfma_f32_16x16x32_bf16 v[28:31], a[188:191], a[48:51], v[28:31]\n\t" \
  "s_waitcnt vmcnt(33) lgkmcnt(0)\n\t" \
  "s_lshl_b32 s90, s3, 7\n\t" \
  "s_add_u32 s90, s90, 0x220000\n\t" \
  "v_mfma_f32_16x16x32_bf16 v[0:3], a[128:131], a[192:195], v[0:3]\n\t" \
  "s_and_b32 s98, s2, 0xc0\n\t" \
  "ds_read_b64_tr_b16 a[160:161], v184 offset:57344\n\t" \
  "s_lshl_b32 s98, s98, 9\n\t" \
  "v_mfma_f32_16x16x32_bf16 v[32:35], a[128:131], a[196:199], v[32:35]\n\t" \
  "s_add_u32 s90, s90, s98\n\t" \
  "ds_read_b64_tr_b16 a[162:163], v184 offset:58368\n\t" \
  "s_and_b32 s98, s2, 0x3\n\t" \
  "v_mfma_f32_16x16x32_bf16 v[64:67], a[128:131], a[200:203], v[64:67]\n\t" \
  "s_lshl_b32 s98, s98, 13\n\t" \
  "ds_read_b64_tr_b16 a[164:165], v185 offset:57344\n\t" \
  "s_add_u32 s90, s90, s98\n\t" \
  "v_mfma_f32_16x16x32_bf16 v[96:99], a[128:131], a[204:207], v[96:99]\n\t" \
  "s_and_b32 s98, s2, 0x4\n\t" \
  "ds_read_b64_tr_b16 a[166:167], v185 offset:58368\n\t" \
  "s_lshl_b32 s98, s98, 10\n\t" \
  "v_mfma_f32_16x16x32_bf16 v[100:103], a[132:135], a[204:207], v[100:103]\n\t" \
  "s_add_u32 s90, s90, s98\n\t" \
  "ds_read_b64_tr_b16 a[168:169], v186 offset:57344\n\t" \
  "s_and_b32 s98, s2, 0x38\n\t" \
  "v_mfma_f32_16x16x32_bf16 v[68:71], a[132:135], a[200:203], v[68:71]\n\t" \
  "s_lshl_b32 s98, s98, 6\n\t" \
  "ds_read_b64_tr_b16 a[170:171], v186 offset:58368\n\t" \
  "s_add_u32 s90, s90, s98\n\t" \
  "v_mfma_f32_16x16x32_bf16 v[36:39], a[132:135], a[196:199], v[36:39]\n\t" \
  "s_lshr_b32 s98, s2, 2\n\t" \
  "ds_read_b64_tr_b16 a[172:173], v187 offset:57344\n\t" \
  "s_xor_b32 s98, s98, s3\n\t" \
  "v_mfma_f32_16x16x32_bf16 v[4:7], a[132:135], a[192:195], v[4:7]\n\t" \
  "s_lshl_b32 s98, s98, 13\n\t" \
  "ds_read_b64_tr_b16 a[174:175], v187 offset:58368\n\t" \
  "s_and_b32 s42, s98, 0x2000\n\t" \
  "v_mfma_f32_16x16x32_bf16 v[8:11], a[136:139], a[192:195], v[8:11]\n\t" \
  "s_lshr_b32 s98, s2, 6\n\t" \
  "ds_read_b64_tr_b16 a[176:177], v188 offset:57344\n\t" \
  "s_lshl_b32 s98, s98, 22\n\t" \
  "v_mfma_f32_16x16x32_bf16 v[40:43], a[136:139], a[196:199], v[40:43]\n\t" \
  "s_add_u32 s42, s42, s98\n\t" \
  "ds_read_b64_tr_b16 a[178:179], v188 offset:58368\n\t" \
  "s_lshl_b32 s47, s3, 15\n\t" \
  "v_mfma_f32_16x16x32_bf16 v[72:75], a[136:139], a[200:203], v[72:75]\n\t" \
  "s_add_u32 s47, s47, 0x1800000\n\t" \
  "ds_read_b64_tr_b16 a[180:181], v189 offset:57344\n\t" \
  "s_add_u32 s47, s47, s42\n\t" \
  "v_mfma_f32_16x16x32_bf16 v[104:107], a[136:139], a[204:207], v[104:107]\n\t" \
  "s_and_b32 s98, s2, 0x38\n\t" \
  "ds_read_b64_tr_b16 a[182:183], v189 offset:58368\n\t" \
  "s_lshl_b32 s98, s98, 16\n\t" \
  "v_mfma_f32_16x16x32_bf16 v[108:111], a[140:143], a[204:207], v[108:111]\n\t" \
  "s_add_u32 s47, s47, s98\n\t" \
  "ds_read_b64_tr_b16 a[184:185], v190 offset:57344\n\t" \
  "s_add_u32 s46, s42, 0x800000\n\t" \
  "v_mfma_f32_16x16x32_bf16 v[76:79], a[140:143], a[200:203], v[76:79]\n\t" \
  "s_and_b32 s98, s2, 0x3\n\t" \
  "ds_read_b64_tr_b16 a[186:187], v190 offset:58368\n\t" \
  "s_lshl_b32 s98, s98, 20\n\t" \
  "v_mfma_f32_16x16x32_bf16 v[44:47], a[140:143], a[196:199], v[44:47]\n\t" \
  "s_add_u32 s46, s46, s98\n\t" \
  "ds_read_b64_tr_b16 a[188:189], v191 offset:57344\n\t" \
  "s_and_b32 s98, s3, 2\n\t" \
  "v_mfma_f32_16x16x32_bf16 v[12:15], a[140:143], a[192:195], v[12:15]\n\t" \
  "s_lshl_b32 s98, s98, 13\n\t" \
  "ds_read_b64_tr_b16 a[190:191], v191 offset:58368\n\t" \
  "s_add_u32 s46, s46, s98\n\t" \
  "v_mfma_f32_16x16x32_bf16 v[16:19], a[144:147], a[192:195], v[16:19]\n\t" \
  "s_and_b32 s98, s3, 1\n\t" \
  "s_lshl_b32 s98, s98, 12\n\t" \
  "s_add_u32 s46, s46, s98\n\t" \
  "v_mfma_f32_16x16x32_bf16 v[48:51], a[144:147], a[196:199], v[48:51]\n\t" \
  "s_lshr_b32 s45, s87, 2\n\t" \
  "s_add_u32 s45, s45, s46\n\t" \
  "s_and_b32 s98, s2, 0x38\n\t" \
  "v_mfma_f32_16x16x32_bf16 v[80:83], a[144:147], a[200:203], v[80:83]\n\t" \
  "s_lshl_b32 s98, s98, 14\n\t" \
  "s_add_u32 s45, s45, s98\n\t" \
  "s_lshr_b32 s98, s2, 1\n\t" \
  "v_mfma_f32_16x16x32_bf16 v[112:115], a[144:147], a[204:207], v[112:115]\n\t" \
  "s_xor_b32 s98, s98, s3\n\t" \
  "s_lshl_b32 s98, s98, 13\n\t" \
  "s_and_b32 s42, s98, 0x2000\n\t" \
  "v_mfma_f32_16x16x32_bf16 v[116:119], a[148:151], a[204:207], v[116:119]\n\t" \
  "s_lshr_b32 s98, s2, 5\n\t" \
  "s_xor_b32 s98, s98, s3\n\t" \
  "s_lshl_b32 s98, s98, 13\n\t" \
  "v_mfma_f32_16x16x32_bf16 v[84:87], a[148:151], a[200:203], v[84:87]\n\t" \
  "s_and_b32 s43, s98, 0x2000\n\t" \
  "s_lshl_b32 s48, s3, 2\n\t" \
  "s_add_u32 s48, s48, 0x8000\n\t" \
  "v_mfma_f32_16x16x32_bf16 v[52:55], a[148:151], a[196:199], v[52:55]\n\t" \
  "s_and_b32 s98, s2, 0x80\n\t" \
  "s_lshl_b32 s98, s98, 7\n\t" \
  "s_add_u32 s48, s48, s98\n\t" \
  "v_mfma_f32_16x16x32_bf16 v[20:23], a[148:151], a[192:195], v[20:23]\n\t" \
  "s_and_b32 s98, s2, 0x4\n\t" \
  "s_lshl_b32 s98, s98, 11\n\t" \
  "s_add_u32 s48, s48, s98\n\t" \
  "v_mfma_f32_16x16x32_bf16 v[24:27], a[152:155], a[192:195], v[24:27]\n\t" \
  "s_and_b32 s98, s2, 0x78\n\t" \
  "s_lshl_b32 s98, s98, 6\n\t" \
  "s_add_u32 s48, s48, s98\n\t" \
  "v_mfma_f32_16x16x32_bf16 v[56:59], a[152:155], a[196:199], v[56:59]\n\t" \
  "s_lshl_b32 s49, s3, 7\n\t" \
  "s_add_u32 s49, s49, 0x20000\n\t" \
  "s_and_b32 s98, s2, 0xc0\n\t" \
  "v_mfma_f32_16x16x32_bf16 v[88:91], a[152:155], a[200:203], v[88:91]\n\t" \
  "s_lshl_b32 s98, s98, 9\n\t" \
  "s_add_u32 s49, s49, s98\n\t" \
  "s_and_b32 s98, s2, 0x2\n\t" \
  "v_mfma_f32_16x16x32_bf16 v[120:123], a[152:155], a[204:207], v[120:123]\n\t" \
  "s_lshl_b32 s98, s98, 13\n\t" \
  "s_add_u32 s49, s49, s98\n\t" \
  "s_and_b32 s98, s2, 0x4\n\t" \
  "v_mfma_f32_16x16x32_bf16 v[124:127], a[156:159], a[204:207], v[124:127]\n\t" \
  "s_lshl_b32 s98, s98, 11\n\t" \
  "s_add_u32 s49, s49, s98\n\t" \
  "s_and_b32 s98, s2, 0x1\n\t" \
  "v_mfma_f32_16x16x32_bf16 v[92:95], a[156:159], a[200:203], v[92:95]\n\t" \
  "s_lshl_b32 s98, s98, 12\n\t" \
  "s_add_u32 s49, s49, s98\n\t" \
  "s_lshl2_add_u32 s49, s39, s49\n\t" \
  "v_mfma_f32_16x16x32_bf16 v[60:63], a[156:159], a[196:199], v[60:63]\n\t" \
  "s_waitcnt vmcnt(29) lgkmcnt(0)\n\t" \
  "s_add_u32 s97, s78, 4718592\n\t" \
  "s_bfe_u32 s97, s97, 0x140003\n\t" \
  "v_mfma_f32_16x16x32_bf16 v[28:31], a[156:159], a[192:195], v[28:31]\n\t" \
  "s_barrier\n\t" \
  "v_mfma_f32_16x16x32_bf16 v[0:3], a[160:163], a[208:211], v[0:3]\n\t" \
  "ds_read_b64_tr_b16 a[128:129], v192 offset:0\n\t" \
  "ds_read_b64_tr_b16 a[130:131], v192 offset:1024\n\t" \
  "ds_read_b64_tr_b16 a[132:133], v193 offset:0\n\t" \
  "s_add_u32 s97, s97, s82\n\t" \
  "v_mfma_f32_16x16x32_bf16 v[32:35], a[160:163], a[212:215], v[32:35]\n\t" \
  "buffer_load_dwordx4 a[32:35], v248, s[16:19], s97 offen sc1\n\t" \
  "ds_read_b64_tr_b16 a[134:135], v193 offset:1024\n\t" \
  "ds_read_b64_tr_b16 a[136:137], v194 offset:0\n\t" \
  "ds_read_b64_tr_b16 a[138:139], v194 offset:1024\n\t" \
  "v_mfma_f32_16x16x32_bf16 v[64:67], a[160:163], a[216:219], v[64:67]\n\t" \
  "ds_read_b64_tr_b16 a[140:141], v195 offset:0\n\t" \
  "ds_read_b64_tr_b16 a[142:143], v195 offset:1024\n\t" \
  "ds_read_b64_tr_b16 a[144:145], v196 offset:0\n\t" \
  "ds_read_b64_tr_b16 a[146:147], v196 offset:1024\n\t" \
  "v_mfma_f32_16x16x32_bf16 v[96:99], a[160:163], a[220:223], v[96:99]\n\t" \
  "ds_read_b64_tr_b16 a[148:149], v197 offset:0\n\t" \
  "ds_read_b64_tr_b16 a[150:151], v197 offset:1024\n\t" \
  "ds_read_b64_tr_b16 a[152:153], v198 offset:0\n\t" \
  "v_mbcnt_lo_u32_b32 v233, -1, 0\n\t" \
  "v_mfma_f32_16x16x32_bf16 v[100:103], a[164:167], a[220:223], v[100:103]\n\t" \
  "buffer_load_dwordx4 a[36:39], v249, s[16:19], s97 offen sc1\n\t" \
  "ds_read_b64_tr_b16 a[154:155], v198 offset:1024\n\t" \
  "s_lshl_b32 s50, s3, 15\n\t" \
  "v_mfma_f32_16x16x32_bf16 v[68:71], a[164:167], a[216:219], v[68:71]\n\t" \
  "s_and_b32 s98, s2, 0x4\n\t" \
  "ds_read_b64_tr_b16 a[156:157], v199 offset:0\n\t" \
  "v_mbcnt_hi_u32_b32 v233, -1, v233\n\t" \
  "v_mfma_f32_16x16x32_bf16 v[36:39], a[164:167], a[212:215], v[36:39]\n\t" \
  "s_lshl_b32 s98, s98, 19\n\t" \
  "ds_read_b64_tr_b16 a[158:159], v199 offset:1024\n\t" \
  "v_lshrrev_b32 v129, 4, v233\n\t" \
  "v_mfma_f32_16x16x32_bf16 v[4:7], a[164:167], a[208:211], v[4:7]\n\t" \
  "s_add_u32 s50, s50, s98\n\t" \
  "ds_read_b64_tr_b16 a[64:65], v192 offset:8192\n\t" \
  "v_and_b32 v130, 15, v233\n\t" \
  "v_mfma_f32_16x16x32_bf16 v[8:11], a[168:171], a[208:211], v[8:11]\n\t" \
  "buffer_load_dwordx4 a[40:43], v250, s[16:19], s97 offen sc1\n\t" \
  "v_mfma_f32_16x16x32_bf16 v[40:43], a[168:171], a[212:215], v[40:43]\n\t" \
  "s_and_b32 s98, s2, 0xc0\n\t" \
  "ds_read_b64_tr_b16 a[66:67], v192 offset:9216\n\t" \
  "v_bfe_u32 v131, v233, 2, 2\n\t" \
  "v_mfma_f32_16x16x32_bf16 v[72:75], a[168:171], a[216:219], v[72:75]\n\t" \
  "s_lshl_b32 s98, s98, 13\n\t" \
  "ds_read_b64_tr_b16 a[68:69], v193 offset:8192\n\t" \
  "v_and_b32 v132, 3, v233\n\t" \
  "v_mfma_f32_16x16x32_bf16 v[104:107], a[168:171], a[220:223], v[104:107]\n\t" \
  "s_add_u32 s50, s50, s98\n\t" \
  "ds_read_b64_tr_b16 a[70:71], v193 offset:9216\n\t" \
  "v_cmp_lt_u32 vcc, 6, v233\n\t" \
  "v_mfma_f32_16x16x32_bf16 v[108:111], a[172:175], a[220:223], v[108:111]\n\t" \
  "buffer_load_dwordx4 a[44:47], v251, s[16:19], s97 offen sc1\n\t" \
  "v_mfma_f32_16x16x32_bf16 v[76:79], a[172:175], a[216:219], v[76:79]\n\t" \
  "s_add_u32 s96, s78, 5767168\n\t" \
  "s_and_b32 s96, s96, 0x780000\n\t" \
  "ds_read_b64_tr_b16 a[72:73], v194 offset:8192\n\t" \
  "s_add_u32 m0, s76, 16384\n\t" \
  "v_mfma_f32_16x16x32_bf16 v[44:47], a[172:175], a[212:215], v[44:47]\n\t" \
  "s_and_b32 s98, s2, 0x3\n\t" \
  "ds_read_b64_tr_b16 a[74:75], v194 offset:9216\n\t" \
  "v_mov_b32 v133, 6272\n\t" \
  "v_mfma_f32_16x16x32_bf16 v[12:15], a[172:175], a[208:211], v[12:15]\n\t" \
  "buffer_load_dwordx4 v200, s[20:23], s96 offen offset:0 lds\n\t" \
  "v_mfma_f32_16x16x32_bf16 v[16:19], a[176:179], a[208:211], v[16:19]\n\t" \
  "s_lshl_b32 s98, s98, 17\n\t" \
  "ds_read_b64_tr_b16 a[76:77], v195 offset:8192\n\t" \
  "v_cndmask_b32 v133, 0, v133, vcc\n\t" \
  "v_mfma_f32_16x16x32_bf16 v[48:51], a[176:179], a[212:215], v[48:51]\n\t" \
  "s_add_u32 s50, s50, s98\n\t" \
  "ds_read_b64_tr_b16 a[78:79], v195 offset:9216\n\t" \
  "v_lshl_add_u32 v133, v233, 7, v133\n\t" \
  "v_mfma_f32_16x16x32_bf16 v[80:83], a[176:179], a[216:219], v[80:83]\n\t" \
  "s_and_b32 s98, s2, 0x38\n\t" \
  "ds_read_b64_tr_b16 a[80:81], v196 offset:8192\n\t" \
  "v_cmp_gt_u32 vcc, 14, v233\n\t" \
  "v_mfma_f32_16x16x32_bf16 v[112:115], a[176:179], a[220:223], v[112:115]\n\t" \
  "buffer_load_dwordx4 v201, s[20:23], s96 offen offset:1024 lds\n\t" \
  "v_mfma_f32_16x16x32_bf16 v[116:119], a[180:183], a[220:223], v[116:119]\n\t" \
  "s_lshl_b32 s98, s98, 5\n\t" \
  "ds_read_b64_tr_b16 a[82:83], v196 offset:9216\n\t" \
  "v_bfrev_b32 v247, 1\n\t" \
  "v_mfma_f32_16x16x32_bf16 v[84:87], a[180:183], a[216:219], v[84:87]\n\t" \
  "s_add_u32 s50, s50, s98\n\t" \
  "ds_read_b64_tr_b16 a[84:85], v197 offset:8192\n\t" \
  "v_cndmask_b32 v245, v247, v133, vcc\n\t" \
  "v_mfma_f32_16x16x32_bf16 v[52:55], a[180:183], a[212:215], v[52:55]\n\t" \
  "s_lshr_b32 s98, s2, 2\n\t" \
  "ds_read_b64_tr_b16 a[86:87], v197 offset:9216\n\t" \
  "v_writelane_b32 v247, s91, 0\n\t" \
  "v_mfma_f32_16x16x32_bf16 v[20:23], a[180:183], a[208:211], v[20:23]\n\t" \
  "buffer_load_dwordx4 v202, s[20:23], s96 offen offset:2048 lds\n\t" \
  ".Lhs3:\n\ts_cbranch_execz .Lhs3n\n\t" \
  "v_mfma_f32_16x16x32_bf16 v[24:27], a[184:187], a[208:211], v[24:27]\n\t" \
  "s_xor_b32 s98, s98, s2\n\t" \
  "ds_read_b64_tr_b16 a[88:89], v198 offset:8192\n\t" \
  "v_lshrrev_b32 v133, 1, v129\n\t" \
  "v_mfma_f32_16x16x32_bf16 v[56:59], a[184:187], a[212:215], v[56:59]\n\t" \
  "s_and_b32 s98, s98, 4\n\t" \
  "ds_read_b64_tr_b16 a[90:91], v198 offset:9216\n\t" \
  "v_and_b32 v134, 1, v129\n\t" \
  "v_mfma_f32_16x16x32_bf16 v[88:91], a[184:187], a[216:219], v[88:91]\n\t" \
  "s_lshl_b32 s44, s98, 4\n\t" \
  "ds_read_b64_tr_b16 a[92:93], v199 offset:8192\n\t" \
  "v_lshl_or_b32 v133, v134, 1, v133\n\t" \
  "v_mfma_f32_16x16x32_bf16 v[120:123], a[184:187], a[220:223], v[120:123]\n\t" \
  "buffer_load_dwordx4 v203, s[20:23], s96 offen offset:3072 lds\n\t" \
  "v_mfma_f32_16x16x32_bf16 v[124:127], a[188:191], a[220:223], v[124:127]\n\t" \
  "s_and_b32 s98, s2, 0x8\n\t" \
  "ds_read_b64_tr_b16 a[94:95], v199 offset:9216\n\t" \
  "v_xor_b32 v133, v133, v130\n\t" \
  "v_mfma_f32_16x16x32_bf16 v[92:95], a[188:191], a[216:219], v[92:95]\n\t" \
  "s_lshl_b32 s98, s98, 2\n\t" \
  "v_lshlrev_b32 v133, 4, v133\n\t" \
  "v_lshl_add_u32 v133, v130, 8, v133\n\t" \
  "v_mfma_f32_16x16x32_bf16 v[60:63], a[188:191], a[212:215], v[60:63]\n\t" \
  "s_waitcnt vmcnt(25) lgkmcnt(0)\n\t" \
  "s_add_u32 s97, s97, s83\n\t" \
  "v_add_u32 v220, 49152, v133\n\t" \
  "v_mfma_f32_16x16x32_bf16 v[28:31], a[188:191], a[208:211], v[28:31]\n\t" \
  "s_barrier\n\t" \
  "v_mfma_f32_16x16x32_bf16 v[0:3], a[128:131], a[224:227], v[0:3]\n\t" \
  "buffer_load_dwordx4 a[48:51], v248, s[16:19], s97 offen sc1\n\t" \
  "v_mfma_f32_16x16x32_bf16 v[0:3], a[64:67], a[240:243], v[0:3]\n\t" \
  "s_add_u32 s44, s44, s98\n\t" \
  "ds_read_b64_tr_b16 a[160:161], v192 offset:16384\n\t" \
  "v_xor_b32 v221, 64, v220\n\t" \
  "v_mfma_f32_16x16x32_bf16 v[32:35], a[64:67], a[244:247], v[32:35]\n\t" \
  "s_and_b32 s98, s2, 0x3\n\t" \
  "ds_read_b64_tr_b16 a[162:163], v192 offset:17408\n\t" \
  "v_xor_b32 v222, 128, v220\n\t" \
  "v_mfma_f32_16x16x32_bf16 v[32:35], a[128:131], a[228:231], v[32:35]\n\t" \
  "s_lshl_b32 s98, s98, 3\n\t" \
  "v_xor_b32 v223, 192, v220\n\t" \
  "v_xor_b32 v133, v130, v129\n\t" \
  "v_mfma_f32_16x16x32_bf16 v[64:67], a[128:131], a[232:235], v[64:67]\n\t" \
  "buffer_load_dwordx4 a[52:55], v249, s[16:19], s97 offen sc1\n\t" \
  "v_mfma_f32_16x16x32_bf16 v[64:67], a[64:67], a[248:251], v[64:67]\n\t" \
  "s_add_u32 s44, s44, s98\n\t" \
  "ds_read_b64_tr_b16 a[164:165], v193 offset:16384\n\t" \
  "v_lshlrev_b32 v133, 4, v133\n\t" \
  "v_mfma_f32_16x16x32_bf16 v[96:99], a[64:67], a[252:255], v[96:99]\n\t" \
  "s_and_b32 s98, s2, 0x4\n\t" \
  "ds_read_b64_tr_b16 a[166:167], v193 offset:17408\n\t" \
  "v_lshl_add_u32 v224, v129, 8, v133\n\t" \
  "v_mfma_f32_16x16x32_bf16 v[96:99], a[128:131], a[236:239], v[96:99]\n\t" \
  "s_lshl_b32 s98, s98, 0\n\t" \
  "ds_read_b64_tr_b16 a[168:169], v194 offset:16384\n\t" \
  "v_xor_b32 v225, 64, v224\n\t" \
  "v_mfma_f32_16x16x32_bf16 v[4:7], a[132:135], a[224:227], v[4:7]\n\t" \
  "buffer_load_dwordx4 a[56:59], v250, s[16:19], s97 offen sc1\n\t" \
  "v_mfma_f32_16x16x32_bf16 v[4:7], a[68:71], a[240:243], v[4:7]\n\t" \
  "s_add_u32 s44, s44, s98\n\t" \
  "ds_read_b64_tr_b16 a[170:171], v194 offset:17408\n\t" \
  "v_xor_b32 v226, 128, v224\n\t" \
  "v_mfma_f32_16x16x32_bf16 v[36:39], a[68:71], a[244:247], v[36:39]\n\t" \
  "s_add_u32 s44, s44, 0x2000\n\t" \
  "ds_read_b64_tr_b16 a[172:173], v195 offset:16384\n\t" \
  "v_xor_b32 v227, 192, v224\n\t" \
  "v_mfma_f32_16x16x32_bf16 v[36:39], a[132:135], a[228:231], v[36:39]\n\t" \
  "v_lshlrev_b32 v133, 3, v132\n\t" \
  "v_lshl_add_u32 v133, v129, 10, v133\n\t" \
  "v_mfma_f32_16x16x32_bf16 v[68:71], a[132:135], a[232:235], v[68:71]\n\t" \
  "buffer_load_dwordx4 a[60:63], v251, s[16:19], s97 offen sc1\n\t" \
  "v_mfma_f32_16x16x32_bf16 v[68:71], a[68:71], a[248:251], v[68:71]\n\t" \
  "s_add_u32 s97, s78, 5242880\n\t" \
  "s_bfe_u32 s97, s97, 0x140003\n\t" \
  "s_add_u32 s97, s97, s82\n\t" \
  "v_mfma_f32_16x16x32_bf16 v[100:103], a[68:71], a[252:255], v[100:103]\n\t" \
  "ds_read_b64_tr_b16 a[174:175], v195 offset:17408\n\t" \
  "v_lshl_add_u32 v228, v131, 6, v133\n\t" \
  "v_mfma_f32_16x16x32_bf16 v[100:103], a[132:135], a[236:239], v[100:103]\n\t" \
  "buffer_load_dwordx4 a[192:195], v248, s[16:19], s97 offen sc1\n\t" \
  "v_mfma_f32_16x16x32_bf16 v[8:11], a[136:139], a[224:227], v[8:11]\n\t" \
  "ds_read_b64_tr_b16 a[176:177], v196 offset:16384\n\t" \
  "v_lshl_add_u32 v228, v134, 5, v228\n\t" \
  "v_mfma_f32_16x16x32_bf16 v[8:11], a[72:75], a[240:243], v[8:11]\n\t" \
  "ds_read_b64_tr_b16 a[178:179], v196 offset:17408\n\t" \
  "v_add_u32 v228, 81920, v228\n\t" \
  "v_mfma_f32_16x16x32_bf16 v[40:43], a[72:75], a[244:247], v[40:43]\n\t" \
  "ds_read_b64_tr_b16 a[180:181], v197 offset:16384\n\t" \
  "v_xor_b32 v229, 32, v228\n\t" \
  "v_mfma_f32_16x16x32_bf16 v[40:43], a[136:139], a[228:231], v[40:43]\n\t" \
  "buffer_load_dwordx4 a[196:199], v249, s[16:19], s97 offen sc1\n\t" \
  "v_mfma_f32_16x16x32_bf16 v[72:75], a[136:139], a[232:235], v[72:75]\n\t" \
  "ds_read_b64_tr_b16 a[182:183], v197 offset:17408\n\t" \
  "v_lshl_add_u32 v244, v131, 8, v133\n\t" \
  "v_mfma_f32_16x16x32_bf16 v[72:75], a[72:75], a[248:251], v[72:75]\n\t" \
  "ds_read_b64_tr_b16 a[184:185], v198 offset:16384\n\t" \
  "v_lshl_add_u32 v244, v131, 5, v244\n\t" \
  "v_mfma_f32_16x16x32_bf16 v[104:107], a[72:75], a[252:255], v[104:107]\n\t" \
  "ds_read_b64_tr_b16 a[186:187], v198 offset:17408\n\t" \
  "v_lshl_add_u32 v244, v134, 7, v244\n\t" \
  "v_mfma_f32_16x16x32_bf16 v[104:107], a[136:139], a[236:239], v[104:107]\n\t" \
  "buffer_load_dwordx4 a[200:203], v250, s[16:19], s97 offen sc1\n\t" \
  "v_mfma_f32_16x16x32_bf16 v[12:15], a[140:143], a[224:227], v[12:15]\n\t" \
  "ds_read_b64_tr_b16 a[188:189], v199 offset:16384\n\t" \
  "v_lshlrev_b32 v133, 4, v132\n\t" \
  "v_mfma_f32_16x16x32_bf16 v[12:15], a[76:79], a[240:243], v[12:15]\n\t" \
  "ds_read_b64_tr_b16 a[190:191], v199 offset:17408\n\t" \
  "v_lshl_add_u32 v133, v129, 6, v133\n\t" \
  "v_mfma_f32_16x16x32_bf16 v[44:47], a[76:79], a[244:247], v[44:47]\n\t" \
  "ds_read_b64_tr_b16 a[96:97], v192 offset:24576\n\t" \
  "v_lshl_add_u32 v230, v131, 8, v133\n\t" \
  "v_mfma_f32_16x16x32_bf16 v[44:47], a[140:143], a[228:231], v[44:47]\n\t" \
  "buffer_load_dwordx4 a[204:207], v251, s[16:19], s97 offen sc1\n\t" \
  "v_mfma_f32_16x16x32_bf16 v[76:79], a[140:143], a[232:235], v[76:79]\n\t" \
  "ds_read_b64_tr_b16 a[98:99], v192 offset:25600\n\t" \
  "v_xor_b32 v231, 32, v230\n\t" \
  "v_mfma_f32_16x16x32_bf16 v[76:79], a[76:79], a[248:251], v[76:79]\n\t" \
  "s_add_u32 s97, s97, s83\n\t" \
  "v_lshlrev_b32 v133, 1, v129\n\t" \
  "v_xor_b32 v133, v133, v130\n\t" \
  "v_mfma_f32_16x16x32_bf16 v[108:111], a[76:79], a[252:255], v[108:111]\n\t" \
  "ds_read_b64_tr_b16 a[100:101], v193 offset:24576\n\t" \
  "v_lshlrev_b32 v133, 4, v133\n\t" \
  "v_mfma_f32_16x16x32_bf16 v[108:111], a[140:143], a[236:239], v[108:111]\n\t" \
  "ds_read_b64_tr_b16 a[102:103], v193 offset:25600\n\t" \
  "v_lshl_add_u32 v240, v129, 11, v133\n\t" \
  "v_mfma_f32_16x16x32_bf16 v[16:19], a[144:147], a[224:227], v[16:19]\n\t" \
  "buffer_load_dwordx4 a[208:211], v248, s[16:19], s97 offen sc1\n\t" \
  "v_mfma_f32_16x16x32_bf16 v[16:19], a[80:83], a[240:243], v[16:19]\n\t" \
  "ds_read_b64_tr_b16 a[104:105], v194 offset:24576\n\t" \
  "v_xor_b32 v241, 0x80, v240\n\t" \
  "v_mfma_f32_16x16x32_bf16 v[48:51], a[80:83], a[244:247], v[48:51]\n\t" \
  "s_waitcnt vmcnt(21)\n\t" \
  "v_lshlrev_b32 v128, 16, v136\n\t" \
  "v_lshrrev_b32 v133, 1, v233\n\t" \
  "v_mfma_f32_16x16x32_bf16 v[48:51], a[144:147], a[228:231], v[48:51]\n\t" \
  "ds_read_b64_tr_b16 a[106:107], v194 offset:25600\n\t" \
  "v_mul_u32_u24 v133, 57344, v133\n\t" \
  "v_mfma_f32_16x16x32_bf16 v[80:83], a[144:147], a[232:235], v[80:83]\n\t" \
  "buffer_load_dwordx4 a[212:215], v249, s[16:19], s97 offen sc1\n\t" \
  "v_mfma_f32_16x16x32_bf16 v[80:83], a[80:83], a[248:251], v[80:83]\n\t" \
  "ds_read_b64_tr_b16 a[108:109], v195 offset:24576\n\t" \
  "v_and_b32 v134, 1, v233\n\t" \
  "v_mfma_f32_16x16x32_bf16 v[112:115], a[80:83], a[252:255], v[112:115]\n\t" \
  "ds_read_b64_tr_b16 a[110:111], v195 offset:25600\n\t" \
  "v_lshl_add_u32 v133, v134, 4, v133\n\t" \
  "v_mfma_f32_16x16x32_bf16 v[112:115], a[144:147], a[236:239], v[112:115]\n\t" \
  "ds_read_b64_tr_b16 a[112:113], v196 offset:24576\n\t" \
  "v_or_b32 v232, s57, v133\n\t" \
  "v_mfma_f32_16x16x32_bf16 v[20:23], a[148:151], a[224:227], v[20:23]\n\t" \
  "buffer_load_dwordx4 a[216:219], v250, s[16:19], s97 offen sc1\n\t" \
  "v_mfma_f32_16x16x32_bf16 v[20:23], a[84:87], a[240:243], v[20:23]\n\t" \
  "ds_read_b64_tr_b16 a[114:115], v196 offset:25600\n\t" \
  "v_and_b32 v136, 0xffff0000, v136\n\t" \
  "v_mfma_f32_16x16x32_bf16 v[52:55], a[84:87], a[244:247], v[52:55]\n\t" \
  "v_mul_f32 v176, v128, v168\n\t" \
  "v_mul_f32 v180, v136, v168\n\t" \
  "v_mfma_f32_16x16x32_bf16 v[52:55], a[148:151], a[228:231], v[52:55]\n\t" \
  "ds_read_b64_tr_b16 a[116:117], v197 offset:24576\n\t" \
  "v_lshlrev_b32 v128, 16, v137\n\t" \
  "v_mfma_f32_16x16x32_bf16 v[84:87], a[148:151], a[232:235], v[84:87]\n\t" \
  "buffer_load_dwordx4 a[220:223], v251, s[16:19], s97 offen sc1\n\t" \
  "v_mfma_f32_16x16x32_bf16 v[84:87], a[84:87], a[248:251], v[84:87]\n\t" \
  "s_add_u32 s96, s78, 6291456\n\t" \
  "s_and_b32 s96, s96, 0x780000\n\t" \
  "s_add_u32 m0, s76, 32768\n\t" \
  "v_mfma_f32_16x16x32_bf16 v[116:119], a[84:87], a[252:255], v[116:119]\n\t" \
  "ds_read_b64_tr_b16 a[118:119], v197 offset:25600\n\t" \
  "v_and_b32 v137, 0xffff0000, v137\n\t" \
  "v_mfma_f32_16x16x32_bf16 v[116:119], a[148:151], a[236:239], v[116:119]\n\t" \
  "buffer_load_dwordx4 v200, s[20:23], s96 offen offset:0 lds\n\t" \
  "v_mfma_f32_16x16x32_bf16 v[24:27], a[152:155], a[224:227], v[24:27]\n\t" \
  "ds_read_b64_tr_b16 a[120:121], v198 offset:24576\n\t" \
  "v_mul_f32 v177, v128, v168\n\t" \
  "v_mfma_f32_16x16x32_bf16 v[24:27], a[88:91], a[240:243], v[24:27]\n\t" \
  "ds_read_b64_tr_b16 a[122:123], v198 offset:25600\n\t" \
  "v_mul_f32 v181, v137, v168\n\t" \
  "v_mfma_f32_16x16x32_bf16 v[56:59], a[88:91], a[244:247], v[56:59]\n\t" \
  "ds_read_b64_tr_b16 a[124:125], v199 offset:24576\n\t" \
  "v_lshlrev_b32 v128, 16, v138\n\t" \
  "v_mfma_f32_16x16x32_bf16 v[56:59], a[152:155], a[228:231], v[56:59]\n\t" \
  "buffer_load_dwordx4 v201, s[20:23], s96 offen offset:1024 lds\n\t" \
  "v_mfma_f32_16x16x32_bf16 v[88:91], a[152:155], a[232:235], v[88:91]\n\t" \
  "ds_read_b64_tr_b16 a[126:127], v199 offset:25600\n\t" \
  "v_and_b32 v138, 0xffff0000, v138\n\t" \
  "v_mfma_f32_16x16x32_bf16 v[88:91], a[88:91], a[248:251], v[88:91]\n\t" \
  "v_mul_f32 v178, v128, v168\n\t" \
  "v_mul_f32 v182, v138, v168\n\t" \
  "v_mfma_f32_16x16x32_bf16 v[120:123], a[88:91], a[252:255], v[120:123]\n\t" \
  "v_lshlrev_b32 v128, 16, v139\n\t" \
  "v_and_b32 v139, 0xffff0000, v139\n\t" \
  "v_mfma_f32_16x16x32_bf16 v[120:123], a[152:155], a[236:239], v[120:123]\n\t" \
  "buffer_load_dwordx4 v202, s[20:23], s96 offen offset:2048 lds\n\t" \
  "v_mfma_f32_16x16x32_bf16 v[28:31], a[156:159], a[224:227], v[28:31]\n\t" \
  "v_mul_f32 v179, v128, v168\n\t" \
  "v_mul_f32 v183, v139, v168\n\t" \
  "v_mfma_f32_16x16x32_bf16 v[28:31], a[92:95], a[240:243], v[28:31]\n\t" \
  "s_waitcnt vmcnt(26)\n\t" \
  "v_lshlrev_b32 v128, 16, v140\n\t" \
  "v_and_b32 v140, 0xffff0000, v140\n\t" \
  "v_mfma_f32_16x16x32_bf16 v[60:63], a[92:95], a[244:247], v[60:63]\n\t" \
  "v_fma_f32 v176, v128, v169, v176\n\t" \
  "v_fma_f32 v180, v140, v169, v180\n\t" \
  "v_mfma_f32_16x16x32_bf16 v[60:63], a[156:159], a[228:231], v[60:63]\n\t" \
  "buffer_load_dwordx4 v203, s[20:23], s96 offen offset:3072 lds\n\t" \
  "v_mfma_f32_16x16x32_bf16 v[92:95], a[156:159], a[232:235], v[92:95]\n\t" \
  "v_lshlrev_b32 v128, 16, v141\n\t" \
  "v_and_b32 v141, 0xffff0000, v141\n\t" \
  "v_mfma_f32_16x16x32_bf16 v[92:95], a[92:95], a[248:251], v[92:95]\n\t" \
  "s_add_u32 s97, s78, 5767168\n\t" \
  "v_fma_f32 v177, v128, v169, v177\n\t" \
  "v_fma_f32 v181, v141, v169, v181\n\t" \
  "v_mfma_f32_16x16x32_bf16 v[124:127], a[92:95], a[252:255], v[124:127]\n\t" \
  "s_waitcnt lgkmcnt(0)\n\t" \
  "s_bfe_u32 s97, s97, 0x140003\n\t" \
  "s_add_u32 s97, s97, s82\n\t" \
  "v_mfma_f32_16x16x32_bf16 v[124:127], a[156:159], a[236:239], v[124:127]\n\t" \
  "s_barrier\n\t" \
  "v_mfma_f32_16x16x32_bf16 v[0:3], a[160:163], a[0:3], v[0:3]\n\t" \
  "buffer_load_dwordx4 a[224:227], v248, s[16:19], s97 offen sc1\n\t" \
  "v_mfma_f32_16x16x32_bf16 v[0:3], a[96:99], a[16:19], v[0:3]\n\t" \
  "ds_read_b64_tr_b16 a[128:129], v192 offset:32768\n\t" \
  "v_lshlrev_b32 v128, 16, v142\n\t" \
  "v_mfma_f32_16x16x32_bf16 v[32:35], a[96:99], a[20:23], v[32:35]\n\t" \
  "ds_read_b64_tr_b16 a[130:131], v192 offset:33792\n\t" \
  "v_and_b32 v142, 0xffff0000, v142\n\t" \
  "v_mfma_f32_16x16x32_bf16 v[32:35], a[160:163], a[4:7], v[32:35]\n\t" \
  "v_fma_f32 v178, v128, v169, v178\n\t" \
  "v_fma_f32 v182, v142, v169, v182\n\t" \
  "v_mfma_f32_16x16x32_bf16 v[64:67], a[160:163], a[8:11], v[64:67]\n\t" \
  "buffer_load_dwordx4 a[228:231], v249, s[16:19], s97 offen sc1\n\t" \
  "v_mfma_f32_16x16x32_bf16 v[64:67], a[96:99], a[24:27], v[64:67]\n\t" \
  "ds_read_b64_tr_b16 a[132:133], v193 offset:32768\n\t" \
  "v_lshlrev_b32 v128, 16, v143\n\t" \
  "v_mfma_f32_16x16x32_bf16 v[96:99], a[96:99], a[28:31], v[96:99]\n\t" \
  "ds_read_b64_tr_b16 a[134:135], v193 offset:33792\n\t" \
  "v_and_b32 v143, 0xffff0000, v143\n\t" \
  "v_mfma_f32_16x16x32_bf16 v[96:99], a[160:163], a[12:15], v[96:99]\n\t" \
  "ds_read_b64_tr_b16 a[136:137], v194 offset:32768\n\t" \
  "v_fma_f32 v179, v128, v169, v179\n\t" \
  "v_mfma_f32_16x16x32_bf16 v[4:7], a[164:167], a[0:3], v[4:7]\n\t" \
  "buffer_load_dwordx4 a[232:235], v250, s[16:19], s97 offen sc1\n\t" \
  "v_mfma_f32_16x16x32_bf16 v[4:7], a[100:103], a[16:19], v[4:7]\n\t" \
  "ds_read_b64_tr_b16 a[138:139], v194 offset:33792\n\t" \
  "v_fma_f32 v183, v143, v169, v183\n\t" \
  "v_mfma_f32_16x16x32_bf16 v[36:39], a[100:103], a[20:23], v[36:39]\n\t" \
  "s_waitcnt vmcnt(29)\n\t" \
  "v_lshlrev_b32 v128, 16, v144\n\t" \
  "v_and_b32 v144, 0xffff0000, v144\n\t" \
  "v_mfma_f32_16x16x32_bf16 v[36:39], a[164:167], a[4:7], v[36:39]\n\t" \
  "ds_read_b64_tr_b16 a[140:141], v195 offset:32768\n\t" \
  "v_fma_f32 v176, v128, v170, v176\n\t" \
  "v_mfma_f32_16x16x32_bf16 v[68:71], a[164:167], a[8:11], v[68:71]\n\t" \
  "buffer_load_dwordx4 a[236:239], v251, s[16:19], s97 offen sc1\n\t" \
  "v_mfma_f32_16x16x32_bf16 v[68:71], a[100:103], a[24:27], v[68:71]\n\t" \
  "s_add_u32 s96, s78, 6815744\n\t" \
  "s_and_b32 s96, s96, 0x780000\n\t" \
  "s_add_u32 m0, s76, 65536\n\t" \
  "v_mfma_f32_16x16x32_bf16 v[100:103], a[100:103], a[28:31], v[100:103]\n\t" \
  "ds_read_b64_tr_b16 a[142:143], v195 offset:33792\n\t" \
  "v_fma_f32 v180, v144, v170, v180\n\t" \
  "v_mfma_f32_16x16x32_bf16 v[100:103], a[164:167], a[12:15], v[100:103]\n\t" \
  "buffer_load_dwordx4 v200, s[20:23], s96 offen offset:0 lds\n\t" \
  "v_mfma_f32_16x16x32_bf16 v[8:11], a[168:171], a[0:3], v[8:11]\n\t" \
  "ds_read_b64_tr_b16 a[144:145], v196 offset:32768\n\t" \
  "v_lshlrev_b32 v128, 16, v145\n\t" \
  "v_mfma_f32_16x16x32_bf16 v[8:11], a[104:107], a[16:19], v[8:11]\n\t" \
  "ds_read_b64_tr_b16 a[146:147], v196 offset:33792\n\t" \
  "v_and_b32 v145, 0xffff0000, v145\n\t" \
  "v_mfma_f32_16x16x32_bf16 v[40:43], a[104:107], a[20:23], v[40:43]\n\t" \
  "ds_read_b64_tr_b16 a[148:149], v197 offset:32768\n\t" \
  "v_fma_f32 v177, v128, v170, v177\n\t" \
  "v_mfma_f32_16x16x32_bf16 v[40:43], a[168:171], a[4:7], v[40:43]\n\t" \
  "buffer_load_dwordx4 v201, s[20:23], s96 offen offset:1024 lds\n\t" \
  "v_mfma_f32_16x16x32_bf16 v[72:75], a[168:171], a[8:11], v[72:75]\n\t" \
  "ds_read_b64_tr_b16 a[150:151], v197 offset:33792\n\t" \
  "v_fma_f32 v181, v145, v170, v181\n\t" \
  "v_mfma_f32_16x16x32_bf16 v[72:75], a[104:107], a[24:27], v[72:75]\n\t" \
  "ds_read_b64_tr_b16 a[152:153], v198 offset:32768\n\t" \
  "v_lshlrev_b32 v128, 16, v146\n\t" \
  "v_mfma_f32_16x16x32_bf16 v[104:107], a[104:107], a[28:31], v[104:107]\n\t" \
  "ds_read_b64_tr_b16 a[154:155], v198 offset:33792\n\t" \
  "v_and_b32 v146, 0xffff0000, v146\n\t" \
  "v_mfma_f32_16x16x32_bf16 v[104:107], a[168:171], a[12:15], v[104:107]\n\t" \
  "buffer_load_dwordx4 v202, s[20:23], s96 offen offset:2048 lds\n\t" \
  "v_mfma_f32_16x16x32_bf16 v[12:15], a[172:175], a[0:3], v[12:15]\n\t" \
  "ds_read_b64_tr_b16 a[156:157], v199 offset:32768\n\t" \
  "v_fma_f32 v178, v128, v170, v178\n\t" \
  "v_mfma_f32_16x16x32_bf16 v[12:15], a[108:111], a[16:19], v[12:15]\n\t" \
  "ds_read_b64_tr_b16 a[158:159], v199 offset:33792\n\t" \
  "v_fma_f32 v182, v146, v170, v182\n\t" \
  "v_mfma_f32_16x16x32_bf16 v[44:47], a[108:111], a[20:23], v[44:47]\n\t" \
  "ds_read_b64_tr_b16 a[64:65], v192 offset:40960\n\t" \
  "v_lshlrev_b32 v128, 16, v147\n\t" \
  "v_mfma_f32_16x16x32_bf16 v[44:47], a[172:175], a[4:7], v[44:47]\n\t" \
  "buffer_load_dwordx4 v203, s[20:23], s96 offen offset:3072 lds\n\t" \
  "v_mfma_f32_16x16x32_bf16 v[76:79], a[172:175], a[8:11], v[76:79]\n\t" \
  "ds_read_b64_tr_b16 a[66:67], v192 offset:41984\n\t" \
  "v_and_b32 v147, 0xffff0000, v147\n\t" \
  "v_mfma_f32_16x16x32_bf16 v[76:79], a[108:111], a[24:27], v[76:79]\n\t" \
  "s_add_u32 s97, s97, s83\n\t" \
  "v_fma_f32 v179, v128, v170, v179\n\t" \
  "v_fma_f32 v183, v147, v170, v183\n\t" \
  "v_mfma_f32_16x16x32_bf16 v[108:111], a[108:111], a[28:31], v[108:111]\n\t" \
  "ds_read_b64_tr_b16 a[68:69], v193 offset:40960\n\t" \
  "v_mfma_f32_16x16x32_bf16 v[108:111], a[172:175], a[12:15], v[108:111]\n\t" \
  "s_waitcnt vmcnt(33)\n\t" \
  "v_lshlrev_b32 v128, 16, v148\n\t" \
  "v_and_b32 v148, 0xffff0000, v148\n\t" \
  "v_mfma_f32_16x16x32_bf16 v[16:19], a[176:179], a[0:3], v[16:19]\n\t" \
  "buffer_load_dwordx4 a[240:243], v248, s[16:19], s97 offen sc1\n\t" \
  "v_mfma_f32_16x16x32_bf16 v[16:19], a[112:115], a[16:19], v[16:19]\n\t" \
  "ds_read_b64_tr_b16 a[70:71], v193 offset:41984\n\t" \
  "v_fma_f32 v176, v128, v171, v176\n\t" \
  "v_mfma_f32_16x16x32_bf16 v[48:51], a[112:115], a[20:23], v[48:51]\n\t" \
  "ds_read_b64_tr_b16 a[72:73], v194 offset:40960\n\t" \
  "v_fma_f32 v180, v148, v171, v180\n\t" \
  "v_mfma_f32_16x16x32_bf16 v[48:51], a[176:179], a[4:7], v[48:51]\n\t" \
  "ds_read_b64_tr_b16 a[74:75], v194 offset:41984\n\t" \
  "v_lshlrev_b32 v128, 16, v149\n\t" \
  "v_mfma_f32_16x16x32_bf16 v[80:83], a[176:179], a[8:11], v[80:83]\n\t" \
  "ds_read_b64_tr_b16 a[76:77], v195 offset:40960\n\t" \
  "v_and_b32 v149, 0xffff0000, v149\n\t" \
  "v_mfma_f32_16x16x32_bf16 v[80:83], a[112:115], a[24:27], v[80:83]\n\t" \
  "v_fma_f32 v177, v128, v171, v177\n\t" \
  "v_fma_f32 v181, v149, v171, v181\n\t" \
  "v_mfma_f32_16x16x32_bf16 v[112:115], a[112:115], a[28:31], v[112:115]\n\t" \
  "ds_read_b64_tr_b16 a[78:79], v195 offset:41984\n\t" \
  "v_lshlrev_b32 v128, 16, v150\n\t" \
  "v_mfma_f32_16x16x32_bf16 v[112:115], a[176:179], a[12:15], v[112:115]\n\t" \
  "ds_read_b64_tr_b16 a[80:81], v196 offset:40960\n\t" \
  "v_and_b32 v150, 0xffff0000, v150\n\t" \
  "v_mfma_f32_16x16x32_bf16 v[20:23], a[180:183], a[0:3], v[20:23]\n\t" \
  "buffer_load_dwordx4 a[244:247], v249, s[16:19], s97 offen sc1\n\t" \
  "v_mfma_f32_16x16x32_bf16 v[20:23], a[116:119], a[16:19], v[20:23]\n\t" \
  "ds_read_b64_tr_b16 a[82:83], v196 offset:41984\n\t" \
  "v_fma_f32 v178, v128, v171, v178\n\t" \
  "v_mfma_f32_16x16x32_bf16 v[52:55], a[116:119], a[20:23], v[52:55]\n\t" \
  "v_fma_f32 v182, v150, v171, v182\n\t" \
  "v_lshlrev_b32 v128, 16, v151\n\t" \
  "v_mfma_f32_16x16x32_bf16 v[52:55], a[180:183], a[4:7], v[52:55]\n\t" \
  "ds_read_b64_tr_b16 a[84:85], v197 offset:40960\n\t" \
  "v_and_b32 v151, 0xffff0000, v151\n\t" \
  "v_mfma_f32_16x16x32_bf16 v[84:87], a[180:183], a[8:11], v[84:87]\n\t" \
  "ds_read_b64_tr_b16 a[86:87], v197 offset:41984\n\t" \
  "v_fma_f32 v179, v128, v171, v179\n\t" \
  "v_mfma_f32_16x16x32_bf16 v[84:87], a[116:119], a[24:27], v[84:87]\n\t" \
  "s_cmp_lt_i32 s91, 0\n\t" \
  "s_cselect_b32 s44, s91, s44\n\t" \
  "v_fma_f32 v183, v151, v171, v183\n\t" \
  "v_mfma_f32_16x16x32_bf16 v[116:119], a[116:119], a[28:31], v[116:119]\n\t" \
  "ds_read_b64_tr_b16 a[88:89], v198 offset:40960\n\t" \
  "v_mfma_f32_16x16x32_bf16 v[116:119], a[180:183], a[12:15], v[116:119]\n\t" \
  "buffer_load_dwordx4 a[248:251], v250, s[16:19], s97 offen sc1\n\t" \
  "v_mfma_f32_16x16x32_bf16 v[24:27], a[184:187], a[0:3], v[24:27]\n\t" \
  "ds_read_b64_tr_b16 a[90:91], v198 offset:41984\n\t" \
  "v_mfma_f32_16x16x32_bf16 v[24:27], a[120:123], a[16:19], v[24:27]\n\t" \
  "ds_read_b64_tr_b16 a[92:93], v199 offset:40960\n\t" \
  "v_mfma_f32_16x16x32_bf16 v[56:59], a[120:123], a[20:23], v[56:59]\n\t" \
  "ds_read_b64_tr_b16 a[94:95], v199 offset:41984\n\t" \
  "v_mfma_f32_16x16x32_bf16 v[56:59], a[184:187], a[4:7], v[56:59]\n\t" \
  "v_mfma_f32_16x16x32_bf16 v[88:91], a[184:187], a[8:11], v[88:91]\n\t" \
  "v_mfma_f32_16x16x32_bf16 v[88:91], a[120:123], a[24:27], v[88:91]\n\t" \
  "buffer_load_dwordx4 a[252:255], v251, s[16:19], s97 offen sc1\n\t" \
  "v_mfma_f32_16x16x32_bf16 v[120:123], a[120:123], a[28:31], v[120:123]\n\t" \
  "v_mfma_f32_16x16x32_bf16 v[120:123], a[184:187], a[12:15], v[120:123]\n\t" \
  "v_mfma_f32_16x16x32_bf16 v[28:31], a[188:191], a[0:3], v[28:31]\n\t" \
  "v_mfma_f32_16x16x32_bf16 v[28:31], a[124:127], a[16:19], v[28:31]\n\t" \
  "v_mfma_f32_16x16x32_bf16 v[60:63], a[124:127], a[20:23], v[60:63]\n\t" \
  "v_mfma_f32_16x16x32_bf16 v[60:63], a[188:191], a[4:7], v[60:63]\n\t" \
  "v_mfma_f32_16x16x32_bf16 v[92:95], a[188:191], a[8:11], v[92:95]\n\t" \
  "v_mfma_f32_16x16x32_bf16 v[92:95], a[124:127], a[24:27], v[92:95]\n\t" \
  "s_add_u32 s97, s78, 6291456\n\t" \
  "s_bfe_u32 s97, s97, 0x140003\n\t" \
  "s_add_u32 s97, s97, s82\n\t" \
  "v_mfma_f32_16x16x32_bf16 v[124:127], a[124:127], a[28:31], v[124:127]\n\t" \
  "s_waitcnt vmcnt(24) lgkmcnt(0)\n\t" \
  "v_mfma_f32_16x16x32_bf16 v[124:127], a[188:191], a[12:15], v[124:127]\n\t" \
  "s_barrier\n\t" \
  "v_mfma_f32_16x16x32_bf16 v[0:3], a[128:131], a[32:35], v[0:3]\n\t" \
  "buffer_load_dwordx4 a[0:3], v248, s[16:19], s97 offen sc1\n\t" \
  "v_mfma_f32_16x16x32_bf16 v[0:3], a[64:67], a[48:51], v[0:3]\n\t" \
  "ds_read_b64_tr_b16 a[160:161], v184 offset:0\n\t" \
  "v_mfma_f32_16x16x32_bf16 v[32:35], a[64:67], a[52:55], v[32:35]\n\t" \
  "ds_read_b64_tr_b16 a[162:163], v184 offset:1024\n\t" \
  "v_mfma_f32_16x16x32_bf16 v[32:35], a[128:131], a[36:39], v[32:35]\n\t" \
  "buffer_load_dwordx4 a[4:7], v249, s[16:19], s97 offen sc1\n\t" \
  "v_mfma_f32_16x16x32_bf16 v[64:67], a[128:131], a[40:43], v[64:67]\n\t" \
  "ds_read_b64_tr_b16 a[164:165], v185 offset:0\n\t" \
  "v_mfma_f32_16x16x32_bf16 v[64:67], a[64:67], a[56:59], v[64:67]\n\t" \
  "buffer_load_dwordx4 a[8:11], v250, s[16:19], s97 offen sc1\n\t" \
  "v_mfma_f32_16x16x32_bf16 v[96:99], a[64:67], a[60:63], v[96:99]\n\t" \
  "ds_read_b64_tr_b16 a[166:167], v185 offset:1024\n\t" \
  "v_mfma_f32_16x16x32_bf16 v[96:99], a[128:131], a[44:47], v[96:99]\n\t" \
  "ds_read_b64_tr_b16 a[168:169], v186 offset:0\n\t" \
  "v_mfma_f32_16x16x32_bf16 v[4:7], a[132:135], a[32:35], v[4:7]\n\t" \
  "buffer_load_dwordx4 a[12:15], v251, s[16:19], s97 offen sc1\n\t" \
  "v_mfma_f32_16x16x32_bf16 v[4:7], a[68:71], a[48:51], v[4:7]\n\t" \
  "s_add_u32 s96, s78, 7340032\n\t" \
  "s_and_b32 s96, s96, 0x780000\n\t" \
  "s_add_u32 m0, s76, 81920\n\t" \
  "v_mfma_f32_16x16x32_bf16 v[36:39], a[68:71], a[52:55], v[36:39]\n\t" \
  "buffer_load_dwordx4 v200, s[20:23], s96 offen offset:0 lds\n\t" \
  "v_mfma_f32_16x16x32_bf16 v[36:39], a[132:135], a[36:39], v[36:39]\n\t" \
  "ds_read_b64_tr_b16 a[170:171], v186 offset:1024\n\t" \
  "v_mfma_f32_16x16x32_bf16 v[68:71], a[132:135], a[40:43], v[68:71]\n\t" \
  "ds_read_b64_tr_b16 a[172:173], v187 offset:0\n\t" \
  "v_mfma_f32_16x16x32_bf16 v[68:71], a[68:71], a[56:59], v[68:71]\n\t" \
  "buffer_load_dwordx4 v201, s[20:23], s96 offen offset:1024 lds\n\t" \
  "v_mfma_f32_16x16x32_bf16 v[100:103], a[68:71], a[60:63], v[100:103]\n\t" \
  "ds_read_b64_tr_b16 a[174:175], v187 offset:1024\n\t" \
  "v_mfma_f32_16x16x32_bf16 v[100:103], a[132:135], a[44:47], v[100:103]\n\t" \
  "buffer_load_dwordx4 v202, s[20:23], s96 offen offset:2048 lds\n\t" \
  "v_mfma_f32_16x16x32_bf16 v[8:11], a[136:139], a[32:35], v[8:11]\n\t" \
  "ds_read_b64_tr_b16 a[176:177], v188 offset:0\n\t" \
  "v_mfma_f32_16x16x32_bf16 v[8:11], a[72:75], a[48:51], v[8:11]\n\t" \
  "ds_read_b64_tr_b16 a[178:179], v188 offset:1024\n\t" \
  "v_mfma_f32_16x16x32_bf16 v[40:43], a[72:75], a[52:55], v[40:43]\n\t" \
  "buffer_load_dwordx4 v203, s[20:23], s96 offen offset:3072 lds\n\t" \
  "v_mfma_f32_16x16x32_bf16 v[40:43], a[136:139], a[36:39], v[40:43]\n\t" \
  "ds_read_b64_tr_b16 a[180:181], v189 offset:0\n\t" \
  "s_add_u32 s97, s97, s83\n\t" \
  "v_mfma_f32_16x16x32_bf16 v[72:75], a[136:139], a[40:43], v[72:75]\n\t" \
  "buffer_load_dwordx4 a[16:19], v248, s[16:19], s97 offen sc1\n\t" \
  "v_mfma_f32_16x16x32_bf16 v[72:75], a[72:75], a[56:59], v[72:75]\n\t" \
  "ds_read_b64_tr_b16 a[182:183], v189 offset:1024\n\t" \
  "v_mfma_f32_16x16x32_bf16 v[104:107], a[72:75], a[60:63], v[104:107]\n\t" \
  "ds_read_b64_tr_b16 a[184:185], v190 offset:0\n\t" \
  "v_mfma_f32_16x16x32_bf16 v[104:107], a[136:139], a[44:47], v[104:107]\n\t" \
  "buffer_load_dwordx4 a[20:23], v249, s[16:19], s97 offen sc1\n\t" \
  "v_mfma_f32_16x16x32_bf16 v[12:15], a[140:143], a[32:35], v[12:15]\n\t" \
  "ds_read_b64_tr_b16 a[186:187], v190 offset:1024\n\t" \
  "v_mfma_f32_16x16x32_bf16 v[12:15], a[76:79], a[48:51], v[12:15]\n\t" \
  "buffer_load_dwordx4 a[24:27], v250, s[16:19], s97 offen sc1\n\t" \
  "v_mfma_f32_16x16x32_bf16 v[44:47], a[76:79], a[52:55], v[44:47]\n\t" \
  "ds_read_b64_tr_b16 a[188:189], v191 offset:0\n\t" \
  "v_mfma_f32_16x16x32_bf16 v[44:47], a[140:143], a[36:39], v[44:47]\n\t" \
  "ds_read_b64_tr_b16 a[190:191], v191 offset:1024\n\t" \
  "v_mfma_f32_16x16x32_bf16 v[76:79], a[140:143], a[40:43], v[76:79]\n\t" \
  "buffer_load_dwordx4 a[28:31], v251, s[16:19], s97 offen sc1\n\t" \
  "v_mfma_f32_16x16x32_bf16 v[76:79], a[76:79], a[56:59], v[76:79]\n\t" \
  "ds_read_b64_tr_b16 a[96:97], v184 offset:8192\n\t" \
  "s_mov_b32 s99, 49152\n\t" \
  "v_mfma_f32_16x16x32_bf16 v[108:111], a[76:79], a[60:63], v[108:111]\n\t" \
  "ds_read_b64_tr_b16 a[98:99], v184 offset:9216\n\t" \
  "v_mfma_f32_16x16x32_bf16 v[108:111], a[140:143], a[44:47], v[108:111]\n\t" \
  "ds_read_b64_tr_b16 a[100:101], v185 offset:8192\n\t" \
  "v_mfma_f32_16x16x32_bf16 v[16:19], a[144:147], a[32:35], v[16:19]\n\t" \
  "buffer_load_dwordx4 v[152:155], v209, s[8:11], s99 offen nt\n\t" \
  "v_mfma_f32_16x16x32_bf16 v[16:19], a[80:83], a[48:51], v[16:19]\n\t" \
  "ds_read_b64_tr_b16 a[102:103], v185 offset:9216\n\t" \
  "s_mov_b32 s99, 61440\n\t" \
  "v_mfma_f32_16x16x32_bf16 v[48:51], a[80:83], a[52:55], v[48:51]\n\t" \
  "ds_read_b64_tr_b16 a[104:105], v186 offset:8192\n\t" \
  "v_mfma_f32_16x16x32_bf16 v[48:51], a[144:147], a[36:39], v[48:51]\n\t" \
  "ds_read_b64_tr_b16 a[106:107], v186 offset:9216\n\t" \
  "v_mfma_f32_16x16x32_bf16 v[80:83], a[144:147], a[40:43], v[80:83]\n\t" \
  "ds_read_b64_tr_b16 a[108:109], v187 offset:8192\n\t" \
  "v_mfma_f32_16x16x32_bf16 v[80:83], a[80:83], a[56:59], v[80:83]\n\t" \
  "buffer_load_dwordx4 v[156:159], v209, s[8:11], s99 offen nt\n\t" \
  "v_mfma_f32_16x16x32_bf16 v[112:115], a[80:83], a[60:63], v[112:115]\n\t" \
  "ds_read_b64_tr_b16 a[110:111], v187 offset:9216\n\t" \
  "s_mov_b32 s99, 73728\n\t" \
  "v_mfma_f32_16x16x32_bf16 v[112:115], a[144:147], a[44:47], v[112:115]\n\t" \
  "ds_read_b64_tr_b16 a[112:113], v188 offset:8192\n\t" \
  "v_mfma_f32_16x16x32_bf16 v[20:23], a[148:151], a[32:35], v[20:23]\n\t" \
  "ds_read_b64_tr_b16 a[114:115], v188 offset:9216\n\t" \
  "v_mfma_f32_16x16x32_bf16 v[20:23], a[84:87], a[48:51], v[20:23]\n\t" \
  "ds_read_b64_tr_b16 a[116:117], v189 offset:8192\n\t" \
  "v_mfma_f32_16x16x32_bf16 v[52:55], a[84:87], a[52:55], v[52:55]\n\t" \
  "ds_read_b64_tr_b16 a[118:119], v189 offset:9216\n\t" \
  "v_mfma_f32_16x16x32_bf16 v[52:55], a[148:151], a[36:39], v[52:55]\n\t" \
  "buffer_load_dwordx4 v[160:163], v209, s[8:11], s99 offen nt\n\t" \
  "v_mfma_f32_16x16x32_bf16 v[84:87], a[148:151], a[40:43], v[84:87]\n\t" \
  "ds_read_b64_tr_b16 a[120:121], v190 offset:8192\n\t" \
  "s_mov_b32 s99, 86016\n\t" \
  "v_mfma_f32_16x16x32_bf16 v[84:87], a[84:87], a[56:59], v[84:87]\n\t" \
  "ds_read_b64_tr_b16 a[122:123], v190 offset:9216\n\t" \
  "v_mfma_f32_16x16x32_bf16 v[116:119], a[84:87], a[60:63], v[116:119]\n\t" \
  "ds_read_b64_tr_b16 a[124:125], v191 offset:8192\n\t" \
  "v_mfma_f32_16x16x32_bf16 v[116:119], a[148:151], a[44:47], v[116:119]\n\t" \
  "ds_read_b64_tr_b16 a[126:127], v191 offset:9216\n\t" \
  "v_mfma_f32_16x16x32_bf16 v[24:27], a[152:155], a[32:35], v[24:27]\n\t" \
  "v_mfma_f32_16x16x32_bf16 v[24:27], a[88:91], a[48:51], v[24:27]\n\t" \
  "buffer_load_dwordx4 v[164:167], v209, s[8:11], s99 offen nt\n\t" \
  "v_mfma_f32_16x16x32_bf16 v[56:59], a[88:91], a[52:55], v[56:59]\n\t" \
  "s_add_u32 s99, s53, 0\n\t" \
  "v_mfma_f32_16x16x32_bf16 v[56:59], a[152:155], a[36:39], v[56:59]\n\t" \
  "v_mfma_f32_16x16x32_bf16 v[88:91], a[152:155], a[40:43], v[88:91]\n\t" \
  "v_mfma_f32_16x16x32_bf16 v[88:91], a[88:91], a[56:59], v[88:91]\n\t" \
  "v_mfma_f32_16x16x32_bf16 v[120:123], a[88:91], a[60:63], v[120:123]\n\t" \
  "v_mfma_f32_16x16x32_bf16 v[120:123], a[152:155], a[44:47], v[120:123]\n\t" \
  "buffer_load_dword v246, v245, s[68:71], s99 offen\n\t" \
  "v_mfma_f32_16x16x32_bf16 v[28:31], a[156:159], a[32:35], v[28:31]\n\t" \
  "v_mfma_f32_16x16x32_bf16 v[28:31], a[92:95], a[48:51], v[28:31]\n\t" \
  "v_mfma_f32_16x16x32_bf16 v[60:63], a[92:95], a[52:55], v[60:63]\n\t" \
  "v_mfma_f32_16x16x32_bf16 v[60:63], a[156:159], a[36:39], v[60:63]\n\t" \
  "v_mfma_f32_16x16x32_bf16 v[92:95], a[156:159], a[40:43], v[92:95]\n\t" \
  "v_mfma_f32_16x16x32_bf16 v[92:95], a[92:95], a[56:59], v[92:95]\n\t" \
  "v_mfma_f32_16x16x32_bf16 v[124:127], a[92:95], a[60:63], v[124:127]\n\t" \
  "s_waitcnt vmcnt(33) lgkmcnt(0)\n\t" \
  "v_mfma_f32_16x16x32_bf16 v[124:127], a[156:159], a[44:47], v[124:127]\n\t" \
  "s_barrier\n\t" \
  "v_mfma_f32_16x16x32_bf16 v[0:3], a[160:163], a[192:195], v[0:3]\n\t" \
  "ds_read_b64_tr_b16 a[128:129], v184 offset:16384\n\t" \
  "v_mfma_f32_16x16x32_bf16 v[0:3], a[96:99], a[208:211], v[0:3]\n\t" \
  "ds_read_b64_tr_b16 a[130:131], v184 offset:17408\n\t" \
  "v_mfma_f32_16x16x32_bf16 v[32:35], a[96:99], a[212:215], v[32:35]\n\t" \
  "ds_read_b64_tr_b16 a[132:133], v185 offset:16384\n\t" \
  "v_mfma_f32_16x16x32_bf16 v[32:35], a[160:163], a[196:199], v[32:35]\n\t" \
  "ds_read_b64_tr_b16 a[134:135], v185 offset:17408\n\t" \
  "v_mfma_f32_16x16x32_bf16 v[64:67], a[160:163], a[200:203], v[64:67]\n\t" \
  "ds_read_b64_tr_b16 a[136:137], v186 offset:16384\n\t" \
  "v_mfma_f32_16x16x32_bf16 v[64:67], a[96:99], a[216:219], v[64:67]\n\t" \
  "ds_read_b64_tr_b16 a[138:139], v186 offset:17408\n\t" \
  "v_mfma_f32_16x16x32_bf16 v[96:99], a[96:99], a[220:223], v[96:99]\n\t" \
  "ds_read_b64_tr_b16 a[140:141], v187 offset:16384\n\t" \
  "v_mfma_f32_16x16x32_bf16 v[96:99], a[160:163], a[204:207], v[96:99]\n\t" \
  "ds_read_b64_tr_b16 a[142:143], v187 offset:17408\n\t" \
  "v_mfma_f32_16x16x32_bf16 v[4:7], a[164:167], a[192:195], v[4:7]\n\t" \
  "ds_read_b64_tr_b16 a[144:145], v188 offset:16384\n\t" \
  "v_mfma_f32_16x16x32_bf16 v[4:7], a[100:103], a[208:211], v[4:7]\n\t" \
  "ds_read_b64_tr_b16 a[146:147], v188 offset:17408\n\t" \
  "v_mfma_f32_16x16x32_bf16 v[36:39], a[100:103], a[212:215], v[36:39]\n\t" \
  "ds_read_b64_tr_b16 a[148:149], v189 offset:16384\n\t" \
  "v_mfma_f32_16x16x32_bf16 v[36:39], a[164:167], a[196:199], v[36:39]\n\t" \
  "ds_read_b64_tr_b16 a[150:151], v189 offset:17408\n\t" \
  "v_mfma_f32_16x16x32_bf16 v[68:71], a[164:167], a[200:203], v[68:71]\n\t" \
  "ds_read_b64_tr_b16 a[152:153], v190 offset:16384\n\t" \
  "v_mfma_f32_16x16x32_bf16 v[68:71], a[100:103], a[216:219], v[68:71]\n\t" \
  "ds_read_b64_tr_b16 a[154:155], v190 offset:17408\n\t" \
  "v_mfma_f32_16x16x32_bf16 v[100:103], a[100:103], a[220:223], v[100:103]\n\t" \
  "ds_read_b64_tr_b16 a[156:157], v191 offset:16384\n\t" \
  "v_mfma_f32_16x16x32_bf16 v[100:103], a[164:167], a[204:207], v[100:103]\n\t" \
  "ds_read_b64_tr_b16 a[158:159], v191 offset:17408\n\t" \
  "v_mfma_f32_16x16x32_bf16 v[8:11], a[168:171], a[192:195], v[8:11]\n\t" \
  "ds_read_b64_tr_b16 a[64:65], v184 offset:24576\n\t" \
  "v_mfma_f32_16x16x32_bf16 v[8:11], a[104:107], a[208:211], v[8:11]\n\t" \
  "v_mfma_f32_16x16x32_bf16 v[40:43], a[104:107], a[212:215], v[40:43]\n\t" \
  "ds_read_b64_tr_b16 a[66:67], v184 offset:25600\n\t" \
  "v_mfma_f32_16x16x32_bf16 v[40:43], a[168:171], a[196:199], v[40:43]\n\t" \
  "v_mfma_f32_16x16x32_bf16 v[72:75], a[168:171], a[200:203], v[72:75]\n\t" \
  "ds_read_b64_tr_b16 a[68:69], v185 offset:24576\n\t" \
  "v_mfma_f32_16x16x32_bf16 v[72:75], a[104:107], a[216:219], v[72:75]\n\t" \
  "ds_read_b64_tr_b16 a[70:71], v185 offset:25600\n\t" \
  "v_mfma_f32_16x16x32_bf16 v[104:107], a[104:107], a[220:223], v[104:107]\n\t" \
  "v_mfma_f32_16x16x32_bf16 v[104:107], a[168:171], a[204:207], v[104:107]\n\t" \
  "ds_read_b64_tr_b16 a[72:73], v186 offset:24576\n\t" \
  "v_mfma_f32_16x16x32_bf16 v[12:15], a[172:175], a[192:195], v[12:15]\n\t" \
  "v_mfma_f32_16x16x32_bf16 v[12:15], a[108:111], a[208:211], v[12:15]\n\t" \
  "ds_read_b64_tr_b16 a[74:75], v186 offset:25600\n\t" \
  "v_mfma_f32_16x16x32_bf16 v[44:47], a[108:111], a[212:215], v[44:47]\n\t" \
  "ds_read_b64_tr_b16 a[76:77], v187 offset:24576\n\t" \
  "v_mfma_f32_16x16x32_bf16 v[44:47], a[172:175], a[196:199], v[44:47]\n\t" \
  ".Lhs4:\n\ts_cbranch_execz .Lhs4n\n\t" \
  "v_mfma_f32_16x16x32_bf16 v[76:79], a[172:175], a[200:203], v[76:79]\n\t" \
  "ds_read_b64_tr_b16 a[78:79], v187 offset:25600\n\t" \
  "v_mfma_f32_16x16x32_bf16 v[76:79], a[108:111], a[216:219], v[76:79]\n\t" \
  "s_add_u32 s97, s78, 6815744\n\t" \
  "ds_read_b64_tr_b16 a[80:81], v188 offset:24576\n\t" \
  "s_bfe_u32 s97, s97, 0x140003\n\t" \
  "v_mfma_f32_16x16x32_bf16 v[108:111], a[108:111], a[220:223], v[108:111]\n\t" \
  "s_add_u32 s97, s97, s82\n\t" \
  "v_mfma_f32_16x16x32_bf16 v[108:111], a[172:175], a[204:207], v[108:111]\n\t" \
  "buffer_load_dwordx4 a[32:35], v248, s[16:19], s97 offen sc1\n\t" \
  "v_mfma_f32_16x16x32_bf16 v[16:19], a[176:179], a[192:195], v[16:19]\n\t" \
  "ds_read_b64_tr_b16 a[82:83], v188 offset:25600\n\t" \
  "v_mfma_f32_16x16x32_bf16 v[16:19], a[112:115], a[208:211], v[16:19]\n\t" \
  "ds_read_b64_tr_b16 a[84:85], v189 offset:24576\n\t" \
  "v_mfma_f32_16x16x32_bf16 v[48:51], a[112:115], a[212:215], v[48:51]\n\t" \
  "buffer_load_dwordx4 a[36:39], v249, s[16:19], s97 offen sc1\n\t" \
  "v_mfma_f32_16x16x32_bf16 v[48:51], a[176:179], a[196:199], v[48:51]\n\t" \
  "ds_read_b64_tr_b16 a[86:87], v189 offset:25600\n\t" \
  "v_mfma_f32_16x16x32_bf16 v[80:83], a[176:179], a[200:203], v[80:83]\n\t" \
  "ds_read_b64_tr_b16 a[88:89], v190 offset:24576\n\t" \
  "v_mfma_f32_16x16x32_bf16 v[80:83], a[112:115], a[216:219], v[80:83]\n\t" \
  "v_mfma_f32_16x16x32_bf16 v[112:115], a[112:115], a[220:223], v[112:115]\n\t" \
  "buffer_load_dwordx4 a[40:43], v250, s[16:19], s97 offen sc1\n\t" \
  "v_mfma_f32_16x16x32_bf16 v[112:115], a[176:179], a[204:207], v[112:115]\n\t" \
  "ds_read_b64_tr_b16 a[90:91], v190 offset:25600\n\t" \
  "v_mfma_f32_16x16x32_bf16 v[20:23], a[180:183], a[192:195], v[20:23]\n\t" \
  "ds_read_b64_tr_b16 a[92:93], v191 offset:24576\n\t" \
  "v_mfma_f32_16x16x32_bf16 v[20:23], a[116:119], a[208:211], v[20:23]\n\t" \
  "ds_read_b64_tr_b16 a[94:95], v191 offset:25600\n\t" \
  "v_mfma_f32_16x16x32_bf16 v[52:55], a[116:119], a[212:215], v[52:55]\n\t" \
  "buffer_load_dwordx4 a[44:47], v251, s[16:19], s97 offen sc1\n\t" \
  "v_mfma_f32_16x16x32_bf16 v[52:55], a[180:183], a[196:199], v[52:55]\n\t" \
  "s_add_u32 s96, s78, 7864320\n\t" \
  "s_and_b32 s96, s96, 0x780000\n\t" \
  "s_add_u32 m0, s76, 98304\n\t" \
  "v_mfma_f32_16x16x32_bf16 v[84:87], a[180:183], a[200:203], v[84:87]\n\t" \
  "v_mfma_f32_16x16x32_bf16 v[84:87], a[116:119], a[216:219], v[84:87]\n\t" \
  "v_mfma_f32_16x16x32_bf16 v[116:119], a[116:119], a[220:223], v[116:119]\n\t" \
  "buffer_load_dwordx4 v200, s[20:23], s96 offen offset:0 lds\n\t" \
  "v_mfma_f32_16x16x32_bf16 v[116:119], a[180:183], a[204:207], v[116:119]\n\t" \
  "v_mfma_f32_16x16x32_bf16 v[24:27], a[184:187], a[192:195], v[24:27]\n\t" \
  "v_mfma_f32_16x16x32_bf16 v[24:27], a[120:123], a[208:211], v[24:27]\n\t" \
  "buffer_load_dwordx4 v201, s[20:23], s96 offen offset:1024 lds\n\t" \
  "v_mfma_f32_16x16x32_bf16 v[56:59], a[120:123], a[212:215], v[56:59]\n\t" \
  "v_mfma_f32_16x16x32_bf16 v[56:59], a[184:187], a[196:199], v[56:59]\n\t" \
  "v_mfma_f32_16x16x32_bf16 v[88:91], a[184:187], a[200:203], v[88:91]\n\t" \
  "v_mfma_f32_16x16x32_bf16 v[88:91], a[120:123], a[216:219], v[88:91]\n\t" \
  "buffer_load_dwordx4 v202, s[20:23], s96 offen offset:2048 lds\n\t" \
  "v_mfma_f32_16x16x32_bf16 v[120:123], a[120:123], a[220:223], v[120:123]\n\t" \
  "v_mfma_f32_16x16x32_bf16 v[120:123], a[184:187], a[204:207], v[120:123]\n\t" \
  "v_mfma_f32_16x16x32_bf16 v[28:31], a[188:191], a[192:195], v[28:31]\n\t" \
  "v_mfma_f32_16x16x32_bf16 v[28:31], a[124:127], a[208:211], v[28:31]\n\t" \
  "buffer_load_dwordx4 v203, s[20:23], s96 offen offset:3072 lds\n\t" \
  "v_mfma_f32_16x16x32_bf16 v[60:63], a[124:127], a[212:215], v[60:63]\n\t" \
  "v_mfma_f32_16x16x32_bf16 v[60:63], a[188:191], a[196:199], v[60:63]\n\t" \
  "v_mfma_f32_16x16x32_bf16 v[92:95], a[188:191], a[200:203], v[92:95]\n\t" \
  "v_mfma_f32_16x16x32_bf16 v[92:95], a[124:127], a[216:219], v[92:95]\n\t" \
  "s_add_u32 s97, s97, s83\n\t" \
  "v_mfma_f32_16x16x32_bf16 v[124:127], a[124:127], a[220:223], v[124:127]\n\t" \
  "s_waitcnt vmcnt(25) lgkmcnt(0)\n\t" \
  "v_mfma_f32_16x16x32_bf16 v[124:127], a[188:191], a[204:207], v[124:127]\n\t" \
  "s_barrier\n\t" \
  "v_mfma_f32_16x16x32_bf16 v[0:3], a[128:131], a[224:227], v[0:3]\n\t" \
  "buffer_load_dwordx4 a[48:51], v248, s[16:19], s97 offen sc1\n\t" \
  "v_mfma_f32_16x16x32_bf16 v[0:3], a[64:67], a[240:243], v[0:3]\n\t" \
  "ds_read_b64_tr_b16 a[160:161], v184 offset:32768\n\t" \
  "v_mfma_f32_16x16x32_bf16 v[32:35], a[64:67], a[244:247], v[32:35]\n\t" \
  "ds_read_b64_tr_b16 a[162:163], v184 offset:33792\n\t" \
  "v_mfma_f32_16x16x32_bf16 v[32:35], a[128:131], a[228:231], v[32:35]\n\t" \
  "buffer_load_dwordx4 a[52:55], v249, s[16:19], s97 offen sc1\n\t" \
  "v_mfma_f32_16x16x32_bf16 v[64:67], a[128:131], a[232:235], v[64:67]\n\t" \
  "ds_read_b64_tr_b16 a[164:165], v185 offset:32768\n\t" \
  "v_mfma_f32_16x16x32_bf16 v[64:67], a[64:67], a[248:251], v[64:67]\n\t" \
  "ds_read_b64_tr_b16 a[166:167], v185 offset:33792\n\t" \
  "v_mfma_f32_16x16x32_bf16 v[96:99], a[64:67], a[252:255], v[96:99]\n\t" \
  "buffer_load_dwordx4 a[56:59], v250, s[16:19], s97 offen sc1\n\t" \
  "v_mfma_f32_16x16x32_bf16 v[96:99], a[128:131], a[236:239], v[96:99]\n\t" \
  "ds_read_b64_tr_b16 a[168:169], v186 offset:32768\n\t" \
  "v_mfma_f32_16x16x32_bf16 v[4:7], a[132:135], a[224:227], v[4:7]\n\t" \
  "ds_read_b64_tr_b16 a[170:171], v186 offset:33792\n\t" \
  "v_mfma_f32_16x16x32_bf16 v[4:7], a[68:71], a[240:243], v[4:7]\n\t" \
  "ds_read_b64_tr_b16 a[172:173], v187 offset:32768\n\t" \
  "v_mfma_f32_16x16x32_bf16 v[36:39], a[68:71], a[244:247], v[36:39]\n\t" \
  "buffer_load_dwordx4 a[60:63], v251, s[16:19], s97 offen sc1\n\t" \
  "v_mfma_f32_16x16x32_bf16 v[36:39], a[132:135], a[228:231], v[36:39]\n\t" \
  "s_add_u32 s97, s78, 7340032\n\t" \
  "ds_read_b64_tr_b16 a[174:175], v187 offset:33792\n\t" \
  "s_bfe_u32 s97, s97, 0x140003\n\t" \
  "v_mfma_f32_16x16x32_bf16 v[68:71], a[132:135], a[232:235], v[68:71]\n\t" \
  "ds_read_b64_tr_b16 a[176:177], v188 offset:32768\n\t" \
  "s_add_u32 s97, s97, s82\n\t" \
  "v_mfma_f32_16x16x32_bf16 v[68:71], a[68:71], a[248:251], v[68:71]\n\t" \
  "ds_read_b64_tr_b16 a[178:179], v188 offset:33792\n\t" \
  "v_mfma_f32_16x16x32_bf16 v[100:103], a[68:71], a[252:255], v[100:103]\n\t" \
  "buffer_load_dwordx4 a[192:195], v248, s[16:19], s97 offen sc1\n\t" \
  "v_mfma_f32_16x16x32_bf16 v[100:103], a[132:135], a[236:239], v[100:103]\n\t" \
  "ds_read_b64_tr_b16 a[180:181], v189 offset:32768\n\t" \
  "v_mfma_f32_16x16x32_bf16 v[8:11], a[136:139], a[224:227], v[8:11]\n\t" \
  "ds_read_b64_tr_b16 a[182:183], v189 offset:33792\n\t" \
  "v_mfma_f32_16x16x32_bf16 v[8:11], a[72:75], a[240:243], v[8:11]\n\t" \
  "buffer_load_dwordx4 a[196:199], v249, s[16:19], s97 offen sc1\n\t" \
  "v_mfma_f32_16x16x32_bf16 v[40:43], a[72:75], a[244:247], v[40:43]\n\t" \
  "ds_read_b64_tr_b16 a[184:185], v190 offset:32768\n\t" \
  "v_mfma_f32_16x16x32_bf16 v[40:43], a[136:139], a[228:231], v[40:43]\n\t" \
  "ds_read_b64_tr_b16 a[186:187], v190 offset:33792\n\t" \
  "v_mfma_f32_16x16x32_bf16 v[72:75], a[136:139], a[232:235], v[72:75]\n\t" \
  "ds_read_b64_tr_b16 a[188:189], v191 offset:32768\n\t" \
  "v_mfma_f32_16x16x32_bf16 v[72:75], a[72:75], a[248:251], v[72:75]\n\t" \
  "buffer_load_dwordx4 a[200:203], v250, s[16:19], s97 offen sc1\n\t" \
  "v_mfma_f32_16x16x32_bf16 v[104:107], a[72:75], a[252:255], v[104:107]\n\t" \
  "ds_read_b64_tr_b16 a[190:191], v191 offset:33792\n\t" \
  "v_mfma_f32_16x16x32_bf16 v[104:107], a[136:139], a[236:239], v[104:107]\n\t" \
  "ds_read_b64_tr_b16 a[96:97], v184 offset:40960\n\t" \
  "v_mfma_f32_16x16x32_bf16 v[12:15], a[140:143], a[224:227], v[12:15]\n\t" \
  "ds_read_b64_tr_b16 a[98:99], v184 offset:41984\n\t" \
  "v_mfma_f32_16x16x32_bf16 v[12:15], a[76:79], a[240:243], v[12:15]\n\t" \
  "buffer_load_dwordx4 a[204:207], v251, s[16:19], s97 offen sc1\n\t" \
  "v_mfma_f32_16x16x32_bf16 v[44:47], a[76:79], a[244:247], v[44:47]\n\t" \
  "ds_read_b64_tr_b16 a[100:101], v185 offset:40960\n\t" \
  "v_mfma_f32_16x16x32_bf16 v[44:47], a[140:143], a[228:231], v[44:47]\n\t" \
  "ds_read_b64_tr_b16 a[102:103], v185 offset:41984\n\t" \
  "v_mfma_f32_16x16x32_bf16 v[76:79], a[140:143], a[232:235], v[76:79]\n\t" \
  "ds_read_b64_tr_b16 a[104:105], v186 offset:40960\n\t" \
  "v_mfma_f32_16x16x32_bf16 v[76:79], a[76:79], a[248:251], v[76:79]\n\t" \
  "ds_read_b64_tr_b16 a[106:107], v186 offset:41984\n\t" \
  "s_add_u32 s97, s97, s83\n\t" \
  "v_mfma_f32_16x16x32_bf16 v[108:111], a[76:79], a[252:255], v[108:111]\n\t" \
  "ds_read_b64_tr_b16 a[108:109], v187 offset:40960\n\t" \
  "v_mfma_f32_16x16x32_bf16 v[108:111], a[140:143], a[236:239], v[108:111]\n\t" \
  "buffer_load_dwordx4 a[208:211], v248, s[16:19], s97 offen sc1\n\t" \
  "v_mfma_f32_16x16x32_bf16 v[16:19], a[144:147], a[224:227], v[16:19]\n\t" \
  "ds_read_b64_tr_b16 a[110:111], v187 offset:41984\n\t" \
  "v_mfma_f32_16x16x32_bf16 v[16:19], a[80:83], a[240:243], v[16:19]\n\t" \
  "ds_read_b64_tr_b16 a[112:113], v188 offset:40960\n\t" \
  "v_mfma_f32_16x16x32_bf16 v[48:51], a[80:83], a[244:247], v[48:51]\n\t" \
  "s_waitcnt vmcnt(21)\n\t" \
  "v_lshlrev_b32 v128, 16, v152\n\t" \
  "v_and_b32 v152, 0xffff0000, v152\n\t" \
  "v_mfma_f32_16x16x32_bf16 v[48:51], a[144:147], a[228:231], v[48:51]\n\t" \
  "v_fma_f32 v176, v128, v172, v176\n\t" \
  "v_fma_f32 v180, v152, v172, v180\n\t" \
  "v_mfma_f32_16x16x32_bf16 v[80:83], a[144:147], a[232:235], v[80:83]\n\t" \
  "v_lshlrev_b32 v128, 16, v153\n\t" \
  "v_and_b32 v153, 0xffff0000, v153\n\t" \
  "v_mfma_f32_16x16x32_bf16 v[80:83], a[80:83], a[248:251], v[80:83]\n\t" \
  "v_fma_f32 v177, v128, v172, v177\n\t" \
  "v_fma_f32 v181, v153, v172, v181\n\t" \
  "v_mfma_f32_16x16x32_bf16 v[112:115], a[80:83], a[252:255], v[112:115]\n\t" \
  "v_lshlrev_b32 v128, 16, v154\n\t" \
  "v_and_b32 v154, 0xffff0000, v154\n\t" \
  "v_mfma_f32_16x16x32_bf16 v[112:115], a[144:147], a[236:239], v[112:115]\n\t" \
  "v_fma_f32 v178, v128, v172, v178\n\t" \
  "v_fma_f32 v182, v154, v172, v182\n\t" \
  "v_mfma_f32_16x16x32_bf16 v[20:23], a[148:151], a[224:227], v[20:23]\n\t" \
  "buffer_load_dwordx4 a[212:215], v249, s[16:19], s97 offen sc1\n\t" \
  "v_mfma_f32_16x16x32_bf16 v[20:23], a[84:87], a[240:243], v[20:23]\n\t" \
  "ds_read_b64_tr_b16 a[114:115], v188 offset:41984\n\t" \
  "v_lshlrev_b32 v128, 16, v155\n\t" \
  "v_mfma_f32_16x16x32_bf16 v[52:55], a[84:87], a[244:247], v[52:55]\n\t" \
  "v_and_b32 v155, 0xffff0000, v155\n\t" \
  "v_fma_f32 v179, v128, v172, v179\n\t" \
  "v_mfma_f32_16x16x32_bf16 v[52:55], a[148:151], a[228:231], v[52:55]\n\t" \
  "ds_read_b64_tr_b16 a[116:117], v189 offset:40960\n\t" \
  "v_fma_f32 v183, v155, v172, v183\n\t" \
  "v_mfma_f32_16x16x32_bf16 v[84:87], a[148:151], a[232:235], v[84:87]\n\t" \
  "s_waitcnt vmcnt(21)\n\t" \
  "v_lshlrev_b32 v128, 16, v156\n\t" \
  "v_and_b32 v156, 0xffff0000, v156\n\t" \
  "v_mfma_f32_16x16x32_bf16 v[84:87], a[84:87], a[248:251], v[84:87]\n\t" \
  "ds_read_b64_tr_b16 a[118:119], v189 offset:41984\n\t" \
  "v_fma_f32 v176, v128, v173, v176\n\t" \
  "v_mfma_f32_16x16x32_bf16 v[116:119], a[84:87], a[252:255], v[116:119]\n\t" \
  "ds_read_b64_tr_b16 a[120:121], v190 offset:40960\n\t" \
  "v_fma_f32 v180, v156, v173, v180\n\t" \
  "v_mfma_f32_16x16x32_bf16 v[116:119], a[148:151], a[236:239], v[116:119]\n\t" \
  "buffer_load_dwordx4 a[216:219], v250, s[16:19], s97 offen sc1\n\t" \
  "v_mfma_f32_16x16x32_bf16 v[24:27], a[152:155], a[224:227], v[24:27]\n\t" \
  "ds_read_b64_tr_b16 a[122:123], v190 offset:41984\n\t" \
  "v_lshlrev_b32 v128, 16, v157\n\t" \
  "v_mfma_f32_16x16x32_bf16 v[24:27], a[88:91], a[240:243], v[24:27]\n\t" \
  "ds_read_b64_tr_b16 a[124:125], v191 offset:40960\n\t" \
  "v_and_b32 v157, 0xffff0000, v157\n\t" \
  "v_mfma_f32_16x16x32_bf16 v[56:59], a[88:91], a[244:247], v[56:59]\n\t" \
  "v_fma_f32 v177, v128, v173, v177\n\t" \
  "v_fma_f32 v181, v157, v173, v181\n\t" \
  "v_mfma_f32_16x16x32_bf16 v[56:59], a[152:155], a[228:231], v[56:59]\n\t" \
  "ds_read_b64_tr_b16 a[126:127], v191 offset:41984\n\t" \
  "v_lshlrev_b32 v128, 16, v158\n\t" \
  "v_mfma_f32_16x16x32_bf16 v[88:91], a[152:155], a[232:235], v[88:91]\n\t" \
  "v_and_b32 v158, 0xffff0000, v158\n\t" \
  "v_fma_f32 v178, v128, v173, v178\n\t" \
  "v_mfma_f32_16x16x32_bf16 v[88:91], a[88:91], a[248:251], v[88:91]\n\t" \
  "v_fma_f32 v182, v158, v173, v182\n\t" \
  "v_lshlrev_b32 v128, 16, v159\n\t" \
  "v_mfma_f32_16x16x32_bf16 v[120:123], a[88:91], a[252:255], v[120:123]\n\t" \
  "v_and_b32 v159, 0xffff0000, v159\n\t" \
  "v_fma_f32 v179, v128, v173, v179\n\t" \
  "v_mfma_f32_16x16x32_bf16 v[120:123], a[152:155], a[236:239], v[120:123]\n\t" \
  "buffer_load_dwordx4 a[220:223], v251, s[16:19], s97 offen sc1\n\t" \
  "v_mfma_f32_16x16x32_bf16 v[28:31], a[156:159], a[224:227], v[28:31]\n\t" \
  "v_fma_f32 v183, v159, v173, v183\n\t" \
  "v_mfma_f32_16x16x32_bf16 v[28:31], a[92:95], a[240:243], v[28:31]\n\t" \
  "s_waitcnt vmcnt(22)\n\t" \
  "v_lshlrev_b32 v128, 16, v160\n\t" \
  "v_and_b32 v160, 0xffff0000, v160\n\t" \
  "v_mfma_f32_16x16x32_bf16 v[60:63], a[92:95], a[244:247], v[60:63]\n\t" \
  "v_fma_f32 v176, v128, v174, v176\n\t" \
  "v_fma_f32 v180, v160, v174, v180\n\t" \
  "v_mfma_f32_16x16x32_bf16 v[60:63], a[156:159], a[228:231], v[60:63]\n\t" \
  "v_lshlrev_b32 v128, 16, v161\n\t" \
  "v_and_b32 v161, 0xffff0000, v161\n\t" \
  "v_mfma_f32_16x16x32_bf16 v[92:95], a[156:159], a[232:235], v[92:95]\n\t" \
  "v_fma_f32 v177, v128, v174, v177\n\t" \
  "v_fma_f32 v181, v161, v174, v181\n\t" \
  "v_mfma_f32_16x16x32_bf16 v[92:95], a[92:95], a[248:251], v[92:95]\n\t" \
  "s_add_u32 s97, s78, 7864320\n\t" \
  "v_lshlrev_b32 v128, 16, v162\n\t" \
  "v_and_b32 v162, 0xffff0000, v162\n\t" \
  "v_mfma_f32_16x16x32_bf16 v[124:127], a[92:95], a[252:255], v[124:127]\n\t" \
  "s_waitcnt lgkmcnt(0)\n\t" \
  "s_bfe_u32 s97, s97, 0x140003\n\t" \
  "s_add_u32 s97, s97, s82\n\t" \
  "v_mfma_f32_16x16x32_bf16 v[124:127], a[156:159], a[236:239], v[124:127]\n\t" \
  "s_barrier\n\t" \
  "v_mfma_f32_16x16x32_bf16 v[0:3], a[160:163], a[0:3], v[0:3]\n\t" \
  "buffer_load_dwordx4 a[224:227], v248, s[16:19], s97 offen sc1\n\t" \
  "v_mfma_f32_16x16x32_bf16 v[0:3], a[96:99], a[16:19], v[0:3]\n\t" \
  "ds_read_b64_tr_b16 a[128:129], v192 offset:0\n\t" \
  "v_fma_f32 v178, v128, v174, v178\n\t" \
  "v_mfma_f32_16x16x32_bf16 v[32:35], a[96:99], a[20:23], v[32:35]\n\t" \
  "ds_read_b64_tr_b16 a[130:131], v192 offset:1024\n\t" \
  "v_fma_f32 v182, v162, v174, v182\n\t" \
  "v_mfma_f32_16x16x32_bf16 v[32:35], a[160:163], a[4:7], v[32:35]\n\t" \
  "v_lshlrev_b32 v128, 16, v163\n\t" \
  "v_and_b32 v163, 0xffff0000, v163\n\t" \
  "v_mfma_f32_16x16x32_bf16 v[64:67], a[160:163], a[8:11], v[64:67]\n\t" \
  "ds_read_b64_tr_b16 a[132:133], v193 offset:0\n\t" \
  "v_fma_f32 v179, v128, v174, v179\n\t" \
  "v_mfma_f32_16x16x32_bf16 v[64:67], a[96:99], a[24:27], v[64:67]\n\t" \
  "ds_read_b64_tr_b16 a[134:135], v193 offset:1024\n\t" \
  "v_fma_f32 v183, v163, v174, v183\n\t" \
  "v_mfma_f32_16x16x32_bf16 v[96:99], a[96:99], a[28:31], v[96:99]\n\t" \
  "s_waitcnt vmcnt(22)\n\t" \
  "v_lshlrev_b32 v128, 16, v164\n\t" \
  "v_and_b32 v164, 0xffff0000, v164\n\t" \
  "v_mfma_f32_16x16x32_bf16 v[96:99], a[160:163], a[12:15], v[96:99]\n\t" \
  "ds_read_b64_tr_b16 a[136:137], v194 offset:0\n\t" \
  "v_fma_f32 v176, v128, v175, v176\n\t" \
  "v_mfma_f32_16x16x32_bf16 v[4:7], a[164:167], a[0:3], v[4:7]\n\t" \
  "buffer_load_dwordx4 a[228:231], v249, s[16:19], s97 offen sc1\n\t" \
  "v_mfma_f32_16x16x32_bf16 v[4:7], a[100:103], a[16:19], v[4:7]\n\t" \
  "ds_read_b64_tr_b16 a[138:139], v194 offset:1024\n\t" \
  "v_fma_f32 v180, v164, v175, v180\n\t" \
  "v_mfma_f32_16x16x32_bf16 v[36:39], a[100:103], a[20:23], v[36:39]\n\t" \
  "ds_read_b64_tr_b16 a[140:141], v195 offset:0\n\t" \
  "v_lshlrev_b32 v128, 16, v165\n\t" \
  "v_mfma_f32_16x16x32_bf16 v[36:39], a[164:167], a[4:7], v[36:39]\n\t" \
  "v_and_b32 v165, 0xffff0000, v165\n\t" \
  "v_fma_f32 v177, v128, v175, v177\n\t" \
  "v_mfma_f32_16x16x32_bf16 v[68:71], a[164:167], a[8:11], v[68:71]\n\t" \
  "ds_read_b64_tr_b16 a[142:143], v195 offset:1024\n\t" \
  "v_fma_f32 v181, v165, v175, v181\n\t" \
  "v_mfma_f32_16x16x32_bf16 v[68:71], a[100:103], a[24:27], v[68:71]\n\t" \
  "ds_read_b64_tr_b16 a[144:145], v196 offset:0\n\t" \
  "v_lshlrev_b32 v128, 16, v166\n\t" \
  "v_mfma_f32_16x16x32_bf16 v[100:103], a[100:103], a[28:31], v[100:103]\n\t" \
  "v_and_b32 v166, 0xffff0000, v166\n\t" \
  "v_fma_f32 v178, v128, v175, v178\n\t" \
  "v_mfma_f32_16x16x32_bf16 v[100:103], a[164:167], a[12:15], v[100:103]\n\t" \
  "buffer_load_dwordx4 a[232:235], v250, s[16:19], s97 offen sc1\n\t" \
  "v_mfma_f32_16x16x32_bf16 v[8:11], a[168:171], a[0:3], v[8:11]\n\t" \
  "ds_read_b64_tr_b16 a[146:147], v196 offset:1024\n\t" \
  "v_fma_f32 v182, v166, v175, v182\n\t" \
  "v_mfma_f32_16x16x32_bf16 v[8:11], a[104:107], a[16:19], v[8:11]\n\t" \
  "ds_read_b64_tr_b16 a[148:149], v197 offset:0\n\t" \
  "v_lshlrev_b32 v128, 16, v167\n\t" \
  "v_mfma_f32_16x16x32_bf16 v[40:43], a[104:107], a[20:23], v[40:43]\n\t" \
  "ds_read_b64_tr_b16 a[150:151], v197 offset:1024\n\t" \
  "v_and_b32 v167, 0xffff0000, v167\n\t" \
  "v_mfma_f32_16x16x32_bf16 v[40:43], a[168:171], a[4:7], v[40:43]\n\t" \
  "v_fma_f32 v179, v128, v175, v179\n\t" \
  "v_fma_f32 v183, v167, v175, v183\n\t" \
  "v_mfma_f32_16x16x32_bf16 v[72:75], a[168:171], a[8:11], v[72:75]\n\t" \
  "ds_read_b64_tr_b16 a[152:153], v198 offset:0\n\t" \
  "v_and_b32 v138, 7, v233\n\t" \
  "v_mfma_f32_16x16x32_bf16 v[72:75], a[104:107], a[24:27], v[72:75]\n\t" \
  "ds_read_b64_tr_b16 a[154:155], v198 offset:1024\n\t" \
  "v_lshlrev_b32 v138, 5, v138\n\t" \
  "v_mfma_f32_16x16x32_bf16 v[104:107], a[104:107], a[28:31], v[104:107]\n\t" \
  "v_permlane32_swap_b32 v176, v178\n\t" \
  "v_mfma_f32_16x16x32_bf16 v[104:107], a[168:171], a[12:15], v[104:107]\n\t" \
  "buffer_load_dwordx4 a[236:239], v251, s[16:19], s97 offen sc1\n\t" \
  "v_mfma_f32_16x16x32_bf16 v[12:15], a[172:175], a[0:3], v[12:15]\n\t" \
  "v_permlane32_swap_b32 v180, v182\n\t" \
  "v_mfma_f32_16x16x32_bf16 v[12:15], a[108:111], a[16:19], v[12:15]\n\t" \
  "ds_read_b64_tr_b16 a[156:157], v199 offset:0\n\t" \
  "v_mfma_f32_16x16x32_bf16 v[44:47], a[108:111], a[20:23], v[44:47]\n\t" \
  "v_permlane32_swap_b32 v177, v179\n\t" \
  "v_mfma_f32_16x16x32_bf16 v[44:47], a[172:175], a[4:7], v[44:47]\n\t" \
  "ds_read_b64_tr_b16 a[158:159], v199 offset:1024\n\t" \
  "v_mfma_f32_16x16x32_bf16 v[76:79], a[172:175], a[8:11], v[76:79]\n\t" \
  "ds_read_b64_tr_b16 a[64:65], v192 offset:8192\n\t" \
  "v_mfma_f32_16x16x32_bf16 v[76:79], a[108:111], a[24:27], v[76:79]\n\t" \
  "s_add_u32 s97, s97, s83\n\t" \
  "v_permlane32_swap_b32 v181, v183\n\t" \
  "v_mfma_f32_16x16x32_bf16 v[108:111], a[108:111], a[28:31], v[108:111]\n\t" \
  "ds_read_b64_tr_b16 a[66:67], v192 offset:9216\n\t" \
  "v_add_f32 v176, v176, v178\n\t" \
  "v_mfma_f32_16x16x32_bf16 v[108:111], a[172:175], a[12:15], v[108:111]\n\t" \
  "ds_read_b64_tr_b16 a[68:69], v193 offset:8192\n\t" \
  "v_add_f32 v177, v177, v179\n\t" \
  "v_mfma_f32_16x16x32_bf16 v[16:19], a[176:179], a[0:3], v[16:19]\n\t" \
  "buffer_load_dwordx4 a[240:243], v248, s[16:19], s97 offen sc1\n\t" \
  "v_mfma_f32_16x16x32_bf16 v[16:19], a[112:115], a[16:19], v[16:19]\n\t" \
  "ds_read_b64_tr_b16 a[70:71], v193 offset:9216\n\t" \
  "v_add_f32 v180, v180, v182\n\t" \
  "v_mfma_f32_16x16x32_bf16 v[48:51], a[112:115], a[20:23], v[48:51]\n\t" \
  "ds_read_b64_tr_b16 a[72:73], v194 offset:8192\n\t" \
  "v_add_f32 v181, v181, v183\n\t" \
  "v_mfma_f32_16x16x32_bf16 v[48:51], a[176:179], a[4:7], v[48:51]\n\t" \
  "ds_read_b64_tr_b16 a[74:75], v194 offset:9216\n\t" \
  "v_lshrrev_b32 v139, 4, v233\n\t" \
  "v_mfma_f32_16x16x32_bf16 v[80:83], a[176:179], a[8:11], v[80:83]\n\t" \
  "v_permlane16_swap_b32 v176, v177\n\t" \
  "v_mfma_f32_16x16x32_bf16 v[80:83], a[112:115], a[24:27], v[80:83]\n\t" \
  "ds_read_b64_tr_b16 a[76:77], v195 offset:8192\n\t" \
  "v_mfma_f32_16x16x32_bf16 v[112:115], a[112:115], a[28:31], v[112:115]\n\t" \
  "buffer_load_dwordx4 a[244:247], v249, s[16:19], s97 offen sc1\n\t" \
  "v_mfma_f32_16x16x32_bf16 v[112:115], a[176:179], a[12:15], v[112:115]\n\t" \
  "ds_read_b64_tr_b16 a[78:79], v195 offset:9216\n\t" \
  "v_mfma_f32_16x16x32_bf16 v[20:23], a[180:183], a[0:3], v[20:23]\n\t" \
  "ds_read_b64_tr_b16 a[80:81], v196 offset:8192\n\t" \
  "v_mfma_f32_16x16x32_bf16 v[20:23], a[116:119], a[16:19], v[20:23]\n\t" \
  "v_permlane16_swap_b32 v180, v181\n\t" \
  "v_mfma_f32_16x16x32_bf16 v[52:55], a[116:119], a[20:23], v[52:55]\n\t" \
  "ds_read_b64_tr_b16 a[82:83], v196 offset:9216\n\t" \
  "v_add_f32 v176, v176, v177\n\t" \
  "v_mfma_f32_16x16x32_bf16 v[52:55], a[180:183], a[4:7], v[52:55]\n\t" \
  "ds_read_b64_tr_b16 a[84:85], v197 offset:8192\n\t" \
  "v_add_f32 v180, v180, v181\n\t" \
  "v_mfma_f32_16x16x32_bf16 v[84:87], a[180:183], a[8:11], v[84:87]\n\t" \
  "buffer_load_dwordx4 a[248:251], v250, s[16:19], s97 offen sc1\n\t" \
  "v_mfma_f32_16x16x32_bf16 v[84:87], a[116:119], a[24:27], v[84:87]\n\t" \
  "ds_read_b64_tr_b16 a[86:87], v197 offset:9216\n\t" \
  "v_lshl_add_u32 v138, v139, 3, v138\n\t" \
  "v_mfma_f32_16x16x32_bf16 v[116:119], a[116:119], a[28:31], v[116:119]\n\t" \
  "ds_read_b64_tr_b16 a[88:89], v198 offset:8192\n\t" \
  "v_and_b32 v139, 0x7fffff00, v219\n\t" \
  "v_mfma_f32_16x16x32_bf16 v[116:119], a[180:183], a[12:15], v[116:119]\n\t" \
  "v_add_f32_dpp v136, v176, v176 row_ror:8 row_mask:0xf bank_mask:0xf\n\t" \
  "v_add_f32_dpp v137, v180, v180 row_ror:8 row_mask:0xf bank_mask:0xf\n\t" \
  "v_mfma_f32_16x16x32_bf16 v[24:27], a[184:187], a[0:3], v[24:27]\n\t" \
  "ds_read_b64_tr_b16 a[90:91], v198 offset:9216\n\t" \
  "v_add_u32 v138, v139, v138\n\t" \
  "v_mfma_f32_16x16x32_bf16 v[24:27], a[120:123], a[16:19], v[24:27]\n\t" \
  "ds_write_b64 v138, v[136:137] offset:1024\n\t" \
  "v_mfma_f32_16x16x32_bf16 v[56:59], a[120:123], a[20:23], v[56:59]\n\t" \
  "buffer_load_dwordx4 a[252:255], v251, s[16:19], s97 offen sc1\n\t" \
  "v_mfma_f32_16x16x32_bf16 v[56:59], a[184:187], a[4:7], v[56:59]\n\t" \
  "s_add_u32 s99, s53, 14336\n\t" \
  "ds_read_b64_tr_b16 a[92:93], v199 offset:8192\n\t" \
  "v_and_b32 v140, 3, v233\n\t" \
  "v_mfma_f32_16x16x32_bf16 v[88:91], a[184:187], a[8:11], v[88:91]\n\t" \
  "ds_read_b64_tr_b16 a[94:95], v199 offset:9216\n\t" \
  "v_bfe_u32 v141, v233, 4, 1\n\t" \
  "v_mfma_f32_16x16x32_bf16 v[88:91], a[120:123], a[24:27], v[88:91]\n\t" \
  "v_lshl_add_u32 v140, v141, 2, v140\n\t" \
  "v_bfe_u32 v141, v233, 2, 2\n\t" \
  "v_mfma_f32_16x16x32_bf16 v[120:123], a[120:123], a[28:31], v[120:123]\n\t" \
  "v_lshlrev_b32 v141, 8, v141\n\t" \
  "v_lshl_add_u32 v141, v140, 3, v141\n\t" \
  "v_mfma_f32_16x16x32_bf16 v[120:123], a[184:187], a[12:15], v[120:123]\n\t" \
  "v_lshl_add_u32 v141, s3, 6, v141\n\t" \
  "v_lshlrev_b32 v140, 4, v140\n\t" \
  "v_mfma_f32_16x16x32_bf16 v[28:31], a[188:191], a[0:3], v[28:31]\n\t" \
  "buffer_load_dword v246, v245, s[68:71], s99 offen\n\t" \
  "v_mfma_f32_16x16x32_bf16 v[28:31], a[124:127], a[16:19], v[28:31]\n\t" \
  "v_and_b32 v142, 0x2c, v233\n\t" \
  "v_lshl_or_b32 v140, v142, 26, v140\n\t" \
  "v_mfma_f32_16x16x32_bf16 v[60:63], a[124:127], a[20:23], v[60:63]\n\t" \
  "v_add_u32 v141, 151552, v141\n\t" \
  "v_mov_b32 v145, v211\n\t" \
  "v_mfma_f32_16x16x32_bf16 v[60:63], a[188:191], a[4:7], v[60:63]\n\t" \
  "v_mov_b32 v147, v211\n\t" \
  "v_mfma_f32_16x16x32_bf16 v[92:95], a[188:191], a[8:11], v[92:95]\n\t" \
  "s_mov_b32 s98, 0x3f803f80\n\t" \
  "v_mov_b32 v212, v217\n\t" \
  "v_add_u32 v210, 65536, v228\n\t" \
  "v_mfma_f32_16x16x32_bf16 v[92:95], a[124:127], a[24:27], v[92:95]\n\t" \
  "v_add_u32 v211, 65536, v229\n\t" \
  "v_mov_b32 v243, s36\n\t" \
  "v_mfma_f32_16x16x32_bf16 v[124:127], a[124:127], a[28:31], v[124:127]\n\t" \
  "s_waitcnt vmcnt(17) lgkmcnt(0)\n\t" \
  "v_and_b32 v208, 1, v233\n\t" \
  "v_mul_u32_u24 v208, 0xaaaa, v208\n\t" \
  "v_mfma_f32_16x16x32_bf16 v[124:127], a[188:191], a[12:15], v[124:127]\n\t" \
  "s_barrier\n\t" \
  "v_mfma_f32_16x16x32_bf16 v[0:3], a[128:131], a[32:35], v[0:3]\n\t" \
  "ds_read_b64_tr_b16 a[160:161], v192 offset:16384\n\t" \
  "v_or_b32 v208, 0x4444, v208\n\t" \
  "v_mfma_f32_16x16x32_bf16 v[0:3], a[64:67], a[48:51], v[0:3]\n\t" \
  "v_lshl_or_b32 v208, v208, 16, v208\n\t" \
  "v_bfe_i32 v216, v233, 1, 1\n\t" \
  "v_mfma_f32_16x16x32_bf16 v[32:35], a[64:67], a[52:55], v[32:35]\n\t" \
  "ds_read_b64_tr_b16 a[162:163], v192 offset:17408\n\t" \
  "v_bfi_b32 v216, v216, 0, s98\n\t" \
  "v_mfma_f32_16x16x32_bf16 v[32:35], a[128:131], a[36:39], v[32:35]\n\t" \
  "v_mov_b32 v217, v216\n\t" \
  "v_mov_b32 v218, v216\n\t" \
  "v_mfma_f32_16x16x32_bf16 v[64:67], a[128:131], a[40:43], v[64:67]\n\t" \
  "ds_read_b64_tr_b16 a[164:165], v193 offset:16384\n\t" \
  "v_mov_b32 v219, v216\n\t" \
  "v_mfma_f32_16x16x32_bf16 v[64:67], a[64:67], a[56:59], v[64:67]\n\t" \
  "ds_read_b64_tr_b16 a[166:167], v193 offset:17408\n\t" \
  "v_mov_b32 v160, 0\n\t" \
  "v_mfma_f32_16x16x32_bf16 v[96:99], a[64:67], a[60:63], v[96:99]\n\t" \
  "v_mov_b32 v161, 0\n\t" \
  "v_mov_b32 v162, 0\n\t" \
  "v_mfma_f32_16x16x32_bf16 v[96:99], a[128:131], a[44:47], v[96:99]\n\t" \
  "ds_read_b64_tr_b16 a[168:169], v194 offset:16384\n\t" \
  "v_mov_b32 v163, 0\n\t" \
  "v_mfma_f32_16x16x32_bf16 v[4:7], a[132:135], a[32:35], v[4:7]\n\t" \
  "v_mov_b32 v164, 0\n\t" \
  "v_mov_b32 v165, 0\n\t" \
  "v_mfma_f32_16x16x32_bf16 v[4:7], a[68:71], a[48:51], v[4:7]\n\t" \
  "ds_read_b64_tr_b16 a[170:171], v194 offset:17408\n\t" \
  "v_mov_b32 v166, 0\n\t" \
  "v_mfma_f32_16x16x32_bf16 v[36:39], a[68:71], a[52:55], v[36:39]\n\t" \
  "ds_read_b64_tr_b16 a[172:173], v195 offset:16384\n\t" \
  "v_mov_b32 v167, 0\n\t" \
  "v_mfma_f32_16x16x32_bf16 v[36:39], a[132:135], a[36:39], v[36:39]\n\t" \
  "v_add_u32 v248, 14336, v240\n\t" \
  "v_add_u32 v249, 21504, v241\n\t" \
  "v_mfma_f32_16x16x32_bf16 v[68:71], a[132:135], a[40:43], v[68:71]\n\t" \
  "ds_read_b64_tr_b16 a[174:175], v195 offset:17408\n\t" \
  "v_add_u32 v241, 7168, v241\n\t" \
  "v_mfma_f32_16x16x32_bf16 v[68:71], a[68:71], a[56:59], v[68:71]\n\t" \
  "ds_read_b64_tr_b16 a[176:177], v196 offset:16384\n\t" \
  "v_lshlrev_b32_e64 v234, 6, s3\n\t" \
  "v_mfma_f32_16x16x32_bf16 v[100:103], a[68:71], a[60:63], v[100:103]\n\t" \
  "v_xor_b32 v234, v234, v244\n\t" \
  "v_xor_b32 v235, 32, v234\n\t" \
  "v_mfma_f32_16x16x32_bf16 v[100:103], a[132:135], a[44:47], v[100:103]\n\t" \
  "ds_read_b64_tr_b16 a[178:179], v196 offset:17408\n\t" \
  "v_add_u32 v236, 65536, v234\n\t" \
  "v_mfma_f32_16x16x32_bf16 v[8:11], a[136:139], a[32:35], v[8:11]\n\t" \
  "v_add_u32 v237, 65536, v235\n\t" \
  "v_add_u32 v238, 131072, v234\n\t" \
  "v_mfma_f32_16x16x32_bf16 v[8:11], a[72:75], a[48:51], v[8:11]\n\t" \
  "ds_read_b64_tr_b16 a[180:181], v197 offset:16384\n\t" \
  "v_add_u32 v239, 131072, v235\n\t" \
  "v_mfma_f32_16x16x32_bf16 v[40:43], a[72:75], a[52:55], v[40:43]\n\t" \
  "ds_read_b64_tr_b16 a[182:183], v197 offset:17408\n\t" \
  "v_lshlrev_b32 v242, 4, v233\n\t" \
  "v_mfma_f32_16x16x32_bf16 v[40:43], a[136:139], a[36:39], v[40:43]\n\t" \
  "v_add_u32 v213, s38, v242\n\t" \
  "v_add_u32 v215, 98304, v213\n\t" \
  "v_mfma_f32_16x16x32_bf16 v[72:75], a[136:139], a[40:43], v[72:75]\n\t" \
  "ds_read_b64_tr_b16 a[184:185], v198 offset:16384\n\t" \
  "v_lshlrev_b32 v254, 9, v233\n\t" \
  "v_mfma_f32_16x16x32_bf16 v[72:75], a[72:75], a[56:59], v[72:75]\n\t" \
  "v_and_b32 v244, 0x38, v233\n\t" \
  "v_lshl_or_b32 v254, v244, 26, v254\n\t" \
  "v_mfma_f32_16x16x32_bf16 v[104:107], a[72:75], a[60:63], v[104:107]\n\t" \
  "ds_read_b64_tr_b16 a[186:187], v198 offset:17408\n\t" \
  "v_bfrev_b32 v244, 1\n\t" \
  "v_mfma_f32_16x16x32_bf16 v[104:107], a[136:139], a[44:47], v[104:107]\n\t" \
  "ds_read_b64_tr_b16 a[188:189], v199 offset:16384\n\t" \
  "v_cmp_eq_u32 vcc, s39, v233\n\t" \
  "v_mfma_f32_16x16x32_bf16 v[12:15], a[140:143], a[32:35], v[12:15]\n\t" \
  "v_add_u32 v253, s39, v233\n\t" \
  "v_bfi_b32 v253, 7, v253, v233\n\t" \
  "v_mfma_f32_16x16x32_bf16 v[12:15], a[76:79], a[48:51], v[12:15]\n\t" \
  "ds_read_b64_tr_b16 a[190:191], v199 offset:17408\n\t" \
  "v_cndmask_b32 v254, v254, v244, vcc\n\t" \
  "v_mfma_f32_16x16x32_bf16 v[44:47], a[76:79], a[52:55], v[44:47]\n\t" \
  "ds_read_b64_tr_b16 a[96:97], v192 offset:24576\n\t" \
  "v_and_b32 v253, 31, v253\n\t" \
  "v_mfma_f32_16x16x32_bf16 v[44:47], a[140:143], a[36:39], v[44:47]\n\t" \
  "v_lshlrev_b32 v253, 2, v253\n\t" \
  "v_mov_b32 v250, v244\n\t" \
  "v_mfma_f32_16x16x32_bf16 v[76:79], a[140:143], a[40:43], v[76:79]\n\t" \
  "ds_read_b64_tr_b16 a[98:99], v192 offset:25600\n\t" \
  "v_writelane_b32 v244, 0, 0\n\t" \
  "v_mfma_f32_16x16x32_bf16 v[76:79], a[76:79], a[56:59], v[76:79]\n\t" \
  "s_add_u32 m0, s76, 16384\n\t" \
  "v_writelane_b32 v250, s44, 0\n\t" \
  "v_lshrrev_b32 v251, 1, v233\n\t" \
  "v_mfma_f32_16x16x32_bf16 v[108:111], a[76:79], a[60:63], v[108:111]\n\t" \
  "ds_read_b64_tr_b16 a[100:101], v193 offset:24576\n\t" \
  "v_lshlrev_b32 v251, 2, v251\n\t" \
  "v_mfma_f32_16x16x32_bf16 v[108:111], a[140:143], a[44:47], v[108:111]\n\t" \
  "ds_read_b64_tr_b16 a[102:103], v193 offset:25600\n\t" \
  "v_add_u32 v251, 8192, v251\n\t" \
  "v_mfma_f32_16x16x32_bf16 v[16:19], a[144:147], a[32:35], v[16:19]\n\t" \
  "buffer_load_dwordx4 v224, s[60:63], s38 offen offset:0 lds\n\t" \
  "v_mfma_f32_16x16x32_bf16 v[16:19], a[80:83], a[48:51], v[16:19]\n\t" \
  "ds_read_b64_tr_b16 a[104:105], v194 offset:24576\n\t" \
  "v_lshrrev_b32 v252, 5, v233\n\t" \
  "v_mfma_f32_16x16x32_bf16 v[48:51], a[80:83], a[52:55], v[48:51]\n\t" \
  "v_mul_u32_u24 v252, 1536, v252\n\t" \
  "v_lshl_add_u32 v252, s3, 9, v252\n\t" \
  "v_mfma_f32_16x16x32_bf16 v[48:51], a[144:147], a[36:39], v[48:51]\n\t" \
  "ds_read_b64_tr_b16 a[106:107], v194 offset:25600\n\t" \
  "v_add_u32 v252, v252, v242\n\t" \
  "v_mfma_f32_16x16x32_bf16 v[80:83], a[144:147], a[40:43], v[80:83]\n\t" \
  "ds_read_b64_tr_b16 a[108:109], v195 offset:24576\n\t" \
  "v_mfma_f32_16x16x32_bf16 v[80:83], a[80:83], a[56:59], v[80:83]\n\t" \
  "buffer_load_dwordx4 v225, s[60:63], s38 offen offset:1024 lds\n\t" \
  "v_mfma_f32_16x16x32_bf16 v[112:115], a[80:83], a[60:63], v[112:115]\n\t" \
  "ds_read_b64_tr_b16 a[110:111], v195 offset:25600\n\t" \
  "v_mfma_f32_16x16x32_bf16 v[112:115], a[144:147], a[44:47], v[112:115]\n\t" \
  "ds_read_b64_tr_b16 a[112:113], v196 offset:24576\n\t" \
  "v_mfma_f32_16x16x32_bf16 v[20:23], a[148:151], a[32:35], v[20:23]\n\t" \
  "buffer_load_dwordx4 v226, s[60:63], s38 offen offset:2048 lds\n\t" \
  "v_mfma_f32_16x16x32_bf16 v[20:23], a[84:87], a[48:51], v[20:23]\n\t" \
  "ds_read_b64_tr_b16 a[114:115], v196 offset:25600\n\t" \
  "v_mfma_f32_16x16x32_bf16 v[52:55], a[84:87], a[52:55], v[52:55]\n\t" \
  "ds_read_b64_tr_b16 a[116:117], v197 offset:24576\n\t" \
  "v_mfma_f32_16x16x32_bf16 v[52:55], a[148:151], a[36:39], v[52:55]\n\t" \
  "buffer_load_dwordx4 v227, s[60:63], s38 offen offset:3072 lds\n\t" \
  "v_mfma_f32_16x16x32_bf16 v[84:87], a[148:151], a[40:43], v[84:87]\n\t" \
  "ds_read_b64_tr_b16 a[118:119], v197 offset:25600\n\t" \
  "s_add_u32 m0, s76, 49152\n\t" \
  "v_mfma_f32_16x16x32_bf16 v[84:87], a[84:87], a[56:59], v[84:87]\n\t" \
  "ds_read_b64_tr_b16 a[120:121], v198 offset:24576\n\t" \
  "v_mfma_f32_16x16x32_bf16 v[116:119], a[84:87], a[60:63], v[116:119]\n\t" \
  "ds_read_b64_tr_b16 a[122:123], v198 offset:25600\n\t" \
  "v_mfma_f32_16x16x32_bf16 v[116:119], a[148:151], a[44:47], v[116:119]\n\t" \
  "ds_read_b64_tr_b16 a[124:125], v199 offset:24576\n\t" \
  "v_mfma_f32_16x16x32_bf16 v[24:27], a[152:155], a[32:35], v[24:27]\n\t" \
  "buffer_load_dwordx4 v230, s[64:67], s38 offen offset:0 lds\n\t" \
  "v_mfma_f32_16x16x32_bf16 v[24:27], a[88:91], a[48:51], v[24:27]\n\t" \
  "ds_read_b64_tr_b16 a[126:127], v199 offset:25600\n\t" \
  "v_mfma_f32_16x16x32_bf16 v[56:59], a[88:91], a[52:55], v[56:59]\n\t" \
  "v_mfma_f32_16x16x32_bf16 v[56:59], a[152:155], a[36:39], v[56:59]\n\t" \
  "v_mfma_f32_16x16x32_bf16 v[88:91], a[152:155], a[40:43], v[88:91]\n\t" \
  "buffer_load_dwordx4 v231, s[64:67], s38 offen offset:1024 lds\n\t" \
  "v_mfma_f32_16x16x32_bf16 v[88:91], a[88:91], a[56:59], v[88:91]\n\t" \
  "s_add_u32 s99, s53, 28672\n\t" \
  "v_mfma_f32_16x16x32_bf16 v[120:123], a[88:91], a[60:63], v[120:123]\n\t" \
  "v_mfma_f32_16x16x32_bf16 v[120:123], a[152:155], a[44:47], v[120:123]\n\t" \
  "v_mfma_f32_16x16x32_bf16 v[28:31], a[156:159], a[32:35], v[28:31]\n\t" \
  "buffer_load_dword v246, v245, s[68:71], s99 offen\n\t" \
  "v_mfma_f32_16x16x32_bf16 v[28:31], a[92:95], a[48:51], v[28:31]\n\t" \
  "v_mfma_f32_16x16x32_bf16 v[60:63], a[92:95], a[52:55], v[60:63]\n\t" \
  "v_mfma_f32_16x16x32_bf16 v[60:63], a[156:159], a[36:39], v[60:63]\n\t" \
  "v_mfma_f32_16x16x32_bf16 v[92:95], a[156:159], a[40:43], v[92:95]\n\t" \
  "v_mfma_f32_16x16x32_bf16 v[92:95], a[92:95], a[56:59], v[92:95]\n\t" \
  "v_mfma_f32_16x16x32_bf16 v[124:127], a[92:95], a[60:63], v[124:127]\n\t" \
  "s_waitcnt vmcnt(16) lgkmcnt(0)\n\t" \
  "v_mfma_f32_16x16x32_bf16 v[124:127], a[156:159], a[44:47], v[124:127]\n\t" \
  "s_barrier\n\t" \
  ".Lhs5:\n\ts_cbranch_execz .Lhs5n\n\t" \
  "v_mfma_f32_16x16x32_bf16 v[0:3], a[160:163], a[192:195], v[0:3]\n\t" \
  "buffer_load_dwordx4 v230, s[64:67], s38 offen offset:2048 lds\n\t" \
  "v_mfma_f32_16x16x32_bf16 v[0:3], a[96:99], a[208:211], v[0:3]\n\t" \
  "ds_read_b64_tr_b16 a[128:129], v192 offset:32768\n\t" \
  "v_mfma_f32_16x16x32_bf16 v[32:35], a[96:99], a[212:215], v[32:35]\n\t" \
  "ds_read_b64_tr_b16 a[130:131], v192 offset:33792\n\t" \
  "v_mfma_f32_16x16x32_bf16 v[32:35], a[160:163], a[196:199], v[32:35]\n\t" \
  "ds_read_b64 v[142:143], v141 offset:1024\n\t" \
  "v_mfma_f32_16x16x32_bf16 v[64:67], a[160:163], a[200:203], v[64:67]\n\t" \
  "ds_read_b64_tr_b16 a[132:133], v193 offset:32768\n\t" \
  "v_mfma_f32_16x16x32_bf16 v[64:67], a[96:99], a[216:219], v[64:67]\n\t" \
  "buffer_load_dwordx4 v231, s[64:67], s38 offen offset:3072 lds\n\t" \
  "v_mfma_f32_16x16x32_bf16 v[96:99], a[96:99], a[220:223], v[96:99]\n\t" \
  "s_add_u32 m0, s76, 32768\n\t" \
  "ds_read_b64_tr_b16 a[134:135], v193 offset:33792\n\t" \
  "s_add_u32 s96, s38, 16384\n\t" \
  "v_mfma_f32_16x16x32_bf16 v[96:99], a[160:163], a[204:207], v[96:99]\n\t" \
  "ds_read_b64_tr_b16 a[136:137], v194 offset:32768\n\t" \
  "v_mfma_f32_16x16x32_bf16 v[4:7], a[164:167], a[192:195], v[4:7]\n\t" \
  "ds_read_b64_tr_b16 a[138:139], v194 offset:33792\n\t" \
  "v_mfma_f32_16x16x32_bf16 v[4:7], a[100:103], a[208:211], v[4:7]\n\t" \
  "s_waitcnt lgkmcnt(4)\n\t" \
  "v_add_f32_dpp v142, v142, v142 row_ror:4 row_mask:0xf bank_mask:0xf\n\t" \
  "v_add_f32_dpp v143, v143, v143 row_ror:4 row_mask:0xf bank_mask:0xf\n\t" \
  "v_mfma_f32_16x16x32_bf16 v[36:39], a[100:103], a[212:215], v[36:39]\n\t" \
  "buffer_load_dwordx4 v224, s[60:63], s96 offen offset:0 lds\n\t" \
  "v_mfma_f32_16x16x32_bf16 v[36:39], a[164:167], a[196:199], v[36:39]\n\t" \
  "ds_read_b64_tr_b16 a[140:141], v195 offset:32768\n\t" \
  "v_add_f32_dpp v144, v142, v142 row_ror:8 row_mask:0xf bank_mask:0xf\n\t" \
  "v_mfma_f32_16x16x32_bf16 v[68:71], a[164:167], a[200:203], v[68:71]\n\t" \
  "ds_read_b64_tr_b16 a[142:143], v195 offset:33792\n\t" \
  "v_add_f32_dpp v146, v143, v143 row_ror:8 row_mask:0xf bank_mask:0xf\n\t" \
  "v_mfma_f32_16x16x32_bf16 v[68:71], a[100:103], a[216:219], v[68:71]\n\t" \
  "buffer_store_dwordx4 v[144:147], v140, s[16:19], s90 offen sc1\n\t" \
  "ds_read_b64_tr_b16 a[144:145], v196 offset:32768\n\t" \
  "ds_read_b64_tr_b16 a[146:147], v196 offset:33792\n\t" \
  "ds_read_b64_tr_b16 a[148:149], v197 offset:32768\n\t" \
  "v_mfma_f32_16x16x32_bf16 v[100:103], a[100:103], a[220:223], v[100:103]\n\t" \
  "buffer_load_dwordx4 v225, s[60:63], s96 offen offset:1024 lds\n\t" \
  "v_mfma_f32_16x16x32_bf16 v[100:103], a[164:167], a[204:207], v[100:103]\n\t" \
  "ds_read_b64_tr_b16 a[150:151], v197 offset:33792\n\t" \
  "v_mfma_f32_16x16x32_bf16 v[8:11], a[168:171], a[192:195], v[8:11]\n\t" \
  "ds_read_b64_tr_b16 a[152:153], v198 offset:32768\n\t" \
  "v_mfma_f32_16x16x32_bf16 v[8:11], a[104:107], a[208:211], v[8:11]\n\t" \
  "ds_read_b64_tr_b16 a[154:155], v198 offset:33792\n\t" \
  "v_mfma_f32_16x16x32_bf16 v[40:43], a[104:107], a[212:215], v[40:43]\n\t" \
  "ds_read_b64_tr_b16 a[156:157], v199 offset:32768\n\t" \
  "v_mfma_f32_16x16x32_bf16 v[40:43], a[168:171], a[196:199], v[40:43]\n\t" \
  "buffer_load_dwordx4 v226, s[60:63], s96 offen offset:2048 lds\n\t" \
  "v_mfma_f32_16x16x32_bf16 v[72:75], a[168:171], a[200:203], v[72:75]\n\t" \
  "ds_read_b64_tr_b16 a[158:159], v199 offset:33792\n\t" \
  "v_mfma_f32_16x16x32_bf16 v[72:75], a[104:107], a[216:219], v[72:75]\n\t" \
  "ds_read_b64_tr_b16 a[64:65], v192 offset:40960\n\t" \
  "v_mfma_f32_16x16x32_bf16 v[104:107], a[104:107], a[220:223], v[104:107]\n\t" \
  "ds_read_b64_tr_b16 a[66:67], v192 offset:41984\n\t" \
  "v_mfma_f32_16x16x32_bf16 v[104:107], a[168:171], a[204:207], v[104:107]\n\t" \
  "ds_read_b64_tr_b16 a[68:69], v193 offset:40960\n\t" \
  "v_mfma_f32_16x16x32_bf16 v[12:15], a[172:175], a[192:195], v[12:15]\n\t" \
  "buffer_load_dwordx4 v227, s[60:63], s96 offen offset:3072 lds\n\t" \
  "v_mfma_f32_16x16x32_bf16 v[12:15], a[108:111], a[208:211], v[12:15]\n\t" \
  "ds_read_b64_tr_b16 a[70:71], v193 offset:41984\n\t" \
  "v_mfma_f32_16x16x32_bf16 v[44:47], a[108:111], a[212:215], v[44:47]\n\t" \
  "ds_read_b64_tr_b16 a[72:73], v194 offset:40960\n\t" \
  "v_mfma_f32_16x16x32_bf16 v[44:47], a[172:175], a[196:199], v[44:47]\n\t" \
  "ds_read_b64_tr_b16 a[74:75], v194 offset:41984\n\t" \
  "v_mfma_f32_16x16x32_bf16 v[76:79], a[172:175], a[200:203], v[76:79]\n\t" \
  "ds_read_b64_tr_b16 a[76:77], v195 offset:40960\n\t" \
  "v_mfma_f32_16x16x32_bf16 v[76:79], a[108:111], a[216:219], v[76:79]\n\t" \
  "ds_read_b64_tr_b16 a[78:79], v195 offset:41984\n\t" \
  "s_add_u32 s99, s53, 43008\n\t" \
  "v_mfma_f32_16x16x32_bf16 v[108:111], a[108:111], a[220:223], v[108:111]\n\t" \
  "ds_read_b64_tr_b16 a[80:81], v196 offset:40960\n\t" \
  "v_mfma_f32_16x16x32_bf16 v[108:111], a[172:175], a[204:207], v[108:111]\n\t" \
  "buffer_load_dword v246, v245, s[68:71], s99 offen\n\t" \
  "v_mfma_f32_16x16x32_bf16 v[16:19], a[176:179], a[192:195], v[16:19]\n\t" \
  "ds_read_b64_tr_b16 a[82:83], v196 offset:41984\n\t" \
  "v_mfma_f32_16x16x32_bf16 v[16:19], a[112:115], a[208:211], v[16:19]\n\t" \
  "ds_read_b64_tr_b16 a[84:85], v197 offset:40960\n\t" \
  "v_mfma_f32_16x16x32_bf16 v[48:51], a[112:115], a[212:215], v[48:51]\n\t" \
  "ds_read_b64_tr_b16 a[86:87], v197 offset:41984\n\t" \
  "v_mfma_f32_16x16x32_bf16 v[48:51], a[176:179], a[196:199], v[48:51]\n\t" \
  "v_mfma_f32_16x16x32_bf16 v[80:83], a[176:179], a[200:203], v[80:83]\n\t" \
  "ds_read_b64_tr_b16 a[88:89], v198 offset:40960\n\t" \
  "v_mfma_f32_16x16x32_bf16 v[80:83], a[112:115], a[216:219], v[80:83]\n\t" \
  "v_mfma_f32_16x16x32_bf16 v[112:115], a[112:115], a[220:223], v[112:115]\n\t" \
  "ds_read_b64_tr_b16 a[90:91], v198 offset:41984\n\t" \
  "v_mfma_f32_16x16x32_bf16 v[112:115], a[176:179], a[204:207], v[112:115]\n\t" \
  "ds_read_b64_tr_b16 a[92:93], v199 offset:40960\n\t" \
  "v_mfma_f32_16x16x32_bf16 v[20:23], a[180:183], a[192:195], v[20:23]\n\t" \
  "v_mfma_f32_16x16x32_bf16 v[20:23], a[116:119], a[208:211], v[20:23]\n\t" \
  "ds_read_b64_tr_b16 a[94:95], v199 offset:41984\n\t" \
  "v_mfma_f32_16x16x32_bf16 v[52:55], a[116:119], a[212:215], v[52:55]\n\t" \
  "v_mfma_f32_16x16x32_bf16 v[52:55], a[180:183], a[196:199], v[52:55]\n\t" \
  "v_mfma_f32_16x16x32_bf16 v[84:87], a[180:183], a[200:203], v[84:87]\n\t" \
  "v_mfma_f32_16x16x32_bf16 v[84:87], a[116:119], a[216:219], v[84:87]\n\t" \
  "v_mfma_f32_16x16x32_bf16 v[116:119], a[116:119], a[220:223], v[116:119]\n\t" \
  "v_mfma_f32_16x16x32_bf16 v[116:119], a[180:183], a[204:207], v[116:119]\n\t" \
  "v_mfma_f32_16x16x32_bf16 v[24:27], a[184:187], a[192:195], v[24:27]\n\t" \
  "v_mfma_f32_16x16x32_bf16 v[24:27], a[120:123], a[208:211], v[24:27]\n\t" \
  "v_mfma_f32_16x16x32_bf16 v[56:59], a[120:123], a[212:215], v[56:59]\n\t" \
  "v_mfma_f32_16x16x32_bf16 v[56:59], a[184:187], a[196:199], v[56:59]\n\t" \
  "v_mfma_f32_16x16x32_bf16 v[88:91], a[184:187], a[200:203], v[88:91]\n\t" \
  "v_mfma_f32_16x16x32_bf16 v[88:91], a[120:123], a[216:219], v[88:91]\n\t" \
  "v_mfma_f32_16x16x32_bf16 v[120:123], a[120:123], a[220:223], v[120:123]\n\t" \
  "v_mfma_f32_16x16x32_bf16 v[120:123], a[184:187], a[204:207], v[120:123]\n\t" \
  "v_mfma_f32_16x16x32_bf16 v[28:31], a[188:191], a[192:195], v[28:31]\n\t" \
  "v_mfma_f32_16x16x32_bf16 v[28:31], a[124:127], a[208:211], v[28:31]\n\t" \
  "v_mfma_f32_16x16x32_bf16 v[60:63], a[124:127], a[212:215], v[60:63]\n\t" \
  "v_mfma_f32_16x16x32_bf16 v[60:63], a[188:191], a[196:199], v[60:63]\n\t" \
  "v_mfma_f32_16x16x32_bf16 v[92:95], a[188:191], a[200:203], v[92:95]\n\t" \
  "v_mfma_f32_16x16x32_bf16 v[92:95], a[124:127], a[216:219], v[92:95]\n\t" \
  "v_mfma_f32_16x16x32_bf16 v[124:127], a[124:127], a[220:223], v[124:127]\n\t" \
  "v_mfma_f32_16x16x32_bf16 v[124:127], a[188:191], a[204:207], v[124:127]\n\t" \
  "s_waitcnt vmcnt(16) lgkmcnt(0)\n\t" \
  FSTP("s_waitcnt lgkmcnt(0)\n\t") \
  FSTP("v_writelane_b32 v255, s94, 52\n\t") \
  FSTP("v_writelane_b32 v255, s92, 53\n\t") \
  FSTA("s_memrealtime s[94:95]\n\t") \
  FSTA("s_memtime s[92:93]\n\t") \
  FSTA("s_waitcnt lgkmcnt(0)\n\t") \
  FSTA("v_writelane_b32 v255, s94, 16\n\t") \
  FSTA("v_writelane_b32 v255, s92, 17\n\t") \
  ".subsection 1\n\t" \
  ".Lfr_s:\n\t" \
  "s_mov_b32 s54, 0\n\t" \
  ".Lfr_r:\n\t" \
  "s_sleep 1\n\t" \
  "buffer_load_dwordx4 v[128:131], v248, s[16:19], s53 offen sc1\n\t" \
  "buffer_load_dwordx4 v[132:135], v248, s[16:19], s58 offen sc1\n\t" \
  "buffer_load_dwordx4 v[136:139], v248, s[16:19], s90 offen sc1\n\t" \
  "buffer_load_dwordx4 v[140:143], v248, s[16:19], s91 offen sc1\n\t" \
  "buffer_load_dwordx4 v[160:163], v249, s[16:19], s96 offen sc1\n\t" \
  "buffer_load_dwordx4 v[164:167], v249, s[16:19], s97 offen sc1\n\t" \
  "buffer_load_dwordx4 v[168:171], v249, s[16:19], s99 offen sc1\n\t" \
  "buffer_load_dwordx4 v[172:175], v249, s[16:19], s57 offen sc1\n\t" \
  "s_waitcnt vmcnt(0)\n\t" \
  "v_xor_b32 v144, v211, v129\n\t" \
  "v_bitop3_b32 v144, v144, v131, v211 bitop3:0xf6\n\t" \
  "v_bitop3_b32 v144, v144, v133, v211 bitop3:0xf6\n\t" \
  "v_bitop3_b32 v144, v144, v135, v211 bitop3:0xf6\n\t" \
  "v_bitop3_b32 v144, v144, v137, v211 bitop3:0xf6\n\t" \
  "v_bitop3_b32 v144, v144, v139, v211 bitop3:0xf6\n\t" \
  "v_bitop3_b32 v144, v144, v141, v211 bitop3:0xf6\n\t" \
  "v_bitop3_b32 v144, v144, v143, v211 bitop3:0xf6\n\t" \
  "v_bitop3_b32 v144, v144, v161, v211 bitop3:0xf6\n\t" \
  "v_bitop3_b32 v144, v144, v163, v211 bitop3:0xf6\n\t" \
  "v_bitop3_b32 v144, v144, v165, v211 bitop3:0xf6\n\t" \
  "v_bitop3_b32 v144, v144, v167, v211 bitop3:0xf6\n\t" \
  "v_bitop3_b32 v144, v144, v169, v211 bitop3:0xf6\n\t" \
  "v_bitop3_b32 v144, v144, v171, v211 bitop3:0xf6\n\t" \
  "v_bitop3_b32 v144, v144, v173, v211 bitop3:0xf6\n\t" \
  "v_bitop3_b32 v144, v144, v175, v211 bitop3:0xf6\n\t" \
  "v_cmp_ne_u32 vcc, 0, v144\n\t" \
  "v_add_f32 v146, v128, v132\n\t" \
  "v_add_f32 v148, v136, v140\n\t" \
  "v_add_f32 v147, v130, v134\n\t" \
  "v_add_f32 v149, v138, v142\n\t" \
  "v_add_f32 v146, v146, v148\n\t" \
  "v_add_f32 v147, v147, v149\n\t" \
  "s_cbranch_vccz .Lfr_k\n\t" \
  "s_add_u32 s54, s54, 1\n\t" \
  "s_cmp_lt_u32 s54, " FQ_CAP "\n\t" \
  "s_cbranch_scc1 .Lfr_r\n\t" \
  "buffer_store_dword v211, off, s[16:19], 0 offset:256 sc1\n\t" \
  "s_branch .Lfr_k\n\t" \
  ".Lfq_s:\n\t" \
  "s_mov_b32 s97, 0\n\t" \
  ".Lfq_r:\n\t" \
  "buffer_load_dword v252, v210, s[16:19], s84 offen sc1\n\t" \
  "s_waitcnt vmcnt(0)\n\t" \
  "v_cmp_ne_u32 vcc, v211, v252\n\t" \
  "s_cbranch_vccz .Lfq_k\n\t" \
  "s_add_u32 s97, s97, 1\n\t" \
  "s_cmp_lt_u32 s97, " FQ_CAP "\n\t" \
  "s_cbranch_scc1 .Lfq_r\n\t" \
  "buffer_store_dword v211, off, s[16:19], 0 offset:256 sc1\n\t" \
  "s_branch .Lfq_k\n\t" \
  ".subsection 0\n\t"
static_assert(W_VOTE == 8192 && W_FLAGR == 32768 && W_FLAGO == 131072 && W_MODB == 2228224 && W_OT == 8388608 && W_XCH == 25165824 && W_EPOCH == 2490368 && W_FLAGH == 2494464 && W_ST1 == 2097152 && W_MODA == 2162688 && W_HT == 4194304 && W_PROBE == 16384);
#define STS(x)
#define AO_STV
#define STA(x)
#define STP(x)
#define AT_ASM \
  "s_cmp_eq_u32 s37, 0\n\t" \
  "s_cbranch_scc1 .Lsf_go\n\t" \
  "v_mfma_f32_16x16x32_bf16 v[28:31], a[156:159], a[224:227], v[28:31]\n\t" \
  "s_add_u32 s77, s51, 131072\n\t" \
  "s_mov_b32 s89, 0\n\t" \
  "s_brev_b32 s78, 1\n\t" \
  "v_mfma_f32_16x16x32_bf16 v[28:31], a[92:95], a[240:243], v[28:31]\n\t" \
  "s_mov_b32 s79, 0x0f800000\n\t" \
  "v_mfma_f32_16x16x32_bf16 v[24:27], a[152:155], a[224:227], v[24:27]\n\t" \
  "s_nop 2\n\t" \
  "v_mfma_f32_16x16x32_bf16 v[24:27], a[88:91], a[240:243], v[24:27]\n\t" \
  "s_nop 2\n\t" \
  "v_mfma_f32_16x16x32_bf16 v[20:23], a[148:151], a[224:227], v[20:23]\n\t" \
  "v_cvt_pk_bf16_f32 v31, v30, v31\n\t" \
  "v_cvt_pk_bf16_f32 v30, v28, v29\n\t" \
  "v_mfma_f32_16x16x32_bf16 v[20:23], a[84:87], a[240:243], v[20:23]\n\t" \
  "v_mfma_f32_16x16x32_bf16 v[16:19], a[144:147], a[224:227], v[16:19]\n\t" \
  "v_cvt_pk_bf16_f32 v28, v24, v25\n\t" \
  "v_cvt_pk_bf16_f32 v29, v26, v27\n\t" \
  "v_mfma_f32_16x16x32_bf16 v[16:19], a[80:83], a[240:243], v[16:19]\n\t" \
  "v_permlane16_swap_b32 v28, v30\n\t" \
  "v_mfma_f32_16x16x32_bf16 v[12:15], a[140:143], a[224:227], v[12:15]\n\t" \
  "v_permlane16_swap_b32 v29, v31\n\t" \
  "v_mfma_f32_16x16x32_bf16 v[12:15], a[76:79], a[240:243], v[12:15]\n\t" \
  "v_cvt_pk_bf16_f32 v27, v22, v23\n\t" \
  "v_cvt_pk_bf16_f32 v26, v20, v21\n\t" \
  "v_mfma_f32_16x16x32_bf16 v[8:11], a[136:139], a[224:227], v[8:11]\n\t" \
  "s_mov_b32 s54, 0\n\t" \
  "v_cvt_pk_bf16_f32 v24, v16, v17\n\t" \
  "v_cvt_pk_bf16_f32 v25, v18, v19\n\t" \
  "v_mfma_f32_16x16x32_bf16 v[8:11], a[72:75], a[240:243], v[8:11]\n\t" \
  "v_permlane16_swap_b32 v24, v26\n\t" \
  "v_mfma_f32_16x16x32_bf16 v[4:7], a[132:135], a[224:227], v[4:7]\n\t" \
  "v_cvt_pk_bf16_f32 v23, v14, v15\n\t" \
  "v_cvt_pk_bf16_f32 v22, v12, v13\n\t" \
  "v_mfma_f32_16x16x32_bf16 v[4:7], a[68:71], a[240:243], v[4:7]\n\t" \
  "v_permlane16_swap_b32 v25, v27\n\t" \
  "v_mfma_f32_16x16x32_bf16 v[0:3], a[128:131], a[224:227], v[0:3]\n\t" \
  "s_waitcnt vmcnt(1)\n\t" \
  "v_cvt_pk_bf16_f32 v20, v8, v9\n\t" \
  "v_cvt_pk_bf16_f32 v21, v10, v11\n\t" \
  "v_mfma_f32_16x16x32_bf16 v[0:3], a[64:67], a[240:243], v[0:3]\n\t" \
  "s_barrier\n\t" \
  "v_mfma_f32_16x16x32_bf16 v[60:63], a[156:159], a[228:231], v[60:63]\n\t" \
  "v_cvt_pk_bf16_f32 v19, v6, v7\n\t" \
  "v_cvt_pk_bf16_f32 v18, v4, v5\n\t" \
  "v_mfma_f32_16x16x32_bf16 v[60:63], a[92:95], a[244:247], v[60:63]\n\t" \
  "v_permlane16_swap_b32 v20, v22\n\t" \
  "v_mfma_f32_16x16x32_bf16 v[56:59], a[152:155], a[228:231], v[56:59]\n\t" \
  "ds_read_b128 a[0:3], v220 offset:0\n\t" \
  "v_cvt_pk_bf16_f32 v16, v0, v1\n\t" \
  "v_mfma_f32_16x16x32_bf16 v[56:59], a[88:91], a[244:247], v[56:59]\n\t" \
  "ds_read_b128 a[4:7], v221 offset:0\n\t" \
  "v_cvt_pk_bf16_f32 v17, v2, v3\n\t" \
  "v_mfma_f32_16x16x32_bf16 v[52:55], a[148:151], a[228:231], v[52:55]\n\t" \
  "v_cvt_pk_bf16_f32 v63, v62, v63\n\t" \
  "v_cvt_pk_bf16_f32 v62, v60, v61\n\t" \
  "v_mfma_f32_16x16x32_bf16 v[52:55], a[84:87], a[244:247], v[52:55]\n\t" \
  "v_permlane16_swap_b32 v21, v23\n\t" \
  "v_mfma_f32_16x16x32_bf16 v[48:51], a[144:147], a[228:231], v[48:51]\n\t" \
  "v_cvt_pk_bf16_f32 v60, v56, v57\n\t" \
  "v_cvt_pk_bf16_f32 v61, v58, v59\n\t" \
  "v_mfma_f32_16x16x32_bf16 v[48:51], a[80:83], a[244:247], v[48:51]\n\t" \
  "v_permlane16_swap_b32 v16, v18\n\t" \
  "v_mfma_f32_16x16x32_bf16 v[44:47], a[140:143], a[228:231], v[44:47]\n\t" \
  "ds_read_b128 a[8:11], v222 offset:0\n\t" \
  "v_cvt_pk_bf16_f32 v59, v54, v55\n\t" \
  "v_mfma_f32_16x16x32_bf16 v[44:47], a[76:79], a[244:247], v[44:47]\n\t" \
  "ds_read_b128 a[12:15], v223 offset:0\n\t" \
  "v_cvt_pk_bf16_f32 v58, v52, v53\n\t" \
  "v_mfma_f32_16x16x32_bf16 v[40:43], a[136:139], a[228:231], v[40:43]\n\t" \
  "v_cvt_pk_bf16_f32 v56, v48, v49\n\t" \
  "v_cvt_pk_bf16_f32 v57, v50, v51\n\t" \
  "v_mfma_f32_16x16x32_bf16 v[40:43], a[72:75], a[244:247], v[40:43]\n\t" \
  "s_add_u32 s99, s53, 0\n\t" \
  "ds_read_b128 a[16:19], v220 offset:4096\n\t" \
  "v_cvt_pk_bf16_f32 v55, v46, v47\n\t" \
  "v_mfma_f32_16x16x32_bf16 v[36:39], a[132:135], a[228:231], v[36:39]\n\t" \
  "ds_read_b128 a[20:23], v221 offset:4096\n\t" \
  "v_cvt_pk_bf16_f32 v54, v44, v45\n\t" \
  "v_mfma_f32_16x16x32_bf16 v[36:39], a[68:71], a[244:247], v[36:39]\n\t" \
  "ds_read_b128 a[24:27], v222 offset:4096\n\t" \
  "v_cvt_pk_bf16_f32 v52, v40, v41\n\t" \
  "v_mfma_f32_16x16x32_bf16 v[32:35], a[128:131], a[228:231], v[32:35]\n\t" \
  "buffer_load_dword v246, v245, s[72:75], s99 offen\n\t" \
  "v_mfma_f32_16x16x32_bf16 v[32:35], a[64:67], a[244:247], v[32:35]\n\t" \
  "ds_read_b128 a[28:31], v223 offset:4096\n\t" \
  "v_cvt_pk_bf16_f32 v53, v42, v43\n\t" \
  "v_mfma_f32_16x16x32_bf16 v[92:95], a[156:159], a[232:235], v[92:95]\n\t" \
  "v_cvt_pk_bf16_f32 v51, v38, v39\n\t" \
  "v_cvt_pk_bf16_f32 v50, v36, v37\n\t" \
  "v_mfma_f32_16x16x32_bf16 v[92:95], a[92:95], a[248:251], v[92:95]\n\t" \
  "v_permlane16_swap_b32 v17, v19\n\t" \
  "v_mfma_f32_16x16x32_bf16 v[88:91], a[152:155], a[232:235], v[88:91]\n\t" \
  "v_cvt_pk_bf16_f32 v48, v32, v33\n\t" \
  "v_cvt_pk_bf16_f32 v49, v34, v35\n\t" \
  "v_mfma_f32_16x16x32_bf16 v[88:91], a[88:91], a[248:251], v[88:91]\n\t" \
  "v_permlane16_swap_b32 v60, v62\n\t" \
  "v_mfma_f32_16x16x32_bf16 v[84:87], a[148:151], a[232:235], v[84:87]\n\t" \
  "ds_read_b128 a[32:35], v220 offset:8192\n\t" \
  "v_cvt_pk_bf16_f32 v95, v94, v95\n\t" \
  "v_mfma_f32_16x16x32_bf16 v[84:87], a[84:87], a[248:251], v[84:87]\n\t" \
  "ds_read_b128 a[36:39], v221 offset:8192\n\t" \
  "v_cvt_pk_bf16_f32 v94, v92, v93\n\t" \
  "v_mfma_f32_16x16x32_bf16 v[80:83], a[144:147], a[232:235], v[80:83]\n\t" \
  "v_cvt_pk_bf16_f32 v92, v88, v89\n\t" \
  "v_cvt_pk_bf16_f32 v93, v90, v91\n\t" \
  "v_mfma_f32_16x16x32_bf16 v[80:83], a[80:83], a[248:251], v[80:83]\n\t" \
  "v_permlane16_swap_b32 v61, v63\n\t" \
  "v_mfma_f32_16x16x32_bf16 v[76:79], a[140:143], a[232:235], v[76:79]\n\t" \
  "buffer_load_dword v246, v247, s[16:19], 0 offen sc1\n\t" \
  "v_mfma_f32_16x16x32_bf16 v[76:79], a[76:79], a[248:251], v[76:79]\n\t" \
  "ds_read_b128 a[40:43], v222 offset:8192\n\t" \
  "v_cvt_pk_bf16_f32 v91, v86, v87\n\t" \
  "v_mfma_f32_16x16x32_bf16 v[72:75], a[136:139], a[232:235], v[72:75]\n\t" \
  "ds_read_b128 a[44:47], v223 offset:8192\n\t" \
  "v_permlane16_swap_b32 v56, v58\n\t" \
  "v_mfma_f32_16x16x32_bf16 v[72:75], a[72:75], a[248:251], v[72:75]\n\t" \
  "v_cvt_pk_bf16_f32 v90, v84, v85\n\t" \
  "v_cvt_pk_bf16_f32 v88, v80, v81\n\t" \
  "v_mfma_f32_16x16x32_bf16 v[68:71], a[132:135], a[232:235], v[68:71]\n\t" \
  "ds_read_b128 a[48:51], v220 offset:12288\n\t" \
  "v_permlane16_swap_b32 v57, v59\n\t" \
  "v_mfma_f32_16x16x32_bf16 v[68:71], a[68:71], a[248:251], v[68:71]\n\t" \
  "ds_read_b128 a[52:55], v221 offset:12288\n\t" \
  "v_cvt_pk_bf16_f32 v89, v82, v83\n\t" \
  "v_mfma_f32_16x16x32_bf16 v[64:67], a[128:131], a[232:235], v[64:67]\n\t" \
  "v_cvt_pk_bf16_f32 v87, v78, v79\n\t" \
  "v_cvt_pk_bf16_f32 v86, v76, v77\n\t" \
  "v_mfma_f32_16x16x32_bf16 v[64:67], a[64:67], a[248:251], v[64:67]\n\t" \
  "s_waitcnt lgkmcnt(10)\n\t" \
  "v_cvt_pk_bf16_f32 v84, v72, v73\n\t" \
  "v_permlane16_swap_b32 v52, v54\n\t" \
  "v_mfma_f32_16x16x32_bf16 v[0:3], a[0:3], v[16:19], 0\n\t" \
  "ds_read_b128 a[56:59], v222 offset:12288\n\t" \
  "v_cvt_pk_bf16_f32 v85, v74, v75\n\t" \
  "v_mfma_f32_16x16x32_bf16 v[0:3], a[4:7], v[20:23], v[0:3]\n\t" \
  "ds_read_b128 a[60:63], v223 offset:12288\n\t" \
  "v_permlane16_swap_b32 v53, v55\n\t" \
  "v_mfma_f32_16x16x32_bf16 v[0:3], a[8:11], v[24:27], v[0:3]\n\t" \
  "v_cvt_pk_bf16_f32 v83, v70, v71\n\t" \
  "v_cvt_pk_bf16_f32 v82, v68, v69\n\t" \
  "v_mfma_f32_16x16x32_bf16 v[0:3], a[12:15], v[28:31], v[0:3]\n\t" \
  "v_cvt_pk_bf16_f32 v80, v64, v65\n\t" \
  "v_cvt_pk_bf16_f32 v81, v66, v67\n\t" \
  "v_mfma_f32_16x16x32_bf16 v[124:127], a[156:159], a[236:239], v[124:127]\n\t" \
  "v_permlane16_swap_b32 v48, v50\n\t" \
  "v_mfma_f32_16x16x32_bf16 v[124:127], a[92:95], a[252:255], v[124:127]\n\t" \
  "s_nop 2\n\t" \
  "v_max3_f32 v194, v0, v1, v2\n\t" \
  "v_mfma_f32_16x16x32_bf16 v[120:123], a[152:155], a[236:239], v[120:123]\n\t" \
  "v_permlane16_swap_b32 v49, v51\n\t" \
  "v_max_f32 v194, v194, v3\n\t" \
  "v_mov_b32 v195, v194\n\t" \
  "v_mfma_f32_16x16x32_bf16 v[120:123], a[88:91], a[252:255], v[120:123]\n\t" \
  "v_cvt_pk_bf16_f32 v127, v126, v127\n\t" \
  "v_permlane32_swap_b32 v194, v195\n\t" \
  "v_max_f32 v194, v194, v195\n\t" \
  "v_mov_b32 v195, v194\n\t" \
  "v_mfma_f32_16x16x32_bf16 v[116:119], a[148:151], a[236:239], v[116:119]\n\t" \
  "s_nop 0\n\t" \
  "v_permlane16_swap_b32 v194, v195\n\t" \
  "v_max_f32 v194, v194, v195\n\t" \
  "v_sub_f32 v172, 0xc2000000, v194\n\t" \
  "v_sub_f32 v173, 0xc2000000, v194\n\t" \
  "v_mfma_f32_16x16x32_bf16 v[116:119], a[84:87], a[252:255], v[116:119]\n\t" \
  "v_cvt_pk_bf16_f32 v126, v124, v125\n\t" \
  "v_permlane16_swap_b32 v92, v94\n\t" \
  "v_sub_f32 v174, 0xc2000000, v194\n\t" \
  "v_sub_f32 v175, 0xc2000000, v194\n\t" \
  "v_mfma_f32_16x16x32_bf16 v[112:115], a[144:147], a[236:239], v[112:115]\n\t" \
  "v_cvt_pk_bf16_f32 v124, v120, v121\n\t" \
  "v_cvt_pk_bf16_f32 v125, v122, v123\n\t" \
  "v_add_f32 v0, v0, v172\n\t" \
  "v_add_f32 v1, v1, v172\n\t" \
  "v_mfma_f32_16x16x32_bf16 v[112:115], a[80:83], a[252:255], v[112:115]\n\t" \
  "v_permlane16_swap_b32 v93, v95\n\t" \
  "v_add_f32 v2, v2, v172\n\t" \
  "v_add_f32 v3, v3, v172\n\t" \
  "v_mfma_f32_16x16x32_bf16 v[108:111], a[140:143], a[236:239], v[108:111]\n\t" \
  "v_cvt_pk_bf16_f32 v123, v118, v119\n\t" \
  "v_exp_f32 v0, v0\n\t" \
  "v_mfma_f32_16x16x32_bf16 v[108:111], a[76:79], a[252:255], v[108:111]\n\t" \
  "v_cvt_pk_bf16_f32 v122, v116, v117\n\t" \
  "v_exp_f32 v1, v1\n\t" \
  "v_mfma_f32_16x16x32_bf16 v[104:107], a[136:139], a[236:239], v[104:107]\n\t" \
  "v_cvt_pk_bf16_f32 v120, v112, v113\n\t" \
  "v_exp_f32 v2, v2\n\t" \
  "v_mfma_f32_16x16x32_bf16 v[104:107], a[72:75], a[252:255], v[104:107]\n\t" \
  "v_cvt_pk_bf16_f32 v121, v114, v115\n\t" \
  "v_exp_f32 v3, v3\n\t" \
  "v_mfma_f32_16x16x32_bf16 v[100:103], a[132:135], a[236:239], v[100:103]\n\t" \
  "s_add_u32 m0, s38, 49152\n\t" \
  "v_permlane16_swap_b32 v88, v90\n\t" \
  "v_mfma_f32_16x16x32_bf16 v[100:103], a[68:71], a[252:255], v[100:103]\n\t" \
  "s_add_u32 s96, s38, 32768\n\t" \
  "v_permlane16_swap_b32 v89, v91\n\t" \
  "v_mfma_f32_16x16x32_bf16 v[96:99], a[128:131], a[236:239], v[96:99]\n\t" \
  "s_waitcnt lgkmcnt(0)\n\t" \
  "v_permlane16_swap_b32 v84, v86\n\t" \
  "v_mfma_f32_16x16x32_bf16 v[96:99], a[64:67], a[252:255], v[96:99]\n\t" \
  "s_barrier\n\t" \
  STS("s_memrealtime s[94:95]\n\t") \
  STS("s_memtime s[92:93]\n\t") \
  "v_mfma_f32_16x16x32_bf16 v[4:7], a[16:19], v[16:19], v[172:175]\n\t" \
  "buffer_load_dwordx4 v224, s[60:63], s96 offen offset:0 lds\n\t" \
  "v_mfma_f32_16x16x32_bf16 v[4:7], a[20:23], v[20:23], v[4:7]\n\t" \
  "v_permlane16_swap_b32 v85, v87\n\t" \
  "v_mfma_f32_16x16x32_bf16 v[4:7], a[24:27], v[24:27], v[4:7]\n\t" \
  "ds_read_b128 a[192:195], v220 offset:16384\n\t" \
  "v_cvt_pk_bf16_f32 v119, v110, v111\n\t" \
  "v_mfma_f32_16x16x32_bf16 v[4:7], a[28:31], v[28:31], v[4:7]\n\t" \
  "v_permlane16_swap_b32 v80, v82\n\t" \
  "v_mfma_f32_16x16x32_bf16 v[8:11], a[32:35], v[16:19], v[172:175]\n\t" \
  "ds_read_b128 a[196:199], v221 offset:16384\n\t" \
  "v_cvt_pk_bf16_f32 v118, v108, v109\n\t" \
  "v_mfma_f32_16x16x32_bf16 v[8:11], a[36:39], v[20:23], v[8:11]\n\t" \
  "v_cvt_pk_bf16_f32 v116, v104, v105\n\t" \
  "v_cvt_pk_bf16_f32 v117, v106, v107\n\t" \
  "s_nop 0\n\t" \
  "v_exp_f32 v4, v4\n\t" \
  "v_mfma_f32_16x16x32_bf16 v[8:11], a[40:43], v[24:27], v[8:11]\n\t" \
  "ds_read_b128 a[200:203], v222 offset:16384\n\t" \
  "v_exp_f32 v5, v5\n\t" \
  "v_exp_f32 v6, v6\n\t" \
  "v_mfma_f32_16x16x32_bf16 v[8:11], a[44:47], v[28:31], v[8:11]\n\t" \
  "v_permlane16_swap_b32 v81, v83\n\t" \
  "v_exp_f32 v7, v7\n\t" \
  "v_mfma_f32_16x16x32_bf16 v[12:15], a[48:51], v[16:19], v[172:175]\n\t" \
  "v_permlane16_swap_b32 v124, v126\n\t" \
  "v_mfma_f32_16x16x32_bf16 v[12:15], a[52:55], v[20:23], v[12:15]\n\t" \
  "buffer_load_dwordx4 v225, s[60:63], s96 offen offset:1024 lds\n\t" \
  "v_mfma_f32_16x16x32_bf16 v[12:15], a[56:59], v[24:27], v[12:15]\n\t" \
  "s_add_u32 s99, s53, 14336\n\t" \
  "ds_read_b128 a[204:207], v223 offset:16384\n\t" \
  "v_exp_f32 v8, v8\n\t" \
  "v_exp_f32 v9, v9\n\t" \
  "v_mfma_f32_16x16x32_bf16 v[12:15], a[60:63], v[28:31], v[12:15]\n\t" \
  "v_cvt_pk_bf16_f32 v115, v102, v103\n\t" \
  "v_exp_f32 v10, v10\n\t" \
  "v_exp_f32 v11, v11\n\t" \
  "v_mfma_f32_16x16x32_bf16 v[32:35], a[0:3], v[48:51], v[172:175]\n\t" \
  STS("v_writelane_b32 v255, s94, 18\n\t") \
  STS("v_writelane_b32 v255, s92, 19\n\t") \
  "buffer_load_dword v246, v245, s[72:75], s99 offen\n\t" \
  "v_mfma_f32_16x16x32_bf16 v[32:35], a[4:7], v[52:55], v[32:35]\n\t" \
  "ds_read_b128 a[208:211], v220 offset:20480\n\t" \
  "v_cvt_pk_bf16_f32 v114, v100, v101\n\t" \
  "v_exp_f32 v12, v12\n\t" \
  "v_mfma_f32_16x16x32_bf16 v[32:35], a[8:11], v[56:59], v[32:35]\n\t" \
  "v_permlane16_swap_b32 v125, v127\n\t" \
  "v_exp_f32 v13, v13\n\t" \
  "v_mfma_f32_16x16x32_bf16 v[32:35], a[12:15], v[60:63], v[32:35]\n\t" \
  "v_permlane16_swap_b32 v120, v122\n\t" \
  "v_exp_f32 v14, v14\n\t" \
  "v_mfma_f32_16x16x32_bf16 v[36:39], a[16:19], v[48:51], v[172:175]\n\t" \
  "ds_read_b128 a[212:215], v221 offset:20480\n\t" \
  "v_exp_f32 v15, v15\n\t" \
  "v_mfma_f32_16x16x32_bf16 v[36:39], a[20:23], v[52:55], v[36:39]\n\t" \
  "buffer_load_dwordx4 v226, s[60:63], s96 offen offset:2048 lds\n\t" \
  "v_mfma_f32_16x16x32_bf16 v[36:39], a[24:27], v[56:59], v[36:39]\n\t" \
  "ds_read_b128 a[216:219], v222 offset:20480\n\t" \
  "v_exp_f32 v32, v32\n\t" \
  "v_mfma_f32_16x16x32_bf16 v[36:39], a[28:31], v[60:63], v[36:39]\n\t" \
  "v_cvt_pk_bf16_f32 v112, v96, v97\n\t" \
  "v_exp_f32 v33, v33\n\t" \
  "v_mfma_f32_16x16x32_bf16 v[40:43], a[32:35], v[48:51], v[172:175]\n\t" \
  "v_cvt_pk_bf16_f32 v113, v98, v99\n\t" \
  "v_exp_f32 v34, v34\n\t" \
  "v_mfma_f32_16x16x32_bf16 v[40:43], a[36:39], v[52:55], v[40:43]\n\t" \
  "ds_read_b128 a[220:223], v223 offset:20480\n\t" \
  "v_exp_f32 v35, v35\n\t" \
  "v_mfma_f32_16x16x32_bf16 v[40:43], a[40:43], v[56:59], v[40:43]\n\t" \
  "v_exp_f32 v36, v36\n\t" \
  "v_mfma_f32_16x16x32_bf16 v[40:43], a[44:47], v[60:63], v[40:43]\n\t" \
  "v_exp_f32 v37, v37\n\t" \
  "v_mfma_f32_16x16x32_bf16 v[44:47], a[48:51], v[48:51], v[172:175]\n\t" \
  "v_exp_f32 v38, v38\n\t" \
  "v_mfma_f32_16x16x32_bf16 v[44:47], a[52:55], v[52:55], v[44:47]\n\t" \
  "v_permlane16_swap_b32 v121, v123\n\t" \
  "v_mfma_f32_16x16x32_bf16 v[44:47], a[56:59], v[56:59], v[44:47]\n\t" \
  "buffer_load_dwordx4 v227, s[60:63], s96 offen offset:3072 lds\n\t" \
  "v_mfma_f32_16x16x32_bf16 v[44:47], a[60:63], v[60:63], v[44:47]\n\t" \
  "ds_read_b128 a[224:227], v220 offset:24576\n\t" \
  "v_exp_f32 v39, v39\n\t" \
  "v_mfma_f32_16x16x32_bf16 v[64:67], a[0:3], v[80:83], v[172:175]\n\t" \
  "v_permlane16_swap_b32 v116, v118\n\t" \
  "v_mfma_f32_16x16x32_bf16 v[64:67], a[4:7], v[84:87], v[64:67]\n\t" \
  "v_exp_f32 v40, v40\n\t" \
  "v_mfma_f32_16x16x32_bf16 v[64:67], a[8:11], v[88:91], v[64:67]\n\t" \
  "s_add_u32 m0, s38, 147456\n\t" \
  "v_permlane16_swap_b32 v117, v119\n\t" \
  "v_mfma_f32_16x16x32_bf16 v[64:67], a[12:15], v[92:95], v[64:67]\n\t" \
  "s_add_u32 s96, s38, 16384\n\t" \
  "v_exp_f32 v41, v41\n\t" \
  "v_mfma_f32_16x16x32_bf16 v[68:71], a[16:19], v[80:83], v[172:175]\n\t" \
  "ds_read_b128 a[228:231], v221 offset:24576\n\t" \
  "v_cvt_pk_bf16_f32 v128, v0, v1\n\t" \
  "v_mfma_f32_16x16x32_bf16 v[68:71], a[20:23], v[84:87], v[68:71]\n\t" \
  "v_permlane16_swap_b32 v112, v114\n\t" \
  "v_mfma_f32_16x16x32_bf16 v[68:71], a[24:27], v[88:91], v[68:71]\n\t" \
  "buffer_load_dwordx4 v230, s[64:67], s96 offen offset:0 lds\n\t" \
  "v_mfma_f32_16x16x32_bf16 v[68:71], a[28:31], v[92:95], v[68:71]\n\t" \
  "ds_read_b128 a[232:235], v222 offset:24576\n\t" \
  "v_exp_f32 v42, v42\n\t" \
  "v_mfma_f32_16x16x32_bf16 v[72:75], a[32:35], v[80:83], v[172:175]\n\t" \
  "v_permlane16_swap_b32 v113, v115\n\t" \
  "v_mfma_f32_16x16x32_bf16 v[72:75], a[36:39], v[84:87], v[72:75]\n\t" \
  "ds_read_b128 a[236:239], v223 offset:24576\n\t" \
  "v_cvt_pk_bf16_f32 v129, v2, v3\n\t" \
  "v_mfma_f32_16x16x32_bf16 v[72:75], a[40:43], v[88:91], v[72:75]\n\t" \
  "v_exp_f32 v43, v43\n\t" \
  "v_mfma_f32_16x16x32_bf16 v[72:75], a[44:47], v[92:95], v[72:75]\n\t" \
  "v_exp_f32 v44, v44\n\t" \
  "v_mfma_f32_16x16x32_bf16 v[76:79], a[48:51], v[80:83], v[172:175]\n\t" \
  "ds_read_b128 a[240:243], v220 offset:28672\n\t" \
  "v_cvt_pk_bf16_f32 v130, v4, v5\n\t" \
  "v_mfma_f32_16x16x32_bf16 v[76:79], a[52:55], v[84:87], v[76:79]\n\t" \
  "v_exp_f32 v45, v45\n\t" \
  "v_mfma_f32_16x16x32_bf16 v[76:79], a[56:59], v[88:91], v[76:79]\n\t" \
  "v_exp_f32 v46, v46\n\t" \
  "v_mfma_f32_16x16x32_bf16 v[76:79], a[60:63], v[92:95], v[76:79]\n\t" \
  "buffer_load_dwordx4 v231, s[64:67], s96 offen offset:1024 lds\n\t" \
  "v_mfma_f32_16x16x32_bf16 v[96:99], a[0:3], v[112:115], v[172:175]\n\t" \
  "ds_read_b128 a[244:247], v221 offset:28672\n\t" \
  "v_cvt_pk_bf16_f32 v131, v6, v7\n\t" \
  "v_mfma_f32_16x16x32_bf16 v[96:99], a[4:7], v[116:119], v[96:99]\n\t" \
  "ds_read_b128 a[248:251], v222 offset:28672\n\t" \
  "v_cvt_pk_bf16_f32 v132, v8, v9\n\t" \
  "v_mfma_f32_16x16x32_bf16 v[96:99], a[8:11], v[120:123], v[96:99]\n\t" \
  "v_exp_f32 v47, v47\n\t" \
  "v_mfma_f32_16x16x32_bf16 v[96:99], a[12:15], v[124:127], v[96:99]\n\t" \
  "v_exp_f32 v64, v64\n\t" \
  "v_mfma_f32_16x16x32_bf16 v[100:103], a[16:19], v[112:115], v[172:175]\n\t" \
  "ds_read_b128 a[252:255], v223 offset:28672\n\t" \
  "v_cvt_pk_bf16_f32 v133, v10, v11\n\t" \
  "v_mfma_f32_16x16x32_bf16 v[100:103], a[20:23], v[116:119], v[100:103]\n\t" \
  "v_exp_f32 v65, v65\n\t" \
  "v_mfma_f32_16x16x32_bf16 v[100:103], a[24:27], v[120:123], v[100:103]\n\t" \
  "v_exp_f32 v66, v66\n\t" \
  "v_mfma_f32_16x16x32_bf16 v[100:103], a[28:31], v[124:127], v[100:103]\n\t" \
  "buffer_load_dwordx4 v230, s[64:67], s96 offen offset:2048 lds\n\t" \
  "v_mfma_f32_16x16x32_bf16 v[104:107], a[32:35], v[112:115], v[172:175]\n\t" \
  "v_exp_f32 v67, v67\n\t" \
  "v_mfma_f32_16x16x32_bf16 v[104:107], a[36:39], v[116:119], v[104:107]\n\t" \
  "v_exp_f32 v68, v68\n\t" \
  "v_mfma_f32_16x16x32_bf16 v[104:107], a[40:43], v[120:123], v[104:107]\n\t" \
  "v_exp_f32 v69, v69\n\t" \
  "v_mfma_f32_16x16x32_bf16 v[104:107], a[44:47], v[124:127], v[104:107]\n\t" \
  "v_exp_f32 v70, v70\n\t" \
  "v_mfma_f32_16x16x32_bf16 v[108:111], a[48:51], v[112:115], v[172:175]\n\t" \
  "v_cvt_pk_bf16_f32 v134, v12, v13\n\t" \
  "v_cvt_pk_bf16_f32 v135, v14, v15\n\t" \
  "v_mfma_f32_16x16x32_bf16 v[108:111], a[52:55], v[116:119], v[108:111]\n\t" \
  "v_exp_f32 v71, v71\n\t" \
  "v_mfma_f32_16x16x32_bf16 v[108:111], a[56:59], v[120:123], v[108:111]\n\t" \
  "v_exp_f32 v72, v72\n\t" \
  "v_mfma_f32_16x16x32_bf16 v[108:111], a[60:63], v[124:127], v[108:111]\n\t" \
  "s_waitcnt lgkmcnt(0)\n\t" \
  "v_cvt_pk_bf16_f32 v136, v32, v33\n\t" \
  "v_cvt_pk_bf16_f32 v137, v34, v35\n\t" \
  STA("s_memrealtime s[94:95]\n\t") \
  STA("s_memtime s[92:93]\n\t") \
  "v_mfma_f32_16x16x32_bf16 v[0:3], a[192:195], v[16:19], v[172:175]\n\t" \
  "buffer_load_dwordx4 v231, s[64:67], s96 offen offset:3072 lds\n\t" \
  "v_mfma_f32_16x16x32_bf16 v[0:3], a[196:199], v[20:23], v[0:3]\n\t" \
  "s_add_u32 s99, s53, 28672\n\t" \
  "ds_read_b64_tr_b16 a[128:129], v228 offset:0\n\t" \
  "v_cvt_pk_bf16_f32 v138, v36, v37\n\t" \
  "v_mfma_f32_16x16x32_bf16 v[0:3], a[200:203], v[24:27], v[0:3]\n\t" \
  "buffer_load_dword v246, v245, s[72:75], s99 offen\n\t" \
  "v_mfma_f32_16x16x32_bf16 v[0:3], a[204:207], v[28:31], v[0:3]\n\t" \
  "ds_read_b64_tr_b16 a[130:131], v228 offset:4096\n\t" \
  "v_cvt_pk_bf16_f32 v139, v38, v39\n\t" \
  "v_mfma_f32_16x16x32_bf16 v[4:7], a[220:223], v[28:31], v[172:175]\n\t" \
  "ds_read_b64_tr_b16 a[132:133], v228 offset:8192\n\t" \
  "v_cvt_pk_bf16_f32 v140, v40, v41\n\t" \
  "v_mfma_f32_16x16x32_bf16 v[4:7], a[216:219], v[24:27], v[4:7]\n\t" \
  "v_exp_f32 v73, v73\n\t" \
  "v_mfma_f32_16x16x32_bf16 v[4:7], a[212:215], v[20:23], v[4:7]\n\t" \
  "ds_read_b64_tr_b16 a[134:135], v228 offset:12288\n\t" \
  "v_cvt_pk_bf16_f32 v141, v42, v43\n\t" \
  "v_mfma_f32_16x16x32_bf16 v[4:7], a[208:211], v[16:19], v[4:7]\n\t" \
  "v_exp_f32 v74, v74\n\t" \
  "v_mfma_f32_16x16x32_bf16 v[8:11], a[224:227], v[16:19], v[172:175]\n\t" \
  "ds_read_b64_tr_b16 a[136:137], v229 offset:0\n\t" \
  "v_cvt_pk_bf16_f32 v142, v44, v45\n\t" \
  "v_mfma_f32_16x16x32_bf16 v[8:11], a[228:231], v[20:23], v[8:11]\n\t" \
  "ds_read_b64_tr_b16 a[138:139], v229 offset:4096\n\t" \
  "v_cvt_pk_bf16_f32 v143, v46, v47\n\t" \
  "v_mfma_f32_16x16x32_bf16 v[8:11], a[232:235], v[24:27], v[8:11]\n\t" \
  "v_exp_f32 v75, v75\n\t" \
  "v_mfma_f32_16x16x32_bf16 v[8:11], a[236:239], v[28:31], v[8:11]\n\t" \
  "ds_read_b64_tr_b16 a[140:141], v229 offset:8192\n\t" \
  "v_lshl_add_u32 v214, v233, 14, s41\n\t" \
  "v_mfma_f32_16x16x32_bf16 v[12:15], a[252:255], v[28:31], v[172:175]\n\t" \
  STA("v_writelane_b32 v255, s94, 26\n\t") \
  STA("v_writelane_b32 v255, s92, 27\n\t") \
  "v_exp_f32 v76, v76\n\t" \
  "v_mfma_f32_16x16x32_bf16 v[12:15], a[248:251], v[24:27], v[12:15]\n\t" \
  "ds_read_b64_tr_b16 a[142:143], v229 offset:12288\n\t" \
  "v_min_u32 v214, .Lfz_z-.Lat_t0-4, v214\n\t" \
  "v_mfma_f32_16x16x32_bf16 v[12:15], a[244:247], v[20:23], v[12:15]\n\t" \
  "s_getpc_b64 s[84:85]\n\t" \
  ".Lat_t0:\n\t" \
  "v_exp_f32 v77, v77\n\t" \
  "v_mfma_f32_16x16x32_bf16 v[12:15], a[240:243], v[16:19], v[12:15]\n\t" \
  "global_load_dword v246, v214, s[84:85]\n\t" \
  "v_mfma_f32_16x16x32_bf16 v[32:35], a[192:195], v[48:51], v[172:175]\n\t" \
  "ds_read_b64_tr_b16 a[144:145], v228 offset:256\n\t" \
  "v_cvt_pk_bf16_f32 v144, v64, v65\n\t" \
  "v_mfma_f32_16x16x32_bf16 v[32:35], a[196:199], v[52:55], v[32:35]\n\t" \
  "ds_read_b64_tr_b16 a[146:147], v228 offset:4352\n\t" \
  "v_cvt_pk_bf16_f32 v145, v66, v67\n\t" \
  "v_mfma_f32_16x16x32_bf16 v[32:35], a[200:203], v[56:59], v[32:35]\n\t" \
  "ds_read_b64_tr_b16 a[148:149], v228 offset:8448\n\t" \
  "v_exp_f32 v78, v78\n\t" \
  "v_mfma_f32_16x16x32_bf16 v[32:35], a[204:207], v[60:63], v[32:35]\n\t" \
  "v_exp_f32 v79, v79\n\t" \
  "v_mfma_f32_16x16x32_bf16 v[36:39], a[220:223], v[60:63], v[172:175]\n\t" \
  "ds_read_b64_tr_b16 a[150:151], v228 offset:12544\n\t" \
  "v_cvt_pk_bf16_f32 v146, v68, v69\n\t" \
  "v_mfma_f32_16x16x32_bf16 v[36:39], a[216:219], v[56:59], v[36:39]\n\t" \
  "v_exp_f32 v96, v96\n\t" \
  "v_mfma_f32_16x16x32_bf16 v[36:39], a[212:215], v[52:55], v[36:39]\n\t" \
  "ds_read_b64_tr_b16 a[152:153], v229 offset:256\n\t" \
  "v_cvt_pk_bf16_f32 v147, v70, v71\n\t" \
  "v_mfma_f32_16x16x32_bf16 v[36:39], a[208:211], v[48:51], v[36:39]\n\t" \
  "v_exp_f32 v97, v97\n\t" \
  "v_mfma_f32_16x16x32_bf16 v[40:43], a[224:227], v[48:51], v[172:175]\n\t" \
  "ds_read_b64_tr_b16 a[154:155], v229 offset:4352\n\t" \
  "v_cvt_pk_bf16_f32 v148, v72, v73\n\t" \
  "v_mfma_f32_16x16x32_bf16 v[40:43], a[228:231], v[52:55], v[40:43]\n\t" \
  "v_exp_f32 v98, v98\n\t" \
  "v_mfma_f32_16x16x32_bf16 v[40:43], a[232:235], v[56:59], v[40:43]\n\t" \
  "ds_read_b64_tr_b16 a[156:157], v229 offset:8448\n\t" \
  "v_cvt_pk_bf16_f32 v149, v74, v75\n\t" \
  "v_mfma_f32_16x16x32_bf16 v[40:43], a[236:239], v[60:63], v[40:43]\n\t" \
  "v_exp_f32 v99, v99\n\t" \
  "v_mfma_f32_16x16x32_bf16 v[44:47], a[252:255], v[60:63], v[172:175]\n\t" \
  "ds_read_b64_tr_b16 a[158:159], v229 offset:12544\n\t" \
  "v_cvt_pk_bf16_f32 v150, v76, v77\n\t" \
  "v_mfma_f32_16x16x32_bf16 v[44:47], a[248:251], v[56:59], v[44:47]\n\t" \
  "v_exp_f32 v100, v100\n\t" \
  "v_mfma_f32_16x16x32_bf16 v[44:47], a[244:247], v[52:55], v[44:47]\n\t" \
  "ds_read_b64_tr_b16 a[160:161], v228 offset:512\n\t" \
  "v_cvt_pk_bf16_f32 v151, v78, v79\n\t" \
  "v_mfma_f32_16x16x32_bf16 v[44:47], a[240:243], v[48:51], v[44:47]\n\t" \
  "ds_read_b64_tr_b16 a[162:163], v228 offset:4608\n\t" \
  "v_cvt_pk_bf16_f32 v152, v96, v97\n\t" \
  "v_mfma_f32_16x16x32_bf16 v[64:67], a[192:195], v[80:83], v[172:175]\n\t" \
  "v_exp_f32 v101, v101\n\t" \
  "v_mfma_f32_16x16x32_bf16 v[64:67], a[196:199], v[84:87], v[64:67]\n\t" \
  "ds_read_b64_tr_b16 a[164:165], v228 offset:8704\n\t" \
  "v_cvt_pk_bf16_f32 v153, v98, v99\n\t" \
  "v_mfma_f32_16x16x32_bf16 v[64:67], a[200:203], v[88:91], v[64:67]\n\t" \
  "v_exp_f32 v102, v102\n\t" \
  "v_mfma_f32_16x16x32_bf16 v[64:67], a[204:207], v[92:95], v[64:67]\n\t" \
  "ds_read_b64_tr_b16 a[166:167], v228 offset:12800\n\t" \
  "v_cvt_pk_bf16_f32 v154, v100, v101\n\t" \
  "v_mfma_f32_16x16x32_bf16 v[68:71], a[220:223], v[92:95], v[172:175]\n\t" \
  "v_exp_f32 v103, v103\n\t" \
  "v_mfma_f32_16x16x32_bf16 v[68:71], a[216:219], v[88:91], v[68:71]\n\t" \
  "ds_read_b64_tr_b16 a[168:169], v229 offset:512\n\t" \
  "v_cvt_pk_bf16_f32 v155, v102, v103\n\t" \
  "v_mfma_f32_16x16x32_bf16 v[68:71], a[212:215], v[84:87], v[68:71]\n\t" \
  ".Lhs6:\n\ts_cbranch_execz .Lhs6n\n\t" \
  "v_exp_f32 v104, v104\n\t" \
  "v_mfma_f32_16x16x32_bf16 v[68:71], a[208:211], v[80:83], v[68:71]\n\t" \
  "v_exp_f32 v105, v105\n\t" \
  "v_mfma_f32_16x16x32_bf16 v[72:75], a[224:227], v[80:83], v[172:175]\n\t" \
  "ds_read_b64_tr_b16 a[170:171], v229 offset:4608\n\t" \
  "v_cvt_pk_bf16_f32 v156, v104, v105\n\t" \
  "v_mfma_f32_16x16x32_bf16 v[72:75], a[228:231], v[84:87], v[72:75]\n\t" \
  "v_exp_f32 v106, v106\n\t" \
  "v_mfma_f32_16x16x32_bf16 v[72:75], a[232:235], v[88:91], v[72:75]\n\t" \
  "v_exp_f32 v107, v107\n\t" \
  "v_mfma_f32_16x16x32_bf16 v[72:75], a[236:239], v[92:95], v[72:75]\n\t" \
  "ds_read_b64_tr_b16 a[172:173], v229 offset:8704\n\t" \
  "v_cvt_pk_bf16_f32 v157, v106, v107\n\t" \
  "v_mfma_f32_16x16x32_bf16 v[76:79], a[252:255], v[92:95], v[172:175]\n\t" \
  "v_exp_f32 v108, v108\n\t" \
  "v_mfma_f32_16x16x32_bf16 v[76:79], a[248:251], v[88:91], v[76:79]\n\t" \
  "v_exp_f32 v109, v109\n\t" \
  "v_mfma_f32_16x16x32_bf16 v[76:79], a[244:247], v[84:87], v[76:79]\n\t" \
  "ds_read_b64_tr_b16 a[174:175], v229 offset:12800\n\t" \
  "v_cvt_pk_bf16_f32 v158, v108, v109\n\t" \
  "v_mfma_f32_16x16x32_bf16 v[76:79], a[240:243], v[80:83], v[76:79]\n\t" \
  "v_exp_f32 v110, v110\n\t" \
  "v_mfma_f32_16x16x32_bf16 v[96:99], a[192:195], v[112:115], v[172:175]\n\t" \
  "v_exp_f32 v111, v111\n\t" \
  "v_mfma_f32_16x16x32_bf16 v[96:99], a[196:199], v[116:119], v[96:99]\n\t" \
  "s_add_u32 s99, s53, 43008\n\t" \
  "ds_read_b64_tr_b16 a[176:177], v228 offset:768\n\t" \
  "v_cvt_pk_bf16_f32 v159, v110, v111\n\t" \
  "v_mfma_f32_16x16x32_bf16 v[96:99], a[200:203], v[120:123], v[96:99]\n\t" \
  "ds_read_b64_tr_b16 a[178:179], v228 offset:4864\n\t" \
  "v_mfma_f32_16x16x32_bf16 v[96:99], a[204:207], v[124:127], v[96:99]\n\t" \
  "ds_read_b64_tr_b16 a[180:181], v228 offset:8960\n\t" \
  "v_mfma_f32_16x16x32_bf16 v[100:103], a[220:223], v[124:127], v[172:175]\n\t" \
  "buffer_load_dword v246, v245, s[72:75], s99 offen\n\t" \
  "v_mfma_f32_16x16x32_bf16 v[100:103], a[216:219], v[120:123], v[100:103]\n\t" \
  "ds_read_b64_tr_b16 a[182:183], v228 offset:13056\n\t" \
  "v_mfma_f32_16x16x32_bf16 v[100:103], a[212:215], v[116:119], v[100:103]\n\t" \
  "ds_read_b64_tr_b16 a[184:185], v229 offset:768\n\t" \
  "v_mfma_f32_16x16x32_bf16 v[100:103], a[208:211], v[112:115], v[100:103]\n\t" \
  "ds_read_b64_tr_b16 a[186:187], v229 offset:4864\n\t" \
  "v_mfma_f32_16x16x32_bf16 v[104:107], a[224:227], v[112:115], v[172:175]\n\t" \
  "ds_read_b64_tr_b16 a[188:189], v229 offset:8960\n\t" \
  "v_mfma_f32_16x16x32_bf16 v[104:107], a[228:231], v[116:119], v[104:107]\n\t" \
  "ds_read_b64_tr_b16 a[190:191], v229 offset:13056\n\t" \
  "v_mfma_f32_16x16x32_bf16 v[104:107], a[232:235], v[120:123], v[104:107]\n\t" \
  "v_exp_f32 v0, v0\n\t" \
  "v_mfma_f32_16x16x32_bf16 v[104:107], a[236:239], v[124:127], v[104:107]\n\t" \
  "v_exp_f32 v1, v1\n\t" \
  "v_mfma_f32_16x16x32_bf16 v[108:111], a[252:255], v[124:127], v[172:175]\n\t" \
  "v_exp_f32 v2, v2\n\t" \
  "v_mfma_f32_16x16x32_bf16 v[108:111], a[248:251], v[120:123], v[108:111]\n\t" \
  "v_exp_f32 v3, v3\n\t" \
  "v_mfma_f32_16x16x32_bf16 v[108:111], a[244:247], v[116:119], v[108:111]\n\t" \
  "s_waitcnt vmcnt(3) lgkmcnt(0)\n\t" \
  "v_exp_f32 v4, v4\n\t" \
  "v_mfma_f32_16x16x32_bf16 v[108:111], a[240:243], v[112:115], v[108:111]\n\t" \
  "s_barrier\n\t" \
  STA("s_memrealtime s[94:95]\n\t") \
  STA("s_memtime s[92:93]\n\t") \
  "v_mfma_f32_16x16x32_bf16 a[0:3], a[128:131], v[128:131], 0\n\t" \
  "v_exp_f32 v5, v5\n\t" \
  "v_mfma_f32_16x16x32_bf16 a[0:3], a[132:135], v[132:135], a[0:3]\n\t" \
  "v_exp_f32 v6, v6\n\t" \
  "v_mfma_f32_16x16x32_bf16 a[4:7], a[136:139], v[128:131], 0\n\t" \
  "v_exp_f32 v7, v7\n\t" \
  "v_mfma_f32_16x16x32_bf16 a[4:7], a[140:143], v[132:135], a[4:7]\n\t" \
  "v_exp_f32 v8, v8\n\t" \
  "v_mfma_f32_16x16x32_bf16 a[8:11], a[144:147], v[128:131], 0\n\t" \
  "v_exp_f32 v9, v9\n\t" \
  "v_mfma_f32_16x16x32_bf16 a[8:11], a[148:151], v[132:135], a[8:11]\n\t" \
  "v_exp_f32 v10, v10\n\t" \
  "v_mfma_f32_16x16x32_bf16 a[12:15], a[152:155], v[128:131], 0\n\t" \
  "v_exp_f32 v11, v11\n\t" \
  "v_mfma_f32_16x16x32_bf16 a[12:15], a[156:159], v[132:135], a[12:15]\n\t" \
  "v_exp_f32 v12, v12\n\t" \
  "v_mfma_f32_16x16x32_bf16 a[16:19], a[160:163], v[128:131], 0\n\t" \
  "v_exp_f32 v13, v13\n\t" \
  "v_mfma_f32_16x16x32_bf16 a[16:19], a[164:167], v[132:135], a[16:19]\n\t" \
  "v_exp_f32 v14, v14\n\t" \
  "v_mfma_f32_16x16x32_bf16 a[20:23], a[168:171], v[128:131], 0\n\t" \
  "v_exp_f32 v15, v15\n\t" \
  "v_mfma_f32_16x16x32_bf16 a[20:23], a[172:175], v[132:135], a[20:23]\n\t" \
  "v_exp_f32 v32, v32\n\t" \
  "v_mfma_f32_16x16x32_bf16 a[24:27], a[176:179], v[128:131], 0\n\t" \
  STA("v_writelane_b32 v255, s94, 28\n\t") \
  STA("v_writelane_b32 v255, s92, 29\n\t") \
  "v_exp_f32 v33, v33\n\t" \
  "v_mfma_f32_16x16x32_bf16 a[24:27], a[180:183], v[132:135], a[24:27]\n\t" \
  "v_exp_f32 v34, v34\n\t" \
  "v_mfma_f32_16x16x32_bf16 a[28:31], a[184:187], v[128:131], 0\n\t" \
  "v_exp_f32 v35, v35\n\t" \
  "v_mfma_f32_16x16x32_bf16 a[28:31], a[188:191], v[132:135], a[28:31]\n\t" \
  "v_exp_f32 v36, v36\n\t" \
  "v_smfmac_f32_16x16x64_bf16 v[160:163], v[216:219], v[128:135], v208\n\t" \
  "s_add_u32 m0, s38, 65536\n\t" \
  "ds_read_b128 a[192:195], v220 offset:0\n\t" \
  "v_cvt_pk_bf16_f32 v128, v0, v1\n\t" \
  "v_mfma_f32_16x16x32_bf16 a[32:35], a[128:131], v[136:139], 0\n\t" \
  "s_add_u32 s96, s38, 49152\n\t" \
  "v_exp_f32 v37, v37\n\t" \
  "v_mfma_f32_16x16x32_bf16 a[32:35], a[132:135], v[140:143], a[32:35]\n\t" \
  "ds_read_b128 a[196:199], v221 offset:0\n\t" \
  "v_cvt_pk_bf16_f32 v129, v2, v3\n\t" \
  "v_mfma_f32_16x16x32_bf16 a[36:39], a[136:139], v[136:139], 0\n\t" \
  "v_exp_f32 v38, v38\n\t" \
  "v_mfma_f32_16x16x32_bf16 a[36:39], a[140:143], v[140:143], a[36:39]\n\t" \
  "buffer_load_dwordx4 v224, s[60:63], s96 offen offset:0 lds\n\t" \
  "v_mfma_f32_16x16x32_bf16 a[40:43], a[144:147], v[136:139], 0\n\t" \
  "ds_read_b128 a[200:203], v222 offset:0\n\t" \
  "v_cvt_pk_bf16_f32 v130, v4, v5\n\t" \
  "v_mfma_f32_16x16x32_bf16 a[40:43], a[148:151], v[140:143], a[40:43]\n\t" \
  "v_exp_f32 v39, v39\n\t" \
  "v_mfma_f32_16x16x32_bf16 a[44:47], a[152:155], v[136:139], 0\n\t" \
  "ds_read_b128 a[204:207], v223 offset:0\n\t" \
  "v_cvt_pk_bf16_f32 v131, v6, v7\n\t" \
  "v_mfma_f32_16x16x32_bf16 a[44:47], a[156:159], v[140:143], a[44:47]\n\t" \
  "v_exp_f32 v40, v40\n\t" \
  "v_mfma_f32_16x16x32_bf16 a[48:51], a[160:163], v[136:139], 0\n\t" \
  "ds_read_b128 a[208:211], v220 offset:4096\n\t" \
  "v_cvt_pk_bf16_f32 v132, v8, v9\n\t" \
  "v_mfma_f32_16x16x32_bf16 a[48:51], a[164:167], v[140:143], a[48:51]\n\t" \
  "buffer_load_dwordx4 v225, s[60:63], s96 offen offset:1024 lds\n\t" \
  "v_mfma_f32_16x16x32_bf16 a[52:55], a[168:171], v[136:139], 0\n\t" \
  "v_exp_f32 v41, v41\n\t" \
  "v_mfma_f32_16x16x32_bf16 a[52:55], a[172:175], v[140:143], a[52:55]\n\t" \
  "ds_read_b128 a[212:215], v221 offset:4096\n\t" \
  "v_cvt_pk_bf16_f32 v133, v10, v11\n\t" \
  "v_mfma_f32_16x16x32_bf16 a[56:59], a[176:179], v[136:139], 0\n\t" \
  "v_exp_f32 v42, v42\n\t" \
  "v_mfma_f32_16x16x32_bf16 a[56:59], a[180:183], v[140:143], a[56:59]\n\t" \
  "v_exp_f32 v43, v43\n\t" \
  "v_mfma_f32_16x16x32_bf16 a[60:63], a[184:187], v[136:139], 0\n\t" \
  "buffer_load_dwordx4 v226, s[60:63], s96 offen offset:2048 lds\n\t" \
  "v_mfma_f32_16x16x32_bf16 a[60:63], a[188:191], v[140:143], a[60:63]\n\t" \
  "ds_read_b128 a[216:219], v222 offset:4096\n\t" \
  "v_cvt_pk_bf16_f32 v134, v12, v13\n\t" \
  "v_smfmac_f32_16x16x64_bf16 v[162:165], v[216:219], v[136:143], v208\n\t" \
  "ds_read_b128 a[220:223], v223 offset:4096\n\t" \
  "v_cvt_pk_bf16_f32 v135, v14, v15\n\t" \
  "v_mfma_f32_16x16x32_bf16 a[64:67], a[128:131], v[144:147], 0\n\t" \
  "v_exp_f32 v44, v44\n\t" \
  "v_mfma_f32_16x16x32_bf16 a[64:67], a[132:135], v[148:151], a[64:67]\n\t" \
  "v_exp_f32 v45, v45\n\t" \
  "v_mfma_f32_16x16x32_bf16 a[68:71], a[136:139], v[144:147], 0\n\t" \
  "ds_read_b128 a[224:227], v220 offset:8192\n\t" \
  "v_cvt_pk_bf16_f32 v136, v32, v33\n\t" \
  "v_mfma_f32_16x16x32_bf16 a[68:71], a[140:143], v[148:151], a[68:71]\n\t" \
  "buffer_load_dwordx4 v227, s[60:63], s96 offen offset:3072 lds\n\t" \
  "v_mfma_f32_16x16x32_bf16 a[72:75], a[144:147], v[144:147], 0\n\t" \
  "s_add_u32 m0, s38, 81920\n\t" \
  "v_exp_f32 v46, v46\n\t" \
  "v_mfma_f32_16x16x32_bf16 a[72:75], a[148:151], v[148:151], a[72:75]\n\t" \
  "s_add_u32 s96, s38, 32768\n\t" \
  "ds_read_b128 a[228:231], v221 offset:8192\n\t" \
  "v_cvt_pk_bf16_f32 v137, v34, v35\n\t" \
  "v_mfma_f32_16x16x32_bf16 a[76:79], a[152:155], v[144:147], 0\n\t" \
  "v_exp_f32 v47, v47\n\t" \
  "v_mfma_f32_16x16x32_bf16 a[76:79], a[156:159], v[148:151], a[76:79]\n\t" \
  "v_exp_f32 v64, v64\n\t" \
  "v_mfma_f32_16x16x32_bf16 a[80:83], a[160:163], v[144:147], 0\n\t" \
  "buffer_load_dwordx4 v230, s[64:67], s96 offen offset:0 lds\n\t" \
  "v_mfma_f32_16x16x32_bf16 a[80:83], a[164:167], v[148:151], a[80:83]\n\t" \
  "ds_read_b128 a[232:235], v222 offset:8192\n\t" \
  "v_cvt_pk_bf16_f32 v138, v36, v37\n\t" \
  "v_mfma_f32_16x16x32_bf16 a[84:87], a[168:171], v[144:147], 0\n\t" \
  "v_exp_f32 v65, v65\n\t" \
  "v_mfma_f32_16x16x32_bf16 a[84:87], a[172:175], v[148:151], a[84:87]\n\t" \
  "ds_read_b128 a[236:239], v223 offset:8192\n\t" \
  "v_cvt_pk_bf16_f32 v139, v38, v39\n\t" \
  "v_mfma_f32_16x16x32_bf16 a[88:91], a[176:179], v[144:147], 0\n\t" \
  "v_exp_f32 v66, v66\n\t" \
  "v_mfma_f32_16x16x32_bf16 a[88:91], a[180:183], v[148:151], a[88:91]\n\t" \
  "ds_read_b128 a[240:243], v220 offset:12288\n\t" \
  "v_cvt_pk_bf16_f32 v140, v40, v41\n\t" \
  "v_mfma_f32_16x16x32_bf16 a[92:95], a[184:187], v[144:147], 0\n\t" \
  "buffer_load_dwordx4 v231, s[64:67], s96 offen offset:1024 lds\n\t" \
  "v_mfma_f32_16x16x32_bf16 a[92:95], a[188:191], v[148:151], a[92:95]\n\t" \
  "v_exp_f32 v67, v67\n\t" \
  "v_smfmac_f32_16x16x64_bf16 v[164:167], v[216:219], v[144:151], v208\n\t" \
  "ds_read_b128 a[244:247], v221 offset:12288\n\t" \
  "v_cvt_pk_bf16_f32 v141, v42, v43\n\t" \
  "v_mfma_f32_16x16x32_bf16 a[96:99], a[128:131], v[152:155], 0\n\t" \
  "v_exp_f32 v68, v68\n\t" \
  "v_mfma_f32_16x16x32_bf16 a[96:99], a[132:135], v[156:159], a[96:99]\n\t" \
  "v_exp_f32 v69, v69\n\t" \
  "v_mfma_f32_16x16x32_bf16 a[100:103], a[136:139], v[152:155], 0\n\t" \
  "buffer_load_dwordx4 v230, s[64:67], s96 offen offset:2048 lds\n\t" \
  "v_mfma_f32_16x16x32_bf16 a[100:103], a[140:143], v[156:159], a[100:103]\n\t" \
  "ds_read_b128 a[248:251], v222 offset:12288\n\t" \
  "v_cvt_pk_bf16_f32 v142, v44, v45\n\t" \
  "v_mfma_f32_16x16x32_bf16 a[104:107], a[144:147], v[152:155], 0\n\t" \
  "v_exp_f32 v70, v70\n\t" \
  "v_mfma_f32_16x16x32_bf16 a[104:107], a[148:151], v[156:159], a[104:107]\n\t" \
  "ds_read_b128 a[252:255], v223 offset:12288\n\t" \
  "v_cvt_pk_bf16_f32 v143, v46, v47\n\t" \
  "v_mfma_f32_16x16x32_bf16 a[108:111], a[152:155], v[152:155], 0\n\t" \
  "v_exp_f32 v71, v71\n\t" \
  "v_mfma_f32_16x16x32_bf16 a[108:111], a[156:159], v[156:159], a[108:111]\n\t" \
  "v_exp_f32 v72, v72\n\t" \
  "v_mfma_f32_16x16x32_bf16 a[112:115], a[160:163], v[152:155], 0\n\t" \
  "buffer_load_dwordx4 v231, s[64:67], s96 offen offset:3072 lds\n\t" \
  "v_mfma_f32_16x16x32_bf16 a[112:115], a[164:167], v[156:159], a[112:115]\n\t" \
  "v_exp_f32 v73, v73\n\t" \
  "v_mfma_f32_16x16x32_bf16 a[116:119], a[168:171], v[152:155], 0\n\t" \
  "v_exp_f32 v74, v74\n\t" \
  "v_mfma_f32_16x16x32_bf16 a[116:119], a[172:175], v[156:159], a[116:119]\n\t" \
  "v_exp_f32 v75, v75\n\t" \
  "v_mfma_f32_16x16x32_bf16 a[120:123], a[176:179], v[152:155], 0\n\t" \
  "v_exp_f32 v76, v76\n\t" \
  "v_mfma_f32_16x16x32_bf16 a[120:123], a[180:183], v[156:159], a[120:123]\n\t" \
  "v_exp_f32 v77, v77\n\t" \
  "v_mfma_f32_16x16x32_bf16 a[124:127], a[184:187], v[152:155], 0\n\t" \
  "v_exp_f32 v78, v78\n\t" \
  "v_mfma_f32_16x16x32_bf16 a[124:127], a[188:191], v[156:159], a[124:127]\n\t" \
  "v_exp_f32 v79, v79\n\t" \
  "v_smfmac_f32_16x16x64_bf16 v[166:169], v[216:219], v[152:159], v208\n\t" \
  "s_waitcnt lgkmcnt(12)\n\t" \
  "v_cvt_pk_bf16_f32 v144, v64, v65\n\t" \
  STA("s_memrealtime s[94:95]\n\t") \
  STA("s_memtime s[92:93]\n\t") \
  "v_mfma_f32_16x16x32_bf16 v[0:3], a[192:195], v[16:19], v[172:175]\n\t" \
  "ds_read_b64_tr_b16 a[128:129], v210 offset:0\n\t" \
  "v_cvt_pk_bf16_f32 v145, v66, v67\n\t" \
  "v_mfma_f32_16x16x32_bf16 v[0:3], a[196:199], v[20:23], v[0:3]\n\t" \
  "v_exp_f32 v96, v96\n\t" \
  "v_mfma_f32_16x16x32_bf16 v[0:3], a[200:203], v[24:27], v[0:3]\n\t" \
  "s_add_u32 m0, s38, 16384\n\t" \
  "ds_read_b64_tr_b16 a[130:131], v210 offset:4096\n\t" \
  "v_cvt_pk_bf16_f32 v146, v68, v69\n\t" \
  "v_mfma_f32_16x16x32_bf16 v[0:3], a[204:207], v[28:31], v[0:3]\n\t" \
  "s_waitcnt lgkmcnt(10)\n\t" \
  "v_exp_f32 v97, v97\n\t" \
  "v_mfma_f32_16x16x32_bf16 v[4:7], a[220:223], v[28:31], v[172:175]\n\t" \
  "s_mov_b32 s96, 0\n\t" \
  "ds_read_b64_tr_b16 a[132:133], v210 offset:8192\n\t" \
  "v_cvt_pk_bf16_f32 v147, v70, v71\n\t" \
  "v_mfma_f32_16x16x32_bf16 v[4:7], a[216:219], v[24:27], v[4:7]\n\t" \
  "v_exp_f32 v98, v98\n\t" \
  "v_mfma_f32_16x16x32_bf16 v[4:7], a[212:215], v[20:23], v[4:7]\n\t" \
  "buffer_load_dwordx4 v232, s[68:71], s96 offen offset:0 lds\n\t" \
  "v_mfma_f32_16x16x32_bf16 v[4:7], a[208:211], v[16:19], v[4:7]\n\t" \
  "s_waitcnt lgkmcnt(7)\n\t" \
  "ds_read_b64_tr_b16 a[134:135], v210 offset:12288\n\t" \
  "v_cvt_pk_bf16_f32 v148, v72, v73\n\t" \
  "v_mfma_f32_16x16x32_bf16 v[8:11], a[224:227], v[16:19], v[172:175]\n\t" \
  "ds_read_b64_tr_b16 a[136:137], v211 offset:0\n\t" \
  "v_cvt_pk_bf16_f32 v149, v74, v75\n\t" \
  "v_mfma_f32_16x16x32_bf16 v[8:11], a[228:231], v[20:23], v[8:11]\n\t" \
  "ds_read_b64_tr_b16 a[138:139], v211 offset:4096\n\t" \
  "v_cvt_pk_bf16_f32 v150, v76, v77\n\t" \
  "v_mfma_f32_16x16x32_bf16 v[8:11], a[232:235], v[24:27], v[8:11]\n\t" \
  "v_exp_f32 v99, v99\n\t" \
  "v_mfma_f32_16x16x32_bf16 v[8:11], a[236:239], v[28:31], v[8:11]\n\t" \
  "s_waitcnt lgkmcnt(6)\n\t" \
  "ds_read_b64_tr_b16 a[140:141], v211 offset:8192\n\t" \
  "v_cvt_pk_bf16_f32 v151, v78, v79\n\t" \
  "v_mfma_f32_16x16x32_bf16 v[12:15], a[252:255], v[28:31], v[172:175]\n\t" \
  STA("v_writelane_b32 v255, s94, 30\n\t") \
  STA("v_writelane_b32 v255, s92, 31\n\t") \
  "v_exp_f32 v100, v100\n\t" \
  "v_mfma_f32_16x16x32_bf16 v[12:15], a[248:251], v[24:27], v[12:15]\n\t" \
  "ds_read_b64_tr_b16 a[142:143], v211 offset:12288\n\t" \
  "v_cvt_pk_bf16_f32 v152, v96, v97\n\t" \
  "v_mfma_f32_16x16x32_bf16 v[12:15], a[244:247], v[20:23], v[12:15]\n\t" \
  "ds_read_b64_tr_b16 a[144:145], v210 offset:256\n\t" \
  "v_cvt_pk_bf16_f32 v153, v98, v99\n\t" \
  "v_mfma_f32_16x16x32_bf16 v[12:15], a[240:243], v[16:19], v[12:15]\n\t" \
  "v_exp_f32 v101, v101\n\t" \
  "v_mfma_f32_16x16x32_bf16 v[32:35], a[192:195], v[48:51], v[172:175]\n\t" \
  "s_mov_b32 s96, 6144\n\t" \
  "ds_read_b64_tr_b16 a[146:147], v210 offset:4352\n\t" \
  "v_cvt_pk_bf16_f32 v154, v100, v101\n\t" \
  "v_mfma_f32_16x16x32_bf16 v[32:35], a[196:199], v[52:55], v[32:35]\n\t" \
  "v_exp_f32 v102, v102\n\t" \
  "v_mfma_f32_16x16x32_bf16 v[32:35], a[200:203], v[56:59], v[32:35]\n\t" \
  "v_exp_f32 v103, v103\n\t" \
  "v_mfma_f32_16x16x32_bf16 v[32:35], a[204:207], v[60:63], v[32:35]\n\t" \
  "ds_read_b64_tr_b16 a[148:149], v210 offset:8448\n\t" \
  "v_cvt_pk_bf16_f32 v155, v102, v103\n\t" \
  "v_mfma_f32_16x16x32_bf16 v[36:39], a[220:223], v[60:63], v[172:175]\n\t" \
  "buffer_load_dwordx4 v232, s[68:71], s96 offen offset:1024 lds\n\t" \
  "v_mfma_f32_16x16x32_bf16 v[36:39], a[216:219], v[56:59], v[36:39]\n\t" \
  "v_exp_f32 v104, v104\n\t" \
  "v_mfma_f32_16x16x32_bf16 v[36:39], a[212:215], v[52:55], v[36:39]\n\t" \
  "v_exp_f32 v105, v105\n\t" \
  "v_mfma_f32_16x16x32_bf16 v[36:39], a[208:211], v[48:51], v[36:39]\n\t" \
  "ds_read_b64_tr_b16 a[150:151], v210 offset:12544\n\t" \
  "v_cvt_pk_bf16_f32 v156, v104, v105\n\t" \
  "v_mfma_f32_16x16x32_bf16 v[40:43], a[224:227], v[48:51], v[172:175]\n\t" \
  "v_exp_f32 v106, v106\n\t" \
  "v_mfma_f32_16x16x32_bf16 v[40:43], a[228:231], v[52:55], v[40:43]\n\t" \
  "v_exp_f32 v107, v107\n\t" \
  "v_mfma_f32_16x16x32_bf16 v[40:43], a[232:235], v[56:59], v[40:43]\n\t" \
  "ds_read_b64_tr_b16 a[152:153], v211 offset:256\n\t" \
  "v_cvt_pk_bf16_f32 v157, v106, v107\n\t" \
  "v_mfma_f32_16x16x32_bf16 v[40:43], a[236:239], v[60:63], v[40:43]\n\t" \
  "v_exp_f32 v108, v108\n\t" \
  "v_mfma_f32_16x16x32_bf16 v[44:47], a[252:255], v[60:63], v[172:175]\n\t" \
  "ds_read_b64_tr_b16 a[154:155], v211 offset:4352\n\t" \
  "v_mfma_f32_16x16x32_bf16 v[44:47], a[248:251], v[56:59], v[44:47]\n\t" \
  "v_exp_f32 v109, v109\n\t" \
  "v_mfma_f32_16x16x32_bf16 v[44:47], a[244:247], v[52:55], v[44:47]\n\t" \
  "s_mov_b32 s96, 12288\n\t" \
  "ds_read_b64_tr_b16 a[156:157], v211 offset:8448\n\t" \
  "v_cvt_pk_bf16_f32 v158, v108, v109\n\t" \
  "v_mfma_f32_16x16x32_bf16 v[44:47], a[240:243], v[48:51], v[44:47]\n\t" \
  "v_exp_f32 v110, v110\n\t" \
  "v_mfma_f32_16x16x32_bf16 v[64:67], a[192:195], v[80:83], v[172:175]\n\t" \
  "ds_read_b64_tr_b16 a[158:159], v211 offset:12544\n\t" \
  "v_mfma_f32_16x16x32_bf16 v[64:67], a[196:199], v[84:87], v[64:67]\n\t" \
  "v_exp_f32 v111, v111\n\t" \
  "v_mfma_f32_16x16x32_bf16 v[64:67], a[200:203], v[88:91], v[64:67]\n\t" \
  "buffer_load_dwordx4 v232, s[68:71], s96 offen offset:2048 lds\n\t" \
  "v_mfma_f32_16x16x32_bf16 v[64:67], a[204:207], v[92:95], v[64:67]\n\t" \
  "ds_read_b64_tr_b16 a[160:161], v210 offset:512\n\t" \
  "v_cvt_pk_bf16_f32 v159, v110, v111\n\t" \
  "v_mfma_f32_16x16x32_bf16 v[68:71], a[220:223], v[92:95], v[172:175]\n\t" \
  "ds_read_b64_tr_b16 a[162:163], v210 offset:4608\n\t" \
  "v_mfma_f32_16x16x32_bf16 v[68:71], a[216:219], v[88:91], v[68:71]\n\t" \
  "ds_read_b64_tr_b16 a[164:165], v210 offset:8704\n\t" \
  "v_mfma_f32_16x16x32_bf16 v[68:71], a[212:215], v[84:87], v[68:71]\n\t" \
  "ds_read_b64_tr_b16 a[166:167], v210 offset:12800\n\t" \
  "v_mfma_f32_16x16x32_bf16 v[68:71], a[208:211], v[80:83], v[68:71]\n\t" \
  "ds_read_b64_tr_b16 a[168:169], v211 offset:512\n\t" \
  "v_mfma_f32_16x16x32_bf16 v[72:75], a[224:227], v[80:83], v[172:175]\n\t" \
  "v_exp_f32 v0, v0\n\t" \
  "v_mfma_f32_16x16x32_bf16 v[72:75], a[228:231], v[84:87], v[72:75]\n\t" \
  "ds_read_b64_tr_b16 a[170:171], v211 offset:4608\n\t" \
  "v_mfma_f32_16x16x32_bf16 v[72:75], a[232:235], v[88:91], v[72:75]\n\t" \
  "ds_read_b64_tr_b16 a[172:173], v211 offset:8704\n\t" \
  "v_mfma_f32_16x16x32_bf16 v[72:75], a[236:239], v[92:95], v[72:75]\n\t" \
  "v_exp_f32 v1, v1\n\t" \
  "v_mfma_f32_16x16x32_bf16 v[76:79], a[252:255], v[92:95], v[172:175]\n\t" \
  "ds_read_b64_tr_b16 a[174:175], v211 offset:12800\n\t" \
  "s_mov_b32 s96, 18432\n\t" \
  "v_mfma_f32_16x16x32_bf16 v[76:79], a[248:251], v[88:91], v[76:79]\n\t" \
  "v_exp_f32 v2, v2\n\t" \
  "v_mfma_f32_16x16x32_bf16 v[76:79], a[244:247], v[84:87], v[76:79]\n\t" \
  "ds_read_b64_tr_b16 a[176:177], v210 offset:768\n\t" \
  "v_mfma_f32_16x16x32_bf16 v[76:79], a[240:243], v[80:83], v[76:79]\n\t" \
  "ds_read_b64_tr_b16 a[178:179], v210 offset:4864\n\t" \
  "v_mfma_f32_16x16x32_bf16 v[96:99], a[192:195], v[112:115], v[172:175]\n\t" \
  "buffer_load_dwordx4 v232, s[68:71], s96 offen offset:3072 lds\n\t" \
  "v_mfma_f32_16x16x32_bf16 v[96:99], a[196:199], v[116:119], v[96:99]\n\t" \
  "ds_read_b64_tr_b16 a[180:181], v210 offset:8960\n\t" \
  "v_mfma_f32_16x16x32_bf16 v[96:99], a[200:203], v[120:123], v[96:99]\n\t" \
  "v_exp_f32 v3, v3\n\t" \
  "v_mfma_f32_16x16x32_bf16 v[96:99], a[204:207], v[124:127], v[96:99]\n\t" \
  "ds_read_b64_tr_b16 a[182:183], v210 offset:13056\n\t" \
  "v_mfma_f32_16x16x32_bf16 v[100:103], a[220:223], v[124:127], v[172:175]\n\t" \
  "ds_read_b64_tr_b16 a[184:185], v211 offset:768\n\t" \
  "v_mfma_f32_16x16x32_bf16 v[100:103], a[216:219], v[120:123], v[100:103]\n\t" \
  "v_exp_f32 v4, v4\n\t" \
  "v_mfma_f32_16x16x32_bf16 v[100:103], a[212:215], v[116:119], v[100:103]\n\t" \
  "ds_read_b64_tr_b16 a[186:187], v211 offset:4864\n\t" \
  "v_mfma_f32_16x16x32_bf16 v[100:103], a[208:211], v[112:115], v[100:103]\n\t" \
  "ds_read_b64_tr_b16 a[188:189], v211 offset:8960\n\t" \
  "v_mfma_f32_16x16x32_bf16 v[104:107], a[224:227], v[112:115], v[172:175]\n\t" \
  "v_exp_f32 v5, v5\n\t" \
  "v_mfma_f32_16x16x32_bf16 v[104:107], a[228:231], v[116:119], v[104:107]\n\t" \
  "ds_read_b64_tr_b16 a[190:191], v211 offset:13056\n\t" \
  "v_mfma_f32_16x16x32_bf16 v[104:107], a[232:235], v[120:123], v[104:107]\n\t" \
  "v_exp_f32 v6, v6\n\t" \
  "v_mfma_f32_16x16x32_bf16 v[104:107], a[236:239], v[124:127], v[104:107]\n\t" \
  "v_exp_f32 v7, v7\n\t" \
  "v_mfma_f32_16x16x32_bf16 v[108:111], a[252:255], v[124:127], v[172:175]\n\t" \
  "v_exp_f32 v8, v8\n\t" \
  "v_mfma_f32_16x16x32_bf16 v[108:111], a[248:251], v[120:123], v[108:111]\n\t" \
  "v_exp_f32 v9, v9\n\t" \
  "v_mfma_f32_16x16x32_bf16 v[108:111], a[244:247], v[116:119], v[108:111]\n\t" \
  "s_waitcnt vmcnt(4) lgkmcnt(0)\n\t" \
  "v_exp_f32 v10, v10\n\t" \
  "v_mfma_f32_16x16x32_bf16 v[108:111], a[240:243], v[112:115], v[108:111]\n\t" \
  "s_barrier\n\t" \
  STA("s_memrealtime s[94:95]\n\t") \
  STA("s_memtime s[92:93]\n\t") \
  "v_mfma_f32_16x16x32_bf16 a[0:3], a[128:131], v[128:131], a[0:3]\n\t" \
  "buffer_store_dword v233, v247, s[16:19], 0 offen sc1\n\t" \
  "v_mfma_f32_16x16x32_bf16 a[0:3], a[132:135], v[132:135], a[0:3]\n\t" \
  "v_exp_f32 v11, v11\n\t" \
  "v_mfma_f32_16x16x32_bf16 a[4:7], a[136:139], v[128:131], a[4:7]\n\t" \
  "v_exp_f32 v12, v12\n\t" \
  "v_mfma_f32_16x16x32_bf16 a[4:7], a[140:143], v[132:135], a[4:7]\n\t" \
  "v_exp_f32 v13, v13\n\t" \
  "v_mfma_f32_16x16x32_bf16 a[8:11], a[144:147], v[128:131], a[8:11]\n\t" \
  "v_exp_f32 v14, v14\n\t" \
  "v_mfma_f32_16x16x32_bf16 a[8:11], a[148:151], v[132:135], a[8:11]\n\t" \
  "v_exp_f32 v15, v15\n\t" \
  "v_mfma_f32_16x16x32_bf16 a[12:15], a[152:155], v[128:131], a[12:15]\n\t" \
  "v_exp_f32 v32, v32\n\t" \
  "v_mfma_f32_16x16x32_bf16 a[12:15], a[156:159], v[132:135], a[12:15]\n\t" \
  "v_exp_f32 v33, v33\n\t" \
  "v_mfma_f32_16x16x32_bf16 a[16:19], a[160:163], v[128:131], a[16:19]\n\t" \
  "v_exp_f32 v34, v34\n\t" \
  "v_mfma_f32_16x16x32_bf16 a[16:19], a[164:167], v[132:135], a[16:19]\n\t" \
  "v_exp_f32 v35, v35\n\t" \
  "v_mfma_f32_16x16x32_bf16 a[20:23], a[168:171], v[128:131], a[20:23]\n\t" \
  "v_exp_f32 v36, v36\n\t" \
  "v_mfma_f32_16x16x32_bf16 a[20:23], a[172:175], v[132:135], a[20:23]\n\t" \
  "v_exp_f32 v37, v37\n\t" \
  "v_mfma_f32_16x16x32_bf16 a[24:27], a[176:179], v[128:131], a[24:27]\n\t" \
  STA("v_writelane_b32 v255, s94, 32\n\t") \
  STA("v_writelane_b32 v255, s92, 33\n\t") \
  "v_exp_f32 v38, v38\n\t" \
  "v_mfma_f32_16x16x32_bf16 a[24:27], a[180:183], v[132:135], a[24:27]\n\t" \
  "v_exp_f32 v39, v39\n\t" \
  "v_mfma_f32_16x16x32_bf16 a[28:31], a[184:187], v[128:131], a[28:31]\n\t" \
  "v_exp_f32 v40, v40\n\t" \
  "v_mfma_f32_16x16x32_bf16 a[28:31], a[188:191], v[132:135], a[28:31]\n\t" \
  "v_exp_f32 v41, v41\n\t" \
  "v_smfmac_f32_16x16x64_bf16 v[160:163], v[216:219], v[128:135], v208\n\t" \
  "ds_read_b128 a[192:195], v220 offset:16384\n\t" \
  "v_cvt_pk_bf16_f32 v128, v0, v1\n\t" \
  "v_mfma_f32_16x16x32_bf16 a[32:35], a[128:131], v[136:139], a[32:35]\n\t" \
  "ds_read_b128 a[196:199], v221 offset:16384\n\t" \
  "v_cvt_pk_bf16_f32 v129, v2, v3\n\t" \
  "v_mfma_f32_16x16x32_bf16 a[32:35], a[132:135], v[140:143], a[32:35]\n\t" \
  "s_add_u32 m0, s38, 32768\n\t" \
  "v_exp_f32 v42, v42\n\t" \
  "v_mfma_f32_16x16x32_bf16 a[36:39], a[136:139], v[136:139], a[36:39]\n\t" \
  "s_mov_b32 s96, 28672\n\t" \
  "v_exp_f32 v43, v43\n\t" \
  "v_mfma_f32_16x16x32_bf16 a[36:39], a[140:143], v[140:143], a[36:39]\n\t" \
  "ds_read_b128 a[200:203], v222 offset:16384\n\t" \
  "v_cvt_pk_bf16_f32 v130, v4, v5\n\t" \
  "v_mfma_f32_16x16x32_bf16 a[40:43], a[144:147], v[136:139], a[40:43]\n\t" \
  "v_exp_f32 v44, v44\n\t" \
  "v_mfma_f32_16x16x32_bf16 a[40:43], a[148:151], v[140:143], a[40:43]\n\t" \
  "buffer_load_dwordx4 v232, s[68:71], s96 offen offset:0 lds\n\t" \
  "v_mfma_f32_16x16x32_bf16 a[44:47], a[152:155], v[136:139], a[44:47]\n\t" \
  "ds_read_b128 a[204:207], v223 offset:16384\n\t" \
  "v_cvt_pk_bf16_f32 v131, v6, v7\n\t" \
  "v_mfma_f32_16x16x32_bf16 a[44:47], a[156:159], v[140:143], a[44:47]\n\t" \
  "v_exp_f32 v45, v45\n\t" \
  "v_mfma_f32_16x16x32_bf16 a[48:51], a[160:163], v[136:139], a[48:51]\n\t" \
  "ds_read_b128 a[208:211], v220 offset:20480\n\t" \
  "v_cvt_pk_bf16_f32 v132, v8, v9\n\t" \
  "v_mfma_f32_16x16x32_bf16 a[48:51], a[164:167], v[140:143], a[48:51]\n\t" \
  "v_exp_f32 v46, v46\n\t" \
  "v_mfma_f32_16x16x32_bf16 a[52:55], a[168:171], v[136:139], a[52:55]\n\t" \
  "v_exp_f32 v47, v47\n\t" \
  "v_mfma_f32_16x16x32_bf16 a[52:55], a[172:175], v[140:143], a[52:55]\n\t" \
  "ds_read_b128 a[212:215], v221 offset:20480\n\t" \
  "v_cvt_pk_bf16_f32 v133, v10, v11\n\t" \
  "v_mfma_f32_16x16x32_bf16 a[56:59], a[176:179], v[136:139], a[56:59]\n\t" \
  "v_exp_f32 v64, v64\n\t" \
  "v_mfma_f32_16x16x32_bf16 a[56:59], a[180:183], v[140:143], a[56:59]\n\t" \
  "s_mov_b32 s96, 34816\n\t" \
  "v_exp_f32 v65, v65\n\t" \
  "v_mfma_f32_16x16x32_bf16 a[60:63], a[184:187], v[136:139], a[60:63]\n\t" \
  "ds_read_b128 a[216:219], v222 offset:20480\n\t" \
  "v_cvt_pk_bf16_f32 v134, v12, v13\n\t" \
  "v_mfma_f32_16x16x32_bf16 a[60:63], a[188:191], v[140:143], a[60:63]\n\t" \
  "v_exp_f32 v66, v66\n\t" \
  "v_smfmac_f32_16x16x64_bf16 v[162:165], v[216:219], v[136:143], v208\n\t" \
  "ds_read_b128 a[220:223], v223 offset:20480\n\t" \
  "v_cvt_pk_bf16_f32 v135, v14, v15\n\t" \
  "v_mfma_f32_16x16x32_bf16 a[64:67], a[128:131], v[144:147], a[64:67]\n\t" \
  "buffer_load_dwordx4 v232, s[68:71], s96 offen offset:1024 lds\n\t" \
  "v_mfma_f32_16x16x32_bf16 a[64:67], a[132:135], v[148:151], a[64:67]\n\t" \
  "v_exp_f32 v67, v67\n\t" \
  "v_mfma_f32_16x16x32_bf16 a[68:71], a[136:139], v[144:147], a[68:71]\n\t" \
  "ds_read_b128 a[224:227], v220 offset:24576\n\t" \
  "v_cvt_pk_bf16_f32 v136, v32, v33\n\t" \
  "v_mfma_f32_16x16x32_bf16 a[68:71], a[140:143], v[148:151], a[68:71]\n\t" \
  "v_exp_f32 v68, v68\n\t" \
  "v_mfma_f32_16x16x32_bf16 a[72:75], a[144:147], v[144:147], a[72:75]\n\t" \
  "s_add_u32 m0, s38, 49152\n\t" \
  "v_exp_f32 v69, v69\n\t" \
  "v_mfma_f32_16x16x32_bf16 a[72:75], a[148:151], v[148:151], a[72:75]\n\t" \
  "s_add_u32 s96, s38, 65536\n\t" \
  "ds_read_b128 a[228:231], v221 offset:24576\n\t" \
  "v_cvt_pk_bf16_f32 v137, v34, v35\n\t" \
  "v_mfma_f32_16x16x32_bf16 a[76:79], a[152:155], v[144:147], a[76:79]\n\t" \
  "v_exp_f32 v70, v70\n\t" \
  "v_mfma_f32_16x16x32_bf16 a[76:79], a[156:159], v[148:151], a[76:79]\n\t" \
  "v_exp_f32 v71, v71\n\t" \
  "v_mfma_f32_16x16x32_bf16 a[80:83], a[160:163], v[144:147], a[80:83]\n\t" \
  "buffer_load_dwordx4 v224, s[60:63], s96 offen offset:0 lds\n\t" \
  "v_mfma_f32_16x16x32_bf16 a[80:83], a[164:167], v[148:151], a[80:83]\n\t" \
  "ds_read_b128 a[232:235], v222 offset:24576\n\t" \
  "v_cvt_pk_bf16_f32 v138, v36, v37\n\t" \
  "v_mfma_f32_16x16x32_bf16 a[84:87], a[168:171], v[144:147], a[84:87]\n\t" \
  "v_exp_f32 v72, v72\n\t" \
  "v_mfma_f32_16x16x32_bf16 a[84:87], a[172:175], v[148:151], a[84:87]\n\t" \
  "buffer_load_dwordx4 v225, s[60:63], s96 offen offset:1024 lds\n\t" \
  "v_mfma_f32_16x16x32_bf16 a[88:91], a[176:179], v[144:147], a[88:91]\n\t" \
  "ds_read_b128 a[236:239], v223 offset:24576\n\t" \
  "v_cvt_pk_bf16_f32 v139, v38, v39\n\t" \
  "v_mfma_f32_16x16x32_bf16 a[88:91], a[180:183], v[148:151], a[88:91]\n\t" \
  "ds_read_b128 a[240:243], v220 offset:28672\n\t" \
  "v_cvt_pk_bf16_f32 v140, v40, v41\n\t" \
  "v_mfma_f32_16x16x32_bf16 a[92:95], a[184:187], v[144:147], a[92:95]\n\t" \
  "buffer_load_dwordx4 v226, s[60:63], s96 offen offset:2048 lds\n\t" \
  "v_mfma_f32_16x16x32_bf16 a[92:95], a[188:191], v[148:151], a[92:95]\n\t" \
  "v_exp_f32 v73, v73\n\t" \
  "v_smfmac_f32_16x16x64_bf16 v[164:167], v[216:219], v[144:151], v208\n\t" \
  "ds_read_b128 a[244:247], v221 offset:28672\n\t" \
  "v_cvt_pk_bf16_f32 v141, v42, v43\n\t" \
  "v_mfma_f32_16x16x32_bf16 a[96:99], a[128:131], v[152:155], a[96:99]\n\t" \
  "buffer_load_dwordx4 v227, s[60:63], s96 offen offset:3072 lds\n\t" \
  "v_mfma_f32_16x16x32_bf16 a[96:99], a[132:135], v[156:159], a[96:99]\n\t" \
  "s_add_u32 m0, s38, 147456\n\t" \
  "s_add_u32 s96, s38, 49152\n\t" \
  "v_cvt_pk_bf16_f32 v142, v44, v45\n\t" \
  "v_mfma_f32_16x16x32_bf16 a[100:103], a[136:139], v[152:155], a[100:103]\n\t" \
  "buffer_load_dwordx4 v230, s[64:67], s96 offen offset:0 lds\n\t" \
  "v_mfma_f32_16x16x32_bf16 a[100:103], a[140:143], v[156:159], a[100:103]\n\t" \
  "ds_read_b128 a[248:251], v222 offset:28672\n\t" \
  "v_cvt_pk_bf16_f32 v143, v46, v47\n\t" \
  "v_mfma_f32_16x16x32_bf16 a[104:107], a[144:147], v[152:155], a[104:107]\n\t" \
  "v_exp_f32 v74, v74\n\t" \
  "v_mfma_f32_16x16x32_bf16 a[104:107], a[148:151], v[156:159], a[104:107]\n\t" \
  "buffer_load_dwordx4 v231, s[64:67], s96 offen offset:1024 lds\n\t" \
  "v_mfma_f32_16x16x32_bf16 a[108:111], a[152:155], v[152:155], a[108:111]\n\t" \
  "ds_read_b128 a[252:255], v223 offset:28672\n\t" \
  "v_cvt_pk_bf16_f32 v144, v64, v65\n\t" \
  "v_mfma_f32_16x16x32_bf16 a[108:111], a[156:159], v[156:159], a[108:111]\n\t" \
  "v_exp_f32 v75, v75\n\t" \
  "v_mfma_f32_16x16x32_bf16 a[112:115], a[160:163], v[152:155], a[112:115]\n\t" \
  "buffer_load_dwordx4 v230, s[64:67], s96 offen offset:2048 lds\n\t" \
  "v_mfma_f32_16x16x32_bf16 a[112:115], a[164:167], v[156:159], a[112:115]\n\t" \
  "v_exp_f32 v76, v76\n\t" \
  "v_mfma_f32_16x16x32_bf16 a[116:119], a[168:171], v[152:155], a[116:119]\n\t" \
  "v_exp_f32 v77, v77\n\t" \
  "v_mfma_f32_16x16x32_bf16 a[116:119], a[172:175], v[156:159], a[116:119]\n\t" \
  "buffer_load_dwordx4 v231, s[64:67], s96 offen offset:3072 lds\n\t" \
  "v_mfma_f32_16x16x32_bf16 a[120:123], a[176:179], v[152:155], a[120:123]\n\t" \
  "v_exp_f32 v78, v78\n\t" \
  "v_mfma_f32_16x16x32_bf16 a[120:123], a[180:183], v[156:159], a[120:123]\n\t" \
  "v_exp_f32 v79, v79\n\t" \
  "v_mfma_f32_16x16x32_bf16 a[124:127], a[184:187], v[152:155], a[124:127]\n\t" \
  "v_exp_f32 v96, v96\n\t" \
  "v_mfma_f32_16x16x32_bf16 a[124:127], a[188:191], v[156:159], a[124:127]\n\t" \
  "v_exp_f32 v97, v97\n\t" \
  "v_smfmac_f32_16x16x64_bf16 v[166:169], v[216:219], v[152:159], v208\n\t" \
  "s_waitcnt lgkmcnt(12)\n\t" \
  "v_cvt_pk_bf16_f32 v145, v66, v67\n\t" \
  STA("s_memrealtime s[94:95]\n\t") \
  STA("s_memtime s[92:93]\n\t") \
  "v_mfma_f32_16x16x32_bf16 v[0:3], a[192:195], v[16:19], v[172:175]\n\t" \
  "ds_read_b64_tr_b16 a[128:129], v228 offset:0\n\t" \
  "v_cvt_pk_bf16_f32 v146, v68, v69\n\t" \
  "v_mfma_f32_16x16x32_bf16 v[0:3], a[196:199], v[20:23], v[0:3]\n\t" \
  "v_exp_f32 v98, v98\n\t" \
  "v_mfma_f32_16x16x32_bf16 v[0:3], a[200:203], v[24:27], v[0:3]\n\t" \
  "s_add_u32 m0, s38, 32768\n\t" \
  "ds_read_b64_tr_b16 a[130:131], v228 offset:4096\n\t" \
  "v_cvt_pk_bf16_f32 v147, v70, v71\n\t" \
  "v_mfma_f32_16x16x32_bf16 v[0:3], a[204:207], v[28:31], v[0:3]\n\t" \
  "s_waitcnt lgkmcnt(10)\n\t" \
  "v_exp_f32 v99, v99\n\t" \
  "v_mfma_f32_16x16x32_bf16 v[4:7], a[220:223], v[28:31], v[172:175]\n\t" \
  "s_mov_b32 s96, 40960\n\t" \
  "ds_read_b64_tr_b16 a[132:133], v228 offset:8192\n\t" \
  "v_cvt_pk_bf16_f32 v148, v72, v73\n\t" \
  "v_mfma_f32_16x16x32_bf16 v[4:7], a[216:219], v[24:27], v[4:7]\n\t" \
  "v_exp_f32 v100, v100\n\t" \
  "v_mfma_f32_16x16x32_bf16 v[4:7], a[212:215], v[20:23], v[4:7]\n\t" \
  "buffer_load_dwordx4 v232, s[68:71], s96 offen offset:2048 lds\n\t" \
  "v_mfma_f32_16x16x32_bf16 v[4:7], a[208:211], v[16:19], v[4:7]\n\t" \
  "s_waitcnt lgkmcnt(7)\n\t" \
  "ds_read_b64_tr_b16 a[134:135], v228 offset:12288\n\t" \
  "v_cvt_pk_bf16_f32 v149, v74, v75\n\t" \
  "v_mfma_f32_16x16x32_bf16 v[8:11], a[224:227], v[16:19], v[172:175]\n\t" \
  "ds_read_b64_tr_b16 a[136:137], v229 offset:0\n\t" \
  "v_cvt_pk_bf16_f32 v150, v76, v77\n\t" \
  "v_mfma_f32_16x16x32_bf16 v[8:11], a[228:231], v[20:23], v[8:11]\n\t" \
  "ds_read_b64_tr_b16 a[138:139], v229 offset:4096\n\t" \
  "v_cvt_pk_bf16_f32 v151, v78, v79\n\t" \
  "v_mfma_f32_16x16x32_bf16 v[8:11], a[232:235], v[24:27], v[8:11]\n\t" \
  "v_exp_f32 v101, v101\n\t" \
  "v_mfma_f32_16x16x32_bf16 v[8:11], a[236:239], v[28:31], v[8:11]\n\t" \
  "s_waitcnt lgkmcnt(6)\n\t" \
  "ds_read_b64_tr_b16 a[140:141], v229 offset:8192\n\t" \
  "v_cvt_pk_bf16_f32 v152, v96, v97\n\t" \
  "v_mfma_f32_16x16x32_bf16 v[12:15], a[252:255], v[28:31], v[172:175]\n\t" \
  STA("v_writelane_b32 v255, s94, 34\n\t") \
  STA("v_writelane_b32 v255, s92, 35\n\t") \
  "v_exp_f32 v102, v102\n\t" \
  "v_mfma_f32_16x16x32_bf16 v[12:15], a[248:251], v[24:27], v[12:15]\n\t" \
  "ds_read_b64_tr_b16 a[142:143], v229 offset:12288\n\t" \
  "v_cvt_pk_bf16_f32 v153, v98, v99\n\t" \
  "v_mfma_f32_16x16x32_bf16 v[12:15], a[244:247], v[20:23], v[12:15]\n\t" \
  "ds_read_b64_tr_b16 a[144:145], v228 offset:256\n\t" \
  "v_cvt_pk_bf16_f32 v154, v100, v101\n\t" \
  "v_mfma_f32_16x16x32_bf16 v[12:15], a[240:243], v[16:19], v[12:15]\n\t" \
  "v_exp_f32 v103, v103\n\t" \
  "v_mfma_f32_16x16x32_bf16 v[32:35], a[192:195], v[48:51], v[172:175]\n\t" \
  "s_mov_b32 s96, 47104\n\t" \
  "ds_read_b64_tr_b16 a[146:147], v228 offset:4352\n\t" \
  "v_cvt_pk_bf16_f32 v155, v102, v103\n\t" \
  "v_mfma_f32_16x16x32_bf16 v[32:35], a[196:199], v[52:55], v[32:35]\n\t" \
  "v_exp_f32 v104, v104\n\t" \
  "v_mfma_f32_16x16x32_bf16 v[32:35], a[200:203], v[56:59], v[32:35]\n\t" \
  "v_exp_f32 v105, v105\n\t" \
  "v_mfma_f32_16x16x32_bf16 v[32:35], a[204:207], v[60:63], v[32:35]\n\t" \
  ".Lhs7:\n\ts_cbranch_execz .Lhs7n\n\t" \
  "ds_read_b64_tr_b16 a[148:149], v228 offset:8448\n\t" \
  "v_cvt_pk_bf16_f32 v156, v104, v105\n\t" \
  "v_mfma_f32_16x16x32_bf16 v[36:39], a[220:223], v[60:63], v[172:175]\n\t" \
  "buffer_load_dwordx4 v232, s[68:71], s96 offen offset:3072 lds\n\t" \
  "v_mfma_f32_16x16x32_bf16 v[36:39], a[216:219], v[56:59], v[36:39]\n\t" \
  "v_exp_f32 v106, v106\n\t" \
  "v_mfma_f32_16x16x32_bf16 v[36:39], a[212:215], v[52:55], v[36:39]\n\t" \
  "v_exp_f32 v107, v107\n\t" \
  "v_mfma_f32_16x16x32_bf16 v[36:39], a[208:211], v[48:51], v[36:39]\n\t" \
  "ds_read_b64_tr_b16 a[150:151], v228 offset:12544\n\t" \
  "v_cvt_pk_bf16_f32 v157, v106, v107\n\t" \
  "v_mfma_f32_16x16x32_bf16 v[40:43], a[224:227], v[48:51], v[172:175]\n\t" \
  "v_exp_f32 v108, v108\n\t" \
  "v_mfma_f32_16x16x32_bf16 v[40:43], a[228:231], v[52:55], v[40:43]\n\t" \
  "v_exp_f32 v109, v109\n\t" \
  "v_mfma_f32_16x16x32_bf16 v[40:43], a[232:235], v[56:59], v[40:43]\n\t" \
  "ds_read_b64_tr_b16 a[152:153], v229 offset:256\n\t" \
  "v_cvt_pk_bf16_f32 v158, v108, v109\n\t" \
  "v_mfma_f32_16x16x32_bf16 v[40:43], a[236:239], v[60:63], v[40:43]\n\t" \
  "v_exp_f32 v110, v110\n\t" \
  "v_mfma_f32_16x16x32_bf16 v[44:47], a[252:255], v[60:63], v[172:175]\n\t" \
  "ds_read_b64_tr_b16 a[154:155], v229 offset:4352\n\t" \
  "v_mfma_f32_16x16x32_bf16 v[44:47], a[248:251], v[56:59], v[44:47]\n\t" \
  "v_exp_f32 v111, v111\n\t" \
  "v_mfma_f32_16x16x32_bf16 v[44:47], a[244:247], v[52:55], v[44:47]\n\t" \
  "s_add_u32 m0, s38, 114688\n\t" \
  "ds_read_b64_tr_b16 a[156:157], v229 offset:8448\n\t" \
  "v_cvt_pk_bf16_f32 v159, v110, v111\n\t" \
  "v_mfma_f32_16x16x32_bf16 v[44:47], a[240:243], v[48:51], v[44:47]\n\t" \
  "ds_read_b64_tr_b16 a[158:159], v229 offset:12544\n\t" \
  "s_mov_b32 s96, 0\n\t" \
  "v_mfma_f32_16x16x32_bf16 v[64:67], a[192:195], v[80:83], v[172:175]\n\t" \
  "ds_read_b64_tr_b16 a[160:161], v228 offset:512\n\t" \
  "v_mfma_f32_16x16x32_bf16 v[64:67], a[196:199], v[84:87], v[64:67]\n\t" \
  "v_exp_f32 v0, v0\n\t" \
  "v_mfma_f32_16x16x32_bf16 v[64:67], a[200:203], v[88:91], v[64:67]\n\t" \
  "buffer_load_dwordx4 v232, s[72:75], s96 offen offset:0 lds\n\t" \
  "v_mfma_f32_16x16x32_bf16 v[64:67], a[204:207], v[92:95], v[64:67]\n\t" \
  "ds_read_b64_tr_b16 a[162:163], v228 offset:4608\n\t" \
  "v_mfma_f32_16x16x32_bf16 v[68:71], a[220:223], v[92:95], v[172:175]\n\t" \
  "ds_read_b64_tr_b16 a[164:165], v228 offset:8704\n\t" \
  "v_mfma_f32_16x16x32_bf16 v[68:71], a[216:219], v[88:91], v[68:71]\n\t" \
  "ds_read_b64_tr_b16 a[166:167], v228 offset:12800\n\t" \
  "v_mfma_f32_16x16x32_bf16 v[68:71], a[212:215], v[84:87], v[68:71]\n\t" \
  "v_exp_f32 v1, v1\n\t" \
  "v_mfma_f32_16x16x32_bf16 v[68:71], a[208:211], v[80:83], v[68:71]\n\t" \
  "ds_read_b64_tr_b16 a[168:169], v229 offset:512\n\t" \
  "v_mfma_f32_16x16x32_bf16 v[72:75], a[224:227], v[80:83], v[172:175]\n\t" \
  "v_exp_f32 v2, v2\n\t" \
  "v_mfma_f32_16x16x32_bf16 v[72:75], a[228:231], v[84:87], v[72:75]\n\t" \
  "ds_read_b64_tr_b16 a[170:171], v229 offset:4608\n\t" \
  "v_mfma_f32_16x16x32_bf16 v[72:75], a[232:235], v[88:91], v[72:75]\n\t" \
  "ds_read_b64_tr_b16 a[172:173], v229 offset:8704\n\t" \
  "v_mfma_f32_16x16x32_bf16 v[72:75], a[236:239], v[92:95], v[72:75]\n\t" \
  "ds_read_b64_tr_b16 a[174:175], v229 offset:12800\n\t" \
  "buffer_load_dword v246, v247, s[16:19], 0 offen sc1\n\t" \
  "v_mfma_f32_16x16x32_bf16 v[76:79], a[252:255], v[92:95], v[172:175]\n\t" \
  "s_mov_b32 s96, 6144\n\t" \
  "v_exp_f32 v3, v3\n\t" \
  "v_mfma_f32_16x16x32_bf16 v[76:79], a[248:251], v[88:91], v[76:79]\n\t" \
  "v_exp_f32 v4, v4\n\t" \
  "v_mfma_f32_16x16x32_bf16 v[76:79], a[244:247], v[84:87], v[76:79]\n\t" \
  "ds_read_b64_tr_b16 a[176:177], v228 offset:768\n\t" \
  "v_mfma_f32_16x16x32_bf16 v[76:79], a[240:243], v[80:83], v[76:79]\n\t" \
  "ds_read_b64_tr_b16 a[178:179], v228 offset:4864\n\t" \
  "v_mfma_f32_16x16x32_bf16 v[96:99], a[192:195], v[112:115], v[172:175]\n\t" \
  "buffer_load_dwordx4 v232, s[72:75], s96 offen offset:1024 lds\n\t" \
  "v_mfma_f32_16x16x32_bf16 v[96:99], a[196:199], v[116:119], v[96:99]\n\t" \
  "ds_read_b64_tr_b16 a[180:181], v228 offset:8960\n\t" \
  "v_mfma_f32_16x16x32_bf16 v[96:99], a[200:203], v[120:123], v[96:99]\n\t" \
  "v_exp_f32 v5, v5\n\t" \
  "v_mfma_f32_16x16x32_bf16 v[96:99], a[204:207], v[124:127], v[96:99]\n\t" \
  "ds_read_b64_tr_b16 a[182:183], v228 offset:13056\n\t" \
  "v_mfma_f32_16x16x32_bf16 v[100:103], a[220:223], v[124:127], v[172:175]\n\t" \
  "ds_read_b64_tr_b16 a[184:185], v229 offset:768\n\t" \
  "v_mfma_f32_16x16x32_bf16 v[100:103], a[216:219], v[120:123], v[100:103]\n\t" \
  "v_exp_f32 v6, v6\n\t" \
  "v_mfma_f32_16x16x32_bf16 v[100:103], a[212:215], v[116:119], v[100:103]\n\t" \
  "ds_read_b64_tr_b16 a[186:187], v229 offset:4864\n\t" \
  "v_mfma_f32_16x16x32_bf16 v[100:103], a[208:211], v[112:115], v[100:103]\n\t" \
  "ds_read_b64_tr_b16 a[188:189], v229 offset:8960\n\t" \
  "v_mfma_f32_16x16x32_bf16 v[104:107], a[224:227], v[112:115], v[172:175]\n\t" \
  "v_exp_f32 v7, v7\n\t" \
  "v_mfma_f32_16x16x32_bf16 v[104:107], a[228:231], v[116:119], v[104:107]\n\t" \
  "ds_read_b64_tr_b16 a[190:191], v229 offset:13056\n\t" \
  "v_mfma_f32_16x16x32_bf16 v[104:107], a[232:235], v[120:123], v[104:107]\n\t" \
  "v_exp_f32 v8, v8\n\t" \
  "v_mfma_f32_16x16x32_bf16 v[104:107], a[236:239], v[124:127], v[104:107]\n\t" \
  "v_exp_f32 v9, v9\n\t" \
  "v_mfma_f32_16x16x32_bf16 v[108:111], a[252:255], v[124:127], v[172:175]\n\t" \
  "v_exp_f32 v10, v10\n\t" \
  "v_mfma_f32_16x16x32_bf16 v[108:111], a[248:251], v[120:123], v[108:111]\n\t" \
  "v_exp_f32 v11, v11\n\t" \
  "v_mfma_f32_16x16x32_bf16 v[108:111], a[244:247], v[116:119], v[108:111]\n\t" \
  "s_waitcnt vmcnt(5) lgkmcnt(0)\n\t" \
  "v_exp_f32 v12, v12\n\t" \
  "v_mfma_f32_16x16x32_bf16 v[108:111], a[240:243], v[112:115], v[108:111]\n\t" \
  "s_barrier\n\t" \
  STA("s_memrealtime s[94:95]\n\t") \
  STA("s_memtime s[92:93]\n\t") \
  "v_mfma_f32_16x16x32_bf16 a[0:3], a[128:131], v[128:131], a[0:3]\n\t" \
  "v_exp_f32 v13, v13\n\t" \
  "v_mfma_f32_16x16x32_bf16 a[0:3], a[132:135], v[132:135], a[0:3]\n\t" \
  "v_exp_f32 v14, v14\n\t" \
  "v_mfma_f32_16x16x32_bf16 a[4:7], a[136:139], v[128:131], a[4:7]\n\t" \
  "v_exp_f32 v15, v15\n\t" \
  "v_mfma_f32_16x16x32_bf16 a[4:7], a[140:143], v[132:135], a[4:7]\n\t" \
  "v_exp_f32 v32, v32\n\t" \
  "v_mfma_f32_16x16x32_bf16 a[8:11], a[144:147], v[128:131], a[8:11]\n\t" \
  "v_exp_f32 v33, v33\n\t" \
  "v_mfma_f32_16x16x32_bf16 a[8:11], a[148:151], v[132:135], a[8:11]\n\t" \
  "v_exp_f32 v34, v34\n\t" \
  "v_mfma_f32_16x16x32_bf16 a[12:15], a[152:155], v[128:131], a[12:15]\n\t" \
  "s_getreg_b32 s98, hwreg(HW_REG_IB_STS, 0, 4)\n\t" \
  "s_cmp_lg_u32 s98, 0\n\t" \
  "s_addc_u32 s54, s54, 0\n\t" \
  "v_mfma_f32_16x16x32_bf16 a[12:15], a[156:159], v[132:135], a[12:15]\n\t" \
  "v_exp_f32 v35, v35\n\t" \
  "v_mfma_f32_16x16x32_bf16 a[16:19], a[160:163], v[128:131], a[16:19]\n\t" \
  "s_getreg_b32 s98, hwreg(HW_REG_IB_STS, 0, 4)\n\t" \
  "s_cmp_lg_u32 s98, 0\n\t" \
  "s_addc_u32 s54, s54, 0\n\t" \
  "v_mfma_f32_16x16x32_bf16 a[16:19], a[164:167], v[132:135], a[16:19]\n\t" \
  "v_exp_f32 v36, v36\n\t" \
  "v_mfma_f32_16x16x32_bf16 a[20:23], a[168:171], v[128:131], a[20:23]\n\t" \
  "s_getreg_b32 s98, hwreg(HW_REG_IB_STS, 0, 4)\n\t" \
  "s_cmp_lg_u32 s98, 0\n\t" \
  "s_addc_u32 s54, s54, 0\n\t" \
  "v_mfma_f32_16x16x32_bf16 a[20:23], a[172:175], v[132:135], a[20:23]\n\t" \
  "v_exp_f32 v37, v37\n\t" \
  "v_mfma_f32_16x16x32_bf16 a[24:27], a[176:179], v[128:131], a[24:27]\n\t" \
  STA("v_writelane_b32 v255, s94, 36\n\t") \
  STA("v_writelane_b32 v255, s92, 37\n\t") \
  "s_getreg_b32 s98, hwreg(HW_REG_IB_STS, 0, 4)\n\t" \
  "s_cmp_lg_u32 s98, 0\n\t" \
  "s_addc_u32 s54, s54, 0\n\t" \
  "v_mfma_f32_16x16x32_bf16 a[24:27], a[180:183], v[132:135], a[24:27]\n\t" \
  "v_exp_f32 v38, v38\n\t" \
  "v_mfma_f32_16x16x32_bf16 a[28:31], a[184:187], v[128:131], a[28:31]\n\t" \
  "v_exp_f32 v39, v39\n\t" \
  "v_mfma_f32_16x16x32_bf16 a[28:31], a[188:191], v[132:135], a[28:31]\n\t" \
  "v_exp_f32 v40, v40\n\t" \
  "v_smfmac_f32_16x16x64_bf16 v[160:163], v[216:219], v[128:135], v208\n\t" \
  "ds_read_b128 a[192:195], v220 offset:0\n\t" \
  "v_cvt_pk_bf16_f32 v128, v0, v1\n\t" \
  "v_mfma_f32_16x16x32_bf16 a[32:35], a[128:131], v[136:139], a[32:35]\n\t" \
  "ds_read_b128 a[196:199], v221 offset:0\n\t" \
  "v_cvt_pk_bf16_f32 v129, v2, v3\n\t" \
  "v_mfma_f32_16x16x32_bf16 a[32:35], a[132:135], v[140:143], a[32:35]\n\t" \
  "s_add_u32 m0, s38, 114688\n\t" \
  "v_exp_f32 v41, v41\n\t" \
  "v_mfma_f32_16x16x32_bf16 a[36:39], a[136:139], v[136:139], a[36:39]\n\t" \
  "s_mov_b32 s96, 12288\n\t" \
  "v_exp_f32 v42, v42\n\t" \
  "v_mfma_f32_16x16x32_bf16 a[36:39], a[140:143], v[140:143], a[36:39]\n\t" \
  "ds_read_b128 a[200:203], v222 offset:0\n\t" \
  "v_cvt_pk_bf16_f32 v130, v4, v5\n\t" \
  "v_mfma_f32_16x16x32_bf16 a[40:43], a[144:147], v[136:139], a[40:43]\n\t" \
  "v_exp_f32 v43, v43\n\t" \
  "v_mfma_f32_16x16x32_bf16 a[40:43], a[148:151], v[140:143], a[40:43]\n\t" \
  "buffer_load_dwordx4 v232, s[72:75], s96 offen offset:2048 lds\n\t" \
  "v_mfma_f32_16x16x32_bf16 a[44:47], a[152:155], v[136:139], a[44:47]\n\t" \
  "ds_read_b128 a[204:207], v223 offset:0\n\t" \
  "v_cvt_pk_bf16_f32 v131, v6, v7\n\t" \
  "v_mfma_f32_16x16x32_bf16 a[44:47], a[156:159], v[140:143], a[44:47]\n\t" \
  "v_exp_f32 v44, v44\n\t" \
  "v_mfma_f32_16x16x32_bf16 a[48:51], a[160:163], v[136:139], a[48:51]\n\t" \
  "ds_read_b128 a[208:211], v220 offset:4096\n\t" \
  "v_cvt_pk_bf16_f32 v132, v8, v9\n\t" \
  "v_mfma_f32_16x16x32_bf16 a[48:51], a[164:167], v[140:143], a[48:51]\n\t" \
  STS("v_writelane_b32 v255, s54, 58\n\t") \
  "s_cmp_ge_u32 s54, 2\n\t" \
  "s_cselect_b32 s98, 0x4000, 0\n\t" \
  "s_or_b32 s98, s98, s36\n\t" \
  "v_mfma_f32_16x16x32_bf16 a[52:55], a[168:171], v[136:139], a[52:55]\n\t" \
  "s_cmp_ge_u32 s54, 3\n\t" \
  "s_cselect_b32 s54, 2.0, 0\n\t" \
  "s_or_b32 s98, s98, s54\n\t" \
  "v_mfma_f32_16x16x32_bf16 a[52:55], a[172:175], v[140:143], a[52:55]\n\t" \
  "ds_read_b128 a[212:215], v221 offset:4096\n\t" \
  "v_mov_b32 v209, s98\n\t" \
  "v_mfma_f32_16x16x32_bf16 a[56:59], a[176:179], v[136:139], a[56:59]\n\t" \
  "v_exp_f32 v45, v45\n\t" \
  "v_mfma_f32_16x16x32_bf16 a[56:59], a[180:183], v[140:143], a[56:59]\n\t" \
  "s_mov_b32 s96, 18432\n\t" \
  "v_exp_f32 v46, v46\n\t" \
  "v_mfma_f32_16x16x32_bf16 a[60:63], a[184:187], v[136:139], a[60:63]\n\t" \
  "ds_read_b128 a[216:219], v222 offset:4096\n\t" \
  "v_cvt_pk_bf16_f32 v133, v10, v11\n\t" \
  "v_mfma_f32_16x16x32_bf16 a[60:63], a[188:191], v[140:143], a[60:63]\n\t" \
  "buffer_store_dword v209, v250, s[16:19], 0 offen sc1\n\t" \
  "ds_read_b128 a[220:223], v223 offset:4096\n\t" \
  "v_smfmac_f32_16x16x64_bf16 v[162:165], v[216:219], v[136:143], v208\n\t" \
  "ds_read_b128 a[224:227], v220 offset:8192\n\t" \
  "v_cvt_pk_bf16_f32 v134, v12, v13\n\t" \
  "v_mfma_f32_16x16x32_bf16 a[64:67], a[128:131], v[144:147], a[64:67]\n\t" \
  "buffer_load_dwordx4 v232, s[72:75], s96 offen offset:3072 lds\n\t" \
  "v_mfma_f32_16x16x32_bf16 a[64:67], a[132:135], v[148:151], a[64:67]\n\t" \
  "v_exp_f32 v47, v47\n\t" \
  "v_mfma_f32_16x16x32_bf16 a[68:71], a[136:139], v[144:147], a[68:71]\n\t" \
  "v_exp_f32 v64, v64\n\t" \
  "v_mfma_f32_16x16x32_bf16 a[68:71], a[140:143], v[148:151], a[68:71]\n\t" \
  "v_exp_f32 v65, v65\n\t" \
  "v_mfma_f32_16x16x32_bf16 a[72:75], a[144:147], v[144:147], a[72:75]\n\t" \
  "s_add_u32 m0, s38, 65536\n\t" \
  "v_exp_f32 v66, v66\n\t" \
  "v_mfma_f32_16x16x32_bf16 a[72:75], a[148:151], v[148:151], a[72:75]\n\t" \
  "s_add_u32 s96, s38, 81920\n\t" \
  "ds_read_b128 a[228:231], v221 offset:8192\n\t" \
  "v_cvt_pk_bf16_f32 v135, v14, v15\n\t" \
  "v_mfma_f32_16x16x32_bf16 a[76:79], a[152:155], v[144:147], a[76:79]\n\t" \
  "v_exp_f32 v67, v67\n\t" \
  "v_mfma_f32_16x16x32_bf16 a[76:79], a[156:159], v[148:151], a[76:79]\n\t" \
  "v_exp_f32 v68, v68\n\t" \
  "v_mfma_f32_16x16x32_bf16 a[80:83], a[160:163], v[144:147], a[80:83]\n\t" \
  "buffer_load_dwordx4 v224, s[60:63], s96 offen offset:0 lds\n\t" \
  "v_mfma_f32_16x16x32_bf16 a[80:83], a[164:167], v[148:151], a[80:83]\n\t" \
  "ds_read_b128 a[232:235], v222 offset:8192\n\t" \
  "v_cvt_pk_bf16_f32 v136, v32, v33\n\t" \
  "v_mfma_f32_16x16x32_bf16 a[84:87], a[168:171], v[144:147], a[84:87]\n\t" \
  "v_exp_f32 v69, v69\n\t" \
  "v_mfma_f32_16x16x32_bf16 a[84:87], a[172:175], v[148:151], a[84:87]\n\t" \
  "buffer_load_dwordx4 v225, s[60:63], s96 offen offset:1024 lds\n\t" \
  "v_mfma_f32_16x16x32_bf16 a[88:91], a[176:179], v[144:147], a[88:91]\n\t" \
  "ds_read_b128 a[236:239], v223 offset:8192\n\t" \
  "v_cvt_pk_bf16_f32 v137, v34, v35\n\t" \
  "v_mfma_f32_16x16x32_bf16 a[88:91], a[180:183], v[148:151], a[88:91]\n\t" \
  "ds_read_b128 a[240:243], v220 offset:12288\n\t" \
  "v_cvt_pk_bf16_f32 v138, v36, v37\n\t" \
  "v_mfma_f32_16x16x32_bf16 a[92:95], a[184:187], v[144:147], a[92:95]\n\t" \
  "buffer_load_dwordx4 v226, s[60:63], s96 offen offset:2048 lds\n\t" \
  "v_mfma_f32_16x16x32_bf16 a[92:95], a[188:191], v[148:151], a[92:95]\n\t" \
  "v_exp_f32 v70, v70\n\t" \
  "v_smfmac_f32_16x16x64_bf16 v[164:167], v[216:219], v[144:151], v208\n\t" \
  "ds_read_b128 a[244:247], v221 offset:12288\n\t" \
  "v_cvt_pk_bf16_f32 v139, v38, v39\n\t" \
  "v_mfma_f32_16x16x32_bf16 a[96:99], a[128:131], v[152:155], a[96:99]\n\t" \
  "buffer_load_dwordx4 v227, s[60:63], s96 offen offset:3072 lds\n\t" \
  "v_mfma_f32_16x16x32_bf16 a[96:99], a[132:135], v[156:159], a[96:99]\n\t" \
  "s_add_u32 m0, s38, 81920\n\t" \
  "v_cvt_pk_bf16_f32 v140, v40, v41\n\t" \
  "s_add_u32 s96, s38, 65536\n\t" \
  "v_mfma_f32_16x16x32_bf16 a[100:103], a[136:139], v[152:155], a[100:103]\n\t" \
  "buffer_load_dwordx4 v230, s[64:67], s96 offen offset:0 lds\n\t" \
  "v_mfma_f32_16x16x32_bf16 a[100:103], a[140:143], v[156:159], a[100:103]\n\t" \
  "ds_read_b128 a[248:251], v222 offset:12288\n\t" \
  "v_cvt_pk_bf16_f32 v141, v42, v43\n\t" \
  "v_mfma_f32_16x16x32_bf16 a[104:107], a[144:147], v[152:155], a[104:107]\n\t" \
  "v_exp_f32 v71, v71\n\t" \
  "v_mfma_f32_16x16x32_bf16 a[104:107], a[148:151], v[156:159], a[104:107]\n\t" \
  "buffer_load_dwordx4 v231, s[64:67], s96 offen offset:1024 lds\n\t" \
  "v_mfma_f32_16x16x32_bf16 a[108:111], a[152:155], v[152:155], a[108:111]\n\t" \
  "ds_read_b128 a[252:255], v223 offset:12288\n\t" \
  "v_cvt_pk_bf16_f32 v142, v44, v45\n\t" \
  "v_mfma_f32_16x16x32_bf16 a[108:111], a[156:159], v[156:159], a[108:111]\n\t" \
  "v_exp_f32 v72, v72\n\t" \
  "v_mfma_f32_16x16x32_bf16 a[112:115], a[160:163], v[152:155], a[112:115]\n\t" \
  "buffer_load_dwordx4 v230, s[64:67], s96 offen offset:2048 lds\n\t" \
  "v_mfma_f32_16x16x32_bf16 a[112:115], a[164:167], v[156:159], a[112:115]\n\t" \
  "v_exp_f32 v73, v73\n\t" \
  "v_mfma_f32_16x16x32_bf16 a[116:119], a[168:171], v[152:155], a[116:119]\n\t" \
  "v_exp_f32 v74, v74\n\t" \
  "v_mfma_f32_16x16x32_bf16 a[116:119], a[172:175], v[156:159], a[116:119]\n\t" \
  "buffer_load_dwordx4 v231, s[64:67], s96 offen offset:3072 lds\n\t" \
  "v_mfma_f32_16x16x32_bf16 a[120:123], a[176:179], v[152:155], a[120:123]\n\t" \
  "v_exp_f32 v75, v75\n\t" \
  "v_mfma_f32_16x16x32_bf16 a[120:123], a[180:183], v[156:159], a[120:123]\n\t" \
  "v_exp_f32 v76, v76\n\t" \
  "v_mfma_f32_16x16x32_bf16 a[124:127], a[184:187], v[152:155], a[124:127]\n\t" \
  "v_exp_f32 v77, v77\n\t" \
  "v_mfma_f32_16x16x32_bf16 a[124:127], a[188:191], v[156:159], a[124:127]\n\t" \
  "v_exp_f32 v78, v78\n\t" \
  "v_smfmac_f32_16x16x64_bf16 v[166:169], v[216:219], v[152:159], v208\n\t" \
  "s_waitcnt lgkmcnt(12)\n\t" \
  "v_cvt_pk_bf16_f32 v143, v46, v47\n\t" \
  STA("s_memrealtime s[94:95]\n\t") \
  STA("s_memtime s[92:93]\n\t") \
  "v_mfma_f32_16x16x32_bf16 v[0:3], a[192:195], v[16:19], v[172:175]\n\t" \
  "ds_read_b64_tr_b16 a[128:129], v210 offset:0\n\t" \
  "v_cvt_pk_bf16_f32 v144, v64, v65\n\t" \
  "v_mfma_f32_16x16x32_bf16 v[0:3], a[196:199], v[20:23], v[0:3]\n\t" \
  "v_exp_f32 v79, v79\n\t" \
  "v_mfma_f32_16x16x32_bf16 v[0:3], a[200:203], v[24:27], v[0:3]\n\t" \
  "s_add_u32 m0, s38, 131072\n\t" \
  "ds_read_b64_tr_b16 a[130:131], v210 offset:4096\n\t" \
  "v_cvt_pk_bf16_f32 v145, v66, v67\n\t" \
  "v_mfma_f32_16x16x32_bf16 v[0:3], a[204:207], v[28:31], v[0:3]\n\t" \
  "s_waitcnt lgkmcnt(10)\n\t" \
  "v_exp_f32 v96, v96\n\t" \
  "v_mfma_f32_16x16x32_bf16 v[4:7], a[220:223], v[28:31], v[172:175]\n\t" \
  "s_mov_b32 s96, 28672\n\t" \
  "ds_read_b64_tr_b16 a[132:133], v210 offset:8192\n\t" \
  "v_cvt_pk_bf16_f32 v146, v68, v69\n\t" \
  "v_mfma_f32_16x16x32_bf16 v[4:7], a[216:219], v[24:27], v[4:7]\n\t" \
  "v_exp_f32 v97, v97\n\t" \
  "v_mfma_f32_16x16x32_bf16 v[4:7], a[212:215], v[20:23], v[4:7]\n\t" \
  "buffer_load_dwordx4 v232, s[72:75], s96 offen offset:0 lds\n\t" \
  "v_mfma_f32_16x16x32_bf16 v[4:7], a[208:211], v[16:19], v[4:7]\n\t" \
  "s_waitcnt lgkmcnt(7)\n\t" \
  "ds_read_b64_tr_b16 a[134:135], v210 offset:12288\n\t" \
  "v_cvt_pk_bf16_f32 v147, v70, v71\n\t" \
  "v_mfma_f32_16x16x32_bf16 v[8:11], a[224:227], v[16:19], v[172:175]\n\t" \
  "ds_read_b64_tr_b16 a[136:137], v211 offset:0\n\t" \
  "v_cvt_pk_bf16_f32 v148, v72, v73\n\t" \
  "v_mfma_f32_16x16x32_bf16 v[8:11], a[228:231], v[20:23], v[8:11]\n\t" \
  "ds_read_b64_tr_b16 a[138:139], v211 offset:4096\n\t" \
  "v_cvt_pk_bf16_f32 v149, v74, v75\n\t" \
  "v_mfma_f32_16x16x32_bf16 v[8:11], a[232:235], v[24:27], v[8:11]\n\t" \
  "v_exp_f32 v98, v98\n\t" \
  "v_mfma_f32_16x16x32_bf16 v[8:11], a[236:239], v[28:31], v[8:11]\n\t" \
  "s_waitcnt lgkmcnt(6)\n\t" \
  "ds_read_b64_tr_b16 a[140:141], v211 offset:8192\n\t" \
  "v_cvt_pk_bf16_f32 v150, v76, v77\n\t" \
  "v_mfma_f32_16x16x32_bf16 v[12:15], a[252:255], v[28:31], v[172:175]\n\t" \
  STA("v_writelane_b32 v255, s94, 38\n\t") \
  STA("v_writelane_b32 v255, s92, 39\n\t") \
  "v_exp_f32 v99, v99\n\t" \
  "v_mfma_f32_16x16x32_bf16 v[12:15], a[248:251], v[24:27], v[12:15]\n\t" \
  "ds_read_b64_tr_b16 a[142:143], v211 offset:12288\n\t" \
  "v_cvt_pk_bf16_f32 v151, v78, v79\n\t" \
  "v_mfma_f32_16x16x32_bf16 v[12:15], a[244:247], v[20:23], v[12:15]\n\t" \
  "ds_read_b64_tr_b16 a[144:145], v210 offset:256\n\t" \
  "v_cvt_pk_bf16_f32 v152, v96, v97\n\t" \
  "v_mfma_f32_16x16x32_bf16 v[12:15], a[240:243], v[16:19], v[12:15]\n\t" \
  "v_exp_f32 v100, v100\n\t" \
  "v_mfma_f32_16x16x32_bf16 v[32:35], a[192:195], v[48:51], v[172:175]\n\t" \
  "s_mov_b32 s96, 34816\n\t" \
  "ds_read_b64_tr_b16 a[146:147], v210 offset:4352\n\t" \
  "v_cvt_pk_bf16_f32 v153, v98, v99\n\t" \
  "v_mfma_f32_16x16x32_bf16 v[32:35], a[196:199], v[52:55], v[32:35]\n\t" \
  "v_exp_f32 v101, v101\n\t" \
  "v_mfma_f32_16x16x32_bf16 v[32:35], a[200:203], v[56:59], v[32:35]\n\t" \
  "ds_read_b64_tr_b16 a[148:149], v210 offset:8448\n\t" \
  "v_cvt_pk_bf16_f32 v154, v100, v101\n\t" \
  "v_mfma_f32_16x16x32_bf16 v[32:35], a[204:207], v[60:63], v[32:35]\n\t" \
  "v_exp_f32 v102, v102\n\t" \
  "v_mfma_f32_16x16x32_bf16 v[36:39], a[220:223], v[60:63], v[172:175]\n\t" \
  "buffer_load_dwordx4 v232, s[72:75], s96 offen offset:1024 lds\n\t" \
  "v_mfma_f32_16x16x32_bf16 v[36:39], a[216:219], v[56:59], v[36:39]\n\t" \
  "v_exp_f32 v103, v103\n\t" \
  "v_mfma_f32_16x16x32_bf16 v[36:39], a[212:215], v[52:55], v[36:39]\n\t" \
  "ds_read_b64_tr_b16 a[150:151], v210 offset:12544\n\t" \
  "v_cvt_pk_bf16_f32 v155, v102, v103\n\t" \
  "v_mfma_f32_16x16x32_bf16 v[36:39], a[208:211], v[48:51], v[36:39]\n\t" \
  "v_exp_f32 v104, v104\n\t" \
  "v_mfma_f32_16x16x32_bf16 v[40:43], a[224:227], v[48:51], v[172:175]\n\t" \
  "v_exp_f32 v105, v105\n\t" \
  "v_mfma_f32_16x16x32_bf16 v[40:43], a[228:231], v[52:55], v[40:43]\n\t" \
  "ds_read_b64_tr_b16 a[152:153], v211 offset:256\n\t" \
  "v_cvt_pk_bf16_f32 v156, v104, v105\n\t" \
  "v_mfma_f32_16x16x32_bf16 v[40:43], a[232:235], v[56:59], v[40:43]\n\t" \
  "v_exp_f32 v106, v106\n\t" \
  "v_mfma_f32_16x16x32_bf16 v[40:43], a[236:239], v[60:63], v[40:43]\n\t" \
  "v_exp_f32 v107, v107\n\t" \
  "v_mfma_f32_16x16x32_bf16 v[44:47], a[252:255], v[60:63], v[172:175]\n\t" \
  "ds_read_b64_tr_b16 a[154:155], v211 offset:4352\n\t" \
  "v_cvt_pk_bf16_f32 v157, v106, v107\n\t" \
  "v_mfma_f32_16x16x32_bf16 v[44:47], a[248:251], v[56:59], v[44:47]\n\t" \
  "v_exp_f32 v108, v108\n\t" \
  "v_mfma_f32_16x16x32_bf16 v[44:47], a[244:247], v[52:55], v[44:47]\n\t" \
  "ds_read_b64_tr_b16 a[156:157], v211 offset:8448\n\t" \
  "s_mov_b32 s96, 40960\n\t" \
  "v_mfma_f32_16x16x32_bf16 v[44:47], a[240:243], v[48:51], v[44:47]\n\t" \
  "v_exp_f32 v109, v109\n\t" \
  "v_mfma_f32_16x16x32_bf16 v[64:67], a[192:195], v[80:83], v[172:175]\n\t" \
  "ds_read_b64_tr_b16 a[158:159], v211 offset:12544\n\t" \
  "v_cvt_pk_bf16_f32 v158, v108, v109\n\t" \
  "v_mfma_f32_16x16x32_bf16 v[64:67], a[196:199], v[84:87], v[64:67]\n\t" \
  "v_exp_f32 v110, v110\n\t" \
  "v_mfma_f32_16x16x32_bf16 v[64:67], a[200:203], v[88:91], v[64:67]\n\t" \
  "buffer_load_dwordx4 v232, s[72:75], s96 offen offset:2048 lds\n\t" \
  "v_mfma_f32_16x16x32_bf16 v[64:67], a[204:207], v[92:95], v[64:67]\n\t" \
  "ds_read_b64_tr_b16 a[160:161], v210 offset:512\n\t" \
  "v_mfma_f32_16x16x32_bf16 v[68:71], a[220:223], v[92:95], v[172:175]\n\t" \
  "ds_read_b64_tr_b16 a[162:163], v210 offset:4608\n\t" \
  "v_mfma_f32_16x16x32_bf16 v[68:71], a[216:219], v[88:91], v[68:71]\n\t" \
  "v_exp_f32 v111, v111\n\t" \
  "v_mfma_f32_16x16x32_bf16 v[68:71], a[212:215], v[84:87], v[68:71]\n\t" \
  "ds_read_b64_tr_b16 a[164:165], v210 offset:8704\n\t" \
  "v_cvt_pk_bf16_f32 v159, v110, v111\n\t" \
  "v_mfma_f32_16x16x32_bf16 v[68:71], a[208:211], v[80:83], v[68:71]\n\t" \
  "ds_read_b64_tr_b16 a[166:167], v210 offset:12800\n\t" \
  "v_mfma_f32_16x16x32_bf16 v[72:75], a[224:227], v[80:83], v[172:175]\n\t" \
  "ds_read_b64_tr_b16 a[168:169], v211 offset:512\n\t" \
  "v_mfma_f32_16x16x32_bf16 v[72:75], a[228:231], v[84:87], v[72:75]\n\t" \
  "ds_read_b64_tr_b16 a[170:171], v211 offset:4608\n\t" \
  "v_mfma_f32_16x16x32_bf16 v[72:75], a[232:235], v[88:91], v[72:75]\n\t" \
  "ds_read_b64_tr_b16 a[172:173], v211 offset:8704\n\t" \
  "v_mfma_f32_16x16x32_bf16 v[72:75], a[236:239], v[92:95], v[72:75]\n\t" \
  "ds_read_b64_tr_b16 a[174:175], v211 offset:12800\n\t" \
  "v_mfma_f32_16x16x32_bf16 v[76:79], a[252:255], v[92:95], v[172:175]\n\t" \
  "s_mov_b32 s96, 47104\n\t" \
  "v_exp_f32 v0, v0\n\t" \
  "v_mfma_f32_16x16x32_bf16 v[76:79], a[248:251], v[88:91], v[76:79]\n\t" \
  "ds_read_b64_tr_b16 a[176:177], v210 offset:768\n\t" \
  "v_mfma_f32_16x16x32_bf16 v[76:79], a[244:247], v[84:87], v[76:79]\n\t" \
  "v_exp_f32 v1, v1\n\t" \
  "v_mfma_f32_16x16x32_bf16 v[76:79], a[240:243], v[80:83], v[76:79]\n\t" \
  "ds_read_b64_tr_b16 a[178:179], v210 offset:4864\n\t" \
  "v_mfma_f32_16x16x32_bf16 v[96:99], a[192:195], v[112:115], v[172:175]\n\t" \
  "buffer_load_dwordx4 v232, s[72:75], s96 offen offset:3072 lds\n\t" \
  "v_mfma_f32_16x16x32_bf16 v[96:99], a[196:199], v[116:119], v[96:99]\n\t" \
  "ds_read_b64_tr_b16 a[180:181], v210 offset:8960\n\t" \
  "v_mfma_f32_16x16x32_bf16 v[96:99], a[200:203], v[120:123], v[96:99]\n\t" \
  "ds_read_b64_tr_b16 a[182:183], v210 offset:13056\n\t" \
  "v_mfma_f32_16x16x32_bf16 v[96:99], a[204:207], v[124:127], v[96:99]\n\t" \
  "s_add_u32 s99, s52, 0\n\t" \
  "v_exp_f32 v2, v2\n\t" \
  "v_mfma_f32_16x16x32_bf16 v[100:103], a[220:223], v[124:127], v[172:175]\n\t" \
  "ds_read_b64_tr_b16 a[184:185], v211 offset:768\n\t" \
  "v_mfma_f32_16x16x32_bf16 v[100:103], a[216:219], v[120:123], v[100:103]\n\t" \
  "ds_read_b64_tr_b16 a[186:187], v211 offset:4864\n\t" \
  "v_mfma_f32_16x16x32_bf16 v[100:103], a[212:215], v[116:119], v[100:103]\n\t" \
  "buffer_load_dword v246, v212, s[24:27], s99 offen\n\t" \
  "v_mfma_f32_16x16x32_bf16 v[100:103], a[208:211], v[112:115], v[100:103]\n\t" \
  "ds_read_b64_tr_b16 a[188:189], v211 offset:8960\n\t" \
  "v_mfma_f32_16x16x32_bf16 v[104:107], a[224:227], v[112:115], v[172:175]\n\t" \
  "v_exp_f32 v3, v3\n\t" \
  "v_mfma_f32_16x16x32_bf16 v[104:107], a[228:231], v[116:119], v[104:107]\n\t" \
  "ds_read_b64_tr_b16 a[190:191], v211 offset:13056\n\t" \
  "v_mfma_f32_16x16x32_bf16 v[104:107], a[232:235], v[120:123], v[104:107]\n\t" \
  "v_exp_f32 v4, v4\n\t" \
  "v_mfma_f32_16x16x32_bf16 v[104:107], a[236:239], v[124:127], v[104:107]\n\t" \
  "v_exp_f32 v5, v5\n\t" \
  "v_mfma_f32_16x16x32_bf16 v[108:111], a[252:255], v[124:127], v[172:175]\n\t" \
  "v_exp_f32 v6, v6\n\t" \
  "v_mfma_f32_16x16x32_bf16 v[108:111], a[248:251], v[120:123], v[108:111]\n\t" \
  "v_exp_f32 v7, v7\n\t" \
  "v_mfma_f32_16x16x32_bf16 v[108:111], a[244:247], v[116:119], v[108:111]\n\t" \
  "s_waitcnt vmcnt(5) lgkmcnt(0)\n\t" \
  "v_exp_f32 v8, v8\n\t" \
  "v_mfma_f32_16x16x32_bf16 v[108:111], a[240:243], v[112:115], v[108:111]\n\t" \
  "s_barrier\n\t" \
  STA("s_memrealtime s[94:95]\n\t") \
  STA("s_memtime s[92:93]\n\t") \
  "v_mfma_f32_16x16x32_bf16 a[0:3], a[128:131], v[128:131], a[0:3]\n\t" \
  "v_exp_f32 v9, v9\n\t" \
  "v_mfma_f32_16x16x32_bf16 a[0:3], a[132:135], v[132:135], a[0:3]\n\t" \
  "v_exp_f32 v10, v10\n\t" \
  "v_mfma_f32_16x16x32_bf16 a[4:7], a[136:139], v[128:131], a[4:7]\n\t" \
  "v_exp_f32 v11, v11\n\t" \
  "v_mfma_f32_16x16x32_bf16 a[4:7], a[140:143], v[132:135], a[4:7]\n\t" \
  "v_exp_f32 v12, v12\n\t" \
  "v_mfma_f32_16x16x32_bf16 a[8:11], a[144:147], v[128:131], a[8:11]\n\t" \
  "v_exp_f32 v13, v13\n\t" \
  "v_mfma_f32_16x16x32_bf16 a[8:11], a[148:151], v[132:135], a[8:11]\n\t" \
  "v_exp_f32 v14, v14\n\t" \
  "v_mfma_f32_16x16x32_bf16 a[12:15], a[152:155], v[128:131], a[12:15]\n\t" \
  "v_exp_f32 v15, v15\n\t" \
  "v_mfma_f32_16x16x32_bf16 a[12:15], a[156:159], v[132:135], a[12:15]\n\t" \
  "v_exp_f32 v32, v32\n\t" \
  "v_mfma_f32_16x16x32_bf16 a[16:19], a[160:163], v[128:131], a[16:19]\n\t" \
  "v_exp_f32 v33, v33\n\t" \
  "v_mfma_f32_16x16x32_bf16 a[16:19], a[164:167], v[132:135], a[16:19]\n\t" \
  "v_exp_f32 v34, v34\n\t" \
  "v_mfma_f32_16x16x32_bf16 a[20:23], a[168:171], v[128:131], a[20:23]\n\t" \
  "v_exp_f32 v35, v35\n\t" \
  "v_mfma_f32_16x16x32_bf16 a[20:23], a[172:175], v[132:135], a[20:23]\n\t" \
  "v_exp_f32 v36, v36\n\t" \
  "v_mfma_f32_16x16x32_bf16 a[24:27], a[176:179], v[128:131], a[24:27]\n\t" \
  STA("v_writelane_b32 v255, s94, 40\n\t") \
  STA("v_writelane_b32 v255, s92, 41\n\t") \
  "v_exp_f32 v37, v37\n\t" \
  "v_mfma_f32_16x16x32_bf16 a[24:27], a[180:183], v[132:135], a[24:27]\n\t" \
  "v_exp_f32 v38, v38\n\t" \
  "v_mfma_f32_16x16x32_bf16 a[28:31], a[184:187], v[128:131], a[28:31]\n\t" \
  "v_exp_f32 v39, v39\n\t" \
  "v_mfma_f32_16x16x32_bf16 a[28:31], a[188:191], v[132:135], a[28:31]\n\t" \
  "v_exp_f32 v40, v40\n\t" \
  "v_smfmac_f32_16x16x64_bf16 v[160:163], v[216:219], v[128:135], v208\n\t" \
  "s_add_u32 m0, s38, 49152\n\t" \
  "ds_read_b128 a[192:195], v220 offset:16384\n\t" \
  "v_cvt_pk_bf16_f32 v128, v0, v1\n\t" \
  "v_mfma_f32_16x16x32_bf16 a[32:35], a[128:131], v[136:139], a[32:35]\n\t" \
  "s_add_u32 s96, s38, 98304\n\t" \
  "ds_read_b128 a[196:199], v221 offset:16384\n\t" \
  "v_cvt_pk_bf16_f32 v129, v2, v3\n\t" \
  "v_mfma_f32_16x16x32_bf16 a[32:35], a[132:135], v[140:143], a[32:35]\n\t" \
  "v_exp_f32 v41, v41\n\t" \
  "v_mfma_f32_16x16x32_bf16 a[36:39], a[136:139], v[136:139], a[36:39]\n\t" \
  "v_exp_f32 v42, v42\n\t" \
  "v_mfma_f32_16x16x32_bf16 a[36:39], a[140:143], v[140:143], a[36:39]\n\t" \
  "buffer_load_dwordx4 v224, s[60:63], s96 offen offset:0 lds\n\t" \
  "v_mfma_f32_16x16x32_bf16 a[40:43], a[144:147], v[136:139], a[40:43]\n\t" \
  "ds_read_b128 a[200:203], v222 offset:16384\n\t" \
  "v_cvt_pk_bf16_f32 v130, v4, v5\n\t" \
  "v_mfma_f32_16x16x32_bf16 a[40:43], a[148:151], v[140:143], a[40:43]\n\t" \
  "v_exp_f32 v43, v43\n\t" \
  "v_mfma_f32_16x16x32_bf16 a[44:47], a[152:155], v[136:139], a[44:47]\n\t" \
  "ds_read_b128 a[204:207], v223 offset:16384\n\t" \
  "v_cvt_pk_bf16_f32 v131, v6, v7\n\t" \
  "v_mfma_f32_16x16x32_bf16 a[44:47], a[156:159], v[140:143], a[44:47]\n\t" \
  "v_exp_f32 v44, v44\n\t" \
  "v_mfma_f32_16x16x32_bf16 a[48:51], a[160:163], v[136:139], a[48:51]\n\t" \
  "ds_read_b128 a[208:211], v220 offset:20480\n\t" \
  "v_cvt_pk_bf16_f32 v132, v8, v9\n\t" \
  "v_mfma_f32_16x16x32_bf16 a[48:51], a[164:167], v[140:143], a[48:51]\n\t" \
  "buffer_load_dwordx4 v225, s[60:63], s96 offen offset:1024 lds\n\t" \
  "v_mfma_f32_16x16x32_bf16 a[52:55], a[168:171], v[136:139], a[52:55]\n\t" \
  "v_exp_f32 v45, v45\n\t" \
  "v_mfma_f32_16x16x32_bf16 a[52:55], a[172:175], v[140:143], a[52:55]\n\t" \
  "ds_read_b128 a[212:215], v221 offset:20480\n\t" \
  "v_cvt_pk_bf16_f32 v133, v10, v11\n\t" \
  "v_mfma_f32_16x16x32_bf16 a[56:59], a[176:179], v[136:139], a[56:59]\n\t" \
  "v_exp_f32 v46, v46\n\t" \
  "v_mfma_f32_16x16x32_bf16 a[56:59], a[180:183], v[140:143], a[56:59]\n\t" \
  "v_exp_f32 v47, v47\n\t" \
  "v_mfma_f32_16x16x32_bf16 a[60:63], a[184:187], v[136:139], a[60:63]\n\t" \
  "buffer_load_dwordx4 v226, s[60:63], s96 offen offset:2048 lds\n\t" \
  "v_mfma_f32_16x16x32_bf16 a[60:63], a[188:191], v[140:143], a[60:63]\n\t" \
  "ds_read_b128 a[216:219], v222 offset:20480\n\t" \
  "v_cvt_pk_bf16_f32 v134, v12, v13\n\t" \
  "v_smfmac_f32_16x16x64_bf16 v[162:165], v[216:219], v[136:143], v208\n\t" \
  "ds_read_b128 a[220:223], v223 offset:20480\n\t" \
  "v_cvt_pk_bf16_f32 v135, v14, v15\n\t" \
  "v_mfma_f32_16x16x32_bf16 a[64:67], a[128:131], v[144:147], a[64:67]\n\t" \
  "v_exp_f32 v64, v64\n\t" \
  "v_mfma_f32_16x16x32_bf16 a[64:67], a[132:135], v[148:151], a[64:67]\n\t" \
  "v_exp_f32 v65, v65\n\t" \
  "v_mfma_f32_16x16x32_bf16 a[68:71], a[136:139], v[144:147], a[68:71]\n\t" \
  "ds_read_b128 a[224:227], v220 offset:24576\n\t" \
  "v_cvt_pk_bf16_f32 v136, v32, v33\n\t" \
  "v_mfma_f32_16x16x32_bf16 a[68:71], a[140:143], v[148:151], a[68:71]\n\t" \
  "buffer_load_dwordx4 v227, s[60:63], s96 offen offset:3072 lds\n\t" \
  "v_mfma_f32_16x16x32_bf16 a[72:75], a[144:147], v[144:147], a[72:75]\n\t" \
  "s_add_u32 m0, s38, 147456\n\t" \
  "v_exp_f32 v66, v66\n\t" \
  "v_mfma_f32_16x16x32_bf16 a[72:75], a[148:151], v[148:151], a[72:75]\n\t" \
  "s_add_u32 s96, s38, 81920\n\t" \
  "ds_read_b128 a[228:231], v221 offset:24576\n\t" \
  "v_cvt_pk_bf16_f32 v137, v34, v35\n\t" \
  "v_mfma_f32_16x16x32_bf16 a[76:79], a[152:155], v[144:147], a[76:79]\n\t" \
  "v_exp_f32 v67, v67\n\t" \
  "v_mfma_f32_16x16x32_bf16 a[76:79], a[156:159], v[148:151], a[76:79]\n\t" \
  "v_exp_f32 v68, v68\n\t" \
  "v_mfma_f32_16x16x32_bf16 a[80:83], a[160:163], v[144:147], a[80:83]\n\t" \
  "buffer_load_dwordx4 v230, s[64:67], s96 offen offset:0 lds\n\t" \
  "v_mfma_f32_16x16x32_bf16 a[80:83], a[164:167], v[148:151], a[80:83]\n\t" \
  "ds_read_b128 a[232:235], v222 offset:24576\n\t" \
  "v_cvt_pk_bf16_f32 v138, v36, v37\n\t" \
  "v_mfma_f32_16x16x32_bf16 a[84:87], a[168:171], v[144:147], a[84:87]\n\t" \
  "v_exp_f32 v69, v69\n\t" \
  "v_mfma_f32_16x16x32_bf16 a[84:87], a[172:175], v[148:151], a[84:87]\n\t" \
  "ds_read_b128 a[236:239], v223 offset:24576\n\t" \
  "v_cvt_pk_bf16_f32 v139, v38, v39\n\t" \
  "v_mfma_f32_16x16x32_bf16 a[88:91], a[176:179], v[144:147], a[88:91]\n\t" \
  "v_exp_f32 v70, v70\n\t" \
  "v_mfma_f32_16x16x32_bf16 a[88:91], a[180:183], v[148:151], a[88:91]\n\t" \
  "ds_read_b128 a[240:243], v220 offset:28672\n\t" \
  "v_cvt_pk_bf16_f32 v140, v40, v41\n\t" \
  "v_mfma_f32_16x16x32_bf16 a[92:95], a[184:187], v[144:147], a[92:95]\n\t" \
  "buffer_load_dwordx4 v231, s[64:67], s96 offen offset:1024 lds\n\t" \
  "v_mfma_f32_16x16x32_bf16 a[92:95], a[188:191], v[148:151], a[92:95]\n\t" \
  "v_exp_f32 v71, v71\n\t" \
  "v_smfmac_f32_16x16x64_bf16 v[164:167], v[216:219], v[144:151], v208\n\t" \
  "ds_read_b128 a[244:247], v221 offset:28672\n\t" \
  "v_cvt_pk_bf16_f32 v141, v42, v43\n\t" \
  "v_mfma_f32_16x16x32_bf16 a[96:99], a[128:131], v[152:155], a[96:99]\n\t" \
  "v_exp_f32 v72, v72\n\t" \
  "v_mfma_f32_16x16x32_bf16 a[96:99], a[132:135], v[156:159], a[96:99]\n\t" \
  "v_exp_f32 v73, v73\n\t" \
  "v_mfma_f32_16x16x32_bf16 a[100:103], a[136:139], v[152:155], a[100:103]\n\t" \
  "buffer_load_dwordx4 v230, s[64:67], s96 offen offset:2048 lds\n\t" \
  "v_mfma_f32_16x16x32_bf16 a[100:103], a[140:143], v[156:159], a[100:103]\n\t" \
  "ds_read_b128 a[248:251], v222 offset:28672\n\t" \
  "v_cvt_pk_bf16_f32 v142, v44, v45\n\t" \
  "v_mfma_f32_16x16x32_bf16 a[104:107], a[144:147], v[152:155], a[104:107]\n\t" \
  "v_exp_f32 v74, v74\n\t" \
  "v_mfma_f32_16x16x32_bf16 a[104:107], a[148:151], v[156:159], a[104:107]\n\t" \
  "ds_read_b128 a[252:255], v223 offset:28672\n\t" \
  "v_cvt_pk_bf16_f32 v143, v46, v47\n\t" \
  "v_mfma_f32_16x16x32_bf16 a[108:111], a[152:155], v[152:155], a[108:111]\n\t" \
  "v_exp_f32 v75, v75\n\t" \
  "v_mfma_f32_16x16x32_bf16 a[108:111], a[156:159], v[156:159], a[108:111]\n\t" \
  "v_exp_f32 v76, v76\n\t" \
  "v_mfma_f32_16x16x32_bf16 a[112:115], a[160:163], v[152:155], a[112:115]\n\t" \
  "buffer_load_dwordx4 v231, s[64:67], s96 offen offset:3072 lds\n\t" \
  "v_mfma_f32_16x16x32_bf16 a[112:115], a[164:167], v[156:159], a[112:115]\n\t" \
  "v_exp_f32 v77, v77\n\t" \
  "v_mfma_f32_16x16x32_bf16 a[116:119], a[168:171], v[152:155], a[116:119]\n\t" \
  "v_exp_f32 v78, v78\n\t" \
  "v_mfma_f32_16x16x32_bf16 a[116:119], a[172:175], v[156:159], a[116:119]\n\t" \
  "v_exp_f32 v79, v79\n\t" \
  "v_mfma_f32_16x16x32_bf16 a[120:123], a[176:179], v[152:155], a[120:123]\n\t" \
  "s_add_u32 s99, s52, 2048\n\t" \
  "v_exp_f32 v96, v96\n\t" \
  "v_mfma_f32_16x16x32_bf16 a[120:123], a[180:183], v[156:159], a[120:123]\n\t" \
  "v_cvt_pk_bf16_f32 v144, v64, v65\n\t" \
  "v_cvt_pk_bf16_f32 v145, v66, v67\n\t" \
  "v_mfma_f32_16x16x32_bf16 a[124:127], a[184:187], v[152:155], a[124:127]\n\t" \
  "v_exp_f32 v97, v97\n\t" \
  "v_mfma_f32_16x16x32_bf16 a[124:127], a[188:191], v[156:159], a[124:127]\n\t" \
  "buffer_load_dword v246, v212, s[24:27], s99 offen\n\t" \
  "v_smfmac_f32_16x16x64_bf16 v[166:169], v[216:219], v[152:159], v208\n\t" \
  "s_waitcnt lgkmcnt(12)\n\t" \
  "v_cvt_pk_bf16_f32 v146, v68, v69\n\t" \
  STA("s_memrealtime s[94:95]\n\t") \
  STA("s_memtime s[92:93]\n\t") \
  "v_mfma_f32_16x16x32_bf16 v[0:3], a[192:195], v[16:19], v[172:175]\n\t" \
  "ds_read_b64_tr_b16 a[128:129], v228 offset:0\n\t" \
  "v_cvt_pk_bf16_f32 v147, v70, v71\n\t" \
  "v_mfma_f32_16x16x32_bf16 v[0:3], a[196:199], v[20:23], v[0:3]\n\t" \
  ".Lhs8:\n\ts_cbranch_execz .Lhs8n\n\t" \
  "v_exp_f32 v98, v98\n\t" \
  "v_mfma_f32_16x16x32_bf16 v[0:3], a[200:203], v[24:27], v[0:3]\n\t" \
  "ds_read_b64_tr_b16 a[130:131], v228 offset:4096\n\t" \
  "v_cvt_pk_bf16_f32 v148, v72, v73\n\t" \
  "v_mfma_f32_16x16x32_bf16 v[0:3], a[204:207], v[28:31], v[0:3]\n\t" \
  "s_waitcnt lgkmcnt(10)\n\t" \
  "v_exp_f32 v99, v99\n\t" \
  "v_mfma_f32_16x16x32_bf16 v[4:7], a[220:223], v[28:31], v[172:175]\n\t" \
  "ds_read_b64_tr_b16 a[132:133], v228 offset:8192\n\t" \
  "v_cvt_pk_bf16_f32 v149, v74, v75\n\t" \
  "v_mfma_f32_16x16x32_bf16 v[4:7], a[216:219], v[24:27], v[4:7]\n\t" \
  "v_exp_f32 v100, v100\n\t" \
  "v_mfma_f32_16x16x32_bf16 v[4:7], a[212:215], v[20:23], v[4:7]\n\t" \
  "ds_read_b64_tr_b16 a[134:135], v228 offset:12288\n\t" \
  "v_cvt_pk_bf16_f32 v150, v76, v77\n\t" \
  "v_mfma_f32_16x16x32_bf16 v[4:7], a[208:211], v[16:19], v[4:7]\n\t" \
  "s_waitcnt lgkmcnt(8)\n\t" \
  "ds_read_b64_tr_b16 a[136:137], v229 offset:0\n\t" \
  "v_cvt_pk_bf16_f32 v151, v78, v79\n\t" \
  "v_mfma_f32_16x16x32_bf16 v[8:11], a[224:227], v[16:19], v[172:175]\n\t" \
  "v_exp_f32 v101, v101\n\t" \
  "v_mfma_f32_16x16x32_bf16 v[8:11], a[228:231], v[20:23], v[8:11]\n\t" \
  "s_add_u32 s99, s52, 4096\n\t" \
  "ds_read_b64_tr_b16 a[138:139], v229 offset:4096\n\t" \
  "v_cvt_pk_bf16_f32 v152, v96, v97\n\t" \
  "v_mfma_f32_16x16x32_bf16 v[8:11], a[232:235], v[24:27], v[8:11]\n\t" \
  "v_exp_f32 v102, v102\n\t" \
  "v_mfma_f32_16x16x32_bf16 v[8:11], a[236:239], v[28:31], v[8:11]\n\t" \
  "s_waitcnt lgkmcnt(6)\n\t" \
  "ds_read_b64_tr_b16 a[140:141], v229 offset:8192\n\t" \
  "v_cvt_pk_bf16_f32 v153, v98, v99\n\t" \
  "v_mfma_f32_16x16x32_bf16 v[12:15], a[252:255], v[28:31], v[172:175]\n\t" \
  STA("v_writelane_b32 v255, s94, 42\n\t") \
  STA("v_writelane_b32 v255, s92, 43\n\t") \
  "buffer_load_dword v246, v212, s[24:27], s99 offen\n\t" \
  "v_mfma_f32_16x16x32_bf16 v[12:15], a[248:251], v[24:27], v[12:15]\n\t" \
  "ds_read_b64_tr_b16 a[142:143], v229 offset:12288\n\t" \
  "v_cvt_pk_bf16_f32 v154, v100, v101\n\t" \
  "v_mfma_f32_16x16x32_bf16 v[12:15], a[244:247], v[20:23], v[12:15]\n\t" \
  "v_exp_f32 v103, v103\n\t" \
  "v_mfma_f32_16x16x32_bf16 v[12:15], a[240:243], v[16:19], v[12:15]\n\t" \
  "ds_read_b64_tr_b16 a[144:145], v228 offset:256\n\t" \
  "v_cvt_pk_bf16_f32 v155, v102, v103\n\t" \
  "v_mfma_f32_16x16x32_bf16 v[32:35], a[192:195], v[48:51], v[172:175]\n\t" \
  "v_exp_f32 v104, v104\n\t" \
  "v_mfma_f32_16x16x32_bf16 v[32:35], a[196:199], v[52:55], v[32:35]\n\t" \
  "v_exp_f32 v105, v105\n\t" \
  "v_mfma_f32_16x16x32_bf16 v[32:35], a[200:203], v[56:59], v[32:35]\n\t" \
  "ds_read_b64_tr_b16 a[146:147], v228 offset:4352\n\t" \
  "v_cvt_pk_bf16_f32 v156, v104, v105\n\t" \
  "v_mfma_f32_16x16x32_bf16 v[32:35], a[204:207], v[60:63], v[32:35]\n\t" \
  "v_exp_f32 v106, v106\n\t" \
  "v_mfma_f32_16x16x32_bf16 v[36:39], a[220:223], v[60:63], v[172:175]\n\t" \
  "v_exp_f32 v107, v107\n\t" \
  "v_mfma_f32_16x16x32_bf16 v[36:39], a[216:219], v[56:59], v[36:39]\n\t" \
  "ds_read_b64_tr_b16 a[148:149], v228 offset:8448\n\t" \
  "v_cvt_pk_bf16_f32 v157, v106, v107\n\t" \
  "v_mfma_f32_16x16x32_bf16 v[36:39], a[212:215], v[52:55], v[36:39]\n\t" \
  "v_exp_f32 v108, v108\n\t" \
  "v_mfma_f32_16x16x32_bf16 v[36:39], a[208:211], v[48:51], v[36:39]\n\t" \
  "v_exp_f32 v109, v109\n\t" \
  "v_mfma_f32_16x16x32_bf16 v[40:43], a[224:227], v[48:51], v[172:175]\n\t" \
  "ds_read_b64_tr_b16 a[150:151], v228 offset:12544\n\t" \
  "v_cvt_pk_bf16_f32 v158, v108, v109\n\t" \
  "v_mfma_f32_16x16x32_bf16 v[40:43], a[228:231], v[52:55], v[40:43]\n\t" \
  "v_exp_f32 v110, v110\n\t" \
  "v_mfma_f32_16x16x32_bf16 v[40:43], a[232:235], v[56:59], v[40:43]\n\t" \
  "ds_read_b64_tr_b16 a[152:153], v229 offset:256\n\t" \
  "v_mfma_f32_16x16x32_bf16 v[40:43], a[236:239], v[60:63], v[40:43]\n\t" \
  "v_exp_f32 v111, v111\n\t" \
  "v_mfma_f32_16x16x32_bf16 v[44:47], a[252:255], v[60:63], v[172:175]\n\t" \
  "ds_read_b64_tr_b16 a[154:155], v229 offset:4352\n\t" \
  "v_cvt_pk_bf16_f32 v159, v110, v111\n\t" \
  "v_mfma_f32_16x16x32_bf16 v[44:47], a[248:251], v[56:59], v[44:47]\n\t" \
  "ds_read_b64_tr_b16 a[156:157], v229 offset:8448\n\t" \
  "v_mfma_f32_16x16x32_bf16 v[44:47], a[244:247], v[52:55], v[44:47]\n\t" \
  "ds_read_b64_tr_b16 a[158:159], v229 offset:12544\n\t" \
  "v_mfma_f32_16x16x32_bf16 v[44:47], a[240:243], v[48:51], v[44:47]\n\t" \
  "v_exp_f32 v0, v0\n\t" \
  "v_mfma_f32_16x16x32_bf16 v[64:67], a[192:195], v[80:83], v[172:175]\n\t" \
  "ds_read_b64_tr_b16 a[160:161], v228 offset:512\n\t" \
  "v_mfma_f32_16x16x32_bf16 v[64:67], a[196:199], v[84:87], v[64:67]\n\t" \
  "ds_read_b64_tr_b16 a[162:163], v228 offset:4608\n\t" \
  "v_mfma_f32_16x16x32_bf16 v[64:67], a[200:203], v[88:91], v[64:67]\n\t" \
  "v_exp_f32 v1, v1\n\t" \
  "v_mfma_f32_16x16x32_bf16 v[64:67], a[204:207], v[92:95], v[64:67]\n\t" \
  "ds_read_b64_tr_b16 a[164:165], v228 offset:8704\n\t" \
  "v_mfma_f32_16x16x32_bf16 v[68:71], a[220:223], v[92:95], v[172:175]\n\t" \
  "v_exp_f32 v2, v2\n\t" \
  "v_mfma_f32_16x16x32_bf16 v[68:71], a[216:219], v[88:91], v[68:71]\n\t" \
  "ds_read_b64_tr_b16 a[166:167], v228 offset:12800\n\t" \
  "v_mfma_f32_16x16x32_bf16 v[68:71], a[212:215], v[84:87], v[68:71]\n\t" \
  "ds_read_b64_tr_b16 a[168:169], v229 offset:512\n\t" \
  "v_mfma_f32_16x16x32_bf16 v[68:71], a[208:211], v[80:83], v[68:71]\n\t" \
  "v_exp_f32 v3, v3\n\t" \
  "v_mfma_f32_16x16x32_bf16 v[72:75], a[224:227], v[80:83], v[172:175]\n\t" \
  "ds_read_b64_tr_b16 a[170:171], v229 offset:4608\n\t" \
  "v_mfma_f32_16x16x32_bf16 v[72:75], a[228:231], v[84:87], v[72:75]\n\t" \
  "v_exp_f32 v4, v4\n\t" \
  "v_mfma_f32_16x16x32_bf16 v[72:75], a[232:235], v[88:91], v[72:75]\n\t" \
  "ds_read_b64_tr_b16 a[172:173], v229 offset:8704\n\t" \
  "v_mfma_f32_16x16x32_bf16 v[72:75], a[236:239], v[92:95], v[72:75]\n\t" \
  "ds_read_b64_tr_b16 a[174:175], v229 offset:12800\n\t" \
  "v_mfma_f32_16x16x32_bf16 v[76:79], a[252:255], v[92:95], v[172:175]\n\t" \
  "v_exp_f32 v5, v5\n\t" \
  "v_mfma_f32_16x16x32_bf16 v[76:79], a[248:251], v[88:91], v[76:79]\n\t" \
  "ds_read_b64_tr_b16 a[176:177], v228 offset:768\n\t" \
  "v_mfma_f32_16x16x32_bf16 v[76:79], a[244:247], v[84:87], v[76:79]\n\t" \
  "ds_read_b64_tr_b16 a[178:179], v228 offset:4864\n\t" \
  "v_mfma_f32_16x16x32_bf16 v[76:79], a[240:243], v[80:83], v[76:79]\n\t" \
  "v_exp_f32 v6, v6\n\t" \
  "v_mfma_f32_16x16x32_bf16 v[96:99], a[192:195], v[112:115], v[172:175]\n\t" \
  "ds_read_b64_tr_b16 a[180:181], v228 offset:8960\n\t" \
  "v_mfma_f32_16x16x32_bf16 v[96:99], a[196:199], v[116:119], v[96:99]\n\t" \
  "v_exp_f32 v7, v7\n\t" \
  "v_mfma_f32_16x16x32_bf16 v[96:99], a[200:203], v[120:123], v[96:99]\n\t" \
  "ds_read_b64_tr_b16 a[182:183], v228 offset:13056\n\t" \
  "v_mfma_f32_16x16x32_bf16 v[96:99], a[204:207], v[124:127], v[96:99]\n\t" \
  "ds_read_b64_tr_b16 a[184:185], v229 offset:768\n\t" \
  "v_mfma_f32_16x16x32_bf16 v[100:103], a[220:223], v[124:127], v[172:175]\n\t" \
  "v_exp_f32 v8, v8\n\t" \
  "v_mfma_f32_16x16x32_bf16 v[100:103], a[216:219], v[120:123], v[100:103]\n\t" \
  "ds_read_b64_tr_b16 a[186:187], v229 offset:4864\n\t" \
  "v_mfma_f32_16x16x32_bf16 v[100:103], a[212:215], v[116:119], v[100:103]\n\t" \
  "ds_read_b64_tr_b16 a[188:189], v229 offset:8960\n\t" \
  "v_mfma_f32_16x16x32_bf16 v[100:103], a[208:211], v[112:115], v[100:103]\n\t" \
  "v_exp_f32 v9, v9\n\t" \
  "v_mfma_f32_16x16x32_bf16 v[104:107], a[224:227], v[112:115], v[172:175]\n\t" \
  "ds_read_b64_tr_b16 a[190:191], v229 offset:13056\n\t" \
  "v_mfma_f32_16x16x32_bf16 v[104:107], a[228:231], v[116:119], v[104:107]\n\t" \
  "v_exp_f32 v10, v10\n\t" \
  "v_mfma_f32_16x16x32_bf16 v[104:107], a[232:235], v[120:123], v[104:107]\n\t" \
  "v_exp_f32 v11, v11\n\t" \
  "v_mfma_f32_16x16x32_bf16 v[104:107], a[236:239], v[124:127], v[104:107]\n\t" \
  "v_exp_f32 v12, v12\n\t" \
  "v_mfma_f32_16x16x32_bf16 v[108:111], a[252:255], v[124:127], v[172:175]\n\t" \
  "v_exp_f32 v13, v13\n\t" \
  "v_mfma_f32_16x16x32_bf16 v[108:111], a[248:251], v[120:123], v[108:111]\n\t" \
  "v_exp_f32 v14, v14\n\t" \
  "v_mfma_f32_16x16x32_bf16 v[108:111], a[244:247], v[116:119], v[108:111]\n\t" \
  "s_waitcnt vmcnt(2) lgkmcnt(0)\n\t" \
  "v_exp_f32 v15, v15\n\t" \
  "v_mfma_f32_16x16x32_bf16 v[108:111], a[240:243], v[112:115], v[108:111]\n\t" \
  "s_barrier\n\t" \
  STA("s_memrealtime s[94:95]\n\t") \
  STA("s_memtime s[92:93]\n\t") \
  "v_mfma_f32_16x16x32_bf16 a[0:3], a[128:131], v[128:131], a[0:3]\n\t" \
  "v_exp_f32 v32, v32\n\t" \
  "v_mfma_f32_16x16x32_bf16 a[0:3], a[132:135], v[132:135], a[0:3]\n\t" \
  "v_exp_f32 v33, v33\n\t" \
  "v_mfma_f32_16x16x32_bf16 a[4:7], a[136:139], v[128:131], a[4:7]\n\t" \
  "v_exp_f32 v34, v34\n\t" \
  "v_mfma_f32_16x16x32_bf16 a[4:7], a[140:143], v[132:135], a[4:7]\n\t" \
  "v_exp_f32 v35, v35\n\t" \
  "v_mfma_f32_16x16x32_bf16 a[8:11], a[144:147], v[128:131], a[8:11]\n\t" \
  "v_exp_f32 v36, v36\n\t" \
  "v_mfma_f32_16x16x32_bf16 a[8:11], a[148:151], v[132:135], a[8:11]\n\t" \
  "v_exp_f32 v37, v37\n\t" \
  "v_mfma_f32_16x16x32_bf16 a[12:15], a[152:155], v[128:131], a[12:15]\n\t" \
  "v_exp_f32 v38, v38\n\t" \
  "v_mfma_f32_16x16x32_bf16 a[12:15], a[156:159], v[132:135], a[12:15]\n\t" \
  "v_exp_f32 v39, v39\n\t" \
  "v_mfma_f32_16x16x32_bf16 a[16:19], a[160:163], v[128:131], a[16:19]\n\t" \
  "buffer_load_dword v209, v251, s[16:19], 0 offen sc1\n\t" \
  "v_mfma_f32_16x16x32_bf16 a[16:19], a[164:167], v[132:135], a[16:19]\n\t" \
  "v_exp_f32 v40, v40\n\t" \
  "v_mfma_f32_16x16x32_bf16 a[20:23], a[168:171], v[128:131], a[20:23]\n\t" \
  "v_exp_f32 v41, v41\n\t" \
  "v_mfma_f32_16x16x32_bf16 a[20:23], a[172:175], v[132:135], a[20:23]\n\t" \
  "v_exp_f32 v42, v42\n\t" \
  "v_mfma_f32_16x16x32_bf16 a[24:27], a[176:179], v[128:131], a[24:27]\n\t" \
  STA("v_writelane_b32 v255, s94, 44\n\t") \
  STA("v_writelane_b32 v255, s92, 45\n\t") \
  "v_exp_f32 v43, v43\n\t" \
  "v_mfma_f32_16x16x32_bf16 a[24:27], a[180:183], v[132:135], a[24:27]\n\t" \
  "v_exp_f32 v44, v44\n\t" \
  "v_mfma_f32_16x16x32_bf16 a[28:31], a[184:187], v[128:131], a[28:31]\n\t" \
  "v_exp_f32 v45, v45\n\t" \
  "v_mfma_f32_16x16x32_bf16 a[28:31], a[188:191], v[132:135], a[28:31]\n\t" \
  "v_exp_f32 v46, v46\n\t" \
  "v_smfmac_f32_16x16x64_bf16 v[160:163], v[216:219], v[128:135], v208\n\t" \
  "s_add_u32 m0, s38, 65536\n\t" \
  "ds_read_b128 a[192:195], v220 offset:0\n\t" \
  "v_cvt_pk_bf16_f32 v128, v0, v1\n\t" \
  "v_mfma_f32_16x16x32_bf16 a[32:35], a[128:131], v[136:139], a[32:35]\n\t" \
  "s_add_u32 s96, s38, 114688\n\t" \
  "ds_read_b128 a[196:199], v221 offset:0\n\t" \
  "v_cvt_pk_bf16_f32 v129, v2, v3\n\t" \
  "v_mfma_f32_16x16x32_bf16 a[32:35], a[132:135], v[140:143], a[32:35]\n\t" \
  "v_exp_f32 v47, v47\n\t" \
  "v_mfma_f32_16x16x32_bf16 a[36:39], a[136:139], v[136:139], a[36:39]\n\t" \
  "v_exp_f32 v64, v64\n\t" \
  "v_mfma_f32_16x16x32_bf16 a[36:39], a[140:143], v[140:143], a[36:39]\n\t" \
  "buffer_load_dwordx4 v224, s[60:63], s96 offen offset:0 lds\n\t" \
  "v_mfma_f32_16x16x32_bf16 a[40:43], a[144:147], v[136:139], a[40:43]\n\t" \
  "ds_read_b128 a[200:203], v222 offset:0\n\t" \
  "v_cvt_pk_bf16_f32 v130, v4, v5\n\t" \
  "v_mfma_f32_16x16x32_bf16 a[40:43], a[148:151], v[140:143], a[40:43]\n\t" \
  "v_exp_f32 v65, v65\n\t" \
  "v_mfma_f32_16x16x32_bf16 a[44:47], a[152:155], v[136:139], a[44:47]\n\t" \
  "ds_read_b128 a[204:207], v223 offset:0\n\t" \
  "v_cvt_pk_bf16_f32 v131, v6, v7\n\t" \
  "v_mfma_f32_16x16x32_bf16 a[44:47], a[156:159], v[140:143], a[44:47]\n\t" \
  "v_exp_f32 v66, v66\n\t" \
  "v_mfma_f32_16x16x32_bf16 a[48:51], a[160:163], v[136:139], a[48:51]\n\t" \
  "ds_read_b128 a[208:211], v220 offset:4096\n\t" \
  "v_cvt_pk_bf16_f32 v132, v8, v9\n\t" \
  "v_mfma_f32_16x16x32_bf16 a[48:51], a[164:167], v[140:143], a[48:51]\n\t" \
  "buffer_load_dwordx4 v225, s[60:63], s96 offen offset:1024 lds\n\t" \
  "v_mfma_f32_16x16x32_bf16 a[52:55], a[168:171], v[136:139], a[52:55]\n\t" \
  "v_exp_f32 v67, v67\n\t" \
  "v_mfma_f32_16x16x32_bf16 a[52:55], a[172:175], v[140:143], a[52:55]\n\t" \
  "ds_read_b128 a[212:215], v221 offset:4096\n\t" \
  "v_cvt_pk_bf16_f32 v133, v10, v11\n\t" \
  "v_mfma_f32_16x16x32_bf16 a[56:59], a[176:179], v[136:139], a[56:59]\n\t" \
  "v_exp_f32 v68, v68\n\t" \
  "v_mfma_f32_16x16x32_bf16 a[56:59], a[180:183], v[140:143], a[56:59]\n\t" \
  "v_exp_f32 v69, v69\n\t" \
  "v_mfma_f32_16x16x32_bf16 a[60:63], a[184:187], v[136:139], a[60:63]\n\t" \
  "buffer_load_dwordx4 v226, s[60:63], s96 offen offset:2048 lds\n\t" \
  "v_mfma_f32_16x16x32_bf16 a[60:63], a[188:191], v[140:143], a[60:63]\n\t" \
  "ds_read_b128 a[216:219], v222 offset:4096\n\t" \
  "v_cvt_pk_bf16_f32 v134, v12, v13\n\t" \
  "v_smfmac_f32_16x16x64_bf16 v[162:165], v[216:219], v[136:143], v208\n\t" \
  "ds_read_b128 v[192:195], v215 offset:32768\n\t" \
  "v_cvt_pk_bf16_f32 v135, v14, v15\n\t" \
  "v_mfma_f32_16x16x32_bf16 a[64:67], a[128:131], v[144:147], a[64:67]\n\t" \
  "ds_read_b128 a[220:223], v223 offset:4096\n\t" \
  "v_cvt_pk_bf16_f32 v136, v32, v33\n\t" \
  "v_mfma_f32_16x16x32_bf16 a[64:67], a[132:135], v[148:151], a[64:67]\n\t" \
  "v_exp_f32 v70, v70\n\t" \
  "v_mfma_f32_16x16x32_bf16 a[68:71], a[136:139], v[144:147], a[68:71]\n\t" \
  "ds_read_b128 a[224:227], v220 offset:8192\n\t" \
  "v_cvt_pk_bf16_f32 v137, v34, v35\n\t" \
  "v_mfma_f32_16x16x32_bf16 a[68:71], a[140:143], v[148:151], a[68:71]\n\t" \
  "buffer_load_dwordx4 v227, s[60:63], s96 offen offset:3072 lds\n\t" \
  "v_mfma_f32_16x16x32_bf16 a[72:75], a[144:147], v[144:147], a[72:75]\n\t" \
  "s_add_u32 m0, s38, 81920\n\t" \
  "v_exp_f32 v71, v71\n\t" \
  "v_mfma_f32_16x16x32_bf16 a[72:75], a[148:151], v[148:151], a[72:75]\n\t" \
  "s_add_u32 s96, s38, 98304\n\t" \
  "ds_read_b128 a[228:231], v221 offset:8192\n\t" \
  "v_cvt_pk_bf16_f32 v138, v36, v37\n\t" \
  "v_mfma_f32_16x16x32_bf16 a[76:79], a[152:155], v[144:147], a[76:79]\n\t" \
  "v_exp_f32 v72, v72\n\t" \
  "v_mfma_f32_16x16x32_bf16 a[76:79], a[156:159], v[148:151], a[76:79]\n\t" \
  "v_exp_f32 v73, v73\n\t" \
  "v_mfma_f32_16x16x32_bf16 a[80:83], a[160:163], v[144:147], a[80:83]\n\t" \
  "buffer_load_dwordx4 v230, s[64:67], s96 offen offset:0 lds\n\t" \
  "v_mfma_f32_16x16x32_bf16 a[80:83], a[164:167], v[148:151], a[80:83]\n\t" \
  "ds_read_b128 a[232:235], v222 offset:8192\n\t" \
  "v_cvt_pk_bf16_f32 v139, v38, v39\n\t" \
  "v_mfma_f32_16x16x32_bf16 a[84:87], a[168:171], v[144:147], a[84:87]\n\t" \
  "v_exp_f32 v74, v74\n\t" \
  "v_mfma_f32_16x16x32_bf16 a[84:87], a[172:175], v[148:151], a[84:87]\n\t" \
  "ds_read_b128 a[236:239], v223 offset:8192\n\t" \
  "v_cvt_pk_bf16_f32 v140, v40, v41\n\t" \
  "v_mfma_f32_16x16x32_bf16 a[88:91], a[176:179], v[144:147], a[88:91]\n\t" \
  "v_exp_f32 v75, v75\n\t" \
  "v_mfma_f32_16x16x32_bf16 a[88:91], a[180:183], v[148:151], a[88:91]\n\t" \
  "ds_read_b128 a[240:243], v220 offset:12288\n\t" \
  "v_cvt_pk_bf16_f32 v141, v42, v43\n\t" \
  "v_mfma_f32_16x16x32_bf16 a[92:95], a[184:187], v[144:147], a[92:95]\n\t" \
  "buffer_load_dwordx4 v231, s[64:67], s96 offen offset:1024 lds\n\t" \
  "v_mfma_f32_16x16x32_bf16 a[92:95], a[188:191], v[148:151], a[92:95]\n\t" \
  "v_exp_f32 v76, v76\n\t" \
  "v_smfmac_f32_16x16x64_bf16 v[164:167], v[216:219], v[144:151], v208\n\t" \
  "ds_read_b128 a[244:247], v221 offset:12288\n\t" \
  "v_cvt_pk_bf16_f32 v142, v44, v45\n\t" \
  "v_mfma_f32_16x16x32_bf16 a[96:99], a[128:131], v[152:155], a[96:99]\n\t" \
  "v_exp_f32 v77, v77\n\t" \
  "v_mfma_f32_16x16x32_bf16 a[96:99], a[132:135], v[156:159], a[96:99]\n\t" \
  "v_exp_f32 v78, v78\n\t" \
  "v_mfma_f32_16x16x32_bf16 a[100:103], a[136:139], v[152:155], a[100:103]\n\t" \
  "buffer_load_dwordx4 v230, s[64:67], s96 offen offset:2048 lds\n\t" \
  "v_mfma_f32_16x16x32_bf16 a[100:103], a[140:143], v[156:159], a[100:103]\n\t" \
  "ds_read_b128 a[248:251], v222 offset:12288\n\t" \
  "v_cvt_pk_bf16_f32 v143, v46, v47\n\t" \
  "v_mfma_f32_16x16x32_bf16 a[104:107], a[144:147], v[152:155], a[104:107]\n\t" \
  "v_exp_f32 v79, v79\n\t" \
  "v_mfma_f32_16x16x32_bf16 a[104:107], a[148:151], v[156:159], a[104:107]\n\t" \
  "ds_read_b128 a[252:255], v223 offset:12288\n\t" \
  "v_cvt_pk_bf16_f32 v144, v64, v65\n\t" \
  "v_mfma_f32_16x16x32_bf16 a[108:111], a[152:155], v[152:155], a[108:111]\n\t" \
  "v_exp_f32 v96, v96\n\t" \
  "v_mfma_f32_16x16x32_bf16 a[108:111], a[156:159], v[156:159], a[108:111]\n\t" \
  "v_exp_f32 v97, v97\n\t" \
  "v_mfma_f32_16x16x32_bf16 a[112:115], a[160:163], v[152:155], a[112:115]\n\t" \
  "buffer_load_dwordx4 v231, s[64:67], s96 offen offset:3072 lds\n\t" \
  "v_mfma_f32_16x16x32_bf16 a[112:115], a[164:167], v[156:159], a[112:115]\n\t" \
  "v_cvt_pk_bf16_f32 v145, v66, v67\n\t" \
  "v_cvt_pk_bf16_f32 v146, v68, v69\n\t" \
  "v_mfma_f32_16x16x32_bf16 a[116:119], a[168:171], v[152:155], a[116:119]\n\t" \
  "v_exp_f32 v98, v98\n\t" \
  "v_mfma_f32_16x16x32_bf16 a[116:119], a[172:175], v[156:159], a[116:119]\n\t" \
  "v_exp_f32 v99, v99\n\t" \
  "v_mfma_f32_16x16x32_bf16 a[120:123], a[176:179], v[152:155], a[120:123]\n\t" \
  "s_add_u32 s99, s52, 6144\n\t" \
  "v_cvt_pk_bf16_f32 v147, v70, v71\n\t" \
  "v_cvt_pk_bf16_f32 v148, v72, v73\n\t" \
  "v_mfma_f32_16x16x32_bf16 a[120:123], a[180:183], v[156:159], a[120:123]\n\t" \
  "v_exp_f32 v100, v100\n\t" \
  "v_mfma_f32_16x16x32_bf16 a[124:127], a[184:187], v[152:155], a[124:127]\n\t" \
  "v_exp_f32 v101, v101\n\t" \
  "v_mfma_f32_16x16x32_bf16 a[124:127], a[188:191], v[156:159], a[124:127]\n\t" \
  "buffer_load_dword v246, v212, s[24:27], s99 offen\n\t" \
  "v_smfmac_f32_16x16x64_bf16 v[166:169], v[216:219], v[152:159], v208\n\t" \
  "s_waitcnt lgkmcnt(13)\n\t" \
  "ds_read_b128 v[196:199], v215 offset:33792\n\t" \
  "v_lshl_add_u32 v214, v233, 14, s41\n\t" \
  STA("s_memrealtime s[94:95]\n\t") \
  STA("s_memtime s[92:93]\n\t") \
  "v_mfma_f32_16x16x32_bf16 v[0:3], a[192:195], v[16:19], v[172:175]\n\t" \
  "ds_read_b64_tr_b16 a[128:129], v210 offset:0\n\t" \
  "v_min_u32 v214, .Lfz_z-.Lat_t1-4, v214\n\t" \
  "v_mfma_f32_16x16x32_bf16 v[0:3], a[196:199], v[20:23], v[0:3]\n\t" \
  "s_getpc_b64 s[84:85]\n\t" \
  ".Lat_t1:\n\t" \
  "v_exp_f32 v102, v102\n\t" \
  "v_mfma_f32_16x16x32_bf16 v[0:3], a[200:203], v[24:27], v[0:3]\n\t" \
  "global_load_dword v246, v214, s[84:85]\n\t" \
  "v_mfma_f32_16x16x32_bf16 v[0:3], a[204:207], v[28:31], v[0:3]\n\t" \
  "s_waitcnt lgkmcnt(10)\n\t" \
  "ds_read_b64_tr_b16 a[130:131], v210 offset:4096\n\t" \
  "v_cvt_pk_bf16_f32 v149, v74, v75\n\t" \
  "v_mfma_f32_16x16x32_bf16 v[4:7], a[220:223], v[28:31], v[172:175]\n\t" \
  "ds_read_b64_tr_b16 a[132:133], v210 offset:8192\n\t" \
  "v_cvt_pk_bf16_f32 v150, v76, v77\n\t" \
  "v_mfma_f32_16x16x32_bf16 v[4:7], a[216:219], v[24:27], v[4:7]\n\t" \
  "v_exp_f32 v103, v103\n\t" \
  "v_mfma_f32_16x16x32_bf16 v[4:7], a[212:215], v[20:23], v[4:7]\n\t" \
  "ds_read_b64_tr_b16 a[134:135], v210 offset:12288\n\t" \
  "v_cvt_pk_bf16_f32 v151, v78, v79\n\t" \
  "v_mfma_f32_16x16x32_bf16 v[4:7], a[208:211], v[16:19], v[4:7]\n\t" \
  "s_waitcnt lgkmcnt(9)\n\t" \
  "ds_read_b64_tr_b16 a[136:137], v211 offset:0\n\t" \
  "v_cvt_pk_bf16_f32 v152, v96, v97\n\t" \
  "v_mfma_f32_16x16x32_bf16 v[8:11], a[224:227], v[16:19], v[172:175]\n\t" \
  "v_exp_f32 v104, v104\n\t" \
  "v_mfma_f32_16x16x32_bf16 v[8:11], a[228:231], v[20:23], v[8:11]\n\t" \
  "ds_read_b64_tr_b16 a[138:139], v211 offset:4096\n\t" \
  "v_cvt_pk_bf16_f32 v153, v98, v99\n\t" \
  "v_mfma_f32_16x16x32_bf16 v[8:11], a[232:235], v[24:27], v[8:11]\n\t" \
  "v_exp_f32 v105, v105\n\t" \
  "v_mfma_f32_16x16x32_bf16 v[8:11], a[236:239], v[28:31], v[8:11]\n\t" \
  "s_waitcnt lgkmcnt(7)\n\t" \
  "ds_read_b64_tr_b16 a[140:141], v211 offset:8192\n\t" \
  "v_cvt_pk_bf16_f32 v154, v100, v101\n\t" \
  "v_mfma_f32_16x16x32_bf16 v[12:15], a[252:255], v[28:31], v[172:175]\n\t" \
  STA("v_writelane_b32 v255, s94, 46\n\t") \
  STA("v_writelane_b32 v255, s92, 47\n\t") \
  "v_exp_f32 v106, v106\n\t" \
  "v_mfma_f32_16x16x32_bf16 v[12:15], a[248:251], v[24:27], v[12:15]\n\t" \
  "ds_read_b64_tr_b16 a[142:143], v211 offset:12288\n\t" \
  "v_cvt_pk_bf16_f32 v155, v102, v103\n\t" \
  "v_mfma_f32_16x16x32_bf16 v[12:15], a[244:247], v[20:23], v[12:15]\n\t" \
  "ds_read_b64_tr_b16 a[144:145], v210 offset:256\n\t" \
  "v_cvt_pk_bf16_f32 v156, v104, v105\n\t" \
  "v_mfma_f32_16x16x32_bf16 v[12:15], a[240:243], v[16:19], v[12:15]\n\t" \
  "v_exp_f32 v107, v107\n\t" \
  "v_mfma_f32_16x16x32_bf16 v[32:35], a[192:195], v[48:51], v[172:175]\n\t" \
  "s_waitcnt vmcnt(10)\n\t" \
  "v_and_b32 v250, 0xbfffbfff, v209\n\t" \
  "v_cmp_ne_u32_e64 s[84:85], s36, v250\n\t" \
  "v_mfma_f32_16x16x32_bf16 v[32:35], a[196:199], v[52:55], v[32:35]\n\t" \
  "s_cmp_lg_u64 s[84:85], 0\n\t" \
  "s_cbranch_scc1 .Lat_v\n\t" \
  ".Lat_w:\n\t" \
  "v_mfma_f32_16x16x32_bf16 v[32:35], a[200:203], v[56:59], v[32:35]\n\t" \
  "v_and_b32 v250, 1, v233\n\t" \
  "v_lshlrev_b32 v250, 4, v250\n\t" \
  "v_mfma_f32_16x16x32_bf16 v[32:35], a[204:207], v[60:63], v[32:35]\n\t" \
  "v_lshrrev_b32 v250, v250, v209\n\t" \
  "v_and_b32 v250, 0x4000, v250\n\t" \
  "v_mfma_f32_16x16x32_bf16 v[36:39], a[220:223], v[60:63], v[172:175]\n\t" \
  "ds_read_b64_tr_b16 a[146:147], v210 offset:4352\n\t" \
  "v_cmp_ne_u32_e64 s[84:85], 0, v250\n\t" \
  STS("s_nop 4\n\t") \
  STS("v_writelane_b32 v255, s84, 60\n\t") \
  STS("v_writelane_b32 v255, s85, 61\n\t") \
  "v_mfma_f32_16x16x32_bf16 v[36:39], a[216:219], v[56:59], v[36:39]\n\t" \
  "s_bcnt1_i32_b32 s98, s84\n\t" \
  "ds_read_b64_tr_b16 a[148:149], v210 offset:8448\n\t" \
  "s_bcnt1_i32_b32 s54, s85\n\t" \
  "v_mfma_f32_16x16x32_bf16 v[36:39], a[212:215], v[52:55], v[36:39]\n\t" \
  "s_cmp_gt_u32 s98, s54\n\t" \
  "s_cselect_b32 s55, 0x2000, 0\n\t" \
  "s_cselect_b32 s98, s84, s85\n\t" \
  "v_mfma_f32_16x16x32_bf16 v[36:39], a[208:211], v[48:51], v[36:39]\n\t" \
  "s_cmp_lg_u32 s55, 0\n\t" \
  "s_cselect_b32 s84, s85, s84\n\t" \
  "s_lshr_b32 s85, s98, 16\n\t" \
  "v_mfma_f32_16x16x32_bf16 v[40:43], a[224:227], v[48:51], v[172:175]\n\t" \
  "s_bcnt1_i32_b32 s85, s85\n\t" \
  "s_lshr_b32 s84, s84, 16\n\t" \
  "s_bcnt1_i32_b32 s84, s84\n\t" \
  "v_mfma_f32_16x16x32_bf16 v[40:43], a[228:231], v[52:55], v[40:43]\n\t" \
  "s_add_u32 s84, s84, 7\n\t" \
  "s_cmp_ge_u32 s85, s84\n\t" \
  "s_cselect_b32 s54, -1, 0\n\t" \
  "v_mfma_f32_16x16x32_bf16 v[40:43], a[232:235], v[56:59], v[40:43]\n\t" \
  "s_xor_b32 s98, s42, s55\n\t" \
  "s_and_b32 s98, s98, s54\n\t" \
  "s_xor_b32 s45, s45, s98\n\t" \
  "v_mfma_f32_16x16x32_bf16 v[40:43], a[236:239], v[60:63], v[40:43]\n\t" \
  "s_xor_b32 s46, s46, s98\n\t" \
  STS("v_writelane_b32 v255, s54, 57\n\t") \
  STS("v_writelane_b32 v255, s55, 59\n\t") \
  "s_xor_b32 s98, s43, s55\n\t" \
  "s_and_b32 s98, s98, s54\n\t" \
  "v_mfma_f32_16x16x32_bf16 v[44:47], a[252:255], v[60:63], v[172:175]\n\t" \
  "s_xor_b32 s47, s47, s98\n\t" \
  "s_xor_b32 s48, s48, s55\n\t" \
  "s_xor_b32 s49, s49, s55\n\t" \
  "v_mfma_f32_16x16x32_bf16 v[44:47], a[248:251], v[56:59], v[44:47]\n\t" \
  "s_xor_b32 s98, s45, 0\n\t" \
  "ds_read_b64_tr_b16 a[150:151], v210 offset:12544\n\t" \
  "v_cvt_pk_bf16_f32 v157, v106, v107\n\t" \
  "v_mfma_f32_16x16x32_bf16 v[44:47], a[244:247], v[52:55], v[44:47]\n\t" \
  "ds_read_b64_tr_b16 a[152:153], v211 offset:256\n\t" \
  "v_mfma_f32_16x16x32_bf16 v[44:47], a[240:243], v[48:51], v[44:47]\n\t" \
  "v_exp_f32 v108, v108\n\t" \
  "v_mfma_f32_16x16x32_bf16 v[64:67], a[192:195], v[80:83], v[172:175]\n\t" \
  "ds_read_b64_tr_b16 a[154:155], v211 offset:4352\n\t" \
  "v_mfma_f32_16x16x32_bf16 v[64:67], a[196:199], v[84:87], v[64:67]\n\t" \
  "v_exp_f32 v109, v109\n\t" \
  "v_mfma_f32_16x16x32_bf16 v[64:67], a[200:203], v[88:91], v[64:67]\n\t" \
  "ds_read_b64_tr_b16 a[156:157], v211 offset:8448\n\t" \
  "v_cvt_pk_bf16_f32 v158, v108, v109\n\t" \
  "v_mfma_f32_16x16x32_bf16 v[64:67], a[204:207], v[92:95], v[64:67]\n\t" \
  "ds_read_b64_tr_b16 a[158:159], v211 offset:12544\n\t" \
  "v_exp_f32 v110, v110\n\t" \
  "v_mfma_f32_16x16x32_bf16 v[68:71], a[220:223], v[92:95], v[172:175]\n\t" \
  "v_exp_f32 v111, v111\n\t" \
  "v_mfma_f32_16x16x32_bf16 v[68:71], a[216:219], v[88:91], v[68:71]\n\t" \
  "ds_read_b64_tr_b16 a[160:161], v210 offset:512\n\t" \
  "v_cvt_pk_bf16_f32 v159, v110, v111\n\t" \
  "v_mfma_f32_16x16x32_bf16 v[68:71], a[212:215], v[84:87], v[68:71]\n\t" \
  "v_exp_f32 v0, v0\n\t" \
  "v_mfma_f32_16x16x32_bf16 v[68:71], a[208:211], v[80:83], v[68:71]\n\t" \
  "ds_read_b64_tr_b16 a[162:163], v210 offset:4608\n\t" \
  "v_mfma_f32_16x16x32_bf16 v[72:75], a[224:227], v[80:83], v[172:175]\n\t" \
  "v_exp_f32 v1, v1\n\t" \
  "v_mfma_f32_16x16x32_bf16 v[72:75], a[228:231], v[84:87], v[72:75]\n\t" \
  "ds_read_b64_tr_b16 a[164:165], v210 offset:8704\n\t" \
  "v_mfma_f32_16x16x32_bf16 v[72:75], a[232:235], v[88:91], v[72:75]\n\t" \
  "ds_read_b64_tr_b16 a[166:167], v210 offset:12800\n\t" \
  "v_exp_f32 v2, v2\n\t" \
  "v_mfma_f32_16x16x32_bf16 v[72:75], a[236:239], v[92:95], v[72:75]\n\t" \
  "ds_read_b64_tr_b16 a[168:169], v211 offset:512\n\t" \
  "v_mfma_f32_16x16x32_bf16 v[76:79], a[252:255], v[92:95], v[172:175]\n\t" \
  "ds_read_b64_tr_b16 a[170:171], v211 offset:4608\n\t" \
  "v_exp_f32 v3, v3\n\t" \
  "v_mfma_f32_16x16x32_bf16 v[76:79], a[248:251], v[88:91], v[76:79]\n\t" \
  "ds_read_b64_tr_b16 a[172:173], v211 offset:8704\n\t" \
  "v_exp_f32 v4, v4\n\t" \
  "v_mfma_f32_16x16x32_bf16 v[76:79], a[244:247], v[84:87], v[76:79]\n\t" \
  "v_exp_f32 v5, v5\n\t" \
  "v_mfma_f32_16x16x32_bf16 v[76:79], a[240:243], v[80:83], v[76:79]\n\t" \
  "v_exp_f32 v6, v6\n\t" \
  "v_mfma_f32_16x16x32_bf16 v[96:99], a[192:195], v[112:115], v[172:175]\n\t" \
  "v_exp_f32 v7, v7\n\t" \
  "v_mfma_f32_16x16x32_bf16 v[96:99], a[196:199], v[116:119], v[96:99]\n\t" \
  "ds_read_b64_tr_b16 a[174:175], v211 offset:12800\n\t" \
  "v_mfma_f32_16x16x32_bf16 v[96:99], a[200:203], v[120:123], v[96:99]\n\t" \
  "ds_read_b64_tr_b16 a[176:177], v210 offset:768\n\t" \
  "v_mfma_f32_16x16x32_bf16 v[96:99], a[204:207], v[124:127], v[96:99]\n\t" \
  "ds_read_b64_tr_b16 a[178:179], v210 offset:4864\n\t" \
  "v_exp_f32 v8, v8\n\t" \
  "v_mfma_f32_16x16x32_bf16 v[100:103], a[220:223], v[124:127], v[172:175]\n\t" \
  "v_exp_f32 v9, v9\n\t" \
  "v_mfma_f32_16x16x32_bf16 v[100:103], a[216:219], v[120:123], v[100:103]\n\t" \
  "v_exp_f32 v10, v10\n\t" \
  "v_mfma_f32_16x16x32_bf16 v[100:103], a[212:215], v[116:119], v[100:103]\n\t" \
  "ds_read_b64_tr_b16 a[180:181], v210 offset:8960\n\t" \
  "v_mfma_f32_16x16x32_bf16 v[100:103], a[208:211], v[112:115], v[100:103]\n\t" \
  "ds_read_b64_tr_b16 a[182:183], v210 offset:13056\n\t" \
  "v_mfma_f32_16x16x32_bf16 v[104:107], a[224:227], v[112:115], v[172:175]\n\t" \
  "ds_read_b64_tr_b16 a[184:185], v211 offset:768\n\t" \
  "v_exp_f32 v11, v11\n\t" \
  "v_mfma_f32_16x16x32_bf16 v[104:107], a[228:231], v[116:119], v[104:107]\n\t" \
  "ds_read_b64_tr_b16 a[186:187], v211 offset:4864\n\t" \
  "v_exp_f32 v12, v12\n\t" \
  "v_mfma_f32_16x16x32_bf16 v[104:107], a[232:235], v[120:123], v[104:107]\n\t" \
  "v_exp_f32 v13, v13\n\t" \
  "v_mfma_f32_16x16x32_bf16 v[104:107], a[236:239], v[124:127], v[104:107]\n\t" \
  "ds_read_b64_tr_b16 a[188:189], v211 offset:8960\n\t" \
  "v_mfma_f32_16x16x32_bf16 v[108:111], a[252:255], v[124:127], v[172:175]\n\t" \
  "ds_read_b64_tr_b16 a[190:191], v211 offset:13056\n\t" \
  "v_exp_f32 v14, v14\n\t" \
  "v_mfma_f32_16x16x32_bf16 v[108:111], a[248:251], v[120:123], v[108:111]\n\t" \
  "v_exp_f32 v15, v15\n\t" \
  "v_mfma_f32_16x16x32_bf16 v[108:111], a[244:247], v[116:119], v[108:111]\n\t" \
  "s_waitcnt vmcnt(2) lgkmcnt(0)\n\t" \
  "v_exp_f32 v32, v32\n\t" \
  "v_mfma_f32_16x16x32_bf16 v[108:111], a[240:243], v[112:115], v[108:111]\n\t" \
  "s_barrier\n\t" \
  STA("s_memrealtime s[94:95]\n\t") \
  STA("s_memtime s[92:93]\n\t") \
  "v_mfma_f32_16x16x32_bf16 a[0:3], a[128:131], v[128:131], a[0:3]\n\t" \
  "v_exp_f32 v33, v33\n\t" \
  "v_mfma_f32_16x16x32_bf16 a[0:3], a[132:135], v[132:135], a[0:3]\n\t" \
  "v_exp_f32 v34, v34\n\t" \
  "v_mfma_f32_16x16x32_bf16 a[4:7], a[136:139], v[128:131], a[4:7]\n\t" \
  "v_exp_f32 v35, v35\n\t" \
  "v_mfma_f32_16x16x32_bf16 a[4:7], a[140:143], v[132:135], a[4:7]\n\t" \
  "v_exp_f32 v36, v36\n\t" \
  "v_mfma_f32_16x16x32_bf16 a[8:11], a[144:147], v[128:131], a[8:11]\n\t" \
  "v_exp_f32 v37, v37\n\t" \
  "v_mfma_f32_16x16x32_bf16 a[8:11], a[148:151], v[132:135], a[8:11]\n\t" \
  "v_exp_f32 v38, v38\n\t" \
  "v_mfma_f32_16x16x32_bf16 a[12:15], a[152:155], v[128:131], a[12:15]\n\t" \
  "v_exp_f32 v39, v39\n\t" \
  "v_mfma_f32_16x16x32_bf16 a[12:15], a[156:159], v[132:135], a[12:15]\n\t" \
  "v_exp_f32 v40, v40\n\t" \
  "v_mfma_f32_16x16x32_bf16 a[16:19], a[160:163], v[128:131], a[16:19]\n\t" \
  "v_exp_f32 v41, v41\n\t" \
  "v_mfma_f32_16x16x32_bf16 a[16:19], a[164:167], v[132:135], a[16:19]\n\t" \
  "v_exp_f32 v42, v42\n\t" \
  "v_mfma_f32_16x16x32_bf16 a[20:23], a[168:171], v[128:131], a[20:23]\n\t" \
  "v_exp_f32 v43, v43\n\t" \
  "v_mfma_f32_16x16x32_bf16 a[20:23], a[172:175], v[132:135], a[20:23]\n\t" \
  "v_exp_f32 v44, v44\n\t" \
  "v_mfma_f32_16x16x32_bf16 a[24:27], a[176:179], v[128:131], a[24:27]\n\t" \
  STA("v_writelane_b32 v255, s94, 48\n\t") \
  STA("v_writelane_b32 v255, s92, 49\n\t") \
  "v_exp_f32 v45, v45\n\t" \
  "v_mfma_f32_16x16x32_bf16 a[24:27], a[180:183], v[132:135], a[24:27]\n\t" \
  "v_exp_f32 v46, v46\n\t" \
  "v_mfma_f32_16x16x32_bf16 a[28:31], a[184:187], v[128:131], a[28:31]\n\t" \
  "ds_read_b128 a[192:195], v220 offset:16384\n\t" \
  "v_exp_f32 v47, v47\n\t" \
  "v_mfma_f32_16x16x32_bf16 a[28:31], a[188:191], v[132:135], a[28:31]\n\t" \
  "v_exp_f32 v64, v64\n\t" \
  "v_smfmac_f32_16x16x64_bf16 v[160:163], v[216:219], v[128:135], v208\n\t" \
  "s_add_u32 m0, s38, 147456\n\t" \
  "ds_read_b128 v[200:203], v215 offset:34816\n\t" \
  "v_exp_f32 v65, v65\n\t" \
  "v_mfma_f32_16x16x32_bf16 a[32:35], a[128:131], v[136:139], a[32:35]\n\t" \
  "s_add_u32 s96, s38, 114688\n\t" \
  "ds_read_b128 a[196:199], v221 offset:16384\n\t" \
  "v_exp_f32 v66, v66\n\t" \
  "v_mfma_f32_16x16x32_bf16 a[32:35], a[132:135], v[140:143], a[32:35]\n\t" \
  "v_exp_f32 v67, v67\n\t" \
  "v_mfma_f32_16x16x32_bf16 a[36:39], a[136:139], v[136:139], a[36:39]\n\t" \
  "v_exp_f32 v68, v68\n\t" \
  "v_mfma_f32_16x16x32_bf16 a[36:39], a[140:143], v[140:143], a[36:39]\n\t" \
  "buffer_load_dwordx4 v230, s[64:67], s96 offen offset:0 lds\n\t" \
  "v_mfma_f32_16x16x32_bf16 a[40:43], a[144:147], v[136:139], a[40:43]\n\t" \
  "ds_read_b128 a[200:203], v222 offset:16384\n\t" \
  "v_exp_f32 v69, v69\n\t" \
  "v_mfma_f32_16x16x32_bf16 a[40:43], a[148:151], v[140:143], a[40:43]\n\t" \
  "v_exp_f32 v70, v70\n\t" \
  "v_mfma_f32_16x16x32_bf16 a[44:47], a[152:155], v[136:139], a[44:47]\n\t" \
  "ds_read_b128 a[204:207], v223 offset:16384\n\t" \
  "v_exp_f32 v71, v71\n\t" \
  "v_mfma_f32_16x16x32_bf16 a[44:47], a[156:159], v[140:143], a[44:47]\n\t" \
  "buffer_load_dwordx4 v231, s[64:67], s96 offen offset:1024 lds\n\t" \
  "v_mfma_f32_16x16x32_bf16 a[48:51], a[160:163], v[136:139], a[48:51]\n\t" \
  "ds_read_b128 a[208:211], v220 offset:20480\n\t" \
  "v_exp_f32 v72, v72\n\t" \
  "v_mfma_f32_16x16x32_bf16 a[48:51], a[164:167], v[140:143], a[48:51]\n\t" \
  "v_exp_f32 v73, v73\n\t" \
  "v_mfma_f32_16x16x32_bf16 a[52:55], a[168:171], v[136:139], a[52:55]\n\t" \
  "v_exp_f32 v74, v74\n\t" \
  "v_mfma_f32_16x16x32_bf16 a[52:55], a[172:175], v[140:143], a[52:55]\n\t" \
  "buffer_load_dwordx4 v230, s[64:67], s96 offen offset:2048 lds\n\t" \
  "v_mfma_f32_16x16x32_bf16 a[56:59], a[176:179], v[136:139], a[56:59]\n\t" \
  "v_exp_f32 v75, v75\n\t" \
  "v_mfma_f32_16x16x32_bf16 a[56:59], a[180:183], v[140:143], a[56:59]\n\t" \
  "v_exp_f32 v76, v76\n\t" \
  "v_mfma_f32_16x16x32_bf16 a[60:63], a[184:187], v[136:139], a[60:63]\n\t" \
  "buffer_load_dwordx4 v231, s[64:67], s96 offen offset:3072 lds\n\t" \
  "v_mfma_f32_16x16x32_bf16 a[60:63], a[188:191], v[140:143], a[60:63]\n\t" \
  "s_lshl_b32 s64, s39, 17\n\t" \
  "ds_read_b128 a[212:215], v221 offset:20480\n\t" \
  "v_cvt_pk_bf16_f32 v128, v0, v1\n\t" \
  "v_smfmac_f32_16x16x64_bf16 v[162:165], v[216:219], v[136:143], v208\n\t" \
  "s_add_u32 s96, s40, 0\n\t" \
  "s_and_b32 s96, s96, 0x1c0000\n\t" \
  "s_add_u32 s96, s96, s51\n\t" \
  "v_mfma_f32_16x16x32_bf16 a[64:67], a[128:131], v[144:147], a[64:67]\n\t" \
  "s_add_u32 m0, s38, 0\n\t" \
  "ds_read_b128 a[216:219], v222 offset:20480\n\t" \
  "v_exp_f32 v77, v77\n\t" \
  "v_mfma_f32_16x16x32_bf16 a[64:67], a[132:135], v[148:151], a[64:67]\n\t" \
  "buffer_load_dwordx4 v240, s[24:27], s96 offen offset:0 lds\n\t" \
  "v_mfma_f32_16x16x32_bf16 a[68:71], a[136:139], v[144:147], a[68:71]\n\t" \
  "s_bfe_u32 s70, s46, 0x20014\n\t" \
  "ds_read_b128 a[220:223], v223 offset:20480\n\t" \
  "v_exp_f32 v78, v78\n\t" \
  "v_mfma_f32_16x16x32_bf16 a[68:71], a[140:143], v[148:151], a[68:71]\n\t" \
  "s_lshl_b32 s70, s70, 14\n\t" \
  "v_exp_f32 v79, v79\n\t" \
  "v_mfma_f32_16x16x32_bf16 a[72:75], a[144:147], v[144:147], a[72:75]\n\t" \
  "s_xor_b32 s67, s70, 49152\n\t" \
  "ds_read_b128 a[224:227], v220 offset:24576\n\t" \
  "v_cvt_pk_bf16_f32 v129, v2, v3\n\t" \
  "v_mfma_f32_16x16x32_bf16 a[72:75], a[148:151], v[148:151], a[72:75]\n\t" \
  "buffer_load_dwordx4 v241, s[24:27], s96 offen offset:1024 lds\n\t" \
  "v_mfma_f32_16x16x32_bf16 a[76:79], a[152:155], v[144:147], a[76:79]\n\t" \
  "s_lshl1_add_u32 s60, s67, s46\n\t" \
  "v_exp_f32 v96, v96\n\t" \
  "v_mfma_f32_16x16x32_bf16 a[76:79], a[156:159], v[148:151], a[76:79]\n\t" \
  "s_xor_b32 s68, s70, 32768\n\t" \
  "ds_read_b128 a[228:231], v221 offset:24576\n\t" \
  "v_cvt_pk_bf16_f32 v130, v4, v5\n\t" \
  "v_mfma_f32_16x16x32_bf16 a[80:83], a[160:163], v[144:147], a[80:83]\n\t" \
  "buffer_load_dwordx4 v248, s[24:27], s96 offen offset:2048 lds\n\t" \
  "v_mfma_f32_16x16x32_bf16 a[80:83], a[164:167], v[148:151], a[80:83]\n\t" \
  "s_lshl1_add_u32 s61, s68, s46\n\t" \
  "ds_read_b128 a[232:235], v222 offset:24576\n\t" \
  "v_exp_f32 v97, v97\n\t" \
  "v_mfma_f32_16x16x32_bf16 a[84:87], a[168:171], v[144:147], a[84:87]\n\t" \
  "s_xor_b32 s69, s70, 16384\n\t" \
  "v_exp_f32 v98, v98\n\t" \
  "v_mfma_f32_16x16x32_bf16 a[84:87], a[172:175], v[148:151], a[84:87]\n\t" \
  "s_lshl1_add_u32 s62, s69, s46\n\t" \
  "v_exp_f32 v99, v99\n\t" \
  "v_mfma_f32_16x16x32_bf16 a[88:91], a[176:179], v[144:147], a[88:91]\n\t" \
  "buffer_load_dwordx4 v249, s[24:27], s96 offen offset:3072 lds\n\t" \
  ".Lhs9:\n\ts_cbranch_execz .Lhs9n\n\t" \
  "v_mfma_f32_16x16x32_bf16 a[88:91], a[180:183], v[148:151], a[88:91]\n\t" \
  "s_lshl1_add_u32 s63, s70, s46\n\t" \
  "ds_read_b128 a[236:239], v223 offset:24576\n\t" \
  "v_cvt_pk_bf16_f32 v131, v6, v7\n\t" \
  "v_mfma_f32_16x16x32_bf16 a[92:95], a[184:187], v[144:147], a[92:95]\n\t" \
  "s_add_u32 s96, s40, 262144\n\t" \
  "s_and_b32 s96, s96, 0x1c0000\n\t" \
  "s_add_u32 s96, s96, s51\n\t" \
  "v_mfma_f32_16x16x32_bf16 a[92:95], a[188:191], v[148:151], a[92:95]\n\t" \
  "s_add_u32 m0, s38, 49152\n\t" \
  "ds_read_b128 a[240:243], v220 offset:28672\n\t" \
  "v_cvt_pk_bf16_f32 v132, v8, v9\n\t" \
  "v_smfmac_f32_16x16x64_bf16 v[164:167], v[216:219], v[144:151], v208\n\t" \
  "buffer_load_dwordx4 v240, s[24:27], s96 offen offset:0 lds\n\t" \
  "v_mfma_f32_16x16x32_bf16 a[96:99], a[128:131], v[152:155], a[96:99]\n\t" \
  "s_mul_i32 s65, s39, 508\n\t" \
  "ds_read_b128 a[244:247], v221 offset:28672\n\t" \
  "v_exp_f32 v100, v100\n\t" \
  "v_mfma_f32_16x16x32_bf16 a[96:99], a[132:135], v[156:159], a[96:99]\n\t" \
  "s_add_u32 s65, s65, s49\n\t" \
  "v_exp_f32 v101, v101\n\t" \
  "v_mfma_f32_16x16x32_bf16 a[100:103], a[136:139], v[152:155], a[100:103]\n\t" \
  "buffer_load_dwordx4 v241, s[24:27], s96 offen offset:1024 lds\n\t" \
  "v_mfma_f32_16x16x32_bf16 a[100:103], a[140:143], v[156:159], a[100:103]\n\t" \
  "s_lshl_b32 s80, s39, 2\n\t" \
  "ds_read_b128 a[248:251], v222 offset:28672\n\t" \
  "v_cvt_pk_bf16_f32 v133, v10, v11\n\t" \
  "v_mfma_f32_16x16x32_bf16 a[104:107], a[144:147], v[152:155], a[104:107]\n\t" \
  "s_add_u32 s80, s80, s3\n\t" \
  "v_exp_f32 v102, v102\n\t" \
  "v_mfma_f32_16x16x32_bf16 a[104:107], a[148:151], v[156:159], a[104:107]\n\t" \
  "s_lshl_b32 s80, s80, 6\n\t" \
  "ds_read_b128 a[252:255], v223 offset:28672\n\t" \
  "v_cvt_pk_bf16_f32 v134, v12, v13\n\t" \
  "v_mfma_f32_16x16x32_bf16 a[108:111], a[152:155], v[152:155], a[108:111]\n\t" \
  "buffer_load_dwordx4 v248, s[24:27], s96 offen offset:2048 lds\n\t" \
  "v_mfma_f32_16x16x32_bf16 a[108:111], a[156:159], v[156:159], a[108:111]\n\t" \
  "s_add_u32 s76, s38, 147456\n\t" \
  "v_cvt_pk_bf16_f32 v135, v14, v15\n\t" \
  "v_exp_f32 v103, v103\n\t" \
  "v_mfma_f32_16x16x32_bf16 a[112:115], a[160:163], v[152:155], a[112:115]\n\t" \
  "s_lshl_b32 s75, s80, 2\n\t" \
  "v_exp_f32 v104, v104\n\t" \
  "v_mfma_f32_16x16x32_bf16 a[112:115], a[164:167], v[156:159], a[112:115]\n\t" \
  "s_add_u32 s75, s75, 2228224\n\t" \
  "v_exp_f32 v105, v105\n\t" \
  "v_mfma_f32_16x16x32_bf16 a[116:119], a[168:171], v[152:155], a[116:119]\n\t" \
  "buffer_load_dwordx4 v249, s[24:27], s96 offen offset:3072 lds\n\t" \
  "v_mfma_f32_16x16x32_bf16 a[116:119], a[172:175], v[156:159], a[116:119]\n\t" \
  "s_lshr_b32 s71, s67, 7\n\t" \
  "v_cvt_pk_bf16_f32 v136, v32, v33\n\t" \
  "v_cvt_pk_bf16_f32 v137, v34, v35\n\t" \
  "v_mfma_f32_16x16x32_bf16 a[120:123], a[176:179], v[152:155], a[120:123]\n\t" \
  "s_add_u32 s71, s71, s48\n\t" \
  "v_cvt_pk_bf16_f32 v138, v36, v37\n\t" \
  "v_exp_f32 v106, v106\n\t" \
  "v_mfma_f32_16x16x32_bf16 a[120:123], a[180:183], v[156:159], a[120:123]\n\t" \
  "s_lshr_b32 s72, s68, 7\n\t" \
  "v_exp_f32 v107, v107\n\t" \
  "v_mfma_f32_16x16x32_bf16 a[124:127], a[184:187], v[152:155], a[124:127]\n\t" \
  "s_add_u32 s72, s72, s48\n\t" \
  "v_exp_f32 v108, v108\n\t" \
  "v_mfma_f32_16x16x32_bf16 a[124:127], a[188:191], v[156:159], a[124:127]\n\t" \
  "s_lshr_b32 s66, s69, 7\n\t" \
  "v_cvt_pk_bf16_f32 v139, v38, v39\n\t" \
  "v_cvt_pk_bf16_f32 v140, v40, v41\n\t" \
  "v_smfmac_f32_16x16x64_bf16 v[166:169], v[216:219], v[152:159], v208\n\t" \
  "s_waitcnt lgkmcnt(12)\n\t" \
  "ds_read_b128 v[204:207], v215 offset:35840\n\t" \
  "v_cvt_pk_bf16_f32 v141, v42, v43\n\t" \
  STA("s_memrealtime s[94:95]\n\t") \
  STA("s_memtime s[92:93]\n\t") \
  "v_mfma_f32_16x16x32_bf16 v[0:3], a[192:195], v[16:19], v[172:175]\n\t" \
  "ds_read_b64_tr_b16 a[128:129], v228 offset:0\n\t" \
  "ds_read_b64_tr_b16 a[130:131], v228 offset:4096\n\t" \
  "ds_read_b64_tr_b16 a[132:133], v228 offset:8192\n\t" \
  "ds_read_b64_tr_b16 a[134:135], v228 offset:12288\n\t" \
  "v_mfma_f32_16x16x32_bf16 v[0:3], a[196:199], v[20:23], v[0:3]\n\t" \
  "s_add_u32 s66, s66, s48\n\t" \
  "ds_read_b64_tr_b16 a[136:137], v229 offset:0\n\t" \
  "v_cvt_pk_bf16_f32 v142, v44, v45\n\t" \
  "v_mfma_f32_16x16x32_bf16 v[0:3], a[200:203], v[24:27], v[0:3]\n\t" \
  "s_lshr_b32 s73, s70, 7\n\t" \
  "ds_read_b64_tr_b16 a[138:139], v229 offset:4096\n\t" \
  "v_exp_f32 v109, v109\n\t" \
  "v_mfma_f32_16x16x32_bf16 v[0:3], a[204:207], v[28:31], v[0:3]\n\t" \
  "s_waitcnt lgkmcnt(15)\n\t" \
  "ds_read_b64_tr_b16 a[140:141], v229 offset:8192\n\t" \
  "v_cvt_pk_bf16_f32 v143, v46, v47\n\t" \
  "v_mfma_f32_16x16x32_bf16 v[4:7], a[220:223], v[28:31], v[172:175]\n\t" \
  "s_add_u32 s73, s73, s48\n\t" \
  "ds_read_b64_tr_b16 a[142:143], v229 offset:12288\n\t" \
  "v_cvt_pk_bf16_f32 v144, v64, v65\n\t" \
  "v_mfma_f32_16x16x32_bf16 v[4:7], a[216:219], v[24:27], v[4:7]\n\t" \
  "s_lshl3_add_u32 s67, s67, s47\n\t" \
  "ds_read_b64_tr_b16 a[144:145], v228 offset:256\n\t" \
  "v_exp_f32 v110, v110\n\t" \
  "v_mfma_f32_16x16x32_bf16 v[4:7], a[212:215], v[20:23], v[4:7]\n\t" \
  "s_lshl3_add_u32 s68, s68, s47\n\t" \
  "ds_read_b64_tr_b16 a[146:147], v228 offset:4352\n\t" \
  "v_exp_f32 v111, v111\n\t" \
  "v_mfma_f32_16x16x32_bf16 v[4:7], a[208:211], v[16:19], v[4:7]\n\t" \
  "s_waitcnt lgkmcnt(15)\n\t" \
  "ds_read_b64_tr_b16 a[148:149], v228 offset:8448\n\t" \
  "v_cvt_pk_bf16_f32 v145, v66, v67\n\t" \
  "v_mfma_f32_16x16x32_bf16 v[8:11], a[224:227], v[16:19], v[172:175]\n\t" \
  "s_add_u32 s68, s68, 4096\n\t" \
  "v_exp_f32 v0, v0\n\t" \
  "v_mfma_f32_16x16x32_bf16 v[8:11], a[228:231], v[20:23], v[8:11]\n\t" \
  "s_lshl3_add_u32 s69, s69, s47\n\t" \
  "ds_read_b64_tr_b16 a[150:151], v228 offset:12544\n\t" \
  "v_cvt_pk_bf16_f32 v146, v68, v69\n\t" \
  "v_mfma_f32_16x16x32_bf16 v[8:11], a[232:235], v[24:27], v[8:11]\n\t" \
  "s_add_u32 s69, s69, 16384\n\t" \
  "ds_read_b64_tr_b16 a[152:153], v229 offset:256\n\t" \
  "v_cvt_pk_bf16_f32 v147, v70, v71\n\t" \
  "v_mfma_f32_16x16x32_bf16 v[8:11], a[236:239], v[28:31], v[8:11]\n\t" \
  "s_waitcnt lgkmcnt(14)\n\t" \
  "v_cvt_pk_bf16_f32 v148, v72, v73\n\t" \
  "v_exp_f32 v1, v1\n\t" \
  "v_mfma_f32_16x16x32_bf16 v[12:15], a[252:255], v[28:31], v[172:175]\n\t" \
  STA("v_writelane_b32 v255, s94, 50\n\t") \
  STA("v_writelane_b32 v255, s92, 51\n\t") \
  "s_lshl3_add_u32 s70, s70, s47\n\t" \
  "v_exp_f32 v2, v2\n\t" \
  "v_mfma_f32_16x16x32_bf16 v[12:15], a[248:251], v[24:27], v[12:15]\n\t" \
  "s_barrier\n\t" \
  "v_mfma_f32_16x16x32_bf16 v[12:15], a[244:247], v[20:23], v[12:15]\n\t" \
  "v_exp_f32 v3, v3\n\t" \
  "v_mfma_f32_16x16x32_bf16 v[12:15], a[240:243], v[16:19], v[12:15]\n\t" \
  "ds_read_b64_tr_b16 a[154:155], v229 offset:4352\n\t" \
  "v_cvt_pk_bf16_f32 v149, v74, v75\n\t" \
  "v_mfma_f32_16x16x32_bf16 v[32:35], a[192:195], v[48:51], v[172:175]\n\t" \
  "s_add_u32 s96, s40, 0\n\t" \
  "v_exp_f32 v4, v4\n\t" \
  "v_mfma_f32_16x16x32_bf16 v[32:35], a[196:199], v[52:55], v[32:35]\n\t" \
  "s_and_b32 s96, s96, 0x1c0000\n\t" \
  "ds_read_b64_tr_b16 a[156:157], v229 offset:8448\n\t" \
  "v_cvt_pk_bf16_f32 v150, v76, v77\n\t" \
  "v_mfma_f32_16x16x32_bf16 v[32:35], a[200:203], v[56:59], v[32:35]\n\t" \
  "s_add_u32 s96, s96, s77\n\t" \
  "v_cvt_pk_bf16_f32 v151, v78, v79\n\t" \
  "s_add_u32 m0, s38, 98304\n\t" \
  "v_mfma_f32_16x16x32_bf16 v[32:35], a[204:207], v[60:63], v[32:35]\n\t" \
  "ds_read_b64_tr_b16 a[158:159], v229 offset:12544\n\t" \
  "v_cvt_pk_bf16_f32 v152, v96, v97\n\t" \
  "v_mfma_f32_16x16x32_bf16 v[36:39], a[220:223], v[60:63], v[172:175]\n\t" \
  "buffer_load_dwordx4 v240, s[24:27], s96 offen offset:0 lds\n\t" \
  "v_mfma_f32_16x16x32_bf16 v[36:39], a[216:219], v[56:59], v[36:39]\n\t" \
  "v_exp_f32 v5, v5\n\t" \
  "v_mfma_f32_16x16x32_bf16 v[36:39], a[212:215], v[52:55], v[36:39]\n\t" \
  "v_exp_f32 v6, v6\n\t" \
  "v_mfma_f32_16x16x32_bf16 v[36:39], a[208:211], v[48:51], v[36:39]\n\t" \
  "ds_read_b64_tr_b16 a[160:161], v228 offset:512\n\t" \
  "v_cvt_pk_bf16_f32 v153, v98, v99\n\t" \
  "v_mfma_f32_16x16x32_bf16 v[40:43], a[224:227], v[48:51], v[172:175]\n\t" \
  "buffer_load_dwordx4 v241, s[24:27], s96 offen offset:1024 lds\n\t" \
  "v_mfma_f32_16x16x32_bf16 v[40:43], a[228:231], v[52:55], v[40:43]\n\t" \
  "v_exp_f32 v7, v7\n\t" \
  "v_mfma_f32_16x16x32_bf16 v[40:43], a[232:235], v[56:59], v[40:43]\n\t" \
  "v_exp_f32 v8, v8\n\t" \
  "v_mfma_f32_16x16x32_bf16 v[40:43], a[236:239], v[60:63], v[40:43]\n\t" \
  "buffer_load_dwordx4 v248, s[24:27], s96 offen offset:2048 lds\n\t" \
  "v_mfma_f32_16x16x32_bf16 v[44:47], a[252:255], v[60:63], v[172:175]\n\t" \
  "v_exp_f32 v9, v9\n\t" \
  "v_mfma_f32_16x16x32_bf16 v[44:47], a[248:251], v[56:59], v[44:47]\n\t" \
  "ds_read_b64_tr_b16 a[162:163], v228 offset:4608\n\t" \
  "v_cvt_pk_bf16_f32 v154, v100, v101\n\t" \
  "v_mfma_f32_16x16x32_bf16 v[44:47], a[244:247], v[52:55], v[44:47]\n\t" \
  "buffer_load_dwordx4 v249, s[24:27], s96 offen offset:3072 lds\n\t" \
  "v_mfma_f32_16x16x32_bf16 v[44:47], a[240:243], v[48:51], v[44:47]\n\t" \
  "s_add_u32 s96, s40, 524288\n\t" \
  "ds_read_b64_tr_b16 a[164:165], v228 offset:8704\n\t" \
  "v_cvt_pk_bf16_f32 v155, v102, v103\n\t" \
  "v_mfma_f32_16x16x32_bf16 v[64:67], a[192:195], v[80:83], v[172:175]\n\t" \
  "s_and_b32 s96, s96, 0x1c0000\n\t" \
  "s_add_u32 s96, s96, s51\n\t" \
  "s_add_u32 m0, s38, 131072\n\t" \
  "v_mfma_f32_16x16x32_bf16 v[64:67], a[196:199], v[84:87], v[64:67]\n\t" \
  "ds_read_b64_tr_b16 a[166:167], v228 offset:12800\n\t" \
  "v_cvt_pk_bf16_f32 v156, v104, v105\n\t" \
  "v_mfma_f32_16x16x32_bf16 v[64:67], a[200:203], v[88:91], v[64:67]\n\t" \
  "s_waitcnt lgkmcnt(15)\n\t" \
  "buffer_load_dwordx4 v240, s[24:27], s96 offen offset:0 lds\n\t" \
  "v_mfma_f32_16x16x32_bf16 v[64:67], a[204:207], v[92:95], v[64:67]\n\t" \
  "ds_read_b64_tr_b16 a[168:169], v229 offset:512\n\t" \
  "v_cvt_pk_bf16_f32 v157, v106, v107\n\t" \
  "v_mfma_f32_16x16x32_bf16 v[68:71], a[220:223], v[92:95], v[172:175]\n\t" \
  "v_exp_f32 v10, v10\n\t" \
  "v_mfma_f32_16x16x32_bf16 v[68:71], a[216:219], v[88:91], v[68:71]\n\t" \
  "buffer_load_dwordx4 v241, s[24:27], s96 offen offset:1024 lds\n\t" \
  "v_mfma_f32_16x16x32_bf16 v[68:71], a[212:215], v[84:87], v[68:71]\n\t" \
  "ds_read_b64_tr_b16 a[170:171], v229 offset:4608\n\t" \
  "v_cvt_pk_bf16_f32 v158, v108, v109\n\t" \
  "v_mfma_f32_16x16x32_bf16 v[68:71], a[208:211], v[80:83], v[68:71]\n\t" \
  "ds_read_b64_tr_b16 a[172:173], v229 offset:8704\n\t" \
  "v_cvt_pk_bf16_f32 v159, v110, v111\n\t" \
  "v_mfma_f32_16x16x32_bf16 v[72:75], a[224:227], v[80:83], v[172:175]\n\t" \
  "buffer_load_dwordx4 v248, s[24:27], s96 offen offset:2048 lds\n\t" \
  "v_mfma_f32_16x16x32_bf16 v[72:75], a[228:231], v[84:87], v[72:75]\n\t" \
  "v_exp_f32 v11, v11\n\t" \
  "v_mfma_f32_16x16x32_bf16 v[72:75], a[232:235], v[88:91], v[72:75]\n\t" \
  "ds_read_b64_tr_b16 a[174:175], v229 offset:12800\n\t" \
  "v_cvt_pk_bf16_f32 v0, v0, v1\n\t" \
  "v_mfma_f32_16x16x32_bf16 v[72:75], a[236:239], v[92:95], v[72:75]\n\t" \
  "v_exp_f32 v12, v12\n\t" \
  "v_mfma_f32_16x16x32_bf16 v[76:79], a[252:255], v[92:95], v[172:175]\n\t" \
  "buffer_load_dwordx4 v249, s[24:27], s96 offen offset:3072 lds\n\t" \
  "v_mfma_f32_16x16x32_bf16 v[76:79], a[248:251], v[88:91], v[76:79]\n\t" \
  "s_add_u32 s96, s40, 262144\n\t" \
  "s_and_b32 s96, s96, 0x1c0000\n\t" \
  "s_add_u32 s96, s96, s77\n\t" \
  "v_mfma_f32_16x16x32_bf16 v[76:79], a[244:247], v[84:87], v[76:79]\n\t" \
  "s_add_u32 m0, s38, 65536\n\t" \
  "ds_read_b64_tr_b16 a[176:177], v228 offset:768\n\t" \
  "v_exp_f32 v13, v13\n\t" \
  "v_mfma_f32_16x16x32_bf16 v[76:79], a[240:243], v[80:83], v[76:79]\n\t" \
  "buffer_load_dwordx4 v240, s[24:27], s96 offen offset:0 lds\n\t" \
  "v_mfma_f32_16x16x32_bf16 v[96:99], a[192:195], v[112:115], v[172:175]\n\t" \
  "ds_read_b64_tr_b16 a[178:179], v228 offset:4864\n\t" \
  "v_cvt_pk_bf16_f32 v1, v2, v3\n\t" \
  "v_mfma_f32_16x16x32_bf16 v[96:99], a[196:199], v[116:119], v[96:99]\n\t" \
  "ds_read_b64_tr_b16 a[180:181], v228 offset:8960\n\t" \
  "v_exp_f32 v14, v14\n\t" \
  "v_mfma_f32_16x16x32_bf16 v[96:99], a[200:203], v[120:123], v[96:99]\n\t" \
  "buffer_load_dwordx4 v241, s[24:27], s96 offen offset:1024 lds\n\t" \
  "v_mfma_f32_16x16x32_bf16 v[96:99], a[204:207], v[124:127], v[96:99]\n\t" \
  "ds_read_b64_tr_b16 a[182:183], v228 offset:13056\n\t" \
  "v_exp_f32 v15, v15\n\t" \
  "v_mfma_f32_16x16x32_bf16 v[100:103], a[220:223], v[124:127], v[172:175]\n\t" \
  "v_exp_f32 v32, v32\n\t" \
  "v_mfma_f32_16x16x32_bf16 v[100:103], a[216:219], v[120:123], v[100:103]\n\t" \
  "ds_read_b64_tr_b16 a[184:185], v229 offset:768\n\t" \
  "v_cvt_pk_bf16_f32 v2, v4, v5\n\t" \
  "v_mfma_f32_16x16x32_bf16 v[100:103], a[212:215], v[116:119], v[100:103]\n\t" \
  "buffer_load_dwordx4 v248, s[24:27], s96 offen offset:2048 lds\n\t" \
  "v_mfma_f32_16x16x32_bf16 v[100:103], a[208:211], v[112:115], v[100:103]\n\t" \
  "ds_read_b64_tr_b16 a[186:187], v229 offset:4864\n\t" \
  "v_exp_f32 v33, v33\n\t" \
  "v_mfma_f32_16x16x32_bf16 v[104:107], a[224:227], v[112:115], v[172:175]\n\t" \
  "ds_read_b64_tr_b16 a[188:189], v229 offset:8960\n\t" \
  "v_cvt_pk_bf16_f32 v3, v6, v7\n\t" \
  "v_mfma_f32_16x16x32_bf16 v[104:107], a[228:231], v[116:119], v[104:107]\n\t" \
  "buffer_load_dwordx4 v249, s[24:27], s96 offen offset:3072 lds\n\t" \
  "v_mfma_f32_16x16x32_bf16 v[104:107], a[232:235], v[120:123], v[104:107]\n\t" \
  "s_add_u32 s96, s40, 524288\n\t" \
  "ds_read_b64_tr_b16 a[190:191], v229 offset:13056\n\t" \
  "v_exp_f32 v34, v34\n\t" \
  "v_mfma_f32_16x16x32_bf16 v[104:107], a[236:239], v[124:127], v[104:107]\n\t" \
  "s_and_b32 s96, s96, 0x1c0000\n\t" \
  "v_exp_f32 v35, v35\n\t" \
  "v_mfma_f32_16x16x32_bf16 v[108:111], a[252:255], v[124:127], v[172:175]\n\t" \
  "s_add_u32 s96, s96, s77\n\t" \
  "v_exp_f32 v36, v36\n\t" \
  "v_mfma_f32_16x16x32_bf16 v[108:111], a[248:251], v[120:123], v[108:111]\n\t" \
  "s_add_u32 m0, s38, 81920\n\t" \
  "v_exp_f32 v37, v37\n\t" \
  "v_mfma_f32_16x16x32_bf16 v[108:111], a[244:247], v[116:119], v[108:111]\n\t" \
  "s_waitcnt vmcnt(8) lgkmcnt(0)\n\t" \
  "v_exp_f32 v38, v38\n\t" \
  "v_mfma_f32_16x16x32_bf16 v[108:111], a[240:243], v[112:115], v[108:111]\n\t" \
  "s_barrier\n\t" \
  STS("s_memrealtime s[94:95]\n\t") \
  STS("s_memtime s[92:93]\n\t") \
  "v_mfma_f32_16x16x32_bf16 a[0:3], a[128:131], v[128:131], a[0:3]\n\t" \
  "buffer_load_dwordx4 v240, s[24:27], s96 offen offset:0 lds\n\t" \
  "ds_read_b64_tr_b16 v[16:17], v210 offset:0\n\t" \
  "ds_read_b128 v[176:179], v215 offset:16384\n\t" \
  "ds_read_b128 v[180:183], v215 offset:17408\n\t" \
  "v_mfma_f32_16x16x32_bf16 a[0:3], a[132:135], v[132:135], a[0:3]\n\t" \
  "ds_read_b64_tr_b16 v[18:19], v210 offset:4096\n\t" \
  "ds_read_b64_tr_b16 v[20:21], v210 offset:8192\n\t" \
  "ds_read_b64_tr_b16 v[22:23], v210 offset:12288\n\t" \
  "ds_read_b64_tr_b16 v[24:25], v211 offset:0\n\t" \
  "v_mfma_f32_16x16x32_bf16 a[4:7], a[136:139], v[128:131], a[4:7]\n\t" \
  "ds_read_b64_tr_b16 v[26:27], v211 offset:4096\n\t" \
  "ds_read_b64_tr_b16 v[28:29], v211 offset:8192\n\t" \
  "ds_read_b64_tr_b16 v[30:31], v211 offset:12288\n\t" \
  "ds_read_b128 v[184:187], v215 offset:18432\n\t" \
  "v_mfma_f32_16x16x32_bf16 a[4:7], a[140:143], v[132:135], a[4:7]\n\t" \
  "buffer_load_dwordx4 v241, s[24:27], s96 offen offset:1024 lds\n\t" \
  "ds_read_b64_tr_b16 v[48:49], v210 offset:256\n\t" \
  "ds_read_b64_tr_b16 v[50:51], v210 offset:4352\n\t" \
  "ds_read_b64_tr_b16 v[52:53], v210 offset:8448\n\t" \
  "v_mfma_f32_16x16x32_bf16 a[8:11], a[144:147], v[128:131], a[8:11]\n\t" \
  "ds_read_b128 v[188:191], v215 offset:19456\n\t" \
  "ds_read_b64_tr_b16 v[54:55], v210 offset:12544\n\t" \
  "v_cvt_pk_bf16_f32 v4, v8, v9\n\t" \
  "v_mfma_f32_16x16x32_bf16 a[8:11], a[148:151], v[132:135], a[8:11]\n\t" \
  "v_exp_f32 v39, v39\n\t" \
  "v_mfma_f32_16x16x32_bf16 a[12:15], a[152:155], v[128:131], a[12:15]\n\t" \
  "buffer_load_dwordx4 v248, s[24:27], s96 offen offset:2048 lds\n\t" \
  "v_mfma_f32_16x16x32_bf16 a[12:15], a[156:159], v[132:135], a[12:15]\n\t" \
  "ds_read_b64_tr_b16 v[56:57], v211 offset:256\n\t" \
  "v_cvt_pk_bf16_f32 v5, v10, v11\n\t" \
  "v_mfma_f32_16x16x32_bf16 a[16:19], a[160:163], v[128:131], a[16:19]\n\t" \
  "v_exp_f32 v40, v40\n\t" \
  "v_mfma_f32_16x16x32_bf16 a[16:19], a[164:167], v[132:135], a[16:19]\n\t" \
  "buffer_load_dwordx4 v249, s[24:27], s96 offen offset:3072 lds\n\t" \
  "v_mfma_f32_16x16x32_bf16 a[20:23], a[168:171], v[128:131], a[20:23]\n\t" \
  "s_add_u32 s96, s40, 786432\n\t" \
  "ds_read_b64_tr_b16 v[58:59], v211 offset:4352\n\t" \
  "v_cvt_pk_bf16_f32 v6, v12, v13\n\t" \
  "v_mfma_f32_16x16x32_bf16 a[20:23], a[172:175], v[132:135], a[20:23]\n\t" \
  "s_and_b32 s96, s96, 0x1c0000\n\t" \
  "ds_read_b64_tr_b16 v[60:61], v211 offset:8448\n\t" \
  "v_exp_f32 v41, v41\n\t" \
  "v_mfma_f32_16x16x32_bf16 a[24:27], a[176:179], v[128:131], a[24:27]\n\t" \
  STS("v_writelane_b32 v255, s94, 20\n\t") \
  STS("v_writelane_b32 v255, s92, 21\n\t") \
  "s_add_u32 s96, s96, s51\n\t" \
  "v_cvt_pk_bf16_f32 v7, v14, v15\n\t" \
  "s_add_u32 m0, s38, 114688\n\t" \
  "v_mfma_f32_16x16x32_bf16 a[24:27], a[180:183], v[132:135], a[24:27]\n\t" \
  "v_exp_f32 v42, v42\n\t" \
  "v_mfma_f32_16x16x32_bf16 a[28:31], a[184:187], v[128:131], a[28:31]\n\t" \
  "s_waitcnt lgkmcnt(4)\n\t" \
  "buffer_load_dwordx4 v240, s[24:27], s96 offen offset:0 lds\n\t" \
  "v_mfma_f32_16x16x32_bf16 a[28:31], a[188:191], v[132:135], a[28:31]\n\t" \
  "ds_read_b64_tr_b16 v[62:63], v211 offset:12544\n\t" \
  "v_cvt_pk_bf16_f32 v32, v32, v33\n\t" \
  "v_smfmac_f32_16x16x64_bf16 v[160:163], v[216:219], v[128:135], v208\n\t" \
  "ds_read_b64_tr_b16 v[80:81], v210 offset:512\n\t" \
  "v_cvt_pk_bf16_f32 v33, v34, v35\n\t" \
  "v_mfma_f32_16x16x32_bf16 a[32:35], a[128:131], v[136:139], a[32:35]\n\t" \
  "buffer_load_dwordx4 v241, s[24:27], s96 offen offset:1024 lds\n\t" \
  "v_mfma_f32_16x16x32_bf16 a[32:35], a[132:135], v[140:143], a[32:35]\n\t" \
  "v_exp_f32 v43, v43\n\t" \
  "v_mfma_f32_16x16x32_bf16 a[36:39], a[136:139], v[136:139], a[36:39]\n\t" \
  "ds_read_b64_tr_b16 v[82:83], v210 offset:4608\n\t" \
  "v_cvt_pk_bf16_f32 v34, v36, v37\n\t" \
  "v_mfma_f32_16x16x32_bf16 a[36:39], a[140:143], v[140:143], a[36:39]\n\t" \
  "buffer_load_dwordx4 v248, s[24:27], s96 offen offset:2048 lds\n\t" \
  "v_mfma_f32_16x16x32_bf16 a[40:43], a[144:147], v[136:139], a[40:43]\n\t" \
  "ds_read_b64_tr_b16 v[84:85], v210 offset:8704\n\t" \
  "v_cvt_pk_bf16_f32 v35, v38, v39\n\t" \
  "v_mfma_f32_16x16x32_bf16 a[40:43], a[148:151], v[140:143], a[40:43]\n\t" \
  "ds_read_b64_tr_b16 v[86:87], v210 offset:12800\n\t" \
  "v_exp_f32 v44, v44\n\t" \
  "v_mfma_f32_16x16x32_bf16 a[44:47], a[152:155], v[136:139], a[44:47]\n\t" \
  "buffer_load_dwordx4 v249, s[24:27], s96 offen offset:3072 lds\n\t" \
  "v_mfma_f32_16x16x32_bf16 a[44:47], a[156:159], v[140:143], a[44:47]\n\t" \
  "ds_read_b64_tr_b16 v[88:89], v211 offset:512\n\t" \
  "v_exp_f32 v45, v45\n\t" \
  "v_mfma_f32_16x16x32_bf16 a[48:51], a[160:163], v[136:139], a[48:51]\n\t" \
  "ds_read_b64_tr_b16 v[90:91], v211 offset:4608\n\t" \
  "v_exp_f32 v46, v46\n\t" \
  "v_mfma_f32_16x16x32_bf16 a[48:51], a[164:167], v[140:143], a[48:51]\n\t" \
  "ds_read_b64_tr_b16 v[92:93], v211 offset:8704\n\t" \
  "v_cvt_pk_bf16_f32 v36, v40, v41\n\t" \
  "v_mfma_f32_16x16x32_bf16 a[52:55], a[168:171], v[136:139], a[52:55]\n\t" \
  "ds_read_b64_tr_b16 v[94:95], v211 offset:12800\n\t" \
  "v_exp_f32 v47, v47\n\t" \
  "v_mfma_f32_16x16x32_bf16 a[52:55], a[172:175], v[140:143], a[52:55]\n\t" \
  "v_exp_f32 v64, v64\n\t" \
  "v_mfma_f32_16x16x32_bf16 a[56:59], a[176:179], v[136:139], a[56:59]\n\t" \
  "ds_read_b64_tr_b16 v[112:113], v210 offset:768\n\t" \
  "v_add_u32 v230, 0x10000, v242\n\t" \
  "v_mfma_f32_16x16x32_bf16 a[56:59], a[180:183], v[140:143], a[56:59]\n\t" \
  "ds_read_b64_tr_b16 v[114:115], v210 offset:4864\n\t" \
  "v_cvt_pk_bf16_f32 v37, v42, v43\n\t" \
  "v_mfma_f32_16x16x32_bf16 a[60:63], a[184:187], v[136:139], a[60:63]\n\t" \
  "ds_read_b64_tr_b16 v[116:117], v210 offset:8960\n\t" \
  "v_exp_f32 v65, v65\n\t" \
  "v_mfma_f32_16x16x32_bf16 a[60:63], a[188:191], v[140:143], a[60:63]\n\t" \
  "ds_read_b64_tr_b16 v[118:119], v210 offset:13056\n\t" \
  "v_add_u32 v232, 0x20000, v242\n\t" \
  "v_smfmac_f32_16x16x64_bf16 v[162:165], v[216:219], v[136:143], v208\n\t" \
  "ds_read_b64_tr_b16 a[192:193], v234 offset:0\n\t" \
  "v_cvt_pk_bf16_f32 v38, v44, v45\n\t" \
  "v_mfma_f32_16x16x32_bf16 a[0:3], v[16:19], v[0:3], a[0:3]\n\t" \
  "ds_read_b64_tr_b16 v[120:121], v211 offset:768\n\t" \
  "v_exp_f32 v66, v66\n\t" \
  "v_mfma_f32_16x16x32_bf16 a[0:3], v[20:23], v[4:7], a[0:3]\n\t" \
  "ds_read_b64_tr_b16 v[122:123], v211 offset:4864\n\t" \
  "v_exp_f32 v67, v67\n\t" \
  "v_smfmac_f32_16x16x64_bf16 v[160:163], v[216:219], v[0:7], v208\n\t" \
  "ds_read_b64_tr_b16 v[124:125], v211 offset:8960\n\t" \
  "v_cvt_pk_bf16_f32 v39, v46, v47\n\t" \
  "v_mfma_f32_16x16x32_bf16 a[4:7], v[24:27], v[0:3], a[4:7]\n\t" \
  "v_exp_f32 v68, v68\n\t" \
  "v_mfma_f32_16x16x32_bf16 a[4:7], v[28:31], v[4:7], a[4:7]\n\t" \
  "s_waitcnt lgkmcnt(15)\n\t" \
  "v_exp_f32 v69, v69\n\t" \
  "v_mfma_f32_16x16x32_bf16 a[8:11], v[48:51], v[0:3], a[8:11]\n\t" \
  "ds_read_b64_tr_b16 v[126:127], v211 offset:13056\n\t" \
  "v_add_f32 v225, v160, v161\n\t" \
  "v_mfma_f32_16x16x32_bf16 a[8:11], v[52:55], v[4:7], a[8:11]\n\t" \
  "v_rcp_f32 v224, v225\n\t" \
  "v_mfma_f32_16x16x32_bf16 a[12:15], v[56:59], v[0:3], a[12:15]\n\t" \
  "v_accvgpr_read_b32 v128, a0\n\t" \
  "v_accvgpr_read_b32 v129, a1\n\t" \
  "v_accvgpr_read_b32 v130, a2\n\t" \
  "v_accvgpr_read_b32 v131, a3\n\t" \
  "v_accvgpr_read_b32 v132, a4\n\t" \
  "v_accvgpr_read_b32 v133, a5\n\t" \
  "v_accvgpr_read_b32 v134, a6\n\t" \
  "v_accvgpr_read_b32 v135, a7\n\t" \
  "v_pk_mul_f32 v[128:129], v[128:129], v[224:225] op_sel_hi:[1,0]\n\t" \
  "v_pk_mul_f32 v[130:131], v[130:131], v[224:225] op_sel_hi:[1,0]\n\t" \
  "v_pk_mul_f32 v[132:133], v[132:133], v[224:225] op_sel_hi:[1,0]\n\t" \
  "v_pk_mul_f32 v[134:135], v[134:135], v[224:225] op_sel_hi:[1,0]\n\t" \
  "v_cvt_pk_bf16_f32 v128, v128, v129\n\t" \
  "v_cvt_pk_bf16_f32 v129, v130, v131\n\t" \
  "v_cvt_pk_bf16_f32 v130, v132, v133\n\t" \
  "v_cvt_pk_bf16_f32 v131, v134, v135\n\t" \
  "buffer_store_dwordx4 v[128:131], v242, s[16:19], s98 offen offset:0 sc1\n\t" \
  "v_mfma_f32_16x16x32_bf16 a[12:15], v[60:63], v[4:7], a[12:15]\n\t" \
  "s_waitcnt lgkmcnt(9)\n\t" \
  "v_cmp_nge_f32 vcc, 0x5f800000, v225\n\t" \
  "v_cmp_nle_f32_e64 s[84:85], s79, v225\n\t" \
  "v_mfma_f32_16x16x32_bf16 a[16:19], v[80:83], v[0:3], a[16:19]\n\t" \
  "s_or_b64 vcc, vcc, s[84:85]\n\t" \
  "s_cselect_b32 s89, s78, s89\n\t" \
  "v_or_b32 v254, s89, v254\n\t" \
  "v_mfma_f32_16x16x32_bf16 a[16:19], v[84:87], v[4:7], a[16:19]\n\t" \
  "ds_read_b64_tr_b16 a[194:195], v234 offset:4096\n\t" \
  "v_exp_f32 v70, v70\n\t" \
  "v_mfma_f32_16x16x32_bf16 a[20:23], v[88:91], v[0:3], a[20:23]\n\t" \
  "v_accvgpr_read_b32 v136, a8\n\t" \
  "v_accvgpr_read_b32 v137, a9\n\t" \
  "v_accvgpr_read_b32 v138, a10\n\t" \
  "v_accvgpr_read_b32 v139, a11\n\t" \
  "v_accvgpr_read_b32 v140, a12\n\t" \
  "v_accvgpr_read_b32 v141, a13\n\t" \
  "v_accvgpr_read_b32 v142, a14\n\t" \
  "v_accvgpr_read_b32 v143, a15\n\t" \
  "v_pk_mul_f32 v[136:137], v[136:137], v[224:225] op_sel_hi:[1,0]\n\t" \
  "v_pk_mul_f32 v[138:139], v[138:139], v[224:225] op_sel_hi:[1,0]\n\t" \
  "v_pk_mul_f32 v[140:141], v[140:141], v[224:225] op_sel_hi:[1,0]\n\t" \
  "v_pk_mul_f32 v[142:143], v[142:143], v[224:225] op_sel_hi:[1,0]\n\t" \
  "v_cvt_pk_bf16_f32 v136, v136, v137\n\t" \
  "v_cvt_pk_bf16_f32 v137, v138, v139\n\t" \
  "v_cvt_pk_bf16_f32 v138, v140, v141\n\t" \
  "v_cvt_pk_bf16_f32 v139, v142, v143\n\t" \
  "buffer_store_dwordx4 v[136:139], v242, s[16:19], s98 offen offset:1024 sc1\n\t" \
  "v_mfma_f32_16x16x32_bf16 a[20:23], v[92:95], v[4:7], a[20:23]\n\t" \
  "s_waitcnt lgkmcnt(1)\n\t" \
  "ds_read_b64_tr_b16 a[196:197], v235 offset:0\n\t" \
  "v_exp_f32 v71, v71\n\t" \
  "v_mfma_f32_16x16x32_bf16 a[24:27], v[112:115], v[0:3], a[24:27]\n\t" \
  "s_add_u32 s96, s40, 786432\n\t" \
  "ds_read_b64_tr_b16 a[198:199], v235 offset:4096\n\t" \
  "v_exp_f32 v72, v72\n\t" \
  "v_mfma_f32_16x16x32_bf16 a[24:27], v[116:119], v[4:7], a[24:27]\n\t" \
  "s_and_b32 s96, s96, 0x1c0000\n\t" \
  "v_exp_f32 v73, v73\n\t" \
  "v_mfma_f32_16x16x32_bf16 a[28:31], v[120:123], v[0:3], a[28:31]\n\t" \
  "s_add_u32 s96, s96, s77\n\t" \
  "v_exp_f32 v74, v74\n\t" \
  "v_mfma_f32_16x16x32_bf16 a[28:31], v[124:127], v[4:7], a[28:31]\n\t" \
  "s_add_u32 m0, s38, 147456\n\t" \
  "v_exp_f32 v75, v75\n\t" \
  "v_mfma_f32_16x16x32_bf16 a[64:67], a[128:131], v[144:147], a[64:67]\n\t" \
  "s_barrier\n\t" \
  "v_mfma_f32_16x16x32_bf16 a[64:67], a[132:135], v[148:151], a[64:67]\n\t" \
  "ds_read_b64_tr_b16 a[200:201], v234 offset:8192\n\t" \
  "v_cvt_pk_bf16_f32 v64, v64, v65\n\t" \
  "v_mfma_f32_16x16x32_bf16 a[68:71], a[136:139], v[144:147], a[68:71]\n\t" \
  "v_accvgpr_read_b32 v0, a16\n\t" \
  "v_accvgpr_read_b32 v1, a17\n\t" \
  "v_accvgpr_read_b32 v2, a18\n\t" \
  "v_accvgpr_read_b32 v3, a19\n\t" \
  "v_accvgpr_read_b32 v4, a20\n\t" \
  "v_accvgpr_read_b32 v5, a21\n\t" \
  "v_accvgpr_read_b32 v6, a22\n\t" \
  "v_accvgpr_read_b32 v7, a23\n\t" \
  "v_pk_mul_f32 v[0:1], v[0:1], v[224:225] op_sel_hi:[1,0]\n\t" \
  "v_pk_mul_f32 v[2:3], v[2:3], v[224:225] op_sel_hi:[1,0]\n\t" \
  "v_pk_mul_f32 v[4:5], v[4:5], v[224:225] op_sel_hi:[1,0]\n\t" \
  "v_pk_mul_f32 v[6:7], v[6:7], v[224:225] op_sel_hi:[1,0]\n\t" \
  "v_cvt_pk_bf16_f32 v0, v0, v1\n\t" \
  "v_cvt_pk_bf16_f32 v1, v2, v3\n\t" \
  "v_cvt_pk_bf16_f32 v2, v4, v5\n\t" \
  "v_cvt_pk_bf16_f32 v3, v6, v7\n\t" \
  "buffer_store_dwordx4 v[0:3], v242, s[16:19], s98 offen offset:2048 sc1\n\t" \
  "v_mfma_f32_16x16x32_bf16 a[68:71], a[140:143], v[148:151], a[68:71]\n\t" \
  "v_accvgpr_read_b32 v8, a24\n\t" \
  "v_accvgpr_read_b32 v9, a25\n\t" \
  "v_accvgpr_read_b32 v10, a26\n\t" \
  "v_accvgpr_read_b32 v11, a27\n\t" \
  "v_accvgpr_read_b32 v12, a28\n\t" \
  "v_accvgpr_read_b32 v13, a29\n\t" \
  "v_accvgpr_read_b32 v14, a30\n\t" \
  "v_accvgpr_read_b32 v15, a31\n\t" \
  "v_pk_mul_f32 v[8:9], v[8:9], v[224:225] op_sel_hi:[1,0]\n\t" \
  "v_pk_mul_f32 v[10:11], v[10:11], v[224:225] op_sel_hi:[1,0]\n\t" \
  "v_pk_mul_f32 v[12:13], v[12:13], v[224:225] op_sel_hi:[1,0]\n\t" \
  "v_pk_mul_f32 v[14:15], v[14:15], v[224:225] op_sel_hi:[1,0]\n\t" \
  "v_cvt_pk_bf16_f32 v8, v8, v9\n\t" \
  "v_cvt_pk_bf16_f32 v9, v10, v11\n\t" \
  "v_cvt_pk_bf16_f32 v10, v12, v13\n\t" \
  "v_cvt_pk_bf16_f32 v11, v14, v15\n\t" \
  "buffer_store_dwordx4 v[8:11], v242, s[16:19], s98 offen offset:3072 sc1\n\t" \
  "v_mfma_f32_16x16x32_bf16 a[72:75], a[144:147], v[144:147], a[72:75]\n\t" \
  "buffer_load_dwordx4 v240, s[24:27], s96 offen offset:0 lds\n\t" \
  "v_mfma_f32_16x16x32_bf16 a[72:75], a[148:151], v[148:151], a[72:75]\n\t" \
  "s_xor_b32 s98, s45, 32768\n\t" \
  "ds_read_b64_tr_b16 a[202:203], v234 offset:12288\n\t" \
  "v_exp_f32 v76, v76\n\t" \
  "v_mfma_f32_16x16x32_bf16 a[76:79], a[152:155], v[144:147], a[76:79]\n\t" \
  "ds_read_b64_tr_b16 a[204:205], v235 offset:8192\n\t" \
  "ds_read_b128 a[224:227], v213 offset:16384\n\t" \
  "v_exp_f32 v77, v77\n\t" \
  "v_mfma_f32_16x16x32_bf16 a[76:79], a[156:159], v[148:151], a[76:79]\n\t" \
  "ds_read_b64_tr_b16 a[206:207], v235 offset:12288\n\t" \
  "ds_read_b128 a[228:231], v213 offset:17408\n\t" \
  "v_cvt_pk_bf16_f32 v65, v66, v67\n\t" \
  "v_mfma_f32_16x16x32_bf16 a[80:83], a[160:163], v[144:147], a[80:83]\n\t" \
  "ds_read_b64_tr_b16 a[208:209], v236 offset:32768\n\t" \
  "ds_read_b128 a[232:235], v213 offset:18432\n\t" \
  "v_cvt_pk_bf16_f32 v66, v68, v69\n\t" \
  "v_mfma_f32_16x16x32_bf16 a[80:83], a[164:167], v[148:151], a[80:83]\n\t" \
  "ds_read_b128 a[236:239], v213 offset:19456\n\t" \
  "v_cvt_pk_bf16_f32 v67, v70, v71\n\t" \
  "v_mfma_f32_16x16x32_bf16 a[84:87], a[168:171], v[144:147], a[84:87]\n\t" \
  "buffer_load_dwordx4 v241, s[24:27], s96 offen offset:1024 lds\n\t" \
  "ds_write_b128 v213, v[128:131] offset:16384\n\t" \
  "v_mfma_f32_16x16x32_bf16 a[84:87], a[172:175], v[148:151], a[84:87]\n\t" \
  "ds_write_b128 v213, v[136:139] offset:17408\n\t" \
  "v_mfma_f32_16x16x32_bf16 a[88:91], a[176:179], v[144:147], a[88:91]\n\t" \
  "ds_write_b128 v213, v[0:3] offset:18432\n\t" \
  "v_mfma_f32_16x16x32_bf16 a[88:91], a[180:183], v[148:151], a[88:91]\n\t" \
  "ds_write_b128 v213, v[8:11] offset:19456\n\t" \
  "v_mfma_f32_16x16x32_bf16 a[92:95], a[184:187], v[144:147], a[92:95]\n\t" \
  "buffer_load_dwordx4 v248, s[24:27], s96 offen offset:2048 lds\n\t" \
  "v_mfma_f32_16x16x32_bf16 a[92:95], a[188:191], v[148:151], a[92:95]\n\t" \
  "ds_read_b64_tr_b16 a[210:211], v236 offset:36864\n\t" \
  "v_exp_f32 v78, v78\n\t" \
  "v_smfmac_f32_16x16x64_bf16 v[164:167], v[216:219], v[144:151], v208\n\t" \
  "buffer_load_dwordx4 v249, s[24:27], s96 offen offset:3072 lds\n\t" \
  "v_mfma_f32_16x16x32_bf16 a[32:35], v[16:19], v[32:35], a[32:35]\n\t" \
  "ds_read_b64_tr_b16 a[212:213], v237 offset:32768\n\t" \
  "v_exp_f32 v79, v79\n\t" \
  "v_mfma_f32_16x16x32_bf16 a[32:35], v[20:23], v[36:39], a[32:35]\n\t" \
  "v_exp_f32 v96, v96\n\t" \
  "s_nop 1\n\t" \
  "v_smfmac_f32_16x16x64_bf16 v[162:165], v[216:219], v[32:39], v208\n\t" \
  "ds_read_b64_tr_b16 a[214:215], v237 offset:36864\n\t" \
  "v_cvt_pk_bf16_f32 v68, v72, v73\n\t" \
  "v_mfma_f32_16x16x32_bf16 a[36:39], v[24:27], v[32:35], a[36:39]\n\t" \
  "v_cvt_pk_bf16_f32 v69, v74, v75\n\t" \
  "v_exp_f32 v97, v97\n\t" \
  "v_mfma_f32_16x16x32_bf16 a[36:39], v[28:31], v[36:39], a[36:39]\n\t" \
  "v_cvt_pk_bf16_f32 v70, v76, v77\n\t" \
  "v_cvt_pk_bf16_f32 v71, v78, v79\n\t" \
  "v_add_f32 v227, v162, v163\n\t" \
  "v_rcp_f32 v226, v227\n\t" \
  "v_mfma_f32_16x16x32_bf16 a[40:43], v[48:51], v[32:35], a[40:43]\n\t" \
  "v_cmp_nge_f32 vcc, 0x5f800000, v227\n\t" \
  "v_cmp_nle_f32_e64 s[84:85], s79, v227\n\t" \
  "v_accvgpr_read_b32 v0, a32\n\t" \
  "v_accvgpr_read_b32 v1, a33\n\t" \
  "v_mfma_f32_16x16x32_bf16 a[40:43], v[52:55], v[36:39], a[40:43]\n\t" \
  "s_or_b64 vcc, vcc, s[84:85]\n\t" \
  "s_cselect_b32 s89, s78, s89\n\t" \
  "v_or_b32 v254, s89, v254\n\t" \
  "v_accvgpr_read_b32 v2, a34\n\t" \
  "v_accvgpr_read_b32 v3, a35\n\t" \
  "v_mfma_f32_16x16x32_bf16 a[44:47], v[56:59], v[32:35], a[44:47]\n\t" \
  "ds_read_b64_tr_b16 a[216:217], v236 offset:40960\n\t" \
  "v_exp_f32 v98, v98\n\t" \
  "v_accvgpr_read_b32 v4, a36\n\t" \
  "v_accvgpr_read_b32 v5, a37\n\t" \
  "v_mfma_f32_16x16x32_bf16 a[44:47], v[60:63], v[36:39], a[44:47]\n\t" \
  "v_accvgpr_read_b32 v6, a38\n\t" \
  "v_accvgpr_read_b32 v7, a39\n\t" \
  "v_mfma_f32_16x16x32_bf16 a[48:51], v[80:83], v[32:35], a[48:51]\n\t" \
  "v_mul_f32 v0, v0, v226\n\t" \
  "v_mul_f32 v1, v1, v226\n\t" \
  "v_mfma_f32_16x16x32_bf16 a[48:51], v[84:87], v[36:39], a[48:51]\n\t" \
  "v_mul_f32 v2, v2, v226\n\t" \
  "v_mul_f32 v3, v3, v226\n\t" \
  "v_mfma_f32_16x16x32_bf16 a[52:55], v[88:91], v[32:35], a[52:55]\n\t" \
  "v_mul_f32 v4, v4, v226\n\t" \
  "v_exp_f32 v99, v99\n\t" \
  "v_mfma_f32_16x16x32_bf16 a[52:55], v[92:95], v[36:39], a[52:55]\n\t" \
  "v_mul_f32 v5, v5, v226\n\t" \
  "v_mul_f32 v6, v6, v226\n\t" \
  "v_mfma_f32_16x16x32_bf16 a[56:59], v[112:115], v[32:35], a[56:59]\n\t" \
  ".Lhs10:\n\ts_cbranch_execz .Lhs10n\n\t" \
  "v_mul_f32 v7, v7, v226\n\t" \
  "v_exp_f32 v100, v100\n\t" \
  "v_mfma_f32_16x16x32_bf16 a[56:59], v[116:119], v[36:39], a[56:59]\n\t" \
  "ds_read_b64_tr_b16 a[218:219], v236 offset:45056\n\t" \
  "v_cvt_pk_bf16_f32 v96, v96, v97\n\t" \
  "v_mfma_f32_16x16x32_bf16 a[60:63], v[120:123], v[32:35], a[60:63]\n\t" \
  "v_exp_f32 v101, v101\n\t" \
  "v_mfma_f32_16x16x32_bf16 a[60:63], v[124:127], v[36:39], a[60:63]\n\t" \
  "v_cvt_pk_bf16_f32 v32, v0, v1\n\t" \
  "v_cvt_pk_bf16_f32 v33, v2, v3\n\t" \
  "v_cvt_pk_bf16_f32 v34, v4, v5\n\t" \
  "v_cvt_pk_bf16_f32 v35, v6, v7\n\t" \
  "v_mfma_f32_16x16x32_bf16 a[96:99], a[128:131], v[152:155], a[96:99]\n\t" \
  "v_accvgpr_read_b32 v8, a40\n\t" \
  "v_accvgpr_read_b32 v9, a41\n\t" \
  "v_accvgpr_read_b32 v10, a42\n\t" \
  "v_accvgpr_read_b32 v11, a43\n\t" \
  "v_mfma_f32_16x16x32_bf16 a[96:99], a[132:135], v[156:159], a[96:99]\n\t" \
  "s_waitcnt vmcnt(8) lgkmcnt(13)\n\t" \
  "v_accvgpr_read_b32 v12, a44\n\t" \
  "v_accvgpr_read_b32 v13, a45\n\t" \
  "v_accvgpr_read_b32 v14, a46\n\t" \
  "v_accvgpr_read_b32 v15, a47\n\t" \
  "v_mfma_f32_16x16x32_bf16 a[100:103], a[136:139], v[152:155], a[100:103]\n\t" \
  "v_mul_f32 v8, v8, v226\n\t" \
  "v_mul_f32 v9, v9, v226\n\t" \
  "s_barrier\n\t" \
  "v_mfma_f32_16x16x32_bf16 a[100:103], a[140:143], v[156:159], a[100:103]\n\t" \
  "buffer_store_dwordx4 v[32:35], v242, s[16:19], s98 offen offset:0 sc1\n\t" \
  "v_mul_f32 v10, v10, v226\n\t" \
  "v_mul_f32 v11, v11, v226\n\t" \
  "v_mul_f32 v12, v12, v226\n\t" \
  "v_mfma_f32_16x16x32_bf16 a[104:107], a[144:147], v[152:155], a[104:107]\n\t" \
  "v_mul_f32 v13, v13, v226\n\t" \
  "v_mul_f32 v14, v14, v226\n\t" \
  "v_mul_f32 v15, v15, v226\n\t" \
  "v_cvt_pk_bf16_f32 v36, v8, v9\n\t" \
  "v_mfma_f32_16x16x32_bf16 a[104:107], a[148:151], v[156:159], a[104:107]\n\t" \
  "v_cvt_pk_bf16_f32 v37, v10, v11\n\t" \
  "v_cvt_pk_bf16_f32 v38, v12, v13\n\t" \
  "v_cvt_pk_bf16_f32 v39, v14, v15\n\t" \
  "v_accvgpr_read_b32 v0, a48\n\t" \
  "v_mfma_f32_16x16x32_bf16 a[108:111], a[152:155], v[152:155], a[108:111]\n\t" \
  "v_accvgpr_read_b32 v1, a49\n\t" \
  "v_accvgpr_read_b32 v2, a50\n\t" \
  "v_accvgpr_read_b32 v3, a51\n\t" \
  "v_accvgpr_read_b32 v4, a52\n\t" \
  "v_mfma_f32_16x16x32_bf16 a[108:111], a[156:159], v[156:159], a[108:111]\n\t" \
  "v_accvgpr_read_b32 v5, a53\n\t" \
  "v_accvgpr_read_b32 v6, a54\n\t" \
  "v_accvgpr_read_b32 v7, a55\n\t" \
  "v_exp_f32 v102, v102\n\t" \
  "v_mfma_f32_16x16x32_bf16 a[112:115], a[160:163], v[152:155], a[112:115]\n\t" \
  "v_mul_f32 v0, v0, v226\n\t" \
  "v_mul_f32 v1, v1, v226\n\t" \
  "v_mul_f32 v2, v2, v226\n\t" \
  "v_exp_f32 v103, v103\n\t" \
  "v_mfma_f32_16x16x32_bf16 a[112:115], a[164:167], v[156:159], a[112:115]\n\t" \
  "v_mul_f32 v3, v3, v226\n\t" \
  "v_mul_f32 v4, v4, v226\n\t" \
  "v_mul_f32 v5, v5, v226\n\t" \
  "v_mul_f32 v6, v6, v226\n\t" \
  "v_mfma_f32_16x16x32_bf16 a[116:119], a[168:171], v[152:155], a[116:119]\n\t" \
  "v_mul_f32 v7, v7, v226\n\t" \
  "v_cvt_pk_bf16_f32 v40, v0, v1\n\t" \
  "v_cvt_pk_bf16_f32 v41, v2, v3\n\t" \
  "v_exp_f32 v104, v104\n\t" \
  "v_mfma_f32_16x16x32_bf16 a[116:119], a[172:175], v[156:159], a[116:119]\n\t" \
  "v_cvt_pk_bf16_f32 v42, v4, v5\n\t" \
  "v_cvt_pk_bf16_f32 v43, v6, v7\n\t" \
  "v_accvgpr_read_b32 v8, a56\n\t" \
  "v_exp_f32 v105, v105\n\t" \
  "v_mfma_f32_16x16x32_bf16 a[120:123], a[176:179], v[152:155], a[120:123]\n\t" \
  "buffer_store_dwordx4 v[36:39], v242, s[16:19], s98 offen offset:1024 sc1\n\t" \
  "v_accvgpr_read_b32 v9, a57\n\t" \
  "v_accvgpr_read_b32 v10, a58\n\t" \
  "v_accvgpr_read_b32 v11, a59\n\t" \
  "v_mfma_f32_16x16x32_bf16 a[120:123], a[180:183], v[156:159], a[120:123]\n\t" \
  "v_accvgpr_read_b32 v12, a60\n\t" \
  "v_accvgpr_read_b32 v13, a61\n\t" \
  "v_accvgpr_read_b32 v14, a62\n\t" \
  "v_accvgpr_read_b32 v15, a63\n\t" \
  "v_mfma_f32_16x16x32_bf16 a[124:127], a[184:187], v[152:155], a[124:127]\n\t" \
  "v_cvt_pk_bf16_f32 v97, v98, v99\n\t" \
  "v_mul_f32 v8, v8, v226\n\t" \
  "v_mul_f32 v9, v9, v226\n\t" \
  "v_exp_f32 v106, v106\n\t" \
  "v_mfma_f32_16x16x32_bf16 a[124:127], a[188:191], v[156:159], a[124:127]\n\t" \
  "v_mul_f32 v10, v10, v226\n\t" \
  "v_mul_f32 v11, v11, v226\n\t" \
  "v_mul_f32 v12, v12, v226\n\t" \
  "v_mul_f32 v13, v13, v226\n\t" \
  "v_smfmac_f32_16x16x64_bf16 v[166:169], v[216:219], v[152:159], v208\n\t" \
  "ds_read_b64_tr_b16 a[220:221], v237 offset:40960\n\t" \
  "v_cvt_pk_bf16_f32 v98, v100, v101\n\t" \
  "v_mul_f32 v14, v14, v226\n\t" \
  "v_exp_f32 v107, v107\n\t" \
  "v_mfma_f32_16x16x32_bf16 a[64:67], v[16:19], v[64:67], a[64:67]\n\t" \
  "s_waitcnt vmcnt(6)\n\t" \
  "buffer_store_dword v243, v254, s[16:19], s49 offen offset:0 sc1\n\t" \
  "ds_read_b64_tr_b16 a[222:223], v237 offset:45056\n\t" \
  "v_mfma_f32_16x16x32_bf16 a[64:67], v[20:23], v[68:71], a[64:67]\n\t" \
  "v_cvt_pk_bf16_f32 v99, v102, v103\n\t" \
  "v_exp_f32 v108, v108\n\t" \
  "v_mul_f32 v15, v15, v226\n\t" \
  "v_smfmac_f32_16x16x64_bf16 v[164:167], v[216:219], v[64:71], v208\n\t" \
  "ds_read_b64_tr_b16 a[128:129], v234 offset:49152\n\t" \
  "v_exp_f32 v109, v109\n\t" \
  "v_mfma_f32_16x16x32_bf16 a[68:71], v[24:27], v[64:67], a[68:71]\n\t" \
  "v_cvt_pk_bf16_f32 v100, v104, v105\n\t" \
  "v_cvt_pk_bf16_f32 v44, v8, v9\n\t" \
  "v_mfma_f32_16x16x32_bf16 a[68:71], v[28:31], v[68:71], a[68:71]\n\t" \
  "buffer_store_dwordx4 v[40:43], v242, s[16:19], s98 offen offset:2048 sc1\n\t" \
  "v_exp_f32 v110, v110\n\t" \
  "v_add_f32 v221, v164, v165\n\t" \
  "v_rcp_f32 v220, v221\n\t" \
  "v_mfma_f32_16x16x32_bf16 a[72:75], v[48:51], v[64:67], a[72:75]\n\t" \
  "v_cmp_nge_f32 vcc, 0x5f800000, v221\n\t" \
  "v_cmp_nle_f32_e64 s[84:85], s79, v221\n\t" \
  "v_mfma_f32_16x16x32_bf16 a[72:75], v[52:55], v[68:71], a[72:75]\n\t" \
  "s_or_b64 vcc, vcc, s[84:85]\n\t" \
  "s_cselect_b32 s89, s78, s89\n\t" \
  "v_or_b32 v254, s89, v254\n\t" \
  "v_mfma_f32_16x16x32_bf16 a[76:79], v[56:59], v[64:67], a[76:79]\n\t" \
  "v_cvt_pk_bf16_f32 v101, v106, v107\n\t" \
  "v_cvt_pk_bf16_f32 v45, v10, v11\n\t" \
  "v_mfma_f32_16x16x32_bf16 a[76:79], v[60:63], v[68:71], a[76:79]\n\t" \
  "v_cvt_pk_bf16_f32 v102, v108, v109\n\t" \
  "v_cvt_pk_bf16_f32 v46, v12, v13\n\t" \
  "v_mfma_f32_16x16x32_bf16 a[80:83], v[80:83], v[64:67], a[80:83]\n\t" \
  "ds_read_b64_tr_b16 a[130:131], v234 offset:53248\n\t" \
  "v_exp_f32 v111, v111\n\t" \
  "v_mfma_f32_16x16x32_bf16 a[80:83], v[84:87], v[68:71], a[80:83]\n\t" \
  "v_cvt_pk_bf16_f32 v103, v110, v111\n\t" \
  "v_cvt_pk_bf16_f32 v47, v14, v15\n\t" \
  "v_mfma_f32_16x16x32_bf16 a[84:87], v[88:91], v[64:67], a[84:87]\n\t" \
  "ds_read_b64_tr_b16 a[132:133], v235 offset:49152\n\t" \
  "v_mfma_f32_16x16x32_bf16 a[84:87], v[92:95], v[68:71], a[84:87]\n\t" \
  "s_add_u32 s96, s40, 1048576\n\t" \
  "ds_read_b128 a[240:243], v213 offset:32768\n\t" \
  "s_and_b32 s96, s96, 0x1c0000\n\t" \
  "v_mfma_f32_16x16x32_bf16 a[88:91], v[112:115], v[64:67], a[88:91]\n\t" \
  "buffer_store_dwordx4 v[44:47], v242, s[16:19], s98 offen offset:3072 sc1\n\t" \
  "ds_read_b128 a[244:247], v213 offset:33792\n\t" \
  "ds_read_b128 a[248:251], v213 offset:34816\n\t" \
  "ds_read_b64_tr_b16 a[134:135], v235 offset:53248\n\t" \
  "v_mfma_f32_16x16x32_bf16 a[88:91], v[116:119], v[68:71], a[88:91]\n\t" \
  "s_add_u32 s96, s96, s51\n\t" \
  "ds_read_b128 a[252:255], v213 offset:35840\n\t" \
  "s_xor_b32 s98, s45, 65536\n\t" \
  "v_mfma_f32_16x16x32_bf16 a[92:95], v[120:123], v[64:67], a[92:95]\n\t" \
  "ds_write_b128 v213, v[32:35] offset:32768\n\t" \
  "v_mfma_f32_16x16x32_bf16 a[92:95], v[124:127], v[68:71], a[92:95]\n\t" \
  "s_waitcnt vmcnt(5) lgkmcnt(9)\n\t" \
  "s_add_u32 m0, s38, 98304\n\t" \
  "v_accvgpr_read_b32 v64, a64\n\t" \
  "v_accvgpr_read_b32 v65, a65\n\t" \
  "v_accvgpr_read_b32 v66, a66\n\t" \
  "v_mfma_f32_16x16x32_bf16 a[96:99], v[16:19], v[96:99], a[96:99]\n\t" \
  "v_accvgpr_read_b32 v67, a67\n\t" \
  "v_accvgpr_read_b32 v68, a68\n\t" \
  "s_barrier\n\t" \
  "v_mfma_f32_16x16x32_bf16 a[96:99], v[20:23], v[100:103], a[96:99]\n\t" \
  "buffer_load_dwordx4 v240, s[24:27], s96 offen offset:0 lds\n\t" \
  "v_accvgpr_read_b32 v69, a69\n\t" \
  "v_accvgpr_read_b32 v70, a70\n\t" \
  "v_smfmac_f32_16x16x64_bf16 v[166:169], v[216:219], v[96:103], v208\n\t" \
  "ds_read_b64_tr_b16 a[136:137], v234 offset:57344\n\t" \
  "ds_read_b128 v[128:131], v242 offset:16384\n\t" \
  "v_accvgpr_read_b32 v71, a71\n\t" \
  "v_mfma_f32_16x16x32_bf16 a[100:103], v[24:27], v[96:99], a[100:103]\n\t" \
  "ds_read_b128 v[132:135], v242 offset:17408\n\t" \
  "v_mul_f32 v64, v64, v220\n\t" \
  "v_mfma_f32_16x16x32_bf16 a[100:103], v[28:31], v[100:103], a[100:103]\n\t" \
  "ds_write_b128 v213, v[36:39] offset:33792\n\t" \
  "v_mfma_f32_16x16x32_bf16 a[104:107], v[48:51], v[96:99], a[104:107]\n\t" \
  "buffer_load_dwordx4 v241, s[24:27], s96 offen offset:1024 lds\n\t" \
  "v_mul_f32 v65, v65, v220\n\t" \
  "v_mfma_f32_16x16x32_bf16 a[104:107], v[52:55], v[100:103], a[104:107]\n\t" \
  "v_add_f32 v223, v166, v167\n\t" \
  "ds_read_b128 v[136:139], v242 offset:18432\n\t" \
  "v_rcp_f32 v222, v223\n\t" \
  "v_mfma_f32_16x16x32_bf16 a[108:111], v[56:59], v[96:99], a[108:111]\n\t" \
  "v_cmp_nge_f32 vcc, 0x5f800000, v223\n\t" \
  "v_cmp_nle_f32_e64 s[84:85], s79, v223\n\t" \
  "v_mfma_f32_16x16x32_bf16 a[108:111], v[60:63], v[100:103], a[108:111]\n\t" \
  "s_or_b64 vcc, vcc, s[84:85]\n\t" \
  "s_cselect_b32 s89, s78, s89\n\t" \
  "v_or_b32 v254, s89, v254\n\t" \
  "v_mul_f32 v66, v66, v220\n\t" \
  "v_mfma_f32_16x16x32_bf16 a[112:115], v[80:83], v[96:99], a[112:115]\n\t" \
  "buffer_load_dwordx4 v248, s[24:27], s96 offen offset:2048 lds\n\t" \
  "v_mfma_f32_16x16x32_bf16 a[112:115], v[84:87], v[100:103], a[112:115]\n\t" \
  "s_cmp_lg_u32 s89, 0\n\t" \
  "s_cbranch_scc1 .Lat_x\n\t" \
  ".Lat_y:\n\t" \
  "v_mfma_f32_16x16x32_bf16 a[116:119], v[88:91], v[96:99], a[116:119]\n\t" \
  "buffer_load_dwordx4 v249, s[24:27], s96 offen offset:3072 lds\n\t" \
  "ds_read_b128 v[140:143], v242 offset:19456\n\t" \
  "v_mfma_f32_16x16x32_bf16 a[116:119], v[92:95], v[100:103], a[116:119]\n\t" \
  "ds_read_b128 v[144:147], v242 offset:20480\n\t" \
  "v_mul_f32 v67, v67, v220\n\t" \
  "v_mfma_f32_16x16x32_bf16 a[120:123], v[112:115], v[96:99], a[120:123]\n\t" \
  "ds_read_b128 v[148:151], v242 offset:21504\n\t" \
  "v_mul_f32 v68, v68, v220\n\t" \
  "v_mfma_f32_16x16x32_bf16 a[120:123], v[116:119], v[100:103], a[120:123]\n\t" \
  "ds_write_b128 v213, v[40:43] offset:34816\n\t" \
  "v_mfma_f32_16x16x32_bf16 a[124:127], v[120:123], v[96:99], a[124:127]\n\t" \
  "ds_read_b64_tr_b16 a[138:139], v234 offset:61440\n\t" \
  "ds_read_b128 v[152:155], v242 offset:22528\n\t" \
  "v_mul_f32 v69, v69, v220\n\t" \
  "v_mul_f32 v70, v70, v220\n\t" \
  "v_mfma_f32_16x16x32_bf16 a[124:127], v[124:127], v[100:103], a[124:127]\n\t" \
  "s_waitcnt lgkmcnt(5)\n\t" \
  "ds_read_b64_tr_b16 a[140:141], v235 offset:57344\n\t" \
  "ds_read_b128 v[156:159], v242 offset:23552\n\t" \
  "v_mul_f32 v71, v71, v220\n\t" \
  ".Lat_join:\n\t" \
  STS("s_memrealtime s[94:95]\n\t") \
  STS("s_memtime s[92:93]\n\t") \
  "v_mfma_f32_16x16x32_bf16 v[0:3], a[192:195], v[128:131], 0\n\t" \
  "v_cvt_pk_bf16_f32 v112, v64, v65\n\t" \
  "v_cvt_pk_bf16_f32 v113, v66, v67\n\t" \
  "v_cvt_pk_bf16_f32 v114, v68, v69\n\t" \
  "v_cvt_pk_bf16_f32 v115, v70, v71\n\t" \
  "v_mfma_f32_16x16x32_bf16 v[0:3], a[200:203], v[132:135], v[0:3]\n\t" \
  "buffer_load_dword v246, v253, s[16:19], s65 offen sc1\n\t" \
  "v_accvgpr_read_b32 v72, a72\n\t" \
  "v_accvgpr_read_b32 v73, a73\n\t" \
  "v_accvgpr_read_b32 v74, a74\n\t" \
  "v_mfma_f32_16x16x32_bf16 v[0:3], a[208:211], v[136:139], v[0:3]\n\t" \
  "v_accvgpr_read_b32 v75, a75\n\t" \
  "v_accvgpr_read_b32 v76, a76\n\t" \
  "v_accvgpr_read_b32 v77, a77\n\t" \
  "v_accvgpr_read_b32 v78, a78\n\t" \
  "v_mfma_f32_16x16x32_bf16 v[0:3], a[216:219], v[140:143], v[0:3]\n\t" \
  "ds_read_b128 v[160:163], v242 offset:24576\n\t" \
  "v_accvgpr_read_b32 v79, a79\n\t" \
  "v_mul_f32 v72, v72, v220\n\t" \
  "v_mul_f32 v73, v73, v220\n\t" \
  "v_mfma_f32_16x16x32_bf16 v[16:19], a[220:223], v[140:143], 0\n\t" \
  "ds_read_b128 v[164:167], v242 offset:25600\n\t" \
  "v_mul_f32 v74, v74, v220\n\t" \
  "v_mul_f32 v75, v75, v220\n\t" \
  "v_mul_f32 v76, v76, v220\n\t" \
  "v_mfma_f32_16x16x32_bf16 v[16:19], a[212:215], v[136:139], v[16:19]\n\t" \
  "ds_read_b128 v[168:171], v242 offset:26624\n\t" \
  "v_mul_f32 v77, v77, v220\n\t" \
  "v_mul_f32 v78, v78, v220\n\t" \
  "v_mul_f32 v79, v79, v220\n\t" \
  "v_mfma_f32_16x16x32_bf16 v[16:19], a[204:207], v[132:135], v[16:19]\n\t" \
  "ds_read_b128 v[172:175], v242 offset:27648\n\t" \
  "v_cvt_pk_bf16_f32 v116, v72, v73\n\t" \
  "v_cvt_pk_bf16_f32 v117, v74, v75\n\t" \
  "v_cvt_pk_bf16_f32 v118, v76, v77\n\t" \
  "v_mfma_f32_16x16x32_bf16 v[16:19], a[196:199], v[128:131], v[16:19]\n\t" \
  "s_waitcnt lgkmcnt(4)\n\t" \
  "ds_read_b64_tr_b16 a[142:143], v235 offset:61440\n\t" \
  "v_cvt_pk_bf16_f32 v119, v78, v79\n\t" \
  "v_accvgpr_read_b32 v64, a80\n\t" \
  "v_accvgpr_read_b32 v65, a81\n\t" \
  "v_mfma_f32_16x16x32_bf16 v[4:7], a[192:195], v[144:147], 0\n\t" \
  "buffer_store_dwordx4 v[112:115], v242, s[16:19], s98 offen offset:0 sc1\n\t" \
  "v_accvgpr_read_b32 v66, a82\n\t" \
  "ds_read_b128 v[128:131], v242 offset:28672\n\t" \
  "ds_write_b128 v213, v[44:47] offset:35840\n\t" \
  "v_mfma_f32_16x16x32_bf16 v[4:7], a[200:203], v[148:151], v[4:7]\n\t" \
  "v_accvgpr_read_b32 v67, a83\n\t" \
  "v_accvgpr_read_b32 v68, a84\n\t" \
  "v_accvgpr_read_b32 v69, a85\n\t" \
  "v_accvgpr_read_b32 v70, a86\n\t" \
  "v_mfma_f32_16x16x32_bf16 v[4:7], a[208:211], v[152:155], v[4:7]\n\t" \
  "ds_read_b64_tr_b16 a[144:145], v236 offset:0\n\t" \
  "ds_read_b128 v[132:135], v242 offset:29696\n\t" \
  "v_accvgpr_read_b32 v71, a87\n\t" \
  "v_mul_f32 v64, v64, v220\n\t" \
  "v_mfma_f32_16x16x32_bf16 v[4:7], a[216:219], v[156:159], v[4:7]\n\t" \
  "v_mul_f32 v65, v65, v220\n\t" \
  "v_mul_f32 v66, v66, v220\n\t" \
  "v_mul_f32 v67, v67, v220\n\t" \
  "v_mul_f32 v68, v68, v220\n\t" \
  "v_mfma_f32_16x16x32_bf16 v[20:23], a[220:223], v[156:159], 0\n\t" \
  STS("v_writelane_b32 v255, s94, 22\n\t") \
  STS("v_writelane_b32 v255, s92, 23\n\t") \
  "ds_read_b64_tr_b16 a[146:147], v236 offset:4096\n\t" \
  "ds_read_b128 v[136:139], v242 offset:30720\n\t" \
  "v_mul_f32 v69, v69, v220\n\t" \
  "v_mul_f32 v70, v70, v220\n\t" \
  "v_mfma_f32_16x16x32_bf16 v[20:23], a[212:215], v[152:155], v[20:23]\n\t" \
  "v_mul_f32 v71, v71, v220\n\t" \
  "v_cvt_pk_bf16_f32 v120, v64, v65\n\t" \
  "v_cvt_pk_bf16_f32 v121, v66, v67\n\t" \
  "v_cvt_pk_bf16_f32 v122, v68, v69\n\t" \
  "v_mfma_f32_16x16x32_bf16 v[20:23], a[204:207], v[148:151], v[20:23]\n\t" \
  "s_waitcnt lgkmcnt(4)\n\t" \
  "ds_read_b64_tr_b16 a[148:149], v237 offset:0\n\t" \
  "ds_read_b128 v[140:143], v242 offset:31744\n\t" \
  "v_cvt_pk_bf16_f32 v123, v70, v71\n\t" \
  "v_accvgpr_read_b32 v72, a88\n\t" \
  "v_mfma_f32_16x16x32_bf16 v[20:23], a[196:199], v[144:147], v[20:23]\n\t" \
  "v_accvgpr_read_b32 v73, a89\n\t" \
  "v_accvgpr_read_b32 v74, a90\n\t" \
  "s_barrier\n\t" \
  "v_mfma_f32_16x16x32_bf16 v[8:11], a[192:195], v[160:163], 0\n\t" \
  "ds_read_b128 v[144:147], v242 offset:32768\n\t" \
  "v_accvgpr_read_b32 v75, a91\n\t" \
  "v_accvgpr_read_b32 v76, a92\n\t" \
  "v_accvgpr_read_b32 v77, a93\n\t" \
  "v_mfma_f32_16x16x32_bf16 v[8:11], a[200:203], v[164:167], v[8:11]\n\t" \
  "ds_read_b64_tr_b16 a[150:151], v237 offset:4096\n\t" \
  "ds_read_b128 v[148:151], v242 offset:33792\n\t" \
  "v_accvgpr_read_b32 v78, a94\n\t" \
  "v_accvgpr_read_b32 v79, a95\n\t" \
  "v_mfma_f32_16x16x32_bf16 v[8:11], a[208:211], v[168:171], v[8:11]\n\t" \
  "buffer_store_dwordx4 v[116:119], v242, s[16:19], s98 offen offset:1024 sc1\n\t" \
  "ds_read_b32 v209, v213 offset:0\n\t" \
  "ds_read_b64_tr_b16 a[152:153], v236 offset:8192\n\t" \
  "ds_read_b64_tr_b16 a[154:155], v236 offset:12288\n\t" \
  "v_mfma_f32_16x16x32_bf16 v[8:11], a[216:219], v[172:175], v[8:11]\n\t" \
  "ds_read_b128 v[152:155], v242 offset:34816\n\t" \
  "v_mul_f32 v72, v72, v220\n\t" \
  "v_mfma_f32_16x16x32_bf16 v[24:27], a[220:223], v[172:175], 0\n\t" \
  "ds_read_b64_tr_b16 a[156:157], v237 offset:8192\n\t" \
  "v_mul_f32 v73, v73, v220\n\t" \
  "v_mfma_f32_16x16x32_bf16 v[24:27], a[212:215], v[168:171], v[24:27]\n\t" \
  "v_mul_f32 v74, v74, v220\n\t" \
  "v_mul_f32 v75, v75, v220\n\t" \
  "v_mfma_f32_16x16x32_bf16 v[24:27], a[204:207], v[164:167], v[24:27]\n\t" \
  "ds_read_b128 v[156:159], v242 offset:35840\n\t" \
  "v_mul_f32 v76, v76, v220\n\t" \
  "v_mfma_f32_16x16x32_bf16 v[24:27], a[196:199], v[160:163], v[24:27]\n\t" \
  "s_waitcnt lgkmcnt(9)\n\t" \
  "s_waitcnt lgkmcnt(5)\n\t" \
  "v_cmp_eq_u32 vcc, s36, v209\n\t" \
  "v_mfma_f32_16x16x32_bf16 v[12:15], a[192:195], v[128:131], 0\n\t" \
  "s_and_b32 vcc_lo, vcc_lo, s37\n\t" \
  "s_cbranch_scc1 .Lat_rd\n\t" \
  "ds_read_b128 v[160:163], v242 offset:36864\n\t" \
  "v_mul_f32 v77, v77, v220\n\t" \
  "v_mfma_f32_16x16x32_bf16 v[12:15], a[200:203], v[132:135], v[12:15]\n\t" \
  "s_waitcnt vmcnt(7)\n\t" \
  "buffer_store_dword v243, v254, s[16:19], s49 offen offset:32 sc1\n\t" \
  "ds_read_b64_tr_b16 a[158:159], v237 offset:12288\n\t" \
  "ds_write_b128 v213, v[112:115] offset:0\n\t" \
  "v_mfma_f32_16x16x32_bf16 v[12:15], a[208:211], v[136:139], v[12:15]\n\t" \
  "ds_write_b128 v213, v[116:119] offset:1024\n\t" \
  "v_mfma_f32_16x16x32_bf16 v[12:15], a[216:219], v[140:143], v[12:15]\n\t" \
  "ds_read_b64_tr_b16 a[160:161], v238 offset:0\n\t" \
  "ds_read_b128 v[164:167], v242 offset:37888\n\t" \
  "ds_read_b128 v[168:171], v242 offset:38912\n\t" \
  "v_mul_f32 v78, v78, v220\n\t" \
  "v_mfma_f32_16x16x32_bf16 v[28:31], a[220:223], v[140:143], 0\n\t" \
  "ds_write_b128 v213, v[120:123] offset:2048\n\t" \
  "v_mfma_f32_16x16x32_bf16 v[28:31], a[212:215], v[136:139], v[28:31]\n\t" \
  "s_add_u32 s97, s64, 131072\n\t" \
  "ds_read_b64_tr_b16 a[162:163], v238 offset:4096\n\t" \
  "v_mul_f32 v79, v79, v220\n\t" \
  "v_mfma_f32_16x16x32_bf16 v[28:31], a[204:207], v[132:135], v[28:31]\n\t" \
  "s_and_b32 s97, s97, 0xe0000\n\t" \
  "ds_read_b128 v[172:175], v242 offset:39936\n\t" \
  "v_cvt_pk_bf16_f32 v124, v72, v73\n\t" \
  "v_mfma_f32_16x16x32_bf16 v[28:31], a[196:199], v[128:131], v[28:31]\n\t" \
  "s_waitcnt lgkmcnt(10)\n\t" \
  "s_waitcnt vmcnt(3)\n\t" \
  "v_cmp_ne_u32 vcc, s36, v246\n\t" \
  STP("s_memrealtime s[94:95]\n\t") \
  STP("s_memtime s[92:93]\n\t") \
  "v_mfma_f32_16x16x32_bf16 v[32:35], a[192:195], v[144:147], 0\n\t" \
  "s_add_u32 s97, s97, s60\n\t" \
  "ds_read_b128 v[128:131], v242 offset:40960\n\t" \
  "s_add_u32 m0, s38, 49152\n\t" \
  "v_mfma_f32_16x16x32_bf16 v[32:35], a[200:203], v[148:151], v[32:35]\n\t" \
  "s_and_b32 vcc_lo, vcc_lo, 0xe\n\t" \
  "s_cbranch_scc1 .Lat_s0\n\t" \
  ".Lat_k0:\n\t" \
  "v_mfma_f32_16x16x32_bf16 v[32:35], a[208:211], v[152:155], v[32:35]\n\t" \
  "buffer_load_dwordx4 v242, s[16:19], s97 offen offset:0 sc1 lds\n\t" \
  "v_mfma_f32_16x16x32_bf16 v[32:35], a[216:219], v[156:159], v[32:35]\n\t" \
  "ds_read_b64_tr_b16 a[164:165], v239 offset:0\n\t" \
  "ds_read_b128 v[132:135], v242 offset:41984\n\t" \
  "v_cvt_pk_bf16_f32 v125, v74, v75\n\t" \
  "v_mfma_f32_16x16x32_bf16 v[48:51], a[220:223], v[156:159], 0\n\t" \
  "ds_read_b128 v[136:139], v242 offset:43008\n\t" \
  "v_cvt_pk_bf16_f32 v126, v76, v77\n\t" \
  "v_mfma_f32_16x16x32_bf16 v[48:51], a[212:215], v[152:155], v[48:51]\n\t" \
  "buffer_load_dwordx4 v242, s[16:19], s97 offen offset:1024 sc1 lds\n\t" \
  "v_mfma_f32_16x16x32_bf16 v[48:51], a[204:207], v[148:151], v[48:51]\n\t" \
  "ds_read_b64_tr_b16 a[166:167], v239 offset:4096\n\t" \
  "v_cvt_pk_bf16_f32 v127, v78, v79\n\t" \
  "v_mfma_f32_16x16x32_bf16 v[48:51], a[196:199], v[144:147], v[48:51]\n\t" \
  "s_waitcnt lgkmcnt(5)\n\t" \
  "ds_read_b64_tr_b16 a[168:169], v238 offset:8192\n\t" \
  "ds_read_b128 v[140:143], v242 offset:44032\n\t" \
  "ds_write_b128 v213, v[124:127] offset:3072\n\t" \
  "v_mfma_f32_16x16x32_bf16 v[36:39], a[192:195], v[160:163], 0\n\t" \
  "buffer_load_dwordx4 v242, s[16:19], s97 offen offset:2048 sc1 lds\n\t" \
  "v_mfma_f32_16x16x32_bf16 v[36:39], a[200:203], v[164:167], v[36:39]\n\t" \
  "ds_read_b64_tr_b16 a[170:171], v238 offset:12288\n\t" \
  "ds_read_b128 v[144:147], v242 offset:45056\n\t" \
  "v_mfma_f32_16x16x32_bf16 v[36:39], a[208:211], v[168:171], v[36:39]\n\t" \
  "ds_read_b64_tr_b16 a[172:173], v239 offset:8192\n\t" \
  "ds_read_b128 v[148:151], v242 offset:46080\n\t" \
  "v_mfma_f32_16x16x32_bf16 v[36:39], a[216:219], v[172:175], v[36:39]\n\t" \
  "buffer_load_dwordx4 v242, s[16:19], s97 offen offset:3072 sc1 lds\n\t" \
  "v_mfma_f32_16x16x32_bf16 v[52:55], a[220:223], v[172:175], 0\n\t" \
  STP("v_writelane_b32 v255, s94, 24\n\t") \
  STP("v_writelane_b32 v255, s92, 25\n\t") \
  "ds_read_b128 v[152:155], v242 offset:47104\n\t" \
  "v_mfma_f32_16x16x32_bf16 v[52:55], a[212:215], v[168:171], v[52:55]\n\t" \
  "ds_read_b64_tr_b16 a[174:175], v239 offset:12288\n\t" \
  "v_mfma_f32_16x16x32_bf16 v[52:55], a[204:207], v[164:167], v[52:55]\n\t" \
  "s_waitcnt lgkmcnt(6)\n\t" \
  "buffer_store_dwordx4 v[120:123], v242, s[16:19], s98 offen offset:2048 sc1\n\t" \
  "ds_read_b64_tr_b16 a[176:177], v236 offset:16384\n\t" \
  "ds_read_b64_tr_b16 a[178:179], v236 offset:20480\n\t" \
  "ds_read_b128 v[156:159], v242 offset:48128\n\t" \
  "v_mfma_f32_16x16x32_bf16 v[52:55], a[196:199], v[160:163], v[52:55]\n\t" \
  "s_barrier\n\t" \
  "v_mfma_f32_16x16x32_bf16 v[40:43], a[192:195], v[128:131], 0\n\t" \
  "ds_read_b128 v[160:163], v242 offset:0\n\t" \
  "v_mfma_f32_16x16x32_bf16 v[40:43], a[200:203], v[132:135], v[40:43]\n\t" \
  "ds_read_b64_tr_b16 a[180:181], v237 offset:16384\n\t" \
  "v_mfma_f32_16x16x32_bf16 v[40:43], a[208:211], v[136:139], v[40:43]\n\t" \
  "ds_read_b128 v[164:167], v242 offset:1024\n\t" \
  "v_mfma_f32_16x16x32_bf16 v[40:43], a[216:219], v[140:143], v[40:43]\n\t" \
  "ds_read_b64_tr_b16 a[182:183], v237 offset:20480\n\t" \
  "v_mfma_f32_16x16x32_bf16 v[56:59], a[220:223], v[140:143], 0\n\t" \
  "ds_read_b128 v[168:171], v242 offset:2048\n\t" \
  "v_mfma_f32_16x16x32_bf16 v[56:59], a[212:215], v[136:139], v[56:59]\n\t" \
  "ds_read_b128 v[172:175], v242 offset:3072\n\t" \
  "v_mfma_f32_16x16x32_bf16 v[56:59], a[204:207], v[132:135], v[56:59]\n\t" \
  "ds_read_b64_tr_b16 a[184:185], v236 offset:24576\n\t" \
  "v_mfma_f32_16x16x32_bf16 v[56:59], a[196:199], v[128:131], v[56:59]\n\t" \
  "s_waitcnt lgkmcnt(7)\n\t" \
  "ds_read_b64_tr_b16 a[186:187], v236 offset:28672\n\t" \
  "v_mfma_f32_16x16x32_bf16 v[44:47], a[192:195], v[144:147], 0\n\t" \
  "ds_read_b128 v[128:131], v242 offset:4096\n\t" \
  "v_mfma_f32_16x16x32_bf16 v[44:47], a[200:203], v[148:151], v[44:47]\n\t" \
  "ds_read_b128 v[132:135], v242 offset:5120\n\t" \
  "v_mfma_f32_16x16x32_bf16 v[44:47], a[208:211], v[152:155], v[44:47]\n\t" \
  "ds_read_b128 v[136:139], v242 offset:6144\n\t" \
  "v_mfma_f32_16x16x32_bf16 v[44:47], a[216:219], v[156:159], v[44:47]\n\t" \
  "ds_read_b64_tr_b16 a[188:189], v237 offset:24576\n\t" \
  "v_mfma_f32_16x16x32_bf16 v[60:63], a[220:223], v[156:159], 0\n\t" \
  "ds_read_b128 v[140:143], v242 offset:7168\n\t" \
  "v_mfma_f32_16x16x32_bf16 v[60:63], a[212:215], v[152:155], v[60:63]\n\t" \
  "ds_read_b64_tr_b16 a[190:191], v237 offset:28672\n\t" \
  "v_mfma_f32_16x16x32_bf16 v[60:63], a[204:207], v[148:151], v[60:63]\n\t" \
  "ds_read_b64_tr_b16 a[0:1], v236 offset:49152\n\t" \
  "v_mfma_f32_16x16x32_bf16 v[60:63], a[196:199], v[144:147], v[60:63]\n\t" \
  "s_waitcnt lgkmcnt(9)\n\t" \
  "ds_read_b64_tr_b16 a[2:3], v236 offset:53248\n\t" \
  STP("s_memrealtime s[94:95]\n\t") \
  STP("s_memtime s[92:93]\n\t") \
  "v_mfma_f32_16x16x32_bf16 v[64:67], a[192:195], v[160:163], 0\n\t" \
  "ds_read_b128 v[144:147], v242 offset:8192\n\t" \
  "v_mfma_f32_16x16x32_bf16 v[64:67], a[200:203], v[164:167], v[64:67]\n\t" \
  "s_add_u32 s97, s64, 262144\n\t" \
  "ds_read_b128 v[148:151], v242 offset:9216\n\t" \
  "s_and_b32 s97, s97, 0xe0000\n\t" \
  "v_mfma_f32_16x16x32_bf16 v[64:67], a[208:211], v[168:171], v[64:67]\n\t" \
  "s_add_u32 s97, s97, s60\n\t" \
  "ds_read_b128 v[152:155], v242 offset:10240\n\t" \
  "s_add_u32 m0, s38, 65536\n\t" \
  "v_mfma_f32_16x16x32_bf16 v[64:67], a[216:219], v[172:175], v[64:67]\n\t" \
  "ds_read_b128 v[156:159], v242 offset:11264\n\t" \
  "v_mfma_f32_16x16x32_bf16 v[80:83], a[220:223], v[172:175], 0\n\t" \
  "ds_read_b64_tr_b16 a[4:5], v237 offset:49152\n\t" \
  "v_mfma_f32_16x16x32_bf16 v[80:83], a[212:215], v[168:171], v[80:83]\n\t" \
  "buffer_load_dword v246, v253, s[16:19], s65 offen sc1\n\t" \
  "v_mfma_f32_16x16x32_bf16 v[80:83], a[204:207], v[164:167], v[80:83]\n\t" \
  "ds_read_b64_tr_b16 a[6:7], v237 offset:53248\n\t" \
  "v_mfma_f32_16x16x32_bf16 v[80:83], a[196:199], v[160:163], v[80:83]\n\t" \
  "s_waitcnt lgkmcnt(9)\n\t" \
  "buffer_load_dwordx4 v242, s[16:19], s97 offen offset:0 sc1 lds\n\t" \
  "v_mfma_f32_16x16x32_bf16 v[68:71], a[192:195], v[128:131], 0\n\t" \
  "s_mov_b64 exec, 0\n\t" \
  "s_cmp_eq_u32 s3, 0\n\t" \
  "s_cbranch_scc1 .Lhc10\n\t" \
  "s_cmp_eq_u32 s3, 1\n\t" \
  "s_cbranch_scc1 .Lhc11\n\t" \
  "s_cmp_eq_u32 s3, 2\n\t" \
  "s_cbranch_scc1 .Lhc12\n\t" \
  "s_branch .Lhc13\n\t" \
  ".Lhr1:\n\t" \
  "s_mov_b64 exec, -1\n\t" \
  "v_mfma_f32_16x16x32_bf16 v[68:71], a[200:203], v[132:135], v[68:71]\n\t" \
  "ds_read_b128 v[160:163], v242 offset:12288\n\t" \
  "v_mfma_f32_16x16x32_bf16 v[68:71], a[208:211], v[136:139], v[68:71]\n\t" \
  "buffer_load_dwordx4 v242, s[16:19], s97 offen offset:1024 sc1 lds\n\t" \
  "v_mfma_f32_16x16x32_bf16 v[68:71], a[216:219], v[140:143], v[68:71]\n\t" \
  "ds_read_b64_tr_b16 a[8:9], v236 offset:57344\n\t" \
  "ds_read_b128 v[164:167], v242 offset:13312\n\t" \
  "v_mfma_f32_16x16x32_bf16 v[84:87], a[220:223], v[140:143], 0\n\t" \
  STP("v_writelane_b32 v255, s94, 26\n\t") \
  STP("v_writelane_b32 v255, s92, 27\n\t") \
  "ds_read_b128 v[168:171], v242 offset:14336\n\t" \
  "v_mfma_f32_16x16x32_bf16 v[84:87], a[212:215], v[136:139], v[84:87]\n\t" \
  "buffer_load_dwordx4 v242, s[16:19], s97 offen offset:2048 sc1 lds\n\t" \
  "v_mfma_f32_16x16x32_bf16 v[84:87], a[204:207], v[132:135], v[84:87]\n\t" \
  "s_waitcnt vmcnt(5) lgkmcnt(6)\n\t" \
  "ds_read_b128 v[172:175], v242 offset:15360\n\t" \
  "v_mfma_f32_16x16x32_bf16 v[84:87], a[196:199], v[128:131], v[84:87]\n\t" \
  "s_barrier\n\t" \
  "v_mfma_f32_16x16x32_bf16 v[72:75], a[192:195], v[144:147], 0\n\t" \
  "buffer_load_dwordx4 v242, s[16:19], s97 offen offset:3072 sc1 lds\n\t" \
  "v_mfma_f32_16x16x32_bf16 v[72:75], a[200:203], v[148:151], v[72:75]\n\t" \
  "s_add_u32 s97, s64, 393216\n\t" \
  "s_and_b32 s97, s97, 0xe0000\n\t" \
  "s_add_u32 s97, s97, s60\n\t" \
  "v_mfma_f32_16x16x32_bf16 v[72:75], a[208:211], v[152:155], v[72:75]\n\t" \
  "s_add_u32 m0, s38, 81920\n\t" \
  "ds_read_b64_tr_b16 a[10:11], v236 offset:61440\n\t" \
  "ds_read_b128 v[128:131], v242 offset:49152\n\t" \
  "v_mfma_f32_16x16x32_bf16 v[72:75], a[216:219], v[156:159], v[72:75]\n\t" \
  "buffer_load_dwordx4 v242, s[16:19], s97 offen offset:0 sc1 lds\n\t" \
  "v_mfma_f32_16x16x32_bf16 v[88:91], a[220:223], v[156:159], 0\n\t" \
  "ds_read_b64_tr_b16 a[12:13], v237 offset:57344\n\t" \
  "ds_read_b128 v[132:135], v242 offset:50176\n\t" \
  "v_mfma_f32_16x16x32_bf16 v[88:91], a[212:215], v[152:155], v[88:91]\n\t" \
  "ds_read_b64_tr_b16 a[14:15], v237 offset:61440\n\t" \
  "ds_read_b128 v[136:139], v242 offset:51200\n\t" \
  "v_mfma_f32_16x16x32_bf16 v[88:91], a[204:207], v[148:151], v[88:91]\n\t" \
  "buffer_load_dwordx4 v242, s[16:19], s97 offen offset:1024 sc1 lds\n\t" \
  "v_mfma_f32_16x16x32_bf16 v[88:91], a[196:199], v[144:147], v[88:91]\n\t" \
  "s_waitcnt lgkmcnt(6)\n\t" \
  "ds_read_b64_tr_b16 a[16:17], v238 offset:16384\n\t" \
  "ds_read_b128 v[140:143], v242 offset:52224\n\t" \
  "v_mfma_f32_16x16x32_bf16 v[76:79], a[192:195], v[160:163], 0\n\t" \
  "ds_read_b128 v[144:147], v242 offset:53248\n\t" \
  "v_mfma_f32_16x16x32_bf16 v[76:79], a[200:203], v[164:167], v[76:79]\n\t" \
  "buffer_load_dwordx4 v242, s[16:19], s97 offen offset:2048 sc1 lds\n\t" \
  "v_mfma_f32_16x16x32_bf16 v[76:79], a[208:211], v[168:171], v[76:79]\n\t" \
  "ds_read_b128 v[148:151], v242 offset:54272\n\t" \
  "v_mfma_f32_16x16x32_bf16 v[76:79], a[216:219], v[172:175], v[76:79]\n\t" \
  "ds_read_b128 v[152:155], v242 offset:55296\n\t" \
  "v_mfma_f32_16x16x32_bf16 v[92:95], a[220:223], v[172:175], 0\n\t" \
  "buffer_load_dwordx4 v242, s[16:19], s97 offen offset:3072 sc1 lds\n\t" \
  "v_mfma_f32_16x16x32_bf16 v[92:95], a[212:215], v[168:171], v[92:95]\n\t" \
  "s_add_u32 s96, s40, 1048576\n\t" \
  "s_and_b32 s96, s96, 0x1c0000\n\t" \
  "s_add_u32 s96, s96, s77\n\t" \
  "v_mfma_f32_16x16x32_bf16 v[92:95], a[204:207], v[164:167], v[92:95]\n\t" \
  "s_add_u32 m0, s38, 131072\n\t" \
  "ds_read_b64_tr_b16 a[18:19], v238 offset:20480\n\t" \
  "ds_read_b128 v[156:159], v242 offset:56320\n\t" \
  "v_mfma_f32_16x16x32_bf16 v[92:95], a[196:199], v[160:163], v[92:95]\n\t" \
  "s_waitcnt lgkmcnt(5)\n\t" \
  "buffer_load_dwordx4 v240, s[24:27], s96 offen offset:0 lds\n\t" \
  STP("s_memrealtime s[94:95]\n\t") \
  STP("s_memtime s[92:93]\n\t") \
  "v_mfma_f32_16x16x32_bf16 v[0:3], a[128:131], v[128:131], v[0:3]\n\t" \
  "ds_read_b64_tr_b16 a[20:21], v239 offset:16384\n\t" \
  "ds_read_b128 v[160:163], v242 offset:57344\n\t" \
  "v_accvgpr_read_b32 v96, a96\n\t" \
  "v_mfma_f32_16x16x32_bf16 v[0:3], a[136:139], v[132:135], v[0:3]\n\t" \
  "ds_read_b64_tr_b16 a[22:23], v239 offset:20480\n\t" \
  "ds_read_b128 v[164:167], v242 offset:58368\n\t" \
  "v_accvgpr_read_b32 v97, a97\n\t" \
  "v_mfma_f32_16x16x32_bf16 v[0:3], a[144:147], v[136:139], v[0:3]\n\t" \
  "buffer_load_dwordx4 v241, s[24:27], s96 offen offset:1024 lds\n\t" \
  "v_mfma_f32_16x16x32_bf16 v[0:3], a[152:155], v[140:143], v[0:3]\n\t" \
  "ds_read_b64_tr_b16 a[24:25], v238 offset:24576\n\t" \
  "ds_read_b128 v[168:171], v242 offset:59392\n\t" \
  "v_accvgpr_read_b32 v98, a98\n\t" \
  "v_mfma_f32_16x16x32_bf16 v[16:19], a[156:159], v[140:143], v[16:19]\n\t" \
  "s_waitcnt vmcnt(10)\n\t" \
  "v_cmp_ne_u32 vcc, s36, v246\n\t" \
  "v_accvgpr_read_b32 v99, a99\n\t" \
  "v_mfma_f32_16x16x32_bf16 v[16:19], a[148:151], v[136:139], v[16:19]\n\t" \
  "buffer_load_dwordx4 v248, s[24:27], s96 offen offset:2048 lds\n\t" \
  "v_mfma_f32_16x16x32_bf16 v[16:19], a[140:143], v[132:135], v[16:19]\n\t" \
  "s_and_b32 vcc_lo, vcc_lo, 0xfefe\n\t" \
  "s_cbranch_scc1 .Lat_s1\n\t" \
  ".Lat_k1:\n\t" \
  "v_mfma_f32_16x16x32_bf16 v[16:19], a[132:135], v[128:131], v[16:19]\n\t" \
  "s_waitcnt lgkmcnt(6)\n\t" \
  "ds_read_b64_tr_b16 a[26:27], v238 offset:28672\n\t" \
  "ds_read_b128 v[172:175], v242 offset:60416\n\t" \
  "v_accvgpr_read_b32 v100, a100\n\t" \
  "v_mfma_f32_16x16x32_bf16 v[4:7], a[128:131], v[144:147], v[4:7]\n\t" \
  "buffer_load_dwordx4 v249, s[24:27], s96 offen offset:3072 lds\n\t" \
  "v_mfma_f32_16x16x32_bf16 v[4:7], a[136:139], v[148:151], v[4:7]\n\t" \
  "s_add_u32 s97, s64, 131072\n\t" \
  "s_and_b32 s97, s97, 0xe0000\n\t" \
  "ds_read_b128 v[128:131], v242 offset:61440\n\t" \
  "s_add_u32 s97, s97, s61\n\t" \
  "v_mfma_f32_16x16x32_bf16 v[4:7], a[144:147], v[152:155], v[4:7]\n\t" \
  "s_add_u32 m0, s38, 32768\n\t" \
  "ds_read_b64_tr_b16 a[28:29], v239 offset:24576\n\t" \
  "ds_read_b128 v[132:135], v242 offset:62464\n\t" \
  "v_accvgpr_read_b32 v101, a101\n\t" \
  "v_mfma_f32_16x16x32_bf16 v[4:7], a[152:155], v[156:159], v[4:7]\n\t" \
  "buffer_load_dwordx4 v242, s[16:19], s97 offen offset:0 sc1 lds\n\t" \
  "v_mfma_f32_16x16x32_bf16 v[20:23], a[156:159], v[156:159], v[20:23]\n\t" \
  STP("v_writelane_b32 v255, s94, 28\n\t") \
  STP("v_writelane_b32 v255, s92, 29\n\t") \
  ".Lhs11:\n\ts_cbranch_execz .Lhs11n\n\t" \
  "ds_read_b128 v[136:139], v242 offset:63488\n\t" \
  "v_accvgpr_read_b32 v102, a102\n\t" \
  "v_mfma_f32_16x16x32_bf16 v[20:23], a[148:151], v[152:155], v[20:23]\n\t" \
  "ds_read_b64_tr_b16 a[30:31], v239 offset:28672\n\t" \
  "v_accvgpr_read_b32 v103, a103\n\t" \
  "v_mfma_f32_16x16x32_bf16 v[20:23], a[140:143], v[148:151], v[20:23]\n\t" \
  "s_waitcnt vmcnt(9) lgkmcnt(5)\n\t" \
  "buffer_load_dwordx4 v242, s[16:19], s97 offen offset:1024 sc1 lds\n\t" \
  "v_mfma_f32_16x16x32_bf16 v[20:23], a[132:135], v[144:147], v[20:23]\n\t" \
  "s_barrier\n\t" \
  "v_mfma_f32_16x16x32_bf16 v[8:11], a[128:131], v[160:163], v[8:11]\n\t" \
  "ds_read_b128 v[140:143], v242 offset:64512\n\t" \
  "v_mul_f32 v96, v96, v222\n\t" \
  "v_mfma_f32_16x16x32_bf16 v[8:11], a[136:139], v[164:167], v[8:11]\n\t" \
  "buffer_load_dwordx4 v242, s[16:19], s97 offen offset:2048 sc1 lds\n\t" \
  "v_mfma_f32_16x16x32_bf16 v[8:11], a[144:147], v[168:171], v[8:11]\n\t" \
  "ds_read_b128 v[144:147], v230 offset:0\n\t" \
  "v_mul_f32 v97, v97, v222\n\t" \
  "v_mfma_f32_16x16x32_bf16 v[8:11], a[152:155], v[172:175], v[8:11]\n\t" \
  "ds_read_b128 v[148:151], v230 offset:1024\n\t" \
  "v_mul_f32 v98, v98, v222\n\t" \
  "v_mfma_f32_16x16x32_bf16 v[24:27], a[156:159], v[172:175], v[24:27]\n\t" \
  "buffer_load_dwordx4 v242, s[16:19], s97 offen offset:3072 sc1 lds\n\t" \
  "v_mfma_f32_16x16x32_bf16 v[24:27], a[148:151], v[168:171], v[24:27]\n\t" \
  "ds_read_b128 v[152:155], v230 offset:2048\n\t" \
  "v_mul_f32 v99, v99, v222\n\t" \
  "v_mfma_f32_16x16x32_bf16 v[24:27], a[140:143], v[164:167], v[24:27]\n\t" \
  "ds_read_b64_tr_b16 a[32:33], v236 offset:32768\n\t" \
  "v_mul_f32 v100, v100, v222\n\t" \
  "v_mfma_f32_16x16x32_bf16 v[24:27], a[132:135], v[160:163], v[24:27]\n\t" \
  "s_waitcnt lgkmcnt(4)\n\t" \
  "buffer_store_dwordx4 v[124:127], v242, s[16:19], s98 offen offset:3072 sc1\n\t" \
  "ds_read_b64_tr_b16 a[34:35], v236 offset:36864\n\t" \
  "ds_read_b64_tr_b16 a[36:37], v237 offset:32768\n\t" \
  "ds_read_b128 v[156:159], v230 offset:3072\n\t" \
  "v_mfma_f32_16x16x32_bf16 v[12:15], a[128:131], v[128:131], v[12:15]\n\t" \
  "s_xor_b32 s98, s45, 98304\n\t" \
  "ds_read_b128 v[160:163], v230 offset:4096\n\t" \
  "v_mul_f32 v101, v101, v222\n\t" \
  "v_mfma_f32_16x16x32_bf16 v[12:15], a[136:139], v[132:135], v[12:15]\n\t" \
  "ds_read_b64_tr_b16 a[38:39], v237 offset:36864\n\t" \
  "v_mul_f32 v102, v102, v222\n\t" \
  "v_mfma_f32_16x16x32_bf16 v[12:15], a[144:147], v[136:139], v[12:15]\n\t" \
  "s_add_u32 s97, s64, 524288\n\t" \
  "ds_read_b128 v[164:167], v230 offset:5120\n\t" \
  "v_mul_f32 v103, v103, v222\n\t" \
  "v_mfma_f32_16x16x32_bf16 v[12:15], a[152:155], v[140:143], v[12:15]\n\t" \
  "s_and_b32 s97, s97, 0xe0000\n\t" \
  "ds_read_b64_tr_b16 a[40:41], v236 offset:40960\n\t" \
  "v_cvt_pk_bf16_f32 v96, v96, v97\n\t" \
  "v_mfma_f32_16x16x32_bf16 v[28:31], a[156:159], v[140:143], v[28:31]\n\t" \
  "s_add_u32 s97, s97, s60\n\t" \
  "ds_read_b128 v[168:171], v230 offset:6144\n\t" \
  "v_cvt_pk_bf16_f32 v97, v98, v99\n\t" \
  "v_mfma_f32_16x16x32_bf16 v[28:31], a[148:151], v[136:139], v[28:31]\n\t" \
  "s_add_u32 m0, s38, 0\n\t" \
  "ds_read_b64_tr_b16 a[42:43], v236 offset:45056\n\t" \
  "v_cvt_pk_bf16_f32 v98, v100, v101\n\t" \
  "v_mfma_f32_16x16x32_bf16 v[28:31], a[140:143], v[132:135], v[28:31]\n\t" \
  "ds_read_b128 v[172:175], v230 offset:7168\n\t" \
  "v_cvt_pk_bf16_f32 v99, v102, v103\n\t" \
  "v_mfma_f32_16x16x32_bf16 v[28:31], a[132:135], v[128:131], v[28:31]\n\t" \
  "s_waitcnt lgkmcnt(7)\n\t" \
  "ds_write_b128 v213, v[96:99] offset:16384\n\t" \
  STP("s_memrealtime s[94:95]\n\t") \
  STP("s_memtime s[92:93]\n\t") \
  "v_mfma_f32_16x16x32_bf16 v[0:3], a[160:163], v[144:147], v[0:3]\n\t" \
  "buffer_load_dwordx4 v242, s[16:19], s97 offen offset:0 sc1 lds\n\t" \
  "v_mfma_f32_16x16x32_bf16 v[0:3], a[168:171], v[148:151], v[0:3]\n\t" \
  "ds_read_b128 v[128:131], v230 offset:8192\n\t" \
  "v_accvgpr_read_b32 v104, a104\n\t" \
  "v_mfma_f32_16x16x32_bf16 v[0:3], a[176:179], v[152:155], v[0:3]\n\t" \
  "ds_read_b64_tr_b16 a[44:45], v237 offset:40960\n\t" \
  "v_accvgpr_read_b32 v105, a105\n\t" \
  "v_mfma_f32_16x16x32_bf16 v[0:3], a[184:187], v[156:159], v[0:3]\n\t" \
  "buffer_load_dwordx4 v242, s[16:19], s97 offen offset:1024 sc1 lds\n\t" \
  "v_mfma_f32_16x16x32_bf16 v[16:19], a[188:191], v[156:159], v[16:19]\n\t" \
  "ds_read_b128 v[132:135], v230 offset:9216\n\t" \
  "v_accvgpr_read_b32 v106, a106\n\t" \
  "v_mfma_f32_16x16x32_bf16 v[16:19], a[180:183], v[152:155], v[16:19]\n\t" \
  "ds_read_b128 v[136:139], v230 offset:10240\n\t" \
  "v_accvgpr_read_b32 v107, a107\n\t" \
  "v_mfma_f32_16x16x32_bf16 v[16:19], a[172:175], v[148:151], v[16:19]\n\t" \
  "ds_read_b64_tr_b16 a[46:47], v237 offset:45056\n\t" \
  "v_accvgpr_read_b32 v108, a108\n\t" \
  "v_mfma_f32_16x16x32_bf16 v[16:19], a[164:167], v[144:147], v[16:19]\n\t" \
  "s_waitcnt lgkmcnt(6)\n\t" \
  "ds_read_b128 v[140:143], v230 offset:11264\n\t" \
  "v_accvgpr_read_b32 v109, a109\n\t" \
  "v_mfma_f32_16x16x32_bf16 v[4:7], a[160:163], v[160:163], v[4:7]\n\t" \
  "ds_read_b128 v[144:147], v230 offset:12288\n\t" \
  "v_accvgpr_read_b32 v110, a110\n\t" \
  "v_mfma_f32_16x16x32_bf16 v[4:7], a[168:171], v[164:167], v[4:7]\n\t" \
  "v_accvgpr_read_b32 v111, a111\n\t" \
  "v_mul_f32 v104, v104, v222\n\t" \
  "v_mfma_f32_16x16x32_bf16 v[4:7], a[176:179], v[168:171], v[4:7]\n\t" \
  "ds_read_b128 v[148:151], v230 offset:13312\n\t" \
  "v_mul_f32 v105, v105, v222\n\t" \
  "v_mfma_f32_16x16x32_bf16 v[4:7], a[184:187], v[172:175], v[4:7]\n\t" \
  "buffer_load_dwordx4 v242, s[16:19], s97 offen offset:2048 sc1 lds\n\t" \
  "v_mfma_f32_16x16x32_bf16 v[20:23], a[188:191], v[172:175], v[20:23]\n\t" \
  STP("v_writelane_b32 v255, s94, 30\n\t") \
  STP("v_writelane_b32 v255, s92, 31\n\t") \
  "ds_read_b128 v[152:155], v230 offset:14336\n\t" \
  "v_mul_f32 v106, v106, v222\n\t" \
  "v_mfma_f32_16x16x32_bf16 v[20:23], a[180:183], v[168:171], v[20:23]\n\t" \
  "v_mul_f32 v107, v107, v222\n\t" \
  "v_mul_f32 v108, v108, v222\n\t" \
  "v_mfma_f32_16x16x32_bf16 v[20:23], a[172:175], v[164:167], v[20:23]\n\t" \
  "s_waitcnt vmcnt(12) lgkmcnt(3)\n\t" \
  "buffer_load_dwordx4 v242, s[16:19], s97 offen offset:3072 sc1 lds\n\t" \
  "v_mfma_f32_16x16x32_bf16 v[20:23], a[164:167], v[160:163], v[20:23]\n\t" \
  "s_barrier\n\t" \
  "v_mfma_f32_16x16x32_bf16 v[8:11], a[160:163], v[128:131], v[8:11]\n\t" \
  "ds_read_b128 v[156:159], v230 offset:15360\n\t" \
  "v_mul_f32 v109, v109, v222\n\t" \
  "v_mfma_f32_16x16x32_bf16 v[8:11], a[168:171], v[132:135], v[8:11]\n\t" \
  "buffer_store_dwordx4 v[96:99], v242, s[16:19], s98 offen offset:0 sc1\n\t" \
  "v_mul_f32 v110, v110, v222\n\t" \
  "ds_read_b128 v[160:163], v230 offset:16384\n\t" \
  "v_mul_f32 v111, v111, v222\n\t" \
  "v_mfma_f32_16x16x32_bf16 v[8:11], a[176:179], v[136:139], v[8:11]\n\t" \
  "s_add_u32 s96, s40, 1310720\n\t" \
  "ds_read_b128 v[164:167], v230 offset:17408\n\t" \
  "v_cvt_pk_bf16_f32 v104, v104, v105\n\t" \
  "v_mfma_f32_16x16x32_bf16 v[8:11], a[184:187], v[140:143], v[8:11]\n\t" \
  "s_and_b32 s96, s96, 0x1c0000\n\t" \
  "v_cvt_pk_bf16_f32 v105, v106, v107\n\t" \
  "v_cvt_pk_bf16_f32 v106, v108, v109\n\t" \
  "v_mfma_f32_16x16x32_bf16 v[24:27], a[188:191], v[140:143], v[24:27]\n\t" \
  "s_add_u32 s96, s96, s51\n\t" \
  "ds_read_b128 v[168:171], v230 offset:18432\n\t" \
  "v_cvt_pk_bf16_f32 v107, v110, v111\n\t" \
  "v_mfma_f32_16x16x32_bf16 v[24:27], a[180:183], v[136:139], v[24:27]\n\t" \
  "ds_write_b128 v213, v[104:107] offset:17408\n\t" \
  "v_mfma_f32_16x16x32_bf16 v[24:27], a[172:175], v[132:135], v[24:27]\n\t" \
  "s_add_u32 m0, s38, 147456\n\t" \
  "ds_read_b128 v[172:175], v230 offset:19456\n\t" \
  "v_accvgpr_read_b32 v96, a112\n\t" \
  "v_mfma_f32_16x16x32_bf16 v[24:27], a[164:167], v[128:131], v[24:27]\n\t" \
  "s_waitcnt lgkmcnt(5)\n\t" \
  "v_accvgpr_read_b32 v97, a113\n\t" \
  "v_accvgpr_read_b32 v98, a114\n\t" \
  "v_mfma_f32_16x16x32_bf16 v[12:15], a[160:163], v[144:147], v[12:15]\n\t" \
  "buffer_load_dwordx4 v240, s[24:27], s96 offen offset:0 lds\n\t" \
  "v_mfma_f32_16x16x32_bf16 v[12:15], a[168:171], v[148:151], v[12:15]\n\t" \
  "ds_read_b128 v[128:131], v230 offset:20480\n\t" \
  "v_accvgpr_read_b32 v99, a115\n\t" \
  "v_mfma_f32_16x16x32_bf16 v[12:15], a[176:179], v[152:155], v[12:15]\n\t" \
  "ds_read_b128 v[132:135], v230 offset:21504\n\t" \
  "v_accvgpr_read_b32 v100, a116\n\t" \
  "v_mfma_f32_16x16x32_bf16 v[12:15], a[184:187], v[156:159], v[12:15]\n\t" \
  "v_accvgpr_read_b32 v101, a117\n\t" \
  "v_accvgpr_read_b32 v102, a118\n\t" \
  "v_mfma_f32_16x16x32_bf16 v[28:31], a[188:191], v[156:159], v[28:31]\n\t" \
  "buffer_load_dwordx4 v241, s[24:27], s96 offen offset:1024 lds\n\t" \
  "v_mfma_f32_16x16x32_bf16 v[28:31], a[180:183], v[152:155], v[28:31]\n\t" \
  "ds_read_b128 v[136:139], v230 offset:22528\n\t" \
  "v_accvgpr_read_b32 v103, a119\n\t" \
  "v_mfma_f32_16x16x32_bf16 v[28:31], a[172:175], v[148:151], v[28:31]\n\t" \
  "ds_read_b128 v[140:143], v230 offset:23552\n\t" \
  "v_mul_f32 v96, v96, v222\n\t" \
  "v_mfma_f32_16x16x32_bf16 v[28:31], a[164:167], v[144:147], v[28:31]\n\t" \
  "s_waitcnt lgkmcnt(4)\n\t" \
  "buffer_load_dwordx4 v248, s[24:27], s96 offen offset:2048 lds\n\t" \
  "v_mfma_f32_16x16x32_bf16 v[0:3], a[0:3], v[160:163], v[0:3]\n\t" \
  "ds_read_b128 v[144:147], v230 offset:24576\n\t" \
  "v_mul_f32 v97, v97, v222\n\t" \
  "v_mfma_f32_16x16x32_bf16 v[0:3], a[8:11], v[164:167], v[0:3]\n\t" \
  "v_mul_f32 v98, v98, v222\n\t" \
  "v_mul_f32 v99, v99, v222\n\t" \
  "v_mfma_f32_16x16x32_bf16 v[0:3], a[16:19], v[168:171], v[0:3]\n\t" \
  "buffer_load_dwordx4 v249, s[24:27], s96 offen offset:3072 lds\n\t" \
  "v_mfma_f32_16x16x32_bf16 v[0:3], a[24:27], v[172:175], v[0:3]\n\t" \
  "s_add_u32 s97, s64, 262144\n\t" \
  "s_and_b32 s97, s97, 0xe0000\n\t" \
  "s_add_u32 s97, s97, s61\n\t" \
  "v_mfma_f32_16x16x32_bf16 v[16:19], a[28:31], v[172:175], v[16:19]\n\t" \
  "s_add_u32 m0, s38, 49152\n\t" \
  "ds_read_b128 v[148:151], v230 offset:25600\n\t" \
  "v_mul_f32 v100, v100, v222\n\t" \
  "v_mfma_f32_16x16x32_bf16 v[16:19], a[20:23], v[168:171], v[16:19]\n\t" \
  "buffer_load_dwordx4 v242, s[16:19], s97 offen offset:0 sc1 lds\n\t" \
  "v_mfma_f32_16x16x32_bf16 v[16:19], a[12:15], v[164:167], v[16:19]\n\t" \
  "ds_read_b128 v[152:155], v230 offset:26624\n\t" \
  "v_mul_f32 v101, v101, v222\n\t" \
  "v_mfma_f32_16x16x32_bf16 v[16:19], a[4:7], v[160:163], v[16:19]\n\t" \
  "s_waitcnt lgkmcnt(3)\n\t" \
  "ds_read_b128 v[156:159], v230 offset:27648\n\t" \
  "v_mul_f32 v102, v102, v222\n\t" \
  "v_mfma_f32_16x16x32_bf16 v[4:7], a[0:3], v[128:131], v[4:7]\n\t" \
  "ds_read_b128 v[160:163], v230 offset:28672\n\t" \
  "v_mul_f32 v103, v103, v222\n\t" \
  "v_mfma_f32_16x16x32_bf16 v[4:7], a[8:11], v[132:135], v[4:7]\n\t" \
  "v_cvt_pk_bf16_f32 v96, v96, v97\n\t" \
  "v_cvt_pk_bf16_f32 v97, v98, v99\n\t" \
  "v_mfma_f32_16x16x32_bf16 v[4:7], a[16:19], v[136:139], v[4:7]\n\t" \
  "ds_read_b128 v[164:167], v230 offset:29696\n\t" \
  "v_cvt_pk_bf16_f32 v98, v100, v101\n\t" \
  "v_mfma_f32_16x16x32_bf16 v[4:7], a[24:27], v[140:143], v[4:7]\n\t" \
  "buffer_load_dwordx4 v242, s[16:19], s97 offen offset:1024 sc1 lds\n\t" \
  "v_mfma_f32_16x16x32_bf16 v[20:23], a[28:31], v[140:143], v[20:23]\n\t" \
  "ds_read_b128 v[168:171], v230 offset:30720\n\t" \
  "v_cvt_pk_bf16_f32 v99, v102, v103\n\t" \
  "v_mfma_f32_16x16x32_bf16 v[20:23], a[20:23], v[136:139], v[20:23]\n\t" \
  "ds_write_b128 v213, v[96:99] offset:18432\n\t" \
  "v_mfma_f32_16x16x32_bf16 v[20:23], a[12:15], v[132:135], v[20:23]\n\t" \
  "s_waitcnt vmcnt(12) lgkmcnt(4)\n\t" \
  "buffer_load_dwordx4 v242, s[16:19], s97 offen offset:2048 sc1 lds\n\t" \
  "v_mfma_f32_16x16x32_bf16 v[20:23], a[4:7], v[128:131], v[20:23]\n\t" \
  "s_barrier\n\t" \
  "v_mfma_f32_16x16x32_bf16 v[8:11], a[0:3], v[144:147], v[8:11]\n\t" \
  "ds_read_b128 v[172:175], v230 offset:31744\n\t" \
  "v_mfma_f32_16x16x32_bf16 v[8:11], a[8:11], v[148:151], v[8:11]\n\t" \
  "buffer_load_dwordx4 v242, s[16:19], s97 offen offset:3072 sc1 lds\n\t" \
  "v_mfma_f32_16x16x32_bf16 v[8:11], a[16:19], v[152:155], v[8:11]\n\t" \
  "ds_read_b128 v[128:131], v242 offset:32768\n\t" \
  "v_mfma_f32_16x16x32_bf16 v[8:11], a[24:27], v[156:159], v[8:11]\n\t" \
  "ds_read_b128 v[132:135], v242 offset:33792\n\t" \
  "v_mfma_f32_16x16x32_bf16 v[24:27], a[28:31], v[156:159], v[24:27]\n\t" \
  "buffer_store_dwordx4 v[104:107], v242, s[16:19], s98 offen offset:1024 sc1\n\t" \
  "ds_read_b64_tr_b16 a[48:49], v238 offset:0\n\t" \
  "ds_read_b64_tr_b16 a[50:51], v238 offset:4096\n\t" \
  "ds_read_b128 v[136:139], v242 offset:34816\n\t" \
  "v_mfma_f32_16x16x32_bf16 v[24:27], a[20:23], v[152:155], v[24:27]\n\t" \
  "v_accvgpr_read_b32 v104, a120\n\t" \
  "v_accvgpr_read_b32 v105, a121\n\t" \
  "v_mfma_f32_16x16x32_bf16 v[24:27], a[12:15], v[148:151], v[24:27]\n\t" \
  "ds_read_b128 v[140:143], v242 offset:35840\n\t" \
  "v_accvgpr_read_b32 v106, a122\n\t" \
  "v_mfma_f32_16x16x32_bf16 v[24:27], a[4:7], v[144:147], v[24:27]\n\t" \
  "s_waitcnt lgkmcnt(6)\n\t" \
  "ds_read_b64_tr_b16 a[52:53], v239 offset:0\n\t" \
  "v_accvgpr_read_b32 v107, a123\n\t" \
  "v_mfma_f32_16x16x32_bf16 v[12:15], a[0:3], v[160:163], v[12:15]\n\t" \
  "s_add_u32 s96, s40, 1310720\n\t" \
  "ds_read_b128 v[144:147], v242 offset:36864\n\t" \
  "v_accvgpr_read_b32 v108, a124\n\t" \
  "v_mfma_f32_16x16x32_bf16 v[12:15], a[8:11], v[164:167], v[12:15]\n\t" \
  "s_and_b32 s96, s96, 0x1c0000\n\t" \
  "ds_read_b64_tr_b16 a[54:55], v239 offset:4096\n\t" \
  "v_accvgpr_read_b32 v109, a125\n\t" \
  "v_mfma_f32_16x16x32_bf16 v[12:15], a[16:19], v[168:171], v[12:15]\n\t" \
  "s_add_u32 s96, s96, s77\n\t" \
  "ds_read_b128 v[148:151], v242 offset:37888\n\t" \
  "v_accvgpr_read_b32 v110, a126\n\t" \
  "v_mfma_f32_16x16x32_bf16 v[12:15], a[24:27], v[172:175], v[12:15]\n\t" \
  "s_add_u32 m0, s38, 65536\n\t" \
  "ds_read_b64_tr_b16 a[56:57], v238 offset:8192\n\t" \
  "v_accvgpr_read_b32 v111, a127\n\t" \
  "v_mfma_f32_16x16x32_bf16 v[28:31], a[28:31], v[172:175], v[28:31]\n\t" \
  "ds_read_b128 v[152:155], v242 offset:38912\n\t" \
  "v_mul_f32 v104, v104, v222\n\t" \
  "v_mfma_f32_16x16x32_bf16 v[28:31], a[20:23], v[168:171], v[28:31]\n\t" \
  "buffer_load_dwordx4 v240, s[24:27], s96 offen offset:0 lds\n\t" \
  "v_mfma_f32_16x16x32_bf16 v[28:31], a[12:15], v[164:167], v[28:31]\n\t" \
  "ds_read_b128 v[156:159], v242 offset:39936\n\t" \
  "v_mul_f32 v105, v105, v222\n\t" \
  "v_mfma_f32_16x16x32_bf16 v[28:31], a[4:7], v[160:163], v[28:31]\n\t" \
  "s_waitcnt lgkmcnt(7)\n\t" \
  "ds_read_b64_tr_b16 a[58:59], v238 offset:12288\n\t" \
  "v_mul_f32 v106, v106, v222\n\t" \
  "v_mfma_f32_16x16x32_bf16 v[32:35], a[128:131], v[128:131], v[32:35]\n\t" \
  "ds_read_b128 v[160:163], v242 offset:40960\n\t" \
  "v_mul_f32 v107, v107, v222\n\t" \
  "v_mfma_f32_16x16x32_bf16 v[32:35], a[136:139], v[132:135], v[32:35]\n\t" \
  "ds_read_b64_tr_b16 a[60:61], v239 offset:8192\n\t" \
  "v_mul_f32 v108, v108, v222\n\t" \
  "v_mfma_f32_16x16x32_bf16 v[32:35], a[144:147], v[136:139], v[32:35]\n\t" \
  "ds_read_b128 v[164:167], v242 offset:41984\n\t" \
  "v_mul_f32 v109, v109, v222\n\t" \
  "v_mfma_f32_16x16x32_bf16 v[32:35], a[152:155], v[140:143], v[32:35]\n\t" \
  "buffer_load_dwordx4 v241, s[24:27], s96 offen offset:1024 lds\n\t" \
  "v_mfma_f32_16x16x32_bf16 v[48:51], a[156:159], v[140:143], v[48:51]\n\t" \
  "ds_read_b128 v[168:171], v242 offset:43008\n\t" \
  "v_mul_f32 v110, v110, v222\n\t" \
  "v_mfma_f32_16x16x32_bf16 v[48:51], a[148:151], v[136:139], v[48:51]\n\t" \
  "ds_read_b64_tr_b16 a[62:63], v239 offset:12288\n\t" \
  "v_mul_f32 v111, v111, v222\n\t" \
  "v_mfma_f32_16x16x32_bf16 v[48:51], a[140:143], v[132:135], v[48:51]\n\t" \
  "ds_read_b128 v[172:175], v242 offset:44032\n\t" \
  "v_cvt_pk_bf16_f32 v104, v104, v105\n\t" \
  "v_mfma_f32_16x16x32_bf16 v[48:51], a[132:135], v[128:131], v[48:51]\n\t" \
  "s_waitcnt lgkmcnt(7)\n\t" \
  "v_cvt_pk_bf16_f32 v105, v106, v107\n\t" \
  "v_cvt_pk_bf16_f32 v106, v108, v109\n\t" \
  "v_mfma_f32_16x16x32_bf16 v[36:39], a[128:131], v[144:147], v[36:39]\n\t" \
  "ds_read_b128 v[128:131], v242 offset:45056\n\t" \
  "v_cvt_pk_bf16_f32 v107, v110, v111\n\t" \
  "v_mfma_f32_16x16x32_bf16 v[36:39], a[136:139], v[148:151], v[36:39]\n\t" \
  "ds_write_b128 v213, v[104:107] offset:19456\n\t" \
  "v_mfma_f32_16x16x32_bf16 v[36:39], a[144:147], v[152:155], v[36:39]\n\t" \
  "ds_read_b128 v[132:135], v242 offset:46080\n\t" \
  "v_mfma_f32_16x16x32_bf16 v[36:39], a[152:155], v[156:159], v[36:39]\n\t" \
  "ds_read_b128 v[136:139], v242 offset:47104\n\t" \
  "v_mfma_f32_16x16x32_bf16 v[52:55], a[156:159], v[156:159], v[52:55]\n\t" \
  "ds_read_b128 v[140:143], v242 offset:48128\n\t" \
  "v_mfma_f32_16x16x32_bf16 v[52:55], a[148:151], v[152:155], v[52:55]\n\t" \
  "v_mfma_f32_16x16x32_bf16 v[52:55], a[140:143], v[148:151], v[52:55]\n\t" \
  "s_waitcnt vmcnt(12) lgkmcnt(5)\n\t" \
  "buffer_load_dwordx4 v248, s[24:27], s96 offen offset:2048 lds\n\t" \
  "v_mfma_f32_16x16x32_bf16 v[52:55], a[132:135], v[144:147], v[52:55]\n\t" \
  "s_barrier\n\t" \
  "v_mfma_f32_16x16x32_bf16 v[40:43], a[128:131], v[160:163], v[40:43]\n\t" \
  "ds_read_b128 v[144:147], v242 offset:0\n\t" \
  "v_mfma_f32_16x16x32_bf16 v[40:43], a[136:139], v[164:167], v[40:43]\n\t" \
  "buffer_load_dwordx4 v249, s[24:27], s96 offen offset:3072 lds\n\t" \
  "v_mfma_f32_16x16x32_bf16 v[40:43], a[144:147], v[168:171], v[40:43]\n\t" \
  "s_add_u32 s96, s40, 1572864\n\t" \
  "ds_read_b128 v[148:151], v242 offset:1024\n\t" \
  "s_and_b32 s96, s96, 0x1c0000\n\t" \
  "v_mfma_f32_16x16x32_bf16 v[40:43], a[152:155], v[172:175], v[40:43]\n\t" \
  "s_add_u32 s96, s96, s51\n\t" \
  "ds_read_b128 v[152:155], v242 offset:2048\n\t" \
  "s_add_u32 m0, s38, 81920\n\t" \
  "v_mfma_f32_16x16x32_bf16 v[56:59], a[156:159], v[172:175], v[56:59]\n\t" \
  "buffer_store_dwordx4 v[96:99], v242, s[16:19], s98 offen offset:2048 sc1\n\t" \
  "ds_read_b128 v[156:159], v242 offset:3072\n\t" \
  "v_mfma_f32_16x16x32_bf16 v[56:59], a[148:151], v[168:171], v[56:59]\n\t" \
  "v_mfma_f32_16x16x32_bf16 v[56:59], a[140:143], v[164:167], v[56:59]\n\t" \
  "v_mfma_f32_16x16x32_bf16 v[56:59], a[132:135], v[160:163], v[56:59]\n\t" \
  "s_waitcnt lgkmcnt(4)\n\t" \
  "buffer_load_dwordx4 v240, s[24:27], s96 offen offset:0 lds\n\t" \
  "v_mfma_f32_16x16x32_bf16 v[44:47], a[128:131], v[128:131], v[44:47]\n\t" \
  "ds_read_b128 v[160:163], v242 offset:4096\n\t" \
  "v_mfma_f32_16x16x32_bf16 v[44:47], a[136:139], v[132:135], v[44:47]\n\t" \
  "ds_read_b128 v[164:167], v242 offset:5120\n\t" \
  "v_mfma_f32_16x16x32_bf16 v[44:47], a[144:147], v[136:139], v[44:47]\n\t" \
  "buffer_load_dwordx4 v241, s[24:27], s96 offen offset:1024 lds\n\t" \
  "v_mfma_f32_16x16x32_bf16 v[44:47], a[152:155], v[140:143], v[44:47]\n\t" \
  "ds_read_b128 v[168:171], v242 offset:6144\n\t" \
  "v_mfma_f32_16x16x32_bf16 v[60:63], a[156:159], v[140:143], v[60:63]\n\t" \
  "ds_read_b128 v[172:175], v242 offset:7168\n\t" \
  "v_mfma_f32_16x16x32_bf16 v[60:63], a[148:151], v[136:139], v[60:63]\n\t" \
  "buffer_load_dwordx4 v248, s[24:27], s96 offen offset:2048 lds\n\t" \
  "v_mfma_f32_16x16x32_bf16 v[60:63], a[140:143], v[132:135], v[60:63]\n\t" \
  "v_mfma_f32_16x16x32_bf16 v[60:63], a[132:135], v[128:131], v[60:63]\n\t" \
  "s_waitcnt lgkmcnt(4)\n\t" \
  "v_mfma_f32_16x16x32_bf16 v[0:3], a[32:35], v[144:147], v[0:3]\n\t" \
  "buffer_load_dwordx4 v249, s[24:27], s96 offen offset:3072 lds\n\t" \
  "v_mfma_f32_16x16x32_bf16 v[0:3], a[40:43], v[148:151], v[0:3]\n\t" \
  "ds_read_b128 v[128:131], v242 offset:8192\n\t" \
  "v_mfma_f32_16x16x32_bf16 v[0:3], a[48:51], v[152:155], v[0:3]\n\t" \
  "ds_read_b128 v[132:135], v242 offset:9216\n\t" \
  "v_mfma_f32_16x16x32_bf16 v[0:3], a[56:59], v[156:159], v[0:3]\n\t" \
  "buffer_store_dwordx4 v[104:107], v242, s[16:19], s98 offen offset:3072 sc1\n\t" \
  "ds_read_b128 v[136:139], v242 offset:10240\n\t" \
  "ds_read_b128 v[140:143], v242 offset:11264\n\t" \
  "v_mfma_f32_16x16x32_bf16 v[16:19], a[60:63], v[156:159], v[16:19]\n\t" \
  "v_mfma_f32_16x16x32_bf16 v[16:19], a[52:55], v[152:155], v[16:19]\n\t" \
  "v_mfma_f32_16x16x32_bf16 v[16:19], a[44:47], v[148:151], v[16:19]\n\t" \
  "v_mfma_f32_16x16x32_bf16 v[16:19], a[36:39], v[144:147], v[16:19]\n\t" \
  "s_waitcnt lgkmcnt(4)\n\t" \
  "v_mfma_f32_16x16x32_bf16 v[4:7], a[32:35], v[160:163], v[4:7]\n\t" \
  "ds_read_b128 v[144:147], v242 offset:12288\n\t" \
  "v_mfma_f32_16x16x32_bf16 v[4:7], a[40:43], v[164:167], v[4:7]\n\t" \
  "ds_read_b128 v[148:151], v242 offset:13312\n\t" \
  "v_mfma_f32_16x16x32_bf16 v[4:7], a[48:51], v[168:171], v[4:7]\n\t" \
  "s_add_u32 s97, s64, 655360\n\t" \
  "ds_read_b128 v[152:155], v242 offset:14336\n\t" \
  "s_and_b32 s97, s97, 0xe0000\n\t" \
  "v_mfma_f32_16x16x32_bf16 v[4:7], a[56:59], v[172:175], v[4:7]\n\t" \
  "s_add_u32 s97, s97, s60\n\t" \
  "ds_read_b128 v[156:159], v242 offset:15360\n\t" \
  "s_add_u32 m0, s38, 131072\n\t" \
  "v_mfma_f32_16x16x32_bf16 v[20:23], a[60:63], v[172:175], v[20:23]\n\t" \
  "v_mfma_f32_16x16x32_bf16 v[20:23], a[52:55], v[168:171], v[20:23]\n\t" \
  "v_mfma_f32_16x16x32_bf16 v[20:23], a[44:47], v[164:167], v[20:23]\n\t" \
  "s_waitcnt vmcnt(11) lgkmcnt(4)\n\t" \
  "v_mfma_f32_16x16x32_bf16 v[20:23], a[36:39], v[160:163], v[20:23]\n\t" \
  "s_barrier\n\t" \
  "v_mfma_f32_16x16x32_bf16 v[8:11], a[32:35], v[128:131], v[8:11]\n\t" \
  "buffer_load_dwordx4 v242, s[16:19], s97 offen offset:0 sc1 lds\n\t" \
  "v_mfma_f32_16x16x32_bf16 v[8:11], a[40:43], v[132:135], v[8:11]\n\t" \
  "ds_read_b128 v[160:163], v242 offset:49152\n\t" \
  "v_mfma_f32_16x16x32_bf16 v[8:11], a[48:51], v[136:139], v[8:11]\n\t" \
  "ds_read_b128 v[164:167], v242 offset:50176\n\t" \
  "v_mfma_f32_16x16x32_bf16 v[8:11], a[56:59], v[140:143], v[8:11]\n\t" \
  "buffer_load_dwordx4 v242, s[16:19], s97 offen offset:1024 sc1 lds\n\t" \
  "v_mfma_f32_16x16x32_bf16 v[24:27], a[60:63], v[140:143], v[24:27]\n\t" \
  "ds_read_b128 v[168:171], v242 offset:51200\n\t" \
  "v_mfma_f32_16x16x32_bf16 v[24:27], a[52:55], v[136:139], v[24:27]\n\t" \
  "ds_read_b64_tr_b16 a[64:65], v238 offset:16384\n\t" \
  "v_mfma_f32_16x16x32_bf16 v[24:27], a[44:47], v[132:135], v[24:27]\n\t" \
  "buffer_load_dwordx4 v242, s[16:19], s97 offen offset:2048 sc1 lds\n\t" \
  "v_mfma_f32_16x16x32_bf16 v[24:27], a[36:39], v[128:131], v[24:27]\n\t" \
  "s_waitcnt lgkmcnt(4)\n\t" \
  "ds_read_b64_tr_b16 a[66:67], v238 offset:20480\n\t" \
  "ds_read_b128 v[172:175], v242 offset:52224\n\t" \
  "v_mfma_f32_16x16x32_bf16 v[12:15], a[32:35], v[144:147], v[12:15]\n\t" \
  "ds_read_b128 v[128:131], v242 offset:53248\n\t" \
  "v_mfma_f32_16x16x32_bf16 v[12:15], a[40:43], v[148:151], v[12:15]\n\t" \
  "ds_read_b64_tr_b16 a[68:69], v239 offset:16384\n\t" \
  "v_mfma_f32_16x16x32_bf16 v[12:15], a[48:51], v[152:155], v[12:15]\n\t" \
  "buffer_load_dwordx4 v242, s[16:19], s97 offen offset:3072 sc1 lds\n\t" \
  "v_mfma_f32_16x16x32_bf16 v[12:15], a[56:59], v[156:159], v[12:15]\n\t" \
  "s_add_u32 s96, s40, 1572864\n\t" \
  "s_and_b32 s96, s96, 0x1c0000\n\t" \
  "s_add_u32 s96, s96, s77\n\t" \
  "v_mfma_f32_16x16x32_bf16 v[28:31], a[60:63], v[156:159], v[28:31]\n\t" \
  "s_add_u32 m0, s38, 32768\n\t" \
  "ds_read_b128 v[132:135], v242 offset:54272\n\t" \
  "v_mfma_f32_16x16x32_bf16 v[28:31], a[52:55], v[152:155], v[28:31]\n\t" \
  "buffer_load_dwordx4 v240, s[24:27], s96 offen offset:0 lds\n\t" \
  "v_mfma_f32_16x16x32_bf16 v[28:31], a[44:47], v[148:151], v[28:31]\n\t" \
  "ds_read_b128 v[136:139], v242 offset:55296\n\t" \
  "v_mfma_f32_16x16x32_bf16 v[28:31], a[36:39], v[144:147], v[28:31]\n\t" \
  "s_waitcnt lgkmcnt(4)\n\t" \
  "ds_read_b128 v[140:143], v242 offset:56320\n\t" \
  "v_mfma_f32_16x16x32_bf16 v[32:35], a[160:163], v[160:163], v[32:35]\n\t" \
  "ds_read_b64_tr_b16 a[70:71], v239 offset:20480\n\t" \
  "v_mfma_f32_16x16x32_bf16 v[32:35], a[168:171], v[164:167], v[32:35]\n\t" \
  "buffer_load_dwordx4 v241, s[24:27], s96 offen offset:1024 lds\n\t" \
  "v_mfma_f32_16x16x32_bf16 v[32:35], a[176:179], v[168:171], v[32:35]\n\t" \
  "ds_read_b128 v[144:147], v242 offset:57344\n\t" \
  "v_mfma_f32_16x16x32_bf16 v[32:35], a[184:187], v[172:175], v[32:35]\n\t" \
  "ds_read_b64_tr_b16 a[72:73], v238 offset:24576\n\t" \
  "v_mfma_f32_16x16x32_bf16 v[48:51], a[188:191], v[172:175], v[48:51]\n\t" \
  "ds_read_b128 v[148:151], v242 offset:58368\n\t" \
  "v_mfma_f32_16x16x32_bf16 v[48:51], a[180:183], v[168:171], v[48:51]\n\t" \
  "ds_read_b128 v[152:155], v242 offset:59392\n\t" \
  "v_mfma_f32_16x16x32_bf16 v[48:51], a[172:175], v[164:167], v[48:51]\n\t" \
  "ds_read_b128 v[156:159], v242 offset:60416\n\t" \
  "ds_read_b64_tr_b16 a[74:75], v238 offset:28672\n\t" \
  "v_mfma_f32_16x16x32_bf16 v[48:51], a[164:167], v[160:163], v[48:51]\n\t" \
  "s_waitcnt lgkmcnt(7)\n\t" \
  "ds_read_b64_tr_b16 a[76:77], v239 offset:24576\n\t" \
  "v_mfma_f32_16x16x32_bf16 v[36:39], a[160:163], v[128:131], v[36:39]\n\t" \
  "ds_read_b64_tr_b16 a[78:79], v239 offset:28672\n\t" \
  "v_mfma_f32_16x16x32_bf16 v[36:39], a[168:171], v[132:135], v[36:39]\n\t" \
  "ds_read_b128 v[160:163], v242 offset:61440\n\t" \
  "v_mfma_f32_16x16x32_bf16 v[36:39], a[176:179], v[136:139], v[36:39]\n\t" \
  "ds_read_b128 v[164:167], v242 offset:62464\n\t" \
  "v_mfma_f32_16x16x32_bf16 v[36:39], a[184:187], v[140:143], v[36:39]\n\t" \
  "ds_read_b128 v[168:171], v242 offset:63488\n\t" \
  "v_mfma_f32_16x16x32_bf16 v[52:55], a[188:191], v[140:143], v[52:55]\n\t" \
  "ds_read_b128 v[172:175], v242 offset:64512\n\t" \
  "v_mfma_f32_16x16x32_bf16 v[52:55], a[180:183], v[136:139], v[52:55]\n\t" \
  "buffer_load_dwordx4 v248, s[24:27], s96 offen offset:2048 lds\n\t" \
  "v_mfma_f32_16x16x32_bf16 v[52:55], a[172:175], v[132:135], v[52:55]\n\t" \
  "s_waitcnt vmcnt(13) lgkmcnt(4)\n\t" \
  "v_mfma_f32_16x16x32_bf16 v[52:55], a[164:167], v[128:131], v[52:55]\n\t" \
  "s_barrier\n\t" \
  "v_mfma_f32_16x16x32_bf16 v[40:43], a[160:163], v[144:147], v[40:43]\n\t" \
  "buffer_load_dwordx4 v249, s[24:27], s96 offen offset:3072 lds\n\t" \
  "v_mfma_f32_16x16x32_bf16 v[40:43], a[168:171], v[148:151], v[40:43]\n\t" \
  "s_add_u32 s97, s64, 393216\n\t" \
  "ds_read_b128 v[128:131], v242 offset:16384\n\t" \
  "s_and_b32 s97, s97, 0xe0000\n\t" \
  "v_mfma_f32_16x16x32_bf16 v[40:43], a[176:179], v[152:155], v[40:43]\n\t" \
  "s_add_u32 s97, s97, s61\n\t" \
  "ds_read_b128 v[132:135], v242 offset:17408\n\t" \
  "s_add_u32 m0, s38, 0\n\t" \
  "v_mfma_f32_16x16x32_bf16 v[40:43], a[184:187], v[156:159], v[40:43]\n\t" \
  "ds_read_b64_tr_b16 a[80:81], v236 offset:0\n\t" \
  "v_mfma_f32_16x16x32_bf16 v[56:59], a[188:191], v[156:159], v[56:59]\n\t" \
  "buffer_load_dwordx4 v242, s[16:19], s97 offen offset:0 sc1 lds\n\t" \
  "v_mfma_f32_16x16x32_bf16 v[56:59], a[180:183], v[152:155], v[56:59]\n\t" \
  "ds_read_b64_tr_b16 a[82:83], v236 offset:4096\n\t" \
  "ds_read_b128 v[136:139], v242 offset:18432\n\t" \
  "v_mfma_f32_16x16x32_bf16 v[56:59], a[172:175], v[148:151], v[56:59]\n\t" \
  "ds_read_b64_tr_b16 a[84:85], v237 offset:0\n\t" \
  "ds_read_b128 v[140:143], v242 offset:19456\n\t" \
  "v_mfma_f32_16x16x32_bf16 v[56:59], a[164:167], v[144:147], v[56:59]\n\t" \
  "s_waitcnt lgkmcnt(7)\n\t" \
  "buffer_load_dwordx4 v242, s[16:19], s97 offen offset:1024 sc1 lds\n\t" \
  "v_mfma_f32_16x16x32_bf16 v[44:47], a[160:163], v[160:163], v[44:47]\n\t" \
  "ds_read_b128 v[144:147], v242 offset:20480\n\t" \
  "v_mfma_f32_16x16x32_bf16 v[44:47], a[168:171], v[164:167], v[44:47]\n\t" \
  "ds_read_b128 v[148:151], v242 offset:21504\n\t" \
  "v_mfma_f32_16x16x32_bf16 v[44:47], a[176:179], v[168:171], v[44:47]\n\t" \
  "ds_read_b64_tr_b16 a[86:87], v237 offset:4096\n\t" \
  "v_mfma_f32_16x16x32_bf16 v[44:47], a[184:187], v[172:175], v[44:47]\n\t" \
  "ds_read_b128 v[152:155], v242 offset:22528\n\t" \
  "v_mfma_f32_16x16x32_bf16 v[60:63], a[188:191], v[172:175], v[60:63]\n\t" \
  "buffer_load_dwordx4 v242, s[16:19], s97 offen offset:2048 sc1 lds\n\t" \
  "v_mfma_f32_16x16x32_bf16 v[60:63], a[180:183], v[168:171], v[60:63]\n\t" \
  "ds_read_b64_tr_b16 a[88:89], v236 offset:8192\n\t" \
  "v_mfma_f32_16x16x32_bf16 v[60:63], a[172:175], v[164:167], v[60:63]\n\t" \
  "ds_read_b128 v[156:159], v242 offset:23552\n\t" \
  "v_mfma_f32_16x16x32_bf16 v[60:63], a[164:167], v[160:163], v[60:63]\n\t" \
  "s_waitcnt lgkmcnt(6)\n\t" \
  "buffer_load_dwordx4 v242, s[16:19], s97 offen offset:3072 sc1 lds\n\t" \
  "v_mfma_f32_16x16x32_bf16 v[96:99], a[192:195], v[128:131], 0\n\t" \
  "s_add_u32 s96, s40, 1835008\n\t" \
  "ds_read_b128 v[160:163], v242 offset:24576\n\t" \
  "s_and_b32 s96, s96, 0x1c0000\n\t" \
  "v_mfma_f32_16x16x32_bf16 v[96:99], a[200:203], v[132:135], v[96:99]\n\t" \
  "s_add_u32 s96, s96, s51\n\t" \
  "ds_read_b64_tr_b16 a[90:91], v236 offset:12288\n\t" \
  "s_add_u32 m0, s38, 147456\n\t" \
  "v_mfma_f32_16x16x32_bf16 v[96:99], a[208:211], v[136:139], v[96:99]\n\t" \
  "s_waitcnt vmcnt(12)\n\t" \
  "buffer_store_dword v243, v254, s[16:19], s49 offen offset:64 sc1\n\t" \
  "ds_read_b128 v[164:167], v242 offset:25600\n\t" \
  "v_mfma_f32_16x16x32_bf16 v[96:99], a[216:219], v[140:143], v[96:99]\n\t" \
  "ds_read_b64_tr_b16 a[92:93], v237 offset:8192\n\t" \
  "v_mfma_f32_16x16x32_bf16 v[112:115], a[220:223], v[140:143], 0\n\t" \
  "buffer_load_dwordx4 v240, s[24:27], s96 offen offset:0 lds\n\t" \
  "v_mfma_f32_16x16x32_bf16 v[112:115], a[212:215], v[136:139], v[112:115]\n\t" \
  "ds_read_b128 v[168:171], v242 offset:26624\n\t" \
  "v_mfma_f32_16x16x32_bf16 v[112:115], a[204:207], v[132:135], v[112:115]\n\t" \
  "ds_read_b128 v[172:175], v242 offset:27648\n\t" \
  "v_mfma_f32_16x16x32_bf16 v[112:115], a[196:199], v[128:131], v[112:115]\n\t" \
  "s_waitcnt lgkmcnt(6)\n\t" \
  "buffer_load_dwordx4 v241, s[24:27], s96 offen offset:1024 lds\n\t" \
  "v_mfma_f32_16x16x32_bf16 v[100:103], a[192:195], v[144:147], 0\n\t" \
  "ds_read_b64_tr_b16 a[94:95], v237 offset:12288\n\t" \
  "v_mfma_f32_16x16x32_bf16 v[100:103], a[200:203], v[148:151], v[100:103]\n\t" \
  "ds_read_b128 v[128:131], v242 offset:28672\n\t" \
  "v_mfma_f32_16x16x32_bf16 v[100:103], a[208:211], v[152:155], v[100:103]\n\t" \
  "ds_read_b128 v[132:135], v242 offset:29696\n\t" \
  "v_mfma_f32_16x16x32_bf16 v[100:103], a[216:219], v[156:159], v[100:103]\n\t" \
  "ds_read_b128 v[136:139], v242 offset:30720\n\t" \
  "v_mfma_f32_16x16x32_bf16 v[116:119], a[220:223], v[156:159], 0\n\t" \
  "ds_read_b128 v[140:143], v242 offset:31744\n\t" \
  "v_mfma_f32_16x16x32_bf16 v[116:119], a[212:215], v[152:155], v[116:119]\n\t" \
  "v_mfma_f32_16x16x32_bf16 v[116:119], a[204:207], v[148:151], v[116:119]\n\t" \
  "s_waitcnt vmcnt(11) lgkmcnt(4)\n\t" \
  "v_mfma_f32_16x16x32_bf16 v[116:119], a[196:199], v[144:147], v[116:119]\n\t" \
  "s_barrier\n\t" \
  "v_mfma_f32_16x16x32_bf16 v[104:107], a[192:195], v[160:163], 0\n\t" \
  "ds_read_b128 v[144:147], v232 offset:0\n\t" \
  "v_mfma_f32_16x16x32_bf16 v[104:107], a[200:203], v[164:167], v[104:107]\n\t" \
  "ds_read_b64_tr_b16 a[96:97], v236 offset:16384\n\t" \
  "v_mfma_f32_16x16x32_bf16 v[104:107], a[208:211], v[168:171], v[104:107]\n\t" \
  "ds_read_b128 v[148:151], v232 offset:1024\n\t" \
  "v_mfma_f32_16x16x32_bf16 v[104:107], a[216:219], v[172:175], v[104:107]\n\t" \
  "ds_read_b128 v[152:155], v232 offset:2048\n\t" \
  "v_mfma_f32_16x16x32_bf16 v[120:123], a[220:223], v[172:175], 0\n\t" \
  "ds_read_b64_tr_b16 a[98:99], v236 offset:20480\n\t" \
  "v_mfma_f32_16x16x32_bf16 v[120:123], a[212:215], v[168:171], v[120:123]\n\t" \
  "ds_read_b128 v[156:159], v232 offset:3072\n\t" \
  "v_mfma_f32_16x16x32_bf16 v[120:123], a[204:207], v[164:167], v[120:123]\n\t" \
  "buffer_load_dwordx4 v248, s[24:27], s96 offen offset:2048 lds\n\t" \
  "v_mfma_f32_16x16x32_bf16 v[120:123], a[196:199], v[160:163], v[120:123]\n\t" \
  "s_waitcnt lgkmcnt(6)\n\t" \
  "ds_read_b64_tr_b16 a[100:101], v237 offset:16384\n\t" \
  "v_mfma_f32_16x16x32_bf16 v[108:111], a[192:195], v[128:131], 0\n\t" \
  "ds_read_b128 v[160:163], v232 offset:4096\n\t" \
  "v_mfma_f32_16x16x32_bf16 v[108:111], a[200:203], v[132:135], v[108:111]\n\t" \
  "ds_read_b64_tr_b16 a[102:103], v237 offset:20480\n\t" \
  "v_mfma_f32_16x16x32_bf16 v[108:111], a[208:211], v[136:139], v[108:111]\n\t" \
  "buffer_load_dwordx4 v249, s[24:27], s96 offen offset:3072 lds\n\t" \
  "v_mfma_f32_16x16x32_bf16 v[108:111], a[216:219], v[140:143], v[108:111]\n\t" \
  "s_add_u32 s97, s64, 786432\n\t" \
  "s_and_b32 s97, s97, 0xe0000\n\t" \
  "s_add_u32 s97, s97, s60\n\t" \
  "v_mfma_f32_16x16x32_bf16 v[124:127], a[220:223], v[140:143], 0\n\t" \
  "s_add_u32 m0, s38, 65536\n\t" \
  "ds_read_b64_tr_b16 a[104:105], v236 offset:24576\n\t" \
  "ds_read_b128 v[164:167], v232 offset:5120\n\t" \
  "v_mfma_f32_16x16x32_bf16 v[124:127], a[212:215], v[136:139], v[124:127]\n\t" \
  "buffer_load_dwordx4 v242, s[16:19], s97 offen offset:0 sc1 lds\n\t" \
  "v_mfma_f32_16x16x32_bf16 v[124:127], a[204:207], v[132:135], v[124:127]\n\t" \
  ".Lhs12:\n\ts_cbranch_execz .Lhs12n\n\t" \
  "ds_read_b64_tr_b16 a[106:107], v236 offset:28672\n\t" \
  "ds_read_b128 v[168:171], v232 offset:6144\n\t" \
  "v_mfma_f32_16x16x32_bf16 v[124:127], a[196:199], v[128:131], v[124:127]\n\t" \
  "s_waitcnt lgkmcnt(7)\n\t" \
  "ds_read_b128 v[172:175], v232 offset:7168\n\t" \
  "v_mfma_f32_16x16x32_bf16 v[0:3], a[64:67], v[144:147], v[0:3]\n\t" \
  "ds_read_b128 v[128:131], v232 offset:8192\n\t" \
  "v_lshrrev_b32 v247, 1, v242\n\t" \
  "v_mfma_f32_16x16x32_bf16 v[0:3], a[72:75], v[148:151], v[0:3]\n\t" \
  "buffer_load_dwordx4 v242, s[16:19], s97 offen offset:1024 sc1 lds\n\t" \
  "v_mfma_f32_16x16x32_bf16 v[0:3], a[80:83], v[152:155], v[0:3]\n\t" \
  "ds_read_b128 v[132:135], v232 offset:9216\n\t" \
  "v_add_u32 v247, s38, v247\n\t" \
  "v_mfma_f32_16x16x32_bf16 v[0:3], a[88:91], v[156:159], v[0:3]\n\t" \
  "ds_read_b64_tr_b16 a[108:109], v237 offset:24576\n\t" \
  "v_mfma_f32_16x16x32_bf16 v[16:19], a[92:95], v[156:159], v[16:19]\n\t" \
  "buffer_load_dwordx4 v242, s[16:19], s97 offen offset:2048 sc1 lds\n\t" \
  "v_mfma_f32_16x16x32_bf16 v[16:19], a[84:87], v[152:155], v[16:19]\n\t" \
  "ds_read_b128 v[136:139], v232 offset:10240\n\t" \
  "v_mfma_f32_16x16x32_bf16 v[16:19], a[76:79], v[148:151], v[16:19]\n\t" \
  "ds_read_b128 v[140:143], v232 offset:11264\n\t" \
  "v_mfma_f32_16x16x32_bf16 v[16:19], a[68:71], v[144:147], v[16:19]\n\t" \
  "s_waitcnt lgkmcnt(5)\n\t" \
  "buffer_load_dwordx4 v242, s[16:19], s97 offen offset:3072 sc1 lds\n\t" \
  "v_mfma_f32_16x16x32_bf16 v[4:7], a[64:67], v[160:163], v[4:7]\n\t" \
  "s_add_u32 s96, s40, 1835008\n\t" \
  "s_and_b32 s96, s96, 0x1c0000\n\t" \
  "s_add_u32 s96, s96, s77\n\t" \
  "v_mfma_f32_16x16x32_bf16 v[4:7], a[72:75], v[164:167], v[4:7]\n\t" \
  "s_add_u32 m0, s38, 49152\n\t" \
  "ds_read_b128 v[144:147], v232 offset:12288\n\t" \
  "v_mfma_f32_16x16x32_bf16 v[4:7], a[80:83], v[168:171], v[4:7]\n\t" \
  "buffer_load_dwordx4 v240, s[24:27], s96 offen offset:0 lds\n\t" \
  "v_mfma_f32_16x16x32_bf16 v[4:7], a[88:91], v[172:175], v[4:7]\n\t" \
  "ds_read_b128 v[148:151], v232 offset:13312\n\t" \
  "ds_read_b64_tr_b16 a[110:111], v237 offset:28672\n\t" \
  "v_mfma_f32_16x16x32_bf16 v[20:23], a[92:95], v[172:175], v[20:23]\n\t" \
  "ds_read_b128 v[152:155], v232 offset:14336\n\t" \
  "v_mfma_f32_16x16x32_bf16 v[20:23], a[84:87], v[168:171], v[20:23]\n\t" \
  "buffer_load_dwordx4 v241, s[24:27], s96 offen offset:1024 lds\n\t" \
  "v_mfma_f32_16x16x32_bf16 v[20:23], a[76:79], v[164:167], v[20:23]\n\t" \
  "s_waitcnt vmcnt(11) lgkmcnt(1)\n\t" \
  "ds_read_b128 v[156:159], v232 offset:15360\n\t" \
  "v_mfma_f32_16x16x32_bf16 v[20:23], a[68:71], v[160:163], v[20:23]\n\t" \
  "s_barrier\n\t" \
  "v_mfma_f32_16x16x32_bf16 v[8:11], a[64:67], v[128:131], v[8:11]\n\t" \
  "buffer_load_dwordx4 v248, s[24:27], s96 offen offset:2048 lds\n\t" \
  "v_mfma_f32_16x16x32_bf16 v[8:11], a[72:75], v[132:135], v[8:11]\n\t" \
  "ds_read_b128 v[160:163], v242 offset:0\n\t" \
  "v_mfma_f32_16x16x32_bf16 v[8:11], a[80:83], v[136:139], v[8:11]\n\t" \
  "ds_read_b128 v[164:167], v242 offset:1024\n\t" \
  "v_mfma_f32_16x16x32_bf16 v[8:11], a[88:91], v[140:143], v[8:11]\n\t" \
  "ds_read_b64_tr_b16 a[112:113], v234 offset:32768\n\t" \
  "v_mfma_f32_16x16x32_bf16 v[24:27], a[92:95], v[140:143], v[24:27]\n\t" \
  "ds_read_b128 v[168:171], v242 offset:2048\n\t" \
  "v_mfma_f32_16x16x32_bf16 v[24:27], a[84:87], v[136:139], v[24:27]\n\t" \
  "buffer_load_dwordx4 v249, s[24:27], s96 offen offset:3072 lds\n\t" \
  "v_mfma_f32_16x16x32_bf16 v[24:27], a[76:79], v[132:135], v[24:27]\n\t" \
  "s_add_u32 s97, s64, 524288\n\t" \
  "s_and_b32 s97, s97, 0xe0000\n\t" \
  "s_add_u32 s97, s97, s61\n\t" \
  "v_mfma_f32_16x16x32_bf16 v[24:27], a[68:71], v[128:131], v[24:27]\n\t" \
  "s_waitcnt lgkmcnt(4)\n\t" \
  "ds_read_b128 v[172:175], v242 offset:3072\n\t" \
  "s_add_u32 m0, s38, 81920\n\t" \
  "v_mfma_f32_16x16x32_bf16 v[12:15], a[64:67], v[144:147], v[12:15]\n\t" \
  "buffer_load_dwordx4 v242, s[16:19], s97 offen offset:0 sc1 lds\n\t" \
  "v_mfma_f32_16x16x32_bf16 v[12:15], a[72:75], v[148:151], v[12:15]\n\t" \
  "ds_read_b128 v[128:131], v242 offset:4096\n\t" \
  "v_mfma_f32_16x16x32_bf16 v[12:15], a[80:83], v[152:155], v[12:15]\n\t" \
  "ds_read_b64_tr_b16 a[114:115], v234 offset:36864\n\t" \
  "v_mfma_f32_16x16x32_bf16 v[12:15], a[88:91], v[156:159], v[12:15]\n\t" \
  "ds_read_b128 v[132:135], v242 offset:5120\n\t" \
  "v_mfma_f32_16x16x32_bf16 v[28:31], a[92:95], v[156:159], v[28:31]\n\t" \
  "ds_read_b64_tr_b16 a[116:117], v235 offset:32768\n\t" \
  "v_mfma_f32_16x16x32_bf16 v[28:31], a[84:87], v[152:155], v[28:31]\n\t" \
  "ds_read_b128 v[136:139], v242 offset:6144\n\t" \
  "v_mfma_f32_16x16x32_bf16 v[28:31], a[76:79], v[148:151], v[28:31]\n\t" \
  "ds_read_b128 v[140:143], v242 offset:7168\n\t" \
  "v_mfma_f32_16x16x32_bf16 v[28:31], a[68:71], v[144:147], v[28:31]\n\t" \
  "s_waitcnt lgkmcnt(6)\n\t" \
  "ds_read_b64_tr_b16 a[118:119], v235 offset:36864\n\t" \
  STP("s_memrealtime s[94:95]\n\t") \
  STP("s_memtime s[92:93]\n\t") \
  "v_mfma_f32_16x16x32_bf16 v[32:35], a[0:3], v[160:163], v[32:35]\n\t" \
  "ds_read_b128 v[144:147], v242 offset:8192\n\t" \
  "v_mfma_f32_16x16x32_bf16 v[32:35], a[8:11], v[164:167], v[32:35]\n\t" \
  "ds_read_b64_tr_b16 a[120:121], v234 offset:40960\n\t" \
  "v_mfma_f32_16x16x32_bf16 v[32:35], a[16:19], v[168:171], v[32:35]\n\t" \
  "ds_read_b128 v[148:151], v242 offset:9216\n\t" \
  "v_mfma_f32_16x16x32_bf16 v[32:35], a[24:27], v[172:175], v[32:35]\n\t" \
  "ds_read_b64_tr_b16 a[122:123], v234 offset:45056\n\t" \
  "v_mfma_f32_16x16x32_bf16 v[48:51], a[28:31], v[172:175], v[48:51]\n\t" \
  "ds_read_b128 v[152:155], v242 offset:10240\n\t" \
  "v_mfma_f32_16x16x32_bf16 v[48:51], a[20:23], v[168:171], v[48:51]\n\t" \
  "ds_read_b64_tr_b16 a[124:125], v235 offset:40960\n\t" \
  "v_mfma_f32_16x16x32_bf16 v[48:51], a[12:15], v[164:167], v[48:51]\n\t" \
  "buffer_load_dwordx4 v242, s[16:19], s97 offen offset:1024 sc1 lds\n\t" \
  "v_mfma_f32_16x16x32_bf16 v[48:51], a[4:7], v[160:163], v[48:51]\n\t" \
  "s_waitcnt lgkmcnt(7)\n\t" \
  "ds_read_b128 v[156:159], v242 offset:11264\n\t" \
  "v_mfma_f32_16x16x32_bf16 v[36:39], a[0:3], v[128:131], v[36:39]\n\t" \
  "ds_read_b64_tr_b16 a[126:127], v235 offset:45056\n\t" \
  "v_mfma_f32_16x16x32_bf16 v[36:39], a[8:11], v[132:135], v[36:39]\n\t" \
  "ds_read_b128 v[160:163], v242 offset:12288\n\t" \
  "v_mfma_f32_16x16x32_bf16 v[36:39], a[16:19], v[136:139], v[36:39]\n\t" \
  "buffer_load_dwordx4 v242, s[16:19], s97 offen offset:2048 sc1 lds\n\t" \
  "v_mfma_f32_16x16x32_bf16 v[36:39], a[24:27], v[140:143], v[36:39]\n\t" \
  "ds_read_b128 v[164:167], v242 offset:13312\n\t" \
  "v_mfma_f32_16x16x32_bf16 v[52:55], a[28:31], v[140:143], v[52:55]\n\t" \
  STP("v_writelane_b32 v255, s94, 32\n\t") \
  STP("v_writelane_b32 v255, s92, 33\n\t") \
  "ds_read_b128 v[168:171], v242 offset:14336\n\t" \
  "v_mfma_f32_16x16x32_bf16 v[52:55], a[20:23], v[136:139], v[52:55]\n\t" \
  "buffer_load_dwordx4 v242, s[16:19], s97 offen offset:3072 sc1 lds\n\t" \
  "v_mfma_f32_16x16x32_bf16 v[52:55], a[12:15], v[132:135], v[52:55]\n\t" \
  "s_waitcnt vmcnt(8) lgkmcnt(3)\n\t" \
  "s_add_u32 s97, s64, 917504\n\t" \
  "s_and_b32 s97, s97, 0xe0000\n\t" \
  "v_mfma_f32_16x16x32_bf16 v[52:55], a[4:7], v[128:131], v[52:55]\n\t" \
  "s_barrier\n\t" \
  "v_mfma_f32_16x16x32_bf16 v[40:43], a[0:3], v[144:147], v[40:43]\n\t" \
  "s_add_u32 s97, s97, s60\n\t" \
  "ds_read_b128 v[172:175], v242 offset:15360\n\t" \
  "s_add_u32 m0, s38, 131072\n\t" \
  "v_mfma_f32_16x16x32_bf16 v[40:43], a[8:11], v[148:151], v[40:43]\n\t" \
  "buffer_load_dwordx4 v242, s[16:19], s97 offen offset:0 sc1 lds\n\t" \
  "v_mfma_f32_16x16x32_bf16 v[40:43], a[16:19], v[152:155], v[40:43]\n\t" \
  "ds_read_b128 v[128:131], v230 offset:0\n\t" \
  "v_mfma_f32_16x16x32_bf16 v[40:43], a[24:27], v[156:159], v[40:43]\n\t" \
  "buffer_load_dwordx4 v242, s[16:19], s97 offen offset:1024 sc1 lds\n\t" \
  "v_mfma_f32_16x16x32_bf16 v[56:59], a[28:31], v[156:159], v[56:59]\n\t" \
  "ds_read_b128 v[132:135], v230 offset:1024\n\t" \
  "v_mfma_f32_16x16x32_bf16 v[56:59], a[20:23], v[152:155], v[56:59]\n\t" \
  "ds_read_b128 v[136:139], v230 offset:2048\n\t" \
  "v_mfma_f32_16x16x32_bf16 v[56:59], a[12:15], v[148:151], v[56:59]\n\t" \
  "buffer_load_dwordx4 v242, s[16:19], s97 offen offset:2048 sc1 lds\n\t" \
  "v_mfma_f32_16x16x32_bf16 v[56:59], a[4:7], v[144:147], v[56:59]\n\t" \
  "s_waitcnt lgkmcnt(3)\n\t" \
  "ds_read_b128 v[140:143], v230 offset:3072\n\t" \
  "v_mfma_f32_16x16x32_bf16 v[44:47], a[0:3], v[160:163], v[44:47]\n\t" \
  "ds_read_b128 v[144:147], v230 offset:4096\n\t" \
  "v_mfma_f32_16x16x32_bf16 v[44:47], a[8:11], v[164:167], v[44:47]\n\t" \
  "ds_read_b128 v[148:151], v230 offset:5120\n\t" \
  "v_mfma_f32_16x16x32_bf16 v[44:47], a[16:19], v[168:171], v[44:47]\n\t" \
  "ds_read_b128 v[152:155], v230 offset:6144\n\t" \
  "v_mfma_f32_16x16x32_bf16 v[44:47], a[24:27], v[172:175], v[44:47]\n\t" \
  "ds_read_b128 v[156:159], v230 offset:7168\n\t" \
  "v_mfma_f32_16x16x32_bf16 v[60:63], a[28:31], v[172:175], v[60:63]\n\t" \
  "buffer_load_dwordx4 v242, s[16:19], s97 offen offset:3072 sc1 lds\n\t" \
  "v_mfma_f32_16x16x32_bf16 v[60:63], a[20:23], v[168:171], v[60:63]\n\t" \
  "s_add_u32 s97, s64, 655360\n\t" \
  "s_and_b32 s97, s97, 0xe0000\n\t" \
  "s_add_u32 s97, s97, s61\n\t" \
  "v_mfma_f32_16x16x32_bf16 v[60:63], a[12:15], v[164:167], v[60:63]\n\t" \
  "s_add_u32 m0, s38, 16384\n\t" \
  "v_mfma_f32_16x16x32_bf16 v[60:63], a[4:7], v[160:163], v[60:63]\n\t" \
  "s_waitcnt lgkmcnt(4)\n\t" \
  "buffer_load_dword v246, v253, s[16:19], s65 offen sc1\n\t" \
  "v_mfma_f32_16x16x32_bf16 v[0:3], a[96:99], v[128:131], v[0:3]\n\t" \
  "ds_read_b128 v[160:163], v230 offset:8192\n\t" \
  "v_mfma_f32_16x16x32_bf16 v[0:3], a[104:107], v[132:135], v[0:3]\n\t" \
  "ds_read_b128 v[164:167], v230 offset:9216\n\t" \
  "v_mfma_f32_16x16x32_bf16 v[0:3], a[112:115], v[136:139], v[0:3]\n\t" \
  "ds_read_b128 v[168:171], v230 offset:10240\n\t" \
  "v_mfma_f32_16x16x32_bf16 v[0:3], a[120:123], v[140:143], v[0:3]\n\t" \
  "buffer_load_dwordx4 v242, s[16:19], s97 offen offset:0 sc1 lds\n\t" \
  "v_mfma_f32_16x16x32_bf16 v[16:19], a[124:127], v[140:143], v[16:19]\n\t" \
  "ds_read_b128 v[172:175], v230 offset:11264\n\t" \
  "v_mfma_f32_16x16x32_bf16 v[16:19], a[116:119], v[136:139], v[16:19]\n\t" \
  "v_mfma_f32_16x16x32_bf16 v[16:19], a[108:111], v[132:135], v[16:19]\n\t" \
  "v_mfma_f32_16x16x32_bf16 v[16:19], a[100:103], v[128:131], v[16:19]\n\t" \
  "s_waitcnt lgkmcnt(4)\n\t" \
  "buffer_load_dwordx4 v242, s[16:19], s97 offen offset:1024 sc1 lds\n\t" \
  "v_mfma_f32_16x16x32_bf16 v[4:7], a[96:99], v[144:147], v[4:7]\n\t" \
  "ds_read_b128 v[128:131], v230 offset:12288\n\t" \
  "v_mfma_f32_16x16x32_bf16 v[4:7], a[104:107], v[148:151], v[4:7]\n\t" \
  "ds_read_b128 v[132:135], v230 offset:13312\n\t" \
  "v_mfma_f32_16x16x32_bf16 v[4:7], a[112:115], v[152:155], v[4:7]\n\t" \
  "buffer_load_dwordx4 v242, s[16:19], s97 offen offset:2048 sc1 lds\n\t" \
  "v_mfma_f32_16x16x32_bf16 v[4:7], a[120:123], v[156:159], v[4:7]\n\t" \
  "ds_read_b128 v[136:139], v230 offset:14336\n\t" \
  "v_mfma_f32_16x16x32_bf16 v[20:23], a[124:127], v[156:159], v[20:23]\n\t" \
  "ds_read_b128 v[140:143], v230 offset:15360\n\t" \
  "v_mfma_f32_16x16x32_bf16 v[20:23], a[116:119], v[152:155], v[20:23]\n\t" \
  "buffer_load_dwordx4 v242, s[16:19], s97 offen offset:3072 sc1 lds\n\t" \
  "v_mfma_f32_16x16x32_bf16 v[20:23], a[108:111], v[148:151], v[20:23]\n\t" \
  "s_waitcnt vmcnt(9) lgkmcnt(4)\n\t" \
  "s_add_u32 s97, s64, 786432\n\t" \
  "s_and_b32 s97, s97, 0xe0000\n\t" \
  "v_mfma_f32_16x16x32_bf16 v[20:23], a[100:103], v[144:147], v[20:23]\n\t" \
  "s_barrier\n\t" \
  "v_mfma_f32_16x16x32_bf16 v[8:11], a[96:99], v[160:163], v[8:11]\n\t" \
  "s_add_u32 s97, s97, s61\n\t" \
  "ds_read_b128 v[144:147], v230 offset:16384\n\t" \
  "ds_read_b128 v[148:151], v230 offset:17408\n\t" \
  "s_add_u32 m0, s38, 0\n\t" \
  "v_mfma_f32_16x16x32_bf16 v[8:11], a[104:107], v[164:167], v[8:11]\n\t" \
  "buffer_load_dwordx4 v242, s[16:19], s97 offen offset:0 sc1 lds\n\t" \
  "ds_read_b64_tr_b16 a[192:193], v238 offset:16384\n\t" \
  "v_mfma_f32_16x16x32_bf16 v[8:11], a[112:115], v[168:171], v[8:11]\n\t" \
  "ds_read_b64_tr_b16 a[194:195], v238 offset:20480\n\t" \
  "ds_read_b128 v[152:155], v230 offset:18432\n\t" \
  "v_mfma_f32_16x16x32_bf16 v[8:11], a[120:123], v[172:175], v[8:11]\n\t" \
  "buffer_load_dwordx4 v242, s[16:19], s97 offen offset:1024 sc1 lds\n\t" \
  "v_mfma_f32_16x16x32_bf16 v[24:27], a[124:127], v[172:175], v[24:27]\n\t" \
  "ds_read_b128 v[156:159], v230 offset:19456\n\t" \
  "v_mfma_f32_16x16x32_bf16 v[24:27], a[116:119], v[168:171], v[24:27]\n\t" \
  "ds_read_b64_tr_b16 a[196:197], v239 offset:16384\n\t" \
  "v_mfma_f32_16x16x32_bf16 v[24:27], a[108:111], v[164:167], v[24:27]\n\t" \
  "buffer_load_dwordx4 v242, s[16:19], s97 offen offset:2048 sc1 lds\n\t" \
  "v_mfma_f32_16x16x32_bf16 v[24:27], a[100:103], v[160:163], v[24:27]\n\t" \
  "s_waitcnt lgkmcnt(7)\n\t" \
  "ds_read_b64_tr_b16 a[198:199], v239 offset:20480\n\t" \
  "v_mfma_f32_16x16x32_bf16 v[12:15], a[96:99], v[128:131], v[12:15]\n\t" \
  "ds_read_b128 v[160:163], v230 offset:20480\n\t" \
  "ds_read_b128 v[164:167], v230 offset:21504\n\t" \
  "v_mfma_f32_16x16x32_bf16 v[12:15], a[104:107], v[132:135], v[12:15]\n\t" \
  "buffer_load_dwordx4 v242, s[16:19], s97 offen offset:3072 sc1 lds\n\t" \
  "v_mfma_f32_16x16x32_bf16 v[12:15], a[112:115], v[136:139], v[12:15]\n\t" \
  "s_add_u32 s97, s64, 917504\n\t" \
  "s_and_b32 s97, s97, 0xe0000\n\t" \
  "ds_read_b128 v[168:171], v230 offset:22528\n\t" \
  "s_add_u32 s97, s97, s61\n\t" \
  "v_mfma_f32_16x16x32_bf16 v[12:15], a[120:123], v[140:143], v[12:15]\n\t" \
  "s_add_u32 m0, s38, 32768\n\t" \
  "ds_read_b128 v[172:175], v230 offset:23552\n\t" \
  "ds_read_b64_tr_b16 a[200:201], v238 offset:24576\n\t" \
  "v_mfma_f32_16x16x32_bf16 v[28:31], a[124:127], v[140:143], v[28:31]\n\t" \
  "buffer_load_dwordx4 v242, s[16:19], s97 offen offset:0 sc1 lds\n\t" \
  "v_mfma_f32_16x16x32_bf16 v[28:31], a[116:119], v[136:139], v[28:31]\n\t" \
  "ds_read_b64_tr_b16 a[202:203], v238 offset:28672\n\t" \
  "v_mfma_f32_16x16x32_bf16 v[28:31], a[108:111], v[132:135], v[28:31]\n\t" \
  "ds_read_b64_tr_b16 a[204:205], v239 offset:24576\n\t" \
  "v_mfma_f32_16x16x32_bf16 v[28:31], a[100:103], v[128:131], v[28:31]\n\t" \
  "s_waitcnt lgkmcnt(9)\n\t" \
  "buffer_load_dwordx4 v242, s[16:19], s97 offen offset:1024 sc1 lds\n\t" \
  "v_mfma_f32_16x16x32_bf16 v[32:35], a[32:35], v[144:147], v[32:35]\n\t" \
  "ds_read_b128 v[128:131], v230 offset:24576\n\t" \
  "ds_read_b128 v[132:135], v230 offset:25600\n\t" \
  "v_mfma_f32_16x16x32_bf16 v[32:35], a[40:43], v[148:151], v[32:35]\n\t" \
  "ds_read_b64_tr_b16 a[206:207], v239 offset:28672\n\t" \
  "ds_read_b128 v[136:139], v230 offset:26624\n\t" \
  "v_mfma_f32_16x16x32_bf16 v[32:35], a[48:51], v[152:155], v[32:35]\n\t" \
  "buffer_load_dwordx4 v242, s[16:19], s97 offen offset:2048 sc1 lds\n\t" \
  "v_mfma_f32_16x16x32_bf16 v[32:35], a[56:59], v[156:159], v[32:35]\n\t" \
  "s_waitcnt vmcnt(11)\n\t" \
  "v_cmp_ne_u32 vcc, s36, v246\n\t" \
  "v_mfma_f32_16x16x32_bf16 v[48:51], a[60:63], v[156:159], v[48:51]\n\t" \
  "s_and_b32 vcc_lo, vcc_lo, 0xfe0000\n\t" \
  "s_cbranch_scc1 .Lat_s2\n\t" \
  ".Lat_k2:\n\t" \
  "v_mfma_f32_16x16x32_bf16 v[48:51], a[52:55], v[152:155], v[48:51]\n\t" \
  "buffer_load_dwordx4 v242, s[16:19], s97 offen offset:3072 sc1 lds\n\t" \
  "v_mfma_f32_16x16x32_bf16 v[48:51], a[44:47], v[148:151], v[48:51]\n\t" \
  "s_add_u32 s97, s64, 131072\n\t" \
  "s_and_b32 s97, s97, 0xe0000\n\t" \
  "ds_read_b128 v[140:143], v230 offset:27648\n\t" \
  "s_add_u32 s97, s97, s62\n\t" \
  "v_mfma_f32_16x16x32_bf16 v[48:51], a[36:39], v[144:147], v[48:51]\n\t" \
  "s_waitcnt lgkmcnt(8)\n\t" \
  "ds_read_b64_tr_b16 a[208:209], v234 offset:49152\n\t" \
  "s_add_u32 m0, s38, 98304\n\t" \
  "v_mfma_f32_16x16x32_bf16 v[36:39], a[32:35], v[160:163], v[36:39]\n\t" \
  "buffer_load_dwordx4 v242, s[16:19], s97 offen offset:0 sc1 lds\n\t" \
  "ds_read_b128 v[144:147], v230 offset:28672\n\t" \
  "ds_read_b128 v[148:151], v230 offset:29696\n\t" \
  "v_mfma_f32_16x16x32_bf16 v[36:39], a[40:43], v[164:167], v[36:39]\n\t" \
  "ds_read_b64_tr_b16 a[210:211], v234 offset:53248\n\t" \
  "ds_read_b128 v[152:155], v230 offset:30720\n\t" \
  "v_mfma_f32_16x16x32_bf16 v[36:39], a[48:51], v[168:171], v[36:39]\n\t" \
  "ds_read_b128 v[156:159], v230 offset:31744\n\t" \
  "v_mfma_f32_16x16x32_bf16 v[36:39], a[56:59], v[172:175], v[36:39]\n\t" \
  "buffer_load_dwordx4 v242, s[16:19], s97 offen offset:1024 sc1 lds\n\t" \
  "v_mfma_f32_16x16x32_bf16 v[52:55], a[60:63], v[172:175], v[52:55]\n\t" \
  "ds_read_b64_tr_b16 a[212:213], v235 offset:49152\n\t" \
  "v_mfma_f32_16x16x32_bf16 v[52:55], a[52:55], v[168:171], v[52:55]\n\t" \
  "ds_read_b64_tr_b16 a[214:215], v235 offset:53248\n\t" \
  "v_mfma_f32_16x16x32_bf16 v[52:55], a[44:47], v[164:167], v[52:55]\n\t" \
  "s_waitcnt vmcnt(10) lgkmcnt(8)\n\t" \
  "buffer_load_dwordx4 v242, s[16:19], s97 offen offset:2048 sc1 lds\n\t" \
  "v_mfma_f32_16x16x32_bf16 v[52:55], a[36:39], v[160:163], v[52:55]\n\t" \
  "s_barrier\n\t" \
  "v_mfma_f32_16x16x32_bf16 v[40:43], a[32:35], v[128:131], v[40:43]\n\t" \
  "ds_read_b128 v[160:163], v232 offset:0\n\t" \
  "v_mfma_f32_16x16x32_bf16 v[40:43], a[40:43], v[132:135], v[40:43]\n\t" \
  "ds_read_b64_tr_b16 a[216:217], v234 offset:57344\n\t" \
  "v_mfma_f32_16x16x32_bf16 v[40:43], a[48:51], v[136:139], v[40:43]\n\t" \
  "buffer_load_dwordx4 v242, s[16:19], s97 offen offset:3072 sc1 lds\n\t" \
  "v_mfma_f32_16x16x32_bf16 v[40:43], a[56:59], v[140:143], v[40:43]\n\t" \
  "s_add_u32 s97, s64, 262144\n\t" \
  "ds_read_b64_tr_b16 a[218:219], v234 offset:61440\n\t" \
  "ds_read_b128 v[164:167], v232 offset:1024\n\t" \
  "s_and_b32 s97, s97, 0xe0000\n\t" \
  "v_mfma_f32_16x16x32_bf16 v[56:59], a[60:63], v[140:143], v[56:59]\n\t" \
  "s_add_u32 s97, s97, s62\n\t" \
  "ds_read_b128 v[168:171], v232 offset:2048\n\t" \
  "s_add_u32 m0, s38, 65536\n\t" \
  "v_mfma_f32_16x16x32_bf16 v[56:59], a[52:55], v[136:139], v[56:59]\n\t" \
  "ds_read_b128 v[172:175], v232 offset:3072\n\t" \
  "v_mfma_f32_16x16x32_bf16 v[56:59], a[44:47], v[132:135], v[56:59]\n\t" \
  "buffer_load_dwordx4 v242, s[16:19], s97 offen offset:0 sc1 lds\n\t" \
  "v_mfma_f32_16x16x32_bf16 v[56:59], a[36:39], v[128:131], v[56:59]\n\t" \
  "s_waitcnt lgkmcnt(8)\n\t" \
  "ds_read_b64_tr_b16 a[220:221], v235 offset:57344\n\t" \
  "v_mfma_f32_16x16x32_bf16 v[44:47], a[32:35], v[144:147], v[44:47]\n\t" \
  "ds_read_b128 v[128:131], v232 offset:4096\n\t" \
  "v_mfma_f32_16x16x32_bf16 v[44:47], a[40:43], v[148:151], v[44:47]\n\t" \
  "buffer_load_dwordx4 v242, s[16:19], s97 offen offset:1024 sc1 lds\n\t" \
  "v_mfma_f32_16x16x32_bf16 v[44:47], a[48:51], v[152:155], v[44:47]\n\t" \
  "ds_read_b64_tr_b16 a[222:223], v235 offset:61440\n\t" \
  "v_mfma_f32_16x16x32_bf16 v[44:47], a[56:59], v[156:159], v[44:47]\n\t" \
  "ds_read_b128 v[132:135], v232 offset:5120\n\t" \
  "v_mfma_f32_16x16x32_bf16 v[60:63], a[60:63], v[156:159], v[60:63]\n\t" \
  "ds_read_b128 v[136:139], v232 offset:6144\n\t" \
  "v_mfma_f32_16x16x32_bf16 v[60:63], a[52:55], v[152:155], v[60:63]\n\t" \
  "buffer_load_dwordx4 v242, s[16:19], s97 offen offset:2048 sc1 lds\n\t" \
  "v_mfma_f32_16x16x32_bf16 v[60:63], a[44:47], v[148:151], v[60:63]\n\t" \
  "ds_read_b128 v[140:143], v232 offset:7168\n\t" \
  "v_mfma_f32_16x16x32_bf16 v[60:63], a[36:39], v[144:147], v[60:63]\n\t" \
  "s_waitcnt lgkmcnt(3)\n\t" \
  "v_mfma_f32_16x16x32_bf16 v[0:3], a[192:195], v[160:163], v[0:3]\n\t" \
  "buffer_load_dwordx4 v242, s[16:19], s97 offen offset:3072 sc1 lds\n\t" \
  "v_mfma_f32_16x16x32_bf16 v[0:3], a[200:203], v[164:167], v[0:3]\n\t" \
  "s_add_u32 s97, s64, 393216\n\t" \
  "ds_read_b128 v[144:147], v232 offset:8192\n\t" \
  "s_and_b32 s97, s97, 0xe0000\n\t" \
  "v_mfma_f32_16x16x32_bf16 v[0:3], a[208:211], v[168:171], v[0:3]\n\t" \
  "s_add_u32 s97, s97, s62\n\t" \
  "ds_read_b128 v[148:151], v232 offset:9216\n\t" \
  "s_add_u32 m0, s38, 114688\n\t" \
  "v_mfma_f32_16x16x32_bf16 v[0:3], a[216:219], v[172:175], v[0:3]\n\t" \
  "ds_read_b128 v[152:155], v232 offset:10240\n\t" \
  "v_mfma_f32_16x16x32_bf16 v[16:19], a[220:223], v[172:175], v[16:19]\n\t" \
  "buffer_load_dwordx4 v242, s[16:19], s97 offen offset:0 sc1 lds\n\t" \
  "v_mfma_f32_16x16x32_bf16 v[16:19], a[212:215], v[168:171], v[16:19]\n\t" \
  "ds_read_b128 v[156:159], v232 offset:11264\n\t" \
  "v_mfma_f32_16x16x32_bf16 v[16:19], a[204:207], v[164:167], v[16:19]\n\t" \
  "s_nop 2\n\t" \
  "v_mfma_f32_16x16x32_bf16 v[16:19], a[196:199], v[160:163], v[16:19]\n\t" \
  "s_waitcnt lgkmcnt(4)\n\t" \
  "buffer_load_dwordx4 v242, s[16:19], s97 offen offset:1024 sc1 lds\n\t" \
  "v_mfma_f32_16x16x32_bf16 v[4:7], a[192:195], v[128:131], v[4:7]\n\t" \
  "ds_read_b128 v[160:163], v232 offset:12288\n\t" \
  "v_cvt_pk_bf16_f32 v208, v0, v1\n\t" \
  "v_mfma_f32_16x16x32_bf16 v[4:7], a[200:203], v[132:135], v[4:7]\n\t" \
  "ds_read_b128 v[164:167], v232 offset:13312\n\t" \
  "v_cvt_pk_bf16_f32 v209, v2, v3\n\t" \
  "v_mfma_f32_16x16x32_bf16 v[4:7], a[208:211], v[136:139], v[4:7]\n\t" \
  "v_cvt_pk_bf16_f32 v210, v16, v17\n\t" \
  "v_cvt_pk_bf16_f32 v211, v18, v19\n\t" \
  "v_mfma_f32_16x16x32_bf16 v[4:7], a[216:219], v[140:143], v[4:7]\n\t" \
  "buffer_load_dwordx4 v242, s[16:19], s97 offen offset:2048 sc1 lds\n\t" \
  "v_mfma_f32_16x16x32_bf16 v[20:23], a[220:223], v[140:143], v[20:23]\n\t" \
  "ds_read_b128 v[168:171], v232 offset:14336\n\t" \
  "v_mfma_f32_16x16x32_bf16 v[20:23], a[212:215], v[136:139], v[20:23]\n\t" \
  "ds_read_b128 v[172:175], v232 offset:15360\n\t" \
  "v_mfma_f32_16x16x32_bf16 v[20:23], a[204:207], v[132:135], v[20:23]\n\t" \
  "s_nop 2\n\t" \
  "v_mfma_f32_16x16x32_bf16 v[20:23], a[196:199], v[128:131], v[20:23]\n\t" \
  "s_waitcnt lgkmcnt(4)\n\t" \
  "v_cvt_pk_bf16_f32 v212, v4, v5\n\t" \
  "v_cvt_pk_bf16_f32 v213, v6, v7\n\t" \
  "v_mfma_f32_16x16x32_bf16 v[8:11], a[192:195], v[144:147], v[8:11]\n\t" \
  "ds_read_b128 v[128:131], v242 offset:16384\n\t" \
  "s_nop 0\n\t" \
  "v_mfma_f32_16x16x32_bf16 v[8:11], a[200:203], v[148:151], v[8:11]\n\t" \
  "ds_read_b128 v[132:135], v242 offset:17408\n\t" \
  "v_cvt_pk_bf16_f32 v214, v20, v21\n\t" \
  "v_mfma_f32_16x16x32_bf16 v[8:11], a[208:211], v[152:155], v[8:11]\n\t" \
  "ds_read_b128 v[136:139], v242 offset:18432\n\t" \
  "v_cvt_pk_bf16_f32 v215, v22, v23\n\t" \
  "v_mfma_f32_16x16x32_bf16 v[8:11], a[216:219], v[156:159], v[8:11]\n\t" \
  "ds_read_b128 v[140:143], v242 offset:19456\n\t" \
  "v_mfma_f32_16x16x32_bf16 v[24:27], a[220:223], v[156:159], v[24:27]\n\t" \
  "v_mfma_f32_16x16x32_bf16 v[24:27], a[212:215], v[152:155], v[24:27]\n\t" \
  "v_mfma_f32_16x16x32_bf16 v[24:27], a[204:207], v[148:151], v[24:27]\n\t" \
  "s_nop 2\n\t" \
  "v_mfma_f32_16x16x32_bf16 v[24:27], a[196:199], v[144:147], v[24:27]\n\t" \
  "s_waitcnt lgkmcnt(4)\n\t" \
  "v_cvt_pk_bf16_f32 v216, v8, v9\n\t" \
  "v_cvt_pk_bf16_f32 v217, v10, v11\n\t" \
  "v_mfma_f32_16x16x32_bf16 v[12:15], a[192:195], v[160:163], v[12:15]\n\t" \
  "ds_read_b128 v[144:147], v242 offset:20480\n\t" \
  "s_nop 0\n\t" \
  "v_mfma_f32_16x16x32_bf16 v[12:15], a[200:203], v[164:167], v[12:15]\n\t" \
  "ds_read_b128 v[148:151], v242 offset:21504\n\t" \
  "v_cvt_pk_bf16_f32 v218, v24, v25\n\t" \
  "v_mfma_f32_16x16x32_bf16 v[12:15], a[208:211], v[168:171], v[12:15]\n\t" \
  "ds_read_b128 v[152:155], v242 offset:22528\n\t" \
  "v_cvt_pk_bf16_f32 v219, v26, v27\n\t" \
  "v_mfma_f32_16x16x32_bf16 v[12:15], a[216:219], v[172:175], v[12:15]\n\t" \
  "buffer_store_dwordx4 v[208:211], v242, s[16:19], s67 offen offset:0 sc1\n\t" \
  "ds_read_b128 v[156:159], v242 offset:23552\n\t" \
  "v_mfma_f32_16x16x32_bf16 v[28:31], a[220:223], v[172:175], v[28:31]\n\t" \
  "v_mfma_f32_16x16x32_bf16 v[28:31], a[212:215], v[168:171], v[28:31]\n\t" \
  "v_mfma_f32_16x16x32_bf16 v[28:31], a[204:207], v[164:167], v[28:31]\n\t" \
  "s_nop 2\n\t" \
  "v_mfma_f32_16x16x32_bf16 v[28:31], a[196:199], v[160:163], v[28:31]\n\t" \
  "s_waitcnt lgkmcnt(4)\n\t" \
  "v_cvt_pk_bf16_f32 v220, v12, v13\n\t" \
  "v_cvt_pk_bf16_f32 v221, v14, v15\n\t" \
  "v_mfma_f32_16x16x32_bf16 v[32:35], a[64:67], v[128:131], v[32:35]\n\t" \
  "buffer_store_dwordx4 v[212:215], v242, s[16:19], s67 offen offset:1024 sc1\n\t" \
  "ds_read_b128 v[160:163], v242 offset:24576\n\t" \
  "ds_read_b128 v[164:167], v242 offset:25600\n\t" \
  "ds_read_b128 v[168:171], v242 offset:26624\n\t" \
  "v_mfma_f32_16x16x32_bf16 v[32:35], a[72:75], v[132:135], v[32:35]\n\t" \
  "v_cvt_pk_bf16_f32 v222, v28, v29\n\t" \
  "v_cvt_pk_bf16_f32 v223, v30, v31\n\t" \
  "v_mfma_f32_16x16x32_bf16 v[32:35], a[80:83], v[136:139], v[32:35]\n\t" \
  "ds_read_b128 v[172:175], v242 offset:27648\n\t" \
  "v_mfma_f32_16x16x32_bf16 v[32:35], a[88:91], v[140:143], v[32:35]\n\t" \
  "v_mfma_f32_16x16x32_bf16 v[48:51], a[92:95], v[140:143], v[48:51]\n\t" \
  "v_mfma_f32_16x16x32_bf16 v[48:51], a[84:87], v[136:139], v[48:51]\n\t" \
  "v_mfma_f32_16x16x32_bf16 v[48:51], a[76:79], v[132:135], v[48:51]\n\t" \
  "v_mfma_f32_16x16x32_bf16 v[48:51], a[68:71], v[128:131], v[48:51]\n\t" \
  "s_waitcnt lgkmcnt(4)\n\t" \
  "v_mfma_f32_16x16x32_bf16 v[36:39], a[64:67], v[144:147], v[36:39]\n\t" \
  "ds_read_b128 v[128:131], v242 offset:28672\n\t" \
  "v_mfma_f32_16x16x32_bf16 v[36:39], a[72:75], v[148:151], v[36:39]\n\t" \
  "ds_read_b128 v[132:135], v242 offset:29696\n\t" \
  "v_mfma_f32_16x16x32_bf16 v[36:39], a[80:83], v[152:155], v[36:39]\n\t" \
  "buffer_store_dwordx4 v[216:219], v242, s[16:19], s67 offen offset:2048 sc1\n\t" \
  "ds_read_b128 v[136:139], v242 offset:30720\n\t" \
  "ds_read_b128 v[140:143], v242 offset:31744\n\t" \
  "v_mfma_f32_16x16x32_bf16 v[36:39], a[88:91], v[156:159], v[36:39]\n\t" \
  "v_mfma_f32_16x16x32_bf16 v[52:55], a[92:95], v[156:159], v[52:55]\n\t" \
  "v_mfma_f32_16x16x32_bf16 v[52:55], a[84:87], v[152:155], v[52:55]\n\t" \
  "v_mfma_f32_16x16x32_bf16 v[52:55], a[76:79], v[148:151], v[52:55]\n\t" \
  "s_waitcnt vmcnt(14) lgkmcnt(4)\n\t" \
  "v_mfma_f32_16x16x32_bf16 v[52:55], a[68:71], v[144:147], v[52:55]\n\t" \
  "s_barrier\n\t" \
  "v_mfma_f32_16x16x32_bf16 v[40:43], a[64:67], v[160:163], v[40:43]\n\t" \
  "ds_read_b128 v[144:147], v242 offset:0\n\t" \
  "v_mfma_f32_16x16x32_bf16 v[40:43], a[72:75], v[164:167], v[40:43]\n\t" \
  "ds_read_b128 v[148:151], v242 offset:1024\n\t" \
  "v_mfma_f32_16x16x32_bf16 v[40:43], a[80:83], v[168:171], v[40:43]\n\t" \
  "ds_read_b128 v[152:155], v242 offset:2048\n\t" \
  "v_mfma_f32_16x16x32_bf16 v[40:43], a[88:91], v[172:175], v[40:43]\n\t" \
  "ds_read_b128 v[156:159], v242 offset:3072\n\t" \
  "v_mfma_f32_16x16x32_bf16 v[56:59], a[92:95], v[172:175], v[56:59]\n\t" \
  "v_mfma_f32_16x16x32_bf16 v[56:59], a[84:87], v[168:171], v[56:59]\n\t" \
  "v_mfma_f32_16x16x32_bf16 v[56:59], a[76:79], v[164:167], v[56:59]\n\t" \
  "v_mfma_f32_16x16x32_bf16 v[56:59], a[68:71], v[160:163], v[56:59]\n\t" \
  "s_waitcnt lgkmcnt(4)\n\t" \
  "buffer_store_dwordx4 v[220:223], v242, s[16:19], s67 offen offset:3072 sc1\n\t" \
  "v_mfma_f32_16x16x32_bf16 v[44:47], a[64:67], v[128:131], v[44:47]\n\t" \
  "ds_read_b128 v[160:163], v242 offset:4096\n\t" \
  "v_mfma_f32_16x16x32_bf16 v[44:47], a[72:75], v[132:135], v[44:47]\n\t" \
  "ds_read_b128 v[164:167], v242 offset:5120\n\t" \
  "v_mfma_f32_16x16x32_bf16 v[44:47], a[80:83], v[136:139], v[44:47]\n\t" \
  "ds_read_b128 v[168:171], v242 offset:6144\n\t" \
  "v_mfma_f32_16x16x32_bf16 v[44:47], a[88:91], v[140:143], v[44:47]\n\t" \
  "ds_read_b128 v[172:175], v242 offset:7168\n\t" \
  "v_mfma_f32_16x16x32_bf16 v[60:63], a[92:95], v[140:143], v[60:63]\n\t" \
  "v_mfma_f32_16x16x32_bf16 v[60:63], a[84:87], v[136:139], v[60:63]\n\t" \
  "v_mfma_f32_16x16x32_bf16 v[60:63], a[76:79], v[132:135], v[60:63]\n\t" \
  "v_mfma_f32_16x16x32_bf16 v[60:63], a[68:71], v[128:131], v[60:63]\n\t" \
  "s_waitcnt lgkmcnt(4)\n\t" \
  "v_mfma_f32_16x16x32_bf16 v[32:35], a[96:99], v[144:147], v[32:35]\n\t" \
  "ds_read_b128 v[128:131], v242 offset:8192\n\t" \
  "v_mfma_f32_16x16x32_bf16 v[32:35], a[104:107], v[148:151], v[32:35]\n\t" \
  "ds_read_b128 v[132:135], v242 offset:9216\n\t" \
  "v_mfma_f32_16x16x32_bf16 v[32:35], a[112:115], v[152:155], v[32:35]\n\t" \
  "ds_read_b128 v[136:139], v242 offset:10240\n\t" \
  "v_mfma_f32_16x16x32_bf16 v[32:35], a[120:123], v[156:159], v[32:35]\n\t" \
  "buffer_load_dwordx4 v242, s[16:19], s97 offen offset:3072 sc1 lds\n\t" \
  "v_mfma_f32_16x16x32_bf16 v[48:51], a[124:127], v[156:159], v[48:51]\n\t" \
  "s_add_u32 s97, s64, 524288\n\t" \
  "ds_read_b128 v[140:143], v242 offset:11264\n\t" \
  "s_and_b32 s97, s97, 0xe0000\n\t" \
  "v_mfma_f32_16x16x32_bf16 v[48:51], a[116:119], v[152:155], v[48:51]\n\t" \
  "s_add_u32 s97, s97, s62\n\t" \
  "s_add_u32 m0, s38, 81920\n\t" \
  "v_mfma_f32_16x16x32_bf16 v[48:51], a[108:111], v[148:151], v[48:51]\n\t" \
  "v_mfma_f32_16x16x32_bf16 v[48:51], a[100:103], v[144:147], v[48:51]\n\t" \
  "s_waitcnt lgkmcnt(4)\n\t" \
  "buffer_load_dwordx4 v242, s[16:19], s97 offen offset:0 sc1 lds\n\t" \
  "v_mfma_f32_16x16x32_bf16 v[36:39], a[96:99], v[160:163], v[36:39]\n\t" \
  "ds_read_b128 v[144:147], v242 offset:12288\n\t" \
  "v_mfma_f32_16x16x32_bf16 v[36:39], a[104:107], v[164:167], v[36:39]\n\t" \
  "ds_read_b128 v[148:151], v242 offset:13312\n\t" \
  "v_mfma_f32_16x16x32_bf16 v[36:39], a[112:115], v[168:171], v[36:39]\n\t" \
  "ds_read_b128 v[152:155], v242 offset:14336\n\t" \
  "v_mfma_f32_16x16x32_bf16 v[36:39], a[120:123], v[172:175], v[36:39]\n\t" \
  "buffer_load_dwordx4 v242, s[16:19], s97 offen offset:1024 sc1 lds\n\t" \
  "v_mfma_f32_16x16x32_bf16 v[52:55], a[124:127], v[172:175], v[52:55]\n\t" \
  "ds_read_b128 v[156:159], v242 offset:15360\n\t" \
  "v_mfma_f32_16x16x32_bf16 v[52:55], a[116:119], v[168:171], v[52:55]\n\t" \
  "v_mfma_f32_16x16x32_bf16 v[52:55], a[108:111], v[164:167], v[52:55]\n\t" \
  "buffer_load_dwordx4 v242, s[16:19], s97 offen offset:2048 sc1 lds\n\t" \
  "v_mfma_f32_16x16x32_bf16 v[52:55], a[100:103], v[160:163], v[52:55]\n\t" \
  "s_waitcnt lgkmcnt(4)\n\t" \
  "v_mfma_f32_16x16x32_bf16 v[40:43], a[96:99], v[128:131], v[40:43]\n\t" \
  "ds_read_b128 v[160:163], v242 offset:32768\n\t" \
  "v_mfma_f32_16x16x32_bf16 v[40:43], a[104:107], v[132:135], v[40:43]\n\t" \
  "ds_read_b128 v[164:167], v242 offset:33792\n\t" \
  "v_mfma_f32_16x16x32_bf16 v[40:43], a[112:115], v[136:139], v[40:43]\n\t" \
  "buffer_load_dwordx4 v242, s[16:19], s97 offen offset:3072 sc1 lds\n\t" \
  "v_mfma_f32_16x16x32_bf16 v[40:43], a[120:123], v[140:143], v[40:43]\n\t" \
  "s_add_u32 s97, s64, 655360\n\t" \
  "s_and_b32 s97, s97, 0xe0000\n\t" \
  "s_add_u32 s97, s97, s62\n\t" \
  "v_mfma_f32_16x16x32_bf16 v[56:59], a[124:127], v[140:143], v[56:59]\n\t" \
  "s_add_u32 m0, s38, 131072\n\t" \
  "ds_read_b128 v[168:171], v242 offset:34816\n\t" \
  "v_mfma_f32_16x16x32_bf16 v[56:59], a[116:119], v[136:139], v[56:59]\n\t" \
  "buffer_load_dwordx4 v242, s[16:19], s97 offen offset:0 sc1 lds\n\t" \
  "v_mfma_f32_16x16x32_bf16 v[56:59], a[108:111], v[132:135], v[56:59]\n\t" \
  "ds_read_b128 v[172:175], v242 offset:35840\n\t" \
  "v_mfma_f32_16x16x32_bf16 v[56:59], a[100:103], v[128:131], v[56:59]\n\t" \
  "s_waitcnt lgkmcnt(4)\n\t" \
  "v_mfma_f32_16x16x32_bf16 v[44:47], a[96:99], v[144:147], v[44:47]\n\t" \
  "ds_read_b128 v[128:131], v242 offset:36864\n\t" \
  "v_mfma_f32_16x16x32_bf16 v[44:47], a[104:107], v[148:151], v[44:47]\n\t" \
  "buffer_load_dwordx4 v242, s[16:19], s97 offen offset:1024 sc1 lds\n\t" \
  "v_mfma_f32_16x16x32_bf16 v[44:47], a[112:115], v[152:155], v[44:47]\n\t" \
  "ds_read_b128 v[132:135], v242 offset:37888\n\t" \
  "v_mfma_f32_16x16x32_bf16 v[44:47], a[120:123], v[156:159], v[44:47]\n\t" \
  "ds_read_b128 v[136:139], v242 offset:38912\n\t" \
  "v_mfma_f32_16x16x32_bf16 v[60:63], a[124:127], v[156:159], v[60:63]\n\t" \
  "buffer_load_dwordx4 v242, s[16:19], s97 offen offset:2048 sc1 lds\n\t" \
  "v_mfma_f32_16x16x32_bf16 v[60:63], a[116:119], v[152:155], v[60:63]\n\t" \
  "ds_read_b128 v[140:143], v242 offset:39936\n\t" \
  "v_mfma_f32_16x16x32_bf16 v[60:63], a[108:111], v[148:151], v[60:63]\n\t" \
  "v_mfma_f32_16x16x32_bf16 v[60:63], a[100:103], v[144:147], v[60:63]\n\t" \
  "s_waitcnt lgkmcnt(4)\n\t" \
  "v_mfma_f32_16x16x32_bf16 v[32:35], a[192:195], v[160:163], v[32:35]\n\t" \
  "buffer_load_dwordx4 v242, s[16:19], s97 offen offset:3072 sc1 lds\n\t" \
  "v_mfma_f32_16x16x32_bf16 v[32:35], a[200:203], v[164:167], v[32:35]\n\t" \
  "s_add_u32 s97, s64, 786432\n\t" \
  "ds_read_b128 v[144:147], v242 offset:40960\n\t" \
  "s_and_b32 s97, s97, 0xe0000\n\t" \
  "v_mfma_f32_16x16x32_bf16 v[32:35], a[208:211], v[168:171], v[32:35]\n\t" \
  "s_add_u32 s97, s97, s62\n\t" \
  "ds_read_b128 v[148:151], v242 offset:41984\n\t" \
  "s_add_u32 m0, s38, 49152\n\t" \
  "v_mfma_f32_16x16x32_bf16 v[32:35], a[216:219], v[172:175], v[32:35]\n\t" \
  "ds_read_b128 v[152:155], v242 offset:43008\n\t" \
  "v_mfma_f32_16x16x32_bf16 v[48:51], a[220:223], v[172:175], v[48:51]\n\t" \
  "ds_read_b128 v[156:159], v242 offset:44032\n\t" \
  "v_mfma_f32_16x16x32_bf16 v[48:51], a[212:215], v[168:171], v[48:51]\n\t" \
  "v_mfma_f32_16x16x32_bf16 v[48:51], a[204:207], v[164:167], v[48:51]\n\t" \
  "buffer_load_dwordx4 v242, s[16:19], s97 offen offset:0 sc1 lds\n\t" \
  "v_mfma_f32_16x16x32_bf16 v[48:51], a[196:199], v[160:163], v[48:51]\n\t" \
  "s_waitcnt lgkmcnt(4)\n\t" \
  "v_cvt_pk_bf16_f32 v0, v32, v33\n\t" \
  "v_cvt_pk_bf16_f32 v1, v34, v35\n\t" \
  "v_mfma_f32_16x16x32_bf16 v[36:39], a[192:195], v[128:131], v[36:39]\n\t" \
  ".Lhs13:\n\ts_cbranch_execz .Lhs13n\n\t" \
  "ds_read_b128 v[160:163], v242 offset:45056\n\t" \
  "s_nop 0\n\t" \
  "v_mfma_f32_16x16x32_bf16 v[36:39], a[200:203], v[132:135], v[36:39]\n\t" \
  "ds_read_b128 v[164:167], v242 offset:46080\n\t" \
  "v_cvt_pk_bf16_f32 v2, v48, v49\n\t" \
  "v_mfma_f32_16x16x32_bf16 v[36:39], a[208:211], v[136:139], v[36:39]\n\t" \
  "ds_read_b128 v[168:171], v242 offset:47104\n\t" \
  "v_cvt_pk_bf16_f32 v3, v50, v51\n\t" \
  "v_mfma_f32_16x16x32_bf16 v[36:39], a[216:219], v[140:143], v[36:39]\n\t" \
  "ds_read_b128 v[172:175], v242 offset:48128\n\t" \
  "v_mfma_f32_16x16x32_bf16 v[52:55], a[220:223], v[140:143], v[52:55]\n\t" \
  "v_mfma_f32_16x16x32_bf16 v[52:55], a[212:215], v[136:139], v[52:55]\n\t" \
  "v_mfma_f32_16x16x32_bf16 v[52:55], a[204:207], v[132:135], v[52:55]\n\t" \
  "s_waitcnt vmcnt(17) lgkmcnt(4)\n\t" \
  "s_nop 1\n\t" \
  "v_mfma_f32_16x16x32_bf16 v[52:55], a[196:199], v[128:131], v[52:55]\n\t" \
  "s_barrier\n\t" \
  "v_mfma_f32_16x16x32_bf16 v[40:43], a[192:195], v[144:147], v[40:43]\n\t" \
  "ds_read_b128 v[128:131], v230 offset:32768\n\t" \
  "v_cvt_pk_bf16_f32 v4, v36, v37\n\t" \
  "v_mfma_f32_16x16x32_bf16 v[40:43], a[200:203], v[148:151], v[40:43]\n\t" \
  "v_cvt_pk_bf16_f32 v5, v38, v39\n\t" \
  "v_and_b32 v235, 15, v233\n\t" \
  "v_mfma_f32_16x16x32_bf16 v[40:43], a[208:211], v[152:155], v[40:43]\n\t" \
  "v_cvt_pk_bf16_f32 v6, v52, v53\n\t" \
  "v_cvt_pk_bf16_f32 v7, v54, v55\n\t" \
  "v_mfma_f32_16x16x32_bf16 v[40:43], a[216:219], v[156:159], v[40:43]\n\t" \
  "buffer_store_dwordx4 v[0:3], v242, s[16:19], s68 offen offset:0 sc1\n\t" \
  "ds_read_b128 v[132:135], v230 offset:33792\n\t" \
  "v_lshrrev_b32 v236, 4, v233\n\t" \
  "v_lshlrev_b32 v234, 13, v236\n\t" \
  "v_mfma_f32_16x16x32_bf16 v[56:59], a[220:223], v[156:159], v[56:59]\n\t" \
  "ds_read_b128 v[136:139], v230 offset:34816\n\t" \
  "v_lshl_add_u32 v234, v235, 4, v234\n\t" \
  "v_mfma_f32_16x16x32_bf16 v[56:59], a[212:215], v[152:155], v[56:59]\n\t" \
  "buffer_load_dwordx4 v242, s[16:19], s97 offen offset:1024 sc1 lds\n\t" \
  "v_mfma_f32_16x16x32_bf16 v[56:59], a[204:207], v[148:151], v[56:59]\n\t" \
  "ds_read_b128 v[140:143], v230 offset:35840\n\t" \
  "v_lshlrev_b32 v240, 7, v236\n\t" \
  "v_mfma_f32_16x16x32_bf16 v[56:59], a[196:199], v[144:147], v[56:59]\n\t" \
  "s_waitcnt lgkmcnt(4)\n\t" \
  "v_cvt_pk_bf16_f32 v8, v40, v41\n\t" \
  "v_cvt_pk_bf16_f32 v9, v42, v43\n\t" \
  "v_mfma_f32_16x16x32_bf16 v[44:47], a[192:195], v[160:163], v[44:47]\n\t" \
  "s_waitcnt vmcnt(12)\n\t" \
  "buffer_store_dword v243, v244, s[16:19], s71 offen offset:0 sc1\n\t" \
  "ds_read_b128 v[144:147], v230 offset:36864\n\t" \
  "v_mfma_f32_16x16x32_bf16 v[44:47], a[200:203], v[164:167], v[44:47]\n\t" \
  "v_cvt_pk_bf16_f32 v10, v56, v57\n\t" \
  "v_cvt_pk_bf16_f32 v11, v58, v59\n\t" \
  "v_mfma_f32_16x16x32_bf16 v[44:47], a[208:211], v[168:171], v[44:47]\n\t" \
  "buffer_store_dwordx4 v[4:7], v242, s[16:19], s68 offen offset:1024 sc1\n\t" \
  "ds_read_b128 v[148:151], v230 offset:37888\n\t" \
  "v_lshl_add_u32 v240, v235, 3, v240\n\t" \
  "v_add_u32 v240, s76, v240\n\t" \
  "v_mfma_f32_16x16x32_bf16 v[44:47], a[216:219], v[172:175], v[44:47]\n\t" \
  "ds_read_b128 v[152:155], v230 offset:38912\n\t" \
  "v_lshlrev_b32 v235, 2, v235\n\t" \
  "v_mfma_f32_16x16x32_bf16 v[60:63], a[220:223], v[172:175], v[60:63]\n\t" \
  "ds_read_b128 v[156:159], v230 offset:39936\n\t" \
  "v_mfma_f32_16x16x32_bf16 v[60:63], a[212:215], v[168:171], v[60:63]\n\t" \
  "v_mfma_f32_16x16x32_bf16 v[60:63], a[204:207], v[164:167], v[60:63]\n\t" \
  "s_nop 2\n\t" \
  "v_mfma_f32_16x16x32_bf16 v[60:63], a[196:199], v[160:163], v[60:63]\n\t" \
  "s_waitcnt lgkmcnt(4)\n\t" \
  "v_cvt_pk_bf16_f32 v12, v44, v45\n\t" \
  "v_cvt_pk_bf16_f32 v13, v46, v47\n\t" \
  STP("s_memrealtime s[94:95]\n\t") \
  STP("s_memtime s[92:93]\n\t") \
  "v_mfma_f32_16x16x32_bf16 v[64:67], a[128:131], v[128:131], v[64:67]\n\t" \
  "ds_read_b128 v[160:163], v230 offset:40960\n\t" \
  "s_nop 0\n\t" \
  "v_mfma_f32_16x16x32_bf16 v[64:67], a[136:139], v[132:135], v[64:67]\n\t" \
  "ds_read_b128 v[164:167], v230 offset:41984\n\t" \
  "v_cvt_pk_bf16_f32 v14, v60, v61\n\t" \
  "v_mfma_f32_16x16x32_bf16 v[64:67], a[144:147], v[136:139], v[64:67]\n\t" \
  "ds_read_b128 v[168:171], v230 offset:43008\n\t" \
  "v_cvt_pk_bf16_f32 v15, v62, v63\n\t" \
  "v_mfma_f32_16x16x32_bf16 v[64:67], a[152:155], v[140:143], v[64:67]\n\t" \
  "ds_read_b128 v[172:175], v230 offset:44032\n\t" \
  "s_add_u32 s78, s75, 0\n\t" \
  "v_mfma_f32_16x16x32_bf16 v[80:83], a[156:159], v[140:143], v[80:83]\n\t" \
  "v_mfma_f32_16x16x32_bf16 v[80:83], a[148:151], v[136:139], v[80:83]\n\t" \
  "v_mfma_f32_16x16x32_bf16 v[80:83], a[140:143], v[132:135], v[80:83]\n\t" \
  "v_mfma_f32_16x16x32_bf16 v[80:83], a[132:135], v[128:131], v[80:83]\n\t" \
  "s_waitcnt lgkmcnt(4)\n\t" \
  "buffer_load_dwordx4 v[208:211], v234, s[16:19], s78 offen sc1\n\t" \
  "v_mfma_f32_16x16x32_bf16 v[68:71], a[128:131], v[144:147], v[68:71]\n\t" \
  "ds_read_b128 v[128:131], v230 offset:45056\n\t" \
  "s_add_u32 s78, s75, 32768\n\t" \
  "v_mfma_f32_16x16x32_bf16 v[68:71], a[136:139], v[148:151], v[68:71]\n\t" \
  "ds_read_b128 v[132:135], v230 offset:46080\n\t" \
  "v_mfma_f32_16x16x32_bf16 v[68:71], a[144:147], v[152:155], v[68:71]\n\t" \
  "ds_read_b128 v[136:139], v230 offset:47104\n\t" \
  "v_mfma_f32_16x16x32_bf16 v[68:71], a[152:155], v[156:159], v[68:71]\n\t" \
  "ds_read_b128 v[140:143], v230 offset:48128\n\t" \
  "v_mfma_f32_16x16x32_bf16 v[84:87], a[156:159], v[156:159], v[84:87]\n\t" \
  STP("v_writelane_b32 v255, s94, 34\n\t") \
  STP("v_writelane_b32 v255, s92, 35\n\t") \
  "buffer_load_dwordx4 v[212:215], v234, s[16:19], s78 offen sc1\n\t" \
  "v_mfma_f32_16x16x32_bf16 v[84:87], a[148:151], v[152:155], v[84:87]\n\t" \
  "s_add_u32 s78, s75, 65536\n\t" \
  "v_mfma_f32_16x16x32_bf16 v[84:87], a[140:143], v[148:151], v[84:87]\n\t" \
  "v_mfma_f32_16x16x32_bf16 v[84:87], a[132:135], v[144:147], v[84:87]\n\t" \
  "s_waitcnt lgkmcnt(4)\n\t" \
  "v_mfma_f32_16x16x32_bf16 v[72:75], a[128:131], v[160:163], v[72:75]\n\t" \
  "ds_read_b128 v[144:147], v230 offset:0\n\t" \
  "v_mfma_f32_16x16x32_bf16 v[72:75], a[136:139], v[164:167], v[72:75]\n\t" \
  "buffer_load_dwordx4 v[216:219], v234, s[16:19], s78 offen sc1\n\t" \
  "v_mfma_f32_16x16x32_bf16 v[72:75], a[144:147], v[168:171], v[72:75]\n\t" \
  "ds_read_b128 v[148:151], v230 offset:1024\n\t" \
  "s_add_u32 s78, s75, 98304\n\t" \
  "v_mfma_f32_16x16x32_bf16 v[72:75], a[152:155], v[172:175], v[72:75]\n\t" \
  "ds_read_b128 v[152:155], v230 offset:2048\n\t" \
  "v_mfma_f32_16x16x32_bf16 v[88:91], a[156:159], v[172:175], v[88:91]\n\t" \
  "ds_read_b128 v[156:159], v230 offset:3072\n\t" \
  "v_mfma_f32_16x16x32_bf16 v[88:91], a[148:151], v[168:171], v[88:91]\n\t" \
  "v_mfma_f32_16x16x32_bf16 v[88:91], a[140:143], v[164:167], v[88:91]\n\t" \
  "buffer_load_dwordx4 v[220:223], v234, s[16:19], s78 offen sc1\n\t" \
  "v_mfma_f32_16x16x32_bf16 v[88:91], a[132:135], v[160:163], v[88:91]\n\t" \
  "s_waitcnt lgkmcnt(4)\n\t" \
  "buffer_load_dword v224, v235, s[32:35], s80 offen\n\t" \
  "v_mfma_f32_16x16x32_bf16 v[76:79], a[128:131], v[128:131], v[76:79]\n\t" \
  "ds_read_b128 v[160:163], v230 offset:4096\n\t" \
  "v_lshrrev_b32 v250, 4, v233\n\t" \
  "v_mfma_f32_16x16x32_bf16 v[76:79], a[136:139], v[132:135], v[76:79]\n\t" \
  "v_and_b32 v238, 15, v233\n\t" \
  "v_xor_b32 v26, v238, v250\n\t" \
  "v_mfma_f32_16x16x32_bf16 v[76:79], a[144:147], v[136:139], v[76:79]\n\t" \
  "ds_read_b128 v[164:167], v230 offset:5120\n\t" \
  "v_lshlrev_b32 v26, 4, v26\n\t" \
  "v_mfma_f32_16x16x32_bf16 v[76:79], a[152:155], v[140:143], v[76:79]\n\t" \
  "v_lshl_add_u32 v26, v250, 11, v26\n\t" \
  "v_xor_b32 v27, 64, v26\n\t" \
  "v_mfma_f32_16x16x32_bf16 v[92:95], a[156:159], v[140:143], v[92:95]\n\t" \
  "ds_read_b128 v[168:171], v230 offset:6144\n\t" \
  "v_xor_b32 v28, 0x80, v26\n\t" \
  "v_mfma_f32_16x16x32_bf16 v[92:95], a[148:151], v[136:139], v[92:95]\n\t" \
  "v_xor_b32 v29, 0xc0, v26\n\t" \
  "v_lshrrev_b32 v30, 5, v233\n\t" \
  "v_mfma_f32_16x16x32_bf16 v[92:95], a[140:143], v[132:135], v[92:95]\n\t" \
  "ds_read_b128 v[172:175], v230 offset:7168\n\t" \
  "v_lshl_add_u32 v30, s3, 2, v30\n\t" \
  "v_mfma_f32_16x16x32_bf16 v[92:95], a[132:135], v[128:131], v[92:95]\n\t" \
  "s_waitcnt lgkmcnt(4)\n\t" \
  "v_xor_b32 v30, v30, v238\n\t" \
  "v_lshlrev_b32 v30, 4, v30\n\t" \
  "v_mfma_f32_16x16x32_bf16 v[64:67], a[160:163], v[144:147], v[64:67]\n\t" \
  "buffer_store_dwordx4 v[8:11], v242, s[16:19], s68 offen offset:2048 sc1\n\t" \
  "ds_read_b128 v[128:131], v230 offset:8192\n\t" \
  "v_lshl_add_u32 v30, v238, 8, v30\n\t" \
  "v_and_b32 v250, 1, v250\n\t" \
  "v_mfma_f32_16x16x32_bf16 v[64:67], a[168:171], v[148:151], v[64:67]\n\t" \
  "v_lshl_add_u32 v30, v250, 3, v30\n\t" \
  "v_add_u32 v238, 0, v30\n\t" \
  "v_mfma_f32_16x16x32_bf16 v[64:67], a[176:179], v[152:155], v[64:67]\n\t" \
  "ds_read_b128 v[132:135], v230 offset:9216\n\t" \
  "v_xor_b32 v239, 32, v238\n\t" \
  "v_mfma_f32_16x16x32_bf16 v[64:67], a[184:187], v[156:159], v[64:67]\n\t" \
  "v_bfe_u32 v237, v233, 1, 2\n\t" \
  "v_bfe_u32 v16, v233, 3, 1\n\t" \
  "v_mfma_f32_16x16x32_bf16 v[80:83], a[188:191], v[156:159], v[80:83]\n\t" \
  "ds_read_b128 v[136:139], v230 offset:10240\n\t" \
  "v_lshlrev_b32 v237, 4, v237\n\t" \
  "v_mfma_f32_16x16x32_bf16 v[80:83], a[180:183], v[152:155], v[80:83]\n\t" \
  "v_lshl_add_u32 v237, v16, 3, v237\n\t" \
  "v_and_b32 v16, 1, v233\n\t" \
  "v_mfma_f32_16x16x32_bf16 v[80:83], a[172:175], v[148:151], v[80:83]\n\t" \
  "ds_read_b128 v[140:143], v230 offset:11264\n\t" \
  "v_lshl_add_u32 v237, v16, 2, v237\n\t" \
  "v_mfma_f32_16x16x32_bf16 v[80:83], a[164:167], v[144:147], v[80:83]\n\t" \
  "s_waitcnt lgkmcnt(4)\n\t" \
  "v_add_u32 v237, s76, v237\n\t" \
  "v_cmp_eq_u32 vcc, 2, v236\n\t" \
  "v_mfma_f32_16x16x32_bf16 v[68:71], a[160:163], v[160:163], v[68:71]\n\t" \
  "ds_read_b128 v[144:147], v230 offset:12288\n\t" \
  "v_mov_b32 v16, 0\n\t" \
  "v_mfma_f32_16x16x32_bf16 v[68:71], a[168:171], v[164:167], v[68:71]\n\t" \
  "v_mov_b32 v17, 0\n\t" \
  "v_mov_b32 v18, 0\n\t" \
  "v_mfma_f32_16x16x32_bf16 v[68:71], a[176:179], v[168:171], v[68:71]\n\t" \
  "ds_read_b128 v[148:151], v230 offset:13312\n\t" \
  "v_mov_b32 v19, 0\n\t" \
  "v_mfma_f32_16x16x32_bf16 v[68:71], a[184:187], v[172:175], v[68:71]\n\t" \
  "v_cndmask_b32 v236, 0, 1.0, vcc\n\t" \
  "v_mov_b32 v20, 0x3f803f80\n\t" \
  "v_mfma_f32_16x16x32_bf16 v[84:87], a[188:191], v[172:175], v[84:87]\n\t" \
  "ds_read_b128 v[152:155], v230 offset:14336\n\t" \
  "v_add_u32 v21, s76, v242\n\t" \
  "v_mfma_f32_16x16x32_bf16 v[84:87], a[180:183], v[168:171], v[84:87]\n\t" \
  "buffer_load_dwordx4 v242, s[16:19], s97 offen offset:2048 sc1 lds\n\t" \
  "v_mfma_f32_16x16x32_bf16 v[84:87], a[172:175], v[164:167], v[84:87]\n\t" \
  "s_waitcnt vmcnt(16) lgkmcnt(3)\n\t" \
  "ds_write_b128 v21, v[16:19] offset:0\n\t" \
  "v_mfma_f32_16x16x32_bf16 v[84:87], a[164:167], v[160:163], v[84:87]\n\t" \
  "ds_write_b128 v21, v[16:19] offset:1024\n\t" \
  "s_barrier\n\t" \
  "v_mfma_f32_16x16x32_bf16 v[72:75], a[160:163], v[128:131], v[72:75]\n\t" \
  "buffer_load_dwordx4 v242, s[16:19], s97 offen offset:3072 sc1 lds\n\t" \
  "v_mfma_f32_16x16x32_bf16 v[72:75], a[168:171], v[132:135], v[72:75]\n\t" \
  "ds_read_b128 v[156:159], v230 offset:15360\n\t" \
  "v_mfma_f32_16x16x32_bf16 v[72:75], a[176:179], v[136:139], v[72:75]\n\t" \
  "ds_read_b128 v[160:163], v230 offset:49152\n\t" \
  "v_mfma_f32_16x16x32_bf16 v[72:75], a[184:187], v[140:143], v[72:75]\n\t" \
  "ds_write_b128 v21, v[16:19] offset:1536\n\t" \
  "v_mfma_f32_16x16x32_bf16 v[88:91], a[188:191], v[140:143], v[88:91]\n\t" \
  "buffer_store_dwordx4 v[12:15], v242, s[16:19], s68 offen offset:3072 sc1\n\t" \
  "ds_read_b128 v[164:167], v230 offset:50176\n\t" \
  "ds_read_b128 v[168:171], v230 offset:51200\n\t" \
  "ds_read_b128 v[172:175], v230 offset:52224\n\t" \
  "v_mfma_f32_16x16x32_bf16 v[88:91], a[180:183], v[136:139], v[88:91]\n\t" \
  "s_mov_b64 exec, 0xffff\n\t" \
  "ds_write_b32 v237, v20 offset:960\n\t" \
  "s_mov_b64 exec, -1\n\t" \
  "v_mfma_f32_16x16x32_bf16 v[88:91], a[172:175], v[132:135], v[88:91]\n\t" \
  "v_mfma_f32_16x16x32_bf16 v[88:91], a[164:167], v[128:131], v[88:91]\n\t" \
  "s_waitcnt lgkmcnt(6)\n\t" \
  "v_mfma_f32_16x16x32_bf16 v[76:79], a[160:163], v[144:147], v[76:79]\n\t" \
  "ds_read_b128 v[128:131], v230 offset:53248\n\t" \
  "v_mfma_f32_16x16x32_bf16 v[76:79], a[168:171], v[148:151], v[76:79]\n\t" \
  "ds_read_b128 v[132:135], v230 offset:54272\n\t" \
  "v_mfma_f32_16x16x32_bf16 v[76:79], a[176:179], v[152:155], v[76:79]\n\t" \
  "s_add_u32 m0, s38, 0\n\t" \
  "ds_read_b128 v[136:139], v230 offset:55296\n\t" \
  "s_add_u32 s96, s50, 0\n\t" \
  "v_mfma_f32_16x16x32_bf16 v[76:79], a[184:187], v[156:159], v[76:79]\n\t" \
  "ds_read_b128 v[140:143], v230 offset:56320\n\t" \
  "v_mfma_f32_16x16x32_bf16 v[92:95], a[188:191], v[156:159], v[92:95]\n\t" \
  "v_mfma_f32_16x16x32_bf16 v[92:95], a[180:183], v[152:155], v[92:95]\n\t" \
  "v_mfma_f32_16x16x32_bf16 v[92:95], a[172:175], v[148:151], v[92:95]\n\t" \
  "v_mfma_f32_16x16x32_bf16 v[92:95], a[164:167], v[144:147], v[92:95]\n\t" \
  "s_waitcnt lgkmcnt(5)\n\t" \
  "v_mfma_f32_16x16x32_bf16 v[64:67], a[0:3], v[160:163], v[64:67]\n\t" \
  "buffer_load_dwordx4 v26, s[4:7], s96 offen offset:0 lds\n\t" \
  "v_mfma_f32_16x16x32_bf16 v[64:67], a[8:11], v[164:167], v[64:67]\n\t" \
  "s_add_u32 s96, s50, 7168\n\t" \
  "ds_read_b128 v[144:147], v230 offset:57344\n\t" \
  "v_mfma_f32_16x16x32_bf16 v[64:67], a[16:19], v[168:171], v[64:67]\n\t" \
  "ds_read_b128 v[148:151], v230 offset:58368\n\t" \
  "v_mfma_f32_16x16x32_bf16 v[64:67], a[24:27], v[172:175], v[64:67]\n\t" \
  "ds_read_b128 v[152:155], v230 offset:59392\n\t" \
  "v_mfma_f32_16x16x32_bf16 v[80:83], a[28:31], v[172:175], v[80:83]\n\t" \
  "ds_read_b128 v[156:159], v230 offset:60416\n\t" \
  "v_mfma_f32_16x16x32_bf16 v[80:83], a[20:23], v[168:171], v[80:83]\n\t" \
  "buffer_load_dwordx4 v27, s[4:7], s96 offen offset:1024 lds\n\t" \
  "v_mfma_f32_16x16x32_bf16 v[80:83], a[12:15], v[164:167], v[80:83]\n\t" \
  "s_add_u32 s96, s50, 14336\n\t" \
  "v_mfma_f32_16x16x32_bf16 v[80:83], a[4:7], v[160:163], v[80:83]\n\t" \
  "s_waitcnt lgkmcnt(4)\n\t" \
  "v_mfma_f32_16x16x32_bf16 v[68:71], a[0:3], v[128:131], v[68:71]\n\t" \
  "ds_read_b128 v[160:163], v230 offset:61440\n\t" \
  "v_mfma_f32_16x16x32_bf16 v[68:71], a[8:11], v[132:135], v[68:71]\n\t" \
  "ds_read_b128 v[164:167], v230 offset:62464\n\t" \
  "v_mfma_f32_16x16x32_bf16 v[68:71], a[16:19], v[136:139], v[68:71]\n\t" \
  "buffer_load_dwordx4 v28, s[4:7], s96 offen offset:2048 lds\n\t" \
  "v_mfma_f32_16x16x32_bf16 v[68:71], a[24:27], v[140:143], v[68:71]\n\t" \
  "s_add_u32 s96, s50, 21504\n\t" \
  "ds_read_b128 v[168:171], v230 offset:63488\n\t" \
  "v_mfma_f32_16x16x32_bf16 v[84:87], a[28:31], v[140:143], v[84:87]\n\t" \
  "ds_read_b128 v[172:175], v230 offset:64512\n\t" \
  "v_mfma_f32_16x16x32_bf16 v[84:87], a[20:23], v[136:139], v[84:87]\n\t" \
  "v_mfma_f32_16x16x32_bf16 v[84:87], a[12:15], v[132:135], v[84:87]\n\t" \
  "v_mfma_f32_16x16x32_bf16 v[84:87], a[4:7], v[128:131], v[84:87]\n\t" \
  "s_waitcnt lgkmcnt(4)\n\t" \
  "buffer_load_dwordx4 v29, s[4:7], s96 offen offset:3072 lds\n\t" \
  "v_mfma_f32_16x16x32_bf16 v[72:75], a[0:3], v[144:147], v[72:75]\n\t" \
  "s_mov_b32 s40, 0x05040100\n\t" \
  "ds_read_b128 v[128:131], v230 offset:16384\n\t" \
  "s_mov_b32 s41, 0x07060302\n\t" \
  "v_mfma_f32_16x16x32_bf16 v[72:75], a[8:11], v[148:151], v[72:75]\n\t" \
  "s_load_dwordx4 s[4:7], s[0:1], 0xc0\n\t" \
  "ds_read_b128 v[132:135], v230 offset:17408\n\t" \
  "s_and_b32 s42, s2, 4\n\t" \
  "v_mfma_f32_16x16x32_bf16 v[72:75], a[16:19], v[152:155], v[72:75]\n\t" \
  "s_add_u32 s97, s64, 917504\n\t" \
  "s_and_b32 s97, s97, 0xe0000\n\t" \
  "s_add_u32 s97, s97, s62\n\t" \
  "v_mfma_f32_16x16x32_bf16 v[72:75], a[24:27], v[156:159], v[72:75]\n\t" \
  "s_add_u32 m0, s38, 16384\n\t" \
  "ds_read_b128 v[136:139], v230 offset:18432\n\t" \
  "s_lshl_b32 s9, s42, 11\n\t" \
  "v_mfma_f32_16x16x32_bf16 v[88:91], a[28:31], v[156:159], v[88:91]\n\t" \
  "buffer_load_dwordx4 v242, s[16:19], s97 offen offset:0 sc1 lds\n\t" \
  "v_mfma_f32_16x16x32_bf16 v[88:91], a[20:23], v[152:155], v[88:91]\n\t" \
  "s_xor_b32 s9, s9, s55\n\t" \
  "ds_read_b128 v[140:143], v230 offset:19456\n\t" \
  "s_lshr_b32 s43, s2, 6\n\t" \
  "v_mfma_f32_16x16x32_bf16 v[88:91], a[12:15], v[148:151], v[88:91]\n\t" \
  "s_add_u32 s42, s42, s43\n\t" \
  "s_and_b32 s43, s2, 3\n\t" \
  "s_lshl2_add_u32 s8, s42, s43\n\t" \
  "v_mfma_f32_16x16x32_bf16 v[88:91], a[4:7], v[144:147], v[88:91]\n\t" \
  "s_waitcnt lgkmcnt(4)\n\t" \
  "s_and_b32 s11, s8, 15\n\t" \
  "s_lshl_b32 s11, s11, 15\n\t" \
  "v_mfma_f32_16x16x32_bf16 v[76:79], a[0:3], v[160:163], v[76:79]\n\t" \
  "buffer_load_dwordx4 v242, s[16:19], s97 offen offset:1024 sc1 lds\n\t" \
  "v_mfma_f32_16x16x32_bf16 v[76:79], a[8:11], v[164:167], v[76:79]\n\t" \
  "s_add_u32 s11, s11, s9\n\t" \
  "ds_read_b128 v[144:147], v230 offset:20480\n\t" \
  "s_add_u32 s11, s11, 524288\n\t" \
  "v_mfma_f32_16x16x32_bf16 v[76:79], a[16:19], v[168:171], v[76:79]\n\t" \
  "s_waitcnt vmcnt(10)\n\t" \
  "v_xor_b32 v246, v243, v209\n\t" \
  "v_bitop3_b32 v246, v246, v211, v243 bitop3:0xf6\n\t" \
  "v_mfma_f32_16x16x32_bf16 v[76:79], a[24:27], v[172:175], v[76:79]\n\t" \
  "s_and_b32 s42, s2, 0x20\n\t" \
  "v_bitop3_b32 v246, v246, v213, v243 bitop3:0xf6\n\t" \
  "v_bitop3_b32 v246, v246, v215, v243 bitop3:0xf6\n\t" \
  "v_mfma_f32_16x16x32_bf16 v[92:95], a[28:31], v[172:175], v[92:95]\n\t" \
  "s_lshl_b32 s42, s42, 9\n\t" \
  "v_bitop3_b32 v246, v246, v217, v243 bitop3:0xf6\n\t" \
  "v_bitop3_b32 v246, v246, v219, v243 bitop3:0xf6\n\t" \
  "v_mfma_f32_16x16x32_bf16 v[92:95], a[20:23], v[168:171], v[92:95]\n\t" \
  "s_add_u32 s10, s11, s42\n\t" \
  "v_bitop3_b32 v246, v246, v221, v243 bitop3:0xf6\n\t" \
  "v_bitop3_b32 v246, v246, v223, v243 bitop3:0xf6\n\t" \
  "v_mfma_f32_16x16x32_bf16 v[92:95], a[12:15], v[164:167], v[92:95]\n\t" \
  "buffer_load_dwordx4 v242, s[16:19], s97 offen offset:2048 sc1 lds\n\t" \
  "v_mfma_f32_16x16x32_bf16 v[92:95], a[4:7], v[160:163], v[92:95]\n\t" \
  "s_waitcnt lgkmcnt(1)\n\t" \
  "ds_read_b128 v[148:151], v230 offset:21504\n\t" \
  "v_cmp_ne_u32 vcc, 0, v246\n\t" \
  "v_mfma_f32_16x16x32_bf16 v[64:67], a[32:35], v[128:131], v[64:67]\n\t" \
  "buffer_load_dwordx4 v242, s[16:19], s97 offen offset:3072 sc1 lds\n\t" \
  "ds_read_b128 v[152:155], v230 offset:22528\n\t" \
  "s_and_b32 s42, s2, 0x18\n\t" \
  "v_mfma_f32_16x16x32_bf16 v[64:67], a[40:43], v[132:135], v[64:67]\n\t" \
  "s_add_u32 s97, s64, 131072\n\t" \
  "s_and_b32 s97, s97, 0xe0000\n\t" \
  "s_add_u32 s97, s97, s63\n\t" \
  "v_mfma_f32_16x16x32_bf16 v[64:67], a[48:51], v[136:139], v[64:67]\n\t" \
  "s_cbranch_vccnz .Lat_s3\n\t" \
  ".Lat_k3:\n\t" \
  "ds_read_b128 v[156:159], v230 offset:23552\n\t" \
  "s_add_u32 m0, s38, 32768\n\t" \
  "v_mfma_f32_16x16x32_bf16 v[64:67], a[56:59], v[140:143], v[64:67]\n\t" \
  "buffer_load_dwordx4 v242, s[16:19], s97 offen offset:0 sc1 lds\n\t" \
  "v_mfma_f32_16x16x32_bf16 v[80:83], a[60:63], v[140:143], v[80:83]\n\t" \
  "s_lshl_b32 s42, s42, 8\n\t" \
  "ds_read_b128 v[160:163], v230 offset:24576\n\t" \
  "v_add_f32 v208, v208, v212\n\t" \
  "v_mfma_f32_16x16x32_bf16 v[80:83], a[52:55], v[136:139], v[80:83]\n\t" \
  "s_add_u32 s10, s10, s42\n\t" \
  "ds_read_b128 v[164:167], v230 offset:25600\n\t" \
  "v_add_f32 v216, v216, v220\n\t" \
  "v_mfma_f32_16x16x32_bf16 v[80:83], a[44:47], v[132:135], v[80:83]\n\t" \
  "s_mul_i32 s42, s3, 0x3000\n\t" \
  "ds_read_b128 v[168:171], v230 offset:26624\n\t" \
  "v_add_f32 v210, v210, v214\n\t" \
  "v_mfma_f32_16x16x32_bf16 v[80:83], a[36:39], v[128:131], v[80:83]\n\t" \
  "s_waitcnt lgkmcnt(3)\n\t" \
  "ds_read_b128 v[172:175], v230 offset:27648\n\t" \
  "v_add_f32 v218, v218, v222\n\t" \
  "v_mfma_f32_16x16x32_bf16 v[68:71], a[32:35], v[144:147], v[68:71]\n\t" \
  "buffer_load_dwordx4 v242, s[16:19], s97 offen offset:1024 sc1 lds\n\t" \
  "v_mfma_f32_16x16x32_bf16 v[68:71], a[40:43], v[148:151], v[68:71]\n\t" \
  "s_and_b32 s42, s42, 0x1000\n\t" \
  "ds_read_b128 v[128:131], v230 offset:28672\n\t" \
  "v_add_f32 v208, v208, v216\n\t" \
  "v_mfma_f32_16x16x32_bf16 v[68:71], a[48:51], v[152:155], v[68:71]\n\t" \
  "s_add_u32 s11, s11, s42\n\t" \
  "ds_read_b128 v[132:135], v230 offset:29696\n\t" \
  "v_add_f32 v210, v210, v218\n\t" \
  "v_mfma_f32_16x16x32_bf16 v[68:71], a[56:59], v[156:159], v[68:71]\n\t" \
  "s_and_b32 s42, s3, 2\n\t" \
  "v_lshlrev_b32 v19, 16, v224\n\t" \
  "v_and_b32 v20, 0xffff0000, v224\n\t" \
  "v_mfma_f32_16x16x32_bf16 v[84:87], a[60:63], v[156:159], v[84:87]\n\t" \
  "s_lshl_b32 s42, s42, 13\n\t" \
  "ds_read_b128 v[136:139], v230 offset:30720\n\t" \
  "v_cvt_pk_bf16_f32 v18, v208, v210\n\t" \
  "v_mfma_f32_16x16x32_bf16 v[84:87], a[52:55], v[152:155], v[84:87]\n\t" \
  "s_add_u32 s11, s11, s42\n\t" \
  "v_lshlrev_b32 v16, 16, v18\n\t" \
  "v_and_b32 v17, 0xffff0000, v18\n\t" \
  "v_mfma_f32_16x16x32_bf16 v[84:87], a[44:47], v[148:151], v[84:87]\n\t" \
  "s_waitcnt vmcnt(11) lgkmcnt(3)\n\t" \
  "ds_read_b128 v[140:143], v230 offset:31744\n\t" \
  "v_add_f32 v16, v16, v236\n\t" \
  "v_mfma_f32_16x16x32_bf16 v[84:87], a[36:39], v[144:147], v[84:87]\n\t" \
  "s_barrier\n\t" \
  "v_mfma_f32_16x16x32_bf16 v[72:75], a[32:35], v[160:163], v[72:75]\n\t" \
  "buffer_load_dwordx4 v242, s[16:19], s97 offen offset:2048 sc1 lds\n\t" \
  "v_mfma_f32_16x16x32_bf16 v[72:75], a[40:43], v[164:167], v[72:75]\n\t" \
  "s_add_u32 s12, s11, 2048\n\t" \
  "ds_read_b128 v[144:147], v232 offset:0\n\t" \
  "v_add_f32 v17, v17, v236\n\t" \
  "v_mfma_f32_16x16x32_bf16 v[72:75], a[48:51], v[168:171], v[72:75]\n\t" \
  "s_sub_u32 s39, s10, s11\n\t" \
  "ds_read_b128 v[148:151], v232 offset:1024\n\t" \
  "v_cvt_pk_bf16_f32 v18, v16, v17\n\t" \
  "v_mfma_f32_16x16x32_bf16 v[72:75], a[56:59], v[172:175], v[72:75]\n\t" \
  "v_lshlrev_b32 v16, 16, v18\n\t" \
  "v_and_b32 v17, 0xffff0000, v18\n\t" \
  "v_mfma_f32_16x16x32_bf16 v[88:91], a[60:63], v[172:175], v[88:91]\n\t" \
  "ds_read_b128 v[152:155], v232 offset:2048\n\t" \
  "v_mfma_f32_16x16x32_bf16 v[88:91], a[52:55], v[168:171], v[88:91]\n\t" \
  "s_waitcnt vmcnt(11)\n\t" \
  "buffer_store_dword v243, v244, s[16:19], s72 offen offset:16 sc1\n\t" \
  "ds_read_b128 v[156:159], v232 offset:3072\n\t" \
  "v_mfma_f32_16x16x32_bf16 v[88:91], a[44:47], v[164:167], v[88:91]\n\t" \
  "ds_write_b64 v240, v[16:17] offset:2560\n\t" \
  "v_mfma_f32_16x16x32_bf16 v[88:91], a[36:39], v[160:163], v[88:91]\n\t" \
  "s_waitcnt lgkmcnt(5)\n\t" \
  "v_mul_f32 v22, v16, v19\n\t" \
  "v_mul_f32 v23, v17, v20\n\t" \
  "v_mfma_f32_16x16x32_bf16 v[76:79], a[32:35], v[128:131], v[76:79]\n\t" \
  "ds_read_b128 v[160:163], v232 offset:4096\n\t" \
  "v_cvt_pk_bf16_f32 v24, v22, v23\n\t" \
  "v_mfma_f32_16x16x32_bf16 v[76:79], a[40:43], v[132:135], v[76:79]\n\t" \
  "v_add_f32 v18, v22, v23\n\t" \
  "v_lshlrev_b32 v19, 16, v24\n\t" \
  "v_mfma_f32_16x16x32_bf16 v[76:79], a[48:51], v[136:139], v[76:79]\n\t" \
  "ds_read_b128 v[164:167], v232 offset:5120\n\t" \
  "v_and_b32 v20, 0xffff0000, v24\n\t" \
  "v_mfma_f32_16x16x32_bf16 v[76:79], a[56:59], v[140:143], v[76:79]\n\t" \
  "v_add_f32_dpp v18, v18, v18 row_ror:8 row_mask:0xf bank_mask:0xf\n\t" \
  "v_sub_f32 v22, v22, v19\n\t" \
  "v_mfma_f32_16x16x32_bf16 v[92:95], a[60:63], v[140:143], v[92:95]\n\t" \
  "ds_read_b128 v[168:171], v232 offset:6144\n\t" \
  "v_sub_f32 v23, v23, v20\n\t" \
  "v_mfma_f32_16x16x32_bf16 v[92:95], a[52:55], v[136:139], v[92:95]\n\t" \
  "v_add_f32_dpp v18, v18, v18 row_ror:4 row_mask:0xf bank_mask:0xf\n\t" \
  "v_cvt_pk_bf16_f32 v25, v22, v23\n\t" \
  "v_mfma_f32_16x16x32_bf16 v[92:95], a[44:47], v[132:135], v[92:95]\n\t" \
  "s_mov_b32 s84, 0\n\t" \
  "ds_read_b128 v[172:175], v232 offset:7168\n\t" \
  "v_add_f32_dpp v18, v18, v18 row_ror:2 row_mask:0xf bank_mask:0xf\n\t" \
  "v_mfma_f32_16x16x32_bf16 v[92:95], a[36:39], v[128:131], v[92:95]\n\t" \
  "s_waitcnt lgkmcnt(5)\n\t" \
  "buffer_load_dwordx4 v242, s[16:19], s97 offen offset:3072 sc1 lds\n\t" \
  "v_mfma_f32_16x16x32_bf16 v[64:67], a[64:67], v[144:147], v[64:67]\n\t" \
  "s_mov_b32 s85, 0xffff\n\t" \
  "ds_read_b128 v[128:131], v232 offset:8192\n\t" \
  "v_add_f32_dpp v18, v18, v18 row_ror:1 row_mask:0xf bank_mask:0xf\n\t" \
  "v_mfma_f32_16x16x32_bf16 v[64:67], a[72:75], v[148:151], v[64:67]\n\t" \
  "s_mov_b64 exec, s[84:85]\n\t" \
  "ds_write_b32 v237, v24 offset:1040\n\t" \
  "ds_write_b32 v237, v25 offset:1120\n\t" \
  "s_mov_b64 exec, -1\n\t" \
  "v_mfma_f32_16x16x32_bf16 v[64:67], a[80:83], v[152:155], v[64:67]\n\t" \
  "s_add_u32 s97, s64, 262144\n\t" \
  "s_and_b32 s97, s97, 0xe0000\n\t" \
  "s_add_u32 s97, s97, s63\n\t" \
  "v_mfma_f32_16x16x32_bf16 v[64:67], a[88:91], v[156:159], v[64:67]\n\t" \
  "s_add_u32 m0, s38, 65536\n\t" \
  "ds_read_b128 v[132:135], v232 offset:9216\n\t" \
  "v_readlane_b32 s82, v18, 32\n\t" \
  "v_mfma_f32_16x16x32_bf16 v[80:83], a[92:95], v[156:159], v[80:83]\n\t" \
  "buffer_load_dwordx4 v242, s[16:19], s97 offen offset:0 sc1 lds\n\t" \
  "v_mfma_f32_16x16x32_bf16 v[80:83], a[84:87], v[152:155], v[80:83]\n\t" \
  "ds_read_b128 v[136:139], v232 offset:10240\n\t" \
  "v_readlane_b32 s83, v18, 16\n\t" \
  "v_mfma_f32_16x16x32_bf16 v[80:83], a[76:79], v[148:151], v[80:83]\n\t" \
  "ds_read_b128 v[140:143], v232 offset:11264\n\t" \
  "v_and_b32 v235, 16, v242\n\t" \
  "v_mfma_f32_16x16x32_bf16 v[80:83], a[68:71], v[144:147], v[80:83]\n\t" \
  "s_waitcnt lgkmcnt(6)\n\t" \
  "s_cmp_eq_u32 s39, 0\n\t" \
  "s_cselect_b32 s11, s12, s11\n\t" \
  "v_mfma_f32_16x16x32_bf16 v[68:71], a[64:67], v[160:163], v[68:71]\n\t" \
  "ds_read_b128 v[144:147], v232 offset:12288\n\t" \
  "v_mfma_f32_16x16x32_bf16 v[68:71], a[72:75], v[164:167], v[68:71]\n\t" \
  "buffer_load_dwordx4 v242, s[16:19], s97 offen offset:1024 sc1 lds\n\t" \
  "v_mfma_f32_16x16x32_bf16 v[68:71], a[80:83], v[168:171], v[68:71]\n\t" \
  "ds_read_b128 v[148:151], v232 offset:13312\n\t" \
  "v_mfma_f32_16x16x32_bf16 v[68:71], a[88:91], v[172:175], v[68:71]\n\t" \
  "s_cmp_eq_u32 s39, 2048\n\t" \
  "s_cselect_b32 s12, s11, s12\n\t" \
  "s_lshl_b32 s42, s3, 10\n\t" \
  "v_mfma_f32_16x16x32_bf16 v[84:87], a[92:95], v[172:175], v[84:87]\n\t" \
  "buffer_load_dwordx4 v242, s[16:19], s97 offen offset:2048 sc1 lds\n\t" \
  "v_mfma_f32_16x16x32_bf16 v[84:87], a[84:87], v[168:171], v[84:87]\n\t" \
  "s_add_u32 s10, s10, s42\n\t" \
  "ds_read_b128 v[152:155], v232 offset:14336\n\t" \
  "s_lshr_b32 s42, s2, 1\n\t" \
  "v_mfma_f32_16x16x32_bf16 v[84:87], a[76:79], v[164:167], v[84:87]\n\t" \
  "s_and_b32 s42, s42, 12\n\t" \
  "ds_read_b128 v[156:159], v232 offset:15360\n\t" \
  "s_or_b32 s42, s42, s43\n\t" \
  "v_mfma_f32_16x16x32_bf16 v[84:87], a[68:71], v[160:163], v[84:87]\n\t" \
  "s_waitcnt lgkmcnt(4)\n\t" \
  "buffer_load_dwordx4 v242, s[16:19], s97 offen offset:3072 sc1 lds\n\t" \
  "v_mfma_f32_16x16x32_bf16 v[72:75], a[64:67], v[128:131], v[72:75]\n\t" \
  "s_add_u32 s97, s64, 393216\n\t" \
  "s_and_b32 s97, s97, 0xe0000\n\t" \
  "s_add_u32 s97, s97, s63\n\t" \
  "v_mfma_f32_16x16x32_bf16 v[72:75], a[72:75], v[132:135], v[72:75]\n\t" \
  "s_add_u32 m0, s38, 98304\n\t" \
  "ds_read_b128 v[160:163], v242 offset:49152\n\t" \
  "s_lshl_b32 s13, s42, 8\n\t" \
  "v_mfma_f32_16x16x32_bf16 v[72:75], a[80:83], v[136:139], v[72:75]\n\t" \
  "buffer_load_dwordx4 v242, s[16:19], s97 offen offset:0 sc1 lds\n\t" \
  "v_mfma_f32_16x16x32_bf16 v[72:75], a[88:91], v[140:143], v[72:75]\n\t" \
  "s_add_u32 s13, s13, s9\n\t" \
  "ds_read_b128 v[164:167], v242 offset:50176\n\t" \
  "s_add_u32 s13, s13, 65536\n\t" \
  "v_mfma_f32_16x16x32_bf16 v[88:91], a[92:95], v[140:143], v[88:91]\n\t" \
  "s_bfe_u32 s44, s2, 0x30003\n\t" \
  "ds_read_b128 v[168:171], v242 offset:51200\n\t" \
  "s_lshl_b32 s42, s3, 13\n\t" \
  "v_mfma_f32_16x16x32_bf16 v[88:91], a[84:87], v[136:139], v[88:91]\n\t" \
  "buffer_load_dwordx4 v242, s[16:19], s97 offen offset:1024 sc1 lds\n\t" \
  "v_mfma_f32_16x16x32_bf16 v[88:91], a[76:79], v[132:135], v[88:91]\n\t" \
  "s_and_b32 s42, s42, 0x2000\n\t" \
  "ds_read_b128 v[172:175], v242 offset:52224\n\t" \
  "s_xor_b32 s14, s42, s55\n\t" \
  "v_mfma_f32_16x16x32_bf16 v[88:91], a[68:71], v[128:131], v[88:91]\n\t" \
  "s_waitcnt lgkmcnt(4)\n\t" \
  "s_lshr_b32 s42, s3, 1\n\t" \
  "s_lshl1_add_u32 s42, s44, s42\n\t" \
  "v_mfma_f32_16x16x32_bf16 v[76:79], a[64:67], v[144:147], v[76:79]\n\t" \
  "s_lshl_b32 s42, s42, 8\n\t" \
  "ds_read_b128 v[128:131], v242 offset:53248\n\t" \
  "s_add_u32 s14, s14, s42\n\t" \
  "v_mfma_f32_16x16x32_bf16 v[76:79], a[72:75], v[148:151], v[76:79]\n\t" \
  "s_lshl3_add_u32 s14, s8, s14\n\t" \
  "ds_read_b128 v[132:135], v242 offset:54272\n\t" \
  "s_add_u32 s14, s14, 65536\n\t" \
  "v_mfma_f32_16x16x32_bf16 v[76:79], a[80:83], v[152:155], v[76:79]\n\t" \
  "s_add_u32 s15, s9, 262144\n\t" \
  "ds_read_b128 v[136:139], v242 offset:55296\n\t" \
  "s_lshl_b32 s42, s8, 13\n\t" \
  "v_mfma_f32_16x16x32_bf16 v[76:79], a[88:91], v[156:159], v[76:79]\n\t" \
  "s_and_b32 s42, s42, 0x3c000\n\t" \
  "ds_read_b128 v[140:143], v242 offset:56320\n\t" \
  "s_add_u32 s20, s15, s42\n\t" \
  "v_mfma_f32_16x16x32_bf16 v[92:95], a[92:95], v[156:159], v[92:95]\n\t" \
  "buffer_load_dwordx4 v242, s[16:19], s97 offen offset:2048 sc1 lds\n\t" \
  "v_mfma_f32_16x16x32_bf16 v[92:95], a[84:87], v[152:155], v[92:95]\n\t" \
  "s_and_b32 s42, s8, 1\n\t" \
  "s_lshl_b32 s42, s42, 12\n\t" \
  "s_add_u32 s20, s20, s42\n\t" \
  "v_mfma_f32_16x16x32_bf16 v[92:95], a[76:79], v[148:151], v[92:95]\n\t" \
  "s_lshl2_add_u32 s42, s44, s3\n\t" \
  "s_lshl_b32 s42, s42, 7\n\t" \
  "s_add_u32 s20, s20, s42\n\t" \
  "v_mfma_f32_16x16x32_bf16 v[92:95], a[68:71], v[144:147], v[92:95]\n\t" \
  "s_waitcnt lgkmcnt(4)\n\t" \
  "s_and_b32 s21, s42, 0x7ff\n\t" \
  "s_lshl_b32 s22, s8, 17\n\t" \
  "v_mfma_f32_16x16x32_bf16 v[64:67], a[96:99], v[160:163], v[64:67]\n\t" \
  "s_lshr_b32 s42, s42, 1\n\t" \
  "ds_read_b128 v[144:147], v242 offset:57344\n\t" \
  "s_add_u32 s22, s22, s42\n\t" \
  "v_mfma_f32_16x16x32_bf16 v[64:67], a[104:107], v[164:167], v[64:67]\n\t" \
  "buffer_load_dwordx4 v242, s[16:19], s97 offen offset:3072 sc1 lds\n\t" \
  "v_mfma_f32_16x16x32_bf16 v[64:67], a[112:115], v[168:171], v[64:67]\n\t" \
  "s_and_b32 s42, s2, 0x20\n\t" \
  "ds_read_b128 v[148:151], v242 offset:58368\n\t" \
  "s_lshl_b32 s42, s42, 8\n\t" \
  "v_mfma_f32_16x16x32_bf16 v[64:67], a[120:123], v[172:175], v[64:67]\n\t" \
  "s_xor_b32 s42, s42, s55\n\t" \
  "ds_read_b128 v[152:155], v242 offset:59392\n\t" \
  "s_add_u32 s21, s21, s42\n\t" \
  "v_mfma_f32_16x16x32_bf16 v[80:83], a[124:127], v[172:175], v[80:83]\n\t" \
  "s_add_u32 s97, s64, 524288\n\t" \
  "s_and_b32 s97, s97, 0xe0000\n\t" \
  "s_add_u32 s97, s97, s63\n\t" \
  "v_mfma_f32_16x16x32_bf16 v[80:83], a[116:119], v[168:171], v[80:83]\n\t" \
  "s_add_u32 m0, s38, 114688\n\t" \
  "ds_read_b128 v[156:159], v242 offset:60416\n\t" \
  "s_lshl_b32 s42, s43, 11\n\t" \
  "v_mfma_f32_16x16x32_bf16 v[80:83], a[108:111], v[164:167], v[80:83]\n\t" \
  "buffer_load_dwordx4 v242, s[16:19], s97 offen offset:0 sc1 lds\n\t" \
  "v_mfma_f32_16x16x32_bf16 v[80:83], a[100:103], v[160:163], v[80:83]\n\t" \
  "s_waitcnt lgkmcnt(4)\n\t" \
  "s_add_u32 s21, s21, s42\n\t" \
  "s_lshr_b32 s42, s8, 2\n\t" \
  "v_mfma_f32_16x16x32_bf16 v[68:71], a[96:99], v[128:131], v[68:71]\n\t" \
  "s_lshl_b32 s42, s42, 14\n\t" \
  "ds_read_b128 v[160:163], v242 offset:61440\n\t" \
  "s_add_u32 s21, s21, s42\n\t" \
  "v_mfma_f32_16x16x32_bf16 v[68:71], a[104:107], v[132:135], v[68:71]\n\t" \
  "buffer_load_dwordx4 v242, s[16:19], s97 offen offset:1024 sc1 lds\n\t" \
  "v_mfma_f32_16x16x32_bf16 v[68:71], a[112:115], v[136:139], v[68:71]\n\t" \
  "s_add_u32 s21, s21, 1966080\n\t" \
  "ds_read_b128 v[164:167], v242 offset:62464\n\t" \
  "s_lshl_b32 s42, s59, 7\n\t" \
  "v_mfma_f32_16x16x32_bf16 v[68:71], a[120:123], v[140:143], v[68:71]\n\t" \
  "s_and_b32 s23, s42, 0xffffc000\n\t" \
  "ds_read_b128 v[168:171], v242 offset:63488\n\t" \
  "s_lshl_b32 s42, s59, 6\n\t" \
  "v_mfma_f32_16x16x32_bf16 v[84:87], a[124:127], v[140:143], v[84:87]\n\t" \
  "s_and_b32 s42, s42, 0x1800\n\t" \
  "ds_read_b128 v[172:175], v242 offset:64512\n\t" \
  "s_add_u32 s23, s23, s42\n\t" \
  "v_mfma_f32_16x16x32_bf16 v[84:87], a[116:119], v[136:139], v[84:87]\n\t" \
  "s_mul_i32 s42, s3, 0x1400\n\t" \
  "s_and_b32 s42, s42, 0x2400\n\t" \
  "s_xor_b32 s42, s42, s55\n\t" \
  "v_mfma_f32_16x16x32_bf16 v[84:87], a[108:111], v[132:135], v[84:87]\n\t" \
  "s_waitcnt vmcnt(10) lgkmcnt(4)\n\t" \
  "buffer_load_dwordx4 v242, s[16:19], s97 offen offset:2048 sc1 lds\n\t" \
  "v_mfma_f32_16x16x32_bf16 v[84:87], a[100:103], v[128:131], v[84:87]\n\t" \
  "s_barrier\n\t" \
  ".Lhs14:\n\ts_cbranch_execz .Lhs14n\n\t" \
  "v_mfma_f32_16x16x32_bf16 v[72:75], a[96:99], v[144:147], v[72:75]\n\t" \
  "s_add_u32 s23, s23, s42\n\t" \
  "ds_read_b128 v[128:131], v242 offset:16384\n\t" \
  "s_add_u32 s23, s23, 1048576\n\t" \
  "v_mfma_f32_16x16x32_bf16 v[72:75], a[104:107], v[148:151], v[72:75]\n\t" \
  "buffer_load_dwordx4 v242, s[16:19], s97 offen offset:3072 sc1 lds\n\t" \
  "v_mfma_f32_16x16x32_bf16 v[72:75], a[112:115], v[152:155], v[72:75]\n\t" \
  "s_add_u32 s97, s64, 655360\n\t" \
  "s_and_b32 s97, s97, 0xe0000\n\t" \
  "s_add_u32 s97, s97, s63\n\t" \
  "v_mfma_f32_16x16x32_bf16 v[72:75], a[120:123], v[156:159], v[72:75]\n\t" \
  "s_add_u32 m0, s38, 81920\n\t" \
  "ds_read_b128 v[132:135], v242 offset:17408\n\t" \
  "v_mfma_f32_16x16x32_bf16 v[88:91], a[124:127], v[156:159], v[88:91]\n\t" \
  "buffer_load_dwordx4 v242, s[16:19], s97 offen offset:0 sc1 lds\n\t" \
  "v_mfma_f32_16x16x32_bf16 v[88:91], a[116:119], v[152:155], v[88:91]\n\t" \
  "ds_read_b128 v[136:139], v242 offset:18432\n\t" \
  "v_mfma_f32_16x16x32_bf16 v[88:91], a[108:111], v[148:151], v[88:91]\n\t" \
  "ds_read_b128 v[140:143], v242 offset:19456\n\t" \
  "v_mfma_f32_16x16x32_bf16 v[88:91], a[100:103], v[144:147], v[88:91]\n\t" \
  "s_waitcnt lgkmcnt(4)\n\t" \
  "buffer_load_dwordx4 v242, s[16:19], s97 offen offset:1024 sc1 lds\n\t" \
  "v_mfma_f32_16x16x32_bf16 v[76:79], a[96:99], v[160:163], v[76:79]\n\t" \
  "ds_read_b128 v[144:147], v242 offset:20480\n\t" \
  "v_mfma_f32_16x16x32_bf16 v[76:79], a[104:107], v[164:167], v[76:79]\n\t" \
  "ds_read_b128 v[148:151], v242 offset:21504\n\t" \
  "v_mfma_f32_16x16x32_bf16 v[76:79], a[112:115], v[168:171], v[76:79]\n\t" \
  "buffer_load_dwordx4 v242, s[16:19], s97 offen offset:2048 sc1 lds\n\t" \
  "v_mfma_f32_16x16x32_bf16 v[76:79], a[120:123], v[172:175], v[76:79]\n\t" \
  "ds_read_b128 v[152:155], v242 offset:22528\n\t" \
  "v_mfma_f32_16x16x32_bf16 v[92:95], a[124:127], v[172:175], v[92:95]\n\t" \
  "ds_read_b128 v[156:159], v242 offset:23552\n\t" \
  "v_mfma_f32_16x16x32_bf16 v[92:95], a[116:119], v[168:171], v[92:95]\n\t" \
  "buffer_load_dwordx4 v242, s[16:19], s97 offen offset:3072 sc1 lds\n\t" \
  "v_mfma_f32_16x16x32_bf16 v[92:95], a[108:111], v[164:167], v[92:95]\n\t" \
  "s_add_u32 s97, s64, 786432\n\t" \
  "s_and_b32 s97, s97, 0xe0000\n\t" \
  "s_add_u32 s97, s97, s63\n\t" \
  "v_mfma_f32_16x16x32_bf16 v[92:95], a[100:103], v[160:163], v[92:95]\n\t" \
  "s_waitcnt lgkmcnt(4)\n\t" \
  "s_add_u32 m0, s38, 131072\n\t" \
  "v_mfma_f32_16x16x32_bf16 v[64:67], a[192:195], v[128:131], v[64:67]\n\t" \
  "buffer_load_dwordx4 v242, s[16:19], s97 offen offset:0 sc1 lds\n\t" \
  "v_mfma_f32_16x16x32_bf16 v[64:67], a[200:203], v[132:135], v[64:67]\n\t" \
  "ds_read_b128 v[160:163], v242 offset:24576\n\t" \
  "v_mfma_f32_16x16x32_bf16 v[64:67], a[208:211], v[136:139], v[64:67]\n\t" \
  "ds_read_b128 v[164:167], v242 offset:25600\n\t" \
  "v_mfma_f32_16x16x32_bf16 v[64:67], a[216:219], v[140:143], v[64:67]\n\t" \
  "buffer_load_dwordx4 v242, s[16:19], s97 offen offset:1024 sc1 lds\n\t" \
  "v_mfma_f32_16x16x32_bf16 v[80:83], a[220:223], v[140:143], v[80:83]\n\t" \
  "ds_read_b128 v[168:171], v242 offset:26624\n\t" \
  "v_mfma_f32_16x16x32_bf16 v[80:83], a[212:215], v[136:139], v[80:83]\n\t" \
  "ds_read_b128 v[172:175], v242 offset:27648\n\t" \
  "v_mfma_f32_16x16x32_bf16 v[80:83], a[204:207], v[132:135], v[80:83]\n\t" \
  "buffer_load_dwordx4 v242, s[16:19], s97 offen offset:2048 sc1 lds\n\t" \
  "v_mfma_f32_16x16x32_bf16 v[80:83], a[196:199], v[128:131], v[80:83]\n\t" \
  "s_waitcnt lgkmcnt(4)\n\t" \
  "v_cvt_pk_bf16_f32 v0, v64, v65\n\t" \
  "v_cvt_pk_bf16_f32 v1, v66, v67\n\t" \
  "v_mfma_f32_16x16x32_bf16 v[68:71], a[192:195], v[144:147], v[68:71]\n\t" \
  "buffer_load_dword v246, v235, s[16:19], s73 offen sc1\n\t" \
  "v_mfma_f32_16x16x32_bf16 v[68:71], a[200:203], v[148:151], v[68:71]\n\t" \
  "buffer_load_dwordx4 v242, s[16:19], s97 offen offset:3072 sc1 lds\n\t" \
  "v_mfma_f32_16x16x32_bf16 v[68:71], a[208:211], v[152:155], v[68:71]\n\t" \
  "ds_read_b128 v[128:131], v242 offset:28672\n\t" \
  "v_cvt_pk_bf16_f32 v2, v80, v81\n\t" \
  "v_mfma_f32_16x16x32_bf16 v[68:71], a[216:219], v[156:159], v[68:71]\n\t" \
  "ds_read_b128 v[132:135], v242 offset:29696\n\t" \
  "v_cvt_pk_bf16_f32 v3, v82, v83\n\t" \
  "v_mfma_f32_16x16x32_bf16 v[84:87], a[220:223], v[156:159], v[84:87]\n\t" \
  "ds_read_b128 v[136:139], v242 offset:30720\n\t" \
  "v_mov_b32 v250, 0x3f80\n\t" \
  "v_mfma_f32_16x16x32_bf16 v[84:87], a[212:215], v[152:155], v[84:87]\n\t" \
  "ds_read_b128 v[140:143], v242 offset:31744\n\t" \
  "v_lshlrev_b32 v250, v235, v250\n\t" \
  "v_mfma_f32_16x16x32_bf16 v[84:87], a[204:207], v[148:151], v[84:87]\n\t" \
  "s_mov_b32 s84, 0x300003\n\t" \
  "s_mov_b32 s85, 0x30000300\n\t" \
  "s_mov_b32 s86, 0xc0000c\n\t" \
  "v_mfma_f32_16x16x32_bf16 v[84:87], a[196:199], v[144:147], v[84:87]\n\t" \
  "s_waitcnt lgkmcnt(4)\n\t" \
  "v_cvt_pk_bf16_f32 v4, v68, v69\n\t" \
  "v_cvt_pk_bf16_f32 v5, v70, v71\n\t" \
  "v_mfma_f32_16x16x32_bf16 v[72:75], a[192:195], v[160:163], v[72:75]\n\t" \
  "buffer_store_dwordx4 v[0:3], v242, s[16:19], s69 offen offset:0 sc1\n\t" \
  "ds_read_b128 v[144:147], v242 offset:32768\n\t" \
  "ds_read_b128 v[148:151], v242 offset:33792\n\t" \
  "ds_read_b128 v[152:155], v242 offset:34816\n\t" \
  "v_mfma_f32_16x16x32_bf16 v[72:75], a[200:203], v[164:167], v[72:75]\n\t" \
  "s_mov_b32 s87, 0xc0000c00\n\t" \
  "v_cvt_pk_bf16_f32 v6, v84, v85\n\t" \
  "v_cvt_pk_bf16_f32 v7, v86, v87\n\t" \
  "v_mfma_f32_16x16x32_bf16 v[72:75], a[208:211], v[168:171], v[72:75]\n\t" \
  "v_cndmask_b32 v248, 0, v250, s[84:85]\n\t" \
  "v_cndmask_b32 v249, 0, v250, s[86:87]\n\t" \
  "v_mfma_f32_16x16x32_bf16 v[72:75], a[216:219], v[172:175], v[72:75]\n\t" \
  "v_and_b32 v236, 15, v233\n\t" \
  "v_lshrrev_b32 v250, 4, v233\n\t" \
  "v_mfma_f32_16x16x32_bf16 v[88:91], a[220:223], v[172:175], v[88:91]\n\t" \
  "v_mul_u32_u24 v236, 80, v236\n\t" \
  "v_lshlrev_b32 v237, 4, v250\n\t" \
  "v_mfma_f32_16x16x32_bf16 v[88:91], a[212:215], v[168:171], v[88:91]\n\t" \
  "v_add_u32 v237, s76, v237\n\t" \
  "v_add_u32 v236, v237, v236\n\t" \
  "v_mfma_f32_16x16x32_bf16 v[88:91], a[204:207], v[164:167], v[88:91]\n\t" \
  "s_add_u32 s78, s70, 4096\n\t" \
  "ds_read_b128 v[156:159], v242 offset:35840\n\t" \
  "s_add_u32 s79, s70, 16384\n\t" \
  "v_mfma_f32_16x16x32_bf16 v[88:91], a[196:199], v[160:163], v[88:91]\n\t" \
  "s_waitcnt lgkmcnt(4)\n\t" \
  "v_cvt_pk_bf16_f32 v8, v72, v73\n\t" \
  "v_cvt_pk_bf16_f32 v9, v74, v75\n\t" \
  "v_mfma_f32_16x16x32_bf16 v[76:79], a[192:195], v[128:131], v[76:79]\n\t" \
  "buffer_store_dwordx4 v[4:7], v242, s[16:19], s69 offen offset:1024 sc1\n\t" \
  "ds_read_b128 v[160:163], v242 offset:36864\n\t" \
  "ds_read_b128 v[164:167], v242 offset:37888\n\t" \
  "ds_read_b128 v[168:171], v242 offset:38912\n\t" \
  "v_mfma_f32_16x16x32_bf16 v[76:79], a[200:203], v[132:135], v[76:79]\n\t" \
  "v_cvt_pk_bf16_f32 v10, v88, v89\n\t" \
  "v_cvt_pk_bf16_f32 v11, v90, v91\n\t" \
  "v_mfma_f32_16x16x32_bf16 v[76:79], a[208:211], v[136:139], v[76:79]\n\t" \
  "ds_read_b128 v[172:175], v242 offset:39936\n\t" \
  "v_mfma_f32_16x16x32_bf16 v[76:79], a[216:219], v[140:143], v[76:79]\n\t" \
  "v_mfma_f32_16x16x32_bf16 v[92:95], a[220:223], v[140:143], v[92:95]\n\t" \
  "v_mfma_f32_16x16x32_bf16 v[92:95], a[212:215], v[136:139], v[92:95]\n\t" \
  "v_mfma_f32_16x16x32_bf16 v[92:95], a[204:207], v[132:135], v[92:95]\n\t" \
  "s_nop 2\n\t" \
  "v_mfma_f32_16x16x32_bf16 v[92:95], a[196:199], v[128:131], v[92:95]\n\t" \
  "s_waitcnt lgkmcnt(4)\n\t" \
  "v_cvt_pk_bf16_f32 v12, v76, v77\n\t" \
  "v_cvt_pk_bf16_f32 v13, v78, v79\n\t" \
  STP("s_memrealtime s[94:95]\n\t") \
  STP("s_memtime s[92:93]\n\t") \
  "v_mfma_f32_16x16x32_bf16 v[96:99], a[128:131], v[144:147], v[96:99]\n\t" \
  "buffer_store_dwordx4 v[8:11], v242, s[16:19], s69 offen offset:2048 sc1\n\t" \
  "ds_read_b128 v[128:131], v242 offset:40960\n\t" \
  "ds_read_b128 v[132:135], v242 offset:41984\n\t" \
  "ds_read_b64 v[208:209], v238 offset:0\n\t" \
  "v_mfma_f32_16x16x32_bf16 v[96:99], a[136:139], v[148:151], v[96:99]\n\t" \
  "v_cvt_pk_bf16_f32 v14, v92, v93\n\t" \
  "v_cvt_pk_bf16_f32 v15, v94, v95\n\t" \
  "v_mfma_f32_16x16x32_bf16 v[96:99], a[144:147], v[152:155], v[96:99]\n\t" \
  "ds_read_b64 v[210:211], v238 offset:4096\n\t" \
  "v_mfma_f32_16x16x32_bf16 v[96:99], a[152:155], v[156:159], v[96:99]\n\t" \
  "ds_read_b128 v[136:139], v242 offset:43008\n\t" \
  "v_mfma_f32_16x16x32_bf16 v[112:115], a[156:159], v[156:159], v[112:115]\n\t" \
  "ds_read_b64 v[212:213], v238 offset:8192\n\t" \
  "v_mfma_f32_16x16x32_bf16 v[112:115], a[148:151], v[152:155], v[112:115]\n\t" \
  "ds_read_b128 v[140:143], v242 offset:44032\n\t" \
  "v_mfma_f32_16x16x32_bf16 v[112:115], a[140:143], v[148:151], v[112:115]\n\t" \
  "ds_read_b64 v[214:215], v238 offset:12288\n\t" \
  "v_mfma_f32_16x16x32_bf16 v[112:115], a[132:135], v[144:147], v[112:115]\n\t" \
  "s_waitcnt lgkmcnt(8)\n\t" \
  "ds_read_b64 v[216:217], v239 offset:0\n\t" \
  "v_mfma_f32_16x16x32_bf16 v[100:103], a[128:131], v[160:163], v[100:103]\n\t" \
  "buffer_store_dwordx4 v[12:15], v242, s[16:19], s69 offen offset:3072 sc1\n\t" \
  "ds_read_b128 v[144:147], v242 offset:45056\n\t" \
  "ds_read_b128 v[148:151], v242 offset:46080\n\t" \
  "ds_read_b128 v[152:155], v242 offset:47104\n\t" \
  "v_mfma_f32_16x16x32_bf16 v[100:103], a[136:139], v[164:167], v[100:103]\n\t" \
  "ds_read_b128 v[156:159], v242 offset:48128\n\t" \
  "v_mfma_f32_16x16x32_bf16 v[100:103], a[144:147], v[168:171], v[100:103]\n\t" \
  "ds_read_b64 v[218:219], v239 offset:4096\n\t" \
  "v_mfma_f32_16x16x32_bf16 v[100:103], a[152:155], v[172:175], v[100:103]\n\t" \
  "ds_read_b64 v[220:221], v239 offset:8192\n\t" \
  "v_mfma_f32_16x16x32_bf16 v[116:119], a[156:159], v[172:175], v[116:119]\n\t" \
  STP("v_writelane_b32 v255, s94, 36\n\t") \
  STP("v_writelane_b32 v255, s92, 37\n\t") \
  "ds_read_b64 v[222:223], v239 offset:12288\n\t" \
  "v_mfma_f32_16x16x32_bf16 v[116:119], a[148:151], v[168:171], v[116:119]\n\t" \
  "s_waitcnt lgkmcnt(0)\n\t" \
  "v_lshlrev_b32 v0, 16, v208\n\t" \
  "v_lshl_add_u32 v224, s3, 10, v242\n\t" \
  "v_mfma_f32_16x16x32_bf16 v[116:119], a[140:143], v[164:167], v[116:119]\n\t" \
  "s_waitcnt vmcnt(17)\n\t" \
  "v_and_b32 v225, 31, v233\n\t" \
  "v_lshlrev_b32 v245, 2, v225\n\t" \
  "v_mfma_f32_16x16x32_bf16 v[116:119], a[132:135], v[160:163], v[116:119]\n\t" \
  "s_barrier\n\t" \
  "v_mfma_f32_16x16x32_bf16 v[104:107], a[128:131], v[128:131], v[104:107]\n\t" \
  "ds_read_b128 v[160:163], v230 offset:0\n\t" \
  "v_lshlrev_b32 v225, 3, v225\n\t" \
  "v_mfma_f32_16x16x32_bf16 v[104:107], a[136:139], v[132:135], v[104:107]\n\t" \
  "v_and_b32 v226, 0xffffffe0, v224\n\t" \
  "v_lshl_add_u32 v227, s8, 6, v233\n\t" \
  "v_mfma_f32_16x16x32_bf16 v[104:107], a[144:147], v[136:139], v[104:107]\n\t" \
  "ds_read_b128 v[164:167], v230 offset:1024\n\t" \
  "v_sub_u32 v227, 0x7ff, v227\n\t" \
  "v_mfma_f32_16x16x32_bf16 v[104:107], a[152:155], v[140:143], v[104:107]\n\t" \
  "v_and_b32 v228, 0x30, v233\n\t" \
  "v_and_b32 v239, 15, v233\n\t" \
  "v_mfma_f32_16x16x32_bf16 v[120:123], a[156:159], v[140:143], v[120:123]\n\t" \
  "s_add_u32 s97, s64, 917504\n\t" \
  "ds_read_b128 v[168:171], v230 offset:2048\n\t" \
  "v_and_b32 v241, 16, v233\n\t" \
  "v_mfma_f32_16x16x32_bf16 v[120:123], a[148:151], v[136:139], v[120:123]\n\t" \
  "s_and_b32 s97, s97, 0xe0000\n\t" \
  "v_lshlrev_b32 v231, 11, v239\n\t" \
  "v_lshl_add_u32 v231, v241, 1, v231\n\t" \
  "v_mfma_f32_16x16x32_bf16 v[120:123], a[140:143], v[132:135], v[120:123]\n\t" \
  "s_add_u32 s97, s97, s63\n\t" \
  "ds_read_b128 v[172:175], v230 offset:3072\n\t" \
  "v_lshrrev_b32 v239, 5, v233\n\t" \
  "v_mfma_f32_16x16x32_bf16 v[120:123], a[132:135], v[128:131], v[120:123]\n\t" \
  "s_add_u32 m0, s38, 16384\n\t" \
  "v_lshl_add_u32 v231, v239, 4, v231\n\t" \
  "v_lshl_add_u32 v253, v239, 6, v241\n\t" \
  "v_mfma_f32_16x16x32_bf16 v[108:111], a[128:131], v[144:147], v[108:111]\n\t" \
  "ds_read_b128 v[128:131], v230 offset:4096\n\t" \
  "v_and_b32 v239, 1, v233\n\t" \
  "v_mfma_f32_16x16x32_bf16 v[108:111], a[136:139], v[148:151], v[108:111]\n\t" \
  "v_lshl_add_u32 v253, v239, 5, v253\n\t" \
  "v_lshl_add_u32 v253, s3, 7, v253\n\t" \
  "v_mfma_f32_16x16x32_bf16 v[108:111], a[144:147], v[152:155], v[108:111]\n\t" \
  "buffer_load_dwordx4 v242, s[16:19], s97 offen offset:0 sc1 lds\n\t" \
  "v_mfma_f32_16x16x32_bf16 v[108:111], a[152:155], v[156:159], v[108:111]\n\t" \
  "ds_read_b128 v[132:135], v230 offset:5120\n\t" \
  "v_and_b32 v239, 0x800, v224\n\t" \
  "v_mfma_f32_16x16x32_bf16 v[124:127], a[156:159], v[156:159], v[124:127]\n\t" \
  "ds_read_b128 v[136:139], v230 offset:6144\n\t" \
  "v_lshl_or_b32 v240, v239, 20, v242\n\t" \
  "v_mfma_f32_16x16x32_bf16 v[124:127], a[148:151], v[152:155], v[124:127]\n\t" \
  "v_mov_b32 v238, s82\n\t" \
  "v_writelane_b32 v238, s83, 1\n\t" \
  "v_mfma_f32_16x16x32_bf16 v[124:127], a[140:143], v[148:151], v[124:127]\n\t" \
  "buffer_load_dwordx4 v242, s[16:19], s97 offen offset:1024 sc1 lds\n\t" \
  "v_mfma_f32_16x16x32_bf16 v[124:127], a[132:135], v[144:147], v[124:127]\n\t" \
  "s_waitcnt lgkmcnt(3)\n\t" \
  "ds_read_b128 v[140:143], v230 offset:7168\n\t" \
  "v_perm_b32 v239, v180, v176, s40\n\t" \
  "v_mfma_f32_16x16x32_bf16 v[96:99], a[160:163], v[160:163], v[96:99]\n\t" \
  "ds_read_b128 v[144:147], v230 offset:8192\n\t" \
  "v_perm_b32 v180, v180, v176, s41\n\t" \
  "v_mfma_f32_16x16x32_bf16 v[96:99], a[168:171], v[164:167], v[96:99]\n\t" \
  "buffer_load_dwordx4 v242, s[16:19], s97 offen offset:2048 sc1 lds\n\t" \
  "v_mfma_f32_16x16x32_bf16 v[96:99], a[176:179], v[168:171], v[96:99]\n\t" \
  "ds_read_b128 v[148:151], v230 offset:9216\n\t" \
  "v_mov_b32 v176, v239\n\t" \
  "v_mfma_f32_16x16x32_bf16 v[96:99], a[184:187], v[172:175], v[96:99]\n\t" \
  "v_perm_b32 v239, v181, v177, s40\n\t" \
  "v_perm_b32 v181, v181, v177, s41\n\t" \
  "v_mfma_f32_16x16x32_bf16 v[112:115], a[188:191], v[172:175], v[112:115]\n\t" \
  "ds_read_b128 v[152:155], v230 offset:10240\n\t" \
  "v_mov_b32 v177, v239\n\t" \
  "v_mfma_f32_16x16x32_bf16 v[112:115], a[180:183], v[168:171], v[112:115]\n\t" \
  "buffer_load_dwordx4 v242, s[16:19], s97 offen offset:3072 sc1 lds\n\t" \
  "v_mfma_f32_16x16x32_bf16 v[112:115], a[172:175], v[164:167], v[112:115]\n\t" \
  "ds_read_b128 v[156:159], v230 offset:11264\n\t" \
  "v_perm_b32 v239, v182, v178, s40\n\t" \
  "v_mfma_f32_16x16x32_bf16 v[112:115], a[164:167], v[160:163], v[112:115]\n\t" \
  "s_waitcnt lgkmcnt(4)\n\t" \
  "v_perm_b32 v182, v182, v178, s41\n\t" \
  "v_mov_b32 v178, v239\n\t" \
  "v_mfma_f32_16x16x32_bf16 v[100:103], a[160:163], v[128:131], v[100:103]\n\t" \
  "ds_read_b128 v[160:163], v230 offset:12288\n\t" \
  "v_perm_b32 v239, v183, v179, s40\n\t" \
  "v_mfma_f32_16x16x32_bf16 v[100:103], a[168:171], v[132:135], v[100:103]\n\t" \
  "v_perm_b32 v183, v183, v179, s41\n\t" \
  "v_mov_b32 v179, v239\n\t" \
  "v_mfma_f32_16x16x32_bf16 v[100:103], a[176:179], v[136:139], v[100:103]\n\t" \
  "ds_read_b128 v[164:167], v230 offset:13312\n\t" \
  "v_perm_b32 v239, v188, v184, s40\n\t" \
  "v_mfma_f32_16x16x32_bf16 v[100:103], a[184:187], v[140:143], v[100:103]\n\t" \
  "v_perm_b32 v188, v188, v184, s41\n\t" \
  "v_mov_b32 v184, v239\n\t" \
  "v_mfma_f32_16x16x32_bf16 v[116:119], a[188:191], v[140:143], v[116:119]\n\t" \
  "ds_read_b128 v[168:171], v230 offset:14336\n\t" \
  "v_perm_b32 v239, v189, v185, s40\n\t" \
  "v_mfma_f32_16x16x32_bf16 v[116:119], a[180:183], v[136:139], v[116:119]\n\t" \
  "v_perm_b32 v189, v189, v185, s41\n\t" \
  "v_mov_b32 v185, v239\n\t" \
  "v_mfma_f32_16x16x32_bf16 v[116:119], a[172:175], v[132:135], v[116:119]\n\t" \
  "ds_read_b128 v[172:175], v230 offset:15360\n\t" \
  "v_and_b32 v1, 0xffff0000, v208\n\t" \
  "v_mfma_f32_16x16x32_bf16 v[116:119], a[164:167], v[128:131], v[116:119]\n\t" \
  "s_waitcnt lgkmcnt(4)\n\t" \
  "v_lshlrev_b32 v2, 16, v209\n\t" \
  "v_perm_b32 v239, v190, v186, s40\n\t" \
  "v_mfma_f32_16x16x32_bf16 v[104:107], a[160:163], v[144:147], v[104:107]\n\t" \
  "s_waitcnt vmcnt(9)\n\t" \
  "v_cmp_ne_u32 vcc, s36, v246\n\t" \
  "v_perm_b32 v190, v190, v186, s41\n\t" \
  "v_mfma_f32_16x16x32_bf16 v[104:107], a[168:171], v[148:151], v[104:107]\n\t" \
  "ds_read_b128 v[128:131], v230 offset:32768\n\t" \
  "v_and_b32 v3, 0xffff0000, v209\n\t" \
  "v_mfma_f32_16x16x32_bf16 v[104:107], a[176:179], v[152:155], v[104:107]\n\t" \
  "s_cbranch_vccnz .Lat_s4\n\t" \
  ".Lat_k4:\n\t" \
  "v_mfma_f32_16x16x32_bf16 v[104:107], a[184:187], v[156:159], v[104:107]\n\t" \
  "buffer_load_dwordx4 v[32:35], v242, s[16:19], s70 offen offset:0 sc1\n\t" \
  "v_mfma_f32_16x16x32_bf16 v[120:123], a[188:191], v[156:159], v[120:123]\n\t" \
  "ds_read_b128 v[132:135], v230 offset:33792\n\t" \
  "v_lshlrev_b32 v4, 16, v210\n\t" \
  "v_mfma_f32_16x16x32_bf16 v[120:123], a[180:183], v[152:155], v[120:123]\n\t" \
  "ds_read_b128 v[136:139], v230 offset:34816\n\t" \
  "v_mov_b32 v186, v239\n\t" \
  "v_mfma_f32_16x16x32_bf16 v[120:123], a[172:175], v[148:151], v[120:123]\n\t" \
  "ds_read_b128 v[140:143], v230 offset:35840\n\t" \
  "v_perm_b32 v239, v191, v187, s40\n\t" \
  "v_mfma_f32_16x16x32_bf16 v[120:123], a[164:167], v[144:147], v[120:123]\n\t" \
  "s_waitcnt lgkmcnt(4)\n\t" \
  "v_and_b32 v5, 0xffff0000, v210\n\t" \
  "v_perm_b32 v191, v191, v187, s41\n\t" \
  "v_mfma_f32_16x16x32_bf16 v[108:111], a[160:163], v[160:163], v[108:111]\n\t" \
  "ds_read_b128 v[144:147], v230 offset:36864\n\t" \
  "v_lshlrev_b32 v6, 16, v211\n\t" \
  "v_mfma_f32_16x16x32_bf16 v[108:111], a[168:171], v[164:167], v[108:111]\n\t" \
  "buffer_load_dwordx4 v[36:39], v242, s[16:19], s70 offen offset:1024 sc1\n\t" \
  "v_mfma_f32_16x16x32_bf16 v[108:111], a[176:179], v[168:171], v[108:111]\n\t" \
  "ds_read_b128 v[148:151], v230 offset:37888\n\t" \
  "v_and_b32 v7, 0xffff0000, v211\n\t" \
  "v_mfma_f32_16x16x32_bf16 v[108:111], a[184:187], v[172:175], v[108:111]\n\t" \
  "v_mov_b32 v187, v239\n\t" \
  "v_perm_b32 v239, v196, v192, s40\n\t" \
  "v_mfma_f32_16x16x32_bf16 v[124:127], a[188:191], v[172:175], v[124:127]\n\t" \
  "ds_read_b128 v[152:155], v230 offset:38912\n\t" \
  "v_lshlrev_b32 v8, 16, v212\n\t" \
  "v_mfma_f32_16x16x32_bf16 v[124:127], a[180:183], v[168:171], v[124:127]\n\t" \
  "v_and_b32 v9, 0xffff0000, v212\n\t" \
  "v_perm_b32 v196, v196, v192, s41\n\t" \
  "v_mfma_f32_16x16x32_bf16 v[124:127], a[172:175], v[164:167], v[124:127]\n\t" \
  "ds_read_b128 v[156:159], v230 offset:39936\n\t" \
  "v_mov_b32 v192, v239\n\t" \
  "v_mfma_f32_16x16x32_bf16 v[124:127], a[164:167], v[160:163], v[124:127]\n\t" \
  "s_waitcnt lgkmcnt(4)\n\t" \
  "buffer_load_dwordx4 v[40:43], v242, s[16:19], s70 offen offset:2048 sc1\n\t" \
  "v_mfma_f32_16x16x32_bf16 v[96:99], a[0:3], v[128:131], v[96:99]\n\t" \
  "ds_read_b128 v[160:163], v230 offset:40960\n\t" \
  "v_lshlrev_b32 v10, 16, v213\n\t" \
  "v_mfma_f32_16x16x32_bf16 v[96:99], a[8:11], v[132:135], v[96:99]\n\t" \
  "v_and_b32 v11, 0xffff0000, v213\n\t" \
  "v_perm_b32 v239, v197, v193, s40\n\t" \
  "v_mfma_f32_16x16x32_bf16 v[96:99], a[16:19], v[136:139], v[96:99]\n\t" \
  "ds_read_b128 v[164:167], v230 offset:41984\n\t" \
  "v_perm_b32 v197, v197, v193, s41\n\t" \
  "v_mfma_f32_16x16x32_bf16 v[96:99], a[24:27], v[140:143], v[96:99]\n\t" \
  "v_lshlrev_b32 v12, 16, v214\n\t" \
  "v_mov_b32 v193, v239\n\t" \
  "v_mfma_f32_16x16x32_bf16 v[112:115], a[28:31], v[140:143], v[112:115]\n\t" \
  "s_waitcnt vmcnt(7)\n\t" \
  "buffer_store_dword v243, v244, s[16:19], s66 offen offset:32 sc1\n\t" \
  "ds_read_b128 v[168:171], v230 offset:43008\n\t" \
  "v_mfma_f32_16x16x32_bf16 v[112:115], a[20:23], v[136:139], v[112:115]\n\t" \
  "v_and_b32 v13, 0xffff0000, v214\n\t" \
  "v_perm_b32 v239, v198, v194, s40\n\t" \
  "v_mfma_f32_16x16x32_bf16 v[112:115], a[12:15], v[132:135], v[112:115]\n\t" \
  "buffer_load_dwordx4 v[44:47], v242, s[16:19], s70 offen offset:3072 sc1\n\t" \
  "v_mfma_f32_16x16x32_bf16 v[112:115], a[4:7], v[128:131], v[112:115]\n\t" \
  "s_waitcnt lgkmcnt(3)\n\t" \
  "ds_read_b128 v[172:175], v230 offset:44032\n\t" \
  "v_lshlrev_b32 v14, 16, v215\n\t" \
  "v_mfma_f32_16x16x32_bf16 v[100:103], a[0:3], v[144:147], v[100:103]\n\t" \
  "ds_read_b128 v[128:131], v230 offset:45056\n\t" \
  "v_perm_b32 v198, v198, v194, s41\n\t" \
  "v_mfma_f32_16x16x32_bf16 v[100:103], a[8:11], v[148:151], v[100:103]\n\t" \
  "v_and_b32 v15, 0xffff0000, v215\n\t" \
  "v_mov_b32 v194, v239\n\t" \
  "v_mfma_f32_16x16x32_bf16 v[100:103], a[16:19], v[152:155], v[100:103]\n\t" \
  "ds_read_b128 v[132:135], v230 offset:46080\n\t" \
  "v_lshlrev_b32 v16, 16, v216\n\t" \
  "v_mfma_f32_16x16x32_bf16 v[100:103], a[24:27], v[156:159], v[100:103]\n\t" \
  "buffer_load_dwordx4 v[48:51], v242, s[16:19], s78 offen offset:0 sc1\n\t" \
  "v_mfma_f32_16x16x32_bf16 v[116:119], a[28:31], v[156:159], v[116:119]\n\t" \
  "ds_read_b128 v[136:139], v230 offset:47104\n\t" \
  "v_perm_b32 v239, v199, v195, s40\n\t" \
  "v_mfma_f32_16x16x32_bf16 v[116:119], a[20:23], v[152:155], v[116:119]\n\t" \
  "v_and_b32 v17, 0xffff0000, v216\n\t" \
  "v_lshlrev_b32 v18, 16, v217\n\t" \
  "v_mfma_f32_16x16x32_bf16 v[116:119], a[12:15], v[148:151], v[116:119]\n\t" \
  "s_waitcnt lgkmcnt(3)\n\t" \
  "ds_read_b128 v[140:143], v230 offset:48128\n\t" \
  "v_perm_b32 v199, v199, v195, s41\n\t" \
  "v_mfma_f32_16x16x32_bf16 v[116:119], a[4:7], v[144:147], v[116:119]\n\t" \
  "s_barrier\n\t" \
  "v_mfma_f32_16x16x32_bf16 v[104:107], a[0:3], v[160:163], v[104:107]\n\t" \
  "ds_read_b128 v[144:147], v230 offset:49152\n\t" \
  "v_mov_b32 v195, v239\n\t" \
  "v_mfma_f32_16x16x32_bf16 v[104:107], a[8:11], v[164:167], v[104:107]\n\t" \
  "buffer_load_dwordx4 v[52:55], v242, s[16:19], s78 offen offset:1024 sc1\n\t" \
  "v_mfma_f32_16x16x32_bf16 v[104:107], a[16:19], v[168:171], v[104:107]\n\t" \
  "ds_read_b128 v[148:151], v230 offset:50176\n\t" \
  "v_and_b32 v19, 0xffff0000, v217\n\t" \
  "v_mfma_f32_16x16x32_bf16 v[104:107], a[24:27], v[172:175], v[104:107]\n\t" \
  "v_lshlrev_b32 v20, 16, v218\n\t" \
  "v_perm_b32 v239, v204, v200, s40\n\t" \
  "v_mfma_f32_16x16x32_bf16 v[120:123], a[28:31], v[172:175], v[120:123]\n\t" \
  "ds_read_b128 v[152:155], v230 offset:51200\n\t" \
  "v_and_b32 v21, 0xffff0000, v218\n\t" \
  "v_mfma_f32_16x16x32_bf16 v[120:123], a[20:23], v[168:171], v[120:123]\n\t" \
  "v_lshlrev_b32 v22, 16, v219\n\t" \
  "v_perm_b32 v204, v204, v200, s41\n\t" \
  "v_mfma_f32_16x16x32_bf16 v[120:123], a[12:15], v[164:167], v[120:123]\n\t" \
  "ds_read_b128 v[156:159], v230 offset:52224\n\t" \
  "v_mov_b32 v200, v239\n\t" \
  "v_mfma_f32_16x16x32_bf16 v[120:123], a[4:7], v[160:163], v[120:123]\n\t" \
  "s_waitcnt lgkmcnt(4)\n\t" \
  "buffer_load_dwordx4 v[56:59], v242, s[16:19], s78 offen offset:2048 sc1\n\t" \
  "v_mfma_f32_16x16x32_bf16 v[108:111], a[0:3], v[128:131], v[108:111]\n\t" \
  "ds_read_b128 v[160:163], v230 offset:53248\n\t" \
  "v_and_b32 v23, 0xffff0000, v219\n\t" \
  "v_mfma_f32_16x16x32_bf16 v[108:111], a[8:11], v[132:135], v[108:111]\n\t" \
  "v_lshlrev_b32 v24, 16, v220\n\t" \
  "v_perm_b32 v239, v205, v201, s40\n\t" \
  "v_mfma_f32_16x16x32_bf16 v[108:111], a[16:19], v[136:139], v[108:111]\n\t" \
  "ds_read_b128 v[164:167], v230 offset:54272\n\t" \
  "v_perm_b32 v205, v205, v201, s41\n\t" \
  "v_mfma_f32_16x16x32_bf16 v[108:111], a[24:27], v[140:143], v[108:111]\n\t" \
  "v_and_b32 v25, 0xffff0000, v220\n\t" \
  "v_mov_b32 v201, v239\n\t" \
  "v_mfma_f32_16x16x32_bf16 v[124:127], a[28:31], v[140:143], v[124:127]\n\t" \
  "ds_read_b128 v[168:171], v230 offset:55296\n\t" \
  "v_lshlrev_b32 v26, 16, v221\n\t" \
  "v_mfma_f32_16x16x32_bf16 v[124:127], a[20:23], v[136:139], v[124:127]\n\t" \
  "buffer_load_dwordx4 v[60:63], v242, s[16:19], s78 offen offset:3072 sc1\n\t" \
  "v_mfma_f32_16x16x32_bf16 v[124:127], a[12:15], v[132:135], v[124:127]\n\t" \
  "ds_read_b128 v[172:175], v230 offset:56320\n\t" \
  "v_accvgpr_write_b32 a128, 0\n\t" \
  "v_mfma_f32_16x16x32_bf16 v[124:127], a[4:7], v[128:131], v[124:127]\n\t" \
  "s_waitcnt lgkmcnt(4)\n\t" \
  "v_accvgpr_write_b32 a129, 0\n\t" \
  "v_accvgpr_write_b32 a130, v248\n\t" \
  "v_mfma_f32_16x16x32_bf16 v[96:99], a[32:35], v[144:147], v[96:99]\n\t" \
  "ds_read_b128 v[128:131], v230 offset:57344\n\t" \
  "v_accvgpr_write_b32 a131, v249\n\t" \
  "v_mfma_f32_16x16x32_bf16 v[96:99], a[40:43], v[148:151], v[96:99]\n\t" \
  "v_accvgpr_write_b32 a132, 0\n\t" \
  "v_accvgpr_write_b32 a133, 0\n\t" \
  "v_mfma_f32_16x16x32_bf16 v[96:99], a[48:51], v[152:155], v[96:99]\n\t" \
  "ds_read_b128 v[132:135], v230 offset:58368\n\t" \
  "v_perm_b32 v239, v206, v202, s40\n\t" \
  "v_mfma_f32_16x16x32_bf16 v[96:99], a[56:59], v[156:159], v[96:99]\n\t" \
  "ds_read_b128 a[176:179], v236 offset:960\n\t" \
  "v_and_b32 v27, 0xffff0000, v221\n\t" \
  "v_mfma_f32_16x16x32_bf16 v[112:115], a[60:63], v[156:159], v[112:115]\n\t" \
  "ds_read_b128 v[136:139], v230 offset:59392\n\t" \
  "v_perm_b32 v206, v206, v202, s41\n\t" \
  "v_mfma_f32_16x16x32_bf16 v[112:115], a[52:55], v[152:155], v[112:115]\n\t" \
  "ds_read_b128 a[180:183], v236 offset:640\n\t" \
  "v_lshlrev_b32 v28, 16, v222\n\t" \
  "v_mfma_f32_16x16x32_bf16 v[112:115], a[44:47], v[148:151], v[112:115]\n\t" \
  "ds_read_b128 v[140:143], v230 offset:60416\n\t" \
  "v_mov_b32 v202, v239\n\t" \
  "v_mfma_f32_16x16x32_bf16 v[112:115], a[36:39], v[144:147], v[112:115]\n\t" \
  "s_waitcnt lgkmcnt(6)\n\t" \
  "ds_read_b128 a[184:187], v236 offset:320\n\t" \
  "v_and_b32 v29, 0xffff0000, v222\n\t" \
  "v_mfma_f32_16x16x32_bf16 v[100:103], a[32:35], v[160:163], v[100:103]\n\t" \
  "ds_read_b128 v[144:147], v230 offset:61440\n\t" \
  "v_perm_b32 v239, v207, v203, s40\n\t" \
  "v_mfma_f32_16x16x32_bf16 v[100:103], a[40:43], v[164:167], v[100:103]\n\t" \
  "ds_read_b128 a[188:191], v236 offset:0\n\t" \
  "v_lshlrev_b32 v30, 16, v223\n\t" \
  "v_mfma_f32_16x16x32_bf16 v[100:103], a[48:51], v[168:171], v[100:103]\n\t" \
  "ds_read_b128 v[148:151], v230 offset:62464\n\t" \
  "v_perm_b32 v207, v207, v203, s41\n\t" \
  "v_mfma_f32_16x16x32_bf16 v[100:103], a[56:59], v[172:175], v[100:103]\n\t" \
  "v_and_b32 v31, 0xffff0000, v223\n\t" \
  "v_mov_b32 v203, v239\n\t" \
  "v_mfma_f32_16x16x32_bf16 v[116:119], a[60:63], v[172:175], v[116:119]\n\t" \
  "ds_read_b128 v[152:155], v230 offset:63488\n\t" \
  "v_accvgpr_read_b32 v239, a224\n\t" \
  "v_mfma_f32_16x16x32_bf16 v[116:119], a[52:55], v[168:171], v[116:119]\n\t" \
  "v_accvgpr_read_b32 v84, a228\n\t" \
  "v_perm_b32 v80, v84, v239, s40\n\t" \
  "v_mfma_f32_16x16x32_bf16 v[116:119], a[44:47], v[164:167], v[116:119]\n\t" \
  "ds_read_b128 v[156:159], v230 offset:64512\n\t" \
  "v_perm_b32 v84, v84, v239, s41\n\t" \
  "v_mfma_f32_16x16x32_bf16 v[116:119], a[36:39], v[160:163], v[116:119]\n\t" \
  "s_waitcnt lgkmcnt(6)\n\t" \
  "v_accvgpr_read_b32 v239, a225\n\t" \
  "v_accvgpr_read_b32 v85, a229\n\t" \
  "v_mfma_f32_16x16x32_bf16 v[104:107], a[32:35], v[128:131], v[104:107]\n\t" \
  "ds_read_b128 v[160:163], v230 offset:16384\n\t" \
  "v_perm_b32 v81, v85, v239, s40\n\t" \
  "v_mfma_f32_16x16x32_bf16 v[104:107], a[40:43], v[132:135], v[104:107]\n\t" \
  "v_perm_b32 v85, v85, v239, s41\n\t" \
  "v_accvgpr_read_b32 v239, a226\n\t" \
  "v_mfma_f32_16x16x32_bf16 v[104:107], a[48:51], v[136:139], v[104:107]\n\t" \
  "ds_read_b128 v[164:167], v230 offset:17408\n\t" \
  "v_accvgpr_read_b32 v86, a230\n\t" \
  "v_mfma_f32_16x16x32_bf16 v[104:107], a[56:59], v[140:143], v[104:107]\n\t" \
  "v_perm_b32 v82, v86, v239, s40\n\t" \
  "v_perm_b32 v86, v86, v239, s41\n\t" \
  "v_mfma_f32_16x16x32_bf16 v[120:123], a[60:63], v[140:143], v[120:123]\n\t" \
  "buffer_load_dword v246, off, s[16:19], s73 offset:32 sc1\n\t" \
  "v_mfma_f32_16x16x32_bf16 v[120:123], a[52:55], v[136:139], v[120:123]\n\t" \
  "ds_read_b128 v[168:171], v230 offset:18432\n\t" \
  "v_accvgpr_read_b32 v239, a227\n\t" \
  "v_mfma_f32_16x16x32_bf16 v[120:123], a[44:47], v[132:135], v[120:123]\n\t" \
  "ds_read_b128 v[172:175], v230 offset:19456\n\t" \
  "v_accvgpr_read_b32 v87, a231\n\t" \
  "v_mfma_f32_16x16x32_bf16 v[120:123], a[36:39], v[128:131], v[120:123]\n\t" \
  "s_waitcnt lgkmcnt(4)\n\t" \
  "v_perm_b32 v83, v87, v239, s40\n\t" \
  "v_perm_b32 v87, v87, v239, s41\n\t" \
  "v_mfma_f32_16x16x32_bf16 v[108:111], a[32:35], v[144:147], v[108:111]\n\t" \
  "ds_read_b128 v[128:131], v230 offset:20480\n\t" \
  "v_accvgpr_read_b32 v239, a232\n\t" \
  "v_mfma_f32_16x16x32_bf16 v[108:111], a[40:43], v[148:151], v[108:111]\n\t" \
  "v_accvgpr_read_b32 v92, a236\n\t" \
  "v_perm_b32 v88, v92, v239, s40\n\t" \
  "v_mfma_f32_16x16x32_bf16 v[108:111], a[48:51], v[152:155], v[108:111]\n\t" \
  "ds_read_b128 v[132:135], v230 offset:21504\n\t" \
  "v_perm_b32 v92, v92, v239, s41\n\t" \
  "v_mfma_f32_16x16x32_bf16 v[108:111], a[56:59], v[156:159], v[108:111]\n\t" \
  "v_accvgpr_read_b32 v239, a233\n\t" \
  "v_accvgpr_read_b32 v93, a237\n\t" \
  "v_mfma_f32_16x16x32_bf16 v[124:127], a[60:63], v[156:159], v[124:127]\n\t" \
  "ds_read_b128 v[136:139], v230 offset:22528\n\t" \
  "v_perm_b32 v89, v93, v239, s40\n\t" \
  "v_mfma_f32_16x16x32_bf16 v[124:127], a[52:55], v[152:155], v[124:127]\n\t" \
  "v_perm_b32 v93, v93, v239, s41\n\t" \
  "v_accvgpr_read_b32 v239, a234\n\t" \
  "v_mfma_f32_16x16x32_bf16 v[124:127], a[44:47], v[148:151], v[124:127]\n\t" \
  "ds_read_b128 v[140:143], v230 offset:23552\n\t" \
  "v_accvgpr_read_b32 v94, a238\n\t" \
  "v_mfma_f32_16x16x32_bf16 v[124:127], a[36:39], v[144:147], v[124:127]\n\t" \
  "s_waitcnt lgkmcnt(4)\n\t" \
  "v_perm_b32 v90, v94, v239, s40\n\t" \
  "v_perm_b32 v94, v94, v239, s41\n\t" \
  "v_mfma_f32_16x16x32_bf16 v[96:99], a[64:67], v[160:163], v[96:99]\n\t" \
  "ds_read_b128 v[144:147], v230 offset:24576\n\t" \
  "v_accvgpr_read_b32 v239, a235\n\t" \
  "v_mfma_f32_16x16x32_bf16 v[96:99], a[72:75], v[164:167], v[96:99]\n\t" \
  "v_accvgpr_read_b32 v95, a239\n\t" \
  "v_perm_b32 v91, v95, v239, s40\n\t" \
  "v_mfma_f32_16x16x32_bf16 v[96:99], a[80:83], v[168:171], v[96:99]\n\t" \
  "ds_read_b128 v[148:151], v230 offset:25600\n\t" \
  "v_perm_b32 v95, v95, v239, s41\n\t" \
  "v_mfma_f32_16x16x32_bf16 v[96:99], a[88:91], v[172:175], v[96:99]\n\t" \
  "v_accvgpr_read_b32 v239, a240\n\t" \
  "v_accvgpr_read_b32 v212, a244\n\t" \
  "v_mfma_f32_16x16x32_bf16 v[112:115], a[92:95], v[172:175], v[112:115]\n\t" \
  "ds_read_b128 v[152:155], v230 offset:26624\n\t" \
  "v_perm_b32 v208, v212, v239, s40\n\t" \
  "v_mfma_f32_16x16x32_bf16 v[112:115], a[84:87], v[168:171], v[112:115]\n\t" \
  "v_perm_b32 v212, v212, v239, s41\n\t" \
  "v_accvgpr_read_b32 v239, a241\n\t" \
  "v_mfma_f32_16x16x32_bf16 v[112:115], a[76:79], v[164:167], v[112:115]\n\t" \
  "ds_read_b128 v[156:159], v230 offset:27648\n\t" \
  "v_accvgpr_read_b32 v213, a245\n\t" \
  "v_mfma_f32_16x16x32_bf16 v[112:115], a[68:71], v[160:163], v[112:115]\n\t" \
  "s_waitcnt lgkmcnt(4)\n\t" \
  "v_perm_b32 v209, v213, v239, s40\n\t" \
  "v_perm_b32 v213, v213, v239, s41\n\t" \
  "v_mfma_f32_16x16x32_bf16 v[100:103], a[64:67], v[128:131], v[100:103]\n\t" \
  "ds_read_b128 v[160:163], v230 offset:28672\n\t" \
  "v_accvgpr_read_b32 v239, a242\n\t" \
  "v_mfma_f32_16x16x32_bf16 v[100:103], a[72:75], v[132:135], v[100:103]\n\t" \
  "v_accvgpr_read_b32 v214, a246\n\t" \
  "v_perm_b32 v210, v214, v239, s40\n\t" \
  "v_mfma_f32_16x16x32_bf16 v[100:103], a[80:83], v[136:139], v[100:103]\n\t" \
  ".Lhs15:\n\ts_cbranch_execz .Lhs15n\n\t" \
  "ds_read_b128 v[164:167], v230 offset:29696\n\t" \
  "v_perm_b32 v214, v214, v239, s41\n\t" \
  "v_mfma_f32_16x16x32_bf16 v[100:103], a[88:91], v[140:143], v[100:103]\n\t" \
  "v_accvgpr_read_b32 v239, a243\n\t" \
  "v_accvgpr_read_b32 v215, a247\n\t" \
  "v_mfma_f32_16x16x32_bf16 v[116:119], a[92:95], v[140:143], v[116:119]\n\t" \
  "ds_read_b128 v[168:171], v230 offset:30720\n\t" \
  "v_perm_b32 v211, v215, v239, s40\n\t" \
  "v_mfma_f32_16x16x32_bf16 v[116:119], a[84:87], v[136:139], v[116:119]\n\t" \
  "v_perm_b32 v215, v215, v239, s41\n\t" \
  "v_accvgpr_read_b32 v239, a248\n\t" \
  "v_mfma_f32_16x16x32_bf16 v[116:119], a[76:79], v[132:135], v[116:119]\n\t" \
  "s_waitcnt vmcnt(10) lgkmcnt(3)\n\t" \
  "ds_read_b128 v[172:175], v230 offset:31744\n\t" \
  "v_accvgpr_read_b32 v220, a252\n\t" \
  "v_mfma_f32_16x16x32_bf16 v[116:119], a[68:71], v[128:131], v[116:119]\n\t" \
  "s_barrier\n\t" \
  "v_mfma_f32_16x16x32_bf16 v[104:107], a[64:67], v[144:147], v[104:107]\n\t" \
  "ds_read_b128 v[128:131], v232 offset:0\n\t" \
  "v_perm_b32 v216, v220, v239, s40\n\t" \
  "v_mfma_f32_16x16x32_bf16 v[104:107], a[72:75], v[148:151], v[104:107]\n\t" \
  "v_perm_b32 v220, v220, v239, s41\n\t" \
  "v_accvgpr_read_b32 v239, a249\n\t" \
  "v_mfma_f32_16x16x32_bf16 v[104:107], a[80:83], v[152:155], v[104:107]\n\t" \
  "ds_read_b128 v[132:135], v232 offset:1024\n\t" \
  "v_accvgpr_read_b32 v221, a253\n\t" \
  "v_mfma_f32_16x16x32_bf16 v[104:107], a[88:91], v[156:159], v[104:107]\n\t" \
  "buffer_load_dword v251, off, s[16:19], s73 offset:32 sc1\n\t" \
  "v_mfma_f32_16x16x32_bf16 v[120:123], a[92:95], v[156:159], v[120:123]\n\t" \
  "ds_read_b128 v[136:139], v232 offset:2048\n\t" \
  "v_perm_b32 v217, v221, v239, s40\n\t" \
  "v_mfma_f32_16x16x32_bf16 v[120:123], a[84:87], v[152:155], v[120:123]\n\t" \
  "v_perm_b32 v221, v221, v239, s41\n\t" \
  "v_accvgpr_read_b32 v239, a250\n\t" \
  "v_mfma_f32_16x16x32_bf16 v[120:123], a[76:79], v[148:151], v[120:123]\n\t" \
  "ds_read_b128 v[140:143], v232 offset:3072\n\t" \
  "v_accvgpr_read_b32 v222, a254\n\t" \
  "v_mfma_f32_16x16x32_bf16 v[120:123], a[68:71], v[144:147], v[120:123]\n\t" \
  "s_waitcnt lgkmcnt(4)\n\t" \
  "v_perm_b32 v218, v222, v239, s40\n\t" \
  "v_perm_b32 v222, v222, v239, s41\n\t" \
  "v_mfma_f32_16x16x32_bf16 v[108:111], a[64:67], v[160:163], v[108:111]\n\t" \
  "s_waitcnt vmcnt(1)\n\t" \
  "v_cmp_ne_u32 vcc, s36, v246\n\t" \
  "v_accvgpr_read_b32 v239, a251\n\t" \
  "v_mfma_f32_16x16x32_bf16 v[108:111], a[72:75], v[164:167], v[108:111]\n\t" \
  "ds_read_b128 v[144:147], v232 offset:4096\n\t" \
  "v_accvgpr_read_b32 v223, a255\n\t" \
  "v_mfma_f32_16x16x32_bf16 v[108:111], a[80:83], v[168:171], v[108:111]\n\t" \
  "s_mov_b32 s88, vcc_lo\n\t" \
  "s_cbranch_vccnz .Lat_n2\n\t" \
  "buffer_load_dwordx4 v[64:67], v242, s[16:19], s79 offen offset:0 sc1\n\t" \
  "buffer_load_dwordx4 v[68:71], v242, s[16:19], s79 offen offset:1024 sc1\n\t" \
  "buffer_load_dwordx4 v[72:75], v242, s[16:19], s79 offen offset:2048 sc1\n\t" \
  "buffer_load_dwordx4 v[76:79], v242, s[16:19], s79 offen offset:3072 sc1\n\t" \
  ".Lat_n2:\n\t" \
  "ds_read_b128 v[148:151], v232 offset:5120\n\t" \
  "v_perm_b32 v219, v223, v239, s40\n\t" \
  "v_mfma_f32_16x16x32_bf16 v[108:111], a[88:91], v[172:175], v[108:111]\n\t" \
  "ds_read_b128 v[152:155], v232 offset:6144\n\t" \
  "v_perm_b32 v223, v223, v239, s41\n\t" \
  "v_mfma_f32_16x16x32_bf16 v[124:127], a[92:95], v[172:175], v[124:127]\n\t" \
  "ds_read_b128 v[156:159], v232 offset:7168\n\t" \
  "v_mfma_f32_16x16x32_bf16 v[124:127], a[84:87], v[168:171], v[124:127]\n\t" \
  "v_mfma_f32_16x16x32_bf16 v[124:127], a[76:79], v[164:167], v[124:127]\n\t" \
  "v_mfma_f32_16x16x32_bf16 v[124:127], a[68:71], v[160:163], v[124:127]\n\t" \
  "s_waitcnt lgkmcnt(4)\n\t" \
  "v_mfma_f32_16x16x32_bf16 v[96:99], a[96:99], v[128:131], v[96:99]\n\t" \
  "ds_read_b128 v[160:163], v232 offset:8192\n\t" \
  "v_mfma_f32_16x16x32_bf16 v[96:99], a[104:107], v[132:135], v[96:99]\n\t" \
  "ds_read_b128 v[164:167], v232 offset:9216\n\t" \
  "v_mfma_f32_16x16x32_bf16 v[96:99], a[112:115], v[136:139], v[96:99]\n\t" \
  "ds_read_b128 v[168:171], v232 offset:10240\n\t" \
  "v_mfma_f32_16x16x32_bf16 v[96:99], a[120:123], v[140:143], v[96:99]\n\t" \
  "ds_read_b128 v[172:175], v232 offset:11264\n\t" \
  "v_mfma_f32_16x16x32_bf16 v[112:115], a[124:127], v[140:143], v[112:115]\n\t" \
  "v_mfma_f32_16x16x32_bf16 v[112:115], a[116:119], v[136:139], v[112:115]\n\t" \
  "v_mfma_f32_16x16x32_bf16 v[112:115], a[108:111], v[132:135], v[112:115]\n\t" \
  "v_mfma_f32_16x16x32_bf16 v[112:115], a[100:103], v[128:131], v[112:115]\n\t" \
  "s_waitcnt lgkmcnt(4)\n\t" \
  "v_mfma_f32_16x16x32_bf16 v[100:103], a[96:99], v[144:147], v[100:103]\n\t" \
  "ds_read_b128 v[128:131], v232 offset:12288\n\t" \
  "v_mfma_f32_16x16x32_bf16 v[100:103], a[104:107], v[148:151], v[100:103]\n\t" \
  "ds_read_b128 v[132:135], v232 offset:13312\n\t" \
  "v_mfma_f32_16x16x32_bf16 v[100:103], a[112:115], v[152:155], v[100:103]\n\t" \
  "ds_read_b128 v[136:139], v232 offset:14336\n\t" \
  "v_mfma_f32_16x16x32_bf16 v[100:103], a[120:123], v[156:159], v[100:103]\n\t" \
  "ds_read_b128 v[140:143], v232 offset:15360\n\t" \
  "v_mfma_f32_16x16x32_bf16 v[116:119], a[124:127], v[156:159], v[116:119]\n\t" \
  "v_mfma_f32_16x16x32_bf16 v[116:119], a[116:119], v[152:155], v[116:119]\n\t" \
  "v_mfma_f32_16x16x32_bf16 v[116:119], a[108:111], v[148:151], v[116:119]\n\t" \
  "v_mfma_f32_16x16x32_bf16 v[116:119], a[100:103], v[144:147], v[116:119]\n\t" \
  "s_waitcnt lgkmcnt(4)\n\t" \
  "v_mfma_f32_16x16x32_bf16 v[104:107], a[96:99], v[160:163], v[104:107]\n\t" \
  "ds_read_b128 v[144:147], v242 offset:16384\n\t" \
  "v_mfma_f32_16x16x32_bf16 v[104:107], a[104:107], v[164:167], v[104:107]\n\t" \
  "ds_read_b128 v[148:151], v242 offset:17408\n\t" \
  "v_mfma_f32_16x16x32_bf16 v[104:107], a[112:115], v[168:171], v[104:107]\n\t" \
  "ds_read_b128 v[152:155], v242 offset:18432\n\t" \
  "v_mfma_f32_16x16x32_bf16 v[104:107], a[120:123], v[172:175], v[104:107]\n\t" \
  "ds_read_b128 v[156:159], v242 offset:19456\n\t" \
  "v_mfma_f32_16x16x32_bf16 v[120:123], a[124:127], v[172:175], v[120:123]\n\t" \
  "v_mfma_f32_16x16x32_bf16 v[120:123], a[116:119], v[168:171], v[120:123]\n\t" \
  "v_mfma_f32_16x16x32_bf16 v[120:123], a[108:111], v[164:167], v[120:123]\n\t" \
  "v_mfma_f32_16x16x32_bf16 v[120:123], a[100:103], v[160:163], v[120:123]\n\t" \
  "s_waitcnt lgkmcnt(4)\n\t" \
  "v_mfma_f32_16x16x32_bf16 v[108:111], a[96:99], v[128:131], v[108:111]\n\t" \
  "s_cmp_lg_u32 s88, 0\n\t" \
  "s_cbranch_scc1 .Lat_m3\n\t" \
  ".Lat_n3:\n\t" \
  "v_mfma_f32_16x16x32_bf16 v[108:111], a[104:107], v[132:135], v[108:111]\n\t" \
  "s_add_u32 m0, s38, 32768\n\t" \
  "ds_read_b128 v[160:163], v242 offset:20480\n\t" \
  "s_mov_b32 s96, 0\n\t" \
  "v_mfma_f32_16x16x32_bf16 v[108:111], a[112:115], v[136:139], v[108:111]\n\t" \
  "buffer_load_dwordx4 v252, s[28:31], s96 offen offset:0 nt lds\n\t" \
  "v_mfma_f32_16x16x32_bf16 v[108:111], a[120:123], v[140:143], v[108:111]\n\t" \
  "ds_read_b128 v[164:167], v242 offset:21504\n\t" \
  "s_mov_b32 s96, 3072\n\t" \
  "v_mfma_f32_16x16x32_bf16 v[124:127], a[124:127], v[140:143], v[124:127]\n\t" \
  "buffer_load_dwordx4 v252, s[28:31], s96 offen offset:1024 nt lds\n\t" \
  "v_mfma_f32_16x16x32_bf16 v[124:127], a[116:119], v[136:139], v[124:127]\n\t" \
  "ds_read_b128 v[168:171], v242 offset:22528\n\t" \
  "s_mov_b32 s96, 6144\n\t" \
  "v_mfma_f32_16x16x32_bf16 v[124:127], a[108:111], v[132:135], v[124:127]\n\t" \
  "buffer_load_dwordx4 v252, s[28:31], s96 offen offset:2048 nt lds\n\t" \
  "v_mfma_f32_16x16x32_bf16 v[124:127], a[100:103], v[128:131], v[124:127]\n\t" \
  "ds_read_b128 v[172:175], v242 offset:23552\n\t" \
  "s_mov_b32 s96, 9216\n\t" \
  "v_mfma_f32_16x16x32_bf16 v[96:99], a[130:133], v[32:35], v[96:99]\n\t" \
  "buffer_load_dwordx4 v252, s[28:31], s96 offen offset:3072 nt lds\n\t" \
  "v_mfma_f32_16x16x32_bf16 v[100:103], a[130:133], v[36:39], v[100:103]\n\t" \
  "s_add_u32 m0, s38, 49152\n\t" \
  "s_mov_b32 s96, 16384\n\t" \
  "v_mfma_f32_16x16x32_bf16 v[104:107], a[130:133], v[40:43], v[104:107]\n\t" \
  "buffer_load_dwordx4 v252, s[28:31], s96 offen offset:0 nt lds\n\t" \
  "v_mfma_f32_16x16x32_bf16 v[108:111], a[130:133], v[44:47], v[108:111]\n\t" \
  "s_mov_b32 s96, 19456\n\t" \
  "v_mfma_f32_16x16x32_bf16 v[112:115], a[128:131], v[32:35], v[112:115]\n\t" \
  "buffer_load_dwordx4 v252, s[28:31], s96 offen offset:1024 nt lds\n\t" \
  "v_mfma_f32_16x16x32_bf16 v[116:119], a[128:131], v[36:39], v[116:119]\n\t" \
  "s_mov_b32 s96, 22528\n\t" \
  "v_mfma_f32_16x16x32_bf16 v[120:123], a[128:131], v[40:43], v[120:123]\n\t" \
  "buffer_load_dwordx4 v252, s[28:31], s96 offen offset:2048 nt lds\n\t" \
  "v_mfma_f32_16x16x32_bf16 v[124:127], a[128:131], v[44:47], v[124:127]\n\t" \
  "s_waitcnt lgkmcnt(4)\n\t" \
  "s_mov_b32 s96, 25600\n\t" \
  "v_mfma_f32_16x16x32_bf16 v[96:99], a[192:195], v[144:147], v[96:99]\n\t" \
  "buffer_load_dwordx4 v252, s[28:31], s96 offen offset:3072 nt lds\n\t" \
  "v_mfma_f32_16x16x32_bf16 v[96:99], a[200:203], v[148:151], v[96:99]\n\t" \
  "ds_read_b128 v[128:131], v242 offset:24576\n\t" \
  "v_mfma_f32_16x16x32_bf16 v[96:99], a[208:211], v[152:155], v[96:99]\n\t" \
  "ds_read_b128 v[132:135], v242 offset:25600\n\t" \
  "v_mfma_f32_16x16x32_bf16 v[96:99], a[216:219], v[156:159], v[96:99]\n\t" \
  "ds_read_b128 v[136:139], v242 offset:26624\n\t" \
  "v_mfma_f32_16x16x32_bf16 v[112:115], a[220:223], v[156:159], v[112:115]\n\t" \
  "ds_read_b128 v[140:143], v242 offset:27648\n\t" \
  "v_mfma_f32_16x16x32_bf16 v[112:115], a[212:215], v[152:155], v[112:115]\n\t" \
  "v_mfma_f32_16x16x32_bf16 v[112:115], a[204:207], v[148:151], v[112:115]\n\t" \
  "v_mfma_f32_16x16x32_bf16 v[112:115], a[196:199], v[144:147], v[112:115]\n\t" \
  "v_mfma_f32_16x16x32_bf16 v[96:99], a[130:133], v[48:51], v[96:99]\n\t" \
  "v_mfma_f32_16x16x32_bf16 v[100:103], a[130:133], v[52:55], v[100:103]\n\t" \
  "v_mfma_f32_16x16x32_bf16 v[104:107], a[130:133], v[56:59], v[104:107]\n\t" \
  "v_mfma_f32_16x16x32_bf16 v[108:111], a[130:133], v[60:63], v[108:111]\n\t" \
  "v_mfma_f32_16x16x32_bf16 v[112:115], a[128:131], v[48:51], v[112:115]\n\t" \
  "v_mfma_f32_16x16x32_bf16 v[116:119], a[128:131], v[52:55], v[116:119]\n\t" \
  "v_mfma_f32_16x16x32_bf16 v[120:123], a[128:131], v[56:59], v[120:123]\n\t" \
  "v_mfma_f32_16x16x32_bf16 v[124:127], a[128:131], v[60:63], v[124:127]\n\t" \
  "s_waitcnt lgkmcnt(4)\n\t" \
  "v_mfma_f32_16x16x32_bf16 v[100:103], a[192:195], v[160:163], v[100:103]\n\t" \
  "ds_read_b128 v[144:147], v242 offset:28672\n\t" \
  "v_mfma_f32_16x16x32_bf16 v[100:103], a[200:203], v[164:167], v[100:103]\n\t" \
  "ds_read_b128 v[148:151], v242 offset:29696\n\t" \
  "v_mfma_f32_16x16x32_bf16 v[100:103], a[208:211], v[168:171], v[100:103]\n\t" \
  "ds_read_b128 v[152:155], v242 offset:30720\n\t" \
  "v_mfma_f32_16x16x32_bf16 v[100:103], a[216:219], v[172:175], v[100:103]\n\t" \
  "ds_read_b128 v[156:159], v242 offset:31744\n\t" \
  "v_mfma_f32_16x16x32_bf16 v[116:119], a[220:223], v[172:175], v[116:119]\n\t" \
  "v_mfma_f32_16x16x32_bf16 v[116:119], a[212:215], v[168:171], v[116:119]\n\t" \
  "v_mfma_f32_16x16x32_bf16 v[116:119], a[204:207], v[164:167], v[116:119]\n\t" \
  "v_mfma_f32_16x16x32_bf16 v[116:119], a[196:199], v[160:163], v[116:119]\n\t" \
  "s_waitcnt lgkmcnt(4)\n\t" \
  "v_mfma_f32_16x16x32_bf16 v[104:107], a[192:195], v[128:131], v[104:107]\n\t" \
  "ds_read_b128 v[160:163], v237 offset:2560\n\t" \
  "v_mfma_f32_16x16x32_bf16 v[104:107], a[200:203], v[132:135], v[104:107]\n\t" \
  "ds_read_b128 v[164:167], v237 offset:2624\n\t" \
  "v_mfma_f32_16x16x32_bf16 v[104:107], a[208:211], v[136:139], v[104:107]\n\t" \
  "v_mfma_f32_16x16x32_bf16 v[104:107], a[216:219], v[140:143], v[104:107]\n\t" \
  "v_mfma_f32_16x16x32_bf16 v[120:123], a[220:223], v[140:143], v[120:123]\n\t" \
  "v_mfma_f32_16x16x32_bf16 v[120:123], a[212:215], v[136:139], v[120:123]\n\t" \
  "v_mfma_f32_16x16x32_bf16 v[120:123], a[204:207], v[132:135], v[120:123]\n\t" \
  "v_mfma_f32_16x16x32_bf16 v[120:123], a[196:199], v[128:131], v[120:123]\n\t" \
  "s_waitcnt lgkmcnt(2)\n\t" \
  "v_mfma_f32_16x16x32_bf16 v[108:111], a[192:195], v[144:147], v[108:111]\n\t" \
  "v_mfma_f32_16x16x32_bf16 v[108:111], a[200:203], v[148:151], v[108:111]\n\t" \
  "v_mfma_f32_16x16x32_bf16 v[108:111], a[208:211], v[152:155], v[108:111]\n\t" \
  "v_mfma_f32_16x16x32_bf16 v[108:111], a[216:219], v[156:159], v[108:111]\n\t" \
  "v_mfma_f32_16x16x32_bf16 v[124:127], a[220:223], v[156:159], v[124:127]\n\t" \
  "v_mfma_f32_16x16x32_bf16 v[124:127], a[212:215], v[152:155], v[124:127]\n\t" \
  "v_mfma_f32_16x16x32_bf16 v[124:127], a[204:207], v[148:151], v[124:127]\n\t" \
  "v_mfma_f32_16x16x32_bf16 v[124:127], a[196:199], v[144:147], v[124:127]\n\t" \
  STP("s_memrealtime s[94:95]\n\t") \
  STP("s_memtime s[92:93]\n\t") \
  "s_cmp_lg_u32 s88, 0\n\t" \
  "s_cbranch_scc1 .Lat_s99\n\t" \
  ".Lat_d2:\n\t" \
  "s_waitcnt vmcnt(8)\n\t" \
  "v_mfma_f32_16x16x32_bf16 v[96:99], a[130:133], v[64:67], v[96:99]\n\t" \
  "v_mfma_f32_16x16x32_bf16 v[100:103], a[130:133], v[68:71], v[100:103]\n\t" \
  "v_mfma_f32_16x16x32_bf16 v[104:107], a[130:133], v[72:75], v[104:107]\n\t" \
  "v_mfma_f32_16x16x32_bf16 v[108:111], a[130:133], v[76:79], v[108:111]\n\t" \
  "v_mfma_f32_16x16x32_bf16 v[112:115], a[128:131], v[64:67], v[112:115]\n\t" \
  "v_mfma_f32_16x16x32_bf16 v[116:119], a[128:131], v[68:71], v[116:119]\n\t" \
  "v_mfma_f32_16x16x32_bf16 v[120:123], a[128:131], v[72:75], v[120:123]\n\t" \
  "v_mfma_f32_16x16x32_bf16 v[124:127], a[128:131], v[76:79], v[124:127]\n\t" \
  "s_waitcnt lgkmcnt(0)\n\t" \
  STP("v_writelane_b32 v255, s94, 38\n\t") \
  STP("v_writelane_b32 v255, s92, 39\n\t") \
  "v_pk_fma_f32 v[0:1], v[160:161], v[96:97], v[0:1]\n\t" \
  "v_pk_fma_f32 v[4:5], v[160:161], v[100:101], v[4:5]\n\t" \
  "v_pk_fma_f32 v[8:9], v[160:161], v[104:105], v[8:9]\n\t" \
  "v_pk_fma_f32 v[12:13], v[160:161], v[108:109], v[12:13]\n\t" \
  "v_pk_fma_f32 v[2:3], v[162:163], v[98:99], v[2:3]\n\t" \
  "v_pk_fma_f32 v[6:7], v[162:163], v[102:103], v[6:7]\n\t" \
  "v_pk_fma_f32 v[10:11], v[162:163], v[106:107], v[10:11]\n\t" \
  "v_pk_fma_f32 v[14:15], v[162:163], v[110:111], v[14:15]\n\t" \
  "v_pk_fma_f32 v[16:17], v[164:165], v[112:113], v[16:17]\n\t" \
  "v_pk_fma_f32 v[20:21], v[164:165], v[116:117], v[20:21]\n\t" \
  "v_pk_fma_f32 v[24:25], v[164:165], v[120:121], v[24:25]\n\t" \
  "v_pk_fma_f32 v[28:29], v[164:165], v[124:125], v[28:29]\n\t" \
  "v_pk_fma_f32 v[18:19], v[166:167], v[114:115], v[18:19]\n\t" \
  "v_pk_fma_f32 v[22:23], v[166:167], v[118:119], v[22:23]\n\t" \
  "v_pk_fma_f32 v[26:27], v[166:167], v[122:123], v[26:27]\n\t" \
  "v_pk_fma_f32 v[30:31], v[166:167], v[126:127], v[30:31]\n\t" \
  "v_cvt_pk_bf16_f32 v128, v0, v1\n\t" \
  "v_cvt_pk_bf16_f32 v132, v4, v5\n\t" \
  "v_cvt_pk_bf16_f32 v136, v8, v9\n\t" \
  "v_cvt_pk_bf16_f32 v140, v12, v13\n\t" \
  "v_cvt_pk_bf16_f32 v129, v2, v3\n\t" \
  "v_cvt_pk_bf16_f32 v133, v6, v7\n\t" \
  "v_cvt_pk_bf16_f32 v137, v10, v11\n\t" \
  "v_cvt_pk_bf16_f32 v141, v14, v15\n\t" \
  "v_cvt_pk_bf16_f32 v130, v16, v17\n\t" \
  "v_cvt_pk_bf16_f32 v134, v20, v21\n\t" \
  "v_cvt_pk_bf16_f32 v138, v24, v25\n\t" \
  "v_cvt_pk_bf16_f32 v142, v28, v29\n\t" \
  "v_cvt_pk_bf16_f32 v131, v18, v19\n\t" \
  "v_cvt_pk_bf16_f32 v135, v22, v23\n\t" \
  "v_cvt_pk_bf16_f32 v139, v26, v27\n\t" \
  "v_cvt_pk_bf16_f32 v143, v30, v31\n\t" \
  "v_dot2_f32_bf16 v64, v128, v128, 0\n\t" \
  "v_dot2_f32_bf16 v65, v132, v132, 0\n\t" \
  "v_dot2_f32_bf16 v66, v136, v136, 0\n\t" \
  "v_dot2_f32_bf16 v67, v140, v140, 0\n\t" \
  "v_dot2_f32_bf16 v64, v129, v129, v64\n\t" \
  "v_dot2_f32_bf16 v65, v133, v133, v65\n\t" \
  "v_dot2_f32_bf16 v66, v137, v137, v66\n\t" \
  "v_dot2_f32_bf16 v67, v141, v141, v67\n\t" \
  "v_dot2_f32_bf16 v64, v130, v130, v64\n\t" \
  "v_dot2_f32_bf16 v65, v134, v134, v65\n\t" \
  "v_dot2_f32_bf16 v66, v138, v138, v66\n\t" \
  "v_dot2_f32_bf16 v67, v142, v142, v67\n\t" \
  "v_dot2_f32_bf16 v64, v131, v131, v64\n\t" \
  "v_dot2_f32_bf16 v65, v135, v135, v65\n\t" \
  "v_dot2_f32_bf16 v66, v139, v139, v66\n\t" \
  "v_dot2_f32_bf16 v67, v143, v143, v67\n\t" \
  "v_mfma_f32_16x16x32_bf16 v[68:71], a[176:179], v[128:131], 0\n\t" \
  "v_mfma_f32_16x16x32_bf16 v[68:71], a[180:183], v[132:135], v[68:71]\n\t" \
  "v_permlane32_swap_b32 v64, v66\n\t" \
  "v_mfma_f32_16x16x32_bf16 v[68:71], a[184:187], v[136:139], v[68:71]\n\t" \
  "v_permlane32_swap_b32 v65, v67\n\t" \
  "v_mfma_f32_16x16x32_bf16 v[68:71], a[188:191], v[140:143], v[68:71]\n\t" \
  "v_add_f32 v64, v64, v66\n\t" \
  "v_add_f32 v65, v65, v67\n\t" \
  "v_mov_b32 v73, s36\n\t" \
  "v_mov_b32 v75, s36\n\t" \
  "v_permlane16_swap_b32 v64, v65\n\t" \
  "s_mov_b32 s81, 0\n\t" \
  "s_nop 1\n\t" \
  "v_add_f32 v70, v69, v70\n\t" \
  "v_add_f32 v69, v64, v65\n\t" \
  "v_mov_b32 v71, v238\n\t" \
  "ds_write_b128 v224, v[68:71]\n\t" \
  "s_waitcnt lgkmcnt(0)\n\t" \
  "s_barrier\n\t" \
  "ds_read_b128 v[96:99], v242 offset:0\n\t" \
  "ds_read_b128 v[100:103], v242 offset:1024\n\t" \
  "ds_read_b128 v[104:107], v242 offset:2048\n\t" \
  "ds_read_b128 v[108:111], v242 offset:3072\n\t" \
  "s_cmp_eq_u32 s3, 0\n\t" \
  "s_cselect_b64 vcc, -1, 0\n\t" \
  "s_waitcnt lgkmcnt(2)\n\t" \
  "v_pk_add_f32 v[96:97], v[96:97], v[100:101]\n\t" \
  "v_pk_add_f32 v[98:99], v[98:99], v[102:103]\n\t" \
  "s_waitcnt lgkmcnt(0)\n\t" \
  "v_pk_add_f32 v[104:105], v[104:105], v[108:109]\n\t" \
  "v_pk_add_f32 v[106:107], v[106:107], v[110:111]\n\t" \
  "v_pk_add_f32 v[64:65], v[96:97], v[104:105]\n\t" \
  "v_pk_add_f32 v[66:67], v[98:99], v[106:107]\n\t" \
  "v_cndmask_b32 v72, v66, v64, vcc\n\t" \
  "v_cndmask_b32 v74, v67, v65, vcc\n\t" \
  "buffer_store_dwordx4 v[72:75], v240, s[16:19], s10 offen sc1\n\t" \
  STP("s_memrealtime s[94:95]\n\t") \
  STP("s_memtime s[92:93]\n\t") \
  STP("s_waitcnt lgkmcnt(0)\n\t") \
  STP("v_writelane_b32 v255, s94, 40\n\t") \
  STP("v_writelane_b32 v255, s92, 41\n\t") \
  "buffer_load_dwordx4 v[96:99], v242, s[16:19], s11 offen offset:0 sc1\n\t" \
  "buffer_load_dwordx4 v[100:103], v242, s[16:19], s11 offen offset:1024 sc1\n\t" \
  "buffer_load_dwordx4 v[104:107], v242, s[16:19], s12 offen offset:0 sc1\n\t" \
  "buffer_load_dwordx4 v[108:111], v242, s[16:19], s12 offen offset:1024 sc1\n\t" \
  "ds_read_b128 v[144:147], v237 offset:2688\n\t" \
  "ds_read_b128 v[148:151], v237 offset:2752\n\t" \
  "ds_read_b128 v[152:155], v237 offset:2816\n\t" \
  "ds_read_b128 v[156:159], v237 offset:2880\n\t" \
  "ds_read_b128 v[168:171], v237 offset:2944\n\t" \
  "ds_read_b128 v[172:175], v237 offset:3008\n\t" \
  "v_lshlrev_b32 v0, 16, v128\n\t" \
  "v_and_b32 v1, 0xffff0000, v128\n\t" \
  "v_lshlrev_b32 v2, 16, v129\n\t" \
  "v_and_b32 v3, 0xffff0000, v129\n\t" \
  "v_lshlrev_b32 v4, 16, v130\n\t" \
  "v_and_b32 v5, 0xffff0000, v130\n\t" \
  "v_lshlrev_b32 v6, 16, v131\n\t" \
  "v_and_b32 v7, 0xffff0000, v131\n\t" \
  "v_lshlrev_b32 v8, 16, v132\n\t" \
  "v_and_b32 v9, 0xffff0000, v132\n\t" \
  "v_lshlrev_b32 v10, 16, v133\n\t" \
  "v_and_b32 v11, 0xffff0000, v133\n\t" \
  "v_lshlrev_b32 v12, 16, v134\n\t" \
  "v_and_b32 v13, 0xffff0000, v134\n\t" \
  "v_lshlrev_b32 v14, 16, v135\n\t" \
  "v_and_b32 v15, 0xffff0000, v135\n\t" \
  "v_lshlrev_b32 v16, 16, v136\n\t" \
  "v_and_b32 v17, 0xffff0000, v136\n\t" \
  "v_lshlrev_b32 v18, 16, v137\n\t" \
  "v_and_b32 v19, 0xffff0000, v137\n\t" \
  "v_lshlrev_b32 v20, 16, v138\n\t" \
  "v_and_b32 v21, 0xffff0000, v138\n\t" \
  "v_lshlrev_b32 v22, 16, v139\n\t" \
  "v_and_b32 v23, 0xffff0000, v139\n\t" \
  "v_lshlrev_b32 v24, 16, v140\n\t" \
  "v_and_b32 v25, 0xffff0000, v140\n\t" \
  "v_lshlrev_b32 v26, 16, v141\n\t" \
  "v_and_b32 v27, 0xffff0000, v141\n\t" \
  "v_lshlrev_b32 v28, 16, v142\n\t" \
  "v_and_b32 v29, 0xffff0000, v142\n\t" \
  "v_lshlrev_b32 v30, 16, v143\n\t" \
  "v_and_b32 v31, 0xffff0000, v143\n\t" \
  "v_lshrrev_b32 v72, 9, v224\n\t" \
  "v_lshlrev_b32 v246, 14, v72\n\t" \
  "v_bfe_u32 v72, v224, 7, 2\n\t" \
  "v_lshl_add_u32 v246, v72, 11, v246\n\t" \
  "v_and_b32 v72, 0x70, v242\n\t" \
  "v_add_u32 v246, v246, v72\n\t" \
  "s_and_b32 s44, s2, 3\n\t" \
  "s_and_b32 s42, s2, 0x38\n\t" \
  "s_lshl_b32 s42, s42, 6\n\t" \
  "s_lshl_b32 s44, s44, 7\n\t" \
  "s_add_u32 s91, s44, s42\n\t" \
  "s_and_b32 s84, s91, 0x7ff\n\t" \
  "s_add_u32 s84, s84, s9\n\t" \
  "s_add_u32 s84, s84, 1048576\n\t" \
  "s_add_u32 s85, s84, 131072\n\t" \
  "s_add_u32 s86, s84, 262144\n\t" \
  "s_add_u32 s87, s84, 393216\n\t" \
  "s_add_u32 s88, s84, 524288\n\t" \
  "s_add_u32 s89, s84, 655360\n\t" \
  "s_add_u32 s90, s84, 786432\n\t" \
  "s_waitcnt vmcnt(5)\n\t" \
  "ds_read2st64_b64 v[32:35], v247 offset0:64 offset1:65\n\t" \
  "ds_read2st64_b64 v[36:39], v247 offset0:66 offset1:67\n\t" \
  "ds_read2st64_b64 v[40:43], v247 offset0:68 offset1:69\n\t" \
  "ds_read2st64_b64 v[44:47], v247 offset0:70 offset1:71\n\t" \
  "ds_read2st64_b64 v[48:51], v247 offset0:96 offset1:97\n\t" \
  "ds_read2st64_b64 v[52:55], v247 offset0:98 offset1:99\n\t" \
  "ds_read2st64_b64 v[56:59], v247 offset0:100 offset1:101\n\t" \
  "ds_read2st64_b64 v[60:63], v247 offset0:102 offset1:103\n\t" \
  "s_waitcnt lgkmcnt(0)\n\t" \
  "v_perm_b32 v112, v34, v32, s40\n\t" \
  "v_perm_b32 v34, v34, v32, s41\n\t" \
  "v_perm_b32 v113, v35, v33, s40\n\t" \
  "v_perm_b32 v35, v35, v33, s41\n\t" \
  "v_perm_b32 v114, v38, v36, s40\n\t" \
  "v_perm_b32 v38, v38, v36, s41\n\t" \
  "v_perm_b32 v115, v39, v37, s40\n\t" \
  "v_perm_b32 v39, v39, v37, s41\n\t" \
  "v_perm_b32 v116, v42, v40, s40\n\t" \
  "v_perm_b32 v42, v42, v40, s41\n\t" \
  "v_perm_b32 v117, v43, v41, s40\n\t" \
  "v_perm_b32 v43, v43, v41, s41\n\t" \
  "v_perm_b32 v118, v46, v44, s40\n\t" \
  "v_perm_b32 v46, v46, v44, s41\n\t" \
  "v_perm_b32 v119, v47, v45, s40\n\t" \
  "v_perm_b32 v47, v47, v45, s41\n\t" \
  "v_perm_b32 v120, v50, v48, s40\n\t" \
  "v_perm_b32 v50, v50, v48, s41\n\t" \
  "v_perm_b32 v121, v51, v49, s40\n\t" \
  "v_perm_b32 v51, v51, v49, s41\n\t" \
  "v_perm_b32 v122, v54, v52, s40\n\t" \
  "v_perm_b32 v54, v54, v52, s41\n\t" \
  "v_perm_b32 v123, v55, v53, s40\n\t" \
  "v_perm_b32 v55, v55, v53, s41\n\t" \
  "v_perm_b32 v124, v58, v56, s40\n\t" \
  "v_perm_b32 v58, v58, v56, s41\n\t" \
  "v_perm_b32 v125, v59, v57, s40\n\t" \
  "v_perm_b32 v59, v59, v57, s41\n\t" \
  "v_perm_b32 v126, v62, v60, s40\n\t" \
  "v_perm_b32 v62, v62, v60, s41\n\t" \
  "v_perm_b32 v127, v63, v61, s40\n\t" \
  "v_perm_b32 v63, v63, v61, s41\n\t" \
  "s_cmp_lt_u32 s39, 4096\n\t" \
  "s_cselect_b64 s[52:53], -1, 0\n\t" \
  "s_waitcnt vmcnt(0)\n\t" \
  "s_branch .Ltl_w1\n\t" \
  ".Ltl_r1:\n\t" \
  "s_sleep 1\n\t" \
  "buffer_load_dwordx4 v[96:99], v242, s[16:19], s11 offen offset:0 sc1\n\t" \
  "buffer_load_dwordx4 v[100:103], v242, s[16:19], s11 offen offset:1024 sc1\n\t" \
  "buffer_load_dwordx4 v[104:107], v242, s[16:19], s12 offen offset:0 sc1\n\t" \
  "buffer_load_dwordx4 v[108:111], v242, s[16:19], s12 offen offset:1024 sc1\n\t" \
  "s_waitcnt vmcnt(0)\n\t" \
  ".Ltl_w1:\n\t" \
  "v_xor_b32 v72, s36, v97\n\t" \
  "v_bitop3_b32 v72, v72, v99, s36 bitop3:0xf6\n\t" \
  "v_bitop3_b32 v72, v72, v101, s36 bitop3:0xf6\n\t" \
  "v_bitop3_b32 v72, v72, v103, s36 bitop3:0xf6\n\t" \
  "v_bitop3_b32 v72, v72, v105, s36 bitop3:0xf6\n\t" \
  "v_bitop3_b32 v72, v72, v107, s36 bitop3:0xf6\n\t" \
  "v_bitop3_b32 v72, v72, v109, s36 bitop3:0xf6\n\t" \
  "v_bitop3_b32 v72, v72, v111, s36 bitop3:0xf6\n\t" \
  "v_cmp_ne_u32 vcc, 0, v72\n\t" \
  "v_cndmask_b32 v96, v96, v64, s[52:53]\n\t" \
  "v_cndmask_b32 v98, v98, v65, s[52:53]\n\t" \
  "v_cndmask_b32 v100, v100, v66, s[52:53]\n\t" \
  "v_cndmask_b32 v102, v102, v67, s[52:53]\n\t" \
  "v_add_f32 v68, v96, v104\n\t" \
  "v_add_f32 v69, v98, v106\n\t" \
  "v_add_f32 v70, v100, v108\n\t" \
  "v_add_f32 v71, v102, v110\n\t" \
  "s_cbranch_vccz .Ltl_k1\n\t" \
  "s_add_u32 s81, s81, 1\n\t" \
  "s_cmp_lt_u32 s81, " FQ_CAP "\n\t" \
  "s_cbranch_scc1 .Ltl_r1\n\t" \
  "buffer_store_dword v243, off, s[16:19], 0 offset:256 sc1\n\t" \
  ".Ltl_k1:\n\t" \
  "ds_write_b128 v224, v[68:71] offset:4096\n\t" \
  "s_waitcnt lgkmcnt(0)\n\t" \
  "s_barrier\n\t" \
  "ds_read_b128 v[96:99], v242 offset:4096\n\t" \
  "ds_read_b128 v[100:103], v242 offset:5120\n\t" \
  "ds_read_b128 v[104:107], v242 offset:6144\n\t" \
  "ds_read_b128 v[108:111], v242 offset:7168\n\t" \
  "buffer_load_dwordx2 v[248:249], v225, s[16:19], s13 offen sc1\n\t" \
  STP("s_memrealtime s[94:95]\n\t") \
  STP("s_memtime s[92:93]\n\t") \
  STP("s_waitcnt lgkmcnt(0)\n\t") \
  STP("v_writelane_b32 v255, s94, 42\n\t") \
  STP("v_writelane_b32 v255, s92, 43\n\t") \
  "s_waitcnt lgkmcnt(2)\n\t" \
  "v_pk_add_f32 v[96:97], v[96:97], v[100:101]\n\t" \
  "v_pk_add_f32 v[98:99], v[98:99], v[102:103]\n\t" \
  "s_waitcnt lgkmcnt(0)\n\t" \
  "v_pk_add_f32 v[104:105], v[104:105], v[108:109]\n\t" \
  "v_pk_add_f32 v[106:107], v[106:107], v[110:111]\n\t" \
  "v_pk_add_f32 v[64:65], v[96:97], v[104:105]\n\t" \
  "v_pk_add_f32 v[66:67], v[98:99], v[106:107]\n\t" \
  "v_mul_f32 v64, 0x3a800000, v64\n\t" \
  "v_mul_f32 v65, 0x3a800000, v65\n\t" \
  "v_readlane_b32 s42, v67, 0\n\t" \
  "v_readlane_b32 s43, v67, 1\n\t" \
  "v_readlane_b32 s53, v67, 2\n\t" \
  "v_fma_f32 v65, -v64, v64, v65\n\t" \
  "v_max_f32 v65, 0, v65\n\t" \
  "v_add_f32 v65, 0x3727c5ac, v65\n\t" \
  "v_rsq_f32 v68, v65\n\t" \
  "v_fma_f32 v66, -v64, s42, v66\n\t" \
  "v_fma_f32 v66, v68, v66, s43\n\t" \
  "v_cvt_pk_bf16_f32 v66, v66, v66\n\t" \
  "v_ashrrev_i32 v67, 31, v66\n\t" \
  "v_or_b32 v67, 0x80000000, v67\n\t" \
  "v_xor_b32 v66, v66, v67\n\t" \
  "v_and_b32 v66, 0xffff0000, v66\n\t" \
  "v_or_b32 v66, v66, v227\n\t" \
  "s_nop 1\n\t" \
  "v_max_u32_dpp v66, v66, v66 quad_perm:[1,0,3,2] row_mask:0xf bank_mask:0xf\n\t" \
  "v_mul_f32_e64 v70, v68, -v64\n\t" \
  "v_mov_b32 v65, s36\n\t" \
  "v_max_u32_dpp v66, v66, v66 quad_perm:[2,3,0,1] row_mask:0xf bank_mask:0xf\n\t" \
  "v_mov_b32 v97, s36\n\t" \
  "v_mov_b32 v99, s36\n\t" \
  "v_max_u32_dpp v66, v66, v66 row_half_mirror row_mask:0xf bank_mask:0xf\n\t" \
  "v_mov_b32 v101, s36\n\t" \
  "v_mov_b32 v103, s36\n\t" \
  "v_max_u32_dpp v66, v66, v66 row_mirror row_mask:0xf bank_mask:0xf\n\t" \
  "v_mov_b32 v105, s36\n\t" \
  "v_mov_b32 v107, s36\n\t" \
  "v_readlane_b32 s46, v66, 0\n\t" \
  "v_readlane_b32 s42, v66, 16\n\t" \
  "v_readlane_b32 s43, v66, 32\n\t" \
  "v_readlane_b32 s44, v66, 48\n\t" \
  "s_max_u32 s46, s46, s42\n\t" \
  "s_max_u32 s43, s43, s44\n\t" \
  "s_max_u32 s46, s46, s43\n\t" \
  "v_mov_b32 v64, s46\n\t" \
  "buffer_store_dwordx2 v[64:65], v244, s[16:19], s14 offen sc1\n\t" \
  STP("s_memrealtime s[94:95]\n\t") \
  STP("s_memtime s[92:93]\n\t") \
  STP("s_waitcnt lgkmcnt(0)\n\t") \
  STP("v_writelane_b32 v255, s94, 44\n\t") \
  STP("v_writelane_b32 v255, s92, 45\n\t") \
  "s_not_b32 s48, s46\n\t" \
  "s_and_b32 s48, s48, 63\n\t" \
  "s_and_b32 s44, s48, 15\n\t" \
  "s_lshl_b32 s50, 0x10001, s44\n\t" \
  "s_mov_b32 s51, s50\n\t" \
  "v_readlane_b32 s52, v68, s48\n\t" \
  "v_readlane_b32 s54, v70, s48\n\t" \
  "s_lshr_b32 s48, s48, 4\n\t" \
  "s_lshl_b32 s44, s48, 2\n\t" \
  "v_mov_b32 v109, s36\n\t" \
  "v_mov_b32 v111, s36\n\t" \
  "s_set_gpr_idx_on s44, gpr_idx(SRC0)\n\t" \
  "v_mov_b32 v104, v128\n\t" \
  "v_mov_b32 v106, v129\n\t" \
  "v_mov_b32 v108, v130\n\t" \
  "v_mov_b32 v110, v131\n\t" \
  "s_set_gpr_idx_off\n\t" \
  "v_mov_b32 v66, s54\n\t" \
  "v_lshlrev_b32 v160, 16, v104\n\t" \
  "v_and_b32 v161, 0xffff0000, v104\n\t" \
  "v_lshlrev_b32 v162, 16, v106\n\t" \
  "v_and_b32 v163, 0xffff0000, v106\n\t" \
  "v_lshlrev_b32 v164, 16, v108\n\t" \
  "v_and_b32 v165, 0xffff0000, v108\n\t" \
  "v_lshlrev_b32 v166, 16, v110\n\t" \
  "v_and_b32 v167, 0xffff0000, v110\n\t" \
  "v_fma_f32 v160, v160, s52, v66\n\t" \
  "v_fma_f32 v161, v161, s52, v66\n\t" \
  "v_fma_f32 v162, v162, s52, v66\n\t" \
  "v_fma_f32 v163, v163, s52, v66\n\t" \
  "v_fma_f32 v164, v164, s52, v66\n\t" \
  "v_fma_f32 v165, v165, s52, v66\n\t" \
  "v_fma_f32 v166, v166, s52, v66\n\t" \
  "v_fma_f32 v167, v167, s52, v66\n\t" \
  "v_pk_fma_f32 v[160:161], v[160:161], v[152:153], v[144:145]\n\t" \
  "v_pk_fma_f32 v[162:163], v[162:163], v[154:155], v[146:147]\n\t" \
  "v_pk_fma_f32 v[164:165], v[164:165], v[156:157], v[148:149]\n\t" \
  "v_pk_fma_f32 v[166:167], v[166:167], v[158:159], v[150:151]\n\t" \
  "v_cvt_pk_bf16_f32 v96, v160, v161\n\t" \
  "v_cvt_pk_bf16_f32 v98, v162, v163\n\t" \
  "v_cvt_pk_bf16_f32 v100, v164, v165\n\t" \
  "v_cvt_pk_bf16_f32 v102, v166, v167\n\t" \
  "s_waitcnt vmcnt(1)\n\t" \
  "v_cmp_eq_u32 vcc, s36, v249\n\t" \
  "v_mov_b32 v69, v68\n\t" \
  "v_mov_b32 v71, v70\n\t" \
  "v_cndmask_b32 v64, 0, v248, vcc\n\t" \
  "s_nop 1\n\t" \
  "v_max_u32_dpp v64, v64, v64 quad_perm:[1,0,3,2] row_mask:0xf bank_mask:0xf\n\t" \
  "v_permlane32_swap_b32 v68, v69\n\t" \
  "v_permlane32_swap_b32 v70, v71\n\t" \
  "v_max_u32_dpp v64, v64, v64 quad_perm:[2,3,0,1] row_mask:0xf bank_mask:0xf\n\t" \
  "v_mov_b32 v160, v68\n\t" \
  "v_mov_b32 v161, v69\n\t" \
  "v_max_u32_dpp v64, v64, v64 row_half_mirror row_mask:0xf bank_mask:0xf\n\t" \
  "v_mov_b32 v162, v70\n\t" \
  "v_mov_b32 v163, v71\n\t" \
  "v_max_u32_dpp v64, v64, v64 row_mirror row_mask:0xf bank_mask:0xf\n\t" \
  "s_nop 0\n\t" \
  "v_readlane_b32 s45, v64, 0\n\t" \
  "v_readlane_b32 s42, v64, 16\n\t" \
  "s_max_u32 s45, s45, s42\n\t" \
  "s_cmp_ge_u32 s46, s45\n\t" \
  "s_cselect_b32 s49, 1, 0\n\t" \
  "s_max_u32 s45, s45, s46\n\t" \
  "s_not_b32 s42, s45\n\t" \
  "s_bfe_u32 s47, s42, 0x50006\n\t" \
  "s_lshl_b32 s43, s42, 7\n\t" \
  "s_and_b32 s43, s43, 0x3c000\n\t" \
  "s_lshl_b32 s42, s42, 6\n\t" \
  "s_and_b32 s42, s42, 0x1000\n\t" \
  "s_add_u32 s42, s42, s43\n\t" \
  "s_add_u32 s42, s42, s15\n\t" \
  "buffer_load_dwordx2 v[248:249], v225, s[16:19], s13 offen sc1\n\t" \
  "buffer_load_dwordx4 v[72:75], v226, s[16:19], s42 offen offset:0 sc1\n\t" \
  "buffer_load_dwordx4 v[76:79], v226, s[16:19], s42 offen offset:16 sc1\n\t" \
  "s_cmp_eq_u32 s49, 0\n\t" \
  "s_cbranch_scc1 .Ltl_nc\n\t" \
  "s_xor_b32 s43, s20, 8192\n\t" \
  "s_mov_b64 exec, s[50:51]\n\t" \
  "buffer_store_dwordx4 v[96:99], v228, s[16:19], s20 offen offset:0 sc1\n\t" \
  "buffer_store_dwordx4 v[100:103], v228, s[16:19], s20 offen offset:64 sc1\n\t" \
  "buffer_store_dwordx4 v[96:99], v228, s[16:19], s43 offen offset:0 sc1\n\t" \
  "buffer_store_dwordx4 v[100:103], v228, s[16:19], s43 offen offset:64 sc1\n\t" \
  "buffer_store_dwordx4 v[104:107], v228, s[16:19], s21 offen offset:0 sc1\n\t" \
  "buffer_store_dwordx4 v[108:111], v228, s[16:19], s21 offen offset:64 sc1\n\t" \
  "s_mov_b64 exec, -1\n\t" \
  ".Ltl_nc:\n\t" \
  "v_permlane16_swap_b32 v68, v160\n\t" \
  "v_permlane16_swap_b32 v69, v161\n\t" \
  "v_permlane16_swap_b32 v70, v162\n\t" \
  "v_permlane16_swap_b32 v71, v163\n\t" \
  "v_pk_fma_f32 v[96:97], v[0:1], v[68:69], v[70:71] op_sel:[0,0,0] op_sel_hi:[1,0,0]\n\t" \
  "v_pk_fma_f32 v[98:99], v[2:3], v[68:69], v[70:71] op_sel:[0,0,0] op_sel_hi:[1,0,0]\n\t" \
  "v_pk_fma_f32 v[100:101], v[4:5], v[68:69], v[70:71] op_sel:[0,0,0] op_sel_hi:[1,0,0]\n\t" \
  "v_pk_fma_f32 v[102:103], v[6:7], v[68:69], v[70:71] op_sel:[0,0,0] op_sel_hi:[1,0,0]\n\t" \
  "v_pk_fma_f32 v[96:97], v[96:97], v[152:153], v[144:145]\n\t" \
  "v_pk_fma_f32 v[98:99], v[98:99], v[154:155], v[146:147]\n\t" \
  "v_pk_fma_f32 v[100:101], v[100:101], v[156:157], v[148:149]\n\t" \
  "v_pk_fma_f32 v[102:103], v[102:103], v[158:159], v[150:151]\n\t" \
  "v_cvt_pk_bf16_f32 v97, v96, v97\n\t" \
  "v_cvt_pk_bf16_f32 v99, v98, v99\n\t" \
  ".Lhs16:\n\ts_cbranch_execz .Lhs16n\n\t" \
  "v_cvt_pk_bf16_f32 v101, v100, v101\n\t" \
  "v_cvt_pk_bf16_f32 v103, v102, v103\n\t" \
  "v_lshlrev_b32 v96, 16, v97\n\t" \
  "v_and_b32 v97, 0xffff0000, v97\n\t" \
  "v_lshlrev_b32 v98, 16, v99\n\t" \
  "v_and_b32 v99, 0xffff0000, v99\n\t" \
  "v_lshlrev_b32 v100, 16, v101\n\t" \
  "v_and_b32 v101, 0xffff0000, v101\n\t" \
  "v_lshlrev_b32 v102, 16, v103\n\t" \
  "v_and_b32 v103, 0xffff0000, v103\n\t" \
  "v_pk_fma_f32 v[96:97], v[168:169], v[96:97], v[0:1]\n\t" \
  "v_pk_fma_f32 v[98:99], v[170:171], v[98:99], v[2:3]\n\t" \
  "v_pk_fma_f32 v[100:101], v[172:173], v[100:101], v[4:5]\n\t" \
  "v_pk_fma_f32 v[102:103], v[174:175], v[102:103], v[6:7]\n\t" \
  "v_cvt_pk_bf16_f32 v128, v96, v97\n\t" \
  "v_cvt_pk_bf16_f32 v129, v98, v99\n\t" \
  "v_cvt_pk_bf16_f32 v130, v100, v101\n\t" \
  "v_cvt_pk_bf16_f32 v131, v102, v103\n\t" \
  "v_pk_fma_f32 v[104:105], v[8:9], v[160:161], v[162:163] op_sel:[0,0,0] op_sel_hi:[1,0,0]\n\t" \
  "v_pk_fma_f32 v[106:107], v[10:11], v[160:161], v[162:163] op_sel:[0,0,0] op_sel_hi:[1,0,0]\n\t" \
  "v_pk_fma_f32 v[108:109], v[12:13], v[160:161], v[162:163] op_sel:[0,0,0] op_sel_hi:[1,0,0]\n\t" \
  "v_pk_fma_f32 v[110:111], v[14:15], v[160:161], v[162:163] op_sel:[0,0,0] op_sel_hi:[1,0,0]\n\t" \
  "v_pk_fma_f32 v[104:105], v[104:105], v[152:153], v[144:145]\n\t" \
  "v_pk_fma_f32 v[106:107], v[106:107], v[154:155], v[146:147]\n\t" \
  "v_pk_fma_f32 v[108:109], v[108:109], v[156:157], v[148:149]\n\t" \
  "v_pk_fma_f32 v[110:111], v[110:111], v[158:159], v[150:151]\n\t" \
  "v_cvt_pk_bf16_f32 v105, v104, v105\n\t" \
  "v_cvt_pk_bf16_f32 v107, v106, v107\n\t" \
  "v_cvt_pk_bf16_f32 v109, v108, v109\n\t" \
  "v_cvt_pk_bf16_f32 v111, v110, v111\n\t" \
  "v_lshlrev_b32 v104, 16, v105\n\t" \
  "v_and_b32 v105, 0xffff0000, v105\n\t" \
  "v_lshlrev_b32 v106, 16, v107\n\t" \
  "v_and_b32 v107, 0xffff0000, v107\n\t" \
  "v_lshlrev_b32 v108, 16, v109\n\t" \
  "v_and_b32 v109, 0xffff0000, v109\n\t" \
  "v_lshlrev_b32 v110, 16, v111\n\t" \
  "v_and_b32 v111, 0xffff0000, v111\n\t" \
  "v_pk_fma_f32 v[104:105], v[168:169], v[104:105], v[8:9]\n\t" \
  "v_pk_fma_f32 v[106:107], v[170:171], v[106:107], v[10:11]\n\t" \
  "v_pk_fma_f32 v[108:109], v[172:173], v[108:109], v[12:13]\n\t" \
  "v_pk_fma_f32 v[110:111], v[174:175], v[110:111], v[14:15]\n\t" \
  "v_cvt_pk_bf16_f32 v132, v104, v105\n\t" \
  "v_cvt_pk_bf16_f32 v133, v106, v107\n\t" \
  "v_cvt_pk_bf16_f32 v134, v108, v109\n\t" \
  "v_cvt_pk_bf16_f32 v135, v110, v111\n\t" \
  "v_pk_fma_f32 v[96:97], v[16:17], v[68:69], v[70:71] op_sel:[0,1,1] op_sel_hi:[1,1,1]\n\t" \
  "v_pk_fma_f32 v[98:99], v[18:19], v[68:69], v[70:71] op_sel:[0,1,1] op_sel_hi:[1,1,1]\n\t" \
  "v_pk_fma_f32 v[100:101], v[20:21], v[68:69], v[70:71] op_sel:[0,1,1] op_sel_hi:[1,1,1]\n\t" \
  "v_pk_fma_f32 v[102:103], v[22:23], v[68:69], v[70:71] op_sel:[0,1,1] op_sel_hi:[1,1,1]\n\t" \
  "v_pk_fma_f32 v[96:97], v[96:97], v[152:153], v[144:145]\n\t" \
  "v_pk_fma_f32 v[98:99], v[98:99], v[154:155], v[146:147]\n\t" \
  "v_pk_fma_f32 v[100:101], v[100:101], v[156:157], v[148:149]\n\t" \
  "v_pk_fma_f32 v[102:103], v[102:103], v[158:159], v[150:151]\n\t" \
  "v_cvt_pk_bf16_f32 v97, v96, v97\n\t" \
  "v_cvt_pk_bf16_f32 v99, v98, v99\n\t" \
  "v_cvt_pk_bf16_f32 v101, v100, v101\n\t" \
  "v_cvt_pk_bf16_f32 v103, v102, v103\n\t" \
  "v_lshlrev_b32 v96, 16, v97\n\t" \
  "v_and_b32 v97, 0xffff0000, v97\n\t" \
  "v_lshlrev_b32 v98, 16, v99\n\t" \
  "v_and_b32 v99, 0xffff0000, v99\n\t" \
  "v_lshlrev_b32 v100, 16, v101\n\t" \
  "v_and_b32 v101, 0xffff0000, v101\n\t" \
  "v_lshlrev_b32 v102, 16, v103\n\t" \
  "v_and_b32 v103, 0xffff0000, v103\n\t" \
  "v_pk_fma_f32 v[96:97], v[168:169], v[96:97], v[16:17]\n\t" \
  "v_pk_fma_f32 v[98:99], v[170:171], v[98:99], v[18:19]\n\t" \
  "v_pk_fma_f32 v[100:101], v[172:173], v[100:101], v[20:21]\n\t" \
  "v_pk_fma_f32 v[102:103], v[174:175], v[102:103], v[22:23]\n\t" \
  "v_cvt_pk_bf16_f32 v136, v96, v97\n\t" \
  "v_cvt_pk_bf16_f32 v137, v98, v99\n\t" \
  "v_cvt_pk_bf16_f32 v138, v100, v101\n\t" \
  "v_cvt_pk_bf16_f32 v139, v102, v103\n\t" \
  "v_pk_fma_f32 v[104:105], v[24:25], v[160:161], v[162:163] op_sel:[0,1,1] op_sel_hi:[1,1,1]\n\t" \
  "v_pk_fma_f32 v[106:107], v[26:27], v[160:161], v[162:163] op_sel:[0,1,1] op_sel_hi:[1,1,1]\n\t" \
  "v_pk_fma_f32 v[108:109], v[28:29], v[160:161], v[162:163] op_sel:[0,1,1] op_sel_hi:[1,1,1]\n\t" \
  "v_pk_fma_f32 v[110:111], v[30:31], v[160:161], v[162:163] op_sel:[0,1,1] op_sel_hi:[1,1,1]\n\t" \
  "v_pk_fma_f32 v[104:105], v[104:105], v[152:153], v[144:145]\n\t" \
  "v_pk_fma_f32 v[106:107], v[106:107], v[154:155], v[146:147]\n\t" \
  "v_pk_fma_f32 v[108:109], v[108:109], v[156:157], v[148:149]\n\t" \
  "v_pk_fma_f32 v[110:111], v[110:111], v[158:159], v[150:151]\n\t" \
  "v_cvt_pk_bf16_f32 v105, v104, v105\n\t" \
  "v_cvt_pk_bf16_f32 v107, v106, v107\n\t" \
  "v_cvt_pk_bf16_f32 v109, v108, v109\n\t" \
  "v_cvt_pk_bf16_f32 v111, v110, v111\n\t" \
  "v_lshlrev_b32 v104, 16, v105\n\t" \
  "v_and_b32 v105, 0xffff0000, v105\n\t" \
  "v_lshlrev_b32 v106, 16, v107\n\t" \
  "v_and_b32 v107, 0xffff0000, v107\n\t" \
  "v_lshlrev_b32 v108, 16, v109\n\t" \
  "v_and_b32 v109, 0xffff0000, v109\n\t" \
  "v_lshlrev_b32 v110, 16, v111\n\t" \
  "v_and_b32 v111, 0xffff0000, v111\n\t" \
  "v_pk_fma_f32 v[104:105], v[168:169], v[104:105], v[24:25]\n\t" \
  "v_pk_fma_f32 v[106:107], v[170:171], v[106:107], v[26:27]\n\t" \
  "v_pk_fma_f32 v[108:109], v[172:173], v[108:109], v[28:29]\n\t" \
  "v_pk_fma_f32 v[110:111], v[174:175], v[110:111], v[30:31]\n\t" \
  "v_cvt_pk_bf16_f32 v140, v104, v105\n\t" \
  "v_cvt_pk_bf16_f32 v141, v106, v107\n\t" \
  "v_cvt_pk_bf16_f32 v142, v108, v109\n\t" \
  "v_cvt_pk_bf16_f32 v143, v110, v111\n\t" \
  "v_permlane16_swap_b32 v128, v130\n\t" \
  "v_permlane16_swap_b32 v129, v131\n\t" \
  "v_permlane16_swap_b32 v132, v134\n\t" \
  "v_permlane16_swap_b32 v133, v135\n\t" \
  "v_permlane16_swap_b32 v136, v138\n\t" \
  "v_permlane16_swap_b32 v137, v139\n\t" \
  "v_permlane16_swap_b32 v140, v142\n\t" \
  "v_permlane16_swap_b32 v141, v143\n\t" \
  STP("s_memrealtime s[94:95]\n\t") \
  STP("s_memtime s[92:93]\n\t") \
  STP("s_waitcnt lgkmcnt(0)\n\t") \
  STP("v_writelane_b32 v255, s94, 46\n\t") \
  STP("v_writelane_b32 v255, s92, 47\n\t") \
  "s_mov_b32 s98, 0\n\t" \
  "s_mov_b32 s81, 0\n\t" \
  "s_lshl_b32 s99, 1, s8\n\t" \
  "s_cmp_eq_u32 s49, 0\n\t" \
  "s_cbranch_scc1 .Ltl_w2\n\t" \
  "s_waitcnt vmcnt(6)\n\t" \
  "s_branch .Ltl_t2\n\t" \
  ".Ltl_s2:\n\t" \
  "s_bcnt1_i32_b32 s54, s54\n\t" \
  "s_cmp_ge_u32 s54, 24\n\t" \
  "s_cselect_b32 s54, 1, s98\n\t" \
  "s_cmp_lg_u32 s54, 0\n\t" \
  "s_cbranch_scc1 .Ltl_q2\n\t" \
  "s_mov_b32 s98, 1\n\t" \
  "s_mov_b64 s[96:97], -1\n\t" \
  "s_cmp_eq_u32 s49, 0\n\t" \
  "s_cselect_b64 s[52:53], 0, s[50:51]\n\t" \
  "s_not_b64 s[52:53], s[52:53]\n\t" \
  "s_cmp_eq_u32 s48, 0\n\t" \
  "s_cselect_b64 exec, s[52:53], s[96:97]\n\t" \
  "s_add_u32 s44, s22, 0\n\t" \
  "buffer_store_dwordx4 v[128:131], v231, s[4:7], s44 offen sc0 sc1\n\t" \
  "s_cmp_eq_u32 s48, 1\n\t" \
  "s_cselect_b64 exec, s[52:53], s[96:97]\n\t" \
  "s_add_u32 s44, s22, 32768\n\t" \
  "buffer_store_dwordx4 v[132:135], v231, s[4:7], s44 offen sc0 sc1\n\t" \
  "s_cmp_eq_u32 s48, 2\n\t" \
  "s_cselect_b64 exec, s[52:53], s[96:97]\n\t" \
  "s_add_u32 s44, s22, 65536\n\t" \
  "buffer_store_dwordx4 v[136:139], v231, s[4:7], s44 offen sc0 sc1\n\t" \
  "s_cmp_eq_u32 s48, 3\n\t" \
  "s_cselect_b64 exec, s[52:53], s[96:97]\n\t" \
  "s_add_u32 s44, s22, 98304\n\t" \
  "buffer_store_dwordx4 v[140:143], v231, s[4:7], s44 offen sc0 sc1\n\t" \
  "s_mov_b64 exec, -1\n\t" \
  ".Ltl_q2:\n\t" \
  "s_add_u32 s81, s81, 1\n\t" \
  "s_cmp_lt_u32 s81, " FQ_CAP "\n\t" \
  "s_cbranch_scc0 .Ltl_g2\n\t" \
  "s_sleep 1\n\t" \
  "s_not_b32 s42, s45\n\t" \
  "s_bfe_u32 s47, s42, 0x50006\n\t" \
  "s_lshl_b32 s43, s42, 7\n\t" \
  "s_and_b32 s43, s43, 0x3c000\n\t" \
  "s_lshl_b32 s42, s42, 6\n\t" \
  "s_and_b32 s42, s42, 0x1000\n\t" \
  "s_add_u32 s42, s42, s43\n\t" \
  "s_add_u32 s42, s42, s15\n\t" \
  "buffer_load_dwordx2 v[248:249], v225, s[16:19], s13 offen sc1\n\t" \
  "buffer_load_dwordx4 v[72:75], v226, s[16:19], s42 offen offset:0 sc1\n\t" \
  "buffer_load_dwordx4 v[76:79], v226, s[16:19], s42 offen offset:16 sc1\n\t" \
  ".Ltl_w2:\n\t" \
  "s_waitcnt vmcnt(0)\n\t" \
  ".Ltl_t2:\n\t" \
  "v_cmp_eq_u32 vcc, s36, v249\n\t" \
  "v_xor_b32 v65, s36, v73\n\t" \
  "v_bitop3_b32 v65, v65, v75, s36 bitop3:0xf6\n\t" \
  "v_cndmask_b32 v64, 0, v248, vcc\n\t" \
  "s_or_b32 s54, vcc_lo, s99\n\t" \
  "s_nop 0\n\t" \
  "v_max_u32_dpp v64, v64, v64 quad_perm:[1,0,3,2] row_mask:0xf bank_mask:0xf\n\t" \
  "v_bitop3_b32 v65, v65, v77, s36 bitop3:0xf6\n\t" \
  "v_bitop3_b32 v65, v65, v79, s36 bitop3:0xf6\n\t" \
  "v_max_u32_dpp v64, v64, v64 quad_perm:[2,3,0,1] row_mask:0xf bank_mask:0xf\n\t" \
  "v_cmp_ne_u32 vcc, 0, v65\n\t" \
  "s_nop 0\n\t" \
  "v_max_u32_dpp v64, v64, v64 row_half_mirror row_mask:0xf bank_mask:0xf\n\t" \
  "s_nop 1\n\t" \
  "v_max_u32_dpp v64, v64, v64 row_mirror row_mask:0xf bank_mask:0xf\n\t" \
  "s_nop 0\n\t" \
  "v_readlane_b32 s45, v64, 0\n\t" \
  "v_readlane_b32 s42, v64, 16\n\t" \
  "s_max_u32 s45, s45, s42\n\t" \
  "s_max_u32 s45, s45, s46\n\t" \
  "s_not_b32 s42, s45\n\t" \
  "s_bfe_u32 s42, s42, 0x50006\n\t" \
  "s_xor_b32 s42, s42, s47\n\t" \
  "s_orn2_b32 s42, s42, s54\n\t" \
  "s_or_b32 s42, s42, vcc_lo\n\t" \
  "s_or_b32 s42, s42, vcc_hi\n\t" \
  "s_cbranch_scc1 .Ltl_s2\n\t" \
  ".Ltl_k2:\n\t" \
  STP("s_memrealtime s[94:95]\n\t") \
  STP("s_memtime s[92:93]\n\t") \
  STP("s_waitcnt lgkmcnt(0)\n\t") \
  STP("v_writelane_b32 v255, s94, 48\n\t") \
  STP("v_writelane_b32 v255, s92, 49\n\t") \
  "s_cmp_lt_i32 s57, 0\n\t" \
  "s_cbranch_scc1 .Ltl_nf\n\t" \
  "v_dot2_f32_bf16 v96, v80, v72, 0\n\t" \
  "v_dot2_f32_bf16 v97, v84, v72, 0\n\t" \
  "v_dot2_f32_bf16 v98, v81, v72, 0\n\t" \
  "v_dot2_f32_bf16 v99, v85, v72, 0\n\t" \
  "v_dot2_f32_bf16 v100, v82, v72, 0\n\t" \
  "v_dot2_f32_bf16 v101, v86, v72, 0\n\t" \
  "v_dot2_f32_bf16 v102, v83, v72, 0\n\t" \
  "v_dot2_f32_bf16 v103, v87, v72, 0\n\t" \
  "v_dot2_f32_bf16 v104, v176, v72, 0\n\t" \
  "v_dot2_f32_bf16 v105, v180, v72, 0\n\t" \
  "v_dot2_f32_bf16 v106, v177, v72, 0\n\t" \
  "v_dot2_f32_bf16 v107, v181, v72, 0\n\t" \
  "v_dot2_f32_bf16 v108, v178, v72, 0\n\t" \
  "v_dot2_f32_bf16 v109, v182, v72, 0\n\t" \
  "v_dot2_f32_bf16 v110, v179, v72, 0\n\t" \
  "v_dot2_f32_bf16 v111, v183, v72, 0\n\t" \
  "v_dot2_f32_bf16 v96, v88, v74, v96\n\t" \
  "v_dot2_f32_bf16 v97, v92, v74, v97\n\t" \
  "v_dot2_f32_bf16 v98, v89, v74, v98\n\t" \
  "v_dot2_f32_bf16 v99, v93, v74, v99\n\t" \
  "v_dot2_f32_bf16 v100, v90, v74, v100\n\t" \
  "v_dot2_f32_bf16 v101, v94, v74, v101\n\t" \
  "v_dot2_f32_bf16 v102, v91, v74, v102\n\t" \
  "v_dot2_f32_bf16 v103, v95, v74, v103\n\t" \
  "v_dot2_f32_bf16 v104, v184, v74, v104\n\t" \
  "v_dot2_f32_bf16 v105, v188, v74, v105\n\t" \
  "v_dot2_f32_bf16 v106, v185, v74, v106\n\t" \
  "v_dot2_f32_bf16 v107, v189, v74, v107\n\t" \
  "v_dot2_f32_bf16 v108, v186, v74, v108\n\t" \
  "v_dot2_f32_bf16 v109, v190, v74, v109\n\t" \
  "v_dot2_f32_bf16 v110, v187, v74, v110\n\t" \
  "v_dot2_f32_bf16 v111, v191, v74, v111\n\t" \
  "v_dot2_f32_bf16 v96, v208, v76, v96\n\t" \
  "v_dot2_f32_bf16 v97, v212, v76, v97\n\t" \
  "v_dot2_f32_bf16 v98, v209, v76, v98\n\t" \
  "v_dot2_f32_bf16 v99, v213, v76, v99\n\t" \
  "v_dot2_f32_bf16 v100, v210, v76, v100\n\t" \
  "v_dot2_f32_bf16 v101, v214, v76, v101\n\t" \
  "v_dot2_f32_bf16 v102, v211, v76, v102\n\t" \
  "v_dot2_f32_bf16 v103, v215, v76, v103\n\t" \
  "v_dot2_f32_bf16 v104, v192, v76, v104\n\t" \
  "v_dot2_f32_bf16 v105, v196, v76, v105\n\t" \
  "v_dot2_f32_bf16 v106, v193, v76, v106\n\t" \
  "v_dot2_f32_bf16 v107, v197, v76, v107\n\t" \
  "v_dot2_f32_bf16 v108, v194, v76, v108\n\t" \
  "v_dot2_f32_bf16 v109, v198, v76, v109\n\t" \
  "v_dot2_f32_bf16 v110, v195, v76, v110\n\t" \
  "v_dot2_f32_bf16 v111, v199, v76, v111\n\t" \
  "v_dot2_f32_bf16 v96, v216, v78, v96\n\t" \
  "v_dot2_f32_bf16 v97, v220, v78, v97\n\t" \
  "v_dot2_f32_bf16 v98, v217, v78, v98\n\t" \
  "v_dot2_f32_bf16 v99, v221, v78, v99\n\t" \
  "v_dot2_f32_bf16 v100, v218, v78, v100\n\t" \
  "v_dot2_f32_bf16 v101, v222, v78, v101\n\t" \
  "v_dot2_f32_bf16 v102, v219, v78, v102\n\t" \
  "v_dot2_f32_bf16 v103, v223, v78, v103\n\t" \
  "v_dot2_f32_bf16 v104, v200, v78, v104\n\t" \
  "v_dot2_f32_bf16 v105, v204, v78, v105\n\t" \
  "v_dot2_f32_bf16 v106, v201, v78, v106\n\t" \
  "v_dot2_f32_bf16 v107, v205, v78, v107\n\t" \
  "v_dot2_f32_bf16 v108, v202, v78, v108\n\t" \
  "v_dot2_f32_bf16 v109, v206, v78, v109\n\t" \
  "v_dot2_f32_bf16 v110, v203, v78, v110\n\t" \
  "v_dot2_f32_bf16 v111, v207, v78, v111\n\t" \
  "v_permlane32_swap_b32 v96, v104\n\t" \
  "v_permlane32_swap_b32 v97, v105\n\t" \
  "v_permlane32_swap_b32 v98, v106\n\t" \
  "v_permlane32_swap_b32 v99, v107\n\t" \
  "v_permlane32_swap_b32 v100, v108\n\t" \
  "v_permlane32_swap_b32 v101, v109\n\t" \
  "v_permlane32_swap_b32 v102, v110\n\t" \
  "v_permlane32_swap_b32 v103, v111\n\t" \
  "v_add_f32 v96, v96, v104\n\t" \
  "v_add_f32 v97, v97, v105\n\t" \
  "v_add_f32 v98, v98, v106\n\t" \
  "v_add_f32 v99, v99, v107\n\t" \
  "v_add_f32 v100, v100, v108\n\t" \
  "v_add_f32 v101, v101, v109\n\t" \
  "v_add_f32 v102, v102, v110\n\t" \
  "v_add_f32 v103, v103, v111\n\t" \
  "v_permlane16_swap_b32 v96, v100\n\t" \
  "v_permlane16_swap_b32 v97, v101\n\t" \
  "v_permlane16_swap_b32 v98, v102\n\t" \
  "v_permlane16_swap_b32 v99, v103\n\t" \
  "v_add_f32 v96, v96, v100\n\t" \
  "v_add_f32 v97, v97, v101\n\t" \
  "v_add_f32 v98, v98, v102\n\t" \
  "v_add_f32 v99, v99, v103\n\t" \
  "v_add_f32_dpp v96, v96, v96 row_ror:8 row_mask:0xf bank_mask:0xf\n\t" \
  "v_add_f32_dpp v97, v97, v97 row_ror:8 row_mask:0xf bank_mask:0xf\n\t" \
  "v_add_f32_dpp v98, v98, v98 row_ror:8 row_mask:0xf bank_mask:0xf\n\t" \
  "v_add_f32_dpp v99, v99, v99 row_ror:8 row_mask:0xf bank_mask:0xf\n\t" \
  "v_add_f32_dpp v96, v96, v96 row_ror:4 row_mask:0xf bank_mask:0xf\n\t" \
  "v_add_f32_dpp v97, v97, v97 row_ror:4 row_mask:0xf bank_mask:0xf\n\t" \
  "v_add_f32_dpp v98, v98, v98 row_ror:4 row_mask:0xf bank_mask:0xf\n\t" \
  "v_add_f32_dpp v99, v99, v99 row_ror:4 row_mask:0xf bank_mask:0xf\n\t" \
  "v_add_f32_dpp v96, v96, v96 row_ror:2 row_mask:0xf bank_mask:0xf\n\t" \
  "v_add_f32_dpp v97, v97, v97 row_ror:2 row_mask:0xf bank_mask:0xf\n\t" \
  "v_add_f32_dpp v98, v98, v98 row_ror:2 row_mask:0xf bank_mask:0xf\n\t" \
  "v_add_f32_dpp v99, v99, v99 row_ror:2 row_mask:0xf bank_mask:0xf\n\t" \
  "s_mov_b32 exec_lo, 0x00030003\n\t" \
  "s_mov_b32 exec_hi, 0x00030003\n\t" \
  "ds_write_b128 v253, v[96:99] offset:8192\n\t" \
  "s_mov_b64 exec, -1\n\t" \
  "v_mov_b32 v101, s36\n\t" \
  "v_mov_b32 v103, s36\n\t" \
  "s_waitcnt lgkmcnt(0)\n\t" \
  "s_barrier\n\t" \
  "v_add_u32 v104, 8192, v245\n\t" \
  "ds_read2_b32 v[96:97], v104 offset1:64\n\t" \
  "ds_read2_b32 v[98:99], v104 offset0:32 offset1:96\n\t" \
  "s_waitcnt lgkmcnt(0)\n\t" \
  "v_pk_add_f32 v[96:97], v[96:97], v[98:99]\n\t" \
  "v_add_f32 v96, v96, v97\n\t" \
  "v_cvt_pk_bf16_f32 v96, v96, v96\n\t" \
  "v_lshlrev_b32 v96, 16, v96\n\t" \
  "v_mov_b32 v97, v96\n\t" \
  "s_nop 1\n\t" \
  "v_permlane16_swap_b32 v96, v97\n\t" \
  "v_mul_f32 v98, 0xbfb8aa3b, v96\n\t" \
  "v_exp_f32 v98, v98\n\t" \
  "s_nop 0\n\t" \
  "v_add_f32 v98, 1.0, v98\n\t" \
  "v_rcp_f32 v98, v98\n\t" \
  "s_nop 0\n\t" \
  "v_mul_f32 v96, v98, v96\n\t" \
  "v_cvt_pk_bf16_f32 v96, v96, v96\n\t" \
  "v_lshlrev_b32 v96, 16, v96\n\t" \
  "v_mul_f32 v96, v97, v96\n\t" \
  "s_nop 1\n\t" \
  "v_mov_b32_dpp v97, v96 quad_perm:[1,0,3,2] row_mask:0xf bank_mask:0xf\n\t" \
  "v_cvt_pk_bf16_f32 v96, v96, v97\n\t" \
  "s_nop 0\n\t" \
  "v_readlane_b32 s60, v96, 0\n\t" \
  "v_readlane_b32 s61, v96, 2\n\t" \
  "v_readlane_b32 s62, v96, 4\n\t" \
  "v_readlane_b32 s63, v96, 6\n\t" \
  "v_readlane_b32 s64, v96, 8\n\t" \
  "v_readlane_b32 s65, v96, 10\n\t" \
  "v_readlane_b32 s66, v96, 12\n\t" \
  "v_readlane_b32 s67, v96, 14\n\t" \
  "v_dot2_f32_bf16 v104, v112, s60, 0\n\t" \
  "v_dot2_f32_bf16 v105, v34, s60, 0\n\t" \
  "v_dot2_f32_bf16 v106, v113, s60, 0\n\t" \
  "v_dot2_f32_bf16 v107, v35, s60, 0\n\t" \
  "v_dot2_f32_bf16 v104, v114, s61, v104\n\t" \
  "v_dot2_f32_bf16 v105, v38, s61, v105\n\t" \
  "v_dot2_f32_bf16 v106, v115, s61, v106\n\t" \
  "v_dot2_f32_bf16 v107, v39, s61, v107\n\t" \
  "v_dot2_f32_bf16 v104, v116, s62, v104\n\t" \
  "v_dot2_f32_bf16 v105, v42, s62, v105\n\t" \
  "v_dot2_f32_bf16 v106, v117, s62, v106\n\t" \
  "v_dot2_f32_bf16 v107, v43, s62, v107\n\t" \
  "v_dot2_f32_bf16 v104, v118, s63, v104\n\t" \
  "v_dot2_f32_bf16 v105, v46, s63, v105\n\t" \
  "v_dot2_f32_bf16 v106, v119, s63, v106\n\t" \
  "v_dot2_f32_bf16 v107, v47, s63, v107\n\t" \
  "v_dot2_f32_bf16 v104, v120, s64, v104\n\t" \
  "v_dot2_f32_bf16 v105, v50, s64, v105\n\t" \
  "v_dot2_f32_bf16 v106, v121, s64, v106\n\t" \
  "v_dot2_f32_bf16 v107, v51, s64, v107\n\t" \
  "v_dot2_f32_bf16 v104, v122, s65, v104\n\t" \
  "v_dot2_f32_bf16 v105, v54, s65, v105\n\t" \
  "v_dot2_f32_bf16 v106, v123, s65, v106\n\t" \
  "v_dot2_f32_bf16 v107, v55, s65, v107\n\t" \
  "v_dot2_f32_bf16 v104, v124, s66, v104\n\t" \
  "v_dot2_f32_bf16 v105, v58, s66, v105\n\t" \
  "v_dot2_f32_bf16 v106, v125, s66, v106\n\t" \
  "v_dot2_f32_bf16 v107, v59, s66, v107\n\t" \
  "v_dot2_f32_bf16 v104, v126, s67, v104\n\t" \
  "v_dot2_f32_bf16 v105, v62, s67, v105\n\t" \
  "v_dot2_f32_bf16 v106, v127, s67, v106\n\t" \
  "v_dot2_f32_bf16 v107, v63, s67, v107\n\t" \
  "s_nop 0\n\t" \
  "v_cvt_pk_bf16_f32 v100, v104, v105\n\t" \
  "s_nop 0\n\t" \
  "v_cvt_pk_bf16_f32 v102, v106, v107\n\t" \
  "buffer_store_dwordx4 v[100:103], v242, s[16:19], s23 offen sc1\n\t" \
  ".Ltl_nf:\n\t" \
  STP("s_memrealtime s[94:95]\n\t") \
  STP("s_memtime s[92:93]\n\t") \
  STP("s_waitcnt lgkmcnt(0)\n\t") \
  STP("v_writelane_b32 v255, s94, 50\n\t") \
  STP("v_writelane_b32 v255, s92, 51\n\t") \
  "s_cmp_eq_u32 s47, s8\n\t" \
  "s_cselect_b64 s[52:53], s[50:51], 0\n\t" \
  "s_cselect_b32 s54, 0, s49\n\t" \
  "s_not_b64 s[52:53], s[52:53]\n\t" \
  "s_mov_b64 s[96:97], -1\n\t" \
  "s_cmp_eq_u32 s98, 0\n\t" \
  "s_cbranch_scc1 .Ltl_so\n\t" \
  "s_cmp_eq_u32 s54, 0\n\t" \
  "s_cbranch_scc1 .Ltl_ns\n\t" \
  "s_mov_b64 s[52:53], s[50:51]\n\t" \
  "s_mov_b64 s[96:97], 0\n\t" \
  ".Ltl_so:\n\t" \
  "s_cmp_eq_u32 s48, 0\n\t" \
  "s_cselect_b64 exec, s[52:53], s[96:97]\n\t" \
  "s_add_u32 s44, s22, 0\n\t" \
  "buffer_store_dwordx4 v[128:131], v231, s[4:7], s44 offen sc0 sc1\n\t" \
  "s_cmp_eq_u32 s48, 1\n\t" \
  "s_cselect_b64 exec, s[52:53], s[96:97]\n\t" \
  "s_add_u32 s44, s22, 32768\n\t" \
  "buffer_store_dwordx4 v[132:135], v231, s[4:7], s44 offen sc0 sc1\n\t" \
  "s_cmp_eq_u32 s48, 2\n\t" \
  "s_cselect_b64 exec, s[52:53], s[96:97]\n\t" \
  "s_add_u32 s44, s22, 65536\n\t" \
  "buffer_store_dwordx4 v[136:139], v231, s[4:7], s44 offen sc0 sc1\n\t" \
  "s_cmp_eq_u32 s48, 3\n\t" \
  "s_cselect_b64 exec, s[52:53], s[96:97]\n\t" \
  "s_add_u32 s44, s22, 98304\n\t" \
  "buffer_store_dwordx4 v[140:143], v231, s[4:7], s44 offen sc0 sc1\n\t" \
  "s_mov_b64 exec, -1\n\t" \
  ".Ltl_ns:\n\t" \
  "s_or_b32 s42, s2, s3\n\t" \
  "s_cbranch_scc1 .Ltl_ne\n\t" \
  "s_mov_b32 s42, 2490368\n\t" \
  "buffer_store_dword v243, v244, s[16:19], s42 offen sc1\n\t" \
  ".Ltl_ne:\n\t" \
  "s_cmp_lt_i32 s57, 0\n\t" \
  "s_cbranch_scc0 .Ltl_end\n\t" \
  "v_and_b32 v1, 15, v233\n\t" \
  "v_lshlrev_b32 v1, 3, v1\n\t" \
  "s_not_b32 s42, s45\n\t" \
  "s_bfe_u32 s47, s42, 0x50006\n\t" \
  "s_lshl_b32 s43, s42, 7\n\t" \
  "s_and_b32 s43, s43, 0x3c000\n\t" \
  "s_lshl_b32 s42, s42, 6\n\t" \
  "s_and_b32 s42, s42, 0x1000\n\t" \
  "s_add_u32 s42, s42, s43\n\t" \
  "s_add_u32 s42, s42, s15\n\t" \
  "s_add_u32 s69, s42, s91\n\t" \
  "s_lshl_b32 s42, s47, 12\n\t" \
  "s_and_b32 s42, s42, 0x1c000\n\t" \
  "s_add_u32 s70, s84, s42\n\t" \
  "s_and_b32 s42, s47, 3\n\t" \
  "s_lshl_b32 s42, s42, 11\n\t" \
  "s_add_u32 s70, s70, s42\n\t" \
  "s_add_u32 s70, s70, 917504\n\t" \
  "s_and_b32 s44, s2, 3\n\t" \
  "s_lshl_b32 s42, s44, 12\n\t" \
  "s_add_u32 s42, s42, 150400\n\t" \
  "v_add_u32 v6, s42, v1\n\t" \
  "ds_read_b64 v[6:7], v6\n\t" \
  "v_and_b32 v5, 0x70, v242\n\t" \
  "v_lshrrev_b32 v3, 2, v228\n\t" \
  "v_add_u32 v3, v3, v5\n\t" \
  "v_lshl_add_u32 v3, s3, 7, v3\n\t" \
  "s_not_b32 s42, s45\n\t" \
  "s_and_b32 s42, s42, 0x7ff\n\t" \
  "s_lshl_b32 s42, s42, 11\n\t" \
  "s_lshr_b32 s71, s91, 1\n\t" \
  "s_add_u32 s71, s71, s42\n\t" \
  "s_ashr_i32 s42, s45, 31\n\t" \
  "s_orn2_b32 s42, 0x80000000, s42\n\t" \
  "s_xor_b32 s42, s42, s45\n\t" \
  "s_and_b32 s42, s42, 0xffff0000\n\t" \
  "v_mov_b32 v2, s42\n\t" \
  "v_mul_f32 v2, 0xbfb8aa3b, v2\n\t" \
  "v_exp_f32 v2, v2\n\t" \
  "s_nop 0\n\t" \
  "v_add_f32 v2, 1.0, v2\n\t" \
  "v_rcp_f32 v2, v2\n\t" \
  "s_nop 0\n\t" \
  "v_cvt_pk_bf16_f32 v2, v2, v2\n\t" \
  "v_lshlrev_b32 v2, 16, v2\n\t" \
  "s_mov_b32 s81, 0\n\t" \
  "s_sleep 16\n\t" \
  ".Ltl_r3:\n\t" \
  "s_sleep 1\n\t" \
  "buffer_load_dwordx4 v[12:15], v246, s[16:19], s84 offen sc1\n\t" \
  "buffer_load_dwordx4 v[16:19], v246, s[16:19], s85 offen sc1\n\t" \
  "buffer_load_dwordx4 v[20:23], v246, s[16:19], s86 offen sc1\n\t" \
  "buffer_load_dwordx4 v[24:27], v246, s[16:19], s87 offen sc1\n\t" \
  "buffer_load_dwordx4 v[28:31], v246, s[16:19], s88 offen sc1\n\t" \
  "buffer_load_dwordx4 v[32:35], v246, s[16:19], s89 offen sc1\n\t" \
  "buffer_load_dwordx4 v[36:39], v246, s[16:19], s90 offen sc1\n\t" \
  "buffer_load_dwordx2 v[8:9], v1, s[16:19], s69 offen sc1\n\t" \
  "buffer_load_dwordx2 v[10:11], v1, s[16:19], s70 offen sc1\n\t" \
  "s_waitcnt vmcnt(0) lgkmcnt(0)\n\t" \
  "v_xor_b32 v4, s36, v9\n\t" \
  "v_bitop3_b32 v4, v4, v11, s36 bitop3:0xf6\n\t" \
  "v_bitop3_b32 v4, v4, v13, s36 bitop3:0xf6\n\t" \
  "v_bitop3_b32 v4, v4, v15, s36 bitop3:0xf6\n\t" \
  "v_bitop3_b32 v4, v4, v17, s36 bitop3:0xf6\n\t" \
  "v_bitop3_b32 v4, v4, v19, s36 bitop3:0xf6\n\t" \
  "v_bitop3_b32 v4, v4, v21, s36 bitop3:0xf6\n\t" \
  "v_bitop3_b32 v4, v4, v23, s36 bitop3:0xf6\n\t" \
  "v_bitop3_b32 v4, v4, v25, s36 bitop3:0xf6\n\t" \
  "v_bitop3_b32 v4, v4, v27, s36 bitop3:0xf6\n\t" \
  "v_bitop3_b32 v4, v4, v29, s36 bitop3:0xf6\n\t" \
  "v_bitop3_b32 v4, v4, v31, s36 bitop3:0xf6\n\t" \
  "v_bitop3_b32 v4, v4, v33, s36 bitop3:0xf6\n\t" \
  "v_bitop3_b32 v4, v4, v35, s36 bitop3:0xf6\n\t" \
  "v_bitop3_b32 v4, v4, v37, s36 bitop3:0xf6\n\t" \
  "v_bitop3_b32 v4, v4, v39, s36 bitop3:0xf6\n\t" \
  "v_cmp_ne_u32 vcc, 0, v4\n\t" \
  "v_lshlrev_b32 v40, 16, v12\n\t" \
  "v_and_b32 v41, 0xffff0000, v12\n\t" \
  "v_lshlrev_b32 v42, 16, v14\n\t" \
  "v_and_b32 v43, 0xffff0000, v14\n\t" \
  "v_lshlrev_b32 v44, 16, v16\n\t" \
  "v_and_b32 v45, 0xffff0000, v16\n\t" \
  "v_lshlrev_b32 v46, 16, v18\n\t" \
  "v_and_b32 v47, 0xffff0000, v18\n\t" \
  "s_cbranch_vccz .Ltl_k3\n\t" \
  "s_add_u32 s81, s81, 1\n\t" \
  "s_cmp_lt_u32 s81, " FQ_CAP "\n\t" \
  "s_cbranch_scc1 .Ltl_r3\n\t" \
  "buffer_store_dword v243, off, s[16:19], 0 offset:256 sc1\n\t" \
  ".Ltl_k3:\n\t" \
  "v_pk_add_f32 v[40:41], v[40:41], v[44:45]\n\t" \
  "v_pk_add_f32 v[42:43], v[42:43], v[46:47]\n\t" \
  "v_lshlrev_b32 v44, 16, v20\n\t" \
  "v_and_b32 v45, 0xffff0000, v20\n\t" \
  "v_lshlrev_b32 v46, 16, v22\n\t" \
  "v_and_b32 v47, 0xffff0000, v22\n\t" \
  "v_pk_add_f32 v[40:41], v[40:41], v[44:45]\n\t" \
  "v_pk_add_f32 v[42:43], v[42:43], v[46:47]\n\t" \
  "v_lshlrev_b32 v44, 16, v24\n\t" \
  "v_and_b32 v45, 0xffff0000, v24\n\t" \
  "v_lshlrev_b32 v46, 16, v26\n\t" \
  "v_and_b32 v47, 0xffff0000, v26\n\t" \
  "v_pk_add_f32 v[40:41], v[40:41], v[44:45]\n\t" \
  "v_pk_add_f32 v[42:43], v[42:43], v[46:47]\n\t" \
  "v_lshlrev_b32 v44, 16, v28\n\t" \
  "v_and_b32 v45, 0xffff0000, v28\n\t" \
  "v_lshlrev_b32 v46, 16, v30\n\t" \
  "v_and_b32 v47, 0xffff0000, v30\n\t" \
  "v_pk_add_f32 v[40:41], v[40:41], v[44:45]\n\t" \
  "v_pk_add_f32 v[42:43], v[42:43], v[46:47]\n\t" \
  "v_lshlrev_b32 v44, 16, v32\n\t" \
  "v_and_b32 v45, 0xffff0000, v32\n\t" \
  "v_lshlrev_b32 v46, 16, v34\n\t" \
  "v_and_b32 v47, 0xffff0000, v34\n\t" \
  "v_pk_add_f32 v[40:41], v[40:41], v[44:45]\n\t" \
  "v_pk_add_f32 v[42:43], v[42:43], v[46:47]\n\t" \
  "v_lshlrev_b32 v44, 16, v36\n\t" \
  "v_and_b32 v45, 0xffff0000, v36\n\t" \
  "v_lshlrev_b32 v46, 16, v38\n\t" \
  "v_and_b32 v47, 0xffff0000, v38\n\t" \
  "v_pk_add_f32 v[40:41], v[40:41], v[44:45]\n\t" \
  "v_pk_add_f32 v[42:43], v[42:43], v[46:47]\n\t" \
  "s_nop 1\n\t" \
  "v_permlane32_swap_b32 v40, v42\n\t" \
  "v_permlane32_swap_b32 v41, v43\n\t" \
  "v_add_f32 v40, v40, v42\n\t" \
  "v_add_f32 v41, v41, v43\n\t" \
  "s_nop 1\n\t" \
  "v_permlane16_swap_b32 v40, v41\n\t" \
  "v_add_f32 v40, v40, v41\n\t" \
  "s_nop 1\n\t" \
  "v_add_f32_dpp v40, v40, v40 row_ror:8 row_mask:0xf bank_mask:0xf\n\t" \
  "ds_write_b32 v3, v40 offset:12288\n\t" \
  "v_lshlrev_b32 v44, 16, v8\n\t" \
  "v_and_b32 v45, 0xffff0000, v8\n\t" \
  "v_lshlrev_b32 v46, 16, v10\n\t" \
  "v_and_b32 v47, 0xffff0000, v10\n\t" \
  "s_waitcnt lgkmcnt(0)\n\t" \
  "s_barrier\n\t" \
  "s_cmp_lg_u32 s3, 0\n\t" \
  "s_cbranch_scc1 .Ltl_end\n\t" \
  "v_add_u32 v4, 12288, v1\n\t" \
  "ds_read2_b64 v[12:15], v4 offset1:16\n\t" \
  "ds_read2_b64 v[16:19], v4 offset0:32 offset1:48\n\t" \
  "v_lshrrev_b32 v1, 1, v1\n\t" \
  "s_waitcnt lgkmcnt(0)\n\t" \
  "v_pk_add_f32 v[12:13], v[12:13], v[14:15]\n\t" \
  "v_pk_add_f32 v[16:17], v[16:17], v[18:19]\n\t" \
  "v_pk_add_f32 v[12:13], v[12:13], v[16:17]\n\t" \
  "v_cvt_pk_bf16_f32 v13, v12, v13\n\t" \
  "v_lshlrev_b32 v12, 16, v13\n\t" \
  "v_and_b32 v13, 0xffff0000, v13\n\t" \
  "v_mul_f32 v12, v12, v2\n\t" \
  "v_mul_f32 v13, v13, v2\n\t" \
  "v_cvt_pk_bf16_f32 v13, v12, v13\n\t" \
  "v_lshlrev_b32 v12, 16, v13\n\t" \
  "v_and_b32 v13, 0xffff0000, v13\n\t" \
  "v_pk_add_f32 v[12:13], v[12:13], v[44:45]\n\t" \
  "v_cvt_pk_bf16_f32 v13, v12, v13\n\t" \
  "v_lshlrev_b32 v12, 16, v13\n\t" \
  "v_and_b32 v13, 0xffff0000, v13\n\t" \
  "v_pk_mul_f32 v[12:13], v[12:13], v[6:7]\n\t" \
  "v_cvt_pk_bf16_f32 v13, v12, v13\n\t" \
  "v_lshlrev_b32 v12, 16, v13\n\t" \
  "v_and_b32 v13, 0xffff0000, v13\n\t" \
  "v_pk_add_f32 v[12:13], v[12:13], v[46:47]\n\t" \
  "v_cvt_pk_bf16_f32 v12, v12, v13\n\t" \
  "s_mov_b64 exec, 0xffff\n\t" \
  "buffer_store_dword v12, v1, s[4:7], s71 offen sc0 sc1\n\t" \
  "s_mov_b64 exec, -1\n\t" \
  ".Ltl_end:\n\t" \
  STP("s_memrealtime s[94:95]\n\t") \
  STP("s_memtime s[92:93]\n\t") \
  STP("s_waitcnt lgkmcnt(0)\n\t") \
  STP("v_writelane_b32 v255, s94, 52\n\t") \
  STP("v_writelane_b32 v255, s92, 53\n\t") \
  ".subsection 1\n\t" \
  ".Lat_x:\n\t" \
  "v_lshlrev_b32 v209, 12, v233\n\t" \
  "v_add_u32 v209, 0, v209\n\t" \
  "s_mov_b64 exec, 15\n\t" \
  "ds_write_b32 v209, v243\n\t" \
  "s_mov_b64 exec, -1\n\t" \
  "s_waitcnt lgkmcnt(0)\n\t" \
  "s_branch .Lat_y\n\t" \
  ".Lat_rd:\n\t" \
  "s_waitcnt vmcnt(0) lgkmcnt(0)\n\t" \
  "s_barrier\n\t" \
  "s_nop 7\n\t" \
  "v_lshl_add_u32 v0, s3, 6, v233\n\t" \
  "s_mov_b32 s37, 0\n\t" \
  "s_branch .Lfr_again\n\t" \
  ".Lat_s0:\n\t" \
  "s_mov_b32 s81, 0\n\t" \
  ".Lat_r0:\n\t" \
  "s_sleep 4\n\t" \
  "buffer_load_dword v246, v253, s[16:19], s65 offen sc1\n\t" \
  "s_waitcnt vmcnt(0)\n\t" \
  "v_cmp_ne_u32 vcc, s36, v246\n\t" \
  "s_and_b32 vcc_lo, vcc_lo, 0xe\n\t" \
  "s_cbranch_scc0 .Lat_k0\n\t" \
  "s_add_u32 s81, s81, 1\n\t" \
  "s_cmp_lt_u32 s81, " FQ_CAP "\n\t" \
  "s_cbranch_scc1 .Lat_r0\n\t" \
  "buffer_store_dword v243, off, s[16:19], 0 offset:256 sc1\n\t" \
  "s_branch .Lat_k0\n\t" \
  ".Lat_s1:\n\t" \
  "s_mov_b32 s81, 0\n\t" \
  ".Lat_r1:\n\t" \
  "s_sleep 4\n\t" \
  "buffer_load_dword v246, v253, s[16:19], s65 offen sc1\n\t" \
  "s_waitcnt vmcnt(0)\n\t" \
  "v_cmp_ne_u32 vcc, s36, v246\n\t" \
  "s_and_b32 vcc_lo, vcc_lo, 0xfefe\n\t" \
  "s_cbranch_scc0 .Lat_k1\n\t" \
  "s_add_u32 s81, s81, 1\n\t" \
  "s_cmp_lt_u32 s81, " FQ_CAP "\n\t" \
  "s_cbranch_scc1 .Lat_r1\n\t" \
  "buffer_store_dword v243, off, s[16:19], 0 offset:256 sc1\n\t" \
  "s_branch .Lat_k1\n\t" \
  ".Lat_s2:\n\t" \
  "s_mov_b32 s81, 0\n\t" \
  ".Lat_r2:\n\t" \
  "s_sleep 4\n\t" \
  "buffer_load_dword v246, v253, s[16:19], s65 offen sc1\n\t" \
  "s_waitcnt vmcnt(0)\n\t" \
  "v_cmp_ne_u32 vcc, s36, v246\n\t" \
  "s_and_b32 vcc_lo, vcc_lo, 0xfe0000\n\t" \
  "s_cbranch_scc0 .Lat_k2\n\t" \
  "s_add_u32 s81, s81, 1\n\t" \
  "s_cmp_lt_u32 s81, " FQ_CAP "\n\t" \
  "s_cbranch_scc1 .Lat_r2\n\t" \
  "buffer_store_dword v243, off, s[16:19], 0 offset:256 sc1\n\t" \
  "s_branch .Lat_k2\n\t" \
  ".Lat_s3:\n\t" \
  "s_mov_b32 s81, 0\n\t" \
  ".Lat_r3:\n\t" \
  "s_sleep 4\n\t" \
  "s_add_u32 s78, s75, 0\n\t" \
  "buffer_load_dwordx4 v[208:211], v234, s[16:19], s78 offen sc1\n\t" \
  "s_add_u32 s78, s75, 32768\n\t" \
  "buffer_load_dwordx4 v[212:215], v234, s[16:19], s78 offen sc1\n\t" \
  "s_add_u32 s78, s75, 65536\n\t" \
  "buffer_load_dwordx4 v[216:219], v234, s[16:19], s78 offen sc1\n\t" \
  "s_add_u32 s78, s75, 98304\n\t" \
  "buffer_load_dwordx4 v[220:223], v234, s[16:19], s78 offen sc1\n\t" \
  "s_waitcnt vmcnt(0)\n\t" \
  "v_xor_b32 v246, v243, v209\n\t" \
  "v_bitop3_b32 v246, v246, v211, v243 bitop3:0xf6\n\t" \
  "v_bitop3_b32 v246, v246, v213, v243 bitop3:0xf6\n\t" \
  "v_bitop3_b32 v246, v246, v215, v243 bitop3:0xf6\n\t" \
  "v_bitop3_b32 v246, v246, v217, v243 bitop3:0xf6\n\t" \
  "v_bitop3_b32 v246, v246, v219, v243 bitop3:0xf6\n\t" \
  "v_bitop3_b32 v246, v246, v221, v243 bitop3:0xf6\n\t" \
  "v_bitop3_b32 v246, v246, v223, v243 bitop3:0xf6\n\t" \
  "v_cmp_ne_u32 vcc, 0, v246\n\t" \
  "s_cbranch_vccz .Lat_k3\n\t" \
  "s_add_u32 s81, s81, 1\n\t" \
  "s_cmp_lt_u32 s81, " FQ_CAP "\n\t" \
  "s_cbranch_scc1 .Lat_r3\n\t" \
  "buffer_store_dword v243, off, s[16:19], 0 offset:256 sc1\n\t" \
  "s_branch .Lat_k3\n\t" \
  ".Lat_s4:\n\t" \
  "s_mov_b32 s81, 0\n\t" \
  ".Lat_r4:\n\t" \
  "s_sleep 4\n\t" \
  "buffer_load_dword v246, v235, s[16:19], s73 offen sc1\n\t" \
  "s_waitcnt vmcnt(0)\n\t" \
  "v_cmp_ne_u32 vcc, s36, v246\n\t" \
  "s_cbranch_vccz .Lat_k4\n\t" \
  "s_add_u32 s81, s81, 1\n\t" \
  "s_cmp_lt_u32 s81, " FQ_CAP "\n\t" \
  "s_cbranch_scc1 .Lat_r4\n\t" \
  "buffer_store_dword v243, off, s[16:19], 0 offset:256 sc1\n\t" \
  "s_branch .Lat_k4\n\t" \
  ".Lat_m3:\n\t" \
  "s_waitcnt vmcnt(0)\n\t" \
  "v_cmp_ne_u32 vcc, s36, v251\n\t" \
  "s_mov_b32 s88, vcc_lo\n\t" \
  "s_cbranch_vccnz .Lat_n3\n\t" \
  "buffer_load_dwordx4 v[64:67], v242, s[16:19], s79 offen offset:0 sc1\n\t" \
  "buffer_load_dwordx4 v[68:71], v242, s[16:19], s79 offen offset:1024 sc1\n\t" \
  "buffer_load_dwordx4 v[72:75], v242, s[16:19], s79 offen offset:2048 sc1\n\t" \
  "buffer_load_dwordx4 v[76:79], v242, s[16:19], s79 offen offset:3072 sc1\n\t" \
  "s_branch .Lat_n3\n\t" \
  ".Lat_v:\n\t" \
  "s_mov_b32 s98, 0\n\t" \
  ".Lat_v1:\n\t" \
  "s_sleep 4\n\t" \
  "buffer_load_dword v209, v251, s[16:19], 0 offen sc1\n\t" \
  "s_waitcnt vmcnt(0)\n\t" \
  "v_and_b32 v250, 0xbfffbfff, v209\n\t" \
  "v_cmp_ne_u32_e64 s[84:85], s36, v250\n\t" \
  "s_cmp_lg_u64 s[84:85], 0\n\t" \
  "s_cbranch_scc0 .Lat_w\n\t" \
  "s_add_u32 s98, s98, 1\n\t" \
  "s_cmp_lt_u32 s98, " FQ_CAP "\n\t" \
  "s_cbranch_scc1 .Lat_v1\n\t" \
  "v_mov_b32 v250, s36\n\t" \
  "buffer_store_dword v250, off, s[16:19], 0 offset:256 sc1\n\t" \
  "s_branch .Lat_w\n\t" \
  ".Lat_s99:\n\t" \
  "s_mov_b32 s81, 0\n\t" \
  ".Lat_r99:\n\t" \
  "buffer_load_dword v246, off, s[16:19], s73 offset:32 sc1\n\t" \
  "s_waitcnt vmcnt(0)\n\t" \
  "v_cmp_ne_u32 vcc, s36, v246\n\t" \
  "s_cbranch_vccz .Lat_k99\n\t" \
  "s_add_u32 s81, s81, 1\n\t" \
  "s_cmp_lt_u32 s81, " FQ_CAP "\n\t" \
  "s_cbranch_scc1 .Lat_r99\n\t" \
  "buffer_store_dword v243, off, s[16:19], 0 offset:256 sc1\n\t" \
  ".Lat_k99:\n\t" \
  "buffer_load_dwordx4 v[64:67], v242, s[16:19], s79 offen offset:0 sc1\n\t" \
  "buffer_load_dwordx4 v[68:71], v242, s[16:19], s79 offen offset:1024 sc1\n\t" \
  "buffer_load_dwordx4 v[72:75], v242, s[16:19], s79 offen offset:2048 sc1\n\t" \
  "buffer_load_dwordx4 v[76:79], v242, s[16:19], s79 offen offset:3072 sc1\n\t" \
  "s_waitcnt vmcnt(0)\n\t" \
  "s_branch .Lat_d2\n\t" \
  ".Ltl_g2:\n\t" \
  "buffer_store_dword v243, off, s[16:19], 0 offset:256 sc1\n\t" \
  "s_branch .Ltl_k2\n\t" \
  "s_endpgm\n\t" \
  ".Lfz_z:\n\t" \
  ".fill 128, 4, 0xBF800000\n\t" \
  ".subsection 2\n\t" \
  ".Lsf_go:\n\t" \
  "v_mfma_f32_16x16x32_bf16 v[28:31], a[156:159], a[224:227], v[28:31]\n\t" \
  "v_mov_b32 v196, 1.0\n\t" \
  "v_mov_b32 v197, 1.0\n\t" \
  "v_mfma_f32_16x16x32_bf16 v[28:31], a[92:95], a[240:243], v[28:31]\n\t" \
  "v_mov_b32 v198, 1.0\n\t" \
  "v_mov_b32 v199, 1.0\n\t" \
  "v_mfma_f32_16x16x32_bf16 v[24:27], a[152:155], a[224:227], v[24:27]\n\t" \
  "s_mov_b32 s99, 0\n\t" \
  "s_nop 1\n\t" \
  "v_mfma_f32_16x16x32_bf16 v[24:27], a[88:91], a[240:243], v[24:27]\n\t" \
  "v_mfma_f32_16x16x32_bf16 v[20:23], a[148:151], a[224:227], v[20:23]\n\t" \
  "v_cvt_pk_bf16_f32 v31, v30, v31\n\t" \
  "v_cvt_pk_bf16_f32 v30, v28, v29\n\t" \
  "v_mfma_f32_16x16x32_bf16 v[20:23], a[84:87], a[240:243], v[20:23]\n\t" \
  "s_nop 2\n\t" \
  "v_mfma_f32_16x16x32_bf16 v[16:19], a[144:147], a[224:227], v[16:19]\n\t" \
  "v_cvt_pk_bf16_f32 v28, v24, v25\n\t" \
  "v_cvt_pk_bf16_f32 v29, v26, v27\n\t" \
  "v_mfma_f32_16x16x32_bf16 v[16:19], a[80:83], a[240:243], v[16:19]\n\t" \
  "v_permlane16_swap_b32 v28, v30\n\t" \
  "v_mfma_f32_16x16x32_bf16 v[12:15], a[140:143], a[224:227], v[12:15]\n\t" \
  "v_cvt_pk_bf16_f32 v27, v22, v23\n\t" \
  "v_cvt_pk_bf16_f32 v26, v20, v21\n\t" \
  "v_mfma_f32_16x16x32_bf16 v[12:15], a[76:79], a[240:243], v[12:15]\n\t" \
  "v_permlane16_swap_b32 v29, v31\n\t" \
  "v_mfma_f32_16x16x32_bf16 v[8:11], a[136:139], a[224:227], v[8:11]\n\t" \
  "v_mfma_f32_16x16x32_bf16 v[8:11], a[72:75], a[240:243], v[8:11]\n\t" \
  "v_cvt_pk_bf16_f32 v24, v16, v17\n\t" \
  "v_cvt_pk_bf16_f32 v25, v18, v19\n\t" \
  "v_mfma_f32_16x16x32_bf16 v[4:7], a[132:135], a[224:227], v[4:7]\n\t" \
  "v_permlane16_swap_b32 v24, v26\n\t" \
  "v_mfma_f32_16x16x32_bf16 v[4:7], a[68:71], a[240:243], v[4:7]\n\t" \
  "v_cvt_pk_bf16_f32 v23, v14, v15\n\t" \
  "v_cvt_pk_bf16_f32 v22, v12, v13\n\t" \
  "v_mfma_f32_16x16x32_bf16 v[0:3], a[128:131], a[224:227], v[0:3]\n\t" \
  "s_waitcnt vmcnt(1)\n\t" \
  "v_cvt_pk_bf16_f32 v20, v8, v9\n\t" \
  "v_cvt_pk_bf16_f32 v21, v10, v11\n\t" \
  "v_mfma_f32_16x16x32_bf16 v[0:3], a[64:67], a[240:243], v[0:3]\n\t" \
  "s_barrier\n\t" \
  "v_mfma_f32_16x16x32_bf16 v[60:63], a[156:159], a[228:231], v[60:63]\n\t" \
  "v_cvt_pk_bf16_f32 v19, v6, v7\n\t" \
  "v_cvt_pk_bf16_f32 v18, v4, v5\n\t" \
  "v_mfma_f32_16x16x32_bf16 v[60:63], a[92:95], a[244:247], v[60:63]\n\t" \
  "v_permlane16_swap_b32 v25, v27\n\t" \
  "v_mfma_f32_16x16x32_bf16 v[56:59], a[152:155], a[228:231], v[56:59]\n\t" \
  "ds_read_b128 a[0:3], v220 offset:0\n\t" \
  "v_cvt_pk_bf16_f32 v16, v0, v1\n\t" \
  "v_mfma_f32_16x16x32_bf16 v[56:59], a[88:91], a[244:247], v[56:59]\n\t" \
  "ds_read_b128 a[4:7], v221 offset:0\n\t" \
  "v_cvt_pk_bf16_f32 v17, v2, v3\n\t" \
  "v_mfma_f32_16x16x32_bf16 v[52:55], a[148:151], a[228:231], v[52:55]\n\t" \
  "v_cvt_pk_bf16_f32 v63, v62, v63\n\t" \
  "v_cvt_pk_bf16_f32 v62, v60, v61\n\t" \
  "v_mfma_f32_16x16x32_bf16 v[52:55], a[84:87], a[244:247], v[52:55]\n\t" \
  "v_permlane16_swap_b32 v20, v22\n\t" \
  "v_mfma_f32_16x16x32_bf16 v[48:51], a[144:147], a[228:231], v[48:51]\n\t" \
  "ds_read_b128 a[8:11], v222 offset:0\n\t" \
  "v_cvt_pk_bf16_f32 v60, v56, v57\n\t" \
  "v_mfma_f32_16x16x32_bf16 v[48:51], a[80:83], a[244:247], v[48:51]\n\t" \
  "ds_read_b128 a[12:15], v223 offset:0\n\t" \
  "v_cvt_pk_bf16_f32 v61, v58, v59\n\t" \
  "v_mfma_f32_16x16x32_bf16 v[44:47], a[140:143], a[228:231], v[44:47]\n\t" \
  "v_cvt_pk_bf16_f32 v59, v54, v55\n\t" \
  "v_cvt_pk_bf16_f32 v58, v52, v53\n\t" \
  "v_mfma_f32_16x16x32_bf16 v[44:47], a[76:79], a[244:247], v[44:47]\n\t" \
  "v_permlane16_swap_b32 v21, v23\n\t" \
  "v_mfma_f32_16x16x32_bf16 v[40:43], a[136:139], a[228:231], v[40:43]\n\t" \
  "ds_read_b128 a[16:19], v220 offset:4096\n\t" \
  "v_cvt_pk_bf16_f32 v56, v48, v49\n\t" \
  "v_mfma_f32_16x16x32_bf16 v[40:43], a[72:75], a[244:247], v[40:43]\n\t" \
  "ds_read_b128 a[20:23], v221 offset:4096\n\t" \
  "v_cvt_pk_bf16_f32 v57, v50, v51\n\t" \
  "v_mfma_f32_16x16x32_bf16 v[36:39], a[132:135], a[228:231], v[36:39]\n\t" \
  "v_cvt_pk_bf16_f32 v55, v46, v47\n\t" \
  "v_cvt_pk_bf16_f32 v54, v44, v45\n\t" \
  "v_mfma_f32_16x16x32_bf16 v[36:39], a[68:71], a[244:247], v[36:39]\n\t" \
  "v_permlane16_swap_b32 v16, v18\n\t" \
  "v_mfma_f32_16x16x32_bf16 v[32:35], a[128:131], a[228:231], v[32:35]\n\t" \
  "ds_read_b128 a[24:27], v222 offset:4096\n\t" \
  "v_cvt_pk_bf16_f32 v52, v40, v41\n\t" \
  "v_mfma_f32_16x16x32_bf16 v[32:35], a[64:67], a[244:247], v[32:35]\n\t" \
  "ds_read_b128 a[28:31], v223 offset:4096\n\t" \
  "v_cvt_pk_bf16_f32 v53, v42, v43\n\t" \
  "v_mfma_f32_16x16x32_bf16 v[92:95], a[156:159], a[232:235], v[92:95]\n\t" \
  "v_cvt_pk_bf16_f32 v51, v38, v39\n\t" \
  "v_cvt_pk_bf16_f32 v50, v36, v37\n\t" \
  "v_mfma_f32_16x16x32_bf16 v[92:95], a[92:95], a[248:251], v[92:95]\n\t" \
  "v_permlane16_swap_b32 v17, v19\n\t" \
  "v_mfma_f32_16x16x32_bf16 v[88:91], a[152:155], a[232:235], v[88:91]\n\t" \
  "v_cvt_pk_bf16_f32 v48, v32, v33\n\t" \
  "v_cvt_pk_bf16_f32 v49, v34, v35\n\t" \
  "v_mfma_f32_16x16x32_bf16 v[88:91], a[88:91], a[248:251], v[88:91]\n\t" \
  "v_permlane16_swap_b32 v60, v62\n\t" \
  "v_mfma_f32_16x16x32_bf16 v[84:87], a[148:151], a[232:235], v[84:87]\n\t" \
  "ds_read_b128 a[32:35], v220 offset:8192\n\t" \
  "v_cvt_pk_bf16_f32 v95, v94, v95\n\t" \
  "v_mfma_f32_16x16x32_bf16 v[84:87], a[84:87], a[248:251], v[84:87]\n\t" \
  "ds_read_b128 a[36:39], v221 offset:8192\n\t" \
  "v_cvt_pk_bf16_f32 v94, v92, v93\n\t" \
  "v_mfma_f32_16x16x32_bf16 v[80:83], a[144:147], a[232:235], v[80:83]\n\t" \
  "v_cvt_pk_bf16_f32 v92, v88, v89\n\t" \
  "v_permlane16_swap_b32 v61, v63\n\t" \
  "v_mfma_f32_16x16x32_bf16 v[80:83], a[80:83], a[248:251], v[80:83]\n\t" \
  "ds_read_b128 a[40:43], v222 offset:8192\n\t" \
  "v_cvt_pk_bf16_f32 v93, v90, v91\n\t" \
  "v_mfma_f32_16x16x32_bf16 v[76:79], a[140:143], a[232:235], v[76:79]\n\t" \
  "v_cvt_pk_bf16_f32 v91, v86, v87\n\t" \
  "v_cvt_pk_bf16_f32 v90, v84, v85\n\t" \
  "v_mfma_f32_16x16x32_bf16 v[76:79], a[76:79], a[248:251], v[76:79]\n\t" \
  "ds_read_b128 a[44:47], v223 offset:8192\n\t" \
  "v_permlane16_swap_b32 v56, v58\n\t" \
  "v_mfma_f32_16x16x32_bf16 v[72:75], a[136:139], a[232:235], v[72:75]\n\t" \
  "v_cvt_pk_bf16_f32 v88, v80, v81\n\t" \
  "v_permlane16_swap_b32 v57, v59\n\t" \
  "v_mfma_f32_16x16x32_bf16 v[72:75], a[72:75], a[248:251], v[72:75]\n\t" \
  "ds_read_b128 a[48:51], v220 offset:12288\n\t" \
  "v_cvt_pk_bf16_f32 v89, v82, v83\n\t" \
  "v_mfma_f32_16x16x32_bf16 v[68:71], a[132:135], a[232:235], v[68:71]\n\t" \
  "ds_read_b128 a[52:55], v221 offset:12288\n\t" \
  "v_cvt_pk_bf16_f32 v87, v78, v79\n\t" \
  "v_mfma_f32_16x16x32_bf16 v[68:71], a[68:71], a[248:251], v[68:71]\n\t" \
  "v_cvt_pk_bf16_f32 v86, v76, v77\n\t" \
  "v_permlane16_swap_b32 v52, v54\n\t" \
  "v_mfma_f32_16x16x32_bf16 v[64:67], a[128:131], a[232:235], v[64:67]\n\t" \
  "ds_read_b128 a[56:59], v222 offset:12288\n\t" \
  "v_permlane16_swap_b32 v53, v55\n\t" \
  "v_mfma_f32_16x16x32_bf16 v[64:67], a[64:67], a[248:251], v[64:67]\n\t" \
  "v_cvt_pk_bf16_f32 v84, v72, v73\n\t" \
  "v_cvt_pk_bf16_f32 v85, v74, v75\n\t" \
  "v_mfma_f32_16x16x32_bf16 v[124:127], a[156:159], a[236:239], v[124:127]\n\t" \
  "ds_read_b128 a[60:63], v223 offset:12288\n\t" \
  "v_cvt_pk_bf16_f32 v83, v70, v71\n\t" \
  "v_mfma_f32_16x16x32_bf16 v[124:127], a[92:95], a[252:255], v[124:127]\n\t" \
  "v_cvt_pk_bf16_f32 v82, v68, v69\n\t" \
  "v_permlane16_swap_b32 v48, v50\n\t" \
  "v_mfma_f32_16x16x32_bf16 v[120:123], a[152:155], a[236:239], v[120:123]\n\t" \
  "v_cvt_pk_bf16_f32 v80, v64, v65\n\t" \
  "v_permlane16_swap_b32 v49, v51\n\t" \
  "v_mfma_f32_16x16x32_bf16 v[120:123], a[88:91], a[252:255], v[120:123]\n\t" \
  "v_permlane16_swap_b32 v92, v94\n\t" \
  "v_mfma_f32_16x16x32_bf16 v[116:119], a[148:151], a[236:239], v[116:119]\n\t" \
  "v_cvt_pk_bf16_f32 v81, v66, v67\n\t" \
  "v_cvt_pk_bf16_f32 v127, v126, v127\n\t" \
  "v_mfma_f32_16x16x32_bf16 v[116:119], a[84:87], a[252:255], v[116:119]\n\t" \
  "v_permlane16_swap_b32 v93, v95\n\t" \
  "v_mfma_f32_16x16x32_bf16 v[112:115], a[144:147], a[236:239], v[112:115]\n\t" \
  "v_cvt_pk_bf16_f32 v126, v124, v125\n\t" \
  "v_cvt_pk_bf16_f32 v124, v120, v121\n\t" \
  "v_mfma_f32_16x16x32_bf16 v[112:115], a[80:83], a[252:255], v[112:115]\n\t" \
  "v_cvt_pk_bf16_f32 v125, v122, v123\n\t" \
  "v_permlane16_swap_b32 v88, v90\n\t" \
  "v_mfma_f32_16x16x32_bf16 v[108:111], a[140:143], a[236:239], v[108:111]\n\t" \
  "v_permlane16_swap_b32 v89, v91\n\t" \
  "v_mfma_f32_16x16x32_bf16 v[108:111], a[76:79], a[252:255], v[108:111]\n\t" \
  "v_cvt_pk_bf16_f32 v123, v118, v119\n\t" \
  "v_cvt_pk_bf16_f32 v122, v116, v117\n\t" \
  "v_mfma_f32_16x16x32_bf16 v[104:107], a[136:139], a[236:239], v[104:107]\n\t" \
  "v_permlane16_swap_b32 v84, v86\n\t" \
  "v_mfma_f32_16x16x32_bf16 v[104:107], a[72:75], a[252:255], v[104:107]\n\t" \
  "v_cvt_pk_bf16_f32 v120, v112, v113\n\t" \
  "v_cvt_pk_bf16_f32 v121, v114, v115\n\t" \
  "v_mfma_f32_16x16x32_bf16 v[100:103], a[132:135], a[236:239], v[100:103]\n\t" \
  "s_add_u32 m0, s38, 49152\n\t" \
  "v_permlane16_swap_b32 v85, v87\n\t" \
  "v_mfma_f32_16x16x32_bf16 v[100:103], a[68:71], a[252:255], v[100:103]\n\t" \
  "s_add_u32 s96, s38, 32768\n\t" \
  "v_permlane16_swap_b32 v80, v82\n\t" \
  "v_mfma_f32_16x16x32_bf16 v[96:99], a[128:131], a[236:239], v[96:99]\n\t" \
  "s_waitcnt lgkmcnt(0)\n\t" \
  "v_permlane16_swap_b32 v81, v83\n\t" \
  "v_mfma_f32_16x16x32_bf16 v[96:99], a[64:67], a[252:255], v[96:99]\n\t" \
  "s_barrier\n\t" \
  "v_mfma_f32_16x16x32_bf16 v[0:3], a[0:3], v[16:19], 0\n\t" \
  "buffer_load_dwordx4 v224, s[60:63], s96 offen offset:0 lds\n\t" \
  "v_mfma_f32_16x16x32_bf16 v[0:3], a[4:7], v[20:23], v[0:3]\n\t" \
  "v_cvt_pk_bf16_f32 v119, v110, v111\n\t" \
  "v_cvt_pk_bf16_f32 v118, v108, v109\n\t" \
  "v_mfma_f32_16x16x32_bf16 v[0:3], a[8:11], v[24:27], v[0:3]\n\t" \
  "v_permlane16_swap_b32 v124, v126\n\t" \
  "v_mfma_f32_16x16x32_bf16 v[0:3], a[12:15], v[28:31], v[0:3]\n\t" \
  "ds_read_b128 a[192:195], v220 offset:16384\n\t" \
  "v_cvt_pk_bf16_f32 v116, v104, v105\n\t" \
  "v_mfma_f32_16x16x32_bf16 v[32:35], a[0:3], v[48:51], 0\n\t" \
  "v_permlane16_swap_b32 v125, v127\n\t" \
  "v_mfma_f32_16x16x32_bf16 v[32:35], a[4:7], v[52:55], v[32:35]\n\t" \
  "ds_read_b128 a[196:199], v221 offset:16384\n\t" \
  "v_cvt_pk_bf16_f32 v117, v106, v107\n\t" \
  "s_nop 0\n\t" \
  "v_max3_f32 v194, v0, v1, v2\n\t" \
  "v_max_f32 v194, v194, v3\n\t" \
  "v_mfma_f32_16x16x32_bf16 v[32:35], a[8:11], v[56:59], v[32:35]\n\t" \
  "v_mov_b32 v195, v194\n\t" \
  "v_cvt_pk_bf16_f32 v115, v102, v103\n\t" \
  "s_nop 0\n\t" \
  "v_permlane32_swap_b32 v194, v195\n\t" \
  "v_mfma_f32_16x16x32_bf16 v[32:35], a[12:15], v[60:63], v[32:35]\n\t" \
  "v_permlane16_swap_b32 v120, v122\n\t" \
  "v_max_f32 v194, v194, v195\n\t" \
  "v_mov_b32 v195, v194\n\t" \
  "s_nop 1\n\t" \
  "v_permlane16_swap_b32 v194, v195\n\t" \
  "v_max_f32 v194, v194, v195\n\t" \
  "v_sub_f32 v176, 0xc2000000, v194\n\t" \
  "v_sub_f32 v177, 0xc2000000, v194\n\t" \
  "v_sub_f32 v178, 0xc2000000, v194\n\t" \
  "v_sub_f32 v179, 0xc2000000, v194\n\t" \
  "s_nop 1\n\t" \
  "v_mfma_f32_16x16x32_bf16 v[4:7], a[16:19], v[16:19], v[176:179]\n\t" \
  "v_add_f32 v0, v0, v176\n\t" \
  "v_add_f32 v1, v1, v176\n\t" \
  "v_add_f32 v2, v2, v176\n\t" \
  "v_add_f32 v3, v3, v176\n\t" \
  "v_mfma_f32_16x16x32_bf16 v[4:7], a[20:23], v[20:23], v[4:7]\n\t" \
  "buffer_load_dwordx4 v225, s[60:63], s96 offen offset:1024 lds\n\t" \
  "v_max3_f32 v194, v32, v33, v34\n\t" \
  "v_max_f32 v194, v194, v35\n\t" \
  "v_mov_b32 v195, v194\n\t" \
  "v_mfma_f32_16x16x32_bf16 v[4:7], a[24:27], v[24:27], v[4:7]\n\t" \
  "ds_read_b128 a[200:203], v222 offset:16384\n\t" \
  "v_permlane32_swap_b32 v194, v195\n\t" \
  "v_max_f32 v194, v194, v195\n\t" \
  "v_mov_b32 v195, v194\n\t" \
  "v_mfma_f32_16x16x32_bf16 v[4:7], a[28:31], v[28:31], v[4:7]\n\t" \
  "v_permlane16_swap_b32 v121, v123\n\t" \
  "v_permlane16_swap_b32 v194, v195\n\t" \
  "v_mfma_f32_16x16x32_bf16 v[8:11], a[32:35], v[16:19], v[176:179]\n\t" \
  "v_max_f32 v194, v194, v195\n\t" \
  "v_sub_f32 v180, 0xc2000000, v194\n\t" \
  "v_mfma_f32_16x16x32_bf16 v[8:11], a[36:39], v[20:23], v[8:11]\n\t" \
  "v_sub_f32 v181, 0xc2000000, v194\n\t" \
  "v_sub_f32 v182, 0xc2000000, v194\n\t" \
  "v_mfma_f32_16x16x32_bf16 v[8:11], a[40:43], v[24:27], v[8:11]\n\t" \
  "v_sub_f32 v183, 0xc2000000, v194\n\t" \
  "v_max3_f32 v192, v4, v5, v6\n\t" \
  "v_mfma_f32_16x16x32_bf16 v[8:11], a[44:47], v[28:31], v[8:11]\n\t" \
  "v_add_f32 v32, v32, v180\n\t" \
  "v_add_f32 v33, v33, v180\n\t" \
  "v_mfma_f32_16x16x32_bf16 v[12:15], a[48:51], v[16:19], v[176:179]\n\t" \
  "v_add_f32 v34, v34, v180\n\t" \
  "v_add_f32 v35, v35, v180\n\t" \
  "v_mfma_f32_16x16x32_bf16 v[12:15], a[52:55], v[20:23], v[12:15]\n\t" \
  "ds_read_b128 a[204:207], v223 offset:16384\n\t" \
  "v_permlane16_swap_b32 v116, v118\n\t" \
  "v_max3_f32 v203, v7, v8, v9\n\t" \
  "v_max3_f32 v192, v192, v10, v11\n\t" \
  "v_mfma_f32_16x16x32_bf16 v[12:15], a[56:59], v[24:27], v[12:15]\n\t" \
  "buffer_load_dwordx4 v226, s[60:63], s96 offen offset:2048 lds\n\t" \
  "v_mfma_f32_16x16x32_bf16 v[12:15], a[60:63], v[28:31], v[12:15]\n\t" \
  "ds_read_b128 a[208:211], v220 offset:20480\n\t" \
  "v_permlane16_swap_b32 v117, v119\n\t" \
  "v_cvt_pk_bf16_f32 v114, v100, v101\n\t" \
  "v_mfma_f32_16x16x32_bf16 v[64:67], a[0:3], v[80:83], 0\n\t" \
  "s_nop 3\n\t" \
  "v_max3_f32 v203, v203, v12, v13\n\t" \
  "v_mfma_f32_16x16x32_bf16 v[64:67], a[4:7], v[84:87], v[64:67]\n\t" \
  "ds_read_b128 a[212:215], v221 offset:20480\n\t" \
  "v_max3_f32 v192, v192, v14, v15\n\t" \
  "v_max_f32 v192, v192, v203\n\t" \
  "v_cmp_lt_f32 vcc, 0x42800000, v192\n\t" \
  "v_mfma_f32_16x16x32_bf16 v[64:67], a[8:11], v[88:91], v[64:67]\n\t" \
  "v_cvt_pk_bf16_f32 v112, v96, v97\n\t" \
  "v_cvt_pk_bf16_f32 v113, v98, v99\n\t" \
  "v_mfma_f32_16x16x32_bf16 v[64:67], a[12:15], v[92:95], v[64:67]\n\t" \
  "s_cbranch_vccnz .Lsf_c00\n\t" \
  ".Lsf_r00:\n\t" \
  "v_mfma_f32_16x16x32_bf16 v[36:39], a[16:19], v[48:51], v[180:183]\n\t" \
  "ds_read_b128 a[216:219], v222 offset:20480\n\t" \
  "v_exp_f32 v0, v0\n\t" \
  "v_exp_f32 v1, v1\n\t" \
  "v_mfma_f32_16x16x32_bf16 v[36:39], a[20:23], v[52:55], v[36:39]\n\t" \
  "ds_read_b128 a[220:223], v223 offset:20480\n\t" \
  "s_nop 0\n\t" \
  "v_max3_f32 v194, v64, v65, v66\n\t" \
  "v_max_f32 v194, v194, v67\n\t" \
  "v_mov_b32 v195, v194\n\t" \
  "v_mfma_f32_16x16x32_bf16 v[36:39], a[24:27], v[56:59], v[36:39]\n\t" \
  "ds_read_b128 a[224:227], v220 offset:24576\n\t" \
  "v_permlane32_swap_b32 v194, v195\n\t" \
  "v_max_f32 v194, v194, v195\n\t" \
  "v_mov_b32 v195, v194\n\t" \
  "v_mfma_f32_16x16x32_bf16 v[36:39], a[28:31], v[60:63], v[36:39]\n\t" \
  "s_nop 0\n\t" \
  "v_permlane16_swap_b32 v194, v195\n\t" \
  "v_max_f32 v194, v194, v195\n\t" \
  "v_sub_f32 v184, 0xc2000000, v194\n\t" \
  "v_sub_f32 v185, 0xc2000000, v194\n\t" \
  "v_mfma_f32_16x16x32_bf16 v[40:43], a[32:35], v[48:51], v[180:183]\n\t" \
  "buffer_load_dwordx4 v227, s[60:63], s96 offen offset:3072 lds\n\t" \
  "v_sub_f32 v186, 0xc2000000, v194\n\t" \
  "v_max3_f32 v192, v36, v37, v38\n\t" \
  "v_sub_f32 v187, 0xc2000000, v194\n\t" \
  "v_mfma_f32_16x16x32_bf16 v[40:43], a[36:39], v[52:55], v[40:43]\n\t" \
  "v_add_f32 v64, v64, v184\n\t" \
  "v_exp_f32 v2, v2\n\t" \
  "v_exp_f32 v3, v3\n\t" \
  "v_mfma_f32_16x16x32_bf16 v[40:43], a[40:43], v[56:59], v[40:43]\n\t" \
  "v_add_f32 v65, v65, v184\n\t" \
  "v_permlane16_swap_b32 v112, v114\n\t" \
  "v_exp_f32 v4, v4\n\t" \
  "v_mfma_f32_16x16x32_bf16 v[40:43], a[44:47], v[60:63], v[40:43]\n\t" \
  "v_add_f32 v66, v66, v184\n\t" \
  "v_exp_f32 v5, v5\n\t" \
  "v_exp_f32 v6, v6\n\t" \
  "v_mfma_f32_16x16x32_bf16 v[44:47], a[48:51], v[48:51], v[180:183]\n\t" \
  "s_add_u32 m0, s38, 147456\n\t" \
  "v_add_f32 v67, v67, v184\n\t" \
  "v_exp_f32 v7, v7\n\t" \
  "v_exp_f32 v8, v8\n\t" \
  "v_mfma_f32_16x16x32_bf16 v[44:47], a[52:55], v[52:55], v[44:47]\n\t" \
  "ds_read_b128 a[228:231], v221 offset:24576\n\t" \
  "v_max3_f32 v203, v39, v40, v41\n\t" \
  "v_max3_f32 v192, v192, v42, v43\n\t" \
  "v_exp_f32 v9, v9\n\t" \
  "v_mfma_f32_16x16x32_bf16 v[44:47], a[56:59], v[56:59], v[44:47]\n\t" \
  "s_add_u32 s96, s38, 16384\n\t" \
  "ds_read_b128 a[232:235], v222 offset:24576\n\t" \
  "v_permlane16_swap_b32 v113, v115\n\t" \
  "v_exp_f32 v10, v10\n\t" \
  "v_mfma_f32_16x16x32_bf16 v[44:47], a[60:63], v[60:63], v[44:47]\n\t" \
  "v_cvt_pk_bf16_f32 v128, v0, v1\n\t" \
  "v_exp_f32 v11, v11\n\t" \
  "v_exp_f32 v12, v12\n\t" \
  "v_mfma_f32_16x16x32_bf16 v[96:99], a[0:3], v[112:115], 0\n\t" \
  "ds_read_b128 a[236:239], v223 offset:24576\n\t" \
  "v_exp_f32 v13, v13\n\t" \
  "v_exp_f32 v14, v14\n\t" \
  "v_mfma_f32_16x16x32_bf16 v[96:99], a[4:7], v[116:119], v[96:99]\n\t" \
  "buffer_load_dwordx4 v230, s[64:67], s96 offen offset:0 lds\n\t" \
  "v_max3_f32 v203, v203, v44, v45\n\t" \
  "v_max3_f32 v192, v192, v46, v47\n\t" \
  "v_max_f32 v192, v192, v203\n\t" \
  "v_mfma_f32_16x16x32_bf16 v[96:99], a[8:11], v[120:123], v[96:99]\n\t" \
  "v_cvt_pk_bf16_f32 v129, v2, v3\n\t" \
  "v_cvt_pk_bf16_f32 v130, v4, v5\n\t" \
  "v_cmp_lt_f32 vcc, 0x42800000, v192\n\t" \
  "v_exp_f32 v15, v15\n\t" \
  "v_mfma_f32_16x16x32_bf16 v[96:99], a[12:15], v[124:127], v[96:99]\n\t" \
  "ds_read_b128 a[240:243], v220 offset:28672\n\t" \
  "s_cbranch_vccnz .Lsf_c01\n\t" \
  ".Lsf_r01:\n\t" \
  "v_mfma_f32_16x16x32_bf16 v[68:71], a[16:19], v[80:83], v[184:187]\n\t" \
  "v_exp_f32 v32, v32\n\t" \
  "v_exp_f32 v33, v33\n\t" \
  "v_mfma_f32_16x16x32_bf16 v[68:71], a[20:23], v[84:87], v[68:71]\n\t" \
  "v_cvt_pk_bf16_f32 v131, v6, v7\n\t" \
  "s_nop 0\n\t" \
  "v_max3_f32 v194, v96, v97, v98\n\t" \
  "v_max_f32 v194, v194, v99\n\t" \
  "v_mov_b32 v195, v194\n\t" \
  "v_mfma_f32_16x16x32_bf16 v[68:71], a[24:27], v[88:91], v[68:71]\n\t" \
  "s_nop 0\n\t" \
  "v_permlane32_swap_b32 v194, v195\n\t" \
  "v_max_f32 v194, v194, v195\n\t" \
  "v_mov_b32 v195, v194\n\t" \
  "v_mfma_f32_16x16x32_bf16 v[68:71], a[28:31], v[92:95], v[68:71]\n\t" \
  "ds_read_b128 a[244:247], v221 offset:28672\n\t" \
  "v_permlane16_swap_b32 v194, v195\n\t" \
  "v_max_f32 v194, v194, v195\n\t" \
  "v_sub_f32 v188, 0xc2000000, v194\n\t" \
  "v_mfma_f32_16x16x32_bf16 v[72:75], a[32:35], v[80:83], v[184:187]\n\t" \
  "v_sub_f32 v189, 0xc2000000, v194\n\t" \
  "v_sub_f32 v190, 0xc2000000, v194\n\t" \
  "v_exp_f32 v34, v34\n\t" \
  "v_max3_f32 v192, v68, v69, v70\n\t" \
  "v_mfma_f32_16x16x32_bf16 v[72:75], a[36:39], v[84:87], v[72:75]\n\t" \
  "v_sub_f32 v191, 0xc2000000, v194\n\t" \
  "v_exp_f32 v35, v35\n\t" \
  "v_exp_f32 v36, v36\n\t" \
  "v_mfma_f32_16x16x32_bf16 v[72:75], a[40:43], v[88:91], v[72:75]\n\t" \
  "buffer_load_dwordx4 v231, s[64:67], s96 offen offset:1024 lds\n\t" \
  "v_exp_f32 v37, v37\n\t" \
  "v_mfma_f32_16x16x32_bf16 v[72:75], a[44:47], v[92:95], v[72:75]\n\t" \
  "v_add_f32 v96, v96, v188\n\t" \
  "v_exp_f32 v38, v38\n\t" \
  "v_exp_f32 v39, v39\n\t" \
  "v_mfma_f32_16x16x32_bf16 v[76:79], a[48:51], v[80:83], v[184:187]\n\t" \
  "v_add_f32 v97, v97, v188\n\t" \
  "v_exp_f32 v40, v40\n\t" \
  "v_exp_f32 v41, v41\n\t" \
  "v_mfma_f32_16x16x32_bf16 v[76:79], a[52:55], v[84:87], v[76:79]\n\t" \
  "v_add_f32 v98, v98, v188\n\t" \
  "v_max3_f32 v203, v71, v72, v73\n\t" \
  "v_max3_f32 v192, v192, v74, v75\n\t" \
  "v_exp_f32 v42, v42\n\t" \
  "v_mfma_f32_16x16x32_bf16 v[76:79], a[56:59], v[88:91], v[76:79]\n\t" \
  "ds_read_b128 a[248:251], v222 offset:28672\n\t" \
  "v_exp_f32 v43, v43\n\t" \
  "v_exp_f32 v44, v44\n\t" \
  "v_mfma_f32_16x16x32_bf16 v[76:79], a[60:63], v[92:95], v[76:79]\n\t" \
  "v_add_f32 v99, v99, v188\n\t" \
  "v_exp_f32 v45, v45\n\t" \
  "v_exp_f32 v46, v46\n\t" \
  "v_mfma_f32_16x16x32_bf16 v[100:103], a[16:19], v[112:115], v[188:191]\n\t" \
  "ds_read_b128 a[252:255], v223 offset:28672\n\t" \
  "v_cvt_pk_bf16_f32 v132, v8, v9\n\t" \
  "v_exp_f32 v47, v47\n\t" \
  "v_mfma_f32_16x16x32_bf16 v[100:103], a[20:23], v[116:119], v[100:103]\n\t" \
  "v_max3_f32 v203, v203, v76, v77\n\t" \
  "v_max3_f32 v192, v192, v78, v79\n\t" \
  "v_max_f32 v192, v192, v203\n\t" \
  "v_cmp_lt_f32 vcc, 0x42800000, v192\n\t" \
  "v_mfma_f32_16x16x32_bf16 v[100:103], a[24:27], v[120:123], v[100:103]\n\t" \
  "buffer_load_dwordx4 v230, s[64:67], s96 offen offset:2048 lds\n\t" \
  "v_mfma_f32_16x16x32_bf16 v[100:103], a[28:31], v[124:127], v[100:103]\n\t" \
  "s_cbranch_vccnz .Lsf_c02\n\t" \
  ".Lsf_r02:\n\t" \
  "v_mfma_f32_16x16x32_bf16 v[104:107], a[32:35], v[112:115], v[188:191]\n\t" \
  "v_cvt_pk_bf16_f32 v133, v10, v11\n\t" \
  "v_exp_f32 v64, v64\n\t" \
  "v_exp_f32 v65, v65\n\t" \
  "v_mfma_f32_16x16x32_bf16 v[104:107], a[36:39], v[116:119], v[104:107]\n\t" \
  "v_cvt_pk_bf16_f32 v134, v12, v13\n\t" \
  "v_cvt_pk_bf16_f32 v135, v14, v15\n\t" \
  "v_exp_f32 v66, v66\n\t" \
  "v_mfma_f32_16x16x32_bf16 v[104:107], a[40:43], v[120:123], v[104:107]\n\t" \
  "v_cvt_pk_bf16_f32 v136, v32, v33\n\t" \
  "v_exp_f32 v67, v67\n\t" \
  "v_max3_f32 v192, v100, v101, v102\n\t" \
  "v_mfma_f32_16x16x32_bf16 v[104:107], a[44:47], v[124:127], v[104:107]\n\t" \
  "v_cvt_pk_bf16_f32 v137, v34, v35\n\t" \
  "v_exp_f32 v68, v68\n\t" \
  "v_mfma_f32_16x16x32_bf16 v[108:111], a[48:51], v[112:115], v[188:191]\n\t" \
  "v_exp_f32 v69, v69\n\t" \
  "v_mfma_f32_16x16x32_bf16 v[108:111], a[52:55], v[116:119], v[108:111]\n\t" \
  "v_cvt_pk_bf16_f32 v138, v36, v37\n\t" \
  "v_exp_f32 v70, v70\n\t" \
  "v_mfma_f32_16x16x32_bf16 v[108:111], a[56:59], v[120:123], v[108:111]\n\t" \
  "v_exp_f32 v71, v71\n\t" \
  "v_max3_f32 v203, v103, v104, v105\n\t" \
  "v_max3_f32 v192, v192, v106, v107\n\t" \
  "v_mfma_f32_16x16x32_bf16 v[108:111], a[60:63], v[124:127], v[108:111]\n\t" \
  "s_waitcnt lgkmcnt(0)\n\t" \
  "buffer_load_dwordx4 v231, s[64:67], s96 offen offset:3072 lds\n\t" \
  "v_mfma_f32_16x16x32_bf16 v[0:3], a[192:195], v[16:19], v[176:179]\n\t" \
  "ds_read_b64_tr_b16 a[128:129], v228 offset:0\n\t" \
  "v_cvt_pk_bf16_f32 v139, v38, v39\n\t" \
  "v_mfma_f32_16x16x32_bf16 v[0:3], a[196:199], v[20:23], v[0:3]\n\t" \
  "v_exp_f32 v72, v72\n\t" \
  "v_mfma_f32_16x16x32_bf16 v[0:3], a[200:203], v[24:27], v[0:3]\n\t" \
  "v_max3_f32 v203, v203, v108, v109\n\t" \
  "v_max3_f32 v192, v192, v110, v111\n\t" \
  "v_mfma_f32_16x16x32_bf16 v[0:3], a[204:207], v[28:31], v[0:3]\n\t" \
  "v_max_f32 v192, v192, v203\n\t" \
  "ds_read_b64_tr_b16 a[130:131], v228 offset:4096\n\t" \
  "v_cmp_lt_f32 vcc, 0x42800000, v192\n\t" \
  "v_mfma_f32_16x16x32_bf16 v[4:7], a[220:223], v[28:31], v[176:179]\n\t" \
  "ds_read_b64_tr_b16 a[132:133], v228 offset:8192\n\t" \
  "v_exp_f32 v73, v73\n\t" \
  "v_mfma_f32_16x16x32_bf16 v[4:7], a[216:219], v[24:27], v[4:7]\n\t" \
  "s_cbranch_vccnz .Lsf_c03\n\t" \
  ".Lsf_r03:\n\t" \
  "v_mfma_f32_16x16x32_bf16 v[4:7], a[212:215], v[20:23], v[4:7]\n\t" \
  "s_cmp_lg_u32 s99, 0\n\t" \
  "s_cbranch_scc1 .Lsf_p0\n\t" \
  ".Lsf_q0:\n\t" \
  "v_mfma_f32_16x16x32_bf16 v[4:7], a[208:211], v[16:19], v[4:7]\n\t" \
  "ds_read_b64_tr_b16 a[134:135], v228 offset:12288\n\t" \
  "v_max3_f32 v192, v0, v1, v2\n\t" \
  "v_mfma_f32_16x16x32_bf16 v[8:11], a[224:227], v[16:19], v[176:179]\n\t" \
  "ds_read_b64_tr_b16 a[136:137], v229 offset:0\n\t" \
  "v_exp_f32 v74, v74\n\t" \
  "v_mfma_f32_16x16x32_bf16 v[8:11], a[228:231], v[20:23], v[8:11]\n\t" \
  "ds_read_b64_tr_b16 a[138:139], v229 offset:4096\n\t" \
  "v_cvt_pk_bf16_f32 v140, v40, v41\n\t" \
  "v_mfma_f32_16x16x32_bf16 v[8:11], a[232:235], v[24:27], v[8:11]\n\t" \
  "v_max3_f32 v203, v3, v4, v5\n\t" \
  "v_max3_f32 v192, v192, v6, v7\n\t" \
  "v_mfma_f32_16x16x32_bf16 v[8:11], a[236:239], v[28:31], v[8:11]\n\t" \
  "ds_read_b64_tr_b16 a[140:141], v229 offset:8192\n\t" \
  "v_exp_f32 v75, v75\n\t" \
  "v_mfma_f32_16x16x32_bf16 v[12:15], a[252:255], v[28:31], v[176:179]\n\t" \
  "v_exp_f32 v76, v76\n\t" \
  "v_mfma_f32_16x16x32_bf16 v[12:15], a[248:251], v[24:27], v[12:15]\n\t" \
  "ds_read_b64_tr_b16 a[142:143], v229 offset:12288\n\t" \
  "v_cvt_pk_bf16_f32 v141, v42, v43\n\t" \
  "v_mfma_f32_16x16x32_bf16 v[12:15], a[244:247], v[20:23], v[12:15]\n\t" \
  "v_max3_f32 v203, v203, v8, v9\n\t" \
  "v_max3_f32 v192, v192, v10, v11\n\t" \
  "v_mfma_f32_16x16x32_bf16 v[12:15], a[240:243], v[16:19], v[12:15]\n\t" \
  "ds_read_b64_tr_b16 a[144:145], v228 offset:256\n\t" \
  "v_exp_f32 v77, v77\n\t" \
  "v_mfma_f32_16x16x32_bf16 v[32:35], a[192:195], v[48:51], v[180:183]\n\t" \
  "ds_read_b64_tr_b16 a[146:147], v228 offset:4352\n\t" \
  "v_cvt_pk_bf16_f32 v142, v44, v45\n\t" \
  "v_mfma_f32_16x16x32_bf16 v[32:35], a[196:199], v[52:55], v[32:35]\n\t" \
  "v_cvt_pk_bf16_f32 v143, v46, v47\n\t" \
  "v_exp_f32 v78, v78\n\t" \
  "v_mfma_f32_16x16x32_bf16 v[32:35], a[200:203], v[56:59], v[32:35]\n\t" \
  "v_max3_f32 v203, v203, v12, v13\n\t" \
  "v_max3_f32 v192, v192, v14, v15\n\t" \
  "v_mfma_f32_16x16x32_bf16 v[32:35], a[204:207], v[60:63], v[32:35]\n\t" \
  "v_max_f32 v192, v192, v203\n\t" \
  "v_cmp_lt_f32 vcc, 0x42800000, v192\n\t" \
  "v_mfma_f32_16x16x32_bf16 v[36:39], a[220:223], v[60:63], v[180:183]\n\t" \
  "ds_read_b64_tr_b16 a[148:149], v228 offset:8448\n\t" \
  "v_cvt_pk_bf16_f32 v144, v64, v65\n\t" \
  "v_mfma_f32_16x16x32_bf16 v[36:39], a[216:219], v[56:59], v[36:39]\n\t" \
  "s_cbranch_vccnz .Lsf_c10\n\t" \
  ".Lsf_r10:\n\t" \
  "v_mfma_f32_16x16x32_bf16 v[36:39], a[212:215], v[52:55], v[36:39]\n\t" \
  "v_max3_f32 v192, v32, v33, v34\n\t" \
  "v_exp_f32 v79, v79\n\t" \
  "v_mfma_f32_16x16x32_bf16 v[36:39], a[208:211], v[48:51], v[36:39]\n\t" \
  "ds_read_b64_tr_b16 a[150:151], v228 offset:12544\n\t" \
  "v_exp_f32 v96, v96\n\t" \
  "v_mfma_f32_16x16x32_bf16 v[40:43], a[224:227], v[48:51], v[180:183]\n\t" \
  "ds_read_b64_tr_b16 a[152:153], v229 offset:256\n\t" \
  "v_exp_f32 v97, v97\n\t" \
  "v_mfma_f32_16x16x32_bf16 v[40:43], a[228:231], v[52:55], v[40:43]\n\t" \
  "ds_read_b64_tr_b16 a[154:155], v229 offset:4352\n\t" \
  "v_cvt_pk_bf16_f32 v145, v66, v67\n\t" \
  "v_mfma_f32_16x16x32_bf16 v[40:43], a[232:235], v[56:59], v[40:43]\n\t" \
  "v_max3_f32 v203, v35, v36, v37\n\t" \
  "v_max3_f32 v192, v192, v38, v39\n\t" \
  "v_mfma_f32_16x16x32_bf16 v[40:43], a[236:239], v[60:63], v[40:43]\n\t" \
  "ds_read_b64_tr_b16 a[156:157], v229 offset:8448\n\t" \
  "v_exp_f32 v98, v98\n\t" \
  "v_mfma_f32_16x16x32_bf16 v[44:47], a[252:255], v[60:63], v[180:183]\n\t" \
  "ds_read_b64_tr_b16 a[158:159], v229 offset:12544\n\t" \
  "v_exp_f32 v99, v99\n\t" \
  "v_mfma_f32_16x16x32_bf16 v[44:47], a[248:251], v[56:59], v[44:47]\n\t" \
  "ds_read_b64_tr_b16 a[160:161], v228 offset:512\n\t" \
  "v_cvt_pk_bf16_f32 v146, v68, v69\n\t" \
  "v_mfma_f32_16x16x32_bf16 v[44:47], a[244:247], v[52:55], v[44:47]\n\t" \
  "v_max3_f32 v203, v203, v40, v41\n\t" \
  "v_max3_f32 v192, v192, v42, v43\n\t" \
  "v_mfma_f32_16x16x32_bf16 v[44:47], a[240:243], v[48:51], v[44:47]\n\t" \
  "v_cvt_pk_bf16_f32 v147, v70, v71\n\t" \
  "v_exp_f32 v100, v100\n\t" \
  "v_mfma_f32_16x16x32_bf16 v[64:67], a[192:195], v[80:83], v[184:187]\n\t" \
  "ds_read_b64_tr_b16 a[162:163], v228 offset:4608\n\t" \
  "v_exp_f32 v101, v101\n\t" \
  "v_mfma_f32_16x16x32_bf16 v[64:67], a[196:199], v[84:87], v[64:67]\n\t" \
  "ds_read_b64_tr_b16 a[164:165], v228 offset:8704\n\t" \
  "v_cvt_pk_bf16_f32 v148, v72, v73\n\t" \
  "v_mfma_f32_16x16x32_bf16 v[64:67], a[200:203], v[88:91], v[64:67]\n\t" \
  "v_max3_f32 v203, v203, v44, v45\n\t" \
  "v_max3_f32 v192, v192, v46, v47\n\t" \
  "v_mfma_f32_16x16x32_bf16 v[64:67], a[204:207], v[92:95], v[64:67]\n\t" \
  "v_max_f32 v192, v192, v203\n\t" \
  "v_cmp_lt_f32 vcc, 0x42800000, v192\n\t" \
  "v_mfma_f32_16x16x32_bf16 v[68:71], a[220:223], v[92:95], v[184:187]\n\t" \
  "ds_read_b64_tr_b16 a[166:167], v228 offset:12800\n\t" \
  "v_exp_f32 v102, v102\n\t" \
  "v_mfma_f32_16x16x32_bf16 v[68:71], a[216:219], v[88:91], v[68:71]\n\t" \
  "s_cbranch_vccnz .Lsf_c11\n\t" \
  ".Lsf_r11:\n\t" \
  "v_mfma_f32_16x16x32_bf16 v[68:71], a[212:215], v[84:87], v[68:71]\n\t" \
  "v_max3_f32 v192, v64, v65, v66\n\t" \
  "v_exp_f32 v103, v103\n\t" \
  "v_mfma_f32_16x16x32_bf16 v[68:71], a[208:211], v[80:83], v[68:71]\n\t" \
  "ds_read_b64_tr_b16 a[168:169], v229 offset:512\n\t" \
  "v_exp_f32 v104, v104\n\t" \
  "v_cvt_pk_bf16_f32 v149, v74, v75\n\t" \
  "v_mfma_f32_16x16x32_bf16 v[72:75], a[224:227], v[80:83], v[184:187]\n\t" \
  "ds_read_b64_tr_b16 a[170:171], v229 offset:4608\n\t" \
  "v_exp_f32 v105, v105\n\t" \
  "v_mfma_f32_16x16x32_bf16 v[72:75], a[228:231], v[84:87], v[72:75]\n\t" \
  "ds_read_b64_tr_b16 a[172:173], v229 offset:8704\n\t" \
  "v_max3_f32 v203, v67, v68, v69\n\t" \
  "v_mfma_f32_16x16x32_bf16 v[72:75], a[232:235], v[88:91], v[72:75]\n\t" \
  "v_max3_f32 v192, v192, v70, v71\n\t" \
  "v_exp_f32 v106, v106\n\t" \
  "v_mfma_f32_16x16x32_bf16 v[72:75], a[236:239], v[92:95], v[72:75]\n\t" \
  "ds_read_b64_tr_b16 a[174:175], v229 offset:12800\n\t" \
  "v_cvt_pk_bf16_f32 v150, v76, v77\n\t" \
  "v_cvt_pk_bf16_f32 v151, v78, v79\n\t" \
  "v_mfma_f32_16x16x32_bf16 v[76:79], a[252:255], v[92:95], v[184:187]\n\t" \
  "ds_read_b64_tr_b16 a[176:177], v228 offset:768\n\t" \
  "v_exp_f32 v107, v107\n\t" \
  "v_mfma_f32_16x16x32_bf16 v[76:79], a[248:251], v[88:91], v[76:79]\n\t" \
  "v_cvt_pk_bf16_f32 v152, v96, v97\n\t" \
  "v_max3_f32 v203, v203, v72, v73\n\t" \
  "v_mfma_f32_16x16x32_bf16 v[76:79], a[244:247], v[84:87], v[76:79]\n\t" \
  "v_max3_f32 v192, v192, v74, v75\n\t" \
  "v_exp_f32 v108, v108\n\t" \
  "v_mfma_f32_16x16x32_bf16 v[76:79], a[240:243], v[80:83], v[76:79]\n\t" \
  "ds_read_b64_tr_b16 a[178:179], v228 offset:4864\n\t" \
  "v_cvt_pk_bf16_f32 v153, v98, v99\n\t" \
  "v_mfma_f32_16x16x32_bf16 v[96:99], a[192:195], v[112:115], v[188:191]\n\t" \
  "ds_read_b64_tr_b16 a[180:181], v228 offset:8960\n\t" \
  "v_exp_f32 v109, v109\n\t" \
  "v_mfma_f32_16x16x32_bf16 v[96:99], a[196:199], v[116:119], v[96:99]\n\t" \
  "v_cvt_pk_bf16_f32 v154, v100, v101\n\t" \
  "v_cvt_pk_bf16_f32 v155, v102, v103\n\t" \
  "v_mfma_f32_16x16x32_bf16 v[96:99], a[200:203], v[120:123], v[96:99]\n\t" \
  "v_max3_f32 v203, v203, v76, v77\n\t" \
  "v_max3_f32 v192, v192, v78, v79\n\t" \
  "v_mfma_f32_16x16x32_bf16 v[96:99], a[204:207], v[124:127], v[96:99]\n\t" \
  "v_max_f32 v192, v192, v203\n\t" \
  "v_cmp_lt_f32 vcc, 0x42800000, v192\n\t" \
  "v_mfma_f32_16x16x32_bf16 v[100:103], a[220:223], v[124:127], v[188:191]\n\t" \
  "ds_read_b64_tr_b16 a[182:183], v228 offset:13056\n\t" \
  "v_exp_f32 v110, v110\n\t" \
  "v_mfma_f32_16x16x32_bf16 v[100:103], a[216:219], v[120:123], v[100:103]\n\t" \
  "s_cbranch_vccnz .Lsf_c12\n\t" \
  ".Lsf_r12:\n\t" \
  "v_mfma_f32_16x16x32_bf16 v[100:103], a[212:215], v[116:119], v[100:103]\n\t" \
  "v_max3_f32 v192, v96, v97, v98\n\t" \
  "v_exp_f32 v111, v111\n\t" \
  "v_cvt_pk_bf16_f32 v156, v104, v105\n\t" \
  "v_mfma_f32_16x16x32_bf16 v[100:103], a[208:211], v[112:115], v[100:103]\n\t" \
  "ds_read_b64_tr_b16 a[184:185], v229 offset:768\n\t" \
  "v_cvt_pk_bf16_f32 v157, v106, v107\n\t" \
  "v_mfma_f32_16x16x32_bf16 v[104:107], a[224:227], v[112:115], v[188:191]\n\t" \
  "ds_read_b64_tr_b16 a[186:187], v229 offset:4864\n\t" \
  "v_cvt_pk_bf16_f32 v158, v108, v109\n\t" \
  "v_mfma_f32_16x16x32_bf16 v[104:107], a[228:231], v[116:119], v[104:107]\n\t" \
  "ds_read_b64_tr_b16 a[188:189], v229 offset:8960\n\t" \
  "v_cvt_pk_bf16_f32 v159, v110, v111\n\t" \
  "v_mfma_f32_16x16x32_bf16 v[104:107], a[232:235], v[120:123], v[104:107]\n\t" \
  "v_max3_f32 v203, v99, v100, v101\n\t" \
  "v_max3_f32 v192, v192, v102, v103\n\t" \
  "v_mfma_f32_16x16x32_bf16 v[104:107], a[236:239], v[124:127], v[104:107]\n\t" \
  "ds_read_b64_tr_b16 a[190:191], v229 offset:13056\n\t" \
  "s_nop 0\n\t" \
  "v_mfma_f32_16x16x32_bf16 v[108:111], a[252:255], v[124:127], v[188:191]\n\t" \
  "v_exp_f32 v0, v0\n\t" \
  "v_mfma_f32_16x16x32_bf16 v[108:111], a[248:251], v[120:123], v[108:111]\n\t" \
  "v_exp_f32 v1, v1\n\t" \
  "v_mfma_f32_16x16x32_bf16 v[108:111], a[244:247], v[116:119], v[108:111]\n\t" \
  "s_waitcnt vmcnt(0) lgkmcnt(0)\n\t" \
  "v_max3_f32 v203, v203, v104, v105\n\t" \
  "v_max3_f32 v192, v192, v106, v107\n\t" \
  "v_mfma_f32_16x16x32_bf16 v[108:111], a[240:243], v[112:115], v[108:111]\n\t" \
  "s_barrier\n\t" \
  "v_mfma_f32_16x16x32_bf16 a[0:3], a[128:131], v[128:131], 0\n\t" \
  "v_exp_f32 v2, v2\n\t" \
  "v_mfma_f32_16x16x32_bf16 a[0:3], a[132:135], v[132:135], a[0:3]\n\t" \
  "v_exp_f32 v3, v3\n\t" \
  "v_mfma_f32_16x16x32_bf16 a[4:7], a[136:139], v[128:131], 0\n\t" \
  "v_exp_f32 v4, v4\n\t" \
  "v_mfma_f32_16x16x32_bf16 a[4:7], a[140:143], v[132:135], a[4:7]\n\t" \
  "v_max3_f32 v203, v203, v108, v109\n\t" \
  "v_max3_f32 v192, v192, v110, v111\n\t" \
  "v_mfma_f32_16x16x32_bf16 a[8:11], a[144:147], v[128:131], 0\n\t" \
  "v_max_f32 v192, v192, v203\n\t" \
  "v_cmp_lt_f32 vcc, 0x42800000, v192\n\t" \
  "v_mfma_f32_16x16x32_bf16 a[8:11], a[148:151], v[132:135], a[8:11]\n\t" \
  "v_exp_f32 v5, v5\n\t" \
  "v_mfma_f32_16x16x32_bf16 a[12:15], a[152:155], v[128:131], 0\n\t" \
  "s_cbranch_vccnz .Lsf_c13\n\t" \
  ".Lsf_r13:\n\t" \
  "v_mfma_f32_16x16x32_bf16 a[12:15], a[156:159], v[132:135], a[12:15]\n\t" \
  "s_add_u32 m0, s38, 65536\n\t" \
  "v_exp_f32 v6, v6\n\t" \
  "v_mfma_f32_16x16x32_bf16 a[16:19], a[160:163], v[128:131], 0\n\t" \
  "s_add_u32 s96, s38, 49152\n\t" \
  "v_exp_f32 v7, v7\n\t" \
  "v_mfma_f32_16x16x32_bf16 a[16:19], a[164:167], v[132:135], a[16:19]\n\t" \
  "v_exp_f32 v8, v8\n\t" \
  "v_mfma_f32_16x16x32_bf16 a[20:23], a[168:171], v[128:131], 0\n\t" \
  "buffer_load_dwordx4 v224, s[60:63], s96 offen offset:0 lds\n\t" \
  "v_mfma_f32_16x16x32_bf16 a[20:23], a[172:175], v[132:135], a[20:23]\n\t" \
  "v_exp_f32 v9, v9\n\t" \
  "v_mfma_f32_16x16x32_bf16 a[24:27], a[176:179], v[128:131], 0\n\t" \
  "v_exp_f32 v10, v10\n\t" \
  "v_mfma_f32_16x16x32_bf16 a[24:27], a[180:183], v[132:135], a[24:27]\n\t" \
  "v_exp_f32 v11, v11\n\t" \
  "v_mfma_f32_16x16x32_bf16 a[28:31], a[184:187], v[128:131], 0\n\t" \
  "v_exp_f32 v12, v12\n\t" \
  "v_mfma_f32_16x16x32_bf16 a[28:31], a[188:191], v[132:135], a[28:31]\n\t" \
  "v_exp_f32 v13, v13\n\t" \
  "v_smfmac_f32_16x16x64_bf16 v[160:163], v[216:219], v[128:135], v208\n\t" \
  "ds_read_b128 a[192:195], v220 offset:0\n\t" \
  "v_cvt_pk_bf16_f32 v128, v0, v1\n\t" \
  "v_mfma_f32_16x16x32_bf16 a[32:35], a[128:131], v[136:139], 0\n\t" \
  "buffer_load_dwordx4 v225, s[60:63], s96 offen offset:1024 lds\n\t" \
  "v_mfma_f32_16x16x32_bf16 a[32:35], a[132:135], v[140:143], a[32:35]\n\t" \
  "v_exp_f32 v14, v14\n\t" \
  "v_mfma_f32_16x16x32_bf16 a[36:39], a[136:139], v[136:139], 0\n\t" \
  "v_exp_f32 v15, v15\n\t" \
  "v_mfma_f32_16x16x32_bf16 a[36:39], a[140:143], v[140:143], a[36:39]\n\t" \
  "ds_read_b128 a[196:199], v221 offset:0\n\t" \
  "v_cvt_pk_bf16_f32 v129, v2, v3\n\t" \
  "v_mfma_f32_16x16x32_bf16 a[40:43], a[144:147], v[136:139], 0\n\t" \
  "v_exp_f32 v32, v32\n\t" \
  "v_mfma_f32_16x16x32_bf16 a[40:43], a[148:151], v[140:143], a[40:43]\n\t" \
  "v_exp_f32 v33, v33\n\t" \
  "v_mfma_f32_16x16x32_bf16 a[44:47], a[152:155], v[136:139], 0\n\t" \
  "buffer_load_dwordx4 v226, s[60:63], s96 offen offset:2048 lds\n\t" \
  "v_mfma_f32_16x16x32_bf16 a[44:47], a[156:159], v[140:143], a[44:47]\n\t" \
  "ds_read_b128 a[200:203], v222 offset:0\n\t" \
  "v_cvt_pk_bf16_f32 v130, v4, v5\n\t" \
  "v_mfma_f32_16x16x32_bf16 a[48:51], a[160:163], v[136:139], 0\n\t" \
  "v_exp_f32 v34, v34\n\t" \
  "v_mfma_f32_16x16x32_bf16 a[48:51], a[164:167], v[140:143], a[48:51]\n\t" \
  "ds_read_b128 a[204:207], v223 offset:0\n\t" \
  "v_cvt_pk_bf16_f32 v131, v6, v7\n\t" \
  "v_mfma_f32_16x16x32_bf16 a[52:55], a[168:171], v[136:139], 0\n\t" \
  "v_exp_f32 v35, v35\n\t" \
  "v_mfma_f32_16x16x32_bf16 a[52:55], a[172:175], v[140:143], a[52:55]\n\t" \
  "ds_read_b128 a[208:211], v220 offset:4096\n\t" \
  "v_cvt_pk_bf16_f32 v132, v8, v9\n\t" \
  "v_mfma_f32_16x16x32_bf16 a[56:59], a[176:179], v[136:139], 0\n\t" \
  "buffer_load_dwordx4 v227, s[60:63], s96 offen offset:3072 lds\n\t" \
  "v_mfma_f32_16x16x32_bf16 a[56:59], a[180:183], v[140:143], a[56:59]\n\t" \
  "v_exp_f32 v36, v36\n\t" \
  "v_mfma_f32_16x16x32_bf16 a[60:63], a[184:187], v[136:139], 0\n\t" \
  "s_add_u32 m0, s38, 81920\n\t" \
  "ds_read_b128 a[212:215], v221 offset:4096\n\t" \
  "v_cvt_pk_bf16_f32 v133, v10, v11\n\t" \
  "v_mfma_f32_16x16x32_bf16 a[60:63], a[188:191], v[140:143], a[60:63]\n\t" \
  "s_add_u32 s96, s38, 32768\n\t" \
  "v_exp_f32 v37, v37\n\t" \
  "v_smfmac_f32_16x16x64_bf16 v[162:165], v[216:219], v[136:143], v208\n\t" \
  "ds_read_b128 a[216:219], v222 offset:4096\n\t" \
  "v_cvt_pk_bf16_f32 v134, v12, v13\n\t" \
  "v_mfma_f32_16x16x32_bf16 a[64:67], a[128:131], v[144:147], 0\n\t" \
  "v_exp_f32 v38, v38\n\t" \
  "v_mfma_f32_16x16x32_bf16 a[64:67], a[132:135], v[148:151], a[64:67]\n\t" \
  "buffer_load_dwordx4 v230, s[64:67], s96 offen offset:0 lds\n\t" \
  "v_mfma_f32_16x16x32_bf16 a[68:71], a[136:139], v[144:147], 0\n\t" \
  "ds_read_b128 a[220:223], v223 offset:4096\n\t" \
  "v_cvt_pk_bf16_f32 v135, v14, v15\n\t" \
  "v_mfma_f32_16x16x32_bf16 a[68:71], a[140:143], v[148:151], a[68:71]\n\t" \
  "v_exp_f32 v39, v39\n\t" \
  "v_mfma_f32_16x16x32_bf16 a[72:75], a[144:147], v[144:147], 0\n\t" \
  "v_exp_f32 v40, v40\n\t" \
  "v_mfma_f32_16x16x32_bf16 a[72:75], a[148:151], v[148:151], a[72:75]\n\t" \
  "ds_read_b128 a[224:227], v220 offset:8192\n\t" \
  "v_cvt_pk_bf16_f32 v136, v32, v33\n\t" \
  "v_mfma_f32_16x16x32_bf16 a[76:79], a[152:155], v[144:147], 0\n\t" \
  "ds_read_b128 a[228:231], v221 offset:8192\n\t" \
  "v_cvt_pk_bf16_f32 v137, v34, v35\n\t" \
  "v_mfma_f32_16x16x32_bf16 a[76:79], a[156:159], v[148:151], a[76:79]\n\t" \
  "v_exp_f32 v41, v41\n\t" \
  "v_mfma_f32_16x16x32_bf16 a[80:83], a[160:163], v[144:147], 0\n\t" \
  "buffer_load_dwordx4 v231, s[64:67], s96 offen offset:1024 lds\n\t" \
  "v_mfma_f32_16x16x32_bf16 a[80:83], a[164:167], v[148:151], a[80:83]\n\t" \
  "v_exp_f32 v42, v42\n\t" \
  "v_mfma_f32_16x16x32_bf16 a[84:87], a[168:171], v[144:147], 0\n\t" \
  "ds_read_b128 a[232:235], v222 offset:8192\n\t" \
  "v_cvt_pk_bf16_f32 v138, v36, v37\n\t" \
  "v_mfma_f32_16x16x32_bf16 a[84:87], a[172:175], v[148:151], a[84:87]\n\t" \
  "v_exp_f32 v43, v43\n\t" \
  "v_mfma_f32_16x16x32_bf16 a[88:91], a[176:179], v[144:147], 0\n\t" \
  "ds_read_b128 a[236:239], v223 offset:8192\n\t" \
  "v_cvt_pk_bf16_f32 v139, v38, v39\n\t" \
  "v_mfma_f32_16x16x32_bf16 a[88:91], a[180:183], v[148:151], a[88:91]\n\t" \
  "v_exp_f32 v44, v44\n\t" \
  "v_mfma_f32_16x16x32_bf16 a[92:95], a[184:187], v[144:147], 0\n\t" \
  "buffer_load_dwordx4 v230, s[64:67], s96 offen offset:2048 lds\n\t" \
  "v_mfma_f32_16x16x32_bf16 a[92:95], a[188:191], v[148:151], a[92:95]\n\t" \
  "ds_read_b128 a[240:243], v220 offset:12288\n\t" \
  "v_cvt_pk_bf16_f32 v140, v40, v41\n\t" \
  "v_smfmac_f32_16x16x64_bf16 v[164:167], v[216:219], v[144:151], v208\n\t" \
  "ds_read_b128 a[244:247], v221 offset:12288\n\t" \
  "v_cvt_pk_bf16_f32 v141, v42, v43\n\t" \
  "v_mfma_f32_16x16x32_bf16 a[96:99], a[128:131], v[152:155], 0\n\t" \
  "v_exp_f32 v45, v45\n\t" \
  "v_mfma_f32_16x16x32_bf16 a[96:99], a[132:135], v[156:159], a[96:99]\n\t" \
  "v_exp_f32 v46, v46\n\t" \
  "v_mfma_f32_16x16x32_bf16 a[100:103], a[136:139], v[152:155], 0\n\t" \
  "v_exp_f32 v47, v47\n\t" \
  "v_mfma_f32_16x16x32_bf16 a[100:103], a[140:143], v[156:159], a[100:103]\n\t" \
  "buffer_load_dwordx4 v231, s[64:67], s96 offen offset:3072 lds\n\t" \
  "v_mfma_f32_16x16x32_bf16 a[104:107], a[144:147], v[152:155], 0\n\t" \
  "ds_read_b128 a[248:251], v222 offset:12288\n\t" \
  "v_cvt_pk_bf16_f32 v142, v44, v45\n\t" \
  "v_mfma_f32_16x16x32_bf16 a[104:107], a[148:151], v[156:159], a[104:107]\n\t" \
  "ds_read_b128 a[252:255], v223 offset:12288\n\t" \
  "v_cvt_pk_bf16_f32 v143, v46, v47\n\t" \
  "v_mfma_f32_16x16x32_bf16 a[108:111], a[152:155], v[152:155], 0\n\t" \
  "v_exp_f32 v64, v64\n\t" \
  "v_mfma_f32_16x16x32_bf16 a[108:111], a[156:159], v[156:159], a[108:111]\n\t" \
  "v_exp_f32 v65, v65\n\t" \
  "v_mfma_f32_16x16x32_bf16 a[112:115], a[160:163], v[152:155], 0\n\t" \
  "v_exp_f32 v66, v66\n\t" \
  "v_mfma_f32_16x16x32_bf16 a[112:115], a[164:167], v[156:159], a[112:115]\n\t" \
  "v_exp_f32 v67, v67\n\t" \
  "v_mfma_f32_16x16x32_bf16 a[116:119], a[168:171], v[152:155], 0\n\t" \
  "v_exp_f32 v68, v68\n\t" \
  "v_mfma_f32_16x16x32_bf16 a[116:119], a[172:175], v[156:159], a[116:119]\n\t" \
  "v_exp_f32 v69, v69\n\t" \
  "v_mfma_f32_16x16x32_bf16 a[120:123], a[176:179], v[152:155], 0\n\t" \
  "v_exp_f32 v70, v70\n\t" \
  "v_mfma_f32_16x16x32_bf16 a[120:123], a[180:183], v[156:159], a[120:123]\n\t" \
  "v_exp_f32 v71, v71\n\t" \
  "v_mfma_f32_16x16x32_bf16 a[124:127], a[184:187], v[152:155], 0\n\t" \
  "v_exp_f32 v72, v72\n\t" \
  "v_mfma_f32_16x16x32_bf16 a[124:127], a[188:191], v[156:159], a[124:127]\n\t" \
  "v_exp_f32 v73, v73\n\t" \
  "v_smfmac_f32_16x16x64_bf16 v[166:169], v[216:219], v[152:159], v208\n\t" \
  "s_waitcnt lgkmcnt(12)\n\t" \
  "s_cmp_lg_u32 s99, 0\n\t" \
  "s_cbranch_scc1 .Lsf_p1\n\t" \
  ".Lsf_q1:\n\t" \
  "v_mfma_f32_16x16x32_bf16 v[0:3], a[192:195], v[16:19], v[176:179]\n\t" \
  "ds_read_b64_tr_b16 a[128:129], v210 offset:0\n\t" \
  "v_cvt_pk_bf16_f32 v144, v64, v65\n\t" \
  "v_mfma_f32_16x16x32_bf16 v[0:3], a[196:199], v[20:23], v[0:3]\n\t" \
  "v_exp_f32 v74, v74\n\t" \
  "v_mfma_f32_16x16x32_bf16 v[0:3], a[200:203], v[24:27], v[0:3]\n\t" \
  "ds_read_b64_tr_b16 a[130:131], v210 offset:4096\n\t" \
  "v_cvt_pk_bf16_f32 v145, v66, v67\n\t" \
  "v_mfma_f32_16x16x32_bf16 v[0:3], a[204:207], v[28:31], v[0:3]\n\t" \
  "s_waitcnt lgkmcnt(10)\n\t" \
  "v_exp_f32 v75, v75\n\t" \
  "v_mfma_f32_16x16x32_bf16 v[4:7], a[220:223], v[28:31], v[176:179]\n\t" \
  "ds_read_b64_tr_b16 a[132:133], v210 offset:8192\n\t" \
  "v_cvt_pk_bf16_f32 v146, v68, v69\n\t" \
  "v_mfma_f32_16x16x32_bf16 v[4:7], a[216:219], v[24:27], v[4:7]\n\t" \
  "v_exp_f32 v76, v76\n\t" \
  "v_mfma_f32_16x16x32_bf16 v[4:7], a[212:215], v[20:23], v[4:7]\n\t" \
  "ds_read_b64_tr_b16 a[134:135], v210 offset:12288\n\t" \
  "v_max3_f32 v192, v0, v1, v2\n\t" \
  "v_mfma_f32_16x16x32_bf16 v[4:7], a[208:211], v[16:19], v[4:7]\n\t" \
  "s_waitcnt lgkmcnt(8)\n\t" \
  "ds_read_b64_tr_b16 a[136:137], v211 offset:0\n\t" \
  "v_cvt_pk_bf16_f32 v147, v70, v71\n\t" \
  "v_mfma_f32_16x16x32_bf16 v[8:11], a[224:227], v[16:19], v[176:179]\n\t" \
  "v_exp_f32 v77, v77\n\t" \
  "v_mfma_f32_16x16x32_bf16 v[8:11], a[228:231], v[20:23], v[8:11]\n\t" \
  "ds_read_b64_tr_b16 a[138:139], v211 offset:4096\n\t" \
  "v_cvt_pk_bf16_f32 v148, v72, v73\n\t" \
  "v_mfma_f32_16x16x32_bf16 v[8:11], a[232:235], v[24:27], v[8:11]\n\t" \
  "v_max3_f32 v203, v3, v4, v5\n\t" \
  "v_max3_f32 v192, v192, v6, v7\n\t" \
  "v_mfma_f32_16x16x32_bf16 v[8:11], a[236:239], v[28:31], v[8:11]\n\t" \
  "s_waitcnt lgkmcnt(6)\n\t" \
  "ds_read_b64_tr_b16 a[140:141], v211 offset:8192\n\t" \
  "v_cvt_pk_bf16_f32 v149, v74, v75\n\t" \
  "v_mfma_f32_16x16x32_bf16 v[12:15], a[252:255], v[28:31], v[176:179]\n\t" \
  "v_exp_f32 v78, v78\n\t" \
  "v_mfma_f32_16x16x32_bf16 v[12:15], a[248:251], v[24:27], v[12:15]\n\t" \
  "ds_read_b64_tr_b16 a[142:143], v211 offset:12288\n\t" \
  "v_cvt_pk_bf16_f32 v150, v76, v77\n\t" \
  "v_mfma_f32_16x16x32_bf16 v[12:15], a[244:247], v[20:23], v[12:15]\n\t" \
  "v_max3_f32 v203, v203, v8, v9\n\t" \
  "v_max3_f32 v192, v192, v10, v11\n\t" \
  "v_mfma_f32_16x16x32_bf16 v[12:15], a[240:243], v[16:19], v[12:15]\n\t" \
  "v_exp_f32 v79, v79\n\t" \
  "v_mfma_f32_16x16x32_bf16 v[32:35], a[192:195], v[48:51], v[180:183]\n\t" \
  "ds_read_b64_tr_b16 a[144:145], v210 offset:256\n\t" \
  "v_cvt_pk_bf16_f32 v151, v78, v79\n\t" \
  "v_mfma_f32_16x16x32_bf16 v[32:35], a[196:199], v[52:55], v[32:35]\n\t" \
  "v_exp_f32 v96, v96\n\t" \
  "v_mfma_f32_16x16x32_bf16 v[32:35], a[200:203], v[56:59], v[32:35]\n\t" \
  "ds_read_b64_tr_b16 a[146:147], v210 offset:4352\n\t" \
  "v_max3_f32 v203, v203, v12, v13\n\t" \
  "v_mfma_f32_16x16x32_bf16 v[32:35], a[204:207], v[60:63], v[32:35]\n\t" \
  "v_max3_f32 v192, v192, v14, v15\n\t" \
  "v_max_f32 v192, v192, v203\n\t" \
  "v_mfma_f32_16x16x32_bf16 v[36:39], a[220:223], v[60:63], v[180:183]\n\t" \
  "ds_read_b64_tr_b16 a[148:149], v210 offset:8448\n\t" \
  "v_cmp_lt_f32 vcc, 0x42800000, v192\n\t" \
  "v_mfma_f32_16x16x32_bf16 v[36:39], a[216:219], v[56:59], v[36:39]\n\t" \
  "v_exp_f32 v97, v97\n\t" \
  "v_mfma_f32_16x16x32_bf16 v[36:39], a[212:215], v[52:55], v[36:39]\n\t" \
  "s_cbranch_vccnz .Lsf_c20\n\t" \
  ".Lsf_r20:\n\t" \
  "v_mfma_f32_16x16x32_bf16 v[36:39], a[208:211], v[48:51], v[36:39]\n\t" \
  "ds_read_b64_tr_b16 a[150:151], v210 offset:12544\n\t" \
  "v_max3_f32 v192, v32, v33, v34\n\t" \
  "v_mfma_f32_16x16x32_bf16 v[40:43], a[224:227], v[48:51], v[180:183]\n\t" \
  "ds_read_b64_tr_b16 a[152:153], v211 offset:256\n\t" \
  "v_cvt_pk_bf16_f32 v152, v96, v97\n\t" \
  "v_mfma_f32_16x16x32_bf16 v[40:43], a[228:231], v[52:55], v[40:43]\n\t" \
  "v_exp_f32 v98, v98\n\t" \
  "v_mfma_f32_16x16x32_bf16 v[40:43], a[232:235], v[56:59], v[40:43]\n\t" \
  "v_max3_f32 v203, v35, v36, v37\n\t" \
  "v_max3_f32 v192, v192, v38, v39\n\t" \
  "v_mfma_f32_16x16x32_bf16 v[40:43], a[236:239], v[60:63], v[40:43]\n\t" \
  "v_exp_f32 v99, v99\n\t" \
  "v_mfma_f32_16x16x32_bf16 v[44:47], a[252:255], v[60:63], v[180:183]\n\t" \
  "ds_read_b64_tr_b16 a[154:155], v211 offset:4352\n\t" \
  "v_cvt_pk_bf16_f32 v153, v98, v99\n\t" \
  "v_mfma_f32_16x16x32_bf16 v[44:47], a[248:251], v[56:59], v[44:47]\n\t" \
  "v_exp_f32 v100, v100\n\t" \
  "v_mfma_f32_16x16x32_bf16 v[44:47], a[244:247], v[52:55], v[44:47]\n\t" \
  "ds_read_b64_tr_b16 a[156:157], v211 offset:8448\n\t" \
  "v_max3_f32 v203, v203, v40, v41\n\t" \
  "v_mfma_f32_16x16x32_bf16 v[44:47], a[240:243], v[48:51], v[44:47]\n\t" \
  "ds_read_b64_tr_b16 a[158:159], v211 offset:12544\n\t" \
  "v_max3_f32 v192, v192, v42, v43\n\t" \
  "v_mfma_f32_16x16x32_bf16 v[64:67], a[192:195], v[80:83], v[184:187]\n\t" \
  "v_exp_f32 v101, v101\n\t" \
  "v_mfma_f32_16x16x32_bf16 v[64:67], a[196:199], v[84:87], v[64:67]\n\t" \
  "ds_read_b64_tr_b16 a[160:161], v210 offset:512\n\t" \
  "v_cvt_pk_bf16_f32 v154, v100, v101\n\t" \
  "v_mfma_f32_16x16x32_bf16 v[64:67], a[200:203], v[88:91], v[64:67]\n\t" \
  "v_max3_f32 v203, v203, v44, v45\n\t" \
  "v_max3_f32 v192, v192, v46, v47\n\t" \
  "v_mfma_f32_16x16x32_bf16 v[64:67], a[204:207], v[92:95], v[64:67]\n\t" \
  "v_max_f32 v192, v192, v203\n\t" \
  "v_cmp_lt_f32 vcc, 0x42800000, v192\n\t" \
  "v_mfma_f32_16x16x32_bf16 v[68:71], a[220:223], v[92:95], v[184:187]\n\t" \
  "v_exp_f32 v102, v102\n\t" \
  "v_mfma_f32_16x16x32_bf16 v[68:71], a[216:219], v[88:91], v[68:71]\n\t" \
  "s_cbranch_vccnz .Lsf_c21\n\t" \
  ".Lsf_r21:\n\t" \
  "v_mfma_f32_16x16x32_bf16 v[68:71], a[212:215], v[84:87], v[68:71]\n\t" \
  "ds_read_b64_tr_b16 a[162:163], v210 offset:4608\n\t" \
  "v_max3_f32 v192, v64, v65, v66\n\t" \
  "v_mfma_f32_16x16x32_bf16 v[68:71], a[208:211], v[80:83], v[68:71]\n\t" \
  "ds_read_b64_tr_b16 a[164:165], v210 offset:8704\n\t" \
  "v_exp_f32 v103, v103\n\t" \
  "v_mfma_f32_16x16x32_bf16 v[72:75], a[224:227], v[80:83], v[184:187]\n\t" \
  "ds_read_b64_tr_b16 a[166:167], v210 offset:12800\n\t" \
  "v_exp_f32 v104, v104\n\t" \
  "v_mfma_f32_16x16x32_bf16 v[72:75], a[228:231], v[84:87], v[72:75]\n\t" \
  "ds_read_b64_tr_b16 a[168:169], v211 offset:512\n\t" \
  "v_exp_f32 v105, v105\n\t" \
  "v_mfma_f32_16x16x32_bf16 v[72:75], a[232:235], v[88:91], v[72:75]\n\t" \
  "v_max3_f32 v203, v67, v68, v69\n\t" \
  "v_max3_f32 v192, v192, v70, v71\n\t" \
  "v_mfma_f32_16x16x32_bf16 v[72:75], a[236:239], v[92:95], v[72:75]\n\t" \
  "ds_read_b64_tr_b16 a[170:171], v211 offset:4608\n\t" \
  "v_exp_f32 v106, v106\n\t" \
  "v_mfma_f32_16x16x32_bf16 v[76:79], a[252:255], v[92:95], v[184:187]\n\t" \
  "ds_read_b64_tr_b16 a[172:173], v211 offset:8704\n\t" \
  "v_exp_f32 v107, v107\n\t" \
  "v_mfma_f32_16x16x32_bf16 v[76:79], a[248:251], v[88:91], v[76:79]\n\t" \
  "ds_read_b64_tr_b16 a[174:175], v211 offset:12800\n\t" \
  "v_cvt_pk_bf16_f32 v155, v102, v103\n\t" \
  "v_mfma_f32_16x16x32_bf16 v[76:79], a[244:247], v[84:87], v[76:79]\n\t" \
  "v_max3_f32 v203, v203, v72, v73\n\t" \
  "v_max3_f32 v192, v192, v74, v75\n\t" \
  "v_mfma_f32_16x16x32_bf16 v[76:79], a[240:243], v[80:83], v[76:79]\n\t" \
  "ds_read_b64_tr_b16 a[176:177], v210 offset:768\n\t" \
  "v_exp_f32 v108, v108\n\t" \
  "v_mfma_f32_16x16x32_bf16 v[96:99], a[192:195], v[112:115], v[188:191]\n\t" \
  "ds_read_b64_tr_b16 a[178:179], v210 offset:4864\n\t" \
  "v_exp_f32 v109, v109\n\t" \
  "v_mfma_f32_16x16x32_bf16 v[96:99], a[196:199], v[116:119], v[96:99]\n\t" \
  "ds_read_b64_tr_b16 a[180:181], v210 offset:8960\n\t" \
  "v_cvt_pk_bf16_f32 v156, v104, v105\n\t" \
  "v_mfma_f32_16x16x32_bf16 v[96:99], a[200:203], v[120:123], v[96:99]\n\t" \
  "v_max3_f32 v203, v203, v76, v77\n\t" \
  "v_max3_f32 v192, v192, v78, v79\n\t" \
  "v_mfma_f32_16x16x32_bf16 v[96:99], a[204:207], v[124:127], v[96:99]\n\t" \
  "v_max_f32 v192, v192, v203\n\t" \
  "v_cmp_lt_f32 vcc, 0x42800000, v192\n\t" \
  "v_mfma_f32_16x16x32_bf16 v[100:103], a[220:223], v[124:127], v[188:191]\n\t" \
  "ds_read_b64_tr_b16 a[182:183], v210 offset:13056\n\t" \
  "v_exp_f32 v110, v110\n\t" \
  "v_mfma_f32_16x16x32_bf16 v[100:103], a[216:219], v[120:123], v[100:103]\n\t" \
  "s_cbranch_vccnz .Lsf_c22\n\t" \
  ".Lsf_r22:\n\t" \
  "v_mfma_f32_16x16x32_bf16 v[100:103], a[212:215], v[116:119], v[100:103]\n\t" \
  "v_max3_f32 v192, v96, v97, v98\n\t" \
  "v_exp_f32 v111, v111\n\t" \
  "v_mfma_f32_16x16x32_bf16 v[100:103], a[208:211], v[112:115], v[100:103]\n\t" \
  "ds_read_b64_tr_b16 a[184:185], v211 offset:768\n\t" \
  "v_cvt_pk_bf16_f32 v157, v106, v107\n\t" \
  "v_mfma_f32_16x16x32_bf16 v[104:107], a[224:227], v[112:115], v[188:191]\n\t" \
  "ds_read_b64_tr_b16 a[186:187], v211 offset:4864\n\t" \
  "v_cvt_pk_bf16_f32 v158, v108, v109\n\t" \
  "v_mfma_f32_16x16x32_bf16 v[104:107], a[228:231], v[116:119], v[104:107]\n\t" \
  "ds_read_b64_tr_b16 a[188:189], v211 offset:8960\n\t" \
  "v_cvt_pk_bf16_f32 v159, v110, v111\n\t" \
  "v_mfma_f32_16x16x32_bf16 v[104:107], a[232:235], v[120:123], v[104:107]\n\t" \
  "v_max3_f32 v203, v99, v100, v101\n\t" \
  "v_max3_f32 v192, v192, v102, v103\n\t" \
  "v_mfma_f32_16x16x32_bf16 v[104:107], a[236:239], v[124:127], v[104:107]\n\t" \
  "ds_read_b64_tr_b16 a[190:191], v211 offset:13056\n\t" \
  "s_nop 0\n\t" \
  "v_mfma_f32_16x16x32_bf16 v[108:111], a[252:255], v[124:127], v[188:191]\n\t" \
  "v_exp_f32 v0, v0\n\t" \
  "v_mfma_f32_16x16x32_bf16 v[108:111], a[248:251], v[120:123], v[108:111]\n\t" \
  "v_exp_f32 v1, v1\n\t" \
  "v_mfma_f32_16x16x32_bf16 v[108:111], a[244:247], v[116:119], v[108:111]\n\t" \
  "s_waitcnt vmcnt(0) lgkmcnt(0)\n\t" \
  "v_max3_f32 v203, v203, v104, v105\n\t" \
  "v_max3_f32 v192, v192, v106, v107\n\t" \
  "v_mfma_f32_16x16x32_bf16 v[108:111], a[240:243], v[112:115], v[108:111]\n\t" \
  "s_barrier\n\t" \
  "v_mfma_f32_16x16x32_bf16 a[0:3], a[128:131], v[128:131], a[0:3]\n\t" \
  "v_exp_f32 v2, v2\n\t" \
  "v_mfma_f32_16x16x32_bf16 a[0:3], a[132:135], v[132:135], a[0:3]\n\t" \
  "v_exp_f32 v3, v3\n\t" \
  "v_mfma_f32_16x16x32_bf16 a[4:7], a[136:139], v[128:131], a[4:7]\n\t" \
  "v_exp_f32 v4, v4\n\t" \
  "v_mfma_f32_16x16x32_bf16 a[4:7], a[140:143], v[132:135], a[4:7]\n\t" \
  "v_max3_f32 v203, v203, v108, v109\n\t" \
  "v_max3_f32 v192, v192, v110, v111\n\t" \
  "v_mfma_f32_16x16x32_bf16 a[8:11], a[144:147], v[128:131], a[8:11]\n\t" \
  "v_max_f32 v192, v192, v203\n\t" \
  "v_cmp_lt_f32 vcc, 0x42800000, v192\n\t" \
  "v_mfma_f32_16x16x32_bf16 a[8:11], a[148:151], v[132:135], a[8:11]\n\t" \
  "v_exp_f32 v5, v5\n\t" \
  "v_mfma_f32_16x16x32_bf16 a[12:15], a[152:155], v[128:131], a[12:15]\n\t" \
  "s_cbranch_vccnz .Lsf_c23\n\t" \
  ".Lsf_r23:\n\t" \
  "v_mfma_f32_16x16x32_bf16 a[12:15], a[156:159], v[132:135], a[12:15]\n\t" \
  "s_add_u32 m0, s38, 49152\n\t" \
  "v_exp_f32 v6, v6\n\t" \
  "v_mfma_f32_16x16x32_bf16 a[16:19], a[160:163], v[128:131], a[16:19]\n\t" \
  "s_add_u32 s96, s38, 65536\n\t" \
  "v_exp_f32 v7, v7\n\t" \
  "v_mfma_f32_16x16x32_bf16 a[16:19], a[164:167], v[132:135], a[16:19]\n\t" \
  "v_exp_f32 v8, v8\n\t" \
  "v_mfma_f32_16x16x32_bf16 a[20:23], a[168:171], v[128:131], a[20:23]\n\t" \
  "buffer_load_dwordx4 v224, s[60:63], s96 offen offset:0 lds\n\t" \
  "v_mfma_f32_16x16x32_bf16 a[20:23], a[172:175], v[132:135], a[20:23]\n\t" \
  "v_exp_f32 v9, v9\n\t" \
  "v_mfma_f32_16x16x32_bf16 a[24:27], a[176:179], v[128:131], a[24:27]\n\t" \
  "v_exp_f32 v10, v10\n\t" \
  "v_mfma_f32_16x16x32_bf16 a[24:27], a[180:183], v[132:135], a[24:27]\n\t" \
  "v_exp_f32 v11, v11\n\t" \
  "v_mfma_f32_16x16x32_bf16 a[28:31], a[184:187], v[128:131], a[28:31]\n\t" \
  "v_exp_f32 v12, v12\n\t" \
  "v_mfma_f32_16x16x32_bf16 a[28:31], a[188:191], v[132:135], a[28:31]\n\t" \
  "v_exp_f32 v13, v13\n\t" \
  "v_smfmac_f32_16x16x64_bf16 v[160:163], v[216:219], v[128:135], v208\n\t" \
  "ds_read_b128 a[192:195], v220 offset:16384\n\t" \
  "v_cvt_pk_bf16_f32 v128, v0, v1\n\t" \
  "v_mfma_f32_16x16x32_bf16 a[32:35], a[128:131], v[136:139], a[32:35]\n\t" \
  "buffer_load_dwordx4 v225, s[60:63], s96 offen offset:1024 lds\n\t" \
  "v_mfma_f32_16x16x32_bf16 a[32:35], a[132:135], v[140:143], a[32:35]\n\t" \
  "v_exp_f32 v14, v14\n\t" \
  "v_mfma_f32_16x16x32_bf16 a[36:39], a[136:139], v[136:139], a[36:39]\n\t" \
  "v_exp_f32 v15, v15\n\t" \
  "v_mfma_f32_16x16x32_bf16 a[36:39], a[140:143], v[140:143], a[36:39]\n\t" \
  "ds_read_b128 a[196:199], v221 offset:16384\n\t" \
  "v_cvt_pk_bf16_f32 v129, v2, v3\n\t" \
  "v_mfma_f32_16x16x32_bf16 a[40:43], a[144:147], v[136:139], a[40:43]\n\t" \
  "v_exp_f32 v32, v32\n\t" \
  "v_mfma_f32_16x16x32_bf16 a[40:43], a[148:151], v[140:143], a[40:43]\n\t" \
  "v_exp_f32 v33, v33\n\t" \
  "v_mfma_f32_16x16x32_bf16 a[44:47], a[152:155], v[136:139], a[44:47]\n\t" \
  "buffer_load_dwordx4 v226, s[60:63], s96 offen offset:2048 lds\n\t" \
  "v_mfma_f32_16x16x32_bf16 a[44:47], a[156:159], v[140:143], a[44:47]\n\t" \
  "ds_read_b128 a[200:203], v222 offset:16384\n\t" \
  "v_cvt_pk_bf16_f32 v130, v4, v5\n\t" \
  "v_mfma_f32_16x16x32_bf16 a[48:51], a[160:163], v[136:139], a[48:51]\n\t" \
  "v_exp_f32 v34, v34\n\t" \
  "v_mfma_f32_16x16x32_bf16 a[48:51], a[164:167], v[140:143], a[48:51]\n\t" \
  "ds_read_b128 a[204:207], v223 offset:16384\n\t" \
  "v_cvt_pk_bf16_f32 v131, v6, v7\n\t" \
  "v_mfma_f32_16x16x32_bf16 a[52:55], a[168:171], v[136:139], a[52:55]\n\t" \
  "v_exp_f32 v35, v35\n\t" \
  "v_mfma_f32_16x16x32_bf16 a[52:55], a[172:175], v[140:143], a[52:55]\n\t" \
  "ds_read_b128 a[208:211], v220 offset:20480\n\t" \
  "v_cvt_pk_bf16_f32 v132, v8, v9\n\t" \
  "v_mfma_f32_16x16x32_bf16 a[56:59], a[176:179], v[136:139], a[56:59]\n\t" \
  "buffer_load_dwordx4 v227, s[60:63], s96 offen offset:3072 lds\n\t" \
  "v_mfma_f32_16x16x32_bf16 a[56:59], a[180:183], v[140:143], a[56:59]\n\t" \
  "v_exp_f32 v36, v36\n\t" \
  "v_mfma_f32_16x16x32_bf16 a[60:63], a[184:187], v[136:139], a[60:63]\n\t" \
  "s_add_u32 m0, s38, 147456\n\t" \
  "ds_read_b128 a[212:215], v221 offset:20480\n\t" \
  "v_cvt_pk_bf16_f32 v133, v10, v11\n\t" \
  "v_mfma_f32_16x16x32_bf16 a[60:63], a[188:191], v[140:143], a[60:63]\n\t" \
  "s_add_u32 s96, s38, 49152\n\t" \
  "v_exp_f32 v37, v37\n\t" \
  "v_smfmac_f32_16x16x64_bf16 v[162:165], v[216:219], v[136:143], v208\n\t" \
  "ds_read_b128 a[216:219], v222 offset:20480\n\t" \
  "v_cvt_pk_bf16_f32 v134, v12, v13\n\t" \
  "v_mfma_f32_16x16x32_bf16 a[64:67], a[128:131], v[144:147], a[64:67]\n\t" \
  "v_exp_f32 v38, v38\n\t" \
  "v_mfma_f32_16x16x32_bf16 a[64:67], a[132:135], v[148:151], a[64:67]\n\t" \
  "buffer_load_dwordx4 v230, s[64:67], s96 offen offset:0 lds\n\t" \
  "v_mfma_f32_16x16x32_bf16 a[68:71], a[136:139], v[144:147], a[68:71]\n\t" \
  "ds_read_b128 a[220:223], v223 offset:20480\n\t" \
  "v_cvt_pk_bf16_f32 v135, v14, v15\n\t" \
  "v_mfma_f32_16x16x32_bf16 a[68:71], a[140:143], v[148:151], a[68:71]\n\t" \
  "v_exp_f32 v39, v39\n\t" \
  "v_mfma_f32_16x16x32_bf16 a[72:75], a[144:147], v[144:147], a[72:75]\n\t" \
  "v_exp_f32 v40, v40\n\t" \
  "v_mfma_f32_16x16x32_bf16 a[72:75], a[148:151], v[148:151], a[72:75]\n\t" \
  "ds_read_b128 a[224:227], v220 offset:24576\n\t" \
  "v_cvt_pk_bf16_f32 v136, v32, v33\n\t" \
  "v_mfma_f32_16x16x32_bf16 a[76:79], a[152:155], v[144:147], a[76:79]\n\t" \
  "ds_read_b128 a[228:231], v221 offset:24576\n\t" \
  "v_cvt_pk_bf16_f32 v137, v34, v35\n\t" \
  "v_mfma_f32_16x16x32_bf16 a[76:79], a[156:159], v[148:151], a[76:79]\n\t" \
  "v_exp_f32 v41, v41\n\t" \
  "v_mfma_f32_16x16x32_bf16 a[80:83], a[160:163], v[144:147], a[80:83]\n\t" \
  "buffer_load_dwordx4 v231, s[64:67], s96 offen offset:1024 lds\n\t" \
  "v_mfma_f32_16x16x32_bf16 a[80:83], a[164:167], v[148:151], a[80:83]\n\t" \
  "v_exp_f32 v42, v42\n\t" \
  "v_mfma_f32_16x16x32_bf16 a[84:87], a[168:171], v[144:147], a[84:87]\n\t" \
  "ds_read_b128 a[232:235], v222 offset:24576\n\t" \
  "v_cvt_pk_bf16_f32 v138, v36, v37\n\t" \
  "v_mfma_f32_16x16x32_bf16 a[84:87], a[172:175], v[148:151], a[84:87]\n\t" \
  "v_exp_f32 v43, v43\n\t" \
  "v_mfma_f32_16x16x32_bf16 a[88:91], a[176:179], v[144:147], a[88:91]\n\t" \
  "ds_read_b128 a[236:239], v223 offset:24576\n\t" \
  "v_cvt_pk_bf16_f32 v139, v38, v39\n\t" \
  "v_mfma_f32_16x16x32_bf16 a[88:91], a[180:183], v[148:151], a[88:91]\n\t" \
  "v_exp_f32 v44, v44\n\t" \
  "v_mfma_f32_16x16x32_bf16 a[92:95], a[184:187], v[144:147], a[92:95]\n\t" \
  "buffer_load_dwordx4 v230, s[64:67], s96 offen offset:2048 lds\n\t" \
  "v_mfma_f32_16x16x32_bf16 a[92:95], a[188:191], v[148:151], a[92:95]\n\t" \
  "ds_read_b128 a[240:243], v220 offset:28672\n\t" \
  "v_cvt_pk_bf16_f32 v140, v40, v41\n\t" \
  "v_smfmac_f32_16x16x64_bf16 v[164:167], v[216:219], v[144:151], v208\n\t" \
  "ds_read_b128 a[244:247], v221 offset:28672\n\t" \
  "v_cvt_pk_bf16_f32 v141, v42, v43\n\t" \
  "v_mfma_f32_16x16x32_bf16 a[96:99], a[128:131], v[152:155], a[96:99]\n\t" \
  "v_exp_f32 v45, v45\n\t" \
  "v_mfma_f32_16x16x32_bf16 a[96:99], a[132:135], v[156:159], a[96:99]\n\t" \
  "v_exp_f32 v46, v46\n\t" \
  "v_mfma_f32_16x16x32_bf16 a[100:103], a[136:139], v[152:155], a[100:103]\n\t" \
  "v_exp_f32 v47, v47\n\t" \
  "v_mfma_f32_16x16x32_bf16 a[100:103], a[140:143], v[156:159], a[100:103]\n\t" \
  "buffer_load_dwordx4 v231, s[64:67], s96 offen offset:3072 lds\n\t" \
  "v_mfma_f32_16x16x32_bf16 a[104:107], a[144:147], v[152:155], a[104:107]\n\t" \
  "ds_read_b128 a[248:251], v222 offset:28672\n\t" \
  "v_cvt_pk_bf16_f32 v142, v44, v45\n\t" \
  "v_mfma_f32_16x16x32_bf16 a[104:107], a[148:151], v[156:159], a[104:107]\n\t" \
  "ds_read_b128 a[252:255], v223 offset:28672\n\t" \
  "v_cvt_pk_bf16_f32 v143, v46, v47\n\t" \
  "v_mfma_f32_16x16x32_bf16 a[108:111], a[152:155], v[152:155], a[108:111]\n\t" \
  "v_exp_f32 v64, v64\n\t" \
  "v_mfma_f32_16x16x32_bf16 a[108:111], a[156:159], v[156:159], a[108:111]\n\t" \
  "v_exp_f32 v65, v65\n\t" \
  "v_mfma_f32_16x16x32_bf16 a[112:115], a[160:163], v[152:155], a[112:115]\n\t" \
  "v_exp_f32 v66, v66\n\t" \
  "v_mfma_f32_16x16x32_bf16 a[112:115], a[164:167], v[156:159], a[112:115]\n\t" \
  "v_exp_f32 v67, v67\n\t" \
  "v_mfma_f32_16x16x32_bf16 a[116:119], a[168:171], v[152:155], a[116:119]\n\t" \
  "v_exp_f32 v68, v68\n\t" \
  "v_mfma_f32_16x16x32_bf16 a[116:119], a[172:175], v[156:159], a[116:119]\n\t" \
  "v_exp_f32 v69, v69\n\t" \
  "v_mfma_f32_16x16x32_bf16 a[120:123], a[176:179], v[152:155], a[120:123]\n\t" \
  "v_exp_f32 v70, v70\n\t" \
  "v_mfma_f32_16x16x32_bf16 a[120:123], a[180:183], v[156:159], a[120:123]\n\t" \
  "v_exp_f32 v71, v71\n\t" \
  "v_mfma_f32_16x16x32_bf16 a[124:127], a[184:187], v[152:155], a[124:127]\n\t" \
  "v_exp_f32 v72, v72\n\t" \
  "v_mfma_f32_16x16x32_bf16 a[124:127], a[188:191], v[156:159], a[124:127]\n\t" \
  "v_exp_f32 v73, v73\n\t" \
  "v_smfmac_f32_16x16x64_bf16 v[166:169], v[216:219], v[152:159], v208\n\t" \
  "s_waitcnt lgkmcnt(12)\n\t" \
  "s_cmp_lg_u32 s99, 0\n\t" \
  "s_cbranch_scc1 .Lsf_p2\n\t" \
  ".Lsf_q2:\n\t" \
  "v_mfma_f32_16x16x32_bf16 v[0:3], a[192:195], v[16:19], v[176:179]\n\t" \
  "ds_read_b64_tr_b16 a[128:129], v228 offset:0\n\t" \
  "v_cvt_pk_bf16_f32 v144, v64, v65\n\t" \
  "v_mfma_f32_16x16x32_bf16 v[0:3], a[196:199], v[20:23], v[0:3]\n\t" \
  "v_exp_f32 v74, v74\n\t" \
  "v_mfma_f32_16x16x32_bf16 v[0:3], a[200:203], v[24:27], v[0:3]\n\t" \
  "ds_read_b64_tr_b16 a[130:131], v228 offset:4096\n\t" \
  "v_cvt_pk_bf16_f32 v145, v66, v67\n\t" \
  "v_mfma_f32_16x16x32_bf16 v[0:3], a[204:207], v[28:31], v[0:3]\n\t" \
  "s_waitcnt lgkmcnt(10)\n\t" \
  "v_exp_f32 v75, v75\n\t" \
  "v_mfma_f32_16x16x32_bf16 v[4:7], a[220:223], v[28:31], v[176:179]\n\t" \
  "ds_read_b64_tr_b16 a[132:133], v228 offset:8192\n\t" \
  "v_cvt_pk_bf16_f32 v146, v68, v69\n\t" \
  "v_mfma_f32_16x16x32_bf16 v[4:7], a[216:219], v[24:27], v[4:7]\n\t" \
  "v_exp_f32 v76, v76\n\t" \
  "v_mfma_f32_16x16x32_bf16 v[4:7], a[212:215], v[20:23], v[4:7]\n\t" \
  "ds_read_b64_tr_b16 a[134:135], v228 offset:12288\n\t" \
  "v_max3_f32 v192, v0, v1, v2\n\t" \
  "v_mfma_f32_16x16x32_bf16 v[4:7], a[208:211], v[16:19], v[4:7]\n\t" \
  "s_waitcnt lgkmcnt(8)\n\t" \
  "ds_read_b64_tr_b16 a[136:137], v229 offset:0\n\t" \
  "v_cvt_pk_bf16_f32 v147, v70, v71\n\t" \
  "v_mfma_f32_16x16x32_bf16 v[8:11], a[224:227], v[16:19], v[176:179]\n\t" \
  "v_exp_f32 v77, v77\n\t" \
  "v_mfma_f32_16x16x32_bf16 v[8:11], a[228:231], v[20:23], v[8:11]\n\t" \
  "ds_read_b64_tr_b16 a[138:139], v229 offset:4096\n\t" \
  "v_cvt_pk_bf16_f32 v148, v72, v73\n\t" \
  "v_mfma_f32_16x16x32_bf16 v[8:11], a[232:235], v[24:27], v[8:11]\n\t" \
  "v_max3_f32 v203, v3, v4, v5\n\t" \
  "v_max3_f32 v192, v192, v6, v7\n\t" \
  "v_mfma_f32_16x16x32_bf16 v[8:11], a[236:239], v[28:31], v[8:11]\n\t" \
  "s_waitcnt lgkmcnt(6)\n\t" \
  "ds_read_b64_tr_b16 a[140:141], v229 offset:8192\n\t" \
  "v_cvt_pk_bf16_f32 v149, v74, v75\n\t" \
  "v_mfma_f32_16x16x32_bf16 v[12:15], a[252:255], v[28:31], v[176:179]\n\t" \
  "v_exp_f32 v78, v78\n\t" \
  "v_mfma_f32_16x16x32_bf16 v[12:15], a[248:251], v[24:27], v[12:15]\n\t" \
  "ds_read_b64_tr_b16 a[142:143], v229 offset:12288\n\t" \
  "v_cvt_pk_bf16_f32 v150, v76, v77\n\t" \
  "v_mfma_f32_16x16x32_bf16 v[12:15], a[244:247], v[20:23], v[12:15]\n\t" \
  "v_max3_f32 v203, v203, v8, v9\n\t" \
  "v_max3_f32 v192, v192, v10, v11\n\t" \
  "v_mfma_f32_16x16x32_bf16 v[12:15], a[240:243], v[16:19], v[12:15]\n\t" \
  "v_exp_f32 v79, v79\n\t" \
  "v_mfma_f32_16x16x32_bf16 v[32:35], a[192:195], v[48:51], v[180:183]\n\t" \
  "ds_read_b64_tr_b16 a[144:145], v228 offset:256\n\t" \
  "v_cvt_pk_bf16_f32 v151, v78, v79\n\t" \
  "v_mfma_f32_16x16x32_bf16 v[32:35], a[196:199], v[52:55], v[32:35]\n\t" \
  "v_exp_f32 v96, v96\n\t" \
  "v_mfma_f32_16x16x32_bf16 v[32:35], a[200:203], v[56:59], v[32:35]\n\t" \
  "ds_read_b64_tr_b16 a[146:147], v228 offset:4352\n\t" \
  "v_max3_f32 v203, v203, v12, v13\n\t" \
  "v_mfma_f32_16x16x32_bf16 v[32:35], a[204:207], v[60:63], v[32:35]\n\t" \
  "v_max3_f32 v192, v192, v14, v15\n\t" \
  "v_max_f32 v192, v192, v203\n\t" \
  "v_mfma_f32_16x16x32_bf16 v[36:39], a[220:223], v[60:63], v[180:183]\n\t" \
  "ds_read_b64_tr_b16 a[148:149], v228 offset:8448\n\t" \
  "v_cmp_lt_f32 vcc, 0x42800000, v192\n\t" \
  "v_mfma_f32_16x16x32_bf16 v[36:39], a[216:219], v[56:59], v[36:39]\n\t" \
  "v_exp_f32 v97, v97\n\t" \
  "v_mfma_f32_16x16x32_bf16 v[36:39], a[212:215], v[52:55], v[36:39]\n\t" \
  "s_cbranch_vccnz .Lsf_c30\n\t" \
  ".Lsf_r30:\n\t" \
  "v_mfma_f32_16x16x32_bf16 v[36:39], a[208:211], v[48:51], v[36:39]\n\t" \
  "ds_read_b64_tr_b16 a[150:151], v228 offset:12544\n\t" \
  "v_max3_f32 v192, v32, v33, v34\n\t" \
  "v_mfma_f32_16x16x32_bf16 v[40:43], a[224:227], v[48:51], v[180:183]\n\t" \
  "ds_read_b64_tr_b16 a[152:153], v229 offset:256\n\t" \
  "v_cvt_pk_bf16_f32 v152, v96, v97\n\t" \
  "v_mfma_f32_16x16x32_bf16 v[40:43], a[228:231], v[52:55], v[40:43]\n\t" \
  "v_exp_f32 v98, v98\n\t" \
  "v_mfma_f32_16x16x32_bf16 v[40:43], a[232:235], v[56:59], v[40:43]\n\t" \
  "v_max3_f32 v203, v35, v36, v37\n\t" \
  "v_max3_f32 v192, v192, v38, v39\n\t" \
  "v_mfma_f32_16x16x32_bf16 v[40:43], a[236:239], v[60:63], v[40:43]\n\t" \
  "v_exp_f32 v99, v99\n\t" \
  "v_mfma_f32_16x16x32_bf16 v[44:47], a[252:255], v[60:63], v[180:183]\n\t" \
  "ds_read_b64_tr_b16 a[154:155], v229 offset:4352\n\t" \
  "v_cvt_pk_bf16_f32 v153, v98, v99\n\t" \
  "v_mfma_f32_16x16x32_bf16 v[44:47], a[248:251], v[56:59], v[44:47]\n\t" \
  "v_exp_f32 v100, v100\n\t" \
  "v_mfma_f32_16x16x32_bf16 v[44:47], a[244:247], v[52:55], v[44:47]\n\t" \
  "ds_read_b64_tr_b16 a[156:157], v229 offset:8448\n\t" \
  "v_max3_f32 v203, v203, v40, v41\n\t" \
  "v_mfma_f32_16x16x32_bf16 v[44:47], a[240:243], v[48:51], v[44:47]\n\t" \
  "ds_read_b64_tr_b16 a[158:159], v229 offset:12544\n\t" \
  "v_max3_f32 v192, v192, v42, v43\n\t" \
  "v_mfma_f32_16x16x32_bf16 v[64:67], a[192:195], v[80:83], v[184:187]\n\t" \
  "v_exp_f32 v101, v101\n\t" \
  "v_mfma_f32_16x16x32_bf16 v[64:67], a[196:199], v[84:87], v[64:67]\n\t" \
  "ds_read_b64_tr_b16 a[160:161], v228 offset:512\n\t" \
  "v_cvt_pk_bf16_f32 v154, v100, v101\n\t" \
  "v_mfma_f32_16x16x32_bf16 v[64:67], a[200:203], v[88:91], v[64:67]\n\t" \
  "v_max3_f32 v203, v203, v44, v45\n\t" \
  "v_max3_f32 v192, v192, v46, v47\n\t" \
  "v_mfma_f32_16x16x32_bf16 v[64:67], a[204:207], v[92:95], v[64:67]\n\t" \
  "v_max_f32 v192, v192, v203\n\t" \
  "v_cmp_lt_f32 vcc, 0x42800000, v192\n\t" \
  "v_mfma_f32_16x16x32_bf16 v[68:71], a[220:223], v[92:95], v[184:187]\n\t" \
  "v_exp_f32 v102, v102\n\t" \
  "v_mfma_f32_16x16x32_bf16 v[68:71], a[216:219], v[88:91], v[68:71]\n\t" \
  "s_cbranch_vccnz .Lsf_c31\n\t" \
  ".Lsf_r31:\n\t" \
  "v_mfma_f32_16x16x32_bf16 v[68:71], a[212:215], v[84:87], v[68:71]\n\t" \
  "ds_read_b64_tr_b16 a[162:163], v228 offset:4608\n\t" \
  "v_max3_f32 v192, v64, v65, v66\n\t" \
  "v_mfma_f32_16x16x32_bf16 v[68:71], a[208:211], v[80:83], v[68:71]\n\t" \
  "ds_read_b64_tr_b16 a[164:165], v228 offset:8704\n\t" \
  "v_exp_f32 v103, v103\n\t" \
  "v_mfma_f32_16x16x32_bf16 v[72:75], a[224:227], v[80:83], v[184:187]\n\t" \
  "ds_read_b64_tr_b16 a[166:167], v228 offset:12800\n\t" \
  "v_exp_f32 v104, v104\n\t" \
  "v_mfma_f32_16x16x32_bf16 v[72:75], a[228:231], v[84:87], v[72:75]\n\t" \
  "ds_read_b64_tr_b16 a[168:169], v229 offset:512\n\t" \
  "v_exp_f32 v105, v105\n\t" \
  "v_mfma_f32_16x16x32_bf16 v[72:75], a[232:235], v[88:91], v[72:75]\n\t" \
  "v_max3_f32 v203, v67, v68, v69\n\t" \
  "v_max3_f32 v192, v192, v70, v71\n\t" \
  "v_mfma_f32_16x16x32_bf16 v[72:75], a[236:239], v[92:95], v[72:75]\n\t" \
  "ds_read_b64_tr_b16 a[170:171], v229 offset:4608\n\t" \
  "v_exp_f32 v106, v106\n\t" \
  "v_mfma_f32_16x16x32_bf16 v[76:79], a[252:255], v[92:95], v[184:187]\n\t" \
  "ds_read_b64_tr_b16 a[172:173], v229 offset:8704\n\t" \
  "v_exp_f32 v107, v107\n\t" \
  "v_mfma_f32_16x16x32_bf16 v[76:79], a[248:251], v[88:91], v[76:79]\n\t" \
  "ds_read_b64_tr_b16 a[174:175], v229 offset:12800\n\t" \
  "v_cvt_pk_bf16_f32 v155, v102, v103\n\t" \
  "v_mfma_f32_16x16x32_bf16 v[76:79], a[244:247], v[84:87], v[76:79]\n\t" \
  "v_max3_f32 v203, v203, v72, v73\n\t" \
  "v_max3_f32 v192, v192, v74, v75\n\t" \
  "v_mfma_f32_16x16x32_bf16 v[76:79], a[240:243], v[80:83], v[76:79]\n\t" \
  "ds_read_b64_tr_b16 a[176:177], v228 offset:768\n\t" \
  "v_exp_f32 v108, v108\n\t" \
  "v_mfma_f32_16x16x32_bf16 v[96:99], a[192:195], v[112:115], v[188:191]\n\t" \
  "ds_read_b64_tr_b16 a[178:179], v228 offset:4864\n\t" \
  "v_exp_f32 v109, v109\n\t" \
  "v_mfma_f32_16x16x32_bf16 v[96:99], a[196:199], v[116:119], v[96:99]\n\t" \
  "ds_read_b64_tr_b16 a[180:181], v228 offset:8960\n\t" \
  "v_cvt_pk_bf16_f32 v156, v104, v105\n\t" \
  "v_mfma_f32_16x16x32_bf16 v[96:99], a[200:203], v[120:123], v[96:99]\n\t" \
  "v_max3_f32 v203, v203, v76, v77\n\t" \
  "v_max3_f32 v192, v192, v78, v79\n\t" \
  "v_mfma_f32_16x16x32_bf16 v[96:99], a[204:207], v[124:127], v[96:99]\n\t" \
  "v_max_f32 v192, v192, v203\n\t" \
  "v_cmp_lt_f32 vcc, 0x42800000, v192\n\t" \
  "v_mfma_f32_16x16x32_bf16 v[100:103], a[220:223], v[124:127], v[188:191]\n\t" \
  "ds_read_b64_tr_b16 a[182:183], v228 offset:13056\n\t" \
  "v_exp_f32 v110, v110\n\t" \
  "v_mfma_f32_16x16x32_bf16 v[100:103], a[216:219], v[120:123], v[100:103]\n\t" \
  "s_cbranch_vccnz .Lsf_c32\n\t" \
  ".Lsf_r32:\n\t" \
  "v_mfma_f32_16x16x32_bf16 v[100:103], a[212:215], v[116:119], v[100:103]\n\t" \
  "v_max3_f32 v192, v96, v97, v98\n\t" \
  "v_exp_f32 v111, v111\n\t" \
  "v_mfma_f32_16x16x32_bf16 v[100:103], a[208:211], v[112:115], v[100:103]\n\t" \
  "ds_read_b64_tr_b16 a[184:185], v229 offset:768\n\t" \
  "v_cvt_pk_bf16_f32 v157, v106, v107\n\t" \
  "v_mfma_f32_16x16x32_bf16 v[104:107], a[224:227], v[112:115], v[188:191]\n\t" \
  "ds_read_b64_tr_b16 a[186:187], v229 offset:4864\n\t" \
  "v_cvt_pk_bf16_f32 v158, v108, v109\n\t" \
  "v_mfma_f32_16x16x32_bf16 v[104:107], a[228:231], v[116:119], v[104:107]\n\t" \
  "ds_read_b64_tr_b16 a[188:189], v229 offset:8960\n\t" \
  "v_cvt_pk_bf16_f32 v159, v110, v111\n\t" \
  "v_mfma_f32_16x16x32_bf16 v[104:107], a[232:235], v[120:123], v[104:107]\n\t" \
  "v_max3_f32 v203, v99, v100, v101\n\t" \
  "v_max3_f32 v192, v192, v102, v103\n\t" \
  "v_mfma_f32_16x16x32_bf16 v[104:107], a[236:239], v[124:127], v[104:107]\n\t" \
  "ds_read_b64_tr_b16 a[190:191], v229 offset:13056\n\t" \
  "s_nop 0\n\t" \
  "v_mfma_f32_16x16x32_bf16 v[108:111], a[252:255], v[124:127], v[188:191]\n\t" \
  "v_exp_f32 v0, v0\n\t" \
  "v_mfma_f32_16x16x32_bf16 v[108:111], a[248:251], v[120:123], v[108:111]\n\t" \
  "v_exp_f32 v1, v1\n\t" \
  "v_mfma_f32_16x16x32_bf16 v[108:111], a[244:247], v[116:119], v[108:111]\n\t" \
  "s_waitcnt vmcnt(0) lgkmcnt(0)\n\t" \
  "v_max3_f32 v203, v203, v104, v105\n\t" \
  "v_max3_f32 v192, v192, v106, v107\n\t" \
  "v_mfma_f32_16x16x32_bf16 v[108:111], a[240:243], v[112:115], v[108:111]\n\t" \
  "s_barrier\n\t" \
  "v_mfma_f32_16x16x32_bf16 a[0:3], a[128:131], v[128:131], a[0:3]\n\t" \
  "v_exp_f32 v2, v2\n\t" \
  "v_mfma_f32_16x16x32_bf16 a[0:3], a[132:135], v[132:135], a[0:3]\n\t" \
  "v_exp_f32 v3, v3\n\t" \
  "v_mfma_f32_16x16x32_bf16 a[4:7], a[136:139], v[128:131], a[4:7]\n\t" \
  "v_exp_f32 v4, v4\n\t" \
  "v_mfma_f32_16x16x32_bf16 a[4:7], a[140:143], v[132:135], a[4:7]\n\t" \
  "v_max3_f32 v203, v203, v108, v109\n\t" \
  "v_max3_f32 v192, v192, v110, v111\n\t" \
  "v_mfma_f32_16x16x32_bf16 a[8:11], a[144:147], v[128:131], a[8:11]\n\t" \
  "v_max_f32 v192, v192, v203\n\t" \
  "v_cmp_lt_f32 vcc, 0x42800000, v192\n\t" \
  "v_mfma_f32_16x16x32_bf16 a[8:11], a[148:151], v[132:135], a[8:11]\n\t" \
  "v_exp_f32 v5, v5\n\t" \
  "v_mfma_f32_16x16x32_bf16 a[12:15], a[152:155], v[128:131], a[12:15]\n\t" \
  "s_cbranch_vccnz .Lsf_c33\n\t" \
  ".Lsf_r33:\n\t" \
  "v_mfma_f32_16x16x32_bf16 a[12:15], a[156:159], v[132:135], a[12:15]\n\t" \
  "s_add_u32 m0, s38, 65536\n\t" \
  "v_exp_f32 v6, v6\n\t" \
  "v_mfma_f32_16x16x32_bf16 a[16:19], a[160:163], v[128:131], a[16:19]\n\t" \
  "s_add_u32 s96, s38, 81920\n\t" \
  "v_exp_f32 v7, v7\n\t" \
  "v_mfma_f32_16x16x32_bf16 a[16:19], a[164:167], v[132:135], a[16:19]\n\t" \
  "v_exp_f32 v8, v8\n\t" \
  "v_mfma_f32_16x16x32_bf16 a[20:23], a[168:171], v[128:131], a[20:23]\n\t" \
  "buffer_load_dwordx4 v224, s[60:63], s96 offen offset:0 lds\n\t" \
  "v_mfma_f32_16x16x32_bf16 a[20:23], a[172:175], v[132:135], a[20:23]\n\t" \
  "v_exp_f32 v9, v9\n\t" \
  "v_mfma_f32_16x16x32_bf16 a[24:27], a[176:179], v[128:131], a[24:27]\n\t" \
  "v_exp_f32 v10, v10\n\t" \
  "v_mfma_f32_16x16x32_bf16 a[24:27], a[180:183], v[132:135], a[24:27]\n\t" \
  "v_exp_f32 v11, v11\n\t" \
  "v_mfma_f32_16x16x32_bf16 a[28:31], a[184:187], v[128:131], a[28:31]\n\t" \
  "v_exp_f32 v12, v12\n\t" \
  "v_mfma_f32_16x16x32_bf16 a[28:31], a[188:191], v[132:135], a[28:31]\n\t" \
  "v_exp_f32 v13, v13\n\t" \
  "v_smfmac_f32_16x16x64_bf16 v[160:163], v[216:219], v[128:135], v208\n\t" \
  "ds_read_b128 a[192:195], v220 offset:0\n\t" \
  "v_cvt_pk_bf16_f32 v128, v0, v1\n\t" \
  "v_mfma_f32_16x16x32_bf16 a[32:35], a[128:131], v[136:139], a[32:35]\n\t" \
  "buffer_load_dwordx4 v225, s[60:63], s96 offen offset:1024 lds\n\t" \
  "v_mfma_f32_16x16x32_bf16 a[32:35], a[132:135], v[140:143], a[32:35]\n\t" \
  "v_exp_f32 v14, v14\n\t" \
  "v_mfma_f32_16x16x32_bf16 a[36:39], a[136:139], v[136:139], a[36:39]\n\t" \
  "v_exp_f32 v15, v15\n\t" \
  "v_mfma_f32_16x16x32_bf16 a[36:39], a[140:143], v[140:143], a[36:39]\n\t" \
  "ds_read_b128 a[196:199], v221 offset:0\n\t" \
  "v_cvt_pk_bf16_f32 v129, v2, v3\n\t" \
  "v_mfma_f32_16x16x32_bf16 a[40:43], a[144:147], v[136:139], a[40:43]\n\t" \
  "v_exp_f32 v32, v32\n\t" \
  "v_mfma_f32_16x16x32_bf16 a[40:43], a[148:151], v[140:143], a[40:43]\n\t" \
  "v_exp_f32 v33, v33\n\t" \
  "v_mfma_f32_16x16x32_bf16 a[44:47], a[152:155], v[136:139], a[44:47]\n\t" \
  "buffer_load_dwordx4 v226, s[60:63], s96 offen offset:2048 lds\n\t" \
  "v_mfma_f32_16x16x32_bf16 a[44:47], a[156:159], v[140:143], a[44:47]\n\t" \
  "ds_read_b128 a[200:203], v222 offset:0\n\t" \
  "v_cvt_pk_bf16_f32 v130, v4, v5\n\t" \
  "v_mfma_f32_16x16x32_bf16 a[48:51], a[160:163], v[136:139], a[48:51]\n\t" \
  "v_exp_f32 v34, v34\n\t" \
  "v_mfma_f32_16x16x32_bf16 a[48:51], a[164:167], v[140:143], a[48:51]\n\t" \
  "ds_read_b128 a[204:207], v223 offset:0\n\t" \
  "v_cvt_pk_bf16_f32 v131, v6, v7\n\t" \
  "v_mfma_f32_16x16x32_bf16 a[52:55], a[168:171], v[136:139], a[52:55]\n\t" \
  "v_exp_f32 v35, v35\n\t" \
  "v_mfma_f32_16x16x32_bf16 a[52:55], a[172:175], v[140:143], a[52:55]\n\t" \
  "ds_read_b128 a[208:211], v220 offset:4096\n\t" \
  "v_cvt_pk_bf16_f32 v132, v8, v9\n\t" \
  "v_mfma_f32_16x16x32_bf16 a[56:59], a[176:179], v[136:139], a[56:59]\n\t" \
  "buffer_load_dwordx4 v227, s[60:63], s96 offen offset:3072 lds\n\t" \
  "v_mfma_f32_16x16x32_bf16 a[56:59], a[180:183], v[140:143], a[56:59]\n\t" \
  "v_exp_f32 v36, v36\n\t" \
  "v_mfma_f32_16x16x32_bf16 a[60:63], a[184:187], v[136:139], a[60:63]\n\t" \
  "s_add_u32 m0, s38, 81920\n\t" \
  "ds_read_b128 a[212:215], v221 offset:4096\n\t" \
  "v_cvt_pk_bf16_f32 v133, v10, v11\n\t" \
  "v_mfma_f32_16x16x32_bf16 a[60:63], a[188:191], v[140:143], a[60:63]\n\t" \
  "s_add_u32 s96, s38, 65536\n\t" \
  "v_exp_f32 v37, v37\n\t" \
  "v_smfmac_f32_16x16x64_bf16 v[162:165], v[216:219], v[136:143], v208\n\t" \
  "ds_read_b128 a[216:219], v222 offset:4096\n\t" \
  "v_cvt_pk_bf16_f32 v134, v12, v13\n\t" \
  "v_mfma_f32_16x16x32_bf16 a[64:67], a[128:131], v[144:147], a[64:67]\n\t" \
  "v_exp_f32 v38, v38\n\t" \
  "v_mfma_f32_16x16x32_bf16 a[64:67], a[132:135], v[148:151], a[64:67]\n\t" \
  "buffer_load_dwordx4 v230, s[64:67], s96 offen offset:0 lds\n\t" \
  "v_mfma_f32_16x16x32_bf16 a[68:71], a[136:139], v[144:147], a[68:71]\n\t" \
  "ds_read_b128 a[220:223], v223 offset:4096\n\t" \
  "v_cvt_pk_bf16_f32 v135, v14, v15\n\t" \
  "v_mfma_f32_16x16x32_bf16 a[68:71], a[140:143], v[148:151], a[68:71]\n\t" \
  "v_exp_f32 v39, v39\n\t" \
  "v_mfma_f32_16x16x32_bf16 a[72:75], a[144:147], v[144:147], a[72:75]\n\t" \
  "v_exp_f32 v40, v40\n\t" \
  "v_mfma_f32_16x16x32_bf16 a[72:75], a[148:151], v[148:151], a[72:75]\n\t" \
  "ds_read_b128 a[224:227], v220 offset:8192\n\t" \
  "v_cvt_pk_bf16_f32 v136, v32, v33\n\t" \
  "v_mfma_f32_16x16x32_bf16 a[76:79], a[152:155], v[144:147], a[76:79]\n\t" \
  "ds_read_b128 a[228:231], v221 offset:8192\n\t" \
  "v_cvt_pk_bf16_f32 v137, v34, v35\n\t" \
  "v_mfma_f32_16x16x32_bf16 a[76:79], a[156:159], v[148:151], a[76:79]\n\t" \
  "v_exp_f32 v41, v41\n\t" \
  "v_mfma_f32_16x16x32_bf16 a[80:83], a[160:163], v[144:147], a[80:83]\n\t" \
  "buffer_load_dwordx4 v231, s[64:67], s96 offen offset:1024 lds\n\t" \
  "v_mfma_f32_16x16x32_bf16 a[80:83], a[164:167], v[148:151], a[80:83]\n\t" \
  "v_exp_f32 v42, v42\n\t" \
  "v_mfma_f32_16x16x32_bf16 a[84:87], a[168:171], v[144:147], a[84:87]\n\t" \
  "ds_read_b128 a[232:235], v222 offset:8192\n\t" \
  "v_cvt_pk_bf16_f32 v138, v36, v37\n\t" \
  "v_mfma_f32_16x16x32_bf16 a[84:87], a[172:175], v[148:151], a[84:87]\n\t" \
  "v_exp_f32 v43, v43\n\t" \
  "v_mfma_f32_16x16x32_bf16 a[88:91], a[176:179], v[144:147], a[88:91]\n\t" \
  "ds_read_b128 a[236:239], v223 offset:8192\n\t" \
  "v_cvt_pk_bf16_f32 v139, v38, v39\n\t" \
  "v_mfma_f32_16x16x32_bf16 a[88:91], a[180:183], v[148:151], a[88:91]\n\t" \
  "v_exp_f32 v44, v44\n\t" \
  "v_mfma_f32_16x16x32_bf16 a[92:95], a[184:187], v[144:147], a[92:95]\n\t" \
  "buffer_load_dwordx4 v230, s[64:67], s96 offen offset:2048 lds\n\t" \
  "v_mfma_f32_16x16x32_bf16 a[92:95], a[188:191], v[148:151], a[92:95]\n\t" \
  "ds_read_b128 a[240:243], v220 offset:12288\n\t" \
  "v_cvt_pk_bf16_f32 v140, v40, v41\n\t" \
  "v_smfmac_f32_16x16x64_bf16 v[164:167], v[216:219], v[144:151], v208\n\t" \
  "ds_read_b128 a[244:247], v221 offset:12288\n\t" \
  "v_cvt_pk_bf16_f32 v141, v42, v43\n\t" \
  "v_mfma_f32_16x16x32_bf16 a[96:99], a[128:131], v[152:155], a[96:99]\n\t" \
  "v_exp_f32 v45, v45\n\t" \
  "v_mfma_f32_16x16x32_bf16 a[96:99], a[132:135], v[156:159], a[96:99]\n\t" \
  "v_exp_f32 v46, v46\n\t" \
  "v_mfma_f32_16x16x32_bf16 a[100:103], a[136:139], v[152:155], a[100:103]\n\t" \
  "v_exp_f32 v47, v47\n\t" \
  "v_mfma_f32_16x16x32_bf16 a[100:103], a[140:143], v[156:159], a[100:103]\n\t" \
  "buffer_load_dwordx4 v231, s[64:67], s96 offen offset:3072 lds\n\t" \
  "v_mfma_f32_16x16x32_bf16 a[104:107], a[144:147], v[152:155], a[104:107]\n\t" \
  "ds_read_b128 a[248:251], v222 offset:12288\n\t" \
  "v_cvt_pk_bf16_f32 v142, v44, v45\n\t" \
  "v_mfma_f32_16x16x32_bf16 a[104:107], a[148:151], v[156:159], a[104:107]\n\t" \
  "ds_read_b128 a[252:255], v223 offset:12288\n\t" \
  "v_cvt_pk_bf16_f32 v143, v46, v47\n\t" \
  "v_mfma_f32_16x16x32_bf16 a[108:111], a[152:155], v[152:155], a[108:111]\n\t" \
  "v_exp_f32 v64, v64\n\t" \
  "v_mfma_f32_16x16x32_bf16 a[108:111], a[156:159], v[156:159], a[108:111]\n\t" \
  "v_exp_f32 v65, v65\n\t" \
  "v_mfma_f32_16x16x32_bf16 a[112:115], a[160:163], v[152:155], a[112:115]\n\t" \
  "v_exp_f32 v66, v66\n\t" \
  "v_mfma_f32_16x16x32_bf16 a[112:115], a[164:167], v[156:159], a[112:115]\n\t" \
  "v_exp_f32 v67, v67\n\t" \
  "v_mfma_f32_16x16x32_bf16 a[116:119], a[168:171], v[152:155], a[116:119]\n\t" \
  "v_exp_f32 v68, v68\n\t" \
  "v_mfma_f32_16x16x32_bf16 a[116:119], a[172:175], v[156:159], a[116:119]\n\t" \
  "v_exp_f32 v69, v69\n\t" \
  "v_mfma_f32_16x16x32_bf16 a[120:123], a[176:179], v[152:155], a[120:123]\n\t" \
  "v_exp_f32 v70, v70\n\t" \
  "v_mfma_f32_16x16x32_bf16 a[120:123], a[180:183], v[156:159], a[120:123]\n\t" \
  "v_exp_f32 v71, v71\n\t" \
  "v_mfma_f32_16x16x32_bf16 a[124:127], a[184:187], v[152:155], a[124:127]\n\t" \
  "v_exp_f32 v72, v72\n\t" \
  "v_mfma_f32_16x16x32_bf16 a[124:127], a[188:191], v[156:159], a[124:127]\n\t" \
  "v_exp_f32 v73, v73\n\t" \
  "v_smfmac_f32_16x16x64_bf16 v[166:169], v[216:219], v[152:159], v208\n\t" \
  "s_waitcnt lgkmcnt(12)\n\t" \
  "s_cmp_lg_u32 s99, 0\n\t" \
  "s_cbranch_scc1 .Lsf_p3\n\t" \
  ".Lsf_q3:\n\t" \
  "v_mfma_f32_16x16x32_bf16 v[0:3], a[192:195], v[16:19], v[176:179]\n\t" \
  "ds_read_b64_tr_b16 a[128:129], v210 offset:0\n\t" \
  "v_cvt_pk_bf16_f32 v144, v64, v65\n\t" \
  "v_mfma_f32_16x16x32_bf16 v[0:3], a[196:199], v[20:23], v[0:3]\n\t" \
  "v_exp_f32 v74, v74\n\t" \
  "v_mfma_f32_16x16x32_bf16 v[0:3], a[200:203], v[24:27], v[0:3]\n\t" \
  "ds_read_b64_tr_b16 a[130:131], v210 offset:4096\n\t" \
  "v_cvt_pk_bf16_f32 v145, v66, v67\n\t" \
  "v_mfma_f32_16x16x32_bf16 v[0:3], a[204:207], v[28:31], v[0:3]\n\t" \
  "s_waitcnt lgkmcnt(10)\n\t" \
  "v_exp_f32 v75, v75\n\t" \
  "v_mfma_f32_16x16x32_bf16 v[4:7], a[220:223], v[28:31], v[176:179]\n\t" \
  "ds_read_b64_tr_b16 a[132:133], v210 offset:8192\n\t" \
  "v_cvt_pk_bf16_f32 v146, v68, v69\n\t" \
  "v_mfma_f32_16x16x32_bf16 v[4:7], a[216:219], v[24:27], v[4:7]\n\t" \
  "v_exp_f32 v76, v76\n\t" \
  "v_mfma_f32_16x16x32_bf16 v[4:7], a[212:215], v[20:23], v[4:7]\n\t" \
  "ds_read_b64_tr_b16 a[134:135], v210 offset:12288\n\t" \
  "v_max3_f32 v192, v0, v1, v2\n\t" \
  "v_mfma_f32_16x16x32_bf16 v[4:7], a[208:211], v[16:19], v[4:7]\n\t" \
  "s_waitcnt lgkmcnt(8)\n\t" \
  "ds_read_b64_tr_b16 a[136:137], v211 offset:0\n\t" \
  "v_cvt_pk_bf16_f32 v147, v70, v71\n\t" \
  "v_mfma_f32_16x16x32_bf16 v[8:11], a[224:227], v[16:19], v[176:179]\n\t" \
  "v_exp_f32 v77, v77\n\t" \
  "v_mfma_f32_16x16x32_bf16 v[8:11], a[228:231], v[20:23], v[8:11]\n\t" \
  "ds_read_b64_tr_b16 a[138:139], v211 offset:4096\n\t" \
  "v_cvt_pk_bf16_f32 v148, v72, v73\n\t" \
  "v_mfma_f32_16x16x32_bf16 v[8:11], a[232:235], v[24:27], v[8:11]\n\t" \
  "v_max3_f32 v203, v3, v4, v5\n\t" \
  "v_max3_f32 v192, v192, v6, v7\n\t" \
  "v_mfma_f32_16x16x32_bf16 v[8:11], a[236:239], v[28:31], v[8:11]\n\t" \
  "s_waitcnt lgkmcnt(6)\n\t" \
  "ds_read_b64_tr_b16 a[140:141], v211 offset:8192\n\t" \
  "v_cvt_pk_bf16_f32 v149, v74, v75\n\t" \
  "v_mfma_f32_16x16x32_bf16 v[12:15], a[252:255], v[28:31], v[176:179]\n\t" \
  "v_exp_f32 v78, v78\n\t" \
  "v_mfma_f32_16x16x32_bf16 v[12:15], a[248:251], v[24:27], v[12:15]\n\t" \
  "ds_read_b64_tr_b16 a[142:143], v211 offset:12288\n\t" \
  "v_cvt_pk_bf16_f32 v150, v76, v77\n\t" \
  "v_mfma_f32_16x16x32_bf16 v[12:15], a[244:247], v[20:23], v[12:15]\n\t" \
  "v_max3_f32 v203, v203, v8, v9\n\t" \
  "v_max3_f32 v192, v192, v10, v11\n\t" \
  "v_mfma_f32_16x16x32_bf16 v[12:15], a[240:243], v[16:19], v[12:15]\n\t" \
  "v_exp_f32 v79, v79\n\t" \
  "v_mfma_f32_16x16x32_bf16 v[32:35], a[192:195], v[48:51], v[180:183]\n\t" \
  "ds_read_b64_tr_b16 a[144:145], v210 offset:256\n\t" \
  "v_cvt_pk_bf16_f32 v151, v78, v79\n\t" \
  "v_mfma_f32_16x16x32_bf16 v[32:35], a[196:199], v[52:55], v[32:35]\n\t" \
  "v_exp_f32 v96, v96\n\t" \
  "v_mfma_f32_16x16x32_bf16 v[32:35], a[200:203], v[56:59], v[32:35]\n\t" \
  "ds_read_b64_tr_b16 a[146:147], v210 offset:4352\n\t" \
  "v_max3_f32 v203, v203, v12, v13\n\t" \
  "v_mfma_f32_16x16x32_bf16 v[32:35], a[204:207], v[60:63], v[32:35]\n\t" \
  "v_max3_f32 v192, v192, v14, v15\n\t" \
  "v_max_f32 v192, v192, v203\n\t" \
  "v_mfma_f32_16x16x32_bf16 v[36:39], a[220:223], v[60:63], v[180:183]\n\t" \
  "ds_read_b64_tr_b16 a[148:149], v210 offset:8448\n\t" \
  "v_cmp_lt_f32 vcc, 0x42800000, v192\n\t" \
  "v_mfma_f32_16x16x32_bf16 v[36:39], a[216:219], v[56:59], v[36:39]\n\t" \
  "v_exp_f32 v97, v97\n\t" \
  "v_mfma_f32_16x16x32_bf16 v[36:39], a[212:215], v[52:55], v[36:39]\n\t" \
  "s_cbranch_vccnz .Lsf_c40\n\t" \
  ".Lsf_r40:\n\t" \
  "v_mfma_f32_16x16x32_bf16 v[36:39], a[208:211], v[48:51], v[36:39]\n\t" \
  "ds_read_b64_tr_b16 a[150:151], v210 offset:12544\n\t" \
  "v_max3_f32 v192, v32, v33, v34\n\t" \
  "v_mfma_f32_16x16x32_bf16 v[40:43], a[224:227], v[48:51], v[180:183]\n\t" \
  "ds_read_b64_tr_b16 a[152:153], v211 offset:256\n\t" \
  "v_cvt_pk_bf16_f32 v152, v96, v97\n\t" \
  "v_mfma_f32_16x16x32_bf16 v[40:43], a[228:231], v[52:55], v[40:43]\n\t" \
  "v_exp_f32 v98, v98\n\t" \
  "v_mfma_f32_16x16x32_bf16 v[40:43], a[232:235], v[56:59], v[40:43]\n\t" \
  "v_max3_f32 v203, v35, v36, v37\n\t" \
  "v_max3_f32 v192, v192, v38, v39\n\t" \
  "v_mfma_f32_16x16x32_bf16 v[40:43], a[236:239], v[60:63], v[40:43]\n\t" \
  "v_exp_f32 v99, v99\n\t" \
  "v_mfma_f32_16x16x32_bf16 v[44:47], a[252:255], v[60:63], v[180:183]\n\t" \
  "ds_read_b64_tr_b16 a[154:155], v211 offset:4352\n\t" \
  "v_cvt_pk_bf16_f32 v153, v98, v99\n\t" \
  "v_mfma_f32_16x16x32_bf16 v[44:47], a[248:251], v[56:59], v[44:47]\n\t" \
  "v_exp_f32 v100, v100\n\t" \
  "v_mfma_f32_16x16x32_bf16 v[44:47], a[244:247], v[52:55], v[44:47]\n\t" \
  "ds_read_b64_tr_b16 a[156:157], v211 offset:8448\n\t" \
  "v_max3_f32 v203, v203, v40, v41\n\t" \
  "v_mfma_f32_16x16x32_bf16 v[44:47], a[240:243], v[48:51], v[44:47]\n\t" \
  "ds_read_b64_tr_b16 a[158:159], v211 offset:12544\n\t" \
  "v_max3_f32 v192, v192, v42, v43\n\t" \
  "v_mfma_f32_16x16x32_bf16 v[64:67], a[192:195], v[80:83], v[184:187]\n\t" \
  "v_exp_f32 v101, v101\n\t" \
  "v_mfma_f32_16x16x32_bf16 v[64:67], a[196:199], v[84:87], v[64:67]\n\t" \
  "ds_read_b64_tr_b16 a[160:161], v210 offset:512\n\t" \
  "v_cvt_pk_bf16_f32 v154, v100, v101\n\t" \
  "v_mfma_f32_16x16x32_bf16 v[64:67], a[200:203], v[88:91], v[64:67]\n\t" \
  "v_max3_f32 v203, v203, v44, v45\n\t" \
  "v_max3_f32 v192, v192, v46, v47\n\t" \
  "v_mfma_f32_16x16x32_bf16 v[64:67], a[204:207], v[92:95], v[64:67]\n\t" \
  "v_max_f32 v192, v192, v203\n\t" \
  "v_cmp_lt_f32 vcc, 0x42800000, v192\n\t" \
  "v_mfma_f32_16x16x32_bf16 v[68:71], a[220:223], v[92:95], v[184:187]\n\t" \
  "v_exp_f32 v102, v102\n\t" \
  "v_mfma_f32_16x16x32_bf16 v[68:71], a[216:219], v[88:91], v[68:71]\n\t" \
  "s_cbranch_vccnz .Lsf_c41\n\t" \
  ".Lsf_r41:\n\t" \
  "v_mfma_f32_16x16x32_bf16 v[68:71], a[212:215], v[84:87], v[68:71]\n\t" \
  "ds_read_b64_tr_b16 a[162:163], v210 offset:4608\n\t" \
  "v_max3_f32 v192, v64, v65, v66\n\t" \
  "v_mfma_f32_16x16x32_bf16 v[68:71], a[208:211], v[80:83], v[68:71]\n\t" \
  "ds_read_b64_tr_b16 a[164:165], v210 offset:8704\n\t" \
  "v_exp_f32 v103, v103\n\t" \
  "v_mfma_f32_16x16x32_bf16 v[72:75], a[224:227], v[80:83], v[184:187]\n\t" \
  "ds_read_b64_tr_b16 a[166:167], v210 offset:12800\n\t" \
  "v_exp_f32 v104, v104\n\t" \
  "v_mfma_f32_16x16x32_bf16 v[72:75], a[228:231], v[84:87], v[72:75]\n\t" \
  "ds_read_b64_tr_b16 a[168:169], v211 offset:512\n\t" \
  "v_exp_f32 v105, v105\n\t" \
  "v_mfma_f32_16x16x32_bf16 v[72:75], a[232:235], v[88:91], v[72:75]\n\t" \
  "v_max3_f32 v203, v67, v68, v69\n\t" \
  "v_max3_f32 v192, v192, v70, v71\n\t" \
  "v_mfma_f32_16x16x32_bf16 v[72:75], a[236:239], v[92:95], v[72:75]\n\t" \
  "ds_read_b64_tr_b16 a[170:171], v211 offset:4608\n\t" \
  "v_exp_f32 v106, v106\n\t" \
  "v_mfma_f32_16x16x32_bf16 v[76:79], a[252:255], v[92:95], v[184:187]\n\t" \
  "ds_read_b64_tr_b16 a[172:173], v211 offset:8704\n\t" \
  "v_exp_f32 v107, v107\n\t" \
  "v_mfma_f32_16x16x32_bf16 v[76:79], a[248:251], v[88:91], v[76:79]\n\t" \
  "ds_read_b64_tr_b16 a[174:175], v211 offset:12800\n\t" \
  "v_cvt_pk_bf16_f32 v155, v102, v103\n\t" \
  "v_mfma_f32_16x16x32_bf16 v[76:79], a[244:247], v[84:87], v[76:79]\n\t" \
  "v_max3_f32 v203, v203, v72, v73\n\t" \
  "v_max3_f32 v192, v192, v74, v75\n\t" \
  "v_mfma_f32_16x16x32_bf16 v[76:79], a[240:243], v[80:83], v[76:79]\n\t" \
  "ds_read_b64_tr_b16 a[176:177], v210 offset:768\n\t" \
  "v_exp_f32 v108, v108\n\t" \
  "v_mfma_f32_16x16x32_bf16 v[96:99], a[192:195], v[112:115], v[188:191]\n\t" \
  "ds_read_b64_tr_b16 a[178:179], v210 offset:4864\n\t" \
  "v_exp_f32 v109, v109\n\t" \
  "v_mfma_f32_16x16x32_bf16 v[96:99], a[196:199], v[116:119], v[96:99]\n\t" \
  "ds_read_b64_tr_b16 a[180:181], v210 offset:8960\n\t" \
  "v_cvt_pk_bf16_f32 v156, v104, v105\n\t" \
  "v_mfma_f32_16x16x32_bf16 v[96:99], a[200:203], v[120:123], v[96:99]\n\t" \
  "v_max3_f32 v203, v203, v76, v77\n\t" \
  "v_max3_f32 v192, v192, v78, v79\n\t" \
  "v_mfma_f32_16x16x32_bf16 v[96:99], a[204:207], v[124:127], v[96:99]\n\t" \
  "v_max_f32 v192, v192, v203\n\t" \
  "v_cmp_lt_f32 vcc, 0x42800000, v192\n\t" \
  "v_mfma_f32_16x16x32_bf16 v[100:103], a[220:223], v[124:127], v[188:191]\n\t" \
  "ds_read_b64_tr_b16 a[182:183], v210 offset:13056\n\t" \
  "v_exp_f32 v110, v110\n\t" \
  "v_mfma_f32_16x16x32_bf16 v[100:103], a[216:219], v[120:123], v[100:103]\n\t" \
  "s_cbranch_vccnz .Lsf_c42\n\t" \
  ".Lsf_r42:\n\t" \
  "v_mfma_f32_16x16x32_bf16 v[100:103], a[212:215], v[116:119], v[100:103]\n\t" \
  "v_max3_f32 v192, v96, v97, v98\n\t" \
  "v_exp_f32 v111, v111\n\t" \
  "v_mfma_f32_16x16x32_bf16 v[100:103], a[208:211], v[112:115], v[100:103]\n\t" \
  "ds_read_b64_tr_b16 a[184:185], v211 offset:768\n\t" \
  "v_cvt_pk_bf16_f32 v157, v106, v107\n\t" \
  "v_mfma_f32_16x16x32_bf16 v[104:107], a[224:227], v[112:115], v[188:191]\n\t" \
  "ds_read_b64_tr_b16 a[186:187], v211 offset:4864\n\t" \
  "v_cvt_pk_bf16_f32 v158, v108, v109\n\t" \
  "v_mfma_f32_16x16x32_bf16 v[104:107], a[228:231], v[116:119], v[104:107]\n\t" \
  "ds_read_b64_tr_b16 a[188:189], v211 offset:8960\n\t" \
  "v_cvt_pk_bf16_f32 v159, v110, v111\n\t" \
  "v_mfma_f32_16x16x32_bf16 v[104:107], a[232:235], v[120:123], v[104:107]\n\t" \
  "v_max3_f32 v203, v99, v100, v101\n\t" \
  "v_max3_f32 v192, v192, v102, v103\n\t" \
  "v_mfma_f32_16x16x32_bf16 v[104:107], a[236:239], v[124:127], v[104:107]\n\t" \
  "ds_read_b64_tr_b16 a[190:191], v211 offset:13056\n\t" \
  "s_nop 0\n\t" \
  "v_mfma_f32_16x16x32_bf16 v[108:111], a[252:255], v[124:127], v[188:191]\n\t" \
  "v_exp_f32 v0, v0\n\t" \
  "v_mfma_f32_16x16x32_bf16 v[108:111], a[248:251], v[120:123], v[108:111]\n\t" \
  "v_exp_f32 v1, v1\n\t" \
  "v_mfma_f32_16x16x32_bf16 v[108:111], a[244:247], v[116:119], v[108:111]\n\t" \
  "s_waitcnt vmcnt(0) lgkmcnt(0)\n\t" \
  "v_max3_f32 v203, v203, v104, v105\n\t" \
  "v_max3_f32 v192, v192, v106, v107\n\t" \
  "v_mfma_f32_16x16x32_bf16 v[108:111], a[240:243], v[112:115], v[108:111]\n\t" \
  "s_barrier\n\t" \
  "v_mfma_f32_16x16x32_bf16 a[0:3], a[128:131], v[128:131], a[0:3]\n\t" \
  "v_exp_f32 v2, v2\n\t" \
  "v_mfma_f32_16x16x32_bf16 a[0:3], a[132:135], v[132:135], a[0:3]\n\t" \
  "v_exp_f32 v3, v3\n\t" \
  "v_mfma_f32_16x16x32_bf16 a[4:7], a[136:139], v[128:131], a[4:7]\n\t" \
  "v_exp_f32 v4, v4\n\t" \
  "v_mfma_f32_16x16x32_bf16 a[4:7], a[140:143], v[132:135], a[4:7]\n\t" \
  "v_max3_f32 v203, v203, v108, v109\n\t" \
  "v_max3_f32 v192, v192, v110, v111\n\t" \
  "v_mfma_f32_16x16x32_bf16 a[8:11], a[144:147], v[128:131], a[8:11]\n\t" \
  "v_max_f32 v192, v192, v203\n\t" \
  "v_cmp_lt_f32 vcc, 0x42800000, v192\n\t" \
  "v_mfma_f32_16x16x32_bf16 a[8:11], a[148:151], v[132:135], a[8:11]\n\t" \
  "v_exp_f32 v5, v5\n\t" \
  "v_mfma_f32_16x16x32_bf16 a[12:15], a[152:155], v[128:131], a[12:15]\n\t" \
  "s_cbranch_vccnz .Lsf_c43\n\t" \
  ".Lsf_r43:\n\t" \
  "v_mfma_f32_16x16x32_bf16 a[12:15], a[156:159], v[132:135], a[12:15]\n\t" \
  "s_add_u32 m0, s38, 49152\n\t" \
  "v_exp_f32 v6, v6\n\t" \
  "v_mfma_f32_16x16x32_bf16 a[16:19], a[160:163], v[128:131], a[16:19]\n\t" \
  "s_add_u32 s96, s38, 98304\n\t" \
  "v_exp_f32 v7, v7\n\t" \
  "v_mfma_f32_16x16x32_bf16 a[16:19], a[164:167], v[132:135], a[16:19]\n\t" \
  "v_exp_f32 v8, v8\n\t" \
  "v_mfma_f32_16x16x32_bf16 a[20:23], a[168:171], v[128:131], a[20:23]\n\t" \
  "buffer_load_dwordx4 v224, s[60:63], s96 offen offset:0 lds\n\t" \
  "v_mfma_f32_16x16x32_bf16 a[20:23], a[172:175], v[132:135], a[20:23]\n\t" \
  "v_exp_f32 v9, v9\n\t" \
  "v_mfma_f32_16x16x32_bf16 a[24:27], a[176:179], v[128:131], a[24:27]\n\t" \
  "v_exp_f32 v10, v10\n\t" \
  "v_mfma_f32_16x16x32_bf16 a[24:27], a[180:183], v[132:135], a[24:27]\n\t" \
  "v_exp_f32 v11, v11\n\t" \
  "v_mfma_f32_16x16x32_bf16 a[28:31], a[184:187], v[128:131], a[28:31]\n\t" \
  "v_exp_f32 v12, v12\n\t" \
  "v_mfma_f32_16x16x32_bf16 a[28:31], a[188:191], v[132:135], a[28:31]\n\t" \
  "v_exp_f32 v13, v13\n\t" \
  "v_smfmac_f32_16x16x64_bf16 v[160:163], v[216:219], v[128:135], v208\n\t" \
  "ds_read_b128 a[192:195], v220 offset:16384\n\t" \
  "v_cvt_pk_bf16_f32 v128, v0, v1\n\t" \
  "v_mfma_f32_16x16x32_bf16 a[32:35], a[128:131], v[136:139], a[32:35]\n\t" \
  "buffer_load_dwordx4 v225, s[60:63], s96 offen offset:1024 lds\n\t" \
  "v_mfma_f32_16x16x32_bf16 a[32:35], a[132:135], v[140:143], a[32:35]\n\t" \
  "v_exp_f32 v14, v14\n\t" \
  "v_mfma_f32_16x16x32_bf16 a[36:39], a[136:139], v[136:139], a[36:39]\n\t" \
  "v_exp_f32 v15, v15\n\t" \
  "v_mfma_f32_16x16x32_bf16 a[36:39], a[140:143], v[140:143], a[36:39]\n\t" \
  "ds_read_b128 a[196:199], v221 offset:16384\n\t" \
  "v_cvt_pk_bf16_f32 v129, v2, v3\n\t" \
  "v_mfma_f32_16x16x32_bf16 a[40:43], a[144:147], v[136:139], a[40:43]\n\t" \
  "v_exp_f32 v32, v32\n\t" \
  "v_mfma_f32_16x16x32_bf16 a[40:43], a[148:151], v[140:143], a[40:43]\n\t" \
  "v_exp_f32 v33, v33\n\t" \
  "v_mfma_f32_16x16x32_bf16 a[44:47], a[152:155], v[136:139], a[44:47]\n\t" \
  "buffer_load_dwordx4 v226, s[60:63], s96 offen offset:2048 lds\n\t" \
  "v_mfma_f32_16x16x32_bf16 a[44:47], a[156:159], v[140:143], a[44:47]\n\t" \
  "ds_read_b128 a[200:203], v222 offset:16384\n\t" \
  "v_cvt_pk_bf16_f32 v130, v4, v5\n\t" \
  "v_mfma_f32_16x16x32_bf16 a[48:51], a[160:163], v[136:139], a[48:51]\n\t" \
  "v_exp_f32 v34, v34\n\t" \
  "v_mfma_f32_16x16x32_bf16 a[48:51], a[164:167], v[140:143], a[48:51]\n\t" \
  "ds_read_b128 a[204:207], v223 offset:16384\n\t" \
  "v_cvt_pk_bf16_f32 v131, v6, v7\n\t" \
  "v_mfma_f32_16x16x32_bf16 a[52:55], a[168:171], v[136:139], a[52:55]\n\t" \
  "v_exp_f32 v35, v35\n\t" \
  "v_mfma_f32_16x16x32_bf16 a[52:55], a[172:175], v[140:143], a[52:55]\n\t" \
  "ds_read_b128 a[208:211], v220 offset:20480\n\t" \
  "v_cvt_pk_bf16_f32 v132, v8, v9\n\t" \
  "v_mfma_f32_16x16x32_bf16 a[56:59], a[176:179], v[136:139], a[56:59]\n\t" \
  "buffer_load_dwordx4 v227, s[60:63], s96 offen offset:3072 lds\n\t" \
  "v_mfma_f32_16x16x32_bf16 a[56:59], a[180:183], v[140:143], a[56:59]\n\t" \
  "v_exp_f32 v36, v36\n\t" \
  "v_mfma_f32_16x16x32_bf16 a[60:63], a[184:187], v[136:139], a[60:63]\n\t" \
  "s_add_u32 m0, s38, 147456\n\t" \
  "ds_read_b128 a[212:215], v221 offset:20480\n\t" \
  "v_cvt_pk_bf16_f32 v133, v10, v11\n\t" \
  "v_mfma_f32_16x16x32_bf16 a[60:63], a[188:191], v[140:143], a[60:63]\n\t" \
  "s_add_u32 s96, s38, 81920\n\t" \
  "v_exp_f32 v37, v37\n\t" \
  "v_smfmac_f32_16x16x64_bf16 v[162:165], v[216:219], v[136:143], v208\n\t" \
  "ds_read_b128 a[216:219], v222 offset:20480\n\t" \
  "v_cvt_pk_bf16_f32 v134, v12, v13\n\t" \
  "v_mfma_f32_16x16x32_bf16 a[64:67], a[128:131], v[144:147], a[64:67]\n\t" \
  "v_exp_f32 v38, v38\n\t" \
  "v_mfma_f32_16x16x32_bf16 a[64:67], a[132:135], v[148:151], a[64:67]\n\t" \
  "buffer_load_dwordx4 v230, s[64:67], s96 offen offset:0 lds\n\t" \
  "v_mfma_f32_16x16x32_bf16 a[68:71], a[136:139], v[144:147], a[68:71]\n\t" \
  "ds_read_b128 a[220:223], v223 offset:20480\n\t" \
  "v_cvt_pk_bf16_f32 v135, v14, v15\n\t" \
  "v_mfma_f32_16x16x32_bf16 a[68:71], a[140:143], v[148:151], a[68:71]\n\t" \
  "v_exp_f32 v39, v39\n\t" \
  "v_mfma_f32_16x16x32_bf16 a[72:75], a[144:147], v[144:147], a[72:75]\n\t" \
  "v_exp_f32 v40, v40\n\t" \
  "v_mfma_f32_16x16x32_bf16 a[72:75], a[148:151], v[148:151], a[72:75]\n\t" \
  "ds_read_b128 a[224:227], v220 offset:24576\n\t" \
  "v_cvt_pk_bf16_f32 v136, v32, v33\n\t" \
  "v_mfma_f32_16x16x32_bf16 a[76:79], a[152:155], v[144:147], a[76:79]\n\t" \
  "ds_read_b128 a[228:231], v221 offset:24576\n\t" \
  "v_cvt_pk_bf16_f32 v137, v34, v35\n\t" \
  "v_mfma_f32_16x16x32_bf16 a[76:79], a[156:159], v[148:151], a[76:79]\n\t" \
  "v_exp_f32 v41, v41\n\t" \
  "v_mfma_f32_16x16x32_bf16 a[80:83], a[160:163], v[144:147], a[80:83]\n\t" \
  "buffer_load_dwordx4 v231, s[64:67], s96 offen offset:1024 lds\n\t" \
  "v_mfma_f32_16x16x32_bf16 a[80:83], a[164:167], v[148:151], a[80:83]\n\t" \
  "v_exp_f32 v42, v42\n\t" \
  "v_mfma_f32_16x16x32_bf16 a[84:87], a[168:171], v[144:147], a[84:87]\n\t" \
  "ds_read_b128 a[232:235], v222 offset:24576\n\t" \
  "v_cvt_pk_bf16_f32 v138, v36, v37\n\t" \
  "v_mfma_f32_16x16x32_bf16 a[84:87], a[172:175], v[148:151], a[84:87]\n\t" \
  "v_exp_f32 v43, v43\n\t" \
  "v_mfma_f32_16x16x32_bf16 a[88:91], a[176:179], v[144:147], a[88:91]\n\t" \
  "ds_read_b128 a[236:239], v223 offset:24576\n\t" \
  "v_cvt_pk_bf16_f32 v139, v38, v39\n\t" \
  "v_mfma_f32_16x16x32_bf16 a[88:91], a[180:183], v[148:151], a[88:91]\n\t" \
  "v_exp_f32 v44, v44\n\t" \
  "v_mfma_f32_16x16x32_bf16 a[92:95], a[184:187], v[144:147], a[92:95]\n\t" \
  "buffer_load_dwordx4 v230, s[64:67], s96 offen offset:2048 lds\n\t" \
  "v_mfma_f32_16x16x32_bf16 a[92:95], a[188:191], v[148:151], a[92:95]\n\t" \
  "ds_read_b128 a[240:243], v220 offset:28672\n\t" \
  "v_cvt_pk_bf16_f32 v140, v40, v41\n\t" \
  "v_smfmac_f32_16x16x64_bf16 v[164:167], v[216:219], v[144:151], v208\n\t" \
  "ds_read_b128 a[244:247], v221 offset:28672\n\t" \
  "v_cvt_pk_bf16_f32 v141, v42, v43\n\t" \
  "v_mfma_f32_16x16x32_bf16 a[96:99], a[128:131], v[152:155], a[96:99]\n\t" \
  "v_exp_f32 v45, v45\n\t" \
  "v_mfma_f32_16x16x32_bf16 a[96:99], a[132:135], v[156:159], a[96:99]\n\t" \
  "v_exp_f32 v46, v46\n\t" \
  "v_mfma_f32_16x16x32_bf16 a[100:103], a[136:139], v[152:155], a[100:103]\n\t" \
  "v_exp_f32 v47, v47\n\t" \
  "v_mfma_f32_16x16x32_bf16 a[100:103], a[140:143], v[156:159], a[100:103]\n\t" \
  "buffer_load_dwordx4 v231, s[64:67], s96 offen offset:3072 lds\n\t" \
  "v_mfma_f32_16x16x32_bf16 a[104:107], a[144:147], v[152:155], a[104:107]\n\t" \
  "ds_read_b128 a[248:251], v222 offset:28672\n\t" \
  "v_cvt_pk_bf16_f32 v142, v44, v45\n\t" \
  "v_mfma_f32_16x16x32_bf16 a[104:107], a[148:151], v[156:159], a[104:107]\n\t" \
  "ds_read_b128 a[252:255], v223 offset:28672\n\t" \
  "v_cvt_pk_bf16_f32 v143, v46, v47\n\t" \
  "v_mfma_f32_16x16x32_bf16 a[108:111], a[152:155], v[152:155], a[108:111]\n\t" \
  "v_exp_f32 v64, v64\n\t" \
  "v_mfma_f32_16x16x32_bf16 a[108:111], a[156:159], v[156:159], a[108:111]\n\t" \
  "v_exp_f32 v65, v65\n\t" \
  "v_mfma_f32_16x16x32_bf16 a[112:115], a[160:163], v[152:155], a[112:115]\n\t" \
  "v_exp_f32 v66, v66\n\t" \
  "v_mfma_f32_16x16x32_bf16 a[112:115], a[164:167], v[156:159], a[112:115]\n\t" \
  "v_exp_f32 v67, v67\n\t" \
  "v_mfma_f32_16x16x32_bf16 a[116:119], a[168:171], v[152:155], a[116:119]\n\t" \
  "v_exp_f32 v68, v68\n\t" \
  "v_mfma_f32_16x16x32_bf16 a[116:119], a[172:175], v[156:159], a[116:119]\n\t" \
  "v_exp_f32 v69, v69\n\t" \
  "v_mfma_f32_16x16x32_bf16 a[120:123], a[176:179], v[152:155], a[120:123]\n\t" \
  "v_exp_f32 v70, v70\n\t" \
  "v_mfma_f32_16x16x32_bf16 a[120:123], a[180:183], v[156:159], a[120:123]\n\t" \
  "v_exp_f32 v71, v71\n\t" \
  "v_mfma_f32_16x16x32_bf16 a[124:127], a[184:187], v[152:155], a[124:127]\n\t" \
  "v_exp_f32 v72, v72\n\t" \
  "v_mfma_f32_16x16x32_bf16 a[124:127], a[188:191], v[156:159], a[124:127]\n\t" \
  "v_exp_f32 v73, v73\n\t" \
  "v_smfmac_f32_16x16x64_bf16 v[166:169], v[216:219], v[152:159], v208\n\t" \
  "s_waitcnt lgkmcnt(12)\n\t" \
  "s_cmp_lg_u32 s99, 0\n\t" \
  "s_cbranch_scc1 .Lsf_p4\n\t" \
  ".Lsf_q4:\n\t" \
  "v_mfma_f32_16x16x32_bf16 v[0:3], a[192:195], v[16:19], v[176:179]\n\t" \
  "ds_read_b64_tr_b16 a[128:129], v228 offset:0\n\t" \
  "v_cvt_pk_bf16_f32 v144, v64, v65\n\t" \
  "v_mfma_f32_16x16x32_bf16 v[0:3], a[196:199], v[20:23], v[0:3]\n\t" \
  "v_exp_f32 v74, v74\n\t" \
  "v_mfma_f32_16x16x32_bf16 v[0:3], a[200:203], v[24:27], v[0:3]\n\t" \
  "ds_read_b64_tr_b16 a[130:131], v228 offset:4096\n\t" \
  "v_cvt_pk_bf16_f32 v145, v66, v67\n\t" \
  "v_mfma_f32_16x16x32_bf16 v[0:3], a[204:207], v[28:31], v[0:3]\n\t" \
  "s_waitcnt lgkmcnt(10)\n\t" \
  "v_exp_f32 v75, v75\n\t" \
  "v_mfma_f32_16x16x32_bf16 v[4:7], a[220:223], v[28:31], v[176:179]\n\t" \
  "ds_read_b64_tr_b16 a[132:133], v228 offset:8192\n\t" \
  "v_cvt_pk_bf16_f32 v146, v68, v69\n\t" \
  "v_mfma_f32_16x16x32_bf16 v[4:7], a[216:219], v[24:27], v[4:7]\n\t" \
  "v_exp_f32 v76, v76\n\t" \
  "v_mfma_f32_16x16x32_bf16 v[4:7], a[212:215], v[20:23], v[4:7]\n\t" \
  "ds_read_b64_tr_b16 a[134:135], v228 offset:12288\n\t" \
  "v_max3_f32 v192, v0, v1, v2\n\t" \
  "v_mfma_f32_16x16x32_bf16 v[4:7], a[208:211], v[16:19], v[4:7]\n\t" \
  "s_waitcnt lgkmcnt(8)\n\t" \
  "ds_read_b64_tr_b16 a[136:137], v229 offset:0\n\t" \
  "v_cvt_pk_bf16_f32 v147, v70, v71\n\t" \
  "v_mfma_f32_16x16x32_bf16 v[8:11], a[224:227], v[16:19], v[176:179]\n\t" \
  "v_exp_f32 v77, v77\n\t" \
  "v_mfma_f32_16x16x32_bf16 v[8:11], a[228:231], v[20:23], v[8:11]\n\t" \
  "ds_read_b64_tr_b16 a[138:139], v229 offset:4096\n\t" \
  "v_cvt_pk_bf16_f32 v148, v72, v73\n\t" \
  "v_mfma_f32_16x16x32_bf16 v[8:11], a[232:235], v[24:27], v[8:11]\n\t" \
  "v_max3_f32 v203, v3, v4, v5\n\t" \
  "v_max3_f32 v192, v192, v6, v7\n\t" \
  "v_mfma_f32_16x16x32_bf16 v[8:11], a[236:239], v[28:31], v[8:11]\n\t" \
  "s_waitcnt lgkmcnt(6)\n\t" \
  "ds_read_b64_tr_b16 a[140:141], v229 offset:8192\n\t" \
  "v_cvt_pk_bf16_f32 v149, v74, v75\n\t" \
  "v_mfma_f32_16x16x32_bf16 v[12:15], a[252:255], v[28:31], v[176:179]\n\t" \
  "v_exp_f32 v78, v78\n\t" \
  "v_mfma_f32_16x16x32_bf16 v[12:15], a[248:251], v[24:27], v[12:15]\n\t" \
  "ds_read_b64_tr_b16 a[142:143], v229 offset:12288\n\t" \
  "v_cvt_pk_bf16_f32 v150, v76, v77\n\t" \
  "v_mfma_f32_16x16x32_bf16 v[12:15], a[244:247], v[20:23], v[12:15]\n\t" \
  "v_max3_f32 v203, v203, v8, v9\n\t" \
  "v_max3_f32 v192, v192, v10, v11\n\t" \
  "v_mfma_f32_16x16x32_bf16 v[12:15], a[240:243], v[16:19], v[12:15]\n\t" \
  "v_exp_f32 v79, v79\n\t" \
  "v_mfma_f32_16x16x32_bf16 v[32:35], a[192:195], v[48:51], v[180:183]\n\t" \
  "ds_read_b64_tr_b16 a[144:145], v228 offset:256\n\t" \
  "v_cvt_pk_bf16_f32 v151, v78, v79\n\t" \
  "v_mfma_f32_16x16x32_bf16 v[32:35], a[196:199], v[52:55], v[32:35]\n\t" \
  "v_exp_f32 v96, v96\n\t" \
  "v_mfma_f32_16x16x32_bf16 v[32:35], a[200:203], v[56:59], v[32:35]\n\t" \
  "ds_read_b64_tr_b16 a[146:147], v228 offset:4352\n\t" \
  "v_max3_f32 v203, v203, v12, v13\n\t" \
  "v_mfma_f32_16x16x32_bf16 v[32:35], a[204:207], v[60:63], v[32:35]\n\t" \
  "v_max3_f32 v192, v192, v14, v15\n\t" \
  "v_max_f32 v192, v192, v203\n\t" \
  "v_mfma_f32_16x16x32_bf16 v[36:39], a[220:223], v[60:63], v[180:183]\n\t" \
  "ds_read_b64_tr_b16 a[148:149], v228 offset:8448\n\t" \
  "v_cmp_lt_f32 vcc, 0x42800000, v192\n\t" \
  "v_mfma_f32_16x16x32_bf16 v[36:39], a[216:219], v[56:59], v[36:39]\n\t" \
  "v_exp_f32 v97, v97\n\t" \
  "v_mfma_f32_16x16x32_bf16 v[36:39], a[212:215], v[52:55], v[36:39]\n\t" \
  "s_cbranch_vccnz .Lsf_c50\n\t" \
  ".Lsf_r50:\n\t" \
  "v_mfma_f32_16x16x32_bf16 v[36:39], a[208:211], v[48:51], v[36:39]\n\t" \
  "ds_read_b64_tr_b16 a[150:151], v228 offset:12544\n\t" \
  "v_max3_f32 v192, v32, v33, v34\n\t" \
  "v_mfma_f32_16x16x32_bf16 v[40:43], a[224:227], v[48:51], v[180:183]\n\t" \
  "ds_read_b64_tr_b16 a[152:153], v229 offset:256\n\t" \
  "v_cvt_pk_bf16_f32 v152, v96, v97\n\t" \
  "v_mfma_f32_16x16x32_bf16 v[40:43], a[228:231], v[52:55], v[40:43]\n\t" \
  "v_exp_f32 v98, v98\n\t" \
  "v_mfma_f32_16x16x32_bf16 v[40:43], a[232:235], v[56:59], v[40:43]\n\t" \
  "v_max3_f32 v203, v35, v36, v37\n\t" \
  "v_max3_f32 v192, v192, v38, v39\n\t" \
  "v_mfma_f32_16x16x32_bf16 v[40:43], a[236:239], v[60:63], v[40:43]\n\t" \
  "v_exp_f32 v99, v99\n\t" \
  "v_mfma_f32_16x16x32_bf16 v[44:47], a[252:255], v[60:63], v[180:183]\n\t" \
  "ds_read_b64_tr_b16 a[154:155], v229 offset:4352\n\t" \
  "v_cvt_pk_bf16_f32 v153, v98, v99\n\t" \
  "v_mfma_f32_16x16x32_bf16 v[44:47], a[248:251], v[56:59], v[44:47]\n\t" \
  "v_exp_f32 v100, v100\n\t" \
  "v_mfma_f32_16x16x32_bf16 v[44:47], a[244:247], v[52:55], v[44:47]\n\t" \
  "ds_read_b64_tr_b16 a[156:157], v229 offset:8448\n\t" \
  "v_max3_f32 v203, v203, v40, v41\n\t" \
  "v_mfma_f32_16x16x32_bf16 v[44:47], a[240:243], v[48:51], v[44:47]\n\t" \
  "ds_read_b64_tr_b16 a[158:159], v229 offset:12544\n\t" \
  "v_max3_f32 v192, v192, v42, v43\n\t" \
  "v_mfma_f32_16x16x32_bf16 v[64:67], a[192:195], v[80:83], v[184:187]\n\t" \
  "v_exp_f32 v101, v101\n\t" \
  "v_mfma_f32_16x16x32_bf16 v[64:67], a[196:199], v[84:87], v[64:67]\n\t" \
  "ds_read_b64_tr_b16 a[160:161], v228 offset:512\n\t" \
  "v_cvt_pk_bf16_f32 v154, v100, v101\n\t" \
  "v_mfma_f32_16x16x32_bf16 v[64:67], a[200:203], v[88:91], v[64:67]\n\t" \
  "v_max3_f32 v203, v203, v44, v45\n\t" \
  "v_max3_f32 v192, v192, v46, v47\n\t" \
  "v_mfma_f32_16x16x32_bf16 v[64:67], a[204:207], v[92:95], v[64:67]\n\t" \
  "v_max_f32 v192, v192, v203\n\t" \
  "v_cmp_lt_f32 vcc, 0x42800000, v192\n\t" \
  "v_mfma_f32_16x16x32_bf16 v[68:71], a[220:223], v[92:95], v[184:187]\n\t" \
  "v_exp_f32 v102, v102\n\t" \
  "v_mfma_f32_16x16x32_bf16 v[68:71], a[216:219], v[88:91], v[68:71]\n\t" \
  "s_cbranch_vccnz .Lsf_c51\n\t" \
  ".Lsf_r51:\n\t" \
  "v_mfma_f32_16x16x32_bf16 v[68:71], a[212:215], v[84:87], v[68:71]\n\t" \
  "ds_read_b64_tr_b16 a[162:163], v228 offset:4608\n\t" \
  "v_max3_f32 v192, v64, v65, v66\n\t" \
  "v_mfma_f32_16x16x32_bf16 v[68:71], a[208:211], v[80:83], v[68:71]\n\t" \
  "ds_read_b64_tr_b16 a[164:165], v228 offset:8704\n\t" \
  "v_exp_f32 v103, v103\n\t" \
  "v_mfma_f32_16x16x32_bf16 v[72:75], a[224:227], v[80:83], v[184:187]\n\t" \
  "ds_read_b64_tr_b16 a[166:167], v228 offset:12800\n\t" \
  "v_exp_f32 v104, v104\n\t" \
  "v_mfma_f32_16x16x32_bf16 v[72:75], a[228:231], v[84:87], v[72:75]\n\t" \
  "ds_read_b64_tr_b16 a[168:169], v229 offset:512\n\t" \
  "v_exp_f32 v105, v105\n\t" \
  "v_mfma_f32_16x16x32_bf16 v[72:75], a[232:235], v[88:91], v[72:75]\n\t" \
  "v_max3_f32 v203, v67, v68, v69\n\t" \
  "v_max3_f32 v192, v192, v70, v71\n\t" \
  "v_mfma_f32_16x16x32_bf16 v[72:75], a[236:239], v[92:95], v[72:75]\n\t" \
  "ds_read_b64_tr_b16 a[170:171], v229 offset:4608\n\t" \
  "v_exp_f32 v106, v106\n\t" \
  "v_mfma_f32_16x16x32_bf16 v[76:79], a[252:255], v[92:95], v[184:187]\n\t" \
  "ds_read_b64_tr_b16 a[172:173], v229 offset:8704\n\t" \
  "v_exp_f32 v107, v107\n\t" \
  "v_mfma_f32_16x16x32_bf16 v[76:79], a[248:251], v[88:91], v[76:79]\n\t" \
  "ds_read_b64_tr_b16 a[174:175], v229 offset:12800\n\t" \
  "v_cvt_pk_bf16_f32 v155, v102, v103\n\t" \
  "v_mfma_f32_16x16x32_bf16 v[76:79], a[244:247], v[84:87], v[76:79]\n\t" \
  "v_max3_f32 v203, v203, v72, v73\n\t" \
  "v_max3_f32 v192, v192, v74, v75\n\t" \
  "v_mfma_f32_16x16x32_bf16 v[76:79], a[240:243], v[80:83], v[76:79]\n\t" \
  "ds_read_b64_tr_b16 a[176:177], v228 offset:768\n\t" \
  "v_exp_f32 v108, v108\n\t" \
  "v_mfma_f32_16x16x32_bf16 v[96:99], a[192:195], v[112:115], v[188:191]\n\t" \
  "ds_read_b64_tr_b16 a[178:179], v228 offset:4864\n\t" \
  "v_exp_f32 v109, v109\n\t" \
  "v_mfma_f32_16x16x32_bf16 v[96:99], a[196:199], v[116:119], v[96:99]\n\t" \
  "ds_read_b64_tr_b16 a[180:181], v228 offset:8960\n\t" \
  "v_cvt_pk_bf16_f32 v156, v104, v105\n\t" \
  "v_mfma_f32_16x16x32_bf16 v[96:99], a[200:203], v[120:123], v[96:99]\n\t" \
  "v_max3_f32 v203, v203, v76, v77\n\t" \
  "v_max3_f32 v192, v192, v78, v79\n\t" \
  "v_mfma_f32_16x16x32_bf16 v[96:99], a[204:207], v[124:127], v[96:99]\n\t" \
  "v_max_f32 v192, v192, v203\n\t" \
  "v_cmp_lt_f32 vcc, 0x42800000, v192\n\t" \
  "v_mfma_f32_16x16x32_bf16 v[100:103], a[220:223], v[124:127], v[188:191]\n\t" \
  "ds_read_b64_tr_b16 a[182:183], v228 offset:13056\n\t" \
  "v_exp_f32 v110, v110\n\t" \
  "v_mfma_f32_16x16x32_bf16 v[100:103], a[216:219], v[120:123], v[100:103]\n\t" \
  "s_cbranch_vccnz .Lsf_c52\n\t" \
  ".Lsf_r52:\n\t" \
  "v_mfma_f32_16x16x32_bf16 v[100:103], a[212:215], v[116:119], v[100:103]\n\t" \
  "v_max3_f32 v192, v96, v97, v98\n\t" \
  "v_exp_f32 v111, v111\n\t" \
  "v_mfma_f32_16x16x32_bf16 v[100:103], a[208:211], v[112:115], v[100:103]\n\t" \
  "ds_read_b64_tr_b16 a[184:185], v229 offset:768\n\t" \
  "v_cvt_pk_bf16_f32 v157, v106, v107\n\t" \
  "v_mfma_f32_16x16x32_bf16 v[104:107], a[224:227], v[112:115], v[188:191]\n\t" \
  "ds_read_b64_tr_b16 a[186:187], v229 offset:4864\n\t" \
  "v_cvt_pk_bf16_f32 v158, v108, v109\n\t" \
  "v_mfma_f32_16x16x32_bf16 v[104:107], a[228:231], v[116:119], v[104:107]\n\t" \
  "ds_read_b64_tr_b16 a[188:189], v229 offset:8960\n\t" \
  "v_cvt_pk_bf16_f32 v159, v110, v111\n\t" \
  "v_mfma_f32_16x16x32_bf16 v[104:107], a[232:235], v[120:123], v[104:107]\n\t" \
  "v_max3_f32 v203, v99, v100, v101\n\t" \
  "v_max3_f32 v192, v192, v102, v103\n\t" \
  "v_mfma_f32_16x16x32_bf16 v[104:107], a[236:239], v[124:127], v[104:107]\n\t" \
  "ds_read_b64_tr_b16 a[190:191], v229 offset:13056\n\t" \
  "s_nop 0\n\t" \
  "v_mfma_f32_16x16x32_bf16 v[108:111], a[252:255], v[124:127], v[188:191]\n\t" \
  "v_exp_f32 v0, v0\n\t" \
  "v_mfma_f32_16x16x32_bf16 v[108:111], a[248:251], v[120:123], v[108:111]\n\t" \
  "v_exp_f32 v1, v1\n\t" \
  "v_mfma_f32_16x16x32_bf16 v[108:111], a[244:247], v[116:119], v[108:111]\n\t" \
  "s_waitcnt vmcnt(0) lgkmcnt(0)\n\t" \
  "v_max3_f32 v203, v203, v104, v105\n\t" \
  "v_max3_f32 v192, v192, v106, v107\n\t" \
  "v_mfma_f32_16x16x32_bf16 v[108:111], a[240:243], v[112:115], v[108:111]\n\t" \
  "s_barrier\n\t" \
  "v_mfma_f32_16x16x32_bf16 a[0:3], a[128:131], v[128:131], a[0:3]\n\t" \
  "v_exp_f32 v2, v2\n\t" \
  "v_mfma_f32_16x16x32_bf16 a[0:3], a[132:135], v[132:135], a[0:3]\n\t" \
  "v_exp_f32 v3, v3\n\t" \
  "v_mfma_f32_16x16x32_bf16 a[4:7], a[136:139], v[128:131], a[4:7]\n\t" \
  "v_exp_f32 v4, v4\n\t" \
  "v_mfma_f32_16x16x32_bf16 a[4:7], a[140:143], v[132:135], a[4:7]\n\t" \
  "v_max3_f32 v203, v203, v108, v109\n\t" \
  "v_max3_f32 v192, v192, v110, v111\n\t" \
  "v_mfma_f32_16x16x32_bf16 a[8:11], a[144:147], v[128:131], a[8:11]\n\t" \
  "v_max_f32 v192, v192, v203\n\t" \
  "v_cmp_lt_f32 vcc, 0x42800000, v192\n\t" \
  "v_mfma_f32_16x16x32_bf16 a[8:11], a[148:151], v[132:135], a[8:11]\n\t" \
  "v_exp_f32 v5, v5\n\t" \
  "v_mfma_f32_16x16x32_bf16 a[12:15], a[152:155], v[128:131], a[12:15]\n\t" \
  "s_cbranch_vccnz .Lsf_c53\n\t" \
  ".Lsf_r53:\n\t" \
  "v_mfma_f32_16x16x32_bf16 a[12:15], a[156:159], v[132:135], a[12:15]\n\t" \
  "s_add_u32 m0, s38, 65536\n\t" \
  "v_exp_f32 v6, v6\n\t" \
  "v_mfma_f32_16x16x32_bf16 a[16:19], a[160:163], v[128:131], a[16:19]\n\t" \
  "s_add_u32 s96, s38, 114688\n\t" \
  "v_exp_f32 v7, v7\n\t" \
  "v_mfma_f32_16x16x32_bf16 a[16:19], a[164:167], v[132:135], a[16:19]\n\t" \
  "v_exp_f32 v8, v8\n\t" \
  "v_mfma_f32_16x16x32_bf16 a[20:23], a[168:171], v[128:131], a[20:23]\n\t" \
  "buffer_load_dwordx4 v224, s[60:63], s96 offen offset:0 lds\n\t" \
  "v_mfma_f32_16x16x32_bf16 a[20:23], a[172:175], v[132:135], a[20:23]\n\t" \
  "v_exp_f32 v9, v9\n\t" \
  "v_mfma_f32_16x16x32_bf16 a[24:27], a[176:179], v[128:131], a[24:27]\n\t" \
  "v_exp_f32 v10, v10\n\t" \
  "v_mfma_f32_16x16x32_bf16 a[24:27], a[180:183], v[132:135], a[24:27]\n\t" \
  "v_exp_f32 v11, v11\n\t" \
  "v_mfma_f32_16x16x32_bf16 a[28:31], a[184:187], v[128:131], a[28:31]\n\t" \
  "v_exp_f32 v12, v12\n\t" \
  "v_mfma_f32_16x16x32_bf16 a[28:31], a[188:191], v[132:135], a[28:31]\n\t" \
  "v_exp_f32 v13, v13\n\t" \
  "v_smfmac_f32_16x16x64_bf16 v[160:163], v[216:219], v[128:135], v208\n\t" \
  "ds_read_b128 a[192:195], v220 offset:0\n\t" \
  "v_cvt_pk_bf16_f32 v128, v0, v1\n\t" \
  "v_mfma_f32_16x16x32_bf16 a[32:35], a[128:131], v[136:139], a[32:35]\n\t" \
  "buffer_load_dwordx4 v225, s[60:63], s96 offen offset:1024 lds\n\t" \
  "v_mfma_f32_16x16x32_bf16 a[32:35], a[132:135], v[140:143], a[32:35]\n\t" \
  "v_exp_f32 v14, v14\n\t" \
  "v_mfma_f32_16x16x32_bf16 a[36:39], a[136:139], v[136:139], a[36:39]\n\t" \
  "ds_read_b128 a[196:199], v221 offset:0\n\t" \
  "v_cvt_pk_bf16_f32 v129, v2, v3\n\t" \
  "v_mfma_f32_16x16x32_bf16 a[36:39], a[140:143], v[140:143], a[36:39]\n\t" \
  "v_exp_f32 v15, v15\n\t" \
  "v_mfma_f32_16x16x32_bf16 a[40:43], a[144:147], v[136:139], a[40:43]\n\t" \
  "v_exp_f32 v32, v32\n\t" \
  "v_mfma_f32_16x16x32_bf16 a[40:43], a[148:151], v[140:143], a[40:43]\n\t" \
  "ds_read_b128 a[200:203], v222 offset:0\n\t" \
  "v_cvt_pk_bf16_f32 v130, v4, v5\n\t" \
  "v_mfma_f32_16x16x32_bf16 a[44:47], a[152:155], v[136:139], a[44:47]\n\t" \
  "buffer_load_dwordx4 v226, s[60:63], s96 offen offset:2048 lds\n\t" \
  "v_mfma_f32_16x16x32_bf16 a[44:47], a[156:159], v[140:143], a[44:47]\n\t" \
  "ds_read_b128 a[204:207], v223 offset:0\n\t" \
  "v_cvt_pk_bf16_f32 v131, v6, v7\n\t" \
  "v_mfma_f32_16x16x32_bf16 a[48:51], a[160:163], v[136:139], a[48:51]\n\t" \
  "v_exp_f32 v33, v33\n\t" \
  "v_mfma_f32_16x16x32_bf16 a[48:51], a[164:167], v[140:143], a[48:51]\n\t" \
  "v_exp_f32 v34, v34\n\t" \
  "v_mfma_f32_16x16x32_bf16 a[52:55], a[168:171], v[136:139], a[52:55]\n\t" \
  "ds_read_b128 a[208:211], v220 offset:4096\n\t" \
  "v_cvt_pk_bf16_f32 v132, v8, v9\n\t" \
  "v_mfma_f32_16x16x32_bf16 a[52:55], a[172:175], v[140:143], a[52:55]\n\t" \
  "v_exp_f32 v35, v35\n\t" \
  "v_mfma_f32_16x16x32_bf16 a[56:59], a[176:179], v[136:139], a[56:59]\n\t" \
  "buffer_load_dwordx4 v227, s[60:63], s96 offen offset:3072 lds\n\t" \
  "v_mfma_f32_16x16x32_bf16 a[56:59], a[180:183], v[140:143], a[56:59]\n\t" \
  "ds_read_b128 a[212:215], v221 offset:4096\n\t" \
  "v_cvt_pk_bf16_f32 v133, v10, v11\n\t" \
  "v_mfma_f32_16x16x32_bf16 a[60:63], a[184:187], v[136:139], a[60:63]\n\t" \
  "s_add_u32 m0, s38, 81920\n\t" \
  "v_exp_f32 v36, v36\n\t" \
  "v_mfma_f32_16x16x32_bf16 a[60:63], a[188:191], v[140:143], a[60:63]\n\t" \
  "s_add_u32 s96, s38, 98304\n\t" \
  "ds_read_b128 a[216:219], v222 offset:4096\n\t" \
  "v_cvt_pk_bf16_f32 v134, v12, v13\n\t" \
  "v_smfmac_f32_16x16x64_bf16 v[162:165], v[216:219], v[136:143], v208\n\t" \
  "ds_read_b128 a[220:223], v223 offset:4096\n\t" \
  "v_cvt_pk_bf16_f32 v135, v14, v15\n\t" \
  "v_mfma_f32_16x16x32_bf16 a[64:67], a[128:131], v[144:147], a[64:67]\n\t" \
  "v_exp_f32 v37, v37\n\t" \
  "v_mfma_f32_16x16x32_bf16 a[64:67], a[132:135], v[148:151], a[64:67]\n\t" \
  "buffer_load_dwordx4 v230, s[64:67], s96 offen offset:0 lds\n\t" \
  "v_mfma_f32_16x16x32_bf16 a[68:71], a[136:139], v[144:147], a[68:71]\n\t" \
  "v_exp_f32 v38, v38\n\t" \
  "v_mfma_f32_16x16x32_bf16 a[68:71], a[140:143], v[148:151], a[68:71]\n\t" \
  "ds_read_b128 a[224:227], v220 offset:8192\n\t" \
  "v_cvt_pk_bf16_f32 v136, v32, v33\n\t" \
  "v_mfma_f32_16x16x32_bf16 a[72:75], a[144:147], v[144:147], a[72:75]\n\t" \
  "v_exp_f32 v39, v39\n\t" \
  "v_mfma_f32_16x16x32_bf16 a[72:75], a[148:151], v[148:151], a[72:75]\n\t" \
  "ds_read_b128 a[228:231], v221 offset:8192\n\t" \
  "v_cvt_pk_bf16_f32 v137, v34, v35\n\t" \
  "v_mfma_f32_16x16x32_bf16 a[76:79], a[152:155], v[144:147], a[76:79]\n\t" \
  "v_exp_f32 v40, v40\n\t" \
  "v_mfma_f32_16x16x32_bf16 a[76:79], a[156:159], v[148:151], a[76:79]\n\t" \
  "v_exp_f32 v41, v41\n\t" \
  "v_mfma_f32_16x16x32_bf16 a[80:83], a[160:163], v[144:147], a[80:83]\n\t" \
  "buffer_load_dwordx4 v231, s[64:67], s96 offen offset:1024 lds\n\t" \
  "v_mfma_f32_16x16x32_bf16 a[80:83], a[164:167], v[148:151], a[80:83]\n\t" \
  "ds_read_b128 a[232:235], v222 offset:8192\n\t" \
  "v_cvt_pk_bf16_f32 v138, v36, v37\n\t" \
  "v_mfma_f32_16x16x32_bf16 a[84:87], a[168:171], v[144:147], a[84:87]\n\t" \
  "v_exp_f32 v42, v42\n\t" \
  "v_mfma_f32_16x16x32_bf16 a[84:87], a[172:175], v[148:151], a[84:87]\n\t" \
  "ds_read_b128 a[236:239], v223 offset:8192\n\t" \
  "v_cvt_pk_bf16_f32 v139, v38, v39\n\t" \
  "v_mfma_f32_16x16x32_bf16 a[88:91], a[176:179], v[144:147], a[88:91]\n\t" \
  "v_exp_f32 v43, v43\n\t" \
  "v_mfma_f32_16x16x32_bf16 a[88:91], a[180:183], v[148:151], a[88:91]\n\t" \
  "ds_read_b128 a[240:243], v220 offset:12288\n\t" \
  "v_cvt_pk_bf16_f32 v140, v40, v41\n\t" \
  "v_mfma_f32_16x16x32_bf16 a[92:95], a[184:187], v[144:147], a[92:95]\n\t" \
  "buffer_load_dwordx4 v230, s[64:67], s96 offen offset:2048 lds\n\t" \
  "v_mfma_f32_16x16x32_bf16 a[92:95], a[188:191], v[148:151], a[92:95]\n\t" \
  "v_exp_f32 v44, v44\n\t" \
  "v_smfmac_f32_16x16x64_bf16 v[164:167], v[216:219], v[144:151], v208\n\t" \
  "ds_read_b128 a[244:247], v221 offset:12288\n\t" \
  "v_cvt_pk_bf16_f32 v141, v42, v43\n\t" \
  "v_mfma_f32_16x16x32_bf16 a[96:99], a[128:131], v[152:155], a[96:99]\n\t" \
  "v_exp_f32 v45, v45\n\t" \
  "v_mfma_f32_16x16x32_bf16 a[96:99], a[132:135], v[156:159], a[96:99]\n\t" \
  "v_exp_f32 v46, v46\n\t" \
  "v_mfma_f32_16x16x32_bf16 a[100:103], a[136:139], v[152:155], a[100:103]\n\t" \
  "ds_read_b128 a[248:251], v222 offset:12288\n\t" \
  "v_cvt_pk_bf16_f32 v142, v44, v45\n\t" \
  "v_mfma_f32_16x16x32_bf16 a[100:103], a[140:143], v[156:159], a[100:103]\n\t" \
  "buffer_load_dwordx4 v231, s[64:67], s96 offen offset:3072 lds\n\t" \
  "v_mfma_f32_16x16x32_bf16 a[104:107], a[144:147], v[152:155], a[104:107]\n\t" \
  "v_exp_f32 v47, v47\n\t" \
  "v_mfma_f32_16x16x32_bf16 a[104:107], a[148:151], v[156:159], a[104:107]\n\t" \
  "ds_read_b128 a[252:255], v223 offset:12288\n\t" \
  "v_cvt_pk_bf16_f32 v143, v46, v47\n\t" \
  "v_mfma_f32_16x16x32_bf16 a[108:111], a[152:155], v[152:155], a[108:111]\n\t" \
  "v_exp_f32 v64, v64\n\t" \
  "v_mfma_f32_16x16x32_bf16 a[108:111], a[156:159], v[156:159], a[108:111]\n\t" \
  "v_exp_f32 v65, v65\n\t" \
  "v_mfma_f32_16x16x32_bf16 a[112:115], a[160:163], v[152:155], a[112:115]\n\t" \
  "v_exp_f32 v66, v66\n\t" \
  "v_mfma_f32_16x16x32_bf16 a[112:115], a[164:167], v[156:159], a[112:115]\n\t" \
  "v_exp_f32 v67, v67\n\t" \
  "v_mfma_f32_16x16x32_bf16 a[116:119], a[168:171], v[152:155], a[116:119]\n\t" \
  "v_exp_f32 v68, v68\n\t" \
  "v_mfma_f32_16x16x32_bf16 a[116:119], a[172:175], v[156:159], a[116:119]\n\t" \
  "v_exp_f32 v69, v69\n\t" \
  "v_mfma_f32_16x16x32_bf16 a[120:123], a[176:179], v[152:155], a[120:123]\n\t" \
  "v_exp_f32 v70, v70\n\t" \
  "v_mfma_f32_16x16x32_bf16 a[120:123], a[180:183], v[156:159], a[120:123]\n\t" \
  "v_exp_f32 v71, v71\n\t" \
  "v_mfma_f32_16x16x32_bf16 a[124:127], a[184:187], v[152:155], a[124:127]\n\t" \
  "v_exp_f32 v72, v72\n\t" \
  "v_mfma_f32_16x16x32_bf16 a[124:127], a[188:191], v[156:159], a[124:127]\n\t" \
  "v_exp_f32 v73, v73\n\t" \
  "v_smfmac_f32_16x16x64_bf16 v[166:169], v[216:219], v[152:159], v208\n\t" \
  "s_waitcnt lgkmcnt(12)\n\t" \
  "s_cmp_lg_u32 s99, 0\n\t" \
  "s_cbranch_scc1 .Lsf_p5\n\t" \
  ".Lsf_q5:\n\t" \
  "v_mfma_f32_16x16x32_bf16 v[0:3], a[192:195], v[16:19], v[176:179]\n\t" \
  "ds_read_b64_tr_b16 a[128:129], v210 offset:0\n\t" \
  "v_cvt_pk_bf16_f32 v144, v64, v65\n\t" \
  "v_mfma_f32_16x16x32_bf16 v[0:3], a[196:199], v[20:23], v[0:3]\n\t" \
  "v_exp_f32 v74, v74\n\t" \
  "v_mfma_f32_16x16x32_bf16 v[0:3], a[200:203], v[24:27], v[0:3]\n\t" \
  "ds_read_b64_tr_b16 a[130:131], v210 offset:4096\n\t" \
  "v_cvt_pk_bf16_f32 v145, v66, v67\n\t" \
  "v_mfma_f32_16x16x32_bf16 v[0:3], a[204:207], v[28:31], v[0:3]\n\t" \
  "s_waitcnt lgkmcnt(10)\n\t" \
  "v_exp_f32 v75, v75\n\t" \
  "v_mfma_f32_16x16x32_bf16 v[4:7], a[220:223], v[28:31], v[176:179]\n\t" \
  "ds_read_b64_tr_b16 a[132:133], v210 offset:8192\n\t" \
  "v_cvt_pk_bf16_f32 v146, v68, v69\n\t" \
  "v_mfma_f32_16x16x32_bf16 v[4:7], a[216:219], v[24:27], v[4:7]\n\t" \
  "v_exp_f32 v76, v76\n\t" \
  "v_mfma_f32_16x16x32_bf16 v[4:7], a[212:215], v[20:23], v[4:7]\n\t" \
  "ds_read_b64_tr_b16 a[134:135], v210 offset:12288\n\t" \
  "v_max3_f32 v192, v0, v1, v2\n\t" \
  "v_mfma_f32_16x16x32_bf16 v[4:7], a[208:211], v[16:19], v[4:7]\n\t" \
  "s_waitcnt lgkmcnt(8)\n\t" \
  "ds_read_b64_tr_b16 a[136:137], v211 offset:0\n\t" \
  "v_cvt_pk_bf16_f32 v147, v70, v71\n\t" \
  "v_mfma_f32_16x16x32_bf16 v[8:11], a[224:227], v[16:19], v[176:179]\n\t" \
  "v_exp_f32 v77, v77\n\t" \
  "v_mfma_f32_16x16x32_bf16 v[8:11], a[228:231], v[20:23], v[8:11]\n\t" \
  "ds_read_b64_tr_b16 a[138:139], v211 offset:4096\n\t" \
  "v_cvt_pk_bf16_f32 v148, v72, v73\n\t" \
  "v_mfma_f32_16x16x32_bf16 v[8:11], a[232:235], v[24:27], v[8:11]\n\t" \
  "v_max3_f32 v203, v3, v4, v5\n\t" \
  "v_max3_f32 v192, v192, v6, v7\n\t" \
  "v_mfma_f32_16x16x32_bf16 v[8:11], a[236:239], v[28:31], v[8:11]\n\t" \
  "s_waitcnt lgkmcnt(6)\n\t" \
  "ds_read_b64_tr_b16 a[140:141], v211 offset:8192\n\t" \
  "v_cvt_pk_bf16_f32 v149, v74, v75\n\t" \
  "v_mfma_f32_16x16x32_bf16 v[12:15], a[252:255], v[28:31], v[176:179]\n\t" \
  "v_exp_f32 v78, v78\n\t" \
  "v_mfma_f32_16x16x32_bf16 v[12:15], a[248:251], v[24:27], v[12:15]\n\t" \
  "ds_read_b64_tr_b16 a[142:143], v211 offset:12288\n\t" \
  "v_cvt_pk_bf16_f32 v150, v76, v77\n\t" \
  "v_mfma_f32_16x16x32_bf16 v[12:15], a[244:247], v[20:23], v[12:15]\n\t" \
  "v_max3_f32 v203, v203, v8, v9\n\t" \
  "v_max3_f32 v192, v192, v10, v11\n\t" \
  "v_mfma_f32_16x16x32_bf16 v[12:15], a[240:243], v[16:19], v[12:15]\n\t" \
  "v_exp_f32 v79, v79\n\t" \
  "v_mfma_f32_16x16x32_bf16 v[32:35], a[192:195], v[48:51], v[180:183]\n\t" \
  "ds_read_b64_tr_b16 a[144:145], v210 offset:256\n\t" \
  "v_cvt_pk_bf16_f32 v151, v78, v79\n\t" \
  "v_mfma_f32_16x16x32_bf16 v[32:35], a[196:199], v[52:55], v[32:35]\n\t" \
  "v_exp_f32 v96, v96\n\t" \
  "v_mfma_f32_16x16x32_bf16 v[32:35], a[200:203], v[56:59], v[32:35]\n\t" \
  "ds_read_b64_tr_b16 a[146:147], v210 offset:4352\n\t" \
  "v_max3_f32 v203, v203, v12, v13\n\t" \
  "v_mfma_f32_16x16x32_bf16 v[32:35], a[204:207], v[60:63], v[32:35]\n\t" \
  "v_max3_f32 v192, v192, v14, v15\n\t" \
  "v_max_f32 v192, v192, v203\n\t" \
  "v_mfma_f32_16x16x32_bf16 v[36:39], a[220:223], v[60:63], v[180:183]\n\t" \
  "ds_read_b64_tr_b16 a[148:149], v210 offset:8448\n\t" \
  "v_cmp_lt_f32 vcc, 0x42800000, v192\n\t" \
  "v_mfma_f32_16x16x32_bf16 v[36:39], a[216:219], v[56:59], v[36:39]\n\t" \
  "v_exp_f32 v97, v97\n\t" \
  "v_mfma_f32_16x16x32_bf16 v[36:39], a[212:215], v[52:55], v[36:39]\n\t" \
  "s_cbranch_vccnz .Lsf_c60\n\t" \
  ".Lsf_r60:\n\t" \
  "v_mfma_f32_16x16x32_bf16 v[36:39], a[208:211], v[48:51], v[36:39]\n\t" \
  "ds_read_b64_tr_b16 a[150:151], v210 offset:12544\n\t" \
  "v_max3_f32 v192, v32, v33, v34\n\t" \
  "v_mfma_f32_16x16x32_bf16 v[40:43], a[224:227], v[48:51], v[180:183]\n\t" \
  "ds_read_b64_tr_b16 a[152:153], v211 offset:256\n\t" \
  "v_cvt_pk_bf16_f32 v152, v96, v97\n\t" \
  "v_mfma_f32_16x16x32_bf16 v[40:43], a[228:231], v[52:55], v[40:43]\n\t" \
  "v_exp_f32 v98, v98\n\t" \
  "v_mfma_f32_16x16x32_bf16 v[40:43], a[232:235], v[56:59], v[40:43]\n\t" \
  "v_max3_f32 v203, v35, v36, v37\n\t" \
  "v_max3_f32 v192, v192, v38, v39\n\t" \
  "v_mfma_f32_16x16x32_bf16 v[40:43], a[236:239], v[60:63], v[40:43]\n\t" \
  "v_exp_f32 v99, v99\n\t" \
  "v_mfma_f32_16x16x32_bf16 v[44:47], a[252:255], v[60:63], v[180:183]\n\t" \
  "ds_read_b64_tr_b16 a[154:155], v211 offset:4352\n\t" \
  "v_cvt_pk_bf16_f32 v153, v98, v99\n\t" \
  "v_mfma_f32_16x16x32_bf16 v[44:47], a[248:251], v[56:59], v[44:47]\n\t" \
  "v_exp_f32 v100, v100\n\t" \
  "v_mfma_f32_16x16x32_bf16 v[44:47], a[244:247], v[52:55], v[44:47]\n\t" \
  "ds_read_b64_tr_b16 a[156:157], v211 offset:8448\n\t" \
  "v_max3_f32 v203, v203, v40, v41\n\t" \
  "v_mfma_f32_16x16x32_bf16 v[44:47], a[240:243], v[48:51], v[44:47]\n\t" \
  "ds_read_b64_tr_b16 a[158:159], v211 offset:12544\n\t" \
  "v_max3_f32 v192, v192, v42, v43\n\t" \
  "v_mfma_f32_16x16x32_bf16 v[64:67], a[192:195], v[80:83], v[184:187]\n\t" \
  "v_exp_f32 v101, v101\n\t" \
  "v_mfma_f32_16x16x32_bf16 v[64:67], a[196:199], v[84:87], v[64:67]\n\t" \
  "ds_read_b64_tr_b16 a[160:161], v210 offset:512\n\t" \
  "v_cvt_pk_bf16_f32 v154, v100, v101\n\t" \
  "v_mfma_f32_16x16x32_bf16 v[64:67], a[200:203], v[88:91], v[64:67]\n\t" \
  "v_max3_f32 v203, v203, v44, v45\n\t" \
  "v_max3_f32 v192, v192, v46, v47\n\t" \
  "v_mfma_f32_16x16x32_bf16 v[64:67], a[204:207], v[92:95], v[64:67]\n\t" \
  "v_max_f32 v192, v192, v203\n\t" \
  "v_cmp_lt_f32 vcc, 0x42800000, v192\n\t" \
  "v_mfma_f32_16x16x32_bf16 v[68:71], a[220:223], v[92:95], v[184:187]\n\t" \
  "v_exp_f32 v102, v102\n\t" \
  "v_mfma_f32_16x16x32_bf16 v[68:71], a[216:219], v[88:91], v[68:71]\n\t" \
  "s_cbranch_vccnz .Lsf_c61\n\t" \
  ".Lsf_r61:\n\t" \
  "v_mfma_f32_16x16x32_bf16 v[68:71], a[212:215], v[84:87], v[68:71]\n\t" \
  "ds_read_b64_tr_b16 a[162:163], v210 offset:4608\n\t" \
  "v_max3_f32 v192, v64, v65, v66\n\t" \
  "v_mfma_f32_16x16x32_bf16 v[68:71], a[208:211], v[80:83], v[68:71]\n\t" \
  "ds_read_b64_tr_b16 a[164:165], v210 offset:8704\n\t" \
  "v_exp_f32 v103, v103\n\t" \
  "v_mfma_f32_16x16x32_bf16 v[72:75], a[224:227], v[80:83], v[184:187]\n\t" \
  "ds_read_b64_tr_b16 a[166:167], v210 offset:12800\n\t" \
  "v_exp_f32 v104, v104\n\t" \
  "v_mfma_f32_16x16x32_bf16 v[72:75], a[228:231], v[84:87], v[72:75]\n\t" \
  "ds_read_b64_tr_b16 a[168:169], v211 offset:512\n\t" \
  "v_exp_f32 v105, v105\n\t" \
  "v_mfma_f32_16x16x32_bf16 v[72:75], a[232:235], v[88:91], v[72:75]\n\t" \
  "v_max3_f32 v203, v67, v68, v69\n\t" \
  "v_max3_f32 v192, v192, v70, v71\n\t" \
  "v_mfma_f32_16x16x32_bf16 v[72:75], a[236:239], v[92:95], v[72:75]\n\t" \
  "ds_read_b64_tr_b16 a[170:171], v211 offset:4608\n\t" \
  "v_exp_f32 v106, v106\n\t" \
  "v_mfma_f32_16x16x32_bf16 v[76:79], a[252:255], v[92:95], v[184:187]\n\t" \
  "ds_read_b64_tr_b16 a[172:173], v211 offset:8704\n\t" \
  "v_exp_f32 v107, v107\n\t" \
  "v_mfma_f32_16x16x32_bf16 v[76:79], a[248:251], v[88:91], v[76:79]\n\t" \
  "ds_read_b64_tr_b16 a[174:175], v211 offset:12800\n\t" \
  "v_cvt_pk_bf16_f32 v155, v102, v103\n\t" \
  "v_mfma_f32_16x16x32_bf16 v[76:79], a[244:247], v[84:87], v[76:79]\n\t" \
  "v_max3_f32 v203, v203, v72, v73\n\t" \
  "v_max3_f32 v192, v192, v74, v75\n\t" \
  "v_mfma_f32_16x16x32_bf16 v[76:79], a[240:243], v[80:83], v[76:79]\n\t" \
  "ds_read_b64_tr_b16 a[176:177], v210 offset:768\n\t" \
  "v_exp_f32 v108, v108\n\t" \
  "v_mfma_f32_16x16x32_bf16 v[96:99], a[192:195], v[112:115], v[188:191]\n\t" \
  "ds_read_b64_tr_b16 a[178:179], v210 offset:4864\n\t" \
  "v_exp_f32 v109, v109\n\t" \
  "v_mfma_f32_16x16x32_bf16 v[96:99], a[196:199], v[116:119], v[96:99]\n\t" \
  "ds_read_b64_tr_b16 a[180:181], v210 offset:8960\n\t" \
  "v_cvt_pk_bf16_f32 v156, v104, v105\n\t" \
  "v_mfma_f32_16x16x32_bf16 v[96:99], a[200:203], v[120:123], v[96:99]\n\t" \
  "v_max3_f32 v203, v203, v76, v77\n\t" \
  "v_max3_f32 v192, v192, v78, v79\n\t" \
  "v_mfma_f32_16x16x32_bf16 v[96:99], a[204:207], v[124:127], v[96:99]\n\t" \
  "v_max_f32 v192, v192, v203\n\t" \
  "v_cmp_lt_f32 vcc, 0x42800000, v192\n\t" \
  "v_mfma_f32_16x16x32_bf16 v[100:103], a[220:223], v[124:127], v[188:191]\n\t" \
  "ds_read_b64_tr_b16 a[182:183], v210 offset:13056\n\t" \
  "v_exp_f32 v110, v110\n\t" \
  "v_mfma_f32_16x16x32_bf16 v[100:103], a[216:219], v[120:123], v[100:103]\n\t" \
  "s_cbranch_vccnz .Lsf_c62\n\t" \
  ".Lsf_r62:\n\t" \
  "v_mfma_f32_16x16x32_bf16 v[100:103], a[212:215], v[116:119], v[100:103]\n\t" \
  "v_max3_f32 v192, v96, v97, v98\n\t" \
  "v_exp_f32 v111, v111\n\t" \
  "v_mfma_f32_16x16x32_bf16 v[100:103], a[208:211], v[112:115], v[100:103]\n\t" \
  "ds_read_b64_tr_b16 a[184:185], v211 offset:768\n\t" \
  "v_cvt_pk_bf16_f32 v157, v106, v107\n\t" \
  "v_mfma_f32_16x16x32_bf16 v[104:107], a[224:227], v[112:115], v[188:191]\n\t" \
  "ds_read_b64_tr_b16 a[186:187], v211 offset:4864\n\t" \
  "v_cvt_pk_bf16_f32 v158, v108, v109\n\t" \
  "v_mfma_f32_16x16x32_bf16 v[104:107], a[228:231], v[116:119], v[104:107]\n\t" \
  "ds_read_b64_tr_b16 a[188:189], v211 offset:8960\n\t" \
  "v_cvt_pk_bf16_f32 v159, v110, v111\n\t" \
  "v_mfma_f32_16x16x32_bf16 v[104:107], a[232:235], v[120:123], v[104:107]\n\t" \
  "v_max3_f32 v203, v99, v100, v101\n\t" \
  "v_max3_f32 v192, v192, v102, v103\n\t" \
  "v_mfma_f32_16x16x32_bf16 v[104:107], a[236:239], v[124:127], v[104:107]\n\t" \
  "ds_read_b64_tr_b16 a[190:191], v211 offset:13056\n\t" \
  "s_nop 0\n\t" \
  "v_mfma_f32_16x16x32_bf16 v[108:111], a[252:255], v[124:127], v[188:191]\n\t" \
  "v_exp_f32 v0, v0\n\t" \
  "v_mfma_f32_16x16x32_bf16 v[108:111], a[248:251], v[120:123], v[108:111]\n\t" \
  "v_exp_f32 v1, v1\n\t" \
  "v_mfma_f32_16x16x32_bf16 v[108:111], a[244:247], v[116:119], v[108:111]\n\t" \
  "s_waitcnt vmcnt(0) lgkmcnt(0)\n\t" \
  "v_max3_f32 v203, v203, v104, v105\n\t" \
  "v_max3_f32 v192, v192, v106, v107\n\t" \
  "v_mfma_f32_16x16x32_bf16 v[108:111], a[240:243], v[112:115], v[108:111]\n\t" \
  "s_barrier\n\t" \
  "v_mfma_f32_16x16x32_bf16 a[0:3], a[128:131], v[128:131], a[0:3]\n\t" \
  "v_exp_f32 v2, v2\n\t" \
  "v_mfma_f32_16x16x32_bf16 a[0:3], a[132:135], v[132:135], a[0:3]\n\t" \
  "v_exp_f32 v3, v3\n\t" \
  "v_mfma_f32_16x16x32_bf16 a[4:7], a[136:139], v[128:131], a[4:7]\n\t" \
  "v_exp_f32 v4, v4\n\t" \
  "v_mfma_f32_16x16x32_bf16 a[4:7], a[140:143], v[132:135], a[4:7]\n\t" \
  "v_max3_f32 v203, v203, v108, v109\n\t" \
  "v_max3_f32 v192, v192, v110, v111\n\t" \
  "v_mfma_f32_16x16x32_bf16 a[8:11], a[144:147], v[128:131], a[8:11]\n\t" \
  "v_max_f32 v192, v192, v203\n\t" \
  "v_cmp_lt_f32 vcc, 0x42800000, v192\n\t" \
  "v_mfma_f32_16x16x32_bf16 a[8:11], a[148:151], v[132:135], a[8:11]\n\t" \
  "v_exp_f32 v5, v5\n\t" \
  "v_mfma_f32_16x16x32_bf16 a[12:15], a[152:155], v[128:131], a[12:15]\n\t" \
  "s_cbranch_vccnz .Lsf_c63\n\t" \
  ".Lsf_r63:\n\t" \
  "v_mfma_f32_16x16x32_bf16 a[12:15], a[156:159], v[132:135], a[12:15]\n\t" \
  "s_add_u32 m0, s38, 147456\n\t" \
  "v_exp_f32 v6, v6\n\t" \
  "v_mfma_f32_16x16x32_bf16 a[16:19], a[160:163], v[128:131], a[16:19]\n\t" \
  "s_add_u32 s96, s38, 114688\n\t" \
  "v_exp_f32 v7, v7\n\t" \
  "v_mfma_f32_16x16x32_bf16 a[16:19], a[164:167], v[132:135], a[16:19]\n\t" \
  "v_exp_f32 v8, v8\n\t" \
  "v_mfma_f32_16x16x32_bf16 a[20:23], a[168:171], v[128:131], a[20:23]\n\t" \
  "buffer_load_dwordx4 v230, s[64:67], s96 offen offset:0 lds\n\t" \
  "v_mfma_f32_16x16x32_bf16 a[20:23], a[172:175], v[132:135], a[20:23]\n\t" \
  "v_exp_f32 v9, v9\n\t" \
  "v_mfma_f32_16x16x32_bf16 a[24:27], a[176:179], v[128:131], a[24:27]\n\t" \
  "v_exp_f32 v10, v10\n\t" \
  "v_mfma_f32_16x16x32_bf16 a[24:27], a[180:183], v[132:135], a[24:27]\n\t" \
  "v_exp_f32 v11, v11\n\t" \
  "v_mfma_f32_16x16x32_bf16 a[28:31], a[184:187], v[128:131], a[28:31]\n\t" \
  "v_exp_f32 v12, v12\n\t" \
  "v_mfma_f32_16x16x32_bf16 a[28:31], a[188:191], v[132:135], a[28:31]\n\t" \
  "v_exp_f32 v13, v13\n\t" \
  "v_smfmac_f32_16x16x64_bf16 v[160:163], v[216:219], v[128:135], v208\n\t" \
  "ds_read_b128 a[192:195], v220 offset:16384\n\t" \
  "v_cvt_pk_bf16_f32 v128, v0, v1\n\t" \
  "v_mfma_f32_16x16x32_bf16 a[32:35], a[128:131], v[136:139], a[32:35]\n\t" \
  "v_exp_f32 v14, v14\n\t" \
  "v_mfma_f32_16x16x32_bf16 a[32:35], a[132:135], v[140:143], a[32:35]\n\t" \
  "v_exp_f32 v15, v15\n\t" \
  "v_mfma_f32_16x16x32_bf16 a[36:39], a[136:139], v[136:139], a[36:39]\n\t" \
  "ds_read_b128 a[196:199], v221 offset:16384\n\t" \
  "v_cvt_pk_bf16_f32 v129, v2, v3\n\t" \
  "v_mfma_f32_16x16x32_bf16 a[36:39], a[140:143], v[140:143], a[36:39]\n\t" \
  "v_exp_f32 v32, v32\n\t" \
  "v_mfma_f32_16x16x32_bf16 a[40:43], a[144:147], v[136:139], a[40:43]\n\t" \
  "ds_read_b128 a[200:203], v222 offset:16384\n\t" \
  "v_cvt_pk_bf16_f32 v130, v4, v5\n\t" \
  "v_mfma_f32_16x16x32_bf16 a[40:43], a[148:151], v[140:143], a[40:43]\n\t" \
  "v_exp_f32 v33, v33\n\t" \
  "v_mfma_f32_16x16x32_bf16 a[44:47], a[152:155], v[136:139], a[44:47]\n\t" \
  "buffer_load_dwordx4 v231, s[64:67], s96 offen offset:1024 lds\n\t" \
  "v_mfma_f32_16x16x32_bf16 a[44:47], a[156:159], v[140:143], a[44:47]\n\t" \
  "ds_read_b128 a[204:207], v223 offset:16384\n\t" \
  "v_cvt_pk_bf16_f32 v131, v6, v7\n\t" \
  "v_mfma_f32_16x16x32_bf16 a[48:51], a[160:163], v[136:139], a[48:51]\n\t" \
  "v_exp_f32 v34, v34\n\t" \
  "v_mfma_f32_16x16x32_bf16 a[48:51], a[164:167], v[140:143], a[48:51]\n\t" \
  "v_exp_f32 v35, v35\n\t" \
  "v_mfma_f32_16x16x32_bf16 a[52:55], a[168:171], v[136:139], a[52:55]\n\t" \
  "ds_read_b128 a[208:211], v220 offset:20480\n\t" \
  "v_cvt_pk_bf16_f32 v132, v8, v9\n\t" \
  "v_mfma_f32_16x16x32_bf16 a[52:55], a[172:175], v[140:143], a[52:55]\n\t" \
  "v_exp_f32 v36, v36\n\t" \
  "v_mfma_f32_16x16x32_bf16 a[56:59], a[176:179], v[136:139], a[56:59]\n\t" \
  "ds_read_b128 a[212:215], v221 offset:20480\n\t" \
  "v_cvt_pk_bf16_f32 v133, v10, v11\n\t" \
  "v_mfma_f32_16x16x32_bf16 a[56:59], a[180:183], v[140:143], a[56:59]\n\t" \
  "v_exp_f32 v37, v37\n\t" \
  "v_mfma_f32_16x16x32_bf16 a[60:63], a[184:187], v[136:139], a[60:63]\n\t" \
  "ds_read_b128 a[216:219], v222 offset:20480\n\t" \
  "v_cvt_pk_bf16_f32 v134, v12, v13\n\t" \
  "v_mfma_f32_16x16x32_bf16 a[60:63], a[188:191], v[140:143], a[60:63]\n\t" \
  "v_exp_f32 v38, v38\n\t" \
  "v_smfmac_f32_16x16x64_bf16 v[162:165], v[216:219], v[136:143], v208\n\t" \
  "ds_read_b128 a[220:223], v223 offset:20480\n\t" \
  "v_cvt_pk_bf16_f32 v135, v14, v15\n\t" \
  "v_mfma_f32_16x16x32_bf16 a[64:67], a[128:131], v[144:147], a[64:67]\n\t" \
  "v_exp_f32 v39, v39\n\t" \
  "v_mfma_f32_16x16x32_bf16 a[64:67], a[132:135], v[148:151], a[64:67]\n\t" \
  "buffer_load_dwordx4 v230, s[64:67], s96 offen offset:2048 lds\n\t" \
  "v_mfma_f32_16x16x32_bf16 a[68:71], a[136:139], v[144:147], a[68:71]\n\t" \
  "ds_read_b128 a[224:227], v220 offset:24576\n\t" \
  "v_cvt_pk_bf16_f32 v136, v32, v33\n\t" \
  "v_mfma_f32_16x16x32_bf16 a[68:71], a[140:143], v[148:151], a[68:71]\n\t" \
  "v_exp_f32 v40, v40\n\t" \
  "v_mfma_f32_16x16x32_bf16 a[72:75], a[144:147], v[144:147], a[72:75]\n\t" \
  "v_exp_f32 v41, v41\n\t" \
  "v_mfma_f32_16x16x32_bf16 a[72:75], a[148:151], v[148:151], a[72:75]\n\t" \
  "ds_read_b128 a[228:231], v221 offset:24576\n\t" \
  "v_cvt_pk_bf16_f32 v137, v34, v35\n\t" \
  "v_mfma_f32_16x16x32_bf16 a[76:79], a[152:155], v[144:147], a[76:79]\n\t" \
  "v_exp_f32 v42, v42\n\t" \
  "v_mfma_f32_16x16x32_bf16 a[76:79], a[156:159], v[148:151], a[76:79]\n\t" \
  "v_exp_f32 v43, v43\n\t" \
  "v_mfma_f32_16x16x32_bf16 a[80:83], a[160:163], v[144:147], a[80:83]\n\t" \
  "ds_read_b128 a[232:235], v222 offset:24576\n\t" \
  "v_cvt_pk_bf16_f32 v138, v36, v37\n\t" \
  "v_mfma_f32_16x16x32_bf16 a[80:83], a[164:167], v[148:151], a[80:83]\n\t" \
  "v_exp_f32 v44, v44\n\t" \
  "v_mfma_f32_16x16x32_bf16 a[84:87], a[168:171], v[144:147], a[84:87]\n\t" \
  "v_exp_f32 v45, v45\n\t" \
  "v_mfma_f32_16x16x32_bf16 a[84:87], a[172:175], v[148:151], a[84:87]\n\t" \
  "ds_read_b128 a[236:239], v223 offset:24576\n\t" \
  "v_cvt_pk_bf16_f32 v139, v38, v39\n\t" \
  "v_mfma_f32_16x16x32_bf16 a[88:91], a[176:179], v[144:147], a[88:91]\n\t" \
  "v_exp_f32 v46, v46\n\t" \
  "v_mfma_f32_16x16x32_bf16 a[88:91], a[180:183], v[148:151], a[88:91]\n\t" \
  "ds_read_b128 a[240:243], v220 offset:28672\n\t" \
  "v_cvt_pk_bf16_f32 v140, v40, v41\n\t" \
  "v_mfma_f32_16x16x32_bf16 a[92:95], a[184:187], v[144:147], a[92:95]\n\t" \
  "buffer_load_dwordx4 v231, s[64:67], s96 offen offset:3072 lds\n\t" \
  "v_mfma_f32_16x16x32_bf16 a[92:95], a[188:191], v[148:151], a[92:95]\n\t" \
  "v_exp_f32 v47, v47\n\t" \
  "v_smfmac_f32_16x16x64_bf16 v[164:167], v[216:219], v[144:151], v208\n\t" \
  "ds_read_b128 a[244:247], v221 offset:28672\n\t" \
  "v_cvt_pk_bf16_f32 v141, v42, v43\n\t" \
  "v_mfma_f32_16x16x32_bf16 a[96:99], a[128:131], v[152:155], a[96:99]\n\t" \
  "v_exp_f32 v64, v64\n\t" \
  "v_mfma_f32_16x16x32_bf16 a[96:99], a[132:135], v[156:159], a[96:99]\n\t" \
  "v_exp_f32 v65, v65\n\t" \
  "v_mfma_f32_16x16x32_bf16 a[100:103], a[136:139], v[152:155], a[100:103]\n\t" \
  "ds_read_b128 a[248:251], v222 offset:28672\n\t" \
  "v_cvt_pk_bf16_f32 v142, v44, v45\n\t" \
  "v_mfma_f32_16x16x32_bf16 a[100:103], a[140:143], v[156:159], a[100:103]\n\t" \
  "v_exp_f32 v66, v66\n\t" \
  "v_mfma_f32_16x16x32_bf16 a[104:107], a[144:147], v[152:155], a[104:107]\n\t" \
  "v_exp_f32 v67, v67\n\t" \
  "v_mfma_f32_16x16x32_bf16 a[104:107], a[148:151], v[156:159], a[104:107]\n\t" \
  "ds_read_b128 a[252:255], v223 offset:28672\n\t" \
  "v_cvt_pk_bf16_f32 v143, v46, v47\n\t" \
  "v_mfma_f32_16x16x32_bf16 a[108:111], a[152:155], v[152:155], a[108:111]\n\t" \
  "v_exp_f32 v68, v68\n\t" \
  "v_mfma_f32_16x16x32_bf16 a[108:111], a[156:159], v[156:159], a[108:111]\n\t" \
  "v_exp_f32 v69, v69\n\t" \
  "v_mfma_f32_16x16x32_bf16 a[112:115], a[160:163], v[152:155], a[112:115]\n\t" \
  "v_exp_f32 v70, v70\n\t" \
  "v_mfma_f32_16x16x32_bf16 a[112:115], a[164:167], v[156:159], a[112:115]\n\t" \
  "v_exp_f32 v71, v71\n\t" \
  "v_mfma_f32_16x16x32_bf16 a[116:119], a[168:171], v[152:155], a[116:119]\n\t" \
  "v_exp_f32 v72, v72\n\t" \
  "v_mfma_f32_16x16x32_bf16 a[116:119], a[172:175], v[156:159], a[116:119]\n\t" \
  "v_exp_f32 v73, v73\n\t" \
  "v_mfma_f32_16x16x32_bf16 a[120:123], a[176:179], v[152:155], a[120:123]\n\t" \
  "v_exp_f32 v74, v74\n\t" \
  "v_mfma_f32_16x16x32_bf16 a[120:123], a[180:183], v[156:159], a[120:123]\n\t" \
  "v_exp_f32 v75, v75\n\t" \
  "v_mfma_f32_16x16x32_bf16 a[124:127], a[184:187], v[152:155], a[124:127]\n\t" \
  "v_exp_f32 v76, v76\n\t" \
  "v_mfma_f32_16x16x32_bf16 a[124:127], a[188:191], v[156:159], a[124:127]\n\t" \
  "v_exp_f32 v77, v77\n\t" \
  "v_smfmac_f32_16x16x64_bf16 v[166:169], v[216:219], v[152:159], v208\n\t" \
  "s_waitcnt lgkmcnt(12)\n\t" \
  "s_cmp_lg_u32 s99, 0\n\t" \
  "s_cbranch_scc1 .Lsf_p6\n\t" \
  ".Lsf_q6:\n\t" \
  "v_mfma_f32_16x16x32_bf16 v[0:3], a[192:195], v[16:19], v[176:179]\n\t" \
  "ds_read_b64_tr_b16 a[128:129], v228 offset:0\n\t" \
  "v_cvt_pk_bf16_f32 v144, v64, v65\n\t" \
  "v_mfma_f32_16x16x32_bf16 v[0:3], a[196:199], v[20:23], v[0:3]\n\t" \
  "v_exp_f32 v78, v78\n\t" \
  "v_mfma_f32_16x16x32_bf16 v[0:3], a[200:203], v[24:27], v[0:3]\n\t" \
  "ds_read_b64_tr_b16 a[130:131], v228 offset:4096\n\t" \
  "v_cvt_pk_bf16_f32 v145, v66, v67\n\t" \
  "v_mfma_f32_16x16x32_bf16 v[0:3], a[204:207], v[28:31], v[0:3]\n\t" \
  "s_waitcnt lgkmcnt(10)\n\t" \
  "v_exp_f32 v79, v79\n\t" \
  "v_mfma_f32_16x16x32_bf16 v[4:7], a[220:223], v[28:31], v[176:179]\n\t" \
  "ds_read_b64_tr_b16 a[132:133], v228 offset:8192\n\t" \
  "v_cvt_pk_bf16_f32 v146, v68, v69\n\t" \
  "v_mfma_f32_16x16x32_bf16 v[4:7], a[216:219], v[24:27], v[4:7]\n\t" \
  "v_exp_f32 v96, v96\n\t" \
  "v_mfma_f32_16x16x32_bf16 v[4:7], a[212:215], v[20:23], v[4:7]\n\t" \
  "ds_read_b64_tr_b16 a[134:135], v228 offset:12288\n\t" \
  "v_max3_f32 v192, v0, v1, v2\n\t" \
  "v_mfma_f32_16x16x32_bf16 v[4:7], a[208:211], v[16:19], v[4:7]\n\t" \
  "s_waitcnt lgkmcnt(8)\n\t" \
  "ds_read_b64_tr_b16 a[136:137], v229 offset:0\n\t" \
  "v_cvt_pk_bf16_f32 v147, v70, v71\n\t" \
  "v_mfma_f32_16x16x32_bf16 v[8:11], a[224:227], v[16:19], v[176:179]\n\t" \
  "v_exp_f32 v97, v97\n\t" \
  "v_mfma_f32_16x16x32_bf16 v[8:11], a[228:231], v[20:23], v[8:11]\n\t" \
  "ds_read_b64_tr_b16 a[138:139], v229 offset:4096\n\t" \
  "v_cvt_pk_bf16_f32 v148, v72, v73\n\t" \
  "v_mfma_f32_16x16x32_bf16 v[8:11], a[232:235], v[24:27], v[8:11]\n\t" \
  "v_max3_f32 v203, v3, v4, v5\n\t" \
  "v_max3_f32 v192, v192, v6, v7\n\t" \
  "v_mfma_f32_16x16x32_bf16 v[8:11], a[236:239], v[28:31], v[8:11]\n\t" \
  "s_waitcnt lgkmcnt(6)\n\t" \
  "ds_read_b64_tr_b16 a[140:141], v229 offset:8192\n\t" \
  "v_cvt_pk_bf16_f32 v149, v74, v75\n\t" \
  "v_mfma_f32_16x16x32_bf16 v[12:15], a[252:255], v[28:31], v[176:179]\n\t" \
  "v_exp_f32 v98, v98\n\t" \
  "v_mfma_f32_16x16x32_bf16 v[12:15], a[248:251], v[24:27], v[12:15]\n\t" \
  "ds_read_b64_tr_b16 a[142:143], v229 offset:12288\n\t" \
  "v_cvt_pk_bf16_f32 v150, v76, v77\n\t" \
  "v_mfma_f32_16x16x32_bf16 v[12:15], a[244:247], v[20:23], v[12:15]\n\t" \
  "v_max3_f32 v203, v203, v8, v9\n\t" \
  "v_max3_f32 v192, v192, v10, v11\n\t" \
  "v_mfma_f32_16x16x32_bf16 v[12:15], a[240:243], v[16:19], v[12:15]\n\t" \
  "ds_read_b64_tr_b16 a[144:145], v228 offset:256\n\t" \
  "v_cvt_pk_bf16_f32 v151, v78, v79\n\t" \
  "v_mfma_f32_16x16x32_bf16 v[32:35], a[192:195], v[48:51], v[180:183]\n\t" \
  "ds_read_b64_tr_b16 a[146:147], v228 offset:4352\n\t" \
  "v_cvt_pk_bf16_f32 v152, v96, v97\n\t" \
  "v_mfma_f32_16x16x32_bf16 v[32:35], a[196:199], v[52:55], v[32:35]\n\t" \
  "v_exp_f32 v99, v99\n\t" \
  "v_mfma_f32_16x16x32_bf16 v[32:35], a[200:203], v[56:59], v[32:35]\n\t" \
  "v_max3_f32 v203, v203, v12, v13\n\t" \
  "v_max3_f32 v192, v192, v14, v15\n\t" \
  "v_mfma_f32_16x16x32_bf16 v[32:35], a[204:207], v[60:63], v[32:35]\n\t" \
  "v_max_f32 v192, v192, v203\n\t" \
  "v_cmp_lt_f32 vcc, 0x42800000, v192\n\t" \
  "v_mfma_f32_16x16x32_bf16 v[36:39], a[220:223], v[60:63], v[180:183]\n\t" \
  "ds_read_b64_tr_b16 a[148:149], v228 offset:8448\n\t" \
  "v_cvt_pk_bf16_f32 v153, v98, v99\n\t" \
  "v_mfma_f32_16x16x32_bf16 v[36:39], a[216:219], v[56:59], v[36:39]\n\t" \
  "s_cbranch_vccnz .Lsf_c70\n\t" \
  ".Lsf_r70:\n\t" \
  "v_mfma_f32_16x16x32_bf16 v[36:39], a[212:215], v[52:55], v[36:39]\n\t" \
  "ds_read_b64_tr_b16 a[150:151], v228 offset:12544\n\t" \
  "v_max3_f32 v192, v32, v33, v34\n\t" \
  "v_mfma_f32_16x16x32_bf16 v[36:39], a[208:211], v[48:51], v[36:39]\n\t" \
  "v_exp_f32 v100, v100\n\t" \
  "v_mfma_f32_16x16x32_bf16 v[40:43], a[224:227], v[48:51], v[180:183]\n\t" \
  "v_exp_f32 v101, v101\n\t" \
  "v_mfma_f32_16x16x32_bf16 v[40:43], a[228:231], v[52:55], v[40:43]\n\t" \
  "ds_read_b64_tr_b16 a[152:153], v229 offset:256\n\t" \
  "v_cvt_pk_bf16_f32 v154, v100, v101\n\t" \
  "v_mfma_f32_16x16x32_bf16 v[40:43], a[232:235], v[56:59], v[40:43]\n\t" \
  "ds_read_b64_tr_b16 a[154:155], v229 offset:4352\n\t" \
  "v_max3_f32 v203, v35, v36, v37\n\t" \
  "v_mfma_f32_16x16x32_bf16 v[40:43], a[236:239], v[60:63], v[40:43]\n\t" \
  "ds_read_b64_tr_b16 a[156:157], v229 offset:8448\n\t" \
  "v_max3_f32 v192, v192, v38, v39\n\t" \
  "v_mfma_f32_16x16x32_bf16 v[44:47], a[252:255], v[60:63], v[180:183]\n\t" \
  "v_exp_f32 v102, v102\n\t" \
  "v_mfma_f32_16x16x32_bf16 v[44:47], a[248:251], v[56:59], v[44:47]\n\t" \
  "v_exp_f32 v103, v103\n\t" \
  "v_mfma_f32_16x16x32_bf16 v[44:47], a[244:247], v[52:55], v[44:47]\n\t" \
  "ds_read_b64_tr_b16 a[158:159], v229 offset:12544\n\t" \
  "v_max3_f32 v203, v203, v40, v41\n\t" \
  "v_mfma_f32_16x16x32_bf16 v[44:47], a[240:243], v[48:51], v[44:47]\n\t" \
  "ds_read_b64_tr_b16 a[160:161], v228 offset:512\n\t" \
  "v_max3_f32 v192, v192, v42, v43\n\t" \
  "v_mfma_f32_16x16x32_bf16 v[64:67], a[192:195], v[80:83], v[184:187]\n\t" \
  "ds_read_b64_tr_b16 a[162:163], v228 offset:4608\n\t" \
  "v_cvt_pk_bf16_f32 v155, v102, v103\n\t" \
  "v_mfma_f32_16x16x32_bf16 v[64:67], a[196:199], v[84:87], v[64:67]\n\t" \
  "v_exp_f32 v104, v104\n\t" \
  "v_mfma_f32_16x16x32_bf16 v[64:67], a[200:203], v[88:91], v[64:67]\n\t" \
  "v_max3_f32 v203, v203, v44, v45\n\t" \
  "v_max3_f32 v192, v192, v46, v47\n\t" \
  "v_mfma_f32_16x16x32_bf16 v[64:67], a[204:207], v[92:95], v[64:67]\n\t" \
  "v_max_f32 v192, v192, v203\n\t" \
  "v_cmp_lt_f32 vcc, 0x42800000, v192\n\t" \
  "v_mfma_f32_16x16x32_bf16 v[68:71], a[220:223], v[92:95], v[184:187]\n\t" \
  "v_exp_f32 v105, v105\n\t" \
  "v_mfma_f32_16x16x32_bf16 v[68:71], a[216:219], v[88:91], v[68:71]\n\t" \
  "s_cbranch_vccnz .Lsf_c71\n\t" \
  ".Lsf_r71:\n\t" \
  "v_mfma_f32_16x16x32_bf16 v[68:71], a[212:215], v[84:87], v[68:71]\n\t" \
  "ds_read_b64_tr_b16 a[164:165], v228 offset:8704\n\t" \
  "v_max3_f32 v192, v64, v65, v66\n\t" \
  "v_mfma_f32_16x16x32_bf16 v[68:71], a[208:211], v[80:83], v[68:71]\n\t" \
  "ds_read_b64_tr_b16 a[166:167], v228 offset:12800\n\t" \
  "v_cvt_pk_bf16_f32 v156, v104, v105\n\t" \
  "v_mfma_f32_16x16x32_bf16 v[72:75], a[224:227], v[80:83], v[184:187]\n\t" \
  "v_exp_f32 v106, v106\n\t" \
  "v_mfma_f32_16x16x32_bf16 v[72:75], a[228:231], v[84:87], v[72:75]\n\t" \
  "v_exp_f32 v107, v107\n\t" \
  "v_mfma_f32_16x16x32_bf16 v[72:75], a[232:235], v[88:91], v[72:75]\n\t" \
  "ds_read_b64_tr_b16 a[168:169], v229 offset:512\n\t" \
  "v_max3_f32 v203, v67, v68, v69\n\t" \
  "v_mfma_f32_16x16x32_bf16 v[72:75], a[236:239], v[92:95], v[72:75]\n\t" \
  "ds_read_b64_tr_b16 a[170:171], v229 offset:4608\n\t" \
  "v_max3_f32 v192, v192, v70, v71\n\t" \
  "v_mfma_f32_16x16x32_bf16 v[76:79], a[252:255], v[92:95], v[184:187]\n\t" \
  "ds_read_b64_tr_b16 a[172:173], v229 offset:8704\n\t" \
  "v_cvt_pk_bf16_f32 v157, v106, v107\n\t" \
  "v_mfma_f32_16x16x32_bf16 v[76:79], a[248:251], v[88:91], v[76:79]\n\t" \
  "v_exp_f32 v108, v108\n\t" \
  "v_mfma_f32_16x16x32_bf16 v[76:79], a[244:247], v[84:87], v[76:79]\n\t" \
  "v_max3_f32 v203, v203, v72, v73\n\t" \
  "v_max3_f32 v192, v192, v74, v75\n\t" \
  "v_mfma_f32_16x16x32_bf16 v[76:79], a[240:243], v[80:83], v[76:79]\n\t" \
  "v_exp_f32 v109, v109\n\t" \
  "v_mfma_f32_16x16x32_bf16 v[96:99], a[192:195], v[112:115], v[188:191]\n\t" \
  "ds_read_b64_tr_b16 a[174:175], v229 offset:12800\n\t" \
  "v_cvt_pk_bf16_f32 v158, v108, v109\n\t" \
  "v_mfma_f32_16x16x32_bf16 v[96:99], a[196:199], v[116:119], v[96:99]\n\t" \
  "v_exp_f32 v110, v110\n\t" \
  "v_mfma_f32_16x16x32_bf16 v[96:99], a[200:203], v[120:123], v[96:99]\n\t" \
  "ds_read_b64_tr_b16 a[176:177], v228 offset:768\n\t" \
  "v_max3_f32 v203, v203, v76, v77\n\t" \
  "v_mfma_f32_16x16x32_bf16 v[96:99], a[204:207], v[124:127], v[96:99]\n\t" \
  "v_max3_f32 v192, v192, v78, v79\n\t" \
  "v_max_f32 v192, v192, v203\n\t" \
  "v_mfma_f32_16x16x32_bf16 v[100:103], a[220:223], v[124:127], v[188:191]\n\t" \
  "ds_read_b64_tr_b16 a[178:179], v228 offset:4864\n\t" \
  "v_cmp_lt_f32 vcc, 0x42800000, v192\n\t" \
  "v_mfma_f32_16x16x32_bf16 v[100:103], a[216:219], v[120:123], v[100:103]\n\t" \
  "ds_read_b64_tr_b16 a[180:181], v228 offset:8960\n\t" \
  "v_exp_f32 v111, v111\n\t" \
  "v_mfma_f32_16x16x32_bf16 v[100:103], a[212:215], v[116:119], v[100:103]\n\t" \
  "s_cbranch_vccnz .Lsf_c72\n\t" \
  ".Lsf_r72:\n\t" \
  "v_mfma_f32_16x16x32_bf16 v[100:103], a[208:211], v[112:115], v[100:103]\n\t" \
  "ds_read_b64_tr_b16 a[182:183], v228 offset:13056\n\t" \
  "v_max3_f32 v192, v96, v97, v98\n\t" \
  "v_mfma_f32_16x16x32_bf16 v[104:107], a[224:227], v[112:115], v[188:191]\n\t" \
  "ds_read_b64_tr_b16 a[184:185], v229 offset:768\n\t" \
  "v_cvt_pk_bf16_f32 v159, v110, v111\n\t" \
  "v_mfma_f32_16x16x32_bf16 v[104:107], a[228:231], v[116:119], v[104:107]\n\t" \
  "ds_read_b64_tr_b16 a[186:187], v229 offset:4864\n\t" \
  "v_mfma_f32_16x16x32_bf16 v[104:107], a[232:235], v[120:123], v[104:107]\n\t" \
  "ds_read_b64_tr_b16 a[188:189], v229 offset:8960\n\t" \
  "v_max3_f32 v203, v99, v100, v101\n\t" \
  "v_mfma_f32_16x16x32_bf16 v[104:107], a[236:239], v[124:127], v[104:107]\n\t" \
  "ds_read_b64_tr_b16 a[190:191], v229 offset:13056\n\t" \
  "v_max3_f32 v192, v192, v102, v103\n\t" \
  "v_mfma_f32_16x16x32_bf16 v[108:111], a[252:255], v[124:127], v[188:191]\n\t" \
  "v_exp_f32 v0, v0\n\t" \
  "v_mfma_f32_16x16x32_bf16 v[108:111], a[248:251], v[120:123], v[108:111]\n\t" \
  "v_exp_f32 v1, v1\n\t" \
  "v_mfma_f32_16x16x32_bf16 v[108:111], a[244:247], v[116:119], v[108:111]\n\t" \
  "s_waitcnt vmcnt(0) lgkmcnt(0)\n\t" \
  "v_max3_f32 v203, v203, v104, v105\n\t" \
  "v_max3_f32 v192, v192, v106, v107\n\t" \
  "v_mfma_f32_16x16x32_bf16 v[108:111], a[240:243], v[112:115], v[108:111]\n\t" \
  "s_barrier\n\t" \
  "v_mfma_f32_16x16x32_bf16 a[0:3], a[128:131], v[128:131], a[0:3]\n\t" \
  "v_exp_f32 v2, v2\n\t" \
  "v_mfma_f32_16x16x32_bf16 a[0:3], a[132:135], v[132:135], a[0:3]\n\t" \
  "ds_read_b64_tr_b16 v[16:17], v210 offset:0\n\t" \
  "v_cvt_pk_bf16_f32 v0, v0, v1\n\t" \
  "v_mfma_f32_16x16x32_bf16 a[4:7], a[136:139], v[128:131], a[4:7]\n\t" \
  "ds_read_b64_tr_b16 v[18:19], v210 offset:4096\n\t" \
  "v_max3_f32 v203, v203, v108, v109\n\t" \
  "v_mfma_f32_16x16x32_bf16 a[4:7], a[140:143], v[132:135], a[4:7]\n\t" \
  "v_max3_f32 v192, v192, v110, v111\n\t" \
  "v_max_f32 v192, v192, v203\n\t" \
  "v_mfma_f32_16x16x32_bf16 a[8:11], a[144:147], v[128:131], a[8:11]\n\t" \
  "ds_read_b64_tr_b16 v[20:21], v210 offset:8192\n\t" \
  "v_cmp_lt_f32 vcc, 0x42800000, v192\n\t" \
  "v_mfma_f32_16x16x32_bf16 a[8:11], a[148:151], v[132:135], a[8:11]\n\t" \
  "v_exp_f32 v3, v3\n\t" \
  "v_mfma_f32_16x16x32_bf16 a[12:15], a[152:155], v[128:131], a[12:15]\n\t" \
  "s_cbranch_vccnz .Lsf_c73\n\t" \
  ".Lsf_r73:\n\t" \
  "v_mfma_f32_16x16x32_bf16 a[12:15], a[156:159], v[132:135], a[12:15]\n\t" \
  "ds_read_b64_tr_b16 v[22:23], v210 offset:12288\n\t" \
  "v_cvt_pk_bf16_f32 v1, v2, v3\n\t" \
  "v_mfma_f32_16x16x32_bf16 a[16:19], a[160:163], v[128:131], a[16:19]\n\t" \
  "v_exp_f32 v4, v4\n\t" \
  "v_mfma_f32_16x16x32_bf16 a[16:19], a[164:167], v[132:135], a[16:19]\n\t" \
  "v_exp_f32 v5, v5\n\t" \
  "v_mfma_f32_16x16x32_bf16 a[20:23], a[168:171], v[128:131], a[20:23]\n\t" \
  "ds_read_b64_tr_b16 v[24:25], v211 offset:0\n\t" \
  "v_cvt_pk_bf16_f32 v2, v4, v5\n\t" \
  "v_mfma_f32_16x16x32_bf16 a[20:23], a[172:175], v[132:135], a[20:23]\n\t" \
  "v_exp_f32 v6, v6\n\t" \
  "v_mfma_f32_16x16x32_bf16 a[24:27], a[176:179], v[128:131], a[24:27]\n\t" \
  "v_exp_f32 v7, v7\n\t" \
  "v_mfma_f32_16x16x32_bf16 a[24:27], a[180:183], v[132:135], a[24:27]\n\t" \
  "ds_read_b64_tr_b16 v[26:27], v211 offset:4096\n\t" \
  "v_cvt_pk_bf16_f32 v3, v6, v7\n\t" \
  "v_mfma_f32_16x16x32_bf16 a[28:31], a[184:187], v[128:131], a[28:31]\n\t" \
  "v_exp_f32 v8, v8\n\t" \
  "v_mfma_f32_16x16x32_bf16 a[28:31], a[188:191], v[132:135], a[28:31]\n\t" \
  "ds_read_b64_tr_b16 v[28:29], v211 offset:8192\n\t" \
  "v_smfmac_f32_16x16x64_bf16 v[160:163], v[216:219], v[128:135], v208\n\t" \
  "ds_read_b64_tr_b16 v[30:31], v211 offset:12288\n\t" \
  "v_mfma_f32_16x16x32_bf16 a[32:35], a[128:131], v[136:139], a[32:35]\n\t" \
  "ds_read_b64_tr_b16 v[48:49], v210 offset:256\n\t" \
  "v_mfma_f32_16x16x32_bf16 a[32:35], a[132:135], v[140:143], a[32:35]\n\t" \
  "ds_read_b64_tr_b16 v[50:51], v210 offset:4352\n\t" \
  "v_mfma_f32_16x16x32_bf16 a[36:39], a[136:139], v[136:139], a[36:39]\n\t" \
  "v_exp_f32 v9, v9\n\t" \
  "v_mfma_f32_16x16x32_bf16 a[36:39], a[140:143], v[140:143], a[36:39]\n\t" \
  "ds_read_b64_tr_b16 v[52:53], v210 offset:8448\n\t" \
  "v_cvt_pk_bf16_f32 v4, v8, v9\n\t" \
  "v_mfma_f32_16x16x32_bf16 a[40:43], a[144:147], v[136:139], a[40:43]\n\t" \
  "v_exp_f32 v10, v10\n\t" \
  "v_mfma_f32_16x16x32_bf16 a[40:43], a[148:151], v[140:143], a[40:43]\n\t" \
  "ds_read_b64_tr_b16 v[54:55], v210 offset:12544\n\t" \
  "v_mfma_f32_16x16x32_bf16 a[44:47], a[152:155], v[136:139], a[44:47]\n\t" \
  "v_exp_f32 v11, v11\n\t" \
  "v_mfma_f32_16x16x32_bf16 a[44:47], a[156:159], v[140:143], a[44:47]\n\t" \
  "ds_read_b64_tr_b16 v[56:57], v211 offset:256\n\t" \
  "v_cvt_pk_bf16_f32 v5, v10, v11\n\t" \
  "v_mfma_f32_16x16x32_bf16 a[48:51], a[160:163], v[136:139], a[48:51]\n\t" \
  "v_exp_f32 v12, v12\n\t" \
  "v_mfma_f32_16x16x32_bf16 a[48:51], a[164:167], v[140:143], a[48:51]\n\t" \
  "v_exp_f32 v13, v13\n\t" \
  "v_mfma_f32_16x16x32_bf16 a[52:55], a[168:171], v[136:139], a[52:55]\n\t" \
  "ds_read_b64_tr_b16 v[58:59], v211 offset:4352\n\t" \
  "v_cvt_pk_bf16_f32 v6, v12, v13\n\t" \
  "v_mfma_f32_16x16x32_bf16 a[52:55], a[172:175], v[140:143], a[52:55]\n\t" \
  "ds_read_b64_tr_b16 v[60:61], v211 offset:8448\n\t" \
  "v_mfma_f32_16x16x32_bf16 a[56:59], a[176:179], v[136:139], a[56:59]\n\t" \
  "v_exp_f32 v14, v14\n\t" \
  "v_mfma_f32_16x16x32_bf16 a[56:59], a[180:183], v[140:143], a[56:59]\n\t" \
  "v_exp_f32 v15, v15\n\t" \
  "v_mfma_f32_16x16x32_bf16 a[60:63], a[184:187], v[136:139], a[60:63]\n\t" \
  "ds_read_b64_tr_b16 v[62:63], v211 offset:12544\n\t" \
  "v_cvt_pk_bf16_f32 v7, v14, v15\n\t" \
  "v_mfma_f32_16x16x32_bf16 a[60:63], a[188:191], v[140:143], a[60:63]\n\t" \
  "v_exp_f32 v32, v32\n\t" \
  "v_smfmac_f32_16x16x64_bf16 v[162:165], v[216:219], v[136:143], v208\n\t" \
  "ds_read_b64_tr_b16 v[80:81], v210 offset:512\n\t" \
  "v_mfma_f32_16x16x32_bf16 a[64:67], a[128:131], v[144:147], a[64:67]\n\t" \
  "v_exp_f32 v33, v33\n\t" \
  "v_mfma_f32_16x16x32_bf16 a[64:67], a[132:135], v[148:151], a[64:67]\n\t" \
  "ds_read_b64_tr_b16 v[82:83], v210 offset:4608\n\t" \
  "v_cvt_pk_bf16_f32 v32, v32, v33\n\t" \
  "v_mfma_f32_16x16x32_bf16 a[68:71], a[136:139], v[144:147], a[68:71]\n\t" \
  "v_exp_f32 v34, v34\n\t" \
  "v_mfma_f32_16x16x32_bf16 a[68:71], a[140:143], v[148:151], a[68:71]\n\t" \
  "ds_read_b64_tr_b16 v[84:85], v210 offset:8704\n\t" \
  "v_mfma_f32_16x16x32_bf16 a[72:75], a[144:147], v[144:147], a[72:75]\n\t" \
  "v_exp_f32 v35, v35\n\t" \
  "v_mfma_f32_16x16x32_bf16 a[72:75], a[148:151], v[148:151], a[72:75]\n\t" \
  "ds_read_b64_tr_b16 v[86:87], v210 offset:12800\n\t" \
  "v_cvt_pk_bf16_f32 v33, v34, v35\n\t" \
  "v_mfma_f32_16x16x32_bf16 a[76:79], a[152:155], v[144:147], a[76:79]\n\t" \
  "v_exp_f32 v36, v36\n\t" \
  "v_mfma_f32_16x16x32_bf16 a[76:79], a[156:159], v[148:151], a[76:79]\n\t" \
  "ds_read_b64_tr_b16 v[88:89], v211 offset:512\n\t" \
  "v_mfma_f32_16x16x32_bf16 a[80:83], a[160:163], v[144:147], a[80:83]\n\t" \
  "v_exp_f32 v37, v37\n\t" \
  "v_mfma_f32_16x16x32_bf16 a[80:83], a[164:167], v[148:151], a[80:83]\n\t" \
  "ds_read_b64_tr_b16 v[90:91], v211 offset:4608\n\t" \
  "v_cvt_pk_bf16_f32 v34, v36, v37\n\t" \
  "v_mfma_f32_16x16x32_bf16 a[84:87], a[168:171], v[144:147], a[84:87]\n\t" \
  "v_exp_f32 v38, v38\n\t" \
  "v_mfma_f32_16x16x32_bf16 a[84:87], a[172:175], v[148:151], a[84:87]\n\t" \
  "v_exp_f32 v39, v39\n\t" \
  "v_mfma_f32_16x16x32_bf16 a[88:91], a[176:179], v[144:147], a[88:91]\n\t" \
  "ds_read_b64_tr_b16 v[92:93], v211 offset:8704\n\t" \
  "v_cvt_pk_bf16_f32 v35, v38, v39\n\t" \
  "v_mfma_f32_16x16x32_bf16 a[88:91], a[180:183], v[148:151], a[88:91]\n\t" \
  "v_exp_f32 v40, v40\n\t" \
  "v_mfma_f32_16x16x32_bf16 a[92:95], a[184:187], v[144:147], a[92:95]\n\t" \
  "ds_read_b64_tr_b16 v[94:95], v211 offset:12800\n\t" \
  "v_mfma_f32_16x16x32_bf16 a[92:95], a[188:191], v[148:151], a[92:95]\n\t" \
  "v_exp_f32 v41, v41\n\t" \
  "v_smfmac_f32_16x16x64_bf16 v[164:167], v[216:219], v[144:151], v208\n\t" \
  "ds_read_b64_tr_b16 v[112:113], v210 offset:768\n\t" \
  "v_cvt_pk_bf16_f32 v36, v40, v41\n\t" \
  "v_mfma_f32_16x16x32_bf16 a[96:99], a[128:131], v[152:155], a[96:99]\n\t" \
  "ds_read_b64_tr_b16 v[114:115], v210 offset:4864\n\t" \
  "v_mfma_f32_16x16x32_bf16 a[96:99], a[132:135], v[156:159], a[96:99]\n\t" \
  "v_exp_f32 v42, v42\n\t" \
  "v_mfma_f32_16x16x32_bf16 a[100:103], a[136:139], v[152:155], a[100:103]\n\t" \
  "ds_read_b64_tr_b16 v[116:117], v210 offset:8960\n\t" \
  "v_mfma_f32_16x16x32_bf16 a[100:103], a[140:143], v[156:159], a[100:103]\n\t" \
  "v_exp_f32 v43, v43\n\t" \
  "v_mfma_f32_16x16x32_bf16 a[104:107], a[144:147], v[152:155], a[104:107]\n\t" \
  "ds_read_b64_tr_b16 v[118:119], v210 offset:13056\n\t" \
  "v_cvt_pk_bf16_f32 v37, v42, v43\n\t" \
  "v_mfma_f32_16x16x32_bf16 a[104:107], a[148:151], v[156:159], a[104:107]\n\t" \
  "v_exp_f32 v44, v44\n\t" \
  "v_mfma_f32_16x16x32_bf16 a[108:111], a[152:155], v[152:155], a[108:111]\n\t" \
  "ds_read_b64_tr_b16 v[120:121], v211 offset:768\n\t" \
  "v_mfma_f32_16x16x32_bf16 a[108:111], a[156:159], v[156:159], a[108:111]\n\t" \
  "v_exp_f32 v45, v45\n\t" \
  "v_mfma_f32_16x16x32_bf16 a[112:115], a[160:163], v[152:155], a[112:115]\n\t" \
  "ds_read_b64_tr_b16 v[122:123], v211 offset:4864\n\t" \
  "v_cvt_pk_bf16_f32 v38, v44, v45\n\t" \
  "v_mfma_f32_16x16x32_bf16 a[112:115], a[164:167], v[156:159], a[112:115]\n\t" \
  "v_exp_f32 v46, v46\n\t" \
  "v_mfma_f32_16x16x32_bf16 a[116:119], a[168:171], v[152:155], a[116:119]\n\t" \
  "v_exp_f32 v47, v47\n\t" \
  "v_mfma_f32_16x16x32_bf16 a[116:119], a[172:175], v[156:159], a[116:119]\n\t" \
  "ds_read_b64_tr_b16 v[124:125], v211 offset:8960\n\t" \
  "v_cvt_pk_bf16_f32 v39, v46, v47\n\t" \
  "v_mfma_f32_16x16x32_bf16 a[120:123], a[176:179], v[152:155], a[120:123]\n\t" \
  "v_exp_f32 v64, v64\n\t" \
  "v_mfma_f32_16x16x32_bf16 a[120:123], a[180:183], v[156:159], a[120:123]\n\t" \
  "ds_read_b64_tr_b16 v[126:127], v211 offset:13056\n\t" \
  "v_mfma_f32_16x16x32_bf16 a[124:127], a[184:187], v[152:155], a[124:127]\n\t" \
  "v_exp_f32 v65, v65\n\t" \
  "v_mfma_f32_16x16x32_bf16 a[124:127], a[188:191], v[156:159], a[124:127]\n\t" \
  "v_exp_f32 v66, v66\n\t" \
  "v_smfmac_f32_16x16x64_bf16 v[166:169], v[216:219], v[152:159], v208\n\t" \
  "s_waitcnt lgkmcnt(15)\n\t" \
  "s_cmp_lg_u32 s99, 0\n\t" \
  "s_cbranch_scc1 .Lsf_p7\n\t" \
  ".Lsf_q7:\n\t" \
  "v_mfma_f32_16x16x32_bf16 a[0:3], v[16:19], v[0:3], a[0:3]\n\t" \
  "v_exp_f32 v67, v67\n\t" \
  "v_mfma_f32_16x16x32_bf16 a[0:3], v[20:23], v[4:7], a[0:3]\n\t" \
  "v_exp_f32 v68, v68\n\t" \
  "v_smfmac_f32_16x16x64_bf16 v[160:163], v[216:219], v[0:7], v208\n\t" \
  "v_cvt_pk_bf16_f32 v64, v64, v65\n\t" \
  "v_mfma_f32_16x16x32_bf16 a[4:7], v[24:27], v[0:3], a[4:7]\n\t" \
  "v_exp_f32 v69, v69\n\t" \
  "v_mfma_f32_16x16x32_bf16 a[4:7], v[28:31], v[4:7], a[4:7]\n\t" \
  "v_exp_f32 v70, v70\n\t" \
  "v_mfma_f32_16x16x32_bf16 a[8:11], v[48:51], v[0:3], a[8:11]\n\t" \
  "v_exp_f32 v71, v71\n\t" \
  "v_mfma_f32_16x16x32_bf16 a[8:11], v[52:55], v[4:7], a[8:11]\n\t" \
  "v_exp_f32 v72, v72\n\t" \
  "v_mfma_f32_16x16x32_bf16 a[12:15], v[56:59], v[0:3], a[12:15]\n\t" \
  "v_exp_f32 v73, v73\n\t" \
  "v_mfma_f32_16x16x32_bf16 a[12:15], v[60:63], v[4:7], a[12:15]\n\t" \
  "s_waitcnt lgkmcnt(8)\n\t" \
  "v_exp_f32 v74, v74\n\t" \
  "v_mfma_f32_16x16x32_bf16 a[16:19], v[80:83], v[0:3], a[16:19]\n\t" \
  "v_exp_f32 v75, v75\n\t" \
  "v_mfma_f32_16x16x32_bf16 a[16:19], v[84:87], v[4:7], a[16:19]\n\t" \
  "v_cvt_pk_bf16_f32 v65, v66, v67\n\t" \
  "v_cvt_pk_bf16_f32 v66, v68, v69\n\t" \
  "v_mfma_f32_16x16x32_bf16 a[20:23], v[88:91], v[0:3], a[20:23]\n\t" \
  "v_exp_f32 v76, v76\n\t" \
  "v_mfma_f32_16x16x32_bf16 a[20:23], v[92:95], v[4:7], a[20:23]\n\t" \
  "s_waitcnt lgkmcnt(0)\n\t" \
  "v_exp_f32 v77, v77\n\t" \
  "v_mfma_f32_16x16x32_bf16 a[24:27], v[112:115], v[0:3], a[24:27]\n\t" \
  "v_cvt_pk_bf16_f32 v67, v70, v71\n\t" \
  "v_cvt_pk_bf16_f32 v68, v72, v73\n\t" \
  "v_mfma_f32_16x16x32_bf16 a[24:27], v[116:119], v[4:7], a[24:27]\n\t" \
  "v_exp_f32 v78, v78\n\t" \
  "v_mfma_f32_16x16x32_bf16 a[28:31], v[120:123], v[0:3], a[28:31]\n\t" \
  "v_exp_f32 v79, v79\n\t" \
  "v_mfma_f32_16x16x32_bf16 a[28:31], v[124:127], v[4:7], a[28:31]\n\t" \
  "v_cvt_pk_bf16_f32 v69, v74, v75\n\t" \
  "v_cvt_pk_bf16_f32 v70, v76, v77\n\t" \
  "v_mfma_f32_16x16x32_bf16 a[32:35], v[16:19], v[32:35], a[32:35]\n\t" \
  "v_exp_f32 v96, v96\n\t" \
  "v_mfma_f32_16x16x32_bf16 a[32:35], v[20:23], v[36:39], a[32:35]\n\t" \
  "v_exp_f32 v97, v97\n\t" \
  "v_smfmac_f32_16x16x64_bf16 v[162:165], v[216:219], v[32:39], v208\n\t" \
  "v_cvt_pk_bf16_f32 v71, v78, v79\n\t" \
  "v_mfma_f32_16x16x32_bf16 a[36:39], v[24:27], v[32:35], a[36:39]\n\t" \
  "v_exp_f32 v98, v98\n\t" \
  "v_mfma_f32_16x16x32_bf16 a[36:39], v[28:31], v[36:39], a[36:39]\n\t" \
  "v_exp_f32 v99, v99\n\t" \
  "v_mfma_f32_16x16x32_bf16 a[40:43], v[48:51], v[32:35], a[40:43]\n\t" \
  "v_exp_f32 v100, v100\n\t" \
  "v_mfma_f32_16x16x32_bf16 a[40:43], v[52:55], v[36:39], a[40:43]\n\t" \
  "v_exp_f32 v101, v101\n\t" \
  "v_mfma_f32_16x16x32_bf16 a[44:47], v[56:59], v[32:35], a[44:47]\n\t" \
  "v_exp_f32 v102, v102\n\t" \
  "v_mfma_f32_16x16x32_bf16 a[44:47], v[60:63], v[36:39], a[44:47]\n\t" \
  "v_exp_f32 v103, v103\n\t" \
  "v_mfma_f32_16x16x32_bf16 a[48:51], v[80:83], v[32:35], a[48:51]\n\t" \
  "v_exp_f32 v104, v104\n\t" \
  "v_mfma_f32_16x16x32_bf16 a[48:51], v[84:87], v[36:39], a[48:51]\n\t" \
  "v_exp_f32 v105, v105\n\t" \
  "v_mfma_f32_16x16x32_bf16 a[52:55], v[88:91], v[32:35], a[52:55]\n\t" \
  "v_cvt_pk_bf16_f32 v96, v96, v97\n\t" \
  "v_cvt_pk_bf16_f32 v97, v98, v99\n\t" \
  "v_mfma_f32_16x16x32_bf16 a[52:55], v[92:95], v[36:39], a[52:55]\n\t" \
  "v_exp_f32 v106, v106\n\t" \
  "v_mfma_f32_16x16x32_bf16 a[56:59], v[112:115], v[32:35], a[56:59]\n\t" \
  "v_exp_f32 v107, v107\n\t" \
  "v_mfma_f32_16x16x32_bf16 a[56:59], v[116:119], v[36:39], a[56:59]\n\t" \
  "v_cvt_pk_bf16_f32 v98, v100, v101\n\t" \
  "v_cvt_pk_bf16_f32 v99, v102, v103\n\t" \
  "v_mfma_f32_16x16x32_bf16 a[60:63], v[120:123], v[32:35], a[60:63]\n\t" \
  "v_exp_f32 v108, v108\n\t" \
  "v_mfma_f32_16x16x32_bf16 a[60:63], v[124:127], v[36:39], a[60:63]\n\t" \
  "v_exp_f32 v109, v109\n\t" \
  "v_mfma_f32_16x16x32_bf16 a[64:67], v[16:19], v[64:67], a[64:67]\n\t" \
  "v_cvt_pk_bf16_f32 v100, v104, v105\n\t" \
  "v_cvt_pk_bf16_f32 v101, v106, v107\n\t" \
  "v_mfma_f32_16x16x32_bf16 a[64:67], v[20:23], v[68:71], a[64:67]\n\t" \
  "v_exp_f32 v110, v110\n\t" \
  "v_smfmac_f32_16x16x64_bf16 v[164:167], v[216:219], v[64:71], v208\n\t" \
  "v_cvt_pk_bf16_f32 v102, v108, v109\n\t" \
  "v_mfma_f32_16x16x32_bf16 a[68:71], v[24:27], v[64:67], a[68:71]\n\t" \
  "v_exp_f32 v111, v111\n\t" \
  "v_mfma_f32_16x16x32_bf16 a[68:71], v[28:31], v[68:71], a[68:71]\n\t" \
  "v_cvt_pk_bf16_f32 v103, v110, v111\n\t" \
  "v_mfma_f32_16x16x32_bf16 a[72:75], v[48:51], v[64:67], a[72:75]\n\t" \
  "v_mfma_f32_16x16x32_bf16 a[72:75], v[52:55], v[68:71], a[72:75]\n\t" \
  "v_mfma_f32_16x16x32_bf16 a[76:79], v[56:59], v[64:67], a[76:79]\n\t" \
  "v_mfma_f32_16x16x32_bf16 a[76:79], v[60:63], v[68:71], a[76:79]\n\t" \
  "v_mfma_f32_16x16x32_bf16 a[80:83], v[80:83], v[64:67], a[80:83]\n\t" \
  "v_mfma_f32_16x16x32_bf16 a[80:83], v[84:87], v[68:71], a[80:83]\n\t" \
  "v_mfma_f32_16x16x32_bf16 a[84:87], v[88:91], v[64:67], a[84:87]\n\t" \
  "v_mfma_f32_16x16x32_bf16 a[84:87], v[92:95], v[68:71], a[84:87]\n\t" \
  "v_mfma_f32_16x16x32_bf16 a[88:91], v[112:115], v[64:67], a[88:91]\n\t" \
  "v_mfma_f32_16x16x32_bf16 a[88:91], v[116:119], v[68:71], a[88:91]\n\t" \
  "v_mfma_f32_16x16x32_bf16 a[92:95], v[120:123], v[64:67], a[92:95]\n\t" \
  "v_mfma_f32_16x16x32_bf16 a[92:95], v[124:127], v[68:71], a[92:95]\n\t" \
  "v_mfma_f32_16x16x32_bf16 a[96:99], v[16:19], v[96:99], a[96:99]\n\t" \
  "v_mfma_f32_16x16x32_bf16 a[96:99], v[20:23], v[100:103], a[96:99]\n\t" \
  "v_smfmac_f32_16x16x64_bf16 v[166:169], v[216:219], v[96:103], v208\n\t" \
  "v_mfma_f32_16x16x32_bf16 a[100:103], v[24:27], v[96:99], a[100:103]\n\t" \
  "v_mfma_f32_16x16x32_bf16 a[100:103], v[28:31], v[100:103], a[100:103]\n\t" \
  "v_mfma_f32_16x16x32_bf16 a[104:107], v[48:51], v[96:99], a[104:107]\n\t" \
  "v_mfma_f32_16x16x32_bf16 a[104:107], v[52:55], v[100:103], a[104:107]\n\t" \
  "v_mfma_f32_16x16x32_bf16 a[108:111], v[56:59], v[96:99], a[108:111]\n\t" \
  "v_mfma_f32_16x16x32_bf16 a[108:111], v[60:63], v[100:103], a[108:111]\n\t" \
  "v_mfma_f32_16x16x32_bf16 a[112:115], v[80:83], v[96:99], a[112:115]\n\t" \
  "v_mfma_f32_16x16x32_bf16 a[112:115], v[84:87], v[100:103], a[112:115]\n\t" \
  "v_mfma_f32_16x16x32_bf16 a[116:119], v[88:91], v[96:99], a[116:119]\n\t" \
  "v_mfma_f32_16x16x32_bf16 a[116:119], v[92:95], v[100:103], a[116:119]\n\t" \
  "v_mfma_f32_16x16x32_bf16 a[120:123], v[112:115], v[96:99], a[120:123]\n\t" \
  "v_mfma_f32_16x16x32_bf16 a[120:123], v[116:119], v[100:103], a[120:123]\n\t" \
  "v_mfma_f32_16x16x32_bf16 a[124:127], v[120:123], v[96:99], a[124:127]\n\t" \
  "v_mfma_f32_16x16x32_bf16 a[124:127], v[124:127], v[100:103], a[124:127]\n\t" \
  "s_nop 7\n\t" \
  "s_waitcnt vmcnt(0)\n\t" \
  "s_barrier\n\t" \
  "s_add_u32 s77, s51, 131072\n\t" \
  "s_mov_b32 s89, 0\n\t" \
  "s_brev_b32 s78, 1\n\t" \
  "s_mov_b32 s79, 0x0f800000\n\t" \
  "s_waitcnt vmcnt(0) lgkmcnt(0)\n\t" \
  "s_barrier\n\t" \
  "s_waitcnt vmcnt(0) lgkmcnt(0)\n\t" \
  "s_barrier\n\t" \
  "s_waitcnt vmcnt(0) lgkmcnt(0)\n\t" \
  "s_barrier\n\t" \
  "s_add_u32 m0, s38, 16384\n\t" \
  "s_mov_b32 s96, 0\n\t" \
  "buffer_load_dwordx4 v232, s[68:71], s96 offen offset:0 lds\n\t" \
  "s_mov_b32 s96, 6144\n\t" \
  "buffer_load_dwordx4 v232, s[68:71], s96 offen offset:1024 lds\n\t" \
  "s_mov_b32 s96, 12288\n\t" \
  "buffer_load_dwordx4 v232, s[68:71], s96 offen offset:2048 lds\n\t" \
  "s_mov_b32 s96, 18432\n\t" \
  "buffer_load_dwordx4 v232, s[68:71], s96 offen offset:3072 lds\n\t" \
  "s_waitcnt vmcnt(0) lgkmcnt(0)\n\t" \
  "s_barrier\n\t" \
  "s_add_u32 m0, s38, 32768\n\t" \
  "s_mov_b32 s96, 28672\n\t" \
  "buffer_load_dwordx4 v232, s[68:71], s96 offen offset:0 lds\n\t" \
  "s_mov_b32 s96, 34816\n\t" \
  "buffer_load_dwordx4 v232, s[68:71], s96 offen offset:1024 lds\n\t" \
  "s_add_u32 m0, s38, 32768\n\t" \
  "s_mov_b32 s96, 40960\n\t" \
  "buffer_load_dwordx4 v232, s[68:71], s96 offen offset:2048 lds\n\t" \
  "s_mov_b32 s96, 47104\n\t" \
  "buffer_load_dwordx4 v232, s[68:71], s96 offen offset:3072 lds\n\t" \
  "s_add_u32 m0, s38, 114688\n\t" \
  "s_mov_b32 s96, 0\n\t" \
  "buffer_load_dwordx4 v232, s[72:75], s96 offen offset:0 lds\n\t" \
  "s_mov_b32 s96, 6144\n\t" \
  "buffer_load_dwordx4 v232, s[72:75], s96 offen offset:1024 lds\n\t" \
  "s_waitcnt vmcnt(0) lgkmcnt(0)\n\t" \
  "s_barrier\n\t" \
  "s_add_u32 m0, s38, 114688\n\t" \
  "s_mov_b32 s96, 12288\n\t" \
  "buffer_load_dwordx4 v232, s[72:75], s96 offen offset:2048 lds\n\t" \
  "s_mov_b32 s96, 18432\n\t" \
  "buffer_load_dwordx4 v232, s[72:75], s96 offen offset:3072 lds\n\t" \
  "s_add_u32 m0, s38, 131072\n\t" \
  "s_mov_b32 s96, 28672\n\t" \
  "buffer_load_dwordx4 v232, s[72:75], s96 offen offset:0 lds\n\t" \
  "s_mov_b32 s96, 34816\n\t" \
  "buffer_load_dwordx4 v232, s[72:75], s96 offen offset:1024 lds\n\t" \
  "s_mov_b32 s96, 40960\n\t" \
  "buffer_load_dwordx4 v232, s[72:75], s96 offen offset:2048 lds\n\t" \
  "s_mov_b32 s96, 47104\n\t" \
  "buffer_load_dwordx4 v232, s[72:75], s96 offen offset:3072 lds\n\t" \
  "s_waitcnt vmcnt(0) lgkmcnt(0)\n\t" \
  "s_barrier\n\t" \
  "s_waitcnt vmcnt(0) lgkmcnt(0)\n\t" \
  "s_barrier\n\t" \
  "buffer_load_dword v209, v251, s[16:19], 0 offen sc1\n\t" \
  "ds_read_b128 v[192:195], v215 offset:32768\n\t" \
  "ds_read_b128 v[196:199], v215 offset:33792\n\t" \
  "s_waitcnt vmcnt(0) lgkmcnt(0)\n\t" \
  "v_and_b32 v250, 0xbfffbfff, v209\n\t" \
  "v_cmp_ne_u32_e64 s[84:85], s36, v250\n\t" \
  "s_cmp_lg_u64 s[84:85], 0\n\t" \
  "s_cbranch_scc1 .Lsf_v\n\t" \
  ".Lsf_w:\n\t" \
  "v_and_b32 v250, 1, v233\n\t" \
  "v_lshlrev_b32 v250, 4, v250\n\t" \
  "v_lshrrev_b32 v250, v250, v209\n\t" \
  "v_and_b32 v250, 0x4000, v250\n\t" \
  "v_cmp_ne_u32_e64 s[84:85], 0, v250\n\t" \
  "s_bcnt1_i32_b32 s98, s84\n\t" \
  "s_bcnt1_i32_b32 s54, s85\n\t" \
  "s_cmp_gt_u32 s98, s54\n\t" \
  "s_cselect_b32 s55, 0x2000, 0\n\t" \
  "s_cselect_b32 s98, s84, s85\n\t" \
  "s_cmp_lg_u32 s55, 0\n\t" \
  "s_cselect_b32 s84, s85, s84\n\t" \
  "s_lshr_b32 s85, s98, 16\n\t" \
  "s_bcnt1_i32_b32 s85, s85\n\t" \
  "s_lshr_b32 s84, s84, 16\n\t" \
  "s_bcnt1_i32_b32 s84, s84\n\t" \
  "s_add_u32 s84, s84, 7\n\t" \
  "s_cmp_ge_u32 s85, s84\n\t" \
  "s_cselect_b32 s54, -1, 0\n\t" \
  "s_xor_b32 s98, s42, s55\n\t" \
  "s_and_b32 s98, s98, s54\n\t" \
  "s_xor_b32 s45, s45, s98\n\t" \
  "s_xor_b32 s46, s46, s98\n\t" \
  "s_xor_b32 s98, s43, s55\n\t" \
  "s_and_b32 s98, s98, s54\n\t" \
  "s_xor_b32 s47, s47, s98\n\t" \
  "s_xor_b32 s48, s48, s55\n\t" \
  "s_xor_b32 s49, s49, s55\n\t" \
  "s_xor_b32 s98, s45, 0\n\t" \
  "s_waitcnt vmcnt(0) lgkmcnt(0)\n\t" \
  "s_barrier\n\t" \
  "ds_read_b128 v[200:203], v215 offset:34816\n\t" \
  "s_lshl_b32 s64, s39, 17\n\t" \
  "s_add_u32 s96, s40, 0\n\t" \
  "s_and_b32 s96, s96, 0x1c0000\n\t" \
  "s_add_u32 s96, s96, s51\n\t" \
  "s_add_u32 m0, s38, 0\n\t" \
  "s_nop 0\n\t" \
  "buffer_load_dwordx4 v240, s[24:27], s96 offen offset:0 lds\n\t" \
  "s_bfe_u32 s70, s46, 0x20014\n\t" \
  "s_lshl_b32 s70, s70, 14\n\t" \
  "s_xor_b32 s67, s70, 49152\n\t" \
  "buffer_load_dwordx4 v241, s[24:27], s96 offen offset:1024 lds\n\t" \
  "s_lshl1_add_u32 s60, s67, s46\n\t" \
  "s_xor_b32 s68, s70, 32768\n\t" \
  "buffer_load_dwordx4 v248, s[24:27], s96 offen offset:2048 lds\n\t" \
  "s_lshl1_add_u32 s61, s68, s46\n\t" \
  "s_xor_b32 s69, s70, 16384\n\t" \
  "s_lshl1_add_u32 s62, s69, s46\n\t" \
  "buffer_load_dwordx4 v249, s[24:27], s96 offen offset:3072 lds\n\t" \
  "s_lshl1_add_u32 s63, s70, s46\n\t" \
  "s_add_u32 s96, s40, 262144\n\t" \
  "s_and_b32 s96, s96, 0x1c0000\n\t" \
  "s_add_u32 s96, s96, s51\n\t" \
  "s_add_u32 m0, s38, 49152\n\t" \
  "s_nop 0\n\t" \
  "buffer_load_dwordx4 v240, s[24:27], s96 offen offset:0 lds\n\t" \
  "s_mul_i32 s65, s39, 508\n\t" \
  "s_add_u32 s65, s65, s49\n\t" \
  "buffer_load_dwordx4 v241, s[24:27], s96 offen offset:1024 lds\n\t" \
  "s_lshl_b32 s80, s39, 2\n\t" \
  "s_add_u32 s80, s80, s3\n\t" \
  "s_lshl_b32 s80, s80, 6\n\t" \
  "buffer_load_dwordx4 v248, s[24:27], s96 offen offset:2048 lds\n\t" \
  "s_add_u32 s76, s38, 147456\n\t" \
  "s_lshl_b32 s75, s80, 2\n\t" \
  "s_add_u32 s75, s75, 2228224\n\t" \
  "buffer_load_dwordx4 v249, s[24:27], s96 offen offset:3072 lds\n\t" \
  "s_lshr_b32 s71, s67, 7\n\t" \
  "s_add_u32 s71, s71, s48\n\t" \
  "s_lshr_b32 s72, s68, 7\n\t" \
  "s_add_u32 s72, s72, s48\n\t" \
  "s_lshr_b32 s66, s69, 7\n\t" \
  "ds_read_b128 v[204:207], v215 offset:35840\n\t" \
  "s_add_u32 s66, s66, s48\n\t" \
  "s_lshr_b32 s73, s70, 7\n\t" \
  "s_add_u32 s73, s73, s48\n\t" \
  "s_lshl3_add_u32 s67, s67, s47\n\t" \
  "s_lshl3_add_u32 s68, s68, s47\n\t" \
  "s_add_u32 s68, s68, 4096\n\t" \
  "s_lshl3_add_u32 s69, s69, s47\n\t" \
  "s_add_u32 s69, s69, 16384\n\t" \
  "s_lshl3_add_u32 s70, s70, s47\n\t" \
  "s_waitcnt vmcnt(0) lgkmcnt(0)\n\t" \
  "s_barrier\n\t" \
  "s_add_u32 s96, s40, 0\n\t" \
  "s_and_b32 s96, s96, 0x1c0000\n\t" \
  "s_add_u32 s96, s96, s77\n\t" \
  "s_add_u32 m0, s38, 98304\n\t" \
  "s_nop 0\n\t" \
  "buffer_load_dwordx4 v240, s[24:27], s96 offen offset:0 lds\n\t" \
  "buffer_load_dwordx4 v241, s[24:27], s96 offen offset:1024 lds\n\t" \
  "buffer_load_dwordx4 v248, s[24:27], s96 offen offset:2048 lds\n\t" \
  "buffer_load_dwordx4 v249, s[24:27], s96 offen offset:3072 lds\n\t" \
  "s_add_u32 s96, s40, 524288\n\t" \
  "s_and_b32 s96, s96, 0x1c0000\n\t" \
  "s_add_u32 s96, s96, s51\n\t" \
  "s_add_u32 m0, s38, 131072\n\t" \
  "s_waitcnt vmcnt(0) lgkmcnt(0)\n\t" \
  "buffer_load_dwordx4 v240, s[24:27], s96 offen offset:0 lds\n\t" \
  "buffer_load_dwordx4 v241, s[24:27], s96 offen offset:1024 lds\n\t" \
  "buffer_load_dwordx4 v248, s[24:27], s96 offen offset:2048 lds\n\t" \
  "buffer_load_dwordx4 v249, s[24:27], s96 offen offset:3072 lds\n\t" \
  "s_add_u32 s96, s40, 262144\n\t" \
  "s_and_b32 s96, s96, 0x1c0000\n\t" \
  "s_add_u32 s96, s96, s77\n\t" \
  "s_add_u32 m0, s38, 65536\n\t" \
  "s_nop 0\n\t" \
  "buffer_load_dwordx4 v240, s[24:27], s96 offen offset:0 lds\n\t" \
  "buffer_load_dwordx4 v241, s[24:27], s96 offen offset:1024 lds\n\t" \
  "buffer_load_dwordx4 v248, s[24:27], s96 offen offset:2048 lds\n\t" \
  "buffer_load_dwordx4 v249, s[24:27], s96 offen offset:3072 lds\n\t" \
  "s_add_u32 s96, s40, 524288\n\t" \
  "s_and_b32 s96, s96, 0x1c0000\n\t" \
  "s_add_u32 s96, s96, s77\n\t" \
  "s_add_u32 m0, s38, 81920\n\t" \
  "s_waitcnt vmcnt(0) lgkmcnt(0)\n\t" \
  "s_barrier\n\t" \
  "buffer_load_dwordx4 v240, s[24:27], s96 offen offset:0 lds\n\t" \
  "ds_read_b128 v[176:179], v215 offset:16384\n\t" \
  "ds_read_b128 v[180:183], v215 offset:17408\n\t" \
  "ds_read_b128 v[184:187], v215 offset:18432\n\t" \
  "buffer_load_dwordx4 v241, s[24:27], s96 offen offset:1024 lds\n\t" \
  "ds_read_b128 v[188:191], v215 offset:19456\n\t" \
  "buffer_load_dwordx4 v248, s[24:27], s96 offen offset:2048 lds\n\t" \
  "buffer_load_dwordx4 v249, s[24:27], s96 offen offset:3072 lds\n\t" \
  "s_add_u32 s96, s40, 786432\n\t" \
  "s_and_b32 s96, s96, 0x1c0000\n\t" \
  "s_add_u32 s96, s96, s51\n\t" \
  "s_add_u32 m0, s38, 114688\n\t" \
  "s_waitcnt vmcnt(0) lgkmcnt(0)\n\t" \
  "buffer_load_dwordx4 v240, s[24:27], s96 offen offset:0 lds\n\t" \
  "buffer_load_dwordx4 v241, s[24:27], s96 offen offset:1024 lds\n\t" \
  "buffer_load_dwordx4 v248, s[24:27], s96 offen offset:2048 lds\n\t" \
  "buffer_load_dwordx4 v249, s[24:27], s96 offen offset:3072 lds\n\t" \
  "v_add_u32 v230, 0x10000, v242\n\t" \
  "v_add_u32 v232, 0x20000, v242\n\t" \
  "ds_read_b64_tr_b16 a[192:193], v234 offset:0\n\t" \
  "v_add_f32 v225, v160, v161\n\t" \
  "v_rcp_f32 v224, v225\n\t" \
  "v_accvgpr_read_b32 v128, a0\n\t" \
  "v_accvgpr_read_b32 v129, a1\n\t" \
  "v_accvgpr_read_b32 v130, a2\n\t" \
  "v_accvgpr_read_b32 v131, a3\n\t" \
  "v_accvgpr_read_b32 v132, a4\n\t" \
  "v_accvgpr_read_b32 v133, a5\n\t" \
  "v_accvgpr_read_b32 v134, a6\n\t" \
  "v_accvgpr_read_b32 v135, a7\n\t" \
  "v_pk_mul_f32 v[128:129], v[128:129], v[224:225] op_sel_hi:[1,0]\n\t" \
  "v_pk_mul_f32 v[130:131], v[130:131], v[224:225] op_sel_hi:[1,0]\n\t" \
  "v_pk_mul_f32 v[132:133], v[132:133], v[224:225] op_sel_hi:[1,0]\n\t" \
  "v_pk_mul_f32 v[134:135], v[134:135], v[224:225] op_sel_hi:[1,0]\n\t" \
  "v_cvt_pk_bf16_f32 v128, v128, v129\n\t" \
  "v_cvt_pk_bf16_f32 v129, v130, v131\n\t" \
  "v_cvt_pk_bf16_f32 v130, v132, v133\n\t" \
  "v_cvt_pk_bf16_f32 v131, v134, v135\n\t" \
  "buffer_store_dwordx4 v[128:131], v242, s[16:19], s98 offen offset:0 sc1\n\t" \
  "ds_read_b64_tr_b16 a[194:195], v234 offset:4096\n\t" \
  "v_accvgpr_read_b32 v136, a8\n\t" \
  "v_accvgpr_read_b32 v137, a9\n\t" \
  "v_accvgpr_read_b32 v138, a10\n\t" \
  "v_accvgpr_read_b32 v139, a11\n\t" \
  "v_accvgpr_read_b32 v140, a12\n\t" \
  "v_accvgpr_read_b32 v141, a13\n\t" \
  "v_accvgpr_read_b32 v142, a14\n\t" \
  "v_accvgpr_read_b32 v143, a15\n\t" \
  "v_pk_mul_f32 v[136:137], v[136:137], v[224:225] op_sel_hi:[1,0]\n\t" \
  "v_pk_mul_f32 v[138:139], v[138:139], v[224:225] op_sel_hi:[1,0]\n\t" \
  "v_pk_mul_f32 v[140:141], v[140:141], v[224:225] op_sel_hi:[1,0]\n\t" \
  "v_pk_mul_f32 v[142:143], v[142:143], v[224:225] op_sel_hi:[1,0]\n\t" \
  "v_cvt_pk_bf16_f32 v136, v136, v137\n\t" \
  "v_cvt_pk_bf16_f32 v137, v138, v139\n\t" \
  "v_cvt_pk_bf16_f32 v138, v140, v141\n\t" \
  "v_cvt_pk_bf16_f32 v139, v142, v143\n\t" \
  "buffer_store_dwordx4 v[136:139], v242, s[16:19], s98 offen offset:1024 sc1\n\t" \
  "ds_read_b64_tr_b16 a[196:197], v235 offset:0\n\t" \
  "s_add_u32 s96, s40, 786432\n\t" \
  "ds_read_b64_tr_b16 a[198:199], v235 offset:4096\n\t" \
  "s_and_b32 s96, s96, 0x1c0000\n\t" \
  "s_add_u32 s96, s96, s77\n\t" \
  "s_add_u32 m0, s38, 147456\n\t" \
  "s_waitcnt vmcnt(0) lgkmcnt(0)\n\t" \
  "s_barrier\n\t" \
  "ds_read_b64_tr_b16 a[200:201], v234 offset:8192\n\t" \
  "v_accvgpr_read_b32 v0, a16\n\t" \
  "v_accvgpr_read_b32 v1, a17\n\t" \
  "v_accvgpr_read_b32 v2, a18\n\t" \
  "v_accvgpr_read_b32 v3, a19\n\t" \
  "v_accvgpr_read_b32 v4, a20\n\t" \
  "v_accvgpr_read_b32 v5, a21\n\t" \
  "v_accvgpr_read_b32 v6, a22\n\t" \
  "v_accvgpr_read_b32 v7, a23\n\t" \
  "v_pk_mul_f32 v[0:1], v[0:1], v[224:225] op_sel_hi:[1,0]\n\t" \
  "v_pk_mul_f32 v[2:3], v[2:3], v[224:225] op_sel_hi:[1,0]\n\t" \
  "v_pk_mul_f32 v[4:5], v[4:5], v[224:225] op_sel_hi:[1,0]\n\t" \
  "v_pk_mul_f32 v[6:7], v[6:7], v[224:225] op_sel_hi:[1,0]\n\t" \
  "v_cvt_pk_bf16_f32 v0, v0, v1\n\t" \
  "v_cvt_pk_bf16_f32 v1, v2, v3\n\t" \
  "v_cvt_pk_bf16_f32 v2, v4, v5\n\t" \
  "v_cvt_pk_bf16_f32 v3, v6, v7\n\t" \
  "buffer_store_dwordx4 v[0:3], v242, s[16:19], s98 offen offset:2048 sc1\n\t" \
  "v_accvgpr_read_b32 v8, a24\n\t" \
  "v_accvgpr_read_b32 v9, a25\n\t" \
  "v_accvgpr_read_b32 v10, a26\n\t" \
  "v_accvgpr_read_b32 v11, a27\n\t" \
  "v_accvgpr_read_b32 v12, a28\n\t" \
  "v_accvgpr_read_b32 v13, a29\n\t" \
  "v_accvgpr_read_b32 v14, a30\n\t" \
  "v_accvgpr_read_b32 v15, a31\n\t" \
  "v_pk_mul_f32 v[8:9], v[8:9], v[224:225] op_sel_hi:[1,0]\n\t" \
  "v_pk_mul_f32 v[10:11], v[10:11], v[224:225] op_sel_hi:[1,0]\n\t" \
  "v_pk_mul_f32 v[12:13], v[12:13], v[224:225] op_sel_hi:[1,0]\n\t" \
  "v_pk_mul_f32 v[14:15], v[14:15], v[224:225] op_sel_hi:[1,0]\n\t" \
  "v_cvt_pk_bf16_f32 v8, v8, v9\n\t" \
  "v_cvt_pk_bf16_f32 v9, v10, v11\n\t" \
  "v_cvt_pk_bf16_f32 v10, v12, v13\n\t" \
  "v_cvt_pk_bf16_f32 v11, v14, v15\n\t" \
  "buffer_store_dwordx4 v[8:11], v242, s[16:19], s98 offen offset:3072 sc1\n\t" \
  "buffer_load_dwordx4 v240, s[24:27], s96 offen offset:0 lds\n\t" \
  "s_xor_b32 s98, s45, 32768\n\t" \
  "ds_read_b64_tr_b16 a[202:203], v234 offset:12288\n\t" \
  "ds_read_b64_tr_b16 a[204:205], v235 offset:8192\n\t" \
  "ds_read_b128 a[224:227], v213 offset:16384\n\t" \
  "ds_read_b64_tr_b16 a[206:207], v235 offset:12288\n\t" \
  "ds_read_b128 a[228:231], v213 offset:17408\n\t" \
  "ds_read_b64_tr_b16 a[208:209], v236 offset:32768\n\t" \
  "ds_read_b128 a[232:235], v213 offset:18432\n\t" \
  "ds_read_b128 a[236:239], v213 offset:19456\n\t" \
  "buffer_load_dwordx4 v241, s[24:27], s96 offen offset:1024 lds\n\t" \
  "ds_write_b128 v213, v[128:131] offset:16384\n\t" \
  "ds_write_b128 v213, v[136:139] offset:17408\n\t" \
  "ds_write_b128 v213, v[0:3] offset:18432\n\t" \
  "ds_write_b128 v213, v[8:11] offset:19456\n\t" \
  "buffer_load_dwordx4 v248, s[24:27], s96 offen offset:2048 lds\n\t" \
  "ds_read_b64_tr_b16 a[210:211], v236 offset:36864\n\t" \
  "buffer_load_dwordx4 v249, s[24:27], s96 offen offset:3072 lds\n\t" \
  "ds_read_b64_tr_b16 a[212:213], v237 offset:32768\n\t" \
  "ds_read_b64_tr_b16 a[214:215], v237 offset:36864\n\t" \
  "v_add_f32 v227, v162, v163\n\t" \
  "v_rcp_f32 v226, v227\n\t" \
  "v_accvgpr_read_b32 v0, a32\n\t" \
  "v_accvgpr_read_b32 v1, a33\n\t" \
  "v_accvgpr_read_b32 v2, a34\n\t" \
  "v_accvgpr_read_b32 v3, a35\n\t" \
  "ds_read_b64_tr_b16 a[216:217], v236 offset:40960\n\t" \
  "v_accvgpr_read_b32 v4, a36\n\t" \
  "v_accvgpr_read_b32 v5, a37\n\t" \
  "v_accvgpr_read_b32 v6, a38\n\t" \
  "v_accvgpr_read_b32 v7, a39\n\t" \
  "v_mul_f32 v0, v0, v226\n\t" \
  "v_mul_f32 v1, v1, v226\n\t" \
  "v_mul_f32 v2, v2, v226\n\t" \
  "v_mul_f32 v3, v3, v226\n\t" \
  "v_mul_f32 v4, v4, v226\n\t" \
  "v_mul_f32 v5, v5, v226\n\t" \
  "v_mul_f32 v6, v6, v226\n\t" \
  "v_mul_f32 v7, v7, v226\n\t" \
  "ds_read_b64_tr_b16 a[218:219], v236 offset:45056\n\t" \
  "v_cvt_pk_bf16_f32 v32, v0, v1\n\t" \
  "v_cvt_pk_bf16_f32 v33, v2, v3\n\t" \
  "v_cvt_pk_bf16_f32 v34, v4, v5\n\t" \
  "v_cvt_pk_bf16_f32 v35, v6, v7\n\t" \
  "v_accvgpr_read_b32 v8, a40\n\t" \
  "v_accvgpr_read_b32 v9, a41\n\t" \
  "v_accvgpr_read_b32 v10, a42\n\t" \
  "v_accvgpr_read_b32 v11, a43\n\t" \
  "v_accvgpr_read_b32 v12, a44\n\t" \
  "v_accvgpr_read_b32 v13, a45\n\t" \
  "v_accvgpr_read_b32 v14, a46\n\t" \
  "v_accvgpr_read_b32 v15, a47\n\t" \
  "v_mul_f32 v8, v8, v226\n\t" \
  "v_mul_f32 v9, v9, v226\n\t" \
  "s_waitcnt vmcnt(0) lgkmcnt(0)\n\t" \
  "s_barrier\n\t" \
  "buffer_store_dwordx4 v[32:35], v242, s[16:19], s98 offen offset:0 sc1\n\t" \
  "v_mul_f32 v10, v10, v226\n\t" \
  "v_mul_f32 v11, v11, v226\n\t" \
  "v_mul_f32 v12, v12, v226\n\t" \
  "v_mul_f32 v13, v13, v226\n\t" \
  "v_mul_f32 v14, v14, v226\n\t" \
  "v_mul_f32 v15, v15, v226\n\t" \
  "v_cvt_pk_bf16_f32 v36, v8, v9\n\t" \
  "v_cvt_pk_bf16_f32 v37, v10, v11\n\t" \
  "v_cvt_pk_bf16_f32 v38, v12, v13\n\t" \
  "v_cvt_pk_bf16_f32 v39, v14, v15\n\t" \
  "v_accvgpr_read_b32 v0, a48\n\t" \
  "v_accvgpr_read_b32 v1, a49\n\t" \
  "v_accvgpr_read_b32 v2, a50\n\t" \
  "v_accvgpr_read_b32 v3, a51\n\t" \
  "v_accvgpr_read_b32 v4, a52\n\t" \
  "v_accvgpr_read_b32 v5, a53\n\t" \
  "v_accvgpr_read_b32 v6, a54\n\t" \
  "v_accvgpr_read_b32 v7, a55\n\t" \
  "v_mul_f32 v0, v0, v226\n\t" \
  "v_mul_f32 v1, v1, v226\n\t" \
  "v_mul_f32 v2, v2, v226\n\t" \
  "v_mul_f32 v3, v3, v226\n\t" \
  "v_mul_f32 v4, v4, v226\n\t" \
  "v_mul_f32 v5, v5, v226\n\t" \
  "v_mul_f32 v6, v6, v226\n\t" \
  "v_mul_f32 v7, v7, v226\n\t" \
  "v_cvt_pk_bf16_f32 v40, v0, v1\n\t" \
  "v_cvt_pk_bf16_f32 v41, v2, v3\n\t" \
  "v_cvt_pk_bf16_f32 v42, v4, v5\n\t" \
  "v_cvt_pk_bf16_f32 v43, v6, v7\n\t" \
  "v_accvgpr_read_b32 v8, a56\n\t" \
  "buffer_store_dwordx4 v[36:39], v242, s[16:19], s98 offen offset:1024 sc1\n\t" \
  "v_accvgpr_read_b32 v9, a57\n\t" \
  "v_accvgpr_read_b32 v10, a58\n\t" \
  "v_accvgpr_read_b32 v11, a59\n\t" \
  "v_accvgpr_read_b32 v12, a60\n\t" \
  "v_accvgpr_read_b32 v13, a61\n\t" \
  "v_accvgpr_read_b32 v14, a62\n\t" \
  "v_accvgpr_read_b32 v15, a63\n\t" \
  "v_mul_f32 v8, v8, v226\n\t" \
  "v_mul_f32 v9, v9, v226\n\t" \
  "v_mul_f32 v10, v10, v226\n\t" \
  "v_mul_f32 v11, v11, v226\n\t" \
  "v_mul_f32 v12, v12, v226\n\t" \
  "v_mul_f32 v13, v13, v226\n\t" \
  "ds_read_b64_tr_b16 a[220:221], v237 offset:40960\n\t" \
  "v_mul_f32 v14, v14, v226\n\t" \
  "s_waitcnt vmcnt(0) lgkmcnt(0)\n\t" \
  "buffer_store_dword v243, v254, s[16:19], s49 offen offset:0 sc1\n\t" \
  "ds_read_b64_tr_b16 a[222:223], v237 offset:45056\n\t" \
  "v_mul_f32 v15, v15, v226\n\t" \
  "ds_read_b64_tr_b16 a[128:129], v234 offset:49152\n\t" \
  "v_cvt_pk_bf16_f32 v44, v8, v9\n\t" \
  "buffer_store_dwordx4 v[40:43], v242, s[16:19], s98 offen offset:2048 sc1\n\t" \
  "v_add_f32 v221, v164, v165\n\t" \
  "v_rcp_f32 v220, v221\n\t" \
  "v_cvt_pk_bf16_f32 v45, v10, v11\n\t" \
  "v_cvt_pk_bf16_f32 v46, v12, v13\n\t" \
  "ds_read_b64_tr_b16 a[130:131], v234 offset:53248\n\t" \
  "v_cvt_pk_bf16_f32 v47, v14, v15\n\t" \
  "ds_read_b64_tr_b16 a[132:133], v235 offset:49152\n\t" \
  "s_add_u32 s96, s40, 1048576\n\t" \
  "ds_read_b128 a[240:243], v213 offset:32768\n\t" \
  "s_and_b32 s96, s96, 0x1c0000\n\t" \
  "buffer_store_dwordx4 v[44:47], v242, s[16:19], s98 offen offset:3072 sc1\n\t" \
  "ds_read_b128 a[244:247], v213 offset:33792\n\t" \
  "ds_read_b128 a[248:251], v213 offset:34816\n\t" \
  "ds_read_b64_tr_b16 a[134:135], v235 offset:53248\n\t" \
  "s_add_u32 s96, s96, s51\n\t" \
  "ds_read_b128 a[252:255], v213 offset:35840\n\t" \
  "s_xor_b32 s98, s45, 65536\n\t" \
  "ds_write_b128 v213, v[32:35] offset:32768\n\t" \
  "s_add_u32 m0, s38, 98304\n\t" \
  "v_accvgpr_read_b32 v64, a64\n\t" \
  "v_accvgpr_read_b32 v65, a65\n\t" \
  "v_accvgpr_read_b32 v66, a66\n\t" \
  "v_accvgpr_read_b32 v67, a67\n\t" \
  "v_accvgpr_read_b32 v68, a68\n\t" \
  "s_waitcnt vmcnt(0) lgkmcnt(0)\n\t" \
  "s_barrier\n\t" \
  "buffer_load_dwordx4 v240, s[24:27], s96 offen offset:0 lds\n\t" \
  "v_accvgpr_read_b32 v69, a69\n\t" \
  "v_accvgpr_read_b32 v70, a70\n\t" \
  "ds_read_b64_tr_b16 a[136:137], v234 offset:57344\n\t" \
  "ds_read_b128 v[128:131], v242 offset:16384\n\t" \
  "v_accvgpr_read_b32 v71, a71\n\t" \
  "ds_read_b128 v[132:135], v242 offset:17408\n\t" \
  "v_mul_f32 v64, v64, v220\n\t" \
  "ds_write_b128 v213, v[36:39] offset:33792\n\t" \
  "buffer_load_dwordx4 v241, s[24:27], s96 offen offset:1024 lds\n\t" \
  "v_mul_f32 v65, v65, v220\n\t" \
  "v_add_f32 v223, v166, v167\n\t" \
  "ds_read_b128 v[136:139], v242 offset:18432\n\t" \
  "v_rcp_f32 v222, v223\n\t" \
  "v_mul_f32 v66, v66, v220\n\t" \
  "buffer_load_dwordx4 v248, s[24:27], s96 offen offset:2048 lds\n\t" \
  "buffer_load_dwordx4 v249, s[24:27], s96 offen offset:3072 lds\n\t" \
  "ds_read_b128 v[140:143], v242 offset:19456\n\t" \
  "ds_read_b128 v[144:147], v242 offset:20480\n\t" \
  "v_mul_f32 v67, v67, v220\n\t" \
  "ds_read_b128 v[148:151], v242 offset:21504\n\t" \
  "v_mul_f32 v68, v68, v220\n\t" \
  "ds_write_b128 v213, v[40:43] offset:34816\n\t" \
  "ds_read_b64_tr_b16 a[138:139], v234 offset:61440\n\t" \
  "ds_read_b128 v[152:155], v242 offset:22528\n\t" \
  "v_mul_f32 v69, v69, v220\n\t" \
  "v_mul_f32 v70, v70, v220\n\t" \
  "ds_read_b64_tr_b16 a[140:141], v235 offset:57344\n\t" \
  "ds_read_b128 v[156:159], v242 offset:23552\n\t" \
  "v_mul_f32 v71, v71, v220\n\t" \
  "s_waitcnt vmcnt(0) lgkmcnt(0)\n\t" \
  "s_nop 1\n\t" \
  "s_branch .Lat_join\n\t" \
  ".Lsf_c00:\n\t" \
  "s_mov_b32 vcc_lo, 0\n\t" \
  "s_branch .Lsf_s0\n\t" \
  ".Lsf_c10:\n\t" \
  "s_mov_b32 vcc_lo, 1\n\t" \
  "s_branch .Lsf_s0\n\t" \
  ".Lsf_c20:\n\t" \
  "s_mov_b32 vcc_lo, 2\n\t" \
  "s_branch .Lsf_s0\n\t" \
  ".Lsf_c30:\n\t" \
  "s_mov_b32 vcc_lo, 3\n\t" \
  "s_branch .Lsf_s0\n\t" \
  ".Lsf_c40:\n\t" \
  "s_mov_b32 vcc_lo, 4\n\t" \
  "s_branch .Lsf_s0\n\t" \
  ".Lsf_c50:\n\t" \
  "s_mov_b32 vcc_lo, 5\n\t" \
  "s_branch .Lsf_s0\n\t" \
  ".Lsf_c60:\n\t" \
  "s_mov_b32 vcc_lo, 6\n\t" \
  "s_branch .Lsf_s0\n\t" \
  ".Lsf_c70:\n\t" \
  "s_mov_b32 vcc_lo, 7\n\t" \
  "s_branch .Lsf_s0\n\t" \
  ".Lsf_s0:\n\t" \
  "s_nop 7\n\t" \
  "s_nop 1\n\t" \
  "v_mov_b32 v193, v192\n\t" \
  "s_nop 1\n\t" \
  "v_permlane32_swap_b32 v192, v193\n\t" \
  "s_nop 1\n\t" \
  "v_max_f32 v192, v192, v193\n\t" \
  "s_nop 1\n\t" \
  "v_mov_b32 v193, v192\n\t" \
  "s_nop 1\n\t" \
  "v_permlane16_swap_b32 v192, v193\n\t" \
  "s_nop 1\n\t" \
  "v_max_f32 v192, v192, v193\n\t" \
  "v_max_f32 v192, 0, v192\n\t" \
  "v_sub_f32 v0, v0, v192\n\t" \
  "v_sub_f32 v1, v1, v192\n\t" \
  "v_sub_f32 v2, v2, v192\n\t" \
  "v_sub_f32 v3, v3, v192\n\t" \
  "v_sub_f32 v4, v4, v192\n\t" \
  "v_sub_f32 v5, v5, v192\n\t" \
  "v_sub_f32 v6, v6, v192\n\t" \
  "v_sub_f32 v7, v7, v192\n\t" \
  "v_sub_f32 v8, v8, v192\n\t" \
  "v_sub_f32 v9, v9, v192\n\t" \
  "v_sub_f32 v10, v10, v192\n\t" \
  "v_sub_f32 v11, v11, v192\n\t" \
  "v_sub_f32 v12, v12, v192\n\t" \
  "v_sub_f32 v13, v13, v192\n\t" \
  "v_sub_f32 v14, v14, v192\n\t" \
  "v_sub_f32 v15, v15, v192\n\t" \
  "v_sub_f32 v176, v176, v192\n\t" \
  "v_sub_f32 v177, v177, v192\n\t" \
  "v_sub_f32 v178, v178, v192\n\t" \
  "v_sub_f32 v179, v179, v192\n\t" \
  "v_exp_f32 v196, -v192\n\t" \
  "s_mov_b32 s99, 1\n\t" \
  "s_nop 1\n\t" \
  "s_cmp_eq_u32 vcc_lo, 0\n\t" \
  "s_cbranch_scc1 .Lsf_r00\n\t" \
  "s_cmp_eq_u32 vcc_lo, 1\n\t" \
  "s_cbranch_scc1 .Lsf_r10\n\t" \
  "s_cmp_eq_u32 vcc_lo, 2\n\t" \
  "s_cbranch_scc1 .Lsf_r20\n\t" \
  "s_cmp_eq_u32 vcc_lo, 3\n\t" \
  "s_cbranch_scc1 .Lsf_r30\n\t" \
  "s_cmp_eq_u32 vcc_lo, 4\n\t" \
  "s_cbranch_scc1 .Lsf_r40\n\t" \
  "s_cmp_eq_u32 vcc_lo, 5\n\t" \
  "s_cbranch_scc1 .Lsf_r50\n\t" \
  "s_cmp_eq_u32 vcc_lo, 6\n\t" \
  "s_cbranch_scc1 .Lsf_r60\n\t" \
  "s_cmp_eq_u32 vcc_lo, 7\n\t" \
  "s_cbranch_scc1 .Lsf_r70\n\t" \
  ".Lsf_c01:\n\t" \
  "s_mov_b32 vcc_lo, 0\n\t" \
  "s_branch .Lsf_s1\n\t" \
  ".Lsf_c11:\n\t" \
  "s_mov_b32 vcc_lo, 1\n\t" \
  "s_branch .Lsf_s1\n\t" \
  ".Lsf_c21:\n\t" \
  "s_mov_b32 vcc_lo, 2\n\t" \
  "s_branch .Lsf_s1\n\t" \
  ".Lsf_c31:\n\t" \
  "s_mov_b32 vcc_lo, 3\n\t" \
  "s_branch .Lsf_s1\n\t" \
  ".Lsf_c41:\n\t" \
  "s_mov_b32 vcc_lo, 4\n\t" \
  "s_branch .Lsf_s1\n\t" \
  ".Lsf_c51:\n\t" \
  "s_mov_b32 vcc_lo, 5\n\t" \
  "s_branch .Lsf_s1\n\t" \
  ".Lsf_c61:\n\t" \
  "s_mov_b32 vcc_lo, 6\n\t" \
  "s_branch .Lsf_s1\n\t" \
  ".Lsf_c71:\n\t" \
  "s_mov_b32 vcc_lo, 7\n\t" \
  "s_branch .Lsf_s1\n\t" \
  ".Lsf_s1:\n\t" \
  "s_nop 7\n\t" \
  "s_nop 1\n\t" \
  "v_mov_b32 v193, v192\n\t" \
  "s_nop 1\n\t" \
  "v_permlane32_swap_b32 v192, v193\n\t" \
  "s_nop 1\n\t" \
  "v_max_f32 v192, v192, v193\n\t" \
  "s_nop 1\n\t" \
  "v_mov_b32 v193, v192\n\t" \
  "s_nop 1\n\t" \
  "v_permlane16_swap_b32 v192, v193\n\t" \
  "s_nop 1\n\t" \
  "v_max_f32 v192, v192, v193\n\t" \
  "v_max_f32 v192, 0, v192\n\t" \
  "v_sub_f32 v32, v32, v192\n\t" \
  "v_sub_f32 v33, v33, v192\n\t" \
  "v_sub_f32 v34, v34, v192\n\t" \
  "v_sub_f32 v35, v35, v192\n\t" \
  "v_sub_f32 v36, v36, v192\n\t" \
  "v_sub_f32 v37, v37, v192\n\t" \
  "v_sub_f32 v38, v38, v192\n\t" \
  "v_sub_f32 v39, v39, v192\n\t" \
  "v_sub_f32 v40, v40, v192\n\t" \
  "v_sub_f32 v41, v41, v192\n\t" \
  "v_sub_f32 v42, v42, v192\n\t" \
  "v_sub_f32 v43, v43, v192\n\t" \
  "v_sub_f32 v44, v44, v192\n\t" \
  "v_sub_f32 v45, v45, v192\n\t" \
  "v_sub_f32 v46, v46, v192\n\t" \
  "v_sub_f32 v47, v47, v192\n\t" \
  "v_sub_f32 v180, v180, v192\n\t" \
  "v_sub_f32 v181, v181, v192\n\t" \
  "v_sub_f32 v182, v182, v192\n\t" \
  "v_sub_f32 v183, v183, v192\n\t" \
  "v_exp_f32 v197, -v192\n\t" \
  "s_mov_b32 s99, 1\n\t" \
  "s_nop 1\n\t" \
  "s_cmp_eq_u32 vcc_lo, 0\n\t" \
  "s_cbranch_scc1 .Lsf_r01\n\t" \
  "s_cmp_eq_u32 vcc_lo, 1\n\t" \
  "s_cbranch_scc1 .Lsf_r11\n\t" \
  "s_cmp_eq_u32 vcc_lo, 2\n\t" \
  "s_cbranch_scc1 .Lsf_r21\n\t" \
  "s_cmp_eq_u32 vcc_lo, 3\n\t" \
  "s_cbranch_scc1 .Lsf_r31\n\t" \
  "s_cmp_eq_u32 vcc_lo, 4\n\t" \
  "s_cbranch_scc1 .Lsf_r41\n\t" \
  "s_cmp_eq_u32 vcc_lo, 5\n\t" \
  "s_cbranch_scc1 .Lsf_r51\n\t" \
  "s_cmp_eq_u32 vcc_lo, 6\n\t" \
  "s_cbranch_scc1 .Lsf_r61\n\t" \
  "s_cmp_eq_u32 vcc_lo, 7\n\t" \
  "s_cbranch_scc1 .Lsf_r71\n\t" \
  ".Lsf_c02:\n\t" \
  "s_mov_b32 vcc_lo, 0\n\t" \
  "s_branch .Lsf_s2\n\t" \
  ".Lsf_c12:\n\t" \
  "s_mov_b32 vcc_lo, 1\n\t" \
  "s_branch .Lsf_s2\n\t" \
  ".Lsf_c22:\n\t" \
  "s_mov_b32 vcc_lo, 2\n\t" \
  "s_branch .Lsf_s2\n\t" \
  ".Lsf_c32:\n\t" \
  "s_mov_b32 vcc_lo, 3\n\t" \
  "s_branch .Lsf_s2\n\t" \
  ".Lsf_c42:\n\t" \
  "s_mov_b32 vcc_lo, 4\n\t" \
  "s_branch .Lsf_s2\n\t" \
  ".Lsf_c52:\n\t" \
  "s_mov_b32 vcc_lo, 5\n\t" \
  "s_branch .Lsf_s2\n\t" \
  ".Lsf_c62:\n\t" \
  "s_mov_b32 vcc_lo, 6\n\t" \
  "s_branch .Lsf_s2\n\t" \
  ".Lsf_c72:\n\t" \
  "s_mov_b32 vcc_lo, 7\n\t" \
  "s_branch .Lsf_s2\n\t" \
  ".Lsf_s2:\n\t" \
  "s_nop 7\n\t" \
  "s_nop 1\n\t" \
  "v_mov_b32 v193, v192\n\t" \
  "s_nop 1\n\t" \
  "v_permlane32_swap_b32 v192, v193\n\t" \
  "s_nop 1\n\t" \
  "v_max_f32 v192, v192, v193\n\t" \
  "s_nop 1\n\t" \
  "v_mov_b32 v193, v192\n\t" \
  "s_nop 1\n\t" \
  "v_permlane16_swap_b32 v192, v193\n\t" \
  "s_nop 1\n\t" \
  "v_max_f32 v192, v192, v193\n\t" \
  "v_max_f32 v192, 0, v192\n\t" \
  "v_sub_f32 v64, v64, v192\n\t" \
  "v_sub_f32 v65, v65, v192\n\t" \
  "v_sub_f32 v66, v66, v192\n\t" \
  "v_sub_f32 v67, v67, v192\n\t" \
  "v_sub_f32 v68, v68, v192\n\t" \
  "v_sub_f32 v69, v69, v192\n\t" \
  "v_sub_f32 v70, v70, v192\n\t" \
  "v_sub_f32 v71, v71, v192\n\t" \
  "v_sub_f32 v72, v72, v192\n\t" \
  "v_sub_f32 v73, v73, v192\n\t" \
  "v_sub_f32 v74, v74, v192\n\t" \
  "v_sub_f32 v75, v75, v192\n\t" \
  "v_sub_f32 v76, v76, v192\n\t" \
  "v_sub_f32 v77, v77, v192\n\t" \
  "v_sub_f32 v78, v78, v192\n\t" \
  "v_sub_f32 v79, v79, v192\n\t" \
  "v_sub_f32 v184, v184, v192\n\t" \
  "v_sub_f32 v185, v185, v192\n\t" \
  "v_sub_f32 v186, v186, v192\n\t" \
  "v_sub_f32 v187, v187, v192\n\t" \
  "v_exp_f32 v198, -v192\n\t" \
  "s_mov_b32 s99, 1\n\t" \
  "s_nop 1\n\t" \
  "s_cmp_eq_u32 vcc_lo, 0\n\t" \
  "s_cbranch_scc1 .Lsf_r02\n\t" \
  "s_cmp_eq_u32 vcc_lo, 1\n\t" \
  "s_cbranch_scc1 .Lsf_r12\n\t" \
  "s_cmp_eq_u32 vcc_lo, 2\n\t" \
  "s_cbranch_scc1 .Lsf_r22\n\t" \
  "s_cmp_eq_u32 vcc_lo, 3\n\t" \
  "s_cbranch_scc1 .Lsf_r32\n\t" \
  "s_cmp_eq_u32 vcc_lo, 4\n\t" \
  "s_cbranch_scc1 .Lsf_r42\n\t" \
  "s_cmp_eq_u32 vcc_lo, 5\n\t" \
  "s_cbranch_scc1 .Lsf_r52\n\t" \
  "s_cmp_eq_u32 vcc_lo, 6\n\t" \
  "s_cbranch_scc1 .Lsf_r62\n\t" \
  "s_cmp_eq_u32 vcc_lo, 7\n\t" \
  "s_cbranch_scc1 .Lsf_r72\n\t" \
  ".Lsf_c03:\n\t" \
  "s_mov_b32 vcc_lo, 0\n\t" \
  "s_branch .Lsf_s3\n\t" \
  ".Lsf_c13:\n\t" \
  "s_mov_b32 vcc_lo, 1\n\t" \
  "s_branch .Lsf_s3\n\t" \
  ".Lsf_c23:\n\t" \
  "s_mov_b32 vcc_lo, 2\n\t" \
  "s_branch .Lsf_s3\n\t" \
  ".Lsf_c33:\n\t" \
  "s_mov_b32 vcc_lo, 3\n\t" \
  "s_branch .Lsf_s3\n\t" \
  ".Lsf_c43:\n\t" \
  "s_mov_b32 vcc_lo, 4\n\t" \
  "s_branch .Lsf_s3\n\t" \
  ".Lsf_c53:\n\t" \
  "s_mov_b32 vcc_lo, 5\n\t" \
  "s_branch .Lsf_s3\n\t" \
  ".Lsf_c63:\n\t" \
  "s_mov_b32 vcc_lo, 6\n\t" \
  "s_branch .Lsf_s3\n\t" \
  ".Lsf_c73:\n\t" \
  "s_mov_b32 vcc_lo, 7\n\t" \
  "s_branch .Lsf_s3\n\t" \
  ".Lsf_s3:\n\t" \
  "s_nop 7\n\t" \
  "s_nop 1\n\t" \
  "v_mov_b32 v193, v192\n\t" \
  "s_nop 1\n\t" \
  "v_permlane32_swap_b32 v192, v193\n\t" \
  "s_nop 1\n\t" \
  "v_max_f32 v192, v192, v193\n\t" \
  "s_nop 1\n\t" \
  "v_mov_b32 v193, v192\n\t" \
  "s_nop 1\n\t" \
  "v_permlane16_swap_b32 v192, v193\n\t" \
  "s_nop 1\n\t" \
  "v_max_f32 v192, v192, v193\n\t" \
  "v_max_f32 v192, 0, v192\n\t" \
  "v_sub_f32 v96, v96, v192\n\t" \
  "v_sub_f32 v97, v97, v192\n\t" \
  "v_sub_f32 v98, v98, v192\n\t" \
  "v_sub_f32 v99, v99, v192\n\t" \
  "v_sub_f32 v100, v100, v192\n\t" \
  "v_sub_f32 v101, v101, v192\n\t" \
  "v_sub_f32 v102, v102, v192\n\t" \
  "v_sub_f32 v103, v103, v192\n\t" \
  "v_sub_f32 v104, v104, v192\n\t" \
  "v_sub_f32 v105, v105, v192\n\t" \
  "v_sub_f32 v106, v106, v192\n\t" \
  "v_sub_f32 v107, v107, v192\n\t" \
  "v_sub_f32 v108, v108, v192\n\t" \
  "v_sub_f32 v109, v109, v192\n\t" \
  "v_sub_f32 v110, v110, v192\n\t" \
  "v_sub_f32 v111, v111, v192\n\t" \
  "v_sub_f32 v188, v188, v192\n\t" \
  "v_sub_f32 v189, v189, v192\n\t" \
  "v_sub_f32 v190, v190, v192\n\t" \
  "v_sub_f32 v191, v191, v192\n\t" \
  "v_exp_f32 v199, -v192\n\t" \
  "s_mov_b32 s99, 1\n\t" \
  "s_nop 1\n\t" \
  "s_cmp_eq_u32 vcc_lo, 0\n\t" \
  "s_cbranch_scc1 .Lsf_r03\n\t" \
  "s_cmp_eq_u32 vcc_lo, 1\n\t" \
  "s_cbranch_scc1 .Lsf_r13\n\t" \
  "s_cmp_eq_u32 vcc_lo, 2\n\t" \
  "s_cbranch_scc1 .Lsf_r23\n\t" \
  "s_cmp_eq_u32 vcc_lo, 3\n\t" \
  "s_cbranch_scc1 .Lsf_r33\n\t" \
  "s_cmp_eq_u32 vcc_lo, 4\n\t" \
  "s_cbranch_scc1 .Lsf_r43\n\t" \
  "s_cmp_eq_u32 vcc_lo, 5\n\t" \
  "s_cbranch_scc1 .Lsf_r53\n\t" \
  "s_cmp_eq_u32 vcc_lo, 6\n\t" \
  "s_cbranch_scc1 .Lsf_r63\n\t" \
  "s_cmp_eq_u32 vcc_lo, 7\n\t" \
  "s_cbranch_scc1 .Lsf_r73\n\t" \
  ".Lsf_p0:\n\t" \
  "s_mov_b32 vcc_lo, 0\n\t" \
  "s_branch .Lsf_rs\n\t" \
  ".Lsf_p1:\n\t" \
  "s_mov_b32 vcc_lo, 1\n\t" \
  "s_branch .Lsf_rs\n\t" \
  ".Lsf_p2:\n\t" \
  "s_mov_b32 vcc_lo, 2\n\t" \
  "s_branch .Lsf_rs\n\t" \
  ".Lsf_p3:\n\t" \
  "s_mov_b32 vcc_lo, 3\n\t" \
  "s_branch .Lsf_rs\n\t" \
  ".Lsf_p4:\n\t" \
  "s_mov_b32 vcc_lo, 4\n\t" \
  "s_branch .Lsf_rs\n\t" \
  ".Lsf_p5:\n\t" \
  "s_mov_b32 vcc_lo, 5\n\t" \
  "s_branch .Lsf_rs\n\t" \
  ".Lsf_p6:\n\t" \
  "s_mov_b32 vcc_lo, 6\n\t" \
  "s_branch .Lsf_rs\n\t" \
  ".Lsf_p7:\n\t" \
  "s_mov_b32 vcc_lo, 7\n\t" \
  "s_branch .Lsf_rs\n\t" \
  ".Lsf_rs:\n\t" \
  "s_nop 7\n\t" \
  "s_nop 7\n\t" \
  "v_accvgpr_read_b32 v193, a0\n\t" \
  "v_mul_f32 v193, v196, v193\n\t" \
  "v_accvgpr_write_b32 a0, v193\n\t" \
  "v_accvgpr_read_b32 v193, a1\n\t" \
  "v_mul_f32 v193, v196, v193\n\t" \
  "v_accvgpr_write_b32 a1, v193\n\t" \
  "v_accvgpr_read_b32 v193, a2\n\t" \
  "v_mul_f32 v193, v196, v193\n\t" \
  "v_accvgpr_write_b32 a2, v193\n\t" \
  "v_accvgpr_read_b32 v193, a3\n\t" \
  "v_mul_f32 v193, v196, v193\n\t" \
  "v_accvgpr_write_b32 a3, v193\n\t" \
  "v_accvgpr_read_b32 v193, a4\n\t" \
  "v_mul_f32 v193, v196, v193\n\t" \
  "v_accvgpr_write_b32 a4, v193\n\t" \
  "v_accvgpr_read_b32 v193, a5\n\t" \
  "v_mul_f32 v193, v196, v193\n\t" \
  "v_accvgpr_write_b32 a5, v193\n\t" \
  "v_accvgpr_read_b32 v193, a6\n\t" \
  "v_mul_f32 v193, v196, v193\n\t" \
  "v_accvgpr_write_b32 a6, v193\n\t" \
  "v_accvgpr_read_b32 v193, a7\n\t" \
  "v_mul_f32 v193, v196, v193\n\t" \
  "v_accvgpr_write_b32 a7, v193\n\t" \
  "v_accvgpr_read_b32 v193, a8\n\t" \
  "v_mul_f32 v193, v196, v193\n\t" \
  "v_accvgpr_write_b32 a8, v193\n\t" \
  "v_accvgpr_read_b32 v193, a9\n\t" \
  "v_mul_f32 v193, v196, v193\n\t" \
  "v_accvgpr_write_b32 a9, v193\n\t" \
  "v_accvgpr_read_b32 v193, a10\n\t" \
  "v_mul_f32 v193, v196, v193\n\t" \
  "v_accvgpr_write_b32 a10, v193\n\t" \
  "v_accvgpr_read_b32 v193, a11\n\t" \
  "v_mul_f32 v193, v196, v193\n\t" \
  "v_accvgpr_write_b32 a11, v193\n\t" \
  "v_accvgpr_read_b32 v193, a12\n\t" \
  "v_mul_f32 v193, v196, v193\n\t" \
  "v_accvgpr_write_b32 a12, v193\n\t" \
  "v_accvgpr_read_b32 v193, a13\n\t" \
  "v_mul_f32 v193, v196, v193\n\t" \
  "v_accvgpr_write_b32 a13, v193\n\t" \
  "v_accvgpr_read_b32 v193, a14\n\t" \
  "v_mul_f32 v193, v196, v193\n\t" \
  "v_accvgpr_write_b32 a14, v193\n\t" \
  "v_accvgpr_read_b32 v193, a15\n\t" \
  "v_mul_f32 v193, v196, v193\n\t" \
  "v_accvgpr_write_b32 a15, v193\n\t" \
  "v_accvgpr_read_b32 v193, a16\n\t" \
  "v_mul_f32 v193, v196, v193\n\t" \
  "v_accvgpr_write_b32 a16, v193\n\t" \
  "v_accvgpr_read_b32 v193, a17\n\t" \
  "v_mul_f32 v193, v196, v193\n\t" \
  "v_accvgpr_write_b32 a17, v193\n\t" \
  "v_accvgpr_read_b32 v193, a18\n\t" \
  "v_mul_f32 v193, v196, v193\n\t" \
  "v_accvgpr_write_b32 a18, v193\n\t" \
  "v_accvgpr_read_b32 v193, a19\n\t" \
  "v_mul_f32 v193, v196, v193\n\t" \
  "v_accvgpr_write_b32 a19, v193\n\t" \
  "v_accvgpr_read_b32 v193, a20\n\t" \
  "v_mul_f32 v193, v196, v193\n\t" \
  "v_accvgpr_write_b32 a20, v193\n\t" \
  "v_accvgpr_read_b32 v193, a21\n\t" \
  "v_mul_f32 v193, v196, v193\n\t" \
  "v_accvgpr_write_b32 a21, v193\n\t" \
  "v_accvgpr_read_b32 v193, a22\n\t" \
  "v_mul_f32 v193, v196, v193\n\t" \
  "v_accvgpr_write_b32 a22, v193\n\t" \
  "v_accvgpr_read_b32 v193, a23\n\t" \
  "v_mul_f32 v193, v196, v193\n\t" \
  "v_accvgpr_write_b32 a23, v193\n\t" \
  "v_accvgpr_read_b32 v193, a24\n\t" \
  "v_mul_f32 v193, v196, v193\n\t" \
  "v_accvgpr_write_b32 a24, v193\n\t" \
  "v_accvgpr_read_b32 v193, a25\n\t" \
  "v_mul_f32 v193, v196, v193\n\t" \
  "v_accvgpr_write_b32 a25, v193\n\t" \
  "v_accvgpr_read_b32 v193, a26\n\t" \
  "v_mul_f32 v193, v196, v193\n\t" \
  "v_accvgpr_write_b32 a26, v193\n\t" \
  "v_accvgpr_read_b32 v193, a27\n\t" \
  "v_mul_f32 v193, v196, v193\n\t" \
  "v_accvgpr_write_b32 a27, v193\n\t" \
  "v_accvgpr_read_b32 v193, a28\n\t" \
  "v_mul_f32 v193, v196, v193\n\t" \
  "v_accvgpr_write_b32 a28, v193\n\t" \
  "v_accvgpr_read_b32 v193, a29\n\t" \
  "v_mul_f32 v193, v196, v193\n\t" \
  "v_accvgpr_write_b32 a29, v193\n\t" \
  "v_accvgpr_read_b32 v193, a30\n\t" \
  "v_mul_f32 v193, v196, v193\n\t" \
  "v_accvgpr_write_b32 a30, v193\n\t" \
  "v_accvgpr_read_b32 v193, a31\n\t" \
  "v_mul_f32 v193, v196, v193\n\t" \
  "v_accvgpr_write_b32 a31, v193\n\t" \
  "v_accvgpr_read_b32 v193, a32\n\t" \
  "v_mul_f32 v193, v197, v193\n\t" \
  "v_accvgpr_write_b32 a32, v193\n\t" \
  "v_accvgpr_read_b32 v193, a33\n\t" \
  "v_mul_f32 v193, v197, v193\n\t" \
  "v_accvgpr_write_b32 a33, v193\n\t" \
  "v_accvgpr_read_b32 v193, a34\n\t" \
  "v_mul_f32 v193, v197, v193\n\t" \
  "v_accvgpr_write_b32 a34, v193\n\t" \
  "v_accvgpr_read_b32 v193, a35\n\t" \
  "v_mul_f32 v193, v197, v193\n\t" \
  "v_accvgpr_write_b32 a35, v193\n\t" \
  "v_accvgpr_read_b32 v193, a36\n\t" \
  "v_mul_f32 v193, v197, v193\n\t" \
  "v_accvgpr_write_b32 a36, v193\n\t" \
  "v_accvgpr_read_b32 v193, a37\n\t" \
  "v_mul_f32 v193, v197, v193\n\t" \
  "v_accvgpr_write_b32 a37, v193\n\t" \
  "v_accvgpr_read_b32 v193, a38\n\t" \
  "v_mul_f32 v193, v197, v193\n\t" \
  "v_accvgpr_write_b32 a38, v193\n\t" \
  "v_accvgpr_read_b32 v193, a39\n\t" \
  "v_mul_f32 v193, v197, v193\n\t" \
  "v_accvgpr_write_b32 a39, v193\n\t" \
  "v_accvgpr_read_b32 v193, a40\n\t" \
  "v_mul_f32 v193, v197, v193\n\t" \
  "v_accvgpr_write_b32 a40, v193\n\t" \
  "v_accvgpr_read_b32 v193, a41\n\t" \
  "v_mul_f32 v193, v197, v193\n\t" \
  "v_accvgpr_write_b32 a41, v193\n\t" \
  "v_accvgpr_read_b32 v193, a42\n\t" \
  "v_mul_f32 v193, v197, v193\n\t" \
  "v_accvgpr_write_b32 a42, v193\n\t" \
  "v_accvgpr_read_b32 v193, a43\n\t" \
  "v_mul_f32 v193, v197, v193\n\t" \
  "v_accvgpr_write_b32 a43, v193\n\t" \
  "v_accvgpr_read_b32 v193, a44\n\t" \
  "v_mul_f32 v193, v197, v193\n\t" \
  "v_accvgpr_write_b32 a44, v193\n\t" \
  "v_accvgpr_read_b32 v193, a45\n\t" \
  "v_mul_f32 v193, v197, v193\n\t" \
  "v_accvgpr_write_b32 a45, v193\n\t" \
  "v_accvgpr_read_b32 v193, a46\n\t" \
  "v_mul_f32 v193, v197, v193\n\t" \
  "v_accvgpr_write_b32 a46, v193\n\t" \
  "v_accvgpr_read_b32 v193, a47\n\t" \
  "v_mul_f32 v193, v197, v193\n\t" \
  "v_accvgpr_write_b32 a47, v193\n\t" \
  "v_accvgpr_read_b32 v193, a48\n\t" \
  "v_mul_f32 v193, v197, v193\n\t" \
  "v_accvgpr_write_b32 a48, v193\n\t" \
  "v_accvgpr_read_b32 v193, a49\n\t" \
  "v_mul_f32 v193, v197, v193\n\t" \
  "v_accvgpr_write_b32 a49, v193\n\t" \
  "v_accvgpr_read_b32 v193, a50\n\t" \
  "v_mul_f32 v193, v197, v193\n\t" \
  "v_accvgpr_write_b32 a50, v193\n\t" \
  "v_accvgpr_read_b32 v193, a51\n\t" \
  "v_mul_f32 v193, v197, v193\n\t" \
  "v_accvgpr_write_b32 a51, v193\n\t" \
  "v_accvgpr_read_b32 v193, a52\n\t" \
  "v_mul_f32 v193, v197, v193\n\t" \
  "v_accvgpr_write_b32 a52, v193\n\t" \
  "v_accvgpr_read_b32 v193, a53\n\t" \
  "v_mul_f32 v193, v197, v193\n\t" \
  "v_accvgpr_write_b32 a53, v193\n\t" \
  "v_accvgpr_read_b32 v193, a54\n\t" \
  "v_mul_f32 v193, v197, v193\n\t" \
  "v_accvgpr_write_b32 a54, v193\n\t" \
  "v_accvgpr_read_b32 v193, a55\n\t" \
  "v_mul_f32 v193, v197, v193\n\t" \
  "v_accvgpr_write_b32 a55, v193\n\t" \
  "v_accvgpr_read_b32 v193, a56\n\t" \
  "v_mul_f32 v193, v197, v193\n\t" \
  "v_accvgpr_write_b32 a56, v193\n\t" \
  "v_accvgpr_read_b32 v193, a57\n\t" \
  "v_mul_f32 v193, v197, v193\n\t" \
  "v_accvgpr_write_b32 a57, v193\n\t" \
  "v_accvgpr_read_b32 v193, a58\n\t" \
  "v_mul_f32 v193, v197, v193\n\t" \
  "v_accvgpr_write_b32 a58, v193\n\t" \
  "v_accvgpr_read_b32 v193, a59\n\t" \
  "v_mul_f32 v193, v197, v193\n\t" \
  "v_accvgpr_write_b32 a59, v193\n\t" \
  "v_accvgpr_read_b32 v193, a60\n\t" \
  "v_mul_f32 v193, v197, v193\n\t" \
  "v_accvgpr_write_b32 a60, v193\n\t" \
  "v_accvgpr_read_b32 v193, a61\n\t" \
  "v_mul_f32 v193, v197, v193\n\t" \
  "v_accvgpr_write_b32 a61, v193\n\t" \
  "v_accvgpr_read_b32 v193, a62\n\t" \
  "v_mul_f32 v193, v197, v193\n\t" \
  "v_accvgpr_write_b32 a62, v193\n\t" \
  "v_accvgpr_read_b32 v193, a63\n\t" \
  "v_mul_f32 v193, v197, v193\n\t" \
  "v_accvgpr_write_b32 a63, v193\n\t" \
  "v_accvgpr_read_b32 v193, a64\n\t" \
  "v_mul_f32 v193, v198, v193\n\t" \
  "v_accvgpr_write_b32 a64, v193\n\t" \
  "v_accvgpr_read_b32 v193, a65\n\t" \
  "v_mul_f32 v193, v198, v193\n\t" \
  "v_accvgpr_write_b32 a65, v193\n\t" \
  "v_accvgpr_read_b32 v193, a66\n\t" \
  "v_mul_f32 v193, v198, v193\n\t" \
  "v_accvgpr_write_b32 a66, v193\n\t" \
  "v_accvgpr_read_b32 v193, a67\n\t" \
  "v_mul_f32 v193, v198, v193\n\t" \
  "v_accvgpr_write_b32 a67, v193\n\t" \
  "v_accvgpr_read_b32 v193, a68\n\t" \
  "v_mul_f32 v193, v198, v193\n\t" \
  "v_accvgpr_write_b32 a68, v193\n\t" \
  "v_accvgpr_read_b32 v193, a69\n\t" \
  "v_mul_f32 v193, v198, v193\n\t" \
  "v_accvgpr_write_b32 a69, v193\n\t" \
  "v_accvgpr_read_b32 v193, a70\n\t" \
  "v_mul_f32 v193, v198, v193\n\t" \
  "v_accvgpr_write_b32 a70, v193\n\t" \
  "v_accvgpr_read_b32 v193, a71\n\t" \
  "v_mul_f32 v193, v198, v193\n\t" \
  "v_accvgpr_write_b32 a71, v193\n\t" \
  "v_accvgpr_read_b32 v193, a72\n\t" \
  "v_mul_f32 v193, v198, v193\n\t" \
  "v_accvgpr_write_b32 a72, v193\n\t" \
  "v_accvgpr_read_b32 v193, a73\n\t" \
  "v_mul_f32 v193, v198, v193\n\t" \
  "v_accvgpr_write_b32 a73, v193\n\t" \
  "v_accvgpr_read_b32 v193, a74\n\t" \
  "v_mul_f32 v193, v198, v193\n\t" \
  "v_accvgpr_write_b32 a74, v193\n\t" \
  "v_accvgpr_read_b32 v193, a75\n\t" \
  "v_mul_f32 v193, v198, v193\n\t" \
  "v_accvgpr_write_b32 a75, v193\n\t" \
  "v_accvgpr_read_b32 v193, a76\n\t" \
  "v_mul_f32 v193, v198, v193\n\t" \
  "v_accvgpr_write_b32 a76, v193\n\t" \
  "v_accvgpr_read_b32 v193, a77\n\t" \
  "v_mul_f32 v193, v198, v193\n\t" \
  "v_accvgpr_write_b32 a77, v193\n\t" \
  "v_accvgpr_read_b32 v193, a78\n\t" \
  "v_mul_f32 v193, v198, v193\n\t" \
  "v_accvgpr_write_b32 a78, v193\n\t" \
  "v_accvgpr_read_b32 v193, a79\n\t" \
  "v_mul_f32 v193, v198, v193\n\t" \
  "v_accvgpr_write_b32 a79, v193\n\t" \
  "v_accvgpr_read_b32 v193, a80\n\t" \
  "v_mul_f32 v193, v198, v193\n\t" \
  "v_accvgpr_write_b32 a80, v193\n\t" \
  "v_accvgpr_read_b32 v193, a81\n\t" \
  "v_mul_f32 v193, v198, v193\n\t" \
  "v_accvgpr_write_b32 a81, v193\n\t" \
  "v_accvgpr_read_b32 v193, a82\n\t" \
  "v_mul_f32 v193, v198, v193\n\t" \
  "v_accvgpr_write_b32 a82, v193\n\t" \
  "v_accvgpr_read_b32 v193, a83\n\t" \
  "v_mul_f32 v193, v198, v193\n\t" \
  "v_accvgpr_write_b32 a83, v193\n\t" \
  "v_accvgpr_read_b32 v193, a84\n\t" \
  "v_mul_f32 v193, v198, v193\n\t" \
  "v_accvgpr_write_b32 a84, v193\n\t" \
  "v_accvgpr_read_b32 v193, a85\n\t" \
  "v_mul_f32 v193, v198, v193\n\t" \
  "v_accvgpr_write_b32 a85, v193\n\t" \
  "v_accvgpr_read_b32 v193, a86\n\t" \
  "v_mul_f32 v193, v198, v193\n\t" \
  "v_accvgpr_write_b32 a86, v193\n\t" \
  "v_accvgpr_read_b32 v193, a87\n\t" \
  "v_mul_f32 v193, v198, v193\n\t" \
  "v_accvgpr_write_b32 a87, v193\n\t" \
  "v_accvgpr_read_b32 v193, a88\n\t" \
  "v_mul_f32 v193, v198, v193\n\t" \
  "v_accvgpr_write_b32 a88, v193\n\t" \
  "v_accvgpr_read_b32 v193, a89\n\t" \
  "v_mul_f32 v193, v198, v193\n\t" \
  "v_accvgpr_write_b32 a89, v193\n\t" \
  "v_accvgpr_read_b32 v193, a90\n\t" \
  "v_mul_f32 v193, v198, v193\n\t" \
  "v_accvgpr_write_b32 a90, v193\n\t" \
  "v_accvgpr_read_b32 v193, a91\n\t" \
  "v_mul_f32 v193, v198, v193\n\t" \
  "v_accvgpr_write_b32 a91, v193\n\t" \
  "v_accvgpr_read_b32 v193, a92\n\t" \
  "v_mul_f32 v193, v198, v193\n\t" \
  "v_accvgpr_write_b32 a92, v193\n\t" \
  "v_accvgpr_read_b32 v193, a93\n\t" \
  "v_mul_f32 v193, v198, v193\n\t" \
  "v_accvgpr_write_b32 a93, v193\n\t" \
  "v_accvgpr_read_b32 v193, a94\n\t" \
  "v_mul_f32 v193, v198, v193\n\t" \
  "v_accvgpr_write_b32 a94, v193\n\t" \
  "v_accvgpr_read_b32 v193, a95\n\t" \
  "v_mul_f32 v193, v198, v193\n\t" \
  "v_accvgpr_write_b32 a95, v193\n\t" \
  "v_accvgpr_read_b32 v193, a96\n\t" \
  "v_mul_f32 v193, v199, v193\n\t" \
  "v_accvgpr_write_b32 a96, v193\n\t" \
  "v_accvgpr_read_b32 v193, a97\n\t" \
  "v_mul_f32 v193, v199, v193\n\t" \
  "v_accvgpr_write_b32 a97, v193\n\t" \
  "v_accvgpr_read_b32 v193, a98\n\t" \
  "v_mul_f32 v193, v199, v193\n\t" \
  "v_accvgpr_write_b32 a98, v193\n\t" \
  "v_accvgpr_read_b32 v193, a99\n\t" \
  "v_mul_f32 v193, v199, v193\n\t" \
  "v_accvgpr_write_b32 a99, v193\n\t" \
  "v_accvgpr_read_b32 v193, a100\n\t" \
  "v_mul_f32 v193, v199, v193\n\t" \
  "v_accvgpr_write_b32 a100, v193\n\t" \
  "v_accvgpr_read_b32 v193, a101\n\t" \
  "v_mul_f32 v193, v199, v193\n\t" \
  "v_accvgpr_write_b32 a101, v193\n\t" \
  "v_accvgpr_read_b32 v193, a102\n\t" \
  "v_mul_f32 v193, v199, v193\n\t" \
  "v_accvgpr_write_b32 a102, v193\n\t" \
  "v_accvgpr_read_b32 v193, a103\n\t" \
  "v_mul_f32 v193, v199, v193\n\t" \
  "v_accvgpr_write_b32 a103, v193\n\t" \
  "v_accvgpr_read_b32 v193, a104\n\t" \
  "v_mul_f32 v193, v199, v193\n\t" \
  "v_accvgpr_write_b32 a104, v193\n\t" \
  "v_accvgpr_read_b32 v193, a105\n\t" \
  "v_mul_f32 v193, v199, v193\n\t" \
  "v_accvgpr_write_b32 a105, v193\n\t" \
  "v_accvgpr_read_b32 v193, a106\n\t" \
  "v_mul_f32 v193, v199, v193\n\t" \
  "v_accvgpr_write_b32 a106, v193\n\t" \
  "v_accvgpr_read_b32 v193, a107\n\t" \
  "v_mul_f32 v193, v199, v193\n\t" \
  "v_accvgpr_write_b32 a107, v193\n\t" \
  "v_accvgpr_read_b32 v193, a108\n\t" \
  "v_mul_f32 v193, v199, v193\n\t" \
  "v_accvgpr_write_b32 a108, v193\n\t" \
  "v_accvgpr_read_b32 v193, a109\n\t" \
  "v_mul_f32 v193, v199, v193\n\t" \
  "v_accvgpr_write_b32 a109, v193\n\t" \
  "v_accvgpr_read_b32 v193, a110\n\t" \
  "v_mul_f32 v193, v199, v193\n\t" \
  "v_accvgpr_write_b32 a110, v193\n\t" \
  "v_accvgpr_read_b32 v193, a111\n\t" \
  "v_mul_f32 v193, v199, v193\n\t" \
  "v_accvgpr_write_b32 a111, v193\n\t" \
  "v_accvgpr_read_b32 v193, a112\n\t" \
  "v_mul_f32 v193, v199, v193\n\t" \
  "v_accvgpr_write_b32 a112, v193\n\t" \
  "v_accvgpr_read_b32 v193, a113\n\t" \
  "v_mul_f32 v193, v199, v193\n\t" \
  "v_accvgpr_write_b32 a113, v193\n\t" \
  "v_accvgpr_read_b32 v193, a114\n\t" \
  "v_mul_f32 v193, v199, v193\n\t" \
  "v_accvgpr_write_b32 a114, v193\n\t" \
  "v_accvgpr_read_b32 v193, a115\n\t" \
  "v_mul_f32 v193, v199, v193\n\t" \
  "v_accvgpr_write_b32 a115, v193\n\t" \
  "v_accvgpr_read_b32 v193, a116\n\t" \
  "v_mul_f32 v193, v199, v193\n\t" \
  "v_accvgpr_write_b32 a116, v193\n\t" \
  "v_accvgpr_read_b32 v193, a117\n\t" \
  "v_mul_f32 v193, v199, v193\n\t" \
  "v_accvgpr_write_b32 a117, v193\n\t" \
  "v_accvgpr_read_b32 v193, a118\n\t" \
  "v_mul_f32 v193, v199, v193\n\t" \
  "v_accvgpr_write_b32 a118, v193\n\t" \
  "v_accvgpr_read_b32 v193, a119\n\t" \
  "v_mul_f32 v193, v199, v193\n\t" \
  "v_accvgpr_write_b32 a119, v193\n\t" \
  "v_accvgpr_read_b32 v193, a120\n\t" \
  "v_mul_f32 v193, v199, v193\n\t" \
  "v_accvgpr_write_b32 a120, v193\n\t" \
  "v_accvgpr_read_b32 v193, a121\n\t" \
  "v_mul_f32 v193, v199, v193\n\t" \
  "v_accvgpr_write_b32 a121, v193\n\t" \
  "v_accvgpr_read_b32 v193, a122\n\t" \
  "v_mul_f32 v193, v199, v193\n\t" \
  "v_accvgpr_write_b32 a122, v193\n\t" \
  "v_accvgpr_read_b32 v193, a123\n\t" \
  "v_mul_f32 v193, v199, v193\n\t" \
  "v_accvgpr_write_b32 a123, v193\n\t" \
  "v_accvgpr_read_b32 v193, a124\n\t" \
  "v_mul_f32 v193, v199, v193\n\t" \
  "v_accvgpr_write_b32 a124, v193\n\t" \
  "v_accvgpr_read_b32 v193, a125\n\t" \
  "v_mul_f32 v193, v199, v193\n\t" \
  "v_accvgpr_write_b32 a125, v193\n\t" \
  "v_accvgpr_read_b32 v193, a126\n\t" \
  "v_mul_f32 v193, v199, v193\n\t" \
  "v_accvgpr_write_b32 a126, v193\n\t" \
  "v_accvgpr_read_b32 v193, a127\n\t" \
  "v_mul_f32 v193, v199, v193\n\t" \
  "v_accvgpr_write_b32 a127, v193\n\t" \
  "v_mul_f32 v160, v196, v160\n\t" \
  "v_mul_f32 v161, v196, v161\n\t" \
  "v_mov_b32 v196, 1.0\n\t" \
  "v_mul_f32 v162, v197, v162\n\t" \
  "v_mul_f32 v163, v197, v163\n\t" \
  "v_mov_b32 v197, 1.0\n\t" \
  "v_mul_f32 v164, v198, v164\n\t" \
  "v_mul_f32 v165, v198, v165\n\t" \
  "v_mov_b32 v198, 1.0\n\t" \
  "v_mul_f32 v166, v199, v166\n\t" \
  "v_mul_f32 v167, v199, v167\n\t" \
  "v_mov_b32 v199, 1.0\n\t" \
  "s_mov_b32 s99, 0\n\t" \
  "s_nop 1\n\t" \
  "s_cmp_eq_u32 vcc_lo, 0\n\t" \
  "s_cbranch_scc1 .Lsf_q0\n\t" \
  "s_cmp_eq_u32 vcc_lo, 1\n\t" \
  "s_cbranch_scc1 .Lsf_q1\n\t" \
  "s_cmp_eq_u32 vcc_lo, 2\n\t" \
  "s_cbranch_scc1 .Lsf_q2\n\t" \
  "s_cmp_eq_u32 vcc_lo, 3\n\t" \
  "s_cbranch_scc1 .Lsf_q3\n\t" \
  "s_cmp_eq_u32 vcc_lo, 4\n\t" \
  "s_cbranch_scc1 .Lsf_q4\n\t" \
  "s_cmp_eq_u32 vcc_lo, 5\n\t" \
  "s_cbranch_scc1 .Lsf_q5\n\t" \
  "s_cmp_eq_u32 vcc_lo, 6\n\t" \
  "s_cbranch_scc1 .Lsf_q6\n\t" \
  "s_cmp_eq_u32 vcc_lo, 7\n\t" \
  "s_cbranch_scc1 .Lsf_q7\n\t" \
  "s_endpgm\n\t" \
  ".Lsf_v:\n\t" \
  "s_mov_b32 s98, 0\n\t" \
  ".Lsf_v1:\n\t" \
  "s_sleep 4\n\t" \
  "buffer_load_dword v209, v251, s[16:19], 0 offen sc1\n\t" \
  "s_waitcnt vmcnt(0)\n\t" \
  "v_and_b32 v250, 0xbfffbfff, v209\n\t" \
  "v_cmp_ne_u32_e64 s[84:85], s36, v250\n\t" \
  "s_cmp_lg_u64 s[84:85], 0\n\t" \
  "s_cbranch_scc0 .Lsf_w\n\t" \
  "s_add_u32 s98, s98, 1\n\t" \
  "s_cmp_lt_u32 s98, " FQ_CAP "\n\t" \
  "s_cbranch_scc1 .Lsf_v1\n\t" \
  "v_mov_b32 v250, s36\n\t" \
  "buffer_store_dword v250, off, s[16:19], 0 offset:256 sc1\n\t" \
  "s_branch .Lsf_w\n\t" \
  ".subsection 0\n\t" \
  ".Lop_end:\n\t" \
  ""
#define OP_GA0 "=&{v[160:163]}"
#define OP_GA1 "=&{v[164:167]}"
#define OP_CW "=&{s82}"
#define OP_BB "=&{s83}"
#define AO_CLOB "v128", "v129", "v130", "v131", "v132", "v133", "v134", "v135", "v136", "v137", "v138", "v139", "v140", "v141", "v142", "v143", "v144", "v145", "v146", "v147", "v148", "v149", "v150", "v151", "v152", "v153", "v154", "v155", "v156", "v157", "v158", "v159", "v168", "v169", "v170", "v171", "v172", "v173", "v174", "v175", "v208", "v209", "v210", "v211", "v212", "v213", "v214", "v215", "v216", "v217", "v218", "v219", "v234", "v235", "v236", "v237", "v238", "v239", "v242", "v243", "v248", "v249", "v250", "v251", "v252", "v254", "s76", "s77", "s78", "s79", "s80", "s81", "s84", "s85", "s86", "s87", "s88", "s89", "s90", "s91", "s92", "s93", "s94", "s95", "s96", "s97", "s98", "s99", "vcc", "scc", "memory"
#define OP_DRD "s_waitcnt vmcnt(5)\n\t" "ds_read2st64_b64 v[32:35], %1 offset0:64 offset1:65\n\t" "ds_read2st64_b64 v[36:39], %1 offset0:66 offset1:67\n\t" "ds_read2st64_b64 v[40:43], %1 offset0:68 offset1:69\n\t" "ds_read2st64_b64 v[44:47], %1 offset0:70 offset1:71\n\t" "ds_read2st64_b64 v[48:51], %1 offset0:96 offset1:97\n\t" "ds_read2st64_b64 v[52:55], %1 offset0:98 offset1:99\n\t" "ds_read2st64_b64 v[56:59], %1 offset0:100 offset1:101\n\t" "ds_read2st64_b64 v[60:63], %1 offset0:102 offset1:103\n\t" "s_waitcnt lgkmcnt(0)"
#define ST_ASM "s_nop 1\n\t" "v_mfma_f32_16x16x32_bf16 v[80:83], a[176:179], v[160:163], 0\n\t" "v_mfma_f32_16x16x32_bf16 v[80:83], a[180:183], v[164:167], v[80:83]\n\t" "v_mfma_f32_16x16x32_bf16 v[80:83], a[184:187], v[168:171], v[80:83]\n\t" "v_mfma_f32_16x16x32_bf16 v[80:83], a[188:191], v[172:175], v[80:83]\n\t" "v_dot2_f32_bf16 v64, v160, v160, 0\n\t" "v_dot2_f32_bf16 v65, v164, v164, 0\n\t" "v_dot2_f32_bf16 v66, v168, v168, 0\n\t" "v_dot2_f32_bf16 v67, v172, v172, 0\n\t" "v_dot2_f32_bf16 v64, v161, v161, v64\n\t" "v_dot2_f32_bf16 v65, v165, v165, v65\n\t" "v_dot2_f32_bf16 v66, v169, v169, v66\n\t" "v_dot2_f32_bf16 v67, v173, v173, v67\n\t" "v_dot2_f32_bf16 v64, v162, v162, v64\n\t" "v_dot2_f32_bf16 v65, v166, v166, v65\n\t" "v_dot2_f32_bf16 v66, v170, v170, v66\n\t" "v_dot2_f32_bf16 v67, v174, v174, v67\n\t" "v_dot2_f32_bf16 v64, v163, v163, v64\n\t" "v_dot2_f32_bf16 v65, v167, v167, v65\n\t" "v_dot2_f32_bf16 v66, v171, v171, v66\n\t" "v_dot2_f32_bf16 v67, v175, v175, v67\n\t" "s_nop 2\n\t"
static_assert(W_MODB == 2228224 && W_VOTE == 8192);
#define AO_T0
static_assert(W_EPOCH == 2490368 && W_GKEY == 65536 && W_CANDH == 262144 && W_STAT == 524288 && W_YG == 1048576 && W_CANDX == 1966080);
#define MAIN_CLOB "v1", "v2", "v3", "v4", "v5", "v6", "v7", "v8", "v9", "v10", "v11", "v12", "v13", "v14", "v15", "v16", "v17", "v18", "v19", "v20", "v21", "v22", "v23", "v24", "v25", "v26", "v27", "v28", "v29", "v30", "v31", "v32", "v33", "v34", "v35", "v36", "v37", "v38", "v39", "v40", "v41", "v42", "v43", "v44", "v45", "v46", "v47", "v48", "v49", "v50", "v51", "v52", "v53", "v54", "v55", "v56", "v57", "v58", "v59", "v60", "v61", "v62", "v63", "v64", "v65", "v66", "v67", "v68", "v69", "v70", "v71", "v72", "v73", "v74", "v75", "v76", "v77", "v78", "v79", "v80", "v81", "v82", "v83", "v84", "v85", "v86", "v87", "v88", "v89", "v90", "v91", "v92", "v93", "v94", "v95", "v96", "v97", "v98", "v99", "v100", "v101", "v102", "v103", "v104", "v105", "v106", "v107", "v108", "v109", "v110", "v111", "v112", "v113", "v114", "v115", "v116", "v117", "v118", "v119", "v120", "v121", "v122", "v123", "v124", "v125", "v126", "v127", "v128", "v129", "v130", "v131", "v132", "v133", "v134", "v135", "v136", "v137", "v138", "v139", "v140", "v141", "v142", "v143", "v144", "v145", "v146", "v147", "v148", "v149", "v150", "v151", "v152", "v153", "v154", "v155", "v156", "v157", "v158", "v159", "v160", "v161", "v162", "v163", "v164", "v165", "v166", "v167", "v168", "v169", "v170", "v171", "v172", "v173", "v174", "v175", "v176", "v177", "v178", "v179", "v180", "v181", "v182", "v183", "v184", "v185", "v186", "v187", "v188", "v189", "v190", "v191", "v192", "v193", "v194", "v195", "v196", "v197", "v198", "v199", "v200", "v201", "v202", "v203", "v204", "v205", "v206", "v207", "v208", "v209", "v210", "v211", "v212", "v213", "v214", "v215", "v216", "v217", "v218", "v219", "v220", "v221", "v222", "v223", "v224", "v225", "v226", "v227", "v228", "v229", "v230", "v231", "v232", "v233", "v234", "v235", "v236", "v237", "v238", "v239", "v240", "v241", "v242", "v243", "v244", "v245", "v246", "v247", "v248", "v249", "v250", "v251", "v252", "v253", "v254", "s4", "s5", "s6", "s7", "s8", "s9", "s10", "s11", "s12", "s13", "s14", "s15", "s16", "s17", "s18", "s19", "s20", "s21", "s22", "s23", "s24", "s25", "s26", "s27", "s28", "s29", "s30", "s31", "s32", "s33", "s34", "s35", "s36", "s37", "s38", "s39", "s40", "s41", "s42", "s43", "s44", "s45", "s46", "s47", "s48", "s49", "s50", "s51", "s52", "s53", "s54", "s55", "s57", "s58", "s59", "s60", "s61", "s62", "s63", "s64", "s65", "s66", "s67", "s68", "s69", "s70", "s71", "s72", "s73", "s74", "s75", "s76", "s77", "s78", "s79", "s80", "s81", "s82", "s83", "s84", "s85", "s86", "s87", "s88", "s89", "s90", "s91", "s92", "s93", "s94", "s95", "s96", "s97", "s98", "s99", "vcc", "scc", "memory"
#define MAIN_STV
__global__ __launch_bounds__(256, 1) __attribute__((aligned(4096))) void k_fused(Args a) {
  Ctx k;
  STAMP_DECL;
  argp_t ap = (argp_t)__builtin_amdgcn_kernarg_segment_ptr();
  unsigned tid = threadIdx.x; k.bid = blockIdx.x;
  asm volatile(FQ_ASM AT_ASM : MAIN_STV "+{v0}"(tid), "+{s[0:1]}"(ap), "+{s2}"(k.bid), "=&{s3}"(k.wid), "=&{s56}"(k.t0) :: MAIN_CLOB, ACLOB_LO, ACLOB_HI);
  asm volatile(HOP_DEFS);
}

extern "C" void kernel_launch(void* const* d_in, const int* in_sizes, int n_in, void* d_out, int out_size, void* d_ws, size_t ws_size,
                              hipStream_t stream) {
  auto D = [](const void* p, unsigned bytes) { Desc d; d.p = p; d.bytes = bytes; d.w3 = SRD3; return d; };
  const size_t win = (size_t)(SKV - WIN) * DH * 2;
  Args a;
  a.x = D(d_in[0], S * DM * 2); a.w_mod = D(d_in[11], DM * 6 * DM * 2); a.c = D(d_in[10], DM * 2); a.ws = D(d_ws, W_END);
  a.wq = D(d_in[3], DM * QD * 2); a.wo = D(d_in[4], QD * DM * 2); a.wd = D(d_in[9], 0); a.w_r = D(d_in[6], DM * 2);
  a.kc = D((const char*)d_in[1] + win, WIN * DH * 2); a.vc = D((const char*)d_in[2] + win, WIN * DH * 2); a.wg = D(d_in[7], DM * FF * 2); a.wu = D(d_in[8], DM * FF * 2);
  a.out = D(d_out, S * DM * 2);
  (void)hipFuncSetAttribute((const void*)k_fused, hipFuncAttributeMaxDynamicSharedMemorySize, 163840);
  k_fused<<<256, 256, 163840, stream>>>(a);
}
```

```cpp
#include <hip/hip_runtime.h>
#include <hip/hip_bf16.h>
#include <stdint.h>
#include <utility>

typedef unsigned short u16;
using bf16x8 = __attribute__((ext_vector_type(8))) short;
using s16x4  = __attribute__((ext_vector_type(4))) short;
using f32x16 = __attribute__((ext_vector_type(16))) float;
using u32x16 = __attribute__((ext_vector_type(16))) unsigned;
using f32x4  = __attribute__((ext_vector_type(4))) float;
using f32x2  = __attribute__((ext_vector_type(2))) float;
using u32x4  = __attribute__((ext_vector_type(4))) unsigned;
using u32x2  = __attribute__((ext_vector_type(2))) unsigned;
using u32x8 = __attribute__((ext_vector_type(8))) unsigned;
using f32x8 = __attribute__((ext_vector_type(8))) float;
using f32x32 = __attribute__((ext_vector_type(32))) float;
using u32x32 = __attribute__((ext_vector_type(32))) unsigned;
typedef __bf16 bf16x2_t __attribute__((ext_vector_type(2)));
#define DI __device__ __forceinline__
#define NTMASK 0
#define NT_SEL(bit) ((NTMASK >> (bit)) & 1)
#if NT_SEL(0)
#define NTX " nt"
#else
#define NTX ""
#endif
#if NT_SEL(1)
#define NTM " nt"
#else
#define NTM ""
#endif
#if NT_SEL(2)
#define NTQ " nt"
#else
#define NTQ ""
#endif
#if NT_SEL(3)
#define NTO " nt"
#else
#define NTO ""
#endif
#if NT_SEL(4)
#define NTG " nt"
#else
#define NTG ""
#endif
#if NT_SEL(5)
#define NTD " nt"
#else
#define NTD ""
#endif
#if NT_SEL(6)
#define NTT " nt"
#else
#define NTT ""
#endif
#if NT_SEL(7)
#define NTF " nt"
#else
#define NTF ""
#endif

constexpr int S = 2048, DM = 1024, HQ = 32, HKV = 4, DH = 128, SKV = 2048, WIN = 512, FF = 3584;
constexpr int QD = HQ * DH;
constexpr float QSCALE = 0.12751743082f;

DI float bf2f(u16 v) { return __uint_as_float(((unsigned)v) << 16); }
DI u16 f2bf(float x) { unsigned u = __float_as_uint(x); u += 0x7fffu + ((u >> 16) & 1u); return (u16)(u >> 16); }
DI unsigned cvtpk(float lo, float hi) { f32x2 v = {lo, hi}; bf16x2_t b = __builtin_convertvector(v, bf16x2_t); return __builtin_bit_cast(unsigned, b); }
DI float lo16f(unsigned w) { return __uint_as_float(w << 16); }
DI float hi16f(unsigned w) { return __uint_as_float(w & 0xffff0000u); }
DI float rbf(float x) { return lo16f(cvtpk(x, x)); }
DI f32x2 up2(unsigned w) { return f32x2{lo16f(w), hi16f(w)}; }
DI float dot2(unsigned a, unsigned b, float c) { return __builtin_amdgcn_fdot2_f32_bf16(__builtin_bit_cast(bf16x2_t, a), __builtin_bit_cast(bf16x2_t, b), c, false); }
DI unsigned plo(unsigned a, unsigned b) { return __builtin_amdgcn_perm(b, a, 0x05040100u); }
DI unsigned phi(unsigned a, unsigned b) { return __builtin_amdgcn_perm(b, a, 0x07060302u); }
DI unsigned rfl(unsigned x) { return __builtin_amdgcn_readfirstlane(x); }
template <int CTRL> DI float dpp_f(float v) { return __int_as_float(__builtin_amdgcn_update_dpp(0, __float_as_int(v), CTRL, 0xf, 0xf, true)); }
template <int CTRL> DI unsigned dpp_u(unsigned v) { return (unsigned)__builtin_amdgcn_update_dpp(0, (int)v, CTRL, 0xf, 0xf, true); }
DI float fold16(float a, float b) { auto r = __builtin_amdgcn_permlane16_swap(__float_as_uint(a), __float_as_uint(b), false, false); return __uint_as_float(r[0]) + __uint_as_float(r[1]); }
DI float fold32(float a, float b) { auto r = __builtin_amdgcn_permlane32_swap(__float_as_uint(a), __float_as_uint(b), false, false); return __uint_as_float(r[0]) + __uint_as_float(r[1]); }
DI float sum16(float v) { return fold16(v, v); }
DI float sum32(float v) { return fold32(v, v); }
DI float sum_row16(float v) { v += dpp_f<0xB1>(v); v += dpp_f<0x4E>(v); v += dpp_f<0x141>(v); v += dpp_f<0x140>(v); return v; }
DI float wave_sum(float v) { return sum32(sum16(sum_row16(v))); }
DI unsigned wave_maxu(unsigned v) {
  v = max(v, dpp_u<0xB1>(v)); v = max(v, dpp_u<0x4E>(v)); v = max(v, dpp_u<0x141>(v)); v = max(v, dpp_u<0x140>(v));
  { auto r = __builtin_amdgcn_permlane16_swap(v, v, false, false); v = max(r[0], r[1]); }
  { auto r = __builtin_amdgcn_permlane32_swap(v, v, false, false); v = max(r[0], r[1]); }
  return v;
}
#define GAP() __builtin_amdgcn_sched_barrier(0)
template <int N> using iseq = std::make_integer_sequence<int, N>;
template <int... Is> using ilist = std::integer_sequence<int, Is...>;
typedef __amdgpu_buffer_rsrc_t srd_t;
#define SRD3 0x00020000
DI srd_t mk_srd(const void* p, unsigned bytes) { return __builtin_amdgcn_make_buffer_rsrc((void*)p, (short)0, (int)bytes, SRD3); }
DI unsigned lane_id() { unsigned l; asm volatile("v_mbcnt_lo_u32_b32 %0, -1, 0\n\tv_mbcnt_hi_u32_b32 %0, -1, %0" : "=v"(l)); return l; }
struct Desc { const void* p; unsigned bytes, w3; };
struct Args { Desc x, w_mod, c, ws, wq, wo, wd, w_r, kc, vc, wg, wu, out; };
typedef const __attribute__((address_space(4))) Args* argp_t;
DI argp_t args() { argp_t p = (argp_t)__builtin_amdgcn_kernarg_segment_ptr(); asm volatile("" : "+s"(p)); return p; }
#define BARRIER_LGKM() do { asm volatile("s_waitcnt lgkmcnt(0)\n\ts_barrier" ::: "memory"); } while (0)

constexpr unsigned W_EPOCH = 0x260000, W_TMO = 256;
constexpr unsigned W_FLAGH = 0x261000;
constexpr unsigned W_VOTE = 0x2000, W_PROBE = 0x4000;
constexpr unsigned W_GKEY = 0x10000;
constexpr unsigned W_FLAGR = 0x8000;
constexpr unsigned W_FLAGO = 0x20000;
constexpr unsigned W_CANDH = 0x40000;
constexpr unsigned W_STAT = 0x80000;
constexpr unsigned W_YG = 0x100000;
constexpr unsigned W_CANDX = 0x1E0000;
constexpr unsigned W_ST1 = 0x200000;
constexpr unsigned W_MODA = 0x210000;
constexpr unsigned W_MODB = 0x220000;
constexpr unsigned W_SIL = 0x240000;
constexpr unsigned W_HT = 4u << 20;
constexpr unsigned W_OT = 8u << 20;
constexpr unsigned W_XCH = 24u << 20;
constexpr unsigned W_END = 40u << 20;
constexpr unsigned TAGM = 0x40004000u, TAGC = 0x9E3739B9u;
#define SPINCAP 262144
constexpr unsigned kSpinCap = SPINCAP;
#define SL0 1
#define SL1 1
#define SL2 1
#define SL3 1
template <int SL = 1> DI bool spin_fail(unsigned& spins, srd_t ws, unsigned tag) {
  __builtin_amdgcn_s_sleep(SL);
  if (++spins > kSpinCap) { __builtin_amdgcn_raw_buffer_store_b32(tag, ws, W_TMO, 0, 16); return true; }
  return (spins & 255u) == 0u && rfl(__builtin_amdgcn_raw_buffer_load_b32(ws, W_TMO, 0, 16)) == tag;
}
DI u32x4 ld16(srd_t ws, unsigned off) { return __builtin_amdgcn_raw_buffer_load_b128(ws, off, 0, 16); }
DI u32x2 ld8(srd_t ws, unsigned off) { return __builtin_amdgcn_raw_buffer_load_b64(ws, off, 0, 16); }
DI void st16(srd_t ws, unsigned off, u32x4 v) { __builtin_amdgcn_raw_buffer_store_b128(v, ws, off, 0, 16); }
DI void st8(srd_t ws, unsigned off, u32x2 v) { __builtin_amdgcn_raw_buffer_store_b64(v, ws, off, 0, 16); }
DI void st4(srd_t ws, unsigned off, unsigned v) { __builtin_amdgcn_raw_buffer_store_b32(v, ws, off, 0, 16); }
DI void wait_flags(srd_t ws, unsigned off, unsigned tag) {
  const unsigned o = off + (lane_id() & 31u) * 4u;
  for (unsigned spins = 0;;) {
    const unsigned f = __builtin_amdgcn_raw_buffer_load_b32(ws, o, 0, 16);
    if (__all(f == tag)) break;
    if (spin_fail(spins, ws, tag)) break;
  }
  asm volatile("" ::: "memory");
}
template <int OFF, bool DEV = false> DI u32x4 ald16(srd_t s, unsigned vo, unsigned so) {
  u32x4 r;
  if constexpr (DEV) asm volatile("buffer_load_dwordx4 %0, %1, %2, %3 offen offset:%4 sc1" : "=v"(r) : "v"(vo), "s"(s), "s"(so), "i"(OFF) : "memory");
  else               asm volatile("buffer_load_dwordx4 %0, %1, %2, %3 offen offset:%4" NTF : "=v"(r) : "v"(vo), "s"(s), "s"(so), "i"(OFF) : "memory");
  return r;
}
template <int OFF> DI u32x2 ald8(srd_t s, unsigned vo, unsigned so) {
  u32x2 r; asm volatile("buffer_load_dwordx2 %0, %1, %2, %3 offen offset:%4" : "=v"(r) : "v"(vo), "s"(s), "s"(so), "i"(OFF) : "memory"); return r;
}
DI u32x2 ald8d(srd_t s, unsigned vo) { u32x2 r; asm volatile("buffer_load_dwordx2 %0, %1, %2, 0 offen sc1" : "=v"(r) : "v"(vo), "s"(s) : "memory"); return r; }
template <int OFF, bool DEV = false> DI unsigned ald4(srd_t s, unsigned vo, unsigned so) {
  unsigned r;
  if constexpr (DEV) asm volatile("buffer_load_dword %0, %1, %2, %3 offen offset:%4 sc1" : "=v"(r) : "v"(vo), "s"(s), "s"(so), "i"(OFF) : "memory");
  else               asm volatile("buffer_load_dword %0, %1, %2, %3 offen offset:%4" : "=v"(r) : "v"(vo), "s"(s), "s"(so), "i"(OFF) : "memory");
  return r;
}
DI void touch(unsigned& d, const void* base, unsigned vo) { asm volatile("global_load_dword %0, %1, %2" : "+v"(d) : "v"(vo), "s"(base) : "memory"); }
DI void touchq(unsigned& d, const void* base, unsigned vo) { asm volatile("global_load_dword %0, %1, %2" NTQ : "+v"(d) : "v"(vo), "s"(base) : "memory"); }
template <class T> DI T lds_ld(unsigned a) { return *reinterpret_cast<const __attribute__((address_space(3))) T*>(a); }
template <class T> DI void lds_st(unsigned a, T v) { *reinterpret_cast<__attribute__((address_space(3))) T*>(a) = v; }
DI float silu_bf(float v) { return rbf(v * __builtin_amdgcn_rcpf(1.f + __expf(-v))); }
struct Ctx { static constexpr unsigned ldsb = 0; unsigned t0, pm, bid, wid, xcd, j, kvh, hl, rblk, tag, cs, nb; srd_t ws; };
#define TAIL2 1
DI unsigned ffn_j(const Ctx& k) { return TAIL2 ? (k.j < 24 ? k.j : 24 + (k.j & 3)) : min(k.j, 27u); }
DI bool ffn_idle(const Ctx& k) { return TAIL2 ? (k.j >> 2) == 6 + (k.xcd & 1) : k.j >= 28; }
constexpr unsigned UNIT = 16384;
#define STAMPA(I) do { } while (0)
#define STAMPO(I) do { } while (0)
#define STAMP_DECL
#define STAMP(I) do { } while (0)
#define STAMP_FLUSH() do { } while (0)
#define STAMP_ARG
#define STAMP_PASS
#define ACLOB_LO "a0","a1","a2","a3","a4","a5","a6","a7","a8","a9","a10","a11","a12","a13","a14","a15","a16","a17","a18","a19","a20","a21","a22","a23","a24","a25","a26","a27","a28","a29","a30","a31","a32","a33","a34","a35","a36","a37","a38","a39","a40","a41","a42","a43","a44","a45","a46","a47","a48","a49","a50","a51","a52","a53","a54","a55","a56","a57","a58","a59","a60","a61","a62","a63","a64","a65","a66","a67","a68","a69","a70","a71","a72","a73","a74","a75","a76","a77","a78","a79","a80","a81","a82","a83","a84","a85","a86","a87","a88","a89","a90","a91","a92","a93","a94","a95","a96","a97","a98","a99","a100","a101","a102","a103","a104","a105","a106","a107","a108","a109","a110","a111","a112","a113","a114","a115","a116","a117","a118","a119","a120","a121","a122","a123","a124","a125","a126","a127"
#define ACLOB_HI "a128","a129","a130","a131","a132","a133","a134","a135","a136","a137","a138","a139","a140","a141","a142","a143","a144","a145","a146","a147","a148","a149","a150","a151","a152","a153","a154","a155","a156","a157","a158","a159","a160","a161","a162","a163","a164","a165","a166","a167","a168","a169","a170","a171","a172","a173","a174","a175","a176","a177","a178","a179","a180","a181","a182","a183","a184","a185","a186","a187","a188","a189","a190","a191","a192","a193","a194","a195","a196","a197","a198","a199","a200","a201","a202","a203","a204","a205","a206","a207","a208","a209","a210","a211","a212","a213","a214","a215","a216","a217","a218","a219","a220","a221","a222","a223","a224","a225","a226","a227","a228","a229","a230","a231","a232","a233","a234","a235","a236","a237","a238","a239","a240","a241","a242","a243","a244","a245","a246","a247","a248","a249","a250","a251","a252","a253","a254","a255"
#define HOP_DEFS ".set .Lhc00, .Lhs0\n\t.set .Lhs0n, .Lhs4\n\t.set .Lhs4n, .Lhs8\n\t.set .Lhs8n, .Lhr0\n\t.set .Lhc01, .Lhs1\n\t.set .Lhs1n, .Lhs5\n\t.set .Lhs5n, .Lhs9\n\t.set .Lhs9n, .Lhr0\n\t.set .Lhc02, .Lhs2\n\t.set .Lhs2n, .Lhs6\n\t.set .Lhs6n, .Lhs10\n\t.set .Lhs10n, .Lhr0\n\t.set .Lhc03, .Lhs3\n\t.set .Lhs3n, .Lhs7\n\t.set .Lhs7n, .Lhr0\n\t.set .Lhc10, .Lhs11\n\t.set .Lhs11n, .Lhs15\n\t.set .Lhs15n, .Lhr1\n\t.set .Lhc11, .Lhs12\n\t.set .Lhs12n, .Lhs16\n\t.set .Lhs16n, .Lhr1\n\t.set .Lhc12, .Lhs13\n\t.set .Lhs13n, .Lhr1\n\t.set .Lhc13, .Lhs14\n\t.set .Lhs14n, .Lhr1"
#define HOP_PAGE(n) asm volatile("s_branch .Lht" #n "\n\t.p2align 12\n.Lht" #n ":\n\ts_cbranch_execz .Lht" #n "n")
#define PAGE_ALIGN() asm volatile("s_branch .Lpa%=\n\t.p2align 12\n.Lpa%=:" ::: "memory")
#define HOP_FRONT() asm volatile("s_branch .Lhf\n\t.p2align 12\n.Lhf:\n\ts_cbranch_execz .Lhfn")
#define HOP_GO(g, w) asm volatile("s_mov_b64 exec, 0\n\ts_cmp_eq_u32 %0, 0\n\ts_cbranch_scc1 .Lhc" #g "0\n\ts_cmp_eq_u32 %0, 1\n\ts_cbranch_scc1 .Lhc" #g "1\n\ts_cmp_eq_u32 %0, 2\n\ts_cbranch_scc1 .Lhc" #g "2\n\ts_branch .Lhc" #g "3\n.Lhr" #g ":\n\ts_mov_b64 exec, -1" :: "s"(w) : "scc", "memory")
#define FQ_STR2(x) #x
#define FQ_STR(x) FQ_STR2(x)
#define FQ_CAP FQ_STR(SPINCAP)
DI bool tags2(const u32x4& a, unsigned tag) { return (a[1] == tag) & (a[3] == tag); }
DI void bcast4(float v, float (&o)[4]) {
  auto a = __builtin_amdgcn_permlane32_swap(__float_as_uint(v), __float_as_uint(v), false, false);
  auto b = __builtin_amdgcn_permlane16_swap(a[0], a[0], false, false);
  auto c = __builtin_amdgcn_permlane16_swap(a[1], a[1], false, false);
  o[0] = __uint_as_float(b[0]); o[1] = __uint_as_float(b[1]); o[2] = __uint_as_float(c[0]); o[3] = __uint_as_float(c[1]);
}
#define FSTP(x)
#define FSTS(x)
#define FSTA(x)
#define FQ_ASM \
  FSTA("v_mov_b32 v255, 0\n\t") \
  FSTA("s_memrealtime s[94:95]\n\t") \
  FSTA("s_memtime s[92:93]\n\t") \
  FSTA("s_waitcnt lgkmcnt(0)\n\t") \
  FSTA("s_or_b32 s94, s94, 1\n\t") \
  FSTA("v_writelane_b32 v255, s94, 0\n\t") \
  FSTA("v_writelane_b32 v255, s92, 1\n\t") \
  "s_mov_b32 s37, 1\n\t" \
  ".Lfr_again:\n\t" \
  "s_load_dwordx16 s[4:19], s[0:1], 0x0\n\t" \
  "s_load_dwordx16 s[20:35], s[0:1], 0x40\n\t" \
  "s_load_dwordx16 s[60:75], s[0:1], 0x80\n\t" \
  "v_readfirstlane_b32 s3, v0\n\t" \
  "v_and_b32 v254, 63, v0\n\t" \
  "s_lshr_b32 s3, s3, 6\n\t" \
  "s_lshr_b32 s39, s2, 3\n\t" \
  "s_lshl_b32 s54, s3, 5\n\t" \
  "s_add_u32 s54, s54, s39\n\t" \
  "s_lshl_b32 s54, s54, 7\n\t" \
  "v_lshl_add_u32 v48, v254, 14, s54\n\t" \
  "v_min_u32 v48, .Lfz_z-.Lfz_a-4, v48\n\t" \
  "s_getpc_b64 s[96:97]\n\t" \
  ".Lfz_a:\n\t" \
  "global_load_dword v246, v48, s[96:97]\n\t" \
  "s_and_b32 s40, s2, 3\n\t" \
  "v_lshrrev_b32 v52, 2, v0\n\t" \
  "v_lshlrev_b32 v244, 4, v254\n\t" \
  "v_lshlrev_b32 v53, 3, v52\n\t" \
  "v_mul_u32_u24 v52, 49152, v52\n\t" \
  "v_and_or_b32 v52, v244, 48, v52\n\t" \
  "s_lshl_b32 s44, s40, 9\n\t" \
  "s_mul_i32 s45, s40, 3145728\n\t" \
  "s_bfe_u32 s52, s2, 0x10002\n\t" \
  "s_lshl_b32 s52, s52, 5\n\t" \
  "s_add_u32 s52, s52, s39\n\t" \
  "s_lshl_b32 s54, s52, 6\n\t" \
  "s_add_u32 s45, s45, s54\n\t" \
  "s_add_u32 s96, s45, 12288\n\t" \
  "s_add_u32 s97, s45, 24576\n\t" \
  "s_add_u32 s99, s45, 36864\n\t" \
  "v_mov_b32 v1, 2490368\n\t" \
  "s_and_b32 s98, s2, 4\n\t" \
  "s_lshr_b32 s54, s2, 6\n\t" \
  "s_or_b32 s98, s98, s54\n\t" \
  "s_lshl_b32 s41, s98, 19\n\t" \
  "s_lshl_b32 s38, s3, 12\n\t" \
  "s_lshl_b32 s77, s3, 13\n\t" \
  "s_and_b32 s54, s2, 0x38\n\t" \
  "s_lshl_b32 s54, s54, 11\n\t" \
  "s_lshl_b32 s55, s40, 17\n\t" \
  "s_or_b32 s46, s41, s38\n\t" \
  "s_or_b32 s46, s46, s54\n\t" \
  "s_or_b32 s46, s46, s55\n\t" \
  "s_xor_b32 s87, s55, 393216\n\t" \
  "v_lshrrev_b32 v245, 4, v254\n\t" \
  "v_lshrrev_b32 v49, 3, v254\n\t" \
  "v_and_b32 v50, 7, v254\n\t" \
  "v_xor_b32 v51, v50, v245\n\t" \
  "v_lshlrev_b32 v51, 4, v51\n\t" \
  "v_lshl_add_u32 v212, v49, 11, v51\n\t" \
  "v_xor_b32 v213, 64, v212\n\t" \
  "v_add_u32 v213, 15360, v213\n\t" \
  "s_bfe_u32 s42, s2, 0x20004\n\t" \
  "s_lshl_b32 s54, s3, 15\n\t" \
  "s_or_b32 s47, s41, s54\n\t" \
  "s_or_b32 s47, s47, s44\n\t" \
  "s_lshl_b32 s54, s42, 7\n\t" \
  "s_or_b32 s47, s47, s54\n\t" \
  "s_and_b32 s43, s2, 8\n\t" \
  "s_lshl_b32 s54, s43, 15\n\t" \
  "s_or_b32 s57, s47, s87\n\t" \
  "s_xor_b32 s57, s57, s54\n\t" \
  "s_xor_b32 s58, s57, 131072\n\t" \
  "s_lshl_b32 s54, s43, 9\n\t" \
  "s_add_u32 s90, s77, s54\n\t" \
  "s_lshl_b32 s78, s40, 21\n\t" \
  "s_lshl_b32 s50, s39, 14\n\t" \
  "s_lshl2_add_u32 s50, s44, s50\n\t" \
  "s_mul_i32 s54, s3, 0x1080\n\t" \
  "s_and_b32 s54, s54, 0x2080\n\t" \
  "s_or_b32 s50, s50, s54\n\t" \
  "v_lshl_add_u32 v54, v50, 8, s50\n\t" \
  "v_and_b32 v51, 3, v49\n\t" \
  "v_lshl_add_u32 v51, v51, 19, v54\n\t" \
  "s_mov_b32 m0, s90\n\t" \
  "s_waitcnt lgkmcnt(0)\n\t" \
  "buffer_load_dwordx2 v[2:3], v53, s[12:15], s44 offen\n\t" \
  "buffer_load_dwordx4 v[4:7], v52, s[8:11], s45 offen nt\n\t" \
  "buffer_load_dwordx4 v[8:11], v52, s[8:11], s96 offen nt\n\t" \
  "buffer_load_dwordx4 v[12:15], v52, s[8:11], s97 offen nt\n\t" \
  "buffer_load_dwordx4 v[16:19], v52, s[8:11], s99 offen nt\n\t" \
  "s_add_u32 s16, s16, 0x1fffff\n\t" \
  "s_addc_u32 s17, s17, 0\n\t" \
  "s_and_b32 s16, s16, 0xffe00000\n\t" \
  "buffer_load_dword v1, v1, s[16:19], 0 offen sc1\n\t" \
  "s_mov_b32 s96, s46\n\t" \
  "v_mov_b32 v253, v0\n\t" \
  "v_and_b32 v247, 15, v254\n\t" \
  "s_bfe_u32 s43, s2, 0x10003\n\t" \
  "v_and_b32 v48, 3, v254\n\t" \
  "v_lshl_add_u32 v48, v48, 2, v245\n\t" \
  "s_lshl_b32 s54, s3, 8\n\t" \
  "s_add_u32 s55, s54, 147456\n\t" \
  "v_lshl_add_u32 v55, v48, 3, s55\n\t" \
  "v_lshlrev_b32 v56, 3, v247\n\t" \
  "v_add_u32 v56, 147456, v56\n\t" \
  "v_and_b32 v48, 0xf0, v0\n\t" \
  "v_lshl_or_b32 v57, v48, 24, v244\n\t" \
  "s_lshl_b32 s52, s52, 8\n\t" \
  "s_lshl_b32 s55, s40, 14\n\t" \
  "s_add_u32 s52, s52, s55\n\t" \
  "s_add_u32 s52, s52, 2162688\n\t" \
  "v_and_b32 v210, 31, v254\n\t" \
  "v_lshrrev_b32 v48, 5, v254\n\t" \
  "v_lshlrev_b32 v58, 4, v48\n\t" \
  "v_lshl_or_b32 v58, v210, 26, v58\n\t" \
  "s_lshr_b32 s46, s46, 7\n\t" \
  "s_add_u32 s46, s46, 2097152\n\t" \
  "v_cvt_f32_u32 v251, v48\n\t" \
  "v_lshlrev_b32 v248, 4, v210\n\t" \
  "v_lshl_or_b32 v248, v48, 13, v248\n\t" \
  "v_lshlrev_b32 v210, 2, v210\n\t" \
  "v_lshlrev_b32 v249, 4, v247\n\t" \
  "s_lshl_b32 s55, s3, 17\n\t" \
  "s_lshl_b32 s49, s43, 18\n\t" \
  "s_xor_b32 s55, s55, s49\n\t" \
  "buffer_load_dwordx4 v[32:35], v244, s[4:7], s96 offen offset:0\n\t" \
  "buffer_load_dwordx4 v[36:39], v244, s[4:7], s96 offen offset:1024\n\t" \
  "buffer_load_dwordx4 v[40:43], v244, s[4:7], s96 offen offset:2048\n\t" \
  "buffer_load_dwordx4 v[44:47], v244, s[4:7], s96 offen offset:3072\n\t" \
  "s_and_b32 s49, s2, 0x38\n\t" \
  "s_lshl2_add_u32 s55, s44, s55\n\t" \
  "s_lshl_b32 s49, s49, 5\n\t" \
  "s_add_u32 s55, s55, s49\n\t" \
  "v_lshlrev_b32 v48, 5, v245\n\t" \
  "v_xor_b32 v48, v48, v249\n\t" \
  "v_lshl_add_u32 v48, v245, 13, v48\n\t" \
  "v_add_u32 v200, s55, v48\n\t" \
  "v_xor_b32 v48, 0x80, v200\n\t" \
  "v_add_u32 v201, 31744, v200\n\t" \
  "v_add_u32 v202, 63488, v48\n\t" \
  "v_add_u32 v203, 95232, v48\n\t" \
  "s_add_u32 s76, s38, 32768\n\t" \
  "s_lshl_b32 s49, s42, 19\n\t" \
  "s_or_b32 s49, s49, s78\n\t" \
  "s_xor_b32 s80, s49, 1048576\n\t" \
  "s_add_u32 s55, s42, 1\n\t" \
  "s_and_b32 s55, s55, 3\n\t" \
  "s_xor_b32 s55, s55, s42\n\t" \
  "s_lshl_b32 s79, s55, 19\n\t" \
  "s_xor_b32 s79, s79, s49\n\t" \
  "s_xor_b32 s81, s79, 1048576\n\t" \
  "s_lshl_b32 s48, s55, 7\n\t" \
  "s_xor_b32 s48, s48, s47\n\t" \
  "s_xor_b32 s88, s47, 256\n\t" \
  "s_xor_b32 s89, s48, 256\n\t" \
  "v_lshl_add_u32 v48, s43, 2, v245\n\t" \
  "v_bfe_u32 v50, v254, 1, 3\n\t" \
  "v_xor_b32 v48, v48, v50\n\t" \
  "v_lshlrev_b32 v48, 4, v48\n\t" \
  "v_lshl_add_u32 v48, v247, 7, v48\n\t" \
  "v_add_u32 v204, s77, v48\n\t" \
  "v_xor_b32 v205, 64, v204\n\t" \
  "v_add_u32 v206, 98304, v204\n\t" \
  "v_add_u32 v207, 98304, v205\n\t" \
  "v_lshlrev_b32 v208, 5, v245\n\t" \
  "v_add_u32 v208, 148480, v208\n\t" \
  "v_lshlrev_b32 v50, 3, v254\n\t" \
  "s_lshl_b32 s55, s43, 7\n\t" \
  "s_or_b32 s55, s55, 256\n\t" \
  "v_xor_b32 v250, s55, v50\n\t" \
  "s_lshl1_add_u32 s55, s54, 148480\n\t" \
  "v_add_u32 v250, s55, v250\n\t" \
  "v_lshl_add_u32 v214, v245, 10, v249\n\t" \
  "v_lshl_add_u32 v214, s43, 12, v214\n\t" \
  "s_lshr_b32 s55, s2, 2\n\t" \
  "s_xor_b32 s55, s55, s3\n\t" \
  "s_and_b32 s55, s55, 1\n\t" \
  "s_lshl1_add_u32 s55, s3, s55\n\t" \
  "s_lshl_b32 s82, s55, 13\n\t" \
  "s_lshr_b32 s55, s2, 6\n\t" \
  "s_lshl_b32 s55, s55, 20\n\t" \
  "s_add_u32 s82, s82, s55\n\t" \
  "s_add_u32 s82, s82, 4194304\n\t" \
  "s_lshr_b32 s86, s49, 3\n\t" \
  "s_add_u32 s86, s86, s82\n\t" \
  "s_lshr_b32 s55, s87, 9\n\t" \
  "s_add_u32 s86, s86, s55\n\t" \
  "s_lshl2_add_u32 s84, s98, s3\n\t" \
  "s_lshl_b32 s84, s84, 7\n\t" \
  "s_add_u32 s84, s84, 2494464\n\t" \
  "s_bfe_u32 s55, s2, 0x30003\n\t" \
  "s_lshl3_add_u32 s55, s40, s55\n\t" \
  "s_lshl2_add_u32 s85, s55, s84\n\t" \
  "s_lshl_b32 s55, s43, 13\n\t" \
  "s_sub_u32 s83, 4096, s55\n\t" \
  "v_and_b32 v50, 0xf8, v50\n\t" \
  "v_bfe_u32 v48, v254, 2, 2\n\t" \
  "v_lshl_add_u32 v50, v48, 8, v50\n\t" \
  "v_lshl_add_u32 v50, v245, 11, v50\n\t" \
  "v_add_u32 v184, 32768, v50\n\t" \
  "v_xor_b32 v185, 32, v184\n\t" \
  "v_xor_b32 v186, 64, v184\n\t" \
  "v_xor_b32 v187, 96, v184\n\t" \
  "v_xor_b32 v188, 128, v184\n\t" \
  "v_xor_b32 v189, 160, v184\n\t" \
  "v_xor_b32 v190, 192, v184\n\t" \
  "v_xor_b32 v191, 224, v184\n\t" \
  "s_lshl_b32 s51, s98, 12\n\t" \
  "s_add_u32 s51, s51, s54\n\t" \
  "s_add_u32 s51, s51, 2097152\n\t" \
  "s_lshr_b32 s55, s87, 7\n\t" \
  "s_add_u32 s51, s51, s55\n\t" \
  "s_add_u32 s55, s42, s3\n\t" \
  "s_and_b32 s55, s55, 3\n\t" \
  "s_lshl2_add_u32 s55, s40, s55\n\t" \
  "s_lshl_b32 s55, s55, 9\n\t" \
  "s_add_u32 s53, s55, 2162688\n\t" \
  "v_min_u32 v49, 2, v49\n\t" \
  "s_bfe_u32 s55, s2, 0x10002\n\t" \
  "s_lshl2_add_u32 s55, s40, s55\n\t" \
  "s_add_u32 s55, s55, 4\n\t" \
  "v_lshl_add_u32 v49, v49, 1, s55\n\t" \
  "v_lshl_add_u32 v215, v49, 19, v54\n\t" \
  "v_add_u32 v216, 3145728, v215\n\t" \
  "v_and_b32 v215, 0x7fffff, v215\n\t" \
  "v_and_b32 v216, 0x7fffff, v216\n\t" \
  "s_mov_b32 s98, 0xffff0000\n\t" \
  "s_mov_b32 s45, 0x05040100\n\t" \
  "s_mov_b32 s50, 0x07060302\n\t" \
  "s_lshr_b32 s44, s2, 6\n\t" \
  "s_lshl_b32 s44, s44, 9\n\t" \
  "v_lshlrev_b32 v53, 1, v0\n\t" \
  "s_waitcnt vmcnt(9)\n\t" \
  "v_lshlrev_b32 v20, 16, v2\n\t" \
  "v_and_b32 v21, s98, v2\n\t" \
  "v_lshlrev_b32 v22, 16, v3\n\t" \
  "v_and_b32 v23, s98, v3\n\t" \
  "v_mul_f32 v24, 0xbfb8aa3b, v20\n\t" \
  "v_mul_f32 v25, 0xbfb8aa3b, v21\n\t" \
  "v_mul_f32 v26, 0xbfb8aa3b, v22\n\t" \
  "v_mul_f32 v27, 0xbfb8aa3b, v23\n\t" \
  "v_exp_f32 v24, v24\n\t" \
  "v_exp_f32 v25, v25\n\t" \
  "v_exp_f32 v26, v26\n\t" \
  "v_exp_f32 v27, v27\n\t" \
  "v_add_f32 v24, 1.0, v24\n\t" \
  "v_add_f32 v25, 1.0, v25\n\t" \
  "v_add_f32 v26, 1.0, v26\n\t" \
  "v_add_f32 v27, 1.0, v27\n\t" \
  "v_rcp_f32 v24, v24\n\t" \
  "v_rcp_f32 v25, v25\n\t" \
  "v_rcp_f32 v26, v26\n\t" \
  "v_rcp_f32 v27, v27\n\t" \
  "v_mul_f32 v20, v24, v20\n\t" \
  "v_mul_f32 v21, v25, v21\n\t" \
  "v_mul_f32 v22, v26, v22\n\t" \
  "v_mul_f32 v23, v27, v23\n\t" \
  "v_cvt_pk_bf16_f32 v2, v20, v21\n\t" \
  "v_cvt_pk_bf16_f32 v3, v22, v23\n\t" \
  FSTP("s_memrealtime s[94:95]\n\t") \
  FSTP("s_memtime s[92:93]\n\t") \
  FSTP("s_waitcnt lgkmcnt(0)\n\t") \
  FSTP("s_or_b32 s94, s94, 1\n\t") \
  FSTP("v_writelane_b32 v255, s94, 54\n\t") \
  FSTP("v_writelane_b32 v255, s92, 55\n\t") \
  "s_waitcnt vmcnt(5)\n\t" \
  FSTA("s_memrealtime s[94:95]\n\t") \
  FSTA("s_memtime s[92:93]\n\t") \
  FSTA("s_waitcnt lgkmcnt(0)\n\t") \
  FSTA("s_or_b32 s94, s94, 1\n\t") \
  FSTA("v_writelane_b32 v255, s94, 2\n\t") \
  FSTA("v_writelane_b32 v255, s92, 3\n\t") \
  "v_perm_b32 v28, v8, v4, s45\n\t" \
  "v_perm_b32 v29, v9, v5, s45\n\t" \
  "v_perm_b32 v30, v10, v6, s45\n\t" \
  "v_perm_b32 v31, v11, v7, s45\n\t" \
  "v_perm_b32 v4, v8, v4, s50\n\t" \
  "v_perm_b32 v5, v9, v5, s50\n\t" \
  "v_perm_b32 v6, v10, v6, s50\n\t" \
  "v_perm_b32 v7, v11, v7, s50\n\t" \
  "v_dot2_f32_bf16 v20, v28, v2, 0\n\t" \
  "v_dot2_f32_bf16 v21, v29, v2, 0\n\t" \
  "v_dot2_f32_bf16 v22, v30, v2, 0\n\t" \
  "v_dot2_f32_bf16 v23, v31, v2, 0\n\t" \
  "v_dot2_f32_bf16 v24, v4, v2, 0\n\t" \
  "v_dot2_f32_bf16 v25, v5, v2, 0\n\t" \
  "v_dot2_f32_bf16 v26, v6, v2, 0\n\t" \
  "v_dot2_f32_bf16 v27, v7, v2, 0\n\t" \
  "v_perm_b32 v28, v16, v12, s45\n\t" \
  "v_perm_b32 v29, v17, v13, s45\n\t" \
  "v_perm_b32 v30, v18, v14, s45\n\t" \
  "v_perm_b32 v31, v19, v15, s45\n\t" \
  "v_perm_b32 v12, v16, v12, s50\n\t" \
  "v_perm_b32 v13, v17, v13, s50\n\t" \
  "v_perm_b32 v14, v18, v14, s50\n\t" \
  "v_perm_b32 v15, v19, v15, s50\n\t" \
  "v_dot2_f32_bf16 v20, v28, v3, v20\n\t" \
  "v_dot2_f32_bf16 v21, v29, v3, v21\n\t" \
  "v_dot2_f32_bf16 v22, v30, v3, v22\n\t" \
  "v_dot2_f32_bf16 v23, v31, v3, v23\n\t" \
  "v_dot2_f32_bf16 v24, v12, v3, v24\n\t" \
  "v_dot2_f32_bf16 v25, v13, v3, v25\n\t" \
  "v_dot2_f32_bf16 v26, v14, v3, v26\n\t" \
  "v_dot2_f32_bf16 v27, v15, v3, v27\n\t" \
  "v_permlane32_swap_b32 v20, v22\n\t" \
  "v_permlane32_swap_b32 v21, v23\n\t" \
  "v_permlane32_swap_b32 v24, v26\n\t" \
  "v_permlane32_swap_b32 v25, v27\n\t" \
  "v_add_f32 v20, v20, v22\n\t" \
  "v_add_f32 v21, v21, v23\n\t" \
  "v_add_f32 v24, v24, v26\n\t" \
  "v_add_f32 v25, v25, v27\n\t" \
  "v_permlane16_swap_b32 v20, v21\n\t" \
  "s_nop 0\n\t" \
  "v_permlane16_swap_b32 v24, v25\n\t" \
  "v_add_f32 v20, v20, v21\n\t" \
  "v_add_f32 v21, v24, v25\n\t" \
  "s_nop 0\n\t" \
  "v_add_f32_dpp v20, v20, v20 row_ror:8 row_mask:0xf bank_mask:0xf\n\t" \
  "v_add_f32_dpp v21, v21, v21 row_ror:8 row_mask:0xf bank_mask:0xf\n\t" \
  "s_nop 0\n\t" \
  "v_add_f32_dpp v20, v20, v20 row_ror:4 row_mask:0xf bank_mask:0xf\n\t" \
  "v_add_f32_dpp v21, v21, v21 row_ror:4 row_mask:0xf bank_mask:0xf\n\t" \
  "ds_write_b64 v55, v[20:21]\n\t" \
  "buffer_load_dwordx4 v212, s[4:7], s57 offen lds\n\t" \
  "buffer_load_dwordx4 v213, s[4:7], s57 offen offset:1024 lds\n\t" \
  "s_add_u32 m0, s90, 2048\n\t" \
  "s_nop 0\n\t" \
  "buffer_load_dwordx4 v212, s[4:7], s58 offen lds\n\t" \
  "buffer_load_dwordx4 v213, s[4:7], s58 offen offset:1024 lds\n\t" \
  "s_xor_b32 m0, s90, 4096\n\t" \
  "s_xor_b32 s97, s57, 262144\n\t" \
  "buffer_load_dwordx4 v212, s[4:7], s97 offen lds\n\t" \
  "buffer_load_dwordx4 v213, s[4:7], s97 offen offset:1024 lds\n\t" \
  "s_xor_b32 m0, s90, 6144\n\t" \
  "s_xor_b32 s97, s57, 393216\n\t" \
  "buffer_load_dwordx4 v212, s[4:7], s97 offen lds\n\t" \
  "buffer_load_dwordx4 v213, s[4:7], s97 offen offset:1024 lds\n\t" \
  "v_mov_b32 v4, 0\n\t" \
  "v_mov_b32 v5, 0\n\t" \
  "v_mov_b32 v6, 0\n\t" \
  "v_mov_b32 v7, 0\n\t" \
  "s_waitcnt vmcnt(12) lgkmcnt(0)\n\t" \
  "s_barrier\n\t" \
  "ds_read_b64 v[8:9], v56 offset:0\n\t" \
  "ds_read_b64 v[10:11], v56 offset:256\n\t" \
  "ds_read_b64 v[12:13], v56 offset:512\n\t" \
  "ds_read_b64 v[14:15], v56 offset:768\n\t" \
  "v_readfirstlane_b32 s36, v1\n\t" \
  "s_or_b32 s36, s36, 0x40004000\n\t" \
  "s_add_u32 s36, s36, 0x9e3739b9\n\t" \
  "s_and_b32 s36, s36, 0xbfffbfff\n\t" \
  "v_mov_b32 v211, s36\n\t" \
  "v_mov_b32 v17, s36\n\t" \
  "v_mov_b32 v19, s36\n\t" \
  "s_waitcnt lgkmcnt(0)\n\t" \
  "v_add_f32 v8, v8, v10\n\t" \
  "v_add_f32 v12, v12, v14\n\t" \
  "v_add_f32 v9, v9, v11\n\t" \
  "v_add_f32 v13, v13, v15\n\t" \
  "v_add_f32 v16, v8, v12\n\t" \
  "v_add_f32 v18, v9, v13\n\t" \
  "buffer_store_dwordx4 v[16:19], v57, s[16:19], s52 offen sc1\n\t" \
  "s_waitcnt vmcnt(9)\n\t" \
  FSTA("s_memrealtime s[94:95]\n\t") \
  FSTA("s_memtime s[92:93]\n\t") \
  FSTA("s_waitcnt lgkmcnt(0)\n\t") \
  FSTA("s_or_b32 s94, s94, 1\n\t") \
  FSTA("v_writelane_b32 v255, s94, 4\n\t") \
  FSTA("v_writelane_b32 v255, s92, 5\n\t") \
  "v_dot2c_f32_bf16 v4, 0x3f803f80, v32\n\t" \
  "v_dot2c_f32_bf16 v6, v32, v32\n\t" \
  "v_dot2c_f32_bf16 v5, 0x3f803f80, v40\n\t" \
  "v_dot2c_f32_bf16 v7, v40, v40\n\t" \
  "v_dot2c_f32_bf16 v4, 0x3f803f80, v33\n\t" \
  "v_dot2c_f32_bf16 v6, v33, v33\n\t" \
  "v_dot2c_f32_bf16 v5, 0x3f803f80, v41\n\t" \
  "v_dot2c_f32_bf16 v7, v41, v41\n\t" \
  "v_dot2c_f32_bf16 v4, 0x3f803f80, v34\n\t" \
  "v_dot2c_f32_bf16 v6, v34, v34\n\t" \
  "v_dot2c_f32_bf16 v5, 0x3f803f80, v42\n\t" \
  "v_dot2c_f32_bf16 v7, v42, v42\n\t" \
  "v_dot2c_f32_bf16 v4, 0x3f803f80, v35\n\t" \
  "v_dot2c_f32_bf16 v6, v35, v35\n\t" \
  "v_dot2c_f32_bf16 v5, 0x3f803f80, v43\n\t" \
  "v_dot2c_f32_bf16 v7, v43, v43\n\t" \
  "v_dot2c_f32_bf16 v4, 0x3f803f80, v36\n\t" \
  "v_dot2c_f32_bf16 v6, v36, v36\n\t" \
  "v_dot2c_f32_bf16 v5, 0x3f803f80, v44\n\t" \
  "v_dot2c_f32_bf16 v7, v44, v44\n\t" \
  "v_dot2c_f32_bf16 v4, 0x3f803f80, v37\n\t" \
  "v_dot2c_f32_bf16 v6, v37, v37\n\t" \
  "v_dot2c_f32_bf16 v5, 0x3f803f80, v45\n\t" \
  "v_dot2c_f32_bf16 v7, v45, v45\n\t" \
  "v_dot2c_f32_bf16 v4, 0x3f803f80, v38\n\t" \
  "v_dot2c_f32_bf16 v6, v38, v38\n\t" \
  "v_dot2c_f32_bf16 v5, 0x3f803f80, v46\n\t" \
  "v_dot2c_f32_bf16 v7, v46, v46\n\t" \
  "v_dot2c_f32_bf16 v4, 0x3f803f80, v39\n\t" \
  "v_dot2c_f32_bf16 v6, v39, v39\n\t" \
  "v_dot2c_f32_bf16 v5, 0x3f803f80, v47\n\t" \
  "v_dot2c_f32_bf16 v7, v47, v47\n\t" \
  "v_mov_b32 v21, s36\n\t" \
  "v_mov_b32 v23, s36\n\t" \
  "v_permlane32_swap_b32 v4, v5\n\t" \
  "v_permlane32_swap_b32 v6, v7\n\t" \
  "v_add_f32 v4, v4, v5\n\t" \
  "v_add_f32 v6, v6, v7\n\t" \
  "s_nop 1\n\t" \
  "v_permlane16_swap_b32 v4, v6\n\t" \
  "v_add_f32 v4, v4, v6\n\t" \
  "s_nop 1\n\t" \
  "v_add_f32_dpp v4, v4, v4 quad_perm:[1,0,3,2] row_mask:0xf bank_mask:0xf\n\t" \
  "s_nop 1\n\t" \
  "v_add_f32_dpp v4, v4, v4 quad_perm:[2,3,0,1] row_mask:0xf bank_mask:0xf\n\t" \
  "s_nop 1\n\t" \
  "v_add_f32_dpp v4, v4, v4 row_half_mirror row_mask:0xf bank_mask:0xf\n\t" \
  "s_nop 1\n\t" \
  "v_add_f32_dpp v4, v4, v4 row_mirror row_mask:0xf bank_mask:0xf\n\t" \
  "v_mul_f32 v4, 0x3a800000, v4\n\t" \
  "v_mov_b32 v5, v4\n\t" \
  "s_nop 1\n\t" \
  "v_permlane16_swap_b32 v4, v5\n\t" \
  "v_fma_f32 v5, -v4, v4, v5\n\t" \
  "v_max_f32 v5, 0, v5\n\t" \
  "v_add_f32 v5, 0x3727c5ac, v5\n\t" \
  "v_rsq_f32 v20, v5\n\t" \
  "s_nop 0\n\t" \
  "v_mul_f32_e64 v22, v20, -v4\n\t" \
  "buffer_store_dwordx4 v[20:23], v58, s[16:19], s46 offen sc1\n\t" \
  FSTA("s_memrealtime s[94:95]\n\t") \
  FSTA("s_memtime s[92:93]\n\t") \
  FSTA("s_waitcnt lgkmcnt(0)\n\t") \
  FSTA("s_or_b32 s94, s94, 1\n\t") \
  FSTA("v_writelane_b32 v255, s94, 6\n\t") \
  FSTA("v_writelane_b32 v255, s92, 7\n\t") \
  "buffer_load_ushort v218, v53, s[12:15], s44 offen\n\t" \
  "s_add_u32 m0, s76, 0\n\t" \
  "s_nop 0\n\t" \
  "buffer_load_dwordx4 v200, s[20:23], s49 offen offset:0 lds\n\t" \
  "buffer_load_dwordx4 v201, s[20:23], s49 offen offset:1024 lds\n\t" \
  "buffer_load_dwordx4 v202, s[20:23], s49 offen offset:2048 lds\n\t" \
  "buffer_load_dwordx4 v203, s[20:23], s49 offen offset:3072 lds\n\t" \
  "s_add_u32 m0, s77, 49152\n\t" \
  "s_xor_b32 s54, s87, 0\n\t" \
  "s_or_b32 s54, s54, s48\n\t" \
  "buffer_load_dwordx4 v212, s[4:7], s54 offen lds\n\t" \
  "buffer_load_dwordx4 v213, s[4:7], s54 offen offset:1024 lds\n\t" \
  "s_add_u32 m0, s77, 51200\n\t" \
  "s_xor_b32 s54, s87, 131072\n\t" \
  "s_or_b32 s54, s54, s48\n\t" \
  "buffer_load_dwordx4 v212, s[4:7], s54 offen lds\n\t" \
  "buffer_load_dwordx4 v213, s[4:7], s54 offen offset:1024 lds\n\t" \
  "s_add_u32 m0, s77, 53248\n\t" \
  "s_xor_b32 s54, s87, 262144\n\t" \
  "s_or_b32 s54, s54, s48\n\t" \
  "buffer_load_dwordx4 v212, s[4:7], s54 offen lds\n\t" \
  "buffer_load_dwordx4 v213, s[4:7], s54 offen offset:1024 lds\n\t" \
  "s_add_u32 m0, s77, 55296\n\t" \
  "s_xor_b32 s54, s87, 393216\n\t" \
  "s_or_b32 s54, s54, s48\n\t" \
  "buffer_load_dwordx4 v212, s[4:7], s54 offen lds\n\t" \
  "buffer_load_dwordx4 v213, s[4:7], s54 offen offset:1024 lds\n\t" \
  "s_add_u32 m0, s76, 49152\n\t" \
  "s_nop 0\n\t" \
  "buffer_load_dwordx4 v200, s[20:23], s79 offen offset:0 lds\n\t" \
  "buffer_load_dwordx4 v201, s[20:23], s79 offen offset:1024 lds\n\t" \
  "buffer_load_dwordx4 v202, s[20:23], s79 offen offset:2048 lds\n\t" \
  "buffer_load_dwordx4 v203, s[20:23], s79 offen offset:3072 lds\n\t" \
  "v_add_u32 v192, 65536, v184\n\t" \
  "v_add_u32 v193, 65536, v185\n\t" \
  "v_add_u32 v194, 65536, v186\n\t" \
  "v_add_u32 v195, 65536, v187\n\t" \
  "v_add_u32 v196, 65536, v188\n\t" \
  "v_add_u32 v197, 65536, v189\n\t" \
  "v_add_u32 v198, 65536, v190\n\t" \
  "v_add_u32 v199, 65536, v191\n\t" \
  "v_lshrrev_b32 v48, 3, v253\n\t" \
  "s_lshr_b32 s54, s2, 6\n\t" \
  "v_lshl_add_u32 v48, s54, 5, v48\n\t" \
  "v_mul_u32_u24 v48, 98304, v48\n\t" \
  "v_and_b32 v49, 0x70, v244\n\t" \
  "s_bfe_u32 s54, s2, 0x10002\n\t" \
  "s_lshl1_add_u32 s54, s40, s54\n\t" \
  "s_lshl_b32 s54, s54, 10\n\t" \
  "s_and_b32 s55, s2, 0x38\n\t" \
  "s_lshl4_add_u32 s54, s55, s54\n\t" \
  "s_add_u32 s54, s54, 4096\n\t" \
  "v_add3_u32 v209, v48, v49, s54\n\t" \
  "v_lshlrev_b32 v219, 2, v253\n\t" \
  "v_add_u32 v219, 151552, v219\n\t" \
  "s_and_b32 s54, s39, 15\n\t" \
  "s_lshl2_add_u32 s54, s54, s3\n\t" \
  "s_lshl_b32 s54, s54, 11\n\t" \
  "s_lshl_b32 s55, s40, 19\n\t" \
  "s_add_u32 s54, s54, s55\n\t" \
  "v_lshl_add_u32 v217, v247, 7, s54\n\t" \
  "s_mov_b64 exec, 0\n\t" \
  "s_cmp_eq_u32 s3, 0\n\t" \
  "s_cbranch_scc1 .Lhc00\n\t" \
  "s_cmp_eq_u32 s3, 1\n\t" \
  "s_cbranch_scc1 .Lhc01\n\t" \
  "s_cmp_eq_u32 s3, 2\n\t" \
  "s_cbranch_scc1 .Lhc02\n\t" \
  "s_branch .Lhc03\n\t" \
  ".Lhr0:\n\t" \
  "s_mov_b64 exec, -1\n\t" \
  "s_sleep 6\n\t" \
  "s_add_u32 s58, s53, 16384\n\t" \
  "s_add_u32 s90, s53, 32768\n\t" \
  "s_add_u32 s91, s53, 49152\n\t" \
  "s_xor_b32 s96, s51, 0\n\t" \
  "s_xor_b32 s97, s51, 1024\n\t" \
  "s_xor_b32 s99, s51, 2048\n\t" \
  "s_xor_b32 s57, s51, 3072\n\t" \
  "buffer_load_dwordx4 v[128:131], v248, s[16:19], s53 offen sc1\n\t" \
  "buffer_load_dwordx4 v[132:135], v248, s[16:19], s58 offen sc1\n\t" \
  "buffer_load_dwordx4 v[136:139], v248, s[16:19], s90 offen sc1\n\t" \
  "buffer_load_dwordx4 v[140:143], v248, s[16:19], s91 offen sc1\n\t" \
  "buffer_load_dwordx4 v[160:163], v249, s[16:19], s96 offen sc1\n\t" \
  "buffer_load_dwordx4 v[164:167], v249, s[16:19], s97 offen sc1\n\t" \
  "buffer_load_dwordx4 v[168:171], v249, s[16:19], s99 offen sc1\n\t" \
  "buffer_load_dwordx4 v[172:175], v249, s[16:19], s57 offen sc1\n\t" \
  "s_bitcmp1_b32 s2, 7\n\t" \
  "s_cselect_b64 s[12:13], s[64:65], s[60:61]\n\t" \
  FSTP("s_memrealtime s[94:95]\n\t") \
  FSTP("s_memtime s[92:93]\n\t") \
  FSTP("s_waitcnt lgkmcnt(0)\n\t") \
  FSTP("s_or_b32 s94, s94, 1\n\t") \
  FSTP("v_writelane_b32 v255, s94, 56\n\t") \
  FSTP("v_writelane_b32 v255, s92, 57\n\t") \
  "s_waitcnt vmcnt(12)\n\t" \
  "ds_read_b128 v[16:19], v204 offset:0\n\t" \
  "ds_read_b128 v[48:51], v204 offset:2048\n\t" \
  "ds_read_b128 v[80:83], v204 offset:4096\n\t" \
  "ds_read_b128 v[112:115], v204 offset:6144\n\t" \
  "ds_read_b128 v[20:23], v205 offset:0\n\t" \
  "ds_read_b128 v[52:55], v205 offset:2048\n\t" \
  "ds_read_b128 v[84:87], v205 offset:4096\n\t" \
  "ds_read_b128 v[116:119], v205 offset:6144\n\t" \
  "ds_read_b128 v[24:27], v204 offset:49152\n\t" \
  "ds_read_b128 v[56:59], v204 offset:51200\n\t" \
  "ds_read_b128 v[88:91], v204 offset:53248\n\t" \
  "ds_read_b128 v[120:123], v204 offset:55296\n\t" \
  "ds_read_b128 v[28:31], v205 offset:49152\n\t" \
  "ds_read_b128 v[60:63], v205 offset:51200\n\t" \
  "ds_read_b128 v[92:95], v205 offset:53248\n\t" \
  "ds_read_b128 v[124:127], v205 offset:55296\n\t" \
  "v_lshlrev_b32 v218, 16, v218\n\t" \
  "v_mul_f32 v145, 0xbfb8aa3b, v218\n\t" \
  "v_exp_f32 v145, v145\n\t" \
  "s_nop 0\n\t" \
  "v_add_f32 v145, 1.0, v145\n\t" \
  "v_rcp_f32 v145, v145\n\t" \
  "s_nop 0\n\t" \
  "v_mul_f32 v218, v145, v218\n\t" \
  "v_cvt_pk_bf16_f32 v218, v218, v218\n\t" \
  "v_lshlrev_b32 v218, 16, v218\n\t" \
  "s_waitcnt lgkmcnt(12)\n\t" \
  "v_lshlrev_b32 v0, 16, v16\n\t" \
  "v_and_b32 v1, s98, v16\n\t" \
  "v_lshlrev_b32 v2, 16, v17\n\t" \
  "v_and_b32 v3, s98, v17\n\t" \
  "v_lshlrev_b32 v4, 16, v18\n\t" \
  "v_and_b32 v5, s98, v18\n\t" \
  "v_lshlrev_b32 v6, 16, v19\n\t" \
  "v_and_b32 v7, s98, v19\n\t" \
  "v_lshlrev_b32 v32, 16, v48\n\t" \
  "v_and_b32 v33, s98, v48\n\t" \
  "v_lshlrev_b32 v34, 16, v49\n\t" \
  "v_and_b32 v35, s98, v49\n\t" \
  "v_lshlrev_b32 v36, 16, v50\n\t" \
  "v_and_b32 v37, s98, v50\n\t" \
  "v_lshlrev_b32 v38, 16, v51\n\t" \
  "v_and_b32 v39, s98, v51\n\t" \
  "v_lshlrev_b32 v64, 16, v80\n\t" \
  "v_and_b32 v65, s98, v80\n\t" \
  "v_lshlrev_b32 v66, 16, v81\n\t" \
  "v_and_b32 v67, s98, v81\n\t" \
  "v_lshlrev_b32 v68, 16, v82\n\t" \
  "v_and_b32 v69, s98, v82\n\t" \
  "v_lshlrev_b32 v70, 16, v83\n\t" \
  "v_and_b32 v71, s98, v83\n\t" \
  "v_lshlrev_b32 v96, 16, v112\n\t" \
  "v_and_b32 v97, s98, v112\n\t" \
  "v_lshlrev_b32 v98, 16, v113\n\t" \
  "v_and_b32 v99, s98, v113\n\t" \
  "v_lshlrev_b32 v100, 16, v114\n\t" \
  "v_and_b32 v101, s98, v114\n\t" \
  "v_lshlrev_b32 v102, 16, v115\n\t" \
  "v_and_b32 v103, s98, v115\n\t" \
  "s_waitcnt lgkmcnt(8)\n\t" \
  "v_lshlrev_b32 v8, 16, v20\n\t" \
  "v_and_b32 v9, s98, v20\n\t" \
  "v_lshlrev_b32 v10, 16, v21\n\t" \
  "v_and_b32 v11, s98, v21\n\t" \
  "v_lshlrev_b32 v12, 16, v22\n\t" \
  "v_and_b32 v13, s98, v22\n\t" \
  "v_lshlrev_b32 v14, 16, v23\n\t" \
  "v_and_b32 v15, s98, v23\n\t" \
  "v_lshlrev_b32 v40, 16, v52\n\t" \
  "v_and_b32 v41, s98, v52\n\t" \
  "v_lshlrev_b32 v42, 16, v53\n\t" \
  "v_and_b32 v43, s98, v53\n\t" \
  "v_lshlrev_b32 v44, 16, v54\n\t" \
  "v_and_b32 v45, s98, v54\n\t" \
  "v_lshlrev_b32 v46, 16, v55\n\t" \
  "v_and_b32 v47, s98, v55\n\t" \
  "v_lshlrev_b32 v72, 16, v84\n\t" \
  "v_and_b32 v73, s98, v84\n\t" \
  "v_lshlrev_b32 v74, 16, v85\n\t" \
  "v_and_b32 v75, s98, v85\n\t" \
  "v_lshlrev_b32 v76, 16, v86\n\t" \
  "v_and_b32 v77, s98, v86\n\t" \
  "v_lshlrev_b32 v78, 16, v87\n\t" \
  "v_and_b32 v79, s98, v87\n\t" \
  "v_lshlrev_b32 v104, 16, v116\n\t" \
  "v_and_b32 v105, s98, v116\n\t" \
  "v_lshlrev_b32 v106, 16, v117\n\t" \
  "v_and_b32 v107, s98, v117\n\t" \
  "v_lshlrev_b32 v108, 16, v118\n\t" \
  "v_and_b32 v109, s98, v118\n\t" \
  "v_lshlrev_b32 v110, 16, v119\n\t" \
  "v_and_b32 v111, s98, v119\n\t" \
  FSTP("s_memrealtime s[94:95]\n\t") \
  FSTP("s_memtime s[92:93]\n\t") \
  FSTP("s_waitcnt lgkmcnt(0)\n\t") \
  FSTP("s_or_b32 s94, s94, 1\n\t") \
  FSTP("v_writelane_b32 v255, s94, 58\n\t") \
  FSTP("v_writelane_b32 v255, s92, 59\n\t") \
  "s_waitcnt vmcnt(0)\n\t" \
  "v_xor_b32 v144, v211, v129\n\t" \
  "v_bitop3_b32 v144, v144, v131, v211 bitop3:0xf6\n\t" \
  "v_bitop3_b32 v144, v144, v133, v211 bitop3:0xf6\n\t" \
  "v_bitop3_b32 v144, v144, v135, v211 bitop3:0xf6\n\t" \
  "v_bitop3_b32 v144, v144, v137, v211 bitop3:0xf6\n\t" \
  "v_bitop3_b32 v144, v144, v139, v211 bitop3:0xf6\n\t" \
  "v_bitop3_b32 v144, v144, v141, v211 bitop3:0xf6\n\t" \
  "v_bitop3_b32 v144, v144, v143, v211 bitop3:0xf6\n\t" \
  "v_bitop3_b32 v144, v144, v161, v211 bitop3:0xf6\n\t" \
  "v_bitop3_b32 v144, v144, v163, v211 bitop3:0xf6\n\t" \
  "v_bitop3_b32 v144, v144, v165, v211 bitop3:0xf6\n\t" \
  "v_bitop3_b32 v144, v144, v167, v211 bitop3:0xf6\n\t" \
  "v_bitop3_b32 v144, v144, v169, v211 bitop3:0xf6\n\t" \
  "v_bitop3_b32 v144, v144, v171, v211 bitop3:0xf6\n\t" \
  "v_bitop3_b32 v144, v144, v173, v211 bitop3:0xf6\n\t" \
  "v_bitop3_b32 v144, v144, v175, v211 bitop3:0xf6\n\t" \
  "v_cmp_ne_u32 vcc, 0, v144\n\t" \
  "v_add_f32 v146, v128, v132\n\t" \
  "v_add_f32 v148, v136, v140\n\t" \
  "v_add_f32 v147, v130, v134\n\t" \
  "v_add_f32 v149, v138, v142\n\t" \
  "v_add_f32 v146, v146, v148\n\t" \
  "v_add_f32 v147, v147, v149\n\t" \
  "s_cbranch_vccnz .Lfr_s\n\t" \
  ".Lfr_k:\n\t" \
  FSTA("s_memrealtime s[94:95]\n\t") \
  FSTA("s_memtime s[92:93]\n\t") \
  FSTA("s_waitcnt lgkmcnt(0)\n\t") \
  FSTA("s_or_b32 s94, s94, 1\n\t") \
  FSTA("v_writelane_b32 v255, s94, 8\n\t") \
  FSTA("v_writelane_b32 v255, s92, 9\n\t") \
  "v_cvt_pk_bf16_f32 v147, v146, v147\n\t" \
  "v_lshlrev_b32 v146, 16, v147\n\t" \
  "v_and_b32 v147, s98, v147\n\t" \
  "v_add_f32 v146, v146, v251\n\t" \
  "v_add_f32 v147, v147, v251\n\t" \
  "v_cvt_pk_bf16_f32 v147, v146, v147\n\t" \
  "v_lshlrev_b32 v146, 16, v147\n\t" \
  "v_and_b32 v147, s98, v147\n\t" \
  "v_mul_f32 v146, 0x3e0293ee, v146\n\t" \
  "v_mul_f32 v147, 0x3e0293ee, v147\n\t" \
  "ds_write_b64 v250, v[146:147]\n\t" \
  "v_pk_fma_f32 v[0:1], v[0:1], v[160:161], v[162:163] op_sel_hi:[1,0,0]\n\t" \
  "v_pk_fma_f32 v[2:3], v[2:3], v[160:161], v[162:163] op_sel_hi:[1,0,0]\n\t" \
  "v_pk_fma_f32 v[4:5], v[4:5], v[160:161], v[162:163] op_sel_hi:[1,0,0]\n\t" \
  "v_pk_fma_f32 v[6:7], v[6:7], v[160:161], v[162:163] op_sel_hi:[1,0,0]\n\t" \
  "v_pk_fma_f32 v[32:33], v[32:33], v[164:165], v[166:167] op_sel_hi:[1,0,0]\n\t" \
  "v_pk_fma_f32 v[34:35], v[34:35], v[164:165], v[166:167] op_sel_hi:[1,0,0]\n\t" \
  "v_pk_fma_f32 v[36:37], v[36:37], v[164:165], v[166:167] op_sel_hi:[1,0,0]\n\t" \
  "v_pk_fma_f32 v[38:39], v[38:39], v[164:165], v[166:167] op_sel_hi:[1,0,0]\n\t" \
  "v_pk_fma_f32 v[64:65], v[64:65], v[168:169], v[170:171] op_sel_hi:[1,0,0]\n\t" \
  "v_pk_fma_f32 v[66:67], v[66:67], v[168:169], v[170:171] op_sel_hi:[1,0,0]\n\t" \
  "v_pk_fma_f32 v[68:69], v[68:69], v[168:169], v[170:171] op_sel_hi:[1,0,0]\n\t" \
  "v_pk_fma_f32 v[70:71], v[70:71], v[168:169], v[170:171] op_sel_hi:[1,0,0]\n\t" \
  "v_pk_fma_f32 v[96:97], v[96:97], v[172:173], v[174:175] op_sel_hi:[1,0,0]\n\t" \
  "v_pk_fma_f32 v[98:99], v[98:99], v[172:173], v[174:175] op_sel_hi:[1,0,0]\n\t" \
  "v_pk_fma_f32 v[100:101], v[100:101], v[172:173], v[174:175] op_sel_hi:[1,0,0]\n\t" \
  "v_pk_fma_f32 v[102:103], v[102:103], v[172:173], v[174:175] op_sel_hi:[1,0,0]\n\t" \
  FSTP("s_memrealtime s[94:95]\n\t") \
  FSTP("s_memtime s[92:93]\n\t") \
  FSTP("s_waitcnt lgkmcnt(0)\n\t") \
  FSTP("s_or_b32 s94, s94, 1\n\t") \
  FSTP("v_writelane_b32 v255, s94, 24\n\t") \
  FSTP("v_writelane_b32 v255, s92, 25\n\t") \
  "s_waitcnt lgkmcnt(0)\n\t" \
  "s_barrier\n\t" \
  FSTP("s_memrealtime s[94:95]\n\t") \
  FSTP("s_memtime s[92:93]\n\t") \
  FSTP("s_waitcnt lgkmcnt(0)\n\t") \
  FSTP("s_or_b32 s94, s94, 1\n\t") \
  FSTP("v_writelane_b32 v255, s94, 26\n\t") \
  FSTP("v_writelane_b32 v255, s92, 27\n\t") \
  "ds_read_b128 v[128:131], v208 offset:0\n\t" \
  "ds_read_b128 v[132:135], v208 offset:16\n\t" \
  "ds_read_b128 v[136:139], v208 offset:256\n\t" \
  "ds_read_b128 v[140:143], v208 offset:272\n\t" \
  "ds_read_b128 v[144:147], v208 offset:128\n\t" \
  "ds_read_b128 v[148:151], v208 offset:144\n\t" \
  "ds_read_b128 v[152:155], v208 offset:384\n\t" \
  "ds_read_b128 v[156:159], v208 offset:400\n\t" \
  "global_load_dword v246, v215, s[20:21]\n\t" \
  "v_pk_fma_f32 v[8:9], v[8:9], v[160:161], v[162:163] op_sel_hi:[1,0,0]\n\t" \
  "v_pk_fma_f32 v[10:11], v[10:11], v[160:161], v[162:163] op_sel_hi:[1,0,0]\n\t" \
  "v_pk_fma_f32 v[12:13], v[12:13], v[160:161], v[162:163] op_sel_hi:[1,0,0]\n\t" \
  "v_pk_fma_f32 v[14:15], v[14:15], v[160:161], v[162:163] op_sel_hi:[1,0,0]\n\t" \
  "v_pk_fma_f32 v[40:41], v[40:41], v[164:165], v[166:167] op_sel_hi:[1,0,0]\n\t" \
  "v_pk_fma_f32 v[42:43], v[42:43], v[164:165], v[166:167] op_sel_hi:[1,0,0]\n\t" \
  "v_pk_fma_f32 v[44:45], v[44:45], v[164:165], v[166:167] op_sel_hi:[1,0,0]\n\t" \
  "v_pk_fma_f32 v[46:47], v[46:47], v[164:165], v[166:167] op_sel_hi:[1,0,0]\n\t" \
  "v_pk_fma_f32 v[72:73], v[72:73], v[168:169], v[170:171] op_sel_hi:[1,0,0]\n\t" \
  "v_pk_fma_f32 v[74:75], v[74:75], v[168:169], v[170:171] op_sel_hi:[1,0,0]\n\t" \
  "v_pk_fma_f32 v[76:77], v[76:77], v[168:169], v[170:171] op_sel_hi:[1,0,0]\n\t" \
  "v_pk_fma_f32 v[78:79], v[78:79], v[168:169], v[170:171] op_sel_hi:[1,0,0]\n\t" \
  "v_pk_fma_f32 v[104:105], v[104:105], v[172:173], v[174:175] op_sel_hi:[1,0,0]\n\t" \
  "v_pk_fma_f32 v[106:107], v[106:107], v[172:173], v[174:175] op_sel_hi:[1,0,0]\n\t" \
  "v_pk_fma_f32 v[108:109], v[108:109], v[172:173], v[174:175] op_sel_hi:[1,0,0]\n\t" \
  "v_pk_fma_f32 v[110:111], v[110:111], v[172:173], v[174:175] op_sel_hi:[1,0,0]\n\t" \
  "s_xor_b32 s97, s86, 0\n\t" \
  "s_waitcnt lgkmcnt(4)\n\t" \
  "v_pk_fma_f32 v[0:1], v[0:1], v[128:129], v[136:137]\n\t" \
  "v_pk_fma_f32 v[2:3], v[2:3], v[130:131], v[138:139]\n\t" \
  "v_pk_fma_f32 v[4:5], v[4:5], v[132:133], v[140:141]\n\t" \
  "v_pk_fma_f32 v[6:7], v[6:7], v[134:135], v[142:143]\n\t" \
  "v_cvt_pk_bf16_f32 v16, v0, v1\n\t" \
  "v_cvt_pk_bf16_f32 v17, v2, v3\n\t" \
  "v_cvt_pk_bf16_f32 v18, v4, v5\n\t" \
  "v_cvt_pk_bf16_f32 v19, v6, v7\n\t" \
  "buffer_store_dwordx4 v[16:19], v214, s[16:19], s97 offen sc1\n\t" \
  "s_xor_b32 s97, s86, 256\n\t" \
  "v_pk_fma_f32 v[32:33], v[32:33], v[128:129], v[136:137]\n\t" \
  "v_pk_fma_f32 v[34:35], v[34:35], v[130:131], v[138:139]\n\t" \
  "v_pk_fma_f32 v[36:37], v[36:37], v[132:133], v[140:141]\n\t" \
  "v_pk_fma_f32 v[38:39], v[38:39], v[134:135], v[142:143]\n\t" \
  "v_cvt_pk_bf16_f32 v48, v32, v33\n\t" \
  "v_cvt_pk_bf16_f32 v49, v34, v35\n\t" \
  "v_cvt_pk_bf16_f32 v50, v36, v37\n\t" \
  "v_cvt_pk_bf16_f32 v51, v38, v39\n\t" \
  "buffer_store_dwordx4 v[48:51], v214, s[16:19], s97 offen sc1\n\t" \
  "s_xor_b32 s97, s86, 512\n\t" \
  "v_pk_fma_f32 v[64:65], v[64:65], v[128:129], v[136:137]\n\t" \
  "v_pk_fma_f32 v[66:67], v[66:67], v[130:131], v[138:139]\n\t" \
  "v_pk_fma_f32 v[68:69], v[68:69], v[132:133], v[140:141]\n\t" \
  "v_pk_fma_f32 v[70:71], v[70:71], v[134:135], v[142:143]\n\t" \
  "v_cvt_pk_bf16_f32 v80, v64, v65\n\t" \
  "v_cvt_pk_bf16_f32 v81, v66, v67\n\t" \
  "v_cvt_pk_bf16_f32 v82, v68, v69\n\t" \
  "v_cvt_pk_bf16_f32 v83, v70, v71\n\t" \
  ".Lhs0:\n\ts_cbranch_execz .Lhs0n\n\t" \
  "buffer_store_dwordx4 v[80:83], v214, s[16:19], s97 offen sc1\n\t" \
  "s_xor_b32 s97, s86, 768\n\t" \
  "v_pk_fma_f32 v[96:97], v[96:97], v[128:129], v[136:137]\n\t" \
  "v_pk_fma_f32 v[98:99], v[98:99], v[130:131], v[138:139]\n\t" \
  "v_pk_fma_f32 v[100:101], v[100:101], v[132:133], v[140:141]\n\t" \
  "v_pk_fma_f32 v[102:103], v[102:103], v[134:135], v[142:143]\n\t" \
  "v_cvt_pk_bf16_f32 v112, v96, v97\n\t" \
  "v_cvt_pk_bf16_f32 v113, v98, v99\n\t" \
  "v_cvt_pk_bf16_f32 v114, v100, v101\n\t" \
  "v_cvt_pk_bf16_f32 v115, v102, v103\n\t" \
  "buffer_store_dwordx4 v[112:115], v214, s[16:19], s97 offen sc1\n\t" \
  "ds_read_b128 v[128:131], v208 offset:512\n\t" \
  "ds_read_b128 v[132:135], v208 offset:528\n\t" \
  "ds_read_b128 v[136:139], v208 offset:768\n\t" \
  "ds_read_b128 v[140:143], v208 offset:784\n\t" \
  FSTP("s_memrealtime s[94:95]\n\t") \
  FSTP("s_memtime s[92:93]\n\t") \
  "s_add_u32 m0, s77, 98304\n\t" \
  "s_xor_b32 s96, s87, 0\n\t" \
  "s_add_u32 s96, s96, s88\n\t" \
  "s_waitcnt lgkmcnt(4)\n\t" \
  "v_pk_fma_f32 v[8:9], v[8:9], v[144:145], v[152:153]\n\t" \
  "v_pk_fma_f32 v[10:11], v[10:11], v[146:147], v[154:155]\n\t" \
  "v_pk_fma_f32 v[12:13], v[12:13], v[148:149], v[156:157]\n\t" \
  "v_pk_fma_f32 v[14:15], v[14:15], v[150:151], v[158:159]\n\t" \
  "v_cvt_pk_bf16_f32 v20, v8, v9\n\t" \
  "v_cvt_pk_bf16_f32 v21, v10, v11\n\t" \
  "v_cvt_pk_bf16_f32 v22, v12, v13\n\t" \
  "v_cvt_pk_bf16_f32 v23, v14, v15\n\t" \
  "buffer_load_dwordx4 v212, s[4:7], s96 offen lds\n\t" \
  "v_pk_fma_f32 v[40:41], v[40:41], v[144:145], v[152:153]\n\t" \
  "v_pk_fma_f32 v[42:43], v[42:43], v[146:147], v[154:155]\n\t" \
  "v_pk_fma_f32 v[44:45], v[44:45], v[148:149], v[156:157]\n\t" \
  "v_pk_fma_f32 v[46:47], v[46:47], v[150:151], v[158:159]\n\t" \
  "v_cvt_pk_bf16_f32 v52, v40, v41\n\t" \
  "v_cvt_pk_bf16_f32 v53, v42, v43\n\t" \
  "v_cvt_pk_bf16_f32 v54, v44, v45\n\t" \
  "v_cvt_pk_bf16_f32 v55, v46, v47\n\t" \
  "buffer_load_dwordx4 v213, s[4:7], s96 offen offset:1024 lds\n\t" \
  "s_add_u32 m0, s77, 100352\n\t" \
  "s_xor_b32 s96, s87, 131072\n\t" \
  "s_add_u32 s96, s96, s88\n\t" \
  "v_pk_fma_f32 v[72:73], v[72:73], v[144:145], v[152:153]\n\t" \
  "v_pk_fma_f32 v[74:75], v[74:75], v[146:147], v[154:155]\n\t" \
  "v_pk_fma_f32 v[76:77], v[76:77], v[148:149], v[156:157]\n\t" \
  "v_pk_fma_f32 v[78:79], v[78:79], v[150:151], v[158:159]\n\t" \
  "v_cvt_pk_bf16_f32 v84, v72, v73\n\t" \
  "v_cvt_pk_bf16_f32 v85, v74, v75\n\t" \
  "v_cvt_pk_bf16_f32 v86, v76, v77\n\t" \
  "v_cvt_pk_bf16_f32 v87, v78, v79\n\t" \
  "buffer_load_dwordx4 v212, s[4:7], s96 offen lds\n\t" \
  "v_pk_fma_f32 v[104:105], v[104:105], v[144:145], v[152:153]\n\t" \
  "v_pk_fma_f32 v[106:107], v[106:107], v[146:147], v[154:155]\n\t" \
  "v_pk_fma_f32 v[108:109], v[108:109], v[148:149], v[156:157]\n\t" \
  "v_pk_fma_f32 v[110:111], v[110:111], v[150:151], v[158:159]\n\t" \
  "v_cvt_pk_bf16_f32 v116, v104, v105\n\t" \
  "v_cvt_pk_bf16_f32 v117, v106, v107\n\t" \
  "v_cvt_pk_bf16_f32 v118, v108, v109\n\t" \
  "v_cvt_pk_bf16_f32 v119, v110, v111\n\t" \
  "buffer_load_dwordx4 v213, s[4:7], s96 offen offset:1024 lds\n\t" \
  "ds_read_b128 v[144:147], v208 offset:640\n\t" \
  "ds_read_b128 v[148:151], v208 offset:656\n\t" \
  "ds_read_b128 v[152:155], v208 offset:896\n\t" \
  "ds_read_b128 v[156:159], v208 offset:912\n\t" \
  FSTP("v_writelane_b32 v255, s94, 28\n\t") \
  FSTP("v_writelane_b32 v255, s92, 29\n\t") \
  FSTP("s_memrealtime s[94:95]\n\t") \
  FSTP("s_memtime s[92:93]\n\t") \
  "s_add_u32 m0, s77, 102400\n\t" \
  "s_xor_b32 s96, s87, 262144\n\t" \
  "s_add_u32 s96, s96, s88\n\t" \
  "v_lshlrev_b32 v176, 16, v24\n\t" \
  "v_and_b32 v177, s98, v24\n\t" \
  "v_lshlrev_b32 v178, 16, v25\n\t" \
  "v_and_b32 v179, s98, v25\n\t" \
  "v_lshlrev_b32 v180, 16, v26\n\t" \
  "v_and_b32 v181, s98, v26\n\t" \
  "v_lshlrev_b32 v182, 16, v27\n\t" \
  "v_and_b32 v183, s98, v27\n\t" \
  "v_pk_fma_f32 v[176:177], v[176:177], v[160:161], v[162:163] op_sel_hi:[1,0,0]\n\t" \
  "v_pk_fma_f32 v[178:179], v[178:179], v[160:161], v[162:163] op_sel_hi:[1,0,0]\n\t" \
  "v_pk_fma_f32 v[180:181], v[180:181], v[160:161], v[162:163] op_sel_hi:[1,0,0]\n\t" \
  "v_pk_fma_f32 v[182:183], v[182:183], v[160:161], v[162:163] op_sel_hi:[1,0,0]\n\t" \
  "s_waitcnt lgkmcnt(4)\n\t" \
  "v_pk_fma_f32 v[176:177], v[176:177], v[128:129], v[136:137]\n\t" \
  "v_pk_fma_f32 v[178:179], v[178:179], v[130:131], v[138:139]\n\t" \
  "v_pk_fma_f32 v[180:181], v[180:181], v[132:133], v[140:141]\n\t" \
  "v_pk_fma_f32 v[182:183], v[182:183], v[134:135], v[142:143]\n\t" \
  "v_cvt_pk_bf16_f32 v24, v176, v177\n\t" \
  "v_cvt_pk_bf16_f32 v25, v178, v179\n\t" \
  "v_cvt_pk_bf16_f32 v26, v180, v181\n\t" \
  "v_cvt_pk_bf16_f32 v27, v182, v183\n\t" \
  "buffer_load_dwordx4 v212, s[4:7], s96 offen lds\n\t" \
  "v_lshlrev_b32 v176, 16, v56\n\t" \
  "v_and_b32 v177, s98, v56\n\t" \
  "v_lshlrev_b32 v178, 16, v57\n\t" \
  "v_and_b32 v179, s98, v57\n\t" \
  "v_lshlrev_b32 v180, 16, v58\n\t" \
  "v_and_b32 v181, s98, v58\n\t" \
  "v_lshlrev_b32 v182, 16, v59\n\t" \
  "v_and_b32 v183, s98, v59\n\t" \
  "v_pk_fma_f32 v[176:177], v[176:177], v[164:165], v[166:167] op_sel_hi:[1,0,0]\n\t" \
  "v_pk_fma_f32 v[178:179], v[178:179], v[164:165], v[166:167] op_sel_hi:[1,0,0]\n\t" \
  "v_pk_fma_f32 v[180:181], v[180:181], v[164:165], v[166:167] op_sel_hi:[1,0,0]\n\t" \
  "v_pk_fma_f32 v[182:183], v[182:183], v[164:165], v[166:167] op_sel_hi:[1,0,0]\n\t" \
  "v_pk_fma_f32 v[176:177], v[176:177], v[128:129], v[136:137]\n\t" \
  "v_pk_fma_f32 v[178:179], v[178:179], v[130:131], v[138:139]\n\t" \
  "v_pk_fma_f32 v[180:181], v[180:181], v[132:133], v[140:141]\n\t" \
  "v_pk_fma_f32 v[182:183], v[182:183], v[134:135], v[142:143]\n\t" \
  "v_cvt_pk_bf16_f32 v56, v176, v177\n\t" \
  "v_cvt_pk_bf16_f32 v57, v178, v179\n\t" \
  "v_cvt_pk_bf16_f32 v58, v180, v181\n\t" \
  "v_cvt_pk_bf16_f32 v59, v182, v183\n\t" \
  "buffer_load_dwordx4 v213, s[4:7], s96 offen offset:1024 lds\n\t" \
  "s_add_u32 m0, s77, 104448\n\t" \
  "s_xor_b32 s96, s87, 393216\n\t" \
  "s_add_u32 s96, s96, s88\n\t" \
  "v_lshlrev_b32 v176, 16, v88\n\t" \
  "v_and_b32 v177, s98, v88\n\t" \
  "v_lshlrev_b32 v178, 16, v89\n\t" \
  "v_and_b32 v179, s98, v89\n\t" \
  "v_lshlrev_b32 v180, 16, v90\n\t" \
  "v_and_b32 v181, s98, v90\n\t" \
  "v_lshlrev_b32 v182, 16, v91\n\t" \
  "v_and_b32 v183, s98, v91\n\t" \
  "v_pk_fma_f32 v[176:177], v[176:177], v[168:169], v[170:171] op_sel_hi:[1,0,0]\n\t" \
  "v_pk_fma_f32 v[178:179], v[178:179], v[168:169], v[170:171] op_sel_hi:[1,0,0]\n\t" \
  "v_pk_fma_f32 v[180:181], v[180:181], v[168:169], v[170:171] op_sel_hi:[1,0,0]\n\t" \
  "v_pk_fma_f32 v[182:183], v[182:183], v[168:169], v[170:171] op_sel_hi:[1,0,0]\n\t" \
  "v_pk_fma_f32 v[176:177], v[176:177], v[128:129], v[136:137]\n\t" \
  "v_pk_fma_f32 v[178:179], v[178:179], v[130:131], v[138:139]\n\t" \
  "v_pk_fma_f32 v[180:181], v[180:181], v[132:133], v[140:141]\n\t" \
  "v_pk_fma_f32 v[182:183], v[182:183], v[134:135], v[142:143]\n\t" \
  "v_cvt_pk_bf16_f32 v88, v176, v177\n\t" \
  "v_cvt_pk_bf16_f32 v89, v178, v179\n\t" \
  "v_cvt_pk_bf16_f32 v90, v180, v181\n\t" \
  "v_cvt_pk_bf16_f32 v91, v182, v183\n\t" \
  "buffer_load_dwordx4 v212, s[4:7], s96 offen lds\n\t" \
  "v_lshlrev_b32 v176, 16, v120\n\t" \
  "v_and_b32 v177, s98, v120\n\t" \
  "v_lshlrev_b32 v178, 16, v121\n\t" \
  "v_and_b32 v179, s98, v121\n\t" \
  "v_lshlrev_b32 v180, 16, v122\n\t" \
  "v_and_b32 v181, s98, v122\n\t" \
  "v_lshlrev_b32 v182, 16, v123\n\t" \
  "v_and_b32 v183, s98, v123\n\t" \
  "v_pk_fma_f32 v[176:177], v[176:177], v[172:173], v[174:175] op_sel_hi:[1,0,0]\n\t" \
  "v_pk_fma_f32 v[178:179], v[178:179], v[172:173], v[174:175] op_sel_hi:[1,0,0]\n\t" \
  "v_pk_fma_f32 v[180:181], v[180:181], v[172:173], v[174:175] op_sel_hi:[1,0,0]\n\t" \
  "v_pk_fma_f32 v[182:183], v[182:183], v[172:173], v[174:175] op_sel_hi:[1,0,0]\n\t" \
  "v_pk_fma_f32 v[176:177], v[176:177], v[128:129], v[136:137]\n\t" \
  "v_pk_fma_f32 v[178:179], v[178:179], v[130:131], v[138:139]\n\t" \
  "v_pk_fma_f32 v[180:181], v[180:181], v[132:133], v[140:141]\n\t" \
  "v_pk_fma_f32 v[182:183], v[182:183], v[134:135], v[142:143]\n\t" \
  "v_cvt_pk_bf16_f32 v120, v176, v177\n\t" \
  "v_cvt_pk_bf16_f32 v121, v178, v179\n\t" \
  "v_cvt_pk_bf16_f32 v122, v180, v181\n\t" \
  "v_cvt_pk_bf16_f32 v123, v182, v183\n\t" \
  "buffer_load_dwordx4 v213, s[4:7], s96 offen offset:1024 lds\n\t" \
  "ds_read_b128 v[128:131], v208 offset:1024\n\t" \
  "ds_read_b128 v[132:135], v208 offset:1040\n\t" \
  "ds_read_b128 v[136:139], v208 offset:1280\n\t" \
  "ds_read_b128 v[140:143], v208 offset:1296\n\t" \
  FSTP("v_writelane_b32 v255, s94, 30\n\t") \
  FSTP("v_writelane_b32 v255, s92, 31\n\t") \
  FSTP("s_memrealtime s[94:95]\n\t") \
  FSTP("s_memtime s[92:93]\n\t") \
  "s_add_u32 m0, s76, 98304\n\t" \
  "v_lshlrev_b32 v176, 16, v28\n\t" \
  "v_and_b32 v177, s98, v28\n\t" \
  "v_lshlrev_b32 v178, 16, v29\n\t" \
  "v_and_b32 v179, s98, v29\n\t" \
  "v_lshlrev_b32 v180, 16, v30\n\t" \
  "v_and_b32 v181, s98, v30\n\t" \
  "v_lshlrev_b32 v182, 16, v31\n\t" \
  "v_and_b32 v183, s98, v31\n\t" \
  "v_pk_fma_f32 v[176:177], v[176:177], v[160:161], v[162:163] op_sel_hi:[1,0,0]\n\t" \
  "v_pk_fma_f32 v[178:179], v[178:179], v[160:161], v[162:163] op_sel_hi:[1,0,0]\n\t" \
  "v_pk_fma_f32 v[180:181], v[180:181], v[160:161], v[162:163] op_sel_hi:[1,0,0]\n\t" \
  "v_pk_fma_f32 v[182:183], v[182:183], v[160:161], v[162:163] op_sel_hi:[1,0,0]\n\t" \
  "s_waitcnt lgkmcnt(4)\n\t" \
  "v_pk_fma_f32 v[176:177], v[176:177], v[144:145], v[152:153]\n\t" \
  "v_pk_fma_f32 v[178:179], v[178:179], v[146:147], v[154:155]\n\t" \
  "v_pk_fma_f32 v[180:181], v[180:181], v[148:149], v[156:157]\n\t" \
  "v_pk_fma_f32 v[182:183], v[182:183], v[150:151], v[158:159]\n\t" \
  "v_cvt_pk_bf16_f32 v28, v176, v177\n\t" \
  "v_cvt_pk_bf16_f32 v29, v178, v179\n\t" \
  "v_cvt_pk_bf16_f32 v30, v180, v181\n\t" \
  "v_cvt_pk_bf16_f32 v31, v182, v183\n\t" \
  "buffer_load_dwordx4 v200, s[20:23], s80 offen offset:0 lds\n\t" \
  "ds_read_b64_tr_b16 a[128:129], v184 offset:0\n\t" \
  "ds_read_b64_tr_b16 a[130:131], v184 offset:1024\n\t" \
  "ds_read_b64_tr_b16 a[132:133], v185 offset:0\n\t" \
  "ds_read_b64_tr_b16 a[134:135], v185 offset:1024\n\t" \
  "v_lshlrev_b32 v176, 16, v60\n\t" \
  "v_and_b32 v177, s98, v60\n\t" \
  "v_lshlrev_b32 v178, 16, v61\n\t" \
  "v_and_b32 v179, s98, v61\n\t" \
  "v_lshlrev_b32 v180, 16, v62\n\t" \
  "v_and_b32 v181, s98, v62\n\t" \
  "v_lshlrev_b32 v182, 16, v63\n\t" \
  "v_and_b32 v183, s98, v63\n\t" \
  "v_pk_fma_f32 v[176:177], v[176:177], v[164:165], v[166:167] op_sel_hi:[1,0,0]\n\t" \
  "v_pk_fma_f32 v[178:179], v[178:179], v[164:165], v[166:167] op_sel_hi:[1,0,0]\n\t" \
  "v_pk_fma_f32 v[180:181], v[180:181], v[164:165], v[166:167] op_sel_hi:[1,0,0]\n\t" \
  "v_pk_fma_f32 v[182:183], v[182:183], v[164:165], v[166:167] op_sel_hi:[1,0,0]\n\t" \
  "v_pk_fma_f32 v[176:177], v[176:177], v[144:145], v[152:153]\n\t" \
  "v_pk_fma_f32 v[178:179], v[178:179], v[146:147], v[154:155]\n\t" \
  "v_pk_fma_f32 v[180:181], v[180:181], v[148:149], v[156:157]\n\t" \
  "v_pk_fma_f32 v[182:183], v[182:183], v[150:151], v[158:159]\n\t" \
  "v_cvt_pk_bf16_f32 v60, v176, v177\n\t" \
  "v_cvt_pk_bf16_f32 v61, v178, v179\n\t" \
  "v_cvt_pk_bf16_f32 v62, v180, v181\n\t" \
  "v_cvt_pk_bf16_f32 v63, v182, v183\n\t" \
  "buffer_load_dwordx4 v201, s[20:23], s80 offen offset:1024 lds\n\t" \
  "ds_read_b64_tr_b16 a[136:137], v186 offset:0\n\t" \
  "ds_read_b64_tr_b16 a[138:139], v186 offset:1024\n\t" \
  "ds_read_b64_tr_b16 a[140:141], v187 offset:0\n\t" \
  "ds_read_b64_tr_b16 a[142:143], v187 offset:1024\n\t" \
  "v_lshlrev_b32 v176, 16, v92\n\t" \
  "v_and_b32 v177, s98, v92\n\t" \
  "v_lshlrev_b32 v178, 16, v93\n\t" \
  "v_and_b32 v179, s98, v93\n\t" \
  "v_lshlrev_b32 v180, 16, v94\n\t" \
  "v_and_b32 v181, s98, v94\n\t" \
  "v_lshlrev_b32 v182, 16, v95\n\t" \
  "v_and_b32 v183, s98, v95\n\t" \
  "v_pk_fma_f32 v[176:177], v[176:177], v[168:169], v[170:171] op_sel_hi:[1,0,0]\n\t" \
  "v_pk_fma_f32 v[178:179], v[178:179], v[168:169], v[170:171] op_sel_hi:[1,0,0]\n\t" \
  "v_pk_fma_f32 v[180:181], v[180:181], v[168:169], v[170:171] op_sel_hi:[1,0,0]\n\t" \
  "v_pk_fma_f32 v[182:183], v[182:183], v[168:169], v[170:171] op_sel_hi:[1,0,0]\n\t" \
  "v_pk_fma_f32 v[176:177], v[176:177], v[144:145], v[152:153]\n\t" \
  "v_pk_fma_f32 v[178:179], v[178:179], v[146:147], v[154:155]\n\t" \
  "v_pk_fma_f32 v[180:181], v[180:181], v[148:149], v[156:157]\n\t" \
  "v_pk_fma_f32 v[182:183], v[182:183], v[150:151], v[158:159]\n\t" \
  "v_cvt_pk_bf16_f32 v92, v176, v177\n\t" \
  "v_cvt_pk_bf16_f32 v93, v178, v179\n\t" \
  "v_cvt_pk_bf16_f32 v94, v180, v181\n\t" \
  "v_cvt_pk_bf16_f32 v95, v182, v183\n\t" \
  "buffer_load_dwordx4 v202, s[20:23], s80 offen offset:2048 lds\n\t" \
  "ds_read_b64_tr_b16 a[144:145], v188 offset:0\n\t" \
  "ds_read_b64_tr_b16 a[146:147], v188 offset:1024\n\t" \
  "ds_read_b64_tr_b16 a[148:149], v189 offset:0\n\t" \
  "ds_read_b64_tr_b16 a[150:151], v189 offset:1024\n\t" \
  "v_lshlrev_b32 v176, 16, v124\n\t" \
  "v_and_b32 v177, s98, v124\n\t" \
  "v_lshlrev_b32 v178, 16, v125\n\t" \
  "v_and_b32 v179, s98, v125\n\t" \
  "v_lshlrev_b32 v180, 16, v126\n\t" \
  "v_and_b32 v181, s98, v126\n\t" \
  "v_lshlrev_b32 v182, 16, v127\n\t" \
  "v_and_b32 v183, s98, v127\n\t" \
  "v_pk_fma_f32 v[176:177], v[176:177], v[172:173], v[174:175] op_sel_hi:[1,0,0]\n\t" \
  "v_pk_fma_f32 v[178:179], v[178:179], v[172:173], v[174:175] op_sel_hi:[1,0,0]\n\t" \
  "v_pk_fma_f32 v[180:181], v[180:181], v[172:173], v[174:175] op_sel_hi:[1,0,0]\n\t" \
  "v_pk_fma_f32 v[182:183], v[182:183], v[172:173], v[174:175] op_sel_hi:[1,0,0]\n\t" \
  "v_pk_fma_f32 v[176:177], v[176:177], v[144:145], v[152:153]\n\t" \
  "v_pk_fma_f32 v[178:179], v[178:179], v[146:147], v[154:155]\n\t" \
  "v_pk_fma_f32 v[180:181], v[180:181], v[148:149], v[156:157]\n\t" \
  "v_pk_fma_f32 v[182:183], v[182:183], v[150:151], v[158:159]\n\t" \
  "v_cvt_pk_bf16_f32 v124, v176, v177\n\t" \
  "v_cvt_pk_bf16_f32 v125, v178, v179\n\t" \
  "v_cvt_pk_bf16_f32 v126, v180, v181\n\t" \
  "v_cvt_pk_bf16_f32 v127, v182, v183\n\t" \
  "buffer_load_dwordx4 v203, s[20:23], s80 offen offset:3072 lds\n\t" \
  "ds_read_b64_tr_b16 a[152:153], v190 offset:0\n\t" \
  "ds_read_b64_tr_b16 a[154:155], v190 offset:1024\n\t" \
  "ds_read_b64_tr_b16 a[156:157], v191 offset:0\n\t" \
  "ds_read_b64_tr_b16 a[158:159], v191 offset:1024\n\t" \
  "ds_read_b128 v[144:147], v208 offset:1152\n\t" \
  "ds_read_b128 v[148:151], v208 offset:1168\n\t" \
  "ds_read_b128 v[152:155], v208 offset:1408\n\t" \
  "ds_read_b128 v[156:159], v208 offset:1424\n\t" \
  FSTP("v_writelane_b32 v255, s94, 32\n\t") \
  FSTP("v_writelane_b32 v255, s92, 33\n\t") \
  FSTP("s_memrealtime s[94:95]\n\t") \
  FSTP("s_memtime s[92:93]\n\t") \
  "s_waitcnt vmcnt(12)\n\t" \
  "s_mov_b64 exec, 1\n\t" \
  "buffer_store_dword v211, off, s[16:19], s85 sc1\n\t" \
  "s_mov_b64 exec, -1\n\t" \
  FSTP("v_writelane_b32 v255, s94, 34\n\t") \
  FSTP("v_writelane_b32 v255, s92, 35\n\t") \
  FSTP("s_memrealtime s[94:95]\n\t") \
  FSTP("s_memtime s[92:93]\n\t") \
  "s_waitcnt lgkmcnt(4)\n\t" \
  "v_mfma_f32_16x16x32_bf16 v[0:3], a[128:131], v[16:19], 0\n\t" \
  "s_add_u32 m0, s77, 0\n\t" \
  "s_xor_b32 s96, s87, 0\n\t" \
  "s_add_u32 s96, s96, s89\n\t" \
  "v_mfma_f32_16x16x32_bf16 v[32:35], a[128:131], v[48:51], 0\n\t" \
  "buffer_load_dwordx4 v212, s[4:7], s96 offen lds\n\t" \
  "v_mfma_f32_16x16x32_bf16 v[64:67], a[128:131], v[80:83], 0\n\t" \
  "ds_read_b64_tr_b16 a[160:161], v184 offset:8192\n\t" \
  "v_mfma_f32_16x16x32_bf16 v[96:99], a[128:131], v[112:115], 0\n\t" \
  "ds_read_b64_tr_b16 a[162:163], v184 offset:9216\n\t" \
  "v_mfma_f32_16x16x32_bf16 v[100:103], a[132:135], v[112:115], 0\n\t" \
  "buffer_load_dwordx4 v213, s[4:7], s96 offen offset:1024 lds\n\t" \
  "v_mfma_f32_16x16x32_bf16 v[68:71], a[132:135], v[80:83], 0\n\t" \
  "s_add_u32 m0, s77, 2048\n\t" \
  "ds_read_b64_tr_b16 a[164:165], v185 offset:8192\n\t" \
  "s_xor_b32 s96, s87, 131072\n\t" \
  "v_mfma_f32_16x16x32_bf16 v[36:39], a[132:135], v[48:51], 0\n\t" \
  "ds_read_b64_tr_b16 a[166:167], v185 offset:9216\n\t" \
  "s_add_u32 s96, s96, s89\n\t" \
  "v_mfma_f32_16x16x32_bf16 v[4:7], a[132:135], v[16:19], 0\n\t" \
  "ds_read_b64_tr_b16 a[168:169], v186 offset:8192\n\t" \
  "v_mfma_f32_16x16x32_bf16 v[8:11], a[136:139], v[16:19], 0\n\t" \
  "buffer_load_dwordx4 v212, s[4:7], s96 offen lds\n\t" \
  "v_mfma_f32_16x16x32_bf16 v[40:43], a[136:139], v[48:51], 0\n\t" \
  "ds_read_b64_tr_b16 a[170:171], v186 offset:9216\n\t" \
  "v_mfma_f32_16x16x32_bf16 v[72:75], a[136:139], v[80:83], 0\n\t" \
  "ds_read_b64_tr_b16 a[172:173], v187 offset:8192\n\t" \
  "v_mfma_f32_16x16x32_bf16 v[104:107], a[136:139], v[112:115], 0\n\t" \
  "buffer_load_dwordx4 v213, s[4:7], s96 offen offset:1024 lds\n\t" \
  "v_mfma_f32_16x16x32_bf16 v[108:111], a[140:143], v[112:115], 0\n\t" \
  "s_add_u32 m0, s77, 4096\n\t" \
  "ds_read_b64_tr_b16 a[174:175], v187 offset:9216\n\t" \
  "s_xor_b32 s96, s87, 262144\n\t" \
  "v_mfma_f32_16x16x32_bf16 v[76:79], a[140:143], v[80:83], 0\n\t" \
  "ds_read_b64_tr_b16 a[176:177], v188 offset:8192\n\t" \
  "s_add_u32 s96, s96, s89\n\t" \
  "v_mfma_f32_16x16x32_bf16 v[44:47], a[140:143], v[48:51], 0\n\t" \
  "buffer_load_dwordx4 v212, s[4:7], s96 offen lds\n\t" \
  "v_mfma_f32_16x16x32_bf16 v[12:15], a[140:143], v[16:19], 0\n\t" \
  "ds_read_b64_tr_b16 a[178:179], v188 offset:9216\n\t" \
  "v_mfma_f32_16x16x32_bf16 a[64:67], a[144:147], v[16:19], 0\n\t" \
  "ds_read_b64_tr_b16 a[180:181], v189 offset:8192\n\t" \
  "v_mfma_f32_16x16x32_bf16 a[68:71], a[144:147], v[48:51], 0\n\t" \
  "buffer_load_dwordx4 v213, s[4:7], s96 offen offset:1024 lds\n\t" \
  "v_mfma_f32_16x16x32_bf16 a[72:75], a[144:147], v[80:83], 0\n\t" \
  "s_add_u32 m0, s77, 6144\n\t" \
  "ds_read_b64_tr_b16 a[182:183], v189 offset:9216\n\t" \
  "s_xor_b32 s96, s87, 393216\n\t" \
  "v_mfma_f32_16x16x32_bf16 a[76:79], a[144:147], v[112:115], 0\n\t" \
  "ds_read_b64_tr_b16 a[184:185], v190 offset:8192\n\t" \
  "s_add_u32 s96, s96, s89\n\t" \
  "v_mfma_f32_16x16x32_bf16 a[92:95], a[148:151], v[112:115], 0\n\t" \
  "ds_read_b64_tr_b16 a[186:187], v190 offset:9216\n\t" \
  "v_mfma_f32_16x16x32_bf16 a[88:91], a[148:151], v[80:83], 0\n\t" \
  "buffer_load_dwordx4 v212, s[4:7], s96 offen lds\n\t" \
  "v_mfma_f32_16x16x32_bf16 a[84:87], a[148:151], v[48:51], 0\n\t" \
  "ds_read_b64_tr_b16 a[188:189], v191 offset:8192\n\t" \
  "v_mfma_f32_16x16x32_bf16 a[80:83], a[148:151], v[16:19], 0\n\t" \
  "ds_read_b64_tr_b16 a[190:191], v191 offset:9216\n\t" \
  "v_mfma_f32_16x16x32_bf16 a[96:99], a[152:155], v[16:19], 0\n\t" \
  "v_mfma_f32_16x16x32_bf16 a[100:103], a[152:155], v[48:51], 0\n\t" \
  "buffer_load_dwordx4 v213, s[4:7], s96 offen offset:1024 lds\n\t" \
  "v_mfma_f32_16x16x32_bf16 a[104:107], a[152:155], v[80:83], 0\n\t" \
  "v_mfma_f32_16x16x32_bf16 a[108:111], a[152:155], v[112:115], 0\n\t" \
  "v_mfma_f32_16x16x32_bf16 a[124:127], a[156:159], v[112:115], 0\n\t" \
  "v_mfma_f32_16x16x32_bf16 a[120:123], a[156:159], v[80:83], 0\n\t" \
  "s_add_u32 m0, s76, 0\n\t" \
  "v_mfma_f32_16x16x32_bf16 a[116:119], a[156:159], v[48:51], 0\n\t" \
  "s_waitcnt lgkmcnt(0)\n\t" \
  "v_mfma_f32_16x16x32_bf16 a[112:115], a[156:159], v[16:19], 0\n\t" \
  "s_waitcnt vmcnt(13)\n\t" \
  "ds_read_b128 v[16:19], v206 offset:0\n\t" \
  "ds_read_b128 v[48:51], v206 offset:2048\n\t" \
  "s_barrier\n\t" \
  FSTP("v_writelane_b32 v255, s94, 36\n\t") \
  FSTP("v_writelane_b32 v255, s92, 37\n\t") \
  FSTP("s_memrealtime s[94:95]\n\t") \
  FSTP("s_memtime s[92:93]\n\t") \
  "v_mfma_f32_16x16x32_bf16 v[0:3], a[160:163], v[20:23], v[0:3]\n\t" \
  "buffer_load_dwordx4 v200, s[20:23], s81 offen offset:0 lds\n\t" \
  "ds_read_b128 v[80:83], v206 offset:4096\n\t" \
  "ds_read_b128 v[112:115], v206 offset:6144\n\t" \
  "v_mfma_f32_16x16x32_bf16 v[32:35], a[160:163], v[52:55], v[32:35]\n\t" \
  "ds_read_b64_tr_b16 a[128:129], v184 offset:49152\n\t" \
  "v_mfma_f32_16x16x32_bf16 v[64:67], a[160:163], v[84:87], v[64:67]\n\t" \
  "ds_read_b64_tr_b16 a[130:131], v184 offset:50176\n\t" \
  "v_mfma_f32_16x16x32_bf16 v[96:99], a[160:163], v[116:119], v[96:99]\n\t" \
  "buffer_load_dwordx4 v201, s[20:23], s81 offen offset:1024 lds\n\t" \
  "v_mfma_f32_16x16x32_bf16 v[100:103], a[164:167], v[116:119], v[100:103]\n\t" \
  "ds_read_b64_tr_b16 a[132:133], v185 offset:49152\n\t" \
  "v_mfma_f32_16x16x32_bf16 v[68:71], a[164:167], v[84:87], v[68:71]\n\t" \
  "s_waitcnt lgkmcnt(3)\n\t" \
  "v_lshlrev_b32 v176, 16, v16\n\t" \
  "v_and_b32 v177, s98, v16\n\t" \
  "v_mfma_f32_16x16x32_bf16 v[36:39], a[164:167], v[52:55], v[36:39]\n\t" \
  "ds_read_b64_tr_b16 a[134:135], v185 offset:50176\n\t" \
  "v_lshlrev_b32 v178, 16, v17\n\t" \
  "v_mfma_f32_16x16x32_bf16 v[4:7], a[164:167], v[20:23], v[4:7]\n\t" \
  "v_and_b32 v179, s98, v17\n\t" \
  "v_lshlrev_b32 v180, 16, v18\n\t" \
  "v_mfma_f32_16x16x32_bf16 v[8:11], a[168:171], v[20:23], v[8:11]\n\t" \
  "buffer_load_dwordx4 v202, s[20:23], s81 offen offset:2048 lds\n\t" \
  "v_mfma_f32_16x16x32_bf16 v[40:43], a[168:171], v[52:55], v[40:43]\n\t" \
  "ds_read_b64_tr_b16 a[136:137], v186 offset:49152\n\t" \
  "v_and_b32 v181, s98, v18\n\t" \
  "v_mfma_f32_16x16x32_bf16 v[72:75], a[168:171], v[84:87], v[72:75]\n\t" \
  "ds_read_b64_tr_b16 a[138:139], v186 offset:50176\n\t" \
  "v_lshlrev_b32 v182, 16, v19\n\t" \
  "v_mfma_f32_16x16x32_bf16 v[104:107], a[168:171], v[116:119], v[104:107]\n\t" \
  "ds_read_b64_tr_b16 a[140:141], v187 offset:49152\n\t" \
  "v_and_b32 v183, s98, v19\n\t" \
  "v_mfma_f32_16x16x32_bf16 v[108:111], a[172:175], v[116:119], v[108:111]\n\t" \
  "buffer_load_dwordx4 v203, s[20:23], s81 offen offset:3072 lds\n\t" \
  "v_mfma_f32_16x16x32_bf16 v[76:79], a[172:175], v[84:87], v[76:79]\n\t" \
  "s_add_u32 s96, s78, 2097152\n\t" \
  "s_and_b32 s96, s96, 0x780000\n\t" \
  "s_add_u32 m0, s76, 16384\n\t" \
  "v_mfma_f32_16x16x32_bf16 v[44:47], a[172:175], v[52:55], v[44:47]\n\t" \
  "ds_read_b64_tr_b16 a[142:143], v187 offset:50176\n\t" \
  "v_lshlrev_b32 v220, 16, v48\n\t" \
  "v_and_b32 v221, s98, v48\n\t" \
  "v_mfma_f32_16x16x32_bf16 v[12:15], a[172:175], v[20:23], v[12:15]\n\t" \
  "buffer_load_dwordx4 v200, s[20:23], s96 offen offset:0 lds\n\t" \
  "v_mfma_f32_16x16x32_bf16 a[64:67], a[176:179], v[20:23], a[64:67]\n\t" \
  "ds_read_b64_tr_b16 a[144:145], v188 offset:49152\n\t" \
  "v_lshlrev_b32 v222, 16, v49\n\t" \
  "v_mfma_f32_16x16x32_bf16 a[68:71], a[176:179], v[52:55], a[68:71]\n\t" \
  "ds_read_b64_tr_b16 a[146:147], v188 offset:50176\n\t" \
  "v_and_b32 v223, s98, v49\n\t" \
  "v_mfma_f32_16x16x32_bf16 a[72:75], a[176:179], v[84:87], a[72:75]\n\t" \
  "ds_read_b64_tr_b16 a[148:149], v189 offset:49152\n\t" \
  "v_lshlrev_b32 v224, 16, v50\n\t" \
  "v_mfma_f32_16x16x32_bf16 a[76:79], a[176:179], v[116:119], a[76:79]\n\t" \
  "buffer_load_dwordx4 v201, s[20:23], s96 offen offset:1024 lds\n\t" \
  "v_mfma_f32_16x16x32_bf16 a[92:95], a[180:183], v[116:119], a[92:95]\n\t" \
  "ds_read_b64_tr_b16 a[150:151], v189 offset:50176\n\t" \
  "v_and_b32 v225, s98, v50\n\t" \
  "v_mfma_f32_16x16x32_bf16 a[88:91], a[180:183], v[84:87], a[88:91]\n\t" \
  "ds_read_b64_tr_b16 a[152:153], v190 offset:49152\n\t" \
  "v_lshlrev_b32 v226, 16, v51\n\t" \
  "v_mfma_f32_16x16x32_bf16 a[84:87], a[180:183], v[52:55], a[84:87]\n\t" \
  "ds_read_b64_tr_b16 a[154:155], v190 offset:50176\n\t" \
  "v_and_b32 v227, s98, v51\n\t" \
  "v_mfma_f32_16x16x32_bf16 a[80:83], a[180:183], v[20:23], a[80:83]\n\t" \
  "buffer_load_dwordx4 v202, s[20:23], s96 offen offset:2048 lds\n\t" \
  "ds_read_b64_tr_b16 a[156:157], v191 offset:49152\n\t" \
  "ds_read_b64_tr_b16 a[158:159], v191 offset:50176\n\t" \
  "v_lshlrev_b32 v228, 16, v80\n\t" \
  "v_pk_fma_f32 v[176:177], v[176:177], v[160:161], v[162:163] op_sel_hi:[1,0,0]\n\t" \
  "v_pk_fma_f32 v[178:179], v[178:179], v[160:161], v[162:163] op_sel_hi:[1,0,0]\n\t" \
  "v_pk_fma_f32 v[180:181], v[180:181], v[160:161], v[162:163] op_sel_hi:[1,0,0]\n\t" \
  "v_pk_fma_f32 v[182:183], v[182:183], v[160:161], v[162:163] op_sel_hi:[1,0,0]\n\t" \
  "v_pk_fma_f32 v[176:177], v[176:177], v[128:129], v[136:137]\n\t" \
  "v_pk_fma_f32 v[178:179], v[178:179], v[130:131], v[138:139]\n\t" \
  "v_pk_fma_f32 v[180:181], v[180:181], v[132:133], v[140:141]\n\t" \
  "v_pk_fma_f32 v[182:183], v[182:183], v[134:135], v[142:143]\n\t" \
  "v_pk_fma_f32 v[220:221], v[220:221], v[164:165], v[166:167] op_sel_hi:[1,0,0]\n\t" \
  "v_pk_fma_f32 v[222:223], v[222:223], v[164:165], v[166:167] op_sel_hi:[1,0,0]\n\t" \
  "v_pk_fma_f32 v[224:225], v[224:225], v[164:165], v[166:167] op_sel_hi:[1,0,0]\n\t" \
  "v_pk_fma_f32 v[226:227], v[226:227], v[164:165], v[166:167] op_sel_hi:[1,0,0]\n\t" \
  "v_pk_fma_f32 v[220:221], v[220:221], v[128:129], v[136:137]\n\t" \
  "v_pk_fma_f32 v[222:223], v[222:223], v[130:131], v[138:139]\n\t" \
  "v_pk_fma_f32 v[224:225], v[224:225], v[132:133], v[140:141]\n\t" \
  "v_pk_fma_f32 v[226:227], v[226:227], v[134:135], v[142:143]\n\t" \
  "v_cvt_pk_bf16_f32 v16, v176, v177\n\t" \
  "v_mfma_f32_16x16x32_bf16 a[96:99], a[184:187], v[20:23], a[96:99]\n\t" \
  "v_cvt_pk_bf16_f32 v17, v178, v179\n\t" \
  "v_cvt_pk_bf16_f32 v18, v180, v181\n\t" \
  "v_and_b32 v229, s98, v80\n\t" \
  "v_mfma_f32_16x16x32_bf16 a[100:103], a[184:187], v[52:55], a[100:103]\n\t" \
  "v_cvt_pk_bf16_f32 v19, v182, v183\n\t" \
  "v_cvt_pk_bf16_f32 v48, v220, v221\n\t" \
  "v_lshlrev_b32 v230, 16, v81\n\t" \
  "v_and_b32 v231, s98, v81\n\t" \
  "v_mfma_f32_16x16x32_bf16 a[104:107], a[184:187], v[84:87], a[104:107]\n\t" \
  "v_lshlrev_b32 v232, 16, v82\n\t" \
  "v_and_b32 v233, s98, v82\n\t" \
  "v_mfma_f32_16x16x32_bf16 a[108:111], a[184:187], v[116:119], a[108:111]\n\t" \
  "buffer_load_dwordx4 v203, s[20:23], s96 offen offset:3072 lds\n\t" \
  "v_mfma_f32_16x16x32_bf16 a[124:127], a[188:191], v[116:119], a[124:127]\n\t" \
  "v_lshlrev_b32 v234, 16, v83\n\t" \
  "v_and_b32 v235, s98, v83\n\t" \
  "v_mfma_f32_16x16x32_bf16 a[120:123], a[188:191], v[84:87], a[120:123]\n\t" \
  "s_add_u32 s96, s78, 2621440\n\t" \
  "v_lshlrev_b32 v236, 16, v112\n\t" \
  "v_and_b32 v237, s98, v112\n\t" \
  "v_mfma_f32_16x16x32_bf16 a[116:119], a[188:191], v[52:55], a[116:119]\n\t" \
  "s_and_b32 s96, s96, 0x780000\n\t" \
  "s_add_u32 m0, s76, 32768\n\t" \
  "v_lshlrev_b32 v238, 16, v113\n\t" \
  "v_mfma_f32_16x16x32_bf16 a[112:115], a[188:191], v[20:23], a[112:115]\n\t" \
  "s_waitcnt lgkmcnt(0)\n\t" \
  "ds_read_b128 v[20:23], v207 offset:0\n\t" \
  "ds_read_b128 v[52:55], v207 offset:2048\n\t" \
  "ds_read_b128 v[84:87], v207 offset:4096\n\t" \
  "v_and_b32 v239, s98, v113\n\t" \
  FSTP("v_writelane_b32 v255, s94, 38\n\t") \
  FSTP("v_writelane_b32 v255, s92, 39\n\t") \
  FSTP("s_memrealtime s[94:95]\n\t") \
  FSTP("s_memtime s[92:93]\n\t") \
  "v_mfma_f32_16x16x32_bf16 v[0:3], a[128:131], v[24:27], v[0:3]\n\t" \
  "buffer_load_dwordx4 v200, s[20:23], s96 offen offset:0 lds\n\t" \
  "ds_read_b128 v[116:119], v207 offset:6144\n\t" \
  "v_mfma_f32_16x16x32_bf16 v[32:35], a[128:131], v[56:59], v[32:35]\n\t" \
  "v_lshlrev_b32 v240, 16, v114\n\t" \
  "v_and_b32 v241, s98, v114\n\t" \
  "v_mfma_f32_16x16x32_bf16 v[64:67], a[128:131], v[88:91], v[64:67]\n\t" \
  "v_lshlrev_b32 v242, 16, v115\n\t" \
  "v_and_b32 v243, s98, v115\n\t" \
  "v_mfma_f32_16x16x32_bf16 v[96:99], a[128:131], v[120:123], v[96:99]\n\t" \
  "ds_read_b64_tr_b16 a[160:161], v184 offset:57344\n\t" \
  "ds_read_b64_tr_b16 a[162:163], v184 offset:58368\n\t" \
  "v_cvt_pk_bf16_f32 v49, v222, v223\n\t" \
  "v_cvt_pk_bf16_f32 v50, v224, v225\n\t" \
  "v_cvt_pk_bf16_f32 v51, v226, v227\n\t" \
  "v_pk_fma_f32 v[228:229], v[228:229], v[168:169], v[170:171] op_sel_hi:[1,0,0]\n\t" \
  "v_pk_fma_f32 v[230:231], v[230:231], v[168:169], v[170:171] op_sel_hi:[1,0,0]\n\t" \
  "v_pk_fma_f32 v[232:233], v[232:233], v[168:169], v[170:171] op_sel_hi:[1,0,0]\n\t" \
  "v_pk_fma_f32 v[234:235], v[234:235], v[168:169], v[170:171] op_sel_hi:[1,0,0]\n\t" \
  "v_pk_fma_f32 v[228:229], v[228:229], v[128:129], v[136:137]\n\t" \
  "v_pk_fma_f32 v[230:231], v[230:231], v[130:131], v[138:139]\n\t" \
  "v_pk_fma_f32 v[232:233], v[232:233], v[132:133], v[140:141]\n\t" \
  "v_pk_fma_f32 v[234:235], v[234:235], v[134:135], v[142:143]\n\t" \
  "v_pk_fma_f32 v[236:237], v[236:237], v[172:173], v[174:175] op_sel_hi:[1,0,0]\n\t" \
  "v_pk_fma_f32 v[238:239], v[238:239], v[172:173], v[174:175] op_sel_hi:[1,0,0]\n\t" \
  "v_pk_fma_f32 v[240:241], v[240:241], v[172:173], v[174:175] op_sel_hi:[1,0,0]\n\t" \
  "v_pk_fma_f32 v[242:243], v[242:243], v[172:173], v[174:175] op_sel_hi:[1,0,0]\n\t" \
  "v_pk_fma_f32 v[236:237], v[236:237], v[128:129], v[136:137]\n\t" \
  "v_pk_fma_f32 v[238:239], v[238:239], v[130:131], v[138:139]\n\t" \
  "v_pk_fma_f32 v[240:241], v[240:241], v[132:133], v[140:141]\n\t" \
  "v_pk_fma_f32 v[242:243], v[242:243], v[134:135], v[142:143]\n\t" \
  "v_cvt_pk_bf16_f32 v80, v228, v229\n\t" \
  "v_mfma_f32_16x16x32_bf16 v[100:103], a[132:135], v[120:123], v[100:103]\n\t" \
  "ds_read_b64_tr_b16 a[164:165], v185 offset:57344\n\t" \
  "v_cvt_pk_bf16_f32 v81, v230, v231\n\t" \
  "v_mfma_f32_16x16x32_bf16 v[68:71], a[132:135], v[88:91], v[68:71]\n\t" \
  "s_waitcnt lgkmcnt(3)\n\t" \
  "v_lshlrev_b32 v176, 16, v20\n\t" \
  "v_and_b32 v177, s98, v20\n\t" \
  "v_mfma_f32_16x16x32_bf16 v[36:39], a[132:135], v[56:59], v[36:39]\n\t" \
  "buffer_load_dwordx4 v201, s[20:23], s96 offen offset:1024 lds\n\t" \
  "v_mfma_f32_16x16x32_bf16 v[4:7], a[132:135], v[24:27], v[4:7]\n\t" \
  "ds_read_b64_tr_b16 a[166:167], v185 offset:58368\n\t" \
  "v_lshlrev_b32 v178, 16, v21\n\t" \
  "v_mfma_f32_16x16x32_bf16 v[8:11], a[136:139], v[24:27], v[8:11]\n\t" \
  "ds_read_b64_tr_b16 a[168:169], v186 offset:57344\n\t" \
  "v_and_b32 v179, s98, v21\n\t" \
  "v_mfma_f32_16x16x32_bf16 v[40:43], a[136:139], v[56:59], v[40:43]\n\t" \
  "v_lshlrev_b32 v180, 16, v22\n\t" \
  "v_and_b32 v181, s98, v22\n\t" \
  "v_mfma_f32_16x16x32_bf16 v[72:75], a[136:139], v[88:91], v[72:75]\n\t" \
  "ds_read_b64_tr_b16 a[170:171], v186 offset:58368\n\t" \
  "v_lshlrev_b32 v182, 16, v23\n\t" \
  "v_mfma_f32_16x16x32_bf16 v[104:107], a[136:139], v[120:123], v[104:107]\n\t" \
  "ds_read_b64_tr_b16 a[172:173], v187 offset:57344\n\t" \
  "v_and_b32 v183, s98, v23\n\t" \
  "v_mfma_f32_16x16x32_bf16 v[108:111], a[140:143], v[120:123], v[108:111]\n\t" \
  "buffer_load_dwordx4 v202, s[20:23], s96 offen offset:2048 lds\n\t" \
  "v_mfma_f32_16x16x32_bf16 v[76:79], a[140:143], v[88:91], v[76:79]\n\t" \
  "ds_read_b64_tr_b16 a[174:175], v187 offset:58368\n\t" \
  "v_lshlrev_b32 v220, 16, v52\n\t" \
  "v_mfma_f32_16x16x32_bf16 v[44:47], a[140:143], v[56:59], v[44:47]\n\t" \
  "v_and_b32 v221, s98, v52\n\t" \
  "v_lshlrev_b32 v222, 16, v53\n\t" \
  "v_mfma_f32_16x16x32_bf16 v[12:15], a[140:143], v[24:27], v[12:15]\n\t" \
  "ds_read_b64_tr_b16 a[176:177], v188 offset:57344\n\t" \
  "v_and_b32 v223, s98, v53\n\t" \
  "v_mfma_f32_16x16x32_bf16 a[64:67], a[144:147], v[24:27], a[64:67]\n\t" \
  "ds_read_b64_tr_b16 a[178:179], v188 offset:58368\n\t" \
  "v_lshlrev_b32 v224, 16, v54\n\t" \
  "v_mfma_f32_16x16x32_bf16 a[68:71], a[144:147], v[56:59], a[68:71]\n\t" \
  "ds_read_b64_tr_b16 a[180:181], v189 offset:57344\n\t" \
  "v_and_b32 v225, s98, v54\n\t" \
  "v_mfma_f32_16x16x32_bf16 a[72:75], a[144:147], v[88:91], a[72:75]\n\t" \
  "buffer_load_dwordx4 v203, s[20:23], s96 offen offset:3072 lds\n\t" \
  "v_mfma_f32_16x16x32_bf16 a[76:79], a[144:147], v[120:123], a[76:79]\n\t" \
  "ds_read_b64_tr_b16 a[182:183], v189 offset:58368\n\t" \
  "v_cvt_pk_bf16_f32 v82, v232, v233\n\t" \
  "v_mfma_f32_16x16x32_bf16 a[92:95], a[148:151], v[120:123], a[92:95]\n\t" \
  "v_lshlrev_b32 v226, 16, v55\n\t" \
  "v_and_b32 v227, s98, v55\n\t" \
  "v_mfma_f32_16x16x32_bf16 a[88:91], a[148:151], v[88:91], a[88:91]\n\t" \
  "ds_read_b64_tr_b16 a[184:185], v190 offset:57344\n\t" \
  "v_cvt_pk_bf16_f32 v83, v234, v235\n\t" \
  "v_cvt_pk_bf16_f32 v112, v236, v237\n\t" \
  "v_cvt_pk_bf16_f32 v113, v238, v239\n\t" \
  "v_pk_fma_f32 v[176:177], v[176:177], v[160:161], v[162:163] op_sel_hi:[1,0,0]\n\t" \
  "v_pk_fma_f32 v[178:179], v[178:179], v[160:161], v[162:163] op_sel_hi:[1,0,0]\n\t" \
  "v_pk_fma_f32 v[180:181], v[180:181], v[160:161], v[162:163] op_sel_hi:[1,0,0]\n\t" \
  "v_pk_fma_f32 v[182:183], v[182:183], v[160:161], v[162:163] op_sel_hi:[1,0,0]\n\t" \
  "v_pk_fma_f32 v[176:177], v[176:177], v[144:145], v[152:153]\n\t" \
  "v_pk_fma_f32 v[178:179], v[178:179], v[146:147], v[154:155]\n\t" \
  "v_pk_fma_f32 v[180:181], v[180:181], v[148:149], v[156:157]\n\t" \
  "v_pk_fma_f32 v[182:183], v[182:183], v[150:151], v[158:159]\n\t" \
  "v_pk_fma_f32 v[220:221], v[220:221], v[164:165], v[166:167] op_sel_hi:[1,0,0]\n\t" \
  "v_pk_fma_f32 v[222:223], v[222:223], v[164:165], v[166:167] op_sel_hi:[1,0,0]\n\t" \
  "v_pk_fma_f32 v[224:225], v[224:225], v[164:165], v[166:167] op_sel_hi:[1,0,0]\n\t" \
  "v_pk_fma_f32 v[226:227], v[226:227], v[164:165], v[166:167] op_sel_hi:[1,0,0]\n\t" \
  "v_pk_fma_f32 v[220:221], v[220:221], v[144:145], v[152:153]\n\t" \
  "v_pk_fma_f32 v[222:223], v[222:223], v[146:147], v[154:155]\n\t" \
  "v_pk_fma_f32 v[224:225], v[224:225], v[148:149], v[156:157]\n\t" \
  "v_pk_fma_f32 v[226:227], v[226:227], v[150:151], v[158:159]\n\t" \
  "v_cvt_pk_bf16_f32 v20, v176, v177\n\t" \
  "v_mfma_f32_16x16x32_bf16 a[84:87], a[148:151], v[56:59], a[84:87]\n\t" \
  "ds_read_b64_tr_b16 a[186:187], v190 offset:58368\n\t" \
  "v_lshlrev_b32 v228, 16, v84\n\t" \
  "v_mfma_f32_16x16x32_bf16 a[80:83], a[148:151], v[24:27], a[80:83]\n\t" \
  "ds_read_b64_tr_b16 a[188:189], v191 offset:57344\n\t" \
  "v_and_b32 v229, s98, v84\n\t" \
  "v_mfma_f32_16x16x32_bf16 a[96:99], a[152:155], v[24:27], a[96:99]\n\t" \
  "global_load_dword v246, v217, s[12:13]\n\t" \
  "v_mfma_f32_16x16x32_bf16 a[100:103], a[152:155], v[56:59], a[100:103]\n\t" \
  "ds_read_b64_tr_b16 a[190:191], v191 offset:58368\n\t" \
  "v_lshlrev_b32 v230, 16, v85\n\t" \
  "v_mfma_f32_16x16x32_bf16 a[104:107], a[152:155], v[88:91], a[104:107]\n\t" \
  "v_and_b32 v231, s98, v85\n\t" \
  "v_lshlrev_b32 v232, 16, v86\n\t" \
  "v_mfma_f32_16x16x32_bf16 a[108:111], a[152:155], v[120:123], a[108:111]\n\t" \
  ".Lhs1:\n\ts_cbranch_execz .Lhs1n\n\t" \
  "v_and_b32 v233, s98, v86\n\t" \
  "v_lshlrev_b32 v234, 16, v87\n\t" \
  "v_mfma_f32_16x16x32_bf16 a[124:127], a[156:159], v[120:123], a[124:127]\n\t" \
  "v_cvt_pk_bf16_f32 v114, v240, v241\n\t" \
  "v_cvt_pk_bf16_f32 v115, v242, v243\n\t" \
  "v_mfma_f32_16x16x32_bf16 a[120:123], a[156:159], v[88:91], a[120:123]\n\t" \
  "s_add_u32 s96, s78, 3145728\n\t" \
  "v_and_b32 v235, s98, v87\n\t" \
  "v_lshlrev_b32 v236, 16, v116\n\t" \
  "v_mfma_f32_16x16x32_bf16 a[116:119], a[156:159], v[56:59], a[116:119]\n\t" \
  "s_waitcnt vmcnt(22) lgkmcnt(0)\n\t" \
  "s_and_b32 s96, s96, 0x780000\n\t" \
  "s_add_u32 m0, s76, 49152\n\t" \
  "v_mfma_f32_16x16x32_bf16 a[112:115], a[156:159], v[24:27], a[112:115]\n\t" \
  "s_barrier\n\t" \
  FSTP("v_writelane_b32 v255, s94, 40\n\t") \
  FSTP("v_writelane_b32 v255, s92, 41\n\t") \
  FSTP("s_memrealtime s[94:95]\n\t") \
  FSTP("s_memtime s[92:93]\n\t") \
  "v_mfma_f32_16x16x32_bf16 v[0:3], a[160:163], v[28:31], v[0:3]\n\t" \
  "buffer_load_dwordx4 v200, s[20:23], s96 offen offset:0 lds\n\t" \
  "ds_read_b64_tr_b16 a[128:129], v192 offset:32768\n\t" \
  "v_mfma_f32_16x16x32_bf16 v[32:35], a[160:163], v[60:63], v[32:35]\n\t" \
  "s_waitcnt vmcnt(14)\n\t" \
  "ds_read_b128 v[24:27], v204 offset:0\n\t" \
  "ds_read_b64_tr_b16 a[130:131], v192 offset:33792\n\t" \
  "v_and_b32 v237, s98, v116\n\t" \
  "v_mfma_f32_16x16x32_bf16 v[64:67], a[160:163], v[92:95], v[64:67]\n\t" \
  "ds_read_b128 v[56:59], v204 offset:2048\n\t" \
  "ds_read_b64_tr_b16 a[132:133], v193 offset:32768\n\t" \
  "v_cvt_pk_bf16_f32 v21, v178, v179\n\t" \
  "v_mfma_f32_16x16x32_bf16 v[96:99], a[160:163], v[124:127], v[96:99]\n\t" \
  "ds_read_b128 v[88:91], v204 offset:4096\n\t" \
  "v_lshlrev_b32 v238, 16, v117\n\t" \
  "v_mfma_f32_16x16x32_bf16 v[100:103], a[164:167], v[124:127], v[100:103]\n\t" \
  "ds_read_b128 v[120:123], v204 offset:6144\n\t" \
  "v_cvt_pk_bf16_f32 v22, v180, v181\n\t" \
  "v_mfma_f32_16x16x32_bf16 v[68:71], a[164:167], v[92:95], v[68:71]\n\t" \
  "buffer_load_dwordx4 v201, s[20:23], s96 offen offset:1024 lds\n\t" \
  "v_mfma_f32_16x16x32_bf16 v[36:39], a[164:167], v[60:63], v[36:39]\n\t" \
  "ds_read_b64_tr_b16 a[134:135], v193 offset:33792\n\t" \
  "v_and_b32 v239, s98, v117\n\t" \
  "v_mfma_f32_16x16x32_bf16 v[4:7], a[164:167], v[28:31], v[4:7]\n\t" \
  "ds_read_b64_tr_b16 a[136:137], v194 offset:32768\n\t" \
  "v_cvt_pk_bf16_f32 v23, v182, v183\n\t" \
  "v_mfma_f32_16x16x32_bf16 v[8:11], a[168:171], v[28:31], v[8:11]\n\t" \
  "buffer_load_dwordx4 v202, s[20:23], s96 offen offset:2048 lds\n\t" \
  "v_mfma_f32_16x16x32_bf16 v[40:43], a[168:171], v[60:63], v[40:43]\n\t" \
  "ds_read_b64_tr_b16 a[138:139], v194 offset:33792\n\t" \
  "v_lshlrev_b32 v240, 16, v118\n\t" \
  "v_mfma_f32_16x16x32_bf16 v[72:75], a[168:171], v[92:95], v[72:75]\n\t" \
  "ds_read_b64_tr_b16 a[140:141], v195 offset:32768\n\t" \
  "v_and_b32 v241, s98, v118\n\t" \
  "v_mfma_f32_16x16x32_bf16 v[104:107], a[168:171], v[124:127], v[104:107]\n\t" \
  "v_lshlrev_b32 v242, 16, v119\n\t" \
  "v_and_b32 v243, s98, v119\n\t" \
  "v_mfma_f32_16x16x32_bf16 v[108:111], a[172:175], v[124:127], v[108:111]\n\t" \
  "buffer_load_dwordx4 v203, s[20:23], s96 offen offset:3072 lds\n\t" \
  "ds_read_b64_tr_b16 a[142:143], v195 offset:33792\n\t" \
  "ds_read_b128 v[128:131], v208 offset:1536\n\t" \
  "v_cvt_pk_bf16_f32 v52, v220, v221\n\t" \
  "v_cvt_pk_bf16_f32 v53, v222, v223\n\t" \
  "v_pk_fma_f32 v[228:229], v[228:229], v[168:169], v[170:171] op_sel_hi:[1,0,0]\n\t" \
  "v_pk_fma_f32 v[230:231], v[230:231], v[168:169], v[170:171] op_sel_hi:[1,0,0]\n\t" \
  "v_pk_fma_f32 v[232:233], v[232:233], v[168:169], v[170:171] op_sel_hi:[1,0,0]\n\t" \
  "v_pk_fma_f32 v[234:235], v[234:235], v[168:169], v[170:171] op_sel_hi:[1,0,0]\n\t" \
  "v_pk_fma_f32 v[228:229], v[228:229], v[144:145], v[152:153]\n\t" \
  "v_pk_fma_f32 v[230:231], v[230:231], v[146:147], v[154:155]\n\t" \
  "v_pk_fma_f32 v[232:233], v[232:233], v[148:149], v[156:157]\n\t" \
  "v_pk_fma_f32 v[234:235], v[234:235], v[150:151], v[158:159]\n\t" \
  "v_pk_fma_f32 v[236:237], v[236:237], v[172:173], v[174:175] op_sel_hi:[1,0,0]\n\t" \
  "v_pk_fma_f32 v[238:239], v[238:239], v[172:173], v[174:175] op_sel_hi:[1,0,0]\n\t" \
  "v_pk_fma_f32 v[240:241], v[240:241], v[172:173], v[174:175] op_sel_hi:[1,0,0]\n\t" \
  "v_pk_fma_f32 v[242:243], v[242:243], v[172:173], v[174:175] op_sel_hi:[1,0,0]\n\t" \
  "v_pk_fma_f32 v[236:237], v[236:237], v[144:145], v[152:153]\n\t" \
  "v_pk_fma_f32 v[238:239], v[238:239], v[146:147], v[154:155]\n\t" \
  "v_pk_fma_f32 v[240:241], v[240:241], v[148:149], v[156:157]\n\t" \
  "v_pk_fma_f32 v[242:243], v[242:243], v[150:151], v[158:159]\n\t" \
  "v_cvt_pk_bf16_f32 v84, v228, v229\n\t" \
  "v_mfma_f32_16x16x32_bf16 v[76:79], a[172:175], v[92:95], v[76:79]\n\t" \
  "s_add_u32 s96, s78, 3670016\n\t" \
  "s_and_b32 s96, s96, 0x780000\n\t" \
  "s_add_u32 m0, s76, 65536\n\t" \
  "v_mfma_f32_16x16x32_bf16 v[44:47], a[172:175], v[60:63], v[44:47]\n\t" \
  "ds_read_b64_tr_b16 a[144:145], v196 offset:32768\n\t" \
  "v_cvt_pk_bf16_f32 v54, v224, v225\n\t" \
  "v_mfma_f32_16x16x32_bf16 v[12:15], a[172:175], v[28:31], v[12:15]\n\t" \
  "buffer_load_dwordx4 v200, s[20:23], s96 offen offset:0 lds\n\t" \
  "v_mfma_f32_16x16x32_bf16 a[64:67], a[176:179], v[28:31], a[64:67]\n\t" \
  "s_waitcnt lgkmcnt(7)\n\t" \
  "v_lshlrev_b32 v176, 16, v24\n\t" \
  "v_and_b32 v177, s98, v24\n\t" \
  "v_mfma_f32_16x16x32_bf16 a[68:71], a[176:179], v[60:63], a[68:71]\n\t" \
  "ds_read_b64_tr_b16 a[146:147], v196 offset:33792\n\t" \
  "v_lshlrev_b32 v178, 16, v25\n\t" \
  "v_mfma_f32_16x16x32_bf16 a[72:75], a[176:179], v[92:95], a[72:75]\n\t" \
  "ds_read_b64_tr_b16 a[148:149], v197 offset:32768\n\t" \
  "v_and_b32 v179, s98, v25\n\t" \
  "v_mfma_f32_16x16x32_bf16 a[76:79], a[176:179], v[124:127], a[76:79]\n\t" \
  "buffer_load_dwordx4 v201, s[20:23], s96 offen offset:1024 lds\n\t" \
  "v_mfma_f32_16x16x32_bf16 a[92:95], a[180:183], v[124:127], a[92:95]\n\t" \
  "ds_read_b64_tr_b16 a[150:151], v197 offset:33792\n\t" \
  "v_lshlrev_b32 v180, 16, v26\n\t" \
  "v_mfma_f32_16x16x32_bf16 a[88:91], a[180:183], v[92:95], a[88:91]\n\t" \
  "ds_read_b64_tr_b16 a[152:153], v198 offset:32768\n\t" \
  "v_and_b32 v181, s98, v26\n\t" \
  "v_mfma_f32_16x16x32_bf16 a[84:87], a[180:183], v[60:63], a[84:87]\n\t" \
  "ds_read_b64_tr_b16 a[154:155], v198 offset:33792\n\t" \
  "v_lshlrev_b32 v182, 16, v27\n\t" \
  "v_mfma_f32_16x16x32_bf16 a[80:83], a[180:183], v[28:31], a[80:83]\n\t" \
  "buffer_load_dwordx4 v202, s[20:23], s96 offen offset:2048 lds\n\t" \
  "v_mfma_f32_16x16x32_bf16 a[96:99], a[184:187], v[28:31], a[96:99]\n\t" \
  "ds_read_b64_tr_b16 a[156:157], v199 offset:32768\n\t" \
  "v_cvt_pk_bf16_f32 v55, v226, v227\n\t" \
  "v_mfma_f32_16x16x32_bf16 a[100:103], a[184:187], v[60:63], a[100:103]\n\t" \
  "ds_read_b64_tr_b16 a[158:159], v199 offset:33792\n\t" \
  "v_and_b32 v183, s98, v27\n\t" \
  "v_mfma_f32_16x16x32_bf16 a[104:107], a[184:187], v[92:95], a[104:107]\n\t" \
  "v_lshlrev_b32 v220, 16, v56\n\t" \
  "v_and_b32 v221, s98, v56\n\t" \
  "v_mfma_f32_16x16x32_bf16 a[108:111], a[184:187], v[124:127], a[108:111]\n\t" \
  "buffer_load_dwordx4 v203, s[20:23], s96 offen offset:3072 lds\n\t" \
  "v_mfma_f32_16x16x32_bf16 a[124:127], a[188:191], v[124:127], a[124:127]\n\t" \
  "v_lshlrev_b32 v222, 16, v57\n\t" \
  "v_and_b32 v223, s98, v57\n\t" \
  "v_mfma_f32_16x16x32_bf16 a[120:123], a[188:191], v[92:95], a[120:123]\n\t" \
  "v_lshlrev_b32 v224, 16, v58\n\t" \
  "v_cvt_pk_bf16_f32 v85, v230, v231\n\t" \
  "v_mfma_f32_16x16x32_bf16 a[116:119], a[188:191], v[60:63], a[116:119]\n\t" \
  "v_and_b32 v225, s98, v58\n\t" \
  "v_lshlrev_b32 v226, 16, v59\n\t" \
  "v_mfma_f32_16x16x32_bf16 a[112:115], a[188:191], v[28:31], a[112:115]\n\t" \
  "s_waitcnt lgkmcnt(0)\n\t" \
  "v_cvt_pk_bf16_f32 v86, v232, v233\n\t" \
  "v_cvt_pk_bf16_f32 v87, v234, v235\n\t" \
  FSTP("v_writelane_b32 v255, s94, 42\n\t") \
  FSTP("v_writelane_b32 v255, s92, 43\n\t") \
  FSTP("s_memrealtime s[94:95]\n\t") \
  FSTP("s_memtime s[92:93]\n\t") \
  "v_mfma_f32_16x16x32_bf16 v[0:3], a[128:131], v[16:19], v[0:3]\n\t" \
  "buffer_load_dword v252, v210, s[16:19], s84 offen sc1\n\t" \
  "v_mfma_f32_16x16x32_bf16 v[32:35], a[128:131], v[48:51], v[32:35]\n\t" \
  "ds_read_b128 v[28:31], v205 offset:0\n\t" \
  "v_and_b32 v227, s98, v59\n\t" \
  "v_mfma_f32_16x16x32_bf16 v[64:67], a[128:131], v[80:83], v[64:67]\n\t" \
  "s_lshr_b32 s99, s87, 9\n\t" \
  "ds_read_b128 v[60:63], v205 offset:2048\n\t" \
  "ds_read_b64_tr_b16 a[160:161], v192 offset:40960\n\t" \
  "v_lshlrev_b32 v228, 16, v88\n\t" \
  "v_mfma_f32_16x16x32_bf16 v[96:99], a[128:131], v[112:115], v[96:99]\n\t" \
  "ds_read_b128 v[92:95], v205 offset:4096\n\t" \
  "ds_read_b64_tr_b16 a[162:163], v192 offset:41984\n\t" \
  "v_xor_b32 v248, s99, v214\n\t" \
  "v_mfma_f32_16x16x32_bf16 v[100:103], a[132:135], v[112:115], v[100:103]\n\t" \
  "v_xor_b32 v249, 256, v248\n\t" \
  "v_xor_b32 v250, 512, v248\n\t" \
  "v_mfma_f32_16x16x32_bf16 v[68:71], a[132:135], v[80:83], v[68:71]\n\t" \
  "v_xor_b32 v251, 768, v248\n\t" \
  "s_add_u32 s96, s78, 4194304\n\t" \
  "v_mfma_f32_16x16x32_bf16 v[36:39], a[132:135], v[48:51], v[36:39]\n\t" \
  "s_and_b32 s96, s96, 0x780000\n\t" \
  "ds_read_b128 v[124:127], v205 offset:6144\n\t" \
  "ds_read_b64_tr_b16 a[164:165], v193 offset:40960\n\t" \
  "s_add_u32 m0, s76, 81920\n\t" \
  "v_mfma_f32_16x16x32_bf16 v[4:7], a[132:135], v[16:19], v[4:7]\n\t" \
  "buffer_load_dwordx4 v200, s[20:23], s96 offen offset:0 lds\n\t" \
  "v_mfma_f32_16x16x32_bf16 v[8:11], a[136:139], v[16:19], v[8:11]\n\t" \
  "ds_read_b64_tr_b16 a[166:167], v193 offset:41984\n\t" \
  "v_and_b32 v229, s98, v88\n\t" \
  "v_mfma_f32_16x16x32_bf16 v[40:43], a[136:139], v[48:51], v[40:43]\n\t" \
  "ds_read_b64_tr_b16 a[168:169], v194 offset:40960\n\t" \
  "v_lshlrev_b32 v230, 16, v89\n\t" \
  "v_mfma_f32_16x16x32_bf16 v[72:75], a[136:139], v[80:83], v[72:75]\n\t" \
  "buffer_load_dwordx4 v201, s[20:23], s96 offen offset:1024 lds\n\t" \
  "v_mfma_f32_16x16x32_bf16 v[104:107], a[136:139], v[112:115], v[104:107]\n\t" \
  "ds_read_b64_tr_b16 a[170:171], v194 offset:41984\n\t" \
  "v_and_b32 v231, s98, v89\n\t" \
  "v_mfma_f32_16x16x32_bf16 v[108:111], a[140:143], v[112:115], v[108:111]\n\t" \
  "ds_read_b64_tr_b16 a[172:173], v195 offset:40960\n\t" \
  "v_cvt_pk_bf16_f32 v116, v236, v237\n\t" \
  "v_mfma_f32_16x16x32_bf16 v[76:79], a[140:143], v[80:83], v[76:79]\n\t" \
  "ds_read_b64_tr_b16 a[174:175], v195 offset:41984\n\t" \
  "v_lshlrev_b32 v232, 16, v90\n\t" \
  "v_mfma_f32_16x16x32_bf16 v[44:47], a[140:143], v[48:51], v[44:47]\n\t" \
  "ds_read_b64_tr_b16 a[176:177], v196 offset:40960\n\t" \
  "v_cvt_pk_bf16_f32 v117, v238, v239\n\t" \
  "v_mfma_f32_16x16x32_bf16 v[12:15], a[140:143], v[16:19], v[12:15]\n\t" \
  "buffer_load_dwordx4 v202, s[20:23], s96 offen offset:2048 lds\n\t" \
  "v_mfma_f32_16x16x32_bf16 a[64:67], a[144:147], v[16:19], a[64:67]\n\t" \
  "ds_read_b64_tr_b16 a[178:179], v196 offset:41984\n\t" \
  "v_and_b32 v233, s98, v90\n\t" \
  "v_mfma_f32_16x16x32_bf16 a[68:71], a[144:147], v[48:51], a[68:71]\n\t" \
  "ds_read_b64_tr_b16 a[180:181], v197 offset:40960\n\t" \
  "v_cvt_pk_bf16_f32 v118, v240, v241\n\t" \
  "v_mfma_f32_16x16x32_bf16 a[72:75], a[144:147], v[80:83], a[72:75]\n\t" \
  "ds_read_b64_tr_b16 a[182:183], v197 offset:41984\n\t" \
  "v_lshlrev_b32 v234, 16, v91\n\t" \
  "v_mfma_f32_16x16x32_bf16 a[76:79], a[144:147], v[112:115], a[76:79]\n\t" \
  "ds_read_b128 v[132:135], v208 offset:1552\n\t" \
  "v_cvt_pk_bf16_f32 v119, v242, v243\n\t" \
  "v_mfma_f32_16x16x32_bf16 a[92:95], a[148:151], v[112:115], a[92:95]\n\t" \
  "buffer_load_dwordx4 v203, s[20:23], s96 offen offset:3072 lds\n\t" \
  "v_mfma_f32_16x16x32_bf16 a[88:91], a[148:151], v[80:83], a[88:91]\n\t" \
  "ds_read_b64_tr_b16 a[184:185], v198 offset:40960\n\t" \
  "v_and_b32 v235, s98, v91\n\t" \
  "v_mfma_f32_16x16x32_bf16 a[84:87], a[148:151], v[48:51], a[84:87]\n\t" \
  "ds_read_b64_tr_b16 a[186:187], v198 offset:41984\n\t" \
  "v_lshlrev_b32 v236, 16, v120\n\t" \
  "v_mfma_f32_16x16x32_bf16 a[80:83], a[148:151], v[16:19], a[80:83]\n\t" \
  "ds_read_b64_tr_b16 a[188:189], v199 offset:40960\n\t" \
  "v_and_b32 v237, s98, v120\n\t" \
  "v_mfma_f32_16x16x32_bf16 a[96:99], a[152:155], v[16:19], a[96:99]\n\t" \
  "ds_read_b128 v[136:139], v208 offset:1792\n\t" \
  "v_lshlrev_b32 v238, 16, v121\n\t" \
  "v_mfma_f32_16x16x32_bf16 a[100:103], a[152:155], v[48:51], a[100:103]\n\t" \
  "global_load_dword v246, v216, s[20:21]\n\t" \
  "v_mfma_f32_16x16x32_bf16 a[104:107], a[152:155], v[80:83], a[104:107]\n\t" \
  "ds_read_b64_tr_b16 a[190:191], v199 offset:41984\n\t" \
  "v_and_b32 v239, s98, v121\n\t" \
  "v_mfma_f32_16x16x32_bf16 a[108:111], a[152:155], v[112:115], a[108:111]\n\t" \
  "ds_read_b128 v[140:143], v208 offset:1808\n\t" \
  "v_lshlrev_b32 v240, 16, v122\n\t" \
  "v_mfma_f32_16x16x32_bf16 a[124:127], a[156:159], v[112:115], a[124:127]\n\t" \
  "ds_read_b128 v[144:147], v208 offset:1664\n\t" \
  "v_and_b32 v241, s98, v122\n\t" \
  "v_lshlrev_b32 v242, 16, v123\n\t" \
  "v_and_b32 v243, s98, v123\n\t" \
  "s_waitcnt lgkmcnt(1)\n\t" \
  "v_pk_fma_f32 v[176:177], v[176:177], v[160:161], v[162:163] op_sel_hi:[1,0,0]\n\t" \
  "v_pk_fma_f32 v[178:179], v[178:179], v[160:161], v[162:163] op_sel_hi:[1,0,0]\n\t" \
  "v_pk_fma_f32 v[180:181], v[180:181], v[160:161], v[162:163] op_sel_hi:[1,0,0]\n\t" \
  "v_pk_fma_f32 v[182:183], v[182:183], v[160:161], v[162:163] op_sel_hi:[1,0,0]\n\t" \
  "v_pk_fma_f32 v[176:177], v[176:177], v[128:129], v[136:137]\n\t" \
  "v_pk_fma_f32 v[178:179], v[178:179], v[130:131], v[138:139]\n\t" \
  "v_pk_fma_f32 v[180:181], v[180:181], v[132:133], v[140:141]\n\t" \
  "v_pk_fma_f32 v[182:183], v[182:183], v[134:135], v[142:143]\n\t" \
  "v_pk_fma_f32 v[220:221], v[220:221], v[164:165], v[166:167] op_sel_hi:[1,0,0]\n\t" \
  "v_pk_fma_f32 v[222:223], v[222:223], v[164:165], v[166:167] op_sel_hi:[1,0,0]\n\t" \
  "v_pk_fma_f32 v[224:225], v[224:225], v[164:165], v[166:167] op_sel_hi:[1,0,0]\n\t" \
  "v_pk_fma_f32 v[226:227], v[226:227], v[164:165], v[166:167] op_sel_hi:[1,0,0]\n\t" \
  "v_pk_fma_f32 v[220:221], v[220:221], v[128:129], v[136:137]\n\t" \
  "v_pk_fma_f32 v[222:223], v[222:223], v[130:131], v[138:139]\n\t" \
  "v_pk_fma_f32 v[224:225], v[224:225], v[132:133], v[140:141]\n\t" \
  "v_pk_fma_f32 v[226:227], v[226:227], v[134:135], v[142:143]\n\t" \
  "v_cvt_pk_bf16_f32 v24, v176, v177\n\t" \
  "v_mfma_f32_16x16x32_bf16 a[120:123], a[156:159], v[80:83], a[120:123]\n\t" \
  "v_cvt_pk_bf16_f32 v25, v178, v179\n\t" \
  "v_cvt_pk_bf16_f32 v26, v180, v181\n\t" \
  "v_cvt_pk_bf16_f32 v27, v182, v183\n\t" \
  "v_cvt_pk_bf16_f32 v56, v220, v221\n\t" \
  "v_cvt_pk_bf16_f32 v57, v222, v223\n\t" \
  "v_pk_fma_f32 v[228:229], v[228:229], v[168:169], v[170:171] op_sel_hi:[1,0,0]\n\t" \
  "v_pk_fma_f32 v[230:231], v[230:231], v[168:169], v[170:171] op_sel_hi:[1,0,0]\n\t" \
  "v_pk_fma_f32 v[232:233], v[232:233], v[168:169], v[170:171] op_sel_hi:[1,0,0]\n\t" \
  "v_pk_fma_f32 v[234:235], v[234:235], v[168:169], v[170:171] op_sel_hi:[1,0,0]\n\t" \
  "v_pk_fma_f32 v[228:229], v[228:229], v[128:129], v[136:137]\n\t" \
  "v_pk_fma_f32 v[230:231], v[230:231], v[130:131], v[138:139]\n\t" \
  "v_pk_fma_f32 v[232:233], v[232:233], v[132:133], v[140:141]\n\t" \
  "v_pk_fma_f32 v[234:235], v[234:235], v[134:135], v[142:143]\n\t" \
  "v_pk_fma_f32 v[236:237], v[236:237], v[172:173], v[174:175] op_sel_hi:[1,0,0]\n\t" \
  "v_pk_fma_f32 v[238:239], v[238:239], v[172:173], v[174:175] op_sel_hi:[1,0,0]\n\t" \
  "v_pk_fma_f32 v[240:241], v[240:241], v[172:173], v[174:175] op_sel_hi:[1,0,0]\n\t" \
  "v_pk_fma_f32 v[242:243], v[242:243], v[172:173], v[174:175] op_sel_hi:[1,0,0]\n\t" \
  "v_pk_fma_f32 v[236:237], v[236:237], v[128:129], v[136:137]\n\t" \
  "v_pk_fma_f32 v[238:239], v[238:239], v[130:131], v[138:139]\n\t" \
  "v_pk_fma_f32 v[240:241], v[240:241], v[132:133], v[140:141]\n\t" \
  "v_pk_fma_f32 v[242:243], v[242:243], v[134:135], v[142:143]\n\t" \
  "v_cvt_pk_bf16_f32 v88, v228, v229\n\t" \
  "v_mfma_f32_16x16x32_bf16 a[116:119], a[156:159], v[48:51], a[116:119]\n\t" \
  "s_waitcnt vmcnt(23)\n\t" \
  "v_cvt_pk_bf16_f32 v58, v224, v225\n\t" \
  "v_cvt_pk_bf16_f32 v59, v226, v227\n\t" \
  "v_mfma_f32_16x16x32_bf16 a[112:115], a[156:159], v[16:19], a[112:115]\n\t" \
  "s_barrier\n\t" \
  FSTP("v_writelane_b32 v255, s94, 44\n\t") \
  FSTP("v_writelane_b32 v255, s92, 45\n\t") \
  FSTP("s_memrealtime s[94:95]\n\t") \
  FSTP("s_memtime s[92:93]\n\t") \
  "v_mfma_f32_16x16x32_bf16 v[0:3], a[160:163], v[20:23], v[0:3]\n\t" \
  "s_waitcnt vmcnt(5)\n\t" \
  "v_cmp_ne_u32 vcc, v211, v252\n\t" \
  "v_cvt_pk_bf16_f32 v89, v230, v231\n\t" \
  "v_mfma_f32_16x16x32_bf16 v[32:35], a[160:163], v[52:55], v[32:35]\n\t" \
  "ds_read_b64_tr_b16 a[128:129], v184 offset:0\n\t" \
  "v_cvt_pk_bf16_f32 v90, v232, v233\n\t" \
  "v_mfma_f32_16x16x32_bf16 v[64:67], a[160:163], v[84:87], v[64:67]\n\t" \
  "s_cbranch_vccnz .Lfq_s\n\t" \
  ".Lfq_k:\n\t" \
  "v_mfma_f32_16x16x32_bf16 v[96:99], a[160:163], v[116:119], v[96:99]\n\t" \
  "s_add_u32 s97, s78, 2097152\n\t" \
  "s_bfe_u32 s97, s97, 0x140003\n\t" \
  "s_add_u32 s97, s97, s82\n\t" \
  "v_mfma_f32_16x16x32_bf16 v[100:103], a[164:167], v[116:119], v[100:103]\n\t" \
  "buffer_load_dwordx4 a[0:3], v248, s[16:19], s97 offen sc1\n\t" \
  "v_mfma_f32_16x16x32_bf16 v[68:71], a[164:167], v[84:87], v[68:71]\n\t" \
  "buffer_load_dwordx4 a[4:7], v249, s[16:19], s97 offen sc1\n\t" \
  "v_mfma_f32_16x16x32_bf16 v[36:39], a[164:167], v[52:55], v[36:39]\n\t" \
  "ds_read_b64_tr_b16 a[130:131], v184 offset:1024\n\t" \
  "v_cvt_pk_bf16_f32 v91, v234, v235\n\t" \
  "v_mfma_f32_16x16x32_bf16 v[4:7], a[164:167], v[20:23], v[4:7]\n\t" \
  "ds_read_b64_tr_b16 a[132:133], v185 offset:0\n\t" \
  "v_cvt_pk_bf16_f32 v120, v236, v237\n\t" \
  "v_mfma_f32_16x16x32_bf16 v[8:11], a[168:171], v[20:23], v[8:11]\n\t" \
  "buffer_load_dwordx4 a[8:11], v250, s[16:19], s97 offen sc1\n\t" \
  "v_mfma_f32_16x16x32_bf16 v[40:43], a[168:171], v[52:55], v[40:43]\n\t" \
  "ds_read_b64_tr_b16 a[134:135], v185 offset:1024\n\t" \
  "v_cvt_pk_bf16_f32 v121, v238, v239\n\t" \
  "v_mfma_f32_16x16x32_bf16 v[72:75], a[168:171], v[84:87], v[72:75]\n\t" \
  "v_lshlrev_b32 v176, 16, v28\n\t" \
  "v_and_b32 v177, s98, v28\n\t" \
  "v_mfma_f32_16x16x32_bf16 v[104:107], a[168:171], v[116:119], v[104:107]\n\t" \
  "ds_read_b64_tr_b16 a[136:137], v186 offset:0\n\t" \
  "v_lshlrev_b32 v178, 16, v29\n\t" \
  "v_mfma_f32_16x16x32_bf16 v[108:111], a[172:175], v[116:119], v[108:111]\n\t" \
  "buffer_load_dwordx4 a[12:15], v251, s[16:19], s97 offen sc1\n\t" \
  "v_mfma_f32_16x16x32_bf16 v[76:79], a[172:175], v[84:87], v[76:79]\n\t" \
  "s_add_u32 s97, s97, s83\n\t" \
  "ds_read_b128 v[148:151], v208 offset:1680\n\t" \
  "ds_read_b64_tr_b16 a[138:139], v186 offset:1024\n\t" \
  "v_and_b32 v179, s98, v29\n\t" \
  "v_mfma_f32_16x16x32_bf16 v[44:47], a[172:175], v[52:55], v[44:47]\n\t" \
  "ds_read_b128 v[152:155], v208 offset:1920\n\t" \
  "ds_read_b64_tr_b16 a[140:141], v187 offset:0\n\t" \
  "v_lshlrev_b32 v180, 16, v30\n\t" \
  "v_mfma_f32_16x16x32_bf16 v[12:15], a[172:175], v[20:23], v[12:15]\n\t" \
  "buffer_load_dwordx4 a[16:19], v248, s[16:19], s97 offen sc1\n\t" \
  "v_mfma_f32_16x16x32_bf16 a[64:67], a[176:179], v[20:23], a[64:67]\n\t" \
  "ds_read_b64_tr_b16 a[142:143], v187 offset:1024\n\t" \
  "v_and_b32 v181, s98, v30\n\t" \
  "v_mfma_f32_16x16x32_bf16 a[68:71], a[176:179], v[52:55], a[68:71]\n\t" \
  "ds_read_b64_tr_b16 a[144:145], v188 offset:0\n\t" \
  "v_lshlrev_b32 v182, 16, v31\n\t" \
  "v_mfma_f32_16x16x32_bf16 a[72:75], a[176:179], v[84:87], a[72:75]\n\t" \
  "ds_read_b64_tr_b16 a[146:147], v188 offset:1024\n\t" \
  "v_and_b32 v183, s98, v31\n\t" \
  "v_mfma_f32_16x16x32_bf16 a[76:79], a[176:179], v[116:119], a[76:79]\n\t" \
  "buffer_load_dwordx4 a[20:23], v249, s[16:19], s97 offen sc1\n\t" \
  "v_mfma_f32_16x16x32_bf16 a[92:95], a[180:183], v[116:119], a[92:95]\n\t" \
  "ds_read_b64_tr_b16 a[148:149], v189 offset:0\n\t" \
  "v_lshlrev_b32 v220, 16, v60\n\t" \
  "v_mfma_f32_16x16x32_bf16 a[88:91], a[180:183], v[84:87], a[88:91]\n\t" \
  "ds_read_b64_tr_b16 a[150:151], v189 offset:1024\n\t" \
  "v_and_b32 v221, s98, v60\n\t" \
  "v_mfma_f32_16x16x32_bf16 a[84:87], a[180:183], v[52:55], a[84:87]\n\t" \
  "ds_read_b64_tr_b16 a[152:153], v190 offset:0\n\t" \
  "v_lshlrev_b32 v222, 16, v61\n\t" \
  "v_mfma_f32_16x16x32_bf16 a[80:83], a[180:183], v[20:23], a[80:83]\n\t" \
  "buffer_load_dwordx4 a[24:27], v250, s[16:19], s97 offen sc1\n\t" \
  "v_mfma_f32_16x16x32_bf16 a[96:99], a[184:187], v[20:23], a[96:99]\n\t" \
  "ds_read_b64_tr_b16 a[154:155], v190 offset:1024\n\t" \
  "v_cvt_pk_bf16_f32 v122, v240, v241\n\t" \
  "v_mfma_f32_16x16x32_bf16 a[100:103], a[184:187], v[52:55], a[100:103]\n\t" \
  "ds_read_b64_tr_b16 a[156:157], v191 offset:0\n\t" \
  "v_and_b32 v223, s98, v61\n\t" \
  "v_mfma_f32_16x16x32_bf16 a[104:107], a[184:187], v[84:87], a[104:107]\n\t" \
  "ds_read_b64_tr_b16 a[158:159], v191 offset:1024\n\t" \
  "v_cvt_pk_bf16_f32 v123, v242, v243\n\t" \
  "v_mfma_f32_16x16x32_bf16 a[108:111], a[184:187], v[116:119], a[108:111]\n\t" \
  "buffer_load_dwordx4 a[28:31], v251, s[16:19], s97 offen sc1\n\t" \
  "v_mfma_f32_16x16x32_bf16 a[124:127], a[188:191], v[116:119], a[124:127]\n\t" \
  "ds_read_b128 v[156:159], v208 offset:1936\n\t" \
  "v_lshlrev_b32 v224, 16, v62\n\t" \
  "v_mfma_f32_16x16x32_bf16 a[120:123], a[188:191], v[84:87], a[120:123]\n\t" \
  "s_add_u32 s97, s78, 2621440\n\t" \
  "v_and_b32 v225, s98, v62\n\t" \
  "v_lshlrev_b32 v226, 16, v63\n\t" \
  "v_mfma_f32_16x16x32_bf16 a[116:119], a[188:191], v[52:55], a[116:119]\n\t" \
  "s_bfe_u32 s97, s97, 0x140003\n\t" \
  "s_add_u32 s97, s97, s82\n\t" \
  "v_and_b32 v227, s98, v63\n\t" \
  "v_mfma_f32_16x16x32_bf16 a[112:115], a[188:191], v[20:23], a[112:115]\n\t" \
  "s_waitcnt lgkmcnt(1)\n\t" \
  "v_lshlrev_b32 v228, 16, v92\n\t" \
  "v_and_b32 v229, s98, v92\n\t" \
  "v_lshlrev_b32 v230, 16, v93\n\t" \
  "v_and_b32 v231, s98, v93\n\t" \
  "s_waitcnt lgkmcnt(0)\n\t" \
  "v_pk_fma_f32 v[176:177], v[176:177], v[160:161], v[162:163] op_sel_hi:[1,0,0]\n\t" \
  "v_pk_fma_f32 v[178:179], v[178:179], v[160:161], v[162:163] op_sel_hi:[1,0,0]\n\t" \
  "v_pk_fma_f32 v[180:181], v[180:181], v[160:161], v[162:163] op_sel_hi:[1,0,0]\n\t" \
  "v_pk_fma_f32 v[182:183], v[182:183], v[160:161], v[162:163] op_sel_hi:[1,0,0]\n\t" \
  "v_pk_fma_f32 v[176:177], v[176:177], v[144:145], v[152:153]\n\t" \
  "v_pk_fma_f32 v[178:179], v[178:179], v[146:147], v[154:155]\n\t" \
  "v_pk_fma_f32 v[180:181], v[180:181], v[148:149], v[156:157]\n\t" \
  "v_pk_fma_f32 v[182:183], v[182:183], v[150:151], v[158:159]\n\t" \
  "v_pk_fma_f32 v[220:221], v[220:221], v[164:165], v[166:167] op_sel_hi:[1,0,0]\n\t" \
  "v_pk_fma_f32 v[222:223], v[222:223], v[164:165], v[166:167] op_sel_hi:[1,0,0]\n\t" \
  "v_pk_fma_f32 v[224:225], v[224:225], v[164:165], v[166:167] op_sel_hi:[1,0,0]\n\t" \
  "v_pk_fma_f32 v[226:227], v[226:227], v[164:165], v[166:167] op_sel_hi:[1,0,0]\n\t" \
  "v_pk_fma_f32 v[220:221], v[220:221], v[144:145], v[152:153]\n\t" \
  "v_pk_fma_f32 v[222:223], v[222:223], v[146:147], v[154:155]\n\t" \
  "v_pk_fma_f32 v[224:225], v[224:225], v[148:149], v[156:157]\n\t" \
  "v_pk_fma_f32 v[226:227], v[226:227], v[150:151], v[158:159]\n\t" \
  "v_cvt_pk_bf16_f32 v28, v176, v177\n\t" \
  FSTP("v_writelane_b32 v255, s94, 46\n\t") \
  FSTP("v_writelane_b32 v255, s92, 47\n\t") \
  FSTP("s_memrealtime s[94:95]\n\t") \
  FSTP("s_memtime s[92:93]\n\t") \
  "v_mfma_f32_16x16x32_bf16 v[0:3], a[128:131], v[24:27], v[0:3]\n\t" \
  "buffer_load_dwordx4 a[32:35], v248, s[16:19], s97 offen sc1\n\t" \
  "v_mfma_f32_16x16x32_bf16 v[32:35], a[128:131], v[56:59], v[32:35]\n\t" \
  "ds_read_b64_tr_b16 a[160:161], v184 offset:8192\n\t" \
  "v_lshlrev_b32 v232, 16, v94\n\t" \
  "v_mfma_f32_16x16x32_bf16 v[64:67], a[128:131], v[88:91], v[64:67]\n\t" \
  "ds_read_b64_tr_b16 a[162:163], v184 offset:9216\n\t" \
  "v_and_b32 v233, s98, v94\n\t" \
  "v_mfma_f32_16x16x32_bf16 v[96:99], a[128:131], v[120:123], v[96:99]\n\t" \
  "v_lshlrev_b32 v234, 16, v95\n\t" \
  "v_and_b32 v235, s98, v95\n\t" \
  "v_mfma_f32_16x16x32_bf16 v[100:103], a[132:135], v[120:123], v[100:103]\n\t" \
  "buffer_load_dwordx4 a[36:39], v249, s[16:19], s97 offen sc1\n\t" \
  "v_mfma_f32_16x16x32_bf16 v[68:71], a[132:135], v[88:91], v[68:71]\n\t" \
  "ds_read_b64_tr_b16 a[164:165], v185 offset:8192\n\t" \
  "v_lshlrev_b32 v236, 16, v124\n\t" \
  "v_mfma_f32_16x16x32_bf16 v[36:39], a[132:135], v[56:59], v[36:39]\n\t" \
  "ds_read_b64_tr_b16 a[166:167], v185 offset:9216\n\t" \
  "v_and_b32 v237, s98, v124\n\t" \
  "v_mfma_f32_16x16x32_bf16 v[4:7], a[132:135], v[24:27], v[4:7]\n\t" \
  "ds_read_b64_tr_b16 a[168:169], v186 offset:8192\n\t" \
  "v_cvt_pk_bf16_f32 v29, v178, v179\n\t" \
  "v_mfma_f32_16x16x32_bf16 v[8:11], a[136:139], v[24:27], v[8:11]\n\t" \
  "buffer_load_dwordx4 a[40:43], v250, s[16:19], s97 offen sc1\n\t" \
  "v_mfma_f32_16x16x32_bf16 v[40:43], a[136:139], v[56:59], v[40:43]\n\t" \
  "ds_read_b64_tr_b16 a[170:171], v186 offset:9216\n\t" \
  "v_lshlrev_b32 v238, 16, v125\n\t" \
  "v_mfma_f32_16x16x32_bf16 v[72:75], a[136:139], v[88:91], v[72:75]\n\t" \
  "ds_read_b64_tr_b16 a[172:173], v187 offset:8192\n\t" \
  "v_and_b32 v239, s98, v125\n\t" \
  "v_mfma_f32_16x16x32_bf16 v[104:107], a[136:139], v[120:123], v[104:107]\n\t" \
  "v_cvt_pk_bf16_f32 v30, v180, v181\n\t" \
  "v_cvt_pk_bf16_f32 v31, v182, v183\n\t" \
  "v_mfma_f32_16x16x32_bf16 v[108:111], a[140:143], v[120:123], v[108:111]\n\t" \
  "buffer_load_dwordx4 a[44:47], v251, s[16:19], s97 offen sc1\n\t" \
  "v_mfma_f32_16x16x32_bf16 v[76:79], a[140:143], v[88:91], v[76:79]\n\t" \
  "s_add_u32 s97, s97, s83\n\t" \
  "ds_read_b64_tr_b16 a[174:175], v187 offset:9216\n\t" \
  "v_lshlrev_b32 v240, 16, v126\n\t" \
  "v_mfma_f32_16x16x32_bf16 v[44:47], a[140:143], v[56:59], v[44:47]\n\t" \
  "ds_read_b64_tr_b16 a[176:177], v188 offset:8192\n\t" \
  "v_and_b32 v241, s98, v126\n\t" \
  "v_mfma_f32_16x16x32_bf16 v[12:15], a[140:143], v[24:27], v[12:15]\n\t" \
  "buffer_load_dwordx4 a[48:51], v248, s[16:19], s97 offen sc1\n\t" \
  "v_mfma_f32_16x16x32_bf16 a[64:67], a[144:147], v[24:27], a[64:67]\n\t" \
  "ds_read_b64_tr_b16 a[178:179], v188 offset:9216\n\t" \
  "v_cvt_pk_bf16_f32 v60, v220, v221\n\t" \
  "v_mfma_f32_16x16x32_bf16 a[68:71], a[144:147], v[56:59], a[68:71]\n\t" \
  "ds_read_b64_tr_b16 a[180:181], v189 offset:8192\n\t" \
  "v_cvt_pk_bf16_f32 v61, v222, v223\n\t" \
  "v_mfma_f32_16x16x32_bf16 a[72:75], a[144:147], v[88:91], a[72:75]\n\t" \
  "ds_read_b64_tr_b16 a[182:183], v189 offset:9216\n\t" \
  "v_lshlrev_b32 v242, 16, v127\n\t" \
  "v_mfma_f32_16x16x32_bf16 a[76:79], a[144:147], v[120:123], a[76:79]\n\t" \
  "buffer_load_dwordx4 a[52:55], v249, s[16:19], s97 offen sc1\n\t" \
  "v_mfma_f32_16x16x32_bf16 a[92:95], a[148:151], v[120:123], a[92:95]\n\t" \
  "ds_read_b64_tr_b16 a[184:185], v190 offset:8192\n\t" \
  "v_and_b32 v243, s98, v127\n\t" \
  "v_mfma_f32_16x16x32_bf16 a[88:91], a[148:151], v[88:91], a[88:91]\n\t" \
  "buffer_load_dwordx4 a[56:59], v250, s[16:19], s97 offen sc1\n\t" \
  "ds_read_b64_tr_b16 a[186:187], v190 offset:9216\n\t" \
  "ds_read_b64_tr_b16 a[188:189], v191 offset:8192\n\t" \
  "v_cvt_pk_bf16_f32 v62, v224, v225\n\t" \
  "v_cvt_pk_bf16_f32 v63, v226, v227\n\t" \
  "v_pk_fma_f32 v[228:229], v[228:229], v[168:169], v[170:171] op_sel_hi:[1,0,0]\n\t" \
  "v_pk_fma_f32 v[230:231], v[230:231], v[168:169], v[170:171] op_sel_hi:[1,0,0]\n\t" \
  "v_pk_fma_f32 v[232:233], v[232:233], v[168:169], v[170:171] op_sel_hi:[1,0,0]\n\t" \
  "v_pk_fma_f32 v[234:235], v[234:235], v[168:169], v[170:171] op_sel_hi:[1,0,0]\n\t" \
  "v_pk_fma_f32 v[228:229], v[228:229], v[144:145], v[152:153]\n\t" \
  "v_pk_fma_f32 v[230:231], v[230:231], v[146:147], v[154:155]\n\t" \
  "v_pk_fma_f32 v[232:233], v[232:233], v[148:149], v[156:157]\n\t" \
  "v_pk_fma_f32 v[234:235], v[234:235], v[150:151], v[158:159]\n\t" \
  "v_pk_fma_f32 v[236:237], v[236:237], v[172:173], v[174:175] op_sel_hi:[1,0,0]\n\t" \
  "v_pk_fma_f32 v[238:239], v[238:239], v[172:173], v[174:175] op_sel_hi:[1,0,0]\n\t" \
  "v_pk_fma_f32 v[240:241], v[240:241], v[172:173], v[174:175] op_sel_hi:[1,0,0]\n\t" \
  "v_pk_fma_f32 v[242:243], v[242:243], v[172:173], v[174:175] op_sel_hi:[1,0,0]\n\t" \
  "v_pk_fma_f32 v[236:237], v[236:237], v[144:145], v[152:153]\n\t" \
  "v_pk_fma_f32 v[238:239], v[238:239], v[146:147], v[154:155]\n\t" \
  "v_pk_fma_f32 v[240:241], v[240:241], v[148:149], v[156:157]\n\t" \
  "v_pk_fma_f32 v[242:243], v[242:243], v[150:151], v[158:159]\n\t" \
  "v_cvt_pk_bf16_f32 v92, v228, v229\n\t" \
  "v_mfma_f32_16x16x32_bf16 a[84:87], a[148:151], v[56:59], a[84:87]\n\t" \
  "v_cvt_pk_bf16_f32 v93, v230, v231\n\t" \
  "v_cvt_pk_bf16_f32 v94, v232, v233\n\t" \
  "v_mfma_f32_16x16x32_bf16 a[80:83], a[148:151], v[24:27], a[80:83]\n\t" \
  "v_cvt_pk_bf16_f32 v95, v234, v235\n\t" \
  "v_cvt_pk_bf16_f32 v124, v236, v237\n\t" \
  "v_mfma_f32_16x16x32_bf16 a[96:99], a[152:155], v[24:27], a[96:99]\n\t" \
  "v_cvt_pk_bf16_f32 v125, v238, v239\n\t" \
  "v_cvt_pk_bf16_f32 v126, v240, v241\n\t" \
  "v_mfma_f32_16x16x32_bf16 a[100:103], a[152:155], v[56:59], a[100:103]\n\t" \
  "ds_read_b64_tr_b16 a[190:191], v191 offset:9216\n\t" \
  "v_cvt_pk_bf16_f32 v127, v242, v243\n\t" \
  "v_mfma_f32_16x16x32_bf16 a[104:107], a[152:155], v[88:91], a[104:107]\n\t" \
  "buffer_load_dwordx4 a[60:63], v251, s[16:19], s97 offen sc1\n\t" \
  "v_mfma_f32_16x16x32_bf16 a[108:111], a[152:155], v[120:123], a[108:111]\n\t" \
  "v_mfma_f32_16x16x32_bf16 a[124:127], a[156:159], v[120:123], a[124:127]\n\t" \
  "v_mfma_f32_16x16x32_bf16 a[120:123], a[156:159], v[88:91], a[120:123]\n\t" \
  "s_add_u32 s97, s78, 3145728\n\t" \
  "s_bfe_u32 s97, s97, 0x140003\n\t" \
  "s_add_u32 s97, s97, s82\n\t" \
  "v_mfma_f32_16x16x32_bf16 a[116:119], a[156:159], v[56:59], a[116:119]\n\t" \
  "s_waitcnt lgkmcnt(0)\n\t" \
  "v_mfma_f32_16x16x32_bf16 a[112:115], a[156:159], v[24:27], a[112:115]\n\t" \
  "s_barrier\n\t" \
  FSTP("v_writelane_b32 v255, s94, 48\n\t") \
  FSTP("v_writelane_b32 v255, s92, 49\n\t") \
  FSTP("s_memrealtime s[94:95]\n\t") \
  FSTP("s_memtime s[92:93]\n\t") \
  "v_mfma_f32_16x16x32_bf16 v[0:3], a[160:163], v[28:31], v[0:3]\n\t" \
  "buffer_load_dwordx4 a[192:195], v248, s[16:19], s97 offen sc1\n\t" \
  "v_mfma_f32_16x16x32_bf16 v[32:35], a[160:163], v[60:63], v[32:35]\n\t" \
  "ds_read_b64_tr_b16 a[128:129], v184 offset:16384\n\t" \
  "v_mfma_f32_16x16x32_bf16 v[64:67], a[160:163], v[92:95], v[64:67]\n\t" \
  "ds_read_b64_tr_b16 a[130:131], v184 offset:17408\n\t" \
  "v_mfma_f32_16x16x32_bf16 v[96:99], a[160:163], v[124:127], v[96:99]\n\t" \
  "ds_write_b32 v219, v218\n\t" \
  "v_mfma_f32_16x16x32_bf16 v[100:103], a[164:167], v[124:127], v[100:103]\n\t" \
  "buffer_load_dwordx4 a[196:199], v249, s[16:19], s97 offen sc1\n\t" \
  "v_mfma_f32_16x16x32_bf16 v[68:71], a[164:167], v[92:95], v[68:71]\n\t" \
  "ds_read_b64_tr_b16 a[132:133], v185 offset:16384\n\t" \
  "v_and_b32 v219, 0x7fffffe0, v219\n\t" \
  "v_mfma_f32_16x16x32_bf16 v[36:39], a[164:167], v[60:63], v[36:39]\n\t" \
  "ds_read_b64_tr_b16 a[134:135], v185 offset:17408\n\t" \
  "v_mfma_f32_16x16x32_bf16 v[4:7], a[164:167], v[28:31], v[4:7]\n\t" \
  "ds_read_b64_tr_b16 a[136:137], v186 offset:16384\n\t" \
  "v_mfma_f32_16x16x32_bf16 v[8:11], a[168:171], v[28:31], v[8:11]\n\t" \
  "buffer_load_dwordx4 a[200:203], v250, s[16:19], s97 offen sc1\n\t" \
  "v_mfma_f32_16x16x32_bf16 v[40:43], a[168:171], v[60:63], v[40:43]\n\t" \
  "ds_read_b64_tr_b16 a[138:139], v186 offset:17408\n\t" \
  "v_mfma_f32_16x16x32_bf16 v[72:75], a[168:171], v[92:95], v[72:75]\n\t" \
  "ds_read_b64_tr_b16 a[140:141], v187 offset:16384\n\t" \
  "v_mfma_f32_16x16x32_bf16 v[104:107], a[168:171], v[124:127], v[104:107]\n\t" \
  "ds_read_b128 v[168:171], v219 offset:0\n\t" \
  "v_mfma_f32_16x16x32_bf16 v[108:111], a[172:175], v[124:127], v[108:111]\n\t" \
  "buffer_load_dwordx4 a[204:207], v251, s[16:19], s97 offen sc1\n\t" \
  "v_mfma_f32_16x16x32_bf16 v[76:79], a[172:175], v[92:95], v[76:79]\n\t" \
  "ds_read_b64_tr_b16 a[142:143], v187 offset:17408\n\t" \
  "s_add_u32 s97, s97, s83\n\t" \
  "v_mfma_f32_16x16x32_bf16 v[44:47], a[172:175], v[60:63], v[44:47]\n\t" \
  "ds_read_b64_tr_b16 a[144:145], v188 offset:16384\n\t" \
  "v_mfma_f32_16x16x32_bf16 v[12:15], a[172:175], v[28:31], v[12:15]\n\t" \
  "buffer_load_dwordx4 a[208:211], v248, s[16:19], s97 offen sc1\n\t" \
  "v_mfma_f32_16x16x32_bf16 a[64:67], a[176:179], v[28:31], a[64:67]\n\t" \
  "ds_read_b64_tr_b16 a[146:147], v188 offset:17408\n\t" \
  "v_mfma_f32_16x16x32_bf16 a[68:71], a[176:179], v[60:63], a[68:71]\n\t" \
  "ds_read_b64_tr_b16 a[148:149], v189 offset:16384\n\t" \
  "v_mfma_f32_16x16x32_bf16 a[72:75], a[176:179], v[92:95], a[72:75]\n\t" \
  "ds_read_b64_tr_b16 a[150:151], v189 offset:17408\n\t" \
  "v_mfma_f32_16x16x32_bf16 a[76:79], a[176:179], v[124:127], a[76:79]\n\t" \
  "buffer_load_dwordx4 a[212:215], v249, s[16:19], s97 offen sc1\n\t" \
  "v_mfma_f32_16x16x32_bf16 a[92:95], a[180:183], v[124:127], a[92:95]\n\t" \
  "ds_read_b64_tr_b16 a[152:153], v190 offset:16384\n\t" \
  "v_mfma_f32_16x16x32_bf16 a[88:91], a[180:183], v[92:95], a[88:91]\n\t" \
  "ds_read_b128 v[172:175], v219 offset:16\n\t" \
  "v_mfma_f32_16x16x32_bf16 a[84:87], a[180:183], v[60:63], a[84:87]\n\t" \
  "ds_read_b64_tr_b16 a[154:155], v190 offset:17408\n\t" \
  "v_mfma_f32_16x16x32_bf16 a[80:83], a[180:183], v[28:31], a[80:83]\n\t" \
  "buffer_load_dwordx4 a[216:219], v250, s[16:19], s97 offen sc1\n\t" \
  "v_mfma_f32_16x16x32_bf16 a[96:99], a[184:187], v[28:31], a[96:99]\n\t" \
  "ds_read_b64_tr_b16 a[156:157], v191 offset:16384\n\t" \
  "v_mfma_f32_16x16x32_bf16 a[100:103], a[184:187], v[60:63], a[100:103]\n\t" \
  "ds_read_b64_tr_b16 a[158:159], v191 offset:17408\n\t" \
  "v_mfma_f32_16x16x32_bf16 a[104:107], a[184:187], v[92:95], a[104:107]\n\t" \
  "buffer_load_dwordx4 a[220:223], v251, s[16:19], s97 offen sc1\n\t" \
  "v_mfma_f32_16x16x32_bf16 a[108:111], a[184:187], v[124:127], a[108:111]\n\t" \
  "v_mfma_f32_16x16x32_bf16 a[124:127], a[188:191], v[124:127], a[124:127]\n\t" \
  "v_mfma_f32_16x16x32_bf16 a[120:123], a[188:191], v[92:95], a[120:123]\n\t" \
  "s_add_u32 s97, s78, 3670016\n\t" \
  "s_bfe_u32 s97, s97, 0x140003\n\t" \
  "s_add_u32 s97, s97, s82\n\t" \
  "v_mfma_f32_16x16x32_bf16 a[116:119], a[188:191], v[60:63], a[116:119]\n\t" \
  "v_mfma_f32_16x16x32_bf16 a[112:115], a[188:191], v[28:31], a[112:115]\n\t" \
  "s_waitcnt vmcnt(20) lgkmcnt(0)\n\t" \
  FSTP("v_writelane_b32 v255, s94, 50\n\t") \
  FSTP("v_writelane_b32 v255, s92, 51\n\t") \
  FSTP("s_memrealtime s[94:95]\n\t") \
  FSTP("s_memtime s[92:93]\n\t") \
  FSTS("s_memrealtime s[94:95]\n\t") \
  FSTS("s_waitcnt lgkmcnt(0)\n\t") \
  FSTS("v_writelane_b32 v255, s94, 14\n\t") \
  "v_mfma_f32_16x16x32_bf16 a[64:67], a[144:147], a[0:3], a[64:67]\n\t" \
  "buffer_load_dwordx4 a[224:227], v248, s[16:19], s97 offen sc1\n\t" \
  "v_mfma_f32_16x16x32_bf16 a[68:71], a[144:147], a[4:7], a[68:71]\n\t" \
  "s_bfe_u32 s39, s2, 0x30003\n\t" \
  "ds_read_b64_tr_b16 a[160:161], v184 offset:24576\n\t" \
  "s_lshl_b32 s40, s39, 18\n\t" \
  "v_mfma_f32_16x16x32_bf16 a[72:75], a[144:147], a[8:11], a[72:75]\n\t" \
  "s_lshr_b32 s98, s2, 3\n\t" \
  "ds_read_b64_tr_b16 a[162:163], v184 offset:25600\n\t" \
  "v_accvgpr_read_b32 v16, a64\n\t" \
  "v_mfma_f32_16x16x32_bf16 a[76:79], a[144:147], a[12:15], a[76:79]\n\t" \
  "s_lshl_b32 s41, s3, 5\n\t" \
  "v_accvgpr_read_b32 v17, a65\n\t" \
  "v_accvgpr_read_b32 v18, a66\n\t" \
  "v_mfma_f32_16x16x32_bf16 a[92:95], a[148:151], a[12:15], a[92:95]\n\t" \
  ".Lhs2:\n\ts_cbranch_execz .Lhs2n\n\t" \
  "buffer_load_dwordx4 a[228:231], v249, s[16:19], s97 offen sc1\n\t" \
  "v_mfma_f32_16x16x32_bf16 a[88:91], a[148:151], a[8:11], a[88:91]\n\t" \
  "s_add_u32 s41, s41, s98\n\t" \
  "ds_read_b64_tr_b16 a[164:165], v185 offset:24576\n\t" \
  "v_accvgpr_read_b32 v19, a67\n\t" \
  "v_mfma_f32_16x16x32_bf16 a[84:87], a[148:151], a[4:7], a[84:87]\n\t" \
  "s_lshl_b32 s41, s41, 7\n\t" \
  "ds_read_b64_tr_b16 a[166:167], v185 offset:25600\n\t" \
  "v_accvgpr_read_b32 v48, a68\n\t" \
  "v_mfma_f32_16x16x32_bf16 a[80:83], a[148:151], a[0:3], a[80:83]\n\t" \
  "s_and_b32 s12, s2, 3\n\t" \
  "ds_read_b64_tr_b16 a[168:169], v186 offset:24576\n\t" \
  "v_accvgpr_read_b32 v49, a69\n\t" \
  "v_mfma_f32_16x16x32_bf16 a[96:99], a[152:155], a[0:3], a[96:99]\n\t" \
  "buffer_load_dwordx4 a[232:235], v250, s[16:19], s97 offen sc1\n\t" \
  "v_mfma_f32_16x16x32_bf16 a[100:103], a[152:155], a[4:7], a[100:103]\n\t" \
  "s_bfe_u32 s13, s2, 0x10002\n\t" \
  "ds_read_b64_tr_b16 a[170:171], v186 offset:25600\n\t" \
  "v_accvgpr_read_b32 v50, a70\n\t" \
  "v_mfma_f32_16x16x32_bf16 a[104:107], a[152:155], a[8:11], a[104:107]\n\t" \
  "s_lshl1_add_u32 s12, s12, s13\n\t" \
  "ds_read_b64_tr_b16 a[172:173], v187 offset:24576\n\t" \
  "v_accvgpr_read_b32 v51, a71\n\t" \
  "v_mfma_f32_16x16x32_bf16 a[108:111], a[152:155], a[12:15], a[108:111]\n\t" \
  "s_bfe_u32 s59, s2, 0x20003\n\t" \
  "v_accvgpr_read_b32 v80, a72\n\t" \
  "v_accvgpr_read_b32 v81, a73\n\t" \
  "v_mfma_f32_16x16x32_bf16 a[124:127], a[156:159], a[12:15], a[124:127]\n\t" \
  "buffer_load_dwordx4 a[236:239], v251, s[16:19], s97 offen sc1\n\t" \
  "v_mfma_f32_16x16x32_bf16 a[120:123], a[156:159], a[8:11], a[120:123]\n\t" \
  "s_add_u32 s96, s78, 4718592\n\t" \
  "s_and_b32 s96, s96, 0x780000\n\t" \
  "s_add_u32 m0, s76, 98304\n\t" \
  "v_mfma_f32_16x16x32_bf16 a[116:119], a[156:159], a[4:7], a[116:119]\n\t" \
  "s_or_b32 s59, s59, 24\n\t" \
  "ds_read_b64_tr_b16 a[174:175], v187 offset:25600\n\t" \
  "v_accvgpr_read_b32 v82, a74\n\t" \
  "v_mfma_f32_16x16x32_bf16 a[112:115], a[156:159], a[0:3], a[112:115]\n\t" \
  "buffer_load_dwordx4 v200, s[20:23], s96 offen offset:0 lds\n\t" \
  "v_mfma_f32_16x16x32_bf16 v[0:3], a[128:131], a[0:3], v[0:3]\n\t" \
  "ds_read_b64_tr_b16 a[176:177], v188 offset:24576\n\t" \
  "v_accvgpr_read_b32 v83, a75\n\t" \
  "v_mfma_f32_16x16x32_bf16 v[32:35], a[128:131], a[4:7], v[32:35]\n\t" \
  "ds_read_b64_tr_b16 a[178:179], v188 offset:25600\n\t" \
  "v_accvgpr_read_b32 v112, a76\n\t" \
  "v_mfma_f32_16x16x32_bf16 v[64:67], a[128:131], a[8:11], v[64:67]\n\t" \
  "ds_read_b64_tr_b16 a[180:181], v189 offset:24576\n\t" \
  "v_accvgpr_read_b32 v113, a77\n\t" \
  "v_mfma_f32_16x16x32_bf16 v[96:99], a[128:131], a[12:15], v[96:99]\n\t" \
  "buffer_load_dwordx4 v201, s[20:23], s96 offen offset:1024 lds\n\t" \
  "v_mfma_f32_16x16x32_bf16 v[100:103], a[132:135], a[12:15], v[100:103]\n\t" \
  "ds_read_b64_tr_b16 a[182:183], v189 offset:25600\n\t" \
  "v_accvgpr_read_b32 v114, a78\n\t" \
  "v_mfma_f32_16x16x32_bf16 v[68:71], a[132:135], a[8:11], v[68:71]\n\t" \
  "ds_read_b64_tr_b16 a[184:185], v190 offset:24576\n\t" \
  "v_accvgpr_read_b32 v115, a79\n\t" \
  "v_mfma_f32_16x16x32_bf16 v[36:39], a[132:135], a[4:7], v[36:39]\n\t" \
  "ds_read_b64_tr_b16 a[186:187], v190 offset:25600\n\t" \
  "v_accvgpr_read_b32 v116, a92\n\t" \
  "v_mfma_f32_16x16x32_bf16 v[4:7], a[132:135], a[0:3], v[4:7]\n\t" \
  "buffer_load_dwordx4 v202, s[20:23], s96 offen offset:2048 lds\n\t" \
  "v_mfma_f32_16x16x32_bf16 v[8:11], a[136:139], a[0:3], v[8:11]\n\t" \
  "ds_read_b64_tr_b16 a[188:189], v191 offset:24576\n\t" \
  "v_accvgpr_read_b32 v117, a93\n\t" \
  "v_mfma_f32_16x16x32_bf16 v[40:43], a[136:139], a[4:7], v[40:43]\n\t" \
  "ds_read_b64_tr_b16 a[190:191], v191 offset:25600\n\t" \
  "v_accvgpr_read_b32 v118, a94\n\t" \
  "v_mfma_f32_16x16x32_bf16 v[72:75], a[136:139], a[8:11], v[72:75]\n\t" \
  "v_accvgpr_read_b32 v119, a95\n\t" \
  "v_accvgpr_read_b32 v84, a88\n\t" \
  "v_mfma_f32_16x16x32_bf16 v[104:107], a[136:139], a[12:15], v[104:107]\n\t" \
  "buffer_load_dwordx4 v203, s[20:23], s96 offen offset:3072 lds\n\t" \
  "v_mfma_f32_16x16x32_bf16 v[108:111], a[140:143], a[12:15], v[108:111]\n\t" \
  "v_accvgpr_read_b32 v85, a89\n\t" \
  "v_accvgpr_read_b32 v86, a90\n\t" \
  "v_mfma_f32_16x16x32_bf16 v[76:79], a[140:143], a[8:11], v[76:79]\n\t" \
  "s_add_u32 s97, s97, s83\n\t" \
  "v_accvgpr_read_b32 v87, a91\n\t" \
  "v_accvgpr_read_b32 v52, a84\n\t" \
  "v_mfma_f32_16x16x32_bf16 v[44:47], a[140:143], a[4:7], v[44:47]\n\t" \
  "s_waitcnt vmcnt(24) lgkmcnt(0)\n\t" \
  "v_accvgpr_read_b32 v53, a85\n\t" \
  "v_accvgpr_read_b32 v54, a86\n\t" \
  "v_mfma_f32_16x16x32_bf16 v[12:15], a[140:143], a[0:3], v[12:15]\n\t" \
  "s_barrier\n\t" \
  "v_mfma_f32_16x16x32_bf16 a[96:99], a[184:187], a[16:19], a[96:99]\n\t" \
  "buffer_load_dwordx4 a[240:243], v248, s[16:19], s97 offen sc1\n\t" \
  "v_mfma_f32_16x16x32_bf16 a[100:103], a[184:187], a[20:23], a[100:103]\n\t" \
  "ds_read_b64_tr_b16 a[128:129], v184 offset:32768\n\t" \
  "v_accvgpr_read_b32 v55, a87\n\t" \
  "v_mfma_f32_16x16x32_bf16 a[104:107], a[184:187], a[24:27], a[104:107]\n\t" \
  "ds_read_b64_tr_b16 a[130:131], v184 offset:33792\n\t" \
  "v_accvgpr_read_b32 v20, a80\n\t" \
  "v_mfma_f32_16x16x32_bf16 a[108:111], a[184:187], a[28:31], a[108:111]\n\t" \
  "v_accvgpr_read_b32 v21, a81\n\t" \
  "v_accvgpr_read_b32 v22, a82\n\t" \
  "v_mfma_f32_16x16x32_bf16 a[124:127], a[188:191], a[28:31], a[124:127]\n\t" \
  "buffer_load_dwordx4 a[244:247], v249, s[16:19], s97 offen sc1\n\t" \
  "v_mfma_f32_16x16x32_bf16 a[120:123], a[188:191], a[24:27], a[120:123]\n\t" \
  "ds_read_b64_tr_b16 a[132:133], v185 offset:32768\n\t" \
  "v_accvgpr_read_b32 v23, a83\n\t" \
  "v_mfma_f32_16x16x32_bf16 a[116:119], a[188:191], a[20:23], a[116:119]\n\t" \
  "ds_read_b64_tr_b16 a[134:135], v185 offset:33792\n\t" \
  "v_accvgpr_read_b32 v24, a96\n\t" \
  "v_mfma_f32_16x16x32_bf16 a[112:115], a[188:191], a[16:19], a[112:115]\n\t" \
  "ds_read_b64_tr_b16 a[136:137], v186 offset:32768\n\t" \
  "v_accvgpr_read_b32 v25, a97\n\t" \
  "v_mfma_f32_16x16x32_bf16 v[0:3], a[160:163], a[16:19], v[0:3]\n\t" \
  "buffer_load_dwordx4 a[248:251], v250, s[16:19], s97 offen sc1\n\t" \
  "v_mfma_f32_16x16x32_bf16 v[32:35], a[160:163], a[20:23], v[32:35]\n\t" \
  "ds_read_b64_tr_b16 a[138:139], v186 offset:33792\n\t" \
  "v_accvgpr_read_b32 v26, a98\n\t" \
  "v_mfma_f32_16x16x32_bf16 v[64:67], a[160:163], a[24:27], v[64:67]\n\t" \
  "ds_read_b64_tr_b16 a[140:141], v187 offset:32768\n\t" \
  "v_accvgpr_read_b32 v27, a99\n\t" \
  "v_mfma_f32_16x16x32_bf16 v[96:99], a[160:163], a[28:31], v[96:99]\n\t" \
  "v_accvgpr_read_b32 v56, a100\n\t" \
  "v_accvgpr_read_b32 v57, a101\n\t" \
  "v_mfma_f32_16x16x32_bf16 v[100:103], a[164:167], a[28:31], v[100:103]\n\t" \
  "buffer_load_dwordx4 a[252:255], v251, s[16:19], s97 offen sc1\n\t" \
  "v_mfma_f32_16x16x32_bf16 v[68:71], a[164:167], a[24:27], v[68:71]\n\t" \
  "s_add_u32 s97, s78, 4194304\n\t" \
  "s_bfe_u32 s97, s97, 0x140003\n\t" \
  "s_add_u32 s97, s97, s82\n\t" \
  "v_mfma_f32_16x16x32_bf16 v[36:39], a[164:167], a[20:23], v[36:39]\n\t" \
  "ds_read_b64_tr_b16 a[142:143], v187 offset:33792\n\t" \
  "v_accvgpr_read_b32 v58, a102\n\t" \
  "v_mfma_f32_16x16x32_bf16 v[4:7], a[164:167], a[16:19], v[4:7]\n\t" \
  "buffer_load_dwordx4 a[0:3], v248, s[16:19], s97 offen sc1\n\t" \
  "v_mfma_f32_16x16x32_bf16 v[8:11], a[168:171], a[16:19], v[8:11]\n\t" \
  "ds_read_b64_tr_b16 a[144:145], v188 offset:32768\n\t" \
  "v_accvgpr_read_b32 v59, a103\n\t" \
  "v_mfma_f32_16x16x32_bf16 v[40:43], a[168:171], a[20:23], v[40:43]\n\t" \
  "ds_read_b64_tr_b16 a[146:147], v188 offset:33792\n\t" \
  "v_accvgpr_read_b32 v88, a104\n\t" \
  "v_mfma_f32_16x16x32_bf16 v[72:75], a[168:171], a[24:27], v[72:75]\n\t" \
  "ds_read_b64_tr_b16 a[148:149], v189 offset:32768\n\t" \
  "v_accvgpr_read_b32 v89, a105\n\t" \
  "v_mfma_f32_16x16x32_bf16 v[104:107], a[168:171], a[28:31], v[104:107]\n\t" \
  "buffer_load_dwordx4 a[4:7], v249, s[16:19], s97 offen sc1\n\t" \
  "v_mfma_f32_16x16x32_bf16 v[108:111], a[172:175], a[28:31], v[108:111]\n\t" \
  "ds_read_b64_tr_b16 a[150:151], v189 offset:33792\n\t" \
  "v_accvgpr_read_b32 v90, a106\n\t" \
  "v_mfma_f32_16x16x32_bf16 v[76:79], a[172:175], a[24:27], v[76:79]\n\t" \
  "ds_read_b64_tr_b16 a[152:153], v190 offset:32768\n\t" \
  "v_accvgpr_read_b32 v91, a107\n\t" \
  "v_mfma_f32_16x16x32_bf16 v[44:47], a[172:175], a[20:23], v[44:47]\n\t" \
  "ds_read_b64_tr_b16 a[154:155], v190 offset:33792\n\t" \
  "v_accvgpr_read_b32 v120, a108\n\t" \
  "v_mfma_f32_16x16x32_bf16 v[12:15], a[172:175], a[16:19], v[12:15]\n\t" \
  "buffer_load_dwordx4 a[8:11], v250, s[16:19], s97 offen sc1\n\t" \
  "v_mfma_f32_16x16x32_bf16 v[16:19], a[176:179], a[16:19], v[16:19]\n\t" \
  "ds_read_b64_tr_b16 a[156:157], v191 offset:32768\n\t" \
  "v_accvgpr_read_b32 v121, a109\n\t" \
  "v_mfma_f32_16x16x32_bf16 v[48:51], a[176:179], a[20:23], v[48:51]\n\t" \
  "ds_read_b64_tr_b16 a[158:159], v191 offset:33792\n\t" \
  "v_accvgpr_read_b32 v122, a110\n\t" \
  "v_mfma_f32_16x16x32_bf16 v[80:83], a[176:179], a[24:27], v[80:83]\n\t" \
  "v_accvgpr_read_b32 v123, a111\n\t" \
  "v_accvgpr_read_b32 v124, a124\n\t" \
  "v_mfma_f32_16x16x32_bf16 v[112:115], a[176:179], a[28:31], v[112:115]\n\t" \
  "buffer_load_dwordx4 a[12:15], v251, s[16:19], s97 offen sc1\n\t" \
  "v_mfma_f32_16x16x32_bf16 v[116:119], a[180:183], a[28:31], v[116:119]\n\t" \
  "v_mbcnt_lo_u32_b32 v217, -1, 0\n\t" \
  "v_mbcnt_hi_u32_b32 v217, -1, v217\n\t" \
  "v_mfma_f32_16x16x32_bf16 v[84:87], a[180:183], a[24:27], v[84:87]\n\t" \
  "v_cmp_gt_u32 vcc, 16, v217\n\t" \
  "v_lshlrev_b32 v217, 7, v217\n\t" \
  "v_mfma_f32_16x16x32_bf16 v[52:55], a[180:183], a[20:23], v[52:55]\n\t" \
  "v_bfrev_b32 v215, 1\n\t" \
  "v_cndmask_b32 v217, v215, v217, vcc\n\t" \
  "v_mfma_f32_16x16x32_bf16 v[20:23], a[180:183], a[16:19], v[20:23]\n\t" \
  "s_waitcnt vmcnt(28) lgkmcnt(0)\n\t" \
  "v_bfe_u32 v215, v217, 10, 1\n\t" \
  "s_mov_b32 s99, 11264\n\t" \
  "v_mfma_f32_16x16x32_bf16 v[0:3], a[128:131], a[32:35], v[0:3]\n\t" \
  "v_mad_u32_u24 v215, v215, s99, v217\n\t" \
  "s_add_u32 s97, s97, s83\n\t" \
  "v_mfma_f32_16x16x32_bf16 v[32:35], a[128:131], a[36:39], v[32:35]\n\t" \
  "buffer_load_dwordx4 a[16:19], v248, s[16:19], s97 offen sc1\n\t" \
  "v_mfma_f32_16x16x32_bf16 v[64:67], a[128:131], a[40:43], v[64:67]\n\t" \
  "ds_read_b64_tr_b16 a[160:161], v184 offset:40960\n\t" \
  "v_accvgpr_read_b32 v125, a125\n\t" \
  "v_mfma_f32_16x16x32_bf16 v[96:99], a[128:131], a[44:47], v[96:99]\n\t" \
  "ds_read_b64_tr_b16 a[162:163], v184 offset:41984\n\t" \
  "v_accvgpr_read_b32 v126, a126\n\t" \
  "v_mfma_f32_16x16x32_bf16 v[100:103], a[132:135], a[44:47], v[100:103]\n\t" \
  "buffer_load_dwordx4 a[20:23], v249, s[16:19], s97 offen sc1\n\t" \
  "v_mfma_f32_16x16x32_bf16 v[68:71], a[132:135], a[40:43], v[68:71]\n\t" \
  "ds_read_b64_tr_b16 a[164:165], v185 offset:40960\n\t" \
  "v_accvgpr_read_b32 v127, a127\n\t" \
  "v_mfma_f32_16x16x32_bf16 v[36:39], a[132:135], a[36:39], v[36:39]\n\t" \
  "ds_read_b64_tr_b16 a[166:167], v185 offset:41984\n\t" \
  "v_accvgpr_read_b32 v92, a120\n\t" \
  "v_mfma_f32_16x16x32_bf16 v[4:7], a[132:135], a[32:35], v[4:7]\n\t" \
  "ds_read_b64_tr_b16 a[168:169], v186 offset:40960\n\t" \
  "v_accvgpr_read_b32 v93, a121\n\t" \
  "v_mfma_f32_16x16x32_bf16 v[8:11], a[136:139], a[32:35], v[8:11]\n\t" \
  "buffer_load_dwordx4 a[24:27], v250, s[16:19], s97 offen sc1\n\t" \
  "v_mfma_f32_16x16x32_bf16 v[40:43], a[136:139], a[36:39], v[40:43]\n\t" \
  "ds_read_b64_tr_b16 a[170:171], v186 offset:41984\n\t" \
  "v_accvgpr_read_b32 v94, a122\n\t" \
  "v_mfma_f32_16x16x32_bf16 v[72:75], a[136:139], a[40:43], v[72:75]\n\t" \
  "ds_read_b64_tr_b16 a[172:173], v187 offset:40960\n\t" \
  "v_accvgpr_read_b32 v95, a123\n\t" \
  "v_mfma_f32_16x16x32_bf16 v[104:107], a[136:139], a[44:47], v[104:107]\n\t" \
  "v_accvgpr_read_b32 v60, a116\n\t" \
  "v_accvgpr_read_b32 v61, a117\n\t" \
  "v_mfma_f32_16x16x32_bf16 v[108:111], a[140:143], a[44:47], v[108:111]\n\t" \
  "buffer_load_dwordx4 a[28:31], v251, s[16:19], s97 offen sc1\n\t" \
  "v_mfma_f32_16x16x32_bf16 v[76:79], a[140:143], a[40:43], v[76:79]\n\t" \
  "s_add_u32 s96, s78, 5242880\n\t" \
  "s_and_b32 s96, s96, 0x780000\n\t" \
  "s_add_u32 m0, s76, 0\n\t" \
  "v_mfma_f32_16x16x32_bf16 v[44:47], a[140:143], a[36:39], v[44:47]\n\t" \
  "ds_read_b64_tr_b16 a[174:175], v187 offset:41984\n\t" \
  "v_accvgpr_read_b32 v62, a118\n\t" \
  "v_mfma_f32_16x16x32_bf16 v[12:15], a[140:143], a[32:35], v[12:15]\n\t" \
  "buffer_load_dwordx4 v200, s[20:23], s96 offen offset:0 lds\n\t" \
  "v_mfma_f32_16x16x32_bf16 v[16:19], a[144:147], a[32:35], v[16:19]\n\t" \
  "ds_read_b64_tr_b16 a[176:177], v188 offset:40960\n\t" \
  "v_accvgpr_read_b32 v63, a119\n\t" \
  "v_mfma_f32_16x16x32_bf16 v[48:51], a[144:147], a[36:39], v[48:51]\n\t" \
  "ds_read_b64_tr_b16 a[178:179], v188 offset:41984\n\t" \
  "v_accvgpr_read_b32 v28, a112\n\t" \
  "v_mfma_f32_16x16x32_bf16 v[80:83], a[144:147], a[40:43], v[80:83]\n\t" \
  "ds_read_b64_tr_b16 a[180:181], v189 offset:40960\n\t" \
  "v_accvgpr_read_b32 v29, a113\n\t" \
  "v_mfma_f32_16x16x32_bf16 v[112:115], a[144:147], a[44:47], v[112:115]\n\t" \
  "buffer_load_dwordx4 v201, s[20:23], s96 offen offset:1024 lds\n\t" \
  "v_mfma_f32_16x16x32_bf16 v[116:119], a[148:151], a[44:47], v[116:119]\n\t" \
  "ds_read_b64_tr_b16 a[182:183], v189 offset:41984\n\t" \
  "v_accvgpr_read_b32 v30, a114\n\t" \
  "v_mfma_f32_16x16x32_bf16 v[84:87], a[148:151], a[40:43], v[84:87]\n\t" \
  "ds_read_b64_tr_b16 a[184:185], v190 offset:40960\n\t" \
  "v_accvgpr_read_b32 v31, a115\n\t" \
  "v_mfma_f32_16x16x32_bf16 v[52:55], a[148:151], a[36:39], v[52:55]\n\t" \
  "ds_read_b64_tr_b16 a[186:187], v190 offset:41984\n\t" \
  "v_mfma_f32_16x16x32_bf16 v[20:23], a[148:151], a[32:35], v[20:23]\n\t" \
  "buffer_load_dwordx4 v202, s[20:23], s96 offen offset:2048 lds\n\t" \
  "v_mfma_f32_16x16x32_bf16 v[24:27], a[152:155], a[32:35], v[24:27]\n\t" \
  "ds_read_b64_tr_b16 a[188:189], v191 offset:40960\n\t" \
  "v_mfma_f32_16x16x32_bf16 v[56:59], a[152:155], a[36:39], v[56:59]\n\t" \
  "ds_read_b64_tr_b16 a[190:191], v191 offset:41984\n\t" \
  "v_mfma_f32_16x16x32_bf16 v[88:91], a[152:155], a[40:43], v[88:91]\n\t" \
  "s_cmp_lt_u32 s98, 24\n\t" \
  "s_cselect_b32 s59, s98, s59\n\t" \
  "s_mul_i32 s53, s98, 57344\n\t" \
  "v_mfma_f32_16x16x32_bf16 v[120:123], a[152:155], a[44:47], v[120:123]\n\t" \
  "buffer_load_dwordx4 v203, s[20:23], s96 offen offset:3072 lds\n\t" \
  "v_mfma_f32_16x16x32_bf16 v[124:127], a[156:159], a[44:47], v[124:127]\n\t" \
  "s_lshl_b32 s14, s59, 5\n\t" \
  "s_sub_u32 s53, s53, s14\n\t" \
  "s_lshl_b32 s52, s98, 16\n\t" \
  "v_mfma_f32_16x16x32_bf16 v[92:95], a[156:159], a[40:43], v[92:95]\n\t" \
  "s_lshl_b32 s14, s3, 14\n\t" \
  "s_add_u32 s52, s52, s14\n\t" \
  "s_lshl_b32 s14, s13, 13\n\t" \
  "v_mfma_f32_16x16x32_bf16 v[60:63], a[156:159], a[36:39], v[60:63]\n\t" \
  "s_waitcnt vmcnt(32) lgkmcnt(0)\n\t" \
  "s_mov_b32 s99, 0\n\t" \
  "s_add_u32 s52, s52, s14\n\t" \
  "v_mfma_f32_16x16x32_bf16 v[28:31], a[156:159], a[32:35], v[28:31]\n\t" \
  "s_barrier\n\t" \
  "v_mfma_f32_16x16x32_bf16 v[0:3], a[160:163], a[48:51], v[0:3]\n\t" \
  "buffer_load_dwordx4 v[136:139], v209, s[8:11], s99 offen nt\n\t" \
  "v_mfma_f32_16x16x32_bf16 v[32:35], a[160:163], a[52:55], v[32:35]\n\t" \
  "s_add_u32 s52, s52, s78\n\t" \
  "ds_read_b64_tr_b16 a[128:129], v184 offset:49152\n\t" \
  "s_lshl_b32 s51, s3, 15\n\t" \
  "v_mfma_f32_16x16x32_bf16 v[64:67], a[160:163], a[56:59], v[64:67]\n\t" \
  "s_add_u32 s51, s51, s78\n\t" \
  "ds_read_b64_tr_b16 a[130:131], v184 offset:50176\n\t" \
  "s_lshl_b32 s14, s39, 8\n\t" \
  "v_mfma_f32_16x16x32_bf16 v[96:99], a[160:163], a[60:63], v[96:99]\n\t" \
  "s_add_u32 s51, s51, s14\n\t" \
  "ds_read_b64_tr_b16 a[132:133], v185 offset:49152\n\t" \
  "s_lshr_b32 s98, s98, 2\n\t" \
  "v_mfma_f32_16x16x32_bf16 v[100:103], a[164:167], a[60:63], v[100:103]\n\t" \
  "s_mov_b32 s99, 12288\n\t" \
  "ds_read_b64_tr_b16 a[134:135], v185 offset:50176\n\t" \
  "s_add_u32 s13, s13, 6\n\t" \
  "v_mfma_f32_16x16x32_bf16 v[68:71], a[164:167], a[56:59], v[68:71]\n\t" \
  "s_cmp_eq_u32 s98, s13\n\t" \
  "s_cselect_b32 s57, 0x80000000, 0\n\t" \
  "s_cselect_b32 s30, 0, 0x8000\n\t" \
  "v_mfma_f32_16x16x32_bf16 v[36:39], a[164:167], a[52:55], v[36:39]\n\t" \
  "buffer_load_dwordx4 v[140:143], v209, s[8:11], s99 offen nt\n\t" \
  "v_mfma_f32_16x16x32_bf16 v[4:7], a[164:167], a[48:51], v[4:7]\n\t" \
  "s_mul_i32 s14, s12, 28\n\t" \
  "ds_read_b64_tr_b16 a[136:137], v186 offset:49152\n\t" \
  "s_add_u32 s59, s59, s14\n\t" \
  "v_mfma_f32_16x16x32_bf16 v[8:11], a[168:171], a[48:51], v[8:11]\n\t" \
  "s_lshl_b32 s14, s59, 15\n\t" \
  "s_add_u32 s28, s28, s14\n\t" \
  "s_addc_u32 s29, s29, 0\n\t" \
  "v_mfma_f32_16x16x32_bf16 v[40:43], a[168:171], a[52:55], v[40:43]\n\t" \
  "s_mul_i32 s14, s3, 1835008\n\t" \
  "ds_read_b64_tr_b16 a[138:139], v186 offset:50176\n\t" \
  "s_lshl_b32 s59, s59, 5\n\t" \
  "v_mfma_f32_16x16x32_bf16 v[72:75], a[168:171], a[56:59], v[72:75]\n\t" \
  "s_mov_b32 s99, 24576\n\t" \
  "ds_read_b64_tr_b16 a[140:141], v187 offset:49152\n\t" \
  "s_add_u32 s14, s14, s59\n\t" \
  "v_mfma_f32_16x16x32_bf16 v[104:107], a[168:171], a[60:63], v[104:107]\n\t" \
  "s_add_u32 s68, s68, s14\n\t" \
  "s_addc_u32 s69, s69, 0\n\t" \
  "v_mfma_f32_16x16x32_bf16 v[108:111], a[172:175], a[60:63], v[108:111]\n\t" \
  "buffer_load_dwordx4 v[144:147], v209, s[8:11], s99 offen nt\n\t" \
  "v_mfma_f32_16x16x32_bf16 v[76:79], a[172:175], a[56:59], v[76:79]\n\t" \
  "ds_read_b64_tr_b16 a[142:143], v187 offset:50176\n\t" \
  "s_mov_b32 s99, 36864\n\t" \
  "v_mfma_f32_16x16x32_bf16 v[44:47], a[172:175], a[52:55], v[44:47]\n\t" \
  "ds_read_b64_tr_b16 a[144:145], v188 offset:49152\n\t" \
  "v_mfma_f32_16x16x32_bf16 v[12:15], a[172:175], a[48:51], v[12:15]\n\t" \
  "ds_read_b64_tr_b16 a[146:147], v188 offset:50176\n\t" \
  "v_mfma_f32_16x16x32_bf16 v[16:19], a[176:179], a[48:51], v[16:19]\n\t" \
  "s_add_u32 s72, s72, s14\n\t" \
  "s_addc_u32 s73, s73, 0\n\t" \
  "s_sub_u32 s70, s70, s14\n\t" \
  "v_mfma_f32_16x16x32_bf16 v[48:51], a[176:179], a[52:55], v[48:51]\n\t" \
  "s_sub_u32 s74, s74, s14\n\t" \
  "ds_read_b64_tr_b16 a[148:149], v189 offset:49152\n\t" \
  "s_lshr_b32 s14, s78, 2\n\t" \
  "v_mfma_f32_16x16x32_bf16 v[80:83], a[176:179], a[56:59], v[80:83]\n\t" \
  "buffer_load_dwordx4 v[148:151], v209, s[8:11], s99 offen nt\n\t" \
  "v_mfma_f32_16x16x32_bf16 v[112:115], a[176:179], a[60:63], v[112:115]\n\t" \
  "ds_read_b64_tr_b16 a[150:151], v189 offset:50176\n\t" \
  "v_mfma_f32_16x16x32_bf16 v[116:119], a[180:183], a[60:63], v[116:119]\n\t" \
  "ds_read_b64_tr_b16 a[152:153], v190 offset:49152\n\t" \
  "v_mfma_f32_16x16x32_bf16 v[84:87], a[180:183], a[56:59], v[84:87]\n\t" \
  "s_add_u32 s60, s60, s14\n\t" \
  "s_addc_u32 s61, s61, 0\n\t" \
  "v_mfma_f32_16x16x32_bf16 v[52:55], a[180:183], a[52:55], v[52:55]\n\t" \
  "ds_read_b64_tr_b16 a[154:155], v190 offset:50176\n\t" \
  "v_mfma_f32_16x16x32_bf16 v[20:23], a[180:183], a[48:51], v[20:23]\n\t" \
  "ds_read_b64_tr_b16 a[156:157], v191 offset:49152\n\t" \
  "v_mfma_f32_16x16x32_bf16 v[24:27], a[184:187], a[48:51], v[24:27]\n\t" \
  "s_add_u32 s64, s64, s14\n\t" \
  "s_addc_u32 s65, s65, 0\n\t" \
  "s_lshl2_add_u32 s58, s2, s3\n\t" \
  "v_mfma_f32_16x16x32_bf16 v[56:59], a[184:187], a[52:55], v[56:59]\n\t" \
  "s_lshl_b32 s58, s58, 11\n\t" \
  "ds_read_b64_tr_b16 a[158:159], v191 offset:50176\n\t" \
  "s_bfe_u32 s13, s2, 0x10004\n\t" \
  "v_mfma_f32_16x16x32_bf16 v[88:91], a[184:187], a[56:59], v[88:91]\n\t" \
  "s_lshl1_add_u32 s12, s12, s13\n\t" \
  "s_lshl_b32 s13, s13, 13\n\t" \
  "s_lshl_b32 s14, s12, 7\n\t" \
  "v_mfma_f32_16x16x32_bf16 v[120:123], a[184:187], a[60:63], v[120:123]\n\t" \
  "s_add_u32 s13, s13, s14\n\t" \
  "s_add_u32 s13, s13, 16384\n\t" \
  "s_lshl_b32 s12, s12, 21\n\t" \
  "v_mfma_f32_16x16x32_bf16 v[124:127], a[188:191], a[60:63], v[124:127]\n\t" \
  "s_add_u32 s12, s12, 8388608\n\t" \
  "s_bitcmp1_b32 s2, 3\n\t" \
  "s_cselect_b32 s91, s12, s13\n\t" \
  "v_mfma_f32_16x16x32_bf16 v[92:95], a[188:191], a[56:59], v[92:95]\n\t" \
  "s_lshr_b32 s12, s57, s3\n\t" \
  "s_cmp_lt_i32 s12, 0\n\t" \
  "s_cselect_b32 s91, s91, 0x80000000\n\t" \
  "v_mfma_f32_16x16x32_bf16 v[60:63], a[188:191], a[52:55], v[60:63]\n\t" \
  "buffer_load_dword v246, v217, s[16:19], s58 offen sc1\n\t" \
  "v_mfma_f32_16x16x32_bf16 v[28:31], a[188:191], a[48:51], v[28:31]\n\t" \
  "s_waitcnt vmcnt(33) lgkmcnt(0)\n\t" \
  "s_lshl_b32 s90, s3, 7\n\t" \
  "s_add_u32 s90, s90, 0x220000\n\t" \
  "v_mfma_f32_16x16x32_bf16 v[0:3], a[128:131], a[192:195], v[0:3]\n\t" \
  "s_and_b32 s98, s2, 0xc0\n\t" \
  "ds_read_b64_tr_b16 a[160:161], v184 offset:57344\n\t" \
  "s_lshl_b32 s98, s98, 9\n\t" \
  "v_mfma_f32_16x16x32_bf16 v[32:35], a[128:131], a[196:199], v[32:35]\n\t" \
  "s_add_u32 s90, s90, s98\n\t" \
  "ds_read_b64_tr_b16 a[162:163], v184 offset:58368\n\t" \
  "s_and_b32 s98, s2, 0x3\n\t" \
  "v_mfma_f32_16x16x32_bf16 v[64:67], a[128:131], a[200:203], v[64:67]\n\t" \
  "s_lshl_b32 s98, s98, 13\n\t" \
  "ds_read_b64_tr_b16 a[164:165], v185 offset:57344\n\t" \
  "s_add_u32 s90, s90, s98\n\t" \
  "v_mfma_f32_16x16x32_bf16 v[96:99], a[128:131], a[204:207], v[96:99]\n\t" \
  "s_and_b32 s98, s2, 0x4\n\t" \
  "ds_read_b64_tr_b16 a[166:167], v185 offset:58368\n\t" \
  "s_lshl_b32 s98, s98, 10\n\t" \
  "v_mfma_f32_16x16x32_bf16 v[100:103], a[132:135], a[204:207], v[100:103]\n\t" \
  "s_add_u32 s90, s90, s98\n\t" \
  "ds_read_b64_tr_b16 a[168:169], v186 offset:57344\n\t" \
  "s_and_b32 s98, s2, 0x38\n\t" \
  "v_mfma_f32_16x16x32_bf16 v[68:71], a[132:135], a[200:203], v[68:71]\n\t" \
  "s_lshl_b32 s98, s98, 6\n\t" \
  "ds_read_b64_tr_b16 a[170:171], v186 offset:58368\n\t" \
  "s_add_u32 s90, s90, s98\n\t" \
  "v_mfma_f32_16x16x32_bf16 v[36:39], a[132:135], a[196:199], v[36:39]\n\t" \
  "s_lshr_b32 s98, s2, 2\n\t" \
  "ds_read_b64_tr_b16 a[172:173], v187 offset:57344\n\t" \
  "s_xor_b32 s98, s98, s3\n\t" \
  "v_mfma_f32_16x16x32_bf16 v[4:7], a[132:135], a[192:195], v[4:7]\n\t" \
  "s_lshl_b32 s98, s98, 13\n\t" \
  "ds_read_b64_tr_b16 a[174:175], v187 offset:58368\n\t" \
  "s_and_b32 s42, s98, 0x2000\n\t" \
  "v_mfma_f32_16x16x32_bf16 v[8:11], a[136:139], a[192:195], v[8:11]\n\t" \
  "s_lshr_b32 s98, s2, 6\n\t" \
  "ds_read_b64_tr_b16 a[176:177], v188 offset:57344\n\t" \
  "s_lshl_b32 s98, s98, 22\n\t" \
  "v_mfma_f32_16x16x32_bf16 v[40:43], a[136:139], a[196:199], v[40:43]\n\t" \
  "s_add_u32 s42, s42, s98\n\t" \
  "ds_read_b64_tr_b16 a[178:179], v188 offset:58368\n\t" \
  "s_lshl_b32 s47, s3, 15\n\t" \
  "v_mfma_f32_16x16x32_bf16 v[72:75], a[136:139], a[200:203], v[72:75]\n\t" \
  "s_add_u32 s47, s47, 0x1800000\n\t" \
  "ds_read_b64_tr_b16 a[180:181], v189 offset:57344\n\t" \
  "s_add_u32 s47, s47, s42\n\t" \
  "v_mfma_f32_16x16x32_bf16 v[104:107], a[136:139], a[204:207], v[104:107]\n\t" \
  "s_and_b32 s98, s2, 0x38\n\t" \
  "ds_read_b64_tr_b16 a[182:183], v189 offset:58368\n\t" \
  "s_lshl_b32 s98, s98, 16\n\t" \
  "v_mfma_f32_16x16x32_bf16 v[108:111], a[140:143], a[204:207], v[108:111]\n\t" \
  "s_add_u32 s47, s47, s98\n\t" \
  "ds_read_b64_tr_b16 a[184:185], v190 offset:57344\n\t" \
  "s_add_u32 s46, s42, 0x800000\n\t" \
  "v_mfma_f32_16x16x32_bf16 v[76:79], a[140:143], a[200:203], v[76:79]\n\t" \
  "s_and_b32 s98, s2, 0x3\n\t" \
  "ds_read_b64_tr_b16 a[186:187], v190 offset:58368\n\t" \
  "s_lshl_b32 s98, s98, 20\n\t" \
  "v_mfma_f32_16x16x32_bf16 v[44:47], a[140:143], a[196:199], v[44:47]\n\t" \
  "s_add_u32 s46, s46, s98\n\t" \
  "ds_read_b64_tr_b16 a[188:189], v191 offset:57344\n\t" \
  "s_and_b32 s98, s3, 2\n\t" \
  "v_mfma_f32_16x16x32_bf16 v[12:15], a[140:143], a[192:195], v[12:15]\n\t" \
  "s_lshl_b32 s98, s98, 13\n\t" \
  "ds_read_b64_tr_b16 a[190:191], v191 offset:58368\n\t" \
  "s_add_u32 s46, s46, s98\n\t" \
  "v_mfma_f32_16x16x32_bf16 v[16:19], a[144:147], a[192:195], v[16:19]\n\t" \
  "s_and_b32 s98, s3, 1\n\t" \
  "s_lshl_b32 s98, s98, 12\n\t" \
  "s_add_u32 s46, s46, s98\n\t" \
  "v_mfma_f32_16x16x32_bf16 v[48:51], a[144:147], a[196:199], v[48:51]\n\t" \
  "s_lshr_b32 s45, s87, 2\n\t" \
  "s_add_u32 s45, s45, s46\n\t" \
  "s_and_b32 s98, s2, 0x38\n\t" \
  "v_mfma_f32_16x16x32_bf16 v[80:83], a[144:147], a[200:203], v[80:83]\n\t" \
  "s_lshl_b32 s98, s98, 14\n\t" \
  "s_add_u32 s45, s45, s98\n\t" \
  "s_lshr_b32 s98, s2, 1\n\t" \
  "v_mfma_f32_16x16x32_bf16 v[112:115], a[144:147], a[204:207], v[112:115]\n\t" \
  "s_xor_b32 s98, s98, s3\n\t" \
  "s_lshl_b32 s98, s98, 13\n\t" \
  "s_and_b32 s42, s98, 0x2000\n\t" \
  "v_mfma_f32_16x16x32_bf16 v[116:119], a[148:151], a[204:207], v[116:119]\n\t" \
  "s_lshr_b32 s98, s2, 5\n\t" \
  "s_xor_b32 s98, s98, s3\n\t" \
  "s_lshl_b32 s98, s98, 13\n\t" \
  "v_mfma_f32_16x16x32_bf16 v[84:87], a[148:151], a[200:203], v[84:87]\n\t" \
  "s_and_b32 s43, s98, 0x2000\n\t" \
  "s_lshl_b32 s48, s3, 2\n\t" \
  "s_add_u32 s48, s48, 0x8000\n\t" \
  "v_mfma_f32_16x16x32_bf16 v[52:55], a[148:151], a[196:199], v[52:55]\n\t" \
  "s_and_b32 s98, s2, 0x80\n\t" \
  "s_lshl_b32 s98, s98, 7\n\t" \
  "s_add_u32 s48, s48, s98\n\t" \
  "v_mfma_f32_16x16x32_bf16 v[20:23], a[148:151], a[192:195], v[20:23]\n\t" \
  "s_and_b32 s98, s2, 0x4\n\t" \
  "s_lshl_b32 s98, s98, 11\n\t" \
  "s_add_u32 s48, s48, s98\n\t" \
  "v_mfma_f32_16x16x32_bf16 v[24:27], a[152:155], a[192:195], v[24:27]\n\t" \
  "s_and_b32 s98, s2, 0x78\n\t" \
  "s_lshl_b32 s98, s98, 6\n\t" \
  "s_add_u32 s48, s48, s98\n\t" \
  "v_mfma_f32_16x16x32_bf16 v[56:59], a[152:155], a[196:199], v[56:59]\n\t" \
  "s_lshl_b32 s49, s3, 7\n\t" \
  "s_add_u32 s49, s49, 0x20000\n\t" \
  "s_and_b32 s98, s2, 0xc0\n\t" \
  "v_mfma_f32_16x16x32_bf16 v[88:91], a[152:155], a[200:203], v[88:91]\n\t" \
  "s_lshl_b32 s98, s98, 9\n\t" \
  "s_add_u32 s49, s49, s98\n\t" \
  "s_and_b32 s98, s2, 0x2\n\t" \
  "v_mfma_f32_16x16x32_bf16 v[120:123], a[152:155], a[204:207], v[120:123]\n\t" \
  "s_lshl_b32 s98, s98, 13\n\t" \
  "s_add_u32 s49, s49, s98\n\t" \
  "s_and_b32 s98, s2, 0x4\n\t" \
  "v_mfma_f32_16x16x32_bf16 v[124:127], a[156:159], a[204:207], v[124:127]\n\t" \
  "s_lshl_b32 s98, s98, 11\n\t" \
  "s_add_u32 s49, s49, s98\n\t" \
  "s_and_b32 s98, s2, 0x1\n\t" \
  "v_mfma_f32_16x16x32_bf16 v[92:95], a[156:159], a[200:203], v[92:95]\n\t" \
  "s_lshl_b32 s98, s98, 12\n\t" \
  "s_add_u32 s49, s49, s98\n\t" \
  "s_lshl2_add_u32 s49, s39, s49\n\t" \
  "v_mfma_f32_16x16x32_bf16 v[60:63], a[156:159], a[196:199], v[60:63]\n\t" \
  "s_waitcnt vmcnt(29) lgkmcnt(0)\n\t" \
  "s_add_u32 s97, s78, 4718592\n\t" \
  "s_bfe_u32 s97, s97, 0x140003\n\t" \
  "v_mfma_f32_16x16x32_bf16 v[28:31], a[156:159], a[192:195], v[28:31]\n\t" \
  "s_barrier\n\t" \
  "v_mfma_f32_16x16x32_bf16 v[0:3], a[160:163], a[208:211], v[0:3]\n\t" \
  "ds_read_b64_tr_b16 a[128:129], v192 offset:0\n\t" \
  "ds_read_b64_tr_b16 a[130:131], v192 offset:1024\n\t" \
  "ds_read_b64_tr_b16 a[132:133], v193 offset:0\n\t" \
  "s_add_u32 s97, s97, s82\n\t" \
  "v_mfma_f32_16x16x32_bf16 v[32:35], a[160:163], a[212:215], v[32:35]\n\t" \
  "buffer_load_dwordx4 a[32:35], v248, s[16:19], s97 offen sc1\n\t" \
  "ds_read_b64_tr_b16 a[134:135], v193 offset:1024\n\t" \
  "ds_read_b64_tr_b16 a[136:137], v194 offset:0\n\t" \
  "ds_read_b64_tr_b16 a[138:139], v194 offset:1024\n\t" \
  "v_mfma_f32_16x16x32_bf16 v[64:67], a[160:163], a[216:219], v[64:67]\n\t" \
  "ds_read_b64_tr_b16 a[140:141], v195 offset:0\n\t" \
  "ds_read_b64_tr_b16 a[142:143], v195 offset:1024\n\t" \
  "ds_read_b64_tr_b16 a[144:145], v196 offset:0\n\t" \
  "ds_read_b64_tr_b16 a[146:147], v196 offset:1024\n\t" \
  "v_mfma_f32_16x16x32_bf16 v[96:99], a[160:163], a[220:223], v[96:99]\n\t" \
  "ds_read_b64_tr_b16 a[148:149], v197 offset:0\n\t" \
  "ds_read_b64_tr_b16 a[150:151], v197 offset:1024\n\t" \
  "ds_read_b64_tr_b16 a[152:153], v198 offset:0\n\t" \
  "v_mbcnt_lo_u32_b32 v233, -1, 0\n\t" \
  "v_mfma_f32_16x16x32_bf16 v[100:103], a[164:167], a[220:223], v[100:103]\n\t" \
  "buffer_load_dwordx4 a[36:39], v249, s[16:19], s97 offen sc1\n\t" \
  "ds_read_b64_tr_b16 a[154:155], v198 offset:1024\n\t" \
  "s_lshl_b32 s50, s3, 15\n\t" \
  "v_mfma_f32_16x16x32_bf16 v[68:71], a[164:167], a[216:219], v[68:71]\n\t" \
  "s_and_b32 s98, s2, 0x4\n\t" \
  "ds_read_b64_tr_b16 a[156:157], v199 offset:0\n\t" \
  "v_mbcnt_hi_u32_b32 v233, -1, v233\n\t" \
  "v_mfma_f32_16x16x32_bf16 v[36:39], a[164:167], a[212:215], v[36:39]\n\t" \
  "s_lshl_b32 s98, s98, 19\n\t" \
  "ds_read_b64_tr_b16 a[158:159], v199 offset:1024\n\t" \
  "v_lshrrev_b32 v129, 4, v233\n\t" \
  "v_mfma_f32_16x16x32_bf16 v[4:7], a[164:167], a[208:211], v[4:7]\n\t" \
  "s_add_u32 s50, s50, s98\n\t" \
  "ds_read_b64_tr_b16 a[64:65], v192 offset:8192\n\t" \
  "v_and_b32 v130, 15, v233\n\t" \
  "v_mfma_f32_16x16x32_bf16 v[8:11], a[168:171], a[208:211], v[8:11]\n\t" \
  "buffer_load_dwordx4 a[40:43], v250, s[16:19], s97 offen sc1\n\t" \
  "v_mfma_f32_16x16x32_bf16 v[40:43], a[168:171], a[212:215], v[40:43]\n\t" \
  "s_and_b32 s98, s2, 0xc0\n\t" \
  "ds_read_b64_tr_b16 a[66:67], v192 offset:9216\n\t" \
  "v_bfe_u32 v131, v233, 2, 2\n\t" \
  "v_mfma_f32_16x16x32_bf16 v[72:75], a[168:171], a[216:219], v[72:75]\n\t" \
  "s_lshl_b32 s98, s98, 13\n\t" \
  "ds_read_b64_tr_b16 a[68:69], v193 offset:8192\n\t" \
  "v_and_b32 v132, 3, v233\n\t" \
  "v_mfma_f32_16x16x32_bf16 v[104:107], a[168:171], a[220:223], v[104:107]\n\t" \
  "s_add_u32 s50, s50, s98\n\t" \
  "ds_read_b64_tr_b16 a[70:71], v193 offset:9216\n\t" \
  "v_cmp_lt_u32 vcc, 6, v233\n\t" \
  "v_mfma_f32_16x16x32_bf16 v[108:111], a[172:175], a[220:223], v[108:111]\n\t" \
  "buffer_load_dwordx4 a[44:47], v251, s[16:19], s97 offen sc1\n\t" \
  "v_mfma_f32_16x16x32_bf16 v[76:79], a[172:175], a[216:219], v[76:79]\n\t" \
  "s_add_u32 s96, s78, 5767168\n\t" \
  "s_and_b32 s96, s96, 0x780000\n\t" \
  "ds_read_b64_tr_b16 a[72:73], v194 offset:8192\n\t" \
  "s_add_u32 m0, s76, 16384\n\t" \
  "v_mfma_f32_16x16x32_bf16 v[44:47], a[172:175], a[212:215], v[44:47]\n\t" \
  "s_and_b32 s98, s2, 0x3\n\t" \
  "ds_read_b64_tr_b16 a[74:75], v194 offset:9216\n\t" \
  "v_mov_b32 v133, 6272\n\t" \
  "v_mfma_f32_16x16x32_bf16 v[12:15], a[172:175], a[208:211], v[12:15]\n\t" \
  "buffer_load_dwordx4 v200, s[20:23], s96 offen offset:0 lds\n\t" \
  "v_mfma_f32_16x16x32_bf16 v[16:19], a[176:179], a[208:211], v[16:19]\n\t" \
  "s_lshl_b32 s98, s98, 17\n\t" \
  "ds_read_b64_tr_b16 a[76:77], v195 offset:8192\n\t" \
  "v_cndmask_b32 v133, 0, v133, vcc\n\t" \
  "v_mfma_f32_16x16x32_bf16 v[48:51], a[176:179], a[212:215], v[48:51]\n\t" \
  "s_add_u32 s50, s50, s98\n\t" \
  "ds_read_b64_tr_b16 a[78:79], v195 offset:9216\n\t" \
  "v_lshl_add_u32 v133, v233, 7, v133\n\t" \
  "v_mfma_f32_16x16x32_bf16 v[80:83], a[176:179], a[216:219], v[80:83]\n\t" \
  "s_and_b32 s98, s2, 0x38\n\t" \
  "ds_read_b64_tr_b16 a[80:81], v196 offset:8192\n\t" \
  "v_cmp_gt_u32 vcc, 14, v233\n\t" \
  "v_mfma_f32_16x16x32_bf16 v[112:115], a[176:179], a[220:223], v[112:115]\n\t" \
  "buffer_load_dwordx4 v201, s[20:23], s96 offen offset:1024 lds\n\t" \
  "v_mfma_f32_16x16x32_bf16 v[116:119], a[180:183], a[220:223], v[116:119]\n\t" \
  "s_lshl_b32 s98, s98, 5\n\t" \
  "ds_read_b64_tr_b16 a[82:83], v196 offset:9216\n\t" \
  "v_bfrev_b32 v247, 1\n\t" \
  "v_mfma_f32_16x16x32_bf16 v[84:87], a[180:183], a[216:219], v[84:87]\n\t" \
  "s_add_u32 s50, s50, s98\n\t" \
  "ds_read_b64_tr_b16 a[84:85], v197 offset:8192\n\t" \
  "v_cndmask_b32 v245, v247, v133, vcc\n\t" \
  "v_mfma_f32_16x16x32_bf16 v[52:55], a[180:183], a[212:215], v[52:55]\n\t" \
  "s_lshr_b32 s98, s2, 2\n\t" \
  "ds_read_b64_tr_b16 a[86:87], v197 offset:9216\n\t" \
  "v_writelane_b32 v247, s91, 0\n\t" \
  "v_mfma_f32_16x16x32_bf16 v[20:23], a[180:183], a[208:211], v[20:23]\n\t" \
  "buffer_load_dwordx4 v202, s[20:23], s96 offen offset:2048 lds\n\t" \
  ".Lhs3:\n\ts_cbranch_execz .Lhs3n\n\t" \
  "v_mfma_f32_16x16x32_bf16 v[24:27], a[184:187], a[208:211], v[24:27]\n\t" \
  "s_xor_b32 s98, s98, s2\n\t" \
  "ds_read_b64_tr_b16 a[88:89], v198 offset:8192\n\t" \
  "v_lshrrev_b32 v133, 1, v129\n\t" \
  "v_mfma_f32_16x16x32_bf16 v[56:59], a[184:187], a[212:215], v[56:59]\n\t" \
  "s_and_b32 s98, s98, 4\n\t" \
  "ds_read_b64_tr_b16 a[90:91], v198 offset:9216\n\t" \
  "v_and_b32 v134, 1, v129\n\t" \
  "v_mfma_f32_16x16x32_bf16 v[88:91], a[184:187], a[216:219], v[88:91]\n\t" \
  "s_lshl_b32 s44, s98, 4\n\t" \
  "ds_read_b64_tr_b16 a[92:93], v199 offset:8192\n\t" \
  "v_lshl_or_b32 v133, v134, 1, v133\n\t" \
  "v_mfma_f32_16x16x32_bf16 v[120:123], a[184:187], a[220:223], v[120:123]\n\t" \
  "buffer_load_dwordx4 v203, s[20:23], s96 offen offset:3072 lds\n\t" \
  "v_mfma_f32_16x16x32_bf16 v[124:127], a[188:191], a[220:223], v[124:127]\n\t" \
  "s_and_b32 s98, s2, 0x8\n\t" \
  "ds_read_b64_tr_b16 a[94:95], v199 offset:9216\n\t" \
  "v_xor_b32 v133, v133, v130\n\t" \
  "v_mfma_f32_16x16x32_bf16 v[92:95], a[188:191], a[216:219], v[92:95]\n\t" \
  "s_lshl_b32 s98, s98, 2\n\t" \
  "v_lshlrev_b32 v133, 4, v133\n\t" \
  "v_lshl_add_u32 v133, v130, 8, v133\n\t" \
  "v_mfma_f32_16x16x32_bf16 v[60:63], a[188:191], a[212:215], v[60:63]\n\t" \
  "s_waitcnt vmcnt(25) lgkmcnt(0)\n\t" \
  "s_add_u32 s97, s97, s83\n\t" \
  "v_add_u32 v220, 49152, v133\n\t" \
  "v_mfma_f32_16x16x32_bf16 v[28:31], a[188:191], a[208:211], v[28:31]\n\t" \
  "s_barrier\n\t" \
  "v_mfma_f32_16x16x32_bf16 v[0:3], a[128:131], a[224:227], v[0:3]\n\t" \
  "buffer_load_dwordx4 a[48:51], v248, s[16:19], s97 offen sc1\n\t" \
  "v_mfma_f32_16x16x32_bf16 v[0:3], a[64:67], a[240:243], v[0:3]\n\t" \
  "s_add_u32 s44, s44, s98\n\t" \
  "ds_read_b64_tr_b16 a[160:161], v192 offset:16384\n\t" \
  "v_xor_b32 v221, 64, v220\n\t" \
  "v_mfma_f32_16x16x32_bf16 v[32:35], a[64:67], a[244:247], v[32:35]\n\t" \
  "s_and_b32 s98, s2, 0x3\n\t" \
  "ds_read_b64_tr_b16 a[162:163], v192 offset:17408\n\t" \
  "v_xor_b32 v222, 128, v220\n\t" \
  "v_mfma_f32_16x16x32_bf16 v[32:35], a[128:131], a[228:231], v[32:35]\n\t" \
  "s_lshl_b32 s98, s98, 3\n\t" \
  "v_xor_b32 v223, 192, v220\n\t" \
  "v_xor_b32 v133, v130, v129\n\t" \
  "v_mfma_f32_16x16x32_bf16 v[64:67], a[128:131], a[232:235], v[64:67]\n\t" \
  "buffer_load_dwordx4 a[52:55], v249, s[16:19], s97 offen sc1\n\t" \
  "v_mfma_f32_16x16x32_bf16 v[64:67], a[64:67], a[248:251], v[64:67]\n\t" \
  "s_add_u32 s44, s44, s98\n\t" \
  "ds_read_b64_tr_b16 a[164:165], v193 offset:16384\n\t" \
  "v_lshlrev_b32 v133, 4, v133\n\t" \
  "v_mfma_f32_16x16x32_bf16 v[96:99], a[64:67], a[252:255], v[96:99]\n\t" \
  "s_and_b32 s98, s2, 0x4\n\t" \
  "ds_read_b64_tr_b16 a[166:167], v193 offset:17408\n\t" \
  "v_lshl_add_u32 v224, v129, 8, v133\n\t" \
  "v_mfma_f32_16x16x32_bf16 v[96:99], a[128:131], a[236:239], v[96:99]\n\t" \
  "s_lshl_b32 s98, s98, 0\n\t" \
  "ds_read_b64_tr_b16 a[168:169], v194 offset:16384\n\t" \
  "v_xor_b32 v225, 64, v224\n\t" \
  "v_mfma_f32_16x16x32_bf16 v[4:7], a[132:135], a[224:227], v[4:7]\n\t" \
  "buffer_load_dwordx4 a[56:59], v250, s[16:19], s97 offen sc1\n\t" \
  "v_mfma_f32_16x16x32_bf16 v[4:7], a[68:71], a[240:243], v[4:7]\n\t" \
  "s_add_u32 s44, s44, s98\n\t" \
  "ds_read_b64_tr_b16 a[170:171], v194 offset:17408\n\t" \
  "v_xor_b32 v226, 128, v224\n\t" \
  "v_mfma_f32_16x16x32_bf16 v[36:39], a[68:71], a[244:247], v[36:39]\n\t" \
  "s_add_u32 s44, s44, 0x2000\n\t" \
  "ds_read_b64_tr_b16 a[172:173], v195 offset:16384\n\t" \
  "v_xor_b32 v227, 192, v224\n\t" \
  "v_mfma_f32_16x16x32_bf16 v[36:39], a[132:135], a[228:231], v[36:39]\n\t" \
  "s_waitcnt vmcnt(15)\n\t" \
  "v_lshlrev_b32 v128, 16, v136\n\t" \
  "v_lshlrev_b32 v133, 3, v132\n\t" \
  "v_mfma_f32_16x16x32_bf16 v[68:71], a[132:135], a[232:235], v[68:71]\n\t" \
  "buffer_load_dwordx4 a[60:63], v251, s[16:19], s97 offen sc1\n\t" \
  "v_mfma_f32_16x16x32_bf16 v[68:71], a[68:71], a[248:251], v[68:71]\n\t" \
  "s_add_u32 s97, s78, 5242880\n\t" \
  "s_bfe_u32 s97, s97, 0x140003\n\t" \
  "s_add_u32 s97, s97, s82\n\t" \
  "v_mfma_f32_16x16x32_bf16 v[100:103], a[68:71], a[252:255], v[100:103]\n\t" \
  "ds_read_b64_tr_b16 a[174:175], v195 offset:17408\n\t" \
  "v_lshl_add_u32 v133, v129, 10, v133\n\t" \
  "v_mfma_f32_16x16x32_bf16 v[100:103], a[132:135], a[236:239], v[100:103]\n\t" \
  "buffer_load_dwordx4 a[192:195], v248, s[16:19], s97 offen sc1\n\t" \
  "v_mfma_f32_16x16x32_bf16 v[8:11], a[136:139], a[224:227], v[8:11]\n\t" \
  "ds_read_b64_tr_b16 a[176:177], v196 offset:16384\n\t" \
  "v_lshl_add_u32 v228, v131, 6, v133\n\t" \
  "v_mfma_f32_16x16x32_bf16 v[8:11], a[72:75], a[240:243], v[8:11]\n\t" \
  "ds_read_b64_tr_b16 a[178:179], v196 offset:17408\n\t" \
  "v_lshl_add_u32 v228, v134, 5, v228\n\t" \
  "v_mfma_f32_16x16x32_bf16 v[40:43], a[72:75], a[244:247], v[40:43]\n\t" \
  "ds_read_b64_tr_b16 a[180:181], v197 offset:16384\n\t" \
  "v_add_u32 v228, 81920, v228\n\t" \
  "v_mfma_f32_16x16x32_bf16 v[40:43], a[136:139], a[228:231], v[40:43]\n\t" \
  "buffer_load_dwordx4 a[196:199], v249, s[16:19], s97 offen sc1\n\t" \
  "v_mfma_f32_16x16x32_bf16 v[72:75], a[136:139], a[232:235], v[72:75]\n\t" \
  "ds_read_b64_tr_b16 a[182:183], v197 offset:17408\n\t" \
  "v_xor_b32 v229, 32, v228\n\t" \
  "v_mfma_f32_16x16x32_bf16 v[72:75], a[72:75], a[248:251], v[72:75]\n\t" \
  "ds_read_b64_tr_b16 a[184:185], v198 offset:16384\n\t" \
  "v_lshl_add_u32 v244, v131, 8, v133\n\t" \
  "v_mfma_f32_16x16x32_bf16 v[104:107], a[72:75], a[252:255], v[104:107]\n\t" \
  "ds_read_b64_tr_b16 a[186:187], v198 offset:17408\n\t" \
  "v_lshl_add_u32 v244, v131, 5, v244\n\t" \
  "v_mfma_f32_16x16x32_bf16 v[104:107], a[136:139], a[236:239], v[104:107]\n\t" \
  "buffer_load_dwordx4 a[200:203], v250, s[16:19], s97 offen sc1\n\t" \
  "v_mfma_f32_16x16x32_bf16 v[12:15], a[140:143], a[224:227], v[12:15]\n\t" \
  "ds_read_b64_tr_b16 a[188:189], v199 offset:16384\n\t" \
  "v_lshl_add_u32 v244, v134, 7, v244\n\t" \
  "v_mfma_f32_16x16x32_bf16 v[12:15], a[76:79], a[240:243], v[12:15]\n\t" \
  "ds_read_b64_tr_b16 a[190:191], v199 offset:17408\n\t" \
  "v_lshlrev_b32 v133, 4, v132\n\t" \
  "v_mfma_f32_16x16x32_bf16 v[44:47], a[76:79], a[244:247], v[44:47]\n\t" \
  "ds_read_b64_tr_b16 a[96:97], v192 offset:24576\n\t" \
  "v_lshl_add_u32 v133, v129, 6, v133\n\t" \
  "v_mfma_f32_16x16x32_bf16 v[44:47], a[140:143], a[228:231], v[44:47]\n\t" \
  "buffer_load_dwordx4 a[204:207], v251, s[16:19], s97 offen sc1\n\t" \
  "v_mfma_f32_16x16x32_bf16 v[76:79], a[140:143], a[232:235], v[76:79]\n\t" \
  "ds_read_b64_tr_b16 a[98:99], v192 offset:25600\n\t" \
  "v_lshl_add_u32 v230, v131, 8, v133\n\t" \
  "v_mfma_f32_16x16x32_bf16 v[76:79], a[76:79], a[248:251], v[76:79]\n\t" \
  "s_add_u32 s97, s97, s83\n\t" \
  "v_xor_b32 v231, 32, v230\n\t" \
  "v_lshlrev_b32 v133, 1, v129\n\t" \
  "v_mfma_f32_16x16x32_bf16 v[108:111], a[76:79], a[252:255], v[108:111]\n\t" \
  "ds_read_b64_tr_b16 a[100:101], v193 offset:24576\n\t" \
  "v_xor_b32 v133, v133, v130\n\t" \
  "v_mfma_f32_16x16x32_bf16 v[108:111], a[140:143], a[236:239], v[108:111]\n\t" \
  "ds_read_b64_tr_b16 a[102:103], v193 offset:25600\n\t" \
  "v_lshlrev_b32 v133, 4, v133\n\t" \
  "v_mfma_f32_16x16x32_bf16 v[16:19], a[144:147], a[224:227], v[16:19]\n\t" \
  "buffer_load_dwordx4 a[208:211], v248, s[16:19], s97 offen sc1\n\t" \
  "v_mfma_f32_16x16x32_bf16 v[16:19], a[80:83], a[240:243], v[16:19]\n\t" \
  "ds_read_b64_tr_b16 a[104:105], v194 offset:24576\n\t" \
  "v_lshl_add_u32 v240, v129, 11, v133\n\t" \
  "v_mfma_f32_16x16x32_bf16 v[48:51], a[80:83], a[244:247], v[48:51]\n\t" \
  "v_xor_b32 v241, 0x80, v240\n\t" \
  "v_lshrrev_b32 v133, 1, v233\n\t" \
  "v_mfma_f32_16x16x32_bf16 v[48:51], a[144:147], a[228:231], v[48:51]\n\t" \
  "ds_read_b64_tr_b16 a[106:107], v194 offset:25600\n\t" \
  "v_mul_u32_u24 v133, 57344, v133\n\t" \
  "v_mfma_f32_16x16x32_bf16 v[80:83], a[144:147], a[232:235], v[80:83]\n\t" \
  "buffer_load_dwordx4 a[212:215], v249, s[16:19], s97 offen sc1\n\t" \
  "v_mfma_f32_16x16x32_bf16 v[80:83], a[80:83], a[248:251], v[80:83]\n\t" \
  "ds_read_b64_tr_b16 a[108:109], v195 offset:24576\n\t" \
  "v_and_b32 v134, 1, v233\n\t" \
  "v_mfma_f32_16x16x32_bf16 v[112:115], a[80:83], a[252:255], v[112:115]\n\t" \
  "ds_read_b64_tr_b16 a[110:111], v195 offset:25600\n\t" \
  "v_lshl_add_u32 v133, v134, 4, v133\n\t" \
  "v_mfma_f32_16x16x32_bf16 v[112:115], a[144:147], a[236:239], v[112:115]\n\t" \
  "ds_read_b64_tr_b16 a[112:113], v196 offset:24576\n\t" \
  "v_or_b32 v232, s57, v133\n\t" \
  "v_mfma_f32_16x16x32_bf16 v[20:23], a[148:151], a[224:227], v[20:23]\n\t" \
  "buffer_load_dwordx4 a[216:219], v250, s[16:19], s97 offen sc1\n\t" \
  "v_mfma_f32_16x16x32_bf16 v[20:23], a[84:87], a[240:243], v[20:23]\n\t" \
  "ds_read_b64_tr_b16 a[114:115], v196 offset:25600\n\t" \
  "v_and_b32 v136, 0xffff0000, v136\n\t" \
  "v_mfma_f32_16x16x32_bf16 v[52:55], a[84:87], a[244:247], v[52:55]\n\t" \
  "v_mul_f32 v176, v128, v168\n\t" \
  "v_mul_f32 v180, v136, v168\n\t" \
  "v_mfma_f32_16x16x32_bf16 v[52:55], a[148:151], a[228:231], v[52:55]\n\t" \
  "ds_read_b64_tr_b16 a[116:117], v197 offset:24576\n\t" \
  "v_lshlrev_b32 v128, 16, v137\n\t" \
  "v_mfma_f32_16x16x32_bf16 v[84:87], a[148:151], a[232:235], v[84:87]\n\t" \
  "buffer_load_dwordx4 a[220:223], v251, s[16:19], s97 offen sc1\n\t" \
  "v_mfma_f32_16x16x32_bf16 v[84:87], a[84:87], a[248:251], v[84:87]\n\t" \
  "s_add_u32 s96, s78, 6291456\n\t" \
  "s_and_b32 s96, s96, 0x780000\n\t" \
  "s_add_u32 m0, s76, 32768\n\t" \
  "v_mfma_f32_16x16x32_bf16 v[116:119], a[84:87], a[252:255], v[116:119]\n\t" \
  "ds_read_b64_tr_b16 a[118:119], v197 offset:25600\n\t" \
  "v_and_b32 v137, 0xffff0000, v137\n\t" \
  "v_mfma_f32_16x16x32_bf16 v[116:119], a[148:151], a[236:239], v[116:119]\n\t" \
  "buffer_load_dwordx4 v200, s[20:23], s96 offen offset:0 lds\n\t" \
  "v_mfma_f32_16x16x32_bf16 v[24:27], a[152:155], a[224:227], v[24:27]\n\t" \
  "ds_read_b64_tr_b16 a[120:121], v198 offset:24576\n\t" \
  "v_mul_f32 v177, v128, v168\n\t" \
  "v_mfma_f32_16x16x32_bf16 v[24:27], a[88:91], a[240:243], v[24:27]\n\t" \
  "ds_read_b64_tr_b16 a[122:123], v198 offset:25600\n\t" \
  "v_mul_f32 v181, v137, v168\n\t" \
  "v_mfma_f32_16x16x32_bf16 v[56:59], a[88:91], a[244:247], v[56:59]\n\t" \
  "ds_read_b64_tr_b16 a[124:125], v199 offset:24576\n\t" \
  "v_lshlrev_b32 v128, 16, v138\n\t" \
  "v_mfma_f32_16x16x32_bf16 v[56:59], a[152:155], a[228:231], v[56:59]\n\t" \
  "buffer_load_dwordx4 v201, s[20:23], s96 offen offset:1024 lds\n\t" \
  "v_mfma_f32_16x16x32_bf16 v[88:91], a[152:155], a[232:235], v[88:91]\n\t" \
  "ds_read_b64_tr_b16 a[126:127], v199 offset:25600\n\t" \
  "v_and_b32 v138, 0xffff0000, v138\n\t" \
  "v_mfma_f32_16x16x32_bf16 v[88:91], a[88:91], a[248:251], v[88:91]\n\t" \
  "v_mul_f32 v178, v128, v168\n\t" \
  "v_mul_f32 v182, v138, v168\n\t" \
  "v_mfma_f32_16x16x32_bf16 v[120:123], a[88:91], a[252:255], v[120:123]\n\t" \
  "v_lshlrev_b32 v128, 16, v139\n\t" \
  "v_and_b32 v139, 0xffff0000, v139\n\t" \
  "v_mfma_f32_16x16x32_bf16 v[120:123], a[152:155], a[236:239], v[120:123]\n\t" \
  "buffer_load_dwordx4 v202, s[20:23], s96 offen offset:2048 lds\n\t" \
  "v_mfma_f32_16x16x32_bf16 v[28:31], a[156:159], a[224:227], v[28:31]\n\t" \
  "v_mul_f32 v179, v128, v168\n\t" \
  "v_mul_f32 v183, v139, v168\n\t" \
  "v_mfma_f32_16x16x32_bf16 v[28:31], a[92:95], a[240:243], v[28:31]\n\t" \
  "s_waitcnt vmcnt(26)\n\t" \
  "v_lshlrev_b32 v128, 16, v140\n\t" \
  "v_and_b32 v140, 0xffff0000, v140\n\t" \
  "v_mfma_f32_16x16x32_bf16 v[60:63], a[92:95], a[244:247], v[60:63]\n\t" \
  "v_fma_f32 v176, v128, v169, v176\n\t" \
  "v_fma_f32 v180, v140, v169, v180\n\t" \
  "v_mfma_f32_16x16x32_bf16 v[60:63], a[156:159], a[228:231], v[60:63]\n\t" \
  "buffer_load_dwordx4 v203, s[20:23], s96 offen offset:3072 lds\n\t" \
  "v_mfma_f32_16x16x32_bf16 v[92:95], a[156:159], a[232:235], v[92:95]\n\t" \
  "v_lshlrev_b32 v128, 16, v141\n\t" \
  "v_and_b32 v141, 0xffff0000, v141\n\t" \
  "v_mfma_f32_16x16x32_bf16 v[92:95], a[92:95], a[248:251], v[92:95]\n\t" \
  "s_add_u32 s97, s78, 5767168\n\t" \
  "v_fma_f32 v177, v128, v169, v177\n\t" \
  "v_fma_f32 v181, v141, v169, v181\n\t" \
  "v_mfma_f32_16x16x32_bf16 v[124:127], a[92:95], a[252:255], v[124:127]\n\t" \
  "s_waitcnt lgkmcnt(0)\n\t" \
  "s_bfe_u32 s97, s97, 0x140003\n\t" \
  "s_add_u32 s97, s97, s82\n\t" \
  "v_mfma_f32_16x16x32_bf16 v[124:127], a[156:159], a[236:239], v[124:127]\n\t" \
  "s_barrier\n\t" \
  "v_mfma_f32_16x16x32_bf16 v[0:3], a[160:163], a[0:3], v[0:3]\n\t" \
  "buffer_load_dwordx4 a[224:227], v248, s[16:19], s97 offen sc1\n\t" \
  "v_mfma_f32_16x16x32_bf16 v[0:3], a[96:99], a[16:19], v[0:3]\n\t" \
  "ds_read_b64_tr_b16 a[128:129], v192 offset:32768\n\t" \
  "v_lshlrev_b32 v128, 16, v142\n\t" \
  "v_mfma_f32_16x16x32_bf16 v[32:35], a[96:99], a[20:23], v[32:35]\n\t" \
  "ds_read_b64_tr_b16 a[130:131], v192 offset:33792\n\t" \
  "v_and_b32 v142, 0xffff0000, v142\n\t" \
  "v_mfma_f32_16x16x32_bf16 v[32:35], a[160:163], a[4:7], v[32:35]\n\t" \
  "v_fma_f32 v178, v128, v169, v178\n\t" \
  "v_fma_f32 v182, v142, v169, v182\n\t" \
  "v_mfma_f32_16x16x32_bf16 v[64:67], a[160:163], a[8:11], v[64:67]\n\t" \
  "buffer_load_dwordx4 a[228:231], v249, s[16:19], s97 offen sc1\n\t" \
  "v_mfma_f32_16x16x32_bf16 v[64:67], a[96:99], a[24:27], v[64:67]\n\t" \
  "ds_read_b64_tr_b16 a[132:133], v193 offset:32768\n\t" \
  "v_lshlrev_b32 v128, 16, v143\n\t" \
  "v_mfma_f32_16x16x32_bf16 v[96:99], a[96:99], a[28:31], v[96:99]\n\t" \
  "ds_read_b64_tr_b16 a[134:135], v193 offset:33792\n\t" \
  "v_and_b32 v143, 0xffff0000, v143\n\t" \
  "v_mfma_f32_16x16x32_bf16 v[96:99], a[160:163], a[12:15], v[96:99]\n\t" \
  "ds_read_b64_tr_b16 a[136:137], v194 offset:32768\n\t" \
  "v_fma_f32 v179, v128, v169, v179\n\t" \
  "v_mfma_f32_16x16x32_bf16 v[4:7], a[164:167], a[0:3], v[4:7]\n\t" \
  "buffer_load_dwordx4 a[232:235], v250, s[16:19], s97 offen sc1\n\t" \
  "v_mfma_f32_16x16x32_bf16 v[4:7], a[100:103], a[16:19], v[4:7]\n\t" \
  "ds_read_b64_tr_b16 a[138:139], v194 offset:33792\n\t" \
  "v_fma_f32 v183, v143, v169, v183\n\t" \
  "v_mfma_f32_16x16x32_bf16 v[36:39], a[100:103], a[20:23], v[36:39]\n\t" \
  "s_waitcnt vmcnt(29)\n\t" \
  "v_lshlrev_b32 v128, 16, v144\n\t" \
  "v_and_b32 v144, 0xffff0000, v144\n\t" \
  "v_mfma_f32_16x16x32_bf16 v[36:39], a[164:167], a[4:7], v[36:39]\n\t" \
  "ds_read_b64_tr_b16 a[140:141], v195 offset:32768\n\t" \
  "v_fma_f32 v176, v128, v170, v176\n\t" \
  "v_mfma_f32_16x16x32_bf16 v[68:71], a[164:167], a[8:11], v[68:71]\n\t" \
  "buffer_load_dwordx4 a[236:239], v251, s[16:19], s97 offen sc1\n\t" \
  "v_mfma_f32_16x16x32_bf16 v[68:71], a[100:103], a[24:27], v[68:71]\n\t" \
  "s_add_u32 s96, s78, 6815744\n\t" \
  "s_and_b32 s96, s96, 0x780000\n\t" \
  "s_add_u32 m0, s76, 65536\n\t" \
  "v_mfma_f32_16x16x32_bf16 v[100:103], a[100:103], a[28:31], v[100:103]\n\t" \
  "ds_read_b64_tr_b16 a[142:143], v195 offset:33792\n\t" \
  "v_fma_f32 v180, v144, v170, v180\n\t" \
  "v_mfma_f32_16x16x32_bf16 v[100:103], a[164:167], a[12:15], v[100:103]\n\t" \
  "buffer_load_dwordx4 v200, s[20:23], s96 offen offset:0 lds\n\t" \
  "v_mfma_f32_16x16x32_bf16 v[8:11], a[168:171], a[0:3], v[8:11]\n\t" \
  "ds_read_b64_tr_b16 a[144:145], v196 offset:32768\n\t" \
  "v_lshlrev_b32 v128, 16, v145\n\t" \
  "v_mfma_f32_16x16x32_bf16 v[8:11], a[104:107], a[16:19], v[8:11]\n\t" \
  "ds_read_b64_tr_b16 a[146:147], v196 offset:33792\n\t" \
  "v_and_b32 v145, 0xffff0000, v145\n\t" \
  "v_mfma_f32_16x16x32_bf16 v[40:43], a[104:107], a[20:23], v[40:43]\n\t" \
  "ds_read_b64_tr_b16 a[148:149], v197 offset:32768\n\t" \
  "v_fma_f32 v177, v128, v170, v177\n\t" \
  "v_mfma_f32_16x16x32_bf16 v[40:43], a[168:171], a[4:7], v[40:43]\n\t" \
  "buffer_load_dwordx4 v201, s[20:23], s96 offen offset:1024 lds\n\t" \
  "v_mfma_f32_16x16x32_bf16 v[72:75], a[168:171], a[8:11], v[72:75]\n\t" \
  "ds_read_b64_tr_b16 a[150:151], v197 offset:33792\n\t" \
  "v_fma_f32 v181, v145, v170, v181\n\t" \
  "v_mfma_f32_16x16x32_bf16 v[72:75], a[104:107], a[24:27], v[72:75]\n\t" \
  "ds_read_b64_tr_b16 a[152:153], v198 offset:32768\n\t" \
  "v_lshlrev_b32 v128, 16, v146\n\t" \
  "v_mfma_f32_16x16x32_bf16 v[104:107], a[104:107], a[28:31], v[104:107]\n\t" \
  "ds_read_b64_tr_b16 a[154:155], v198 offset:33792\n\t" \
  "v_and_b32 v146, 0xffff0000, v146\n\t" \
  "v_mfma_f32_16x16x32_bf16 v[104:107], a[168:171], a[12:15], v[104:107]\n\t" \
  "buffer_load_dwordx4 v202, s[20:23], s96 offen offset:2048 lds\n\t" \
  "v_mfma_f32_16x16x32_bf16 v[12:15], a[172:175], a[0:3], v[12:15]\n\t" \
  "ds_read_b64_tr_b16 a[156:157], v199 offset:32768\n\t" \
  "v_fma_f32 v178, v128, v170, v178\n\t" \
  "v_mfma_f32_16x16x32_bf16 v[12:15], a[108:111], a[16:19], v[12:15]\n\t" \
  "ds_read_b64_tr_b16 a[158:159], v199 offset:33792\n\t" \
  "v_fma_f32 v182, v146, v170, v182\n\t" \
  "v_mfma_f32_16x16x32_bf16 v[44:47], a[108:111], a[20:23], v[44:47]\n\t" \
  "ds_read_b64_tr_b16 a[64:65], v192 offset:40960\n\t" \
  "v_lshlrev_b32 v128, 16, v147\n\t" \
  "v_mfma_f32_16x16x32_bf16 v[44:47], a[172:175], a[4:7], v[44:47]\n\t" \
  "buffer_load_dwordx4 v203, s[20:23], s96 offen offset:3072 lds\n\t" \
  "v_mfma_f32_16x16x32_bf16 v[76:79], a[172:175], a[8:11], v[76:79]\n\t" \
  "ds_read_b64_tr_b16 a[66:67], v192 offset:41984\n\t" \
  "v_and_b32 v147, 0xffff0000, v147\n\t" \
  "v_mfma_f32_16x16x32_bf16 v[76:79], a[108:111], a[24:27], v[76:79]\n\t" \
  "s_add_u32 s97, s97, s83\n\t" \
  "v_fma_f32 v179, v128, v170, v179\n\t" \
  "v_fma_f32 v183, v147, v170, v183\n\t" \
  "v_mfma_f32_16x16x32_bf16 v[108:111], a[108:111], a[28:31], v[108:111]\n\t" \
  "ds_read_b64_tr_b16 a[68:69], v193 offset:40960\n\t" \
  "v_mfma_f32_16x16x32_bf16 v[108:111], a[172:175], a[12:15], v[108:111]\n\t" \
  "s_waitcnt vmcnt(33)\n\t" \
  "v_lshlrev_b32 v128, 16, v148\n\t" \
  "v_and_b32 v148, 0xffff0000, v148\n\t" \
  "v_mfma_f32_16x16x32_bf16 v[16:19], a[176:179], a[0:3], v[16:19]\n\t" \
  "buffer_load_dwordx4 a[240:243], v248, s[16:19], s97 offen sc1\n\t" \
  "v_mfma_f32_16x16x32_bf16 v[16:19], a[112:115], a[16:19], v[16:19]\n\t" \
  "ds_read_b64_tr_b16 a[70:71], v193 offset:41984\n\t" \
  "v_fma_f32 v176, v128, v171, v176\n\t" \
  "v_mfma_f32_16x16x32_bf16 v[48:51], a[112:115], a[20:23], v[48:51]\n\t" \
  "ds_read_b64_tr_b16 a[72:73], v194 offset:40960\n\t" \
  "v_fma_f32 v180, v148, v171, v180\n\t" \
  "v_mfma_f32_16x16x32_bf16 v[48:51], a[176:179], a[4:7], v[48:51]\n\t" \
  "ds_read_b64_tr_b16 a[74:75], v194 offset:41984\n\t" \
  "v_lshlrev_b32 v128, 16, v149\n\t" \
  "v_mfma_f32_16x16x32_bf16 v[80:83], a[176:179], a[8:11], v[80:83]\n\t" \
  "ds_read_b64_tr_b16 a[76:77], v195 offset:40960\n\t" \
  "v_and_b32 v149, 0xffff0000, v149\n\t" \
  "v_mfma_f32_16x16x32_bf16 v[80:83], a[112:115], a[24:27], v[80:83]\n\t" \
  "v_fma_f32 v177, v128, v171, v177\n\t" \
  "v_fma_f32 v181, v149, v171, v181\n\t" \
  "v_mfma_f32_16x16x32_bf16 v[112:115], a[112:115], a[28:31], v[112:115]\n\t" \
  "ds_read_b64_tr_b16 a[78:79], v195 offset:41984\n\t" \
  "v_lshlrev_b32 v128, 16, v150\n\t" \
  "v_mfma_f32_16x16x32_bf16 v[112:115], a[176:179], a[12:15], v[112:115]\n\t" \
  "ds_read_b64_tr_b16 a[80:81], v196 offset:40960\n\t" \
  "v_and_b32 v150, 0xffff0000, v150\n\t" \
  "v_mfma_f32_16x16x32_bf16 v[20:23], a[180:183], a[0:3], v[20:23]\n\t" \
  "buffer_load_dwordx4 a[244:247], v249, s[16:19], s97 offen sc1\n\t" \
  "v_mfma_f32_16x16x32_bf16 v[20:23], a[116:119], a[16:19], v[20:23]\n\t" \
  "ds_read_b64_tr_b16 a[82:83], v196 offset:41984\n\t" \
  "v_fma_f32 v178, v128, v171, v178\n\t" \
  "v_mfma_f32_16x16x32_bf16 v[52:55], a[116:119], a[20:23], v[52:55]\n\t" \
  "v_fma_f32 v182, v150, v171, v182\n\t" \
  "v_lshlrev_b32 v128, 16, v151\n\t" \
  "v_mfma_f32_16x16x32_bf16 v[52:55], a[180:183], a[4:7], v[52:55]\n\t" \
  "ds_read_b64_tr_b16 a[84:85], v197 offset:40960\n\t" \
  "v_and_b32 v151, 0xffff0000, v151\n\t" \
  "v_mfma_f32_16x16x32_bf16 v[84:87], a[180:183], a[8:11], v[84:87]\n\t" \
  "ds_read_b64_tr_b16 a[86:87], v197 offset:41984\n\t" \
  "v_fma_f32 v179, v128, v171, v179\n\t" \
  "v_mfma_f32_16x16x32_bf16 v[84:87], a[116:119], a[24:27], v[84:87]\n\t" \
  "s_cmp_lt_i32 s91, 0\n\t" \
  "s_cselect_b32 s44, s91, s44\n\t" \
  "v_fma_f32 v183, v151, v171, v183\n\t" \
  "v_mfma_f32_16x16x32_bf16 v[116:119], a[116:119], a[28:31], v[116:119]\n\t" \
  "ds_read_b64_tr_b16 a[88:89], v198 offset:40960\n\t" \
  "v_mfma_f32_16x16x32_bf16 v[116:119], a[180:183], a[12:15], v[116:119]\n\t" \
  "buffer_load_dwordx4 a[248:251], v250, s[16:19], s97 offen sc1\n\t" \
  "v_mfma_f32_16x16x32_bf16 v[24:27], a[184:187], a[0:3], v[24:27]\n\t" \
  "ds_read_b64_tr_b16 a[90:91], v198 offset:41984\n\t" \
  "v_mfma_f32_16x16x32_bf16 v[24:27], a[120:123], a[16:19], v[24:27]\n\t" \
  "ds_read_b64_tr_b16 a[92:93], v199 offset:40960\n\t" \
  "v_mfma_f32_16x16x32_bf16 v[56:59], a[120:123], a[20:23], v[56:59]\n\t" \
  "ds_read_b64_tr_b16 a[94:95], v199 offset:41984\n\t" \
  "v_mfma_f32_16x16x32_bf16 v[56:59], a[184:187], a[4:7], v[56:59]\n\t" \
  "v_mfma_f32_16x16x32_bf16 v[88:91], a[184:187], a[8:11], v[88:91]\n\t" \
  "v_mfma_f32_16x16x32_bf16 v[88:91], a[120:123], a[24:27], v[88:91]\n\t" \
  "buffer_load_dwordx4 a[252:255], v251, s[16:19], s97 offen sc1\n\t" \
  "v_mfma_f32_16x16x32_bf16 v[120:123], a[120:123], a[28:31], v[120:123]\n\t" \
  "v_mfma_f32_16x16x32_bf16 v[120:123], a[184:187], a[12:15], v[120:123]\n\t" \
  "v_mfma_f32_16x16x32_bf16 v[28:31], a[188:191], a[0:3], v[28:31]\n\t" \
  "v_mfma_f32_16x16x32_bf16 v[28:31], a[124:127], a[16:19], v[28:31]\n\t" \
  "v_mfma_f32_16x16x32_bf16 v[60:63], a[124:127], a[20:23], v[60:63]\n\t" \
  "v_mfma_f32_16x16x32_bf16 v[60:63], a[188:191], a[4:7], v[60:63]\n\t" \
  "v_mfma_f32_16x16x32_bf16 v[92:95], a[188:191], a[8:11], v[92:95]\n\t" \
  "v_mfma_f32_16x16x32_bf16 v[92:95], a[124:127], a[24:27], v[92:95]\n\t" \
  "s_add_u32 s97, s78, 6291456\n\t" \
  "s_bfe_u32 s97, s97, 0x140003\n\t" \
  "s_add_u32 s97, s97, s82\n\t" \
  "v_mfma_f32_16x16x32_bf16 v[124:127], a[124:127], a[28:31], v[124:127]\n\t" \
  "s_waitcnt vmcnt(24) lgkmcnt(0)\n\t" \
  "v_mfma_f32_16x16x32_bf16 v[124:127], a[188:191], a[12:15], v[124:127]\n\t" \
  "s_barrier\n\t" \
  "v_mfma_f32_16x16x32_bf16 v[0:3], a[128:131], a[32:35], v[0:3]\n\t" \
  "buffer_load_dwordx4 a[0:3], v248, s[16:19], s97 offen sc1\n\t" \
  "v_mfma_f32_16x16x32_bf16 v[0:3], a[64:67], a[48:51], v[0:3]\n\t" \
  "ds_read_b64_tr_b16 a[160:161], v184 offset:0\n\t" \
  "v_mfma_f32_16x16x32_bf16 v[32:35], a[64:67], a[52:55], v[32:35]\n\t" \
  "ds_read_b64_tr_b16 a[162:163], v184 offset:1024\n\t" \
  "v_mfma_f32_16x16x32_bf16 v[32:35], a[128:131], a[36:39], v[32:35]\n\t" \
  "buffer_load_dwordx4 a[4:7], v249, s[16:19], s97 offen sc1\n\t" \
  "v_mfma_f32_16x16x32_bf16 v[64:67], a[128:131], a[40:43], v[64:67]\n\t" \
  "ds_read_b64_tr_b16 a[164:165], v185 offset:0\n\t" \
  "v_mfma_f32_16x16x32_bf16 v[64:67], a[64:67], a[56:59], v[64:67]\n\t" \
  "buffer_load_dwordx4 a[8:11], v250, s[16:19], s97 offen sc1\n\t" \
  "v_mfma_f32_16x16x32_bf16 v[96:99], a[64:67], a[60:63], v[96:99]\n\t" \
  "ds_read_b64_tr_b16 a[166:167], v185 offset:1024\n\t" \
  "v_mfma_f32_16x16x32_bf16 v[96:99], a[128:131], a[44:47], v[96:99]\n\t" \
  "ds_read_b64_tr_b16 a[168:169], v186 offset:0\n\t" \
  "v_mfma_f32_16x16x32_bf16 v[4:7], a[132:135], a[32:35], v[4:7]\n\t" \
  "buffer_load_dwordx4 a[12:15], v251, s[16:19], s97 offen sc1\n\t" \
  "v_mfma_f32_16x16x32_bf16 v[4:7], a[68:71], a[48:51], v[4:7]\n\t" \
  "s_add_u32 s96, s78, 7340032\n\t" \
  "s_and_b32 s96, s96, 0x780000\n\t" \
  "s_add_u32 m0, s76, 81920\n\t" \
  "v_mfma_f32_16x16x32_bf16 v[36:39], a[68:71], a[52:55], v[36:39]\n\t" \
  "buffer_load_dwordx4 v200, s[20:23], s96 offen offset:0 lds\n\t" \
  "v_mfma_f32_16x16x32_bf16 v[36:39], a[132:135], a[36:39], v[36:39]\n\t" \
  "ds_read_b64_tr_b16 a[170:171], v186 offset:1024\n\t" \
  "v_mfma_f32_16x16x32_bf16 v[68:71], a[132:135], a[40:43], v[68:71]\n\t" \
  "ds_read_b64_tr_b16 a[172:173], v187 offset:0\n\t" \
  "v_mfma_f32_16x16x32_bf16 v[68:71], a[68:71], a[56:59], v[68:71]\n\t" \
  "buffer_load_dwordx4 v201, s[20:23], s96 offen offset:1024 lds\n\t" \
  "v_mfma_f32_16x16x32_bf16 v[100:103], a[68:71], a[60:63], v[100:103]\n\t" \
  "ds_read_b64_tr_b16 a[174:175], v187 offset:1024\n\t" \
  "v_mfma_f32_16x16x32_bf16 v[100:103], a[132:135], a[44:47], v[100:103]\n\t" \
  "buffer_load_dwordx4 v202, s[20:23], s96 offen offset:2048 lds\n\t" \
  "v_mfma_f32_16x16x32_bf16 v[8:11], a[136:139], a[32:35], v[8:11]\n\t" \
  "ds_read_b64_tr_b16 a[176:177], v188 offset:0\n\t" \
  "v_mfma_f32_16x16x32_bf16 v[8:11], a[72:75], a[48:51], v[8:11]\n\t" \
  "ds_read_b64_tr_b16 a[178:179], v188 offset:1024\n\t" \
  "v_mfma_f32_16x16x32_bf16 v[40:43], a[72:75], a[52:55], v[40:43]\n\t" \
  "buffer_load_dwordx4 v203, s[20:23], s96 offen offset:3072 lds\n\t" \
  "v_mfma_f32_16x16x32_bf16 v[40:43], a[136:139], a[36:39], v[40:43]\n\t" \
  "ds_read_b64_tr_b16 a[180:181], v189 offset:0\n\t" \
  "s_add_u32 s97, s97, s83\n\t" \
  "v_mfma_f32_16x16x32_bf16 v[72:75], a[136:139], a[40:43], v[72:75]\n\t" \
  "buffer_load_dwordx4 a[16:19], v248, s[16:19], s97 offen sc1\n\t" \
  "v_mfma_f32_16x16x32_bf16 v[72:75], a[72:75], a[56:59], v[72:75]\n\t" \
  "ds_read_b64_tr_b16 a[182:183], v189 offset:1024\n\t" \
  "v_mfma_f32_16x16x32_bf16 v[104:107], a[72:75], a[60:63], v[104:107]\n\t" \
  "ds_read_b64_tr_b16 a[184:185], v190 offset:0\n\t" \
  "v_mfma_f32_16x16x32_bf16 v[104:107], a[136:139], a[44:47], v[104:107]\n\t" \
  "buffer_load_dwordx4 a[20:23], v249, s[16:19], s97 offen sc1\n\t" \
  "v_mfma_f32_16x16x32_bf16 v[12:15], a[140:143], a[32:35], v[12:15]\n\t" \
  "ds_read_b64_tr_b16 a[186:187], v190 offset:1024\n\t" \
  "v_mfma_f32_16x16x32_bf16 v[12:15], a[76:79], a[48:51], v[12:15]\n\t" \
  "buffer_load_dwordx4 a[24:27], v250, s[16:19], s97 offen sc1\n\t" \
  "v_mfma_f32_16x16x32_bf16 v[44:47], a[76:79], a[52:55], v[44:47]\n\t" \
  "ds_read_b64_tr_b16 a[188:189], v191 offset:0\n\t" \
  "v_mfma_f32_16x16x32_bf16 v[44:47], a[140:143], a[36:39], v[44:47]\n\t" \
  "ds_read_b64_tr_b16 a[190:191], v191 offset:1024\n\t" \
  "v_mfma_f32_16x16x32_bf16 v[76:79], a[140:143], a[40:43], v[76:79]\n\t" \
  "buffer_load_dwordx4 a[28:31], v251, s[16:19], s97 offen sc1\n\t" \
  "v_mfma_f32_16x16x32_bf16 v[76:79], a[76:79], a[56:59], v[76:79]\n\t" \
  "ds_read_b64_tr_b16 a[96:97], v184 offset:8192\n\t" \
  "s_mov_b32 s99, 49152\n\t" \
  "v_mfma_f32_16x16x32_bf16 v[108:111], a[76:79], a[60:63], v[108:111]\n\t" \
  "ds_read_b64_tr_b16 a[98:99], v184 offset:9216\n\t" \
  "v_mfma_f32_16x16x32_bf16 v[108:111], a[140:143], a[44:47], v[108:111]\n\t" \
  "ds_read_b64_tr_b16 a[100:101], v185 offset:8192\n\t" \
  "v_mfma_f32_16x16x32_bf16 v[16:19], a[144:147], a[32:35], v[16:19]\n\t" \
  "buffer_load_dwordx4 v[152:155], v209, s[8:11], s99 offen nt\n\t" \
  "v_mfma_f32_16x16x32_bf16 v[16:19], a[80:83], a[48:51], v[16:19]\n\t" \
  "ds_read_b64_tr_b16 a[102:103], v185 offset:9216\n\t" \
  "s_mov_b32 s99, 61440\n\t" \
  "v_mfma_f32_16x16x32_bf16 v[48:51], a[80:83], a[52:55], v[48:51]\n\t" \
  "ds_read_b64_tr_b16 a[104:105], v186 offset:8192\n\t" \
  "v_mfma_f32_16x16x32_bf16 v[48:51], a[144:147], a[36:39], v[48:51]\n\t" \
  "ds_read_b64_tr_b16 a[106:107], v186 offset:9216\n\t" \
  "v_mfma_f32_16x16x32_bf16 v[80:83], a[144:147], a[40:43], v[80:83]\n\t" \
  "ds_read_b64_tr_b16 a[108:109], v187 offset:8192\n\t" \
  "v_mfma_f32_16x16x32_bf16 v[80:83], a[80:83], a[56:59], v[80:83]\n\t" \
  "buffer_load_dwordx4 v[156:159], v209, s[8:11], s99 offen nt\n\t" \
  "v_mfma_f32_16x16x32_bf16 v[112:115], a[80:83], a[60:63], v[112:115]\n\t" \
  "ds_read_b64_tr_b16 a[110:111], v187 offset:9216\n\t" \
  "s_mov_b32 s99, 73728\n\t" \
  "v_mfma_f32_16x16x32_bf16 v[112:115], a[144:147], a[44:47], v[112:115]\n\t" \
  "ds_read_b64_tr_b16 a[112:113], v188 offset:8192\n\t" \
  "v_mfma_f32_16x16x32_bf16 v[20:23], a[148:151], a[32:35], v[20:23]\n\t" \
  "ds_read_b64_tr_b16 a[114:115], v188 offset:9216\n\t" \
  "v_mfma_f32_16x16x32_bf16 v[20:23], a[84:87], a[48:51], v[20:23]\n\t" \
  "ds_read_b64_tr_b16 a[116:117], v189 offset:8192\n\t" \
  "v_mfma_f32_16x16x32_bf16 v[52:55], a[84:87], a[52:55], v[52:55]\n\t" \
  "ds_read_b64_tr_b16 a[118:119], v189 offset:9216\n\t" \
  "v_mfma_f32_16x16x32_bf16 v[52:55], a[148:151], a[36:39], v[52:55]\n\t" \
  "buffer_load_dwordx4 v[160:163], v209, s[8:11], s99 offen nt\n\t" \
  "v_mfma_f32_16x16x32_bf16 v[84:87], a[148:151], a[40:43], v[84:87]\n\t" \
  "ds_read_b64_tr_b16 a[120:121], v190 offset:8192\n\t" \
  "s_mov_b32 s99, 86016\n\t" \
  "v_mfma_f32_16x16x32_bf16 v[84:87], a[84:87], a[56:59], v[84:87]\n\t" \
  "ds_read_b64_tr_b16 a[122:123], v190 offset:9216\n\t" \
  "v_mfma_f32_16x16x32_bf16 v[116:119], a[84:87], a[60:63], v[116:119]\n\t" \
  "ds_read_b64_tr_b16 a[124:125], v191 offset:8192\n\t" \
  "v_mfma_f32_16x16x32_bf16 v[116:119], a[148:151], a[44:47], v[116:119]\n\t" \
  "ds_read_b64_tr_b16 a[126:127], v191 offset:9216\n\t" \
  "v_mfma_f32_16x16x32_bf16 v[24:27], a[152:155], a[32:35], v[24:27]\n\t" \
  "v_mfma_f32_16x16x32_bf16 v[24:27], a[88:91], a[48:51], v[24:27]\n\t" \
  "buffer_load_dwordx4 v[164:167], v209, s[8:11], s99 offen nt\n\t" \
  "v_mfma_f32_16x16x32_bf16 v[56:59], a[88:91], a[52:55], v[56:59]\n\t" \
  "s_add_u32 s99, s53, 0\n\t" \
  "v_mfma_f32_16x16x32_bf16 v[56:59], a[152:155], a[36:39], v[56:59]\n\t" \
  "v_mfma_f32_16x16x32_bf16 v[88:91], a[152:155], a[40:43], v[88:91]\n\t" \
  "v_mfma_f32_16x16x32_bf16 v[88:91], a[88:91], a[56:59], v[88:91]\n\t" \
  "v_mfma_f32_16x16x32_bf16 v[120:123], a[88:91], a[60:63], v[120:123]\n\t" \
  "v_mfma_f32_16x16x32_bf16 v[120:123], a[152:155], a[44:47], v[120:123]\n\t" \
  "buffer_load_dword v246, v245, s[68:71], s99 offen\n\t" \
  "v_mfma_f32_16x16x32_bf16 v[28:31], a[156:159], a[32:35], v[28:31]\n\t" \
  "v_mfma_f32_16x16x32_bf16 v[28:31], a[92:95], a[48:51], v[28:31]\n\t" \
  "v_mfma_f32_16x16x32_bf16 v[60:63], a[92:95], a[52:55], v[60:63]\n\t" \
  "v_mfma_f32_16x16x32_bf16 v[60:63], a[156:159], a[36:39], v[60:63]\n\t" \
  "v_mfma_f32_16x16x32_bf16 v[92:95], a[156:159], a[40:43], v[92:95]\n\t" \
  "v_mfma_f32_16x16x32_bf16 v[92:95], a[92:95], a[56:59], v[92:95]\n\t" \
  "v_mfma_f32_16x16x32_bf16 v[124:127], a[92:95], a[60:63], v[124:127]\n\t" \
  "s_waitcnt vmcnt(33) lgkmcnt(0)\n\t" \
  "v_mfma_f32_16x16x32_bf16 v[124:127], a[156:159], a[44:47], v[124:127]\n\t" \
  "s_barrier\n\t" \
  "v_mfma_f32_16x16x32_bf16 v[0:3], a[160:163], a[192:195], v[0:3]\n\t" \
  "ds_read_b64_tr_b16 a[128:129], v184 offset:16384\n\t" \
  "v_mfma_f32_16x16x32_bf16 v[0:3], a[96:99], a[208:211], v[0:3]\n\t" \
  "ds_read_b64_tr_b16 a[130:131], v184 offset:17408\n\t" \
  "v_mfma_f32_16x16x32_bf16 v[32:35], a[96:99], a[212:215], v[32:35]\n\t" \
  "ds_read_b64_tr_b16 a[132:133], v185 offset:16384\n\t" \
  "v_mfma_f32_16x16x32_bf16 v[32:35], a[160:163], a[196:199], v[32:35]\n\t" \
  "ds_read_b64_tr_b16 a[134:135], v185 offset:17408\n\t" \
  "v_mfma_f32_16x16x32_bf16 v[64:67], a[160:163], a[200:203], v[64:67]\n\t" \
  "ds_read_b64_tr_b16 a[136:137], v186 offset:16384\n\t" \
  "v_mfma_f32_16x16x32_bf16 v[64:67], a[96:99], a[216:219], v[64:67]\n\t" \
  "ds_read_b64_tr_b16 a[138:139], v186 offset:17408\n\t" \
  "v_mfma_f32_16x16x32_bf16 v[96:99], a[96:99], a[220:223], v[96:99]\n\t" \
  "ds_read_b64_tr_b16 a[140:141], v187 offset:16384\n\t" \
  "v_mfma_f32_16x16x32_bf16 v[96:99], a[160:163], a[204:207], v[96:99]\n\t" \
  "ds_read_b64_tr_b16 a[142:143], v187 offset:17408\n\t" \
  "v_mfma_f32_16x16x32_bf16 v[4:7], a[164:167], a[192:195], v[4:7]\n\t" \
  "ds_read_b64_tr_b16 a[144:145], v188 offset:16384\n\t" \
  "v_mfma_f32_16x16x32_bf16 v[4:7], a[100:103], a[208:211], v[4:7]\n\t" \
  "ds_read_b64_tr_b16 a[146:147], v188 offset:17408\n\t" \
  "v_mfma_f32_16x16x32_bf16 v[36:39], a[100:103], a[212:215], v[36:39]\n\t" \
  "ds_read_b64_tr_b16 a[148:149], v189 offset:16384\n\t" \
  "v_mfma_f32_16x16x32_bf16 v[36:39], a[164:167], a[196:199], v[36:39]\n\t" \
  "ds_read_b64_tr_b16 a[150:151], v189 offset:17408\n\t" \
  "v_mfma_f32_16x16x32_bf16 v[68:71], a[164:167], a[200:203], v[68:71]\n\t" \
  "ds_read_b64_tr_b16 a[152:153], v190 offset:16384\n\t" \
  "v_mfma_f32_16x16x32_bf16 v[68:71], a[100:103], a[216:219], v[68:71]\n\t" \
  "ds_read_b64_tr_b16 a[154:155], v190 offset:17408\n\t" \
  "v_mfma_f32_16x16x32_bf16 v[100:103], a[100:103], a[220:223], v[100:103]\n\t" \
  "ds_read_b64_tr_b16 a[156:157], v191 offset:16384\n\t" \
  "v_mfma_f32_16x16x32_bf16 v[100:103], a[164:167], a[204:207], v[100:103]\n\t" \
  "ds_read_b64_tr_b16 a[158:159], v191 offset:17408\n\t" \
  "v_mfma_f32_16x16x32_bf16 v[8:11], a[168:171], a[192:195], v[8:11]\n\t" \
  "ds_read_b64_tr_b16 a[64:65], v184 offset:24576\n\t" \
  "v_mfma_f32_16x16x32_bf16 v[8:11], a[104:107], a[208:211], v[8:11]\n\t" \
  "v_mfma_f32_16x16x32_bf16 v[40:43], a[104:107], a[212:215], v[40:43]\n\t" \
  "ds_read_b64_tr_b16 a[66:67], v184 offset:25600\n\t" \
  "v_mfma_f32_16x16x32_bf16 v[40:43], a[168:171], a[196:199], v[40:43]\n\t" \
  "v_mfma_f32_16x16x32_bf16 v[72:75], a[168:171], a[200:203], v[72:75]\n\t" \
  "ds_read_b64_tr_b16 a[68:69], v185 offset:24576\n\t" \
  "v_mfma_f32_16x16x32_bf16 v[72:75], a[104:107], a[216:219], v[72:75]\n\t" \
  "ds_read_b64_tr_b16 a[70:71], v185 offset:25600\n\t" \
  "v_mfma_f32_16x16x32_bf16 v[104:107], a[104:107], a[220:223], v[104:107]\n\t" \
  "v_mfma_f32_16x16x32_bf16 v[104:107], a[168:171], a[204:207], v[104:107]\n\t" \
  "ds_read_b64_tr_b16 a[72:73], v186 offset:24576\n\t" \
  "v_mfma_f32_16x16x32_bf16 v[12:15], a[172:175], a[192:195], v[12:15]\n\t" \
  "v_mfma_f32_16x16x32_bf16 v[12:15], a[108:111], a[208:211], v[12:15]\n\t" \
  "ds_read_b64_tr_b16 a[74:75], v186 offset:25600\n\t" \
  "v_mfma_f32_16x16x32_bf16 v[44:47], a[108:111], a[212:215], v[44:47]\n\t" \
  "ds_read_b64_tr_b16 a[76:77], v187 offset:24576\n\t" \
  "v_mfma_f32_16x16x32_bf16 v[44:47], a[172:175], a[196:199], v[44:47]\n\t" \
  ".Lhs4:\n\ts_cbranch_execz .Lhs4n\n\t" \
  "v_mfma_f32_16x16x32_bf16 v[76:79], a[172:175], a[200:203], v[76:79]\n\t" \
  "ds_read_b64_tr_b16 a[78:79], v187 offset:25600\n\t" \
  "v_mfma_f32_16x16x32_bf16 v[76:79], a[108:111], a[216:219], v[76:79]\n\t" \
  "s_add_u32 s97, s78, 6815744\n\t" \
  "ds_read_b64_tr_b16 a[80:81], v188 offset:24576\n\t" \
  "s_bfe_u32 s97, s97, 0x140003\n\t" \
  "v_mfma_f32_16x16x32_bf16 v[108:111], a[108:111], a[220:223], v[108:111]\n\t" \
  "s_add_u32 s97, s97, s82\n\t" \
  "v_mfma_f32_16x16x32_bf16 v[108:111], a[172:175], a[204:207], v[108:111]\n\t" \
  "buffer_load_dwordx4 a[32:35], v248, s[16:19], s97 offen sc1\n\t" \
  "v_mfma_f32_16x16x32_bf16 v[16:19], a[176:179], a[192:195], v[16:19]\n\t" \
  "ds_read_b64_tr_b16 a[82:83], v188 offset:25600\n\t" \
  "v_mfma_f32_16x16x32_bf16 v[16:19], a[112:115], a[208:211], v[16:19]\n\t" \
  "ds_read_b64_tr_b16 a[84:85], v189 offset:24576\n\t" \
  "v_mfma_f32_16x16x32_bf16 v[48:51], a[112:115], a[212:215], v[48:51]\n\t" \
  "buffer_load_dwordx4 a[36:39], v249, s[16:19], s97 offen sc1\n\t" \
  "v_mfma_f32_16x16x32_bf16 v[48:51], a[176:179], a[196:199], v[48:51]\n\t" \
  "ds_read_b64_tr_b16 a[86:87], v189 offset:25600\n\t" \
  "v_mfma_f32_16x16x32_bf16 v[80:83], a[176:179], a[200:203], v[80:83]\n\t" \
  "ds_read_b64_tr_b16 a[88:89], v190 offset:24576\n\t" \
  "v_mfma_f32_16x16x32_bf16 v[80:83], a[112:115], a[216:219], v[80:83]\n\t" \
  "v_mfma_f32_16x16x32_bf16 v[112:115], a[112:115], a[220:223], v[112:115]\n\t" \
  "buffer_load_dwordx4 a[40:43], v250, s[16:19], s97 offen sc1\n\t" \
  "v_mfma_f32_16x16x32_bf16 v[112:115], a[176:179], a[204:207], v[112:115]\n\t" \
  "ds_read_b64_tr_b16 a[90:91], v190 offset:25600\n\t" \
  "v_mfma_f32_16x16x32_bf16 v[20:23], a[180:183], a[192:195], v[20:23]\n\t" \
  "ds_read_b64_tr_b16 a[92:93], v191 offset:24576\n\t" \
  "v_mfma_f32_16x16x32_bf16 v[20:23], a[116:119], a[208:211], v[20:23]\n\t" \
  "ds_read_b64_tr_b16 a[94:95], v191 offset:25600\n\t" \
  "v_mfma_f32_16x16x32_bf16 v[52:55], a[116:119], a[212:215], v[52:55]\n\t" \
  "buffer_load_dwordx4 a[44:47], v251, s[16:19], s97 offen sc1\n\t" \
  "v_mfma_f32_16x16x32_bf16 v[52:55], a[180:183], a[196:199], v[52:55]\n\t" \
  "s_add_u32 s96, s78, 7864320\n\t" \
  "s_and_b32 s96, s96, 0x780000\n\t" \
  "s_add_u32 m0, s76, 98304\n\t" \
  "v_mfma_f32_16x16x32_bf16 v[84:87], a[180:183], a[200:203], v[84:87]\n\t" \
  "v_mfma_f32_16x16x32_bf16 v[84:87], a[116:119], a[216:219], v[84:87]\n\t" \
  "v_mfma_f32_16x16x32_bf16 v[116:119], a[116:119], a[220:223], v[116:119]\n\t" \
  "buffer_load_dwordx4 v200, s[20:23], s96 offen offset:0 lds\n\t" \
  "v_mfma_f32_16x16x32_bf16 v[116:119], a[180:183], a[204:207], v[116:119]\n\t" \
  "v_mfma_f32_16x16x32_bf16 v[24:27], a[184:187], a[192:195], v[24:27]\n\t" \
  "v_mfma_f32_16x16x32_bf16 v[24:27], a[120:123], a[208:211], v[24:27]\n\t" \
  "buffer_load_dwordx4 v201, s[20:23], s96 offen offset:1024 lds\n\t" \
  "v_mfma_f32_16x16x32_bf16 v[56:59], a[120:123], a[212:215], v[56:59]\n\t" \
  "v_mfma_f32_16x16x32_bf16 v[56:59], a[184:187], a[196:199], v[56:59]\n\t" \
  "v_mfma_f32_16x16x32_bf16 v[88:91], a[184:187], a[200:203], v[88:91]\n\t" \
  "v_mfma_f32_16x16x32_bf16 v[88:91], a[120:123], a[216:219], v[88:91]\n\t" \
  "buffer_load_dwordx4 v202, s[20:23], s96 offen offset:2048 lds\n\t" \
  "v_mfma_f32_16x16x32_bf16 v[120:123], a[120:123], a[220:223], v[120:123]\n\t" \
  "v_mfma_f32_16x16x32_bf16 v[120:123], a[184:187], a[204:207], v[120:123]\n\t" \
  "v_mfma_f32_16x16x32_bf16 v[28:31], a[188:191], a[192:195], v[28:31]\n\t" \
  "v_mfma_f32_16x16x32_bf16 v[28:31], a[124:127], a[208:211], v[28:31]\n\t" \
  "buffer_load_dwordx4 v203, s[20:23], s96 offen offset:3072 lds\n\t" \
  "v_mfma_f32_16x16x32_bf16 v[60:63], a[124:127], a[212:215], v[60:63]\n\t" \
  "v_mfma_f32_16x16x32_bf16 v[60:63], a[188:191], a[196:199], v[60:63]\n\t" \
  "v_mfma_f32_16x16x32_bf16 v[92:95], a[188:191], a[200:203], v[92:95]\n\t" \
  "v_mfma_f32_16x16x32_bf16 v[92:95], a[124:127], a[216:219], v[92:95]\n\t" \
  "s_add_u32 s97, s97, s83\n\t" \
  "v_mfma_f32_16x16x32_bf16 v[124:127], a[124:127], a[220:223], v[124:127]\n\t" \
  "s_waitcnt vmcnt(25) lgkmcnt(0)\n\t" \
  "v_mfma_f32_16x16x32_bf16 v[124:127], a[188:191], a[204:207], v[124:127]\n\t" \
  "s_barrier\n\t" \
  "v_mfma_f32_16x16x32_bf16 v[0:3], a[128:131], a[224:227], v[0:3]\n\t" \
  "buffer_load_dwordx4 a[48:51], v248, s[16:19], s97 offen sc1\n\t" \
  "v_mfma_f32_16x16x32_bf16 v[0:3], a[64:67], a[240:243], v[0:3]\n\t" \
  "ds_read_b64_tr_b16 a[160:161], v184 offset:32768\n\t" \
  "v_mfma_f32_16x16x32_bf16 v[32:35], a[64:67], a[244:247], v[32:35]\n\t" \
  "ds_read_b64_tr_b16 a[162:163], v184 offset:33792\n\t" \
  "v_mfma_f32_16x16x32_bf16 v[32:35], a[128:131], a[228:231], v[32:35]\n\t" \
  "buffer_load_dwordx4 a[52:55], v249, s[16:19], s97 offen sc1\n\t" \
  "v_mfma_f32_16x16x32_bf16 v[64:67], a[128:131], a[232:235], v[64:67]\n\t" \
  "s_waitcnt vmcnt(14)\n\t" \
  "v_lshlrev_b32 v128, 16, v152\n\t" \
  "v_and_b32 v152, 0xffff0000, v152\n\t" \
  "v_mfma_f32_16x16x32_bf16 v[64:67], a[64:67], a[248:251], v[64:67]\n\t" \
  "ds_read_b64_tr_b16 a[164:165], v185 offset:32768\n\t" \
  "v_fma_f32 v176, v128, v172, v176\n\t" \
  "v_mfma_f32_16x16x32_bf16 v[96:99], a[64:67], a[252:255], v[96:99]\n\t" \
  "ds_read_b64_tr_b16 a[166:167], v185 offset:33792\n\t" \
  "v_fma_f32 v180, v152, v172, v180\n\t" \
  "v_mfma_f32_16x16x32_bf16 v[96:99], a[128:131], a[236:239], v[96:99]\n\t" \
  "ds_read_b64_tr_b16 a[168:169], v186 offset:32768\n\t" \
  "v_lshlrev_b32 v128, 16, v153\n\t" \
  "v_mfma_f32_16x16x32_bf16 v[4:7], a[132:135], a[224:227], v[4:7]\n\t" \
  "buffer_load_dwordx4 a[56:59], v250, s[16:19], s97 offen sc1\n\t" \
  "v_mfma_f32_16x16x32_bf16 v[4:7], a[68:71], a[240:243], v[4:7]\n\t" \
  "ds_read_b64_tr_b16 a[170:171], v186 offset:33792\n\t" \
  "v_and_b32 v153, 0xffff0000, v153\n\t" \
  "v_mfma_f32_16x16x32_bf16 v[36:39], a[68:71], a[244:247], v[36:39]\n\t" \
  "ds_read_b64_tr_b16 a[172:173], v187 offset:32768\n\t" \
  "v_fma_f32 v177, v128, v172, v177\n\t" \
  "v_mfma_f32_16x16x32_bf16 v[36:39], a[132:135], a[228:231], v[36:39]\n\t" \
  "v_fma_f32 v181, v153, v172, v181\n\t" \
  "v_lshlrev_b32 v128, 16, v154\n\t" \
  "v_mfma_f32_16x16x32_bf16 v[68:71], a[132:135], a[232:235], v[68:71]\n\t" \
  "buffer_load_dwordx4 a[60:63], v251, s[16:19], s97 offen sc1\n\t" \
  "v_mfma_f32_16x16x32_bf16 v[68:71], a[68:71], a[248:251], v[68:71]\n\t" \
  "s_add_u32 s97, s78, 7340032\n\t" \
  "s_bfe_u32 s97, s97, 0x140003\n\t" \
  "s_add_u32 s97, s97, s82\n\t" \
  "v_mfma_f32_16x16x32_bf16 v[100:103], a[68:71], a[252:255], v[100:103]\n\t" \
  "ds_read_b64_tr_b16 a[174:175], v187 offset:33792\n\t" \
  "v_and_b32 v154, 0xffff0000, v154\n\t" \
  "v_mfma_f32_16x16x32_bf16 v[100:103], a[132:135], a[236:239], v[100:103]\n\t" \
  "buffer_load_dwordx4 a[192:195], v248, s[16:19], s97 offen sc1\n\t" \
  "v_mfma_f32_16x16x32_bf16 v[8:11], a[136:139], a[224:227], v[8:11]\n\t" \
  "ds_read_b64_tr_b16 a[176:177], v188 offset:32768\n\t" \
  "v_fma_f32 v178, v128, v172, v178\n\t" \
  "v_mfma_f32_16x16x32_bf16 v[8:11], a[72:75], a[240:243], v[8:11]\n\t" \
  "ds_read_b64_tr_b16 a[178:179], v188 offset:33792\n\t" \
  "v_fma_f32 v182, v154, v172, v182\n\t" \
  "v_mfma_f32_16x16x32_bf16 v[40:43], a[72:75], a[244:247], v[40:43]\n\t" \
  "ds_read_b64_tr_b16 a[180:181], v189 offset:32768\n\t" \
  "v_lshlrev_b32 v128, 16, v155\n\t" \
  "v_mfma_f32_16x16x32_bf16 v[40:43], a[136:139], a[228:231], v[40:43]\n\t" \
  "buffer_load_dwordx4 a[196:199], v249, s[16:19], s97 offen sc1\n\t" \
  "v_mfma_f32_16x16x32_bf16 v[72:75], a[136:139], a[232:235], v[72:75]\n\t" \
  "ds_read_b64_tr_b16 a[182:183], v189 offset:33792\n\t" \
  "v_and_b32 v155, 0xffff0000, v155\n\t" \
  "v_mfma_f32_16x16x32_bf16 v[72:75], a[72:75], a[248:251], v[72:75]\n\t" \
  "ds_read_b64_tr_b16 a[184:185], v190 offset:32768\n\t" \
  "v_fma_f32 v179, v128, v172, v179\n\t" \
  "v_mfma_f32_16x16x32_bf16 v[104:107], a[72:75], a[252:255], v[104:107]\n\t" \
  "ds_read_b64_tr_b16 a[186:187], v190 offset:33792\n\t" \
  "v_fma_f32 v183, v155, v172, v183\n\t" \
  "v_mfma_f32_16x16x32_bf16 v[104:107], a[136:139], a[236:239], v[104:107]\n\t" \
  "buffer_load_dwordx4 a[200:203], v250, s[16:19], s97 offen sc1\n\t" \
  "v_mfma_f32_16x16x32_bf16 v[12:15], a[140:143], a[224:227], v[12:15]\n\t" \
  "s_waitcnt vmcnt(18)\n\t" \
  "v_lshlrev_b32 v128, 16, v156\n\t" \
  "v_and_b32 v156, 0xffff0000, v156\n\t" \
  "v_mfma_f32_16x16x32_bf16 v[12:15], a[76:79], a[240:243], v[12:15]\n\t" \
  "ds_read_b64_tr_b16 a[188:189], v191 offset:32768\n\t" \
  "v_fma_f32 v176, v128, v173, v176\n\t" \
  "v_mfma_f32_16x16x32_bf16 v[44:47], a[76:79], a[244:247], v[44:47]\n\t" \
  "ds_read_b64_tr_b16 a[190:191], v191 offset:33792\n\t" \
  "v_fma_f32 v180, v156, v173, v180\n\t" \
  "v_mfma_f32_16x16x32_bf16 v[44:47], a[140:143], a[228:231], v[44:47]\n\t" \
  "buffer_load_dwordx4 a[204:207], v251, s[16:19], s97 offen sc1\n\t" \
  "v_mfma_f32_16x16x32_bf16 v[76:79], a[140:143], a[232:235], v[76:79]\n\t" \
  "ds_read_b64_tr_b16 a[96:97], v184 offset:40960\n\t" \
  "v_lshlrev_b32 v128, 16, v157\n\t" \
  "v_mfma_f32_16x16x32_bf16 v[76:79], a[76:79], a[248:251], v[76:79]\n\t" \
  "s_add_u32 s97, s97, s83\n\t" \
  "ds_read_b64_tr_b16 a[98:99], v184 offset:41984\n\t" \
  "v_and_b32 v157, 0xffff0000, v157\n\t" \
  "v_mfma_f32_16x16x32_bf16 v[108:111], a[76:79], a[252:255], v[108:111]\n\t" \
  "ds_read_b64_tr_b16 a[100:101], v185 offset:40960\n\t" \
  "v_fma_f32 v177, v128, v173, v177\n\t" \
  "v_mfma_f32_16x16x32_bf16 v[108:111], a[140:143], a[236:239], v[108:111]\n\t" \
  "ds_read_b64_tr_b16 a[102:103], v185 offset:41984\n\t" \
  "v_fma_f32 v181, v157, v173, v181\n\t" \
  "v_mfma_f32_16x16x32_bf16 v[16:19], a[144:147], a[224:227], v[16:19]\n\t" \
  "buffer_load_dwordx4 a[208:211], v248, s[16:19], s97 offen sc1\n\t" \
  "v_mfma_f32_16x16x32_bf16 v[16:19], a[80:83], a[240:243], v[16:19]\n\t" \
  "ds_read_b64_tr_b16 a[104:105], v186 offset:40960\n\t" \
  "v_lshlrev_b32 v128, 16, v158\n\t" \
  "v_mfma_f32_16x16x32_bf16 v[48:51], a[80:83], a[244:247], v[48:51]\n\t" \
  "v_and_b32 v158, 0xffff0000, v158\n\t" \
  "v_fma_f32 v178, v128, v173, v178\n\t" \
  "v_mfma_f32_16x16x32_bf16 v[48:51], a[144:147], a[228:231], v[48:51]\n\t" \
  "ds_read_b64_tr_b16 a[106:107], v186 offset:41984\n\t" \
  "v_fma_f32 v182, v158, v173, v182\n\t" \
  "v_mfma_f32_16x16x32_bf16 v[80:83], a[144:147], a[232:235], v[80:83]\n\t" \
  "ds_read_b64_tr_b16 a[108:109], v187 offset:40960\n\t" \
  "v_lshlrev_b32 v128, 16, v159\n\t" \
  "v_mfma_f32_16x16x32_bf16 v[80:83], a[80:83], a[248:251], v[80:83]\n\t" \
  "v_and_b32 v159, 0xffff0000, v159\n\t" \
  "v_fma_f32 v179, v128, v173, v179\n\t" \
  "v_mfma_f32_16x16x32_bf16 v[112:115], a[80:83], a[252:255], v[112:115]\n\t" \
  "ds_read_b64_tr_b16 a[110:111], v187 offset:41984\n\t" \
  "v_fma_f32 v183, v159, v173, v183\n\t" \
  "v_mfma_f32_16x16x32_bf16 v[112:115], a[144:147], a[236:239], v[112:115]\n\t" \
  "s_waitcnt vmcnt(19)\n\t" \
  "v_lshlrev_b32 v128, 16, v160\n\t" \
  "v_and_b32 v160, 0xffff0000, v160\n\t" \
  "v_mfma_f32_16x16x32_bf16 v[20:23], a[148:151], a[224:227], v[20:23]\n\t" \
  "buffer_load_dwordx4 a[212:215], v249, s[16:19], s97 offen sc1\n\t" \
  "v_mfma_f32_16x16x32_bf16 v[20:23], a[84:87], a[240:243], v[20:23]\n\t" \
  "ds_read_b64_tr_b16 a[112:113], v188 offset:40960\n\t" \
  "v_fma_f32 v176, v128, v174, v176\n\t" \
  "v_mfma_f32_16x16x32_bf16 v[52:55], a[84:87], a[244:247], v[52:55]\n\t" \
  "ds_read_b64_tr_b16 a[114:115], v188 offset:41984\n\t" \
  "v_fma_f32 v180, v160, v174, v180\n\t" \
  "v_mfma_f32_16x16x32_bf16 v[52:55], a[148:151], a[228:231], v[52:55]\n\t" \
  "ds_read_b64_tr_b16 a[116:117], v189 offset:40960\n\t" \
  "v_lshlrev_b32 v128, 16, v161\n\t" \
  "v_mfma_f32_16x16x32_bf16 v[84:87], a[148:151], a[232:235], v[84:87]\n\t" \
  "ds_read_b64_tr_b16 a[118:119], v189 offset:41984\n\t" \
  "v_and_b32 v161, 0xffff0000, v161\n\t" \
  "v_mfma_f32_16x16x32_bf16 v[84:87], a[84:87], a[248:251], v[84:87]\n\t" \
  "v_fma_f32 v177, v128, v174, v177\n\t" \
  "v_fma_f32 v181, v161, v174, v181\n\t" \
  "v_mfma_f32_16x16x32_bf16 v[116:119], a[84:87], a[252:255], v[116:119]\n\t" \
  "ds_read_b64_tr_b16 a[120:121], v190 offset:40960\n\t" \
  "v_lshlrev_b32 v128, 16, v162\n\t" \
  "v_mfma_f32_16x16x32_bf16 v[116:119], a[148:151], a[236:239], v[116:119]\n\t" \
  "buffer_load_dwordx4 a[216:219], v250, s[16:19], s97 offen sc1\n\t" \
  "v_mfma_f32_16x16x32_bf16 v[24:27], a[152:155], a[224:227], v[24:27]\n\t" \
  "ds_read_b64_tr_b16 a[122:123], v190 offset:41984\n\t" \
  "v_and_b32 v162, 0xffff0000, v162\n\t" \
  "v_mfma_f32_16x16x32_bf16 v[24:27], a[88:91], a[240:243], v[24:27]\n\t" \
  "ds_read_b64_tr_b16 a[124:125], v191 offset:40960\n\t" \
  "v_fma_f32 v178, v128, v174, v178\n\t" \
  "v_mfma_f32_16x16x32_bf16 v[56:59], a[88:91], a[244:247], v[56:59]\n\t" \
  "v_fma_f32 v182, v162, v174, v182\n\t" \
  "v_lshlrev_b32 v128, 16, v163\n\t" \
  "v_mfma_f32_16x16x32_bf16 v[56:59], a[152:155], a[228:231], v[56:59]\n\t" \
  "ds_read_b64_tr_b16 a[126:127], v191 offset:41984\n\t" \
  "v_and_b32 v163, 0xffff0000, v163\n\t" \
  "v_mfma_f32_16x16x32_bf16 v[88:91], a[152:155], a[232:235], v[88:91]\n\t" \
  "v_fma_f32 v179, v128, v174, v179\n\t" \
  "v_fma_f32 v183, v163, v174, v183\n\t" \
  "v_mfma_f32_16x16x32_bf16 v[88:91], a[88:91], a[248:251], v[88:91]\n\t" \
  "s_waitcnt vmcnt(20)\n\t" \
  "v_lshlrev_b32 v128, 16, v164\n\t" \
  "v_and_b32 v164, 0xffff0000, v164\n\t" \
  "v_mfma_f32_16x16x32_bf16 v[120:123], a[88:91], a[252:255], v[120:123]\n\t" \
  "v_fma_f32 v176, v128, v175, v176\n\t" \
  "v_fma_f32 v180, v164, v175, v180\n\t" \
  "v_mfma_f32_16x16x32_bf16 v[120:123], a[152:155], a[236:239], v[120:123]\n\t" \
  "buffer_load_dwordx4 a[220:223], v251, s[16:19], s97 offen sc1\n\t" \
  "v_mfma_f32_16x16x32_bf16 v[28:31], a[156:159], a[224:227], v[28:31]\n\t" \
  "v_lshlrev_b32 v128, 16, v165\n\t" \
  "v_and_b32 v165, 0xffff0000, v165\n\t" \
  "v_mfma_f32_16x16x32_bf16 v[28:31], a[92:95], a[240:243], v[28:31]\n\t" \
  "v_fma_f32 v177, v128, v175, v177\n\t" \
  "v_fma_f32 v181, v165, v175, v181\n\t" \
  "v_mfma_f32_16x16x32_bf16 v[60:63], a[92:95], a[244:247], v[60:63]\n\t" \
  "v_lshlrev_b32 v128, 16, v166\n\t" \
  "v_and_b32 v166, 0xffff0000, v166\n\t" \
  "v_mfma_f32_16x16x32_bf16 v[60:63], a[156:159], a[228:231], v[60:63]\n\t" \
  "v_fma_f32 v178, v128, v175, v178\n\t" \
  "v_fma_f32 v182, v166, v175, v182\n\t" \
  "v_mfma_f32_16x16x32_bf16 v[92:95], a[156:159], a[232:235], v[92:95]\n\t" \
  "v_lshlrev_b32 v128, 16, v167\n\t" \
  "v_and_b32 v167, 0xffff0000, v167\n\t" \
  "v_mfma_f32_16x16x32_bf16 v[92:95], a[92:95], a[248:251], v[92:95]\n\t" \
  "s_add_u32 s97, s78, 7864320\n\t" \
  "v_fma_f32 v179, v128, v175, v179\n\t" \
  "v_fma_f32 v183, v167, v175, v183\n\t" \
  "v_mfma_f32_16x16x32_bf16 v[124:127], a[92:95], a[252:255], v[124:127]\n\t" \
  "s_waitcnt lgkmcnt(0)\n\t" \
  "s_bfe_u32 s97, s97, 0x140003\n\t" \
  "s_add_u32 s97, s97, s82\n\t" \
  "v_mfma_f32_16x16x32_bf16 v[124:127], a[156:159], a[236:239], v[124:127]\n\t" \
  "s_barrier\n\t" \
  "v_mfma_f32_16x16x32_bf16 v[0:3], a[160:163], a[0:3], v[0:3]\n\t" \
  "buffer_load_dwordx4 a[224:227], v248, s[16:19], s97 offen sc1\n\t" \
  "v_mfma_f32_16x16x32_bf16 v[0:3], a[96:99], a[16:19], v[0:3]\n\t" \
  "ds_read_b64_tr_b16 a[128:129], v192 offset:0\n\t" \
  "v_and_b32 v138, 7, v233\n\t" \
  "v_mfma_f32_16x16x32_bf16 v[32:35], a[96:99], a[20:23], v[32:35]\n\t" \
  "ds_read_b64_tr_b16 a[130:131], v192 offset:1024\n\t" \
  "v_lshlrev_b32 v138, 5, v138\n\t" \
  "v_mfma_f32_16x16x32_bf16 v[32:35], a[160:163], a[4:7], v[32:35]\n\t" \
  "v_permlane32_swap_b32 v176, v178\n\t" \
  "v_mfma_f32_16x16x32_bf16 v[64:67], a[160:163], a[8:11], v[64:67]\n\t" \
  "buffer_load_dwordx4 a[228:231], v249, s[16:19], s97 offen sc1\n\t" \
  "v_mfma_f32_16x16x32_bf16 v[64:67], a[96:99], a[24:27], v[64:67]\n\t" \
  "v_permlane32_swap_b32 v180, v182\n\t" \
  "v_mfma_f32_16x16x32_bf16 v[96:99], a[96:99], a[28:31], v[96:99]\n\t" \
  "v_permlane32_swap_b32 v177, v179\n\t" \
  "v_mfma_f32_16x16x32_bf16 v[96:99], a[160:163], a[12:15], v[96:99]\n\t" \
  "v_permlane32_swap_b32 v181, v183\n\t" \
  "v_mfma_f32_16x16x32_bf16 v[4:7], a[164:167], a[0:3], v[4:7]\n\t" \
  "buffer_load_dwordx4 a[232:235], v250, s[16:19], s97 offen sc1\n\t" \
  "v_mfma_f32_16x16x32_bf16 v[4:7], a[100:103], a[16:19], v[4:7]\n\t" \
  "ds_read_b64_tr_b16 a[132:133], v193 offset:0\n\t" \
  "v_add_f32 v176, v176, v178\n\t" \
  "v_mfma_f32_16x16x32_bf16 v[36:39], a[100:103], a[20:23], v[36:39]\n\t" \
  "ds_read_b64_tr_b16 a[134:135], v193 offset:1024\n\t" \
  "v_add_f32 v177, v177, v179\n\t" \
  "v_mfma_f32_16x16x32_bf16 v[36:39], a[164:167], a[4:7], v[36:39]\n\t" \
  "ds_read_b64_tr_b16 a[136:137], v194 offset:0\n\t" \
  "v_add_f32 v180, v180, v182\n\t" \
  "v_mfma_f32_16x16x32_bf16 v[68:71], a[164:167], a[8:11], v[68:71]\n\t" \
  "buffer_load_dwordx4 a[236:239], v251, s[16:19], s97 offen sc1\n\t" \
  "v_mfma_f32_16x16x32_bf16 v[68:71], a[100:103], a[24:27], v[68:71]\n\t" \
  "s_add_u32 s97, s97, s83\n\t" \
  "ds_read_b64_tr_b16 a[138:139], v194 offset:1024\n\t" \
  "v_add_f32 v181, v181, v183\n\t" \
  "v_mfma_f32_16x16x32_bf16 v[100:103], a[100:103], a[28:31], v[100:103]\n\t" \
  "ds_read_b64_tr_b16 a[140:141], v195 offset:0\n\t" \
  "v_lshrrev_b32 v139, 4, v233\n\t" \
  "v_mfma_f32_16x16x32_bf16 v[100:103], a[164:167], a[12:15], v[100:103]\n\t" \
  "buffer_load_dwordx4 a[240:243], v248, s[16:19], s97 offen sc1\n\t" \
  "v_mfma_f32_16x16x32_bf16 v[8:11], a[168:171], a[0:3], v[8:11]\n\t" \
  "ds_read_b64_tr_b16 a[142:143], v195 offset:1024\n\t" \
  "v_mfma_f32_16x16x32_bf16 v[8:11], a[104:107], a[16:19], v[8:11]\n\t" \
  "v_permlane16_swap_b32 v176, v177\n\t" \
  "v_mfma_f32_16x16x32_bf16 v[40:43], a[104:107], a[20:23], v[40:43]\n\t" \
  "ds_read_b64_tr_b16 a[144:145], v196 offset:0\n\t" \
  "v_mfma_f32_16x16x32_bf16 v[40:43], a[168:171], a[4:7], v[40:43]\n\t" \
  "buffer_load_dwordx4 a[244:247], v249, s[16:19], s97 offen sc1\n\t" \
  "v_mfma_f32_16x16x32_bf16 v[72:75], a[168:171], a[8:11], v[72:75]\n\t" \
  "ds_read_b64_tr_b16 a[146:147], v196 offset:1024\n\t" \
  "v_mfma_f32_16x16x32_bf16 v[72:75], a[104:107], a[24:27], v[72:75]\n\t" \
  "ds_read_b64_tr_b16 a[148:149], v197 offset:0\n\t" \
  "v_mfma_f32_16x16x32_bf16 v[104:107], a[104:107], a[28:31], v[104:107]\n\t" \
  "v_permlane16_swap_b32 v180, v181\n\t" \
  "v_mfma_f32_16x16x32_bf16 v[104:107], a[168:171], a[12:15], v[104:107]\n\t" \
  "buffer_load_dwordx4 a[248:251], v250, s[16:19], s97 offen sc1\n\t" \
  "v_mfma_f32_16x16x32_bf16 v[12:15], a[172:175], a[0:3], v[12:15]\n\t" \
  "ds_read_b64_tr_b16 a[150:151], v197 offset:1024\n\t" \
  "v_add_f32 v176, v176, v177\n\t" \
  "v_mfma_f32_16x16x32_bf16 v[12:15], a[108:111], a[16:19], v[12:15]\n\t" \
  "ds_read_b64_tr_b16 a[152:153], v198 offset:0\n\t" \
  "v_add_f32 v180, v180, v181\n\t" \
  "v_mfma_f32_16x16x32_bf16 v[44:47], a[108:111], a[20:23], v[44:47]\n\t" \
  "ds_read_b64_tr_b16 a[154:155], v198 offset:1024\n\t" \
  "v_lshl_add_u32 v138, v139, 3, v138\n\t" \
  "v_mfma_f32_16x16x32_bf16 v[44:47], a[172:175], a[4:7], v[44:47]\n\t" \
  "buffer_load_dwordx4 a[252:255], v251, s[16:19], s97 offen sc1\n\t" \
  "v_mfma_f32_16x16x32_bf16 v[76:79], a[172:175], a[8:11], v[76:79]\n\t" \
  "ds_read_b64_tr_b16 a[156:157], v199 offset:0\n\t" \
  "v_and_b32 v139, 0x7fffff00, v219\n\t" \
  "v_mfma_f32_16x16x32_bf16 v[76:79], a[108:111], a[24:27], v[76:79]\n\t" \
  "s_add_u32 m0, s76, 16384\n\t" \
  "ds_read_b64_tr_b16 a[158:159], v199 offset:1024\n\t" \
  "v_add_f32_dpp v136, v176, v176 row_ror:8 row_mask:0xf bank_mask:0xf\n\t" \
  "v_mfma_f32_16x16x32_bf16 v[108:111], a[108:111], a[28:31], v[108:111]\n\t" \
  "ds_read_b64_tr_b16 a[64:65], v192 offset:8192\n\t" \
  "v_add_f32_dpp v137, v180, v180 row_ror:8 row_mask:0xf bank_mask:0xf\n\t" \
  "v_mfma_f32_16x16x32_bf16 v[108:111], a[172:175], a[12:15], v[108:111]\n\t" \
  "ds_read_b64_tr_b16 a[66:67], v192 offset:9216\n\t" \
  "v_add_u32 v138, v139, v138\n\t" \
  "v_mfma_f32_16x16x32_bf16 v[16:19], a[176:179], a[0:3], v[16:19]\n\t" \
  "buffer_load_dwordx4 v224, s[60:63], s38 offen offset:0 lds\n\t" \
  "v_mfma_f32_16x16x32_bf16 v[16:19], a[112:115], a[16:19], v[16:19]\n\t" \
  "ds_write_b64 v138, v[136:137] offset:1024\n\t" \
  "v_mfma_f32_16x16x32_bf16 v[48:51], a[112:115], a[20:23], v[48:51]\n\t" \
  "ds_read_b64_tr_b16 a[68:69], v193 offset:8192\n\t" \
  "v_and_b32 v140, 3, v233\n\t" \
  "v_mfma_f32_16x16x32_bf16 v[48:51], a[176:179], a[4:7], v[48:51]\n\t" \
  "ds_read_b64_tr_b16 a[70:71], v193 offset:9216\n\t" \
  "v_bfe_u32 v141, v233, 4, 1\n\t" \
  "v_mfma_f32_16x16x32_bf16 v[80:83], a[176:179], a[8:11], v[80:83]\n\t" \
  "ds_read_b64_tr_b16 a[72:73], v194 offset:8192\n\t" \
  "v_lshl_add_u32 v140, v141, 2, v140\n\t" \
  "v_mfma_f32_16x16x32_bf16 v[80:83], a[112:115], a[24:27], v[80:83]\n\t" \
  "buffer_load_dwordx4 v225, s[60:63], s38 offen offset:1024 lds\n\t" \
  "v_mfma_f32_16x16x32_bf16 v[112:115], a[112:115], a[28:31], v[112:115]\n\t" \
  "ds_read_b64_tr_b16 a[74:75], v194 offset:9216\n\t" \
  "v_bfe_u32 v141, v233, 2, 2\n\t" \
  "v_mfma_f32_16x16x32_bf16 v[112:115], a[176:179], a[12:15], v[112:115]\n\t" \
  "ds_read_b64_tr_b16 a[76:77], v195 offset:8192\n\t" \
  "v_lshlrev_b32 v141, 8, v141\n\t" \
  "v_mfma_f32_16x16x32_bf16 v[20:23], a[180:183], a[0:3], v[20:23]\n\t" \
  "ds_read_b64_tr_b16 a[78:79], v195 offset:9216\n\t" \
  "v_lshl_add_u32 v141, v140, 3, v141\n\t" \
  "v_mfma_f32_16x16x32_bf16 v[20:23], a[116:119], a[16:19], v[20:23]\n\t" \
  "ds_read_b64_tr_b16 a[80:81], v196 offset:8192\n\t" \
  "v_lshl_add_u32 v141, s3, 6, v141\n\t" \
  "v_mfma_f32_16x16x32_bf16 v[52:55], a[116:119], a[20:23], v[52:55]\n\t" \
  "buffer_load_dwordx4 v226, s[60:63], s38 offen offset:2048 lds\n\t" \
  "v_mfma_f32_16x16x32_bf16 v[52:55], a[180:183], a[4:7], v[52:55]\n\t" \
  "ds_read_b64_tr_b16 a[82:83], v196 offset:9216\n\t" \
  "v_lshlrev_b32 v140, 4, v140\n\t" \
  "v_mfma_f32_16x16x32_bf16 v[84:87], a[180:183], a[8:11], v[84:87]\n\t" \
  "ds_read_b64_tr_b16 a[84:85], v197 offset:8192\n\t" \
  "v_and_b32 v142, 0x2c, v233\n\t" \
  "v_mfma_f32_16x16x32_bf16 v[84:87], a[116:119], a[24:27], v[84:87]\n\t" \
  "ds_read_b64_tr_b16 a[86:87], v197 offset:9216\n\t" \
  "v_lshl_or_b32 v140, v142, 26, v140\n\t" \
  "v_mfma_f32_16x16x32_bf16 v[116:119], a[116:119], a[28:31], v[116:119]\n\t" \
  "ds_read_b64_tr_b16 a[88:89], v198 offset:8192\n\t" \
  "v_add_u32 v141, 151552, v141\n\t" \
  "v_mfma_f32_16x16x32_bf16 v[116:119], a[180:183], a[12:15], v[116:119]\n\t" \
  "buffer_load_dwordx4 v227, s[60:63], s38 offen offset:3072 lds\n\t" \
  "v_mfma_f32_16x16x32_bf16 v[24:27], a[184:187], a[0:3], v[24:27]\n\t" \
  "s_add_u32 m0, s76, 49152\n\t" \
  "ds_read_b64_tr_b16 a[90:91], v198 offset:9216\n\t" \
  "v_mov_b32 v145, v211\n\t" \
  "v_mfma_f32_16x16x32_bf16 v[24:27], a[120:123], a[16:19], v[24:27]\n\t" \
  "ds_read_b64_tr_b16 a[92:93], v199 offset:8192\n\t" \
  "v_mov_b32 v147, v211\n\t" \
  "v_mfma_f32_16x16x32_bf16 v[56:59], a[120:123], a[20:23], v[56:59]\n\t" \
  "s_mov_b32 s98, 0x3f803f80\n\t" \
  "v_mov_b32 v212, v217\n\t" \
  "v_add_u32 v210, 65536, v228\n\t" \
  "v_mfma_f32_16x16x32_bf16 v[56:59], a[184:187], a[4:7], v[56:59]\n\t" \
  "ds_read_b64_tr_b16 a[94:95], v199 offset:9216\n\t" \
  "v_add_u32 v211, 65536, v229\n\t" \
  "v_mfma_f32_16x16x32_bf16 v[88:91], a[184:187], a[8:11], v[88:91]\n\t" \
  "buffer_load_dwordx4 v230, s[64:67], s38 offen offset:0 lds\n\t" \
  "v_mfma_f32_16x16x32_bf16 v[88:91], a[120:123], a[24:27], v[88:91]\n\t" \
  "v_mov_b32 v243, s36\n\t" \
  "v_and_b32 v208, 1, v233\n\t" \
  "v_mfma_f32_16x16x32_bf16 v[120:123], a[120:123], a[28:31], v[120:123]\n\t" \
  "v_mul_u32_u24 v208, 0xaaaa, v208\n\t" \
  "v_or_b32 v208, 0x4444, v208\n\t" \
  "v_mfma_f32_16x16x32_bf16 v[120:123], a[184:187], a[12:15], v[120:123]\n\t" \
  "v_lshl_or_b32 v208, v208, 16, v208\n\t" \
  "v_mov_b32 v216, s98\n\t" \
  "v_mfma_f32_16x16x32_bf16 v[28:31], a[188:191], a[0:3], v[28:31]\n\t" \
  "v_mov_b32 v217, s98\n\t" \
  "v_mov_b32 v218, s98\n\t" \
  "v_mfma_f32_16x16x32_bf16 v[28:31], a[124:127], a[16:19], v[28:31]\n\t" \
  "buffer_load_dwordx4 v231, s[64:67], s38 offen offset:1024 lds\n\t" \
  "v_mfma_f32_16x16x32_bf16 v[60:63], a[124:127], a[20:23], v[60:63]\n\t" \
  "v_mov_b32 v219, s98\n\t" \
  "v_mov_b32 v160, 0\n\t" \
  "v_mfma_f32_16x16x32_bf16 v[60:63], a[188:191], a[4:7], v[60:63]\n\t" \
  "v_mov_b32 v161, 0\n\t" \
  "v_mov_b32 v164, 0\n\t" \
  "v_mfma_f32_16x16x32_bf16 v[92:95], a[188:191], a[8:11], v[92:95]\n\t" \
  "v_mov_b32 v165, 0\n\t" \
  "v_mov_b32 v168, 0\n\t" \
  "v_mfma_f32_16x16x32_bf16 v[92:95], a[124:127], a[24:27], v[92:95]\n\t" \
  "v_mov_b32 v169, 0\n\t" \
  "v_mov_b32 v172, 0\n\t" \
  "v_mfma_f32_16x16x32_bf16 v[124:127], a[124:127], a[28:31], v[124:127]\n\t" \
  "s_waitcnt vmcnt(22) lgkmcnt(0)\n\t" \
  "v_mov_b32 v173, 0\n\t" \
  "v_add_u32 v248, 14336, v240\n\t" \
  "v_mfma_f32_16x16x32_bf16 v[124:127], a[188:191], a[12:15], v[124:127]\n\t" \
  "s_barrier\n\t" \
  "v_mfma_f32_16x16x32_bf16 v[0:3], a[128:131], a[32:35], v[0:3]\n\t" \
  "buffer_load_dwordx4 v230, s[64:67], s38 offen offset:2048 lds\n\t" \
  "v_mfma_f32_16x16x32_bf16 v[0:3], a[64:67], a[48:51], v[0:3]\n\t" \
  "ds_read_b64_tr_b16 a[160:161], v192 offset:16384\n\t" \
  "v_add_u32 v249, 21504, v241\n\t" \
  "v_mfma_f32_16x16x32_bf16 v[32:35], a[64:67], a[52:55], v[32:35]\n\t" \
  "ds_read_b64_tr_b16 a[162:163], v192 offset:17408\n\t" \
  "v_add_u32 v241, 7168, v241\n\t" \
  "v_mfma_f32_16x16x32_bf16 v[32:35], a[128:131], a[36:39], v[32:35]\n\t" \
  "v_lshlrev_b32_e64 v234, 6, s3\n\t" \
  "v_xor_b32 v234, v234, v244\n\t" \
  "v_mfma_f32_16x16x32_bf16 v[64:67], a[128:131], a[40:43], v[64:67]\n\t" \
  "ds_read_b64_tr_b16 a[164:165], v193 offset:16384\n\t" \
  "v_xor_b32 v235, 32, v234\n\t" \
  "v_mfma_f32_16x16x32_bf16 v[64:67], a[64:67], a[56:59], v[64:67]\n\t" \
  "buffer_load_dwordx4 v231, s[64:67], s38 offen offset:3072 lds\n\t" \
  "v_mfma_f32_16x16x32_bf16 v[96:99], a[64:67], a[60:63], v[96:99]\n\t" \
  "s_add_u32 m0, s76, 32768\n\t" \
  "ds_read_b64_tr_b16 a[166:167], v193 offset:17408\n\t" \
  "v_add_u32 v236, 65536, v234\n\t" \
  "v_mfma_f32_16x16x32_bf16 v[96:99], a[128:131], a[44:47], v[96:99]\n\t" \
  "s_add_u32 s96, s38, 16384\n\t" \
  "ds_read_b64_tr_b16 a[168:169], v194 offset:16384\n\t" \
  "v_add_u32 v237, 65536, v235\n\t" \
  "v_mfma_f32_16x16x32_bf16 v[4:7], a[132:135], a[32:35], v[4:7]\n\t" \
  "v_add_u32 v238, 131072, v234\n\t" \
  "v_add_u32 v239, 131072, v235\n\t" \
  "v_mfma_f32_16x16x32_bf16 v[4:7], a[68:71], a[48:51], v[4:7]\n\t" \
  "buffer_load_dwordx4 v224, s[60:63], s96 offen offset:0 lds\n\t" \
  "v_mfma_f32_16x16x32_bf16 v[36:39], a[68:71], a[52:55], v[36:39]\n\t" \
  "ds_read_b64_tr_b16 a[170:171], v194 offset:17408\n\t" \
  "v_lshlrev_b32 v242, 4, v233\n\t" \
  "v_mfma_f32_16x16x32_bf16 v[36:39], a[132:135], a[36:39], v[36:39]\n\t" \
  "ds_read_b64_tr_b16 a[172:173], v195 offset:16384\n\t" \
  "v_add_u32 v213, s38, v242\n\t" \
  "v_mfma_f32_16x16x32_bf16 v[68:71], a[132:135], a[40:43], v[68:71]\n\t" \
  "ds_read_b64_tr_b16 a[174:175], v195 offset:17408\n\t" \
  "v_add_u32 v215, 98304, v213\n\t" \
  "v_mfma_f32_16x16x32_bf16 v[68:71], a[68:71], a[56:59], v[68:71]\n\t" \
  "buffer_load_dwordx4 v225, s[60:63], s96 offen offset:1024 lds\n\t" \
  "v_mfma_f32_16x16x32_bf16 v[100:103], a[68:71], a[60:63], v[100:103]\n\t" \
  "ds_read_b64_tr_b16 a[176:177], v196 offset:16384\n\t" \
  "v_lshlrev_b32 v254, 9, v233\n\t" \
  "v_mfma_f32_16x16x32_bf16 v[100:103], a[132:135], a[44:47], v[100:103]\n\t" \
  "ds_read_b64_tr_b16 a[178:179], v196 offset:17408\n\t" \
  "v_and_b32 v244, 0x38, v233\n\t" \
  "v_mfma_f32_16x16x32_bf16 v[8:11], a[136:139], a[32:35], v[8:11]\n\t" \
  "v_lshl_or_b32 v254, v244, 26, v254\n\t" \
  "v_bfrev_b32 v244, 1\n\t" \
  "v_mfma_f32_16x16x32_bf16 v[8:11], a[72:75], a[48:51], v[8:11]\n\t" \
  "ds_read_b64_tr_b16 a[180:181], v197 offset:16384\n\t" \
  "v_cmp_eq_u32 vcc, s39, v233\n\t" \
  "v_mfma_f32_16x16x32_bf16 v[40:43], a[72:75], a[52:55], v[40:43]\n\t" \
  "buffer_load_dwordx4 v226, s[60:63], s96 offen offset:2048 lds\n\t" \
  "v_mfma_f32_16x16x32_bf16 v[40:43], a[136:139], a[36:39], v[40:43]\n\t" \
  "ds_read_b64_tr_b16 a[182:183], v197 offset:17408\n\t" \
  "v_add_u32 v253, s39, v233\n\t" \
  "v_mfma_f32_16x16x32_bf16 v[72:75], a[136:139], a[40:43], v[72:75]\n\t" \
  "ds_read_b64_tr_b16 a[184:185], v198 offset:16384\n\t" \
  "v_bfi_b32 v253, 7, v253, v233\n\t" \
  "v_mfma_f32_16x16x32_bf16 v[72:75], a[72:75], a[56:59], v[72:75]\n\t" \
  "v_cndmask_b32 v254, v254, v244, vcc\n\t" \
  "v_and_b32 v253, 31, v253\n\t" \
  "v_mfma_f32_16x16x32_bf16 v[104:107], a[72:75], a[60:63], v[104:107]\n\t" \
  "buffer_load_dwordx4 v227, s[60:63], s96 offen offset:3072 lds\n\t" \
  "v_mfma_f32_16x16x32_bf16 v[104:107], a[136:139], a[44:47], v[104:107]\n\t" \
  "s_add_u32 s99, s53, 14336\n\t" \
  "ds_read_b64_tr_b16 a[186:187], v198 offset:17408\n\t" \
  "v_lshlrev_b32 v253, 2, v253\n\t" \
  "v_mfma_f32_16x16x32_bf16 v[12:15], a[140:143], a[32:35], v[12:15]\n\t" \
  "ds_read_b64_tr_b16 a[188:189], v199 offset:16384\n\t" \
  "v_mov_b32 v250, v244\n\t" \
  "v_mfma_f32_16x16x32_bf16 v[12:15], a[76:79], a[48:51], v[12:15]\n\t" \
  "ds_read_b64_tr_b16 a[190:191], v199 offset:17408\n\t" \
  "v_writelane_b32 v244, 0, 0\n\t" \
  "v_mfma_f32_16x16x32_bf16 v[44:47], a[76:79], a[52:55], v[44:47]\n\t" \
  "buffer_load_dword v246, v245, s[68:71], s99 offen\n\t" \
  "v_mfma_f32_16x16x32_bf16 v[44:47], a[140:143], a[36:39], v[44:47]\n\t" \
  "ds_read_b64_tr_b16 a[96:97], v192 offset:24576\n\t" \
  "v_writelane_b32 v250, s44, 0\n\t" \
  "v_mfma_f32_16x16x32_bf16 v[76:79], a[140:143], a[40:43], v[76:79]\n\t" \
  "ds_read_b64_tr_b16 a[98:99], v192 offset:25600\n\t" \
  "v_lshrrev_b32 v251, 1, v233\n\t" \
  "v_mfma_f32_16x16x32_bf16 v[76:79], a[76:79], a[56:59], v[76:79]\n\t" \
  "s_add_u32 s99, s53, 28672\n\t" \
  "ds_read_b64 v[142:143], v141 offset:1024\n\t" \
  "v_lshlrev_b32 v251, 2, v251\n\t" \
  "v_mfma_f32_16x16x32_bf16 v[108:111], a[76:79], a[60:63], v[108:111]\n\t" \
  "ds_read_b64_tr_b16 a[100:101], v193 offset:24576\n\t" \
  "v_add_u32 v251, 8192, v251\n\t" \
  "v_mfma_f32_16x16x32_bf16 v[108:111], a[140:143], a[44:47], v[108:111]\n\t" \
  "ds_read_b64_tr_b16 a[102:103], v193 offset:25600\n\t" \
  "v_lshlrev_b32 v209, 10, v233\n\t" \
  "v_mfma_f32_16x16x32_bf16 v[16:19], a[144:147], a[32:35], v[16:19]\n\t" \
  "buffer_load_dword v246, v245, s[68:71], s99 offen\n\t" \
  "v_mfma_f32_16x16x32_bf16 v[16:19], a[80:83], a[48:51], v[16:19]\n\t" \
  "ds_read_b64_tr_b16 a[104:105], v194 offset:24576\n\t" \
  "v_and_b32 v214, 0x38, v233\n\t" \
  "v_mfma_f32_16x16x32_bf16 v[48:51], a[80:83], a[52:55], v[48:51]\n\t" \
  "v_lshl_or_b32 v207, v214, 26, v209\n\t" \
  "v_lshrrev_b32 v252, 5, v233\n\t" \
  "v_mfma_f32_16x16x32_bf16 v[48:51], a[144:147], a[36:39], v[48:51]\n\t" \
  "ds_read_b64_tr_b16 a[106:107], v194 offset:25600\n\t" \
  "v_mul_u32_u24 v252, 1536, v252\n\t" \
  "v_mfma_f32_16x16x32_bf16 v[80:83], a[144:147], a[40:43], v[80:83]\n\t" \
  "ds_read_b64_tr_b16 a[108:109], v195 offset:24576\n\t" \
  "v_lshl_add_u32 v252, s3, 9, v252\n\t" \
  "v_mfma_f32_16x16x32_bf16 v[80:83], a[80:83], a[56:59], v[80:83]\n\t" \
  "s_waitcnt lgkmcnt(5)\n\t" \
  "v_add_f32_dpp v142, v142, v142 row_ror:4 row_mask:0xf bank_mask:0xf\n\t" \
  "v_add_f32_dpp v143, v143, v143 row_ror:4 row_mask:0xf bank_mask:0xf\n\t" \
  "v_mfma_f32_16x16x32_bf16 v[112:115], a[80:83], a[60:63], v[112:115]\n\t" \
  "ds_read_b64_tr_b16 a[110:111], v195 offset:25600\n\t" \
  "v_add_f32_dpp v144, v142, v142 row_ror:8 row_mask:0xf bank_mask:0xf\n\t" \
  "v_mfma_f32_16x16x32_bf16 v[112:115], a[144:147], a[44:47], v[112:115]\n\t" \
  "ds_read_b64_tr_b16 a[112:113], v196 offset:24576\n\t" \
  "v_add_f32_dpp v146, v143, v143 row_ror:8 row_mask:0xf bank_mask:0xf\n\t" \
  "v_mfma_f32_16x16x32_bf16 v[20:23], a[148:151], a[32:35], v[20:23]\n\t" \
  "buffer_store_dwordx4 v[144:147], v140, s[16:19], s90 offen sc1\n\t" \
  "ds_read_b64_tr_b16 a[114:115], v196 offset:25600\n\t" \
  "ds_read_b64_tr_b16 a[116:117], v197 offset:24576\n\t" \
  "v_add_u32 v252, v252, v242\n\t" \
  "v_mfma_f32_16x16x32_bf16 v[20:23], a[84:87], a[48:51], v[20:23]\n\t" \
  "ds_read_b64_tr_b16 a[118:119], v197 offset:25600\n\t" \
  "v_mfma_f32_16x16x32_bf16 v[52:55], a[84:87], a[52:55], v[52:55]\n\t" \
  "ds_read_b64_tr_b16 a[120:121], v198 offset:24576\n\t" \
  "v_mfma_f32_16x16x32_bf16 v[52:55], a[148:151], a[36:39], v[52:55]\n\t" \
  "ds_read_b64_tr_b16 a[122:123], v198 offset:25600\n\t" \
  "v_mfma_f32_16x16x32_bf16 v[84:87], a[148:151], a[40:43], v[84:87]\n\t" \
  "ds_read_b64_tr_b16 a[124:125], v199 offset:24576\n\t" \
  "v_mfma_f32_16x16x32_bf16 v[84:87], a[84:87], a[56:59], v[84:87]\n\t" \
  "ds_read_b64_tr_b16 a[126:127], v199 offset:25600\n\t" \
  "v_mfma_f32_16x16x32_bf16 v[116:119], a[84:87], a[60:63], v[116:119]\n\t" \
  "v_mfma_f32_16x16x32_bf16 v[116:119], a[148:151], a[44:47], v[116:119]\n\t" \
  ".Lhs5:\n\ts_cbranch_execz .Lhs5n\n\t" \
  "v_mfma_f32_16x16x32_bf16 v[24:27], a[152:155], a[32:35], v[24:27]\n\t" \
  "v_mfma_f32_16x16x32_bf16 v[24:27], a[88:91], a[48:51], v[24:27]\n\t" \
  "v_mfma_f32_16x16x32_bf16 v[56:59], a[88:91], a[52:55], v[56:59]\n\t" \
  "v_mfma_f32_16x16x32_bf16 v[56:59], a[152:155], a[36:39], v[56:59]\n\t" \
  "v_mfma_f32_16x16x32_bf16 v[88:91], a[152:155], a[40:43], v[88:91]\n\t" \
  "v_mfma_f32_16x16x32_bf16 v[88:91], a[88:91], a[56:59], v[88:91]\n\t" \
  "v_mfma_f32_16x16x32_bf16 v[120:123], a[88:91], a[60:63], v[120:123]\n\t" \
  "v_mfma_f32_16x16x32_bf16 v[120:123], a[152:155], a[44:47], v[120:123]\n\t" \
  "v_mfma_f32_16x16x32_bf16 v[28:31], a[156:159], a[32:35], v[28:31]\n\t" \
  "v_mfma_f32_16x16x32_bf16 v[28:31], a[92:95], a[48:51], v[28:31]\n\t" \
  "v_mfma_f32_16x16x32_bf16 v[60:63], a[92:95], a[52:55], v[60:63]\n\t" \
  "v_mfma_f32_16x16x32_bf16 v[60:63], a[156:159], a[36:39], v[60:63]\n\t" \
  "v_mfma_f32_16x16x32_bf16 v[92:95], a[156:159], a[40:43], v[92:95]\n\t" \
  "v_mfma_f32_16x16x32_bf16 v[92:95], a[92:95], a[56:59], v[92:95]\n\t" \
  "v_mfma_f32_16x16x32_bf16 v[124:127], a[92:95], a[60:63], v[124:127]\n\t" \
  "v_mfma_f32_16x16x32_bf16 v[124:127], a[156:159], a[44:47], v[124:127]\n\t" \
  "s_waitcnt vmcnt(15) lgkmcnt(0)\n\t" \
  FSTP("s_waitcnt lgkmcnt(0)\n\t") \
  FSTP("v_writelane_b32 v255, s94, 52\n\t") \
  FSTP("v_writelane_b32 v255, s92, 53\n\t") \
  FSTA("s_memrealtime s[94:95]\n\t") \
  FSTA("s_memtime s[92:93]\n\t") \
  FSTA("s_waitcnt lgkmcnt(0)\n\t") \
  FSTA("v_writelane_b32 v255, s94, 16\n\t") \
  FSTA("v_writelane_b32 v255, s92, 17\n\t") \
  ".subsection 1\n\t" \
  ".Lfr_s:\n\t" \
  "s_mov_b32 s54, 0\n\t" \
  ".Lfr_r:\n\t" \
  "s_sleep 1\n\t" \
  "buffer_load_dwordx4 v[128:131], v248, s[16:19], s53 offen sc1\n\t" \
  "buffer_load_dwordx4 v[132:135], v248, s[16:19], s58 offen sc1\n\t" \
  "buffer_load_dwordx4 v[136:139], v248, s[16:19], s90 offen sc1\n\t" \
  "buffer_load_dwordx4 v[140:143], v248, s[16:19], s91 offen sc1\n\t" \
  "buffer_load_dwordx4 v[160:163], v249, s[16:19], s96 offen sc1\n\t" \
  "buffer_load_dwordx4 v[164:167], v249, s[16:19], s97 offen sc1\n\t" \
  "buffer_load_dwordx4 v[168:171], v249, s[16:19], s99 offen sc1\n\t" \
  "buffer_load_dwordx4 v[172:175], v249, s[16:19], s57 offen sc1\n\t" \
  "s_waitcnt vmcnt(0)\n\t" \
  "v_xor_b32 v144, v211, v129\n\t" \
  "v_bitop3_b32 v144, v144, v131, v211 bitop3:0xf6\n\t" \
  "v_bitop3_b32 v144, v144, v133, v211 bitop3:0xf6\n\t" \
  "v_bitop3_b32 v144, v144, v135, v211 bitop3:0xf6\n\t" \
  "v_bitop3_b32 v144, v144, v137, v211 bitop3:0xf6\n\t" \
  "v_bitop3_b32 v144, v144, v139, v211 bitop3:0xf6\n\t" \
  "v_bitop3_b32 v144, v144, v141, v211 bitop3:0xf6\n\t" \
  "v_bitop3_b32 v144, v144, v143, v211 bitop3:0xf6\n\t" \
  "v_bitop3_b32 v144, v144, v161, v211 bitop3:0xf6\n\t" \
  "v_bitop3_b32 v144, v144, v163, v211 bitop3:0xf6\n\t" \
  "v_bitop3_b32 v144, v144, v165, v211 bitop3:0xf6\n\t" \
  "v_bitop3_b32 v144, v144, v167, v211 bitop3:0xf6\n\t" \
  "v_bitop3_b32 v144, v144, v169, v211 bitop3:0xf6\n\t" \
  "v_bitop3_b32 v144, v144, v171, v211 bitop3:0xf6\n\t" \
  "v_bitop3_b32 v144, v144, v173, v211 bitop3:0xf6\n\t" \
  "v_bitop3_b32 v144, v144, v175, v211 bitop3:0xf6\n\t" \
  "v_cmp_ne_u32 vcc, 0, v144\n\t" \
  "v_add_f32 v146, v128, v132\n\t" \
  "v_add_f32 v148, v136, v140\n\t" \
  "v_add_f32 v147, v130, v134\n\t" \
  "v_add_f32 v149, v138, v142\n\t" \
  "v_add_f32 v146, v146, v148\n\t" \
  "v_add_f32 v147, v147, v149\n\t" \
  "s_cbranch_vccz .Lfr_k\n\t" \
  "s_add_u32 s54, s54, 1\n\t" \
  "s_cmp_lt_u32 s54, " FQ_CAP "\n\t" \
  "s_cbranch_scc1 .Lfr_r\n\t" \
  "buffer_store_dword v211, off, s[16:19], 0 offset:256 sc1\n\t" \
  "s_branch .Lfr_k\n\t" \
  ".Lfq_s:\n\t" \
  "s_mov_b32 s97, 0\n\t" \
  ".Lfq_r:\n\t" \
  "buffer_load_dword v252, v210, s[16:19], s84 offen sc1\n\t" \
  "s_waitcnt vmcnt(0)\n\t" \
  "v_cmp_ne_u32 vcc, v211, v252\n\t" \
  "s_cbranch_vccz .Lfq_k\n\t" \
  "s_add_u32 s97, s97, 1\n\t" \
  "s_cmp_lt_u32 s97, " FQ_CAP "\n\t" \
  "s_cbranch_scc1 .Lfq_r\n\t" \
  "buffer_store_dword v211, off, s[16:19], 0 offset:256 sc1\n\t" \
  "s_branch .Lfq_k\n\t" \
  ".subsection 0\n\t"
static_assert(W_VOTE == 8192 && W_FLAGR == 32768 && W_FLAGO == 131072 && W_MODB == 2228224 && W_OT == 8388608 && W_XCH == 25165824 && W_EPOCH == 2490368 && W_FLAGH == 2494464 && W_ST1 == 2097152 && W_MODA == 2162688 && W_HT == 4194304 && W_PROBE == 16384);
#define STS(x)
#define AO_STV
#define STA(x)
#define STP(x)
#define AT_ASM \
  "s_cmp_eq_u32 s37, 0\n\t" \
  "s_cbranch_scc1 .Lsf_go\n\t" \
  "s_barrier\n\t" \
  "v_mfma_f32_16x16x32_bf16 v[28:31], a[188:191], a[192:195], v[28:31]\n\t" \
  "s_add_u32 s77, s51, 131072\n\t" \
  "ds_read_b64_tr_b16 a[156:157], v199 offset:32768\n\t" \
  "s_mov_b32 s89, 0\n\t" \
  "v_mfma_f32_16x16x32_bf16 v[28:31], a[124:127], a[208:211], v[28:31]\n\t" \
  "s_brev_b32 s78, 1\n\t" \
  "ds_read_b64_tr_b16 a[158:159], v199 offset:33792\n\t" \
  "s_mov_b32 s79, 0x0f800000\n\t" \
  "v_mfma_f32_16x16x32_bf16 v[24:27], a[184:187], a[192:195], v[24:27]\n\t" \
  "ds_read_b64_tr_b16 a[92:93], v199 offset:40960\n\t" \
  "v_mfma_f32_16x16x32_bf16 v[24:27], a[120:123], a[208:211], v[24:27]\n\t" \
  "ds_read_b64_tr_b16 a[94:95], v199 offset:41984\n\t" \
  "v_mfma_f32_16x16x32_bf16 v[20:23], a[180:183], a[192:195], v[20:23]\n\t" \
  "ds_read_b64_tr_b16 a[152:153], v198 offset:32768\n\t" \
  "v_mfma_f32_16x16x32_bf16 v[20:23], a[116:119], a[208:211], v[20:23]\n\t" \
  "ds_read_b64_tr_b16 a[154:155], v198 offset:33792\n\t" \
  "v_mfma_f32_16x16x32_bf16 v[16:19], a[176:179], a[192:195], v[16:19]\n\t" \
  "ds_read_b64_tr_b16 a[88:89], v198 offset:40960\n\t" \
  "v_mfma_f32_16x16x32_bf16 v[16:19], a[112:115], a[208:211], v[16:19]\n\t" \
  "ds_read_b64_tr_b16 a[90:91], v198 offset:41984\n\t" \
  "v_mfma_f32_16x16x32_bf16 v[12:15], a[172:175], a[192:195], v[12:15]\n\t" \
  "ds_read_b64_tr_b16 a[148:149], v197 offset:32768\n\t" \
  "v_mfma_f32_16x16x32_bf16 v[12:15], a[108:111], a[208:211], v[12:15]\n\t" \
  "ds_read_b64_tr_b16 a[150:151], v197 offset:33792\n\t" \
  "v_mfma_f32_16x16x32_bf16 v[8:11], a[168:171], a[192:195], v[8:11]\n\t" \
  "ds_read_b64_tr_b16 a[84:85], v197 offset:40960\n\t" \
  "s_mov_b32 s54, 0\n\t" \
  "v_mfma_f32_16x16x32_bf16 v[8:11], a[104:107], a[208:211], v[8:11]\n\t" \
  "ds_read_b64_tr_b16 a[86:87], v197 offset:41984\n\t" \
  "v_mfma_f32_16x16x32_bf16 v[4:7], a[164:167], a[192:195], v[4:7]\n\t" \
  "ds_read_b64_tr_b16 a[144:145], v196 offset:32768\n\t" \
  "v_mfma_f32_16x16x32_bf16 v[4:7], a[100:103], a[208:211], v[4:7]\n\t" \
  "ds_read_b64_tr_b16 a[146:147], v196 offset:33792\n\t" \
  "v_mfma_f32_16x16x32_bf16 v[0:3], a[160:163], a[192:195], v[0:3]\n\t" \
  "ds_read_b64_tr_b16 a[80:81], v196 offset:40960\n\t" \
  "v_mfma_f32_16x16x32_bf16 v[0:3], a[96:99], a[208:211], v[0:3]\n\t" \
  "ds_read_b64_tr_b16 a[82:83], v196 offset:41984\n\t" \
  "v_mfma_f32_16x16x32_bf16 v[60:63], a[188:191], a[196:199], v[60:63]\n\t" \
  "ds_read_b64_tr_b16 a[140:141], v195 offset:32768\n\t" \
  "v_mfma_f32_16x16x32_bf16 v[60:63], a[124:127], a[212:215], v[60:63]\n\t" \
  "ds_read_b64_tr_b16 a[142:143], v195 offset:33792\n\t" \
  "v_mfma_f32_16x16x32_bf16 v[56:59], a[184:187], a[196:199], v[56:59]\n\t" \
  "ds_read_b64_tr_b16 a[76:77], v195 offset:40960\n\t" \
  "v_mfma_f32_16x16x32_bf16 v[56:59], a[120:123], a[212:215], v[56:59]\n\t" \
  "ds_read_b64_tr_b16 a[78:79], v195 offset:41984\n\t" \
  "v_mfma_f32_16x16x32_bf16 v[52:55], a[180:183], a[196:199], v[52:55]\n\t" \
  "ds_read_b64_tr_b16 a[136:137], v194 offset:32768\n\t" \
  "v_mfma_f32_16x16x32_bf16 v[52:55], a[116:119], a[212:215], v[52:55]\n\t" \
  "ds_read_b64_tr_b16 a[138:139], v194 offset:33792\n\t" \
  "v_mfma_f32_16x16x32_bf16 v[48:51], a[176:179], a[196:199], v[48:51]\n\t" \
  "ds_read_b64_tr_b16 a[72:73], v194 offset:40960\n\t" \
  "v_mfma_f32_16x16x32_bf16 v[48:51], a[112:115], a[212:215], v[48:51]\n\t" \
  "ds_read_b64_tr_b16 a[74:75], v194 offset:41984\n\t" \
  "v_mfma_f32_16x16x32_bf16 v[44:47], a[172:175], a[196:199], v[44:47]\n\t" \
  "ds_read_b64_tr_b16 a[132:133], v193 offset:32768\n\t" \
  "v_mfma_f32_16x16x32_bf16 v[44:47], a[108:111], a[212:215], v[44:47]\n\t" \
  "ds_read_b64_tr_b16 a[134:135], v193 offset:33792\n\t" \
  "v_mfma_f32_16x16x32_bf16 v[40:43], a[168:171], a[196:199], v[40:43]\n\t" \
  "ds_read_b64_tr_b16 a[68:69], v193 offset:40960\n\t" \
  "v_mfma_f32_16x16x32_bf16 v[40:43], a[104:107], a[212:215], v[40:43]\n\t" \
  "ds_read_b64_tr_b16 a[70:71], v193 offset:41984\n\t" \
  "v_mfma_f32_16x16x32_bf16 v[36:39], a[164:167], a[196:199], v[36:39]\n\t" \
  "ds_read_b64_tr_b16 a[128:129], v192 offset:32768\n\t" \
  "v_mfma_f32_16x16x32_bf16 v[36:39], a[100:103], a[212:215], v[36:39]\n\t" \
  "ds_read_b64_tr_b16 a[130:131], v192 offset:33792\n\t" \
  "v_mfma_f32_16x16x32_bf16 v[32:35], a[160:163], a[196:199], v[32:35]\n\t" \
  "ds_read_b64_tr_b16 a[64:65], v192 offset:40960\n\t" \
  "v_mfma_f32_16x16x32_bf16 v[32:35], a[96:99], a[212:215], v[32:35]\n\t" \
  "s_waitcnt lgkmcnt(15)\n\t" \
  "ds_read_b64_tr_b16 a[66:67], v192 offset:41984\n\t" \
  "v_mfma_f32_16x16x32_bf16 v[28:31], a[156:159], a[224:227], v[28:31]\n\t" \
  "v_mfma_f32_16x16x32_bf16 v[28:31], a[92:95], a[240:243], v[28:31]\n\t" \
  "v_mfma_f32_16x16x32_bf16 v[24:27], a[152:155], a[224:227], v[24:27]\n\t" \
  "s_waitcnt vmcnt(3)\n\t" \
  "s_nop 1\n\t" \
  "v_mfma_f32_16x16x32_bf16 v[24:27], a[88:91], a[240:243], v[24:27]\n\t" \
  "s_barrier\n\t" \
  "v_mfma_f32_16x16x32_bf16 v[20:23], a[148:151], a[224:227], v[20:23]\n\t" \
  "ds_read_b128 a[0:3], v220 offset:0\n\t" \
  "v_cvt_pk_bf16_f32 v31, v30, v31\n\t" \
  "v_mfma_f32_16x16x32_bf16 v[20:23], a[84:87], a[240:243], v[20:23]\n\t" \
  "ds_read_b128 a[4:7], v221 offset:0\n\t" \
  "v_cvt_pk_bf16_f32 v30, v28, v29\n\t" \
  "v_mfma_f32_16x16x32_bf16 v[16:19], a[144:147], a[224:227], v[16:19]\n\t" \
  "v_cvt_pk_bf16_f32 v28, v24, v25\n\t" \
  "v_cvt_pk_bf16_f32 v29, v26, v27\n\t" \
  "v_mfma_f32_16x16x32_bf16 v[16:19], a[80:83], a[240:243], v[16:19]\n\t" \
  "s_waitcnt lgkmcnt(10)\n\t" \
  "v_permlane16_swap_b32 v28, v30\n\t" \
  "v_mfma_f32_16x16x32_bf16 v[12:15], a[140:143], a[224:227], v[12:15]\n\t" \
  "ds_read_b128 a[8:11], v222 offset:0\n\t" \
  "v_cvt_pk_bf16_f32 v27, v22, v23\n\t" \
  "v_mfma_f32_16x16x32_bf16 v[12:15], a[76:79], a[240:243], v[12:15]\n\t" \
  "v_permlane16_swap_b32 v29, v31\n\t" \
  "v_mfma_f32_16x16x32_bf16 v[8:11], a[136:139], a[224:227], v[8:11]\n\t" \
  "ds_read_b128 a[12:15], v223 offset:0\n\t" \
  "v_cvt_pk_bf16_f32 v26, v20, v21\n\t" \
  "v_mfma_f32_16x16x32_bf16 v[8:11], a[72:75], a[240:243], v[8:11]\n\t" \
  "s_waitcnt lgkmcnt(4)\n\t" \
  "v_cvt_pk_bf16_f32 v24, v16, v17\n\t" \
  "v_cvt_pk_bf16_f32 v25, v18, v19\n\t" \
  "v_mfma_f32_16x16x32_bf16 v[4:7], a[132:135], a[224:227], v[4:7]\n\t" \
  "ds_read_b128 a[16:19], v220 offset:4096\n\t" \
  "v_cvt_pk_bf16_f32 v23, v14, v15\n\t" \
  "v_mfma_f32_16x16x32_bf16 v[4:7], a[68:71], a[240:243], v[4:7]\n\t" \
  "v_cvt_pk_bf16_f32 v22, v12, v13\n\t" \
  "v_cvt_pk_bf16_f32 v20, v8, v9\n\t" \
  "v_mfma_f32_16x16x32_bf16 v[0:3], a[128:131], a[224:227], v[0:3]\n\t" \
  "ds_read_b128 a[20:23], v221 offset:4096\n\t" \
  "v_cvt_pk_bf16_f32 v21, v10, v11\n\t" \
  "v_mfma_f32_16x16x32_bf16 v[0:3], a[64:67], a[240:243], v[0:3]\n\t" \
  "v_permlane16_swap_b32 v24, v26\n\t" \
  "v_mfma_f32_16x16x32_bf16 v[60:63], a[156:159], a[228:231], v[60:63]\n\t" \
  "ds_read_b128 a[24:27], v222 offset:4096\n\t" \
  "v_cvt_pk_bf16_f32 v19, v6, v7\n\t" \
  "v_mfma_f32_16x16x32_bf16 v[60:63], a[92:95], a[244:247], v[60:63]\n\t" \
  "v_cvt_pk_bf16_f32 v18, v4, v5\n\t" \
  "v_permlane16_swap_b32 v25, v27\n\t" \
  "v_mfma_f32_16x16x32_bf16 v[56:59], a[152:155], a[228:231], v[56:59]\n\t" \
  "ds_read_b128 a[28:31], v223 offset:4096\n\t" \
  "v_cvt_pk_bf16_f32 v16, v0, v1\n\t" \
  "v_mfma_f32_16x16x32_bf16 v[56:59], a[88:91], a[244:247], v[56:59]\n\t" \
  "ds_read_b128 a[32:35], v220 offset:8192\n\t" \
  "v_permlane16_swap_b32 v20, v22\n\t" \
  "v_mfma_f32_16x16x32_bf16 v[52:55], a[148:151], a[228:231], v[52:55]\n\t" \
  "v_permlane16_swap_b32 v21, v23\n\t" \
  "v_mfma_f32_16x16x32_bf16 v[52:55], a[84:87], a[244:247], v[52:55]\n\t" \
  "ds_read_b128 a[36:39], v221 offset:8192\n\t" \
  "v_cvt_pk_bf16_f32 v17, v2, v3\n\t" \
  "v_mfma_f32_16x16x32_bf16 v[48:51], a[144:147], a[228:231], v[48:51]\n\t" \
  "v_permlane16_swap_b32 v16, v18\n\t" \
  "v_mfma_f32_16x16x32_bf16 v[48:51], a[80:83], a[244:247], v[48:51]\n\t" \
  "ds_read_b128 a[40:43], v222 offset:8192\n\t" \
  "v_cvt_pk_bf16_f32 v63, v62, v63\n\t" \
  "v_mfma_f32_16x16x32_bf16 v[44:47], a[140:143], a[228:231], v[44:47]\n\t" \
  "v_permlane16_swap_b32 v17, v19\n\t" \
  "v_mfma_f32_16x16x32_bf16 v[44:47], a[76:79], a[244:247], v[44:47]\n\t" \
  "ds_read_b128 a[44:47], v223 offset:8192\n\t" \
  "v_cvt_pk_bf16_f32 v62, v60, v61\n\t" \
  "v_mfma_f32_16x16x32_bf16 v[40:43], a[136:139], a[228:231], v[40:43]\n\t" \
  "v_cvt_pk_bf16_f32 v60, v56, v57\n\t" \
  "v_cvt_pk_bf16_f32 v61, v58, v59\n\t" \
  "v_mfma_f32_16x16x32_bf16 v[40:43], a[72:75], a[244:247], v[40:43]\n\t" \
  "ds_read_b128 a[48:51], v220 offset:12288\n\t" \
  "v_cvt_pk_bf16_f32 v59, v54, v55\n\t" \
  "v_mfma_f32_16x16x32_bf16 v[36:39], a[132:135], a[228:231], v[36:39]\n\t" \
  "buffer_load_dword v246, v247, s[16:19], 0 offen sc1\n\t" \
  "v_mfma_f32_16x16x32_bf16 v[36:39], a[68:71], a[244:247], v[36:39]\n\t" \
  "ds_read_b128 a[52:55], v221 offset:12288\n\t" \
  "v_cvt_pk_bf16_f32 v58, v52, v53\n\t" \
  "v_mfma_f32_16x16x32_bf16 v[32:35], a[128:131], a[228:231], v[32:35]\n\t" \
  "ds_read_b128 a[56:59], v222 offset:12288\n\t" \
  "v_cvt_pk_bf16_f32 v56, v48, v49\n\t" \
  "v_mfma_f32_16x16x32_bf16 v[32:35], a[64:67], a[244:247], v[32:35]\n\t" \
  "s_waitcnt lgkmcnt(11)\n\t" \
  "ds_read_b128 a[60:63], v223 offset:12288\n\t" \
  "v_cvt_pk_bf16_f32 v57, v50, v51\n\t" \
  "v_mfma_f32_16x16x32_bf16 v[0:3], a[0:3], v[16:19], 0\n\t" \
  "v_cvt_pk_bf16_f32 v55, v46, v47\n\t" \
  "v_cvt_pk_bf16_f32 v54, v44, v45\n\t" \
  "v_mfma_f32_16x16x32_bf16 v[0:3], a[4:7], v[20:23], v[0:3]\n\t" \
  "v_permlane16_swap_b32 v60, v62\n\t" \
  "v_mfma_f32_16x16x32_bf16 v[0:3], a[8:11], v[24:27], v[0:3]\n\t" \
  "v_cvt_pk_bf16_f32 v52, v40, v41\n\t" \
  "v_cvt_pk_bf16_f32 v53, v42, v43\n\t" \
  "v_mfma_f32_16x16x32_bf16 v[0:3], a[12:15], v[28:31], v[0:3]\n\t" \
  "s_waitcnt lgkmcnt(8)\n\t" \
  "v_permlane16_swap_b32 v61, v63\n\t" \
  "v_mfma_f32_16x16x32_bf16 v[4:7], a[16:19], v[16:19], 0\n\t" \
  "s_add_u32 m0, s38, 49152\n\t" \
  "v_permlane16_swap_b32 v56, v58\n\t" \
  "v_mfma_f32_16x16x32_bf16 v[4:7], a[20:23], v[20:23], v[4:7]\n\t" \
  "s_add_u32 s96, s38, 32768\n\t" \
  "v_cvt_pk_bf16_f32 v51, v38, v39\n\t" \
  "v_cvt_pk_bf16_f32 v50, v36, v37\n\t" \
  "v_mfma_f32_16x16x32_bf16 v[4:7], a[24:27], v[24:27], v[4:7]\n\t" \
  "s_waitcnt lgkmcnt(0)\n\t" \
  "v_permlane16_swap_b32 v57, v59\n\t" \
  "v_mfma_f32_16x16x32_bf16 v[4:7], a[28:31], v[28:31], v[4:7]\n\t" \
  "s_barrier\n\t" \
  "v_mfma_f32_16x16x32_bf16 v[92:95], a[188:191], a[200:203], v[92:95]\n\t" \
  "buffer_load_dwordx4 v224, s[60:63], s96 offen offset:0 lds\n\t" \
  "v_mfma_f32_16x16x32_bf16 v[92:95], a[124:127], a[216:219], v[92:95]\n\t" \
  "v_cvt_pk_bf16_f32 v48, v32, v33\n\t" \
  "v_max3_f32 v200, v0, v1, v2\n\t" \
  "v_mfma_f32_16x16x32_bf16 v[88:91], a[184:187], a[200:203], v[88:91]\n\t" \
  "v_cvt_pk_bf16_f32 v49, v34, v35\n\t" \
  "v_max3_f32 v200, v200, v3, v4\n\t" \
  "v_mfma_f32_16x16x32_bf16 v[88:91], a[120:123], a[216:219], v[88:91]\n\t" \
  "v_max3_f32 v200, v200, v5, v6\n\t" \
  "v_max_f32 v200, v200, v7\n\t" \
  "v_mfma_f32_16x16x32_bf16 v[84:87], a[180:183], a[200:203], v[84:87]\n\t" \
  "v_mov_b32 v201, v200\n\t" \
  "s_nop 0\n\t" \
  "v_mfma_f32_16x16x32_bf16 v[84:87], a[116:119], a[216:219], v[84:87]\n\t" \
  "v_permlane32_swap_b32 v200, v201\n\t" \
  "v_mfma_f32_16x16x32_bf16 v[80:83], a[176:179], a[200:203], v[80:83]\n\t" \
  "v_max_f32 v200, v200, v201\n\t" \
  "v_mov_b32 v201, v200\n\t" \
  "v_mfma_f32_16x16x32_bf16 v[80:83], a[112:115], a[216:219], v[80:83]\n\t" \
  "buffer_load_dwordx4 v225, s[60:63], s96 offen offset:1024 lds\n\t" \
  "v_mfma_f32_16x16x32_bf16 v[76:79], a[172:175], a[200:203], v[76:79]\n\t" \
  "v_permlane16_swap_b32 v200, v201\n\t" \
  "v_mfma_f32_16x16x32_bf16 v[76:79], a[108:111], a[216:219], v[76:79]\n\t" \
  "v_max_f32 v200, v200, v201\n\t" \
  "v_sub_f32 v176, 0xc2c00000, v200\n\t" \
  "v_mfma_f32_16x16x32_bf16 v[72:75], a[168:171], a[200:203], v[72:75]\n\t" \
  "v_sub_f32 v177, 0xc2c00000, v200\n\t" \
  "v_sub_f32 v178, 0xc2c00000, v200\n\t" \
  "v_mfma_f32_16x16x32_bf16 v[72:75], a[104:107], a[216:219], v[72:75]\n\t" \
  "buffer_load_dwordx4 v226, s[60:63], s96 offen offset:2048 lds\n\t" \
  "v_mfma_f32_16x16x32_bf16 v[68:71], a[164:167], a[200:203], v[68:71]\n\t" \
  "v_sub_f32 v179, 0xc2c00000, v200\n\t" \
  "v_permlane16_swap_b32 v52, v54\n\t" \
  "v_mfma_f32_16x16x32_bf16 v[68:71], a[100:103], a[216:219], v[68:71]\n\t" \
  "v_add_f32 v0, v0, v176\n\t" \
  "v_permlane16_swap_b32 v53, v55\n\t" \
  "v_mfma_f32_16x16x32_bf16 v[64:67], a[160:163], a[200:203], v[64:67]\n\t" \
  "v_add_f32 v1, v1, v176\n\t" \
  "v_add_f32 v2, v2, v176\n\t" \
  "v_mfma_f32_16x16x32_bf16 v[64:67], a[96:99], a[216:219], v[64:67]\n\t" \
  "buffer_load_dwordx4 v227, s[60:63], s96 offen offset:3072 lds\n\t" \
  "v_mfma_f32_16x16x32_bf16 v[8:11], a[32:35], v[16:19], v[176:179]\n\t" \
  "s_add_u32 m0, s38, 147456\n\t" \
  "v_add_f32 v3, v3, v176\n\t" \
  "v_permlane16_swap_b32 v48, v50\n\t" \
  "v_mfma_f32_16x16x32_bf16 v[8:11], a[36:39], v[20:23], v[8:11]\n\t" \
  "s_add_u32 s96, s38, 16384\n\t" \
  "v_add_f32 v4, v4, v176\n\t" \
  "v_permlane16_swap_b32 v49, v51\n\t" \
  "v_mfma_f32_16x16x32_bf16 v[8:11], a[40:43], v[24:27], v[8:11]\n\t" \
  "v_exp_f32 v0, v0\n\t" \
  "v_mfma_f32_16x16x32_bf16 v[8:11], a[44:47], v[28:31], v[8:11]\n\t" \
  "v_exp_f32 v1, v1\n\t" \
  "v_mfma_f32_16x16x32_bf16 v[12:15], a[48:51], v[16:19], v[176:179]\n\t" \
  "buffer_load_dwordx4 v230, s[64:67], s96 offen offset:0 lds\n\t" \
  "v_mfma_f32_16x16x32_bf16 v[12:15], a[52:55], v[20:23], v[12:15]\n\t" \
  "v_add_f32 v5, v5, v176\n\t" \
  "v_add_f32 v6, v6, v176\n\t" \
  "v_mfma_f32_16x16x32_bf16 v[12:15], a[56:59], v[24:27], v[12:15]\n\t" \
  "v_exp_f32 v2, v2\n\t" \
  "v_mfma_f32_16x16x32_bf16 v[12:15], a[60:63], v[28:31], v[12:15]\n\t" \
  "v_exp_f32 v3, v3\n\t" \
  "v_mfma_f32_16x16x32_bf16 v[32:35], a[0:3], v[48:51], 0\n\t" \
  "v_add_f32 v7, v7, v176\n\t" \
  "v_cvt_pk_bf16_f32 v128, v0, v1\n\t" \
  "v_mfma_f32_16x16x32_bf16 v[32:35], a[4:7], v[52:55], v[32:35]\n\t" \
  "buffer_load_dwordx4 v231, s[64:67], s96 offen offset:1024 lds\n\t" \
  "v_mfma_f32_16x16x32_bf16 v[32:35], a[8:11], v[56:59], v[32:35]\n\t" \
  "v_exp_f32 v4, v4\n\t" \
  "v_mfma_f32_16x16x32_bf16 v[32:35], a[12:15], v[60:63], v[32:35]\n\t" \
  "v_exp_f32 v5, v5\n\t" \
  "v_mfma_f32_16x16x32_bf16 v[36:39], a[16:19], v[48:51], 0\n\t" \
  "v_exp_f32 v6, v6\n\t" \
  "v_mfma_f32_16x16x32_bf16 v[36:39], a[20:23], v[52:55], v[36:39]\n\t" \
  "v_exp_f32 v7, v7\n\t" \
  "v_mfma_f32_16x16x32_bf16 v[36:39], a[24:27], v[56:59], v[36:39]\n\t" \
  "buffer_load_dwordx4 v230, s[64:67], s96 offen offset:2048 lds\n\t" \
  "v_mfma_f32_16x16x32_bf16 v[36:39], a[28:31], v[60:63], v[36:39]\n\t" \
  "v_max3_f32 v200, v32, v33, v34\n\t" \
  "v_cvt_pk_bf16_f32 v129, v2, v3\n\t" \
  "v_mfma_f32_16x16x32_bf16 v[92:95], a[156:159], a[232:235], v[92:95]\n\t" \
  "v_exp_f32 v8, v8\n\t" \
  "v_mfma_f32_16x16x32_bf16 v[92:95], a[92:95], a[248:251], v[92:95]\n\t" \
  "v_exp_f32 v9, v9\n\t" \
  "v_mfma_f32_16x16x32_bf16 v[88:91], a[152:155], a[232:235], v[88:91]\n\t" \
  "v_exp_f32 v10, v10\n\t" \
  "v_max3_f32 v200, v200, v35, v36\n\t" \
  "v_max3_f32 v200, v200, v37, v38\n\t" \
  "v_mfma_f32_16x16x32_bf16 v[88:91], a[88:91], a[248:251], v[88:91]\n\t" \
  "v_max_f32 v200, v200, v39\n\t" \
  "v_mov_b32 v201, v200\n\t" \
  "v_cvt_pk_bf16_f32 v95, v94, v95\n\t" \
  "v_cvt_pk_bf16_f32 v94, v92, v93\n\t" \
  "v_mfma_f32_16x16x32_bf16 v[84:87], a[148:151], a[232:235], v[84:87]\n\t" \
  "buffer_load_dwordx4 v231, s[64:67], s96 offen offset:3072 lds\n\t" \
  "v_mfma_f32_16x16x32_bf16 v[84:87], a[84:87], a[248:251], v[84:87]\n\t" \
  "v_permlane32_swap_b32 v200, v201\n\t" \
  "v_mfma_f32_16x16x32_bf16 v[80:83], a[144:147], a[232:235], v[80:83]\n\t" \
  "v_max_f32 v200, v200, v201\n\t" \
  "v_mov_b32 v201, v200\n\t" \
  "v_mfma_f32_16x16x32_bf16 v[80:83], a[80:83], a[248:251], v[80:83]\n\t" \
  "s_add_u32 s99, s53, 43008\n\t" \
  "v_permlane16_swap_b32 v200, v201\n\t" \
  "v_cvt_pk_bf16_f32 v92, v88, v89\n\t" \
  "v_cvt_pk_bf16_f32 v93, v90, v91\n\t" \
  "v_mfma_f32_16x16x32_bf16 v[76:79], a[140:143], a[232:235], v[76:79]\n\t" \
  "v_max_f32 v200, v200, v201\n\t" \
  "v_sub_f32 v180, 0xc2c00000, v200\n\t" \
  "v_mfma_f32_16x16x32_bf16 v[76:79], a[76:79], a[248:251], v[76:79]\n\t" \
  "v_sub_f32 v181, 0xc2c00000, v200\n\t" \
  "v_sub_f32 v182, 0xc2c00000, v200\n\t" \
  "v_mfma_f32_16x16x32_bf16 v[72:75], a[136:139], a[232:235], v[72:75]\n\t" \
  "v_cvt_pk_bf16_f32 v91, v86, v87\n\t" \
  "v_sub_f32 v183, 0xc2c00000, v200\n\t" \
  "v_mfma_f32_16x16x32_bf16 v[72:75], a[72:75], a[248:251], v[72:75]\n\t" \
  "buffer_load_dword v246, v245, s[68:71], s99 offen\n\t" \
  "v_mfma_f32_16x16x32_bf16 v[68:71], a[132:135], a[232:235], v[68:71]\n\t" \
  "v_cvt_pk_bf16_f32 v90, v84, v85\n\t" \
  "v_cvt_pk_bf16_f32 v88, v80, v81\n\t" \
  "v_mfma_f32_16x16x32_bf16 v[68:71], a[68:71], a[248:251], v[68:71]\n\t" \
  "v_cvt_pk_bf16_f32 v89, v82, v83\n\t" \
  "v_permlane16_swap_b32 v92, v94\n\t" \
  "v_mfma_f32_16x16x32_bf16 v[64:67], a[128:131], a[232:235], v[64:67]\n\t" \
  "v_cvt_pk_bf16_f32 v87, v78, v79\n\t" \
  "v_permlane16_swap_b32 v93, v95\n\t" \
  "v_mfma_f32_16x16x32_bf16 v[64:67], a[64:67], a[248:251], v[64:67]\n\t" \
  "v_cvt_pk_bf16_f32 v86, v76, v77\n\t" \
  "v_exp_f32 v11, v11\n\t" \
  "v_mfma_f32_16x16x32_bf16 v[124:127], a[188:191], a[204:207], v[124:127]\n\t" \
  "v_cvt_pk_bf16_f32 v84, v72, v73\n\t" \
  "v_cvt_pk_bf16_f32 v85, v74, v75\n\t" \
  "v_mfma_f32_16x16x32_bf16 v[124:127], a[124:127], a[220:223], v[124:127]\n\t" \
  "v_cvt_pk_bf16_f32 v83, v70, v71\n\t" \
  "v_permlane16_swap_b32 v88, v90\n\t" \
  "v_mfma_f32_16x16x32_bf16 v[120:123], a[184:187], a[204:207], v[120:123]\n\t" \
  "v_cvt_pk_bf16_f32 v82, v68, v69\n\t" \
  "v_exp_f32 v12, v12\n\t" \
  "v_mfma_f32_16x16x32_bf16 v[120:123], a[120:123], a[220:223], v[120:123]\n\t" \
  "v_permlane16_swap_b32 v89, v91\n\t" \
  "v_mfma_f32_16x16x32_bf16 v[116:119], a[180:183], a[204:207], v[116:119]\n\t" \
  "v_cvt_pk_bf16_f32 v80, v64, v65\n\t" \
  "v_add_f32 v32, v32, v180\n\t" \
  "v_mfma_f32_16x16x32_bf16 v[116:119], a[116:119], a[220:223], v[116:119]\n\t" \
  "s_add_u32 s99, s53, 0\n\t" \
  "v_exp_f32 v13, v13\n\t" \
  "v_mfma_f32_16x16x32_bf16 v[112:115], a[176:179], a[204:207], v[112:115]\n\t" \
  "v_permlane16_swap_b32 v84, v86\n\t" \
  "v_mfma_f32_16x16x32_bf16 v[112:115], a[112:115], a[220:223], v[112:115]\n\t" \
  "v_cvt_pk_bf16_f32 v81, v66, v67\n\t" \
  "v_add_f32 v33, v33, v180\n\t" \
  "v_mfma_f32_16x16x32_bf16 v[108:111], a[172:175], a[204:207], v[108:111]\n\t" \
  "buffer_load_dword v246, v245, s[72:75], s99 offen\n\t" \
  "v_mfma_f32_16x16x32_bf16 v[108:111], a[108:111], a[220:223], v[108:111]\n\t" \
  "v_add_f32 v34, v34, v180\n\t" \
  "v_exp_f32 v14, v14\n\t" \
  "v_mfma_f32_16x16x32_bf16 v[104:107], a[168:171], a[204:207], v[104:107]\n\t" \
  "v_permlane16_swap_b32 v85, v87\n\t" \
  "v_mfma_f32_16x16x32_bf16 v[104:107], a[104:107], a[220:223], v[104:107]\n\t" \
  "v_exp_f32 v15, v15\n\t" \
  "v_mfma_f32_16x16x32_bf16 v[100:103], a[164:167], a[204:207], v[100:103]\n\t" \
  "v_permlane16_swap_b32 v80, v82\n\t" \
  "v_mfma_f32_16x16x32_bf16 v[100:103], a[100:103], a[220:223], v[100:103]\n\t" \
  "v_add_f32 v35, v35, v180\n\t" \
  "v_add_f32 v36, v36, v180\n\t" \
  "v_mfma_f32_16x16x32_bf16 v[96:99], a[160:163], a[204:207], v[96:99]\n\t" \
  "v_add_f32 v37, v37, v180\n\t" \
  "v_exp_f32 v32, v32\n\t" \
  "v_mfma_f32_16x16x32_bf16 v[96:99], a[96:99], a[220:223], v[96:99]\n\t" \
  "v_permlane16_swap_b32 v81, v83\n\t" \
  "v_mfma_f32_16x16x32_bf16 v[124:127], a[156:159], a[236:239], v[124:127]\n\t" \
  "v_exp_f32 v33, v33\n\t" \
  "v_mfma_f32_16x16x32_bf16 v[124:127], a[92:95], a[252:255], v[124:127]\n\t" \
  "v_exp_f32 v34, v34\n\t" \
  "v_mfma_f32_16x16x32_bf16 v[120:123], a[152:155], a[236:239], v[120:123]\n\t" \
  "v_add_f32 v38, v38, v180\n\t" \
  "v_add_f32 v39, v39, v180\n\t" \
  "v_mfma_f32_16x16x32_bf16 v[120:123], a[88:91], a[252:255], v[120:123]\n\t" \
  "v_exp_f32 v35, v35\n\t" \
  "v_mfma_f32_16x16x32_bf16 v[116:119], a[148:151], a[236:239], v[116:119]\n\t" \
  "v_cvt_pk_bf16_f32 v130, v4, v5\n\t" \
  "v_cvt_pk_bf16_f32 v127, v126, v127\n\t" \
  "v_mfma_f32_16x16x32_bf16 v[116:119], a[84:87], a[252:255], v[116:119]\n\t" \
  "v_cvt_pk_bf16_f32 v126, v124, v125\n\t" \
  "v_cvt_pk_bf16_f32 v131, v6, v7\n\t" \
  "v_mfma_f32_16x16x32_bf16 v[112:115], a[144:147], a[236:239], v[112:115]\n\t" \
  "v_cvt_pk_bf16_f32 v124, v120, v121\n\t" \
  "v_cvt_pk_bf16_f32 v125, v122, v123\n\t" \
  "v_mfma_f32_16x16x32_bf16 v[112:115], a[80:83], a[252:255], v[112:115]\n\t" \
  "v_permlane16_swap_b32 v124, v126\n\t" \
  "v_mfma_f32_16x16x32_bf16 v[108:111], a[140:143], a[236:239], v[108:111]\n\t" \
  "v_cvt_pk_bf16_f32 v123, v118, v119\n\t" \
  "v_permlane16_swap_b32 v125, v127\n\t" \
  "v_mfma_f32_16x16x32_bf16 v[108:111], a[76:79], a[252:255], v[108:111]\n\t" \
  "v_exp_f32 v36, v36\n\t" \
  "v_cvt_pk_bf16_f32 v122, v116, v117\n\t" \
  "v_mfma_f32_16x16x32_bf16 v[104:107], a[136:139], a[236:239], v[104:107]\n\t" \
  "v_exp_f32 v37, v37\n\t" \
  "v_cvt_pk_bf16_f32 v120, v112, v113\n\t" \
  "v_cvt_pk_bf16_f32 v121, v114, v115\n\t" \
  "v_mfma_f32_16x16x32_bf16 v[104:107], a[72:75], a[252:255], v[104:107]\n\t" \
  "s_nop 0\n\t" \
  "v_cvt_pk_bf16_f32 v119, v110, v111\n\t" \
  "v_permlane16_swap_b32 v120, v122\n\t" \
  "v_cvt_pk_bf16_f32 v118, v108, v109\n\t" \
  "v_mfma_f32_16x16x32_bf16 v[100:103], a[132:135], a[236:239], v[100:103]\n\t" \
  "v_permlane16_swap_b32 v121, v123\n\t" \
  "v_mfma_f32_16x16x32_bf16 v[100:103], a[68:71], a[252:255], v[100:103]\n\t" \
  "v_exp_f32 v38, v38\n\t" \
  "v_mfma_f32_16x16x32_bf16 v[96:99], a[128:131], a[236:239], v[96:99]\n\t" \
  "v_cvt_pk_bf16_f32 v116, v104, v105\n\t" \
  "v_cvt_pk_bf16_f32 v117, v106, v107\n\t" \
  "v_mfma_f32_16x16x32_bf16 v[96:99], a[64:67], a[252:255], v[96:99]\n\t" \
  "v_permlane16_swap_b32 v116, v118\n\t" \
  STS("s_memrealtime s[94:95]\n\t") \
  STS("s_memtime s[92:93]\n\t") \
  "v_mfma_f32_16x16x32_bf16 v[64:67], a[0:3], v[80:83], 0\n\t" \
  "ds_read_b128 a[192:195], v220 offset:16384\n\t" \
  "v_cvt_pk_bf16_f32 v115, v102, v103\n\t" \
  "v_mfma_f32_16x16x32_bf16 v[64:67], a[4:7], v[84:87], v[64:67]\n\t" \
  "ds_read_b128 a[196:199], v221 offset:16384\n\t" \
  "v_permlane16_swap_b32 v117, v119\n\t" \
  "v_mfma_f32_16x16x32_bf16 v[64:67], a[8:11], v[88:91], v[64:67]\n\t" \
  "ds_read_b128 a[200:203], v222 offset:16384\n\t" \
  "v_cvt_pk_bf16_f32 v114, v100, v101\n\t" \
  "v_mfma_f32_16x16x32_bf16 v[64:67], a[12:15], v[92:95], v[64:67]\n\t" \
  "v_cvt_pk_bf16_f32 v112, v96, v97\n\t" \
  "v_exp_f32 v39, v39\n\t" \
  "v_mfma_f32_16x16x32_bf16 v[68:71], a[16:19], v[80:83], 0\n\t" \
  "v_cvt_pk_bf16_f32 v113, v98, v99\n\t" \
  "v_cvt_pk_bf16_f32 v132, v8, v9\n\t" \
  "v_mfma_f32_16x16x32_bf16 v[68:71], a[20:23], v[84:87], v[68:71]\n\t" \
  "v_permlane16_swap_b32 v112, v114\n\t" \
  "v_mfma_f32_16x16x32_bf16 v[68:71], a[24:27], v[88:91], v[68:71]\n\t" \
  "ds_read_b128 a[204:207], v223 offset:16384\n\t" \
  "v_max3_f32 v200, v64, v65, v66\n\t" \
  "v_mfma_f32_16x16x32_bf16 v[68:71], a[28:31], v[92:95], v[68:71]\n\t" \
  "v_permlane16_swap_b32 v113, v115\n\t" \
  "v_mfma_f32_16x16x32_bf16 v[40:43], a[32:35], v[48:51], v[180:183]\n\t" \
  "ds_read_b128 a[208:211], v220 offset:20480\n\t" \
  "v_cvt_pk_bf16_f32 v133, v10, v11\n\t" \
  "v_mfma_f32_16x16x32_bf16 v[40:43], a[36:39], v[52:55], v[40:43]\n\t" \
  "ds_read_b128 a[212:215], v221 offset:20480\n\t" \
  "v_cvt_pk_bf16_f32 v134, v12, v13\n\t" \
  "s_nop 0\n\t" \
  "v_max3_f32 v200, v200, v67, v68\n\t" \
  "v_max3_f32 v200, v200, v69, v70\n\t" \
  "v_mfma_f32_16x16x32_bf16 v[40:43], a[40:43], v[56:59], v[40:43]\n\t" \
  "v_max_f32 v200, v200, v71\n\t" \
  "v_mov_b32 v201, v200\n\t" \
  "v_mfma_f32_16x16x32_bf16 v[40:43], a[44:47], v[60:63], v[40:43]\n\t" \
  "ds_read_b128 a[216:219], v222 offset:20480\n\t" \
  "v_permlane32_swap_b32 v200, v201\n\t" \
  "v_max_f32 v200, v200, v201\n\t" \
  "v_mov_b32 v201, v200\n\t" \
  "v_mfma_f32_16x16x32_bf16 v[44:47], a[48:51], v[48:51], v[180:183]\n\t" \
  STS("v_writelane_b32 v255, s94, 18\n\t") \
  STS("v_writelane_b32 v255, s92, 19\n\t") \
  "s_nop 0\n\t" \
  "v_permlane16_swap_b32 v200, v201\n\t" \
  "v_max_f32 v200, v200, v201\n\t" \
  "v_mfma_f32_16x16x32_bf16 v[44:47], a[52:55], v[52:55], v[44:47]\n\t" \
  "v_sub_f32 v184, 0xc2c00000, v200\n\t" \
  "v_sub_f32 v185, 0xc2c00000, v200\n\t" \
  "v_exp_f32 v40, v40\n\t" \
  "v_mfma_f32_16x16x32_bf16 v[44:47], a[56:59], v[56:59], v[44:47]\n\t" \
  "v_sub_f32 v186, 0xc2c00000, v200\n\t" \
  "v_sub_f32 v187, 0xc2c00000, v200\n\t" \
  "v_mfma_f32_16x16x32_bf16 v[44:47], a[60:63], v[60:63], v[44:47]\n\t" \
  "ds_read_b128 a[220:223], v223 offset:20480\n\t" \
  "v_add_f32 v64, v64, v184\n\t" \
  "v_exp_f32 v41, v41\n\t" \
  "v_mfma_f32_16x16x32_bf16 v[96:99], a[0:3], v[112:115], 0\n\t" \
  "ds_read_b128 a[224:227], v220 offset:24576\n\t" \
  "v_exp_f32 v42, v42\n\t" \
  "v_mfma_f32_16x16x32_bf16 v[96:99], a[4:7], v[116:119], v[96:99]\n\t" \
  "ds_read_b128 a[228:231], v221 offset:24576\n\t" \
  "v_exp_f32 v43, v43\n\t" \
  "v_mfma_f32_16x16x32_bf16 v[96:99], a[8:11], v[120:123], v[96:99]\n\t" \
  "ds_read_b128 a[232:235], v222 offset:24576\n\t" \
  "v_exp_f32 v44, v44\n\t" \
  "v_mfma_f32_16x16x32_bf16 v[96:99], a[12:15], v[124:127], v[96:99]\n\t" \
  "v_add_f32 v65, v65, v184\n\t" \
  "v_exp_f32 v45, v45\n\t" \
  "v_mfma_f32_16x16x32_bf16 v[100:103], a[16:19], v[112:115], 0\n\t" \
  "ds_read_b128 a[236:239], v223 offset:24576\n\t" \
  "v_exp_f32 v46, v46\n\t" \
  "v_mfma_f32_16x16x32_bf16 v[100:103], a[20:23], v[116:119], v[100:103]\n\t" \
  "ds_read_b128 a[240:243], v220 offset:28672\n\t" \
  "v_add_f32 v66, v66, v184\n\t" \
  "v_mfma_f32_16x16x32_bf16 v[100:103], a[24:27], v[120:123], v[100:103]\n\t" \
  "v_exp_f32 v47, v47\n\t" \
  "v_max3_f32 v200, v96, v97, v98\n\t" \
  "v_mfma_f32_16x16x32_bf16 v[100:103], a[28:31], v[124:127], v[100:103]\n\t" \
  "ds_read_b128 a[244:247], v221 offset:28672\n\t" \
  "v_add_f32 v67, v67, v184\n\t" \
  "v_mfma_f32_16x16x32_bf16 v[72:75], a[32:35], v[80:83], v[184:187]\n\t" \
  "v_exp_f32 v64, v64\n\t" \
  "v_mfma_f32_16x16x32_bf16 v[72:75], a[36:39], v[84:87], v[72:75]\n\t" \
  "ds_read_b128 a[248:251], v222 offset:28672\n\t" \
  "v_add_f32 v68, v68, v184\n\t" \
  "s_nop 0\n\t" \
  "v_max3_f32 v200, v200, v99, v100\n\t" \
  "v_max3_f32 v200, v200, v101, v102\n\t" \
  "v_mfma_f32_16x16x32_bf16 v[72:75], a[40:43], v[88:91], v[72:75]\n\t" \
  "v_exp_f32 v65, v65\n\t" \
  "v_max_f32 v200, v200, v103\n\t" \
  "v_mov_b32 v201, v200\n\t" \
  "v_mfma_f32_16x16x32_bf16 v[72:75], a[44:47], v[92:95], v[72:75]\n\t" \
  "ds_read_b128 a[252:255], v223 offset:28672\n\t" \
  "v_permlane32_swap_b32 v200, v201\n\t" \
  "v_max_f32 v200, v200, v201\n\t" \
  "v_mov_b32 v201, v200\n\t" \
  "v_mfma_f32_16x16x32_bf16 v[76:79], a[48:51], v[80:83], v[184:187]\n\t" \
  "v_exp_f32 v66, v66\n\t" \
  "v_permlane16_swap_b32 v200, v201\n\t" \
  "v_mfma_f32_16x16x32_bf16 v[76:79], a[52:55], v[84:87], v[76:79]\n\t" \
  "v_max_f32 v200, v200, v201\n\t" \
  "v_sub_f32 v188, 0xc2c00000, v200\n\t" \
  "v_mfma_f32_16x16x32_bf16 v[76:79], a[56:59], v[88:91], v[76:79]\n\t" \
  "v_sub_f32 v189, 0xc2c00000, v200\n\t" \
  "v_sub_f32 v190, 0xc2c00000, v200\n\t" \
  "v_mfma_f32_16x16x32_bf16 v[76:79], a[60:63], v[92:95], v[76:79]\n\t" \
  "v_add_f32 v69, v69, v184\n\t" \
  "v_exp_f32 v67, v67\n\t" \
  "v_sub_f32 v191, 0xc2c00000, v200\n\t" \
  "s_nop 1\n\t" \
  "v_mfma_f32_16x16x32_bf16 v[104:107], a[32:35], v[112:115], v[188:191]\n\t" \
  "v_add_f32 v70, v70, v184\n\t" \
  "v_add_f32 v71, v71, v184\n\t" \
  "v_mfma_f32_16x16x32_bf16 v[104:107], a[36:39], v[116:119], v[104:107]\n\t" \
  "v_cvt_pk_bf16_f32 v135, v14, v15\n\t" \
  "v_exp_f32 v68, v68\n\t" \
  "v_mfma_f32_16x16x32_bf16 v[104:107], a[40:43], v[120:123], v[104:107]\n\t" \
  "v_cvt_pk_bf16_f32 v136, v32, v33\n\t" \
  "v_exp_f32 v69, v69\n\t" \
  "v_mfma_f32_16x16x32_bf16 v[104:107], a[44:47], v[124:127], v[104:107]\n\t" \
  "s_add_u32 s99, s53, 14336\n\t" \
  "v_exp_f32 v70, v70\n\t" \
  "v_mfma_f32_16x16x32_bf16 v[108:111], a[48:51], v[112:115], v[188:191]\n\t" \
  "v_exp_f32 v71, v71\n\t" \
  "v_mfma_f32_16x16x32_bf16 v[108:111], a[52:55], v[116:119], v[108:111]\n\t" \
  "v_exp_f32 v72, v72\n\t" \
  "v_mfma_f32_16x16x32_bf16 v[108:111], a[56:59], v[120:123], v[108:111]\n\t" \
  "buffer_load_dword v246, v245, s[72:75], s99 offen\n\t" \
  "v_mfma_f32_16x16x32_bf16 v[108:111], a[60:63], v[124:127], v[108:111]\n\t" \
  "s_waitcnt lgkmcnt(0)\n\t" \
  "v_exp_f32 v73, v73\n\t" \
  STA("s_memrealtime s[94:95]\n\t") \
  STA("s_memtime s[92:93]\n\t") \
  "v_mfma_f32_16x16x32_bf16 v[0:3], a[192:195], v[16:19], v[176:179]\n\t" \
  "ds_read_b64_tr_b16 a[128:129], v228 offset:0\n\t" \
  "v_cvt_pk_bf16_f32 v137, v34, v35\n\t" \
  "v_mfma_f32_16x16x32_bf16 v[0:3], a[196:199], v[20:23], v[0:3]\n\t" \
  "ds_read_b64_tr_b16 a[130:131], v228 offset:4096\n\t" \
  "v_cvt_pk_bf16_f32 v138, v36, v37\n\t" \
  "v_mfma_f32_16x16x32_bf16 v[0:3], a[200:203], v[24:27], v[0:3]\n\t" \
  "ds_read_b64_tr_b16 a[132:133], v228 offset:8192\n\t" \
  "v_cvt_pk_bf16_f32 v139, v38, v39\n\t" \
  "v_mfma_f32_16x16x32_bf16 v[0:3], a[204:207], v[28:31], v[0:3]\n\t" \
  "ds_read_b64_tr_b16 a[134:135], v228 offset:12288\n\t" \
  "v_exp_f32 v74, v74\n\t" \
  "v_mfma_f32_16x16x32_bf16 v[4:7], a[220:223], v[28:31], v[176:179]\n\t" \
  "v_exp_f32 v75, v75\n\t" \
  "v_mfma_f32_16x16x32_bf16 v[4:7], a[216:219], v[24:27], v[4:7]\n\t" \
  "ds_read_b64_tr_b16 a[136:137], v229 offset:0\n\t" \
  "v_add_f32 v96, v96, v188\n\t" \
  "v_mfma_f32_16x16x32_bf16 v[4:7], a[212:215], v[20:23], v[4:7]\n\t" \
  "ds_read_b64_tr_b16 a[138:139], v229 offset:4096\n\t" \
  "v_cvt_pk_bf16_f32 v140, v40, v41\n\t" \
  "v_mfma_f32_16x16x32_bf16 v[4:7], a[208:211], v[16:19], v[4:7]\n\t" \
  "ds_read_b64_tr_b16 a[140:141], v229 offset:8192\n\t" \
  "v_exp_f32 v76, v76\n\t" \
  "v_mfma_f32_16x16x32_bf16 v[8:11], a[224:227], v[16:19], v[176:179]\n\t" \
  "v_exp_f32 v77, v77\n\t" \
  "v_mfma_f32_16x16x32_bf16 v[8:11], a[228:231], v[20:23], v[8:11]\n\t" \
  "v_add_f32 v97, v97, v188\n\t" \
  "v_cvt_pk_bf16_f32 v141, v42, v43\n\t" \
  "v_mfma_f32_16x16x32_bf16 v[8:11], a[232:235], v[24:27], v[8:11]\n\t" \
  "ds_read_b64_tr_b16 a[142:143], v229 offset:12288\n\t" \
  "v_add_f32 v98, v98, v188\n\t" \
  "v_mfma_f32_16x16x32_bf16 v[8:11], a[236:239], v[28:31], v[8:11]\n\t" \
  "ds_read_b64_tr_b16 a[144:145], v228 offset:256\n\t" \
  "v_lshl_add_u32 v214, v233, 14, s41\n\t" \
  "v_mfma_f32_16x16x32_bf16 v[12:15], a[252:255], v[28:31], v[176:179]\n\t" \
  STA("v_writelane_b32 v255, s94, 26\n\t") \
  STA("v_writelane_b32 v255, s92, 27\n\t") \
  "v_exp_f32 v78, v78\n\t" \
  "v_mfma_f32_16x16x32_bf16 v[12:15], a[248:251], v[24:27], v[12:15]\n\t" \
  "v_cvt_pk_bf16_f32 v142, v44, v45\n\t" \
  "v_min_u32 v214, .Lfz_z-.Lat_t0-4, v214\n\t" \
  "v_mfma_f32_16x16x32_bf16 v[12:15], a[244:247], v[20:23], v[12:15]\n\t" \
  "s_getpc_b64 s[84:85]\n\t" \
  ".Lat_t0:\n\t" \
  "v_exp_f32 v79, v79\n\t" \
  "v_mfma_f32_16x16x32_bf16 v[12:15], a[240:243], v[16:19], v[12:15]\n\t" \
  "global_load_dword v246, v214, s[84:85]\n\t" \
  "v_mfma_f32_16x16x32_bf16 v[32:35], a[192:195], v[48:51], v[180:183]\n\t" \
  "ds_read_b64_tr_b16 a[146:147], v228 offset:4352\n\t" \
  "v_add_f32 v99, v99, v188\n\t" \
  "v_mfma_f32_16x16x32_bf16 v[32:35], a[196:199], v[52:55], v[32:35]\n\t" \
  ".Lhs6:\n\ts_cbranch_execz .Lhs6n\n\t" \
  "v_exp_f32 v96, v96\n\t" \
  "v_mfma_f32_16x16x32_bf16 v[32:35], a[200:203], v[56:59], v[32:35]\n\t" \
  "ds_read_b64_tr_b16 a[148:149], v228 offset:8448\n\t" \
  "v_cvt_pk_bf16_f32 v143, v46, v47\n\t" \
  "v_mfma_f32_16x16x32_bf16 v[32:35], a[204:207], v[60:63], v[32:35]\n\t" \
  "v_add_f32 v100, v100, v188\n\t" \
  "v_cvt_pk_bf16_f32 v144, v64, v65\n\t" \
  "v_mfma_f32_16x16x32_bf16 v[36:39], a[220:223], v[60:63], v[180:183]\n\t" \
  "ds_read_b64_tr_b16 a[150:151], v228 offset:12544\n\t" \
  "v_add_f32 v101, v101, v188\n\t" \
  "v_mfma_f32_16x16x32_bf16 v[36:39], a[216:219], v[56:59], v[36:39]\n\t" \
  "ds_read_b64_tr_b16 a[152:153], v229 offset:256\n\t" \
  "v_cvt_pk_bf16_f32 v145, v66, v67\n\t" \
  "v_mfma_f32_16x16x32_bf16 v[36:39], a[212:215], v[52:55], v[36:39]\n\t" \
  "v_add_f32 v102, v102, v188\n\t" \
  "v_exp_f32 v97, v97\n\t" \
  "v_mfma_f32_16x16x32_bf16 v[36:39], a[208:211], v[48:51], v[36:39]\n\t" \
  "ds_read_b64_tr_b16 a[154:155], v229 offset:4352\n\t" \
  "v_cvt_pk_bf16_f32 v146, v68, v69\n\t" \
  "v_mfma_f32_16x16x32_bf16 v[40:43], a[224:227], v[48:51], v[180:183]\n\t" \
  "v_exp_f32 v98, v98\n\t" \
  "v_mfma_f32_16x16x32_bf16 v[40:43], a[228:231], v[52:55], v[40:43]\n\t" \
  "ds_read_b64_tr_b16 a[156:157], v229 offset:8448\n\t" \
  "v_add_f32 v103, v103, v188\n\t" \
  "v_mfma_f32_16x16x32_bf16 v[40:43], a[232:235], v[56:59], v[40:43]\n\t" \
  "v_exp_f32 v99, v99\n\t" \
  "v_mfma_f32_16x16x32_bf16 v[40:43], a[236:239], v[60:63], v[40:43]\n\t" \
  "ds_read_b64_tr_b16 a[158:159], v229 offset:12544\n\t" \
  "v_cvt_pk_bf16_f32 v147, v70, v71\n\t" \
  "v_mfma_f32_16x16x32_bf16 v[44:47], a[252:255], v[60:63], v[180:183]\n\t" \
  "v_exp_f32 v100, v100\n\t" \
  "v_mfma_f32_16x16x32_bf16 v[44:47], a[248:251], v[56:59], v[44:47]\n\t" \
  "ds_read_b64_tr_b16 a[160:161], v228 offset:512\n\t" \
  "v_cvt_pk_bf16_f32 v148, v72, v73\n\t" \
  "v_mfma_f32_16x16x32_bf16 v[44:47], a[244:247], v[52:55], v[44:47]\n\t" \
  "ds_read_b64_tr_b16 a[162:163], v228 offset:4608\n\t" \
  "v_cvt_pk_bf16_f32 v149, v74, v75\n\t" \
  "v_mfma_f32_16x16x32_bf16 v[44:47], a[240:243], v[48:51], v[44:47]\n\t" \
  "v_cvt_pk_bf16_f32 v150, v76, v77\n\t" \
  "v_exp_f32 v101, v101\n\t" \
  "v_mfma_f32_16x16x32_bf16 v[64:67], a[192:195], v[80:83], v[184:187]\n\t" \
  "ds_read_b64_tr_b16 a[164:165], v228 offset:8704\n\t" \
  "v_cvt_pk_bf16_f32 v151, v78, v79\n\t" \
  "v_mfma_f32_16x16x32_bf16 v[64:67], a[196:199], v[84:87], v[64:67]\n\t" \
  "v_exp_f32 v102, v102\n\t" \
  "v_mfma_f32_16x16x32_bf16 v[64:67], a[200:203], v[88:91], v[64:67]\n\t" \
  "v_exp_f32 v103, v103\n\t" \
  "v_mfma_f32_16x16x32_bf16 v[64:67], a[204:207], v[92:95], v[64:67]\n\t" \
  "ds_read_b64_tr_b16 a[166:167], v228 offset:12800\n\t" \
  "v_cvt_pk_bf16_f32 v152, v96, v97\n\t" \
  "v_mfma_f32_16x16x32_bf16 v[68:71], a[220:223], v[92:95], v[184:187]\n\t" \
  "ds_read_b64_tr_b16 a[168:169], v229 offset:512\n\t" \
  "v_cvt_pk_bf16_f32 v153, v98, v99\n\t" \
  "v_mfma_f32_16x16x32_bf16 v[68:71], a[216:219], v[88:91], v[68:71]\n\t" \
  "v_exp_f32 v104, v104\n\t" \
  "v_mfma_f32_16x16x32_bf16 v[68:71], a[212:215], v[84:87], v[68:71]\n\t" \
  "ds_read_b64_tr_b16 a[170:171], v229 offset:4608\n\t" \
  "v_cvt_pk_bf16_f32 v154, v100, v101\n\t" \
  "v_mfma_f32_16x16x32_bf16 v[68:71], a[208:211], v[80:83], v[68:71]\n\t" \
  "ds_read_b64_tr_b16 a[172:173], v229 offset:8704\n\t" \
  "v_exp_f32 v105, v105\n\t" \
  "v_mfma_f32_16x16x32_bf16 v[72:75], a[224:227], v[80:83], v[184:187]\n\t" \
  "v_exp_f32 v106, v106\n\t" \
  "v_mfma_f32_16x16x32_bf16 v[72:75], a[228:231], v[84:87], v[72:75]\n\t" \
  "ds_read_b64_tr_b16 a[174:175], v229 offset:12800\n\t" \
  "v_cvt_pk_bf16_f32 v155, v102, v103\n\t" \
  "v_mfma_f32_16x16x32_bf16 v[72:75], a[232:235], v[88:91], v[72:75]\n\t" \
  "v_exp_f32 v107, v107\n\t" \
  "v_mfma_f32_16x16x32_bf16 v[72:75], a[236:239], v[92:95], v[72:75]\n\t" \
  "ds_read_b64_tr_b16 a[176:177], v228 offset:768\n\t" \
  "v_cvt_pk_bf16_f32 v156, v104, v105\n\t" \
  "v_mfma_f32_16x16x32_bf16 v[76:79], a[252:255], v[92:95], v[184:187]\n\t" \
  "v_exp_f32 v108, v108\n\t" \
  "v_mfma_f32_16x16x32_bf16 v[76:79], a[248:251], v[88:91], v[76:79]\n\t" \
  "ds_read_b64_tr_b16 a[178:179], v228 offset:4864\n\t" \
  "v_cvt_pk_bf16_f32 v157, v106, v107\n\t" \
  "v_mfma_f32_16x16x32_bf16 v[76:79], a[244:247], v[84:87], v[76:79]\n\t" \
  "v_exp_f32 v109, v109\n\t" \
  "v_mfma_f32_16x16x32_bf16 v[76:79], a[240:243], v[80:83], v[76:79]\n\t" \
  "ds_read_b64_tr_b16 a[180:181], v228 offset:8960\n\t" \
  "v_cvt_pk_bf16_f32 v158, v108, v109\n\t" \
  "v_mfma_f32_16x16x32_bf16 v[96:99], a[192:195], v[112:115], v[188:191]\n\t" \
  "v_exp_f32 v110, v110\n\t" \
  "v_mfma_f32_16x16x32_bf16 v[96:99], a[196:199], v[116:119], v[96:99]\n\t" \
  "v_exp_f32 v111, v111\n\t" \
  "v_mfma_f32_16x16x32_bf16 v[96:99], a[200:203], v[120:123], v[96:99]\n\t" \
  "ds_read_b64_tr_b16 a[182:183], v228 offset:13056\n\t" \
  "v_cvt_pk_bf16_f32 v159, v110, v111\n\t" \
  "v_mfma_f32_16x16x32_bf16 v[96:99], a[204:207], v[124:127], v[96:99]\n\t" \
  "v_exp_f32 v0, v0\n\t" \
  "v_mfma_f32_16x16x32_bf16 v[100:103], a[220:223], v[124:127], v[188:191]\n\t" \
  "ds_read_b64_tr_b16 a[184:185], v229 offset:768\n\t" \
  "v_mfma_f32_16x16x32_bf16 v[100:103], a[216:219], v[120:123], v[100:103]\n\t" \
  "v_exp_f32 v1, v1\n\t" \
  "v_mfma_f32_16x16x32_bf16 v[100:103], a[212:215], v[116:119], v[100:103]\n\t" \
  "ds_read_b64_tr_b16 a[186:187], v229 offset:4864\n\t" \
  "v_mfma_f32_16x16x32_bf16 v[100:103], a[208:211], v[112:115], v[100:103]\n\t" \
  "s_add_u32 s99, s53, 28672\n\t" \
  "v_exp_f32 v2, v2\n\t" \
  "v_mfma_f32_16x16x32_bf16 v[104:107], a[224:227], v[112:115], v[188:191]\n\t" \
  "ds_read_b64_tr_b16 a[188:189], v229 offset:8960\n\t" \
  "v_mfma_f32_16x16x32_bf16 v[104:107], a[228:231], v[116:119], v[104:107]\n\t" \
  "ds_read_b64_tr_b16 a[190:191], v229 offset:13056\n\t" \
  "v_mfma_f32_16x16x32_bf16 v[104:107], a[232:235], v[120:123], v[104:107]\n\t" \
  "buffer_load_dword v246, v245, s[72:75], s99 offen\n\t" \
  "v_mfma_f32_16x16x32_bf16 v[104:107], a[236:239], v[124:127], v[104:107]\n\t" \
  "v_exp_f32 v3, v3\n\t" \
  "v_mfma_f32_16x16x32_bf16 v[108:111], a[252:255], v[124:127], v[188:191]\n\t" \
  "v_exp_f32 v4, v4\n\t" \
  "v_mfma_f32_16x16x32_bf16 v[108:111], a[248:251], v[120:123], v[108:111]\n\t" \
  "v_exp_f32 v5, v5\n\t" \
  "v_mfma_f32_16x16x32_bf16 v[108:111], a[244:247], v[116:119], v[108:111]\n\t" \
  "s_waitcnt vmcnt(5) lgkmcnt(0)\n\t" \
  "v_exp_f32 v6, v6\n\t" \
  "v_mfma_f32_16x16x32_bf16 v[108:111], a[240:243], v[112:115], v[108:111]\n\t" \
  "s_barrier\n\t" \
  STA("s_memrealtime s[94:95]\n\t") \
  STA("s_memtime s[92:93]\n\t") \
  "v_mfma_f32_16x16x32_bf16 a[0:3], a[128:131], v[128:131], 0\n\t" \
  "v_exp_f32 v7, v7\n\t" \
  "v_mfma_f32_16x16x32_bf16 a[0:3], a[132:135], v[132:135], a[0:3]\n\t" \
  "v_exp_f32 v8, v8\n\t" \
  "v_mfma_f32_16x16x32_bf16 a[4:7], a[136:139], v[128:131], 0\n\t" \
  "v_exp_f32 v9, v9\n\t" \
  "v_mfma_f32_16x16x32_bf16 a[4:7], a[140:143], v[132:135], a[4:7]\n\t" \
  "v_exp_f32 v10, v10\n\t" \
  "v_mfma_f32_16x16x32_bf16 a[8:11], a[144:147], v[128:131], 0\n\t" \
  "v_exp_f32 v11, v11\n\t" \
  "v_mfma_f32_16x16x32_bf16 a[8:11], a[148:151], v[132:135], a[8:11]\n\t" \
  "v_exp_f32 v12, v12\n\t" \
  "v_mfma_f32_16x16x32_bf16 a[12:15], a[152:155], v[128:131], 0\n\t" \
  "v_exp_f32 v13, v13\n\t" \
  "v_mfma_f32_16x16x32_bf16 a[12:15], a[156:159], v[132:135], a[12:15]\n\t" \
  "v_exp_f32 v14, v14\n\t" \
  "v_mfma_f32_16x16x32_bf16 a[16:19], a[160:163], v[128:131], 0\n\t" \
  "v_exp_f32 v15, v15\n\t" \
  "v_mfma_f32_16x16x32_bf16 a[16:19], a[164:167], v[132:135], a[16:19]\n\t" \
  "v_exp_f32 v32, v32\n\t" \
  "v_mfma_f32_16x16x32_bf16 a[20:23], a[168:171], v[128:131], 0\n\t" \
  "v_exp_f32 v33, v33\n\t" \
  "v_mfma_f32_16x16x32_bf16 a[20:23], a[172:175], v[132:135], a[20:23]\n\t" \
  "v_exp_f32 v34, v34\n\t" \
  "v_mfma_f32_16x16x32_bf16 a[24:27], a[176:179], v[128:131], 0\n\t" \
  STA("v_writelane_b32 v255, s94, 28\n\t") \
  STA("v_writelane_b32 v255, s92, 29\n\t") \
  "v_exp_f32 v35, v35\n\t" \
  "v_mfma_f32_16x16x32_bf16 a[24:27], a[180:183], v[132:135], a[24:27]\n\t" \
  "v_exp_f32 v36, v36\n\t" \
  "v_mfma_f32_16x16x32_bf16 a[28:31], a[184:187], v[128:131], 0\n\t" \
  "v_exp_f32 v37, v37\n\t" \
  "v_mfma_f32_16x16x32_bf16 a[28:31], a[188:191], v[132:135], a[28:31]\n\t" \
  "v_exp_f32 v38, v38\n\t" \
  "v_smfmac_f32_16x16x64_bf16 v[160:163], v[216:219], v[128:135], v208\n\t" \
  "s_add_u32 m0, s38, 65536\n\t" \
  "ds_read_b128 a[192:195], v220 offset:0\n\t" \
  "v_cvt_pk_bf16_f32 v128, v0, v1\n\t" \
  "v_mfma_f32_16x16x32_bf16 a[32:35], a[128:131], v[136:139], 0\n\t" \
  "s_add_u32 s96, s38, 49152\n\t" \
  "ds_read_b128 a[196:199], v221 offset:0\n\t" \
  "v_cvt_pk_bf16_f32 v129, v2, v3\n\t" \
  "v_mfma_f32_16x16x32_bf16 a[32:35], a[132:135], v[140:143], a[32:35]\n\t" \
  "v_exp_f32 v39, v39\n\t" \
  "v_mfma_f32_16x16x32_bf16 a[36:39], a[136:139], v[136:139], 0\n\t" \
  "v_exp_f32 v40, v40\n\t" \
  "v_mfma_f32_16x16x32_bf16 a[36:39], a[140:143], v[140:143], a[36:39]\n\t" \
  "buffer_load_dwordx4 v224, s[60:63], s96 offen offset:0 lds\n\t" \
  "v_mfma_f32_16x16x32_bf16 a[40:43], a[144:147], v[136:139], 0\n\t" \
  "ds_read_b128 a[200:203], v222 offset:0\n\t" \
  "v_cvt_pk_bf16_f32 v130, v4, v5\n\t" \
  "v_mfma_f32_16x16x32_bf16 a[40:43], a[148:151], v[140:143], a[40:43]\n\t" \
  "v_exp_f32 v41, v41\n\t" \
  "v_mfma_f32_16x16x32_bf16 a[44:47], a[152:155], v[136:139], 0\n\t" \
  "ds_read_b128 a[204:207], v223 offset:0\n\t" \
  "v_cvt_pk_bf16_f32 v131, v6, v7\n\t" \
  "v_mfma_f32_16x16x32_bf16 a[44:47], a[156:159], v[140:143], a[44:47]\n\t" \
  "v_exp_f32 v42, v42\n\t" \
  "v_mfma_f32_16x16x32_bf16 a[48:51], a[160:163], v[136:139], 0\n\t" \
  "ds_read_b128 a[208:211], v220 offset:4096\n\t" \
  "v_cvt_pk_bf16_f32 v132, v8, v9\n\t" \
  "v_mfma_f32_16x16x32_bf16 a[48:51], a[164:167], v[140:143], a[48:51]\n\t" \
  "buffer_load_dwordx4 v225, s[60:63], s96 offen offset:1024 lds\n\t" \
  "v_mfma_f32_16x16x32_bf16 a[52:55], a[168:171], v[136:139], 0\n\t" \
  "v_exp_f32 v43, v43\n\t" \
  "v_mfma_f32_16x16x32_bf16 a[52:55], a[172:175], v[140:143], a[52:55]\n\t" \
  "ds_read_b128 a[212:215], v221 offset:4096\n\t" \
  "v_cvt_pk_bf16_f32 v133, v10, v11\n\t" \
  "v_mfma_f32_16x16x32_bf16 a[56:59], a[176:179], v[136:139], 0\n\t" \
  "v_exp_f32 v44, v44\n\t" \
  "v_mfma_f32_16x16x32_bf16 a[56:59], a[180:183], v[140:143], a[56:59]\n\t" \
  "v_exp_f32 v45, v45\n\t" \
  "v_mfma_f32_16x16x32_bf16 a[60:63], a[184:187], v[136:139], 0\n\t" \
  "buffer_load_dwordx4 v226, s[60:63], s96 offen offset:2048 lds\n\t" \
  "v_mfma_f32_16x16x32_bf16 a[60:63], a[188:191], v[140:143], a[60:63]\n\t" \
  "ds_read_b128 a[216:219], v222 offset:4096\n\t" \
  "v_cvt_pk_bf16_f32 v134, v12, v13\n\t" \
  "v_smfmac_f32_16x16x64_bf16 v[164:167], v[216:219], v[136:143], v208\n\t" \
  "ds_read_b128 a[220:223], v223 offset:4096\n\t" \
  "v_cvt_pk_bf16_f32 v135, v14, v15\n\t" \
  "v_mfma_f32_16x16x32_bf16 a[64:67], a[128:131], v[144:147], 0\n\t" \
  "v_exp_f32 v46, v46\n\t" \
  "v_mfma_f32_16x16x32_bf16 a[64:67], a[132:135], v[148:151], a[64:67]\n\t" \
  "v_exp_f32 v47, v47\n\t" \
  "v_mfma_f32_16x16x32_bf16 a[68:71], a[136:139], v[144:147], 0\n\t" \
  "ds_read_b128 a[224:227], v220 offset:8192\n\t" \
  "v_cvt_pk_bf16_f32 v136, v32, v33\n\t" \
  "v_mfma_f32_16x16x32_bf16 a[68:71], a[140:143], v[148:151], a[68:71]\n\t" \
  "buffer_load_dwordx4 v227, s[60:63], s96 offen offset:3072 lds\n\t" \
  "v_mfma_f32_16x16x32_bf16 a[72:75], a[144:147], v[144:147], 0\n\t" \
  "s_add_u32 m0, s38, 81920\n\t" \
  "v_exp_f32 v64, v64\n\t" \
  "v_mfma_f32_16x16x32_bf16 a[72:75], a[148:151], v[148:151], a[72:75]\n\t" \
  "s_add_u32 s96, s38, 32768\n\t" \
  "ds_read_b128 a[228:231], v221 offset:8192\n\t" \
  "v_cvt_pk_bf16_f32 v137, v34, v35\n\t" \
  "v_mfma_f32_16x16x32_bf16 a[76:79], a[152:155], v[144:147], 0\n\t" \
  "v_exp_f32 v65, v65\n\t" \
  "v_mfma_f32_16x16x32_bf16 a[76:79], a[156:159], v[148:151], a[76:79]\n\t" \
  "s_add_u32 s99, s53, 43008\n\t" \
  "v_exp_f32 v66, v66\n\t" \
  "v_mfma_f32_16x16x32_bf16 a[80:83], a[160:163], v[144:147], 0\n\t" \
  "buffer_load_dwordx4 v230, s[64:67], s96 offen offset:0 lds\n\t" \
  "v_mfma_f32_16x16x32_bf16 a[80:83], a[164:167], v[148:151], a[80:83]\n\t" \
  "ds_read_b128 a[232:235], v222 offset:8192\n\t" \
  "v_cvt_pk_bf16_f32 v138, v36, v37\n\t" \
  "v_mfma_f32_16x16x32_bf16 a[84:87], a[168:171], v[144:147], 0\n\t" \
  "buffer_load_dword v246, v245, s[72:75], s99 offen\n\t" \
  "v_mfma_f32_16x16x32_bf16 a[84:87], a[172:175], v[148:151], a[84:87]\n\t" \
  "ds_read_b128 a[236:239], v223 offset:8192\n\t" \
  "v_cvt_pk_bf16_f32 v139, v38, v39\n\t" \
  "v_mfma_f32_16x16x32_bf16 a[88:91], a[176:179], v[144:147], 0\n\t" \
  "v_exp_f32 v67, v67\n\t" \
  "v_mfma_f32_16x16x32_bf16 a[88:91], a[180:183], v[148:151], a[88:91]\n\t" \
  "ds_read_b128 a[240:243], v220 offset:12288\n\t" \
  "v_cvt_pk_bf16_f32 v140, v40, v41\n\t" \
  "v_mfma_f32_16x16x32_bf16 a[92:95], a[184:187], v[144:147], 0\n\t" \
  "buffer_load_dwordx4 v231, s[64:67], s96 offen offset:1024 lds\n\t" \
  "v_mfma_f32_16x16x32_bf16 a[92:95], a[188:191], v[148:151], a[92:95]\n\t" \
  "v_exp_f32 v68, v68\n\t" \
  "v_smfmac_f32_16x16x64_bf16 v[168:171], v[216:219], v[144:151], v208\n\t" \
  "ds_read_b128 a[244:247], v221 offset:12288\n\t" \
  "v_cvt_pk_bf16_f32 v141, v42, v43\n\t" \
  "v_mfma_f32_16x16x32_bf16 a[96:99], a[128:131], v[152:155], 0\n\t" \
  "v_exp_f32 v69, v69\n\t" \
  "v_mfma_f32_16x16x32_bf16 a[96:99], a[132:135], v[156:159], a[96:99]\n\t" \
  "v_exp_f32 v70, v70\n\t" \
  "v_mfma_f32_16x16x32_bf16 a[100:103], a[136:139], v[152:155], 0\n\t" \
  "buffer_load_dwordx4 v230, s[64:67], s96 offen offset:2048 lds\n\t" \
  "v_mfma_f32_16x16x32_bf16 a[100:103], a[140:143], v[156:159], a[100:103]\n\t" \
  "ds_read_b128 a[248:251], v222 offset:12288\n\t" \
  "v_cvt_pk_bf16_f32 v142, v44, v45\n\t" \
  "v_mfma_f32_16x16x32_bf16 a[104:107], a[144:147], v[152:155], 0\n\t" \
  "v_exp_f32 v71, v71\n\t" \
  "v_mfma_f32_16x16x32_bf16 a[104:107], a[148:151], v[156:159], a[104:107]\n\t" \
  "ds_read_b128 a[252:255], v223 offset:12288\n\t" \
  "v_cvt_pk_bf16_f32 v143, v46, v47\n\t" \
  "v_mfma_f32_16x16x32_bf16 a[108:111], a[152:155], v[152:155], 0\n\t" \
  "v_exp_f32 v72, v72\n\t" \
  "v_mfma_f32_16x16x32_bf16 a[108:111], a[156:159], v[156:159], a[108:111]\n\t" \
  "v_exp_f32 v73, v73\n\t" \
  "v_mfma_f32_16x16x32_bf16 a[112:115], a[160:163], v[152:155], 0\n\t" \
  "buffer_load_dwordx4 v231, s[64:67], s96 offen offset:3072 lds\n\t" \
  "v_mfma_f32_16x16x32_bf16 a[112:115], a[164:167], v[156:159], a[112:115]\n\t" \
  "v_exp_f32 v74, v74\n\t" \
  "v_mfma_f32_16x16x32_bf16 a[116:119], a[168:171], v[152:155], 0\n\t" \
  "v_exp_f32 v75, v75\n\t" \
  "v_mfma_f32_16x16x32_bf16 a[116:119], a[172:175], v[156:159], a[116:119]\n\t" \
  "v_exp_f32 v76, v76\n\t" \
  "v_mfma_f32_16x16x32_bf16 a[120:123], a[176:179], v[152:155], 0\n\t" \
  "v_exp_f32 v77, v77\n\t" \
  "v_mfma_f32_16x16x32_bf16 a[120:123], a[180:183], v[156:159], a[120:123]\n\t" \
  "v_exp_f32 v78, v78\n\t" \
  "v_mfma_f32_16x16x32_bf16 a[124:127], a[184:187], v[152:155], 0\n\t" \
  "v_exp_f32 v79, v79\n\t" \
  "v_mfma_f32_16x16x32_bf16 a[124:127], a[188:191], v[156:159], a[124:127]\n\t" \
  "v_exp_f32 v96, v96\n\t" \
  "v_smfmac_f32_16x16x64_bf16 v[172:175], v[216:219], v[152:159], v208\n\t" \
  "s_waitcnt lgkmcnt(12)\n\t" \
  "v_cvt_pk_bf16_f32 v144, v64, v65\n\t" \
  STA("s_memrealtime s[94:95]\n\t") \
  STA("s_memtime s[92:93]\n\t") \
  "v_mfma_f32_16x16x32_bf16 v[0:3], a[192:195], v[16:19], v[176:179]\n\t" \
  "ds_read_b64_tr_b16 a[128:129], v210 offset:0\n\t" \
  "v_cvt_pk_bf16_f32 v145, v66, v67\n\t" \
  "v_mfma_f32_16x16x32_bf16 v[0:3], a[196:199], v[20:23], v[0:3]\n\t" \
  "v_exp_f32 v97, v97\n\t" \
  "v_mfma_f32_16x16x32_bf16 v[0:3], a[200:203], v[24:27], v[0:3]\n\t" \
  "s_add_u32 m0, s38, 16384\n\t" \
  "ds_read_b64_tr_b16 a[130:131], v210 offset:4096\n\t" \
  "v_cvt_pk_bf16_f32 v146, v68, v69\n\t" \
  "v_mfma_f32_16x16x32_bf16 v[0:3], a[204:207], v[28:31], v[0:3]\n\t" \
  "s_waitcnt lgkmcnt(10)\n\t" \
  "v_exp_f32 v98, v98\n\t" \
  "v_mfma_f32_16x16x32_bf16 v[4:7], a[220:223], v[28:31], v[176:179]\n\t" \
  "s_mov_b32 s96, 0\n\t" \
  "ds_read_b64_tr_b16 a[132:133], v210 offset:8192\n\t" \
  "v_cvt_pk_bf16_f32 v147, v70, v71\n\t" \
  "v_mfma_f32_16x16x32_bf16 v[4:7], a[216:219], v[24:27], v[4:7]\n\t" \
  "v_exp_f32 v99, v99\n\t" \
  "v_mfma_f32_16x16x32_bf16 v[4:7], a[212:215], v[20:23], v[4:7]\n\t" \
  "buffer_load_dwordx4 v232, s[68:71], s96 offen offset:0 lds\n\t" \
  "v_mfma_f32_16x16x32_bf16 v[4:7], a[208:211], v[16:19], v[4:7]\n\t" \
  "s_waitcnt lgkmcnt(7)\n\t" \
  "ds_read_b64_tr_b16 a[134:135], v210 offset:12288\n\t" \
  "v_cvt_pk_bf16_f32 v148, v72, v73\n\t" \
  "v_mfma_f32_16x16x32_bf16 v[8:11], a[224:227], v[16:19], v[176:179]\n\t" \
  "ds_read_b64_tr_b16 a[136:137], v211 offset:0\n\t" \
  "v_cvt_pk_bf16_f32 v149, v74, v75\n\t" \
  "v_mfma_f32_16x16x32_bf16 v[8:11], a[228:231], v[20:23], v[8:11]\n\t" \
  "ds_read_b64_tr_b16 a[138:139], v211 offset:4096\n\t" \
  "v_cvt_pk_bf16_f32 v150, v76, v77\n\t" \
  "v_mfma_f32_16x16x32_bf16 v[8:11], a[232:235], v[24:27], v[8:11]\n\t" \
  "v_exp_f32 v100, v100\n\t" \
  "v_mfma_f32_16x16x32_bf16 v[8:11], a[236:239], v[28:31], v[8:11]\n\t" \
  "s_waitcnt lgkmcnt(6)\n\t" \
  "ds_read_b64_tr_b16 a[140:141], v211 offset:8192\n\t" \
  "v_cvt_pk_bf16_f32 v151, v78, v79\n\t" \
  "v_mfma_f32_16x16x32_bf16 v[12:15], a[252:255], v[28:31], v[176:179]\n\t" \
  STA("v_writelane_b32 v255, s94, 30\n\t") \
  STA("v_writelane_b32 v255, s92, 31\n\t") \
  "v_exp_f32 v101, v101\n\t" \
  "v_mfma_f32_16x16x32_bf16 v[12:15], a[248:251], v[24:27], v[12:15]\n\t" \
  "ds_read_b64_tr_b16 a[142:143], v211 offset:12288\n\t" \
  "v_cvt_pk_bf16_f32 v152, v96, v97\n\t" \
  "v_mfma_f32_16x16x32_bf16 v[12:15], a[244:247], v[20:23], v[12:15]\n\t" \
  "ds_read_b64_tr_b16 a[144:145], v210 offset:256\n\t" \
  "v_cvt_pk_bf16_f32 v153, v98, v99\n\t" \
  "v_mfma_f32_16x16x32_bf16 v[12:15], a[240:243], v[16:19], v[12:15]\n\t" \
  "v_exp_f32 v102, v102\n\t" \
  "v_mfma_f32_16x16x32_bf16 v[32:35], a[192:195], v[48:51], v[180:183]\n\t" \
  "s_mov_b32 s96, 6144\n\t" \
  "ds_read_b64_tr_b16 a[146:147], v210 offset:4352\n\t" \
  "v_cvt_pk_bf16_f32 v154, v100, v101\n\t" \
  "v_mfma_f32_16x16x32_bf16 v[32:35], a[196:199], v[52:55], v[32:35]\n\t" \
  "v_exp_f32 v103, v103\n\t" \
  "v_mfma_f32_16x16x32_bf16 v[32:35], a[200:203], v[56:59], v[32:35]\n\t" \
  "ds_read_b64_tr_b16 a[148:149], v210 offset:8448\n\t" \
  "v_cvt_pk_bf16_f32 v155, v102, v103\n\t" \
  "v_mfma_f32_16x16x32_bf16 v[32:35], a[204:207], v[60:63], v[32:35]\n\t" \
  "v_exp_f32 v104, v104\n\t" \
  "v_mfma_f32_16x16x32_bf16 v[36:39], a[220:223], v[60:63], v[180:183]\n\t" \
  "buffer_load_dwordx4 v232, s[68:71], s96 offen offset:1024 lds\n\t" \
  "v_mfma_f32_16x16x32_bf16 v[36:39], a[216:219], v[56:59], v[36:39]\n\t" \
  "v_exp_f32 v105, v105\n\t" \
  "v_mfma_f32_16x16x32_bf16 v[36:39], a[212:215], v[52:55], v[36:39]\n\t" \
  "ds_read_b64_tr_b16 a[150:151], v210 offset:12544\n\t" \
  "v_cvt_pk_bf16_f32 v156, v104, v105\n\t" \
  "v_mfma_f32_16x16x32_bf16 v[36:39], a[208:211], v[48:51], v[36:39]\n\t" \
  "v_exp_f32 v106, v106\n\t" \
  "v_mfma_f32_16x16x32_bf16 v[40:43], a[224:227], v[48:51], v[180:183]\n\t" \
  "v_exp_f32 v107, v107\n\t" \
  "v_mfma_f32_16x16x32_bf16 v[40:43], a[228:231], v[52:55], v[40:43]\n\t" \
  "ds_read_b64_tr_b16 a[152:153], v211 offset:256\n\t" \
  "v_cvt_pk_bf16_f32 v157, v106, v107\n\t" \
  "v_mfma_f32_16x16x32_bf16 v[40:43], a[232:235], v[56:59], v[40:43]\n\t" \
  "v_exp_f32 v108, v108\n\t" \
  "v_mfma_f32_16x16x32_bf16 v[40:43], a[236:239], v[60:63], v[40:43]\n\t" \
  "v_exp_f32 v109, v109\n\t" \
  "v_mfma_f32_16x16x32_bf16 v[44:47], a[252:255], v[60:63], v[180:183]\n\t" \
  "ds_read_b64_tr_b16 a[154:155], v211 offset:4352\n\t" \
  "v_cvt_pk_bf16_f32 v158, v108, v109\n\t" \
  "v_mfma_f32_16x16x32_bf16 v[44:47], a[248:251], v[56:59], v[44:47]\n\t" \
  "v_exp_f32 v110, v110\n\t" \
  "v_mfma_f32_16x16x32_bf16 v[44:47], a[244:247], v[52:55], v[44:47]\n\t" \
  "ds_read_b64_tr_b16 a[156:157], v211 offset:8448\n\t" \
  "s_mov_b32 s96, 12288\n\t" \
  "v_mfma_f32_16x16x32_bf16 v[44:47], a[240:243], v[48:51], v[44:47]\n\t" \
  "v_exp_f32 v111, v111\n\t" \
  "v_mfma_f32_16x16x32_bf16 v[64:67], a[192:195], v[80:83], v[184:187]\n\t" \
  "ds_read_b64_tr_b16 a[158:159], v211 offset:12544\n\t" \
  "v_cvt_pk_bf16_f32 v159, v110, v111\n\t" \
  "v_mfma_f32_16x16x32_bf16 v[64:67], a[196:199], v[84:87], v[64:67]\n\t" \
  "ds_read_b64_tr_b16 a[160:161], v210 offset:512\n\t" \
  "v_mfma_f32_16x16x32_bf16 v[64:67], a[200:203], v[88:91], v[64:67]\n\t" \
  "buffer_load_dwordx4 v232, s[68:71], s96 offen offset:2048 lds\n\t" \
  "v_mfma_f32_16x16x32_bf16 v[64:67], a[204:207], v[92:95], v[64:67]\n\t" \
  "ds_read_b64_tr_b16 a[162:163], v210 offset:4608\n\t" \
  "v_mfma_f32_16x16x32_bf16 v[68:71], a[220:223], v[92:95], v[184:187]\n\t" \
  "ds_read_b64_tr_b16 a[164:165], v210 offset:8704\n\t" \
  "v_mfma_f32_16x16x32_bf16 v[68:71], a[216:219], v[88:91], v[68:71]\n\t" \
  "ds_read_b64_tr_b16 a[166:167], v210 offset:12800\n\t" \
  "v_mfma_f32_16x16x32_bf16 v[68:71], a[212:215], v[84:87], v[68:71]\n\t" \
  "v_exp_f32 v0, v0\n\t" \
  "v_mfma_f32_16x16x32_bf16 v[68:71], a[208:211], v[80:83], v[68:71]\n\t" \
  "ds_read_b64_tr_b16 a[168:169], v211 offset:512\n\t" \
  "v_mfma_f32_16x16x32_bf16 v[72:75], a[224:227], v[80:83], v[184:187]\n\t" \
  "v_exp_f32 v1, v1\n\t" \
  "v_mfma_f32_16x16x32_bf16 v[72:75], a[228:231], v[84:87], v[72:75]\n\t" \
  "ds_read_b64_tr_b16 a[170:171], v211 offset:4608\n\t" \
  "v_mfma_f32_16x16x32_bf16 v[72:75], a[232:235], v[88:91], v[72:75]\n\t" \
  "ds_read_b64_tr_b16 a[172:173], v211 offset:8704\n\t" \
  "v_mfma_f32_16x16x32_bf16 v[72:75], a[236:239], v[92:95], v[72:75]\n\t" \
  "v_exp_f32 v2, v2\n\t" \
  "v_mfma_f32_16x16x32_bf16 v[76:79], a[252:255], v[92:95], v[184:187]\n\t" \
  "ds_read_b64_tr_b16 a[174:175], v211 offset:12800\n\t" \
  "s_mov_b32 s96, 18432\n\t" \
  "v_mfma_f32_16x16x32_bf16 v[76:79], a[248:251], v[88:91], v[76:79]\n\t" \
  "ds_read_b64_tr_b16 a[176:177], v210 offset:768\n\t" \
  "v_mfma_f32_16x16x32_bf16 v[76:79], a[244:247], v[84:87], v[76:79]\n\t" \
  "v_exp_f32 v3, v3\n\t" \
  "v_mfma_f32_16x16x32_bf16 v[76:79], a[240:243], v[80:83], v[76:79]\n\t" \
  "ds_read_b64_tr_b16 a[178:179], v210 offset:4864\n\t" \
  "v_mfma_f32_16x16x32_bf16 v[96:99], a[192:195], v[112:115], v[188:191]\n\t" \
  "buffer_load_dwordx4 v232, s[68:71], s96 offen offset:3072 lds\n\t" \
  "v_mfma_f32_16x16x32_bf16 v[96:99], a[196:199], v[116:119], v[96:99]\n\t" \
  "ds_read_b64_tr_b16 a[180:181], v210 offset:8960\n\t" \
  "v_mfma_f32_16x16x32_bf16 v[96:99], a[200:203], v[120:123], v[96:99]\n\t" \
  "ds_read_b64_tr_b16 a[182:183], v210 offset:13056\n\t" \
  "v_mfma_f32_16x16x32_bf16 v[96:99], a[204:207], v[124:127], v[96:99]\n\t" \
  "v_exp_f32 v4, v4\n\t" \
  "v_mfma_f32_16x16x32_bf16 v[100:103], a[220:223], v[124:127], v[188:191]\n\t" \
  "ds_read_b64_tr_b16 a[184:185], v211 offset:768\n\t" \
  "v_mfma_f32_16x16x32_bf16 v[100:103], a[216:219], v[120:123], v[100:103]\n\t" \
  "v_exp_f32 v5, v5\n\t" \
  "v_mfma_f32_16x16x32_bf16 v[100:103], a[212:215], v[116:119], v[100:103]\n\t" \
  "ds_read_b64_tr_b16 a[186:187], v211 offset:4864\n\t" \
  "v_mfma_f32_16x16x32_bf16 v[100:103], a[208:211], v[112:115], v[100:103]\n\t" \
  "ds_read_b64_tr_b16 a[188:189], v211 offset:8960\n\t" \
  "v_mfma_f32_16x16x32_bf16 v[104:107], a[224:227], v[112:115], v[188:191]\n\t" \
  "v_exp_f32 v6, v6\n\t" \
  "v_mfma_f32_16x16x32_bf16 v[104:107], a[228:231], v[116:119], v[104:107]\n\t" \
  "ds_read_b64_tr_b16 a[190:191], v211 offset:13056\n\t" \
  "v_mfma_f32_16x16x32_bf16 v[104:107], a[232:235], v[120:123], v[104:107]\n\t" \
  "v_exp_f32 v7, v7\n\t" \
  "v_mfma_f32_16x16x32_bf16 v[104:107], a[236:239], v[124:127], v[104:107]\n\t" \
  "v_exp_f32 v8, v8\n\t" \
  "v_mfma_f32_16x16x32_bf16 v[108:111], a[252:255], v[124:127], v[188:191]\n\t" \
  "v_exp_f32 v9, v9\n\t" \
  "v_mfma_f32_16x16x32_bf16 v[108:111], a[248:251], v[120:123], v[108:111]\n\t" \
  "v_exp_f32 v10, v10\n\t" \
  "v_mfma_f32_16x16x32_bf16 v[108:111], a[244:247], v[116:119], v[108:111]\n\t" \
  "s_waitcnt vmcnt(4) lgkmcnt(0)\n\t" \
  "v_exp_f32 v11, v11\n\t" \
  "v_mfma_f32_16x16x32_bf16 v[108:111], a[240:243], v[112:115], v[108:111]\n\t" \
  "s_barrier\n\t" \
  STA("s_memrealtime s[94:95]\n\t") \
  STA("s_memtime s[92:93]\n\t") \
  "v_mfma_f32_16x16x32_bf16 a[0:3], a[128:131], v[128:131], a[0:3]\n\t" \
  "buffer_store_dword v233, v247, s[16:19], 0 offen sc1\n\t" \
  "v_mfma_f32_16x16x32_bf16 a[0:3], a[132:135], v[132:135], a[0:3]\n\t" \
  "v_exp_f32 v12, v12\n\t" \
  "v_mfma_f32_16x16x32_bf16 a[4:7], a[136:139], v[128:131], a[4:7]\n\t" \
  "v_exp_f32 v13, v13\n\t" \
  "v_mfma_f32_16x16x32_bf16 a[4:7], a[140:143], v[132:135], a[4:7]\n\t" \
  "v_exp_f32 v14, v14\n\t" \
  "v_mfma_f32_16x16x32_bf16 a[8:11], a[144:147], v[128:131], a[8:11]\n\t" \
  "v_exp_f32 v15, v15\n\t" \
  "v_mfma_f32_16x16x32_bf16 a[8:11], a[148:151], v[132:135], a[8:11]\n\t" \
  "v_exp_f32 v32, v32\n\t" \
  "v_mfma_f32_16x16x32_bf16 a[12:15], a[152:155], v[128:131], a[12:15]\n\t" \
  "v_exp_f32 v33, v33\n\t" \
  "v_mfma_f32_16x16x32_bf16 a[12:15], a[156:159], v[132:135], a[12:15]\n\t" \
  "v_exp_f32 v34, v34\n\t" \
  "v_mfma_f32_16x16x32_bf16 a[16:19], a[160:163], v[128:131], a[16:19]\n\t" \
  "v_exp_f32 v35, v35\n\t" \
  "v_mfma_f32_16x16x32_bf16 a[16:19], a[164:167], v[132:135], a[16:19]\n\t" \
  "v_exp_f32 v36, v36\n\t" \
  "v_mfma_f32_16x16x32_bf16 a[20:23], a[168:171], v[128:131], a[20:23]\n\t" \
  "v_exp_f32 v37, v37\n\t" \
  "v_mfma_f32_16x16x32_bf16 a[20:23], a[172:175], v[132:135], a[20:23]\n\t" \
  "v_exp_f32 v38, v38\n\t" \
  "v_mfma_f32_16x16x32_bf16 a[24:27], a[176:179], v[128:131], a[24:27]\n\t" \
  STA("v_writelane_b32 v255, s94, 32\n\t") \
  STA("v_writelane_b32 v255, s92, 33\n\t") \
  "v_exp_f32 v39, v39\n\t" \
  "v_mfma_f32_16x16x32_bf16 a[24:27], a[180:183], v[132:135], a[24:27]\n\t" \
  "v_exp_f32 v40, v40\n\t" \
  "v_mfma_f32_16x16x32_bf16 a[28:31], a[184:187], v[128:131], a[28:31]\n\t" \
  "v_exp_f32 v41, v41\n\t" \
  "v_mfma_f32_16x16x32_bf16 a[28:31], a[188:191], v[132:135], a[28:31]\n\t" \
  "v_exp_f32 v42, v42\n\t" \
  "v_smfmac_f32_16x16x64_bf16 v[160:163], v[216:219], v[128:135], v208\n\t" \
  "ds_read_b128 a[192:195], v220 offset:16384\n\t" \
  "v_cvt_pk_bf16_f32 v128, v0, v1\n\t" \
  "v_mfma_f32_16x16x32_bf16 a[32:35], a[128:131], v[136:139], a[32:35]\n\t" \
  "ds_read_b128 a[196:199], v221 offset:16384\n\t" \
  "v_cvt_pk_bf16_f32 v129, v2, v3\n\t" \
  "v_mfma_f32_16x16x32_bf16 a[32:35], a[132:135], v[140:143], a[32:35]\n\t" \
  "s_add_u32 m0, s38, 32768\n\t" \
  "v_exp_f32 v43, v43\n\t" \
  "v_mfma_f32_16x16x32_bf16 a[36:39], a[136:139], v[136:139], a[36:39]\n\t" \
  "s_mov_b32 s96, 28672\n\t" \
  "v_exp_f32 v44, v44\n\t" \
  "v_mfma_f32_16x16x32_bf16 a[36:39], a[140:143], v[140:143], a[36:39]\n\t" \
  "ds_read_b128 a[200:203], v222 offset:16384\n\t" \
  "v_cvt_pk_bf16_f32 v130, v4, v5\n\t" \
  "v_mfma_f32_16x16x32_bf16 a[40:43], a[144:147], v[136:139], a[40:43]\n\t" \
  "v_exp_f32 v45, v45\n\t" \
  "v_mfma_f32_16x16x32_bf16 a[40:43], a[148:151], v[140:143], a[40:43]\n\t" \
  "buffer_load_dwordx4 v232, s[68:71], s96 offen offset:0 lds\n\t" \
  "v_mfma_f32_16x16x32_bf16 a[44:47], a[152:155], v[136:139], a[44:47]\n\t" \
  "ds_read_b128 a[204:207], v223 offset:16384\n\t" \
  "v_cvt_pk_bf16_f32 v131, v6, v7\n\t" \
  "v_mfma_f32_16x16x32_bf16 a[44:47], a[156:159], v[140:143], a[44:47]\n\t" \
  "v_exp_f32 v46, v46\n\t" \
  "v_mfma_f32_16x16x32_bf16 a[48:51], a[160:163], v[136:139], a[48:51]\n\t" \
  "ds_read_b128 a[208:211], v220 offset:20480\n\t" \
  "v_cvt_pk_bf16_f32 v132, v8, v9\n\t" \
  "v_mfma_f32_16x16x32_bf16 a[48:51], a[164:167], v[140:143], a[48:51]\n\t" \
  "v_exp_f32 v47, v47\n\t" \
  "v_mfma_f32_16x16x32_bf16 a[52:55], a[168:171], v[136:139], a[52:55]\n\t" \
  "v_exp_f32 v64, v64\n\t" \
  "v_mfma_f32_16x16x32_bf16 a[52:55], a[172:175], v[140:143], a[52:55]\n\t" \
  "ds_read_b128 a[212:215], v221 offset:20480\n\t" \
  "v_cvt_pk_bf16_f32 v133, v10, v11\n\t" \
  "v_mfma_f32_16x16x32_bf16 a[56:59], a[176:179], v[136:139], a[56:59]\n\t" \
  "v_exp_f32 v65, v65\n\t" \
  "v_mfma_f32_16x16x32_bf16 a[56:59], a[180:183], v[140:143], a[56:59]\n\t" \
  "s_mov_b32 s96, 34816\n\t" \
  "v_exp_f32 v66, v66\n\t" \
  "v_mfma_f32_16x16x32_bf16 a[60:63], a[184:187], v[136:139], a[60:63]\n\t" \
  "ds_read_b128 a[216:219], v222 offset:20480\n\t" \
  "v_cvt_pk_bf16_f32 v134, v12, v13\n\t" \
  "v_mfma_f32_16x16x32_bf16 a[60:63], a[188:191], v[140:143], a[60:63]\n\t" \
  "v_exp_f32 v67, v67\n\t" \
  "v_smfmac_f32_16x16x64_bf16 v[164:167], v[216:219], v[136:143], v208\n\t" \
  "ds_read_b128 a[220:223], v223 offset:20480\n\t" \
  "v_cvt_pk_bf16_f32 v135, v14, v15\n\t" \
  "v_mfma_f32_16x16x32_bf16 a[64:67], a[128:131], v[144:147], a[64:67]\n\t" \
  "buffer_load_dwordx4 v232, s[68:71], s96 offen offset:1024 lds\n\t" \
  "v_mfma_f32_16x16x32_bf16 a[64:67], a[132:135], v[148:151], a[64:67]\n\t" \
  "v_exp_f32 v68, v68\n\t" \
  "v_mfma_f32_16x16x32_bf16 a[68:71], a[136:139], v[144:147], a[68:71]\n\t" \
  "ds_read_b128 a[224:227], v220 offset:24576\n\t" \
  "v_cvt_pk_bf16_f32 v136, v32, v33\n\t" \
  "v_mfma_f32_16x16x32_bf16 a[68:71], a[140:143], v[148:151], a[68:71]\n\t" \
  "v_exp_f32 v69, v69\n\t" \
  "v_mfma_f32_16x16x32_bf16 a[72:75], a[144:147], v[144:147], a[72:75]\n\t" \
  "s_add_u32 m0, s38, 49152\n\t" \
  "v_exp_f32 v70, v70\n\t" \
  "v_mfma_f32_16x16x32_bf16 a[72:75], a[148:151], v[148:151], a[72:75]\n\t" \
  "s_add_u32 s96, s38, 65536\n\t" \
  "ds_read_b128 a[228:231], v221 offset:24576\n\t" \
  "v_cvt_pk_bf16_f32 v137, v34, v35\n\t" \
  "v_mfma_f32_16x16x32_bf16 a[76:79], a[152:155], v[144:147], a[76:79]\n\t" \
  "v_exp_f32 v71, v71\n\t" \
  "v_mfma_f32_16x16x32_bf16 a[76:79], a[156:159], v[148:151], a[76:79]\n\t" \
  "v_exp_f32 v72, v72\n\t" \
  "v_mfma_f32_16x16x32_bf16 a[80:83], a[160:163], v[144:147], a[80:83]\n\t" \
  "buffer_load_dwordx4 v224, s[60:63], s96 offen offset:0 lds\n\t" \
  "v_mfma_f32_16x16x32_bf16 a[80:83], a[164:167], v[148:151], a[80:83]\n\t" \
  "ds_read_b128 a[232:235], v222 offset:24576\n\t" \
  "v_cvt_pk_bf16_f32 v138, v36, v37\n\t" \
  "v_mfma_f32_16x16x32_bf16 a[84:87], a[168:171], v[144:147], a[84:87]\n\t" \
  "v_exp_f32 v73, v73\n\t" \
  "v_mfma_f32_16x16x32_bf16 a[84:87], a[172:175], v[148:151], a[84:87]\n\t" \
  "buffer_load_dwordx4 v225, s[60:63], s96 offen offset:1024 lds\n\t" \
  "v_mfma_f32_16x16x32_bf16 a[88:91], a[176:179], v[144:147], a[88:91]\n\t" \
  "ds_read_b128 a[236:239], v223 offset:24576\n\t" \
  "v_cvt_pk_bf16_f32 v139, v38, v39\n\t" \
  "v_mfma_f32_16x16x32_bf16 a[88:91], a[180:183], v[148:151], a[88:91]\n\t" \
  "ds_read_b128 a[240:243], v220 offset:28672\n\t" \
  "v_cvt_pk_bf16_f32 v140, v40, v41\n\t" \
  "v_mfma_f32_16x16x32_bf16 a[92:95], a[184:187], v[144:147], a[92:95]\n\t" \
  "buffer_load_dwordx4 v226, s[60:63], s96 offen offset:2048 lds\n\t" \
  "v_mfma_f32_16x16x32_bf16 a[92:95], a[188:191], v[148:151], a[92:95]\n\t" \
  "v_exp_f32 v74, v74\n\t" \
  "v_smfmac_f32_16x16x64_bf16 v[168:171], v[216:219], v[144:151], v208\n\t" \
  "ds_read_b128 a[244:247], v221 offset:28672\n\t" \
  "v_cvt_pk_bf16_f32 v141, v42, v43\n\t" \
  "v_mfma_f32_16x16x32_bf16 a[96:99], a[128:131], v[152:155], a[96:99]\n\t" \
  "buffer_load_dwordx4 v227, s[60:63], s96 offen offset:3072 lds\n\t" \
  "v_mfma_f32_16x16x32_bf16 a[96:99], a[132:135], v[156:159], a[96:99]\n\t" \
  "s_add_u32 m0, s38, 147456\n\t" \
  "s_add_u32 s96, s38, 49152\n\t" \
  "v_cvt_pk_bf16_f32 v142, v44, v45\n\t" \
  "v_mfma_f32_16x16x32_bf16 a[100:103], a[136:139], v[152:155], a[100:103]\n\t" \
  "buffer_load_dwordx4 v230, s[64:67], s96 offen offset:0 lds\n\t" \
  "v_mfma_f32_16x16x32_bf16 a[100:103], a[140:143], v[156:159], a[100:103]\n\t" \
  "ds_read_b128 a[248:251], v222 offset:28672\n\t" \
  "v_cvt_pk_bf16_f32 v143, v46, v47\n\t" \
  "v_mfma_f32_16x16x32_bf16 a[104:107], a[144:147], v[152:155], a[104:107]\n\t" \
  "v_exp_f32 v75, v75\n\t" \
  "v_mfma_f32_16x16x32_bf16 a[104:107], a[148:151], v[156:159], a[104:107]\n\t" \
  "buffer_load_dwordx4 v231, s[64:67], s96 offen offset:1024 lds\n\t" \
  "v_mfma_f32_16x16x32_bf16 a[108:111], a[152:155], v[152:155], a[108:111]\n\t" \
  "ds_read_b128 a[252:255], v223 offset:28672\n\t" \
  "v_cvt_pk_bf16_f32 v144, v64, v65\n\t" \
  "v_mfma_f32_16x16x32_bf16 a[108:111], a[156:159], v[156:159], a[108:111]\n\t" \
  "v_exp_f32 v76, v76\n\t" \
  "v_mfma_f32_16x16x32_bf16 a[112:115], a[160:163], v[152:155], a[112:115]\n\t" \
  "buffer_load_dwordx4 v230, s[64:67], s96 offen offset:2048 lds\n\t" \
  "v_mfma_f32_16x16x32_bf16 a[112:115], a[164:167], v[156:159], a[112:115]\n\t" \
  "v_exp_f32 v77, v77\n\t" \
  "v_mfma_f32_16x16x32_bf16 a[116:119], a[168:171], v[152:155], a[116:119]\n\t" \
  "v_exp_f32 v78, v78\n\t" \
  "v_mfma_f32_16x16x32_bf16 a[116:119], a[172:175], v[156:159], a[116:119]\n\t" \
  "buffer_load_dwordx4 v231, s[64:67], s96 offen offset:3072 lds\n\t" \
  "v_mfma_f32_16x16x32_bf16 a[120:123], a[176:179], v[152:155], a[120:123]\n\t" \
  "v_exp_f32 v79, v79\n\t" \
  "v_mfma_f32_16x16x32_bf16 a[120:123], a[180:183], v[156:159], a[120:123]\n\t" \
  "v_exp_f32 v96, v96\n\t" \
  "v_mfma_f32_16x16x32_bf16 a[124:127], a[184:187], v[152:155], a[124:127]\n\t" \
  "v_exp_f32 v97, v97\n\t" \
  "v_mfma_f32_16x16x32_bf16 a[124:127], a[188:191], v[156:159], a[124:127]\n\t" \
  "v_cvt_pk_bf16_f32 v145, v66, v67\n\t" \
  "v_cvt_pk_bf16_f32 v146, v68, v69\n\t" \
  "v_smfmac_f32_16x16x64_bf16 v[172:175], v[216:219], v[152:159], v208\n\t" \
  ".Lhs7:\n\ts_cbranch_execz .Lhs7n\n\t" \
  "s_waitcnt lgkmcnt(12)\n\t" \
  "v_cvt_pk_bf16_f32 v147, v70, v71\n\t" \
  STA("s_memrealtime s[94:95]\n\t") \
  STA("s_memtime s[92:93]\n\t") \
  "v_mfma_f32_16x16x32_bf16 v[0:3], a[192:195], v[16:19], v[176:179]\n\t" \
  "ds_read_b64_tr_b16 a[128:129], v228 offset:0\n\t" \
  "v_cvt_pk_bf16_f32 v148, v72, v73\n\t" \
  "v_mfma_f32_16x16x32_bf16 v[0:3], a[196:199], v[20:23], v[0:3]\n\t" \
  "v_exp_f32 v98, v98\n\t" \
  "v_mfma_f32_16x16x32_bf16 v[0:3], a[200:203], v[24:27], v[0:3]\n\t" \
  "s_add_u32 m0, s38, 32768\n\t" \
  "ds_read_b64_tr_b16 a[130:131], v228 offset:4096\n\t" \
  "v_cvt_pk_bf16_f32 v149, v74, v75\n\t" \
  "v_mfma_f32_16x16x32_bf16 v[0:3], a[204:207], v[28:31], v[0:3]\n\t" \
  "s_waitcnt lgkmcnt(10)\n\t" \
  "v_exp_f32 v99, v99\n\t" \
  "v_mfma_f32_16x16x32_bf16 v[4:7], a[220:223], v[28:31], v[176:179]\n\t" \
  "s_mov_b32 s96, 40960\n\t" \
  "ds_read_b64_tr_b16 a[132:133], v228 offset:8192\n\t" \
  "v_cvt_pk_bf16_f32 v150, v76, v77\n\t" \
  "v_mfma_f32_16x16x32_bf16 v[4:7], a[216:219], v[24:27], v[4:7]\n\t" \
  "v_exp_f32 v100, v100\n\t" \
  "v_mfma_f32_16x16x32_bf16 v[4:7], a[212:215], v[20:23], v[4:7]\n\t" \
  "buffer_load_dwordx4 v232, s[68:71], s96 offen offset:2048 lds\n\t" \
  "v_mfma_f32_16x16x32_bf16 v[4:7], a[208:211], v[16:19], v[4:7]\n\t" \
  "s_waitcnt lgkmcnt(7)\n\t" \
  "ds_read_b64_tr_b16 a[134:135], v228 offset:12288\n\t" \
  "v_cvt_pk_bf16_f32 v151, v78, v79\n\t" \
  "v_mfma_f32_16x16x32_bf16 v[8:11], a[224:227], v[16:19], v[176:179]\n\t" \
  "ds_read_b64_tr_b16 a[136:137], v229 offset:0\n\t" \
  "v_cvt_pk_bf16_f32 v152, v96, v97\n\t" \
  "v_mfma_f32_16x16x32_bf16 v[8:11], a[228:231], v[20:23], v[8:11]\n\t" \
  "ds_read_b64_tr_b16 a[138:139], v229 offset:4096\n\t" \
  "v_cvt_pk_bf16_f32 v153, v98, v99\n\t" \
  "v_mfma_f32_16x16x32_bf16 v[8:11], a[232:235], v[24:27], v[8:11]\n\t" \
  "v_exp_f32 v101, v101\n\t" \
  "v_mfma_f32_16x16x32_bf16 v[8:11], a[236:239], v[28:31], v[8:11]\n\t" \
  "s_waitcnt lgkmcnt(6)\n\t" \
  "ds_read_b64_tr_b16 a[140:141], v229 offset:8192\n\t" \
  "v_cvt_pk_bf16_f32 v154, v100, v101\n\t" \
  "v_mfma_f32_16x16x32_bf16 v[12:15], a[252:255], v[28:31], v[176:179]\n\t" \
  STA("v_writelane_b32 v255, s94, 34\n\t") \
  STA("v_writelane_b32 v255, s92, 35\n\t") \
  "v_exp_f32 v102, v102\n\t" \
  "v_mfma_f32_16x16x32_bf16 v[12:15], a[248:251], v[24:27], v[12:15]\n\t" \
  "v_exp_f32 v103, v103\n\t" \
  "v_mfma_f32_16x16x32_bf16 v[12:15], a[244:247], v[20:23], v[12:15]\n\t" \
  "ds_read_b64_tr_b16 a[142:143], v229 offset:12288\n\t" \
  "v_cvt_pk_bf16_f32 v155, v102, v103\n\t" \
  "v_mfma_f32_16x16x32_bf16 v[12:15], a[240:243], v[16:19], v[12:15]\n\t" \
  "v_exp_f32 v104, v104\n\t" \
  "v_mfma_f32_16x16x32_bf16 v[32:35], a[192:195], v[48:51], v[180:183]\n\t" \
  "s_mov_b32 s96, 47104\n\t" \
  "v_exp_f32 v105, v105\n\t" \
  "v_mfma_f32_16x16x32_bf16 v[32:35], a[196:199], v[52:55], v[32:35]\n\t" \
  "ds_read_b64_tr_b16 a[144:145], v228 offset:256\n\t" \
  "v_cvt_pk_bf16_f32 v156, v104, v105\n\t" \
  "v_mfma_f32_16x16x32_bf16 v[32:35], a[200:203], v[56:59], v[32:35]\n\t" \
  "v_exp_f32 v106, v106\n\t" \
  "v_mfma_f32_16x16x32_bf16 v[32:35], a[204:207], v[60:63], v[32:35]\n\t" \
  "v_exp_f32 v107, v107\n\t" \
  "v_mfma_f32_16x16x32_bf16 v[36:39], a[220:223], v[60:63], v[180:183]\n\t" \
  "buffer_load_dwordx4 v232, s[68:71], s96 offen offset:3072 lds\n\t" \
  "v_mfma_f32_16x16x32_bf16 v[36:39], a[216:219], v[56:59], v[36:39]\n\t" \
  "ds_read_b64_tr_b16 a[146:147], v228 offset:4352\n\t" \
  "v_cvt_pk_bf16_f32 v157, v106, v107\n\t" \
  "v_mfma_f32_16x16x32_bf16 v[36:39], a[212:215], v[52:55], v[36:39]\n\t" \
  "v_exp_f32 v108, v108\n\t" \
  "v_mfma_f32_16x16x32_bf16 v[36:39], a[208:211], v[48:51], v[36:39]\n\t" \
  "ds_read_b64_tr_b16 a[148:149], v228 offset:8448\n\t" \
  "v_mfma_f32_16x16x32_bf16 v[40:43], a[224:227], v[48:51], v[180:183]\n\t" \
  "ds_read_b64_tr_b16 a[150:151], v228 offset:12544\n\t" \
  "v_mfma_f32_16x16x32_bf16 v[40:43], a[228:231], v[52:55], v[40:43]\n\t" \
  "v_exp_f32 v109, v109\n\t" \
  "v_mfma_f32_16x16x32_bf16 v[40:43], a[232:235], v[56:59], v[40:43]\n\t" \
  "ds_read_b64_tr_b16 a[152:153], v229 offset:256\n\t" \
  "v_cvt_pk_bf16_f32 v158, v108, v109\n\t" \
  "v_mfma_f32_16x16x32_bf16 v[40:43], a[236:239], v[60:63], v[40:43]\n\t" \
  "v_exp_f32 v110, v110\n\t" \
  "v_mfma_f32_16x16x32_bf16 v[44:47], a[252:255], v[60:63], v[180:183]\n\t" \
  "ds_read_b64_tr_b16 a[154:155], v229 offset:4352\n\t" \
  "v_mfma_f32_16x16x32_bf16 v[44:47], a[248:251], v[56:59], v[44:47]\n\t" \
  "v_exp_f32 v111, v111\n\t" \
  "v_mfma_f32_16x16x32_bf16 v[44:47], a[244:247], v[52:55], v[44:47]\n\t" \
  "s_add_u32 m0, s38, 114688\n\t" \
  "ds_read_b64_tr_b16 a[156:157], v229 offset:8448\n\t" \
  "v_cvt_pk_bf16_f32 v159, v110, v111\n\t" \
  "v_mfma_f32_16x16x32_bf16 v[44:47], a[240:243], v[48:51], v[44:47]\n\t" \
  "ds_read_b64_tr_b16 a[158:159], v229 offset:12544\n\t" \
  "s_mov_b32 s96, 0\n\t" \
  "v_mfma_f32_16x16x32_bf16 v[64:67], a[192:195], v[80:83], v[184:187]\n\t" \
  "ds_read_b64_tr_b16 a[160:161], v228 offset:512\n\t" \
  "v_mfma_f32_16x16x32_bf16 v[64:67], a[196:199], v[84:87], v[64:67]\n\t" \
  "v_exp_f32 v0, v0\n\t" \
  "v_mfma_f32_16x16x32_bf16 v[64:67], a[200:203], v[88:91], v[64:67]\n\t" \
  "buffer_load_dwordx4 v232, s[72:75], s96 offen offset:0 lds\n\t" \
  "v_mfma_f32_16x16x32_bf16 v[64:67], a[204:207], v[92:95], v[64:67]\n\t" \
  "ds_read_b64_tr_b16 a[162:163], v228 offset:4608\n\t" \
  "v_mfma_f32_16x16x32_bf16 v[68:71], a[220:223], v[92:95], v[184:187]\n\t" \
  "ds_read_b64_tr_b16 a[164:165], v228 offset:8704\n\t" \
  "v_mfma_f32_16x16x32_bf16 v[68:71], a[216:219], v[88:91], v[68:71]\n\t" \
  "ds_read_b64_tr_b16 a[166:167], v228 offset:12800\n\t" \
  "v_mfma_f32_16x16x32_bf16 v[68:71], a[212:215], v[84:87], v[68:71]\n\t" \
  "v_exp_f32 v1, v1\n\t" \
  "v_mfma_f32_16x16x32_bf16 v[68:71], a[208:211], v[80:83], v[68:71]\n\t" \
  "ds_read_b64_tr_b16 a[168:169], v229 offset:512\n\t" \
  "v_mfma_f32_16x16x32_bf16 v[72:75], a[224:227], v[80:83], v[184:187]\n\t" \
  "v_exp_f32 v2, v2\n\t" \
  "v_mfma_f32_16x16x32_bf16 v[72:75], a[228:231], v[84:87], v[72:75]\n\t" \
  "ds_read_b64_tr_b16 a[170:171], v229 offset:4608\n\t" \
  "v_mfma_f32_16x16x32_bf16 v[72:75], a[232:235], v[88:91], v[72:75]\n\t" \
  "ds_read_b64_tr_b16 a[172:173], v229 offset:8704\n\t" \
  "v_mfma_f32_16x16x32_bf16 v[72:75], a[236:239], v[92:95], v[72:75]\n\t" \
  "ds_read_b64_tr_b16 a[174:175], v229 offset:12800\n\t" \
  "buffer_load_dword v246, v247, s[16:19], 0 offen sc1\n\t" \
  "v_mfma_f32_16x16x32_bf16 v[76:79], a[252:255], v[92:95], v[184:187]\n\t" \
  "s_mov_b32 s96, 6144\n\t" \
  "v_exp_f32 v3, v3\n\t" \
  "v_mfma_f32_16x16x32_bf16 v[76:79], a[248:251], v[88:91], v[76:79]\n\t" \
  "v_exp_f32 v4, v4\n\t" \
  "v_mfma_f32_16x16x32_bf16 v[76:79], a[244:247], v[84:87], v[76:79]\n\t" \
  "ds_read_b64_tr_b16 a[176:177], v228 offset:768\n\t" \
  "v_mfma_f32_16x16x32_bf16 v[76:79], a[240:243], v[80:83], v[76:79]\n\t" \
  "ds_read_b64_tr_b16 a[178:179], v228 offset:4864\n\t" \
  "v_mfma_f32_16x16x32_bf16 v[96:99], a[192:195], v[112:115], v[188:191]\n\t" \
  "buffer_load_dwordx4 v232, s[72:75], s96 offen offset:1024 lds\n\t" \
  "v_mfma_f32_16x16x32_bf16 v[96:99], a[196:199], v[116:119], v[96:99]\n\t" \
  "ds_read_b64_tr_b16 a[180:181], v228 offset:8960\n\t" \
  "v_mfma_f32_16x16x32_bf16 v[96:99], a[200:203], v[120:123], v[96:99]\n\t" \
  "v_exp_f32 v5, v5\n\t" \
  "v_mfma_f32_16x16x32_bf16 v[96:99], a[204:207], v[124:127], v[96:99]\n\t" \
  "ds_read_b64_tr_b16 a[182:183], v228 offset:13056\n\t" \
  "v_mfma_f32_16x16x32_bf16 v[100:103], a[220:223], v[124:127], v[188:191]\n\t" \
  "ds_read_b64_tr_b16 a[184:185], v229 offset:768\n\t" \
  "v_mfma_f32_16x16x32_bf16 v[100:103], a[216:219], v[120:123], v[100:103]\n\t" \
  "v_exp_f32 v6, v6\n\t" \
  "v_mfma_f32_16x16x32_bf16 v[100:103], a[212:215], v[116:119], v[100:103]\n\t" \
  "ds_read_b64_tr_b16 a[186:187], v229 offset:4864\n\t" \
  "v_mfma_f32_16x16x32_bf16 v[100:103], a[208:211], v[112:115], v[100:103]\n\t" \
  "ds_read_b64_tr_b16 a[188:189], v229 offset:8960\n\t" \
  "v_mfma_f32_16x16x32_bf16 v[104:107], a[224:227], v[112:115], v[188:191]\n\t" \
  "v_exp_f32 v7, v7\n\t" \
  "v_mfma_f32_16x16x32_bf16 v[104:107], a[228:231], v[116:119], v[104:107]\n\t" \
  "ds_read_b64_tr_b16 a[190:191], v229 offset:13056\n\t" \
  "v_mfma_f32_16x16x32_bf16 v[104:107], a[232:235], v[120:123], v[104:107]\n\t" \
  "v_exp_f32 v8, v8\n\t" \
  "v_mfma_f32_16x16x32_bf16 v[104:107], a[236:239], v[124:127], v[104:107]\n\t" \
  "v_exp_f32 v9, v9\n\t" \
  "v_mfma_f32_16x16x32_bf16 v[108:111], a[252:255], v[124:127], v[188:191]\n\t" \
  "v_exp_f32 v10, v10\n\t" \
  "v_mfma_f32_16x16x32_bf16 v[108:111], a[248:251], v[120:123], v[108:111]\n\t" \
  "v_exp_f32 v11, v11\n\t" \
  "v_mfma_f32_16x16x32_bf16 v[108:111], a[244:247], v[116:119], v[108:111]\n\t" \
  "s_waitcnt vmcnt(5) lgkmcnt(0)\n\t" \
  "v_exp_f32 v12, v12\n\t" \
  "v_mfma_f32_16x16x32_bf16 v[108:111], a[240:243], v[112:115], v[108:111]\n\t" \
  "s_barrier\n\t" \
  STA("s_memrealtime s[94:95]\n\t") \
  STA("s_memtime s[92:93]\n\t") \
  "v_mfma_f32_16x16x32_bf16 a[0:3], a[128:131], v[128:131], a[0:3]\n\t" \
  "v_exp_f32 v13, v13\n\t" \
  "v_mfma_f32_16x16x32_bf16 a[0:3], a[132:135], v[132:135], a[0:3]\n\t" \
  "v_exp_f32 v14, v14\n\t" \
  "v_mfma_f32_16x16x32_bf16 a[4:7], a[136:139], v[128:131], a[4:7]\n\t" \
  "v_exp_f32 v15, v15\n\t" \
  "v_mfma_f32_16x16x32_bf16 a[4:7], a[140:143], v[132:135], a[4:7]\n\t" \
  "v_exp_f32 v32, v32\n\t" \
  "v_mfma_f32_16x16x32_bf16 a[8:11], a[144:147], v[128:131], a[8:11]\n\t" \
  "v_exp_f32 v33, v33\n\t" \
  "v_mfma_f32_16x16x32_bf16 a[8:11], a[148:151], v[132:135], a[8:11]\n\t" \
  "v_exp_f32 v34, v34\n\t" \
  "v_mfma_f32_16x16x32_bf16 a[12:15], a[152:155], v[128:131], a[12:15]\n\t" \
  "s_getreg_b32 s98, hwreg(HW_REG_IB_STS, 0, 4)\n\t" \
  "s_cmp_lg_u32 s98, 0\n\t" \
  "s_addc_u32 s54, s54, 0\n\t" \
  "v_mfma_f32_16x16x32_bf16 a[12:15], a[156:159], v[132:135], a[12:15]\n\t" \
  "v_exp_f32 v35, v35\n\t" \
  "v_mfma_f32_16x16x32_bf16 a[16:19], a[160:163], v[128:131], a[16:19]\n\t" \
  "s_getreg_b32 s98, hwreg(HW_REG_IB_STS, 0, 4)\n\t" \
  "s_cmp_lg_u32 s98, 0\n\t" \
  "s_addc_u32 s54, s54, 0\n\t" \
  "v_mfma_f32_16x16x32_bf16 a[16:19], a[164:167], v[132:135], a[16:19]\n\t" \
  "v_exp_f32 v36, v36\n\t" \
  "v_mfma_f32_16x16x32_bf16 a[20:23], a[168:171], v[128:131], a[20:23]\n\t" \
  "s_getreg_b32 s98, hwreg(HW_REG_IB_STS, 0, 4)\n\t" \
  "s_cmp_lg_u32 s98, 0\n\t" \
  "s_addc_u32 s54, s54, 0\n\t" \
  "v_mfma_f32_16x16x32_bf16 a[20:23], a[172:175], v[132:135], a[20:23]\n\t" \
  "v_exp_f32 v37, v37\n\t" \
  "v_mfma_f32_16x16x32_bf16 a[24:27], a[176:179], v[128:131], a[24:27]\n\t" \
  STA("v_writelane_b32 v255, s94, 36\n\t") \
  STA("v_writelane_b32 v255, s92, 37\n\t") \
  "s_getreg_b32 s98, hwreg(HW_REG_IB_STS, 0, 4)\n\t" \
  "s_cmp_lg_u32 s98, 0\n\t" \
  "s_addc_u32 s54, s54, 0\n\t" \
  "v_mfma_f32_16x16x32_bf16 a[24:27], a[180:183], v[132:135], a[24:27]\n\t" \
  "v_exp_f32 v38, v38\n\t" \
  "v_mfma_f32_16x16x32_bf16 a[28:31], a[184:187], v[128:131], a[28:31]\n\t" \
  "v_exp_f32 v39, v39\n\t" \
  "v_mfma_f32_16x16x32_bf16 a[28:31], a[188:191], v[132:135], a[28:31]\n\t" \
  "v_exp_f32 v40, v40\n\t" \
  "v_smfmac_f32_16x16x64_bf16 v[160:163], v[216:219], v[128:135], v208\n\t" \
  "ds_read_b128 a[192:195], v220 offset:0\n\t" \
  "v_cvt_pk_bf16_f32 v128, v0, v1\n\t" \
  "v_mfma_f32_16x16x32_bf16 a[32:35], a[128:131], v[136:139], a[32:35]\n\t" \
  "ds_read_b128 a[196:199], v221 offset:0\n\t" \
  "v_cvt_pk_bf16_f32 v129, v2, v3\n\t" \
  "v_mfma_f32_16x16x32_bf16 a[32:35], a[132:135], v[140:143], a[32:35]\n\t" \
  "s_add_u32 m0, s38, 114688\n\t" \
  "v_exp_f32 v41, v41\n\t" \
  "v_mfma_f32_16x16x32_bf16 a[36:39], a[136:139], v[136:139], a[36:39]\n\t" \
  "s_mov_b32 s96, 12288\n\t" \
  "v_exp_f32 v42, v42\n\t" \
  "v_mfma_f32_16x16x32_bf16 a[36:39], a[140:143], v[140:143], a[36:39]\n\t" \
  "ds_read_b128 a[200:203], v222 offset:0\n\t" \
  "v_cvt_pk_bf16_f32 v130, v4, v5\n\t" \
  "v_mfma_f32_16x16x32_bf16 a[40:43], a[144:147], v[136:139], a[40:43]\n\t" \
  "v_exp_f32 v43, v43\n\t" \
  "v_mfma_f32_16x16x32_bf16 a[40:43], a[148:151], v[140:143], a[40:43]\n\t" \
  "buffer_load_dwordx4 v232, s[72:75], s96 offen offset:2048 lds\n\t" \
  "v_mfma_f32_16x16x32_bf16 a[44:47], a[152:155], v[136:139], a[44:47]\n\t" \
  "ds_read_b128 a[204:207], v223 offset:0\n\t" \
  "v_cvt_pk_bf16_f32 v131, v6, v7\n\t" \
  "v_mfma_f32_16x16x32_bf16 a[44:47], a[156:159], v[140:143], a[44:47]\n\t" \
  "v_exp_f32 v44, v44\n\t" \
  "v_mfma_f32_16x16x32_bf16 a[48:51], a[160:163], v[136:139], a[48:51]\n\t" \
  "ds_read_b128 a[208:211], v220 offset:4096\n\t" \
  "v_cvt_pk_bf16_f32 v132, v8, v9\n\t" \
  "v_mfma_f32_16x16x32_bf16 a[48:51], a[164:167], v[140:143], a[48:51]\n\t" \
  STS("v_writelane_b32 v255, s54, 58\n\t") \
  "s_cmp_ge_u32 s54, 2\n\t" \
  "s_cselect_b32 s98, 0x4000, 0\n\t" \
  "s_or_b32 s98, s98, s36\n\t" \
  "v_mfma_f32_16x16x32_bf16 a[52:55], a[168:171], v[136:139], a[52:55]\n\t" \
  "s_cmp_ge_u32 s54, 3\n\t" \
  "s_cselect_b32 s54, 2.0, 0\n\t" \
  "s_or_b32 s98, s98, s54\n\t" \
  "v_mfma_f32_16x16x32_bf16 a[52:55], a[172:175], v[140:143], a[52:55]\n\t" \
  "ds_read_b128 a[212:215], v221 offset:4096\n\t" \
  "v_mov_b32 v209, s98\n\t" \
  "v_mfma_f32_16x16x32_bf16 a[56:59], a[176:179], v[136:139], a[56:59]\n\t" \
  "v_exp_f32 v45, v45\n\t" \
  "v_mfma_f32_16x16x32_bf16 a[56:59], a[180:183], v[140:143], a[56:59]\n\t" \
  "s_mov_b32 s96, 18432\n\t" \
  "v_exp_f32 v46, v46\n\t" \
  "v_mfma_f32_16x16x32_bf16 a[60:63], a[184:187], v[136:139], a[60:63]\n\t" \
  "ds_read_b128 a[216:219], v222 offset:4096\n\t" \
  "v_cvt_pk_bf16_f32 v133, v10, v11\n\t" \
  "v_mfma_f32_16x16x32_bf16 a[60:63], a[188:191], v[140:143], a[60:63]\n\t" \
  "buffer_store_dword v209, v250, s[16:19], 0 offen sc1\n\t" \
  "ds_read_b128 a[220:223], v223 offset:4096\n\t" \
  "v_smfmac_f32_16x16x64_bf16 v[164:167], v[216:219], v[136:143], v208\n\t" \
  "ds_read_b128 a[224:227], v220 offset:8192\n\t" \
  "v_cvt_pk_bf16_f32 v134, v12, v13\n\t" \
  "v_mfma_f32_16x16x32_bf16 a[64:67], a[128:131], v[144:147], a[64:67]\n\t" \
  "buffer_load_dwordx4 v232, s[72:75], s96 offen offset:3072 lds\n\t" \
  "v_mfma_f32_16x16x32_bf16 a[64:67], a[132:135], v[148:151], a[64:67]\n\t" \
  "v_exp_f32 v47, v47\n\t" \
  "v_mfma_f32_16x16x32_bf16 a[68:71], a[136:139], v[144:147], a[68:71]\n\t" \
  "v_exp_f32 v64, v64\n\t" \
  "v_mfma_f32_16x16x32_bf16 a[68:71], a[140:143], v[148:151], a[68:71]\n\t" \
  "v_exp_f32 v65, v65\n\t" \
  "v_mfma_f32_16x16x32_bf16 a[72:75], a[144:147], v[144:147], a[72:75]\n\t" \
  "s_add_u32 m0, s38, 65536\n\t" \
  "v_exp_f32 v66, v66\n\t" \
  "v_mfma_f32_16x16x32_bf16 a[72:75], a[148:151], v[148:151], a[72:75]\n\t" \
  "s_add_u32 s96, s38, 81920\n\t" \
  "ds_read_b128 a[228:231], v221 offset:8192\n\t" \
  "v_cvt_pk_bf16_f32 v135, v14, v15\n\t" \
  "v_mfma_f32_16x16x32_bf16 a[76:79], a[152:155], v[144:147], a[76:79]\n\t" \
  "v_exp_f32 v67, v67\n\t" \
  "v_mfma_f32_16x16x32_bf16 a[76:79], a[156:159], v[148:151], a[76:79]\n\t" \
  "v_exp_f32 v68, v68\n\t" \
  "v_mfma_f32_16x16x32_bf16 a[80:83], a[160:163], v[144:147], a[80:83]\n\t" \
  "buffer_load_dwordx4 v224, s[60:63], s96 offen offset:0 lds\n\t" \
  "v_mfma_f32_16x16x32_bf16 a[80:83], a[164:167], v[148:151], a[80:83]\n\t" \
  "ds_read_b128 a[232:235], v222 offset:8192\n\t" \
  "v_cvt_pk_bf16_f32 v136, v32, v33\n\t" \
  "v_mfma_f32_16x16x32_bf16 a[84:87], a[168:171], v[144:147], a[84:87]\n\t" \
  "v_exp_f32 v69, v69\n\t" \
  "v_mfma_f32_16x16x32_bf16 a[84:87], a[172:175], v[148:151], a[84:87]\n\t" \
  "buffer_load_dwordx4 v225, s[60:63], s96 offen offset:1024 lds\n\t" \
  "v_mfma_f32_16x16x32_bf16 a[88:91], a[176:179], v[144:147], a[88:91]\n\t" \
  "ds_read_b128 a[236:239], v223 offset:8192\n\t" \
  "v_cvt_pk_bf16_f32 v137, v34, v35\n\t" \
  "v_mfma_f32_16x16x32_bf16 a[88:91], a[180:183], v[148:151], a[88:91]\n\t" \
  "ds_read_b128 a[240:243], v220 offset:12288\n\t" \
  "v_cvt_pk_bf16_f32 v138, v36, v37\n\t" \
  "v_mfma_f32_16x16x32_bf16 a[92:95], a[184:187], v[144:147], a[92:95]\n\t" \
  "buffer_load_dwordx4 v226, s[60:63], s96 offen offset:2048 lds\n\t" \
  "v_mfma_f32_16x16x32_bf16 a[92:95], a[188:191], v[148:151], a[92:95]\n\t" \
  "v_exp_f32 v70, v70\n\t" \
  "v_smfmac_f32_16x16x64_bf16 v[168:171], v[216:219], v[144:151], v208\n\t" \
  "ds_read_b128 a[244:247], v221 offset:12288\n\t" \
  "v_cvt_pk_bf16_f32 v139, v38, v39\n\t" \
  "v_mfma_f32_16x16x32_bf16 a[96:99], a[128:131], v[152:155], a[96:99]\n\t" \
  "buffer_load_dwordx4 v227, s[60:63], s96 offen offset:3072 lds\n\t" \
  "v_mfma_f32_16x16x32_bf16 a[96:99], a[132:135], v[156:159], a[96:99]\n\t" \
  "s_add_u32 m0, s38, 81920\n\t" \
  "v_cvt_pk_bf16_f32 v140, v40, v41\n\t" \
  "s_add_u32 s96, s38, 65536\n\t" \
  "v_mfma_f32_16x16x32_bf16 a[100:103], a[136:139], v[152:155], a[100:103]\n\t" \
  "buffer_load_dwordx4 v230, s[64:67], s96 offen offset:0 lds\n\t" \
  "v_mfma_f32_16x16x32_bf16 a[100:103], a[140:143], v[156:159], a[100:103]\n\t" \
  "ds_read_b128 a[248:251], v222 offset:12288\n\t" \
  "v_cvt_pk_bf16_f32 v141, v42, v43\n\t" \
  "v_mfma_f32_16x16x32_bf16 a[104:107], a[144:147], v[152:155], a[104:107]\n\t" \
  "v_exp_f32 v71, v71\n\t" \
  "v_mfma_f32_16x16x32_bf16 a[104:107], a[148:151], v[156:159], a[104:107]\n\t" \
  "buffer_load_dwordx4 v231, s[64:67], s96 offen offset:1024 lds\n\t" \
  "v_mfma_f32_16x16x32_bf16 a[108:111], a[152:155], v[152:155], a[108:111]\n\t" \
  "ds_read_b128 a[252:255], v223 offset:12288\n\t" \
  "v_cvt_pk_bf16_f32 v142, v44, v45\n\t" \
  "v_mfma_f32_16x16x32_bf16 a[108:111], a[156:159], v[156:159], a[108:111]\n\t" \
  "v_exp_f32 v72, v72\n\t" \
  "v_mfma_f32_16x16x32_bf16 a[112:115], a[160:163], v[152:155], a[112:115]\n\t" \
  "buffer_load_dwordx4 v230, s[64:67], s96 offen offset:2048 lds\n\t" \
  "v_mfma_f32_16x16x32_bf16 a[112:115], a[164:167], v[156:159], a[112:115]\n\t" \
  "v_exp_f32 v73, v73\n\t" \
  "v_mfma_f32_16x16x32_bf16 a[116:119], a[168:171], v[152:155], a[116:119]\n\t" \
  "v_exp_f32 v74, v74\n\t" \
  "v_mfma_f32_16x16x32_bf16 a[116:119], a[172:175], v[156:159], a[116:119]\n\t" \
  "buffer_load_dwordx4 v231, s[64:67], s96 offen offset:3072 lds\n\t" \
  "v_mfma_f32_16x16x32_bf16 a[120:123], a[176:179], v[152:155], a[120:123]\n\t" \
  "v_exp_f32 v75, v75\n\t" \
  "v_mfma_f32_16x16x32_bf16 a[120:123], a[180:183], v[156:159], a[120:123]\n\t" \
  "v_exp_f32 v76, v76\n\t" \
  "v_mfma_f32_16x16x32_bf16 a[124:127], a[184:187], v[152:155], a[124:127]\n\t" \
  "v_exp_f32 v77, v77\n\t" \
  "v_mfma_f32_16x16x32_bf16 a[124:127], a[188:191], v[156:159], a[124:127]\n\t" \
  "v_exp_f32 v78, v78\n\t" \
  "v_smfmac_f32_16x16x64_bf16 v[172:175], v[216:219], v[152:159], v208\n\t" \
  "s_waitcnt lgkmcnt(12)\n\t" \
  "v_cvt_pk_bf16_f32 v143, v46, v47\n\t" \
  STA("s_memrealtime s[94:95]\n\t") \
  STA("s_memtime s[92:93]\n\t") \
  "v_mfma_f32_16x16x32_bf16 v[0:3], a[192:195], v[16:19], v[176:179]\n\t" \
  "ds_read_b64_tr_b16 a[128:129], v210 offset:0\n\t" \
  "v_cvt_pk_bf16_f32 v144, v64, v65\n\t" \
  "v_mfma_f32_16x16x32_bf16 v[0:3], a[196:199], v[20:23], v[0:3]\n\t" \
  "v_exp_f32 v79, v79\n\t" \
  "v_mfma_f32_16x16x32_bf16 v[0:3], a[200:203], v[24:27], v[0:3]\n\t" \
  "s_add_u32 m0, s38, 131072\n\t" \
  "ds_read_b64_tr_b16 a[130:131], v210 offset:4096\n\t" \
  "v_cvt_pk_bf16_f32 v145, v66, v67\n\t" \
  "v_mfma_f32_16x16x32_bf16 v[0:3], a[204:207], v[28:31], v[0:3]\n\t" \
  "s_waitcnt lgkmcnt(10)\n\t" \
  "v_exp_f32 v96, v96\n\t" \
  "v_mfma_f32_16x16x32_bf16 v[4:7], a[220:223], v[28:31], v[176:179]\n\t" \
  "s_mov_b32 s96, 28672\n\t" \
  "ds_read_b64_tr_b16 a[132:133], v210 offset:8192\n\t" \
  "v_cvt_pk_bf16_f32 v146, v68, v69\n\t" \
  "v_mfma_f32_16x16x32_bf16 v[4:7], a[216:219], v[24:27], v[4:7]\n\t" \
  "v_exp_f32 v97, v97\n\t" \
  "v_mfma_f32_16x16x32_bf16 v[4:7], a[212:215], v[20:23], v[4:7]\n\t" \
  "buffer_load_dwordx4 v232, s[72:75], s96 offen offset:0 lds\n\t" \
  "v_mfma_f32_16x16x32_bf16 v[4:7], a[208:211], v[16:19], v[4:7]\n\t" \
  "s_waitcnt lgkmcnt(7)\n\t" \
  "ds_read_b64_tr_b16 a[134:135], v210 offset:12288\n\t" \
  "v_cvt_pk_bf16_f32 v147, v70, v71\n\t" \
  "v_mfma_f32_16x16x32_bf16 v[8:11], a[224:227], v[16:19], v[176:179]\n\t" \
  "ds_read_b64_tr_b16 a[136:137], v211 offset:0\n\t" \
  "v_cvt_pk_bf16_f32 v148, v72, v73\n\t" \
  "v_mfma_f32_16x16x32_bf16 v[8:11], a[228:231], v[20:23], v[8:11]\n\t" \
  "ds_read_b64_tr_b16 a[138:139], v211 offset:4096\n\t" \
  "v_cvt_pk_bf16_f32 v149, v74, v75\n\t" \
  "v_mfma_f32_16x16x32_bf16 v[8:11], a[232:235], v[24:27], v[8:11]\n\t" \
  "v_exp_f32 v98, v98\n\t" \
  "v_mfma_f32_16x16x32_bf16 v[8:11], a[236:239], v[28:31], v[8:11]\n\t" \
  "s_waitcnt lgkmcnt(6)\n\t" \
  "ds_read_b64_tr_b16 a[140:141], v211 offset:8192\n\t" \
  "v_cvt_pk_bf16_f32 v150, v76, v77\n\t" \
  "v_mfma_f32_16x16x32_bf16 v[12:15], a[252:255], v[28:31], v[176:179]\n\t" \
  STA("v_writelane_b32 v255, s94, 38\n\t") \
  STA("v_writelane_b32 v255, s92, 39\n\t") \
  "v_exp_f32 v99, v99\n\t" \
  "v_mfma_f32_16x16x32_bf16 v[12:15], a[248:251], v[24:27], v[12:15]\n\t" \
  "ds_read_b64_tr_b16 a[142:143], v211 offset:12288\n\t" \
  "v_cvt_pk_bf16_f32 v151, v78, v79\n\t" \
  "v_mfma_f32_16x16x32_bf16 v[12:15], a[244:247], v[20:23], v[12:15]\n\t" \
  "ds_read_b64_tr_b16 a[144:145], v210 offset:256\n\t" \
  "v_cvt_pk_bf16_f32 v152, v96, v97\n\t" \
  "v_mfma_f32_16x16x32_bf16 v[12:15], a[240:243], v[16:19], v[12:15]\n\t" \
  "v_exp_f32 v100, v100\n\t" \
  "v_mfma_f32_16x16x32_bf16 v[32:35], a[192:195], v[48:51], v[180:183]\n\t" \
  "s_mov_b32 s96, 34816\n\t" \
  "ds_read_b64_tr_b16 a[146:147], v210 offset:4352\n\t" \
  "v_cvt_pk_bf16_f32 v153, v98, v99\n\t" \
  "v_mfma_f32_16x16x32_bf16 v[32:35], a[196:199], v[52:55], v[32:35]\n\t" \
  "v_exp_f32 v101, v101\n\t" \
  "v_mfma_f32_16x16x32_bf16 v[32:35], a[200:203], v[56:59], v[32:35]\n\t" \
  "ds_read_b64_tr_b16 a[148:149], v210 offset:8448\n\t" \
  "v_cvt_pk_bf16_f32 v154, v100, v101\n\t" \
  "v_mfma_f32_16x16x32_bf16 v[32:35], a[204:207], v[60:63], v[32:35]\n\t" \
  "v_exp_f32 v102, v102\n\t" \
  "v_mfma_f32_16x16x32_bf16 v[36:39], a[220:223], v[60:63], v[180:183]\n\t" \
  "buffer_load_dwordx4 v232, s[72:75], s96 offen offset:1024 lds\n\t" \
  "v_mfma_f32_16x16x32_bf16 v[36:39], a[216:219], v[56:59], v[36:39]\n\t" \
  "v_exp_f32 v103, v103\n\t" \
  "v_mfma_f32_16x16x32_bf16 v[36:39], a[212:215], v[52:55], v[36:39]\n\t" \
  "ds_read_b64_tr_b16 a[150:151], v210 offset:12544\n\t" \
  "v_cvt_pk_bf16_f32 v155, v102, v103\n\t" \
  "v_mfma_f32_16x16x32_bf16 v[36:39], a[208:211], v[48:51], v[36:39]\n\t" \
  "v_exp_f32 v104, v104\n\t" \
  "v_mfma_f32_16x16x32_bf16 v[40:43], a[224:227], v[48:51], v[180:183]\n\t" \
  "v_exp_f32 v105, v105\n\t" \
  "v_mfma_f32_16x16x32_bf16 v[40:43], a[228:231], v[52:55], v[40:43]\n\t" \
  "ds_read_b64_tr_b16 a[152:153], v211 offset:256\n\t" \
  "v_cvt_pk_bf16_f32 v156, v104, v105\n\t" \
  "v_mfma_f32_16x16x32_bf16 v[40:43], a[232:235], v[56:59], v[40:43]\n\t" \
  "v_exp_f32 v106, v106\n\t" \
  "v_mfma_f32_16x16x32_bf16 v[40:43], a[236:239], v[60:63], v[40:43]\n\t" \
  "v_exp_f32 v107, v107\n\t" \
  "v_mfma_f32_16x16x32_bf16 v[44:47], a[252:255], v[60:63], v[180:183]\n\t" \
  "ds_read_b64_tr_b16 a[154:155], v211 offset:4352\n\t" \
  "v_cvt_pk_bf16_f32 v157, v106, v107\n\t" \
  "v_mfma_f32_16x16x32_bf16 v[44:47], a[248:251], v[56:59], v[44:47]\n\t" \
  "v_exp_f32 v108, v108\n\t" \
  "v_mfma_f32_16x16x32_bf16 v[44:47], a[244:247], v[52:55], v[44:47]\n\t" \
  "ds_read_b64_tr_b16 a[156:157], v211 offset:8448\n\t" \
  "s_mov_b32 s96, 40960\n\t" \
  "v_mfma_f32_16x16x32_bf16 v[44:47], a[240:243], v[48:51], v[44:47]\n\t" \
  "v_exp_f32 v109, v109\n\t" \
  "v_mfma_f32_16x16x32_bf16 v[64:67], a[192:195], v[80:83], v[184:187]\n\t" \
  "ds_read_b64_tr_b16 a[158:159], v211 offset:12544\n\t" \
  "v_cvt_pk_bf16_f32 v158, v108, v109\n\t" \
  "v_mfma_f32_16x16x32_bf16 v[64:67], a[196:199], v[84:87], v[64:67]\n\t" \
  "v_exp_f32 v110, v110\n\t" \
  "v_mfma_f32_16x16x32_bf16 v[64:67], a[200:203], v[88:91], v[64:67]\n\t" \
  "buffer_load_dwordx4 v232, s[72:75], s96 offen offset:2048 lds\n\t" \
  "v_mfma_f32_16x16x32_bf16 v[64:67], a[204:207], v[92:95], v[64:67]\n\t" \
  "ds_read_b64_tr_b16 a[160:161], v210 offset:512\n\t" \
  "v_mfma_f32_16x16x32_bf16 v[68:71], a[220:223], v[92:95], v[184:187]\n\t" \
  "ds_read_b64_tr_b16 a[162:163], v210 offset:4608\n\t" \
  "v_mfma_f32_16x16x32_bf16 v[68:71], a[216:219], v[88:91], v[68:71]\n\t" \
  "v_exp_f32 v111, v111\n\t" \
  "v_mfma_f32_16x16x32_bf16 v[68:71], a[212:215], v[84:87], v[68:71]\n\t" \
  "ds_read_b64_tr_b16 a[164:165], v210 offset:8704\n\t" \
  "v_cvt_pk_bf16_f32 v159, v110, v111\n\t" \
  "v_mfma_f32_16x16x32_bf16 v[68:71], a[208:211], v[80:83], v[68:71]\n\t" \
  "ds_read_b64_tr_b16 a[166:167], v210 offset:12800\n\t" \
  "v_mfma_f32_16x16x32_bf16 v[72:75], a[224:227], v[80:83], v[184:187]\n\t" \
  "ds_read_b64_tr_b16 a[168:169], v211 offset:512\n\t" \
  "v_mfma_f32_16x16x32_bf16 v[72:75], a[228:231], v[84:87], v[72:75]\n\t" \
  "ds_read_b64_tr_b16 a[170:171], v211 offset:4608\n\t" \
  "v_mfma_f32_16x16x32_bf16 v[72:75], a[232:235], v[88:91], v[72:75]\n\t" \
  "ds_read_b64_tr_b16 a[172:173], v211 offset:8704\n\t" \
  "v_mfma_f32_16x16x32_bf16 v[72:75], a[236:239], v[92:95], v[72:75]\n\t" \
  "ds_read_b64_tr_b16 a[174:175], v211 offset:12800\n\t" \
  "v_mfma_f32_16x16x32_bf16 v[76:79], a[252:255], v[92:95], v[184:187]\n\t" \
  "s_mov_b32 s96, 47104\n\t" \
  "v_exp_f32 v0, v0\n\t" \
  "v_mfma_f32_16x16x32_bf16 v[76:79], a[248:251], v[88:91], v[76:79]\n\t" \
  "ds_read_b64_tr_b16 a[176:177], v210 offset:768\n\t" \
  "v_mfma_f32_16x16x32_bf16 v[76:79], a[244:247], v[84:87], v[76:79]\n\t" \
  "v_exp_f32 v1, v1\n\t" \
  "v_mfma_f32_16x16x32_bf16 v[76:79], a[240:243], v[80:83], v[76:79]\n\t" \
  "ds_read_b64_tr_b16 a[178:179], v210 offset:4864\n\t" \
  "v_mfma_f32_16x16x32_bf16 v[96:99], a[192:195], v[112:115], v[188:191]\n\t" \
  "buffer_load_dwordx4 v232, s[72:75], s96 offen offset:3072 lds\n\t" \
  "v_mfma_f32_16x16x32_bf16 v[96:99], a[196:199], v[116:119], v[96:99]\n\t" \
  "ds_read_b64_tr_b16 a[180:181], v210 offset:8960\n\t" \
  "v_mfma_f32_16x16x32_bf16 v[96:99], a[200:203], v[120:123], v[96:99]\n\t" \
  "ds_read_b64_tr_b16 a[182:183], v210 offset:13056\n\t" \
  "v_mfma_f32_16x16x32_bf16 v[96:99], a[204:207], v[124:127], v[96:99]\n\t" \
  "s_add_u32 s99, s52, 0\n\t" \
  "v_exp_f32 v2, v2\n\t" \
  "v_mfma_f32_16x16x32_bf16 v[100:103], a[220:223], v[124:127], v[188:191]\n\t" \
  "ds_read_b64_tr_b16 a[184:185], v211 offset:768\n\t" \
  "v_mfma_f32_16x16x32_bf16 v[100:103], a[216:219], v[120:123], v[100:103]\n\t" \
  "ds_read_b64_tr_b16 a[186:187], v211 offset:4864\n\t" \
  "v_mfma_f32_16x16x32_bf16 v[100:103], a[212:215], v[116:119], v[100:103]\n\t" \
  "buffer_load_dword v246, v212, s[24:27], s99 offen\n\t" \
  "v_mfma_f32_16x16x32_bf16 v[100:103], a[208:211], v[112:115], v[100:103]\n\t" \
  "ds_read_b64_tr_b16 a[188:189], v211 offset:8960\n\t" \
  "v_mfma_f32_16x16x32_bf16 v[104:107], a[224:227], v[112:115], v[188:191]\n\t" \
  "v_exp_f32 v3, v3\n\t" \
  "v_mfma_f32_16x16x32_bf16 v[104:107], a[228:231], v[116:119], v[104:107]\n\t" \
  "ds_read_b64_tr_b16 a[190:191], v211 offset:13056\n\t" \
  "v_mfma_f32_16x16x32_bf16 v[104:107], a[232:235], v[120:123], v[104:107]\n\t" \
  "v_exp_f32 v4, v4\n\t" \
  "v_mfma_f32_16x16x32_bf16 v[104:107], a[236:239], v[124:127], v[104:107]\n\t" \
  "v_exp_f32 v5, v5\n\t" \
  "v_mfma_f32_16x16x32_bf16 v[108:111], a[252:255], v[124:127], v[188:191]\n\t" \
  "v_exp_f32 v6, v6\n\t" \
  "v_mfma_f32_16x16x32_bf16 v[108:111], a[248:251], v[120:123], v[108:111]\n\t" \
  "v_exp_f32 v7, v7\n\t" \
  "v_mfma_f32_16x16x32_bf16 v[108:111], a[244:247], v[116:119], v[108:111]\n\t" \
  "s_waitcnt vmcnt(5) lgkmcnt(0)\n\t" \
  "v_exp_f32 v8, v8\n\t" \
  "v_mfma_f32_16x16x32_bf16 v[108:111], a[240:243], v[112:115], v[108:111]\n\t" \
  "s_barrier\n\t" \
  STA("s_memrealtime s[94:95]\n\t") \
  STA("s_memtime s[92:93]\n\t") \
  "v_mfma_f32_16x16x32_bf16 a[0:3], a[128:131], v[128:131], a[0:3]\n\t" \
  "v_exp_f32 v9, v9\n\t" \
  "v_mfma_f32_16x16x32_bf16 a[0:3], a[132:135], v[132:135], a[0:3]\n\t" \
  "v_exp_f32 v10, v10\n\t" \
  "v_mfma_f32_16x16x32_bf16 a[4:7], a[136:139], v[128:131], a[4:7]\n\t" \
  "v_exp_f32 v11, v11\n\t" \
  "v_mfma_f32_16x16x32_bf16 a[4:7], a[140:143], v[132:135], a[4:7]\n\t" \
  "v_exp_f32 v12, v12\n\t" \
  "v_mfma_f32_16x16x32_bf16 a[8:11], a[144:147], v[128:131], a[8:11]\n\t" \
  "v_exp_f32 v13, v13\n\t" \
  "v_mfma_f32_16x16x32_bf16 a[8:11], a[148:151], v[132:135], a[8:11]\n\t" \
  "v_exp_f32 v14, v14\n\t" \
  "v_mfma_f32_16x16x32_bf16 a[12:15], a[152:155], v[128:131], a[12:15]\n\t" \
  "v_exp_f32 v15, v15\n\t" \
  "v_mfma_f32_16x16x32_bf16 a[12:15], a[156:159], v[132:135], a[12:15]\n\t" \
  "v_exp_f32 v32, v32\n\t" \
  "v_mfma_f32_16x16x32_bf16 a[16:19], a[160:163], v[128:131], a[16:19]\n\t" \
  "v_exp_f32 v33, v33\n\t" \
  "v_mfma_f32_16x16x32_bf16 a[16:19], a[164:167], v[132:135], a[16:19]\n\t" \
  "v_exp_f32 v34, v34\n\t" \
  "v_mfma_f32_16x16x32_bf16 a[20:23], a[168:171], v[128:131], a[20:23]\n\t" \
  "v_exp_f32 v35, v35\n\t" \
  "v_mfma_f32_16x16x32_bf16 a[20:23], a[172:175], v[132:135], a[20:23]\n\t" \
  "v_exp_f32 v36, v36\n\t" \
  "v_mfma_f32_16x16x32_bf16 a[24:27], a[176:179], v[128:131], a[24:27]\n\t" \
  STA("v_writelane_b32 v255, s94, 40\n\t") \
  STA("v_writelane_b32 v255, s92, 41\n\t") \
  "v_exp_f32 v37, v37\n\t" \
  "v_mfma_f32_16x16x32_bf16 a[24:27], a[180:183], v[132:135], a[24:27]\n\t" \
  "v_exp_f32 v38, v38\n\t" \
  "v_mfma_f32_16x16x32_bf16 a[28:31], a[184:187], v[128:131], a[28:31]\n\t" \
  "v_exp_f32 v39, v39\n\t" \
  "v_mfma_f32_16x16x32_bf16 a[28:31], a[188:191], v[132:135], a[28:31]\n\t" \
  "v_exp_f32 v40, v40\n\t" \
  "v_smfmac_f32_16x16x64_bf16 v[160:163], v[216:219], v[128:135], v208\n\t" \
  "s_add_u32 m0, s38, 49152\n\t" \
  "ds_read_b128 a[192:195], v220 offset:16384\n\t" \
  "v_cvt_pk_bf16_f32 v128, v0, v1\n\t" \
  "v_mfma_f32_16x16x32_bf16 a[32:35], a[128:131], v[136:139], a[32:35]\n\t" \
  "s_add_u32 s96, s38, 98304\n\t" \
  "ds_read_b128 a[196:199], v221 offset:16384\n\t" \
  "v_cvt_pk_bf16_f32 v129, v2, v3\n\t" \
  "v_mfma_f32_16x16x32_bf16 a[32:35], a[132:135], v[140:143], a[32:35]\n\t" \
  "v_exp_f32 v41, v41\n\t" \
  "v_mfma_f32_16x16x32_bf16 a[36:39], a[136:139], v[136:139], a[36:39]\n\t" \
  "v_exp_f32 v42, v42\n\t" \
  "v_mfma_f32_16x16x32_bf16 a[36:39], a[140:143], v[140:143], a[36:39]\n\t" \
  "buffer_load_dwordx4 v224, s[60:63], s96 offen offset:0 lds\n\t" \
  "v_mfma_f32_16x16x32_bf16 a[40:43], a[144:147], v[136:139], a[40:43]\n\t" \
  "ds_read_b128 a[200:203], v222 offset:16384\n\t" \
  "v_cvt_pk_bf16_f32 v130, v4, v5\n\t" \
  "v_mfma_f32_16x16x32_bf16 a[40:43], a[148:151], v[140:143], a[40:43]\n\t" \
  "v_exp_f32 v43, v43\n\t" \
  "v_mfma_f32_16x16x32_bf16 a[44:47], a[152:155], v[136:139], a[44:47]\n\t" \
  "ds_read_b128 a[204:207], v223 offset:16384\n\t" \
  "v_cvt_pk_bf16_f32 v131, v6, v7\n\t" \
  "v_mfma_f32_16x16x32_bf16 a[44:47], a[156:159], v[140:143], a[44:47]\n\t" \
  "v_exp_f32 v44, v44\n\t" \
  "v_mfma_f32_16x16x32_bf16 a[48:51], a[160:163], v[136:139], a[48:51]\n\t" \
  "ds_read_b128 a[208:211], v220 offset:20480\n\t" \
  "v_cvt_pk_bf16_f32 v132, v8, v9\n\t" \
  "v_mfma_f32_16x16x32_bf16 a[48:51], a[164:167], v[140:143], a[48:51]\n\t" \
  "buffer_load_dwordx4 v225, s[60:63], s96 offen offset:1024 lds\n\t" \
  "v_mfma_f32_16x16x32_bf16 a[52:55], a[168:171], v[136:139], a[52:55]\n\t" \
  "v_exp_f32 v45, v45\n\t" \
  "v_mfma_f32_16x16x32_bf16 a[52:55], a[172:175], v[140:143], a[52:55]\n\t" \
  "ds_read_b128 a[212:215], v221 offset:20480\n\t" \
  "v_cvt_pk_bf16_f32 v133, v10, v11\n\t" \
  "v_mfma_f32_16x16x32_bf16 a[56:59], a[176:179], v[136:139], a[56:59]\n\t" \
  "v_exp_f32 v46, v46\n\t" \
  "v_mfma_f32_16x16x32_bf16 a[56:59], a[180:183], v[140:143], a[56:59]\n\t" \
  "v_exp_f32 v47, v47\n\t" \
  "v_mfma_f32_16x16x32_bf16 a[60:63], a[184:187], v[136:139], a[60:63]\n\t" \
  "buffer_load_dwordx4 v226, s[60:63], s96 offen offset:2048 lds\n\t" \
  "v_mfma_f32_16x16x32_bf16 a[60:63], a[188:191], v[140:143], a[60:63]\n\t" \
  "ds_read_b128 a[216:219], v222 offset:20480\n\t" \
  "v_cvt_pk_bf16_f32 v134, v12, v13\n\t" \
  "v_smfmac_f32_16x16x64_bf16 v[164:167], v[216:219], v[136:143], v208\n\t" \
  "ds_read_b128 a[220:223], v223 offset:20480\n\t" \
  "v_cvt_pk_bf16_f32 v135, v14, v15\n\t" \
  "v_mfma_f32_16x16x32_bf16 a[64:67], a[128:131], v[144:147], a[64:67]\n\t" \
  "v_exp_f32 v64, v64\n\t" \
  "v_mfma_f32_16x16x32_bf16 a[64:67], a[132:135], v[148:151], a[64:67]\n\t" \
  "v_exp_f32 v65, v65\n\t" \
  "v_mfma_f32_16x16x32_bf16 a[68:71], a[136:139], v[144:147], a[68:71]\n\t" \
  "ds_read_b128 a[224:227], v220 offset:24576\n\t" \
  "v_cvt_pk_bf16_f32 v136, v32, v33\n\t" \
  "v_mfma_f32_16x16x32_bf16 a[68:71], a[140:143], v[148:151], a[68:71]\n\t" \
  "buffer_load_dwordx4 v227, s[60:63], s96 offen offset:3072 lds\n\t" \
  "v_mfma_f32_16x16x32_bf16 a[72:75], a[144:147], v[144:147], a[72:75]\n\t" \
  "s_add_u32 m0, s38, 147456\n\t" \
  "v_exp_f32 v66, v66\n\t" \
  "v_mfma_f32_16x16x32_bf16 a[72:75], a[148:151], v[148:151], a[72:75]\n\t" \
  "s_add_u32 s96, s38, 81920\n\t" \
  "ds_read_b128 a[228:231], v221 offset:24576\n\t" \
  "v_cvt_pk_bf16_f32 v137, v34, v35\n\t" \
  "v_mfma_f32_16x16x32_bf16 a[76:79], a[152:155], v[144:147], a[76:79]\n\t" \
  "v_exp_f32 v67, v67\n\t" \
  "v_mfma_f32_16x16x32_bf16 a[76:79], a[156:159], v[148:151], a[76:79]\n\t" \
  "v_exp_f32 v68, v68\n\t" \
  "v_mfma_f32_16x16x32_bf16 a[80:83], a[160:163], v[144:147], a[80:83]\n\t" \
  "buffer_load_dwordx4 v230, s[64:67], s96 offen offset:0 lds\n\t" \
  "v_mfma_f32_16x16x32_bf16 a[80:83], a[164:167], v[148:151], a[80:83]\n\t" \
  "ds_read_b128 a[232:235], v222 offset:24576\n\t" \
  "v_cvt_pk_bf16_f32 v138, v36, v37\n\t" \
  "v_mfma_f32_16x16x32_bf16 a[84:87], a[168:171], v[144:147], a[84:87]\n\t" \
  ".Lhs8:\n\ts_cbranch_execz .Lhs8n\n\t" \
  "v_exp_f32 v69, v69\n\t" \
  "v_mfma_f32_16x16x32_bf16 a[84:87], a[172:175], v[148:151], a[84:87]\n\t" \
  "ds_read_b128 a[236:239], v223 offset:24576\n\t" \
  "v_cvt_pk_bf16_f32 v139, v38, v39\n\t" \
  "v_mfma_f32_16x16x32_bf16 a[88:91], a[176:179], v[144:147], a[88:91]\n\t" \
  "v_exp_f32 v70, v70\n\t" \
  "v_mfma_f32_16x16x32_bf16 a[88:91], a[180:183], v[148:151], a[88:91]\n\t" \
  "ds_read_b128 a[240:243], v220 offset:28672\n\t" \
  "v_cvt_pk_bf16_f32 v140, v40, v41\n\t" \
  "v_mfma_f32_16x16x32_bf16 a[92:95], a[184:187], v[144:147], a[92:95]\n\t" \
  "buffer_load_dwordx4 v231, s[64:67], s96 offen offset:1024 lds\n\t" \
  "v_mfma_f32_16x16x32_bf16 a[92:95], a[188:191], v[148:151], a[92:95]\n\t" \
  "v_exp_f32 v71, v71\n\t" \
  "v_smfmac_f32_16x16x64_bf16 v[168:171], v[216:219], v[144:151], v208\n\t" \
  "ds_read_b128 a[244:247], v221 offset:28672\n\t" \
  "v_cvt_pk_bf16_f32 v141, v42, v43\n\t" \
  "v_mfma_f32_16x16x32_bf16 a[96:99], a[128:131], v[152:155], a[96:99]\n\t" \
  "v_exp_f32 v72, v72\n\t" \
  "v_mfma_f32_16x16x32_bf16 a[96:99], a[132:135], v[156:159], a[96:99]\n\t" \
  "v_exp_f32 v73, v73\n\t" \
  "v_mfma_f32_16x16x32_bf16 a[100:103], a[136:139], v[152:155], a[100:103]\n\t" \
  "buffer_load_dwordx4 v230, s[64:67], s96 offen offset:2048 lds\n\t" \
  "v_mfma_f32_16x16x32_bf16 a[100:103], a[140:143], v[156:159], a[100:103]\n\t" \
  "ds_read_b128 a[248:251], v222 offset:28672\n\t" \
  "v_cvt_pk_bf16_f32 v142, v44, v45\n\t" \
  "v_mfma_f32_16x16x32_bf16 a[104:107], a[144:147], v[152:155], a[104:107]\n\t" \
  "v_exp_f32 v74, v74\n\t" \
  "v_mfma_f32_16x16x32_bf16 a[104:107], a[148:151], v[156:159], a[104:107]\n\t" \
  "ds_read_b128 a[252:255], v223 offset:28672\n\t" \
  "v_cvt_pk_bf16_f32 v143, v46, v47\n\t" \
  "v_mfma_f32_16x16x32_bf16 a[108:111], a[152:155], v[152:155], a[108:111]\n\t" \
  "v_exp_f32 v75, v75\n\t" \
  "v_mfma_f32_16x16x32_bf16 a[108:111], a[156:159], v[156:159], a[108:111]\n\t" \
  "v_exp_f32 v76, v76\n\t" \
  "v_mfma_f32_16x16x32_bf16 a[112:115], a[160:163], v[152:155], a[112:115]\n\t" \
  "buffer_load_dwordx4 v231, s[64:67], s96 offen offset:3072 lds\n\t" \
  "v_mfma_f32_16x16x32_bf16 a[112:115], a[164:167], v[156:159], a[112:115]\n\t" \
  "v_exp_f32 v77, v77\n\t" \
  "v_mfma_f32_16x16x32_bf16 a[116:119], a[168:171], v[152:155], a[116:119]\n\t" \
  "v_exp_f32 v78, v78\n\t" \
  "v_mfma_f32_16x16x32_bf16 a[116:119], a[172:175], v[156:159], a[116:119]\n\t" \
  "v_exp_f32 v79, v79\n\t" \
  "v_mfma_f32_16x16x32_bf16 a[120:123], a[176:179], v[152:155], a[120:123]\n\t" \
  "s_add_u32 s99, s52, 2048\n\t" \
  "v_exp_f32 v96, v96\n\t" \
  "v_mfma_f32_16x16x32_bf16 a[120:123], a[180:183], v[156:159], a[120:123]\n\t" \
  "v_cvt_pk_bf16_f32 v144, v64, v65\n\t" \
  "v_cvt_pk_bf16_f32 v145, v66, v67\n\t" \
  "v_mfma_f32_16x16x32_bf16 a[124:127], a[184:187], v[152:155], a[124:127]\n\t" \
  "v_exp_f32 v97, v97\n\t" \
  "v_mfma_f32_16x16x32_bf16 a[124:127], a[188:191], v[156:159], a[124:127]\n\t" \
  "buffer_load_dword v246, v212, s[24:27], s99 offen\n\t" \
  "v_smfmac_f32_16x16x64_bf16 v[172:175], v[216:219], v[152:159], v208\n\t" \
  "s_waitcnt lgkmcnt(12)\n\t" \
  "v_cvt_pk_bf16_f32 v146, v68, v69\n\t" \
  STA("s_memrealtime s[94:95]\n\t") \
  STA("s_memtime s[92:93]\n\t") \
  "v_mfma_f32_16x16x32_bf16 v[0:3], a[192:195], v[16:19], v[176:179]\n\t" \
  "ds_read_b64_tr_b16 a[128:129], v228 offset:0\n\t" \
  "v_cvt_pk_bf16_f32 v147, v70, v71\n\t" \
  "v_mfma_f32_16x16x32_bf16 v[0:3], a[196:199], v[20:23], v[0:3]\n\t" \
  "v_exp_f32 v98, v98\n\t" \
  "v_mfma_f32_16x16x32_bf16 v[0:3], a[200:203], v[24:27], v[0:3]\n\t" \
  "ds_read_b64_tr_b16 a[130:131], v228 offset:4096\n\t" \
  "v_cvt_pk_bf16_f32 v148, v72, v73\n\t" \
  "v_mfma_f32_16x16x32_bf16 v[0:3], a[204:207], v[28:31], v[0:3]\n\t" \
  "s_waitcnt lgkmcnt(10)\n\t" \
  "v_exp_f32 v99, v99\n\t" \
  "v_mfma_f32_16x16x32_bf16 v[4:7], a[220:223], v[28:31], v[176:179]\n\t" \
  "ds_read_b64_tr_b16 a[132:133], v228 offset:8192\n\t" \
  "v_cvt_pk_bf16_f32 v149, v74, v75\n\t" \
  "v_mfma_f32_16x16x32_bf16 v[4:7], a[216:219], v[24:27], v[4:7]\n\t" \
  "v_exp_f32 v100, v100\n\t" \
  "v_mfma_f32_16x16x32_bf16 v[4:7], a[212:215], v[20:23], v[4:7]\n\t" \
  "ds_read_b64_tr_b16 a[134:135], v228 offset:12288\n\t" \
  "v_cvt_pk_bf16_f32 v150, v76, v77\n\t" \
  "v_mfma_f32_16x16x32_bf16 v[4:7], a[208:211], v[16:19], v[4:7]\n\t" \
  "s_waitcnt lgkmcnt(8)\n\t" \
  "ds_read_b64_tr_b16 a[136:137], v229 offset:0\n\t" \
  "v_cvt_pk_bf16_f32 v151, v78, v79\n\t" \
  "v_mfma_f32_16x16x32_bf16 v[8:11], a[224:227], v[16:19], v[176:179]\n\t" \
  "v_exp_f32 v101, v101\n\t" \
  "v_mfma_f32_16x16x32_bf16 v[8:11], a[228:231], v[20:23], v[8:11]\n\t" \
  "s_add_u32 s99, s52, 4096\n\t" \
  "ds_read_b64_tr_b16 a[138:139], v229 offset:4096\n\t" \
  "v_cvt_pk_bf16_f32 v152, v96, v97\n\t" \
  "v_mfma_f32_16x16x32_bf16 v[8:11], a[232:235], v[24:27], v[8:11]\n\t" \
  "v_exp_f32 v102, v102\n\t" \
  "v_mfma_f32_16x16x32_bf16 v[8:11], a[236:239], v[28:31], v[8:11]\n\t" \
  "s_waitcnt lgkmcnt(6)\n\t" \
  "ds_read_b64_tr_b16 a[140:141], v229 offset:8192\n\t" \
  "v_cvt_pk_bf16_f32 v153, v98, v99\n\t" \
  "v_mfma_f32_16x16x32_bf16 v[12:15], a[252:255], v[28:31], v[176:179]\n\t" \
  STA("v_writelane_b32 v255, s94, 42\n\t") \
  STA("v_writelane_b32 v255, s92, 43\n\t") \
  "buffer_load_dword v246, v212, s[24:27], s99 offen\n\t" \
  "v_mfma_f32_16x16x32_bf16 v[12:15], a[248:251], v[24:27], v[12:15]\n\t" \
  "ds_read_b64_tr_b16 a[142:143], v229 offset:12288\n\t" \
  "v_cvt_pk_bf16_f32 v154, v100, v101\n\t" \
  "v_mfma_f32_16x16x32_bf16 v[12:15], a[244:247], v[20:23], v[12:15]\n\t" \
  "v_exp_f32 v103, v103\n\t" \
  "v_mfma_f32_16x16x32_bf16 v[12:15], a[240:243], v[16:19], v[12:15]\n\t" \
  "ds_read_b64_tr_b16 a[144:145], v228 offset:256\n\t" \
  "v_cvt_pk_bf16_f32 v155, v102, v103\n\t" \
  "v_mfma_f32_16x16x32_bf16 v[32:35], a[192:195], v[48:51], v[180:183]\n\t" \
  "v_exp_f32 v104, v104\n\t" \
  "v_mfma_f32_16x16x32_bf16 v[32:35], a[196:199], v[52:55], v[32:35]\n\t" \
  "v_exp_f32 v105, v105\n\t" \
  "v_mfma_f32_16x16x32_bf16 v[32:35], a[200:203], v[56:59], v[32:35]\n\t" \
  "ds_read_b64_tr_b16 a[146:147], v228 offset:4352\n\t" \
  "v_cvt_pk_bf16_f32 v156, v104, v105\n\t" \
  "v_mfma_f32_16x16x32_bf16 v[32:35], a[204:207], v[60:63], v[32:35]\n\t" \
  "v_exp_f32 v106, v106\n\t" \
  "v_mfma_f32_16x16x32_bf16 v[36:39], a[220:223], v[60:63], v[180:183]\n\t" \
  "v_exp_f32 v107, v107\n\t" \
  "v_mfma_f32_16x16x32_bf16 v[36:39], a[216:219], v[56:59], v[36:39]\n\t" \
  "ds_read_b64_tr_b16 a[148:149], v228 offset:8448\n\t" \
  "v_cvt_pk_bf16_f32 v157, v106, v107\n\t" \
  "v_mfma_f32_16x16x32_bf16 v[36:39], a[212:215], v[52:55], v[36:39]\n\t" \
  "v_exp_f32 v108, v108\n\t" \
  "v_mfma_f32_16x16x32_bf16 v[36:39], a[208:211], v[48:51], v[36:39]\n\t" \
  "v_exp_f32 v109, v109\n\t" \
  "v_mfma_f32_16x16x32_bf16 v[40:43], a[224:227], v[48:51], v[180:183]\n\t" \
  "ds_read_b64_tr_b16 a[150:151], v228 offset:12544\n\t" \
  "v_cvt_pk_bf16_f32 v158, v108, v109\n\t" \
  "v_mfma_f32_16x16x32_bf16 v[40:43], a[228:231], v[52:55], v[40:43]\n\t" \
  "v_exp_f32 v110, v110\n\t" \
  "v_mfma_f32_16x16x32_bf16 v[40:43], a[232:235], v[56:59], v[40:43]\n\t" \
  "ds_read_b64_tr_b16 a[152:153], v229 offset:256\n\t" \
  "v_mfma_f32_16x16x32_bf16 v[40:43], a[236:239], v[60:63], v[40:43]\n\t" \
  "v_exp_f32 v111, v111\n\t" \
  "v_mfma_f32_16x16x32_bf16 v[44:47], a[252:255], v[60:63], v[180:183]\n\t" \
  "ds_read_b64_tr_b16 a[154:155], v229 offset:4352\n\t" \
  "v_cvt_pk_bf16_f32 v159, v110, v111\n\t" \
  "v_mfma_f32_16x16x32_bf16 v[44:47], a[248:251], v[56:59], v[44:47]\n\t" \
  "ds_read_b64_tr_b16 a[156:157], v229 offset:8448\n\t" \
  "v_mfma_f32_16x16x32_bf16 v[44:47], a[244:247], v[52:55], v[44:47]\n\t" \
  "ds_read_b64_tr_b16 a[158:159], v229 offset:12544\n\t" \
  "v_mfma_f32_16x16x32_bf16 v[44:47], a[240:243], v[48:51], v[44:47]\n\t" \
  "v_exp_f32 v0, v0\n\t" \
  "v_mfma_f32_16x16x32_bf16 v[64:67], a[192:195], v[80:83], v[184:187]\n\t" \
  "ds_read_b64_tr_b16 a[160:161], v228 offset:512\n\t" \
  "v_mfma_f32_16x16x32_bf16 v[64:67], a[196:199], v[84:87], v[64:67]\n\t" \
  "ds_read_b64_tr_b16 a[162:163], v228 offset:4608\n\t" \
  "v_mfma_f32_16x16x32_bf16 v[64:67], a[200:203], v[88:91], v[64:67]\n\t" \
  "v_exp_f32 v1, v1\n\t" \
  "v_mfma_f32_16x16x32_bf16 v[64:67], a[204:207], v[92:95], v[64:67]\n\t" \
  "ds_read_b64_tr_b16 a[164:165], v228 offset:8704\n\t" \
  "v_mfma_f32_16x16x32_bf16 v[68:71], a[220:223], v[92:95], v[184:187]\n\t" \
  "v_exp_f32 v2, v2\n\t" \
  "v_mfma_f32_16x16x32_bf16 v[68:71], a[216:219], v[88:91], v[68:71]\n\t" \
  "ds_read_b64_tr_b16 a[166:167], v228 offset:12800\n\t" \
  "v_mfma_f32_16x16x32_bf16 v[68:71], a[212:215], v[84:87], v[68:71]\n\t" \
  "ds_read_b64_tr_b16 a[168:169], v229 offset:512\n\t" \
  "v_mfma_f32_16x16x32_bf16 v[68:71], a[208:211], v[80:83], v[68:71]\n\t" \
  "v_exp_f32 v3, v3\n\t" \
  "v_mfma_f32_16x16x32_bf16 v[72:75], a[224:227], v[80:83], v[184:187]\n\t" \
  "ds_read_b64_tr_b16 a[170:171], v229 offset:4608\n\t" \
  "v_mfma_f32_16x16x32_bf16 v[72:75], a[228:231], v[84:87], v[72:75]\n\t" \
  "v_exp_f32 v4, v4\n\t" \
  "v_mfma_f32_16x16x32_bf16 v[72:75], a[232:235], v[88:91], v[72:75]\n\t" \
  "ds_read_b64_tr_b16 a[172:173], v229 offset:8704\n\t" \
  "v_mfma_f32_16x16x32_bf16 v[72:75], a[236:239], v[92:95], v[72:75]\n\t" \
  "ds_read_b64_tr_b16 a[174:175], v229 offset:12800\n\t" \
  "v_mfma_f32_16x16x32_bf16 v[76:79], a[252:255], v[92:95], v[184:187]\n\t" \
  "v_exp_f32 v5, v5\n\t" \
  "v_mfma_f32_16x16x32_bf16 v[76:79], a[248:251], v[88:91], v[76:79]\n\t" \
  "ds_read_b64_tr_b16 a[176:177], v228 offset:768\n\t" \
  "v_mfma_f32_16x16x32_bf16 v[76:79], a[244:247], v[84:87], v[76:79]\n\t" \
  "ds_read_b64_tr_b16 a[178:179], v228 offset:4864\n\t" \
  "v_mfma_f32_16x16x32_bf16 v[76:79], a[240:243], v[80:83], v[76:79]\n\t" \
  "v_exp_f32 v6, v6\n\t" \
  "v_mfma_f32_16x16x32_bf16 v[96:99], a[192:195], v[112:115], v[188:191]\n\t" \
  "ds_read_b64_tr_b16 a[180:181], v228 offset:8960\n\t" \
  "v_mfma_f32_16x16x32_bf16 v[96:99], a[196:199], v[116:119], v[96:99]\n\t" \
  "v_exp_f32 v7, v7\n\t" \
  "v_mfma_f32_16x16x32_bf16 v[96:99], a[200:203], v[120:123], v[96:99]\n\t" \
  "ds_read_b64_tr_b16 a[182:183], v228 offset:13056\n\t" \
  "v_mfma_f32_16x16x32_bf16 v[96:99], a[204:207], v[124:127], v[96:99]\n\t" \
  "ds_read_b64_tr_b16 a[184:185], v229 offset:768\n\t" \
  "v_mfma_f32_16x16x32_bf16 v[100:103], a[220:223], v[124:127], v[188:191]\n\t" \
  "v_exp_f32 v8, v8\n\t" \
  "v_mfma_f32_16x16x32_bf16 v[100:103], a[216:219], v[120:123], v[100:103]\n\t" \
  "ds_read_b64_tr_b16 a[186:187], v229 offset:4864\n\t" \
  "v_mfma_f32_16x16x32_bf16 v[100:103], a[212:215], v[116:119], v[100:103]\n\t" \
  "ds_read_b64_tr_b16 a[188:189], v229 offset:8960\n\t" \
  "v_mfma_f32_16x16x32_bf16 v[100:103], a[208:211], v[112:115], v[100:103]\n\t" \
  "v_exp_f32 v9, v9\n\t" \
  "v_mfma_f32_16x16x32_bf16 v[104:107], a[224:227], v[112:115], v[188:191]\n\t" \
  "ds_read_b64_tr_b16 a[190:191], v229 offset:13056\n\t" \
  "v_mfma_f32_16x16x32_bf16 v[104:107], a[228:231], v[116:119], v[104:107]\n\t" \
  "v_exp_f32 v10, v10\n\t" \
  "v_mfma_f32_16x16x32_bf16 v[104:107], a[232:235], v[120:123], v[104:107]\n\t" \
  "v_exp_f32 v11, v11\n\t" \
  "v_mfma_f32_16x16x32_bf16 v[104:107], a[236:239], v[124:127], v[104:107]\n\t" \
  "v_exp_f32 v12, v12\n\t" \
  "v_mfma_f32_16x16x32_bf16 v[108:111], a[252:255], v[124:127], v[188:191]\n\t" \
  "v_exp_f32 v13, v13\n\t" \
  "v_mfma_f32_16x16x32_bf16 v[108:111], a[248:251], v[120:123], v[108:111]\n\t" \
  "v_exp_f32 v14, v14\n\t" \
  "v_mfma_f32_16x16x32_bf16 v[108:111], a[244:247], v[116:119], v[108:111]\n\t" \
  "s_waitcnt vmcnt(2) lgkmcnt(0)\n\t" \
  "v_exp_f32 v15, v15\n\t" \
  "v_mfma_f32_16x16x32_bf16 v[108:111], a[240:243], v[112:115], v[108:111]\n\t" \
  "s_barrier\n\t" \
  STA("s_memrealtime s[94:95]\n\t") \
  STA("s_memtime s[92:93]\n\t") \
  "v_mfma_f32_16x16x32_bf16 a[0:3], a[128:131], v[128:131], a[0:3]\n\t" \
  "v_exp_f32 v32, v32\n\t" \
  "v_mfma_f32_16x16x32_bf16 a[0:3], a[132:135], v[132:135], a[0:3]\n\t" \
  "v_exp_f32 v33, v33\n\t" \
  "v_mfma_f32_16x16x32_bf16 a[4:7], a[136:139], v[128:131], a[4:7]\n\t" \
  "v_exp_f32 v34, v34\n\t" \
  "v_mfma_f32_16x16x32_bf16 a[4:7], a[140:143], v[132:135], a[4:7]\n\t" \
  "v_exp_f32 v35, v35\n\t" \
  "v_mfma_f32_16x16x32_bf16 a[8:11], a[144:147], v[128:131], a[8:11]\n\t" \
  "v_exp_f32 v36, v36\n\t" \
  "v_mfma_f32_16x16x32_bf16 a[8:11], a[148:151], v[132:135], a[8:11]\n\t" \
  "v_exp_f32 v37, v37\n\t" \
  "v_mfma_f32_16x16x32_bf16 a[12:15], a[152:155], v[128:131], a[12:15]\n\t" \
  "v_exp_f32 v38, v38\n\t" \
  "v_mfma_f32_16x16x32_bf16 a[12:15], a[156:159], v[132:135], a[12:15]\n\t" \
  "v_exp_f32 v39, v39\n\t" \
  "v_mfma_f32_16x16x32_bf16 a[16:19], a[160:163], v[128:131], a[16:19]\n\t" \
  "buffer_load_dword v209, v251, s[16:19], 0 offen sc1\n\t" \
  "v_mfma_f32_16x16x32_bf16 a[16:19], a[164:167], v[132:135], a[16:19]\n\t" \
  "v_exp_f32 v40, v40\n\t" \
  "v_mfma_f32_16x16x32_bf16 a[20:23], a[168:171], v[128:131], a[20:23]\n\t" \
  "v_exp_f32 v41, v41\n\t" \
  "v_mfma_f32_16x16x32_bf16 a[20:23], a[172:175], v[132:135], a[20:23]\n\t" \
  "v_exp_f32 v42, v42\n\t" \
  "v_mfma_f32_16x16x32_bf16 a[24:27], a[176:179], v[128:131], a[24:27]\n\t" \
  STA("v_writelane_b32 v255, s94, 44\n\t") \
  STA("v_writelane_b32 v255, s92, 45\n\t") \
  "v_exp_f32 v43, v43\n\t" \
  "v_mfma_f32_16x16x32_bf16 a[24:27], a[180:183], v[132:135], a[24:27]\n\t" \
  "v_exp_f32 v44, v44\n\t" \
  "v_mfma_f32_16x16x32_bf16 a[28:31], a[184:187], v[128:131], a[28:31]\n\t" \
  "v_exp_f32 v45, v45\n\t" \
  "v_mfma_f32_16x16x32_bf16 a[28:31], a[188:191], v[132:135], a[28:31]\n\t" \
  "v_exp_f32 v46, v46\n\t" \
  "v_smfmac_f32_16x16x64_bf16 v[160:163], v[216:219], v[128:135], v208\n\t" \
  "s_add_u32 m0, s38, 65536\n\t" \
  "ds_read_b128 a[192:195], v220 offset:0\n\t" \
  "v_cvt_pk_bf16_f32 v128, v0, v1\n\t" \
  "v_mfma_f32_16x16x32_bf16 a[32:35], a[128:131], v[136:139], a[32:35]\n\t" \
  "s_add_u32 s96, s38, 114688\n\t" \
  "ds_read_b128 a[196:199], v221 offset:0\n\t" \
  "v_cvt_pk_bf16_f32 v129, v2, v3\n\t" \
  "v_mfma_f32_16x16x32_bf16 a[32:35], a[132:135], v[140:143], a[32:35]\n\t" \
  "v_exp_f32 v47, v47\n\t" \
  "v_mfma_f32_16x16x32_bf16 a[36:39], a[136:139], v[136:139], a[36:39]\n\t" \
  "v_exp_f32 v64, v64\n\t" \
  "v_mfma_f32_16x16x32_bf16 a[36:39], a[140:143], v[140:143], a[36:39]\n\t" \
  "buffer_load_dwordx4 v224, s[60:63], s96 offen offset:0 lds\n\t" \
  "v_mfma_f32_16x16x32_bf16 a[40:43], a[144:147], v[136:139], a[40:43]\n\t" \
  "ds_read_b128 a[200:203], v222 offset:0\n\t" \
  "v_cvt_pk_bf16_f32 v130, v4, v5\n\t" \
  "v_mfma_f32_16x16x32_bf16 a[40:43], a[148:151], v[140:143], a[40:43]\n\t" \
  "v_exp_f32 v65, v65\n\t" \
  "v_mfma_f32_16x16x32_bf16 a[44:47], a[152:155], v[136:139], a[44:47]\n\t" \
  "ds_read_b128 a[204:207], v223 offset:0\n\t" \
  "v_cvt_pk_bf16_f32 v131, v6, v7\n\t" \
  "v_mfma_f32_16x16x32_bf16 a[44:47], a[156:159], v[140:143], a[44:47]\n\t" \
  "v_exp_f32 v66, v66\n\t" \
  "v_mfma_f32_16x16x32_bf16 a[48:51], a[160:163], v[136:139], a[48:51]\n\t" \
  "ds_read_b128 a[208:211], v220 offset:4096\n\t" \
  "v_cvt_pk_bf16_f32 v132, v8, v9\n\t" \
  "v_mfma_f32_16x16x32_bf16 a[48:51], a[164:167], v[140:143], a[48:51]\n\t" \
  "buffer_load_dwordx4 v225, s[60:63], s96 offen offset:1024 lds\n\t" \
  "v_mfma_f32_16x16x32_bf16 a[52:55], a[168:171], v[136:139], a[52:55]\n\t" \
  "v_exp_f32 v67, v67\n\t" \
  "v_mfma_f32_16x16x32_bf16 a[52:55], a[172:175], v[140:143], a[52:55]\n\t" \
  "ds_read_b128 a[212:215], v221 offset:4096\n\t" \
  "v_cvt_pk_bf16_f32 v133, v10, v11\n\t" \
  "v_mfma_f32_16x16x32_bf16 a[56:59], a[176:179], v[136:139], a[56:59]\n\t" \
  "v_exp_f32 v68, v68\n\t" \
  "v_mfma_f32_16x16x32_bf16 a[56:59], a[180:183], v[140:143], a[56:59]\n\t" \
  "v_exp_f32 v69, v69\n\t" \
  "v_mfma_f32_16x16x32_bf16 a[60:63], a[184:187], v[136:139], a[60:63]\n\t" \
  "buffer_load_dwordx4 v226, s[60:63], s96 offen offset:2048 lds\n\t" \
  "v_mfma_f32_16x16x32_bf16 a[60:63], a[188:191], v[140:143], a[60:63]\n\t" \
  "ds_read_b128 a[216:219], v222 offset:4096\n\t" \
  "v_cvt_pk_bf16_f32 v134, v12, v13\n\t" \
  "v_smfmac_f32_16x16x64_bf16 v[164:167], v[216:219], v[136:143], v208\n\t" \
  "ds_read_b128 v[192:195], v215 offset:32768\n\t" \
  "v_cvt_pk_bf16_f32 v135, v14, v15\n\t" \
  "v_mfma_f32_16x16x32_bf16 a[64:67], a[128:131], v[144:147], a[64:67]\n\t" \
  "ds_read_b128 a[220:223], v223 offset:4096\n\t" \
  "v_cvt_pk_bf16_f32 v136, v32, v33\n\t" \
  "v_mfma_f32_16x16x32_bf16 a[64:67], a[132:135], v[148:151], a[64:67]\n\t" \
  "v_exp_f32 v70, v70\n\t" \
  "v_mfma_f32_16x16x32_bf16 a[68:71], a[136:139], v[144:147], a[68:71]\n\t" \
  "ds_read_b128 a[224:227], v220 offset:8192\n\t" \
  "v_cvt_pk_bf16_f32 v137, v34, v35\n\t" \
  "v_mfma_f32_16x16x32_bf16 a[68:71], a[140:143], v[148:151], a[68:71]\n\t" \
  "buffer_load_dwordx4 v227, s[60:63], s96 offen offset:3072 lds\n\t" \
  "v_mfma_f32_16x16x32_bf16 a[72:75], a[144:147], v[144:147], a[72:75]\n\t" \
  "s_add_u32 m0, s38, 81920\n\t" \
  "v_exp_f32 v71, v71\n\t" \
  "v_mfma_f32_16x16x32_bf16 a[72:75], a[148:151], v[148:151], a[72:75]\n\t" \
  "s_add_u32 s96, s38, 98304\n\t" \
  "ds_read_b128 a[228:231], v221 offset:8192\n\t" \
  "v_cvt_pk_bf16_f32 v138, v36, v37\n\t" \
  "v_mfma_f32_16x16x32_bf16 a[76:79], a[152:155], v[144:147], a[76:79]\n\t" \
  "v_exp_f32 v72, v72\n\t" \
  "v_mfma_f32_16x16x32_bf16 a[76:79], a[156:159], v[148:151], a[76:79]\n\t" \
  "v_exp_f32 v73, v73\n\t" \
  "v_mfma_f32_16x16x32_bf16 a[80:83], a[160:163], v[144:147], a[80:83]\n\t" \
  "buffer_load_dwordx4 v230, s[64:67], s96 offen offset:0 lds\n\t" \
  "v_mfma_f32_16x16x32_bf16 a[80:83], a[164:167], v[148:151], a[80:83]\n\t" \
  "ds_read_b128 a[232:235], v222 offset:8192\n\t" \
  "v_cvt_pk_bf16_f32 v139, v38, v39\n\t" \
  "v_mfma_f32_16x16x32_bf16 a[84:87], a[168:171], v[144:147], a[84:87]\n\t" \
  "v_exp_f32 v74, v74\n\t" \
  "v_mfma_f32_16x16x32_bf16 a[84:87], a[172:175], v[148:151], a[84:87]\n\t" \
  "ds_read_b128 a[236:239], v223 offset:8192\n\t" \
  "v_cvt_pk_bf16_f32 v140, v40, v41\n\t" \
  "v_mfma_f32_16x16x32_bf16 a[88:91], a[176:179], v[144:147], a[88:91]\n\t" \
  "v_exp_f32 v75, v75\n\t" \
  "v_mfma_f32_16x16x32_bf16 a[88:91], a[180:183], v[148:151], a[88:91]\n\t" \
  "ds_read_b128 a[240:243], v220 offset:12288\n\t" \
  "v_cvt_pk_bf16_f32 v141, v42, v43\n\t" \
  "v_mfma_f32_16x16x32_bf16 a[92:95], a[184:187], v[144:147], a[92:95]\n\t" \
  "buffer_load_dwordx4 v231, s[64:67], s96 offen offset:1024 lds\n\t" \
  "v_mfma_f32_16x16x32_bf16 a[92:95], a[188:191], v[148:151], a[92:95]\n\t" \
  "v_exp_f32 v76, v76\n\t" \
  "v_smfmac_f32_16x16x64_bf16 v[168:171], v[216:219], v[144:151], v208\n\t" \
  "ds_read_b128 a[244:247], v221 offset:12288\n\t" \
  "v_cvt_pk_bf16_f32 v142, v44, v45\n\t" \
  "v_mfma_f32_16x16x32_bf16 a[96:99], a[128:131], v[152:155], a[96:99]\n\t" \
  "v_exp_f32 v77, v77\n\t" \
  "v_mfma_f32_16x16x32_bf16 a[96:99], a[132:135], v[156:159], a[96:99]\n\t" \
  "v_exp_f32 v78, v78\n\t" \
  "v_mfma_f32_16x16x32_bf16 a[100:103], a[136:139], v[152:155], a[100:103]\n\t" \
  "buffer_load_dwordx4 v230, s[64:67], s96 offen offset:2048 lds\n\t" \
  "v_mfma_f32_16x16x32_bf16 a[100:103], a[140:143], v[156:159], a[100:103]\n\t" \
  "ds_read_b128 a[248:251], v222 offset:12288\n\t" \
  "v_cvt_pk_bf16_f32 v143, v46, v47\n\t" \
  "v_mfma_f32_16x16x32_bf16 a[104:107], a[144:147], v[152:155], a[104:107]\n\t" \
  "v_exp_f32 v79, v79\n\t" \
  "v_mfma_f32_16x16x32_bf16 a[104:107], a[148:151], v[156:159], a[104:107]\n\t" \
  "ds_read_b128 a[252:255], v223 offset:12288\n\t" \
  "v_cvt_pk_bf16_f32 v144, v64, v65\n\t" \
  "v_mfma_f32_16x16x32_bf16 a[108:111], a[152:155], v[152:155], a[108:111]\n\t" \
  "v_exp_f32 v96, v96\n\t" \
  "v_mfma_f32_16x16x32_bf16 a[108:111], a[156:159], v[156:159], a[108:111]\n\t" \
  "v_exp_f32 v97, v97\n\t" \
  "v_mfma_f32_16x16x32_bf16 a[112:115], a[160:163], v[152:155], a[112:115]\n\t" \
  "buffer_load_dwordx4 v231, s[64:67], s96 offen offset:3072 lds\n\t" \
  "v_mfma_f32_16x16x32_bf16 a[112:115], a[164:167], v[156:159], a[112:115]\n\t" \
  "v_cvt_pk_bf16_f32 v145, v66, v67\n\t" \
  "v_cvt_pk_bf16_f32 v146, v68, v69\n\t" \
  "v_mfma_f32_16x16x32_bf16 a[116:119], a[168:171], v[152:155], a[116:119]\n\t" \
  "v_exp_f32 v98, v98\n\t" \
  "v_mfma_f32_16x16x32_bf16 a[116:119], a[172:175], v[156:159], a[116:119]\n\t" \
  "v_exp_f32 v99, v99\n\t" \
  "v_mfma_f32_16x16x32_bf16 a[120:123], a[176:179], v[152:155], a[120:123]\n\t" \
  "s_add_u32 s99, s52, 6144\n\t" \
  "v_cvt_pk_bf16_f32 v147, v70, v71\n\t" \
  "v_cvt_pk_bf16_f32 v148, v72, v73\n\t" \
  "v_mfma_f32_16x16x32_bf16 a[120:123], a[180:183], v[156:159], a[120:123]\n\t" \
  "v_exp_f32 v100, v100\n\t" \
  "v_mfma_f32_16x16x32_bf16 a[124:127], a[184:187], v[152:155], a[124:127]\n\t" \
  "v_exp_f32 v101, v101\n\t" \
  "v_mfma_f32_16x16x32_bf16 a[124:127], a[188:191], v[156:159], a[124:127]\n\t" \
  "buffer_load_dword v246, v212, s[24:27], s99 offen\n\t" \
  "v_smfmac_f32_16x16x64_bf16 v[172:175], v[216:219], v[152:159], v208\n\t" \
  "s_waitcnt lgkmcnt(13)\n\t" \
  "ds_read_b128 v[196:199], v215 offset:33792\n\t" \
  "v_lshl_add_u32 v214, v233, 14, s41\n\t" \
  STA("s_memrealtime s[94:95]\n\t") \
  STA("s_memtime s[92:93]\n\t") \
  "v_mfma_f32_16x16x32_bf16 v[0:3], a[192:195], v[16:19], v[176:179]\n\t" \
  "ds_read_b64_tr_b16 a[128:129], v210 offset:0\n\t" \
  "v_min_u32 v214, .Lfz_z-.Lat_t1-4, v214\n\t" \
  "v_mfma_f32_16x16x32_bf16 v[0:3], a[196:199], v[20:23], v[0:3]\n\t" \
  "s_getpc_b64 s[84:85]\n\t" \
  ".Lat_t1:\n\t" \
  "v_exp_f32 v102, v102\n\t" \
  "v_mfma_f32_16x16x32_bf16 v[0:3], a[200:203], v[24:27], v[0:3]\n\t" \
  "global_load_dword v246, v214, s[84:85]\n\t" \
  "v_mfma_f32_16x16x32_bf16 v[0:3], a[204:207], v[28:31], v[0:3]\n\t" \
  "s_waitcnt lgkmcnt(10)\n\t" \
  "ds_read_b64_tr_b16 a[130:131], v210 offset:4096\n\t" \
  "v_cvt_pk_bf16_f32 v149, v74, v75\n\t" \
  "v_mfma_f32_16x16x32_bf16 v[4:7], a[220:223], v[28:31], v[176:179]\n\t" \
  "ds_read_b64_tr_b16 a[132:133], v210 offset:8192\n\t" \
  "v_cvt_pk_bf16_f32 v150, v76, v77\n\t" \
  "v_mfma_f32_16x16x32_bf16 v[4:7], a[216:219], v[24:27], v[4:7]\n\t" \
  "v_exp_f32 v103, v103\n\t" \
  "v_mfma_f32_16x16x32_bf16 v[4:7], a[212:215], v[20:23], v[4:7]\n\t" \
  "ds_read_b64_tr_b16 a[134:135], v210 offset:12288\n\t" \
  "v_cvt_pk_bf16_f32 v151, v78, v79\n\t" \
  "v_mfma_f32_16x16x32_bf16 v[4:7], a[208:211], v[16:19], v[4:7]\n\t" \
  "s_waitcnt lgkmcnt(9)\n\t" \
  "ds_read_b64_tr_b16 a[136:137], v211 offset:0\n\t" \
  "v_cvt_pk_bf16_f32 v152, v96, v97\n\t" \
  "v_mfma_f32_16x16x32_bf16 v[8:11], a[224:227], v[16:19], v[176:179]\n\t" \
  "v_exp_f32 v104, v104\n\t" \
  "v_mfma_f32_16x16x32_bf16 v[8:11], a[228:231], v[20:23], v[8:11]\n\t" \
  "ds_read_b64_tr_b16 a[138:139], v211 offset:4096\n\t" \
  "v_cvt_pk_bf16_f32 v153, v98, v99\n\t" \
  "v_mfma_f32_16x16x32_bf16 v[8:11], a[232:235], v[24:27], v[8:11]\n\t" \
  "v_exp_f32 v105, v105\n\t" \
  "v_mfma_f32_16x16x32_bf16 v[8:11], a[236:239], v[28:31], v[8:11]\n\t" \
  "s_waitcnt lgkmcnt(7)\n\t" \
  "ds_read_b64_tr_b16 a[140:141], v211 offset:8192\n\t" \
  "v_cvt_pk_bf16_f32 v154, v100, v101\n\t" \
  "v_mfma_f32_16x16x32_bf16 v[12:15], a[252:255], v[28:31], v[176:179]\n\t" \
  STA("v_writelane_b32 v255, s94, 46\n\t") \
  STA("v_writelane_b32 v255, s92, 47\n\t") \
  "v_exp_f32 v106, v106\n\t" \
  "v_mfma_f32_16x16x32_bf16 v[12:15], a[248:251], v[24:27], v[12:15]\n\t" \
  "ds_read_b64_tr_b16 a[142:143], v211 offset:12288\n\t" \
  "v_cvt_pk_bf16_f32 v155, v102, v103\n\t" \
  "v_mfma_f32_16x16x32_bf16 v[12:15], a[244:247], v[20:23], v[12:15]\n\t" \
  "ds_read_b64_tr_b16 a[144:145], v210 offset:256\n\t" \
  "v_cvt_pk_bf16_f32 v156, v104, v105\n\t" \
  "v_mfma_f32_16x16x32_bf16 v[12:15], a[240:243], v[16:19], v[12:15]\n\t" \
  "v_exp_f32 v107, v107\n\t" \
  "v_mfma_f32_16x16x32_bf16 v[32:35], a[192:195], v[48:51], v[180:183]\n\t" \
  "s_waitcnt vmcnt(10)\n\t" \
  "v_and_b32 v250, 0xbfffbfff, v209\n\t" \
  "v_cmp_ne_u32_e64 s[84:85], s36, v250\n\t" \
  "v_mfma_f32_16x16x32_bf16 v[32:35], a[196:199], v[52:55], v[32:35]\n\t" \
  "s_cmp_lg_u64 s[84:85], 0\n\t" \
  "s_cbranch_scc1 .Lat_v\n\t" \
  ".Lat_w:\n\t" \
  "v_mfma_f32_16x16x32_bf16 v[32:35], a[200:203], v[56:59], v[32:35]\n\t" \
  "v_and_b32 v250, 1, v233\n\t" \
  "v_lshlrev_b32 v250, 4, v250\n\t" \
  "v_mfma_f32_16x16x32_bf16 v[32:35], a[204:207], v[60:63], v[32:35]\n\t" \
  "v_lshrrev_b32 v250, v250, v209\n\t" \
  "v_and_b32 v250, 0x4000, v250\n\t" \
  "v_mfma_f32_16x16x32_bf16 v[36:39], a[220:223], v[60:63], v[180:183]\n\t" \
  "ds_read_b64_tr_b16 a[146:147], v210 offset:4352\n\t" \
  "v_cmp_ne_u32_e64 s[84:85], 0, v250\n\t" \
  STS("s_nop 4\n\t") \
  STS("v_writelane_b32 v255, s84, 60\n\t") \
  STS("v_writelane_b32 v255, s85, 61\n\t") \
  "v_mfma_f32_16x16x32_bf16 v[36:39], a[216:219], v[56:59], v[36:39]\n\t" \
  "s_bcnt1_i32_b32 s98, s84\n\t" \
  "ds_read_b64_tr_b16 a[148:149], v210 offset:8448\n\t" \
  "s_bcnt1_i32_b32 s54, s85\n\t" \
  "v_mfma_f32_16x16x32_bf16 v[36:39], a[212:215], v[52:55], v[36:39]\n\t" \
  "s_cmp_gt_u32 s98, s54\n\t" \
  "s_cselect_b32 s55, 0x2000, 0\n\t" \
  "s_cselect_b32 s98, s84, s85\n\t" \
  "v_mfma_f32_16x16x32_bf16 v[36:39], a[208:211], v[48:51], v[36:39]\n\t" \
  "s_cmp_lg_u32 s55, 0\n\t" \
  "s_cselect_b32 s84, s85, s84\n\t" \
  "s_lshr_b32 s85, s98, 16\n\t" \
  "v_mfma_f32_16x16x32_bf16 v[40:43], a[224:227], v[48:51], v[180:183]\n\t" \
  "s_bcnt1_i32_b32 s85, s85\n\t" \
  "s_lshr_b32 s84, s84, 16\n\t" \
  "s_bcnt1_i32_b32 s84, s84\n\t" \
  "v_mfma_f32_16x16x32_bf16 v[40:43], a[228:231], v[52:55], v[40:43]\n\t" \
  "s_add_u32 s84, s84, 7\n\t" \
  "s_cmp_ge_u32 s85, s84\n\t" \
  "s_cselect_b32 s54, -1, 0\n\t" \
  "v_mfma_f32_16x16x32_bf16 v[40:43], a[232:235], v[56:59], v[40:43]\n\t" \
  "s_xor_b32 s98, s42, s55\n\t" \
  "s_and_b32 s98, s98, s54\n\t" \
  "s_xor_b32 s45, s45, s98\n\t" \
  "v_mfma_f32_16x16x32_bf16 v[40:43], a[236:239], v[60:63], v[40:43]\n\t" \
  "s_xor_b32 s46, s46, s98\n\t" \
  STS("v_writelane_b32 v255, s54, 57\n\t") \
  STS("v_writelane_b32 v255, s55, 59\n\t") \
  "s_xor_b32 s98, s43, s55\n\t" \
  "s_and_b32 s98, s98, s54\n\t" \
  "v_mfma_f32_16x16x32_bf16 v[44:47], a[252:255], v[60:63], v[180:183]\n\t" \
  "s_xor_b32 s47, s47, s98\n\t" \
  "s_xor_b32 s48, s48, s55\n\t" \
  "s_xor_b32 s49, s49, s55\n\t" \
  "v_mfma_f32_16x16x32_bf16 v[44:47], a[248:251], v[56:59], v[44:47]\n\t" \
  "s_xor_b32 s98, s45, 0\n\t" \
  "ds_read_b64_tr_b16 a[150:151], v210 offset:12544\n\t" \
  "v_cvt_pk_bf16_f32 v157, v106, v107\n\t" \
  "v_mfma_f32_16x16x32_bf16 v[44:47], a[244:247], v[52:55], v[44:47]\n\t" \
  "v_exp_f32 v108, v108\n\t" \
  "v_mfma_f32_16x16x32_bf16 v[44:47], a[240:243], v[48:51], v[44:47]\n\t" \
  "ds_read_b64_tr_b16 a[152:153], v211 offset:256\n\t" \
  "v_mfma_f32_16x16x32_bf16 v[64:67], a[192:195], v[80:83], v[184:187]\n\t" \
  "v_exp_f32 v109, v109\n\t" \
  "v_mfma_f32_16x16x32_bf16 v[64:67], a[196:199], v[84:87], v[64:67]\n\t" \
  "ds_read_b64_tr_b16 a[154:155], v211 offset:4352\n\t" \
  "v_cvt_pk_bf16_f32 v158, v108, v109\n\t" \
  "v_mfma_f32_16x16x32_bf16 v[64:67], a[200:203], v[88:91], v[64:67]\n\t" \
  "ds_read_b64_tr_b16 a[156:157], v211 offset:8448\n\t" \
  "v_mfma_f32_16x16x32_bf16 v[64:67], a[204:207], v[92:95], v[64:67]\n\t" \
  "v_exp_f32 v110, v110\n\t" \
  "v_mfma_f32_16x16x32_bf16 v[68:71], a[220:223], v[92:95], v[184:187]\n\t" \
  "ds_read_b64_tr_b16 a[158:159], v211 offset:12544\n\t" \
  "v_mfma_f32_16x16x32_bf16 v[68:71], a[216:219], v[88:91], v[68:71]\n\t" \
  "v_exp_f32 v111, v111\n\t" \
  "v_mfma_f32_16x16x32_bf16 v[68:71], a[212:215], v[84:87], v[68:71]\n\t" \
  "ds_read_b64_tr_b16 a[160:161], v210 offset:512\n\t" \
  "v_cvt_pk_bf16_f32 v159, v110, v111\n\t" \
  "v_mfma_f32_16x16x32_bf16 v[68:71], a[208:211], v[80:83], v[68:71]\n\t" \
  "ds_read_b64_tr_b16 a[162:163], v210 offset:4608\n\t" \
  "v_exp_f32 v0, v0\n\t" \
  "v_mfma_f32_16x16x32_bf16 v[72:75], a[224:227], v[80:83], v[184:187]\n\t" \
  "v_exp_f32 v1, v1\n\t" \
  "v_mfma_f32_16x16x32_bf16 v[72:75], a[228:231], v[84:87], v[72:75]\n\t" \
  "ds_read_b64_tr_b16 a[164:165], v210 offset:8704\n\t" \
  "v_mfma_f32_16x16x32_bf16 v[72:75], a[232:235], v[88:91], v[72:75]\n\t" \
  "v_exp_f32 v2, v2\n\t" \
  "v_mfma_f32_16x16x32_bf16 v[72:75], a[236:239], v[92:95], v[72:75]\n\t" \
  "ds_read_b64_tr_b16 a[166:167], v210 offset:12800\n\t" \
  "v_mfma_f32_16x16x32_bf16 v[76:79], a[252:255], v[92:95], v[184:187]\n\t" \
  "ds_read_b64_tr_b16 a[168:169], v211 offset:512\n\t" \
  "v_exp_f32 v3, v3\n\t" \
  "v_mfma_f32_16x16x32_bf16 v[76:79], a[248:251], v[88:91], v[76:79]\n\t" \
  "v_exp_f32 v4, v4\n\t" \
  "v_mfma_f32_16x16x32_bf16 v[76:79], a[244:247], v[84:87], v[76:79]\n\t" \
  "ds_read_b64_tr_b16 a[170:171], v211 offset:4608\n\t" \
  "v_mfma_f32_16x16x32_bf16 v[76:79], a[240:243], v[80:83], v[76:79]\n\t" \
  "ds_read_b64_tr_b16 a[172:173], v211 offset:8704\n\t" \
  "v_exp_f32 v5, v5\n\t" \
  "v_mfma_f32_16x16x32_bf16 v[96:99], a[192:195], v[112:115], v[188:191]\n\t" \
  "v_exp_f32 v6, v6\n\t" \
  "v_mfma_f32_16x16x32_bf16 v[96:99], a[196:199], v[116:119], v[96:99]\n\t" \
  "ds_read_b64_tr_b16 a[174:175], v211 offset:12800\n\t" \
  "v_mfma_f32_16x16x32_bf16 v[96:99], a[200:203], v[120:123], v[96:99]\n\t" \
  "ds_read_b64_tr_b16 a[176:177], v210 offset:768\n\t" \
  "v_exp_f32 v7, v7\n\t" \
  "v_mfma_f32_16x16x32_bf16 v[96:99], a[204:207], v[124:127], v[96:99]\n\t" \
  "v_exp_f32 v8, v8\n\t" \
  "v_mfma_f32_16x16x32_bf16 v[100:103], a[220:223], v[124:127], v[188:191]\n\t" \
  "v_exp_f32 v9, v9\n\t" \
  "v_mfma_f32_16x16x32_bf16 v[100:103], a[216:219], v[120:123], v[100:103]\n\t" \
  "ds_read_b64_tr_b16 a[178:179], v210 offset:4864\n\t" \
  "v_mfma_f32_16x16x32_bf16 v[100:103], a[212:215], v[116:119], v[100:103]\n\t" \
  "ds_read_b64_tr_b16 a[180:181], v210 offset:8960\n\t" \
  "v_mfma_f32_16x16x32_bf16 v[100:103], a[208:211], v[112:115], v[100:103]\n\t" \
  "ds_read_b64_tr_b16 a[182:183], v210 offset:13056\n\t" \
  "v_exp_f32 v10, v10\n\t" \
  "v_mfma_f32_16x16x32_bf16 v[104:107], a[224:227], v[112:115], v[188:191]\n\t" \
  "ds_read_b64_tr_b16 a[184:185], v211 offset:768\n\t" \
  "v_exp_f32 v11, v11\n\t" \
  "v_mfma_f32_16x16x32_bf16 v[104:107], a[228:231], v[116:119], v[104:107]\n\t" \
  "ds_read_b64_tr_b16 a[186:187], v211 offset:4864\n\t" \
  "v_exp_f32 v12, v12\n\t" \
  "v_mfma_f32_16x16x32_bf16 v[104:107], a[232:235], v[120:123], v[104:107]\n\t" \
  "v_exp_f32 v13, v13\n\t" \
  "v_mfma_f32_16x16x32_bf16 v[104:107], a[236:239], v[124:127], v[104:107]\n\t" \
  "v_exp_f32 v14, v14\n\t" \
  "v_mfma_f32_16x16x32_bf16 v[108:111], a[252:255], v[124:127], v[188:191]\n\t" \
  "ds_read_b64_tr_b16 a[188:189], v211 offset:8960\n\t" \
  "v_mfma_f32_16x16x32_bf16 v[108:111], a[248:251], v[120:123], v[108:111]\n\t" \
  "ds_read_b64_tr_b16 a[190:191], v211 offset:13056\n\t" \
  "v_exp_f32 v15, v15\n\t" \
  "v_mfma_f32_16x16x32_bf16 v[108:111], a[244:247], v[116:119], v[108:111]\n\t" \
  "s_waitcnt vmcnt(2) lgkmcnt(0)\n\t" \
  "v_exp_f32 v32, v32\n\t" \
  "v_mfma_f32_16x16x32_bf16 v[108:111], a[240:243], v[112:115], v[108:111]\n\t" \
  "s_barrier\n\t" \
  STA("s_memrealtime s[94:95]\n\t") \
  STA("s_memtime s[92:93]\n\t") \
  "v_mfma_f32_16x16x32_bf16 a[0:3], a[128:131], v[128:131], a[0:3]\n\t" \
  "v_exp_f32 v33, v33\n\t" \
  "v_mfma_f32_16x16x32_bf16 a[0:3], a[132:135], v[132:135], a[0:3]\n\t" \
  "v_exp_f32 v34, v34\n\t" \
  "v_mfma_f32_16x16x32_bf16 a[4:7], a[136:139], v[128:131], a[4:7]\n\t" \
  "v_exp_f32 v35, v35\n\t" \
  "v_mfma_f32_16x16x32_bf16 a[4:7], a[140:143], v[132:135], a[4:7]\n\t" \
  "v_exp_f32 v36, v36\n\t" \
  "v_mfma_f32_16x16x32_bf16 a[8:11], a[144:147], v[128:131], a[8:11]\n\t" \
  "v_exp_f32 v37, v37\n\t" \
  "v_mfma_f32_16x16x32_bf16 a[8:11], a[148:151], v[132:135], a[8:11]\n\t" \
  "v_exp_f32 v38, v38\n\t" \
  "v_mfma_f32_16x16x32_bf16 a[12:15], a[152:155], v[128:131], a[12:15]\n\t" \
  "v_exp_f32 v39, v39\n\t" \
  "v_mfma_f32_16x16x32_bf16 a[12:15], a[156:159], v[132:135], a[12:15]\n\t" \
  "v_exp_f32 v40, v40\n\t" \
  "v_mfma_f32_16x16x32_bf16 a[16:19], a[160:163], v[128:131], a[16:19]\n\t" \
  "v_exp_f32 v41, v41\n\t" \
  "v_mfma_f32_16x16x32_bf16 a[16:19], a[164:167], v[132:135], a[16:19]\n\t" \
  "v_exp_f32 v42, v42\n\t" \
  "v_mfma_f32_16x16x32_bf16 a[20:23], a[168:171], v[128:131], a[20:23]\n\t" \
  "v_exp_f32 v43, v43\n\t" \
  "v_mfma_f32_16x16x32_bf16 a[20:23], a[172:175], v[132:135], a[20:23]\n\t" \
  "v_exp_f32 v44, v44\n\t" \
  "v_mfma_f32_16x16x32_bf16 a[24:27], a[176:179], v[128:131], a[24:27]\n\t" \
  STA("v_writelane_b32 v255, s94, 48\n\t") \
  STA("v_writelane_b32 v255, s92, 49\n\t") \
  "v_exp_f32 v45, v45\n\t" \
  "v_mfma_f32_16x16x32_bf16 a[24:27], a[180:183], v[132:135], a[24:27]\n\t" \
  "v_exp_f32 v46, v46\n\t" \
  "v_mfma_f32_16x16x32_bf16 a[28:31], a[184:187], v[128:131], a[28:31]\n\t" \
  "ds_read_b128 a[192:195], v220 offset:16384\n\t" \
  "v_exp_f32 v47, v47\n\t" \
  "v_mfma_f32_16x16x32_bf16 a[28:31], a[188:191], v[132:135], a[28:31]\n\t" \
  "v_exp_f32 v64, v64\n\t" \
  "v_smfmac_f32_16x16x64_bf16 v[160:163], v[216:219], v[128:135], v208\n\t" \
  "s_add_u32 m0, s38, 147456\n\t" \
  "ds_read_b128 v[200:203], v215 offset:34816\n\t" \
  "v_cvt_pk_bf16_f32 v128, v0, v1\n\t" \
  "v_mfma_f32_16x16x32_bf16 a[32:35], a[128:131], v[136:139], a[32:35]\n\t" \
  "s_add_u32 s96, s38, 114688\n\t" \
  "ds_read_b128 a[196:199], v221 offset:16384\n\t" \
  "v_exp_f32 v65, v65\n\t" \
  "v_mfma_f32_16x16x32_bf16 a[32:35], a[132:135], v[140:143], a[32:35]\n\t" \
  "v_exp_f32 v66, v66\n\t" \
  "v_mfma_f32_16x16x32_bf16 a[36:39], a[136:139], v[136:139], a[36:39]\n\t" \
  "v_exp_f32 v67, v67\n\t" \
  "v_mfma_f32_16x16x32_bf16 a[36:39], a[140:143], v[140:143], a[36:39]\n\t" \
  "buffer_load_dwordx4 v230, s[64:67], s96 offen offset:0 lds\n\t" \
  "v_mfma_f32_16x16x32_bf16 a[40:43], a[144:147], v[136:139], a[40:43]\n\t" \
  "ds_read_b128 a[200:203], v222 offset:16384\n\t" \
  "v_exp_f32 v68, v68\n\t" \
  "v_mfma_f32_16x16x32_bf16 a[40:43], a[148:151], v[140:143], a[40:43]\n\t" \
  "v_exp_f32 v69, v69\n\t" \
  "v_mfma_f32_16x16x32_bf16 a[44:47], a[152:155], v[136:139], a[44:47]\n\t" \
  "v_exp_f32 v70, v70\n\t" \
  "v_mfma_f32_16x16x32_bf16 a[44:47], a[156:159], v[140:143], a[44:47]\n\t" \
  "buffer_load_dwordx4 v231, s[64:67], s96 offen offset:1024 lds\n\t" \
  "v_mfma_f32_16x16x32_bf16 a[48:51], a[160:163], v[136:139], a[48:51]\n\t" \
  "ds_read_b128 a[204:207], v223 offset:16384\n\t" \
  "v_exp_f32 v71, v71\n\t" \
  "v_mfma_f32_16x16x32_bf16 a[48:51], a[164:167], v[140:143], a[48:51]\n\t" \
  ".Lhs9:\n\ts_cbranch_execz .Lhs9n\n\t" \
  "v_exp_f32 v72, v72\n\t" \
  "v_mfma_f32_16x16x32_bf16 a[52:55], a[168:171], v[136:139], a[52:55]\n\t" \
  "v_exp_f32 v73, v73\n\t" \
  "v_mfma_f32_16x16x32_bf16 a[52:55], a[172:175], v[140:143], a[52:55]\n\t" \
  "buffer_load_dwordx4 v230, s[64:67], s96 offen offset:2048 lds\n\t" \
  "v_mfma_f32_16x16x32_bf16 a[56:59], a[176:179], v[136:139], a[56:59]\n\t" \
  "v_exp_f32 v74, v74\n\t" \
  "v_mfma_f32_16x16x32_bf16 a[56:59], a[180:183], v[140:143], a[56:59]\n\t" \
  "ds_read_b128 a[208:211], v220 offset:20480\n\t" \
  "v_cvt_pk_bf16_f32 v129, v2, v3\n\t" \
  "v_mfma_f32_16x16x32_bf16 a[60:63], a[184:187], v[136:139], a[60:63]\n\t" \
  "buffer_load_dwordx4 v231, s[64:67], s96 offen offset:3072 lds\n\t" \
  "v_mfma_f32_16x16x32_bf16 a[60:63], a[188:191], v[140:143], a[60:63]\n\t" \
  "s_lshl_b32 s64, s39, 17\n\t" \
  "ds_read_b128 a[212:215], v221 offset:20480\n\t" \
  "v_cvt_pk_bf16_f32 v130, v4, v5\n\t" \
  "v_smfmac_f32_16x16x64_bf16 v[164:167], v[216:219], v[136:143], v208\n\t" \
  "s_add_u32 s96, s40, 0\n\t" \
  "s_and_b32 s96, s96, 0x1c0000\n\t" \
  "s_add_u32 s96, s96, s51\n\t" \
  "v_mfma_f32_16x16x32_bf16 a[64:67], a[128:131], v[144:147], a[64:67]\n\t" \
  "s_add_u32 m0, s38, 0\n\t" \
  "ds_read_b128 a[216:219], v222 offset:20480\n\t" \
  "v_exp_f32 v75, v75\n\t" \
  "v_mfma_f32_16x16x32_bf16 a[64:67], a[132:135], v[148:151], a[64:67]\n\t" \
  "buffer_load_dwordx4 v240, s[24:27], s96 offen offset:0 lds\n\t" \
  "v_mfma_f32_16x16x32_bf16 a[68:71], a[136:139], v[144:147], a[68:71]\n\t" \
  "s_bfe_u32 s70, s46, 0x20014\n\t" \
  "ds_read_b128 a[220:223], v223 offset:20480\n\t" \
  "v_exp_f32 v76, v76\n\t" \
  "v_mfma_f32_16x16x32_bf16 a[68:71], a[140:143], v[148:151], a[68:71]\n\t" \
  "s_lshl_b32 s70, s70, 14\n\t" \
  "ds_read_b128 a[224:227], v220 offset:24576\n\t" \
  "v_exp_f32 v77, v77\n\t" \
  "v_mfma_f32_16x16x32_bf16 a[72:75], a[144:147], v[144:147], a[72:75]\n\t" \
  "s_xor_b32 s67, s70, 49152\n\t" \
  "v_exp_f32 v78, v78\n\t" \
  "v_mfma_f32_16x16x32_bf16 a[72:75], a[148:151], v[148:151], a[72:75]\n\t" \
  "buffer_load_dwordx4 v241, s[24:27], s96 offen offset:1024 lds\n\t" \
  "v_mfma_f32_16x16x32_bf16 a[76:79], a[152:155], v[144:147], a[76:79]\n\t" \
  "s_lshl1_add_u32 s60, s67, s46\n\t" \
  "v_exp_f32 v79, v79\n\t" \
  "v_mfma_f32_16x16x32_bf16 a[76:79], a[156:159], v[148:151], a[76:79]\n\t" \
  "s_xor_b32 s68, s70, 32768\n\t" \
  "v_exp_f32 v96, v96\n\t" \
  "v_mfma_f32_16x16x32_bf16 a[80:83], a[160:163], v[144:147], a[80:83]\n\t" \
  "buffer_load_dwordx4 v248, s[24:27], s96 offen offset:2048 lds\n\t" \
  "v_mfma_f32_16x16x32_bf16 a[80:83], a[164:167], v[148:151], a[80:83]\n\t" \
  "s_lshl1_add_u32 s61, s68, s46\n\t" \
  "ds_read_b128 a[228:231], v221 offset:24576\n\t" \
  "v_exp_f32 v97, v97\n\t" \
  "v_mfma_f32_16x16x32_bf16 a[84:87], a[168:171], v[144:147], a[84:87]\n\t" \
  "s_xor_b32 s69, s70, 16384\n\t" \
  "ds_read_b128 a[232:235], v222 offset:24576\n\t" \
  "v_cvt_pk_bf16_f32 v131, v6, v7\n\t" \
  "v_mfma_f32_16x16x32_bf16 a[84:87], a[172:175], v[148:151], a[84:87]\n\t" \
  "s_lshl1_add_u32 s62, s69, s46\n\t" \
  "v_exp_f32 v98, v98\n\t" \
  "v_mfma_f32_16x16x32_bf16 a[88:91], a[176:179], v[144:147], a[88:91]\n\t" \
  "buffer_load_dwordx4 v249, s[24:27], s96 offen offset:3072 lds\n\t" \
  "v_mfma_f32_16x16x32_bf16 a[88:91], a[180:183], v[148:151], a[88:91]\n\t" \
  "s_lshl1_add_u32 s63, s70, s46\n\t" \
  "ds_read_b128 a[236:239], v223 offset:24576\n\t" \
  "v_exp_f32 v99, v99\n\t" \
  "v_mfma_f32_16x16x32_bf16 a[92:95], a[184:187], v[144:147], a[92:95]\n\t" \
  "s_add_u32 s96, s40, 262144\n\t" \
  "s_and_b32 s96, s96, 0x1c0000\n\t" \
  "s_add_u32 s96, s96, s51\n\t" \
  "v_mfma_f32_16x16x32_bf16 a[92:95], a[188:191], v[148:151], a[92:95]\n\t" \
  "s_add_u32 m0, s38, 49152\n\t" \
  "ds_read_b128 a[240:243], v220 offset:28672\n\t" \
  "v_cvt_pk_bf16_f32 v132, v8, v9\n\t" \
  "v_smfmac_f32_16x16x64_bf16 v[168:171], v[216:219], v[144:151], v208\n\t" \
  "buffer_load_dwordx4 v240, s[24:27], s96 offen offset:0 lds\n\t" \
  "v_mfma_f32_16x16x32_bf16 a[96:99], a[128:131], v[152:155], a[96:99]\n\t" \
  "s_mul_i32 s65, s39, 508\n\t" \
  "ds_read_b128 a[244:247], v221 offset:28672\n\t" \
  "v_exp_f32 v100, v100\n\t" \
  "v_mfma_f32_16x16x32_bf16 a[96:99], a[132:135], v[156:159], a[96:99]\n\t" \
  "s_add_u32 s65, s65, s49\n\t" \
  "v_exp_f32 v101, v101\n\t" \
  "v_mfma_f32_16x16x32_bf16 a[100:103], a[136:139], v[152:155], a[100:103]\n\t" \
  "buffer_load_dwordx4 v241, s[24:27], s96 offen offset:1024 lds\n\t" \
  "v_mfma_f32_16x16x32_bf16 a[100:103], a[140:143], v[156:159], a[100:103]\n\t" \
  "s_lshl_b32 s80, s39, 2\n\t" \
  "ds_read_b128 a[248:251], v222 offset:28672\n\t" \
  "v_cvt_pk_bf16_f32 v133, v10, v11\n\t" \
  "v_mfma_f32_16x16x32_bf16 a[104:107], a[144:147], v[152:155], a[104:107]\n\t" \
  "s_add_u32 s80, s80, s3\n\t" \
  "v_exp_f32 v102, v102\n\t" \
  "v_mfma_f32_16x16x32_bf16 a[104:107], a[148:151], v[156:159], a[104:107]\n\t" \
  "s_lshl_b32 s80, s80, 6\n\t" \
  "ds_read_b128 a[252:255], v223 offset:28672\n\t" \
  "v_cvt_pk_bf16_f32 v134, v12, v13\n\t" \
  "v_mfma_f32_16x16x32_bf16 a[108:111], a[152:155], v[152:155], a[108:111]\n\t" \
  "buffer_load_dwordx4 v248, s[24:27], s96 offen offset:2048 lds\n\t" \
  "v_mfma_f32_16x16x32_bf16 a[108:111], a[156:159], v[156:159], a[108:111]\n\t" \
  "s_add_u32 s76, s38, 147456\n\t" \
  "v_cvt_pk_bf16_f32 v135, v14, v15\n\t" \
  "v_exp_f32 v103, v103\n\t" \
  "v_mfma_f32_16x16x32_bf16 a[112:115], a[160:163], v[152:155], a[112:115]\n\t" \
  "s_lshl_b32 s75, s80, 2\n\t" \
  "v_exp_f32 v104, v104\n\t" \
  "v_mfma_f32_16x16x32_bf16 a[112:115], a[164:167], v[156:159], a[112:115]\n\t" \
  "s_add_u32 s75, s75, 2228224\n\t" \
  "v_exp_f32 v105, v105\n\t" \
  "v_mfma_f32_16x16x32_bf16 a[116:119], a[168:171], v[152:155], a[116:119]\n\t" \
  "buffer_load_dwordx4 v249, s[24:27], s96 offen offset:3072 lds\n\t" \
  "v_mfma_f32_16x16x32_bf16 a[116:119], a[172:175], v[156:159], a[116:119]\n\t" \
  "s_lshr_b32 s71, s67, 7\n\t" \
  "v_cvt_pk_bf16_f32 v136, v32, v33\n\t" \
  "v_cvt_pk_bf16_f32 v137, v34, v35\n\t" \
  "v_mfma_f32_16x16x32_bf16 a[120:123], a[176:179], v[152:155], a[120:123]\n\t" \
  "s_add_u32 s71, s71, s48\n\t" \
  "v_cvt_pk_bf16_f32 v138, v36, v37\n\t" \
  "v_exp_f32 v106, v106\n\t" \
  "v_mfma_f32_16x16x32_bf16 a[120:123], a[180:183], v[156:159], a[120:123]\n\t" \
  "s_lshr_b32 s72, s68, 7\n\t" \
  "v_exp_f32 v107, v107\n\t" \
  "v_mfma_f32_16x16x32_bf16 a[124:127], a[184:187], v[152:155], a[124:127]\n\t" \
  "s_add_u32 s72, s72, s48\n\t" \
  "v_exp_f32 v108, v108\n\t" \
  "v_mfma_f32_16x16x32_bf16 a[124:127], a[188:191], v[156:159], a[124:127]\n\t" \
  "s_lshr_b32 s66, s69, 7\n\t" \
  "v_cvt_pk_bf16_f32 v139, v38, v39\n\t" \
  "v_cvt_pk_bf16_f32 v140, v40, v41\n\t" \
  "v_smfmac_f32_16x16x64_bf16 v[172:175], v[216:219], v[152:159], v208\n\t" \
  "s_waitcnt lgkmcnt(12)\n\t" \
  "ds_read_b128 v[204:207], v215 offset:35840\n\t" \
  "v_cvt_pk_bf16_f32 v141, v42, v43\n\t" \
  STA("s_memrealtime s[94:95]\n\t") \
  STA("s_memtime s[92:93]\n\t") \
  "v_mfma_f32_16x16x32_bf16 v[0:3], a[192:195], v[16:19], v[176:179]\n\t" \
  "ds_read_b64_tr_b16 a[128:129], v228 offset:0\n\t" \
  "ds_read_b64_tr_b16 a[130:131], v228 offset:4096\n\t" \
  "ds_read_b64_tr_b16 a[132:133], v228 offset:8192\n\t" \
  "ds_read_b64_tr_b16 a[134:135], v228 offset:12288\n\t" \
  "v_mfma_f32_16x16x32_bf16 v[0:3], a[196:199], v[20:23], v[0:3]\n\t" \
  "s_add_u32 s66, s66, s48\n\t" \
  "ds_read_b64_tr_b16 a[136:137], v229 offset:0\n\t" \
  "v_cvt_pk_bf16_f32 v142, v44, v45\n\t" \
  "v_mfma_f32_16x16x32_bf16 v[0:3], a[200:203], v[24:27], v[0:3]\n\t" \
  "s_lshr_b32 s73, s70, 7\n\t" \
  "ds_read_b64_tr_b16 a[138:139], v229 offset:4096\n\t" \
  "v_exp_f32 v109, v109\n\t" \
  "v_mfma_f32_16x16x32_bf16 v[0:3], a[204:207], v[28:31], v[0:3]\n\t" \
  "s_waitcnt lgkmcnt(15)\n\t" \
  "ds_read_b64_tr_b16 a[140:141], v229 offset:8192\n\t" \
  "v_cvt_pk_bf16_f32 v143, v46, v47\n\t" \
  "v_mfma_f32_16x16x32_bf16 v[4:7], a[220:223], v[28:31], v[176:179]\n\t" \
  "s_add_u32 s73, s73, s48\n\t" \
  "ds_read_b64_tr_b16 a[142:143], v229 offset:12288\n\t" \
  "v_cvt_pk_bf16_f32 v144, v64, v65\n\t" \
  "v_mfma_f32_16x16x32_bf16 v[4:7], a[216:219], v[24:27], v[4:7]\n\t" \
  "s_lshl3_add_u32 s67, s67, s47\n\t" \
  "ds_read_b64_tr_b16 a[144:145], v228 offset:256\n\t" \
  "v_exp_f32 v110, v110\n\t" \
  "v_mfma_f32_16x16x32_bf16 v[4:7], a[212:215], v[20:23], v[4:7]\n\t" \
  "s_lshl3_add_u32 s68, s68, s47\n\t" \
  "ds_read_b64_tr_b16 a[146:147], v228 offset:4352\n\t" \
  "v_exp_f32 v111, v111\n\t" \
  "v_mfma_f32_16x16x32_bf16 v[4:7], a[208:211], v[16:19], v[4:7]\n\t" \
  "s_waitcnt lgkmcnt(15)\n\t" \
  "ds_read_b64_tr_b16 a[148:149], v228 offset:8448\n\t" \
  "v_cvt_pk_bf16_f32 v145, v66, v67\n\t" \
  "v_mfma_f32_16x16x32_bf16 v[8:11], a[224:227], v[16:19], v[176:179]\n\t" \
  "s_add_u32 s68, s68, 4096\n\t" \
  "v_exp_f32 v0, v0\n\t" \
  "v_mfma_f32_16x16x32_bf16 v[8:11], a[228:231], v[20:23], v[8:11]\n\t" \
  "s_lshl3_add_u32 s69, s69, s47\n\t" \
  "ds_read_b64_tr_b16 a[150:151], v228 offset:12544\n\t" \
  "v_cvt_pk_bf16_f32 v146, v68, v69\n\t" \
  "v_mfma_f32_16x16x32_bf16 v[8:11], a[232:235], v[24:27], v[8:11]\n\t" \
  "s_add_u32 s69, s69, 16384\n\t" \
  "ds_read_b64_tr_b16 a[152:153], v229 offset:256\n\t" \
  "v_cvt_pk_bf16_f32 v147, v70, v71\n\t" \
  "v_mfma_f32_16x16x32_bf16 v[8:11], a[236:239], v[28:31], v[8:11]\n\t" \
  "s_waitcnt lgkmcnt(14)\n\t" \
  "v_cvt_pk_bf16_f32 v148, v72, v73\n\t" \
  "v_exp_f32 v1, v1\n\t" \
  "v_mfma_f32_16x16x32_bf16 v[12:15], a[252:255], v[28:31], v[176:179]\n\t" \
  STA("v_writelane_b32 v255, s94, 50\n\t") \
  STA("v_writelane_b32 v255, s92, 51\n\t") \
  "s_lshl3_add_u32 s70, s70, s47\n\t" \
  "v_exp_f32 v2, v2\n\t" \
  "v_mfma_f32_16x16x32_bf16 v[12:15], a[248:251], v[24:27], v[12:15]\n\t" \
  "s_barrier\n\t" \
  "v_mfma_f32_16x16x32_bf16 v[12:15], a[244:247], v[20:23], v[12:15]\n\t" \
  "v_exp_f32 v3, v3\n\t" \
  "v_mfma_f32_16x16x32_bf16 v[12:15], a[240:243], v[16:19], v[12:15]\n\t" \
  "ds_read_b64_tr_b16 a[154:155], v229 offset:4352\n\t" \
  "v_cvt_pk_bf16_f32 v149, v74, v75\n\t" \
  "v_mfma_f32_16x16x32_bf16 v[32:35], a[192:195], v[48:51], v[180:183]\n\t" \
  "s_add_u32 s96, s40, 0\n\t" \
  "v_exp_f32 v4, v4\n\t" \
  "v_mfma_f32_16x16x32_bf16 v[32:35], a[196:199], v[52:55], v[32:35]\n\t" \
  "s_and_b32 s96, s96, 0x1c0000\n\t" \
  "ds_read_b64_tr_b16 a[156:157], v229 offset:8448\n\t" \
  "v_cvt_pk_bf16_f32 v150, v76, v77\n\t" \
  "v_mfma_f32_16x16x32_bf16 v[32:35], a[200:203], v[56:59], v[32:35]\n\t" \
  "s_add_u32 s96, s96, s77\n\t" \
  "v_cvt_pk_bf16_f32 v151, v78, v79\n\t" \
  "s_add_u32 m0, s38, 98304\n\t" \
  "v_mfma_f32_16x16x32_bf16 v[32:35], a[204:207], v[60:63], v[32:35]\n\t" \
  "ds_read_b64_tr_b16 a[158:159], v229 offset:12544\n\t" \
  "v_cvt_pk_bf16_f32 v152, v96, v97\n\t" \
  "v_mfma_f32_16x16x32_bf16 v[36:39], a[220:223], v[60:63], v[180:183]\n\t" \
  "buffer_load_dwordx4 v240, s[24:27], s96 offen offset:0 lds\n\t" \
  "v_mfma_f32_16x16x32_bf16 v[36:39], a[216:219], v[56:59], v[36:39]\n\t" \
  "v_exp_f32 v5, v5\n\t" \
  "v_mfma_f32_16x16x32_bf16 v[36:39], a[212:215], v[52:55], v[36:39]\n\t" \
  "v_exp_f32 v6, v6\n\t" \
  "v_mfma_f32_16x16x32_bf16 v[36:39], a[208:211], v[48:51], v[36:39]\n\t" \
  "ds_read_b64_tr_b16 a[160:161], v228 offset:512\n\t" \
  "v_cvt_pk_bf16_f32 v153, v98, v99\n\t" \
  "v_mfma_f32_16x16x32_bf16 v[40:43], a[224:227], v[48:51], v[180:183]\n\t" \
  "buffer_load_dwordx4 v241, s[24:27], s96 offen offset:1024 lds\n\t" \
  "v_mfma_f32_16x16x32_bf16 v[40:43], a[228:231], v[52:55], v[40:43]\n\t" \
  "v_exp_f32 v7, v7\n\t" \
  "v_mfma_f32_16x16x32_bf16 v[40:43], a[232:235], v[56:59], v[40:43]\n\t" \
  "v_exp_f32 v8, v8\n\t" \
  "v_mfma_f32_16x16x32_bf16 v[40:43], a[236:239], v[60:63], v[40:43]\n\t" \
  "buffer_load_dwordx4 v248, s[24:27], s96 offen offset:2048 lds\n\t" \
  "v_mfma_f32_16x16x32_bf16 v[44:47], a[252:255], v[60:63], v[180:183]\n\t" \
  "v_exp_f32 v9, v9\n\t" \
  "v_mfma_f32_16x16x32_bf16 v[44:47], a[248:251], v[56:59], v[44:47]\n\t" \
  "ds_read_b64_tr_b16 a[162:163], v228 offset:4608\n\t" \
  "v_cvt_pk_bf16_f32 v154, v100, v101\n\t" \
  "v_mfma_f32_16x16x32_bf16 v[44:47], a[244:247], v[52:55], v[44:47]\n\t" \
  "buffer_load_dwordx4 v249, s[24:27], s96 offen offset:3072 lds\n\t" \
  "v_mfma_f32_16x16x32_bf16 v[44:47], a[240:243], v[48:51], v[44:47]\n\t" \
  "s_add_u32 s96, s40, 524288\n\t" \
  "ds_read_b64_tr_b16 a[164:165], v228 offset:8704\n\t" \
  "v_cvt_pk_bf16_f32 v155, v102, v103\n\t" \
  "v_mfma_f32_16x16x32_bf16 v[64:67], a[192:195], v[80:83], v[184:187]\n\t" \
  "s_and_b32 s96, s96, 0x1c0000\n\t" \
  "s_add_u32 s96, s96, s51\n\t" \
  "s_add_u32 m0, s38, 131072\n\t" \
  "v_mfma_f32_16x16x32_bf16 v[64:67], a[196:199], v[84:87], v[64:67]\n\t" \
  "ds_read_b64_tr_b16 a[166:167], v228 offset:12800\n\t" \
  "v_cvt_pk_bf16_f32 v156, v104, v105\n\t" \
  "v_mfma_f32_16x16x32_bf16 v[64:67], a[200:203], v[88:91], v[64:67]\n\t" \
  "s_waitcnt lgkmcnt(15)\n\t" \
  "buffer_load_dwordx4 v240, s[24:27], s96 offen offset:0 lds\n\t" \
  "v_mfma_f32_16x16x32_bf16 v[64:67], a[204:207], v[92:95], v[64:67]\n\t" \
  "ds_read_b64_tr_b16 a[168:169], v229 offset:512\n\t" \
  "v_cvt_pk_bf16_f32 v157, v106, v107\n\t" \
  "v_mfma_f32_16x16x32_bf16 v[68:71], a[220:223], v[92:95], v[184:187]\n\t" \
  "v_exp_f32 v10, v10\n\t" \
  "v_mfma_f32_16x16x32_bf16 v[68:71], a[216:219], v[88:91], v[68:71]\n\t" \
  "buffer_load_dwordx4 v241, s[24:27], s96 offen offset:1024 lds\n\t" \
  "v_mfma_f32_16x16x32_bf16 v[68:71], a[212:215], v[84:87], v[68:71]\n\t" \
  "ds_read_b64_tr_b16 a[170:171], v229 offset:4608\n\t" \
  "v_cvt_pk_bf16_f32 v158, v108, v109\n\t" \
  "v_mfma_f32_16x16x32_bf16 v[68:71], a[208:211], v[80:83], v[68:71]\n\t" \
  "ds_read_b64_tr_b16 a[172:173], v229 offset:8704\n\t" \
  "v_cvt_pk_bf16_f32 v159, v110, v111\n\t" \
  "v_mfma_f32_16x16x32_bf16 v[72:75], a[224:227], v[80:83], v[184:187]\n\t" \
  "buffer_load_dwordx4 v248, s[24:27], s96 offen offset:2048 lds\n\t" \
  "v_mfma_f32_16x16x32_bf16 v[72:75], a[228:231], v[84:87], v[72:75]\n\t" \
  "v_exp_f32 v11, v11\n\t" \
  "v_mfma_f32_16x16x32_bf16 v[72:75], a[232:235], v[88:91], v[72:75]\n\t" \
  "ds_read_b64_tr_b16 a[174:175], v229 offset:12800\n\t" \
  "v_cvt_pk_bf16_f32 v0, v0, v1\n\t" \
  "v_mfma_f32_16x16x32_bf16 v[72:75], a[236:239], v[92:95], v[72:75]\n\t" \
  "v_exp_f32 v12, v12\n\t" \
  "v_mfma_f32_16x16x32_bf16 v[76:79], a[252:255], v[92:95], v[184:187]\n\t" \
  "buffer_load_dwordx4 v249, s[24:27], s96 offen offset:3072 lds\n\t" \
  "v_mfma_f32_16x16x32_bf16 v[76:79], a[248:251], v[88:91], v[76:79]\n\t" \
  "s_add_u32 s96, s40, 262144\n\t" \
  "s_and_b32 s96, s96, 0x1c0000\n\t" \
  "s_add_u32 s96, s96, s77\n\t" \
  "v_mfma_f32_16x16x32_bf16 v[76:79], a[244:247], v[84:87], v[76:79]\n\t" \
  "s_add_u32 m0, s38, 65536\n\t" \
  "ds_read_b64_tr_b16 a[176:177], v228 offset:768\n\t" \
  "v_cvt_pk_bf16_f32 v1, v2, v3\n\t" \
  "v_mfma_f32_16x16x32_bf16 v[76:79], a[240:243], v[80:83], v[76:79]\n\t" \
  "buffer_load_dwordx4 v240, s[24:27], s96 offen offset:0 lds\n\t" \
  "v_mfma_f32_16x16x32_bf16 v[96:99], a[192:195], v[112:115], v[188:191]\n\t" \
  "ds_read_b64_tr_b16 a[178:179], v228 offset:4864\n\t" \
  "v_cvt_pk_bf16_f32 v2, v4, v5\n\t" \
  "v_mfma_f32_16x16x32_bf16 v[96:99], a[196:199], v[116:119], v[96:99]\n\t" \
  "ds_read_b64_tr_b16 a[180:181], v228 offset:8960\n\t" \
  "v_exp_f32 v13, v13\n\t" \
  "v_mfma_f32_16x16x32_bf16 v[96:99], a[200:203], v[120:123], v[96:99]\n\t" \
  "buffer_load_dwordx4 v241, s[24:27], s96 offen offset:1024 lds\n\t" \
  "v_mfma_f32_16x16x32_bf16 v[96:99], a[204:207], v[124:127], v[96:99]\n\t" \
  "ds_read_b64_tr_b16 a[182:183], v228 offset:13056\n\t" \
  "v_exp_f32 v14, v14\n\t" \
  "v_mfma_f32_16x16x32_bf16 v[100:103], a[220:223], v[124:127], v[188:191]\n\t" \
  "ds_read_b64_tr_b16 a[184:185], v229 offset:768\n\t" \
  "v_cvt_pk_bf16_f32 v3, v6, v7\n\t" \
  "v_mfma_f32_16x16x32_bf16 v[100:103], a[216:219], v[120:123], v[100:103]\n\t" \
  "ds_read_b64_tr_b16 a[186:187], v229 offset:4864\n\t" \
  "v_exp_f32 v15, v15\n\t" \
  "v_mfma_f32_16x16x32_bf16 v[100:103], a[212:215], v[116:119], v[100:103]\n\t" \
  "buffer_load_dwordx4 v248, s[24:27], s96 offen offset:2048 lds\n\t" \
  "v_mfma_f32_16x16x32_bf16 v[100:103], a[208:211], v[112:115], v[100:103]\n\t" \
  "ds_read_b64_tr_b16 a[188:189], v229 offset:8960\n\t" \
  "v_exp_f32 v32, v32\n\t" \
  "v_mfma_f32_16x16x32_bf16 v[104:107], a[224:227], v[112:115], v[188:191]\n\t" \
  "ds_read_b64_tr_b16 a[190:191], v229 offset:13056\n\t" \
  "v_exp_f32 v33, v33\n\t" \
  "v_mfma_f32_16x16x32_bf16 v[104:107], a[228:231], v[116:119], v[104:107]\n\t" \
  "buffer_load_dwordx4 v249, s[24:27], s96 offen offset:3072 lds\n\t" \
  "v_mfma_f32_16x16x32_bf16 v[104:107], a[232:235], v[120:123], v[104:107]\n\t" \
  "s_add_u32 s96, s40, 524288\n\t" \
  "v_exp_f32 v34, v34\n\t" \
  "v_mfma_f32_16x16x32_bf16 v[104:107], a[236:239], v[124:127], v[104:107]\n\t" \
  "s_and_b32 s96, s96, 0x1c0000\n\t" \
  "v_exp_f32 v35, v35\n\t" \
  "v_mfma_f32_16x16x32_bf16 v[108:111], a[252:255], v[124:127], v[188:191]\n\t" \
  "s_add_u32 s96, s96, s77\n\t" \
  "v_exp_f32 v36, v36\n\t" \
  "v_mfma_f32_16x16x32_bf16 v[108:111], a[248:251], v[120:123], v[108:111]\n\t" \
  "s_add_u32 m0, s38, 81920\n\t" \
  "v_exp_f32 v37, v37\n\t" \
  "v_mfma_f32_16x16x32_bf16 v[108:111], a[244:247], v[116:119], v[108:111]\n\t" \
  "s_waitcnt vmcnt(8) lgkmcnt(0)\n\t" \
  "v_exp_f32 v38, v38\n\t" \
  "v_mfma_f32_16x16x32_bf16 v[108:111], a[240:243], v[112:115], v[108:111]\n\t" \
  "s_barrier\n\t" \
  STS("s_memrealtime s[94:95]\n\t") \
  STS("s_memtime s[92:93]\n\t") \
  "v_mfma_f32_16x16x32_bf16 a[0:3], a[128:131], v[128:131], a[0:3]\n\t" \
  "buffer_load_dwordx4 v240, s[24:27], s96 offen offset:0 lds\n\t" \
  "ds_read_b64_tr_b16 v[16:17], v210 offset:0\n\t" \
  "ds_read_b128 v[176:179], v215 offset:16384\n\t" \
  "ds_read_b128 v[180:183], v215 offset:17408\n\t" \
  "v_mfma_f32_16x16x32_bf16 a[0:3], a[132:135], v[132:135], a[0:3]\n\t" \
  "ds_read_b64_tr_b16 v[18:19], v210 offset:4096\n\t" \
  "ds_read_b64_tr_b16 v[20:21], v210 offset:8192\n\t" \
  "ds_read_b64_tr_b16 v[22:23], v210 offset:12288\n\t" \
  "ds_read_b64_tr_b16 v[24:25], v211 offset:0\n\t" \
  "v_mfma_f32_16x16x32_bf16 a[4:7], a[136:139], v[128:131], a[4:7]\n\t" \
  "ds_read_b64_tr_b16 v[26:27], v211 offset:4096\n\t" \
  "ds_read_b64_tr_b16 v[28:29], v211 offset:8192\n\t" \
  "ds_read_b64_tr_b16 v[30:31], v211 offset:12288\n\t" \
  "ds_read_b128 v[184:187], v215 offset:18432\n\t" \
  "v_mfma_f32_16x16x32_bf16 a[4:7], a[140:143], v[132:135], a[4:7]\n\t" \
  "buffer_load_dwordx4 v241, s[24:27], s96 offen offset:1024 lds\n\t" \
  "ds_read_b64_tr_b16 v[48:49], v210 offset:256\n\t" \
  "ds_read_b64_tr_b16 v[50:51], v210 offset:4352\n\t" \
  "ds_read_b64_tr_b16 v[52:53], v210 offset:8448\n\t" \
  "v_mfma_f32_16x16x32_bf16 a[8:11], a[144:147], v[128:131], a[8:11]\n\t" \
  "ds_read_b128 v[188:191], v215 offset:19456\n\t" \
  "ds_read_b64_tr_b16 v[54:55], v210 offset:12544\n\t" \
  "v_cvt_pk_bf16_f32 v4, v8, v9\n\t" \
  "v_mfma_f32_16x16x32_bf16 a[8:11], a[148:151], v[132:135], a[8:11]\n\t" \
  "v_exp_f32 v39, v39\n\t" \
  "v_mfma_f32_16x16x32_bf16 a[12:15], a[152:155], v[128:131], a[12:15]\n\t" \
  "buffer_load_dwordx4 v248, s[24:27], s96 offen offset:2048 lds\n\t" \
  "v_mfma_f32_16x16x32_bf16 a[12:15], a[156:159], v[132:135], a[12:15]\n\t" \
  "ds_read_b64_tr_b16 v[56:57], v211 offset:256\n\t" \
  "v_cvt_pk_bf16_f32 v5, v10, v11\n\t" \
  "v_mfma_f32_16x16x32_bf16 a[16:19], a[160:163], v[128:131], a[16:19]\n\t" \
  "v_exp_f32 v40, v40\n\t" \
  "v_mfma_f32_16x16x32_bf16 a[16:19], a[164:167], v[132:135], a[16:19]\n\t" \
  "buffer_load_dwordx4 v249, s[24:27], s96 offen offset:3072 lds\n\t" \
  "v_mfma_f32_16x16x32_bf16 a[20:23], a[168:171], v[128:131], a[20:23]\n\t" \
  "s_add_u32 s96, s40, 786432\n\t" \
  "ds_read_b64_tr_b16 v[58:59], v211 offset:4352\n\t" \
  "v_cvt_pk_bf16_f32 v6, v12, v13\n\t" \
  "v_mfma_f32_16x16x32_bf16 a[20:23], a[172:175], v[132:135], a[20:23]\n\t" \
  "s_and_b32 s96, s96, 0x1c0000\n\t" \
  "ds_read_b64_tr_b16 v[60:61], v211 offset:8448\n\t" \
  "v_cvt_pk_bf16_f32 v7, v14, v15\n\t" \
  "v_mfma_f32_16x16x32_bf16 a[24:27], a[176:179], v[128:131], a[24:27]\n\t" \
  STS("v_writelane_b32 v255, s94, 20\n\t") \
  STS("v_writelane_b32 v255, s92, 21\n\t") \
  "s_add_u32 s96, s96, s51\n\t" \
  "s_add_u32 m0, s38, 114688\n\t" \
  "v_cvt_pk_bf16_f32 v32, v32, v33\n\t" \
  "v_mfma_f32_16x16x32_bf16 a[24:27], a[180:183], v[132:135], a[24:27]\n\t" \
  "v_exp_f32 v41, v41\n\t" \
  "v_mfma_f32_16x16x32_bf16 a[28:31], a[184:187], v[128:131], a[28:31]\n\t" \
  "s_waitcnt lgkmcnt(4)\n\t" \
  "buffer_load_dwordx4 v240, s[24:27], s96 offen offset:0 lds\n\t" \
  "v_mfma_f32_16x16x32_bf16 a[28:31], a[188:191], v[132:135], a[28:31]\n\t" \
  "ds_read_b64_tr_b16 v[62:63], v211 offset:12544\n\t" \
  "v_cvt_pk_bf16_f32 v33, v34, v35\n\t" \
  "v_smfmac_f32_16x16x64_bf16 v[160:163], v[216:219], v[128:135], v208\n\t" \
  "ds_read_b64_tr_b16 v[80:81], v210 offset:512\n\t" \
  "v_cvt_pk_bf16_f32 v34, v36, v37\n\t" \
  "v_mfma_f32_16x16x32_bf16 a[32:35], a[128:131], v[136:139], a[32:35]\n\t" \
  "buffer_load_dwordx4 v241, s[24:27], s96 offen offset:1024 lds\n\t" \
  "v_mfma_f32_16x16x32_bf16 a[32:35], a[132:135], v[140:143], a[32:35]\n\t" \
  "v_exp_f32 v42, v42\n\t" \
  "v_mfma_f32_16x16x32_bf16 a[36:39], a[136:139], v[136:139], a[36:39]\n\t" \
  "ds_read_b64_tr_b16 v[82:83], v210 offset:4608\n\t" \
  "v_cvt_pk_bf16_f32 v35, v38, v39\n\t" \
  "v_mfma_f32_16x16x32_bf16 a[36:39], a[140:143], v[140:143], a[36:39]\n\t" \
  "buffer_load_dwordx4 v248, s[24:27], s96 offen offset:2048 lds\n\t" \
  "v_mfma_f32_16x16x32_bf16 a[40:43], a[144:147], v[136:139], a[40:43]\n\t" \
  "ds_read_b64_tr_b16 v[84:85], v210 offset:8704\n\t" \
  "v_cvt_pk_bf16_f32 v36, v40, v41\n\t" \
  "v_mfma_f32_16x16x32_bf16 a[40:43], a[148:151], v[140:143], a[40:43]\n\t" \
  "ds_read_b64_tr_b16 v[86:87], v210 offset:12800\n\t" \
  "v_exp_f32 v43, v43\n\t" \
  "v_mfma_f32_16x16x32_bf16 a[44:47], a[152:155], v[136:139], a[44:47]\n\t" \
  "buffer_load_dwordx4 v249, s[24:27], s96 offen offset:3072 lds\n\t" \
  "v_mfma_f32_16x16x32_bf16 a[44:47], a[156:159], v[140:143], a[44:47]\n\t" \
  "ds_read_b64_tr_b16 v[88:89], v211 offset:512\n\t" \
  "v_exp_f32 v44, v44\n\t" \
  "v_mfma_f32_16x16x32_bf16 a[48:51], a[160:163], v[136:139], a[48:51]\n\t" \
  "ds_read_b64_tr_b16 v[90:91], v211 offset:4608\n\t" \
  "v_exp_f32 v45, v45\n\t" \
  "v_mfma_f32_16x16x32_bf16 a[48:51], a[164:167], v[140:143], a[48:51]\n\t" \
  "ds_read_b64_tr_b16 v[92:93], v211 offset:8704\n\t" \
  "v_add_u32 v230, 0x10000, v242\n\t" \
  "v_mfma_f32_16x16x32_bf16 a[52:55], a[168:171], v[136:139], a[52:55]\n\t" \
  "ds_read_b64_tr_b16 v[94:95], v211 offset:12800\n\t" \
  "v_exp_f32 v46, v46\n\t" \
  "v_mfma_f32_16x16x32_bf16 a[52:55], a[172:175], v[140:143], a[52:55]\n\t" \
  "v_exp_f32 v47, v47\n\t" \
  "v_mfma_f32_16x16x32_bf16 a[56:59], a[176:179], v[136:139], a[56:59]\n\t" \
  "ds_read_b64_tr_b16 v[112:113], v210 offset:768\n\t" \
  "v_cvt_pk_bf16_f32 v37, v42, v43\n\t" \
  "v_mfma_f32_16x16x32_bf16 a[56:59], a[180:183], v[140:143], a[56:59]\n\t" \
  "ds_read_b64_tr_b16 v[114:115], v210 offset:4864\n\t" \
  "v_add_u32 v232, 0x20000, v242\n\t" \
  "v_mfma_f32_16x16x32_bf16 a[60:63], a[184:187], v[136:139], a[60:63]\n\t" \
  "ds_read_b64_tr_b16 v[116:117], v210 offset:8960\n\t" \
  "v_exp_f32 v64, v64\n\t" \
  "v_mfma_f32_16x16x32_bf16 a[60:63], a[188:191], v[140:143], a[60:63]\n\t" \
  "ds_read_b64_tr_b16 v[118:119], v210 offset:13056\n\t" \
  "v_cvt_pk_bf16_f32 v38, v44, v45\n\t" \
  "v_smfmac_f32_16x16x64_bf16 v[164:167], v[216:219], v[136:143], v208\n\t" \
  "ds_read_b64_tr_b16 a[192:193], v234 offset:0\n\t" \
  "v_cvt_pk_bf16_f32 v39, v46, v47\n\t" \
  "v_mfma_f32_16x16x32_bf16 a[0:3], v[16:19], v[0:3], a[0:3]\n\t" \
  "ds_read_b64_tr_b16 v[120:121], v211 offset:768\n\t" \
  "v_exp_f32 v65, v65\n\t" \
  "v_mfma_f32_16x16x32_bf16 a[0:3], v[20:23], v[4:7], a[0:3]\n\t" \
  "ds_read_b64_tr_b16 v[122:123], v211 offset:4864\n\t" \
  "v_exp_f32 v66, v66\n\t" \
  "v_smfmac_f32_16x16x64_bf16 v[160:163], v[216:219], v[0:7], v208\n\t" \
  "ds_read_b64_tr_b16 v[124:125], v211 offset:8960\n\t" \
  "v_cvt_pk_bf16_f32 v64, v64, v65\n\t" \
  "v_mfma_f32_16x16x32_bf16 a[4:7], v[24:27], v[0:3], a[4:7]\n\t" \
  "v_exp_f32 v67, v67\n\t" \
  "v_mfma_f32_16x16x32_bf16 a[4:7], v[28:31], v[4:7], a[4:7]\n\t" \
  "s_waitcnt lgkmcnt(15)\n\t" \
  "v_exp_f32 v68, v68\n\t" \
  "v_mfma_f32_16x16x32_bf16 a[8:11], v[48:51], v[0:3], a[8:11]\n\t" \
  "ds_read_b64_tr_b16 v[126:127], v211 offset:13056\n\t" \
  "v_add_f32 v225, v160, v161\n\t" \
  "v_mfma_f32_16x16x32_bf16 a[8:11], v[52:55], v[4:7], a[8:11]\n\t" \
  "v_rcp_f32 v224, v225\n\t" \
  "v_mfma_f32_16x16x32_bf16 a[12:15], v[56:59], v[0:3], a[12:15]\n\t" \
  "v_accvgpr_read_b32 v128, a0\n\t" \
  "v_accvgpr_read_b32 v129, a1\n\t" \
  "v_accvgpr_read_b32 v130, a2\n\t" \
  "v_accvgpr_read_b32 v131, a3\n\t" \
  "v_accvgpr_read_b32 v132, a4\n\t" \
  "v_accvgpr_read_b32 v133, a5\n\t" \
  "v_accvgpr_read_b32 v134, a6\n\t" \
  "v_accvgpr_read_b32 v135, a7\n\t" \
  "v_pk_mul_f32 v[128:129], v[128:129], v[224:225] op_sel_hi:[1,0]\n\t" \
  "v_pk_mul_f32 v[130:131], v[130:131], v[224:225] op_sel_hi:[1,0]\n\t" \
  "v_pk_mul_f32 v[132:133], v[132:133], v[224:225] op_sel_hi:[1,0]\n\t" \
  "v_pk_mul_f32 v[134:135], v[134:135], v[224:225] op_sel_hi:[1,0]\n\t" \
  "v_cvt_pk_bf16_f32 v128, v128, v129\n\t" \
  "v_cvt_pk_bf16_f32 v129, v130, v131\n\t" \
  "v_cvt_pk_bf16_f32 v130, v132, v133\n\t" \
  "v_cvt_pk_bf16_f32 v131, v134, v135\n\t" \
  "buffer_store_dwordx4 v[128:131], v242, s[16:19], s98 offen offset:0 sc1\n\t" \
  "v_mfma_f32_16x16x32_bf16 a[12:15], v[60:63], v[4:7], a[12:15]\n\t" \
  "s_waitcnt lgkmcnt(9)\n\t" \
  "v_cmp_nge_f32 vcc, 0x5f800000, v225\n\t" \
  "v_exp_f32 v69, v69\n\t" \
  "v_mfma_f32_16x16x32_bf16 a[16:19], v[80:83], v[0:3], a[16:19]\n\t" \
  "s_cmp_lg_u64 vcc, 0\n\t" \
  "s_cselect_b32 s89, s78, s89\n\t" \
  "v_or_b32 v254, s89, v254\n\t" \
  "v_mfma_f32_16x16x32_bf16 a[16:19], v[84:87], v[4:7], a[16:19]\n\t" \
  "ds_read_b64_tr_b16 a[194:195], v234 offset:4096\n\t" \
  "v_exp_f32 v70, v70\n\t" \
  "v_mfma_f32_16x16x32_bf16 a[20:23], v[88:91], v[0:3], a[20:23]\n\t" \
  "v_accvgpr_read_b32 v136, a8\n\t" \
  "v_accvgpr_read_b32 v137, a9\n\t" \
  "v_accvgpr_read_b32 v138, a10\n\t" \
  "v_accvgpr_read_b32 v139, a11\n\t" \
  "v_accvgpr_read_b32 v140, a12\n\t" \
  "v_accvgpr_read_b32 v141, a13\n\t" \
  "v_accvgpr_read_b32 v142, a14\n\t" \
  "v_accvgpr_read_b32 v143, a15\n\t" \
  "v_pk_mul_f32 v[136:137], v[136:137], v[224:225] op_sel_hi:[1,0]\n\t" \
  "v_pk_mul_f32 v[138:139], v[138:139], v[224:225] op_sel_hi:[1,0]\n\t" \
  "v_pk_mul_f32 v[140:141], v[140:141], v[224:225] op_sel_hi:[1,0]\n\t" \
  "v_pk_mul_f32 v[142:143], v[142:143], v[224:225] op_sel_hi:[1,0]\n\t" \
  "v_cvt_pk_bf16_f32 v136, v136, v137\n\t" \
  "v_cvt_pk_bf16_f32 v137, v138, v139\n\t" \
  "v_cvt_pk_bf16_f32 v138, v140, v141\n\t" \
  "v_cvt_pk_bf16_f32 v139, v142, v143\n\t" \
  "buffer_store_dwordx4 v[136:139], v242, s[16:19], s98 offen offset:1024 sc1\n\t" \
  "v_mfma_f32_16x16x32_bf16 a[20:23], v[92:95], v[4:7], a[20:23]\n\t" \
  "s_waitcnt lgkmcnt(1)\n\t" \
  "ds_read_b64_tr_b16 a[196:197], v235 offset:0\n\t" \
  "v_exp_f32 v71, v71\n\t" \
  "v_mfma_f32_16x16x32_bf16 a[24:27], v[112:115], v[0:3], a[24:27]\n\t" \
  "s_add_u32 s96, s40, 786432\n\t" \
  "ds_read_b64_tr_b16 a[198:199], v235 offset:4096\n\t" \
  "v_exp_f32 v72, v72\n\t" \
  "v_mfma_f32_16x16x32_bf16 a[24:27], v[116:119], v[4:7], a[24:27]\n\t" \
  "s_and_b32 s96, s96, 0x1c0000\n\t" \
  "ds_read_b64_tr_b16 a[200:201], v234 offset:8192\n\t" \
  "v_cvt_pk_bf16_f32 v65, v66, v67\n\t" \
  "v_mfma_f32_16x16x32_bf16 a[28:31], v[120:123], v[0:3], a[28:31]\n\t" \
  "s_add_u32 s96, s96, s77\n\t" \
  "v_exp_f32 v73, v73\n\t" \
  "v_mfma_f32_16x16x32_bf16 a[28:31], v[124:127], v[4:7], a[28:31]\n\t" \
  "s_add_u32 m0, s38, 147456\n\t" \
  "v_exp_f32 v74, v74\n\t" \
  "v_mfma_f32_16x16x32_bf16 a[64:67], a[128:131], v[144:147], a[64:67]\n\t" \
  "s_barrier\n\t" \
  "v_mfma_f32_16x16x32_bf16 a[64:67], a[132:135], v[148:151], a[64:67]\n\t" \
  "v_exp_f32 v75, v75\n\t" \
  "v_mfma_f32_16x16x32_bf16 a[68:71], a[136:139], v[144:147], a[68:71]\n\t" \
  "v_accvgpr_read_b32 v0, a16\n\t" \
  "v_accvgpr_read_b32 v1, a17\n\t" \
  "v_accvgpr_read_b32 v2, a18\n\t" \
  "v_accvgpr_read_b32 v3, a19\n\t" \
  "v_accvgpr_read_b32 v4, a20\n\t" \
  "v_accvgpr_read_b32 v5, a21\n\t" \
  "v_accvgpr_read_b32 v6, a22\n\t" \
  "v_accvgpr_read_b32 v7, a23\n\t" \
  "v_pk_mul_f32 v[0:1], v[0:1], v[224:225] op_sel_hi:[1,0]\n\t" \
  "v_pk_mul_f32 v[2:3], v[2:3], v[224:225] op_sel_hi:[1,0]\n\t" \
  "v_pk_mul_f32 v[4:5], v[4:5], v[224:225] op_sel_hi:[1,0]\n\t" \
  "v_pk_mul_f32 v[6:7], v[6:7], v[224:225] op_sel_hi:[1,0]\n\t" \
  "v_cvt_pk_bf16_f32 v0, v0, v1\n\t" \
  "v_cvt_pk_bf16_f32 v1, v2, v3\n\t" \
  "v_cvt_pk_bf16_f32 v2, v4, v5\n\t" \
  "v_cvt_pk_bf16_f32 v3, v6, v7\n\t" \
  "buffer_store_dwordx4 v[0:3], v242, s[16:19], s98 offen offset:2048 sc1\n\t" \
  "v_mfma_f32_16x16x32_bf16 a[68:71], a[140:143], v[148:151], a[68:71]\n\t" \
  "v_accvgpr_read_b32 v8, a24\n\t" \
  "v_accvgpr_read_b32 v9, a25\n\t" \
  "v_accvgpr_read_b32 v10, a26\n\t" \
  "v_accvgpr_read_b32 v11, a27\n\t" \
  "v_accvgpr_read_b32 v12, a28\n\t" \
  "v_accvgpr_read_b32 v13, a29\n\t" \
  "v_accvgpr_read_b32 v14, a30\n\t" \
  "v_accvgpr_read_b32 v15, a31\n\t" \
  "v_pk_mul_f32 v[8:9], v[8:9], v[224:225] op_sel_hi:[1,0]\n\t" \
  "v_pk_mul_f32 v[10:11], v[10:11], v[224:225] op_sel_hi:[1,0]\n\t" \
  "v_pk_mul_f32 v[12:13], v[12:13], v[224:225] op_sel_hi:[1,0]\n\t" \
  "v_pk_mul_f32 v[14:15], v[14:15], v[224:225] op_sel_hi:[1,0]\n\t" \
  "v_cvt_pk_bf16_f32 v8, v8, v9\n\t" \
  "v_cvt_pk_bf16_f32 v9, v10, v11\n\t" \
  "v_cvt_pk_bf16_f32 v10, v12, v13\n\t" \
  "v_cvt_pk_bf16_f32 v11, v14, v15\n\t" \
  "buffer_store_dwordx4 v[8:11], v242, s[16:19], s98 offen offset:3072 sc1\n\t" \
  "v_mfma_f32_16x16x32_bf16 a[72:75], a[144:147], v[144:147], a[72:75]\n\t" \
  "buffer_load_dwordx4 v240, s[24:27], s96 offen offset:0 lds\n\t" \
  "v_mfma_f32_16x16x32_bf16 a[72:75], a[148:151], v[148:151], a[72:75]\n\t" \
  "s_xor_b32 s98, s45, 32768\n\t" \
  "ds_read_b64_tr_b16 a[202:203], v234 offset:12288\n\t" \
  "v_exp_f32 v76, v76\n\t" \
  "v_mfma_f32_16x16x32_bf16 a[76:79], a[152:155], v[144:147], a[76:79]\n\t" \
  "ds_read_b64_tr_b16 a[204:205], v235 offset:8192\n\t" \
  "ds_read_b128 a[224:227], v213 offset:16384\n\t" \
  "v_exp_f32 v77, v77\n\t" \
  "v_mfma_f32_16x16x32_bf16 a[76:79], a[156:159], v[148:151], a[76:79]\n\t" \
  "ds_read_b64_tr_b16 a[206:207], v235 offset:12288\n\t" \
  "ds_read_b128 a[228:231], v213 offset:17408\n\t" \
  "v_cvt_pk_bf16_f32 v66, v68, v69\n\t" \
  "v_mfma_f32_16x16x32_bf16 a[80:83], a[160:163], v[144:147], a[80:83]\n\t" \
  "ds_read_b64_tr_b16 a[208:209], v236 offset:32768\n\t" \
  "ds_read_b128 a[232:235], v213 offset:18432\n\t" \
  "v_cvt_pk_bf16_f32 v67, v70, v71\n\t" \
  "v_mfma_f32_16x16x32_bf16 a[80:83], a[164:167], v[148:151], a[80:83]\n\t" \
  "v_exp_f32 v78, v78\n\t" \
  "v_mfma_f32_16x16x32_bf16 a[84:87], a[168:171], v[144:147], a[84:87]\n\t" \
  "buffer_load_dwordx4 v241, s[24:27], s96 offen offset:1024 lds\n\t" \
  "v_mfma_f32_16x16x32_bf16 a[84:87], a[172:175], v[148:151], a[84:87]\n\t" \
  "ds_read_b128 a[236:239], v213 offset:19456\n\t" \
  "v_cvt_pk_bf16_f32 v68, v72, v73\n\t" \
  "v_mfma_f32_16x16x32_bf16 a[88:91], a[176:179], v[144:147], a[88:91]\n\t" \
  ".Lhs10:\n\ts_cbranch_execz .Lhs10n\n\t" \
  "ds_write_b128 v213, v[128:131] offset:16384\n\t" \
  "v_mfma_f32_16x16x32_bf16 a[88:91], a[180:183], v[148:151], a[88:91]\n\t" \
  "buffer_load_dwordx4 v248, s[24:27], s96 offen offset:2048 lds\n\t" \
  "ds_write_b128 v213, v[136:139] offset:17408\n\t" \
  "v_mfma_f32_16x16x32_bf16 a[92:95], a[184:187], v[144:147], a[92:95]\n\t" \
  "ds_write_b128 v213, v[0:3] offset:18432\n\t" \
  "v_mfma_f32_16x16x32_bf16 a[92:95], a[188:191], v[148:151], a[92:95]\n\t" \
  "ds_write_b128 v213, v[8:11] offset:19456\n\t" \
  "v_smfmac_f32_16x16x64_bf16 v[168:171], v[216:219], v[144:151], v208\n\t" \
  "buffer_load_dwordx4 v249, s[24:27], s96 offen offset:3072 lds\n\t" \
  "v_mfma_f32_16x16x32_bf16 a[32:35], v[16:19], v[32:35], a[32:35]\n\t" \
  "ds_read_b64_tr_b16 a[210:211], v236 offset:36864\n\t" \
  "v_cvt_pk_bf16_f32 v69, v74, v75\n\t" \
  "v_mfma_f32_16x16x32_bf16 a[32:35], v[20:23], v[36:39], a[32:35]\n\t" \
  "ds_read_b64_tr_b16 a[212:213], v237 offset:32768\n\t" \
  "v_exp_f32 v79, v79\n\t" \
  "v_smfmac_f32_16x16x64_bf16 v[164:167], v[216:219], v[32:39], v208\n\t" \
  "ds_read_b64_tr_b16 a[214:215], v237 offset:36864\n\t" \
  "v_cvt_pk_bf16_f32 v70, v76, v77\n\t" \
  "v_mfma_f32_16x16x32_bf16 a[36:39], v[24:27], v[32:35], a[36:39]\n\t" \
  "v_exp_f32 v96, v96\n\t" \
  "v_mfma_f32_16x16x32_bf16 a[36:39], v[28:31], v[36:39], a[36:39]\n\t" \
  "s_nop 2\n\t" \
  "v_add_f32 v227, v164, v165\n\t" \
  "v_rcp_f32 v226, v227\n\t" \
  "v_mfma_f32_16x16x32_bf16 a[40:43], v[48:51], v[32:35], a[40:43]\n\t" \
  "ds_read_b64_tr_b16 a[216:217], v236 offset:40960\n\t" \
  "v_cmp_nge_f32 vcc, 0x5f800000, v227\n\t" \
  "v_accvgpr_read_b32 v0, a32\n\t" \
  "v_accvgpr_read_b32 v1, a33\n\t" \
  "v_mfma_f32_16x16x32_bf16 a[40:43], v[52:55], v[36:39], a[40:43]\n\t" \
  "s_cmp_lg_u64 vcc, 0\n\t" \
  "s_cselect_b32 s89, s78, s89\n\t" \
  "v_or_b32 v254, s89, v254\n\t" \
  "v_accvgpr_read_b32 v2, a34\n\t" \
  "v_accvgpr_read_b32 v3, a35\n\t" \
  "v_mfma_f32_16x16x32_bf16 a[44:47], v[56:59], v[32:35], a[44:47]\n\t" \
  "v_exp_f32 v97, v97\n\t" \
  "v_accvgpr_read_b32 v4, a36\n\t" \
  "v_accvgpr_read_b32 v5, a37\n\t" \
  "v_mfma_f32_16x16x32_bf16 a[44:47], v[60:63], v[36:39], a[44:47]\n\t" \
  "v_accvgpr_read_b32 v6, a38\n\t" \
  "v_accvgpr_read_b32 v7, a39\n\t" \
  "v_mfma_f32_16x16x32_bf16 a[48:51], v[80:83], v[32:35], a[48:51]\n\t" \
  "v_mul_f32 v0, v0, v226\n\t" \
  "v_mul_f32 v1, v1, v226\n\t" \
  "v_mfma_f32_16x16x32_bf16 a[48:51], v[84:87], v[36:39], a[48:51]\n\t" \
  "v_mul_f32 v2, v2, v226\n\t" \
  "v_exp_f32 v98, v98\n\t" \
  "v_mfma_f32_16x16x32_bf16 a[52:55], v[88:91], v[32:35], a[52:55]\n\t" \
  "v_mul_f32 v3, v3, v226\n\t" \
  "v_exp_f32 v99, v99\n\t" \
  "v_mfma_f32_16x16x32_bf16 a[52:55], v[92:95], v[36:39], a[52:55]\n\t" \
  "v_mul_f32 v4, v4, v226\n\t" \
  "v_mul_f32 v5, v5, v226\n\t" \
  "v_mfma_f32_16x16x32_bf16 a[56:59], v[112:115], v[32:35], a[56:59]\n\t" \
  "v_mul_f32 v6, v6, v226\n\t" \
  "v_exp_f32 v100, v100\n\t" \
  "v_mfma_f32_16x16x32_bf16 a[56:59], v[116:119], v[36:39], a[56:59]\n\t" \
  "ds_read_b64_tr_b16 a[218:219], v236 offset:45056\n\t" \
  "v_mul_f32 v7, v7, v226\n\t" \
  "v_mfma_f32_16x16x32_bf16 a[60:63], v[120:123], v[32:35], a[60:63]\n\t" \
  "v_exp_f32 v101, v101\n\t" \
  "v_mfma_f32_16x16x32_bf16 a[60:63], v[124:127], v[36:39], a[60:63]\n\t" \
  "v_cvt_pk_bf16_f32 v32, v0, v1\n\t" \
  "v_cvt_pk_bf16_f32 v33, v2, v3\n\t" \
  "v_cvt_pk_bf16_f32 v34, v4, v5\n\t" \
  "v_cvt_pk_bf16_f32 v35, v6, v7\n\t" \
  "v_mfma_f32_16x16x32_bf16 a[96:99], a[128:131], v[152:155], a[96:99]\n\t" \
  "v_accvgpr_read_b32 v8, a40\n\t" \
  "v_accvgpr_read_b32 v9, a41\n\t" \
  "v_accvgpr_read_b32 v10, a42\n\t" \
  "v_accvgpr_read_b32 v11, a43\n\t" \
  "v_mfma_f32_16x16x32_bf16 a[96:99], a[132:135], v[156:159], a[96:99]\n\t" \
  "s_waitcnt vmcnt(8) lgkmcnt(13)\n\t" \
  "v_accvgpr_read_b32 v12, a44\n\t" \
  "v_accvgpr_read_b32 v13, a45\n\t" \
  "v_accvgpr_read_b32 v14, a46\n\t" \
  "v_accvgpr_read_b32 v15, a47\n\t" \
  "v_mfma_f32_16x16x32_bf16 a[100:103], a[136:139], v[152:155], a[100:103]\n\t" \
  "v_mul_f32 v8, v8, v226\n\t" \
  "v_mul_f32 v9, v9, v226\n\t" \
  "s_barrier\n\t" \
  "v_mfma_f32_16x16x32_bf16 a[100:103], a[140:143], v[156:159], a[100:103]\n\t" \
  "buffer_store_dwordx4 v[32:35], v242, s[16:19], s98 offen offset:0 sc1\n\t" \
  "v_mul_f32 v10, v10, v226\n\t" \
  "v_mul_f32 v11, v11, v226\n\t" \
  "v_mul_f32 v12, v12, v226\n\t" \
  "v_mfma_f32_16x16x32_bf16 a[104:107], a[144:147], v[152:155], a[104:107]\n\t" \
  "v_mul_f32 v13, v13, v226\n\t" \
  "v_mul_f32 v14, v14, v226\n\t" \
  "v_mul_f32 v15, v15, v226\n\t" \
  "v_cvt_pk_bf16_f32 v36, v8, v9\n\t" \
  "v_mfma_f32_16x16x32_bf16 a[104:107], a[148:151], v[156:159], a[104:107]\n\t" \
  "v_cvt_pk_bf16_f32 v37, v10, v11\n\t" \
  "v_cvt_pk_bf16_f32 v38, v12, v13\n\t" \
  "v_cvt_pk_bf16_f32 v39, v14, v15\n\t" \
  "v_accvgpr_read_b32 v0, a48\n\t" \
  "v_mfma_f32_16x16x32_bf16 a[108:111], a[152:155], v[152:155], a[108:111]\n\t" \
  "v_accvgpr_read_b32 v1, a49\n\t" \
  "v_accvgpr_read_b32 v2, a50\n\t" \
  "v_accvgpr_read_b32 v3, a51\n\t" \
  "v_exp_f32 v102, v102\n\t" \
  "v_mfma_f32_16x16x32_bf16 a[108:111], a[156:159], v[156:159], a[108:111]\n\t" \
  "v_accvgpr_read_b32 v4, a52\n\t" \
  "v_accvgpr_read_b32 v5, a53\n\t" \
  "v_accvgpr_read_b32 v6, a54\n\t" \
  "v_accvgpr_read_b32 v7, a55\n\t" \
  "v_mfma_f32_16x16x32_bf16 a[112:115], a[160:163], v[152:155], a[112:115]\n\t" \
  "v_mul_f32 v0, v0, v226\n\t" \
  "v_mul_f32 v1, v1, v226\n\t" \
  "v_mul_f32 v2, v2, v226\n\t" \
  "v_exp_f32 v103, v103\n\t" \
  "v_mfma_f32_16x16x32_bf16 a[112:115], a[164:167], v[156:159], a[112:115]\n\t" \
  "v_cvt_pk_bf16_f32 v71, v78, v79\n\t" \
  "v_mul_f32 v3, v3, v226\n\t" \
  "v_mul_f32 v4, v4, v226\n\t" \
  "v_mul_f32 v5, v5, v226\n\t" \
  "v_mfma_f32_16x16x32_bf16 a[116:119], a[168:171], v[152:155], a[116:119]\n\t" \
  "v_mul_f32 v6, v6, v226\n\t" \
  "v_mul_f32 v7, v7, v226\n\t" \
  "v_cvt_pk_bf16_f32 v40, v0, v1\n\t" \
  "v_exp_f32 v104, v104\n\t" \
  "v_mfma_f32_16x16x32_bf16 a[116:119], a[172:175], v[156:159], a[116:119]\n\t" \
  "v_cvt_pk_bf16_f32 v96, v96, v97\n\t" \
  "v_cvt_pk_bf16_f32 v41, v2, v3\n\t" \
  "v_cvt_pk_bf16_f32 v42, v4, v5\n\t" \
  "v_cvt_pk_bf16_f32 v43, v6, v7\n\t" \
  "v_mfma_f32_16x16x32_bf16 a[120:123], a[176:179], v[152:155], a[120:123]\n\t" \
  "buffer_store_dwordx4 v[36:39], v242, s[16:19], s98 offen offset:1024 sc1\n\t" \
  "v_accvgpr_read_b32 v8, a56\n\t" \
  "v_accvgpr_read_b32 v9, a57\n\t" \
  "v_accvgpr_read_b32 v10, a58\n\t" \
  "v_mfma_f32_16x16x32_bf16 a[120:123], a[180:183], v[156:159], a[120:123]\n\t" \
  "v_accvgpr_read_b32 v11, a59\n\t" \
  "v_accvgpr_read_b32 v12, a60\n\t" \
  "v_accvgpr_read_b32 v13, a61\n\t" \
  "v_exp_f32 v105, v105\n\t" \
  "v_mfma_f32_16x16x32_bf16 a[124:127], a[184:187], v[152:155], a[124:127]\n\t" \
  "v_cvt_pk_bf16_f32 v97, v98, v99\n\t" \
  "v_accvgpr_read_b32 v14, a62\n\t" \
  "v_accvgpr_read_b32 v15, a63\n\t" \
  "v_mul_f32 v8, v8, v226\n\t" \
  "v_mfma_f32_16x16x32_bf16 a[124:127], a[188:191], v[156:159], a[124:127]\n\t" \
  "v_mul_f32 v9, v9, v226\n\t" \
  "v_mul_f32 v10, v10, v226\n\t" \
  "v_mul_f32 v11, v11, v226\n\t" \
  "v_exp_f32 v106, v106\n\t" \
  "v_smfmac_f32_16x16x64_bf16 v[172:175], v[216:219], v[152:159], v208\n\t" \
  "v_cvt_pk_bf16_f32 v98, v100, v101\n\t" \
  "v_mul_f32 v12, v12, v226\n\t" \
  "v_mul_f32 v13, v13, v226\n\t" \
  "v_mul_f32 v14, v14, v226\n\t" \
  "v_mfma_f32_16x16x32_bf16 a[64:67], v[16:19], v[64:67], a[64:67]\n\t" \
  "s_waitcnt vmcnt(6)\n\t" \
  "buffer_store_dword v243, v254, s[16:19], s49 offen offset:0 sc1\n\t" \
  "ds_read_b64_tr_b16 a[220:221], v237 offset:40960\n\t" \
  "v_mfma_f32_16x16x32_bf16 a[64:67], v[20:23], v[68:71], a[64:67]\n\t" \
  "ds_read_b64_tr_b16 a[222:223], v237 offset:45056\n\t" \
  "v_cvt_pk_bf16_f32 v99, v102, v103\n\t" \
  "v_exp_f32 v107, v107\n\t" \
  "v_smfmac_f32_16x16x64_bf16 v[168:171], v[216:219], v[64:71], v208\n\t" \
  "ds_read_b64_tr_b16 a[128:129], v234 offset:49152\n\t" \
  "v_exp_f32 v108, v108\n\t" \
  "v_mfma_f32_16x16x32_bf16 a[68:71], v[24:27], v[64:67], a[68:71]\n\t" \
  "v_cvt_pk_bf16_f32 v100, v104, v105\n\t" \
  "v_exp_f32 v109, v109\n\t" \
  "v_mul_f32 v15, v15, v226\n\t" \
  "v_mfma_f32_16x16x32_bf16 a[68:71], v[28:31], v[68:71], a[68:71]\n\t" \
  "buffer_store_dwordx4 v[40:43], v242, s[16:19], s98 offen offset:2048 sc1\n\t" \
  "v_add_f32 v221, v168, v169\n\t" \
  "v_rcp_f32 v220, v221\n\t" \
  "v_cmp_nge_f32 vcc, 0x5f800000, v221\n\t" \
  "v_mfma_f32_16x16x32_bf16 a[72:75], v[48:51], v[64:67], a[72:75]\n\t" \
  "s_cmp_lg_u64 vcc, 0\n\t" \
  "s_cselect_b32 s89, s78, s89\n\t" \
  "v_or_b32 v254, s89, v254\n\t" \
  "v_mfma_f32_16x16x32_bf16 a[72:75], v[52:55], v[68:71], a[72:75]\n\t" \
  "v_cvt_pk_bf16_f32 v44, v8, v9\n\t" \
  "v_cvt_pk_bf16_f32 v45, v10, v11\n\t" \
  "v_mfma_f32_16x16x32_bf16 a[76:79], v[56:59], v[64:67], a[76:79]\n\t" \
  "v_cvt_pk_bf16_f32 v101, v106, v107\n\t" \
  "v_exp_f32 v110, v110\n\t" \
  "v_mfma_f32_16x16x32_bf16 a[76:79], v[60:63], v[68:71], a[76:79]\n\t" \
  "ds_read_b64_tr_b16 a[130:131], v234 offset:53248\n\t" \
  "v_cvt_pk_bf16_f32 v46, v12, v13\n\t" \
  "v_mfma_f32_16x16x32_bf16 a[80:83], v[80:83], v[64:67], a[80:83]\n\t" \
  "v_cvt_pk_bf16_f32 v102, v108, v109\n\t" \
  "v_cvt_pk_bf16_f32 v47, v14, v15\n\t" \
  "v_mfma_f32_16x16x32_bf16 a[80:83], v[84:87], v[68:71], a[80:83]\n\t" \
  "ds_read_b64_tr_b16 a[132:133], v235 offset:49152\n\t" \
  "v_exp_f32 v111, v111\n\t" \
  "v_mfma_f32_16x16x32_bf16 a[84:87], v[88:91], v[64:67], a[84:87]\n\t" \
  "ds_read_b128 a[240:243], v213 offset:32768\n\t" \
  "v_cvt_pk_bf16_f32 v103, v110, v111\n\t" \
  "v_mfma_f32_16x16x32_bf16 a[84:87], v[92:95], v[68:71], a[84:87]\n\t" \
  "s_add_u32 s96, s40, 1048576\n\t" \
  "ds_read_b128 a[244:247], v213 offset:33792\n\t" \
  "s_and_b32 s96, s96, 0x1c0000\n\t" \
  "v_mfma_f32_16x16x32_bf16 a[88:91], v[112:115], v[64:67], a[88:91]\n\t" \
  "buffer_store_dwordx4 v[44:47], v242, s[16:19], s98 offen offset:3072 sc1\n\t" \
  "ds_read_b128 a[248:251], v213 offset:34816\n\t" \
  "ds_read_b128 a[252:255], v213 offset:35840\n\t" \
  "ds_read_b64_tr_b16 a[134:135], v235 offset:53248\n\t" \
  "v_mfma_f32_16x16x32_bf16 a[88:91], v[116:119], v[68:71], a[88:91]\n\t" \
  "ds_write_b128 v213, v[32:35] offset:32768\n\t" \
  "v_mfma_f32_16x16x32_bf16 a[92:95], v[120:123], v[64:67], a[92:95]\n\t" \
  "ds_write_b128 v213, v[36:39] offset:33792\n\t" \
  "v_mfma_f32_16x16x32_bf16 a[92:95], v[124:127], v[68:71], a[92:95]\n\t" \
  "s_waitcnt vmcnt(5) lgkmcnt(10)\n\t" \
  "s_add_u32 s96, s96, s51\n\t" \
  "s_add_u32 m0, s38, 98304\n\t" \
  "v_accvgpr_read_b32 v64, a64\n\t" \
  "v_accvgpr_read_b32 v65, a65\n\t" \
  "v_mfma_f32_16x16x32_bf16 a[96:99], v[16:19], v[96:99], a[96:99]\n\t" \
  "v_accvgpr_read_b32 v66, a66\n\t" \
  "v_accvgpr_read_b32 v67, a67\n\t" \
  "s_barrier\n\t" \
  "v_mfma_f32_16x16x32_bf16 a[96:99], v[20:23], v[100:103], a[96:99]\n\t" \
  "buffer_load_dwordx4 v240, s[24:27], s96 offen offset:0 lds\n\t" \
  "v_accvgpr_read_b32 v68, a68\n\t" \
  "v_accvgpr_read_b32 v69, a69\n\t" \
  "v_smfmac_f32_16x16x64_bf16 v[172:175], v[216:219], v[96:103], v208\n\t" \
  "s_xor_b32 s98, s45, 65536\n\t" \
  "ds_read_b128 v[128:131], v242 offset:16384\n\t" \
  "v_accvgpr_read_b32 v70, a70\n\t" \
  "v_accvgpr_read_b32 v71, a71\n\t" \
  "v_mfma_f32_16x16x32_bf16 a[100:103], v[24:27], v[96:99], a[100:103]\n\t" \
  "ds_read_b128 v[132:135], v242 offset:17408\n\t" \
  "v_mul_f32 v64, v64, v220\n\t" \
  "v_mfma_f32_16x16x32_bf16 a[100:103], v[28:31], v[100:103], a[100:103]\n\t" \
  "v_add_f32 v223, v172, v173\n\t" \
  "v_rcp_f32 v222, v223\n\t" \
  "v_mfma_f32_16x16x32_bf16 a[104:107], v[48:51], v[96:99], a[104:107]\n\t" \
  "buffer_load_dwordx4 v241, s[24:27], s96 offen offset:1024 lds\n\t" \
  "v_mfma_f32_16x16x32_bf16 a[104:107], v[52:55], v[100:103], a[104:107]\n\t" \
  "ds_read_b128 v[136:139], v242 offset:18432\n\t" \
  "v_cmp_nge_f32 vcc, 0x5f800000, v223\n\t" \
  "v_mfma_f32_16x16x32_bf16 a[108:111], v[56:59], v[96:99], a[108:111]\n\t" \
  "s_cmp_lg_u64 vcc, 0\n\t" \
  "s_cselect_b32 s89, s78, s89\n\t" \
  "v_or_b32 v254, s89, v254\n\t" \
  "v_mfma_f32_16x16x32_bf16 a[108:111], v[60:63], v[100:103], a[108:111]\n\t" \
  "buffer_load_dwordx4 v248, s[24:27], s96 offen offset:2048 lds\n\t" \
  "v_mul_f32 v65, v65, v220\n\t" \
  "v_mfma_f32_16x16x32_bf16 a[112:115], v[80:83], v[96:99], a[112:115]\n\t" \
  "s_cmp_lg_u32 s89, 0\n\t" \
  "s_cbranch_scc1 .Lat_x\n\t" \
  ".Lat_y:\n\t" \
  "v_mfma_f32_16x16x32_bf16 a[112:115], v[84:87], v[100:103], a[112:115]\n\t" \
  "ds_write_b128 v213, v[40:43] offset:34816\n\t" \
  "v_mfma_f32_16x16x32_bf16 a[116:119], v[88:91], v[96:99], a[116:119]\n\t" \
  "buffer_load_dwordx4 v249, s[24:27], s96 offen offset:3072 lds\n\t" \
  "ds_read_b128 v[140:143], v242 offset:19456\n\t" \
  "v_mul_f32 v66, v66, v220\n\t" \
  "v_mfma_f32_16x16x32_bf16 a[116:119], v[92:95], v[100:103], a[116:119]\n\t" \
  "ds_read_b64_tr_b16 a[136:137], v234 offset:57344\n\t" \
  "ds_read_b128 v[144:147], v242 offset:20480\n\t" \
  "v_mul_f32 v67, v67, v220\n\t" \
  "v_mfma_f32_16x16x32_bf16 a[120:123], v[112:115], v[96:99], a[120:123]\n\t" \
  "ds_read_b128 v[148:151], v242 offset:21504\n\t" \
  "v_mul_f32 v68, v68, v220\n\t" \
  "v_mfma_f32_16x16x32_bf16 a[120:123], v[116:119], v[100:103], a[120:123]\n\t" \
  "ds_write_b128 v213, v[44:47] offset:35840\n\t" \
  "v_mfma_f32_16x16x32_bf16 a[124:127], v[120:123], v[96:99], a[124:127]\n\t" \
  "ds_read_b64_tr_b16 a[138:139], v234 offset:61440\n\t" \
  "ds_read_b128 v[152:155], v242 offset:22528\n\t" \
  "v_mul_f32 v69, v69, v220\n\t" \
  "v_mul_f32 v70, v70, v220\n\t" \
  "v_mfma_f32_16x16x32_bf16 a[124:127], v[124:127], v[100:103], a[124:127]\n\t" \
  "s_waitcnt lgkmcnt(6)\n\t" \
  "ds_read_b64_tr_b16 a[140:141], v235 offset:57344\n\t" \
  "ds_read_b128 v[156:159], v242 offset:23552\n\t" \
  "v_mul_f32 v71, v71, v220\n\t" \
  ".Lat_join:\n\t" \
  STS("s_memrealtime s[94:95]\n\t") \
  STS("s_memtime s[92:93]\n\t") \
  "v_mfma_f32_16x16x32_bf16 v[0:3], a[192:195], v[128:131], 0\n\t" \
  "v_cvt_pk_bf16_f32 v112, v64, v65\n\t" \
  "v_cvt_pk_bf16_f32 v113, v66, v67\n\t" \
  "v_cvt_pk_bf16_f32 v114, v68, v69\n\t" \
  "v_cvt_pk_bf16_f32 v115, v70, v71\n\t" \
  "v_mfma_f32_16x16x32_bf16 v[0:3], a[200:203], v[132:135], v[0:3]\n\t" \
  "buffer_load_dword v246, v253, s[16:19], s65 offen sc1\n\t" \
  "v_accvgpr_read_b32 v72, a72\n\t" \
  "v_accvgpr_read_b32 v73, a73\n\t" \
  "v_accvgpr_read_b32 v74, a74\n\t" \
  "v_mfma_f32_16x16x32_bf16 v[0:3], a[208:211], v[136:139], v[0:3]\n\t" \
  "v_accvgpr_read_b32 v75, a75\n\t" \
  "v_accvgpr_read_b32 v76, a76\n\t" \
  "v_accvgpr_read_b32 v77, a77\n\t" \
  "v_accvgpr_read_b32 v78, a78\n\t" \
  "v_mfma_f32_16x16x32_bf16 v[0:3], a[216:219], v[140:143], v[0:3]\n\t" \
  "ds_read_b128 v[160:163], v242 offset:24576\n\t" \
  "v_accvgpr_read_b32 v79, a79\n\t" \
  "v_mul_f32 v72, v72, v220\n\t" \
  "v_mul_f32 v73, v73, v220\n\t" \
  "v_mfma_f32_16x16x32_bf16 v[16:19], a[220:223], v[140:143], 0\n\t" \
  "ds_read_b128 v[164:167], v242 offset:25600\n\t" \
  "v_mul_f32 v74, v74, v220\n\t" \
  "v_mul_f32 v75, v75, v220\n\t" \
  "v_mul_f32 v76, v76, v220\n\t" \
  "v_mfma_f32_16x16x32_bf16 v[16:19], a[212:215], v[136:139], v[16:19]\n\t" \
  "ds_read_b128 v[168:171], v242 offset:26624\n\t" \
  "v_mul_f32 v77, v77, v220\n\t" \
  "v_mul_f32 v78, v78, v220\n\t" \
  "v_mul_f32 v79, v79, v220\n\t" \
  "v_mfma_f32_16x16x32_bf16 v[16:19], a[204:207], v[132:135], v[16:19]\n\t" \
  "ds_read_b128 v[172:175], v242 offset:27648\n\t" \
  "v_cvt_pk_bf16_f32 v116, v72, v73\n\t" \
  "v_cvt_pk_bf16_f32 v117, v74, v75\n\t" \
  "v_cvt_pk_bf16_f32 v118, v76, v77\n\t" \
  "v_mfma_f32_16x16x32_bf16 v[16:19], a[196:199], v[128:131], v[16:19]\n\t" \
  "s_waitcnt lgkmcnt(4)\n\t" \
  "ds_read_b64_tr_b16 a[142:143], v235 offset:61440\n\t" \
  "v_cvt_pk_bf16_f32 v119, v78, v79\n\t" \
  "v_accvgpr_read_b32 v64, a80\n\t" \
  "v_accvgpr_read_b32 v65, a81\n\t" \
  "v_mfma_f32_16x16x32_bf16 v[4:7], a[192:195], v[144:147], 0\n\t" \
  "buffer_store_dwordx4 v[112:115], v242, s[16:19], s98 offen offset:0 sc1\n\t" \
  "v_accvgpr_read_b32 v66, a82\n\t" \
  "ds_read_b128 v[128:131], v242 offset:28672\n\t" \
  "v_accvgpr_read_b32 v67, a83\n\t" \
  "v_mfma_f32_16x16x32_bf16 v[4:7], a[200:203], v[148:151], v[4:7]\n\t" \
  "v_accvgpr_read_b32 v68, a84\n\t" \
  "v_accvgpr_read_b32 v69, a85\n\t" \
  "v_accvgpr_read_b32 v70, a86\n\t" \
  "v_accvgpr_read_b32 v71, a87\n\t" \
  "v_mfma_f32_16x16x32_bf16 v[4:7], a[208:211], v[152:155], v[4:7]\n\t" \
  "ds_read_b64_tr_b16 a[144:145], v236 offset:0\n\t" \
  "ds_read_b128 v[132:135], v242 offset:29696\n\t" \
  "v_mul_f32 v64, v64, v220\n\t" \
  "v_mul_f32 v65, v65, v220\n\t" \
  "v_mfma_f32_16x16x32_bf16 v[4:7], a[216:219], v[156:159], v[4:7]\n\t" \
  "v_mul_f32 v66, v66, v220\n\t" \
  "v_mul_f32 v67, v67, v220\n\t" \
  "v_mul_f32 v68, v68, v220\n\t" \
  "v_mul_f32 v69, v69, v220\n\t" \
  "v_mfma_f32_16x16x32_bf16 v[20:23], a[220:223], v[156:159], 0\n\t" \
  STS("v_writelane_b32 v255, s94, 22\n\t") \
  STS("v_writelane_b32 v255, s92, 23\n\t") \
  "ds_read_b64_tr_b16 a[146:147], v236 offset:4096\n\t" \
  "ds_read_b128 v[136:139], v242 offset:30720\n\t" \
  "v_mul_f32 v70, v70, v220\n\t" \
  "v_mul_f32 v71, v71, v220\n\t" \
  "v_mfma_f32_16x16x32_bf16 v[20:23], a[212:215], v[152:155], v[20:23]\n\t" \
  "v_cvt_pk_bf16_f32 v120, v64, v65\n\t" \
  "v_cvt_pk_bf16_f32 v121, v66, v67\n\t" \
  "v_cvt_pk_bf16_f32 v122, v68, v69\n\t" \
  "v_cvt_pk_bf16_f32 v123, v70, v71\n\t" \
  "v_mfma_f32_16x16x32_bf16 v[20:23], a[204:207], v[148:151], v[20:23]\n\t" \
  "s_waitcnt lgkmcnt(5)\n\t" \
  "ds_read_b64_tr_b16 a[148:149], v237 offset:0\n\t" \
  "ds_read_b128 v[140:143], v242 offset:31744\n\t" \
  "v_accvgpr_read_b32 v72, a88\n\t" \
  "v_accvgpr_read_b32 v73, a89\n\t" \
  "v_mfma_f32_16x16x32_bf16 v[20:23], a[196:199], v[144:147], v[20:23]\n\t" \
  "v_accvgpr_read_b32 v74, a90\n\t" \
  "v_accvgpr_read_b32 v75, a91\n\t" \
  "s_barrier\n\t" \
  "v_mfma_f32_16x16x32_bf16 v[8:11], a[192:195], v[160:163], 0\n\t" \
  "ds_read_b128 v[144:147], v242 offset:32768\n\t" \
  "v_accvgpr_read_b32 v76, a92\n\t" \
  "v_accvgpr_read_b32 v77, a93\n\t" \
  "v_accvgpr_read_b32 v78, a94\n\t" \
  "v_mfma_f32_16x16x32_bf16 v[8:11], a[200:203], v[164:167], v[8:11]\n\t" \
  "ds_read_b32 v209, v213 offset:0\n\t" \
  "ds_read_b64_tr_b16 a[150:151], v237 offset:4096\n\t" \
  "ds_read_b128 v[148:151], v242 offset:33792\n\t" \
  "v_accvgpr_read_b32 v79, a95\n\t" \
  "v_mfma_f32_16x16x32_bf16 v[8:11], a[208:211], v[168:171], v[8:11]\n\t" \
  "buffer_store_dwordx4 v[116:119], v242, s[16:19], s98 offen offset:1024 sc1\n\t" \
  "ds_read_b64_tr_b16 a[152:153], v236 offset:8192\n\t" \
  "ds_read_b128 v[152:155], v242 offset:34816\n\t" \
  "ds_read_b64_tr_b16 a[154:155], v236 offset:12288\n\t" \
  "v_mfma_f32_16x16x32_bf16 v[8:11], a[216:219], v[172:175], v[8:11]\n\t" \
  "ds_read_b64_tr_b16 a[156:157], v237 offset:8192\n\t" \
  "v_mul_f32 v72, v72, v220\n\t" \
  "v_mfma_f32_16x16x32_bf16 v[24:27], a[220:223], v[172:175], 0\n\t" \
  "v_mul_f32 v73, v73, v220\n\t" \
  "v_mul_f32 v74, v74, v220\n\t" \
  "v_mfma_f32_16x16x32_bf16 v[24:27], a[212:215], v[168:171], v[24:27]\n\t" \
  "ds_read_b128 v[156:159], v242 offset:35840\n\t" \
  "v_mul_f32 v75, v75, v220\n\t" \
  "v_mfma_f32_16x16x32_bf16 v[24:27], a[204:207], v[164:167], v[24:27]\n\t" \
  "s_waitcnt lgkmcnt(7)\n\t" \
  "v_cmp_eq_u32 vcc, s36, v209\n\t" \
  "v_mul_f32 v76, v76, v220\n\t" \
  "v_mfma_f32_16x16x32_bf16 v[24:27], a[196:199], v[160:163], v[24:27]\n\t" \
  "s_and_b32 vcc_lo, vcc_lo, s37\n\t" \
  "s_cbranch_scc1 .Lat_rd\n\t" \
  "v_mfma_f32_16x16x32_bf16 v[12:15], a[192:195], v[128:131], 0\n\t" \
  "s_waitcnt vmcnt(7)\n\t" \
  "buffer_store_dword v243, v254, s[16:19], s49 offen offset:32 sc1\n\t" \
  "ds_read_b128 v[160:163], v242 offset:36864\n\t" \
  "v_mfma_f32_16x16x32_bf16 v[12:15], a[200:203], v[132:135], v[12:15]\n\t" \
  "ds_write_b128 v213, v[112:115] offset:0\n\t" \
  "v_mfma_f32_16x16x32_bf16 v[12:15], a[208:211], v[136:139], v[12:15]\n\t" \
  "ds_read_b64_tr_b16 a[158:159], v237 offset:12288\n\t" \
  "ds_read_b128 v[164:167], v242 offset:37888\n\t" \
  "v_mul_f32 v77, v77, v220\n\t" \
  "v_mfma_f32_16x16x32_bf16 v[12:15], a[216:219], v[140:143], v[12:15]\n\t" \
  "ds_read_b64_tr_b16 a[160:161], v238 offset:0\n\t" \
  "ds_read_b128 v[168:171], v242 offset:38912\n\t" \
  "v_mul_f32 v78, v78, v220\n\t" \
  "v_mfma_f32_16x16x32_bf16 v[28:31], a[220:223], v[140:143], 0\n\t" \
  "ds_write_b128 v213, v[116:119] offset:1024\n\t" \
  "v_mfma_f32_16x16x32_bf16 v[28:31], a[212:215], v[136:139], v[28:31]\n\t" \
  "ds_write_b128 v213, v[120:123] offset:2048\n\t" \
  "v_mfma_f32_16x16x32_bf16 v[28:31], a[204:207], v[132:135], v[28:31]\n\t" \
  "s_add_u32 s97, s64, 131072\n\t" \
  "ds_read_b64_tr_b16 a[162:163], v238 offset:4096\n\t" \
  "ds_read_b128 v[172:175], v242 offset:39936\n\t" \
  "v_mul_f32 v79, v79, v220\n\t" \
  "v_mfma_f32_16x16x32_bf16 v[28:31], a[196:199], v[128:131], v[28:31]\n\t" \
  "s_waitcnt lgkmcnt(10)\n\t" \
  "s_waitcnt vmcnt(3)\n\t" \
  "v_cmp_ne_u32 vcc, s36, v246\n\t" \
  "v_cvt_pk_bf16_f32 v124, v72, v73\n\t" \
  STP("s_memrealtime s[94:95]\n\t") \
  STP("s_memtime s[92:93]\n\t") \
  "v_mfma_f32_16x16x32_bf16 v[32:35], a[192:195], v[144:147], 0\n\t" \
  "s_and_b32 s97, s97, 0xe0000\n\t" \
  "s_add_u32 s97, s97, s60\n\t" \
  "s_add_u32 m0, s38, 49152\n\t" \
  "v_mfma_f32_16x16x32_bf16 v[32:35], a[200:203], v[148:151], v[32:35]\n\t" \
  "s_and_b32 vcc_lo, vcc_lo, 0xe\n\t" \
  "s_cbranch_scc1 .Lat_s0\n\t" \
  ".Lat_k0:\n\t" \
  "ds_read_b128 v[128:131], v242 offset:40960\n\t" \
  "v_cvt_pk_bf16_f32 v125, v74, v75\n\t" \
  "v_mfma_f32_16x16x32_bf16 v[32:35], a[208:211], v[152:155], v[32:35]\n\t" \
  "buffer_load_dwordx4 v242, s[16:19], s97 offen offset:0 sc1 lds\n\t" \
  "v_mfma_f32_16x16x32_bf16 v[32:35], a[216:219], v[156:159], v[32:35]\n\t" \
  "ds_read_b64_tr_b16 a[164:165], v239 offset:0\n\t" \
  "ds_read_b128 v[132:135], v242 offset:41984\n\t" \
  "v_cvt_pk_bf16_f32 v126, v76, v77\n\t" \
  "v_mfma_f32_16x16x32_bf16 v[48:51], a[220:223], v[156:159], 0\n\t" \
  "ds_read_b128 v[136:139], v242 offset:43008\n\t" \
  "v_cvt_pk_bf16_f32 v127, v78, v79\n\t" \
  "v_mfma_f32_16x16x32_bf16 v[48:51], a[212:215], v[152:155], v[48:51]\n\t" \
  "buffer_load_dwordx4 v242, s[16:19], s97 offen offset:1024 sc1 lds\n\t" \
  "ds_write_b128 v213, v[124:127] offset:3072\n\t" \
  "v_mfma_f32_16x16x32_bf16 v[48:51], a[204:207], v[148:151], v[48:51]\n\t" \
  "ds_read_b64_tr_b16 a[166:167], v239 offset:4096\n\t" \
  "v_mfma_f32_16x16x32_bf16 v[48:51], a[196:199], v[144:147], v[48:51]\n\t" \
  "s_waitcnt lgkmcnt(6)\n\t" \
  "ds_read_b64_tr_b16 a[168:169], v238 offset:8192\n\t" \
  "ds_read_b128 v[140:143], v242 offset:44032\n\t" \
  "v_mfma_f32_16x16x32_bf16 v[36:39], a[192:195], v[160:163], 0\n\t" \
  "buffer_load_dwordx4 v242, s[16:19], s97 offen offset:2048 sc1 lds\n\t" \
  "v_mfma_f32_16x16x32_bf16 v[36:39], a[200:203], v[164:167], v[36:39]\n\t" \
  "ds_read_b64_tr_b16 a[170:171], v238 offset:12288\n\t" \
  "ds_read_b128 v[144:147], v242 offset:45056\n\t" \
  "v_mfma_f32_16x16x32_bf16 v[36:39], a[208:211], v[168:171], v[36:39]\n\t" \
  "ds_read_b64_tr_b16 a[172:173], v239 offset:8192\n\t" \
  "ds_read_b128 v[148:151], v242 offset:46080\n\t" \
  "v_mfma_f32_16x16x32_bf16 v[36:39], a[216:219], v[172:175], v[36:39]\n\t" \
  "buffer_load_dwordx4 v242, s[16:19], s97 offen offset:3072 sc1 lds\n\t" \
  "v_mfma_f32_16x16x32_bf16 v[52:55], a[220:223], v[172:175], 0\n\t" \
  STP("v_writelane_b32 v255, s94, 24\n\t") \
  STP("v_writelane_b32 v255, s92, 25\n\t") \
  "ds_read_b128 v[152:155], v242 offset:47104\n\t" \
  "v_mfma_f32_16x16x32_bf16 v[52:55], a[212:215], v[168:171], v[52:55]\n\t" \
  "ds_read_b64_tr_b16 a[174:175], v239 offset:12288\n\t" \
  "v_mfma_f32_16x16x32_bf16 v[52:55], a[204:207], v[164:167], v[52:55]\n\t" \
  "s_waitcnt lgkmcnt(6)\n\t" \
  "buffer_store_dwordx4 v[120:123], v242, s[16:19], s98 offen offset:2048 sc1\n\t" \
  "ds_read_b64_tr_b16 a[176:177], v236 offset:16384\n\t" \
  "ds_read_b64_tr_b16 a[178:179], v236 offset:20480\n\t" \
  "ds_read_b128 v[156:159], v242 offset:48128\n\t" \
  "v_mfma_f32_16x16x32_bf16 v[52:55], a[196:199], v[160:163], v[52:55]\n\t" \
  "s_barrier\n\t" \
  "v_mfma_f32_16x16x32_bf16 v[40:43], a[192:195], v[128:131], 0\n\t" \
  "ds_read_b128 v[160:163], v242 offset:0\n\t" \
  "v_mfma_f32_16x16x32_bf16 v[40:43], a[200:203], v[132:135], v[40:43]\n\t" \
  "ds_read_b64_tr_b16 a[180:181], v237 offset:16384\n\t" \
  "v_mfma_f32_16x16x32_bf16 v[40:43], a[208:211], v[136:139], v[40:43]\n\t" \
  "ds_read_b128 v[164:167], v242 offset:1024\n\t" \
  "v_mfma_f32_16x16x32_bf16 v[40:43], a[216:219], v[140:143], v[40:43]\n\t" \
  "ds_read_b64_tr_b16 a[182:183], v237 offset:20480\n\t" \
  "v_mfma_f32_16x16x32_bf16 v[56:59], a[220:223], v[140:143], 0\n\t" \
  "ds_read_b128 v[168:171], v242 offset:2048\n\t" \
  "v_mfma_f32_16x16x32_bf16 v[56:59], a[212:215], v[136:139], v[56:59]\n\t" \
  "ds_read_b128 v[172:175], v242 offset:3072\n\t" \
  "v_mfma_f32_16x16x32_bf16 v[56:59], a[204:207], v[132:135], v[56:59]\n\t" \
  "ds_read_b64_tr_b16 a[184:185], v236 offset:24576\n\t" \
  "v_mfma_f32_16x16x32_bf16 v[56:59], a[196:199], v[128:131], v[56:59]\n\t" \
  "s_waitcnt lgkmcnt(7)\n\t" \
  "ds_read_b64_tr_b16 a[186:187], v236 offset:28672\n\t" \
  "v_mfma_f32_16x16x32_bf16 v[44:47], a[192:195], v[144:147], 0\n\t" \
  "ds_read_b128 v[128:131], v242 offset:4096\n\t" \
  "v_mfma_f32_16x16x32_bf16 v[44:47], a[200:203], v[148:151], v[44:47]\n\t" \
  "ds_read_b128 v[132:135], v242 offset:5120\n\t" \
  "v_mfma_f32_16x16x32_bf16 v[44:47], a[208:211], v[152:155], v[44:47]\n\t" \
  "ds_read_b128 v[136:139], v242 offset:6144\n\t" \
  "v_mfma_f32_16x16x32_bf16 v[44:47], a[216:219], v[156:159], v[44:47]\n\t" \
  "ds_read_b64_tr_b16 a[188:189], v237 offset:24576\n\t" \
  "v_mfma_f32_16x16x32_bf16 v[60:63], a[220:223], v[156:159], 0\n\t" \
  "ds_read_b128 v[140:143], v242 offset:7168\n\t" \
  "v_mfma_f32_16x16x32_bf16 v[60:63], a[212:215], v[152:155], v[60:63]\n\t" \
  "ds_read_b64_tr_b16 a[190:191], v237 offset:28672\n\t" \
  "v_mfma_f32_16x16x32_bf16 v[60:63], a[204:207], v[148:151], v[60:63]\n\t" \
  "ds_read_b64_tr_b16 a[0:1], v236 offset:49152\n\t" \
  "v_mfma_f32_16x16x32_bf16 v[60:63], a[196:199], v[144:147], v[60:63]\n\t" \
  "s_waitcnt lgkmcnt(9)\n\t" \
  "ds_read_b64_tr_b16 a[2:3], v236 offset:53248\n\t" \
  STP("s_memrealtime s[94:95]\n\t") \
  STP("s_memtime s[92:93]\n\t") \
  "v_mfma_f32_16x16x32_bf16 v[64:67], a[192:195], v[160:163], 0\n\t" \
  "ds_read_b128 v[144:147], v242 offset:8192\n\t" \
  "v_mfma_f32_16x16x32_bf16 v[64:67], a[200:203], v[164:167], v[64:67]\n\t" \
  "s_add_u32 s97, s64, 262144\n\t" \
  "ds_read_b128 v[148:151], v242 offset:9216\n\t" \
  "s_and_b32 s97, s97, 0xe0000\n\t" \
  "v_mfma_f32_16x16x32_bf16 v[64:67], a[208:211], v[168:171], v[64:67]\n\t" \
  "s_add_u32 s97, s97, s60\n\t" \
  "ds_read_b128 v[152:155], v242 offset:10240\n\t" \
  "s_add_u32 m0, s38, 65536\n\t" \
  "v_mfma_f32_16x16x32_bf16 v[64:67], a[216:219], v[172:175], v[64:67]\n\t" \
  "ds_read_b128 v[156:159], v242 offset:11264\n\t" \
  "v_mfma_f32_16x16x32_bf16 v[80:83], a[220:223], v[172:175], 0\n\t" \
  "ds_read_b64_tr_b16 a[4:5], v237 offset:49152\n\t" \
  "v_mfma_f32_16x16x32_bf16 v[80:83], a[212:215], v[168:171], v[80:83]\n\t" \
  "buffer_load_dword v246, v253, s[16:19], s65 offen sc1\n\t" \
  "v_mfma_f32_16x16x32_bf16 v[80:83], a[204:207], v[164:167], v[80:83]\n\t" \
  "ds_read_b64_tr_b16 a[6:7], v237 offset:53248\n\t" \
  "v_mfma_f32_16x16x32_bf16 v[80:83], a[196:199], v[160:163], v[80:83]\n\t" \
  "s_waitcnt lgkmcnt(9)\n\t" \
  "buffer_load_dwordx4 v242, s[16:19], s97 offen offset:0 sc1 lds\n\t" \
  "v_mfma_f32_16x16x32_bf16 v[68:71], a[192:195], v[128:131], 0\n\t" \
  "s_mov_b64 exec, 0\n\t" \
  "s_cmp_eq_u32 s3, 0\n\t" \
  "s_cbranch_scc1 .Lhc10\n\t" \
  "s_cmp_eq_u32 s3, 1\n\t" \
  "s_cbranch_scc1 .Lhc11\n\t" \
  "s_cmp_eq_u32 s3, 2\n\t" \
  "s_cbranch_scc1 .Lhc12\n\t" \
  "s_branch .Lhc13\n\t" \
  ".Lhr1:\n\t" \
  "s_mov_b64 exec, -1\n\t" \
  "v_mfma_f32_16x16x32_bf16 v[68:71], a[200:203], v[132:135], v[68:71]\n\t" \
  "ds_read_b128 v[160:163], v242 offset:12288\n\t" \
  "v_mfma_f32_16x16x32_bf16 v[68:71], a[208:211], v[136:139], v[68:71]\n\t" \
  "buffer_load_dwordx4 v242, s[16:19], s97 offen offset:1024 sc1 lds\n\t" \
  "v_mfma_f32_16x16x32_bf16 v[68:71], a[216:219], v[140:143], v[68:71]\n\t" \
  "ds_read_b64_tr_b16 a[8:9], v236 offset:57344\n\t" \
  "ds_read_b128 v[164:167], v242 offset:13312\n\t" \
  "v_mfma_f32_16x16x32_bf16 v[84:87], a[220:223], v[140:143], 0\n\t" \
  STP("v_writelane_b32 v255, s94, 26\n\t") \
  STP("v_writelane_b32 v255, s92, 27\n\t") \
  "ds_read_b128 v[168:171], v242 offset:14336\n\t" \
  "v_mfma_f32_16x16x32_bf16 v[84:87], a[212:215], v[136:139], v[84:87]\n\t" \
  "buffer_load_dwordx4 v242, s[16:19], s97 offen offset:2048 sc1 lds\n\t" \
  "v_mfma_f32_16x16x32_bf16 v[84:87], a[204:207], v[132:135], v[84:87]\n\t" \
  "s_waitcnt vmcnt(5) lgkmcnt(6)\n\t" \
  "ds_read_b128 v[172:175], v242 offset:15360\n\t" \
  "v_mfma_f32_16x16x32_bf16 v[84:87], a[196:199], v[128:131], v[84:87]\n\t" \
  "s_barrier\n\t" \
  "v_mfma_f32_16x16x32_bf16 v[72:75], a[192:195], v[144:147], 0\n\t" \
  "buffer_load_dwordx4 v242, s[16:19], s97 offen offset:3072 sc1 lds\n\t" \
  "v_mfma_f32_16x16x32_bf16 v[72:75], a[200:203], v[148:151], v[72:75]\n\t" \
  "s_add_u32 s97, s64, 393216\n\t" \
  "s_and_b32 s97, s97, 0xe0000\n\t" \
  "s_add_u32 s97, s97, s60\n\t" \
  "v_mfma_f32_16x16x32_bf16 v[72:75], a[208:211], v[152:155], v[72:75]\n\t" \
  "s_add_u32 m0, s38, 81920\n\t" \
  "ds_read_b64_tr_b16 a[10:11], v236 offset:61440\n\t" \
  "ds_read_b128 v[128:131], v242 offset:49152\n\t" \
  "v_mfma_f32_16x16x32_bf16 v[72:75], a[216:219], v[156:159], v[72:75]\n\t" \
  "buffer_load_dwordx4 v242, s[16:19], s97 offen offset:0 sc1 lds\n\t" \
  "v_mfma_f32_16x16x32_bf16 v[88:91], a[220:223], v[156:159], 0\n\t" \
  "ds_read_b64_tr_b16 a[12:13], v237 offset:57344\n\t" \
  "ds_read_b128 v[132:135], v242 offset:50176\n\t" \
  "v_mfma_f32_16x16x32_bf16 v[88:91], a[212:215], v[152:155], v[88:91]\n\t" \
  "ds_read_b64_tr_b16 a[14:15], v237 offset:61440\n\t" \
  "ds_read_b128 v[136:139], v242 offset:51200\n\t" \
  "v_mfma_f32_16x16x32_bf16 v[88:91], a[204:207], v[148:151], v[88:91]\n\t" \
  "buffer_load_dwordx4 v242, s[16:19], s97 offen offset:1024 sc1 lds\n\t" \
  "v_mfma_f32_16x16x32_bf16 v[88:91], a[196:199], v[144:147], v[88:91]\n\t" \
  "s_waitcnt lgkmcnt(6)\n\t" \
  "ds_read_b64_tr_b16 a[16:17], v238 offset:16384\n\t" \
  "ds_read_b128 v[140:143], v242 offset:52224\n\t" \
  "v_mfma_f32_16x16x32_bf16 v[76:79], a[192:195], v[160:163], 0\n\t" \
  "ds_read_b128 v[144:147], v242 offset:53248\n\t" \
  "v_mfma_f32_16x16x32_bf16 v[76:79], a[200:203], v[164:167], v[76:79]\n\t" \
  "buffer_load_dwordx4 v242, s[16:19], s97 offen offset:2048 sc1 lds\n\t" \
  "v_mfma_f32_16x16x32_bf16 v[76:79], a[208:211], v[168:171], v[76:79]\n\t" \
  "ds_read_b128 v[148:151], v242 offset:54272\n\t" \
  "v_mfma_f32_16x16x32_bf16 v[76:79], a[216:219], v[172:175], v[76:79]\n\t" \
  "ds_read_b128 v[152:155], v242 offset:55296\n\t" \
  "v_mfma_f32_16x16x32_bf16 v[92:95], a[220:223], v[172:175], 0\n\t" \
  "buffer_load_dwordx4 v242, s[16:19], s97 offen offset:3072 sc1 lds\n\t" \
  "v_mfma_f32_16x16x32_bf16 v[92:95], a[212:215], v[168:171], v[92:95]\n\t" \
  "s_add_u32 s96, s40, 1048576\n\t" \
  "s_and_b32 s96, s96, 0x1c0000\n\t" \
  "s_add_u32 s96, s96, s77\n\t" \
  "v_mfma_f32_16x16x32_bf16 v[92:95], a[204:207], v[164:167], v[92:95]\n\t" \
  "s_add_u32 m0, s38, 131072\n\t" \
  "ds_read_b64_tr_b16 a[18:19], v238 offset:20480\n\t" \
  "ds_read_b128 v[156:159], v242 offset:56320\n\t" \
  ".Lhs11:\n\ts_cbranch_execz .Lhs11n\n\t" \
  "v_mfma_f32_16x16x32_bf16 v[92:95], a[196:199], v[160:163], v[92:95]\n\t" \
  "s_waitcnt lgkmcnt(5)\n\t" \
  "buffer_load_dwordx4 v240, s[24:27], s96 offen offset:0 lds\n\t" \
  STP("s_memrealtime s[94:95]\n\t") \
  STP("s_memtime s[92:93]\n\t") \
  "v_mfma_f32_16x16x32_bf16 v[0:3], a[128:131], v[128:131], v[0:3]\n\t" \
  "ds_read_b64_tr_b16 a[20:21], v239 offset:16384\n\t" \
  "ds_read_b128 v[160:163], v242 offset:57344\n\t" \
  "v_accvgpr_read_b32 v96, a96\n\t" \
  "v_mfma_f32_16x16x32_bf16 v[0:3], a[136:139], v[132:135], v[0:3]\n\t" \
  "ds_read_b64_tr_b16 a[22:23], v239 offset:20480\n\t" \
  "ds_read_b128 v[164:167], v242 offset:58368\n\t" \
  "v_accvgpr_read_b32 v97, a97\n\t" \
  "v_mfma_f32_16x16x32_bf16 v[0:3], a[144:147], v[136:139], v[0:3]\n\t" \
  "buffer_load_dwordx4 v241, s[24:27], s96 offen offset:1024 lds\n\t" \
  "v_mfma_f32_16x16x32_bf16 v[0:3], a[152:155], v[140:143], v[0:3]\n\t" \
  "ds_read_b64_tr_b16 a[24:25], v238 offset:24576\n\t" \
  "ds_read_b128 v[168:171], v242 offset:59392\n\t" \
  "v_accvgpr_read_b32 v98, a98\n\t" \
  "v_mfma_f32_16x16x32_bf16 v[16:19], a[156:159], v[140:143], v[16:19]\n\t" \
  "s_waitcnt vmcnt(10)\n\t" \
  "v_cmp_ne_u32 vcc, s36, v246\n\t" \
  "v_accvgpr_read_b32 v99, a99\n\t" \
  "v_mfma_f32_16x16x32_bf16 v[16:19], a[148:151], v[136:139], v[16:19]\n\t" \
  "buffer_load_dwordx4 v248, s[24:27], s96 offen offset:2048 lds\n\t" \
  "v_mfma_f32_16x16x32_bf16 v[16:19], a[140:143], v[132:135], v[16:19]\n\t" \
  "s_and_b32 vcc_lo, vcc_lo, 0xfefe\n\t" \
  "s_cbranch_scc1 .Lat_s1\n\t" \
  ".Lat_k1:\n\t" \
  "v_mfma_f32_16x16x32_bf16 v[16:19], a[132:135], v[128:131], v[16:19]\n\t" \
  "s_waitcnt lgkmcnt(6)\n\t" \
  "ds_read_b64_tr_b16 a[26:27], v238 offset:28672\n\t" \
  "ds_read_b128 v[172:175], v242 offset:60416\n\t" \
  "v_accvgpr_read_b32 v100, a100\n\t" \
  "v_mfma_f32_16x16x32_bf16 v[4:7], a[128:131], v[144:147], v[4:7]\n\t" \
  "buffer_load_dwordx4 v249, s[24:27], s96 offen offset:3072 lds\n\t" \
  "v_mfma_f32_16x16x32_bf16 v[4:7], a[136:139], v[148:151], v[4:7]\n\t" \
  "s_add_u32 s97, s64, 131072\n\t" \
  "s_and_b32 s97, s97, 0xe0000\n\t" \
  "ds_read_b128 v[128:131], v242 offset:61440\n\t" \
  "s_add_u32 s97, s97, s61\n\t" \
  "v_mfma_f32_16x16x32_bf16 v[4:7], a[144:147], v[152:155], v[4:7]\n\t" \
  "s_add_u32 m0, s38, 32768\n\t" \
  "ds_read_b64_tr_b16 a[28:29], v239 offset:24576\n\t" \
  "ds_read_b128 v[132:135], v242 offset:62464\n\t" \
  "v_accvgpr_read_b32 v101, a101\n\t" \
  "v_mfma_f32_16x16x32_bf16 v[4:7], a[152:155], v[156:159], v[4:7]\n\t" \
  "buffer_load_dwordx4 v242, s[16:19], s97 offen offset:0 sc1 lds\n\t" \
  "v_mfma_f32_16x16x32_bf16 v[20:23], a[156:159], v[156:159], v[20:23]\n\t" \
  STP("v_writelane_b32 v255, s94, 28\n\t") \
  STP("v_writelane_b32 v255, s92, 29\n\t") \
  "ds_read_b128 v[136:139], v242 offset:63488\n\t" \
  "v_accvgpr_read_b32 v102, a102\n\t" \
  "v_mfma_f32_16x16x32_bf16 v[20:23], a[148:151], v[152:155], v[20:23]\n\t" \
  "ds_read_b64_tr_b16 a[30:31], v239 offset:28672\n\t" \
  "v_accvgpr_read_b32 v103, a103\n\t" \
  "v_mfma_f32_16x16x32_bf16 v[20:23], a[140:143], v[148:151], v[20:23]\n\t" \
  "s_waitcnt vmcnt(9) lgkmcnt(5)\n\t" \
  "buffer_load_dwordx4 v242, s[16:19], s97 offen offset:1024 sc1 lds\n\t" \
  "v_mfma_f32_16x16x32_bf16 v[20:23], a[132:135], v[144:147], v[20:23]\n\t" \
  "s_barrier\n\t" \
  "v_mfma_f32_16x16x32_bf16 v[8:11], a[128:131], v[160:163], v[8:11]\n\t" \
  "ds_read_b128 v[140:143], v242 offset:64512\n\t" \
  "v_mul_f32 v96, v96, v222\n\t" \
  "v_mfma_f32_16x16x32_bf16 v[8:11], a[136:139], v[164:167], v[8:11]\n\t" \
  "buffer_load_dwordx4 v242, s[16:19], s97 offen offset:2048 sc1 lds\n\t" \
  "v_mfma_f32_16x16x32_bf16 v[8:11], a[144:147], v[168:171], v[8:11]\n\t" \
  "ds_read_b128 v[144:147], v230 offset:0\n\t" \
  "v_mul_f32 v97, v97, v222\n\t" \
  "v_mfma_f32_16x16x32_bf16 v[8:11], a[152:155], v[172:175], v[8:11]\n\t" \
  "ds_read_b128 v[148:151], v230 offset:1024\n\t" \
  "v_mul_f32 v98, v98, v222\n\t" \
  "v_mfma_f32_16x16x32_bf16 v[24:27], a[156:159], v[172:175], v[24:27]\n\t" \
  "buffer_load_dwordx4 v242, s[16:19], s97 offen offset:3072 sc1 lds\n\t" \
  "v_mfma_f32_16x16x32_bf16 v[24:27], a[148:151], v[168:171], v[24:27]\n\t" \
  "ds_read_b128 v[152:155], v230 offset:2048\n\t" \
  "v_mul_f32 v99, v99, v222\n\t" \
  "v_mfma_f32_16x16x32_bf16 v[24:27], a[140:143], v[164:167], v[24:27]\n\t" \
  "ds_read_b64_tr_b16 a[32:33], v236 offset:32768\n\t" \
  "v_mul_f32 v100, v100, v222\n\t" \
  "v_mfma_f32_16x16x32_bf16 v[24:27], a[132:135], v[160:163], v[24:27]\n\t" \
  "s_waitcnt lgkmcnt(4)\n\t" \
  "buffer_store_dwordx4 v[124:127], v242, s[16:19], s98 offen offset:3072 sc1\n\t" \
  "ds_read_b64_tr_b16 a[34:35], v236 offset:36864\n\t" \
  "ds_read_b64_tr_b16 a[36:37], v237 offset:32768\n\t" \
  "ds_read_b128 v[156:159], v230 offset:3072\n\t" \
  "v_mfma_f32_16x16x32_bf16 v[12:15], a[128:131], v[128:131], v[12:15]\n\t" \
  "s_xor_b32 s98, s45, 98304\n\t" \
  "ds_read_b128 v[160:163], v230 offset:4096\n\t" \
  "v_mul_f32 v101, v101, v222\n\t" \
  "v_mfma_f32_16x16x32_bf16 v[12:15], a[136:139], v[132:135], v[12:15]\n\t" \
  "ds_read_b64_tr_b16 a[38:39], v237 offset:36864\n\t" \
  "v_mul_f32 v102, v102, v222\n\t" \
  "v_mfma_f32_16x16x32_bf16 v[12:15], a[144:147], v[136:139], v[12:15]\n\t" \
  "s_add_u32 s97, s64, 524288\n\t" \
  "ds_read_b128 v[164:167], v230 offset:5120\n\t" \
  "v_mul_f32 v103, v103, v222\n\t" \
  "v_mfma_f32_16x16x32_bf16 v[12:15], a[152:155], v[140:143], v[12:15]\n\t" \
  "s_and_b32 s97, s97, 0xe0000\n\t" \
  "ds_read_b64_tr_b16 a[40:41], v236 offset:40960\n\t" \
  "v_cvt_pk_bf16_f32 v96, v96, v97\n\t" \
  "v_mfma_f32_16x16x32_bf16 v[28:31], a[156:159], v[140:143], v[28:31]\n\t" \
  "s_add_u32 s97, s97, s60\n\t" \
  "ds_read_b128 v[168:171], v230 offset:6144\n\t" \
  "v_cvt_pk_bf16_f32 v97, v98, v99\n\t" \
  "v_mfma_f32_16x16x32_bf16 v[28:31], a[148:151], v[136:139], v[28:31]\n\t" \
  "s_add_u32 m0, s38, 0\n\t" \
  "ds_read_b64_tr_b16 a[42:43], v236 offset:45056\n\t" \
  "v_cvt_pk_bf16_f32 v98, v100, v101\n\t" \
  "v_mfma_f32_16x16x32_bf16 v[28:31], a[140:143], v[132:135], v[28:31]\n\t" \
  "ds_read_b128 v[172:175], v230 offset:7168\n\t" \
  "v_cvt_pk_bf16_f32 v99, v102, v103\n\t" \
  "v_mfma_f32_16x16x32_bf16 v[28:31], a[132:135], v[128:131], v[28:31]\n\t" \
  "s_waitcnt lgkmcnt(7)\n\t" \
  "ds_write_b128 v213, v[96:99] offset:16384\n\t" \
  STP("s_memrealtime s[94:95]\n\t") \
  STP("s_memtime s[92:93]\n\t") \
  "v_mfma_f32_16x16x32_bf16 v[0:3], a[160:163], v[144:147], v[0:3]\n\t" \
  "buffer_load_dwordx4 v242, s[16:19], s97 offen offset:0 sc1 lds\n\t" \
  "v_mfma_f32_16x16x32_bf16 v[0:3], a[168:171], v[148:151], v[0:3]\n\t" \
  "ds_read_b128 v[128:131], v230 offset:8192\n\t" \
  "v_accvgpr_read_b32 v104, a104\n\t" \
  "v_mfma_f32_16x16x32_bf16 v[0:3], a[176:179], v[152:155], v[0:3]\n\t" \
  "ds_read_b64_tr_b16 a[44:45], v237 offset:40960\n\t" \
  "v_accvgpr_read_b32 v105, a105\n\t" \
  "v_mfma_f32_16x16x32_bf16 v[0:3], a[184:187], v[156:159], v[0:3]\n\t" \
  "buffer_load_dwordx4 v242, s[16:19], s97 offen offset:1024 sc1 lds\n\t" \
  "v_mfma_f32_16x16x32_bf16 v[16:19], a[188:191], v[156:159], v[16:19]\n\t" \
  "ds_read_b128 v[132:135], v230 offset:9216\n\t" \
  "v_accvgpr_read_b32 v106, a106\n\t" \
  "v_mfma_f32_16x16x32_bf16 v[16:19], a[180:183], v[152:155], v[16:19]\n\t" \
  "ds_read_b128 v[136:139], v230 offset:10240\n\t" \
  "v_accvgpr_read_b32 v107, a107\n\t" \
  "v_mfma_f32_16x16x32_bf16 v[16:19], a[172:175], v[148:151], v[16:19]\n\t" \
  "ds_read_b64_tr_b16 a[46:47], v237 offset:45056\n\t" \
  "v_accvgpr_read_b32 v108, a108\n\t" \
  "v_mfma_f32_16x16x32_bf16 v[16:19], a[164:167], v[144:147], v[16:19]\n\t" \
  "s_waitcnt lgkmcnt(6)\n\t" \
  "ds_read_b128 v[140:143], v230 offset:11264\n\t" \
  "v_accvgpr_read_b32 v109, a109\n\t" \
  "v_mfma_f32_16x16x32_bf16 v[4:7], a[160:163], v[160:163], v[4:7]\n\t" \
  "ds_read_b128 v[144:147], v230 offset:12288\n\t" \
  "v_accvgpr_read_b32 v110, a110\n\t" \
  "v_mfma_f32_16x16x32_bf16 v[4:7], a[168:171], v[164:167], v[4:7]\n\t" \
  "v_accvgpr_read_b32 v111, a111\n\t" \
  "v_mul_f32 v104, v104, v222\n\t" \
  "v_mfma_f32_16x16x32_bf16 v[4:7], a[176:179], v[168:171], v[4:7]\n\t" \
  "ds_read_b128 v[148:151], v230 offset:13312\n\t" \
  "v_mul_f32 v105, v105, v222\n\t" \
  "v_mfma_f32_16x16x32_bf16 v[4:7], a[184:187], v[172:175], v[4:7]\n\t" \
  "buffer_load_dwordx4 v242, s[16:19], s97 offen offset:2048 sc1 lds\n\t" \
  "v_mfma_f32_16x16x32_bf16 v[20:23], a[188:191], v[172:175], v[20:23]\n\t" \
  STP("v_writelane_b32 v255, s94, 30\n\t") \
  STP("v_writelane_b32 v255, s92, 31\n\t") \
  "ds_read_b128 v[152:155], v230 offset:14336\n\t" \
  "v_mul_f32 v106, v106, v222\n\t" \
  "v_mfma_f32_16x16x32_bf16 v[20:23], a[180:183], v[168:171], v[20:23]\n\t" \
  "v_mul_f32 v107, v107, v222\n\t" \
  "v_mul_f32 v108, v108, v222\n\t" \
  "v_mfma_f32_16x16x32_bf16 v[20:23], a[172:175], v[164:167], v[20:23]\n\t" \
  "s_waitcnt vmcnt(12) lgkmcnt(3)\n\t" \
  "buffer_load_dwordx4 v242, s[16:19], s97 offen offset:3072 sc1 lds\n\t" \
  "v_mfma_f32_16x16x32_bf16 v[20:23], a[164:167], v[160:163], v[20:23]\n\t" \
  "s_barrier\n\t" \
  "v_mfma_f32_16x16x32_bf16 v[8:11], a[160:163], v[128:131], v[8:11]\n\t" \
  "ds_read_b128 v[156:159], v230 offset:15360\n\t" \
  "v_mul_f32 v109, v109, v222\n\t" \
  "v_mfma_f32_16x16x32_bf16 v[8:11], a[168:171], v[132:135], v[8:11]\n\t" \
  "buffer_store_dwordx4 v[96:99], v242, s[16:19], s98 offen offset:0 sc1\n\t" \
  "v_mul_f32 v110, v110, v222\n\t" \
  "ds_read_b128 v[160:163], v230 offset:16384\n\t" \
  "v_mul_f32 v111, v111, v222\n\t" \
  "v_mfma_f32_16x16x32_bf16 v[8:11], a[176:179], v[136:139], v[8:11]\n\t" \
  "s_add_u32 s96, s40, 1310720\n\t" \
  "ds_read_b128 v[164:167], v230 offset:17408\n\t" \
  "v_cvt_pk_bf16_f32 v104, v104, v105\n\t" \
  "v_mfma_f32_16x16x32_bf16 v[8:11], a[184:187], v[140:143], v[8:11]\n\t" \
  "s_and_b32 s96, s96, 0x1c0000\n\t" \
  "v_cvt_pk_bf16_f32 v105, v106, v107\n\t" \
  "v_cvt_pk_bf16_f32 v106, v108, v109\n\t" \
  "v_mfma_f32_16x16x32_bf16 v[24:27], a[188:191], v[140:143], v[24:27]\n\t" \
  "s_add_u32 s96, s96, s51\n\t" \
  "ds_read_b128 v[168:171], v230 offset:18432\n\t" \
  "v_cvt_pk_bf16_f32 v107, v110, v111\n\t" \
  "v_mfma_f32_16x16x32_bf16 v[24:27], a[180:183], v[136:139], v[24:27]\n\t" \
  "ds_write_b128 v213, v[104:107] offset:17408\n\t" \
  "v_mfma_f32_16x16x32_bf16 v[24:27], a[172:175], v[132:135], v[24:27]\n\t" \
  "s_add_u32 m0, s38, 147456\n\t" \
  "ds_read_b128 v[172:175], v230 offset:19456\n\t" \
  "v_accvgpr_read_b32 v96, a112\n\t" \
  "v_mfma_f32_16x16x32_bf16 v[24:27], a[164:167], v[128:131], v[24:27]\n\t" \
  "s_waitcnt lgkmcnt(5)\n\t" \
  "v_accvgpr_read_b32 v97, a113\n\t" \
  "v_accvgpr_read_b32 v98, a114\n\t" \
  "v_mfma_f32_16x16x32_bf16 v[12:15], a[160:163], v[144:147], v[12:15]\n\t" \
  "buffer_load_dwordx4 v240, s[24:27], s96 offen offset:0 lds\n\t" \
  "v_mfma_f32_16x16x32_bf16 v[12:15], a[168:171], v[148:151], v[12:15]\n\t" \
  "ds_read_b128 v[128:131], v230 offset:20480\n\t" \
  "v_accvgpr_read_b32 v99, a115\n\t" \
  "v_mfma_f32_16x16x32_bf16 v[12:15], a[176:179], v[152:155], v[12:15]\n\t" \
  "ds_read_b128 v[132:135], v230 offset:21504\n\t" \
  "v_accvgpr_read_b32 v100, a116\n\t" \
  "v_mfma_f32_16x16x32_bf16 v[12:15], a[184:187], v[156:159], v[12:15]\n\t" \
  "v_accvgpr_read_b32 v101, a117\n\t" \
  "v_accvgpr_read_b32 v102, a118\n\t" \
  "v_mfma_f32_16x16x32_bf16 v[28:31], a[188:191], v[156:159], v[28:31]\n\t" \
  "buffer_load_dwordx4 v241, s[24:27], s96 offen offset:1024 lds\n\t" \
  "v_mfma_f32_16x16x32_bf16 v[28:31], a[180:183], v[152:155], v[28:31]\n\t" \
  "ds_read_b128 v[136:139], v230 offset:22528\n\t" \
  "v_accvgpr_read_b32 v103, a119\n\t" \
  "v_mfma_f32_16x16x32_bf16 v[28:31], a[172:175], v[148:151], v[28:31]\n\t" \
  "ds_read_b128 v[140:143], v230 offset:23552\n\t" \
  "v_mul_f32 v96, v96, v222\n\t" \
  "v_mfma_f32_16x16x32_bf16 v[28:31], a[164:167], v[144:147], v[28:31]\n\t" \
  "s_waitcnt lgkmcnt(4)\n\t" \
  "buffer_load_dwordx4 v248, s[24:27], s96 offen offset:2048 lds\n\t" \
  "v_mfma_f32_16x16x32_bf16 v[0:3], a[0:3], v[160:163], v[0:3]\n\t" \
  "ds_read_b128 v[144:147], v230 offset:24576\n\t" \
  "v_mul_f32 v97, v97, v222\n\t" \
  "v_mfma_f32_16x16x32_bf16 v[0:3], a[8:11], v[164:167], v[0:3]\n\t" \
  "v_mul_f32 v98, v98, v222\n\t" \
  "v_mul_f32 v99, v99, v222\n\t" \
  "v_mfma_f32_16x16x32_bf16 v[0:3], a[16:19], v[168:171], v[0:3]\n\t" \
  "buffer_load_dwordx4 v249, s[24:27], s96 offen offset:3072 lds\n\t" \
  "v_mfma_f32_16x16x32_bf16 v[0:3], a[24:27], v[172:175], v[0:3]\n\t" \
  "s_add_u32 s97, s64, 262144\n\t" \
  "s_and_b32 s97, s97, 0xe0000\n\t" \
  "s_add_u32 s97, s97, s61\n\t" \
  "v_mfma_f32_16x16x32_bf16 v[16:19], a[28:31], v[172:175], v[16:19]\n\t" \
  "s_add_u32 m0, s38, 49152\n\t" \
  "ds_read_b128 v[148:151], v230 offset:25600\n\t" \
  "v_mul_f32 v100, v100, v222\n\t" \
  "v_mfma_f32_16x16x32_bf16 v[16:19], a[20:23], v[168:171], v[16:19]\n\t" \
  "buffer_load_dwordx4 v242, s[16:19], s97 offen offset:0 sc1 lds\n\t" \
  "v_mfma_f32_16x16x32_bf16 v[16:19], a[12:15], v[164:167], v[16:19]\n\t" \
  "ds_read_b128 v[152:155], v230 offset:26624\n\t" \
  "v_mul_f32 v101, v101, v222\n\t" \
  "v_mfma_f32_16x16x32_bf16 v[16:19], a[4:7], v[160:163], v[16:19]\n\t" \
  "s_waitcnt lgkmcnt(3)\n\t" \
  "ds_read_b128 v[156:159], v230 offset:27648\n\t" \
  "v_mul_f32 v102, v102, v222\n\t" \
  "v_mfma_f32_16x16x32_bf16 v[4:7], a[0:3], v[128:131], v[4:7]\n\t" \
  "ds_read_b128 v[160:163], v230 offset:28672\n\t" \
  "v_mul_f32 v103, v103, v222\n\t" \
  "v_mfma_f32_16x16x32_bf16 v[4:7], a[8:11], v[132:135], v[4:7]\n\t" \
  "v_cvt_pk_bf16_f32 v96, v96, v97\n\t" \
  "v_cvt_pk_bf16_f32 v97, v98, v99\n\t" \
  "v_mfma_f32_16x16x32_bf16 v[4:7], a[16:19], v[136:139], v[4:7]\n\t" \
  "ds_read_b128 v[164:167], v230 offset:29696\n\t" \
  "v_cvt_pk_bf16_f32 v98, v100, v101\n\t" \
  "v_mfma_f32_16x16x32_bf16 v[4:7], a[24:27], v[140:143], v[4:7]\n\t" \
  "buffer_load_dwordx4 v242, s[16:19], s97 offen offset:1024 sc1 lds\n\t" \
  "v_mfma_f32_16x16x32_bf16 v[20:23], a[28:31], v[140:143], v[20:23]\n\t" \
  "ds_read_b128 v[168:171], v230 offset:30720\n\t" \
  "v_cvt_pk_bf16_f32 v99, v102, v103\n\t" \
  "v_mfma_f32_16x16x32_bf16 v[20:23], a[20:23], v[136:139], v[20:23]\n\t" \
  "ds_write_b128 v213, v[96:99] offset:18432\n\t" \
  "v_mfma_f32_16x16x32_bf16 v[20:23], a[12:15], v[132:135], v[20:23]\n\t" \
  "s_waitcnt vmcnt(12) lgkmcnt(4)\n\t" \
  "buffer_load_dwordx4 v242, s[16:19], s97 offen offset:2048 sc1 lds\n\t" \
  "v_mfma_f32_16x16x32_bf16 v[20:23], a[4:7], v[128:131], v[20:23]\n\t" \
  "s_barrier\n\t" \
  "v_mfma_f32_16x16x32_bf16 v[8:11], a[0:3], v[144:147], v[8:11]\n\t" \
  "ds_read_b128 v[172:175], v230 offset:31744\n\t" \
  "v_mfma_f32_16x16x32_bf16 v[8:11], a[8:11], v[148:151], v[8:11]\n\t" \
  "buffer_load_dwordx4 v242, s[16:19], s97 offen offset:3072 sc1 lds\n\t" \
  "v_mfma_f32_16x16x32_bf16 v[8:11], a[16:19], v[152:155], v[8:11]\n\t" \
  "ds_read_b128 v[128:131], v242 offset:32768\n\t" \
  "v_mfma_f32_16x16x32_bf16 v[8:11], a[24:27], v[156:159], v[8:11]\n\t" \
  "ds_read_b128 v[132:135], v242 offset:33792\n\t" \
  "v_mfma_f32_16x16x32_bf16 v[24:27], a[28:31], v[156:159], v[24:27]\n\t" \
  "buffer_store_dwordx4 v[104:107], v242, s[16:19], s98 offen offset:1024 sc1\n\t" \
  "ds_read_b64_tr_b16 a[48:49], v238 offset:0\n\t" \
  "ds_read_b64_tr_b16 a[50:51], v238 offset:4096\n\t" \
  "ds_read_b128 v[136:139], v242 offset:34816\n\t" \
  "v_mfma_f32_16x16x32_bf16 v[24:27], a[20:23], v[152:155], v[24:27]\n\t" \
  "v_accvgpr_read_b32 v104, a120\n\t" \
  "v_accvgpr_read_b32 v105, a121\n\t" \
  "v_mfma_f32_16x16x32_bf16 v[24:27], a[12:15], v[148:151], v[24:27]\n\t" \
  "ds_read_b128 v[140:143], v242 offset:35840\n\t" \
  "v_accvgpr_read_b32 v106, a122\n\t" \
  "v_mfma_f32_16x16x32_bf16 v[24:27], a[4:7], v[144:147], v[24:27]\n\t" \
  "s_waitcnt lgkmcnt(6)\n\t" \
  "ds_read_b64_tr_b16 a[52:53], v239 offset:0\n\t" \
  "v_accvgpr_read_b32 v107, a123\n\t" \
  "v_mfma_f32_16x16x32_bf16 v[12:15], a[0:3], v[160:163], v[12:15]\n\t" \
  "s_add_u32 s96, s40, 1310720\n\t" \
  "ds_read_b128 v[144:147], v242 offset:36864\n\t" \
  "v_accvgpr_read_b32 v108, a124\n\t" \
  "v_mfma_f32_16x16x32_bf16 v[12:15], a[8:11], v[164:167], v[12:15]\n\t" \
  "s_and_b32 s96, s96, 0x1c0000\n\t" \
  "ds_read_b64_tr_b16 a[54:55], v239 offset:4096\n\t" \
  "v_accvgpr_read_b32 v109, a125\n\t" \
  "v_mfma_f32_16x16x32_bf16 v[12:15], a[16:19], v[168:171], v[12:15]\n\t" \
  "s_add_u32 s96, s96, s77\n\t" \
  "ds_read_b128 v[148:151], v242 offset:37888\n\t" \
  "v_accvgpr_read_b32 v110, a126\n\t" \
  "v_mfma_f32_16x16x32_bf16 v[12:15], a[24:27], v[172:175], v[12:15]\n\t" \
  "s_add_u32 m0, s38, 65536\n\t" \
  "ds_read_b64_tr_b16 a[56:57], v238 offset:8192\n\t" \
  "v_accvgpr_read_b32 v111, a127\n\t" \
  "v_mfma_f32_16x16x32_bf16 v[28:31], a[28:31], v[172:175], v[28:31]\n\t" \
  "ds_read_b128 v[152:155], v242 offset:38912\n\t" \
  "v_mul_f32 v104, v104, v222\n\t" \
  "v_mfma_f32_16x16x32_bf16 v[28:31], a[20:23], v[168:171], v[28:31]\n\t" \
  "buffer_load_dwordx4 v240, s[24:27], s96 offen offset:0 lds\n\t" \
  "v_mfma_f32_16x16x32_bf16 v[28:31], a[12:15], v[164:167], v[28:31]\n\t" \
  "ds_read_b128 v[156:159], v242 offset:39936\n\t" \
  "v_mul_f32 v105, v105, v222\n\t" \
  "v_mfma_f32_16x16x32_bf16 v[28:31], a[4:7], v[160:163], v[28:31]\n\t" \
  "s_waitcnt lgkmcnt(7)\n\t" \
  "ds_read_b64_tr_b16 a[58:59], v238 offset:12288\n\t" \
  "v_mul_f32 v106, v106, v222\n\t" \
  "v_mfma_f32_16x16x32_bf16 v[32:35], a[128:131], v[128:131], v[32:35]\n\t" \
  "ds_read_b128 v[160:163], v242 offset:40960\n\t" \
  "v_mul_f32 v107, v107, v222\n\t" \
  "v_mfma_f32_16x16x32_bf16 v[32:35], a[136:139], v[132:135], v[32:35]\n\t" \
  "ds_read_b64_tr_b16 a[60:61], v239 offset:8192\n\t" \
  "v_mul_f32 v108, v108, v222\n\t" \
  "v_mfma_f32_16x16x32_bf16 v[32:35], a[144:147], v[136:139], v[32:35]\n\t" \
  "ds_read_b128 v[164:167], v242 offset:41984\n\t" \
  "v_mul_f32 v109, v109, v222\n\t" \
  "v_mfma_f32_16x16x32_bf16 v[32:35], a[152:155], v[140:143], v[32:35]\n\t" \
  "buffer_load_dwordx4 v241, s[24:27], s96 offen offset:1024 lds\n\t" \
  "v_mfma_f32_16x16x32_bf16 v[48:51], a[156:159], v[140:143], v[48:51]\n\t" \
  "ds_read_b128 v[168:171], v242 offset:43008\n\t" \
  "v_mul_f32 v110, v110, v222\n\t" \
  "v_mfma_f32_16x16x32_bf16 v[48:51], a[148:151], v[136:139], v[48:51]\n\t" \
  "ds_read_b64_tr_b16 a[62:63], v239 offset:12288\n\t" \
  "v_mul_f32 v111, v111, v222\n\t" \
  "v_mfma_f32_16x16x32_bf16 v[48:51], a[140:143], v[132:135], v[48:51]\n\t" \
  "ds_read_b128 v[172:175], v242 offset:44032\n\t" \
  "v_cvt_pk_bf16_f32 v104, v104, v105\n\t" \
  "v_mfma_f32_16x16x32_bf16 v[48:51], a[132:135], v[128:131], v[48:51]\n\t" \
  "s_waitcnt lgkmcnt(7)\n\t" \
  "v_cvt_pk_bf16_f32 v105, v106, v107\n\t" \
  "v_cvt_pk_bf16_f32 v106, v108, v109\n\t" \
  "v_mfma_f32_16x16x32_bf16 v[36:39], a[128:131], v[144:147], v[36:39]\n\t" \
  "ds_read_b128 v[128:131], v242 offset:45056\n\t" \
  "v_cvt_pk_bf16_f32 v107, v110, v111\n\t" \
  "v_mfma_f32_16x16x32_bf16 v[36:39], a[136:139], v[148:151], v[36:39]\n\t" \
  "ds_write_b128 v213, v[104:107] offset:19456\n\t" \
  "v_mfma_f32_16x16x32_bf16 v[36:39], a[144:147], v[152:155], v[36:39]\n\t" \
  "ds_read_b128 v[132:135], v242 offset:46080\n\t" \
  "v_mfma_f32_16x16x32_bf16 v[36:39], a[152:155], v[156:159], v[36:39]\n\t" \
  "ds_read_b128 v[136:139], v242 offset:47104\n\t" \
  "v_mfma_f32_16x16x32_bf16 v[52:55], a[156:159], v[156:159], v[52:55]\n\t" \
  "ds_read_b128 v[140:143], v242 offset:48128\n\t" \
  "v_mfma_f32_16x16x32_bf16 v[52:55], a[148:151], v[152:155], v[52:55]\n\t" \
  "v_mfma_f32_16x16x32_bf16 v[52:55], a[140:143], v[148:151], v[52:55]\n\t" \
  "s_waitcnt vmcnt(12) lgkmcnt(5)\n\t" \
  "buffer_load_dwordx4 v248, s[24:27], s96 offen offset:2048 lds\n\t" \
  "v_mfma_f32_16x16x32_bf16 v[52:55], a[132:135], v[144:147], v[52:55]\n\t" \
  "s_barrier\n\t" \
  "v_mfma_f32_16x16x32_bf16 v[40:43], a[128:131], v[160:163], v[40:43]\n\t" \
  "ds_read_b128 v[144:147], v242 offset:0\n\t" \
  "v_mfma_f32_16x16x32_bf16 v[40:43], a[136:139], v[164:167], v[40:43]\n\t" \
  "buffer_load_dwordx4 v249, s[24:27], s96 offen offset:3072 lds\n\t" \
  "v_mfma_f32_16x16x32_bf16 v[40:43], a[144:147], v[168:171], v[40:43]\n\t" \
  "s_add_u32 s96, s40, 1572864\n\t" \
  "ds_read_b128 v[148:151], v242 offset:1024\n\t" \
  "s_and_b32 s96, s96, 0x1c0000\n\t" \
  "v_mfma_f32_16x16x32_bf16 v[40:43], a[152:155], v[172:175], v[40:43]\n\t" \
  "s_add_u32 s96, s96, s51\n\t" \
  "ds_read_b128 v[152:155], v242 offset:2048\n\t" \
  "s_add_u32 m0, s38, 81920\n\t" \
  "v_mfma_f32_16x16x32_bf16 v[56:59], a[156:159], v[172:175], v[56:59]\n\t" \
  "buffer_store_dwordx4 v[96:99], v242, s[16:19], s98 offen offset:2048 sc1\n\t" \
  "ds_read_b128 v[156:159], v242 offset:3072\n\t" \
  "v_mfma_f32_16x16x32_bf16 v[56:59], a[148:151], v[168:171], v[56:59]\n\t" \
  "v_mfma_f32_16x16x32_bf16 v[56:59], a[140:143], v[164:167], v[56:59]\n\t" \
  "v_mfma_f32_16x16x32_bf16 v[56:59], a[132:135], v[160:163], v[56:59]\n\t" \
  "s_waitcnt lgkmcnt(4)\n\t" \
  "buffer_load_dwordx4 v240, s[24:27], s96 offen offset:0 lds\n\t" \
  "v_mfma_f32_16x16x32_bf16 v[44:47], a[128:131], v[128:131], v[44:47]\n\t" \
  "ds_read_b128 v[160:163], v242 offset:4096\n\t" \
  "v_mfma_f32_16x16x32_bf16 v[44:47], a[136:139], v[132:135], v[44:47]\n\t" \
  "ds_read_b128 v[164:167], v242 offset:5120\n\t" \
  "v_mfma_f32_16x16x32_bf16 v[44:47], a[144:147], v[136:139], v[44:47]\n\t" \
  "buffer_load_dwordx4 v241, s[24:27], s96 offen offset:1024 lds\n\t" \
  "v_mfma_f32_16x16x32_bf16 v[44:47], a[152:155], v[140:143], v[44:47]\n\t" \
  "ds_read_b128 v[168:171], v242 offset:6144\n\t" \
  "v_mfma_f32_16x16x32_bf16 v[60:63], a[156:159], v[140:143], v[60:63]\n\t" \
  "ds_read_b128 v[172:175], v242 offset:7168\n\t" \
  "v_mfma_f32_16x16x32_bf16 v[60:63], a[148:151], v[136:139], v[60:63]\n\t" \
  "buffer_load_dwordx4 v248, s[24:27], s96 offen offset:2048 lds\n\t" \
  "v_mfma_f32_16x16x32_bf16 v[60:63], a[140:143], v[132:135], v[60:63]\n\t" \
  "v_mfma_f32_16x16x32_bf16 v[60:63], a[132:135], v[128:131], v[60:63]\n\t" \
  "s_waitcnt lgkmcnt(4)\n\t" \
  "v_mfma_f32_16x16x32_bf16 v[0:3], a[32:35], v[144:147], v[0:3]\n\t" \
  "buffer_load_dwordx4 v249, s[24:27], s96 offen offset:3072 lds\n\t" \
  "v_mfma_f32_16x16x32_bf16 v[0:3], a[40:43], v[148:151], v[0:3]\n\t" \
  "ds_read_b128 v[128:131], v242 offset:8192\n\t" \
  "v_mfma_f32_16x16x32_bf16 v[0:3], a[48:51], v[152:155], v[0:3]\n\t" \
  "ds_read_b128 v[132:135], v242 offset:9216\n\t" \
  "v_mfma_f32_16x16x32_bf16 v[0:3], a[56:59], v[156:159], v[0:3]\n\t" \
  "buffer_store_dwordx4 v[104:107], v242, s[16:19], s98 offen offset:3072 sc1\n\t" \
  "ds_read_b128 v[136:139], v242 offset:10240\n\t" \
  "ds_read_b128 v[140:143], v242 offset:11264\n\t" \
  "v_mfma_f32_16x16x32_bf16 v[16:19], a[60:63], v[156:159], v[16:19]\n\t" \
  "v_mfma_f32_16x16x32_bf16 v[16:19], a[52:55], v[152:155], v[16:19]\n\t" \
  "v_mfma_f32_16x16x32_bf16 v[16:19], a[44:47], v[148:151], v[16:19]\n\t" \
  "v_mfma_f32_16x16x32_bf16 v[16:19], a[36:39], v[144:147], v[16:19]\n\t" \
  "s_waitcnt lgkmcnt(4)\n\t" \
  "v_mfma_f32_16x16x32_bf16 v[4:7], a[32:35], v[160:163], v[4:7]\n\t" \
  "ds_read_b128 v[144:147], v242 offset:12288\n\t" \
  "v_mfma_f32_16x16x32_bf16 v[4:7], a[40:43], v[164:167], v[4:7]\n\t" \
  "ds_read_b128 v[148:151], v242 offset:13312\n\t" \
  "v_mfma_f32_16x16x32_bf16 v[4:7], a[48:51], v[168:171], v[4:7]\n\t" \
  "s_add_u32 s97, s64, 655360\n\t" \
  "ds_read_b128 v[152:155], v242 offset:14336\n\t" \
  "s_and_b32 s97, s97, 0xe0000\n\t" \
  "v_mfma_f32_16x16x32_bf16 v[4:7], a[56:59], v[172:175], v[4:7]\n\t" \
  "s_add_u32 s97, s97, s60\n\t" \
  "ds_read_b128 v[156:159], v242 offset:15360\n\t" \
  "s_add_u32 m0, s38, 131072\n\t" \
  "v_mfma_f32_16x16x32_bf16 v[20:23], a[60:63], v[172:175], v[20:23]\n\t" \
  "v_mfma_f32_16x16x32_bf16 v[20:23], a[52:55], v[168:171], v[20:23]\n\t" \
  "v_mfma_f32_16x16x32_bf16 v[20:23], a[44:47], v[164:167], v[20:23]\n\t" \
  "s_waitcnt vmcnt(11) lgkmcnt(4)\n\t" \
  "v_mfma_f32_16x16x32_bf16 v[20:23], a[36:39], v[160:163], v[20:23]\n\t" \
  "s_barrier\n\t" \
  "v_mfma_f32_16x16x32_bf16 v[8:11], a[32:35], v[128:131], v[8:11]\n\t" \
  "buffer_load_dwordx4 v242, s[16:19], s97 offen offset:0 sc1 lds\n\t" \
  "v_mfma_f32_16x16x32_bf16 v[8:11], a[40:43], v[132:135], v[8:11]\n\t" \
  "ds_read_b128 v[160:163], v242 offset:49152\n\t" \
  "v_mfma_f32_16x16x32_bf16 v[8:11], a[48:51], v[136:139], v[8:11]\n\t" \
  "ds_read_b128 v[164:167], v242 offset:50176\n\t" \
  "v_mfma_f32_16x16x32_bf16 v[8:11], a[56:59], v[140:143], v[8:11]\n\t" \
  "buffer_load_dwordx4 v242, s[16:19], s97 offen offset:1024 sc1 lds\n\t" \
  "v_mfma_f32_16x16x32_bf16 v[24:27], a[60:63], v[140:143], v[24:27]\n\t" \
  "ds_read_b128 v[168:171], v242 offset:51200\n\t" \
  "v_mfma_f32_16x16x32_bf16 v[24:27], a[52:55], v[136:139], v[24:27]\n\t" \
  "ds_read_b64_tr_b16 a[64:65], v238 offset:16384\n\t" \
  "v_mfma_f32_16x16x32_bf16 v[24:27], a[44:47], v[132:135], v[24:27]\n\t" \
  "buffer_load_dwordx4 v242, s[16:19], s97 offen offset:2048 sc1 lds\n\t" \
  "v_mfma_f32_16x16x32_bf16 v[24:27], a[36:39], v[128:131], v[24:27]\n\t" \
  "s_waitcnt lgkmcnt(4)\n\t" \
  "ds_read_b64_tr_b16 a[66:67], v238 offset:20480\n\t" \
  "ds_read_b128 v[172:175], v242 offset:52224\n\t" \
  "v_mfma_f32_16x16x32_bf16 v[12:15], a[32:35], v[144:147], v[12:15]\n\t" \
  "ds_read_b128 v[128:131], v242 offset:53248\n\t" \
  "v_mfma_f32_16x16x32_bf16 v[12:15], a[40:43], v[148:151], v[12:15]\n\t" \
  "ds_read_b64_tr_b16 a[68:69], v239 offset:16384\n\t" \
  "v_mfma_f32_16x16x32_bf16 v[12:15], a[48:51], v[152:155], v[12:15]\n\t" \
  "buffer_load_dwordx4 v242, s[16:19], s97 offen offset:3072 sc1 lds\n\t" \
  "v_mfma_f32_16x16x32_bf16 v[12:15], a[56:59], v[156:159], v[12:15]\n\t" \
  "s_add_u32 s96, s40, 1572864\n\t" \
  "s_and_b32 s96, s96, 0x1c0000\n\t" \
  "s_add_u32 s96, s96, s77\n\t" \
  "v_mfma_f32_16x16x32_bf16 v[28:31], a[60:63], v[156:159], v[28:31]\n\t" \
  "s_add_u32 m0, s38, 32768\n\t" \
  "ds_read_b128 v[132:135], v242 offset:54272\n\t" \
  "v_mfma_f32_16x16x32_bf16 v[28:31], a[52:55], v[152:155], v[28:31]\n\t" \
  "buffer_load_dwordx4 v240, s[24:27], s96 offen offset:0 lds\n\t" \
  "v_mfma_f32_16x16x32_bf16 v[28:31], a[44:47], v[148:151], v[28:31]\n\t" \
  "ds_read_b128 v[136:139], v242 offset:55296\n\t" \
  "v_mfma_f32_16x16x32_bf16 v[28:31], a[36:39], v[144:147], v[28:31]\n\t" \
  "s_waitcnt lgkmcnt(4)\n\t" \
  "ds_read_b128 v[140:143], v242 offset:56320\n\t" \
  "v_mfma_f32_16x16x32_bf16 v[32:35], a[160:163], v[160:163], v[32:35]\n\t" \
  "ds_read_b64_tr_b16 a[70:71], v239 offset:20480\n\t" \
  "v_mfma_f32_16x16x32_bf16 v[32:35], a[168:171], v[164:167], v[32:35]\n\t" \
  "buffer_load_dwordx4 v241, s[24:27], s96 offen offset:1024 lds\n\t" \
  "v_mfma_f32_16x16x32_bf16 v[32:35], a[176:179], v[168:171], v[32:35]\n\t" \
  "ds_read_b128 v[144:147], v242 offset:57344\n\t" \
  "v_mfma_f32_16x16x32_bf16 v[32:35], a[184:187], v[172:175], v[32:35]\n\t" \
  "ds_read_b64_tr_b16 a[72:73], v238 offset:24576\n\t" \
  "v_mfma_f32_16x16x32_bf16 v[48:51], a[188:191], v[172:175], v[48:51]\n\t" \
  "ds_read_b128 v[148:151], v242 offset:58368\n\t" \
  "v_mfma_f32_16x16x32_bf16 v[48:51], a[180:183], v[168:171], v[48:51]\n\t" \
  "ds_read_b128 v[152:155], v242 offset:59392\n\t" \
  "v_mfma_f32_16x16x32_bf16 v[48:51], a[172:175], v[164:167], v[48:51]\n\t" \
  "ds_read_b128 v[156:159], v242 offset:60416\n\t" \
  "ds_read_b64_tr_b16 a[74:75], v238 offset:28672\n\t" \
  "v_mfma_f32_16x16x32_bf16 v[48:51], a[164:167], v[160:163], v[48:51]\n\t" \
  "s_waitcnt lgkmcnt(7)\n\t" \
  "ds_read_b64_tr_b16 a[76:77], v239 offset:24576\n\t" \
  "v_mfma_f32_16x16x32_bf16 v[36:39], a[160:163], v[128:131], v[36:39]\n\t" \
  "ds_read_b64_tr_b16 a[78:79], v239 offset:28672\n\t" \
  "v_mfma_f32_16x16x32_bf16 v[36:39], a[168:171], v[132:135], v[36:39]\n\t" \
  "ds_read_b128 v[160:163], v242 offset:61440\n\t" \
  "v_mfma_f32_16x16x32_bf16 v[36:39], a[176:179], v[136:139], v[36:39]\n\t" \
  "ds_read_b128 v[164:167], v242 offset:62464\n\t" \
  "v_mfma_f32_16x16x32_bf16 v[36:39], a[184:187], v[140:143], v[36:39]\n\t" \
  "ds_read_b128 v[168:171], v242 offset:63488\n\t" \
  "v_mfma_f32_16x16x32_bf16 v[52:55], a[188:191], v[140:143], v[52:55]\n\t" \
  "ds_read_b128 v[172:175], v242 offset:64512\n\t" \
  "v_mfma_f32_16x16x32_bf16 v[52:55], a[180:183], v[136:139], v[52:55]\n\t" \
  "buffer_load_dwordx4 v248, s[24:27], s96 offen offset:2048 lds\n\t" \
  "v_mfma_f32_16x16x32_bf16 v[52:55], a[172:175], v[132:135], v[52:55]\n\t" \
  "s_waitcnt vmcnt(13) lgkmcnt(4)\n\t" \
  "v_mfma_f32_16x16x32_bf16 v[52:55], a[164:167], v[128:131], v[52:55]\n\t" \
  "s_barrier\n\t" \
  "v_mfma_f32_16x16x32_bf16 v[40:43], a[160:163], v[144:147], v[40:43]\n\t" \
  "buffer_load_dwordx4 v249, s[24:27], s96 offen offset:3072 lds\n\t" \
  "v_mfma_f32_16x16x32_bf16 v[40:43], a[168:171], v[148:151], v[40:43]\n\t" \
  "s_add_u32 s97, s64, 393216\n\t" \
  "ds_read_b128 v[128:131], v242 offset:16384\n\t" \
  "s_and_b32 s97, s97, 0xe0000\n\t" \
  "v_mfma_f32_16x16x32_bf16 v[40:43], a[176:179], v[152:155], v[40:43]\n\t" \
  "s_add_u32 s97, s97, s61\n\t" \
  "ds_read_b128 v[132:135], v242 offset:17408\n\t" \
  "s_add_u32 m0, s38, 0\n\t" \
  "v_mfma_f32_16x16x32_bf16 v[40:43], a[184:187], v[156:159], v[40:43]\n\t" \
  "ds_read_b64_tr_b16 a[80:81], v236 offset:0\n\t" \
  "v_mfma_f32_16x16x32_bf16 v[56:59], a[188:191], v[156:159], v[56:59]\n\t" \
  "buffer_load_dwordx4 v242, s[16:19], s97 offen offset:0 sc1 lds\n\t" \
  "v_mfma_f32_16x16x32_bf16 v[56:59], a[180:183], v[152:155], v[56:59]\n\t" \
  "ds_read_b64_tr_b16 a[82:83], v236 offset:4096\n\t" \
  "ds_read_b128 v[136:139], v242 offset:18432\n\t" \
  "v_mfma_f32_16x16x32_bf16 v[56:59], a[172:175], v[148:151], v[56:59]\n\t" \
  "ds_read_b64_tr_b16 a[84:85], v237 offset:0\n\t" \
  "ds_read_b128 v[140:143], v242 offset:19456\n\t" \
  "v_mfma_f32_16x16x32_bf16 v[56:59], a[164:167], v[144:147], v[56:59]\n\t" \
  "s_waitcnt lgkmcnt(7)\n\t" \
  "buffer_load_dwordx4 v242, s[16:19], s97 offen offset:1024 sc1 lds\n\t" \
  "v_mfma_f32_16x16x32_bf16 v[44:47], a[160:163], v[160:163], v[44:47]\n\t" \
  "ds_read_b128 v[144:147], v242 offset:20480\n\t" \
  "v_mfma_f32_16x16x32_bf16 v[44:47], a[168:171], v[164:167], v[44:47]\n\t" \
  "ds_read_b128 v[148:151], v242 offset:21504\n\t" \
  "v_mfma_f32_16x16x32_bf16 v[44:47], a[176:179], v[168:171], v[44:47]\n\t" \
  "ds_read_b64_tr_b16 a[86:87], v237 offset:4096\n\t" \
  "v_mfma_f32_16x16x32_bf16 v[44:47], a[184:187], v[172:175], v[44:47]\n\t" \
  "ds_read_b128 v[152:155], v242 offset:22528\n\t" \
  "v_mfma_f32_16x16x32_bf16 v[60:63], a[188:191], v[172:175], v[60:63]\n\t" \
  "buffer_load_dwordx4 v242, s[16:19], s97 offen offset:2048 sc1 lds\n\t" \
  "v_mfma_f32_16x16x32_bf16 v[60:63], a[180:183], v[168:171], v[60:63]\n\t" \
  "ds_read_b64_tr_b16 a[88:89], v236 offset:8192\n\t" \
  "v_mfma_f32_16x16x32_bf16 v[60:63], a[172:175], v[164:167], v[60:63]\n\t" \
  "ds_read_b128 v[156:159], v242 offset:23552\n\t" \
  "v_mfma_f32_16x16x32_bf16 v[60:63], a[164:167], v[160:163], v[60:63]\n\t" \
  "s_waitcnt lgkmcnt(6)\n\t" \
  "buffer_load_dwordx4 v242, s[16:19], s97 offen offset:3072 sc1 lds\n\t" \
  "v_mfma_f32_16x16x32_bf16 v[96:99], a[192:195], v[128:131], 0\n\t" \
  "s_add_u32 s96, s40, 1835008\n\t" \
  "ds_read_b128 v[160:163], v242 offset:24576\n\t" \
  "s_and_b32 s96, s96, 0x1c0000\n\t" \
  "v_mfma_f32_16x16x32_bf16 v[96:99], a[200:203], v[132:135], v[96:99]\n\t" \
  "s_add_u32 s96, s96, s51\n\t" \
  "ds_read_b64_tr_b16 a[90:91], v236 offset:12288\n\t" \
  "s_add_u32 m0, s38, 147456\n\t" \
  "v_mfma_f32_16x16x32_bf16 v[96:99], a[208:211], v[136:139], v[96:99]\n\t" \
  "s_waitcnt vmcnt(12)\n\t" \
  "buffer_store_dword v243, v254, s[16:19], s49 offen offset:64 sc1\n\t" \
  "ds_read_b128 v[164:167], v242 offset:25600\n\t" \
  "v_mfma_f32_16x16x32_bf16 v[96:99], a[216:219], v[140:143], v[96:99]\n\t" \
  "ds_read_b64_tr_b16 a[92:93], v237 offset:8192\n\t" \
  "v_mfma_f32_16x16x32_bf16 v[112:115], a[220:223], v[140:143], 0\n\t" \
  "buffer_load_dwordx4 v240, s[24:27], s96 offen offset:0 lds\n\t" \
  "v_mfma_f32_16x16x32_bf16 v[112:115], a[212:215], v[136:139], v[112:115]\n\t" \
  "ds_read_b128 v[168:171], v242 offset:26624\n\t" \
  "v_mfma_f32_16x16x32_bf16 v[112:115], a[204:207], v[132:135], v[112:115]\n\t" \
  "ds_read_b128 v[172:175], v242 offset:27648\n\t" \
  "v_mfma_f32_16x16x32_bf16 v[112:115], a[196:199], v[128:131], v[112:115]\n\t" \
  "s_waitcnt lgkmcnt(6)\n\t" \
  "buffer_load_dwordx4 v241, s[24:27], s96 offen offset:1024 lds\n\t" \
  "v_mfma_f32_16x16x32_bf16 v[100:103], a[192:195], v[144:147], 0\n\t" \
  ".Lhs12:\n\ts_cbranch_execz .Lhs12n\n\t" \
  "ds_read_b64_tr_b16 a[94:95], v237 offset:12288\n\t" \
  "v_mfma_f32_16x16x32_bf16 v[100:103], a[200:203], v[148:151], v[100:103]\n\t" \
  "ds_read_b128 v[128:131], v242 offset:28672\n\t" \
  "v_mfma_f32_16x16x32_bf16 v[100:103], a[208:211], v[152:155], v[100:103]\n\t" \
  "ds_read_b128 v[132:135], v242 offset:29696\n\t" \
  "v_mfma_f32_16x16x32_bf16 v[100:103], a[216:219], v[156:159], v[100:103]\n\t" \
  "ds_read_b128 v[136:139], v242 offset:30720\n\t" \
  "v_mfma_f32_16x16x32_bf16 v[116:119], a[220:223], v[156:159], 0\n\t" \
  "ds_read_b128 v[140:143], v242 offset:31744\n\t" \
  "v_mfma_f32_16x16x32_bf16 v[116:119], a[212:215], v[152:155], v[116:119]\n\t" \
  "v_mfma_f32_16x16x32_bf16 v[116:119], a[204:207], v[148:151], v[116:119]\n\t" \
  "s_waitcnt vmcnt(11) lgkmcnt(4)\n\t" \
  "v_mfma_f32_16x16x32_bf16 v[116:119], a[196:199], v[144:147], v[116:119]\n\t" \
  "s_barrier\n\t" \
  "v_mfma_f32_16x16x32_bf16 v[104:107], a[192:195], v[160:163], 0\n\t" \
  "ds_read_b128 v[144:147], v232 offset:0\n\t" \
  "v_mfma_f32_16x16x32_bf16 v[104:107], a[200:203], v[164:167], v[104:107]\n\t" \
  "ds_read_b64_tr_b16 a[96:97], v236 offset:16384\n\t" \
  "v_mfma_f32_16x16x32_bf16 v[104:107], a[208:211], v[168:171], v[104:107]\n\t" \
  "ds_read_b128 v[148:151], v232 offset:1024\n\t" \
  "v_mfma_f32_16x16x32_bf16 v[104:107], a[216:219], v[172:175], v[104:107]\n\t" \
  "ds_read_b128 v[152:155], v232 offset:2048\n\t" \
  "v_mfma_f32_16x16x32_bf16 v[120:123], a[220:223], v[172:175], 0\n\t" \
  "ds_read_b64_tr_b16 a[98:99], v236 offset:20480\n\t" \
  "v_mfma_f32_16x16x32_bf16 v[120:123], a[212:215], v[168:171], v[120:123]\n\t" \
  "ds_read_b128 v[156:159], v232 offset:3072\n\t" \
  "v_mfma_f32_16x16x32_bf16 v[120:123], a[204:207], v[164:167], v[120:123]\n\t" \
  "buffer_load_dwordx4 v248, s[24:27], s96 offen offset:2048 lds\n\t" \
  "v_mfma_f32_16x16x32_bf16 v[120:123], a[196:199], v[160:163], v[120:123]\n\t" \
  "s_waitcnt lgkmcnt(6)\n\t" \
  "ds_read_b64_tr_b16 a[100:101], v237 offset:16384\n\t" \
  "v_mfma_f32_16x16x32_bf16 v[108:111], a[192:195], v[128:131], 0\n\t" \
  "ds_read_b128 v[160:163], v232 offset:4096\n\t" \
  "v_mfma_f32_16x16x32_bf16 v[108:111], a[200:203], v[132:135], v[108:111]\n\t" \
  "ds_read_b64_tr_b16 a[102:103], v237 offset:20480\n\t" \
  "v_mfma_f32_16x16x32_bf16 v[108:111], a[208:211], v[136:139], v[108:111]\n\t" \
  "buffer_load_dwordx4 v249, s[24:27], s96 offen offset:3072 lds\n\t" \
  "v_mfma_f32_16x16x32_bf16 v[108:111], a[216:219], v[140:143], v[108:111]\n\t" \
  "s_add_u32 s97, s64, 786432\n\t" \
  "s_and_b32 s97, s97, 0xe0000\n\t" \
  "s_add_u32 s97, s97, s60\n\t" \
  "v_mfma_f32_16x16x32_bf16 v[124:127], a[220:223], v[140:143], 0\n\t" \
  "s_add_u32 m0, s38, 65536\n\t" \
  "ds_read_b64_tr_b16 a[104:105], v236 offset:24576\n\t" \
  "ds_read_b128 v[164:167], v232 offset:5120\n\t" \
  "v_mfma_f32_16x16x32_bf16 v[124:127], a[212:215], v[136:139], v[124:127]\n\t" \
  "buffer_load_dwordx4 v242, s[16:19], s97 offen offset:0 sc1 lds\n\t" \
  "v_mfma_f32_16x16x32_bf16 v[124:127], a[204:207], v[132:135], v[124:127]\n\t" \
  "ds_read_b64_tr_b16 a[106:107], v236 offset:28672\n\t" \
  "ds_read_b128 v[168:171], v232 offset:6144\n\t" \
  "v_mfma_f32_16x16x32_bf16 v[124:127], a[196:199], v[128:131], v[124:127]\n\t" \
  "s_waitcnt lgkmcnt(7)\n\t" \
  "ds_read_b128 v[172:175], v232 offset:7168\n\t" \
  "v_mfma_f32_16x16x32_bf16 v[0:3], a[64:67], v[144:147], v[0:3]\n\t" \
  "ds_read_b128 v[128:131], v232 offset:8192\n\t" \
  "v_lshrrev_b32 v247, 1, v242\n\t" \
  "v_mfma_f32_16x16x32_bf16 v[0:3], a[72:75], v[148:151], v[0:3]\n\t" \
  "buffer_load_dwordx4 v242, s[16:19], s97 offen offset:1024 sc1 lds\n\t" \
  "v_mfma_f32_16x16x32_bf16 v[0:3], a[80:83], v[152:155], v[0:3]\n\t" \
  "ds_read_b128 v[132:135], v232 offset:9216\n\t" \
  "v_add_u32 v247, s38, v247\n\t" \
  "v_mfma_f32_16x16x32_bf16 v[0:3], a[88:91], v[156:159], v[0:3]\n\t" \
  "ds_read_b64_tr_b16 a[108:109], v237 offset:24576\n\t" \
  "v_mfma_f32_16x16x32_bf16 v[16:19], a[92:95], v[156:159], v[16:19]\n\t" \
  "buffer_load_dwordx4 v242, s[16:19], s97 offen offset:2048 sc1 lds\n\t" \
  "v_mfma_f32_16x16x32_bf16 v[16:19], a[84:87], v[152:155], v[16:19]\n\t" \
  "ds_read_b128 v[136:139], v232 offset:10240\n\t" \
  "v_mfma_f32_16x16x32_bf16 v[16:19], a[76:79], v[148:151], v[16:19]\n\t" \
  "ds_read_b128 v[140:143], v232 offset:11264\n\t" \
  "v_mfma_f32_16x16x32_bf16 v[16:19], a[68:71], v[144:147], v[16:19]\n\t" \
  "s_waitcnt lgkmcnt(5)\n\t" \
  "buffer_load_dwordx4 v242, s[16:19], s97 offen offset:3072 sc1 lds\n\t" \
  "v_mfma_f32_16x16x32_bf16 v[4:7], a[64:67], v[160:163], v[4:7]\n\t" \
  "s_add_u32 s96, s40, 1835008\n\t" \
  "s_and_b32 s96, s96, 0x1c0000\n\t" \
  "s_add_u32 s96, s96, s77\n\t" \
  "v_mfma_f32_16x16x32_bf16 v[4:7], a[72:75], v[164:167], v[4:7]\n\t" \
  "s_add_u32 m0, s38, 49152\n\t" \
  "ds_read_b128 v[144:147], v232 offset:12288\n\t" \
  "v_mfma_f32_16x16x32_bf16 v[4:7], a[80:83], v[168:171], v[4:7]\n\t" \
  "buffer_load_dwordx4 v240, s[24:27], s96 offen offset:0 lds\n\t" \
  "v_mfma_f32_16x16x32_bf16 v[4:7], a[88:91], v[172:175], v[4:7]\n\t" \
  "ds_read_b128 v[148:151], v232 offset:13312\n\t" \
  "ds_read_b64_tr_b16 a[110:111], v237 offset:28672\n\t" \
  "v_mfma_f32_16x16x32_bf16 v[20:23], a[92:95], v[172:175], v[20:23]\n\t" \
  "ds_read_b128 v[152:155], v232 offset:14336\n\t" \
  "v_mfma_f32_16x16x32_bf16 v[20:23], a[84:87], v[168:171], v[20:23]\n\t" \
  "buffer_load_dwordx4 v241, s[24:27], s96 offen offset:1024 lds\n\t" \
  "v_mfma_f32_16x16x32_bf16 v[20:23], a[76:79], v[164:167], v[20:23]\n\t" \
  "s_waitcnt vmcnt(11) lgkmcnt(1)\n\t" \
  "ds_read_b128 v[156:159], v232 offset:15360\n\t" \
  "v_mfma_f32_16x16x32_bf16 v[20:23], a[68:71], v[160:163], v[20:23]\n\t" \
  "s_barrier\n\t" \
  "v_mfma_f32_16x16x32_bf16 v[8:11], a[64:67], v[128:131], v[8:11]\n\t" \
  "buffer_load_dwordx4 v248, s[24:27], s96 offen offset:2048 lds\n\t" \
  "v_mfma_f32_16x16x32_bf16 v[8:11], a[72:75], v[132:135], v[8:11]\n\t" \
  "ds_read_b128 v[160:163], v242 offset:0\n\t" \
  "v_mfma_f32_16x16x32_bf16 v[8:11], a[80:83], v[136:139], v[8:11]\n\t" \
  "ds_read_b128 v[164:167], v242 offset:1024\n\t" \
  "v_mfma_f32_16x16x32_bf16 v[8:11], a[88:91], v[140:143], v[8:11]\n\t" \
  "ds_read_b64_tr_b16 a[112:113], v234 offset:32768\n\t" \
  "v_mfma_f32_16x16x32_bf16 v[24:27], a[92:95], v[140:143], v[24:27]\n\t" \
  "ds_read_b128 v[168:171], v242 offset:2048\n\t" \
  "v_mfma_f32_16x16x32_bf16 v[24:27], a[84:87], v[136:139], v[24:27]\n\t" \
  "buffer_load_dwordx4 v249, s[24:27], s96 offen offset:3072 lds\n\t" \
  "v_mfma_f32_16x16x32_bf16 v[24:27], a[76:79], v[132:135], v[24:27]\n\t" \
  "s_add_u32 s97, s64, 524288\n\t" \
  "s_and_b32 s97, s97, 0xe0000\n\t" \
  "s_add_u32 s97, s97, s61\n\t" \
  "v_mfma_f32_16x16x32_bf16 v[24:27], a[68:71], v[128:131], v[24:27]\n\t" \
  "s_waitcnt lgkmcnt(4)\n\t" \
  "ds_read_b128 v[172:175], v242 offset:3072\n\t" \
  "s_add_u32 m0, s38, 81920\n\t" \
  "v_mfma_f32_16x16x32_bf16 v[12:15], a[64:67], v[144:147], v[12:15]\n\t" \
  "buffer_load_dwordx4 v242, s[16:19], s97 offen offset:0 sc1 lds\n\t" \
  "v_mfma_f32_16x16x32_bf16 v[12:15], a[72:75], v[148:151], v[12:15]\n\t" \
  "ds_read_b128 v[128:131], v242 offset:4096\n\t" \
  "v_mfma_f32_16x16x32_bf16 v[12:15], a[80:83], v[152:155], v[12:15]\n\t" \
  "ds_read_b64_tr_b16 a[114:115], v234 offset:36864\n\t" \
  "v_mfma_f32_16x16x32_bf16 v[12:15], a[88:91], v[156:159], v[12:15]\n\t" \
  "ds_read_b128 v[132:135], v242 offset:5120\n\t" \
  "v_mfma_f32_16x16x32_bf16 v[28:31], a[92:95], v[156:159], v[28:31]\n\t" \
  "ds_read_b64_tr_b16 a[116:117], v235 offset:32768\n\t" \
  "v_mfma_f32_16x16x32_bf16 v[28:31], a[84:87], v[152:155], v[28:31]\n\t" \
  "ds_read_b128 v[136:139], v242 offset:6144\n\t" \
  "v_mfma_f32_16x16x32_bf16 v[28:31], a[76:79], v[148:151], v[28:31]\n\t" \
  "ds_read_b128 v[140:143], v242 offset:7168\n\t" \
  "v_mfma_f32_16x16x32_bf16 v[28:31], a[68:71], v[144:147], v[28:31]\n\t" \
  "s_waitcnt lgkmcnt(6)\n\t" \
  "ds_read_b64_tr_b16 a[118:119], v235 offset:36864\n\t" \
  STP("s_memrealtime s[94:95]\n\t") \
  STP("s_memtime s[92:93]\n\t") \
  "v_mfma_f32_16x16x32_bf16 v[32:35], a[0:3], v[160:163], v[32:35]\n\t" \
  "ds_read_b128 v[144:147], v242 offset:8192\n\t" \
  "v_mfma_f32_16x16x32_bf16 v[32:35], a[8:11], v[164:167], v[32:35]\n\t" \
  "ds_read_b64_tr_b16 a[120:121], v234 offset:40960\n\t" \
  "v_mfma_f32_16x16x32_bf16 v[32:35], a[16:19], v[168:171], v[32:35]\n\t" \
  "ds_read_b128 v[148:151], v242 offset:9216\n\t" \
  "v_mfma_f32_16x16x32_bf16 v[32:35], a[24:27], v[172:175], v[32:35]\n\t" \
  "ds_read_b64_tr_b16 a[122:123], v234 offset:45056\n\t" \
  "v_mfma_f32_16x16x32_bf16 v[48:51], a[28:31], v[172:175], v[48:51]\n\t" \
  "ds_read_b128 v[152:155], v242 offset:10240\n\t" \
  "v_mfma_f32_16x16x32_bf16 v[48:51], a[20:23], v[168:171], v[48:51]\n\t" \
  "ds_read_b64_tr_b16 a[124:125], v235 offset:40960\n\t" \
  "v_mfma_f32_16x16x32_bf16 v[48:51], a[12:15], v[164:167], v[48:51]\n\t" \
  "buffer_load_dwordx4 v242, s[16:19], s97 offen offset:1024 sc1 lds\n\t" \
  "v_mfma_f32_16x16x32_bf16 v[48:51], a[4:7], v[160:163], v[48:51]\n\t" \
  "s_waitcnt lgkmcnt(7)\n\t" \
  "ds_read_b128 v[156:159], v242 offset:11264\n\t" \
  "v_mfma_f32_16x16x32_bf16 v[36:39], a[0:3], v[128:131], v[36:39]\n\t" \
  "ds_read_b64_tr_b16 a[126:127], v235 offset:45056\n\t" \
  "v_mfma_f32_16x16x32_bf16 v[36:39], a[8:11], v[132:135], v[36:39]\n\t" \
  "ds_read_b128 v[160:163], v242 offset:12288\n\t" \
  "v_mfma_f32_16x16x32_bf16 v[36:39], a[16:19], v[136:139], v[36:39]\n\t" \
  "buffer_load_dwordx4 v242, s[16:19], s97 offen offset:2048 sc1 lds\n\t" \
  "v_mfma_f32_16x16x32_bf16 v[36:39], a[24:27], v[140:143], v[36:39]\n\t" \
  "ds_read_b128 v[164:167], v242 offset:13312\n\t" \
  "v_mfma_f32_16x16x32_bf16 v[52:55], a[28:31], v[140:143], v[52:55]\n\t" \
  STP("v_writelane_b32 v255, s94, 32\n\t") \
  STP("v_writelane_b32 v255, s92, 33\n\t") \
  "ds_read_b128 v[168:171], v242 offset:14336\n\t" \
  "v_mfma_f32_16x16x32_bf16 v[52:55], a[20:23], v[136:139], v[52:55]\n\t" \
  "buffer_load_dwordx4 v242, s[16:19], s97 offen offset:3072 sc1 lds\n\t" \
  "v_mfma_f32_16x16x32_bf16 v[52:55], a[12:15], v[132:135], v[52:55]\n\t" \
  "s_waitcnt vmcnt(8) lgkmcnt(3)\n\t" \
  "s_add_u32 s97, s64, 917504\n\t" \
  "s_and_b32 s97, s97, 0xe0000\n\t" \
  "v_mfma_f32_16x16x32_bf16 v[52:55], a[4:7], v[128:131], v[52:55]\n\t" \
  "s_barrier\n\t" \
  "v_mfma_f32_16x16x32_bf16 v[40:43], a[0:3], v[144:147], v[40:43]\n\t" \
  "s_add_u32 s97, s97, s60\n\t" \
  "ds_read_b128 v[172:175], v242 offset:15360\n\t" \
  "s_add_u32 m0, s38, 131072\n\t" \
  "v_mfma_f32_16x16x32_bf16 v[40:43], a[8:11], v[148:151], v[40:43]\n\t" \
  "buffer_load_dwordx4 v242, s[16:19], s97 offen offset:0 sc1 lds\n\t" \
  "v_mfma_f32_16x16x32_bf16 v[40:43], a[16:19], v[152:155], v[40:43]\n\t" \
  "ds_read_b128 v[128:131], v230 offset:0\n\t" \
  "v_mfma_f32_16x16x32_bf16 v[40:43], a[24:27], v[156:159], v[40:43]\n\t" \
  "buffer_load_dwordx4 v242, s[16:19], s97 offen offset:1024 sc1 lds\n\t" \
  "v_mfma_f32_16x16x32_bf16 v[56:59], a[28:31], v[156:159], v[56:59]\n\t" \
  "ds_read_b128 v[132:135], v230 offset:1024\n\t" \
  "v_mfma_f32_16x16x32_bf16 v[56:59], a[20:23], v[152:155], v[56:59]\n\t" \
  "ds_read_b128 v[136:139], v230 offset:2048\n\t" \
  "v_mfma_f32_16x16x32_bf16 v[56:59], a[12:15], v[148:151], v[56:59]\n\t" \
  "buffer_load_dwordx4 v242, s[16:19], s97 offen offset:2048 sc1 lds\n\t" \
  "v_mfma_f32_16x16x32_bf16 v[56:59], a[4:7], v[144:147], v[56:59]\n\t" \
  "s_waitcnt lgkmcnt(3)\n\t" \
  "ds_read_b128 v[140:143], v230 offset:3072\n\t" \
  "v_mfma_f32_16x16x32_bf16 v[44:47], a[0:3], v[160:163], v[44:47]\n\t" \
  "ds_read_b128 v[144:147], v230 offset:4096\n\t" \
  "v_mfma_f32_16x16x32_bf16 v[44:47], a[8:11], v[164:167], v[44:47]\n\t" \
  "ds_read_b128 v[148:151], v230 offset:5120\n\t" \
  "v_mfma_f32_16x16x32_bf16 v[44:47], a[16:19], v[168:171], v[44:47]\n\t" \
  "ds_read_b128 v[152:155], v230 offset:6144\n\t" \
  "v_mfma_f32_16x16x32_bf16 v[44:47], a[24:27], v[172:175], v[44:47]\n\t" \
  "ds_read_b128 v[156:159], v230 offset:7168\n\t" \
  "v_mfma_f32_16x16x32_bf16 v[60:63], a[28:31], v[172:175], v[60:63]\n\t" \
  "buffer_load_dwordx4 v242, s[16:19], s97 offen offset:3072 sc1 lds\n\t" \
  "v_mfma_f32_16x16x32_bf16 v[60:63], a[20:23], v[168:171], v[60:63]\n\t" \
  "s_add_u32 s97, s64, 655360\n\t" \
  "s_and_b32 s97, s97, 0xe0000\n\t" \
  "s_add_u32 s97, s97, s61\n\t" \
  "v_mfma_f32_16x16x32_bf16 v[60:63], a[12:15], v[164:167], v[60:63]\n\t" \
  "s_add_u32 m0, s38, 16384\n\t" \
  "v_mfma_f32_16x16x32_bf16 v[60:63], a[4:7], v[160:163], v[60:63]\n\t" \
  "s_waitcnt lgkmcnt(4)\n\t" \
  "buffer_load_dword v246, v253, s[16:19], s65 offen sc1\n\t" \
  "v_mfma_f32_16x16x32_bf16 v[0:3], a[96:99], v[128:131], v[0:3]\n\t" \
  "ds_read_b128 v[160:163], v230 offset:8192\n\t" \
  "v_mfma_f32_16x16x32_bf16 v[0:3], a[104:107], v[132:135], v[0:3]\n\t" \
  "ds_read_b128 v[164:167], v230 offset:9216\n\t" \
  "v_mfma_f32_16x16x32_bf16 v[0:3], a[112:115], v[136:139], v[0:3]\n\t" \
  "ds_read_b128 v[168:171], v230 offset:10240\n\t" \
  "v_mfma_f32_16x16x32_bf16 v[0:3], a[120:123], v[140:143], v[0:3]\n\t" \
  "buffer_load_dwordx4 v242, s[16:19], s97 offen offset:0 sc1 lds\n\t" \
  "v_mfma_f32_16x16x32_bf16 v[16:19], a[124:127], v[140:143], v[16:19]\n\t" \
  "ds_read_b128 v[172:175], v230 offset:11264\n\t" \
  "v_mfma_f32_16x16x32_bf16 v[16:19], a[116:119], v[136:139], v[16:19]\n\t" \
  "v_mfma_f32_16x16x32_bf16 v[16:19], a[108:111], v[132:135], v[16:19]\n\t" \
  "v_mfma_f32_16x16x32_bf16 v[16:19], a[100:103], v[128:131], v[16:19]\n\t" \
  "s_waitcnt lgkmcnt(4)\n\t" \
  "buffer_load_dwordx4 v242, s[16:19], s97 offen offset:1024 sc1 lds\n\t" \
  "v_mfma_f32_16x16x32_bf16 v[4:7], a[96:99], v[144:147], v[4:7]\n\t" \
  "ds_read_b128 v[128:131], v230 offset:12288\n\t" \
  "v_mfma_f32_16x16x32_bf16 v[4:7], a[104:107], v[148:151], v[4:7]\n\t" \
  "ds_read_b128 v[132:135], v230 offset:13312\n\t" \
  "v_mfma_f32_16x16x32_bf16 v[4:7], a[112:115], v[152:155], v[4:7]\n\t" \
  "buffer_load_dwordx4 v242, s[16:19], s97 offen offset:2048 sc1 lds\n\t" \
  "v_mfma_f32_16x16x32_bf16 v[4:7], a[120:123], v[156:159], v[4:7]\n\t" \
  "ds_read_b128 v[136:139], v230 offset:14336\n\t" \
  "v_mfma_f32_16x16x32_bf16 v[20:23], a[124:127], v[156:159], v[20:23]\n\t" \
  "ds_read_b128 v[140:143], v230 offset:15360\n\t" \
  "v_mfma_f32_16x16x32_bf16 v[20:23], a[116:119], v[152:155], v[20:23]\n\t" \
  "buffer_load_dwordx4 v242, s[16:19], s97 offen offset:3072 sc1 lds\n\t" \
  "v_mfma_f32_16x16x32_bf16 v[20:23], a[108:111], v[148:151], v[20:23]\n\t" \
  "s_waitcnt vmcnt(9) lgkmcnt(4)\n\t" \
  "s_add_u32 s97, s64, 786432\n\t" \
  "s_and_b32 s97, s97, 0xe0000\n\t" \
  "v_mfma_f32_16x16x32_bf16 v[20:23], a[100:103], v[144:147], v[20:23]\n\t" \
  "s_barrier\n\t" \
  "v_mfma_f32_16x16x32_bf16 v[8:11], a[96:99], v[160:163], v[8:11]\n\t" \
  "s_add_u32 s97, s97, s61\n\t" \
  "ds_read_b128 v[144:147], v230 offset:16384\n\t" \
  "ds_read_b128 v[148:151], v230 offset:17408\n\t" \
  "s_add_u32 m0, s38, 0\n\t" \
  "v_mfma_f32_16x16x32_bf16 v[8:11], a[104:107], v[164:167], v[8:11]\n\t" \
  "buffer_load_dwordx4 v242, s[16:19], s97 offen offset:0 sc1 lds\n\t" \
  "ds_read_b64_tr_b16 a[192:193], v238 offset:16384\n\t" \
  "v_mfma_f32_16x16x32_bf16 v[8:11], a[112:115], v[168:171], v[8:11]\n\t" \
  "ds_read_b64_tr_b16 a[194:195], v238 offset:20480\n\t" \
  "ds_read_b128 v[152:155], v230 offset:18432\n\t" \
  "v_mfma_f32_16x16x32_bf16 v[8:11], a[120:123], v[172:175], v[8:11]\n\t" \
  "buffer_load_dwordx4 v242, s[16:19], s97 offen offset:1024 sc1 lds\n\t" \
  "v_mfma_f32_16x16x32_bf16 v[24:27], a[124:127], v[172:175], v[24:27]\n\t" \
  "ds_read_b128 v[156:159], v230 offset:19456\n\t" \
  "v_mfma_f32_16x16x32_bf16 v[24:27], a[116:119], v[168:171], v[24:27]\n\t" \
  "ds_read_b64_tr_b16 a[196:197], v239 offset:16384\n\t" \
  "v_mfma_f32_16x16x32_bf16 v[24:27], a[108:111], v[164:167], v[24:27]\n\t" \
  "buffer_load_dwordx4 v242, s[16:19], s97 offen offset:2048 sc1 lds\n\t" \
  "v_mfma_f32_16x16x32_bf16 v[24:27], a[100:103], v[160:163], v[24:27]\n\t" \
  "s_waitcnt lgkmcnt(7)\n\t" \
  "ds_read_b64_tr_b16 a[198:199], v239 offset:20480\n\t" \
  "v_mfma_f32_16x16x32_bf16 v[12:15], a[96:99], v[128:131], v[12:15]\n\t" \
  "ds_read_b128 v[160:163], v230 offset:20480\n\t" \
  "ds_read_b128 v[164:167], v230 offset:21504\n\t" \
  "v_mfma_f32_16x16x32_bf16 v[12:15], a[104:107], v[132:135], v[12:15]\n\t" \
  "buffer_load_dwordx4 v242, s[16:19], s97 offen offset:3072 sc1 lds\n\t" \
  "v_mfma_f32_16x16x32_bf16 v[12:15], a[112:115], v[136:139], v[12:15]\n\t" \
  "s_add_u32 s97, s64, 917504\n\t" \
  "s_and_b32 s97, s97, 0xe0000\n\t" \
  "ds_read_b128 v[168:171], v230 offset:22528\n\t" \
  "s_add_u32 s97, s97, s61\n\t" \
  "v_mfma_f32_16x16x32_bf16 v[12:15], a[120:123], v[140:143], v[12:15]\n\t" \
  "s_add_u32 m0, s38, 32768\n\t" \
  "ds_read_b128 v[172:175], v230 offset:23552\n\t" \
  "ds_read_b64_tr_b16 a[200:201], v238 offset:24576\n\t" \
  "v_mfma_f32_16x16x32_bf16 v[28:31], a[124:127], v[140:143], v[28:31]\n\t" \
  "buffer_load_dwordx4 v242, s[16:19], s97 offen offset:0 sc1 lds\n\t" \
  "v_mfma_f32_16x16x32_bf16 v[28:31], a[116:119], v[136:139], v[28:31]\n\t" \
  "ds_read_b64_tr_b16 a[202:203], v238 offset:28672\n\t" \
  "v_mfma_f32_16x16x32_bf16 v[28:31], a[108:111], v[132:135], v[28:31]\n\t" \
  "ds_read_b64_tr_b16 a[204:205], v239 offset:24576\n\t" \
  "v_mfma_f32_16x16x32_bf16 v[28:31], a[100:103], v[128:131], v[28:31]\n\t" \
  "s_waitcnt lgkmcnt(9)\n\t" \
  "buffer_load_dwordx4 v242, s[16:19], s97 offen offset:1024 sc1 lds\n\t" \
  "v_mfma_f32_16x16x32_bf16 v[32:35], a[32:35], v[144:147], v[32:35]\n\t" \
  "ds_read_b128 v[128:131], v230 offset:24576\n\t" \
  "ds_read_b128 v[132:135], v230 offset:25600\n\t" \
  "v_mfma_f32_16x16x32_bf16 v[32:35], a[40:43], v[148:151], v[32:35]\n\t" \
  "ds_read_b64_tr_b16 a[206:207], v239 offset:28672\n\t" \
  "ds_read_b128 v[136:139], v230 offset:26624\n\t" \
  "v_mfma_f32_16x16x32_bf16 v[32:35], a[48:51], v[152:155], v[32:35]\n\t" \
  "buffer_load_dwordx4 v242, s[16:19], s97 offen offset:2048 sc1 lds\n\t" \
  "v_mfma_f32_16x16x32_bf16 v[32:35], a[56:59], v[156:159], v[32:35]\n\t" \
  "s_waitcnt vmcnt(11)\n\t" \
  "v_cmp_ne_u32 vcc, s36, v246\n\t" \
  "v_mfma_f32_16x16x32_bf16 v[48:51], a[60:63], v[156:159], v[48:51]\n\t" \
  "s_and_b32 vcc_lo, vcc_lo, 0xfe0000\n\t" \
  "s_cbranch_scc1 .Lat_s2\n\t" \
  ".Lat_k2:\n\t" \
  "v_mfma_f32_16x16x32_bf16 v[48:51], a[52:55], v[152:155], v[48:51]\n\t" \
  "buffer_load_dwordx4 v242, s[16:19], s97 offen offset:3072 sc1 lds\n\t" \
  "v_mfma_f32_16x16x32_bf16 v[48:51], a[44:47], v[148:151], v[48:51]\n\t" \
  "s_add_u32 s97, s64, 131072\n\t" \
  "s_and_b32 s97, s97, 0xe0000\n\t" \
  "ds_read_b128 v[140:143], v230 offset:27648\n\t" \
  "s_add_u32 s97, s97, s62\n\t" \
  "v_mfma_f32_16x16x32_bf16 v[48:51], a[36:39], v[144:147], v[48:51]\n\t" \
  "s_waitcnt lgkmcnt(8)\n\t" \
  "ds_read_b64_tr_b16 a[208:209], v234 offset:49152\n\t" \
  "s_add_u32 m0, s38, 98304\n\t" \
  "v_mfma_f32_16x16x32_bf16 v[36:39], a[32:35], v[160:163], v[36:39]\n\t" \
  "buffer_load_dwordx4 v242, s[16:19], s97 offen offset:0 sc1 lds\n\t" \
  "ds_read_b128 v[144:147], v230 offset:28672\n\t" \
  "ds_read_b128 v[148:151], v230 offset:29696\n\t" \
  "v_mfma_f32_16x16x32_bf16 v[36:39], a[40:43], v[164:167], v[36:39]\n\t" \
  "ds_read_b64_tr_b16 a[210:211], v234 offset:53248\n\t" \
  "ds_read_b128 v[152:155], v230 offset:30720\n\t" \
  "v_mfma_f32_16x16x32_bf16 v[36:39], a[48:51], v[168:171], v[36:39]\n\t" \
  "ds_read_b128 v[156:159], v230 offset:31744\n\t" \
  "v_mfma_f32_16x16x32_bf16 v[36:39], a[56:59], v[172:175], v[36:39]\n\t" \
  "buffer_load_dwordx4 v242, s[16:19], s97 offen offset:1024 sc1 lds\n\t" \
  "v_mfma_f32_16x16x32_bf16 v[52:55], a[60:63], v[172:175], v[52:55]\n\t" \
  "ds_read_b64_tr_b16 a[212:213], v235 offset:49152\n\t" \
  "v_mfma_f32_16x16x32_bf16 v[52:55], a[52:55], v[168:171], v[52:55]\n\t" \
  "ds_read_b64_tr_b16 a[214:215], v235 offset:53248\n\t" \
  "v_mfma_f32_16x16x32_bf16 v[52:55], a[44:47], v[164:167], v[52:55]\n\t" \
  "s_waitcnt vmcnt(10) lgkmcnt(8)\n\t" \
  "buffer_load_dwordx4 v242, s[16:19], s97 offen offset:2048 sc1 lds\n\t" \
  "v_mfma_f32_16x16x32_bf16 v[52:55], a[36:39], v[160:163], v[52:55]\n\t" \
  "s_barrier\n\t" \
  "v_mfma_f32_16x16x32_bf16 v[40:43], a[32:35], v[128:131], v[40:43]\n\t" \
  "ds_read_b128 v[160:163], v232 offset:0\n\t" \
  "v_mfma_f32_16x16x32_bf16 v[40:43], a[40:43], v[132:135], v[40:43]\n\t" \
  "ds_read_b64_tr_b16 a[216:217], v234 offset:57344\n\t" \
  "v_mfma_f32_16x16x32_bf16 v[40:43], a[48:51], v[136:139], v[40:43]\n\t" \
  "buffer_load_dwordx4 v242, s[16:19], s97 offen offset:3072 sc1 lds\n\t" \
  "v_mfma_f32_16x16x32_bf16 v[40:43], a[56:59], v[140:143], v[40:43]\n\t" \
  "s_add_u32 s97, s64, 262144\n\t" \
  "ds_read_b64_tr_b16 a[218:219], v234 offset:61440\n\t" \
  "ds_read_b128 v[164:167], v232 offset:1024\n\t" \
  "s_and_b32 s97, s97, 0xe0000\n\t" \
  "v_mfma_f32_16x16x32_bf16 v[56:59], a[60:63], v[140:143], v[56:59]\n\t" \
  "s_add_u32 s97, s97, s62\n\t" \
  "ds_read_b128 v[168:171], v232 offset:2048\n\t" \
  "s_add_u32 m0, s38, 65536\n\t" \
  "v_mfma_f32_16x16x32_bf16 v[56:59], a[52:55], v[136:139], v[56:59]\n\t" \
  "ds_read_b128 v[172:175], v232 offset:3072\n\t" \
  "v_mfma_f32_16x16x32_bf16 v[56:59], a[44:47], v[132:135], v[56:59]\n\t" \
  "buffer_load_dwordx4 v242, s[16:19], s97 offen offset:0 sc1 lds\n\t" \
  "v_mfma_f32_16x16x32_bf16 v[56:59], a[36:39], v[128:131], v[56:59]\n\t" \
  "s_waitcnt lgkmcnt(8)\n\t" \
  "ds_read_b64_tr_b16 a[220:221], v235 offset:57344\n\t" \
  "v_mfma_f32_16x16x32_bf16 v[44:47], a[32:35], v[144:147], v[44:47]\n\t" \
  "ds_read_b128 v[128:131], v232 offset:4096\n\t" \
  "v_mfma_f32_16x16x32_bf16 v[44:47], a[40:43], v[148:151], v[44:47]\n\t" \
  "buffer_load_dwordx4 v242, s[16:19], s97 offen offset:1024 sc1 lds\n\t" \
  "v_mfma_f32_16x16x32_bf16 v[44:47], a[48:51], v[152:155], v[44:47]\n\t" \
  "ds_read_b64_tr_b16 a[222:223], v235 offset:61440\n\t" \
  "v_mfma_f32_16x16x32_bf16 v[44:47], a[56:59], v[156:159], v[44:47]\n\t" \
  "ds_read_b128 v[132:135], v232 offset:5120\n\t" \
  "v_mfma_f32_16x16x32_bf16 v[60:63], a[60:63], v[156:159], v[60:63]\n\t" \
  "ds_read_b128 v[136:139], v232 offset:6144\n\t" \
  "v_mfma_f32_16x16x32_bf16 v[60:63], a[52:55], v[152:155], v[60:63]\n\t" \
  "buffer_load_dwordx4 v242, s[16:19], s97 offen offset:2048 sc1 lds\n\t" \
  "v_mfma_f32_16x16x32_bf16 v[60:63], a[44:47], v[148:151], v[60:63]\n\t" \
  "ds_read_b128 v[140:143], v232 offset:7168\n\t" \
  "v_mfma_f32_16x16x32_bf16 v[60:63], a[36:39], v[144:147], v[60:63]\n\t" \
  "s_waitcnt lgkmcnt(3)\n\t" \
  "v_mfma_f32_16x16x32_bf16 v[0:3], a[192:195], v[160:163], v[0:3]\n\t" \
  "buffer_load_dwordx4 v242, s[16:19], s97 offen offset:3072 sc1 lds\n\t" \
  "v_mfma_f32_16x16x32_bf16 v[0:3], a[200:203], v[164:167], v[0:3]\n\t" \
  "s_add_u32 s97, s64, 393216\n\t" \
  "ds_read_b128 v[144:147], v232 offset:8192\n\t" \
  "s_and_b32 s97, s97, 0xe0000\n\t" \
  "v_mfma_f32_16x16x32_bf16 v[0:3], a[208:211], v[168:171], v[0:3]\n\t" \
  "s_add_u32 s97, s97, s62\n\t" \
  "ds_read_b128 v[148:151], v232 offset:9216\n\t" \
  "s_add_u32 m0, s38, 114688\n\t" \
  "v_mfma_f32_16x16x32_bf16 v[0:3], a[216:219], v[172:175], v[0:3]\n\t" \
  "ds_read_b128 v[152:155], v232 offset:10240\n\t" \
  "v_mfma_f32_16x16x32_bf16 v[16:19], a[220:223], v[172:175], v[16:19]\n\t" \
  "buffer_load_dwordx4 v242, s[16:19], s97 offen offset:0 sc1 lds\n\t" \
  "v_mfma_f32_16x16x32_bf16 v[16:19], a[212:215], v[168:171], v[16:19]\n\t" \
  "ds_read_b128 v[156:159], v232 offset:11264\n\t" \
  "v_mfma_f32_16x16x32_bf16 v[16:19], a[204:207], v[164:167], v[16:19]\n\t" \
  "s_nop 2\n\t" \
  "v_mfma_f32_16x16x32_bf16 v[16:19], a[196:199], v[160:163], v[16:19]\n\t" \
  "s_waitcnt lgkmcnt(4)\n\t" \
  "buffer_load_dwordx4 v242, s[16:19], s97 offen offset:1024 sc1 lds\n\t" \
  "v_mfma_f32_16x16x32_bf16 v[4:7], a[192:195], v[128:131], v[4:7]\n\t" \
  "ds_read_b128 v[160:163], v232 offset:12288\n\t" \
  "v_cvt_pk_bf16_f32 v208, v0, v1\n\t" \
  "v_mfma_f32_16x16x32_bf16 v[4:7], a[200:203], v[132:135], v[4:7]\n\t" \
  "ds_read_b128 v[164:167], v232 offset:13312\n\t" \
  "v_cvt_pk_bf16_f32 v209, v2, v3\n\t" \
  "v_mfma_f32_16x16x32_bf16 v[4:7], a[208:211], v[136:139], v[4:7]\n\t" \
  "v_cvt_pk_bf16_f32 v210, v16, v17\n\t" \
  "v_cvt_pk_bf16_f32 v211, v18, v19\n\t" \
  "v_mfma_f32_16x16x32_bf16 v[4:7], a[216:219], v[140:143], v[4:7]\n\t" \
  "buffer_load_dwordx4 v242, s[16:19], s97 offen offset:2048 sc1 lds\n\t" \
  "v_mfma_f32_16x16x32_bf16 v[20:23], a[220:223], v[140:143], v[20:23]\n\t" \
  "ds_read_b128 v[168:171], v232 offset:14336\n\t" \
  "v_mfma_f32_16x16x32_bf16 v[20:23], a[212:215], v[136:139], v[20:23]\n\t" \
  "ds_read_b128 v[172:175], v232 offset:15360\n\t" \
  "v_mfma_f32_16x16x32_bf16 v[20:23], a[204:207], v[132:135], v[20:23]\n\t" \
  "s_nop 2\n\t" \
  "v_mfma_f32_16x16x32_bf16 v[20:23], a[196:199], v[128:131], v[20:23]\n\t" \
  "s_waitcnt lgkmcnt(4)\n\t" \
  "v_cvt_pk_bf16_f32 v212, v4, v5\n\t" \
  "v_cvt_pk_bf16_f32 v213, v6, v7\n\t" \
  "v_mfma_f32_16x16x32_bf16 v[8:11], a[192:195], v[144:147], v[8:11]\n\t" \
  "ds_read_b128 v[128:131], v242 offset:16384\n\t" \
  "s_nop 0\n\t" \
  "v_mfma_f32_16x16x32_bf16 v[8:11], a[200:203], v[148:151], v[8:11]\n\t" \
  "ds_read_b128 v[132:135], v242 offset:17408\n\t" \
  "v_cvt_pk_bf16_f32 v214, v20, v21\n\t" \
  "v_mfma_f32_16x16x32_bf16 v[8:11], a[208:211], v[152:155], v[8:11]\n\t" \
  "ds_read_b128 v[136:139], v242 offset:18432\n\t" \
  "v_cvt_pk_bf16_f32 v215, v22, v23\n\t" \
  "v_mfma_f32_16x16x32_bf16 v[8:11], a[216:219], v[156:159], v[8:11]\n\t" \
  "ds_read_b128 v[140:143], v242 offset:19456\n\t" \
  "v_mfma_f32_16x16x32_bf16 v[24:27], a[220:223], v[156:159], v[24:27]\n\t" \
  "v_mfma_f32_16x16x32_bf16 v[24:27], a[212:215], v[152:155], v[24:27]\n\t" \
  "v_mfma_f32_16x16x32_bf16 v[24:27], a[204:207], v[148:151], v[24:27]\n\t" \
  "s_nop 2\n\t" \
  "v_mfma_f32_16x16x32_bf16 v[24:27], a[196:199], v[144:147], v[24:27]\n\t" \
  "s_waitcnt lgkmcnt(4)\n\t" \
  "v_cvt_pk_bf16_f32 v216, v8, v9\n\t" \
  "v_cvt_pk_bf16_f32 v217, v10, v11\n\t" \
  "v_mfma_f32_16x16x32_bf16 v[12:15], a[192:195], v[160:163], v[12:15]\n\t" \
  "ds_read_b128 v[144:147], v242 offset:20480\n\t" \
  "s_nop 0\n\t" \
  "v_mfma_f32_16x16x32_bf16 v[12:15], a[200:203], v[164:167], v[12:15]\n\t" \
  "ds_read_b128 v[148:151], v242 offset:21504\n\t" \
  "v_cvt_pk_bf16_f32 v218, v24, v25\n\t" \
  "v_mfma_f32_16x16x32_bf16 v[12:15], a[208:211], v[168:171], v[12:15]\n\t" \
  "ds_read_b128 v[152:155], v242 offset:22528\n\t" \
  "v_cvt_pk_bf16_f32 v219, v26, v27\n\t" \
  "v_mfma_f32_16x16x32_bf16 v[12:15], a[216:219], v[172:175], v[12:15]\n\t" \
  "buffer_store_dwordx4 v[208:211], v242, s[16:19], s67 offen offset:0 sc1\n\t" \
  "ds_read_b128 v[156:159], v242 offset:23552\n\t" \
  "v_mfma_f32_16x16x32_bf16 v[28:31], a[220:223], v[172:175], v[28:31]\n\t" \
  "v_mfma_f32_16x16x32_bf16 v[28:31], a[212:215], v[168:171], v[28:31]\n\t" \
  "v_mfma_f32_16x16x32_bf16 v[28:31], a[204:207], v[164:167], v[28:31]\n\t" \
  "s_nop 2\n\t" \
  "v_mfma_f32_16x16x32_bf16 v[28:31], a[196:199], v[160:163], v[28:31]\n\t" \
  "s_waitcnt lgkmcnt(4)\n\t" \
  "v_cvt_pk_bf16_f32 v220, v12, v13\n\t" \
  "v_cvt_pk_bf16_f32 v221, v14, v15\n\t" \
  "v_mfma_f32_16x16x32_bf16 v[32:35], a[64:67], v[128:131], v[32:35]\n\t" \
  "buffer_store_dwordx4 v[212:215], v242, s[16:19], s67 offen offset:1024 sc1\n\t" \
  "ds_read_b128 v[160:163], v242 offset:24576\n\t" \
  "ds_read_b128 v[164:167], v242 offset:25600\n\t" \
  "ds_read_b128 v[168:171], v242 offset:26624\n\t" \
  "v_mfma_f32_16x16x32_bf16 v[32:35], a[72:75], v[132:135], v[32:35]\n\t" \
  "v_cvt_pk_bf16_f32 v222, v28, v29\n\t" \
  "v_cvt_pk_bf16_f32 v223, v30, v31\n\t" \
  "v_mfma_f32_16x16x32_bf16 v[32:35], a[80:83], v[136:139], v[32:35]\n\t" \
  "ds_read_b128 v[172:175], v242 offset:27648\n\t" \
  "v_mfma_f32_16x16x32_bf16 v[32:35], a[88:91], v[140:143], v[32:35]\n\t" \
  "v_mfma_f32_16x16x32_bf16 v[48:51], a[92:95], v[140:143], v[48:51]\n\t" \
  "v_mfma_f32_16x16x32_bf16 v[48:51], a[84:87], v[136:139], v[48:51]\n\t" \
  "v_mfma_f32_16x16x32_bf16 v[48:51], a[76:79], v[132:135], v[48:51]\n\t" \
  "v_mfma_f32_16x16x32_bf16 v[48:51], a[68:71], v[128:131], v[48:51]\n\t" \
  "s_waitcnt lgkmcnt(4)\n\t" \
  "v_mfma_f32_16x16x32_bf16 v[36:39], a[64:67], v[144:147], v[36:39]\n\t" \
  "ds_read_b128 v[128:131], v242 offset:28672\n\t" \
  "v_mfma_f32_16x16x32_bf16 v[36:39], a[72:75], v[148:151], v[36:39]\n\t" \
  "ds_read_b128 v[132:135], v242 offset:29696\n\t" \
  "v_mfma_f32_16x16x32_bf16 v[36:39], a[80:83], v[152:155], v[36:39]\n\t" \
  "buffer_store_dwordx4 v[216:219], v242, s[16:19], s67 offen offset:2048 sc1\n\t" \
  "ds_read_b128 v[136:139], v242 offset:30720\n\t" \
  "ds_read_b128 v[140:143], v242 offset:31744\n\t" \
  "v_mfma_f32_16x16x32_bf16 v[36:39], a[88:91], v[156:159], v[36:39]\n\t" \
  "v_mfma_f32_16x16x32_bf16 v[52:55], a[92:95], v[156:159], v[52:55]\n\t" \
  "v_mfma_f32_16x16x32_bf16 v[52:55], a[84:87], v[152:155], v[52:55]\n\t" \
  "v_mfma_f32_16x16x32_bf16 v[52:55], a[76:79], v[148:151], v[52:55]\n\t" \
  "s_waitcnt vmcnt(14) lgkmcnt(4)\n\t" \
  "v_mfma_f32_16x16x32_bf16 v[52:55], a[68:71], v[144:147], v[52:55]\n\t" \
  "s_barrier\n\t" \
  "v_mfma_f32_16x16x32_bf16 v[40:43], a[64:67], v[160:163], v[40:43]\n\t" \
  "ds_read_b128 v[144:147], v242 offset:0\n\t" \
  "v_mfma_f32_16x16x32_bf16 v[40:43], a[72:75], v[164:167], v[40:43]\n\t" \
  "ds_read_b128 v[148:151], v242 offset:1024\n\t" \
  "v_mfma_f32_16x16x32_bf16 v[40:43], a[80:83], v[168:171], v[40:43]\n\t" \
  "ds_read_b128 v[152:155], v242 offset:2048\n\t" \
  "v_mfma_f32_16x16x32_bf16 v[40:43], a[88:91], v[172:175], v[40:43]\n\t" \
  "ds_read_b128 v[156:159], v242 offset:3072\n\t" \
  "v_mfma_f32_16x16x32_bf16 v[56:59], a[92:95], v[172:175], v[56:59]\n\t" \
  "v_mfma_f32_16x16x32_bf16 v[56:59], a[84:87], v[168:171], v[56:59]\n\t" \
  "v_mfma_f32_16x16x32_bf16 v[56:59], a[76:79], v[164:167], v[56:59]\n\t" \
  "v_mfma_f32_16x16x32_bf16 v[56:59], a[68:71], v[160:163], v[56:59]\n\t" \
  "s_waitcnt lgkmcnt(4)\n\t" \
  "buffer_store_dwordx4 v[220:223], v242, s[16:19], s67 offen offset:3072 sc1\n\t" \
  "v_mfma_f32_16x16x32_bf16 v[44:47], a[64:67], v[128:131], v[44:47]\n\t" \
  "ds_read_b128 v[160:163], v242 offset:4096\n\t" \
  "v_mfma_f32_16x16x32_bf16 v[44:47], a[72:75], v[132:135], v[44:47]\n\t" \
  "ds_read_b128 v[164:167], v242 offset:5120\n\t" \
  "v_mfma_f32_16x16x32_bf16 v[44:47], a[80:83], v[136:139], v[44:47]\n\t" \
  "ds_read_b128 v[168:171], v242 offset:6144\n\t" \
  "v_mfma_f32_16x16x32_bf16 v[44:47], a[88:91], v[140:143], v[44:47]\n\t" \
  "ds_read_b128 v[172:175], v242 offset:7168\n\t" \
  "v_mfma_f32_16x16x32_bf16 v[60:63], a[92:95], v[140:143], v[60:63]\n\t" \
  "v_mfma_f32_16x16x32_bf16 v[60:63], a[84:87], v[136:139], v[60:63]\n\t" \
  "v_mfma_f32_16x16x32_bf16 v[60:63], a[76:79], v[132:135], v[60:63]\n\t" \
  "v_mfma_f32_16x16x32_bf16 v[60:63], a[68:71], v[128:131], v[60:63]\n\t" \
  "s_waitcnt lgkmcnt(4)\n\t" \
  "v_mfma_f32_16x16x32_bf16 v[32:35], a[96:99], v[144:147], v[32:35]\n\t" \
  "ds_read_b128 v[128:131], v242 offset:8192\n\t" \
  "v_mfma_f32_16x16x32_bf16 v[32:35], a[104:107], v[148:151], v[32:35]\n\t" \
  "ds_read_b128 v[132:135], v242 offset:9216\n\t" \
  "v_mfma_f32_16x16x32_bf16 v[32:35], a[112:115], v[152:155], v[32:35]\n\t" \
  "ds_read_b128 v[136:139], v242 offset:10240\n\t" \
  "v_mfma_f32_16x16x32_bf16 v[32:35], a[120:123], v[156:159], v[32:35]\n\t" \
  "buffer_load_dwordx4 v242, s[16:19], s97 offen offset:3072 sc1 lds\n\t" \
  "v_mfma_f32_16x16x32_bf16 v[48:51], a[124:127], v[156:159], v[48:51]\n\t" \
  "s_add_u32 s97, s64, 524288\n\t" \
  "ds_read_b128 v[140:143], v242 offset:11264\n\t" \
  "s_and_b32 s97, s97, 0xe0000\n\t" \
  "v_mfma_f32_16x16x32_bf16 v[48:51], a[116:119], v[152:155], v[48:51]\n\t" \
  "s_add_u32 s97, s97, s62\n\t" \
  "s_add_u32 m0, s38, 81920\n\t" \
  "v_mfma_f32_16x16x32_bf16 v[48:51], a[108:111], v[148:151], v[48:51]\n\t" \
  "v_mfma_f32_16x16x32_bf16 v[48:51], a[100:103], v[144:147], v[48:51]\n\t" \
  "s_waitcnt lgkmcnt(4)\n\t" \
  "buffer_load_dwordx4 v242, s[16:19], s97 offen offset:0 sc1 lds\n\t" \
  "v_mfma_f32_16x16x32_bf16 v[36:39], a[96:99], v[160:163], v[36:39]\n\t" \
  "ds_read_b128 v[144:147], v242 offset:12288\n\t" \
  "v_mfma_f32_16x16x32_bf16 v[36:39], a[104:107], v[164:167], v[36:39]\n\t" \
  "ds_read_b128 v[148:151], v242 offset:13312\n\t" \
  "v_mfma_f32_16x16x32_bf16 v[36:39], a[112:115], v[168:171], v[36:39]\n\t" \
  "ds_read_b128 v[152:155], v242 offset:14336\n\t" \
  "v_mfma_f32_16x16x32_bf16 v[36:39], a[120:123], v[172:175], v[36:39]\n\t" \
  "buffer_load_dwordx4 v242, s[16:19], s97 offen offset:1024 sc1 lds\n\t" \
  "v_mfma_f32_16x16x32_bf16 v[52:55], a[124:127], v[172:175], v[52:55]\n\t" \
  "ds_read_b128 v[156:159], v242 offset:15360\n\t" \
  "v_mfma_f32_16x16x32_bf16 v[52:55], a[116:119], v[168:171], v[52:55]\n\t" \
  "v_mfma_f32_16x16x32_bf16 v[52:55], a[108:111], v[164:167], v[52:55]\n\t" \
  "buffer_load_dwordx4 v242, s[16:19], s97 offen offset:2048 sc1 lds\n\t" \
  "v_mfma_f32_16x16x32_bf16 v[52:55], a[100:103], v[160:163], v[52:55]\n\t" \
  "s_waitcnt lgkmcnt(4)\n\t" \
  "v_mfma_f32_16x16x32_bf16 v[40:43], a[96:99], v[128:131], v[40:43]\n\t" \
  "ds_read_b128 v[160:163], v242 offset:32768\n\t" \
  "v_mfma_f32_16x16x32_bf16 v[40:43], a[104:107], v[132:135], v[40:43]\n\t" \
  ".Lhs13:\n\ts_cbranch_execz .Lhs13n\n\t" \
  "ds_read_b128 v[164:167], v242 offset:33792\n\t" \
  "v_mfma_f32_16x16x32_bf16 v[40:43], a[112:115], v[136:139], v[40:43]\n\t" \
  "buffer_load_dwordx4 v242, s[16:19], s97 offen offset:3072 sc1 lds\n\t" \
  "v_mfma_f32_16x16x32_bf16 v[40:43], a[120:123], v[140:143], v[40:43]\n\t" \
  "s_add_u32 s97, s64, 655360\n\t" \
  "s_and_b32 s97, s97, 0xe0000\n\t" \
  "s_add_u32 s97, s97, s62\n\t" \
  "v_mfma_f32_16x16x32_bf16 v[56:59], a[124:127], v[140:143], v[56:59]\n\t" \
  "s_add_u32 m0, s38, 131072\n\t" \
  "ds_read_b128 v[168:171], v242 offset:34816\n\t" \
  "v_mfma_f32_16x16x32_bf16 v[56:59], a[116:119], v[136:139], v[56:59]\n\t" \
  "buffer_load_dwordx4 v242, s[16:19], s97 offen offset:0 sc1 lds\n\t" \
  "v_mfma_f32_16x16x32_bf16 v[56:59], a[108:111], v[132:135], v[56:59]\n\t" \
  "ds_read_b128 v[172:175], v242 offset:35840\n\t" \
  "v_mfma_f32_16x16x32_bf16 v[56:59], a[100:103], v[128:131], v[56:59]\n\t" \
  "s_waitcnt lgkmcnt(4)\n\t" \
  "v_mfma_f32_16x16x32_bf16 v[44:47], a[96:99], v[144:147], v[44:47]\n\t" \
  "ds_read_b128 v[128:131], v242 offset:36864\n\t" \
  "v_mfma_f32_16x16x32_bf16 v[44:47], a[104:107], v[148:151], v[44:47]\n\t" \
  "buffer_load_dwordx4 v242, s[16:19], s97 offen offset:1024 sc1 lds\n\t" \
  "v_mfma_f32_16x16x32_bf16 v[44:47], a[112:115], v[152:155], v[44:47]\n\t" \
  "ds_read_b128 v[132:135], v242 offset:37888\n\t" \
  "v_mfma_f32_16x16x32_bf16 v[44:47], a[120:123], v[156:159], v[44:47]\n\t" \
  "ds_read_b128 v[136:139], v242 offset:38912\n\t" \
  "v_mfma_f32_16x16x32_bf16 v[60:63], a[124:127], v[156:159], v[60:63]\n\t" \
  "buffer_load_dwordx4 v242, s[16:19], s97 offen offset:2048 sc1 lds\n\t" \
  "v_mfma_f32_16x16x32_bf16 v[60:63], a[116:119], v[152:155], v[60:63]\n\t" \
  "ds_read_b128 v[140:143], v242 offset:39936\n\t" \
  "v_mfma_f32_16x16x32_bf16 v[60:63], a[108:111], v[148:151], v[60:63]\n\t" \
  "v_mfma_f32_16x16x32_bf16 v[60:63], a[100:103], v[144:147], v[60:63]\n\t" \
  "s_waitcnt lgkmcnt(4)\n\t" \
  "v_mfma_f32_16x16x32_bf16 v[32:35], a[192:195], v[160:163], v[32:35]\n\t" \
  "buffer_load_dwordx4 v242, s[16:19], s97 offen offset:3072 sc1 lds\n\t" \
  "v_mfma_f32_16x16x32_bf16 v[32:35], a[200:203], v[164:167], v[32:35]\n\t" \
  "s_add_u32 s97, s64, 786432\n\t" \
  "ds_read_b128 v[144:147], v242 offset:40960\n\t" \
  "s_and_b32 s97, s97, 0xe0000\n\t" \
  "v_mfma_f32_16x16x32_bf16 v[32:35], a[208:211], v[168:171], v[32:35]\n\t" \
  "s_add_u32 s97, s97, s62\n\t" \
  "ds_read_b128 v[148:151], v242 offset:41984\n\t" \
  "s_add_u32 m0, s38, 49152\n\t" \
  "v_mfma_f32_16x16x32_bf16 v[32:35], a[216:219], v[172:175], v[32:35]\n\t" \
  "ds_read_b128 v[152:155], v242 offset:43008\n\t" \
  "v_mfma_f32_16x16x32_bf16 v[48:51], a[220:223], v[172:175], v[48:51]\n\t" \
  "ds_read_b128 v[156:159], v242 offset:44032\n\t" \
  "v_mfma_f32_16x16x32_bf16 v[48:51], a[212:215], v[168:171], v[48:51]\n\t" \
  "v_mfma_f32_16x16x32_bf16 v[48:51], a[204:207], v[164:167], v[48:51]\n\t" \
  "buffer_load_dwordx4 v242, s[16:19], s97 offen offset:0 sc1 lds\n\t" \
  "v_mfma_f32_16x16x32_bf16 v[48:51], a[196:199], v[160:163], v[48:51]\n\t" \
  "s_waitcnt lgkmcnt(4)\n\t" \
  "v_cvt_pk_bf16_f32 v0, v32, v33\n\t" \
  "v_cvt_pk_bf16_f32 v1, v34, v35\n\t" \
  "v_mfma_f32_16x16x32_bf16 v[36:39], a[192:195], v[128:131], v[36:39]\n\t" \
  "ds_read_b128 v[160:163], v242 offset:45056\n\t" \
  "s_nop 0\n\t" \
  "v_mfma_f32_16x16x32_bf16 v[36:39], a[200:203], v[132:135], v[36:39]\n\t" \
  "ds_read_b128 v[164:167], v242 offset:46080\n\t" \
  "v_cvt_pk_bf16_f32 v2, v48, v49\n\t" \
  "v_mfma_f32_16x16x32_bf16 v[36:39], a[208:211], v[136:139], v[36:39]\n\t" \
  "ds_read_b128 v[168:171], v242 offset:47104\n\t" \
  "v_cvt_pk_bf16_f32 v3, v50, v51\n\t" \
  "v_mfma_f32_16x16x32_bf16 v[36:39], a[216:219], v[140:143], v[36:39]\n\t" \
  "ds_read_b128 v[172:175], v242 offset:48128\n\t" \
  "v_mfma_f32_16x16x32_bf16 v[52:55], a[220:223], v[140:143], v[52:55]\n\t" \
  "v_mfma_f32_16x16x32_bf16 v[52:55], a[212:215], v[136:139], v[52:55]\n\t" \
  "v_mfma_f32_16x16x32_bf16 v[52:55], a[204:207], v[132:135], v[52:55]\n\t" \
  "s_waitcnt vmcnt(17) lgkmcnt(4)\n\t" \
  "s_nop 1\n\t" \
  "v_mfma_f32_16x16x32_bf16 v[52:55], a[196:199], v[128:131], v[52:55]\n\t" \
  "s_barrier\n\t" \
  "v_mfma_f32_16x16x32_bf16 v[40:43], a[192:195], v[144:147], v[40:43]\n\t" \
  "ds_read_b128 v[128:131], v230 offset:32768\n\t" \
  "v_cvt_pk_bf16_f32 v4, v36, v37\n\t" \
  "v_mfma_f32_16x16x32_bf16 v[40:43], a[200:203], v[148:151], v[40:43]\n\t" \
  "v_cvt_pk_bf16_f32 v5, v38, v39\n\t" \
  "v_and_b32 v235, 15, v233\n\t" \
  "v_mfma_f32_16x16x32_bf16 v[40:43], a[208:211], v[152:155], v[40:43]\n\t" \
  "v_cvt_pk_bf16_f32 v6, v52, v53\n\t" \
  "v_cvt_pk_bf16_f32 v7, v54, v55\n\t" \
  "v_mfma_f32_16x16x32_bf16 v[40:43], a[216:219], v[156:159], v[40:43]\n\t" \
  "buffer_store_dwordx4 v[0:3], v242, s[16:19], s68 offen offset:0 sc1\n\t" \
  "ds_read_b128 v[132:135], v230 offset:33792\n\t" \
  "v_lshrrev_b32 v236, 4, v233\n\t" \
  "v_lshlrev_b32 v234, 13, v236\n\t" \
  "v_mfma_f32_16x16x32_bf16 v[56:59], a[220:223], v[156:159], v[56:59]\n\t" \
  "ds_read_b128 v[136:139], v230 offset:34816\n\t" \
  "v_lshl_add_u32 v234, v235, 4, v234\n\t" \
  "v_mfma_f32_16x16x32_bf16 v[56:59], a[212:215], v[152:155], v[56:59]\n\t" \
  "buffer_load_dwordx4 v242, s[16:19], s97 offen offset:1024 sc1 lds\n\t" \
  "v_mfma_f32_16x16x32_bf16 v[56:59], a[204:207], v[148:151], v[56:59]\n\t" \
  "ds_read_b128 v[140:143], v230 offset:35840\n\t" \
  "v_lshlrev_b32 v240, 7, v236\n\t" \
  "v_mfma_f32_16x16x32_bf16 v[56:59], a[196:199], v[144:147], v[56:59]\n\t" \
  "s_waitcnt lgkmcnt(4)\n\t" \
  "v_cvt_pk_bf16_f32 v8, v40, v41\n\t" \
  "v_cvt_pk_bf16_f32 v9, v42, v43\n\t" \
  "v_mfma_f32_16x16x32_bf16 v[44:47], a[192:195], v[160:163], v[44:47]\n\t" \
  "s_waitcnt vmcnt(12)\n\t" \
  "buffer_store_dword v243, v244, s[16:19], s71 offen offset:0 sc1\n\t" \
  "ds_read_b128 v[144:147], v230 offset:36864\n\t" \
  "v_mfma_f32_16x16x32_bf16 v[44:47], a[200:203], v[164:167], v[44:47]\n\t" \
  "v_cvt_pk_bf16_f32 v10, v56, v57\n\t" \
  "v_cvt_pk_bf16_f32 v11, v58, v59\n\t" \
  "v_mfma_f32_16x16x32_bf16 v[44:47], a[208:211], v[168:171], v[44:47]\n\t" \
  "buffer_store_dwordx4 v[4:7], v242, s[16:19], s68 offen offset:1024 sc1\n\t" \
  "ds_read_b128 v[148:151], v230 offset:37888\n\t" \
  "v_lshl_add_u32 v240, v235, 3, v240\n\t" \
  "v_add_u32 v240, s76, v240\n\t" \
  "v_mfma_f32_16x16x32_bf16 v[44:47], a[216:219], v[172:175], v[44:47]\n\t" \
  "ds_read_b128 v[152:155], v230 offset:38912\n\t" \
  "v_lshlrev_b32 v235, 2, v235\n\t" \
  "v_mfma_f32_16x16x32_bf16 v[60:63], a[220:223], v[172:175], v[60:63]\n\t" \
  "ds_read_b128 v[156:159], v230 offset:39936\n\t" \
  "v_mfma_f32_16x16x32_bf16 v[60:63], a[212:215], v[168:171], v[60:63]\n\t" \
  "v_mfma_f32_16x16x32_bf16 v[60:63], a[204:207], v[164:167], v[60:63]\n\t" \
  "s_nop 2\n\t" \
  "v_mfma_f32_16x16x32_bf16 v[60:63], a[196:199], v[160:163], v[60:63]\n\t" \
  "s_waitcnt lgkmcnt(4)\n\t" \
  "v_cvt_pk_bf16_f32 v12, v44, v45\n\t" \
  "v_cvt_pk_bf16_f32 v13, v46, v47\n\t" \
  STP("s_memrealtime s[94:95]\n\t") \
  STP("s_memtime s[92:93]\n\t") \
  "v_mfma_f32_16x16x32_bf16 v[64:67], a[128:131], v[128:131], v[64:67]\n\t" \
  "ds_read_b128 v[160:163], v230 offset:40960\n\t" \
  "s_nop 0\n\t" \
  "v_mfma_f32_16x16x32_bf16 v[64:67], a[136:139], v[132:135], v[64:67]\n\t" \
  "ds_read_b128 v[164:167], v230 offset:41984\n\t" \
  "v_cvt_pk_bf16_f32 v14, v60, v61\n\t" \
  "v_mfma_f32_16x16x32_bf16 v[64:67], a[144:147], v[136:139], v[64:67]\n\t" \
  "ds_read_b128 v[168:171], v230 offset:43008\n\t" \
  "v_cvt_pk_bf16_f32 v15, v62, v63\n\t" \
  "v_mfma_f32_16x16x32_bf16 v[64:67], a[152:155], v[140:143], v[64:67]\n\t" \
  "ds_read_b128 v[172:175], v230 offset:44032\n\t" \
  "s_add_u32 s78, s75, 0\n\t" \
  "v_mfma_f32_16x16x32_bf16 v[80:83], a[156:159], v[140:143], v[80:83]\n\t" \
  "v_mfma_f32_16x16x32_bf16 v[80:83], a[148:151], v[136:139], v[80:83]\n\t" \
  "v_mfma_f32_16x16x32_bf16 v[80:83], a[140:143], v[132:135], v[80:83]\n\t" \
  "v_mfma_f32_16x16x32_bf16 v[80:83], a[132:135], v[128:131], v[80:83]\n\t" \
  "s_waitcnt lgkmcnt(4)\n\t" \
  "buffer_load_dwordx4 v[208:211], v234, s[16:19], s78 offen sc1\n\t" \
  "v_mfma_f32_16x16x32_bf16 v[68:71], a[128:131], v[144:147], v[68:71]\n\t" \
  "ds_read_b128 v[128:131], v230 offset:45056\n\t" \
  "s_add_u32 s78, s75, 32768\n\t" \
  "v_mfma_f32_16x16x32_bf16 v[68:71], a[136:139], v[148:151], v[68:71]\n\t" \
  "ds_read_b128 v[132:135], v230 offset:46080\n\t" \
  "v_mfma_f32_16x16x32_bf16 v[68:71], a[144:147], v[152:155], v[68:71]\n\t" \
  "ds_read_b128 v[136:139], v230 offset:47104\n\t" \
  "v_mfma_f32_16x16x32_bf16 v[68:71], a[152:155], v[156:159], v[68:71]\n\t" \
  "ds_read_b128 v[140:143], v230 offset:48128\n\t" \
  "v_mfma_f32_16x16x32_bf16 v[84:87], a[156:159], v[156:159], v[84:87]\n\t" \
  STP("v_writelane_b32 v255, s94, 34\n\t") \
  STP("v_writelane_b32 v255, s92, 35\n\t") \
  "buffer_load_dwordx4 v[212:215], v234, s[16:19], s78 offen sc1\n\t" \
  "v_mfma_f32_16x16x32_bf16 v[84:87], a[148:151], v[152:155], v[84:87]\n\t" \
  "s_add_u32 s78, s75, 65536\n\t" \
  "v_mfma_f32_16x16x32_bf16 v[84:87], a[140:143], v[148:151], v[84:87]\n\t" \
  "v_mfma_f32_16x16x32_bf16 v[84:87], a[132:135], v[144:147], v[84:87]\n\t" \
  "s_waitcnt lgkmcnt(4)\n\t" \
  "v_mfma_f32_16x16x32_bf16 v[72:75], a[128:131], v[160:163], v[72:75]\n\t" \
  "ds_read_b128 v[144:147], v230 offset:0\n\t" \
  "v_mfma_f32_16x16x32_bf16 v[72:75], a[136:139], v[164:167], v[72:75]\n\t" \
  "buffer_load_dwordx4 v[216:219], v234, s[16:19], s78 offen sc1\n\t" \
  "v_mfma_f32_16x16x32_bf16 v[72:75], a[144:147], v[168:171], v[72:75]\n\t" \
  "ds_read_b128 v[148:151], v230 offset:1024\n\t" \
  "s_add_u32 s78, s75, 98304\n\t" \
  "v_mfma_f32_16x16x32_bf16 v[72:75], a[152:155], v[172:175], v[72:75]\n\t" \
  "ds_read_b128 v[152:155], v230 offset:2048\n\t" \
  "v_mfma_f32_16x16x32_bf16 v[88:91], a[156:159], v[172:175], v[88:91]\n\t" \
  "ds_read_b128 v[156:159], v230 offset:3072\n\t" \
  "v_mfma_f32_16x16x32_bf16 v[88:91], a[148:151], v[168:171], v[88:91]\n\t" \
  "v_mfma_f32_16x16x32_bf16 v[88:91], a[140:143], v[164:167], v[88:91]\n\t" \
  "buffer_load_dwordx4 v[220:223], v234, s[16:19], s78 offen sc1\n\t" \
  "v_mfma_f32_16x16x32_bf16 v[88:91], a[132:135], v[160:163], v[88:91]\n\t" \
  "s_waitcnt lgkmcnt(4)\n\t" \
  "buffer_load_dword v224, v235, s[32:35], s80 offen\n\t" \
  "v_mfma_f32_16x16x32_bf16 v[76:79], a[128:131], v[128:131], v[76:79]\n\t" \
  "ds_read_b128 v[160:163], v230 offset:4096\n\t" \
  "v_lshrrev_b32 v250, 4, v233\n\t" \
  "v_mfma_f32_16x16x32_bf16 v[76:79], a[136:139], v[132:135], v[76:79]\n\t" \
  "v_and_b32 v238, 15, v233\n\t" \
  "v_xor_b32 v26, v238, v250\n\t" \
  "v_mfma_f32_16x16x32_bf16 v[76:79], a[144:147], v[136:139], v[76:79]\n\t" \
  "ds_read_b128 v[164:167], v230 offset:5120\n\t" \
  "v_lshlrev_b32 v26, 4, v26\n\t" \
  "v_mfma_f32_16x16x32_bf16 v[76:79], a[152:155], v[140:143], v[76:79]\n\t" \
  "v_lshl_add_u32 v26, v250, 11, v26\n\t" \
  "v_xor_b32 v27, 64, v26\n\t" \
  "v_mfma_f32_16x16x32_bf16 v[92:95], a[156:159], v[140:143], v[92:95]\n\t" \
  "ds_read_b128 v[168:171], v230 offset:6144\n\t" \
  "v_xor_b32 v28, 0x80, v26\n\t" \
  "v_mfma_f32_16x16x32_bf16 v[92:95], a[148:151], v[136:139], v[92:95]\n\t" \
  "v_xor_b32 v29, 0xc0, v26\n\t" \
  "v_lshrrev_b32 v30, 5, v233\n\t" \
  "v_mfma_f32_16x16x32_bf16 v[92:95], a[140:143], v[132:135], v[92:95]\n\t" \
  "ds_read_b128 v[172:175], v230 offset:7168\n\t" \
  "v_lshl_add_u32 v30, s3, 2, v30\n\t" \
  "v_mfma_f32_16x16x32_bf16 v[92:95], a[132:135], v[128:131], v[92:95]\n\t" \
  "s_waitcnt lgkmcnt(4)\n\t" \
  "v_xor_b32 v30, v30, v238\n\t" \
  "v_lshlrev_b32 v30, 4, v30\n\t" \
  "v_mfma_f32_16x16x32_bf16 v[64:67], a[160:163], v[144:147], v[64:67]\n\t" \
  "buffer_store_dwordx4 v[8:11], v242, s[16:19], s68 offen offset:2048 sc1\n\t" \
  "ds_read_b128 v[128:131], v230 offset:8192\n\t" \
  "v_lshl_add_u32 v30, v238, 8, v30\n\t" \
  "v_and_b32 v250, 1, v250\n\t" \
  "v_mfma_f32_16x16x32_bf16 v[64:67], a[168:171], v[148:151], v[64:67]\n\t" \
  "v_lshl_add_u32 v30, v250, 3, v30\n\t" \
  "v_add_u32 v238, 0, v30\n\t" \
  "v_mfma_f32_16x16x32_bf16 v[64:67], a[176:179], v[152:155], v[64:67]\n\t" \
  "ds_read_b128 v[132:135], v230 offset:9216\n\t" \
  "v_xor_b32 v239, 32, v238\n\t" \
  "v_mfma_f32_16x16x32_bf16 v[64:67], a[184:187], v[156:159], v[64:67]\n\t" \
  "v_bfe_u32 v237, v233, 1, 2\n\t" \
  "v_bfe_u32 v16, v233, 3, 1\n\t" \
  "v_mfma_f32_16x16x32_bf16 v[80:83], a[188:191], v[156:159], v[80:83]\n\t" \
  "ds_read_b128 v[136:139], v230 offset:10240\n\t" \
  "v_lshlrev_b32 v237, 4, v237\n\t" \
  "v_mfma_f32_16x16x32_bf16 v[80:83], a[180:183], v[152:155], v[80:83]\n\t" \
  "v_lshl_add_u32 v237, v16, 3, v237\n\t" \
  "v_and_b32 v16, 1, v233\n\t" \
  "v_mfma_f32_16x16x32_bf16 v[80:83], a[172:175], v[148:151], v[80:83]\n\t" \
  "ds_read_b128 v[140:143], v230 offset:11264\n\t" \
  "v_lshl_add_u32 v237, v16, 2, v237\n\t" \
  "v_mfma_f32_16x16x32_bf16 v[80:83], a[164:167], v[144:147], v[80:83]\n\t" \
  "s_waitcnt lgkmcnt(4)\n\t" \
  "v_add_u32 v237, s76, v237\n\t" \
  "v_cmp_eq_u32 vcc, 2, v236\n\t" \
  "v_mfma_f32_16x16x32_bf16 v[68:71], a[160:163], v[160:163], v[68:71]\n\t" \
  "ds_read_b128 v[144:147], v230 offset:12288\n\t" \
  "v_mov_b32 v16, 0\n\t" \
  "v_mfma_f32_16x16x32_bf16 v[68:71], a[168:171], v[164:167], v[68:71]\n\t" \
  "v_mov_b32 v17, 0\n\t" \
  "v_mov_b32 v18, 0\n\t" \
  "v_mfma_f32_16x16x32_bf16 v[68:71], a[176:179], v[168:171], v[68:71]\n\t" \
  "ds_read_b128 v[148:151], v230 offset:13312\n\t" \
  "v_mov_b32 v19, 0\n\t" \
  "v_mfma_f32_16x16x32_bf16 v[68:71], a[184:187], v[172:175], v[68:71]\n\t" \
  "v_cndmask_b32 v236, 0, 1.0, vcc\n\t" \
  "v_mov_b32 v20, 0x3f803f80\n\t" \
  "v_mfma_f32_16x16x32_bf16 v[84:87], a[188:191], v[172:175], v[84:87]\n\t" \
  "ds_read_b128 v[152:155], v230 offset:14336\n\t" \
  "v_add_u32 v21, s76, v242\n\t" \
  "v_mfma_f32_16x16x32_bf16 v[84:87], a[180:183], v[168:171], v[84:87]\n\t" \
  "buffer_load_dwordx4 v242, s[16:19], s97 offen offset:2048 sc1 lds\n\t" \
  "v_mfma_f32_16x16x32_bf16 v[84:87], a[172:175], v[164:167], v[84:87]\n\t" \
  "s_waitcnt vmcnt(16) lgkmcnt(3)\n\t" \
  "ds_write_b128 v21, v[16:19] offset:0\n\t" \
  "v_mfma_f32_16x16x32_bf16 v[84:87], a[164:167], v[160:163], v[84:87]\n\t" \
  "ds_write_b128 v21, v[16:19] offset:1024\n\t" \
  "s_barrier\n\t" \
  "v_mfma_f32_16x16x32_bf16 v[72:75], a[160:163], v[128:131], v[72:75]\n\t" \
  "buffer_load_dwordx4 v242, s[16:19], s97 offen offset:3072 sc1 lds\n\t" \
  "v_mfma_f32_16x16x32_bf16 v[72:75], a[168:171], v[132:135], v[72:75]\n\t" \
  "ds_read_b128 v[156:159], v230 offset:15360\n\t" \
  "v_mfma_f32_16x16x32_bf16 v[72:75], a[176:179], v[136:139], v[72:75]\n\t" \
  "ds_read_b128 v[160:163], v230 offset:49152\n\t" \
  "v_mfma_f32_16x16x32_bf16 v[72:75], a[184:187], v[140:143], v[72:75]\n\t" \
  "ds_write_b128 v21, v[16:19] offset:1536\n\t" \
  "v_mfma_f32_16x16x32_bf16 v[88:91], a[188:191], v[140:143], v[88:91]\n\t" \
  "buffer_store_dwordx4 v[12:15], v242, s[16:19], s68 offen offset:3072 sc1\n\t" \
  "ds_read_b128 v[164:167], v230 offset:50176\n\t" \
  "ds_read_b128 v[168:171], v230 offset:51200\n\t" \
  "ds_read_b128 v[172:175], v230 offset:52224\n\t" \
  "v_mfma_f32_16x16x32_bf16 v[88:91], a[180:183], v[136:139], v[88:91]\n\t" \
  "s_mov_b64 exec, 0xffff\n\t" \
  "ds_write_b32 v237, v20 offset:960\n\t" \
  "s_mov_b64 exec, -1\n\t" \
  "v_mfma_f32_16x16x32_bf16 v[88:91], a[172:175], v[132:135], v[88:91]\n\t" \
  "v_mfma_f32_16x16x32_bf16 v[88:91], a[164:167], v[128:131], v[88:91]\n\t" \
  "s_waitcnt lgkmcnt(6)\n\t" \
  "v_mfma_f32_16x16x32_bf16 v[76:79], a[160:163], v[144:147], v[76:79]\n\t" \
  "ds_read_b128 v[128:131], v230 offset:53248\n\t" \
  "v_mfma_f32_16x16x32_bf16 v[76:79], a[168:171], v[148:151], v[76:79]\n\t" \
  "ds_read_b128 v[132:135], v230 offset:54272\n\t" \
  "v_mfma_f32_16x16x32_bf16 v[76:79], a[176:179], v[152:155], v[76:79]\n\t" \
  "s_add_u32 m0, s38, 0\n\t" \
  "ds_read_b128 v[136:139], v230 offset:55296\n\t" \
  "s_add_u32 s96, s50, 0\n\t" \
  "v_mfma_f32_16x16x32_bf16 v[76:79], a[184:187], v[156:159], v[76:79]\n\t" \
  "ds_read_b128 v[140:143], v230 offset:56320\n\t" \
  "v_mfma_f32_16x16x32_bf16 v[92:95], a[188:191], v[156:159], v[92:95]\n\t" \
  "v_mfma_f32_16x16x32_bf16 v[92:95], a[180:183], v[152:155], v[92:95]\n\t" \
  "v_mfma_f32_16x16x32_bf16 v[92:95], a[172:175], v[148:151], v[92:95]\n\t" \
  "v_mfma_f32_16x16x32_bf16 v[92:95], a[164:167], v[144:147], v[92:95]\n\t" \
  "s_waitcnt lgkmcnt(5)\n\t" \
  "v_mfma_f32_16x16x32_bf16 v[64:67], a[0:3], v[160:163], v[64:67]\n\t" \
  "buffer_load_dwordx4 v26, s[4:7], s96 offen offset:0 lds\n\t" \
  "v_mfma_f32_16x16x32_bf16 v[64:67], a[8:11], v[164:167], v[64:67]\n\t" \
  "s_add_u32 s96, s50, 7168\n\t" \
  "ds_read_b128 v[144:147], v230 offset:57344\n\t" \
  "v_mfma_f32_16x16x32_bf16 v[64:67], a[16:19], v[168:171], v[64:67]\n\t" \
  "ds_read_b128 v[148:151], v230 offset:58368\n\t" \
  "v_mfma_f32_16x16x32_bf16 v[64:67], a[24:27], v[172:175], v[64:67]\n\t" \
  "ds_read_b128 v[152:155], v230 offset:59392\n\t" \
  "v_mfma_f32_16x16x32_bf16 v[80:83], a[28:31], v[172:175], v[80:83]\n\t" \
  "ds_read_b128 v[156:159], v230 offset:60416\n\t" \
  "v_mfma_f32_16x16x32_bf16 v[80:83], a[20:23], v[168:171], v[80:83]\n\t" \
  "buffer_load_dwordx4 v27, s[4:7], s96 offen offset:1024 lds\n\t" \
  "v_mfma_f32_16x16x32_bf16 v[80:83], a[12:15], v[164:167], v[80:83]\n\t" \
  "s_add_u32 s96, s50, 14336\n\t" \
  "v_mfma_f32_16x16x32_bf16 v[80:83], a[4:7], v[160:163], v[80:83]\n\t" \
  "s_waitcnt lgkmcnt(4)\n\t" \
  "v_mfma_f32_16x16x32_bf16 v[68:71], a[0:3], v[128:131], v[68:71]\n\t" \
  "ds_read_b128 v[160:163], v230 offset:61440\n\t" \
  "v_mfma_f32_16x16x32_bf16 v[68:71], a[8:11], v[132:135], v[68:71]\n\t" \
  "ds_read_b128 v[164:167], v230 offset:62464\n\t" \
  "v_mfma_f32_16x16x32_bf16 v[68:71], a[16:19], v[136:139], v[68:71]\n\t" \
  "buffer_load_dwordx4 v28, s[4:7], s96 offen offset:2048 lds\n\t" \
  "v_mfma_f32_16x16x32_bf16 v[68:71], a[24:27], v[140:143], v[68:71]\n\t" \
  "s_add_u32 s96, s50, 21504\n\t" \
  "ds_read_b128 v[168:171], v230 offset:63488\n\t" \
  "v_mfma_f32_16x16x32_bf16 v[84:87], a[28:31], v[140:143], v[84:87]\n\t" \
  "ds_read_b128 v[172:175], v230 offset:64512\n\t" \
  "v_mfma_f32_16x16x32_bf16 v[84:87], a[20:23], v[136:139], v[84:87]\n\t" \
  "v_mfma_f32_16x16x32_bf16 v[84:87], a[12:15], v[132:135], v[84:87]\n\t" \
  "v_mfma_f32_16x16x32_bf16 v[84:87], a[4:7], v[128:131], v[84:87]\n\t" \
  "s_waitcnt lgkmcnt(4)\n\t" \
  "buffer_load_dwordx4 v29, s[4:7], s96 offen offset:3072 lds\n\t" \
  "v_mfma_f32_16x16x32_bf16 v[72:75], a[0:3], v[144:147], v[72:75]\n\t" \
  "s_mov_b32 s40, 0x05040100\n\t" \
  "ds_read_b128 v[128:131], v230 offset:16384\n\t" \
  "s_mov_b32 s41, 0x07060302\n\t" \
  "v_mfma_f32_16x16x32_bf16 v[72:75], a[8:11], v[148:151], v[72:75]\n\t" \
  "s_load_dwordx4 s[4:7], s[0:1], 0xc0\n\t" \
  "ds_read_b128 v[132:135], v230 offset:17408\n\t" \
  "s_and_b32 s42, s2, 4\n\t" \
  "v_mfma_f32_16x16x32_bf16 v[72:75], a[16:19], v[152:155], v[72:75]\n\t" \
  "s_add_u32 s97, s64, 917504\n\t" \
  "s_and_b32 s97, s97, 0xe0000\n\t" \
  "s_add_u32 s97, s97, s62\n\t" \
  "v_mfma_f32_16x16x32_bf16 v[72:75], a[24:27], v[156:159], v[72:75]\n\t" \
  "s_add_u32 m0, s38, 16384\n\t" \
  "ds_read_b128 v[136:139], v230 offset:18432\n\t" \
  "s_lshl_b32 s9, s42, 11\n\t" \
  "v_mfma_f32_16x16x32_bf16 v[88:91], a[28:31], v[156:159], v[88:91]\n\t" \
  "buffer_load_dwordx4 v242, s[16:19], s97 offen offset:0 sc1 lds\n\t" \
  "v_mfma_f32_16x16x32_bf16 v[88:91], a[20:23], v[152:155], v[88:91]\n\t" \
  "s_xor_b32 s9, s9, s55\n\t" \
  "ds_read_b128 v[140:143], v230 offset:19456\n\t" \
  "s_lshr_b32 s43, s2, 6\n\t" \
  "v_mfma_f32_16x16x32_bf16 v[88:91], a[12:15], v[148:151], v[88:91]\n\t" \
  "s_add_u32 s42, s42, s43\n\t" \
  "s_and_b32 s43, s2, 3\n\t" \
  "s_lshl2_add_u32 s8, s42, s43\n\t" \
  "v_mfma_f32_16x16x32_bf16 v[88:91], a[4:7], v[144:147], v[88:91]\n\t" \
  "s_waitcnt lgkmcnt(4)\n\t" \
  "s_and_b32 s11, s8, 15\n\t" \
  "s_lshl_b32 s11, s11, 15\n\t" \
  "v_mfma_f32_16x16x32_bf16 v[76:79], a[0:3], v[160:163], v[76:79]\n\t" \
  "buffer_load_dwordx4 v242, s[16:19], s97 offen offset:1024 sc1 lds\n\t" \
  "v_mfma_f32_16x16x32_bf16 v[76:79], a[8:11], v[164:167], v[76:79]\n\t" \
  "s_add_u32 s11, s11, s9\n\t" \
  "ds_read_b128 v[144:147], v230 offset:20480\n\t" \
  "s_add_u32 s11, s11, 524288\n\t" \
  "v_mfma_f32_16x16x32_bf16 v[76:79], a[16:19], v[168:171], v[76:79]\n\t" \
  "s_waitcnt vmcnt(10)\n\t" \
  "v_xor_b32 v246, v243, v209\n\t" \
  "v_bitop3_b32 v246, v246, v211, v243 bitop3:0xf6\n\t" \
  "v_mfma_f32_16x16x32_bf16 v[76:79], a[24:27], v[172:175], v[76:79]\n\t" \
  "s_and_b32 s42, s2, 0x20\n\t" \
  "v_bitop3_b32 v246, v246, v213, v243 bitop3:0xf6\n\t" \
  "v_bitop3_b32 v246, v246, v215, v243 bitop3:0xf6\n\t" \
  "v_mfma_f32_16x16x32_bf16 v[92:95], a[28:31], v[172:175], v[92:95]\n\t" \
  "s_lshl_b32 s42, s42, 9\n\t" \
  "v_bitop3_b32 v246, v246, v217, v243 bitop3:0xf6\n\t" \
  "v_bitop3_b32 v246, v246, v219, v243 bitop3:0xf6\n\t" \
  "v_mfma_f32_16x16x32_bf16 v[92:95], a[20:23], v[168:171], v[92:95]\n\t" \
  "s_add_u32 s10, s11, s42\n\t" \
  "v_bitop3_b32 v246, v246, v221, v243 bitop3:0xf6\n\t" \
  "v_bitop3_b32 v246, v246, v223, v243 bitop3:0xf6\n\t" \
  "v_mfma_f32_16x16x32_bf16 v[92:95], a[12:15], v[164:167], v[92:95]\n\t" \
  "buffer_load_dwordx4 v242, s[16:19], s97 offen offset:2048 sc1 lds\n\t" \
  "v_mfma_f32_16x16x32_bf16 v[92:95], a[4:7], v[160:163], v[92:95]\n\t" \
  "s_waitcnt lgkmcnt(1)\n\t" \
  "ds_read_b128 v[148:151], v230 offset:21504\n\t" \
  "v_cmp_ne_u32 vcc, 0, v246\n\t" \
  "v_mfma_f32_16x16x32_bf16 v[64:67], a[32:35], v[128:131], v[64:67]\n\t" \
  "buffer_load_dwordx4 v242, s[16:19], s97 offen offset:3072 sc1 lds\n\t" \
  "ds_read_b128 v[152:155], v230 offset:22528\n\t" \
  "s_and_b32 s42, s2, 0x18\n\t" \
  "v_mfma_f32_16x16x32_bf16 v[64:67], a[40:43], v[132:135], v[64:67]\n\t" \
  "s_add_u32 s97, s64, 131072\n\t" \
  "s_and_b32 s97, s97, 0xe0000\n\t" \
  "s_add_u32 s97, s97, s63\n\t" \
  "v_mfma_f32_16x16x32_bf16 v[64:67], a[48:51], v[136:139], v[64:67]\n\t" \
  "s_cbranch_vccnz .Lat_s3\n\t" \
  ".Lat_k3:\n\t" \
  "ds_read_b128 v[156:159], v230 offset:23552\n\t" \
  "s_add_u32 m0, s38, 32768\n\t" \
  "v_mfma_f32_16x16x32_bf16 v[64:67], a[56:59], v[140:143], v[64:67]\n\t" \
  "buffer_load_dwordx4 v242, s[16:19], s97 offen offset:0 sc1 lds\n\t" \
  "v_mfma_f32_16x16x32_bf16 v[80:83], a[60:63], v[140:143], v[80:83]\n\t" \
  "s_lshl_b32 s42, s42, 8\n\t" \
  "ds_read_b128 v[160:163], v230 offset:24576\n\t" \
  "v_add_f32 v208, v208, v212\n\t" \
  "v_mfma_f32_16x16x32_bf16 v[80:83], a[52:55], v[136:139], v[80:83]\n\t" \
  "s_add_u32 s10, s10, s42\n\t" \
  "ds_read_b128 v[164:167], v230 offset:25600\n\t" \
  "v_add_f32 v216, v216, v220\n\t" \
  "v_mfma_f32_16x16x32_bf16 v[80:83], a[44:47], v[132:135], v[80:83]\n\t" \
  "s_mul_i32 s42, s3, 0x3000\n\t" \
  "ds_read_b128 v[168:171], v230 offset:26624\n\t" \
  "v_add_f32 v210, v210, v214\n\t" \
  "v_mfma_f32_16x16x32_bf16 v[80:83], a[36:39], v[128:131], v[80:83]\n\t" \
  "s_waitcnt lgkmcnt(3)\n\t" \
  "ds_read_b128 v[172:175], v230 offset:27648\n\t" \
  "v_add_f32 v218, v218, v222\n\t" \
  "v_mfma_f32_16x16x32_bf16 v[68:71], a[32:35], v[144:147], v[68:71]\n\t" \
  "buffer_load_dwordx4 v242, s[16:19], s97 offen offset:1024 sc1 lds\n\t" \
  "v_mfma_f32_16x16x32_bf16 v[68:71], a[40:43], v[148:151], v[68:71]\n\t" \
  "s_and_b32 s42, s42, 0x1000\n\t" \
  "ds_read_b128 v[128:131], v230 offset:28672\n\t" \
  "v_add_f32 v208, v208, v216\n\t" \
  "v_mfma_f32_16x16x32_bf16 v[68:71], a[48:51], v[152:155], v[68:71]\n\t" \
  "s_add_u32 s11, s11, s42\n\t" \
  "ds_read_b128 v[132:135], v230 offset:29696\n\t" \
  "v_add_f32 v210, v210, v218\n\t" \
  "v_mfma_f32_16x16x32_bf16 v[68:71], a[56:59], v[156:159], v[68:71]\n\t" \
  "s_and_b32 s42, s3, 2\n\t" \
  "v_lshlrev_b32 v19, 16, v224\n\t" \
  "v_and_b32 v20, 0xffff0000, v224\n\t" \
  "v_mfma_f32_16x16x32_bf16 v[84:87], a[60:63], v[156:159], v[84:87]\n\t" \
  "s_lshl_b32 s42, s42, 13\n\t" \
  "ds_read_b128 v[136:139], v230 offset:30720\n\t" \
  "v_cvt_pk_bf16_f32 v18, v208, v210\n\t" \
  "v_mfma_f32_16x16x32_bf16 v[84:87], a[52:55], v[152:155], v[84:87]\n\t" \
  "s_add_u32 s11, s11, s42\n\t" \
  "v_lshlrev_b32 v16, 16, v18\n\t" \
  "v_and_b32 v17, 0xffff0000, v18\n\t" \
  "v_mfma_f32_16x16x32_bf16 v[84:87], a[44:47], v[148:151], v[84:87]\n\t" \
  "s_waitcnt vmcnt(11) lgkmcnt(3)\n\t" \
  "ds_read_b128 v[140:143], v230 offset:31744\n\t" \
  "v_add_f32 v16, v16, v236\n\t" \
  "v_mfma_f32_16x16x32_bf16 v[84:87], a[36:39], v[144:147], v[84:87]\n\t" \
  "s_barrier\n\t" \
  "v_mfma_f32_16x16x32_bf16 v[72:75], a[32:35], v[160:163], v[72:75]\n\t" \
  "buffer_load_dwordx4 v242, s[16:19], s97 offen offset:2048 sc1 lds\n\t" \
  "v_mfma_f32_16x16x32_bf16 v[72:75], a[40:43], v[164:167], v[72:75]\n\t" \
  "s_add_u32 s12, s11, 2048\n\t" \
  "ds_read_b128 v[144:147], v232 offset:0\n\t" \
  "v_add_f32 v17, v17, v236\n\t" \
  "v_mfma_f32_16x16x32_bf16 v[72:75], a[48:51], v[168:171], v[72:75]\n\t" \
  "s_sub_u32 s39, s10, s11\n\t" \
  "ds_read_b128 v[148:151], v232 offset:1024\n\t" \
  "v_cvt_pk_bf16_f32 v18, v16, v17\n\t" \
  "v_mfma_f32_16x16x32_bf16 v[72:75], a[56:59], v[172:175], v[72:75]\n\t" \
  "v_lshlrev_b32 v16, 16, v18\n\t" \
  "v_and_b32 v17, 0xffff0000, v18\n\t" \
  "v_mfma_f32_16x16x32_bf16 v[88:91], a[60:63], v[172:175], v[88:91]\n\t" \
  "ds_read_b128 v[152:155], v232 offset:2048\n\t" \
  "v_mfma_f32_16x16x32_bf16 v[88:91], a[52:55], v[168:171], v[88:91]\n\t" \
  "s_waitcnt vmcnt(11)\n\t" \
  "buffer_store_dword v243, v244, s[16:19], s72 offen offset:16 sc1\n\t" \
  "ds_read_b128 v[156:159], v232 offset:3072\n\t" \
  "v_mfma_f32_16x16x32_bf16 v[88:91], a[44:47], v[164:167], v[88:91]\n\t" \
  "ds_write_b64 v240, v[16:17] offset:2560\n\t" \
  "v_mfma_f32_16x16x32_bf16 v[88:91], a[36:39], v[160:163], v[88:91]\n\t" \
  "s_waitcnt lgkmcnt(5)\n\t" \
  "v_mul_f32 v22, v16, v19\n\t" \
  "v_mul_f32 v23, v17, v20\n\t" \
  "v_mfma_f32_16x16x32_bf16 v[76:79], a[32:35], v[128:131], v[76:79]\n\t" \
  "ds_read_b128 v[160:163], v232 offset:4096\n\t" \
  "v_cvt_pk_bf16_f32 v24, v22, v23\n\t" \
  "v_mfma_f32_16x16x32_bf16 v[76:79], a[40:43], v[132:135], v[76:79]\n\t" \
  "v_add_f32 v18, v22, v23\n\t" \
  "v_lshlrev_b32 v19, 16, v24\n\t" \
  "v_mfma_f32_16x16x32_bf16 v[76:79], a[48:51], v[136:139], v[76:79]\n\t" \
  "ds_read_b128 v[164:167], v232 offset:5120\n\t" \
  "v_and_b32 v20, 0xffff0000, v24\n\t" \
  "v_mfma_f32_16x16x32_bf16 v[76:79], a[56:59], v[140:143], v[76:79]\n\t" \
  "v_add_f32_dpp v18, v18, v18 row_ror:8 row_mask:0xf bank_mask:0xf\n\t" \
  "v_sub_f32 v22, v22, v19\n\t" \
  "v_mfma_f32_16x16x32_bf16 v[92:95], a[60:63], v[140:143], v[92:95]\n\t" \
  "ds_read_b128 v[168:171], v232 offset:6144\n\t" \
  "v_sub_f32 v23, v23, v20\n\t" \
  "v_mfma_f32_16x16x32_bf16 v[92:95], a[52:55], v[136:139], v[92:95]\n\t" \
  "v_add_f32_dpp v18, v18, v18 row_ror:4 row_mask:0xf bank_mask:0xf\n\t" \
  "v_cvt_pk_bf16_f32 v25, v22, v23\n\t" \
  "v_mfma_f32_16x16x32_bf16 v[92:95], a[44:47], v[132:135], v[92:95]\n\t" \
  "s_mov_b32 s84, 0\n\t" \
  "ds_read_b128 v[172:175], v232 offset:7168\n\t" \
  "v_add_f32_dpp v18, v18, v18 row_ror:2 row_mask:0xf bank_mask:0xf\n\t" \
  "v_mfma_f32_16x16x32_bf16 v[92:95], a[36:39], v[128:131], v[92:95]\n\t" \
  "s_waitcnt lgkmcnt(5)\n\t" \
  "buffer_load_dwordx4 v242, s[16:19], s97 offen offset:3072 sc1 lds\n\t" \
  "v_mfma_f32_16x16x32_bf16 v[64:67], a[64:67], v[144:147], v[64:67]\n\t" \
  "s_mov_b32 s85, 0xffff\n\t" \
  "ds_read_b128 v[128:131], v232 offset:8192\n\t" \
  "v_add_f32_dpp v18, v18, v18 row_ror:1 row_mask:0xf bank_mask:0xf\n\t" \
  "v_mfma_f32_16x16x32_bf16 v[64:67], a[72:75], v[148:151], v[64:67]\n\t" \
  "s_mov_b64 exec, s[84:85]\n\t" \
  "ds_write_b32 v237, v24 offset:1040\n\t" \
  "ds_write_b32 v237, v25 offset:1120\n\t" \
  "s_mov_b64 exec, -1\n\t" \
  "v_mfma_f32_16x16x32_bf16 v[64:67], a[80:83], v[152:155], v[64:67]\n\t" \
  "s_add_u32 s97, s64, 262144\n\t" \
  "s_and_b32 s97, s97, 0xe0000\n\t" \
  "s_add_u32 s97, s97, s63\n\t" \
  "v_mfma_f32_16x16x32_bf16 v[64:67], a[88:91], v[156:159], v[64:67]\n\t" \
  "s_add_u32 m0, s38, 65536\n\t" \
  "ds_read_b128 v[132:135], v232 offset:9216\n\t" \
  "v_readlane_b32 s82, v18, 32\n\t" \
  "v_mfma_f32_16x16x32_bf16 v[80:83], a[92:95], v[156:159], v[80:83]\n\t" \
  "buffer_load_dwordx4 v242, s[16:19], s97 offen offset:0 sc1 lds\n\t" \
  "v_mfma_f32_16x16x32_bf16 v[80:83], a[84:87], v[152:155], v[80:83]\n\t" \
  "ds_read_b128 v[136:139], v232 offset:10240\n\t" \
  "v_readlane_b32 s83, v18, 16\n\t" \
  "v_mfma_f32_16x16x32_bf16 v[80:83], a[76:79], v[148:151], v[80:83]\n\t" \
  "ds_read_b128 v[140:143], v232 offset:11264\n\t" \
  "v_and_b32 v235, 16, v242\n\t" \
  "v_mfma_f32_16x16x32_bf16 v[80:83], a[68:71], v[144:147], v[80:83]\n\t" \
  "s_waitcnt lgkmcnt(6)\n\t" \
  "s_cmp_eq_u32 s39, 0\n\t" \
  "s_cselect_b32 s11, s12, s11\n\t" \
  "v_mfma_f32_16x16x32_bf16 v[68:71], a[64:67], v[160:163], v[68:71]\n\t" \
  "ds_read_b128 v[144:147], v232 offset:12288\n\t" \
  "v_mfma_f32_16x16x32_bf16 v[68:71], a[72:75], v[164:167], v[68:71]\n\t" \
  "buffer_load_dwordx4 v242, s[16:19], s97 offen offset:1024 sc1 lds\n\t" \
  "v_mfma_f32_16x16x32_bf16 v[68:71], a[80:83], v[168:171], v[68:71]\n\t" \
  "ds_read_b128 v[148:151], v232 offset:13312\n\t" \
  "v_mfma_f32_16x16x32_bf16 v[68:71], a[88:91], v[172:175], v[68:71]\n\t" \
  "s_cmp_eq_u32 s39, 2048\n\t" \
  "s_cselect_b32 s12, s11, s12\n\t" \
  "s_lshl_b32 s42, s3, 10\n\t" \
  "v_mfma_f32_16x16x32_bf16 v[84:87], a[92:95], v[172:175], v[84:87]\n\t" \
  "buffer_load_dwordx4 v242, s[16:19], s97 offen offset:2048 sc1 lds\n\t" \
  "v_mfma_f32_16x16x32_bf16 v[84:87], a[84:87], v[168:171], v[84:87]\n\t" \
  "s_add_u32 s10, s10, s42\n\t" \
  "ds_read_b128 v[152:155], v232 offset:14336\n\t" \
  "s_lshr_b32 s42, s2, 1\n\t" \
  "v_mfma_f32_16x16x32_bf16 v[84:87], a[76:79], v[164:167], v[84:87]\n\t" \
  "s_and_b32 s42, s42, 12\n\t" \
  "ds_read_b128 v[156:159], v232 offset:15360\n\t" \
  "s_or_b32 s42, s42, s43\n\t" \
  "v_mfma_f32_16x16x32_bf16 v[84:87], a[68:71], v[160:163], v[84:87]\n\t" \
  "s_waitcnt lgkmcnt(4)\n\t" \
  "buffer_load_dwordx4 v242, s[16:19], s97 offen offset:3072 sc1 lds\n\t" \
  "v_mfma_f32_16x16x32_bf16 v[72:75], a[64:67], v[128:131], v[72:75]\n\t" \
  "s_add_u32 s97, s64, 393216\n\t" \
  "s_and_b32 s97, s97, 0xe0000\n\t" \
  "s_add_u32 s97, s97, s63\n\t" \
  "v_mfma_f32_16x16x32_bf16 v[72:75], a[72:75], v[132:135], v[72:75]\n\t" \
  "s_add_u32 m0, s38, 98304\n\t" \
  "ds_read_b128 v[160:163], v242 offset:49152\n\t" \
  "s_lshl_b32 s13, s42, 8\n\t" \
  "v_mfma_f32_16x16x32_bf16 v[72:75], a[80:83], v[136:139], v[72:75]\n\t" \
  "buffer_load_dwordx4 v242, s[16:19], s97 offen offset:0 sc1 lds\n\t" \
  "v_mfma_f32_16x16x32_bf16 v[72:75], a[88:91], v[140:143], v[72:75]\n\t" \
  "s_add_u32 s13, s13, s9\n\t" \
  "ds_read_b128 v[164:167], v242 offset:50176\n\t" \
  "s_add_u32 s13, s13, 65536\n\t" \
  "v_mfma_f32_16x16x32_bf16 v[88:91], a[92:95], v[140:143], v[88:91]\n\t" \
  "s_bfe_u32 s44, s2, 0x30003\n\t" \
  "ds_read_b128 v[168:171], v242 offset:51200\n\t" \
  "s_lshl_b32 s42, s3, 13\n\t" \
  "v_mfma_f32_16x16x32_bf16 v[88:91], a[84:87], v[136:139], v[88:91]\n\t" \
  "buffer_load_dwordx4 v242, s[16:19], s97 offen offset:1024 sc1 lds\n\t" \
  "v_mfma_f32_16x16x32_bf16 v[88:91], a[76:79], v[132:135], v[88:91]\n\t" \
  "s_and_b32 s42, s42, 0x2000\n\t" \
  "ds_read_b128 v[172:175], v242 offset:52224\n\t" \
  "s_xor_b32 s14, s42, s55\n\t" \
  "v_mfma_f32_16x16x32_bf16 v[88:91], a[68:71], v[128:131], v[88:91]\n\t" \
  "s_waitcnt lgkmcnt(4)\n\t" \
  "s_lshr_b32 s42, s3, 1\n\t" \
  "s_lshl1_add_u32 s42, s44, s42\n\t" \
  "v_mfma_f32_16x16x32_bf16 v[76:79], a[64:67], v[144:147], v[76:79]\n\t" \
  "s_lshl_b32 s42, s42, 8\n\t" \
  "ds_read_b128 v[128:131], v242 offset:53248\n\t" \
  "s_add_u32 s14, s14, s42\n\t" \
  "v_mfma_f32_16x16x32_bf16 v[76:79], a[72:75], v[148:151], v[76:79]\n\t" \
  "s_lshl3_add_u32 s14, s8, s14\n\t" \
  "ds_read_b128 v[132:135], v242 offset:54272\n\t" \
  "s_add_u32 s14, s14, 65536\n\t" \
  "v_mfma_f32_16x16x32_bf16 v[76:79], a[80:83], v[152:155], v[76:79]\n\t" \
  "s_add_u32 s15, s9, 262144\n\t" \
  "ds_read_b128 v[136:139], v242 offset:55296\n\t" \
  "s_lshl_b32 s42, s8, 13\n\t" \
  "v_mfma_f32_16x16x32_bf16 v[76:79], a[88:91], v[156:159], v[76:79]\n\t" \
  "s_and_b32 s42, s42, 0x3c000\n\t" \
  "ds_read_b128 v[140:143], v242 offset:56320\n\t" \
  "s_add_u32 s20, s15, s42\n\t" \
  "v_mfma_f32_16x16x32_bf16 v[92:95], a[92:95], v[156:159], v[92:95]\n\t" \
  "buffer_load_dwordx4 v242, s[16:19], s97 offen offset:2048 sc1 lds\n\t" \
  "v_mfma_f32_16x16x32_bf16 v[92:95], a[84:87], v[152:155], v[92:95]\n\t" \
  ".Lhs14:\n\ts_cbranch_execz .Lhs14n\n\t" \
  "s_and_b32 s42, s8, 1\n\t" \
  "s_lshl_b32 s42, s42, 12\n\t" \
  "s_add_u32 s20, s20, s42\n\t" \
  "v_mfma_f32_16x16x32_bf16 v[92:95], a[76:79], v[148:151], v[92:95]\n\t" \
  "s_lshl2_add_u32 s42, s44, s3\n\t" \
  "s_lshl_b32 s42, s42, 7\n\t" \
  "s_add_u32 s20, s20, s42\n\t" \
  "v_mfma_f32_16x16x32_bf16 v[92:95], a[68:71], v[144:147], v[92:95]\n\t" \
  "s_waitcnt lgkmcnt(4)\n\t" \
  "s_and_b32 s21, s42, 0x7ff\n\t" \
  "s_lshl_b32 s22, s8, 17\n\t" \
  "v_mfma_f32_16x16x32_bf16 v[64:67], a[96:99], v[160:163], v[64:67]\n\t" \
  "s_lshr_b32 s42, s42, 1\n\t" \
  "ds_read_b128 v[144:147], v242 offset:57344\n\t" \
  "s_add_u32 s22, s22, s42\n\t" \
  "v_mfma_f32_16x16x32_bf16 v[64:67], a[104:107], v[164:167], v[64:67]\n\t" \
  "buffer_load_dwordx4 v242, s[16:19], s97 offen offset:3072 sc1 lds\n\t" \
  "v_mfma_f32_16x16x32_bf16 v[64:67], a[112:115], v[168:171], v[64:67]\n\t" \
  "s_and_b32 s42, s2, 0x20\n\t" \
  "ds_read_b128 v[148:151], v242 offset:58368\n\t" \
  "s_lshl_b32 s42, s42, 8\n\t" \
  "v_mfma_f32_16x16x32_bf16 v[64:67], a[120:123], v[172:175], v[64:67]\n\t" \
  "s_xor_b32 s42, s42, s55\n\t" \
  "ds_read_b128 v[152:155], v242 offset:59392\n\t" \
  "s_add_u32 s21, s21, s42\n\t" \
  "v_mfma_f32_16x16x32_bf16 v[80:83], a[124:127], v[172:175], v[80:83]\n\t" \
  "s_add_u32 s97, s64, 524288\n\t" \
  "s_and_b32 s97, s97, 0xe0000\n\t" \
  "s_add_u32 s97, s97, s63\n\t" \
  "v_mfma_f32_16x16x32_bf16 v[80:83], a[116:119], v[168:171], v[80:83]\n\t" \
  "s_add_u32 m0, s38, 114688\n\t" \
  "ds_read_b128 v[156:159], v242 offset:60416\n\t" \
  "s_lshl_b32 s42, s43, 11\n\t" \
  "v_mfma_f32_16x16x32_bf16 v[80:83], a[108:111], v[164:167], v[80:83]\n\t" \
  "buffer_load_dwordx4 v242, s[16:19], s97 offen offset:0 sc1 lds\n\t" \
  "v_mfma_f32_16x16x32_bf16 v[80:83], a[100:103], v[160:163], v[80:83]\n\t" \
  "s_waitcnt lgkmcnt(4)\n\t" \
  "s_add_u32 s21, s21, s42\n\t" \
  "s_lshr_b32 s42, s8, 2\n\t" \
  "v_mfma_f32_16x16x32_bf16 v[68:71], a[96:99], v[128:131], v[68:71]\n\t" \
  "s_lshl_b32 s42, s42, 14\n\t" \
  "ds_read_b128 v[160:163], v242 offset:61440\n\t" \
  "s_add_u32 s21, s21, s42\n\t" \
  "v_mfma_f32_16x16x32_bf16 v[68:71], a[104:107], v[132:135], v[68:71]\n\t" \
  "buffer_load_dwordx4 v242, s[16:19], s97 offen offset:1024 sc1 lds\n\t" \
  "v_mfma_f32_16x16x32_bf16 v[68:71], a[112:115], v[136:139], v[68:71]\n\t" \
  "s_add_u32 s21, s21, 1966080\n\t" \
  "ds_read_b128 v[164:167], v242 offset:62464\n\t" \
  "s_lshl_b32 s42, s59, 7\n\t" \
  "v_mfma_f32_16x16x32_bf16 v[68:71], a[120:123], v[140:143], v[68:71]\n\t" \
  "s_and_b32 s23, s42, 0xffffc000\n\t" \
  "ds_read_b128 v[168:171], v242 offset:63488\n\t" \
  "s_lshl_b32 s42, s59, 6\n\t" \
  "v_mfma_f32_16x16x32_bf16 v[84:87], a[124:127], v[140:143], v[84:87]\n\t" \
  "s_and_b32 s42, s42, 0x1800\n\t" \
  "ds_read_b128 v[172:175], v242 offset:64512\n\t" \
  "s_add_u32 s23, s23, s42\n\t" \
  "v_mfma_f32_16x16x32_bf16 v[84:87], a[116:119], v[136:139], v[84:87]\n\t" \
  "s_mul_i32 s42, s3, 0x1400\n\t" \
  "s_and_b32 s42, s42, 0x2400\n\t" \
  "s_xor_b32 s42, s42, s55\n\t" \
  "v_mfma_f32_16x16x32_bf16 v[84:87], a[108:111], v[132:135], v[84:87]\n\t" \
  "s_waitcnt vmcnt(10) lgkmcnt(4)\n\t" \
  "buffer_load_dwordx4 v242, s[16:19], s97 offen offset:2048 sc1 lds\n\t" \
  "v_mfma_f32_16x16x32_bf16 v[84:87], a[100:103], v[128:131], v[84:87]\n\t" \
  "s_barrier\n\t" \
  "v_mfma_f32_16x16x32_bf16 v[72:75], a[96:99], v[144:147], v[72:75]\n\t" \
  "s_add_u32 s23, s23, s42\n\t" \
  "ds_read_b128 v[128:131], v242 offset:16384\n\t" \
  "s_add_u32 s23, s23, 1048576\n\t" \
  "v_mfma_f32_16x16x32_bf16 v[72:75], a[104:107], v[148:151], v[72:75]\n\t" \
  "buffer_load_dwordx4 v242, s[16:19], s97 offen offset:3072 sc1 lds\n\t" \
  "v_mfma_f32_16x16x32_bf16 v[72:75], a[112:115], v[152:155], v[72:75]\n\t" \
  "s_add_u32 s97, s64, 655360\n\t" \
  "s_and_b32 s97, s97, 0xe0000\n\t" \
  "s_add_u32 s97, s97, s63\n\t" \
  "v_mfma_f32_16x16x32_bf16 v[72:75], a[120:123], v[156:159], v[72:75]\n\t" \
  "s_add_u32 m0, s38, 81920\n\t" \
  "ds_read_b128 v[132:135], v242 offset:17408\n\t" \
  "v_mfma_f32_16x16x32_bf16 v[88:91], a[124:127], v[156:159], v[88:91]\n\t" \
  "buffer_load_dwordx4 v242, s[16:19], s97 offen offset:0 sc1 lds\n\t" \
  "v_mfma_f32_16x16x32_bf16 v[88:91], a[116:119], v[152:155], v[88:91]\n\t" \
  "ds_read_b128 v[136:139], v242 offset:18432\n\t" \
  "v_mfma_f32_16x16x32_bf16 v[88:91], a[108:111], v[148:151], v[88:91]\n\t" \
  "ds_read_b128 v[140:143], v242 offset:19456\n\t" \
  "v_mfma_f32_16x16x32_bf16 v[88:91], a[100:103], v[144:147], v[88:91]\n\t" \
  "s_waitcnt lgkmcnt(4)\n\t" \
  "buffer_load_dwordx4 v242, s[16:19], s97 offen offset:1024 sc1 lds\n\t" \
  "v_mfma_f32_16x16x32_bf16 v[76:79], a[96:99], v[160:163], v[76:79]\n\t" \
  "ds_read_b128 v[144:147], v242 offset:20480\n\t" \
  "v_mfma_f32_16x16x32_bf16 v[76:79], a[104:107], v[164:167], v[76:79]\n\t" \
  "ds_read_b128 v[148:151], v242 offset:21504\n\t" \
  "v_mfma_f32_16x16x32_bf16 v[76:79], a[112:115], v[168:171], v[76:79]\n\t" \
  "buffer_load_dwordx4 v242, s[16:19], s97 offen offset:2048 sc1 lds\n\t" \
  "v_mfma_f32_16x16x32_bf16 v[76:79], a[120:123], v[172:175], v[76:79]\n\t" \
  "ds_read_b128 v[152:155], v242 offset:22528\n\t" \
  "v_mfma_f32_16x16x32_bf16 v[92:95], a[124:127], v[172:175], v[92:95]\n\t" \
  "ds_read_b128 v[156:159], v242 offset:23552\n\t" \
  "v_mfma_f32_16x16x32_bf16 v[92:95], a[116:119], v[168:171], v[92:95]\n\t" \
  "buffer_load_dwordx4 v242, s[16:19], s97 offen offset:3072 sc1 lds\n\t" \
  "v_mfma_f32_16x16x32_bf16 v[92:95], a[108:111], v[164:167], v[92:95]\n\t" \
  "s_add_u32 s97, s64, 786432\n\t" \
  "s_and_b32 s97, s97, 0xe0000\n\t" \
  "s_add_u32 s97, s97, s63\n\t" \
  "v_mfma_f32_16x16x32_bf16 v[92:95], a[100:103], v[160:163], v[92:95]\n\t" \
  "s_waitcnt lgkmcnt(4)\n\t" \
  "s_add_u32 m0, s38, 131072\n\t" \
  "v_mfma_f32_16x16x32_bf16 v[64:67], a[192:195], v[128:131], v[64:67]\n\t" \
  "buffer_load_dwordx4 v242, s[16:19], s97 offen offset:0 sc1 lds\n\t" \
  "v_mfma_f32_16x16x32_bf16 v[64:67], a[200:203], v[132:135], v[64:67]\n\t" \
  "ds_read_b128 v[160:163], v242 offset:24576\n\t" \
  "v_mfma_f32_16x16x32_bf16 v[64:67], a[208:211], v[136:139], v[64:67]\n\t" \
  "ds_read_b128 v[164:167], v242 offset:25600\n\t" \
  "v_mfma_f32_16x16x32_bf16 v[64:67], a[216:219], v[140:143], v[64:67]\n\t" \
  "buffer_load_dwordx4 v242, s[16:19], s97 offen offset:1024 sc1 lds\n\t" \
  "v_mfma_f32_16x16x32_bf16 v[80:83], a[220:223], v[140:143], v[80:83]\n\t" \
  "ds_read_b128 v[168:171], v242 offset:26624\n\t" \
  "v_mfma_f32_16x16x32_bf16 v[80:83], a[212:215], v[136:139], v[80:83]\n\t" \
  "ds_read_b128 v[172:175], v242 offset:27648\n\t" \
  "v_mfma_f32_16x16x32_bf16 v[80:83], a[204:207], v[132:135], v[80:83]\n\t" \
  "buffer_load_dwordx4 v242, s[16:19], s97 offen offset:2048 sc1 lds\n\t" \
  "v_mfma_f32_16x16x32_bf16 v[80:83], a[196:199], v[128:131], v[80:83]\n\t" \
  "s_waitcnt lgkmcnt(4)\n\t" \
  "v_cvt_pk_bf16_f32 v0, v64, v65\n\t" \
  "v_cvt_pk_bf16_f32 v1, v66, v67\n\t" \
  "v_mfma_f32_16x16x32_bf16 v[68:71], a[192:195], v[144:147], v[68:71]\n\t" \
  "buffer_load_dword v246, v235, s[16:19], s73 offen sc1\n\t" \
  "v_mfma_f32_16x16x32_bf16 v[68:71], a[200:203], v[148:151], v[68:71]\n\t" \
  "buffer_load_dwordx4 v242, s[16:19], s97 offen offset:3072 sc1 lds\n\t" \
  "v_mfma_f32_16x16x32_bf16 v[68:71], a[208:211], v[152:155], v[68:71]\n\t" \
  "ds_read_b128 v[128:131], v242 offset:28672\n\t" \
  "v_cvt_pk_bf16_f32 v2, v80, v81\n\t" \
  "v_mfma_f32_16x16x32_bf16 v[68:71], a[216:219], v[156:159], v[68:71]\n\t" \
  "ds_read_b128 v[132:135], v242 offset:29696\n\t" \
  "v_cvt_pk_bf16_f32 v3, v82, v83\n\t" \
  "v_mfma_f32_16x16x32_bf16 v[84:87], a[220:223], v[156:159], v[84:87]\n\t" \
  "ds_read_b128 v[136:139], v242 offset:30720\n\t" \
  "v_mov_b32 v250, 0x3f80\n\t" \
  "v_mfma_f32_16x16x32_bf16 v[84:87], a[212:215], v[152:155], v[84:87]\n\t" \
  "ds_read_b128 v[140:143], v242 offset:31744\n\t" \
  "v_lshlrev_b32 v250, v235, v250\n\t" \
  "v_mfma_f32_16x16x32_bf16 v[84:87], a[204:207], v[148:151], v[84:87]\n\t" \
  "s_mov_b32 s84, 0x300003\n\t" \
  "s_mov_b32 s85, 0x30000300\n\t" \
  "s_mov_b32 s86, 0xc0000c\n\t" \
  "v_mfma_f32_16x16x32_bf16 v[84:87], a[196:199], v[144:147], v[84:87]\n\t" \
  "s_waitcnt lgkmcnt(4)\n\t" \
  "v_cvt_pk_bf16_f32 v4, v68, v69\n\t" \
  "v_cvt_pk_bf16_f32 v5, v70, v71\n\t" \
  "v_mfma_f32_16x16x32_bf16 v[72:75], a[192:195], v[160:163], v[72:75]\n\t" \
  "buffer_store_dwordx4 v[0:3], v242, s[16:19], s69 offen offset:0 sc1\n\t" \
  "ds_read_b128 v[144:147], v242 offset:32768\n\t" \
  "ds_read_b128 v[148:151], v242 offset:33792\n\t" \
  "ds_read_b128 v[152:155], v242 offset:34816\n\t" \
  "v_mfma_f32_16x16x32_bf16 v[72:75], a[200:203], v[164:167], v[72:75]\n\t" \
  "s_mov_b32 s87, 0xc0000c00\n\t" \
  "v_cvt_pk_bf16_f32 v6, v84, v85\n\t" \
  "v_cvt_pk_bf16_f32 v7, v86, v87\n\t" \
  "v_mfma_f32_16x16x32_bf16 v[72:75], a[208:211], v[168:171], v[72:75]\n\t" \
  "v_cndmask_b32 v248, 0, v250, s[84:85]\n\t" \
  "v_cndmask_b32 v249, 0, v250, s[86:87]\n\t" \
  "v_mfma_f32_16x16x32_bf16 v[72:75], a[216:219], v[172:175], v[72:75]\n\t" \
  "v_and_b32 v236, 15, v233\n\t" \
  "v_lshrrev_b32 v250, 4, v233\n\t" \
  "v_mfma_f32_16x16x32_bf16 v[88:91], a[220:223], v[172:175], v[88:91]\n\t" \
  "v_mul_u32_u24 v236, 80, v236\n\t" \
  "v_lshlrev_b32 v237, 4, v250\n\t" \
  "v_mfma_f32_16x16x32_bf16 v[88:91], a[212:215], v[168:171], v[88:91]\n\t" \
  "v_add_u32 v237, s76, v237\n\t" \
  "v_add_u32 v236, v237, v236\n\t" \
  "v_mfma_f32_16x16x32_bf16 v[88:91], a[204:207], v[164:167], v[88:91]\n\t" \
  "s_add_u32 s78, s70, 4096\n\t" \
  "ds_read_b128 v[156:159], v242 offset:35840\n\t" \
  "s_add_u32 s79, s70, 16384\n\t" \
  "v_mfma_f32_16x16x32_bf16 v[88:91], a[196:199], v[160:163], v[88:91]\n\t" \
  "s_waitcnt lgkmcnt(4)\n\t" \
  "v_cvt_pk_bf16_f32 v8, v72, v73\n\t" \
  "v_cvt_pk_bf16_f32 v9, v74, v75\n\t" \
  "v_mfma_f32_16x16x32_bf16 v[76:79], a[192:195], v[128:131], v[76:79]\n\t" \
  "buffer_store_dwordx4 v[4:7], v242, s[16:19], s69 offen offset:1024 sc1\n\t" \
  "ds_read_b128 v[160:163], v242 offset:36864\n\t" \
  "ds_read_b128 v[164:167], v242 offset:37888\n\t" \
  "ds_read_b128 v[168:171], v242 offset:38912\n\t" \
  "v_mfma_f32_16x16x32_bf16 v[76:79], a[200:203], v[132:135], v[76:79]\n\t" \
  "v_cvt_pk_bf16_f32 v10, v88, v89\n\t" \
  "v_cvt_pk_bf16_f32 v11, v90, v91\n\t" \
  "v_mfma_f32_16x16x32_bf16 v[76:79], a[208:211], v[136:139], v[76:79]\n\t" \
  "ds_read_b128 v[172:175], v242 offset:39936\n\t" \
  "v_mfma_f32_16x16x32_bf16 v[76:79], a[216:219], v[140:143], v[76:79]\n\t" \
  "v_mfma_f32_16x16x32_bf16 v[92:95], a[220:223], v[140:143], v[92:95]\n\t" \
  "v_mfma_f32_16x16x32_bf16 v[92:95], a[212:215], v[136:139], v[92:95]\n\t" \
  "v_mfma_f32_16x16x32_bf16 v[92:95], a[204:207], v[132:135], v[92:95]\n\t" \
  "s_nop 2\n\t" \
  "v_mfma_f32_16x16x32_bf16 v[92:95], a[196:199], v[128:131], v[92:95]\n\t" \
  "s_waitcnt lgkmcnt(4)\n\t" \
  "v_cvt_pk_bf16_f32 v12, v76, v77\n\t" \
  "v_cvt_pk_bf16_f32 v13, v78, v79\n\t" \
  STP("s_memrealtime s[94:95]\n\t") \
  STP("s_memtime s[92:93]\n\t") \
  "v_mfma_f32_16x16x32_bf16 v[96:99], a[128:131], v[144:147], v[96:99]\n\t" \
  "buffer_store_dwordx4 v[8:11], v242, s[16:19], s69 offen offset:2048 sc1\n\t" \
  "ds_read_b128 v[128:131], v242 offset:40960\n\t" \
  "ds_read_b128 v[132:135], v242 offset:41984\n\t" \
  "ds_read_b64 v[208:209], v238 offset:0\n\t" \
  "v_mfma_f32_16x16x32_bf16 v[96:99], a[136:139], v[148:151], v[96:99]\n\t" \
  "v_cvt_pk_bf16_f32 v14, v92, v93\n\t" \
  "v_cvt_pk_bf16_f32 v15, v94, v95\n\t" \
  "v_mfma_f32_16x16x32_bf16 v[96:99], a[144:147], v[152:155], v[96:99]\n\t" \
  "ds_read_b64 v[210:211], v238 offset:4096\n\t" \
  "v_mfma_f32_16x16x32_bf16 v[96:99], a[152:155], v[156:159], v[96:99]\n\t" \
  "ds_read_b128 v[136:139], v242 offset:43008\n\t" \
  "v_mfma_f32_16x16x32_bf16 v[112:115], a[156:159], v[156:159], v[112:115]\n\t" \
  "ds_read_b64 v[212:213], v238 offset:8192\n\t" \
  "v_mfma_f32_16x16x32_bf16 v[112:115], a[148:151], v[152:155], v[112:115]\n\t" \
  "ds_read_b128 v[140:143], v242 offset:44032\n\t" \
  "v_mfma_f32_16x16x32_bf16 v[112:115], a[140:143], v[148:151], v[112:115]\n\t" \
  "ds_read_b64 v[214:215], v238 offset:12288\n\t" \
  "v_mfma_f32_16x16x32_bf16 v[112:115], a[132:135], v[144:147], v[112:115]\n\t" \
  "s_waitcnt lgkmcnt(8)\n\t" \
  "ds_read_b64 v[216:217], v239 offset:0\n\t" \
  "v_mfma_f32_16x16x32_bf16 v[100:103], a[128:131], v[160:163], v[100:103]\n\t" \
  "buffer_store_dwordx4 v[12:15], v242, s[16:19], s69 offen offset:3072 sc1\n\t" \
  "ds_read_b128 v[144:147], v242 offset:45056\n\t" \
  "ds_read_b128 v[148:151], v242 offset:46080\n\t" \
  "ds_read_b128 v[152:155], v242 offset:47104\n\t" \
  "v_mfma_f32_16x16x32_bf16 v[100:103], a[136:139], v[164:167], v[100:103]\n\t" \
  "ds_read_b128 v[156:159], v242 offset:48128\n\t" \
  "v_mfma_f32_16x16x32_bf16 v[100:103], a[144:147], v[168:171], v[100:103]\n\t" \
  "ds_read_b64 v[218:219], v239 offset:4096\n\t" \
  "v_mfma_f32_16x16x32_bf16 v[100:103], a[152:155], v[172:175], v[100:103]\n\t" \
  "ds_read_b64 v[220:221], v239 offset:8192\n\t" \
  "v_mfma_f32_16x16x32_bf16 v[116:119], a[156:159], v[172:175], v[116:119]\n\t" \
  STP("v_writelane_b32 v255, s94, 36\n\t") \
  STP("v_writelane_b32 v255, s92, 37\n\t") \
  "ds_read_b64 v[222:223], v239 offset:12288\n\t" \
  "v_mfma_f32_16x16x32_bf16 v[116:119], a[148:151], v[168:171], v[116:119]\n\t" \
  "s_waitcnt lgkmcnt(0)\n\t" \
  "v_lshlrev_b32 v0, 16, v208\n\t" \
  "v_lshl_add_u32 v224, s3, 10, v242\n\t" \
  "v_mfma_f32_16x16x32_bf16 v[116:119], a[140:143], v[164:167], v[116:119]\n\t" \
  "s_waitcnt vmcnt(17)\n\t" \
  "v_and_b32 v225, 31, v233\n\t" \
  "v_lshlrev_b32 v245, 2, v225\n\t" \
  "v_mfma_f32_16x16x32_bf16 v[116:119], a[132:135], v[160:163], v[116:119]\n\t" \
  "s_barrier\n\t" \
  "v_mfma_f32_16x16x32_bf16 v[104:107], a[128:131], v[128:131], v[104:107]\n\t" \
  "ds_read_b128 v[160:163], v230 offset:0\n\t" \
  "v_lshlrev_b32 v225, 3, v225\n\t" \
  "v_mfma_f32_16x16x32_bf16 v[104:107], a[136:139], v[132:135], v[104:107]\n\t" \
  "v_and_b32 v226, 0xffffffe0, v224\n\t" \
  "v_lshl_add_u32 v227, s8, 6, v233\n\t" \
  "v_mfma_f32_16x16x32_bf16 v[104:107], a[144:147], v[136:139], v[104:107]\n\t" \
  "ds_read_b128 v[164:167], v230 offset:1024\n\t" \
  "v_sub_u32 v227, 0x7ff, v227\n\t" \
  "v_mfma_f32_16x16x32_bf16 v[104:107], a[152:155], v[140:143], v[104:107]\n\t" \
  "v_and_b32 v228, 0x30, v233\n\t" \
  "v_and_b32 v239, 15, v233\n\t" \
  "v_mfma_f32_16x16x32_bf16 v[120:123], a[156:159], v[140:143], v[120:123]\n\t" \
  "s_add_u32 s97, s64, 917504\n\t" \
  "ds_read_b128 v[168:171], v230 offset:2048\n\t" \
  "v_and_b32 v241, 16, v233\n\t" \
  "v_mfma_f32_16x16x32_bf16 v[120:123], a[148:151], v[136:139], v[120:123]\n\t" \
  "s_and_b32 s97, s97, 0xe0000\n\t" \
  "v_lshlrev_b32 v231, 11, v239\n\t" \
  "v_lshl_add_u32 v231, v241, 1, v231\n\t" \
  "v_mfma_f32_16x16x32_bf16 v[120:123], a[140:143], v[132:135], v[120:123]\n\t" \
  "s_add_u32 s97, s97, s63\n\t" \
  "ds_read_b128 v[172:175], v230 offset:3072\n\t" \
  "v_lshrrev_b32 v239, 5, v233\n\t" \
  "v_mfma_f32_16x16x32_bf16 v[120:123], a[132:135], v[128:131], v[120:123]\n\t" \
  "s_add_u32 m0, s38, 16384\n\t" \
  "v_lshl_add_u32 v231, v239, 4, v231\n\t" \
  "v_lshl_add_u32 v253, v239, 6, v241\n\t" \
  "v_mfma_f32_16x16x32_bf16 v[108:111], a[128:131], v[144:147], v[108:111]\n\t" \
  "ds_read_b128 v[128:131], v230 offset:4096\n\t" \
  "v_and_b32 v239, 1, v233\n\t" \
  "v_mfma_f32_16x16x32_bf16 v[108:111], a[136:139], v[148:151], v[108:111]\n\t" \
  "v_lshl_add_u32 v253, v239, 5, v253\n\t" \
  "v_lshl_add_u32 v253, s3, 7, v253\n\t" \
  "v_mfma_f32_16x16x32_bf16 v[108:111], a[144:147], v[152:155], v[108:111]\n\t" \
  "buffer_load_dwordx4 v242, s[16:19], s97 offen offset:0 sc1 lds\n\t" \
  "v_mfma_f32_16x16x32_bf16 v[108:111], a[152:155], v[156:159], v[108:111]\n\t" \
  "ds_read_b128 v[132:135], v230 offset:5120\n\t" \
  "v_and_b32 v239, 0x800, v224\n\t" \
  "v_mfma_f32_16x16x32_bf16 v[124:127], a[156:159], v[156:159], v[124:127]\n\t" \
  "ds_read_b128 v[136:139], v230 offset:6144\n\t" \
  "v_lshl_or_b32 v240, v239, 20, v242\n\t" \
  "v_mfma_f32_16x16x32_bf16 v[124:127], a[148:151], v[152:155], v[124:127]\n\t" \
  "v_mov_b32 v238, s82\n\t" \
  "v_writelane_b32 v238, s83, 1\n\t" \
  "v_mfma_f32_16x16x32_bf16 v[124:127], a[140:143], v[148:151], v[124:127]\n\t" \
  "buffer_load_dwordx4 v242, s[16:19], s97 offen offset:1024 sc1 lds\n\t" \
  "v_mfma_f32_16x16x32_bf16 v[124:127], a[132:135], v[144:147], v[124:127]\n\t" \
  "s_waitcnt lgkmcnt(3)\n\t" \
  "ds_read_b128 v[140:143], v230 offset:7168\n\t" \
  "v_perm_b32 v239, v180, v176, s40\n\t" \
  "v_mfma_f32_16x16x32_bf16 v[96:99], a[160:163], v[160:163], v[96:99]\n\t" \
  "ds_read_b128 v[144:147], v230 offset:8192\n\t" \
  "v_perm_b32 v180, v180, v176, s41\n\t" \
  "v_mfma_f32_16x16x32_bf16 v[96:99], a[168:171], v[164:167], v[96:99]\n\t" \
  "buffer_load_dwordx4 v242, s[16:19], s97 offen offset:2048 sc1 lds\n\t" \
  "v_mfma_f32_16x16x32_bf16 v[96:99], a[176:179], v[168:171], v[96:99]\n\t" \
  "ds_read_b128 v[148:151], v230 offset:9216\n\t" \
  "v_mov_b32 v176, v239\n\t" \
  "v_mfma_f32_16x16x32_bf16 v[96:99], a[184:187], v[172:175], v[96:99]\n\t" \
  "v_perm_b32 v239, v181, v177, s40\n\t" \
  "v_perm_b32 v181, v181, v177, s41\n\t" \
  "v_mfma_f32_16x16x32_bf16 v[112:115], a[188:191], v[172:175], v[112:115]\n\t" \
  "ds_read_b128 v[152:155], v230 offset:10240\n\t" \
  "v_mov_b32 v177, v239\n\t" \
  "v_mfma_f32_16x16x32_bf16 v[112:115], a[180:183], v[168:171], v[112:115]\n\t" \
  "buffer_load_dwordx4 v242, s[16:19], s97 offen offset:3072 sc1 lds\n\t" \
  "v_mfma_f32_16x16x32_bf16 v[112:115], a[172:175], v[164:167], v[112:115]\n\t" \
  "ds_read_b128 v[156:159], v230 offset:11264\n\t" \
  "v_perm_b32 v239, v182, v178, s40\n\t" \
  "v_mfma_f32_16x16x32_bf16 v[112:115], a[164:167], v[160:163], v[112:115]\n\t" \
  "s_waitcnt lgkmcnt(4)\n\t" \
  "v_perm_b32 v182, v182, v178, s41\n\t" \
  "v_mov_b32 v178, v239\n\t" \
  "v_mfma_f32_16x16x32_bf16 v[100:103], a[160:163], v[128:131], v[100:103]\n\t" \
  "ds_read_b128 v[160:163], v230 offset:12288\n\t" \
  "v_perm_b32 v239, v183, v179, s40\n\t" \
  "v_mfma_f32_16x16x32_bf16 v[100:103], a[168:171], v[132:135], v[100:103]\n\t" \
  "v_perm_b32 v183, v183, v179, s41\n\t" \
  "v_mov_b32 v179, v239\n\t" \
  "v_mfma_f32_16x16x32_bf16 v[100:103], a[176:179], v[136:139], v[100:103]\n\t" \
  "ds_read_b128 v[164:167], v230 offset:13312\n\t" \
  "v_perm_b32 v239, v188, v184, s40\n\t" \
  "v_mfma_f32_16x16x32_bf16 v[100:103], a[184:187], v[140:143], v[100:103]\n\t" \
  "v_perm_b32 v188, v188, v184, s41\n\t" \
  "v_mov_b32 v184, v239\n\t" \
  "v_mfma_f32_16x16x32_bf16 v[116:119], a[188:191], v[140:143], v[116:119]\n\t" \
  "ds_read_b128 v[168:171], v230 offset:14336\n\t" \
  "v_perm_b32 v239, v189, v185, s40\n\t" \
  "v_mfma_f32_16x16x32_bf16 v[116:119], a[180:183], v[136:139], v[116:119]\n\t" \
  "v_perm_b32 v189, v189, v185, s41\n\t" \
  "v_mov_b32 v185, v239\n\t" \
  "v_mfma_f32_16x16x32_bf16 v[116:119], a[172:175], v[132:135], v[116:119]\n\t" \
  "ds_read_b128 v[172:175], v230 offset:15360\n\t" \
  "v_and_b32 v1, 0xffff0000, v208\n\t" \
  "v_mfma_f32_16x16x32_bf16 v[116:119], a[164:167], v[128:131], v[116:119]\n\t" \
  "s_waitcnt lgkmcnt(4)\n\t" \
  "v_lshlrev_b32 v2, 16, v209\n\t" \
  "v_perm_b32 v239, v190, v186, s40\n\t" \
  "v_mfma_f32_16x16x32_bf16 v[104:107], a[160:163], v[144:147], v[104:107]\n\t" \
  "s_waitcnt vmcnt(9)\n\t" \
  "v_cmp_ne_u32 vcc, s36, v246\n\t" \
  "v_perm_b32 v190, v190, v186, s41\n\t" \
  "v_mfma_f32_16x16x32_bf16 v[104:107], a[168:171], v[148:151], v[104:107]\n\t" \
  "ds_read_b128 v[128:131], v230 offset:32768\n\t" \
  "v_and_b32 v3, 0xffff0000, v209\n\t" \
  "v_mfma_f32_16x16x32_bf16 v[104:107], a[176:179], v[152:155], v[104:107]\n\t" \
  "s_cbranch_vccnz .Lat_s4\n\t" \
  ".Lat_k4:\n\t" \
  "v_mfma_f32_16x16x32_bf16 v[104:107], a[184:187], v[156:159], v[104:107]\n\t" \
  "buffer_load_dwordx4 v[32:35], v242, s[16:19], s70 offen offset:0 sc1\n\t" \
  "v_mfma_f32_16x16x32_bf16 v[120:123], a[188:191], v[156:159], v[120:123]\n\t" \
  "ds_read_b128 v[132:135], v230 offset:33792\n\t" \
  "v_lshlrev_b32 v4, 16, v210\n\t" \
  "v_mfma_f32_16x16x32_bf16 v[120:123], a[180:183], v[152:155], v[120:123]\n\t" \
  "ds_read_b128 v[136:139], v230 offset:34816\n\t" \
  "v_mov_b32 v186, v239\n\t" \
  "v_mfma_f32_16x16x32_bf16 v[120:123], a[172:175], v[148:151], v[120:123]\n\t" \
  "ds_read_b128 v[140:143], v230 offset:35840\n\t" \
  "v_perm_b32 v239, v191, v187, s40\n\t" \
  "v_mfma_f32_16x16x32_bf16 v[120:123], a[164:167], v[144:147], v[120:123]\n\t" \
  "s_waitcnt lgkmcnt(4)\n\t" \
  "v_and_b32 v5, 0xffff0000, v210\n\t" \
  "v_perm_b32 v191, v191, v187, s41\n\t" \
  "v_mfma_f32_16x16x32_bf16 v[108:111], a[160:163], v[160:163], v[108:111]\n\t" \
  "ds_read_b128 v[144:147], v230 offset:36864\n\t" \
  "v_lshlrev_b32 v6, 16, v211\n\t" \
  "v_mfma_f32_16x16x32_bf16 v[108:111], a[168:171], v[164:167], v[108:111]\n\t" \
  "buffer_load_dwordx4 v[36:39], v242, s[16:19], s70 offen offset:1024 sc1\n\t" \
  "v_mfma_f32_16x16x32_bf16 v[108:111], a[176:179], v[168:171], v[108:111]\n\t" \
  "ds_read_b128 v[148:151], v230 offset:37888\n\t" \
  "v_and_b32 v7, 0xffff0000, v211\n\t" \
  "v_mfma_f32_16x16x32_bf16 v[108:111], a[184:187], v[172:175], v[108:111]\n\t" \
  "v_mov_b32 v187, v239\n\t" \
  "v_perm_b32 v239, v196, v192, s40\n\t" \
  "v_mfma_f32_16x16x32_bf16 v[124:127], a[188:191], v[172:175], v[124:127]\n\t" \
  "ds_read_b128 v[152:155], v230 offset:38912\n\t" \
  "v_lshlrev_b32 v8, 16, v212\n\t" \
  "v_mfma_f32_16x16x32_bf16 v[124:127], a[180:183], v[168:171], v[124:127]\n\t" \
  "v_and_b32 v9, 0xffff0000, v212\n\t" \
  "v_perm_b32 v196, v196, v192, s41\n\t" \
  "v_mfma_f32_16x16x32_bf16 v[124:127], a[172:175], v[164:167], v[124:127]\n\t" \
  "ds_read_b128 v[156:159], v230 offset:39936\n\t" \
  "v_mov_b32 v192, v239\n\t" \
  "v_mfma_f32_16x16x32_bf16 v[124:127], a[164:167], v[160:163], v[124:127]\n\t" \
  "s_waitcnt lgkmcnt(4)\n\t" \
  "buffer_load_dwordx4 v[40:43], v242, s[16:19], s70 offen offset:2048 sc1\n\t" \
  "v_mfma_f32_16x16x32_bf16 v[96:99], a[0:3], v[128:131], v[96:99]\n\t" \
  "ds_read_b128 v[160:163], v230 offset:40960\n\t" \
  "v_lshlrev_b32 v10, 16, v213\n\t" \
  "v_mfma_f32_16x16x32_bf16 v[96:99], a[8:11], v[132:135], v[96:99]\n\t" \
  "v_and_b32 v11, 0xffff0000, v213\n\t" \
  "v_perm_b32 v239, v197, v193, s40\n\t" \
  "v_mfma_f32_16x16x32_bf16 v[96:99], a[16:19], v[136:139], v[96:99]\n\t" \
  "ds_read_b128 v[164:167], v230 offset:41984\n\t" \
  "v_perm_b32 v197, v197, v193, s41\n\t" \
  "v_mfma_f32_16x16x32_bf16 v[96:99], a[24:27], v[140:143], v[96:99]\n\t" \
  "v_lshlrev_b32 v12, 16, v214\n\t" \
  "v_mov_b32 v193, v239\n\t" \
  "v_mfma_f32_16x16x32_bf16 v[112:115], a[28:31], v[140:143], v[112:115]\n\t" \
  "s_waitcnt vmcnt(7)\n\t" \
  "buffer_store_dword v243, v244, s[16:19], s66 offen offset:32 sc1\n\t" \
  "ds_read_b128 v[168:171], v230 offset:43008\n\t" \
  "v_mfma_f32_16x16x32_bf16 v[112:115], a[20:23], v[136:139], v[112:115]\n\t" \
  "v_and_b32 v13, 0xffff0000, v214\n\t" \
  "v_perm_b32 v239, v198, v194, s40\n\t" \
  "v_mfma_f32_16x16x32_bf16 v[112:115], a[12:15], v[132:135], v[112:115]\n\t" \
  "buffer_load_dwordx4 v[44:47], v242, s[16:19], s70 offen offset:3072 sc1\n\t" \
  "v_mfma_f32_16x16x32_bf16 v[112:115], a[4:7], v[128:131], v[112:115]\n\t" \
  "s_waitcnt lgkmcnt(3)\n\t" \
  "ds_read_b128 v[172:175], v230 offset:44032\n\t" \
  "v_lshlrev_b32 v14, 16, v215\n\t" \
  "v_mfma_f32_16x16x32_bf16 v[100:103], a[0:3], v[144:147], v[100:103]\n\t" \
  "ds_read_b128 v[128:131], v230 offset:45056\n\t" \
  "v_perm_b32 v198, v198, v194, s41\n\t" \
  "v_mfma_f32_16x16x32_bf16 v[100:103], a[8:11], v[148:151], v[100:103]\n\t" \
  "v_and_b32 v15, 0xffff0000, v215\n\t" \
  "v_mov_b32 v194, v239\n\t" \
  "v_mfma_f32_16x16x32_bf16 v[100:103], a[16:19], v[152:155], v[100:103]\n\t" \
  "ds_read_b128 v[132:135], v230 offset:46080\n\t" \
  "v_lshlrev_b32 v16, 16, v216\n\t" \
  "v_mfma_f32_16x16x32_bf16 v[100:103], a[24:27], v[156:159], v[100:103]\n\t" \
  "buffer_load_dwordx4 v[48:51], v242, s[16:19], s78 offen offset:0 sc1\n\t" \
  "v_mfma_f32_16x16x32_bf16 v[116:119], a[28:31], v[156:159], v[116:119]\n\t" \
  "ds_read_b128 v[136:139], v230 offset:47104\n\t" \
  "v_perm_b32 v239, v199, v195, s40\n\t" \
  "v_mfma_f32_16x16x32_bf16 v[116:119], a[20:23], v[152:155], v[116:119]\n\t" \
  "v_and_b32 v17, 0xffff0000, v216\n\t" \
  "v_lshlrev_b32 v18, 16, v217\n\t" \
  "v_mfma_f32_16x16x32_bf16 v[116:119], a[12:15], v[148:151], v[116:119]\n\t" \
  "s_waitcnt lgkmcnt(3)\n\t" \
  "ds_read_b128 v[140:143], v230 offset:48128\n\t" \
  "v_perm_b32 v199, v199, v195, s41\n\t" \
  "v_mfma_f32_16x16x32_bf16 v[116:119], a[4:7], v[144:147], v[116:119]\n\t" \
  "s_barrier\n\t" \
  "v_mfma_f32_16x16x32_bf16 v[104:107], a[0:3], v[160:163], v[104:107]\n\t" \
  "ds_read_b128 v[144:147], v230 offset:49152\n\t" \
  "v_mov_b32 v195, v239\n\t" \
  "v_mfma_f32_16x16x32_bf16 v[104:107], a[8:11], v[164:167], v[104:107]\n\t" \
  "buffer_load_dwordx4 v[52:55], v242, s[16:19], s78 offen offset:1024 sc1\n\t" \
  "v_mfma_f32_16x16x32_bf16 v[104:107], a[16:19], v[168:171], v[104:107]\n\t" \
  "ds_read_b128 v[148:151], v230 offset:50176\n\t" \
  "v_and_b32 v19, 0xffff0000, v217\n\t" \
  "v_mfma_f32_16x16x32_bf16 v[104:107], a[24:27], v[172:175], v[104:107]\n\t" \
  "v_lshlrev_b32 v20, 16, v218\n\t" \
  "v_perm_b32 v239, v204, v200, s40\n\t" \
  "v_mfma_f32_16x16x32_bf16 v[120:123], a[28:31], v[172:175], v[120:123]\n\t" \
  "ds_read_b128 v[152:155], v230 offset:51200\n\t" \
  "v_and_b32 v21, 0xffff0000, v218\n\t" \
  "v_mfma_f32_16x16x32_bf16 v[120:123], a[20:23], v[168:171], v[120:123]\n\t" \
  "v_lshlrev_b32 v22, 16, v219\n\t" \
  "v_perm_b32 v204, v204, v200, s41\n\t" \
  "v_mfma_f32_16x16x32_bf16 v[120:123], a[12:15], v[164:167], v[120:123]\n\t" \
  "ds_read_b128 v[156:159], v230 offset:52224\n\t" \
  "v_mov_b32 v200, v239\n\t" \
  "v_mfma_f32_16x16x32_bf16 v[120:123], a[4:7], v[160:163], v[120:123]\n\t" \
  "s_waitcnt lgkmcnt(4)\n\t" \
  "buffer_load_dwordx4 v[56:59], v242, s[16:19], s78 offen offset:2048 sc1\n\t" \
  "v_mfma_f32_16x16x32_bf16 v[108:111], a[0:3], v[128:131], v[108:111]\n\t" \
  "ds_read_b128 v[160:163], v230 offset:53248\n\t" \
  "v_and_b32 v23, 0xffff0000, v219\n\t" \
  "v_mfma_f32_16x16x32_bf16 v[108:111], a[8:11], v[132:135], v[108:111]\n\t" \
  "v_lshlrev_b32 v24, 16, v220\n\t" \
  "v_perm_b32 v239, v205, v201, s40\n\t" \
  "v_mfma_f32_16x16x32_bf16 v[108:111], a[16:19], v[136:139], v[108:111]\n\t" \
  "ds_read_b128 v[164:167], v230 offset:54272\n\t" \
  "v_perm_b32 v205, v205, v201, s41\n\t" \
  "v_mfma_f32_16x16x32_bf16 v[108:111], a[24:27], v[140:143], v[108:111]\n\t" \
  "v_and_b32 v25, 0xffff0000, v220\n\t" \
  "v_mov_b32 v201, v239\n\t" \
  "v_mfma_f32_16x16x32_bf16 v[124:127], a[28:31], v[140:143], v[124:127]\n\t" \
  "ds_read_b128 v[168:171], v230 offset:55296\n\t" \
  "v_lshlrev_b32 v26, 16, v221\n\t" \
  "v_mfma_f32_16x16x32_bf16 v[124:127], a[20:23], v[136:139], v[124:127]\n\t" \
  "buffer_load_dwordx4 v[60:63], v242, s[16:19], s78 offen offset:3072 sc1\n\t" \
  "v_mfma_f32_16x16x32_bf16 v[124:127], a[12:15], v[132:135], v[124:127]\n\t" \
  "ds_read_b128 v[172:175], v230 offset:56320\n\t" \
  "v_accvgpr_write_b32 a128, 0\n\t" \
  "v_mfma_f32_16x16x32_bf16 v[124:127], a[4:7], v[128:131], v[124:127]\n\t" \
  "s_waitcnt lgkmcnt(4)\n\t" \
  "v_accvgpr_write_b32 a129, 0\n\t" \
  "v_accvgpr_write_b32 a130, v248\n\t" \
  "v_mfma_f32_16x16x32_bf16 v[96:99], a[32:35], v[144:147], v[96:99]\n\t" \
  "ds_read_b128 v[128:131], v230 offset:57344\n\t" \
  "v_accvgpr_write_b32 a131, v249\n\t" \
  "v_mfma_f32_16x16x32_bf16 v[96:99], a[40:43], v[148:151], v[96:99]\n\t" \
  "v_accvgpr_write_b32 a132, 0\n\t" \
  "v_accvgpr_write_b32 a133, 0\n\t" \
  "v_mfma_f32_16x16x32_bf16 v[96:99], a[48:51], v[152:155], v[96:99]\n\t" \
  "ds_read_b128 v[132:135], v230 offset:58368\n\t" \
  "v_perm_b32 v239, v206, v202, s40\n\t" \
  "v_mfma_f32_16x16x32_bf16 v[96:99], a[56:59], v[156:159], v[96:99]\n\t" \
  "ds_read_b128 a[176:179], v236 offset:960\n\t" \
  "v_and_b32 v27, 0xffff0000, v221\n\t" \
  "v_mfma_f32_16x16x32_bf16 v[112:115], a[60:63], v[156:159], v[112:115]\n\t" \
  "ds_read_b128 v[136:139], v230 offset:59392\n\t" \
  "v_perm_b32 v206, v206, v202, s41\n\t" \
  "v_mfma_f32_16x16x32_bf16 v[112:115], a[52:55], v[152:155], v[112:115]\n\t" \
  "ds_read_b128 a[180:183], v236 offset:640\n\t" \
  "v_lshlrev_b32 v28, 16, v222\n\t" \
  "v_mfma_f32_16x16x32_bf16 v[112:115], a[44:47], v[148:151], v[112:115]\n\t" \
  "ds_read_b128 v[140:143], v230 offset:60416\n\t" \
  "v_mov_b32 v202, v239\n\t" \
  "v_mfma_f32_16x16x32_bf16 v[112:115], a[36:39], v[144:147], v[112:115]\n\t" \
  "s_waitcnt lgkmcnt(6)\n\t" \
  "ds_read_b128 a[184:187], v236 offset:320\n\t" \
  "v_and_b32 v29, 0xffff0000, v222\n\t" \
  "v_mfma_f32_16x16x32_bf16 v[100:103], a[32:35], v[160:163], v[100:103]\n\t" \
  "ds_read_b128 v[144:147], v230 offset:61440\n\t" \
  "v_perm_b32 v239, v207, v203, s40\n\t" \
  "v_mfma_f32_16x16x32_bf16 v[100:103], a[40:43], v[164:167], v[100:103]\n\t" \
  "ds_read_b128 a[188:191], v236 offset:0\n\t" \
  "v_lshlrev_b32 v30, 16, v223\n\t" \
  "v_mfma_f32_16x16x32_bf16 v[100:103], a[48:51], v[168:171], v[100:103]\n\t" \
  "ds_read_b128 v[148:151], v230 offset:62464\n\t" \
  "v_perm_b32 v207, v207, v203, s41\n\t" \
  "v_mfma_f32_16x16x32_bf16 v[100:103], a[56:59], v[172:175], v[100:103]\n\t" \
  "v_and_b32 v31, 0xffff0000, v223\n\t" \
  "v_mov_b32 v203, v239\n\t" \
  "v_mfma_f32_16x16x32_bf16 v[116:119], a[60:63], v[172:175], v[116:119]\n\t" \
  "ds_read_b128 v[152:155], v230 offset:63488\n\t" \
  "v_accvgpr_read_b32 v239, a224\n\t" \
  "v_mfma_f32_16x16x32_bf16 v[116:119], a[52:55], v[168:171], v[116:119]\n\t" \
  "v_accvgpr_read_b32 v84, a228\n\t" \
  "v_perm_b32 v80, v84, v239, s40\n\t" \
  "v_mfma_f32_16x16x32_bf16 v[116:119], a[44:47], v[164:167], v[116:119]\n\t" \
  "ds_read_b128 v[156:159], v230 offset:64512\n\t" \
  "v_perm_b32 v84, v84, v239, s41\n\t" \
  "v_mfma_f32_16x16x32_bf16 v[116:119], a[36:39], v[160:163], v[116:119]\n\t" \
  "s_waitcnt lgkmcnt(6)\n\t" \
  "v_accvgpr_read_b32 v239, a225\n\t" \
  "v_accvgpr_read_b32 v85, a229\n\t" \
  "v_mfma_f32_16x16x32_bf16 v[104:107], a[32:35], v[128:131], v[104:107]\n\t" \
  "ds_read_b128 v[160:163], v230 offset:16384\n\t" \
  "v_perm_b32 v81, v85, v239, s40\n\t" \
  "v_mfma_f32_16x16x32_bf16 v[104:107], a[40:43], v[132:135], v[104:107]\n\t" \
  "v_perm_b32 v85, v85, v239, s41\n\t" \
  "v_accvgpr_read_b32 v239, a226\n\t" \
  "v_mfma_f32_16x16x32_bf16 v[104:107], a[48:51], v[136:139], v[104:107]\n\t" \
  "ds_read_b128 v[164:167], v230 offset:17408\n\t" \
  "v_accvgpr_read_b32 v86, a230\n\t" \
  "v_mfma_f32_16x16x32_bf16 v[104:107], a[56:59], v[140:143], v[104:107]\n\t" \
  "v_perm_b32 v82, v86, v239, s40\n\t" \
  "v_perm_b32 v86, v86, v239, s41\n\t" \
  "v_mfma_f32_16x16x32_bf16 v[120:123], a[60:63], v[140:143], v[120:123]\n\t" \
  "buffer_load_dword v246, off, s[16:19], s73 offset:32 sc1\n\t" \
  "v_mfma_f32_16x16x32_bf16 v[120:123], a[52:55], v[136:139], v[120:123]\n\t" \
  "ds_read_b128 v[168:171], v230 offset:18432\n\t" \
  "v_accvgpr_read_b32 v239, a227\n\t" \
  "v_mfma_f32_16x16x32_bf16 v[120:123], a[44:47], v[132:135], v[120:123]\n\t" \
  "ds_read_b128 v[172:175], v230 offset:19456\n\t" \
  "v_accvgpr_read_b32 v87, a231\n\t" \
  "v_mfma_f32_16x16x32_bf16 v[120:123], a[36:39], v[128:131], v[120:123]\n\t" \
  "s_waitcnt lgkmcnt(4)\n\t" \
  "v_perm_b32 v83, v87, v239, s40\n\t" \
  "v_perm_b32 v87, v87, v239, s41\n\t" \
  "v_mfma_f32_16x16x32_bf16 v[108:111], a[32:35], v[144:147], v[108:111]\n\t" \
  "ds_read_b128 v[128:131], v230 offset:20480\n\t" \
  "v_accvgpr_read_b32 v239, a232\n\t" \
  "v_mfma_f32_16x16x32_bf16 v[108:111], a[40:43], v[148:151], v[108:111]\n\t" \
  "v_accvgpr_read_b32 v92, a236\n\t" \
  "v_perm_b32 v88, v92, v239, s40\n\t" \
  "v_mfma_f32_16x16x32_bf16 v[108:111], a[48:51], v[152:155], v[108:111]\n\t" \
  "ds_read_b128 v[132:135], v230 offset:21504\n\t" \
  "v_perm_b32 v92, v92, v239, s41\n\t" \
  "v_mfma_f32_16x16x32_bf16 v[108:111], a[56:59], v[156:159], v[108:111]\n\t" \
  ".Lhs15:\n\ts_cbranch_execz .Lhs15n\n\t" \
  "v_accvgpr_read_b32 v239, a233\n\t" \
  "v_accvgpr_read_b32 v93, a237\n\t" \
  "v_mfma_f32_16x16x32_bf16 v[124:127], a[60:63], v[156:159], v[124:127]\n\t" \
  "ds_read_b128 v[136:139], v230 offset:22528\n\t" \
  "v_perm_b32 v89, v93, v239, s40\n\t" \
  "v_mfma_f32_16x16x32_bf16 v[124:127], a[52:55], v[152:155], v[124:127]\n\t" \
  "v_perm_b32 v93, v93, v239, s41\n\t" \
  "v_accvgpr_read_b32 v239, a234\n\t" \
  "v_mfma_f32_16x16x32_bf16 v[124:127], a[44:47], v[148:151], v[124:127]\n\t" \
  "ds_read_b128 v[140:143], v230 offset:23552\n\t" \
  "v_accvgpr_read_b32 v94, a238\n\t" \
  "v_mfma_f32_16x16x32_bf16 v[124:127], a[36:39], v[144:147], v[124:127]\n\t" \
  "s_waitcnt lgkmcnt(4)\n\t" \
  "v_perm_b32 v90, v94, v239, s40\n\t" \
  "v_perm_b32 v94, v94, v239, s41\n\t" \
  "v_mfma_f32_16x16x32_bf16 v[96:99], a[64:67], v[160:163], v[96:99]\n\t" \
  "ds_read_b128 v[144:147], v230 offset:24576\n\t" \
  "v_accvgpr_read_b32 v239, a235\n\t" \
  "v_mfma_f32_16x16x32_bf16 v[96:99], a[72:75], v[164:167], v[96:99]\n\t" \
  "v_accvgpr_read_b32 v95, a239\n\t" \
  "v_perm_b32 v91, v95, v239, s40\n\t" \
  "v_mfma_f32_16x16x32_bf16 v[96:99], a[80:83], v[168:171], v[96:99]\n\t" \
  "ds_read_b128 v[148:151], v230 offset:25600\n\t" \
  "v_perm_b32 v95, v95, v239, s41\n\t" \
  "v_mfma_f32_16x16x32_bf16 v[96:99], a[88:91], v[172:175], v[96:99]\n\t" \
  "v_accvgpr_read_b32 v239, a240\n\t" \
  "v_accvgpr_read_b32 v212, a244\n\t" \
  "v_mfma_f32_16x16x32_bf16 v[112:115], a[92:95], v[172:175], v[112:115]\n\t" \
  "ds_read_b128 v[152:155], v230 offset:26624\n\t" \
  "v_perm_b32 v208, v212, v239, s40\n\t" \
  "v_mfma_f32_16x16x32_bf16 v[112:115], a[84:87], v[168:171], v[112:115]\n\t" \
  "v_perm_b32 v212, v212, v239, s41\n\t" \
  "v_accvgpr_read_b32 v239, a241\n\t" \
  "v_mfma_f32_16x16x32_bf16 v[112:115], a[76:79], v[164:167], v[112:115]\n\t" \
  "ds_read_b128 v[156:159], v230 offset:27648\n\t" \
  "v_accvgpr_read_b32 v213, a245\n\t" \
  "v_mfma_f32_16x16x32_bf16 v[112:115], a[68:71], v[160:163], v[112:115]\n\t" \
  "s_waitcnt lgkmcnt(4)\n\t" \
  "v_perm_b32 v209, v213, v239, s40\n\t" \
  "v_perm_b32 v213, v213, v239, s41\n\t" \
  "v_mfma_f32_16x16x32_bf16 v[100:103], a[64:67], v[128:131], v[100:103]\n\t" \
  "ds_read_b128 v[160:163], v230 offset:28672\n\t" \
  "v_accvgpr_read_b32 v239, a242\n\t" \
  "v_mfma_f32_16x16x32_bf16 v[100:103], a[72:75], v[132:135], v[100:103]\n\t" \
  "v_accvgpr_read_b32 v214, a246\n\t" \
  "v_perm_b32 v210, v214, v239, s40\n\t" \
  "v_mfma_f32_16x16x32_bf16 v[100:103], a[80:83], v[136:139], v[100:103]\n\t" \
  "ds_read_b128 v[164:167], v230 offset:29696\n\t" \
  "v_perm_b32 v214, v214, v239, s41\n\t" \
  "v_mfma_f32_16x16x32_bf16 v[100:103], a[88:91], v[140:143], v[100:103]\n\t" \
  "v_accvgpr_read_b32 v239, a243\n\t" \
  "v_accvgpr_read_b32 v215, a247\n\t" \
  "v_mfma_f32_16x16x32_bf16 v[116:119], a[92:95], v[140:143], v[116:119]\n\t" \
  "ds_read_b128 v[168:171], v230 offset:30720\n\t" \
  "v_perm_b32 v211, v215, v239, s40\n\t" \
  "v_mfma_f32_16x16x32_bf16 v[116:119], a[84:87], v[136:139], v[116:119]\n\t" \
  "v_perm_b32 v215, v215, v239, s41\n\t" \
  "v_accvgpr_read_b32 v239, a248\n\t" \
  "v_mfma_f32_16x16x32_bf16 v[116:119], a[76:79], v[132:135], v[116:119]\n\t" \
  "s_waitcnt vmcnt(10) lgkmcnt(3)\n\t" \
  "ds_read_b128 v[172:175], v230 offset:31744\n\t" \
  "v_accvgpr_read_b32 v220, a252\n\t" \
  "v_mfma_f32_16x16x32_bf16 v[116:119], a[68:71], v[128:131], v[116:119]\n\t" \
  "s_barrier\n\t" \
  "v_mfma_f32_16x16x32_bf16 v[104:107], a[64:67], v[144:147], v[104:107]\n\t" \
  "ds_read_b128 v[128:131], v232 offset:0\n\t" \
  "v_perm_b32 v216, v220, v239, s40\n\t" \
  "v_mfma_f32_16x16x32_bf16 v[104:107], a[72:75], v[148:151], v[104:107]\n\t" \
  "v_perm_b32 v220, v220, v239, s41\n\t" \
  "v_accvgpr_read_b32 v239, a249\n\t" \
  "v_mfma_f32_16x16x32_bf16 v[104:107], a[80:83], v[152:155], v[104:107]\n\t" \
  "ds_read_b128 v[132:135], v232 offset:1024\n\t" \
  "v_accvgpr_read_b32 v221, a253\n\t" \
  "v_mfma_f32_16x16x32_bf16 v[104:107], a[88:91], v[156:159], v[104:107]\n\t" \
  "buffer_load_dword v251, off, s[16:19], s73 offset:32 sc1\n\t" \
  "v_mfma_f32_16x16x32_bf16 v[120:123], a[92:95], v[156:159], v[120:123]\n\t" \
  "ds_read_b128 v[136:139], v232 offset:2048\n\t" \
  "v_perm_b32 v217, v221, v239, s40\n\t" \
  "v_mfma_f32_16x16x32_bf16 v[120:123], a[84:87], v[152:155], v[120:123]\n\t" \
  "v_perm_b32 v221, v221, v239, s41\n\t" \
  "v_accvgpr_read_b32 v239, a250\n\t" \
  "v_mfma_f32_16x16x32_bf16 v[120:123], a[76:79], v[148:151], v[120:123]\n\t" \
  "ds_read_b128 v[140:143], v232 offset:3072\n\t" \
  "v_accvgpr_read_b32 v222, a254\n\t" \
  "v_mfma_f32_16x16x32_bf16 v[120:123], a[68:71], v[144:147], v[120:123]\n\t" \
  "s_waitcnt lgkmcnt(4)\n\t" \
  "v_perm_b32 v218, v222, v239, s40\n\t" \
  "v_perm_b32 v222, v222, v239, s41\n\t" \
  "v_mfma_f32_16x16x32_bf16 v[108:111], a[64:67], v[160:163], v[108:111]\n\t" \
  "s_waitcnt vmcnt(1)\n\t" \
  "v_cmp_ne_u32 vcc, s36, v246\n\t" \
  "v_accvgpr_read_b32 v239, a251\n\t" \
  "v_mfma_f32_16x16x32_bf16 v[108:111], a[72:75], v[164:167], v[108:111]\n\t" \
  "ds_read_b128 v[144:147], v232 offset:4096\n\t" \
  "v_accvgpr_read_b32 v223, a255\n\t" \
  "v_mfma_f32_16x16x32_bf16 v[108:111], a[80:83], v[168:171], v[108:111]\n\t" \
  "s_mov_b32 s88, vcc_lo\n\t" \
  "s_cbranch_vccnz .Lat_n2\n\t" \
  "buffer_load_dwordx4 v[64:67], v242, s[16:19], s79 offen offset:0 sc1\n\t" \
  "buffer_load_dwordx4 v[68:71], v242, s[16:19], s79 offen offset:1024 sc1\n\t" \
  "buffer_load_dwordx4 v[72:75], v242, s[16:19], s79 offen offset:2048 sc1\n\t" \
  "buffer_load_dwordx4 v[76:79], v242, s[16:19], s79 offen offset:3072 sc1\n\t" \
  ".Lat_n2:\n\t" \
  "ds_read_b128 v[148:151], v232 offset:5120\n\t" \
  "v_perm_b32 v219, v223, v239, s40\n\t" \
  "v_mfma_f32_16x16x32_bf16 v[108:111], a[88:91], v[172:175], v[108:111]\n\t" \
  "ds_read_b128 v[152:155], v232 offset:6144\n\t" \
  "v_perm_b32 v223, v223, v239, s41\n\t" \
  "v_mfma_f32_16x16x32_bf16 v[124:127], a[92:95], v[172:175], v[124:127]\n\t" \
  "ds_read_b128 v[156:159], v232 offset:7168\n\t" \
  "v_mfma_f32_16x16x32_bf16 v[124:127], a[84:87], v[168:171], v[124:127]\n\t" \
  "v_mfma_f32_16x16x32_bf16 v[124:127], a[76:79], v[164:167], v[124:127]\n\t" \
  "v_mfma_f32_16x16x32_bf16 v[124:127], a[68:71], v[160:163], v[124:127]\n\t" \
  "s_waitcnt lgkmcnt(4)\n\t" \
  "v_mfma_f32_16x16x32_bf16 v[96:99], a[96:99], v[128:131], v[96:99]\n\t" \
  "ds_read_b128 v[160:163], v232 offset:8192\n\t" \
  "v_mfma_f32_16x16x32_bf16 v[96:99], a[104:107], v[132:135], v[96:99]\n\t" \
  "ds_read_b128 v[164:167], v232 offset:9216\n\t" \
  "v_mfma_f32_16x16x32_bf16 v[96:99], a[112:115], v[136:139], v[96:99]\n\t" \
  "ds_read_b128 v[168:171], v232 offset:10240\n\t" \
  "v_mfma_f32_16x16x32_bf16 v[96:99], a[120:123], v[140:143], v[96:99]\n\t" \
  "ds_read_b128 v[172:175], v232 offset:11264\n\t" \
  "v_mfma_f32_16x16x32_bf16 v[112:115], a[124:127], v[140:143], v[112:115]\n\t" \
  "v_mfma_f32_16x16x32_bf16 v[112:115], a[116:119], v[136:139], v[112:115]\n\t" \
  "v_mfma_f32_16x16x32_bf16 v[112:115], a[108:111], v[132:135], v[112:115]\n\t" \
  "v_mfma_f32_16x16x32_bf16 v[112:115], a[100:103], v[128:131], v[112:115]\n\t" \
  "s_waitcnt lgkmcnt(4)\n\t" \
  "v_mfma_f32_16x16x32_bf16 v[100:103], a[96:99], v[144:147], v[100:103]\n\t" \
  "ds_read_b128 v[128:131], v232 offset:12288\n\t" \
  "v_mfma_f32_16x16x32_bf16 v[100:103], a[104:107], v[148:151], v[100:103]\n\t" \
  "ds_read_b128 v[132:135], v232 offset:13312\n\t" \
  "v_mfma_f32_16x16x32_bf16 v[100:103], a[112:115], v[152:155], v[100:103]\n\t" \
  "ds_read_b128 v[136:139], v232 offset:14336\n\t" \
  "v_mfma_f32_16x16x32_bf16 v[100:103], a[120:123], v[156:159], v[100:103]\n\t" \
  "ds_read_b128 v[140:143], v232 offset:15360\n\t" \
  "v_mfma_f32_16x16x32_bf16 v[116:119], a[124:127], v[156:159], v[116:119]\n\t" \
  "v_mfma_f32_16x16x32_bf16 v[116:119], a[116:119], v[152:155], v[116:119]\n\t" \
  "v_mfma_f32_16x16x32_bf16 v[116:119], a[108:111], v[148:151], v[116:119]\n\t" \
  "v_mfma_f32_16x16x32_bf16 v[116:119], a[100:103], v[144:147], v[116:119]\n\t" \
  "s_waitcnt lgkmcnt(4)\n\t" \
  "v_mfma_f32_16x16x32_bf16 v[104:107], a[96:99], v[160:163], v[104:107]\n\t" \
  "ds_read_b128 v[144:147], v242 offset:16384\n\t" \
  "v_mfma_f32_16x16x32_bf16 v[104:107], a[104:107], v[164:167], v[104:107]\n\t" \
  "ds_read_b128 v[148:151], v242 offset:17408\n\t" \
  "v_mfma_f32_16x16x32_bf16 v[104:107], a[112:115], v[168:171], v[104:107]\n\t" \
  "ds_read_b128 v[152:155], v242 offset:18432\n\t" \
  "v_mfma_f32_16x16x32_bf16 v[104:107], a[120:123], v[172:175], v[104:107]\n\t" \
  "ds_read_b128 v[156:159], v242 offset:19456\n\t" \
  "v_mfma_f32_16x16x32_bf16 v[120:123], a[124:127], v[172:175], v[120:123]\n\t" \
  "v_mfma_f32_16x16x32_bf16 v[120:123], a[116:119], v[168:171], v[120:123]\n\t" \
  "v_mfma_f32_16x16x32_bf16 v[120:123], a[108:111], v[164:167], v[120:123]\n\t" \
  "v_mfma_f32_16x16x32_bf16 v[120:123], a[100:103], v[160:163], v[120:123]\n\t" \
  "s_waitcnt lgkmcnt(4)\n\t" \
  "v_mfma_f32_16x16x32_bf16 v[108:111], a[96:99], v[128:131], v[108:111]\n\t" \
  "s_cmp_lg_u32 s88, 0\n\t" \
  "s_cbranch_scc1 .Lat_m3\n\t" \
  ".Lat_n3:\n\t" \
  "v_mfma_f32_16x16x32_bf16 v[108:111], a[104:107], v[132:135], v[108:111]\n\t" \
  "s_add_u32 m0, s38, 32768\n\t" \
  "ds_read_b128 v[160:163], v242 offset:20480\n\t" \
  "s_mov_b32 s96, 0\n\t" \
  "v_mfma_f32_16x16x32_bf16 v[108:111], a[112:115], v[136:139], v[108:111]\n\t" \
  "buffer_load_dwordx4 v252, s[28:31], s96 offen offset:0 nt lds\n\t" \
  "v_mfma_f32_16x16x32_bf16 v[108:111], a[120:123], v[140:143], v[108:111]\n\t" \
  "ds_read_b128 v[164:167], v242 offset:21504\n\t" \
  "s_mov_b32 s96, 3072\n\t" \
  "v_mfma_f32_16x16x32_bf16 v[124:127], a[124:127], v[140:143], v[124:127]\n\t" \
  "buffer_load_dwordx4 v252, s[28:31], s96 offen offset:1024 nt lds\n\t" \
  "v_mfma_f32_16x16x32_bf16 v[124:127], a[116:119], v[136:139], v[124:127]\n\t" \
  "ds_read_b128 v[168:171], v242 offset:22528\n\t" \
  "s_mov_b32 s96, 6144\n\t" \
  "v_mfma_f32_16x16x32_bf16 v[124:127], a[108:111], v[132:135], v[124:127]\n\t" \
  "buffer_load_dwordx4 v252, s[28:31], s96 offen offset:2048 nt lds\n\t" \
  "v_mfma_f32_16x16x32_bf16 v[124:127], a[100:103], v[128:131], v[124:127]\n\t" \
  "ds_read_b128 v[172:175], v242 offset:23552\n\t" \
  "s_mov_b32 s96, 9216\n\t" \
  "v_mfma_f32_16x16x32_bf16 v[96:99], a[130:133], v[32:35], v[96:99]\n\t" \
  "buffer_load_dwordx4 v252, s[28:31], s96 offen offset:3072 nt lds\n\t" \
  "v_mfma_f32_16x16x32_bf16 v[100:103], a[130:133], v[36:39], v[100:103]\n\t" \
  "s_add_u32 m0, s38, 49152\n\t" \
  "s_mov_b32 s96, 16384\n\t" \
  "v_mfma_f32_16x16x32_bf16 v[104:107], a[130:133], v[40:43], v[104:107]\n\t" \
  "buffer_load_dwordx4 v252, s[28:31], s96 offen offset:0 nt lds\n\t" \
  "v_mfma_f32_16x16x32_bf16 v[108:111], a[130:133], v[44:47], v[108:111]\n\t" \
  "s_mov_b32 s96, 19456\n\t" \
  "v_mfma_f32_16x16x32_bf16 v[112:115], a[128:131], v[32:35], v[112:115]\n\t" \
  "buffer_load_dwordx4 v252, s[28:31], s96 offen offset:1024 nt lds\n\t" \
  "v_mfma_f32_16x16x32_bf16 v[116:119], a[128:131], v[36:39], v[116:119]\n\t" \
  "s_mov_b32 s96, 22528\n\t" \
  "v_mfma_f32_16x16x32_bf16 v[120:123], a[128:131], v[40:43], v[120:123]\n\t" \
  "buffer_load_dwordx4 v252, s[28:31], s96 offen offset:2048 nt lds\n\t" \
  "v_mfma_f32_16x16x32_bf16 v[124:127], a[128:131], v[44:47], v[124:127]\n\t" \
  "s_waitcnt lgkmcnt(4)\n\t" \
  "s_mov_b32 s96, 25600\n\t" \
  "v_mfma_f32_16x16x32_bf16 v[96:99], a[192:195], v[144:147], v[96:99]\n\t" \
  "buffer_load_dwordx4 v252, s[28:31], s96 offen offset:3072 nt lds\n\t" \
  "v_mfma_f32_16x16x32_bf16 v[96:99], a[200:203], v[148:151], v[96:99]\n\t" \
  "ds_read_b128 v[128:131], v242 offset:24576\n\t" \
  "v_mfma_f32_16x16x32_bf16 v[96:99], a[208:211], v[152:155], v[96:99]\n\t" \
  "ds_read_b128 v[132:135], v242 offset:25600\n\t" \
  "v_mfma_f32_16x16x32_bf16 v[96:99], a[216:219], v[156:159], v[96:99]\n\t" \
  "ds_read_b128 v[136:139], v242 offset:26624\n\t" \
  "v_mfma_f32_16x16x32_bf16 v[112:115], a[220:223], v[156:159], v[112:115]\n\t" \
  "ds_read_b128 v[140:143], v242 offset:27648\n\t" \
  "v_mfma_f32_16x16x32_bf16 v[112:115], a[212:215], v[152:155], v[112:115]\n\t" \
  "v_mfma_f32_16x16x32_bf16 v[112:115], a[204:207], v[148:151], v[112:115]\n\t" \
  "v_mfma_f32_16x16x32_bf16 v[112:115], a[196:199], v[144:147], v[112:115]\n\t" \
  "v_mfma_f32_16x16x32_bf16 v[96:99], a[130:133], v[48:51], v[96:99]\n\t" \
  "v_mfma_f32_16x16x32_bf16 v[100:103], a[130:133], v[52:55], v[100:103]\n\t" \
  "v_mfma_f32_16x16x32_bf16 v[104:107], a[130:133], v[56:59], v[104:107]\n\t" \
  "v_mfma_f32_16x16x32_bf16 v[108:111], a[130:133], v[60:63], v[108:111]\n\t" \
  "v_mfma_f32_16x16x32_bf16 v[112:115], a[128:131], v[48:51], v[112:115]\n\t" \
  "v_mfma_f32_16x16x32_bf16 v[116:119], a[128:131], v[52:55], v[116:119]\n\t" \
  "v_mfma_f32_16x16x32_bf16 v[120:123], a[128:131], v[56:59], v[120:123]\n\t" \
  "v_mfma_f32_16x16x32_bf16 v[124:127], a[128:131], v[60:63], v[124:127]\n\t" \
  "s_waitcnt lgkmcnt(4)\n\t" \
  "v_mfma_f32_16x16x32_bf16 v[100:103], a[192:195], v[160:163], v[100:103]\n\t" \
  "ds_read_b128 v[144:147], v242 offset:28672\n\t" \
  "v_mfma_f32_16x16x32_bf16 v[100:103], a[200:203], v[164:167], v[100:103]\n\t" \
  "ds_read_b128 v[148:151], v242 offset:29696\n\t" \
  "v_mfma_f32_16x16x32_bf16 v[100:103], a[208:211], v[168:171], v[100:103]\n\t" \
  "ds_read_b128 v[152:155], v242 offset:30720\n\t" \
  "v_mfma_f32_16x16x32_bf16 v[100:103], a[216:219], v[172:175], v[100:103]\n\t" \
  "ds_read_b128 v[156:159], v242 offset:31744\n\t" \
  "v_mfma_f32_16x16x32_bf16 v[116:119], a[220:223], v[172:175], v[116:119]\n\t" \
  "v_mfma_f32_16x16x32_bf16 v[116:119], a[212:215], v[168:171], v[116:119]\n\t" \
  "v_mfma_f32_16x16x32_bf16 v[116:119], a[204:207], v[164:167], v[116:119]\n\t" \
  "v_mfma_f32_16x16x32_bf16 v[116:119], a[196:199], v[160:163], v[116:119]\n\t" \
  "s_waitcnt lgkmcnt(4)\n\t" \
  "v_mfma_f32_16x16x32_bf16 v[104:107], a[192:195], v[128:131], v[104:107]\n\t" \
  "ds_read_b128 v[160:163], v237 offset:2560\n\t" \
  "v_mfma_f32_16x16x32_bf16 v[104:107], a[200:203], v[132:135], v[104:107]\n\t" \
  "ds_read_b128 v[164:167], v237 offset:2624\n\t" \
  "v_mfma_f32_16x16x32_bf16 v[104:107], a[208:211], v[136:139], v[104:107]\n\t" \
  "v_mfma_f32_16x16x32_bf16 v[104:107], a[216:219], v[140:143], v[104:107]\n\t" \
  "v_mfma_f32_16x16x32_bf16 v[120:123], a[220:223], v[140:143], v[120:123]\n\t" \
  "v_mfma_f32_16x16x32_bf16 v[120:123], a[212:215], v[136:139], v[120:123]\n\t" \
  "v_mfma_f32_16x16x32_bf16 v[120:123], a[204:207], v[132:135], v[120:123]\n\t" \
  "v_mfma_f32_16x16x32_bf16 v[120:123], a[196:199], v[128:131], v[120:123]\n\t" \
  "s_waitcnt lgkmcnt(2)\n\t" \
  "v_mfma_f32_16x16x32_bf16 v[108:111], a[192:195], v[144:147], v[108:111]\n\t" \
  "v_mfma_f32_16x16x32_bf16 v[108:111], a[200:203], v[148:151], v[108:111]\n\t" \
  "v_mfma_f32_16x16x32_bf16 v[108:111], a[208:211], v[152:155], v[108:111]\n\t" \
  "v_mfma_f32_16x16x32_bf16 v[108:111], a[216:219], v[156:159], v[108:111]\n\t" \
  "v_mfma_f32_16x16x32_bf16 v[124:127], a[220:223], v[156:159], v[124:127]\n\t" \
  "v_mfma_f32_16x16x32_bf16 v[124:127], a[212:215], v[152:155], v[124:127]\n\t" \
  "v_mfma_f32_16x16x32_bf16 v[124:127], a[204:207], v[148:151], v[124:127]\n\t" \
  "v_mfma_f32_16x16x32_bf16 v[124:127], a[196:199], v[144:147], v[124:127]\n\t" \
  STP("s_memrealtime s[94:95]\n\t") \
  STP("s_memtime s[92:93]\n\t") \
  "s_cmp_lg_u32 s88, 0\n\t" \
  "s_cbranch_scc1 .Lat_s99\n\t" \
  ".Lat_d2:\n\t" \
  "s_waitcnt vmcnt(8)\n\t" \
  "v_mfma_f32_16x16x32_bf16 v[96:99], a[130:133], v[64:67], v[96:99]\n\t" \
  "v_mfma_f32_16x16x32_bf16 v[100:103], a[130:133], v[68:71], v[100:103]\n\t" \
  "v_mfma_f32_16x16x32_bf16 v[104:107], a[130:133], v[72:75], v[104:107]\n\t" \
  "v_mfma_f32_16x16x32_bf16 v[108:111], a[130:133], v[76:79], v[108:111]\n\t" \
  "v_mfma_f32_16x16x32_bf16 v[112:115], a[128:131], v[64:67], v[112:115]\n\t" \
  "v_mfma_f32_16x16x32_bf16 v[116:119], a[128:131], v[68:71], v[116:119]\n\t" \
  "v_mfma_f32_16x16x32_bf16 v[120:123], a[128:131], v[72:75], v[120:123]\n\t" \
  "v_mfma_f32_16x16x32_bf16 v[124:127], a[128:131], v[76:79], v[124:127]\n\t" \
  "s_waitcnt lgkmcnt(0)\n\t" \
  STP("v_writelane_b32 v255, s94, 38\n\t") \
  STP("v_writelane_b32 v255, s92, 39\n\t") \
  "v_pk_fma_f32 v[0:1], v[160:161], v[96:97], v[0:1]\n\t" \
  "v_pk_fma_f32 v[4:5], v[160:161], v[100:101], v[4:5]\n\t" \
  "v_pk_fma_f32 v[8:9], v[160:161], v[104:105], v[8:9]\n\t" \
  "v_pk_fma_f32 v[12:13], v[160:161], v[108:109], v[12:13]\n\t" \
  "v_pk_fma_f32 v[2:3], v[162:163], v[98:99], v[2:3]\n\t" \
  "v_pk_fma_f32 v[6:7], v[162:163], v[102:103], v[6:7]\n\t" \
  "v_pk_fma_f32 v[10:11], v[162:163], v[106:107], v[10:11]\n\t" \
  "v_pk_fma_f32 v[14:15], v[162:163], v[110:111], v[14:15]\n\t" \
  "v_pk_fma_f32 v[16:17], v[164:165], v[112:113], v[16:17]\n\t" \
  "v_pk_fma_f32 v[20:21], v[164:165], v[116:117], v[20:21]\n\t" \
  "v_pk_fma_f32 v[24:25], v[164:165], v[120:121], v[24:25]\n\t" \
  "v_pk_fma_f32 v[28:29], v[164:165], v[124:125], v[28:29]\n\t" \
  "v_pk_fma_f32 v[18:19], v[166:167], v[114:115], v[18:19]\n\t" \
  "v_pk_fma_f32 v[22:23], v[166:167], v[118:119], v[22:23]\n\t" \
  "v_pk_fma_f32 v[26:27], v[166:167], v[122:123], v[26:27]\n\t" \
  "v_pk_fma_f32 v[30:31], v[166:167], v[126:127], v[30:31]\n\t" \
  "v_cvt_pk_bf16_f32 v128, v0, v1\n\t" \
  "v_cvt_pk_bf16_f32 v132, v4, v5\n\t" \
  "v_cvt_pk_bf16_f32 v136, v8, v9\n\t" \
  "v_cvt_pk_bf16_f32 v140, v12, v13\n\t" \
  "v_cvt_pk_bf16_f32 v129, v2, v3\n\t" \
  "v_cvt_pk_bf16_f32 v133, v6, v7\n\t" \
  "v_cvt_pk_bf16_f32 v137, v10, v11\n\t" \
  "v_cvt_pk_bf16_f32 v141, v14, v15\n\t" \
  "v_cvt_pk_bf16_f32 v130, v16, v17\n\t" \
  "v_cvt_pk_bf16_f32 v134, v20, v21\n\t" \
  "v_cvt_pk_bf16_f32 v138, v24, v25\n\t" \
  "v_cvt_pk_bf16_f32 v142, v28, v29\n\t" \
  "v_cvt_pk_bf16_f32 v131, v18, v19\n\t" \
  "v_cvt_pk_bf16_f32 v135, v22, v23\n\t" \
  "v_cvt_pk_bf16_f32 v139, v26, v27\n\t" \
  "v_cvt_pk_bf16_f32 v143, v30, v31\n\t" \
  "v_dot2_f32_bf16 v64, v128, v128, 0\n\t" \
  "v_dot2_f32_bf16 v65, v132, v132, 0\n\t" \
  "v_dot2_f32_bf16 v66, v136, v136, 0\n\t" \
  "v_dot2_f32_bf16 v67, v140, v140, 0\n\t" \
  "v_dot2_f32_bf16 v64, v129, v129, v64\n\t" \
  "v_dot2_f32_bf16 v65, v133, v133, v65\n\t" \
  "v_dot2_f32_bf16 v66, v137, v137, v66\n\t" \
  "v_dot2_f32_bf16 v67, v141, v141, v67\n\t" \
  "v_dot2_f32_bf16 v64, v130, v130, v64\n\t" \
  "v_dot2_f32_bf16 v65, v134, v134, v65\n\t" \
  "v_dot2_f32_bf16 v66, v138, v138, v66\n\t" \
  "v_dot2_f32_bf16 v67, v142, v142, v67\n\t" \
  "v_dot2_f32_bf16 v64, v131, v131, v64\n\t" \
  "v_dot2_f32_bf16 v65, v135, v135, v65\n\t" \
  "v_dot2_f32_bf16 v66, v139, v139, v66\n\t" \
  "v_dot2_f32_bf16 v67, v143, v143, v67\n\t" \
  "v_mfma_f32_16x16x32_bf16 v[68:71], a[176:179], v[128:131], 0\n\t" \
  "v_mfma_f32_16x16x32_bf16 v[68:71], a[180:183], v[132:135], v[68:71]\n\t" \
  "v_permlane32_swap_b32 v64, v66\n\t" \
  "v_mfma_f32_16x16x32_bf16 v[68:71], a[184:187], v[136:139], v[68:71]\n\t" \
  "v_permlane32_swap_b32 v65, v67\n\t" \
  "v_mfma_f32_16x16x32_bf16 v[68:71], a[188:191], v[140:143], v[68:71]\n\t" \
  "v_add_f32 v64, v64, v66\n\t" \
  "v_add_f32 v65, v65, v67\n\t" \
  "v_mov_b32 v73, s36\n\t" \
  "v_mov_b32 v75, s36\n\t" \
  "v_permlane16_swap_b32 v64, v65\n\t" \
  "s_mov_b32 s81, 0\n\t" \
  "s_nop 1\n\t" \
  "v_add_f32 v70, v69, v70\n\t" \
  "v_add_f32 v69, v64, v65\n\t" \
  "v_mov_b32 v71, v238\n\t" \
  "ds_write_b128 v224, v[68:71]\n\t" \
  "s_waitcnt lgkmcnt(0)\n\t" \
  "s_barrier\n\t" \
  "ds_read_b128 v[96:99], v242 offset:0\n\t" \
  "ds_read_b128 v[100:103], v242 offset:1024\n\t" \
  "ds_read_b128 v[104:107], v242 offset:2048\n\t" \
  "ds_read_b128 v[108:111], v242 offset:3072\n\t" \
  "s_cmp_eq_u32 s3, 0\n\t" \
  "s_cselect_b64 vcc, -1, 0\n\t" \
  "s_waitcnt lgkmcnt(2)\n\t" \
  "v_pk_add_f32 v[96:97], v[96:97], v[100:101]\n\t" \
  "v_pk_add_f32 v[98:99], v[98:99], v[102:103]\n\t" \
  "s_waitcnt lgkmcnt(0)\n\t" \
  "v_pk_add_f32 v[104:105], v[104:105], v[108:109]\n\t" \
  "v_pk_add_f32 v[106:107], v[106:107], v[110:111]\n\t" \
  "v_pk_add_f32 v[64:65], v[96:97], v[104:105]\n\t" \
  "v_pk_add_f32 v[66:67], v[98:99], v[106:107]\n\t" \
  "v_cndmask_b32 v72, v66, v64, vcc\n\t" \
  "v_cndmask_b32 v74, v67, v65, vcc\n\t" \
  "buffer_store_dwordx4 v[72:75], v240, s[16:19], s10 offen sc1\n\t" \
  STP("s_memrealtime s[94:95]\n\t") \
  STP("s_memtime s[92:93]\n\t") \
  STP("s_waitcnt lgkmcnt(0)\n\t") \
  STP("v_writelane_b32 v255, s94, 40\n\t") \
  STP("v_writelane_b32 v255, s92, 41\n\t") \
  "buffer_load_dwordx4 v[96:99], v242, s[16:19], s11 offen offset:0 sc1\n\t" \
  "buffer_load_dwordx4 v[100:103], v242, s[16:19], s11 offen offset:1024 sc1\n\t" \
  "buffer_load_dwordx4 v[104:107], v242, s[16:19], s12 offen offset:0 sc1\n\t" \
  "buffer_load_dwordx4 v[108:111], v242, s[16:19], s12 offen offset:1024 sc1\n\t" \
  "ds_read_b128 v[144:147], v237 offset:2688\n\t" \
  "ds_read_b128 v[148:151], v237 offset:2752\n\t" \
  "ds_read_b128 v[152:155], v237 offset:2816\n\t" \
  "ds_read_b128 v[156:159], v237 offset:2880\n\t" \
  "ds_read_b128 v[168:171], v237 offset:2944\n\t" \
  "ds_read_b128 v[172:175], v237 offset:3008\n\t" \
  "v_lshlrev_b32 v0, 16, v128\n\t" \
  "v_and_b32 v1, 0xffff0000, v128\n\t" \
  "v_lshlrev_b32 v2, 16, v129\n\t" \
  "v_and_b32 v3, 0xffff0000, v129\n\t" \
  "v_lshlrev_b32 v4, 16, v130\n\t" \
  "v_and_b32 v5, 0xffff0000, v130\n\t" \
  "v_lshlrev_b32 v6, 16, v131\n\t" \
  "v_and_b32 v7, 0xffff0000, v131\n\t" \
  "v_lshlrev_b32 v8, 16, v132\n\t" \
  "v_and_b32 v9, 0xffff0000, v132\n\t" \
  "v_lshlrev_b32 v10, 16, v133\n\t" \
  "v_and_b32 v11, 0xffff0000, v133\n\t" \
  "v_lshlrev_b32 v12, 16, v134\n\t" \
  "v_and_b32 v13, 0xffff0000, v134\n\t" \
  "v_lshlrev_b32 v14, 16, v135\n\t" \
  "v_and_b32 v15, 0xffff0000, v135\n\t" \
  "v_lshlrev_b32 v16, 16, v136\n\t" \
  "v_and_b32 v17, 0xffff0000, v136\n\t" \
  "v_lshlrev_b32 v18, 16, v137\n\t" \
  "v_and_b32 v19, 0xffff0000, v137\n\t" \
  "v_lshlrev_b32 v20, 16, v138\n\t" \
  "v_and_b32 v21, 0xffff0000, v138\n\t" \
  "v_lshlrev_b32 v22, 16, v139\n\t" \
  "v_and_b32 v23, 0xffff0000, v139\n\t" \
  "v_lshlrev_b32 v24, 16, v140\n\t" \
  "v_and_b32 v25, 0xffff0000, v140\n\t" \
  "v_lshlrev_b32 v26, 16, v141\n\t" \
  "v_and_b32 v27, 0xffff0000, v141\n\t" \
  "v_lshlrev_b32 v28, 16, v142\n\t" \
  "v_and_b32 v29, 0xffff0000, v142\n\t" \
  "v_lshlrev_b32 v30, 16, v143\n\t" \
  "v_and_b32 v31, 0xffff0000, v143\n\t" \
  "v_lshrrev_b32 v72, 9, v224\n\t" \
  "v_lshlrev_b32 v246, 14, v72\n\t" \
  "v_bfe_u32 v72, v224, 7, 2\n\t" \
  "v_lshl_add_u32 v246, v72, 11, v246\n\t" \
  "v_and_b32 v72, 0x70, v242\n\t" \
  "v_add_u32 v246, v246, v72\n\t" \
  "s_and_b32 s44, s2, 3\n\t" \
  "s_and_b32 s42, s2, 0x38\n\t" \
  "s_lshl_b32 s42, s42, 6\n\t" \
  "s_lshl_b32 s44, s44, 7\n\t" \
  "s_add_u32 s91, s44, s42\n\t" \
  "s_and_b32 s84, s91, 0x7ff\n\t" \
  "s_add_u32 s84, s84, s9\n\t" \
  "s_add_u32 s84, s84, 1048576\n\t" \
  "s_add_u32 s85, s84, 131072\n\t" \
  "s_add_u32 s86, s84, 262144\n\t" \
  "s_add_u32 s87, s84, 393216\n\t" \
  "s_add_u32 s88, s84, 524288\n\t" \
  "s_add_u32 s89, s84, 655360\n\t" \
  "s_add_u32 s90, s84, 786432\n\t" \
  "s_waitcnt vmcnt(5)\n\t" \
  "ds_read2st64_b64 v[32:35], v247 offset0:64 offset1:65\n\t" \
  "ds_read2st64_b64 v[36:39], v247 offset0:66 offset1:67\n\t" \
  "ds_read2st64_b64 v[40:43], v247 offset0:68 offset1:69\n\t" \
  "ds_read2st64_b64 v[44:47], v247 offset0:70 offset1:71\n\t" \
  "ds_read2st64_b64 v[48:51], v247 offset0:96 offset1:97\n\t" \
  "ds_read2st64_b64 v[52:55], v247 offset0:98 offset1:99\n\t" \
  "ds_read2st64_b64 v[56:59], v247 offset0:100 offset1:101\n\t" \
  "ds_read2st64_b64 v[60:63], v247 offset0:102 offset1:103\n\t" \
  "s_waitcnt lgkmcnt(0)\n\t" \
  "v_perm_b32 v112, v34, v32, s40\n\t" \
  "v_perm_b32 v34, v34, v32, s41\n\t" \
  "v_perm_b32 v113, v35, v33, s40\n\t" \
  "v_perm_b32 v35, v35, v33, s41\n\t" \
  "v_perm_b32 v114, v38, v36, s40\n\t" \
  "v_perm_b32 v38, v38, v36, s41\n\t" \
  "v_perm_b32 v115, v39, v37, s40\n\t" \
  "v_perm_b32 v39, v39, v37, s41\n\t" \
  "v_perm_b32 v116, v42, v40, s40\n\t" \
  "v_perm_b32 v42, v42, v40, s41\n\t" \
  "v_perm_b32 v117, v43, v41, s40\n\t" \
  "v_perm_b32 v43, v43, v41, s41\n\t" \
  "v_perm_b32 v118, v46, v44, s40\n\t" \
  "v_perm_b32 v46, v46, v44, s41\n\t" \
  "v_perm_b32 v119, v47, v45, s40\n\t" \
  "v_perm_b32 v47, v47, v45, s41\n\t" \
  "v_perm_b32 v120, v50, v48, s40\n\t" \
  "v_perm_b32 v50, v50, v48, s41\n\t" \
  "v_perm_b32 v121, v51, v49, s40\n\t" \
  "v_perm_b32 v51, v51, v49, s41\n\t" \
  "v_perm_b32 v122, v54, v52, s40\n\t" \
  "v_perm_b32 v54, v54, v52, s41\n\t" \
  "v_perm_b32 v123, v55, v53, s40\n\t" \
  "v_perm_b32 v55, v55, v53, s41\n\t" \
  "v_perm_b32 v124, v58, v56, s40\n\t" \
  "v_perm_b32 v58, v58, v56, s41\n\t" \
  "v_perm_b32 v125, v59, v57, s40\n\t" \
  "v_perm_b32 v59, v59, v57, s41\n\t" \
  "v_perm_b32 v126, v62, v60, s40\n\t" \
  "v_perm_b32 v62, v62, v60, s41\n\t" \
  "v_perm_b32 v127, v63, v61, s40\n\t" \
  "v_perm_b32 v63, v63, v61, s41\n\t" \
  "s_cmp_lt_u32 s39, 4096\n\t" \
  "s_cselect_b64 s[52:53], -1, 0\n\t" \
  "s_waitcnt vmcnt(0)\n\t" \
  "s_branch .Ltl_w1\n\t" \
  ".Ltl_r1:\n\t" \
  "s_sleep 1\n\t" \
  "buffer_load_dwordx4 v[96:99], v242, s[16:19], s11 offen offset:0 sc1\n\t" \
  "buffer_load_dwordx4 v[100:103], v242, s[16:19], s11 offen offset:1024 sc1\n\t" \
  "buffer_load_dwordx4 v[104:107], v242, s[16:19], s12 offen offset:0 sc1\n\t" \
  "buffer_load_dwordx4 v[108:111], v242, s[16:19], s12 offen offset:1024 sc1\n\t" \
  "s_waitcnt vmcnt(0)\n\t" \
  ".Ltl_w1:\n\t" \
  "v_xor_b32 v72, s36, v97\n\t" \
  "v_bitop3_b32 v72, v72, v99, s36 bitop3:0xf6\n\t" \
  "v_bitop3_b32 v72, v72, v101, s36 bitop3:0xf6\n\t" \
  "v_bitop3_b32 v72, v72, v103, s36 bitop3:0xf6\n\t" \
  "v_bitop3_b32 v72, v72, v105, s36 bitop3:0xf6\n\t" \
  "v_bitop3_b32 v72, v72, v107, s36 bitop3:0xf6\n\t" \
  "v_bitop3_b32 v72, v72, v109, s36 bitop3:0xf6\n\t" \
  "v_bitop3_b32 v72, v72, v111, s36 bitop3:0xf6\n\t" \
  "v_cmp_ne_u32 vcc, 0, v72\n\t" \
  "v_cndmask_b32 v96, v96, v64, s[52:53]\n\t" \
  "v_cndmask_b32 v98, v98, v65, s[52:53]\n\t" \
  "v_cndmask_b32 v100, v100, v66, s[52:53]\n\t" \
  "v_cndmask_b32 v102, v102, v67, s[52:53]\n\t" \
  "v_add_f32 v68, v96, v104\n\t" \
  "v_add_f32 v69, v98, v106\n\t" \
  "v_add_f32 v70, v100, v108\n\t" \
  "v_add_f32 v71, v102, v110\n\t" \
  "s_cbranch_vccz .Ltl_k1\n\t" \
  "s_add_u32 s81, s81, 1\n\t" \
  "s_cmp_lt_u32 s81, " FQ_CAP "\n\t" \
  "s_cbranch_scc1 .Ltl_r1\n\t" \
  "buffer_store_dword v243, off, s[16:19], 0 offset:256 sc1\n\t" \
  ".Ltl_k1:\n\t" \
  "ds_write_b128 v224, v[68:71] offset:4096\n\t" \
  "s_waitcnt lgkmcnt(0)\n\t" \
  "s_barrier\n\t" \
  "ds_read_b128 v[96:99], v242 offset:4096\n\t" \
  "ds_read_b128 v[100:103], v242 offset:5120\n\t" \
  "ds_read_b128 v[104:107], v242 offset:6144\n\t" \
  "ds_read_b128 v[108:111], v242 offset:7168\n\t" \
  "buffer_load_dwordx2 v[248:249], v225, s[16:19], s13 offen sc1\n\t" \
  STP("s_memrealtime s[94:95]\n\t") \
  STP("s_memtime s[92:93]\n\t") \
  STP("s_waitcnt lgkmcnt(0)\n\t") \
  STP("v_writelane_b32 v255, s94, 42\n\t") \
  STP("v_writelane_b32 v255, s92, 43\n\t") \
  "s_waitcnt lgkmcnt(2)\n\t" \
  "v_pk_add_f32 v[96:97], v[96:97], v[100:101]\n\t" \
  "v_pk_add_f32 v[98:99], v[98:99], v[102:103]\n\t" \
  "s_waitcnt lgkmcnt(0)\n\t" \
  "v_pk_add_f32 v[104:105], v[104:105], v[108:109]\n\t" \
  "v_pk_add_f32 v[106:107], v[106:107], v[110:111]\n\t" \
  "v_pk_add_f32 v[64:65], v[96:97], v[104:105]\n\t" \
  "v_pk_add_f32 v[66:67], v[98:99], v[106:107]\n\t" \
  "v_mul_f32 v64, 0x3a800000, v64\n\t" \
  "v_mul_f32 v65, 0x3a800000, v65\n\t" \
  "v_readlane_b32 s42, v67, 0\n\t" \
  "v_readlane_b32 s43, v67, 1\n\t" \
  "v_readlane_b32 s53, v67, 2\n\t" \
  "v_fma_f32 v65, -v64, v64, v65\n\t" \
  "v_max_f32 v65, 0, v65\n\t" \
  "v_add_f32 v65, 0x3727c5ac, v65\n\t" \
  "v_rsq_f32 v68, v65\n\t" \
  "v_fma_f32 v66, -v64, s42, v66\n\t" \
  "v_fma_f32 v66, v68, v66, s43\n\t" \
  "v_cvt_pk_bf16_f32 v66, v66, v66\n\t" \
  "v_ashrrev_i32 v67, 31, v66\n\t" \
  "v_or_b32 v67, 0x80000000, v67\n\t" \
  "v_xor_b32 v66, v66, v67\n\t" \
  "v_and_b32 v66, 0xffff0000, v66\n\t" \
  "v_or_b32 v66, v66, v227\n\t" \
  "s_nop 1\n\t" \
  "v_max_u32_dpp v66, v66, v66 quad_perm:[1,0,3,2] row_mask:0xf bank_mask:0xf\n\t" \
  "v_mul_f32_e64 v70, v68, -v64\n\t" \
  "v_mov_b32 v65, s36\n\t" \
  "v_max_u32_dpp v66, v66, v66 quad_perm:[2,3,0,1] row_mask:0xf bank_mask:0xf\n\t" \
  "v_mov_b32 v97, s36\n\t" \
  "v_mov_b32 v99, s36\n\t" \
  "v_max_u32_dpp v66, v66, v66 row_half_mirror row_mask:0xf bank_mask:0xf\n\t" \
  "v_mov_b32 v101, s36\n\t" \
  "v_mov_b32 v103, s36\n\t" \
  "v_max_u32_dpp v66, v66, v66 row_mirror row_mask:0xf bank_mask:0xf\n\t" \
  "v_mov_b32 v105, s36\n\t" \
  "v_mov_b32 v107, s36\n\t" \
  "v_readlane_b32 s46, v66, 0\n\t" \
  "v_readlane_b32 s42, v66, 16\n\t" \
  "v_readlane_b32 s43, v66, 32\n\t" \
  "v_readlane_b32 s44, v66, 48\n\t" \
  "s_max_u32 s46, s46, s42\n\t" \
  "s_max_u32 s43, s43, s44\n\t" \
  "s_max_u32 s46, s46, s43\n\t" \
  "v_mov_b32 v64, s46\n\t" \
  "buffer_store_dwordx2 v[64:65], v244, s[16:19], s14 offen sc1\n\t" \
  STP("s_memrealtime s[94:95]\n\t") \
  STP("s_memtime s[92:93]\n\t") \
  STP("s_waitcnt lgkmcnt(0)\n\t") \
  STP("v_writelane_b32 v255, s94, 44\n\t") \
  STP("v_writelane_b32 v255, s92, 45\n\t") \
  "s_not_b32 s48, s46\n\t" \
  "s_and_b32 s48, s48, 63\n\t" \
  "s_and_b32 s44, s48, 15\n\t" \
  "s_lshl_b32 s50, 0x10001, s44\n\t" \
  "s_mov_b32 s51, s50\n\t" \
  "v_readlane_b32 s52, v68, s48\n\t" \
  "v_readlane_b32 s54, v70, s48\n\t" \
  "s_lshr_b32 s48, s48, 4\n\t" \
  "s_lshl_b32 s44, s48, 2\n\t" \
  "v_mov_b32 v109, s36\n\t" \
  "v_mov_b32 v111, s36\n\t" \
  "s_set_gpr_idx_on s44, gpr_idx(SRC0)\n\t" \
  "v_mov_b32 v104, v128\n\t" \
  "v_mov_b32 v106, v129\n\t" \
  "v_mov_b32 v108, v130\n\t" \
  "v_mov_b32 v110, v131\n\t" \
  "s_set_gpr_idx_off\n\t" \
  "v_mov_b32 v66, s54\n\t" \
  "v_lshlrev_b32 v160, 16, v104\n\t" \
  "v_and_b32 v161, 0xffff0000, v104\n\t" \
  "v_lshlrev_b32 v162, 16, v106\n\t" \
  "v_and_b32 v163, 0xffff0000, v106\n\t" \
  "v_lshlrev_b32 v164, 16, v108\n\t" \
  "v_and_b32 v165, 0xffff0000, v108\n\t" \
  "v_lshlrev_b32 v166, 16, v110\n\t" \
  "v_and_b32 v167, 0xffff0000, v110\n\t" \
  "v_fma_f32 v160, v160, s52, v66\n\t" \
  "v_fma_f32 v161, v161, s52, v66\n\t" \
  "v_fma_f32 v162, v162, s52, v66\n\t" \
  "v_fma_f32 v163, v163, s52, v66\n\t" \
  "v_fma_f32 v164, v164, s52, v66\n\t" \
  "v_fma_f32 v165, v165, s52, v66\n\t" \
  "v_fma_f32 v166, v166, s52, v66\n\t" \
  "v_fma_f32 v167, v167, s52, v66\n\t" \
  "v_pk_fma_f32 v[160:161], v[160:161], v[152:153], v[144:145]\n\t" \
  "v_pk_fma_f32 v[162:163], v[162:163], v[154:155], v[146:147]\n\t" \
  "v_pk_fma_f32 v[164:165], v[164:165], v[156:157], v[148:149]\n\t" \
  "v_pk_fma_f32 v[166:167], v[166:167], v[158:159], v[150:151]\n\t" \
  "v_cvt_pk_bf16_f32 v96, v160, v161\n\t" \
  "v_cvt_pk_bf16_f32 v98, v162, v163\n\t" \
  ".Lhs16:\n\ts_cbranch_execz .Lhs16n\n\t" \
  "v_cvt_pk_bf16_f32 v100, v164, v165\n\t" \
  "v_cvt_pk_bf16_f32 v102, v166, v167\n\t" \
  "s_waitcnt vmcnt(1)\n\t" \
  "v_cmp_eq_u32 vcc, s36, v249\n\t" \
  "v_mov_b32 v69, v68\n\t" \
  "v_mov_b32 v71, v70\n\t" \
  "v_cndmask_b32 v64, 0, v248, vcc\n\t" \
  "s_nop 1\n\t" \
  "v_max_u32_dpp v64, v64, v64 quad_perm:[1,0,3,2] row_mask:0xf bank_mask:0xf\n\t" \
  "v_permlane32_swap_b32 v68, v69\n\t" \
  "v_permlane32_swap_b32 v70, v71\n\t" \
  "v_max_u32_dpp v64, v64, v64 quad_perm:[2,3,0,1] row_mask:0xf bank_mask:0xf\n\t" \
  "v_mov_b32 v160, v68\n\t" \
  "v_mov_b32 v161, v69\n\t" \
  "v_max_u32_dpp v64, v64, v64 row_half_mirror row_mask:0xf bank_mask:0xf\n\t" \
  "v_mov_b32 v162, v70\n\t" \
  "v_mov_b32 v163, v71\n\t" \
  "v_max_u32_dpp v64, v64, v64 row_mirror row_mask:0xf bank_mask:0xf\n\t" \
  "s_nop 0\n\t" \
  "v_readlane_b32 s45, v64, 0\n\t" \
  "v_readlane_b32 s42, v64, 16\n\t" \
  "s_max_u32 s45, s45, s42\n\t" \
  "s_cmp_ge_u32 s46, s45\n\t" \
  "s_cselect_b32 s49, 1, 0\n\t" \
  "s_max_u32 s45, s45, s46\n\t" \
  "s_not_b32 s42, s45\n\t" \
  "s_bfe_u32 s47, s42, 0x50006\n\t" \
  "s_lshl_b32 s43, s42, 7\n\t" \
  "s_and_b32 s43, s43, 0x3c000\n\t" \
  "s_lshl_b32 s42, s42, 6\n\t" \
  "s_and_b32 s42, s42, 0x1000\n\t" \
  "s_add_u32 s42, s42, s43\n\t" \
  "s_add_u32 s42, s42, s15\n\t" \
  "buffer_load_dwordx2 v[248:249], v225, s[16:19], s13 offen sc1\n\t" \
  "buffer_load_dwordx4 v[72:75], v226, s[16:19], s42 offen offset:0 sc1\n\t" \
  "buffer_load_dwordx4 v[76:79], v226, s[16:19], s42 offen offset:16 sc1\n\t" \
  "s_cmp_eq_u32 s49, 0\n\t" \
  "s_cbranch_scc1 .Ltl_nc\n\t" \
  "s_xor_b32 s43, s20, 8192\n\t" \
  "s_mov_b64 exec, s[50:51]\n\t" \
  "buffer_store_dwordx4 v[96:99], v228, s[16:19], s20 offen offset:0 sc1\n\t" \
  "buffer_store_dwordx4 v[100:103], v228, s[16:19], s20 offen offset:64 sc1\n\t" \
  "buffer_store_dwordx4 v[96:99], v228, s[16:19], s43 offen offset:0 sc1\n\t" \
  "buffer_store_dwordx4 v[100:103], v228, s[16:19], s43 offen offset:64 sc1\n\t" \
  "buffer_store_dwordx4 v[104:107], v228, s[16:19], s21 offen offset:0 sc1\n\t" \
  "buffer_store_dwordx4 v[108:111], v228, s[16:19], s21 offen offset:64 sc1\n\t" \
  "s_mov_b64 exec, -1\n\t" \
  ".Ltl_nc:\n\t" \
  "v_permlane16_swap_b32 v68, v160\n\t" \
  "v_permlane16_swap_b32 v69, v161\n\t" \
  "v_permlane16_swap_b32 v70, v162\n\t" \
  "v_permlane16_swap_b32 v71, v163\n\t" \
  "v_pk_fma_f32 v[96:97], v[0:1], v[68:69], v[70:71] op_sel:[0,0,0] op_sel_hi:[1,0,0]\n\t" \
  "v_pk_fma_f32 v[98:99], v[2:3], v[68:69], v[70:71] op_sel:[0,0,0] op_sel_hi:[1,0,0]\n\t" \
  "v_pk_fma_f32 v[100:101], v[4:5], v[68:69], v[70:71] op_sel:[0,0,0] op_sel_hi:[1,0,0]\n\t" \
  "v_pk_fma_f32 v[102:103], v[6:7], v[68:69], v[70:71] op_sel:[0,0,0] op_sel_hi:[1,0,0]\n\t" \
  "v_pk_fma_f32 v[96:97], v[96:97], v[152:153], v[144:145]\n\t" \
  "v_pk_fma_f32 v[98:99], v[98:99], v[154:155], v[146:147]\n\t" \
  "v_pk_fma_f32 v[100:101], v[100:101], v[156:157], v[148:149]\n\t" \
  "v_pk_fma_f32 v[102:103], v[102:103], v[158:159], v[150:151]\n\t" \
  "v_cvt_pk_bf16_f32 v97, v96, v97\n\t" \
  "v_cvt_pk_bf16_f32 v99, v98, v99\n\t" \
  "v_cvt_pk_bf16_f32 v101, v100, v101\n\t" \
  "v_cvt_pk_bf16_f32 v103, v102, v103\n\t" \
  "v_lshlrev_b32 v96, 16, v97\n\t" \
  "v_and_b32 v97, 0xffff0000, v97\n\t" \
  "v_lshlrev_b32 v98, 16, v99\n\t" \
  "v_and_b32 v99, 0xffff0000, v99\n\t" \
  "v_lshlrev_b32 v100, 16, v101\n\t" \
  "v_and_b32 v101, 0xffff0000, v101\n\t" \
  "v_lshlrev_b32 v102, 16, v103\n\t" \
  "v_and_b32 v103, 0xffff0000, v103\n\t" \
  "v_pk_fma_f32 v[96:97], v[168:169], v[96:97], v[0:1]\n\t" \
  "v_pk_fma_f32 v[98:99], v[170:171], v[98:99], v[2:3]\n\t" \
  "v_pk_fma_f32 v[100:101], v[172:173], v[100:101], v[4:5]\n\t" \
  "v_pk_fma_f32 v[102:103], v[174:175], v[102:103], v[6:7]\n\t" \
  "v_cvt_pk_bf16_f32 v128, v96, v97\n\t" \
  "v_cvt_pk_bf16_f32 v129, v98, v99\n\t" \
  "v_cvt_pk_bf16_f32 v130, v100, v101\n\t" \
  "v_cvt_pk_bf16_f32 v131, v102, v103\n\t" \
  "v_pk_fma_f32 v[104:105], v[8:9], v[160:161], v[162:163] op_sel:[0,0,0] op_sel_hi:[1,0,0]\n\t" \
  "v_pk_fma_f32 v[106:107], v[10:11], v[160:161], v[162:163] op_sel:[0,0,0] op_sel_hi:[1,0,0]\n\t" \
  "v_pk_fma_f32 v[108:109], v[12:13], v[160:161], v[162:163] op_sel:[0,0,0] op_sel_hi:[1,0,0]\n\t" \
  "v_pk_fma_f32 v[110:111], v[14:15], v[160:161], v[162:163] op_sel:[0,0,0] op_sel_hi:[1,0,0]\n\t" \
  "v_pk_fma_f32 v[104:105], v[104:105], v[152:153], v[144:145]\n\t" \
  "v_pk_fma_f32 v[106:107], v[106:107], v[154:155], v[146:147]\n\t" \
  "v_pk_fma_f32 v[108:109], v[108:109], v[156:157], v[148:149]\n\t" \
  "v_pk_fma_f32 v[110:111], v[110:111], v[158:159], v[150:151]\n\t" \
  "v_cvt_pk_bf16_f32 v105, v104, v105\n\t" \
  "v_cvt_pk_bf16_f32 v107, v106, v107\n\t" \
  "v_cvt_pk_bf16_f32 v109, v108, v109\n\t" \
  "v_cvt_pk_bf16_f32 v111, v110, v111\n\t" \
  "v_lshlrev_b32 v104, 16, v105\n\t" \
  "v_and_b32 v105, 0xffff0000, v105\n\t" \
  "v_lshlrev_b32 v106, 16, v107\n\t" \
  "v_and_b32 v107, 0xffff0000, v107\n\t" \
  "v_lshlrev_b32 v108, 16, v109\n\t" \
  "v_and_b32 v109, 0xffff0000, v109\n\t" \
  "v_lshlrev_b32 v110, 16, v111\n\t" \
  "v_and_b32 v111, 0xffff0000, v111\n\t" \
  "v_pk_fma_f32 v[104:105], v[168:169], v[104:105], v[8:9]\n\t" \
  "v_pk_fma_f32 v[106:107], v[170:171], v[106:107], v[10:11]\n\t" \
  "v_pk_fma_f32 v[108:109], v[172:173], v[108:109], v[12:13]\n\t" \
  "v_pk_fma_f32 v[110:111], v[174:175], v[110:111], v[14:15]\n\t" \
  "v_cvt_pk_bf16_f32 v132, v104, v105\n\t" \
  "v_cvt_pk_bf16_f32 v133, v106, v107\n\t" \
  "v_cvt_pk_bf16_f32 v134, v108, v109\n\t" \
  "v_cvt_pk_bf16_f32 v135, v110, v111\n\t" \
  "v_pk_fma_f32 v[96:97], v[16:17], v[68:69], v[70:71] op_sel:[0,1,1] op_sel_hi:[1,1,1]\n\t" \
  "v_pk_fma_f32 v[98:99], v[18:19], v[68:69], v[70:71] op_sel:[0,1,1] op_sel_hi:[1,1,1]\n\t" \
  "v_pk_fma_f32 v[100:101], v[20:21], v[68:69], v[70:71] op_sel:[0,1,1] op_sel_hi:[1,1,1]\n\t" \
  "v_pk_fma_f32 v[102:103], v[22:23], v[68:69], v[70:71] op_sel:[0,1,1] op_sel_hi:[1,1,1]\n\t" \
  "v_pk_fma_f32 v[96:97], v[96:97], v[152:153], v[144:145]\n\t" \
  "v_pk_fma_f32 v[98:99], v[98:99], v[154:155], v[146:147]\n\t" \
  "v_pk_fma_f32 v[100:101], v[100:101], v[156:157], v[148:149]\n\t" \
  "v_pk_fma_f32 v[102:103], v[102:103], v[158:159], v[150:151]\n\t" \
  "v_cvt_pk_bf16_f32 v97, v96, v97\n\t" \
  "v_cvt_pk_bf16_f32 v99, v98, v99\n\t" \
  "v_cvt_pk_bf16_f32 v101, v100, v101\n\t" \
  "v_cvt_pk_bf16_f32 v103, v102, v103\n\t" \
  "v_lshlrev_b32 v96, 16, v97\n\t" \
  "v_and_b32 v97, 0xffff0000, v97\n\t" \
  "v_lshlrev_b32 v98, 16, v99\n\t" \
  "v_and_b32 v99, 0xffff0000, v99\n\t" \
  "v_lshlrev_b32 v100, 16, v101\n\t" \
  "v_and_b32 v101, 0xffff0000, v101\n\t" \
  "v_lshlrev_b32 v102, 16, v103\n\t" \
  "v_and_b32 v103, 0xffff0000, v103\n\t" \
  "v_pk_fma_f32 v[96:97], v[168:169], v[96:97], v[16:17]\n\t" \
  "v_pk_fma_f32 v[98:99], v[170:171], v[98:99], v[18:19]\n\t" \
  "v_pk_fma_f32 v[100:101], v[172:173], v[100:101], v[20:21]\n\t" \
  "v_pk_fma_f32 v[102:103], v[174:175], v[102:103], v[22:23]\n\t" \
  "v_cvt_pk_bf16_f32 v136, v96, v97\n\t" \
  "v_cvt_pk_bf16_f32 v137, v98, v99\n\t" \
  "v_cvt_pk_bf16_f32 v138, v100, v101\n\t" \
  "v_cvt_pk_bf16_f32 v139, v102, v103\n\t" \
  "v_pk_fma_f32 v[104:105], v[24:25], v[160:161], v[162:163] op_sel:[0,1,1] op_sel_hi:[1,1,1]\n\t" \
  "v_pk_fma_f32 v[106:107], v[26:27], v[160:161], v[162:163] op_sel:[0,1,1] op_sel_hi:[1,1,1]\n\t" \
  "v_pk_fma_f32 v[108:109], v[28:29], v[160:161], v[162:163] op_sel:[0,1,1] op_sel_hi:[1,1,1]\n\t" \
  "v_pk_fma_f32 v[110:111], v[30:31], v[160:161], v[162:163] op_sel:[0,1,1] op_sel_hi:[1,1,1]\n\t" \
  "v_pk_fma_f32 v[104:105], v[104:105], v[152:153], v[144:145]\n\t" \
  "v_pk_fma_f32 v[106:107], v[106:107], v[154:155], v[146:147]\n\t" \
  "v_pk_fma_f32 v[108:109], v[108:109], v[156:157], v[148:149]\n\t" \
  "v_pk_fma_f32 v[110:111], v[110:111], v[158:159], v[150:151]\n\t" \
  "v_cvt_pk_bf16_f32 v105, v104, v105\n\t" \
  "v_cvt_pk_bf16_f32 v107, v106, v107\n\t" \
  "v_cvt_pk_bf16_f32 v109, v108, v109\n\t" \
  "v_cvt_pk_bf16_f32 v111, v110, v111\n\t" \
  "v_lshlrev_b32 v104, 16, v105\n\t" \
  "v_and_b32 v105, 0xffff0000, v105\n\t" \
  "v_lshlrev_b32 v106, 16, v107\n\t" \
  "v_and_b32 v107, 0xffff0000, v107\n\t" \
  "v_lshlrev_b32 v108, 16, v109\n\t" \
  "v_and_b32 v109, 0xffff0000, v109\n\t" \
  "v_lshlrev_b32 v110, 16, v111\n\t" \
  "v_and_b32 v111, 0xffff0000, v111\n\t" \
  "v_pk_fma_f32 v[104:105], v[168:169], v[104:105], v[24:25]\n\t" \
  "v_pk_fma_f32 v[106:107], v[170:171], v[106:107], v[26:27]\n\t" \
  "v_pk_fma_f32 v[108:109], v[172:173], v[108:109], v[28:29]\n\t" \
  "v_pk_fma_f32 v[110:111], v[174:175], v[110:111], v[30:31]\n\t" \
  "v_cvt_pk_bf16_f32 v140, v104, v105\n\t" \
  "v_cvt_pk_bf16_f32 v141, v106, v107\n\t" \
  "v_cvt_pk_bf16_f32 v142, v108, v109\n\t" \
  "v_cvt_pk_bf16_f32 v143, v110, v111\n\t" \
  "v_permlane16_swap_b32 v128, v130\n\t" \
  "v_permlane16_swap_b32 v129, v131\n\t" \
  "v_permlane16_swap_b32 v132, v134\n\t" \
  "v_permlane16_swap_b32 v133, v135\n\t" \
  "v_permlane16_swap_b32 v136, v138\n\t" \
  "v_permlane16_swap_b32 v137, v139\n\t" \
  "v_permlane16_swap_b32 v140, v142\n\t" \
  "v_permlane16_swap_b32 v141, v143\n\t" \
  STP("s_memrealtime s[94:95]\n\t") \
  STP("s_memtime s[92:93]\n\t") \
  STP("s_waitcnt lgkmcnt(0)\n\t") \
  STP("v_writelane_b32 v255, s94, 46\n\t") \
  STP("v_writelane_b32 v255, s92, 47\n\t") \
  "s_mov_b32 s98, 0\n\t" \
  "s_mov_b32 s81, 0\n\t" \
  "s_lshl_b32 s99, 1, s8\n\t" \
  "s_cmp_eq_u32 s49, 0\n\t" \
  "s_cbranch_scc1 .Ltl_w2\n\t" \
  "s_waitcnt vmcnt(6)\n\t" \
  "s_branch .Ltl_t2\n\t" \
  ".Ltl_s2:\n\t" \
  "s_bcnt1_i32_b32 s54, s54\n\t" \
  "s_cmp_ge_u32 s54, 24\n\t" \
  "s_cselect_b32 s54, 1, s98\n\t" \
  "s_cmp_lg_u32 s54, 0\n\t" \
  "s_cbranch_scc1 .Ltl_q2\n\t" \
  "s_mov_b32 s98, 1\n\t" \
  "s_mov_b64 s[96:97], -1\n\t" \
  "s_cmp_eq_u32 s49, 0\n\t" \
  "s_cselect_b64 s[52:53], 0, s[50:51]\n\t" \
  "s_not_b64 s[52:53], s[52:53]\n\t" \
  "s_cmp_eq_u32 s48, 0\n\t" \
  "s_cselect_b64 exec, s[52:53], s[96:97]\n\t" \
  "s_add_u32 s44, s22, 0\n\t" \
  "buffer_store_dwordx4 v[128:131], v231, s[4:7], s44 offen sc0 sc1\n\t" \
  "s_cmp_eq_u32 s48, 1\n\t" \
  "s_cselect_b64 exec, s[52:53], s[96:97]\n\t" \
  "s_add_u32 s44, s22, 32768\n\t" \
  "buffer_store_dwordx4 v[132:135], v231, s[4:7], s44 offen sc0 sc1\n\t" \
  "s_cmp_eq_u32 s48, 2\n\t" \
  "s_cselect_b64 exec, s[52:53], s[96:97]\n\t" \
  "s_add_u32 s44, s22, 65536\n\t" \
  "buffer_store_dwordx4 v[136:139], v231, s[4:7], s44 offen sc0 sc1\n\t" \
  "s_cmp_eq_u32 s48, 3\n\t" \
  "s_cselect_b64 exec, s[52:53], s[96:97]\n\t" \
  "s_add_u32 s44, s22, 98304\n\t" \
  "buffer_store_dwordx4 v[140:143], v231, s[4:7], s44 offen sc0 sc1\n\t" \
  "s_mov_b64 exec, -1\n\t" \
  ".Ltl_q2:\n\t" \
  "s_add_u32 s81, s81, 1\n\t" \
  "s_cmp_lt_u32 s81, " FQ_CAP "\n\t" \
  "s_cbranch_scc0 .Ltl_g2\n\t" \
  "s_sleep 1\n\t" \
  "s_not_b32 s42, s45\n\t" \
  "s_bfe_u32 s47, s42, 0x50006\n\t" \
  "s_lshl_b32 s43, s42, 7\n\t" \
  "s_and_b32 s43, s43, 0x3c000\n\t" \
  "s_lshl_b32 s42, s42, 6\n\t" \
  "s_and_b32 s42, s42, 0x1000\n\t" \
  "s_add_u32 s42, s42, s43\n\t" \
  "s_add_u32 s42, s42, s15\n\t" \
  "buffer_load_dwordx2 v[248:249], v225, s[16:19], s13 offen sc1\n\t" \
  "buffer_load_dwordx4 v[72:75], v226, s[16:19], s42 offen offset:0 sc1\n\t" \
  "buffer_load_dwordx4 v[76:79], v226, s[16:19], s42 offen offset:16 sc1\n\t" \
  ".Ltl_w2:\n\t" \
  "s_waitcnt vmcnt(0)\n\t" \
  ".Ltl_t2:\n\t" \
  "v_cmp_eq_u32 vcc, s36, v249\n\t" \
  "v_xor_b32 v65, s36, v73\n\t" \
  "v_bitop3_b32 v65, v65, v75, s36 bitop3:0xf6\n\t" \
  "v_cndmask_b32 v64, 0, v248, vcc\n\t" \
  "s_or_b32 s54, vcc_lo, s99\n\t" \
  "s_nop 0\n\t" \
  "v_max_u32_dpp v64, v64, v64 quad_perm:[1,0,3,2] row_mask:0xf bank_mask:0xf\n\t" \
  "v_bitop3_b32 v65, v65, v77, s36 bitop3:0xf6\n\t" \
  "v_bitop3_b32 v65, v65, v79, s36 bitop3:0xf6\n\t" \
  "v_max_u32_dpp v64, v64, v64 quad_perm:[2,3,0,1] row_mask:0xf bank_mask:0xf\n\t" \
  "v_cmp_ne_u32 vcc, 0, v65\n\t" \
  "s_nop 0\n\t" \
  "v_max_u32_dpp v64, v64, v64 row_half_mirror row_mask:0xf bank_mask:0xf\n\t" \
  "s_nop 1\n\t" \
  "v_max_u32_dpp v64, v64, v64 row_mirror row_mask:0xf bank_mask:0xf\n\t" \
  "s_nop 0\n\t" \
  "v_readlane_b32 s45, v64, 0\n\t" \
  "v_readlane_b32 s42, v64, 16\n\t" \
  "s_max_u32 s45, s45, s42\n\t" \
  "s_max_u32 s45, s45, s46\n\t" \
  "s_not_b32 s42, s45\n\t" \
  "s_bfe_u32 s42, s42, 0x50006\n\t" \
  "s_xor_b32 s42, s42, s47\n\t" \
  "s_orn2_b32 s42, s42, s54\n\t" \
  "s_or_b32 s42, s42, vcc_lo\n\t" \
  "s_or_b32 s42, s42, vcc_hi\n\t" \
  "s_cbranch_scc1 .Ltl_s2\n\t" \
  ".Ltl_k2:\n\t" \
  STP("s_memrealtime s[94:95]\n\t") \
  STP("s_memtime s[92:93]\n\t") \
  STP("s_waitcnt lgkmcnt(0)\n\t") \
  STP("v_writelane_b32 v255, s94, 48\n\t") \
  STP("v_writelane_b32 v255, s92, 49\n\t") \
  "s_cmp_lt_i32 s57, 0\n\t" \
  "s_cbranch_scc1 .Ltl_nf\n\t" \
  "v_dot2_f32_bf16 v96, v80, v72, 0\n\t" \
  "v_dot2_f32_bf16 v97, v84, v72, 0\n\t" \
  "v_dot2_f32_bf16 v98, v81, v72, 0\n\t" \
  "v_dot2_f32_bf16 v99, v85, v72, 0\n\t" \
  "v_dot2_f32_bf16 v100, v82, v72, 0\n\t" \
  "v_dot2_f32_bf16 v101, v86, v72, 0\n\t" \
  "v_dot2_f32_bf16 v102, v83, v72, 0\n\t" \
  "v_dot2_f32_bf16 v103, v87, v72, 0\n\t" \
  "v_dot2_f32_bf16 v104, v176, v72, 0\n\t" \
  "v_dot2_f32_bf16 v105, v180, v72, 0\n\t" \
  "v_dot2_f32_bf16 v106, v177, v72, 0\n\t" \
  "v_dot2_f32_bf16 v107, v181, v72, 0\n\t" \
  "v_dot2_f32_bf16 v108, v178, v72, 0\n\t" \
  "v_dot2_f32_bf16 v109, v182, v72, 0\n\t" \
  "v_dot2_f32_bf16 v110, v179, v72, 0\n\t" \
  "v_dot2_f32_bf16 v111, v183, v72, 0\n\t" \
  "v_dot2_f32_bf16 v96, v88, v74, v96\n\t" \
  "v_dot2_f32_bf16 v97, v92, v74, v97\n\t" \
  "v_dot2_f32_bf16 v98, v89, v74, v98\n\t" \
  "v_dot2_f32_bf16 v99, v93, v74, v99\n\t" \
  "v_dot2_f32_bf16 v100, v90, v74, v100\n\t" \
  "v_dot2_f32_bf16 v101, v94, v74, v101\n\t" \
  "v_dot2_f32_bf16 v102, v91, v74, v102\n\t" \
  "v_dot2_f32_bf16 v103, v95, v74, v103\n\t" \
  "v_dot2_f32_bf16 v104, v184, v74, v104\n\t" \
  "v_dot2_f32_bf16 v105, v188, v74, v105\n\t" \
  "v_dot2_f32_bf16 v106, v185, v74, v106\n\t" \
  "v_dot2_f32_bf16 v107, v189, v74, v107\n\t" \
  "v_dot2_f32_bf16 v108, v186, v74, v108\n\t" \
  "v_dot2_f32_bf16 v109, v190, v74, v109\n\t" \
  "v_dot2_f32_bf16 v110, v187, v74, v110\n\t" \
  "v_dot2_f32_bf16 v111, v191, v74, v111\n\t" \
  "v_dot2_f32_bf16 v96, v208, v76, v96\n\t" \
  "v_dot2_f32_bf16 v97, v212, v76, v97\n\t" \
  "v_dot2_f32_bf16 v98, v209, v76, v98\n\t" \
  "v_dot2_f32_bf16 v99, v213, v76, v99\n\t" \
  "v_dot2_f32_bf16 v100, v210, v76, v100\n\t" \
  "v_dot2_f32_bf16 v101, v214, v76, v101\n\t" \
  "v_dot2_f32_bf16 v102, v211, v76, v102\n\t" \
  "v_dot2_f32_bf16 v103, v215, v76, v103\n\t" \
  "v_dot2_f32_bf16 v104, v192, v76, v104\n\t" \
  "v_dot2_f32_bf16 v105, v196, v76, v105\n\t" \
  "v_dot2_f32_bf16 v106, v193, v76, v106\n\t" \
  "v_dot2_f32_bf16 v107, v197, v76, v107\n\t" \
  "v_dot2_f32_bf16 v108, v194, v76, v108\n\t" \
  "v_dot2_f32_bf16 v109, v198, v76, v109\n\t" \
  "v_dot2_f32_bf16 v110, v195, v76, v110\n\t" \
  "v_dot2_f32_bf16 v111, v199, v76, v111\n\t" \
  "v_dot2_f32_bf16 v96, v216, v78, v96\n\t" \
  "v_dot2_f32_bf16 v97, v220, v78, v97\n\t" \
  "v_dot2_f32_bf16 v98, v217, v78, v98\n\t" \
  "v_dot2_f32_bf16 v99, v221, v78, v99\n\t" \
  "v_dot2_f32_bf16 v100, v218, v78, v100\n\t" \
  "v_dot2_f32_bf16 v101, v222, v78, v101\n\t" \
  "v_dot2_f32_bf16 v102, v219, v78, v102\n\t" \
  "v_dot2_f32_bf16 v103, v223, v78, v103\n\t" \
  "v_dot2_f32_bf16 v104, v200, v78, v104\n\t" \
  "v_dot2_f32_bf16 v105, v204, v78, v105\n\t" \
  "v_dot2_f32_bf16 v106, v201, v78, v106\n\t" \
  "v_dot2_f32_bf16 v107, v205, v78, v107\n\t" \
  "v_dot2_f32_bf16 v108, v202, v78, v108\n\t" \
  "v_dot2_f32_bf16 v109, v206, v78, v109\n\t" \
  "v_dot2_f32_bf16 v110, v203, v78, v110\n\t" \
  "v_dot2_f32_bf16 v111, v207, v78, v111\n\t" \
  "v_permlane32_swap_b32 v96, v104\n\t" \
  "v_permlane32_swap_b32 v97, v105\n\t" \
  "v_permlane32_swap_b32 v98, v106\n\t" \
  "v_permlane32_swap_b32 v99, v107\n\t" \
  "v_permlane32_swap_b32 v100, v108\n\t" \
  "v_permlane32_swap_b32 v101, v109\n\t" \
  "v_permlane32_swap_b32 v102, v110\n\t" \
  "v_permlane32_swap_b32 v103, v111\n\t" \
  "v_add_f32 v96, v96, v104\n\t" \
  "v_add_f32 v97, v97, v105\n\t" \
  "v_add_f32 v98, v98, v106\n\t" \
  "v_add_f32 v99, v99, v107\n\t" \
  "v_add_f32 v100, v100, v108\n\t" \
  "v_add_f32 v101, v101, v109\n\t" \
  "v_add_f32 v102, v102, v110\n\t" \
  "v_add_f32 v103, v103, v111\n\t" \
  "v_permlane16_swap_b32 v96, v100\n\t" \
  "v_permlane16_swap_b32 v97, v101\n\t" \
  "v_permlane16_swap_b32 v98, v102\n\t" \
  "v_permlane16_swap_b32 v99, v103\n\t" \
  "v_add_f32 v96, v96, v100\n\t" \
  "v_add_f32 v97, v97, v101\n\t" \
  "v_add_f32 v98, v98, v102\n\t" \
  "v_add_f32 v99, v99, v103\n\t" \
  "v_add_f32_dpp v96, v96, v96 row_ror:8 row_mask:0xf bank_mask:0xf\n\t" \
  "v_add_f32_dpp v97, v97, v97 row_ror:8 row_mask:0xf bank_mask:0xf\n\t" \
  "v_add_f32_dpp v98, v98, v98 row_ror:8 row_mask:0xf bank_mask:0xf\n\t" \
  "v_add_f32_dpp v99, v99, v99 row_ror:8 row_mask:0xf bank_mask:0xf\n\t" \
  "v_add_f32_dpp v96, v96, v96 row_ror:4 row_mask:0xf bank_mask:0xf\n\t" \
  "v_add_f32_dpp v97, v97, v97 row_ror:4 row_mask:0xf bank_mask:0xf\n\t" \
  "v_add_f32_dpp v98, v98, v98 row_ror:4 row_mask:0xf bank_mask:0xf\n\t" \
  "v_add_f32_dpp v99, v99, v99 row_ror:4 row_mask:0xf bank_mask:0xf\n\t" \
  "v_add_f32_dpp v96, v96, v96 row_ror:2 row_mask:0xf bank_mask:0xf\n\t" \
  "v_add_f32_dpp v97, v97, v97 row_ror:2 row_mask:0xf bank_mask:0xf\n\t" \
  "v_add_f32_dpp v98, v98, v98 row_ror:2 row_mask:0xf bank_mask:0xf\n\t" \
  "v_add_f32_dpp v99, v99, v99 row_ror:2 row_mask:0xf bank_mask:0xf\n\t" \
  "s_mov_b32 exec_lo, 0x00030003\n\t" \
  "s_mov_b32 exec_hi, 0x00030003\n\t" \
  "ds_write_b128 v253, v[96:99] offset:8192\n\t" \
  "s_mov_b64 exec, -1\n\t" \
  "v_mov_b32 v101, s36\n\t" \
  "v_mov_b32 v103, s36\n\t" \
  "s_waitcnt lgkmcnt(0)\n\t" \
  "s_barrier\n\t" \
  "v_add_u32 v104, 8192, v245\n\t" \
  "ds_read2_b32 v[96:97], v104 offset1:64\n\t" \
  "ds_read2_b32 v[98:99], v104 offset0:32 offset1:96\n\t" \
  "s_waitcnt lgkmcnt(0)\n\t" \
  "v_pk_add_f32 v[96:97], v[96:97], v[98:99]\n\t" \
  "v_add_f32 v96, v96, v97\n\t" \
  "v_cvt_pk_bf16_f32 v96, v96, v96\n\t" \
  "v_lshlrev_b32 v96, 16, v96\n\t" \
  "v_mov_b32 v97, v96\n\t" \
  "s_nop 1\n\t" \
  "v_permlane16_swap_b32 v96, v97\n\t" \
  "v_mul_f32 v98, 0xbfb8aa3b, v96\n\t" \
  "v_exp_f32 v98, v98\n\t" \
  "s_nop 0\n\t" \
  "v_add_f32 v98, 1.0, v98\n\t" \
  "v_rcp_f32 v98, v98\n\t" \
  "s_nop 0\n\t" \
  "v_mul_f32 v96, v98, v96\n\t" \
  "v_cvt_pk_bf16_f32 v96, v96, v96\n\t" \
  "v_lshlrev_b32 v96, 16, v96\n\t" \
  "v_mul_f32 v96, v97, v96\n\t" \
  "s_nop 1\n\t" \
  "v_mov_b32_dpp v97, v96 quad_perm:[1,0,3,2] row_mask:0xf bank_mask:0xf\n\t" \
  "v_cvt_pk_bf16_f32 v96, v96, v97\n\t" \
  "s_nop 0\n\t" \
  "v_readlane_b32 s60, v96, 0\n\t" \
  "v_readlane_b32 s61, v96, 2\n\t" \
  "v_readlane_b32 s62, v96, 4\n\t" \
  "v_readlane_b32 s63, v96, 6\n\t" \
  "v_readlane_b32 s64, v96, 8\n\t" \
  "v_readlane_b32 s65, v96, 10\n\t" \
  "v_readlane_b32 s66, v96, 12\n\t" \
  "v_readlane_b32 s67, v96, 14\n\t" \
  "v_dot2_f32_bf16 v104, v112, s60, 0\n\t" \
  "v_dot2_f32_bf16 v105, v34, s60, 0\n\t" \
  "v_dot2_f32_bf16 v106, v113, s60, 0\n\t" \
  "v_dot2_f32_bf16 v107, v35, s60, 0\n\t" \
  "v_dot2_f32_bf16 v104, v114, s61, v104\n\t" \
  "v_dot2_f32_bf16 v105, v38, s61, v105\n\t" \
  "v_dot2_f32_bf16 v106, v115, s61, v106\n\t" \
  "v_dot2_f32_bf16 v107, v39, s61, v107\n\t" \
  "v_dot2_f32_bf16 v104, v116, s62, v104\n\t" \
  "v_dot2_f32_bf16 v105, v42, s62, v105\n\t" \
  "v_dot2_f32_bf16 v106, v117, s62, v106\n\t" \
  "v_dot2_f32_bf16 v107, v43, s62, v107\n\t" \
  "v_dot2_f32_bf16 v104, v118, s63, v104\n\t" \
  "v_dot2_f32_bf16 v105, v46, s63, v105\n\t" \
  "v_dot2_f32_bf16 v106, v119, s63, v106\n\t" \
  "v_dot2_f32_bf16 v107, v47, s63, v107\n\t" \
  "v_dot2_f32_bf16 v104, v120, s64, v104\n\t" \
  "v_dot2_f32_bf16 v105, v50, s64, v105\n\t" \
  "v_dot2_f32_bf16 v106, v121, s64, v106\n\t" \
  "v_dot2_f32_bf16 v107, v51, s64, v107\n\t" \
  "v_dot2_f32_bf16 v104, v122, s65, v104\n\t" \
  "v_dot2_f32_bf16 v105, v54, s65, v105\n\t" \
  "v_dot2_f32_bf16 v106, v123, s65, v106\n\t" \
  "v_dot2_f32_bf16 v107, v55, s65, v107\n\t" \
  "v_dot2_f32_bf16 v104, v124, s66, v104\n\t" \
  "v_dot2_f32_bf16 v105, v58, s66, v105\n\t" \
  "v_dot2_f32_bf16 v106, v125, s66, v106\n\t" \
  "v_dot2_f32_bf16 v107, v59, s66, v107\n\t" \
  "v_dot2_f32_bf16 v104, v126, s67, v104\n\t" \
  "v_dot2_f32_bf16 v105, v62, s67, v105\n\t" \
  "v_dot2_f32_bf16 v106, v127, s67, v106\n\t" \
  "v_dot2_f32_bf16 v107, v63, s67, v107\n\t" \
  "s_nop 0\n\t" \
  "v_cvt_pk_bf16_f32 v100, v104, v105\n\t" \
  "s_nop 0\n\t" \
  "v_cvt_pk_bf16_f32 v102, v106, v107\n\t" \
  "buffer_store_dwordx4 v[100:103], v242, s[16:19], s23 offen sc1\n\t" \
  ".Ltl_nf:\n\t" \
  STP("s_memrealtime s[94:95]\n\t") \
  STP("s_memtime s[92:93]\n\t") \
  STP("s_waitcnt lgkmcnt(0)\n\t") \
  STP("v_writelane_b32 v255, s94, 50\n\t") \
  STP("v_writelane_b32 v255, s92, 51\n\t") \
  "s_cmp_eq_u32 s47, s8\n\t" \
  "s_cselect_b64 s[52:53], s[50:51], 0\n\t" \
  "s_cselect_b32 s54, 0, s49\n\t" \
  "s_not_b64 s[52:53], s[52:53]\n\t" \
  "s_mov_b64 s[96:97], -1\n\t" \
  "s_cmp_eq_u32 s98, 0\n\t" \
  "s_cbranch_scc1 .Ltl_so\n\t" \
  "s_cmp_eq_u32 s54, 0\n\t" \
  "s_cbranch_scc1 .Ltl_ns\n\t" \
  "s_mov_b64 s[52:53], s[50:51]\n\t" \
  "s_mov_b64 s[96:97], 0\n\t" \
  ".Ltl_so:\n\t" \
  "s_cmp_eq_u32 s48, 0\n\t" \
  "s_cselect_b64 exec, s[52:53], s[96:97]\n\t" \
  "s_add_u32 s44, s22, 0\n\t" \
  "buffer_store_dwordx4 v[128:131], v231, s[4:7], s44 offen sc0 sc1\n\t" \
  "s_cmp_eq_u32 s48, 1\n\t" \
  "s_cselect_b64 exec, s[52:53], s[96:97]\n\t" \
  "s_add_u32 s44, s22, 32768\n\t" \
  "buffer_store_dwordx4 v[132:135], v231, s[4:7], s44 offen sc0 sc1\n\t" \
  "s_cmp_eq_u32 s48, 2\n\t" \
  "s_cselect_b64 exec, s[52:53], s[96:97]\n\t" \
  "s_add_u32 s44, s22, 65536\n\t" \
  "buffer_store_dwordx4 v[136:139], v231, s[4:7], s44 offen sc0 sc1\n\t" \
  "s_cmp_eq_u32 s48, 3\n\t" \
  "s_cselect_b64 exec, s[52:53], s[96:97]\n\t" \
  "s_add_u32 s44, s22, 98304\n\t" \
  "buffer_store_dwordx4 v[140:143], v231, s[4:7], s44 offen sc0 sc1\n\t" \
  "s_mov_b64 exec, -1\n\t" \
  ".Ltl_ns:\n\t" \
  "s_or_b32 s42, s2, s3\n\t" \
  "s_cbranch_scc1 .Ltl_ne\n\t" \
  "s_mov_b32 s42, 2490368\n\t" \
  "buffer_store_dword v243, v244, s[16:19], s42 offen sc1\n\t" \
  ".Ltl_ne:\n\t" \
  "s_cmp_lt_i32 s57, 0\n\t" \
  "s_cbranch_scc0 .Ltl_end\n\t" \
  "v_and_b32 v1, 15, v233\n\t" \
  "v_lshlrev_b32 v1, 3, v1\n\t" \
  "s_not_b32 s42, s45\n\t" \
  "s_bfe_u32 s47, s42, 0x50006\n\t" \
  "s_lshl_b32 s43, s42, 7\n\t" \
  "s_and_b32 s43, s43, 0x3c000\n\t" \
  "s_lshl_b32 s42, s42, 6\n\t" \
  "s_and_b32 s42, s42, 0x1000\n\t" \
  "s_add_u32 s42, s42, s43\n\t" \
  "s_add_u32 s42, s42, s15\n\t" \
  "s_add_u32 s69, s42, s91\n\t" \
  "s_lshl_b32 s42, s47, 12\n\t" \
  "s_and_b32 s42, s42, 0x1c000\n\t" \
  "s_add_u32 s70, s84, s42\n\t" \
  "s_and_b32 s42, s47, 3\n\t" \
  "s_lshl_b32 s42, s42, 11\n\t" \
  "s_add_u32 s70, s70, s42\n\t" \
  "s_add_u32 s70, s70, 917504\n\t" \
  "s_and_b32 s44, s2, 3\n\t" \
  "s_lshl_b32 s42, s44, 12\n\t" \
  "s_add_u32 s42, s42, 150400\n\t" \
  "v_add_u32 v6, s42, v1\n\t" \
  "ds_read_b64 v[6:7], v6\n\t" \
  "v_and_b32 v5, 0x70, v242\n\t" \
  "v_lshrrev_b32 v3, 2, v228\n\t" \
  "v_add_u32 v3, v3, v5\n\t" \
  "v_lshl_add_u32 v3, s3, 7, v3\n\t" \
  "s_not_b32 s42, s45\n\t" \
  "s_and_b32 s42, s42, 0x7ff\n\t" \
  "s_lshl_b32 s42, s42, 11\n\t" \
  "s_lshr_b32 s71, s91, 1\n\t" \
  "s_add_u32 s71, s71, s42\n\t" \
  "s_ashr_i32 s42, s45, 31\n\t" \
  "s_orn2_b32 s42, 0x80000000, s42\n\t" \
  "s_xor_b32 s42, s42, s45\n\t" \
  "s_and_b32 s42, s42, 0xffff0000\n\t" \
  "v_mov_b32 v2, s42\n\t" \
  "v_mul_f32 v2, 0xbfb8aa3b, v2\n\t" \
  "v_exp_f32 v2, v2\n\t" \
  "s_nop 0\n\t" \
  "v_add_f32 v2, 1.0, v2\n\t" \
  "v_rcp_f32 v2, v2\n\t" \
  "s_nop 0\n\t" \
  "v_cvt_pk_bf16_f32 v2, v2, v2\n\t" \
  "v_lshlrev_b32 v2, 16, v2\n\t" \
  "s_mov_b32 s81, 0\n\t" \
  "s_sleep 16\n\t" \
  ".Ltl_r3:\n\t" \
  "s_sleep 1\n\t" \
  "buffer_load_dwordx4 v[12:15], v246, s[16:19], s84 offen sc1\n\t" \
  "buffer_load_dwordx4 v[16:19], v246, s[16:19], s85 offen sc1\n\t" \
  "buffer_load_dwordx4 v[20:23], v246, s[16:19], s86 offen sc1\n\t" \
  "buffer_load_dwordx4 v[24:27], v246, s[16:19], s87 offen sc1\n\t" \
  "buffer_load_dwordx4 v[28:31], v246, s[16:19], s88 offen sc1\n\t" \
  "buffer_load_dwordx4 v[32:35], v246, s[16:19], s89 offen sc1\n\t" \
  "buffer_load_dwordx4 v[36:39], v246, s[16:19], s90 offen sc1\n\t" \
  "buffer_load_dwordx2 v[8:9], v1, s[16:19], s69 offen sc1\n\t" \
  "buffer_load_dwordx2 v[10:11], v1, s[16:19], s70 offen sc1\n\t" \
  "s_waitcnt vmcnt(0) lgkmcnt(0)\n\t" \
  "v_xor_b32 v4, s36, v9\n\t" \
  "v_bitop3_b32 v4, v4, v11, s36 bitop3:0xf6\n\t" \
  "v_bitop3_b32 v4, v4, v13, s36 bitop3:0xf6\n\t" \
  "v_bitop3_b32 v4, v4, v15, s36 bitop3:0xf6\n\t" \
  "v_bitop3_b32 v4, v4, v17, s36 bitop3:0xf6\n\t" \
  "v_bitop3_b32 v4, v4, v19, s36 bitop3:0xf6\n\t" \
  "v_bitop3_b32 v4, v4, v21, s36 bitop3:0xf6\n\t" \
  "v_bitop3_b32 v4, v4, v23, s36 bitop3:0xf6\n\t" \
  "v_bitop3_b32 v4, v4, v25, s36 bitop3:0xf6\n\t" \
  "v_bitop3_b32 v4, v4, v27, s36 bitop3:0xf6\n\t" \
  "v_bitop3_b32 v4, v4, v29, s36 bitop3:0xf6\n\t" \
  "v_bitop3_b32 v4, v4, v31, s36 bitop3:0xf6\n\t" \
  "v_bitop3_b32 v4, v4, v33, s36 bitop3:0xf6\n\t" \
  "v_bitop3_b32 v4, v4, v35, s36 bitop3:0xf6\n\t" \
  "v_bitop3_b32 v4, v4, v37, s36 bitop3:0xf6\n\t" \
  "v_bitop3_b32 v4, v4, v39, s36 bitop3:0xf6\n\t" \
  "v_cmp_ne_u32 vcc, 0, v4\n\t" \
  "v_lshlrev_b32 v40, 16, v12\n\t" \
  "v_and_b32 v41, 0xffff0000, v12\n\t" \
  "v_lshlrev_b32 v42, 16, v14\n\t" \
  "v_and_b32 v43, 0xffff0000, v14\n\t" \
  "v_lshlrev_b32 v44, 16, v16\n\t" \
  "v_and_b32 v45, 0xffff0000, v16\n\t" \
  "v_lshlrev_b32 v46, 16, v18\n\t" \
  "v_and_b32 v47, 0xffff0000, v18\n\t" \
  "s_cbranch_vccz .Ltl_k3\n\t" \
  "s_add_u32 s81, s81, 1\n\t" \
  "s_cmp_lt_u32 s81, " FQ_CAP "\n\t" \
  "s_cbranch_scc1 .Ltl_r3\n\t" \
  "buffer_store_dword v243, off, s[16:19], 0 offset:256 sc1\n\t" \
  ".Ltl_k3:\n\t" \
  "v_pk_add_f32 v[40:41], v[40:41], v[44:45]\n\t" \
  "v_pk_add_f32 v[42:43], v[42:43], v[46:47]\n\t" \
  "v_lshlrev_b32 v44, 16, v20\n\t" \
  "v_and_b32 v45, 0xffff0000, v20\n\t" \
  "v_lshlrev_b32 v46, 16, v22\n\t" \
  "v_and_b32 v47, 0xffff0000, v22\n\t" \
  "v_pk_add_f32 v[40:41], v[40:41], v[44:45]\n\t" \
  "v_pk_add_f32 v[42:43], v[42:43], v[46:47]\n\t" \
  "v_lshlrev_b32 v44, 16, v24\n\t" \
  "v_and_b32 v45, 0xffff0000, v24\n\t" \
  "v_lshlrev_b32 v46, 16, v26\n\t" \
  "v_and_b32 v47, 0xffff0000, v26\n\t" \
  "v_pk_add_f32 v[40:41], v[40:41], v[44:45]\n\t" \
  "v_pk_add_f32 v[42:43], v[42:43], v[46:47]\n\t" \
  "v_lshlrev_b32 v44, 16, v28\n\t" \
  "v_and_b32 v45, 0xffff0000, v28\n\t" \
  "v_lshlrev_b32 v46, 16, v30\n\t" \
  "v_and_b32 v47, 0xffff0000, v30\n\t" \
  "v_pk_add_f32 v[40:41], v[40:41], v[44:45]\n\t" \
  "v_pk_add_f32 v[42:43], v[42:43], v[46:47]\n\t" \
  "v_lshlrev_b32 v44, 16, v32\n\t" \
  "v_and_b32 v45, 0xffff0000, v32\n\t" \
  "v_lshlrev_b32 v46, 16, v34\n\t" \
  "v_and_b32 v47, 0xffff0000, v34\n\t" \
  "v_pk_add_f32 v[40:41], v[40:41], v[44:45]\n\t" \
  "v_pk_add_f32 v[42:43], v[42:43], v[46:47]\n\t" \
  "v_lshlrev_b32 v44, 16, v36\n\t" \
  "v_and_b32 v45, 0xffff0000, v36\n\t" \
  "v_lshlrev_b32 v46, 16, v38\n\t" \
  "v_and_b32 v47, 0xffff0000, v38\n\t" \
  "v_pk_add_f32 v[40:41], v[40:41], v[44:45]\n\t" \
  "v_pk_add_f32 v[42:43], v[42:43], v[46:47]\n\t" \
  "s_nop 1\n\t" \
  "v_permlane32_swap_b32 v40, v42\n\t" \
  "v_permlane32_swap_b32 v41, v43\n\t" \
  "v_add_f32 v40, v40, v42\n\t" \
  "v_add_f32 v41, v41, v43\n\t" \
  "s_nop 1\n\t" \
  "v_permlane16_swap_b32 v40, v41\n\t" \
  "v_add_f32 v40, v40, v41\n\t" \
  "s_nop 1\n\t" \
  "v_add_f32_dpp v40, v40, v40 row_ror:8 row_mask:0xf bank_mask:0xf\n\t" \
  "ds_write_b32 v3, v40 offset:12288\n\t" \
  "v_lshlrev_b32 v44, 16, v8\n\t" \
  "v_and_b32 v45, 0xffff0000, v8\n\t" \
  "v_lshlrev_b32 v46, 16, v10\n\t" \
  "v_and_b32 v47, 0xffff0000, v10\n\t" \
  "s_waitcnt lgkmcnt(0)\n\t" \
  "s_barrier\n\t" \
  "s_cmp_lg_u32 s3, 0\n\t" \
  "s_cbranch_scc1 .Ltl_end\n\t" \
  "v_add_u32 v4, 12288, v1\n\t" \
  "ds_read2_b64 v[12:15], v4 offset1:16\n\t" \
  "ds_read2_b64 v[16:19], v4 offset0:32 offset1:48\n\t" \
  "v_lshrrev_b32 v1, 1, v1\n\t" \
  "s_waitcnt lgkmcnt(0)\n\t" \
  "v_pk_add_f32 v[12:13], v[12:13], v[14:15]\n\t" \
  "v_pk_add_f32 v[16:17], v[16:17], v[18:19]\n\t" \
  "v_pk_add_f32 v[12:13], v[12:13], v[16:17]\n\t" \
  "v_cvt_pk_bf16_f32 v13, v12, v13\n\t" \
  "v_lshlrev_b32 v12, 16, v13\n\t" \
  "v_and_b32 v13, 0xffff0000, v13\n\t" \
  "v_mul_f32 v12, v12, v2\n\t" \
  "v_mul_f32 v13, v13, v2\n\t" \
  "v_cvt_pk_bf16_f32 v13, v12, v13\n\t" \
  "v_lshlrev_b32 v12, 16, v13\n\t" \
  "v_and_b32 v13, 0xffff0000, v13\n\t" \
  "v_pk_add_f32 v[12:13], v[12:13], v[44:45]\n\t" \
  "v_cvt_pk_bf16_f32 v13, v12, v13\n\t" \
  "v_lshlrev_b32 v12, 16, v13\n\t" \
  "v_and_b32 v13, 0xffff0000, v13\n\t" \
  "v_pk_mul_f32 v[12:13], v[12:13], v[6:7]\n\t" \
  "v_cvt_pk_bf16_f32 v13, v12, v13\n\t" \
  "v_lshlrev_b32 v12, 16, v13\n\t" \
  "v_and_b32 v13, 0xffff0000, v13\n\t" \
  "v_pk_add_f32 v[12:13], v[12:13], v[46:47]\n\t" \
  "v_cvt_pk_bf16_f32 v12, v12, v13\n\t" \
  "s_mov_b64 exec, 0xffff\n\t" \
  "buffer_store_dword v12, v1, s[4:7], s71 offen sc0 sc1\n\t" \
  "s_mov_b64 exec, -1\n\t" \
  ".Ltl_end:\n\t" \
  STP("s_memrealtime s[94:95]\n\t") \
  STP("s_memtime s[92:93]\n\t") \
  STP("s_waitcnt lgkmcnt(0)\n\t") \
  STP("v_writelane_b32 v255, s94, 52\n\t") \
  STP("v_writelane_b32 v255, s92, 53\n\t") \
  ".subsection 1\n\t" \
  ".Lat_x:\n\t" \
  "v_lshlrev_b32 v209, 12, v233\n\t" \
  "v_add_u32 v209, 0, v209\n\t" \
  "s_mov_b64 exec, 15\n\t" \
  "ds_write_b32 v209, v243\n\t" \
  "s_mov_b64 exec, -1\n\t" \
  "s_waitcnt lgkmcnt(0)\n\t" \
  "s_branch .Lat_y\n\t" \
  ".Lat_rd:\n\t" \
  "s_waitcnt vmcnt(0) lgkmcnt(0)\n\t" \
  "s_barrier\n\t" \
  "s_nop 7\n\t" \
  "v_lshl_add_u32 v0, s3, 6, v233\n\t" \
  "s_mov_b32 s37, 0\n\t" \
  "s_branch .Lfr_again\n\t" \
  ".Lat_s0:\n\t" \
  "s_mov_b32 s81, 0\n\t" \
  ".Lat_r0:\n\t" \
  "s_sleep 4\n\t" \
  "buffer_load_dword v246, v253, s[16:19], s65 offen sc1\n\t" \
  "s_waitcnt vmcnt(0)\n\t" \
  "v_cmp_ne_u32 vcc, s36, v246\n\t" \
  "s_and_b32 vcc_lo, vcc_lo, 0xe\n\t" \
  "s_cbranch_scc0 .Lat_k0\n\t" \
  "s_add_u32 s81, s81, 1\n\t" \
  "s_cmp_lt_u32 s81, " FQ_CAP "\n\t" \
  "s_cbranch_scc1 .Lat_r0\n\t" \
  "buffer_store_dword v243, off, s[16:19], 0 offset:256 sc1\n\t" \
  "s_branch .Lat_k0\n\t" \
  ".Lat_s1:\n\t" \
  "s_mov_b32 s81, 0\n\t" \
  ".Lat_r1:\n\t" \
  "s_sleep 4\n\t" \
  "buffer_load_dword v246, v253, s[16:19], s65 offen sc1\n\t" \
  "s_waitcnt vmcnt(0)\n\t" \
  "v_cmp_ne_u32 vcc, s36, v246\n\t" \
  "s_and_b32 vcc_lo, vcc_lo, 0xfefe\n\t" \
  "s_cbranch_scc0 .Lat_k1\n\t" \
  "s_add_u32 s81, s81, 1\n\t" \
  "s_cmp_lt_u32 s81, " FQ_CAP "\n\t" \
  "s_cbranch_scc1 .Lat_r1\n\t" \
  "buffer_store_dword v243, off, s[16:19], 0 offset:256 sc1\n\t" \
  "s_branch .Lat_k1\n\t" \
  ".Lat_s2:\n\t" \
  "s_mov_b32 s81, 0\n\t" \
  ".Lat_r2:\n\t" \
  "s_sleep 4\n\t" \
  "buffer_load_dword v246, v253, s[16:19], s65 offen sc1\n\t" \
  "s_waitcnt vmcnt(0)\n\t" \
  "v_cmp_ne_u32 vcc, s36, v246\n\t" \
  "s_and_b32 vcc_lo, vcc_lo, 0xfe0000\n\t" \
  "s_cbranch_scc0 .Lat_k2\n\t" \
  "s_add_u32 s81, s81, 1\n\t" \
  "s_cmp_lt_u32 s81, " FQ_CAP "\n\t" \
  "s_cbranch_scc1 .Lat_r2\n\t" \
  "buffer_store_dword v243, off, s[16:19], 0 offset:256 sc1\n\t" \
  "s_branch .Lat_k2\n\t" \
  ".Lat_s3:\n\t" \
  "s_mov_b32 s81, 0\n\t" \
  ".Lat_r3:\n\t" \
  "s_sleep 4\n\t" \
  "s_add_u32 s78, s75, 0\n\t" \
  "buffer_load_dwordx4 v[208:211], v234, s[16:19], s78 offen sc1\n\t" \
  "s_add_u32 s78, s75, 32768\n\t" \
  "buffer_load_dwordx4 v[212:215], v234, s[16:19], s78 offen sc1\n\t" \
  "s_add_u32 s78, s75, 65536\n\t" \
  "buffer_load_dwordx4 v[216:219], v234, s[16:19], s78 offen sc1\n\t" \
  "s_add_u32 s78, s75, 98304\n\t" \
  "buffer_load_dwordx4 v[220:223], v234, s[16:19], s78 offen sc1\n\t" \
  "s_waitcnt vmcnt(0)\n\t" \
  "v_xor_b32 v246, v243, v209\n\t" \
  "v_bitop3_b32 v246, v246, v211, v243 bitop3:0xf6\n\t" \
  "v_bitop3_b32 v246, v246, v213, v243 bitop3:0xf6\n\t" \
  "v_bitop3_b32 v246, v246, v215, v243 bitop3:0xf6\n\t" \
  "v_bitop3_b32 v246, v246, v217, v243 bitop3:0xf6\n\t" \
  "v_bitop3_b32 v246, v246, v219, v243 bitop3:0xf6\n\t" \
  "v_bitop3_b32 v246, v246, v221, v243 bitop3:0xf6\n\t" \
  "v_bitop3_b32 v246, v246, v223, v243 bitop3:0xf6\n\t" \
  "v_cmp_ne_u32 vcc, 0, v246\n\t" \
  "s_cbranch_vccz .Lat_k3\n\t" \
  "s_add_u32 s81, s81, 1\n\t" \
  "s_cmp_lt_u32 s81, " FQ_CAP "\n\t" \
  "s_cbranch_scc1 .Lat_r3\n\t" \
  "buffer_store_dword v243, off, s[16:19], 0 offset:256 sc1\n\t" \
  "s_branch .Lat_k3\n\t" \
  ".Lat_s4:\n\t" \
  "s_mov_b32 s81, 0\n\t" \
  ".Lat_r4:\n\t" \
  "s_sleep 4\n\t" \
  "buffer_load_dword v246, v235, s[16:19], s73 offen sc1\n\t" \
  "s_waitcnt vmcnt(0)\n\t" \
  "v_cmp_ne_u32 vcc, s36, v246\n\t" \
  "s_cbranch_vccz .Lat_k4\n\t" \
  "s_add_u32 s81, s81, 1\n\t" \
  "s_cmp_lt_u32 s81, " FQ_CAP "\n\t" \
  "s_cbranch_scc1 .Lat_r4\n\t" \
  "buffer_store_dword v243, off, s[16:19], 0 offset:256 sc1\n\t" \
  "s_branch .Lat_k4\n\t" \
  ".Lat_m3:\n\t" \
  "s_waitcnt vmcnt(0)\n\t" \
  "v_cmp_ne_u32 vcc, s36, v251\n\t" \
  "s_mov_b32 s88, vcc_lo\n\t" \
  "s_cbranch_vccnz .Lat_n3\n\t" \
  "buffer_load_dwordx4 v[64:67], v242, s[16:19], s79 offen offset:0 sc1\n\t" \
  "buffer_load_dwordx4 v[68:71], v242, s[16:19], s79 offen offset:1024 sc1\n\t" \
  "buffer_load_dwordx4 v[72:75], v242, s[16:19], s79 offen offset:2048 sc1\n\t" \
  "buffer_load_dwordx4 v[76:79], v242, s[16:19], s79 offen offset:3072 sc1\n\t" \
  "s_branch .Lat_n3\n\t" \
  ".Lat_v:\n\t" \
  "s_mov_b32 s98, 0\n\t" \
  ".Lat_v1:\n\t" \
  "s_sleep 4\n\t" \
  "buffer_load_dword v209, v251, s[16:19], 0 offen sc1\n\t" \
  "s_waitcnt vmcnt(0)\n\t" \
  "v_and_b32 v250, 0xbfffbfff, v209\n\t" \
  "v_cmp_ne_u32_e64 s[84:85], s36, v250\n\t" \
  "s_cmp_lg_u64 s[84:85], 0\n\t" \
  "s_cbranch_scc0 .Lat_w\n\t" \
  "s_add_u32 s98, s98, 1\n\t" \
  "s_cmp_lt_u32 s98, " FQ_CAP "\n\t" \
  "s_cbranch_scc1 .Lat_v1\n\t" \
  "v_mov_b32 v250, s36\n\t" \
  "buffer_store_dword v250, off, s[16:19], 0 offset:256 sc1\n\t" \
  "s_branch .Lat_w\n\t" \
  ".Lat_s99:\n\t" \
  "s_mov_b32 s81, 0\n\t" \
  ".Lat_r99:\n\t" \
  "buffer_load_dword v246, off, s[16:19], s73 offset:32 sc1\n\t" \
  "s_waitcnt vmcnt(0)\n\t" \
  "v_cmp_ne_u32 vcc, s36, v246\n\t" \
  "s_cbranch_vccz .Lat_k99\n\t" \
  "s_add_u32 s81, s81, 1\n\t" \
  "s_cmp_lt_u32 s81, " FQ_CAP "\n\t" \
  "s_cbranch_scc1 .Lat_r99\n\t" \
  "buffer_store_dword v243, off, s[16:19], 0 offset:256 sc1\n\t" \
  ".Lat_k99:\n\t" \
  "buffer_load_dwordx4 v[64:67], v242, s[16:19], s79 offen offset:0 sc1\n\t" \
  "buffer_load_dwordx4 v[68:71], v242, s[16:19], s79 offen offset:1024 sc1\n\t" \
  "buffer_load_dwordx4 v[72:75], v242, s[16:19], s79 offen offset:2048 sc1\n\t" \
  "buffer_load_dwordx4 v[76:79], v242, s[16:19], s79 offen offset:3072 sc1\n\t" \
  "s_waitcnt vmcnt(0)\n\t" \
  "s_branch .Lat_d2\n\t" \
  ".Ltl_g2:\n\t" \
  "buffer_store_dword v243, off, s[16:19], 0 offset:256 sc1\n\t" \
  "s_branch .Ltl_k2\n\t" \
  "s_endpgm\n\t" \
  ".Lfz_z:\n\t" \
  ".fill 128, 4, 0xBF800000\n\t" \
  ".subsection 2\n\t" \
  ".Lsf_go:\n\t" \
  "s_barrier\n\t" \
  "v_mfma_f32_16x16x32_bf16 v[28:31], a[188:191], a[192:195], v[28:31]\n\t" \
  "ds_read_b64_tr_b16 a[156:157], v199 offset:32768\n\t" \
  "v_mfma_f32_16x16x32_bf16 v[28:31], a[124:127], a[208:211], v[28:31]\n\t" \
  "ds_read_b64_tr_b16 a[158:159], v199 offset:33792\n\t" \
  "v_mfma_f32_16x16x32_bf16 v[24:27], a[184:187], a[192:195], v[24:27]\n\t" \
  "ds_read_b64_tr_b16 a[92:93], v199 offset:40960\n\t" \
  "v_mfma_f32_16x16x32_bf16 v[24:27], a[120:123], a[208:211], v[24:27]\n\t" \
  "ds_read_b64_tr_b16 a[94:95], v199 offset:41984\n\t" \
  "v_mfma_f32_16x16x32_bf16 v[20:23], a[180:183], a[192:195], v[20:23]\n\t" \
  "ds_read_b64_tr_b16 a[152:153], v198 offset:32768\n\t" \
  "v_mfma_f32_16x16x32_bf16 v[20:23], a[116:119], a[208:211], v[20:23]\n\t" \
  "ds_read_b64_tr_b16 a[154:155], v198 offset:33792\n\t" \
  "v_mfma_f32_16x16x32_bf16 v[16:19], a[176:179], a[192:195], v[16:19]\n\t" \
  "ds_read_b64_tr_b16 a[88:89], v198 offset:40960\n\t" \
  "v_mfma_f32_16x16x32_bf16 v[16:19], a[112:115], a[208:211], v[16:19]\n\t" \
  "ds_read_b64_tr_b16 a[90:91], v198 offset:41984\n\t" \
  "v_mfma_f32_16x16x32_bf16 v[12:15], a[172:175], a[192:195], v[12:15]\n\t" \
  "ds_read_b64_tr_b16 a[148:149], v197 offset:32768\n\t" \
  "v_mfma_f32_16x16x32_bf16 v[12:15], a[108:111], a[208:211], v[12:15]\n\t" \
  "ds_read_b64_tr_b16 a[150:151], v197 offset:33792\n\t" \
  "v_mfma_f32_16x16x32_bf16 v[8:11], a[168:171], a[192:195], v[8:11]\n\t" \
  "ds_read_b64_tr_b16 a[84:85], v197 offset:40960\n\t" \
  "v_mfma_f32_16x16x32_bf16 v[8:11], a[104:107], a[208:211], v[8:11]\n\t" \
  "ds_read_b64_tr_b16 a[86:87], v197 offset:41984\n\t" \
  "v_mfma_f32_16x16x32_bf16 v[4:7], a[164:167], a[192:195], v[4:7]\n\t" \
  "ds_read_b64_tr_b16 a[144:145], v196 offset:32768\n\t" \
  "v_mfma_f32_16x16x32_bf16 v[4:7], a[100:103], a[208:211], v[4:7]\n\t" \
  "ds_read_b64_tr_b16 a[146:147], v196 offset:33792\n\t" \
  "v_mfma_f32_16x16x32_bf16 v[0:3], a[160:163], a[192:195], v[0:3]\n\t" \
  "ds_read_b64_tr_b16 a[80:81], v196 offset:40960\n\t" \
  "v_mfma_f32_16x16x32_bf16 v[0:3], a[96:99], a[208:211], v[0:3]\n\t" \
  "ds_read_b64_tr_b16 a[82:83], v196 offset:41984\n\t" \
  "v_mfma_f32_16x16x32_bf16 v[60:63], a[188:191], a[196:199], v[60:63]\n\t" \
  "ds_read_b64_tr_b16 a[140:141], v195 offset:32768\n\t" \
  "v_mfma_f32_16x16x32_bf16 v[60:63], a[124:127], a[212:215], v[60:63]\n\t" \
  "ds_read_b64_tr_b16 a[142:143], v195 offset:33792\n\t" \
  "v_mfma_f32_16x16x32_bf16 v[56:59], a[184:187], a[196:199], v[56:59]\n\t" \
  "ds_read_b64_tr_b16 a[76:77], v195 offset:40960\n\t" \
  "v_mfma_f32_16x16x32_bf16 v[56:59], a[120:123], a[212:215], v[56:59]\n\t" \
  "ds_read_b64_tr_b16 a[78:79], v195 offset:41984\n\t" \
  "v_mfma_f32_16x16x32_bf16 v[52:55], a[180:183], a[196:199], v[52:55]\n\t" \
  "ds_read_b64_tr_b16 a[136:137], v194 offset:32768\n\t" \
  "v_mfma_f32_16x16x32_bf16 v[52:55], a[116:119], a[212:215], v[52:55]\n\t" \
  "ds_read_b64_tr_b16 a[138:139], v194 offset:33792\n\t" \
  "v_mfma_f32_16x16x32_bf16 v[48:51], a[176:179], a[196:199], v[48:51]\n\t" \
  "ds_read_b64_tr_b16 a[72:73], v194 offset:40960\n\t" \
  "v_mfma_f32_16x16x32_bf16 v[48:51], a[112:115], a[212:215], v[48:51]\n\t" \
  "ds_read_b64_tr_b16 a[74:75], v194 offset:41984\n\t" \
  "v_mfma_f32_16x16x32_bf16 v[44:47], a[172:175], a[196:199], v[44:47]\n\t" \
  "ds_read_b64_tr_b16 a[132:133], v193 offset:32768\n\t" \
  "v_mfma_f32_16x16x32_bf16 v[44:47], a[108:111], a[212:215], v[44:47]\n\t" \
  "ds_read_b64_tr_b16 a[134:135], v193 offset:33792\n\t" \
  "v_mfma_f32_16x16x32_bf16 v[40:43], a[168:171], a[196:199], v[40:43]\n\t" \
  "ds_read_b64_tr_b16 a[68:69], v193 offset:40960\n\t" \
  "v_mfma_f32_16x16x32_bf16 v[40:43], a[104:107], a[212:215], v[40:43]\n\t" \
  "ds_read_b64_tr_b16 a[70:71], v193 offset:41984\n\t" \
  "v_mfma_f32_16x16x32_bf16 v[36:39], a[164:167], a[196:199], v[36:39]\n\t" \
  "ds_read_b64_tr_b16 a[128:129], v192 offset:32768\n\t" \
  "v_mfma_f32_16x16x32_bf16 v[36:39], a[100:103], a[212:215], v[36:39]\n\t" \
  "ds_read_b64_tr_b16 a[130:131], v192 offset:33792\n\t" \
  "v_mfma_f32_16x16x32_bf16 v[32:35], a[160:163], a[196:199], v[32:35]\n\t" \
  "ds_read_b64_tr_b16 a[64:65], v192 offset:40960\n\t" \
  "v_mfma_f32_16x16x32_bf16 v[32:35], a[96:99], a[212:215], v[32:35]\n\t" \
  "s_waitcnt lgkmcnt(15)\n\t" \
  "ds_read_b64_tr_b16 a[66:67], v192 offset:41984\n\t" \
  "v_mfma_f32_16x16x32_bf16 v[28:31], a[156:159], a[224:227], v[28:31]\n\t" \
  "v_mfma_f32_16x16x32_bf16 v[28:31], a[92:95], a[240:243], v[28:31]\n\t" \
  "v_mfma_f32_16x16x32_bf16 v[24:27], a[152:155], a[224:227], v[24:27]\n\t" \
  "s_waitcnt vmcnt(3)\n\t" \
  "s_nop 1\n\t" \
  "v_mfma_f32_16x16x32_bf16 v[24:27], a[88:91], a[240:243], v[24:27]\n\t" \
  "s_barrier\n\t" \
  "v_mfma_f32_16x16x32_bf16 v[20:23], a[148:151], a[224:227], v[20:23]\n\t" \
  "ds_read_b128 a[0:3], v220 offset:0\n\t" \
  "v_cvt_pk_bf16_f32 v31, v30, v31\n\t" \
  "v_mfma_f32_16x16x32_bf16 v[20:23], a[84:87], a[240:243], v[20:23]\n\t" \
  "ds_read_b128 a[4:7], v221 offset:0\n\t" \
  "v_cvt_pk_bf16_f32 v30, v28, v29\n\t" \
  "v_mfma_f32_16x16x32_bf16 v[16:19], a[144:147], a[224:227], v[16:19]\n\t" \
  "v_cvt_pk_bf16_f32 v28, v24, v25\n\t" \
  "v_cvt_pk_bf16_f32 v29, v26, v27\n\t" \
  "v_mfma_f32_16x16x32_bf16 v[16:19], a[80:83], a[240:243], v[16:19]\n\t" \
  "s_waitcnt lgkmcnt(10)\n\t" \
  "v_permlane16_swap_b32 v28, v30\n\t" \
  "v_mfma_f32_16x16x32_bf16 v[12:15], a[140:143], a[224:227], v[12:15]\n\t" \
  "ds_read_b128 a[8:11], v222 offset:0\n\t" \
  "v_cvt_pk_bf16_f32 v27, v22, v23\n\t" \
  "v_mfma_f32_16x16x32_bf16 v[12:15], a[76:79], a[240:243], v[12:15]\n\t" \
  "v_permlane16_swap_b32 v29, v31\n\t" \
  "v_mfma_f32_16x16x32_bf16 v[8:11], a[136:139], a[224:227], v[8:11]\n\t" \
  "ds_read_b128 a[12:15], v223 offset:0\n\t" \
  "v_cvt_pk_bf16_f32 v26, v20, v21\n\t" \
  "v_mfma_f32_16x16x32_bf16 v[8:11], a[72:75], a[240:243], v[8:11]\n\t" \
  "s_waitcnt lgkmcnt(4)\n\t" \
  "v_cvt_pk_bf16_f32 v24, v16, v17\n\t" \
  "v_cvt_pk_bf16_f32 v25, v18, v19\n\t" \
  "v_mfma_f32_16x16x32_bf16 v[4:7], a[132:135], a[224:227], v[4:7]\n\t" \
  "ds_read_b128 a[16:19], v220 offset:4096\n\t" \
  "v_cvt_pk_bf16_f32 v23, v14, v15\n\t" \
  "v_mfma_f32_16x16x32_bf16 v[4:7], a[68:71], a[240:243], v[4:7]\n\t" \
  "v_cvt_pk_bf16_f32 v22, v12, v13\n\t" \
  "v_cvt_pk_bf16_f32 v20, v8, v9\n\t" \
  "v_mfma_f32_16x16x32_bf16 v[0:3], a[128:131], a[224:227], v[0:3]\n\t" \
  "ds_read_b128 a[20:23], v221 offset:4096\n\t" \
  "v_mov_b32 v196, 1.0\n\t" \
  "v_mfma_f32_16x16x32_bf16 v[0:3], a[64:67], a[240:243], v[0:3]\n\t" \
  "ds_read_b128 a[24:27], v222 offset:4096\n\t" \
  "v_permlane16_swap_b32 v24, v26\n\t" \
  "v_mfma_f32_16x16x32_bf16 v[60:63], a[156:159], a[228:231], v[60:63]\n\t" \
  "v_mov_b32 v197, 1.0\n\t" \
  "v_cvt_pk_bf16_f32 v21, v10, v11\n\t" \
  "v_mfma_f32_16x16x32_bf16 v[60:63], a[92:95], a[244:247], v[60:63]\n\t" \
  "v_mov_b32 v198, 1.0\n\t" \
  "v_permlane16_swap_b32 v25, v27\n\t" \
  "v_mfma_f32_16x16x32_bf16 v[56:59], a[152:155], a[228:231], v[56:59]\n\t" \
  "ds_read_b128 a[28:31], v223 offset:4096\n\t" \
  "v_mov_b32 v199, 1.0\n\t" \
  "v_mfma_f32_16x16x32_bf16 v[56:59], a[88:91], a[244:247], v[56:59]\n\t" \
  "s_mov_b32 s99, 0\n\t" \
  "ds_read_b128 a[32:35], v220 offset:8192\n\t" \
  "v_cvt_pk_bf16_f32 v19, v6, v7\n\t" \
  "v_mfma_f32_16x16x32_bf16 v[52:55], a[148:151], a[228:231], v[52:55]\n\t" \
  "v_cvt_pk_bf16_f32 v18, v4, v5\n\t" \
  "v_permlane16_swap_b32 v20, v22\n\t" \
  "v_mfma_f32_16x16x32_bf16 v[52:55], a[84:87], a[244:247], v[52:55]\n\t" \
  "ds_read_b128 a[36:39], v221 offset:8192\n\t" \
  "v_cvt_pk_bf16_f32 v16, v0, v1\n\t" \
  "v_mfma_f32_16x16x32_bf16 v[48:51], a[144:147], a[228:231], v[48:51]\n\t" \
  "v_cvt_pk_bf16_f32 v17, v2, v3\n\t" \
  "v_permlane16_swap_b32 v21, v23\n\t" \
  "v_mfma_f32_16x16x32_bf16 v[48:51], a[80:83], a[244:247], v[48:51]\n\t" \
  "ds_read_b128 a[40:43], v222 offset:8192\n\t" \
  "v_cvt_pk_bf16_f32 v63, v62, v63\n\t" \
  "v_mfma_f32_16x16x32_bf16 v[44:47], a[140:143], a[228:231], v[44:47]\n\t" \
  "v_permlane16_swap_b32 v16, v18\n\t" \
  "v_mfma_f32_16x16x32_bf16 v[44:47], a[76:79], a[244:247], v[44:47]\n\t" \
  "ds_read_b128 a[44:47], v223 offset:8192\n\t" \
  "v_permlane16_swap_b32 v17, v19\n\t" \
  "v_mfma_f32_16x16x32_bf16 v[40:43], a[136:139], a[228:231], v[40:43]\n\t" \
  "v_cvt_pk_bf16_f32 v62, v60, v61\n\t" \
  "v_cvt_pk_bf16_f32 v60, v56, v57\n\t" \
  "v_mfma_f32_16x16x32_bf16 v[40:43], a[72:75], a[244:247], v[40:43]\n\t" \
  "ds_read_b128 a[48:51], v220 offset:12288\n\t" \
  "v_cvt_pk_bf16_f32 v61, v58, v59\n\t" \
  "v_mfma_f32_16x16x32_bf16 v[36:39], a[132:135], a[228:231], v[36:39]\n\t" \
  "v_cvt_pk_bf16_f32 v59, v54, v55\n\t" \
  "v_cvt_pk_bf16_f32 v58, v52, v53\n\t" \
  "v_mfma_f32_16x16x32_bf16 v[36:39], a[68:71], a[244:247], v[36:39]\n\t" \
  "ds_read_b128 a[52:55], v221 offset:12288\n\t" \
  "v_cvt_pk_bf16_f32 v56, v48, v49\n\t" \
  "v_mfma_f32_16x16x32_bf16 v[32:35], a[128:131], a[228:231], v[32:35]\n\t" \
  "ds_read_b128 a[56:59], v222 offset:12288\n\t" \
  "v_cvt_pk_bf16_f32 v57, v50, v51\n\t" \
  "v_mfma_f32_16x16x32_bf16 v[32:35], a[64:67], a[244:247], v[32:35]\n\t" \
  "s_waitcnt lgkmcnt(11)\n\t" \
  "ds_read_b128 a[60:63], v223 offset:12288\n\t" \
  "v_cvt_pk_bf16_f32 v55, v46, v47\n\t" \
  "v_mfma_f32_16x16x32_bf16 v[0:3], a[0:3], v[16:19], 0\n\t" \
  "v_cvt_pk_bf16_f32 v54, v44, v45\n\t" \
  "v_cvt_pk_bf16_f32 v52, v40, v41\n\t" \
  "v_mfma_f32_16x16x32_bf16 v[0:3], a[4:7], v[20:23], v[0:3]\n\t" \
  "v_permlane16_swap_b32 v60, v62\n\t" \
  "v_mfma_f32_16x16x32_bf16 v[0:3], a[8:11], v[24:27], v[0:3]\n\t" \
  "v_permlane16_swap_b32 v61, v63\n\t" \
  "v_mfma_f32_16x16x32_bf16 v[0:3], a[12:15], v[28:31], v[0:3]\n\t" \
  "s_waitcnt lgkmcnt(8)\n\t" \
  "v_permlane16_swap_b32 v56, v58\n\t" \
  "v_mfma_f32_16x16x32_bf16 v[4:7], a[16:19], v[16:19], 0\n\t" \
  "s_add_u32 m0, s38, 49152\n\t" \
  "v_cvt_pk_bf16_f32 v53, v42, v43\n\t" \
  "v_cvt_pk_bf16_f32 v51, v38, v39\n\t" \
  "v_mfma_f32_16x16x32_bf16 v[4:7], a[20:23], v[20:23], v[4:7]\n\t" \
  "s_add_u32 s96, s38, 32768\n\t" \
  "v_permlane16_swap_b32 v57, v59\n\t" \
  "v_mfma_f32_16x16x32_bf16 v[4:7], a[24:27], v[24:27], v[4:7]\n\t" \
  "s_waitcnt lgkmcnt(0)\n\t" \
  "v_cvt_pk_bf16_f32 v50, v36, v37\n\t" \
  "v_cvt_pk_bf16_f32 v48, v32, v33\n\t" \
  "v_mfma_f32_16x16x32_bf16 v[4:7], a[28:31], v[28:31], v[4:7]\n\t" \
  "s_barrier\n\t" \
  "v_mfma_f32_16x16x32_bf16 v[92:95], a[188:191], a[200:203], v[92:95]\n\t" \
  "buffer_load_dwordx4 v224, s[60:63], s96 offen offset:0 lds\n\t" \
  "v_mfma_f32_16x16x32_bf16 v[92:95], a[124:127], a[216:219], v[92:95]\n\t" \
  "v_cvt_pk_bf16_f32 v49, v34, v35\n\t" \
  "v_max3_f32 v200, v0, v1, v2\n\t" \
  "v_mfma_f32_16x16x32_bf16 v[88:91], a[184:187], a[200:203], v[88:91]\n\t" \
  "v_permlane16_swap_b32 v52, v54\n\t" \
  "v_mfma_f32_16x16x32_bf16 v[88:91], a[120:123], a[216:219], v[88:91]\n\t" \
  "v_max3_f32 v200, v200, v3, v4\n\t" \
  "v_max3_f32 v200, v200, v5, v6\n\t" \
  "v_mfma_f32_16x16x32_bf16 v[84:87], a[180:183], a[200:203], v[84:87]\n\t" \
  "v_max_f32 v200, v200, v7\n\t" \
  "v_mov_b32 v201, v200\n\t" \
  "v_mfma_f32_16x16x32_bf16 v[84:87], a[116:119], a[216:219], v[84:87]\n\t" \
  "v_permlane16_swap_b32 v53, v55\n\t" \
  "v_mfma_f32_16x16x32_bf16 v[80:83], a[176:179], a[200:203], v[80:83]\n\t" \
  "v_permlane32_swap_b32 v200, v201\n\t" \
  "v_mfma_f32_16x16x32_bf16 v[80:83], a[112:115], a[216:219], v[80:83]\n\t" \
  "v_max_f32 v200, v200, v201\n\t" \
  "v_mov_b32 v201, v200\n\t" \
  "v_mfma_f32_16x16x32_bf16 v[76:79], a[172:175], a[200:203], v[76:79]\n\t" \
  "v_permlane16_swap_b32 v48, v50\n\t" \
  "v_mfma_f32_16x16x32_bf16 v[76:79], a[108:111], a[216:219], v[76:79]\n\t" \
  "v_permlane16_swap_b32 v200, v201\n\t" \
  "v_mfma_f32_16x16x32_bf16 v[72:75], a[168:171], a[200:203], v[72:75]\n\t" \
  "v_max_f32 v200, v200, v201\n\t" \
  "v_sub_f32 v176, 0xc2c00000, v200\n\t" \
  "v_mfma_f32_16x16x32_bf16 v[72:75], a[104:107], a[216:219], v[72:75]\n\t" \
  "v_sub_f32 v177, 0xc2c00000, v200\n\t" \
  "v_sub_f32 v178, 0xc2c00000, v200\n\t" \
  "v_mfma_f32_16x16x32_bf16 v[68:71], a[164:167], a[200:203], v[68:71]\n\t" \
  "v_sub_f32 v179, 0xc2c00000, v200\n\t" \
  "v_mfma_f32_16x16x32_bf16 v[68:71], a[100:103], a[216:219], v[68:71]\n\t" \
  "v_permlane16_swap_b32 v49, v51\n\t" \
  "v_mfma_f32_16x16x32_bf16 v[64:67], a[160:163], a[200:203], v[64:67]\n\t" \
  "v_add_f32 v0, v0, v176\n\t" \
  "v_add_f32 v1, v1, v176\n\t" \
  "v_mfma_f32_16x16x32_bf16 v[64:67], a[96:99], a[216:219], v[64:67]\n\t" \
  "v_add_f32 v2, v2, v176\n\t" \
  "v_add_f32 v3, v3, v176\n\t" \
  "v_mfma_f32_16x16x32_bf16 v[8:11], a[32:35], v[16:19], v[176:179]\n\t" \
  "buffer_load_dwordx4 v225, s[60:63], s96 offen offset:1024 lds\n\t" \
  "v_mfma_f32_16x16x32_bf16 v[8:11], a[36:39], v[20:23], v[8:11]\n\t" \
  "v_add_f32 v4, v4, v176\n\t" \
  "v_add_f32 v5, v5, v176\n\t" \
  "v_mfma_f32_16x16x32_bf16 v[8:11], a[40:43], v[24:27], v[8:11]\n\t" \
  "v_add_f32 v6, v6, v176\n\t" \
  "v_add_f32 v7, v7, v176\n\t" \
  "v_mfma_f32_16x16x32_bf16 v[8:11], a[44:47], v[28:31], v[8:11]\n\t" \
  "s_nop 2\n\t" \
  "v_mfma_f32_16x16x32_bf16 v[12:15], a[48:51], v[16:19], v[176:179]\n\t" \
  "s_nop 2\n\t" \
  "v_mfma_f32_16x16x32_bf16 v[12:15], a[52:55], v[20:23], v[12:15]\n\t" \
  "v_max3_f32 v204, v8, v9, v10\n\t" \
  "v_mfma_f32_16x16x32_bf16 v[12:15], a[56:59], v[24:27], v[12:15]\n\t" \
  "s_nop 2\n\t" \
  "v_mfma_f32_16x16x32_bf16 v[12:15], a[60:63], v[28:31], v[12:15]\n\t" \
  "s_nop 2\n\t" \
  "v_mfma_f32_16x16x32_bf16 v[32:35], a[0:3], v[48:51], 0\n\t" \
  "s_nop 2\n\t" \
  "v_mfma_f32_16x16x32_bf16 v[32:35], a[4:7], v[52:55], v[32:35]\n\t" \
  "v_max3_f32 v203, v11, v12, v13\n\t" \
  "v_max3_f32 v204, v204, v14, v15\n\t" \
  "v_mfma_f32_16x16x32_bf16 v[32:35], a[8:11], v[56:59], v[32:35]\n\t" \
  "v_max_f32 v204, v204, v203\n\t" \
  "v_cmp_lt_f32 vcc, 0x42800000, v204\n\t" \
  "v_mfma_f32_16x16x32_bf16 v[32:35], a[12:15], v[60:63], v[32:35]\n\t" \
  "s_nop 2\n\t" \
  "v_mfma_f32_16x16x32_bf16 v[36:39], a[16:19], v[48:51], 0\n\t" \
  "s_cbranch_vccnz .Lsf_c00\n\t" \
  ".Lsf_r00:\n\t" \
  "v_mfma_f32_16x16x32_bf16 v[36:39], a[20:23], v[52:55], v[36:39]\n\t" \
  "v_exp_f32 v0, v0\n\t" \
  "v_mfma_f32_16x16x32_bf16 v[36:39], a[24:27], v[56:59], v[36:39]\n\t" \
  "v_exp_f32 v1, v1\n\t" \
  "v_mfma_f32_16x16x32_bf16 v[36:39], a[28:31], v[60:63], v[36:39]\n\t" \
  "v_max3_f32 v200, v32, v33, v34\n\t" \
  "v_cvt_pk_bf16_f32 v128, v0, v1\n\t" \
  "v_mfma_f32_16x16x32_bf16 v[92:95], a[156:159], a[232:235], v[92:95]\n\t" \
  "buffer_load_dwordx4 v226, s[60:63], s96 offen offset:2048 lds\n\t" \
  "v_mfma_f32_16x16x32_bf16 v[92:95], a[92:95], a[248:251], v[92:95]\n\t" \
  "v_exp_f32 v2, v2\n\t" \
  "v_mfma_f32_16x16x32_bf16 v[88:91], a[152:155], a[232:235], v[88:91]\n\t" \
  "v_exp_f32 v3, v3\n\t" \
  "v_mfma_f32_16x16x32_bf16 v[88:91], a[88:91], a[248:251], v[88:91]\n\t" \
  "v_max3_f32 v200, v200, v35, v36\n\t" \
  "v_max3_f32 v200, v200, v37, v38\n\t" \
  "v_mfma_f32_16x16x32_bf16 v[84:87], a[148:151], a[232:235], v[84:87]\n\t" \
  "v_max_f32 v200, v200, v39\n\t" \
  "v_mov_b32 v201, v200\n\t" \
  "v_mfma_f32_16x16x32_bf16 v[84:87], a[84:87], a[248:251], v[84:87]\n\t" \
  "v_cvt_pk_bf16_f32 v95, v94, v95\n\t" \
  "v_cvt_pk_bf16_f32 v94, v92, v93\n\t" \
  "v_mfma_f32_16x16x32_bf16 v[80:83], a[144:147], a[232:235], v[80:83]\n\t" \
  "v_cvt_pk_bf16_f32 v92, v88, v89\n\t" \
  "v_permlane32_swap_b32 v200, v201\n\t" \
  "v_mfma_f32_16x16x32_bf16 v[80:83], a[80:83], a[248:251], v[80:83]\n\t" \
  "v_max_f32 v200, v200, v201\n\t" \
  "v_mov_b32 v201, v200\n\t" \
  "v_mfma_f32_16x16x32_bf16 v[76:79], a[140:143], a[232:235], v[76:79]\n\t" \
  "v_cvt_pk_bf16_f32 v93, v90, v91\n\t" \
  "v_cvt_pk_bf16_f32 v91, v86, v87\n\t" \
  "v_mfma_f32_16x16x32_bf16 v[76:79], a[76:79], a[248:251], v[76:79]\n\t" \
  "v_permlane16_swap_b32 v200, v201\n\t" \
  "v_mfma_f32_16x16x32_bf16 v[72:75], a[136:139], a[232:235], v[72:75]\n\t" \
  "v_max_f32 v200, v200, v201\n\t" \
  "v_sub_f32 v180, 0xc2c00000, v200\n\t" \
  "v_mfma_f32_16x16x32_bf16 v[72:75], a[72:75], a[248:251], v[72:75]\n\t" \
  "v_sub_f32 v181, 0xc2c00000, v200\n\t" \
  "v_sub_f32 v182, 0xc2c00000, v200\n\t" \
  "v_mfma_f32_16x16x32_bf16 v[68:71], a[132:135], a[232:235], v[68:71]\n\t" \
  "v_cvt_pk_bf16_f32 v90, v84, v85\n\t" \
  "v_sub_f32 v183, 0xc2c00000, v200\n\t" \
  "v_mfma_f32_16x16x32_bf16 v[68:71], a[68:71], a[248:251], v[68:71]\n\t" \
  "v_cvt_pk_bf16_f32 v88, v80, v81\n\t" \
  "v_cvt_pk_bf16_f32 v89, v82, v83\n\t" \
  "v_mfma_f32_16x16x32_bf16 v[64:67], a[128:131], a[232:235], v[64:67]\n\t" \
  "v_cvt_pk_bf16_f32 v87, v78, v79\n\t" \
  "v_permlane16_swap_b32 v92, v94\n\t" \
  "v_mfma_f32_16x16x32_bf16 v[64:67], a[64:67], a[248:251], v[64:67]\n\t" \
  "v_cvt_pk_bf16_f32 v86, v76, v77\n\t" \
  "v_permlane16_swap_b32 v93, v95\n\t" \
  "v_mfma_f32_16x16x32_bf16 v[124:127], a[188:191], a[204:207], v[124:127]\n\t" \
  "buffer_load_dwordx4 v227, s[60:63], s96 offen offset:3072 lds\n\t" \
  "v_mfma_f32_16x16x32_bf16 v[124:127], a[124:127], a[220:223], v[124:127]\n\t" \
  "v_cvt_pk_bf16_f32 v84, v72, v73\n\t" \
  "v_permlane16_swap_b32 v88, v90\n\t" \
  "v_mfma_f32_16x16x32_bf16 v[120:123], a[184:187], a[204:207], v[120:123]\n\t" \
  "v_cvt_pk_bf16_f32 v85, v74, v75\n\t" \
  "v_cvt_pk_bf16_f32 v83, v70, v71\n\t" \
  "v_mfma_f32_16x16x32_bf16 v[120:123], a[120:123], a[220:223], v[120:123]\n\t" \
  "v_cvt_pk_bf16_f32 v82, v68, v69\n\t" \
  "v_permlane16_swap_b32 v89, v91\n\t" \
  "v_mfma_f32_16x16x32_bf16 v[116:119], a[180:183], a[204:207], v[116:119]\n\t" \
  "v_cvt_pk_bf16_f32 v80, v64, v65\n\t" \
  "v_permlane16_swap_b32 v84, v86\n\t" \
  "v_mfma_f32_16x16x32_bf16 v[116:119], a[116:119], a[220:223], v[116:119]\n\t" \
  "v_exp_f32 v4, v4\n\t" \
  "v_mfma_f32_16x16x32_bf16 v[112:115], a[176:179], a[204:207], v[112:115]\n\t" \
  "v_permlane16_swap_b32 v85, v87\n\t" \
  "v_mfma_f32_16x16x32_bf16 v[112:115], a[112:115], a[220:223], v[112:115]\n\t" \
  "v_cvt_pk_bf16_f32 v81, v66, v67\n\t" \
  "v_add_f32 v32, v32, v180\n\t" \
  "v_mfma_f32_16x16x32_bf16 v[108:111], a[172:175], a[204:207], v[108:111]\n\t" \
  "v_exp_f32 v5, v5\n\t" \
  "v_mfma_f32_16x16x32_bf16 v[108:111], a[108:111], a[220:223], v[108:111]\n\t" \
  "v_permlane16_swap_b32 v80, v82\n\t" \
  "v_mfma_f32_16x16x32_bf16 v[104:107], a[168:171], a[204:207], v[104:107]\n\t" \
  "v_exp_f32 v6, v6\n\t" \
  "v_mfma_f32_16x16x32_bf16 v[104:107], a[104:107], a[220:223], v[104:107]\n\t" \
  "v_add_f32 v33, v33, v180\n\t" \
  "v_add_f32 v34, v34, v180\n\t" \
  "v_mfma_f32_16x16x32_bf16 v[100:103], a[164:167], a[204:207], v[100:103]\n\t" \
  "s_add_u32 m0, s38, 147456\n\t" \
  "v_permlane16_swap_b32 v81, v83\n\t" \
  "v_mfma_f32_16x16x32_bf16 v[100:103], a[100:103], a[220:223], v[100:103]\n\t" \
  "s_add_u32 s96, s38, 16384\n\t" \
  "v_exp_f32 v7, v7\n\t" \
  "v_mfma_f32_16x16x32_bf16 v[96:99], a[160:163], a[204:207], v[96:99]\n\t" \
  "v_exp_f32 v8, v8\n\t" \
  "v_mfma_f32_16x16x32_bf16 v[96:99], a[96:99], a[220:223], v[96:99]\n\t" \
  "v_add_f32 v35, v35, v180\n\t" \
  "v_add_f32 v36, v36, v180\n\t" \
  "v_mfma_f32_16x16x32_bf16 v[124:127], a[156:159], a[236:239], v[124:127]\n\t" \
  "buffer_load_dwordx4 v230, s[64:67], s96 offen offset:0 lds\n\t" \
  "v_mfma_f32_16x16x32_bf16 v[124:127], a[92:95], a[252:255], v[124:127]\n\t" \
  "v_exp_f32 v9, v9\n\t" \
  "v_mfma_f32_16x16x32_bf16 v[120:123], a[152:155], a[236:239], v[120:123]\n\t" \
  "v_exp_f32 v10, v10\n\t" \
  "v_mfma_f32_16x16x32_bf16 v[120:123], a[88:91], a[252:255], v[120:123]\n\t" \
  "v_add_f32 v37, v37, v180\n\t" \
  "v_add_f32 v38, v38, v180\n\t" \
  "v_mfma_f32_16x16x32_bf16 v[116:119], a[148:151], a[236:239], v[116:119]\n\t" \
  "v_add_f32 v39, v39, v180\n\t" \
  "v_cvt_pk_bf16_f32 v127, v126, v127\n\t" \
  "v_mfma_f32_16x16x32_bf16 v[116:119], a[84:87], a[252:255], v[116:119]\n\t" \
  "v_cvt_pk_bf16_f32 v126, v124, v125\n\t" \
  "v_cvt_pk_bf16_f32 v129, v2, v3\n\t" \
  "v_mfma_f32_16x16x32_bf16 v[112:115], a[144:147], a[236:239], v[112:115]\n\t" \
  "v_cvt_pk_bf16_f32 v124, v120, v121\n\t" \
  "v_cvt_pk_bf16_f32 v125, v122, v123\n\t" \
  "v_mfma_f32_16x16x32_bf16 v[112:115], a[80:83], a[252:255], v[112:115]\n\t" \
  "v_permlane16_swap_b32 v124, v126\n\t" \
  "v_mfma_f32_16x16x32_bf16 v[108:111], a[140:143], a[236:239], v[108:111]\n\t" \
  "v_cvt_pk_bf16_f32 v123, v118, v119\n\t" \
  "v_permlane16_swap_b32 v125, v127\n\t" \
  "v_mfma_f32_16x16x32_bf16 v[108:111], a[76:79], a[252:255], v[108:111]\n\t" \
  "v_exp_f32 v11, v11\n\t" \
  "v_cvt_pk_bf16_f32 v122, v116, v117\n\t" \
  "v_mfma_f32_16x16x32_bf16 v[104:107], a[136:139], a[236:239], v[104:107]\n\t" \
  "v_exp_f32 v12, v12\n\t" \
  "v_cvt_pk_bf16_f32 v120, v112, v113\n\t" \
  "v_cvt_pk_bf16_f32 v121, v114, v115\n\t" \
  "v_mfma_f32_16x16x32_bf16 v[104:107], a[72:75], a[252:255], v[104:107]\n\t" \
  "s_nop 0\n\t" \
  "v_cvt_pk_bf16_f32 v119, v110, v111\n\t" \
  "v_permlane16_swap_b32 v120, v122\n\t" \
  "v_cvt_pk_bf16_f32 v118, v108, v109\n\t" \
  "v_mfma_f32_16x16x32_bf16 v[100:103], a[132:135], a[236:239], v[100:103]\n\t" \
  "v_permlane16_swap_b32 v121, v123\n\t" \
  "v_mfma_f32_16x16x32_bf16 v[100:103], a[68:71], a[252:255], v[100:103]\n\t" \
  "v_cvt_pk_bf16_f32 v130, v4, v5\n\t" \
  "v_cvt_pk_bf16_f32 v116, v104, v105\n\t" \
  "v_mfma_f32_16x16x32_bf16 v[96:99], a[128:131], a[236:239], v[96:99]\n\t" \
  "v_cvt_pk_bf16_f32 v117, v106, v107\n\t" \
  "v_cvt_pk_bf16_f32 v131, v6, v7\n\t" \
  "v_mfma_f32_16x16x32_bf16 v[96:99], a[64:67], a[252:255], v[96:99]\n\t" \
  "v_cvt_pk_bf16_f32 v132, v8, v9\n\t" \
  "v_permlane16_swap_b32 v116, v118\n\t" \
  "v_cvt_pk_bf16_f32 v115, v102, v103\n\t" \
  "v_mfma_f32_16x16x32_bf16 v[64:67], a[0:3], v[80:83], 0\n\t" \
  "buffer_load_dwordx4 v231, s[64:67], s96 offen offset:1024 lds\n\t" \
  "v_mfma_f32_16x16x32_bf16 v[64:67], a[4:7], v[84:87], v[64:67]\n\t" \
  "ds_read_b128 a[192:195], v220 offset:16384\n\t" \
  "v_permlane16_swap_b32 v117, v119\n\t" \
  "v_mfma_f32_16x16x32_bf16 v[64:67], a[8:11], v[88:91], v[64:67]\n\t" \
  "ds_read_b128 a[196:199], v221 offset:16384\n\t" \
  "v_exp_f32 v13, v13\n\t" \
  "v_mfma_f32_16x16x32_bf16 v[64:67], a[12:15], v[92:95], v[64:67]\n\t" \
  "ds_read_b128 a[200:203], v222 offset:16384\n\t" \
  "v_cvt_pk_bf16_f32 v114, v100, v101\n\t" \
  "v_mfma_f32_16x16x32_bf16 v[68:71], a[16:19], v[80:83], 0\n\t" \
  "ds_read_b128 a[204:207], v223 offset:16384\n\t" \
  "v_cvt_pk_bf16_f32 v112, v96, v97\n\t" \
  "v_mfma_f32_16x16x32_bf16 v[68:71], a[20:23], v[84:87], v[68:71]\n\t" \
  "ds_read_b128 a[208:211], v220 offset:20480\n\t" \
  "v_exp_f32 v14, v14\n\t" \
  "v_max3_f32 v200, v64, v65, v66\n\t" \
  "v_mfma_f32_16x16x32_bf16 v[68:71], a[24:27], v[88:91], v[68:71]\n\t" \
  "ds_read_b128 a[212:215], v221 offset:20480\n\t" \
  "v_cvt_pk_bf16_f32 v113, v98, v99\n\t" \
  "v_mfma_f32_16x16x32_bf16 v[68:71], a[28:31], v[92:95], v[68:71]\n\t" \
  "v_permlane16_swap_b32 v112, v114\n\t" \
  "v_mfma_f32_16x16x32_bf16 v[40:43], a[32:35], v[48:51], v[180:183]\n\t" \
  "v_permlane16_swap_b32 v113, v115\n\t" \
  "v_mfma_f32_16x16x32_bf16 v[40:43], a[36:39], v[52:55], v[40:43]\n\t" \
  "s_nop 3\n\t" \
  "v_max3_f32 v200, v200, v67, v68\n\t" \
  "v_mfma_f32_16x16x32_bf16 v[40:43], a[40:43], v[56:59], v[40:43]\n\t" \
  "v_max3_f32 v200, v200, v69, v70\n\t" \
  "v_exp_f32 v15, v15\n\t" \
  "v_max_f32 v200, v200, v71\n\t" \
  "v_mov_b32 v201, v200\n\t" \
  "v_mfma_f32_16x16x32_bf16 v[40:43], a[44:47], v[60:63], v[40:43]\n\t" \
  "ds_read_b128 a[216:219], v222 offset:20480\n\t" \
  "v_permlane32_swap_b32 v200, v201\n\t" \
  "v_max_f32 v200, v200, v201\n\t" \
  "v_mov_b32 v201, v200\n\t" \
  "v_mfma_f32_16x16x32_bf16 v[44:47], a[48:51], v[48:51], v[180:183]\n\t" \
  "s_nop 0\n\t" \
  "v_permlane16_swap_b32 v200, v201\n\t" \
  "v_max_f32 v200, v200, v201\n\t" \
  "v_sub_f32 v184, 0xc2c00000, v200\n\t" \
  "v_sub_f32 v185, 0xc2c00000, v200\n\t" \
  "v_mfma_f32_16x16x32_bf16 v[44:47], a[52:55], v[52:55], v[44:47]\n\t" \
  "ds_read_b128 a[220:223], v223 offset:20480\n\t" \
  "v_max3_f32 v204, v40, v41, v42\n\t" \
  "v_sub_f32 v186, 0xc2c00000, v200\n\t" \
  "v_sub_f32 v187, 0xc2c00000, v200\n\t" \
  "v_mfma_f32_16x16x32_bf16 v[44:47], a[56:59], v[56:59], v[44:47]\n\t" \
  "ds_read_b128 a[224:227], v220 offset:24576\n\t" \
  "v_add_f32 v64, v64, v184\n\t" \
  "v_add_f32 v65, v65, v184\n\t" \
  "v_mfma_f32_16x16x32_bf16 v[44:47], a[60:63], v[60:63], v[44:47]\n\t" \
  "v_add_f32 v66, v66, v184\n\t" \
  "v_add_f32 v67, v67, v184\n\t" \
  "v_mfma_f32_16x16x32_bf16 v[96:99], a[0:3], v[112:115], 0\n\t" \
  "buffer_load_dwordx4 v230, s[64:67], s96 offen offset:2048 lds\n\t" \
  "v_mfma_f32_16x16x32_bf16 v[96:99], a[4:7], v[116:119], v[96:99]\n\t" \
  "ds_read_b128 a[228:231], v221 offset:24576\n\t" \
  "v_add_f32 v68, v68, v184\n\t" \
  "s_nop 0\n\t" \
  "v_max3_f32 v203, v43, v44, v45\n\t" \
  "v_max3_f32 v204, v204, v46, v47\n\t" \
  "v_mfma_f32_16x16x32_bf16 v[96:99], a[8:11], v[120:123], v[96:99]\n\t" \
  "ds_read_b128 a[232:235], v222 offset:24576\n\t" \
  "v_add_f32 v69, v69, v184\n\t" \
  "v_max_f32 v204, v204, v203\n\t" \
  "v_cmp_lt_f32 vcc, 0x42800000, v204\n\t" \
  "v_mfma_f32_16x16x32_bf16 v[96:99], a[12:15], v[124:127], v[96:99]\n\t" \
  "v_add_f32 v70, v70, v184\n\t" \
  "v_add_f32 v71, v71, v184\n\t" \
  "v_mfma_f32_16x16x32_bf16 v[100:103], a[16:19], v[112:115], 0\n\t" \
  "s_cbranch_vccnz .Lsf_c01\n\t" \
  ".Lsf_r01:\n\t" \
  "v_mfma_f32_16x16x32_bf16 v[100:103], a[20:23], v[116:119], v[100:103]\n\t" \
  "ds_read_b128 a[236:239], v223 offset:24576\n\t" \
  "v_exp_f32 v32, v32\n\t" \
  "v_exp_f32 v33, v33\n\t" \
  "v_mfma_f32_16x16x32_bf16 v[100:103], a[24:27], v[120:123], v[100:103]\n\t" \
  "v_max3_f32 v200, v96, v97, v98\n\t" \
  "v_exp_f32 v34, v34\n\t" \
  "v_exp_f32 v35, v35\n\t" \
  "v_mfma_f32_16x16x32_bf16 v[100:103], a[28:31], v[124:127], v[100:103]\n\t" \
  "ds_read_b128 a[240:243], v220 offset:28672\n\t" \
  "v_exp_f32 v36, v36\n\t" \
  "v_exp_f32 v37, v37\n\t" \
  "v_mfma_f32_16x16x32_bf16 v[72:75], a[32:35], v[80:83], v[184:187]\n\t" \
  "ds_read_b128 a[244:247], v221 offset:28672\n\t" \
  "v_exp_f32 v38, v38\n\t" \
  "v_exp_f32 v39, v39\n\t" \
  "v_mfma_f32_16x16x32_bf16 v[72:75], a[36:39], v[84:87], v[72:75]\n\t" \
  "v_max3_f32 v200, v200, v99, v100\n\t" \
  "v_max3_f32 v200, v200, v101, v102\n\t" \
  "v_max_f32 v200, v200, v103\n\t" \
  "v_mov_b32 v201, v200\n\t" \
  "v_mfma_f32_16x16x32_bf16 v[72:75], a[40:43], v[88:91], v[72:75]\n\t" \
  "ds_read_b128 a[248:251], v222 offset:28672\n\t" \
  "v_permlane32_swap_b32 v200, v201\n\t" \
  "v_max_f32 v200, v200, v201\n\t" \
  "v_mov_b32 v201, v200\n\t" \
  "v_mfma_f32_16x16x32_bf16 v[72:75], a[44:47], v[92:95], v[72:75]\n\t" \
  "s_nop 0\n\t" \
  "v_permlane16_swap_b32 v200, v201\n\t" \
  "v_max_f32 v200, v200, v201\n\t" \
  "v_sub_f32 v188, 0xc2c00000, v200\n\t" \
  "v_sub_f32 v189, 0xc2c00000, v200\n\t" \
  "v_mfma_f32_16x16x32_bf16 v[76:79], a[48:51], v[80:83], v[184:187]\n\t" \
  "ds_read_b128 a[252:255], v223 offset:28672\n\t" \
  "v_sub_f32 v190, 0xc2c00000, v200\n\t" \
  "v_exp_f32 v40, v40\n\t" \
  "v_sub_f32 v191, 0xc2c00000, v200\n\t" \
  "v_mfma_f32_16x16x32_bf16 v[76:79], a[52:55], v[84:87], v[76:79]\n\t" \
  "v_max3_f32 v204, v72, v73, v74\n\t" \
  "v_exp_f32 v41, v41\n\t" \
  "v_exp_f32 v42, v42\n\t" \
  "v_mfma_f32_16x16x32_bf16 v[76:79], a[56:59], v[88:91], v[76:79]\n\t" \
  "v_add_f32 v96, v96, v188\n\t" \
  "v_exp_f32 v43, v43\n\t" \
  "v_exp_f32 v44, v44\n\t" \
  "v_mfma_f32_16x16x32_bf16 v[76:79], a[60:63], v[92:95], v[76:79]\n\t" \
  "v_add_f32 v97, v97, v188\n\t" \
  "v_exp_f32 v45, v45\n\t" \
  "v_exp_f32 v46, v46\n\t" \
  "v_mfma_f32_16x16x32_bf16 v[104:107], a[32:35], v[112:115], v[188:191]\n\t" \
  "buffer_load_dwordx4 v231, s[64:67], s96 offen offset:3072 lds\n\t" \
  "v_exp_f32 v47, v47\n\t" \
  "v_mfma_f32_16x16x32_bf16 v[104:107], a[36:39], v[116:119], v[104:107]\n\t" \
  "s_nop 0\n\t" \
  "v_max3_f32 v203, v75, v76, v77\n\t" \
  "v_max3_f32 v204, v204, v78, v79\n\t" \
  "v_max_f32 v204, v204, v203\n\t" \
  "v_cmp_lt_f32 vcc, 0x42800000, v204\n\t" \
  "v_mfma_f32_16x16x32_bf16 v[104:107], a[40:43], v[120:123], v[104:107]\n\t" \
  "v_cvt_pk_bf16_f32 v133, v10, v11\n\t" \
  "v_cvt_pk_bf16_f32 v134, v12, v13\n\t" \
  "v_add_f32 v98, v98, v188\n\t" \
  "v_mfma_f32_16x16x32_bf16 v[104:107], a[44:47], v[124:127], v[104:107]\n\t" \
  "v_add_f32 v99, v99, v188\n\t" \
  "v_add_f32 v100, v100, v188\n\t" \
  "s_cbranch_vccnz .Lsf_c02\n\t" \
  ".Lsf_r02:\n\t" \
  "v_mfma_f32_16x16x32_bf16 v[108:111], a[48:51], v[112:115], v[188:191]\n\t" \
  "v_add_f32 v101, v101, v188\n\t" \
  "v_exp_f32 v64, v64\n\t" \
  "v_exp_f32 v65, v65\n\t" \
  "v_mfma_f32_16x16x32_bf16 v[108:111], a[52:55], v[116:119], v[108:111]\n\t" \
  "v_max3_f32 v204, v104, v105, v106\n\t" \
  "v_exp_f32 v66, v66\n\t" \
  "v_exp_f32 v67, v67\n\t" \
  "v_mfma_f32_16x16x32_bf16 v[108:111], a[56:59], v[120:123], v[108:111]\n\t" \
  "v_add_f32 v102, v102, v188\n\t" \
  "v_exp_f32 v68, v68\n\t" \
  "v_exp_f32 v69, v69\n\t" \
  "v_mfma_f32_16x16x32_bf16 v[108:111], a[60:63], v[124:127], v[108:111]\n\t" \
  "s_waitcnt lgkmcnt(0)\n\t" \
  "v_add_f32 v103, v103, v188\n\t" \
  "v_cvt_pk_bf16_f32 v135, v14, v15\n\t" \
  "v_exp_f32 v70, v70\n\t" \
  "v_mfma_f32_16x16x32_bf16 v[0:3], a[192:195], v[16:19], v[176:179]\n\t" \
  "ds_read_b64_tr_b16 a[128:129], v228 offset:0\n\t" \
  "v_exp_f32 v71, v71\n\t" \
  "v_mfma_f32_16x16x32_bf16 v[0:3], a[196:199], v[20:23], v[0:3]\n\t" \
  "ds_read_b64_tr_b16 a[130:131], v228 offset:4096\n\t" \
  "v_exp_f32 v72, v72\n\t" \
  "v_max3_f32 v203, v107, v108, v109\n\t" \
  "v_mfma_f32_16x16x32_bf16 v[0:3], a[200:203], v[24:27], v[0:3]\n\t" \
  "v_max3_f32 v204, v204, v110, v111\n\t" \
  "ds_read_b64_tr_b16 a[132:133], v228 offset:8192\n\t" \
  "v_max_f32 v204, v204, v203\n\t" \
  "v_mfma_f32_16x16x32_bf16 v[0:3], a[204:207], v[28:31], v[0:3]\n\t" \
  "ds_read_b64_tr_b16 a[134:135], v228 offset:12288\n\t" \
  "v_cmp_lt_f32 vcc, 0x42800000, v204\n\t" \
  "v_mfma_f32_16x16x32_bf16 v[4:7], a[220:223], v[28:31], v[176:179]\n\t" \
  "ds_read_b64_tr_b16 a[136:137], v229 offset:0\n\t" \
  "v_exp_f32 v73, v73\n\t" \
  "v_mfma_f32_16x16x32_bf16 v[4:7], a[216:219], v[24:27], v[4:7]\n\t" \
  "s_cbranch_vccnz .Lsf_c03\n\t" \
  ".Lsf_r03:\n\t" \
  "v_mfma_f32_16x16x32_bf16 v[4:7], a[212:215], v[20:23], v[4:7]\n\t" \
  "s_cmp_lg_u32 s99, 0\n\t" \
  "s_cbranch_scc1 .Lsf_p0\n\t" \
  ".Lsf_q0:\n\t" \
  "v_mfma_f32_16x16x32_bf16 v[4:7], a[208:211], v[16:19], v[4:7]\n\t" \
  "ds_read_b64_tr_b16 a[138:139], v229 offset:4096\n\t" \
  "v_max3_f32 v204, v0, v1, v2\n\t" \
  "v_cvt_pk_bf16_f32 v136, v32, v33\n\t" \
  "v_mfma_f32_16x16x32_bf16 v[8:11], a[224:227], v[16:19], v[176:179]\n\t" \
  "v_cvt_pk_bf16_f32 v137, v34, v35\n\t" \
  "v_exp_f32 v74, v74\n\t" \
  "v_mfma_f32_16x16x32_bf16 v[8:11], a[228:231], v[20:23], v[8:11]\n\t" \
  "ds_read_b64_tr_b16 a[140:141], v229 offset:8192\n\t" \
  "v_exp_f32 v75, v75\n\t" \
  "v_mfma_f32_16x16x32_bf16 v[8:11], a[232:235], v[24:27], v[8:11]\n\t" \
  "v_max3_f32 v203, v3, v4, v5\n\t" \
  "v_max3_f32 v204, v204, v6, v7\n\t" \
  "v_mfma_f32_16x16x32_bf16 v[8:11], a[236:239], v[28:31], v[8:11]\n\t" \
  "ds_read_b64_tr_b16 a[142:143], v229 offset:12288\n\t" \
  "v_exp_f32 v76, v76\n\t" \
  "v_mfma_f32_16x16x32_bf16 v[12:15], a[252:255], v[28:31], v[176:179]\n\t" \
  "v_cvt_pk_bf16_f32 v138, v36, v37\n\t" \
  "v_cvt_pk_bf16_f32 v139, v38, v39\n\t" \
  "v_mfma_f32_16x16x32_bf16 v[12:15], a[248:251], v[24:27], v[12:15]\n\t" \
  "v_exp_f32 v77, v77\n\t" \
  "v_mfma_f32_16x16x32_bf16 v[12:15], a[244:247], v[20:23], v[12:15]\n\t" \
  "v_max3_f32 v203, v203, v8, v9\n\t" \
  "v_max3_f32 v204, v204, v10, v11\n\t" \
  "v_mfma_f32_16x16x32_bf16 v[12:15], a[240:243], v[16:19], v[12:15]\n\t" \
  "ds_read_b64_tr_b16 a[144:145], v228 offset:256\n\t" \
  "v_cvt_pk_bf16_f32 v140, v40, v41\n\t" \
  "v_mfma_f32_16x16x32_bf16 v[32:35], a[192:195], v[48:51], v[180:183]\n\t" \
  "ds_read_b64_tr_b16 a[146:147], v228 offset:4352\n\t" \
  "v_cvt_pk_bf16_f32 v141, v42, v43\n\t" \
  "v_mfma_f32_16x16x32_bf16 v[32:35], a[196:199], v[52:55], v[32:35]\n\t" \
  "v_cvt_pk_bf16_f32 v142, v44, v45\n\t" \
  "v_exp_f32 v78, v78\n\t" \
  "v_mfma_f32_16x16x32_bf16 v[32:35], a[200:203], v[56:59], v[32:35]\n\t" \
  "v_max3_f32 v203, v203, v12, v13\n\t" \
  "v_max3_f32 v204, v204, v14, v15\n\t" \
  "v_mfma_f32_16x16x32_bf16 v[32:35], a[204:207], v[60:63], v[32:35]\n\t" \
  "v_max_f32 v204, v204, v203\n\t" \
  "v_cmp_lt_f32 vcc, 0x42800000, v204\n\t" \
  "v_mfma_f32_16x16x32_bf16 v[36:39], a[220:223], v[60:63], v[180:183]\n\t" \
  "ds_read_b64_tr_b16 a[148:149], v228 offset:8448\n\t" \
  "v_exp_f32 v79, v79\n\t" \
  "v_mfma_f32_16x16x32_bf16 v[36:39], a[216:219], v[56:59], v[36:39]\n\t" \
  "s_cbranch_vccnz .Lsf_c10\n\t" \
  ".Lsf_r10:\n\t" \
  "v_mfma_f32_16x16x32_bf16 v[36:39], a[212:215], v[52:55], v[36:39]\n\t" \
  "ds_read_b64_tr_b16 a[150:151], v228 offset:12544\n\t" \
  "v_max3_f32 v204, v32, v33, v34\n\t" \
  "v_mfma_f32_16x16x32_bf16 v[36:39], a[208:211], v[48:51], v[36:39]\n\t" \
  "ds_read_b64_tr_b16 a[152:153], v229 offset:256\n\t" \
  "v_exp_f32 v96, v96\n\t" \
  "v_mfma_f32_16x16x32_bf16 v[40:43], a[224:227], v[48:51], v[180:183]\n\t" \
  "ds_read_b64_tr_b16 a[154:155], v229 offset:4352\n\t" \
  "v_exp_f32 v97, v97\n\t" \
  "v_mfma_f32_16x16x32_bf16 v[40:43], a[228:231], v[52:55], v[40:43]\n\t" \
  "v_cvt_pk_bf16_f32 v143, v46, v47\n\t" \
  "v_cvt_pk_bf16_f32 v144, v64, v65\n\t" \
  "v_mfma_f32_16x16x32_bf16 v[40:43], a[232:235], v[56:59], v[40:43]\n\t" \
  "v_max3_f32 v203, v35, v36, v37\n\t" \
  "v_max3_f32 v204, v204, v38, v39\n\t" \
  "v_mfma_f32_16x16x32_bf16 v[40:43], a[236:239], v[60:63], v[40:43]\n\t" \
  "ds_read_b64_tr_b16 a[156:157], v229 offset:8448\n\t" \
  "v_exp_f32 v98, v98\n\t" \
  "v_mfma_f32_16x16x32_bf16 v[44:47], a[252:255], v[60:63], v[180:183]\n\t" \
  "ds_read_b64_tr_b16 a[158:159], v229 offset:12544\n\t" \
  "v_exp_f32 v99, v99\n\t" \
  "v_mfma_f32_16x16x32_bf16 v[44:47], a[248:251], v[56:59], v[44:47]\n\t" \
  "v_cvt_pk_bf16_f32 v145, v66, v67\n\t" \
  "v_cvt_pk_bf16_f32 v146, v68, v69\n\t" \
  "v_mfma_f32_16x16x32_bf16 v[44:47], a[244:247], v[52:55], v[44:47]\n\t" \
  "v_max3_f32 v203, v203, v40, v41\n\t" \
  "v_max3_f32 v204, v204, v42, v43\n\t" \
  "v_mfma_f32_16x16x32_bf16 v[44:47], a[240:243], v[48:51], v[44:47]\n\t" \
  "ds_read_b64_tr_b16 a[160:161], v228 offset:512\n\t" \
  "v_exp_f32 v100, v100\n\t" \
  "v_mfma_f32_16x16x32_bf16 v[64:67], a[192:195], v[80:83], v[184:187]\n\t" \
  "ds_read_b64_tr_b16 a[162:163], v228 offset:4608\n\t" \
  "v_exp_f32 v101, v101\n\t" \
  "v_mfma_f32_16x16x32_bf16 v[64:67], a[196:199], v[84:87], v[64:67]\n\t" \
  "ds_read_b64_tr_b16 a[164:165], v228 offset:8704\n\t" \
  "v_cvt_pk_bf16_f32 v147, v70, v71\n\t" \
  "v_mfma_f32_16x16x32_bf16 v[64:67], a[200:203], v[88:91], v[64:67]\n\t" \
  "v_max3_f32 v203, v203, v44, v45\n\t" \
  "v_max3_f32 v204, v204, v46, v47\n\t" \
  "v_mfma_f32_16x16x32_bf16 v[64:67], a[204:207], v[92:95], v[64:67]\n\t" \
  "v_max_f32 v204, v204, v203\n\t" \
  "v_cmp_lt_f32 vcc, 0x42800000, v204\n\t" \
  "v_mfma_f32_16x16x32_bf16 v[68:71], a[220:223], v[92:95], v[184:187]\n\t" \
  "ds_read_b64_tr_b16 a[166:167], v228 offset:12800\n\t" \
  "v_exp_f32 v102, v102\n\t" \
  "v_mfma_f32_16x16x32_bf16 v[68:71], a[216:219], v[88:91], v[68:71]\n\t" \
  "s_cbranch_vccnz .Lsf_c11\n\t" \
  ".Lsf_r11:\n\t" \
  "v_mfma_f32_16x16x32_bf16 v[68:71], a[212:215], v[84:87], v[68:71]\n\t" \
  "v_max3_f32 v204, v64, v65, v66\n\t" \
  "v_exp_f32 v103, v103\n\t" \
  "v_cvt_pk_bf16_f32 v148, v72, v73\n\t" \
  "v_mfma_f32_16x16x32_bf16 v[68:71], a[208:211], v[80:83], v[68:71]\n\t" \
  "ds_read_b64_tr_b16 a[168:169], v229 offset:512\n\t" \
  "v_exp_f32 v104, v104\n\t" \
  "v_cvt_pk_bf16_f32 v149, v74, v75\n\t" \
  "v_mfma_f32_16x16x32_bf16 v[72:75], a[224:227], v[80:83], v[184:187]\n\t" \
  "ds_read_b64_tr_b16 a[170:171], v229 offset:4608\n\t" \
  "v_exp_f32 v105, v105\n\t" \
  "v_mfma_f32_16x16x32_bf16 v[72:75], a[228:231], v[84:87], v[72:75]\n\t" \
  "ds_read_b64_tr_b16 a[172:173], v229 offset:8704\n\t" \
  "v_max3_f32 v203, v67, v68, v69\n\t" \
  "v_mfma_f32_16x16x32_bf16 v[72:75], a[232:235], v[88:91], v[72:75]\n\t" \
  "v_max3_f32 v204, v204, v70, v71\n\t" \
  "v_exp_f32 v106, v106\n\t" \
  "v_mfma_f32_16x16x32_bf16 v[72:75], a[236:239], v[92:95], v[72:75]\n\t" \
  "ds_read_b64_tr_b16 a[174:175], v229 offset:12800\n\t" \
  "v_cvt_pk_bf16_f32 v150, v76, v77\n\t" \
  "v_cvt_pk_bf16_f32 v151, v78, v79\n\t" \
  "v_mfma_f32_16x16x32_bf16 v[76:79], a[252:255], v[92:95], v[184:187]\n\t" \
  "ds_read_b64_tr_b16 a[176:177], v228 offset:768\n\t" \
  "v_exp_f32 v107, v107\n\t" \
  "v_mfma_f32_16x16x32_bf16 v[76:79], a[248:251], v[88:91], v[76:79]\n\t" \
  "v_cvt_pk_bf16_f32 v152, v96, v97\n\t" \
  "v_max3_f32 v203, v203, v72, v73\n\t" \
  "v_mfma_f32_16x16x32_bf16 v[76:79], a[244:247], v[84:87], v[76:79]\n\t" \
  "v_max3_f32 v204, v204, v74, v75\n\t" \
  "v_exp_f32 v108, v108\n\t" \
  "v_mfma_f32_16x16x32_bf16 v[76:79], a[240:243], v[80:83], v[76:79]\n\t" \
  "ds_read_b64_tr_b16 a[178:179], v228 offset:4864\n\t" \
  "v_cvt_pk_bf16_f32 v153, v98, v99\n\t" \
  "v_mfma_f32_16x16x32_bf16 v[96:99], a[192:195], v[112:115], v[188:191]\n\t" \
  "ds_read_b64_tr_b16 a[180:181], v228 offset:8960\n\t" \
  "v_exp_f32 v109, v109\n\t" \
  "v_mfma_f32_16x16x32_bf16 v[96:99], a[196:199], v[116:119], v[96:99]\n\t" \
  "v_cvt_pk_bf16_f32 v154, v100, v101\n\t" \
  "v_cvt_pk_bf16_f32 v155, v102, v103\n\t" \
  "v_mfma_f32_16x16x32_bf16 v[96:99], a[200:203], v[120:123], v[96:99]\n\t" \
  "v_max3_f32 v203, v203, v76, v77\n\t" \
  "v_max3_f32 v204, v204, v78, v79\n\t" \
  "v_mfma_f32_16x16x32_bf16 v[96:99], a[204:207], v[124:127], v[96:99]\n\t" \
  "v_max_f32 v204, v204, v203\n\t" \
  "v_cmp_lt_f32 vcc, 0x42800000, v204\n\t" \
  "v_mfma_f32_16x16x32_bf16 v[100:103], a[220:223], v[124:127], v[188:191]\n\t" \
  "ds_read_b64_tr_b16 a[182:183], v228 offset:13056\n\t" \
  "v_exp_f32 v110, v110\n\t" \
  "v_mfma_f32_16x16x32_bf16 v[100:103], a[216:219], v[120:123], v[100:103]\n\t" \
  "s_cbranch_vccnz .Lsf_c12\n\t" \
  ".Lsf_r12:\n\t" \
  "v_mfma_f32_16x16x32_bf16 v[100:103], a[212:215], v[116:119], v[100:103]\n\t" \
  "v_max3_f32 v204, v96, v97, v98\n\t" \
  "v_exp_f32 v111, v111\n\t" \
  "v_cvt_pk_bf16_f32 v156, v104, v105\n\t" \
  "v_mfma_f32_16x16x32_bf16 v[100:103], a[208:211], v[112:115], v[100:103]\n\t" \
  "ds_read_b64_tr_b16 a[184:185], v229 offset:768\n\t" \
  "v_cvt_pk_bf16_f32 v157, v106, v107\n\t" \
  "v_mfma_f32_16x16x32_bf16 v[104:107], a[224:227], v[112:115], v[188:191]\n\t" \
  "ds_read_b64_tr_b16 a[186:187], v229 offset:4864\n\t" \
  "v_cvt_pk_bf16_f32 v158, v108, v109\n\t" \
  "v_mfma_f32_16x16x32_bf16 v[104:107], a[228:231], v[116:119], v[104:107]\n\t" \
  "ds_read_b64_tr_b16 a[188:189], v229 offset:8960\n\t" \
  "v_cvt_pk_bf16_f32 v159, v110, v111\n\t" \
  "v_mfma_f32_16x16x32_bf16 v[104:107], a[232:235], v[120:123], v[104:107]\n\t" \
  "v_max3_f32 v203, v99, v100, v101\n\t" \
  "v_max3_f32 v204, v204, v102, v103\n\t" \
  "v_mfma_f32_16x16x32_bf16 v[104:107], a[236:239], v[124:127], v[104:107]\n\t" \
  "ds_read_b64_tr_b16 a[190:191], v229 offset:13056\n\t" \
  "s_nop 0\n\t" \
  "v_mfma_f32_16x16x32_bf16 v[108:111], a[252:255], v[124:127], v[188:191]\n\t" \
  "v_exp_f32 v0, v0\n\t" \
  "v_mfma_f32_16x16x32_bf16 v[108:111], a[248:251], v[120:123], v[108:111]\n\t" \
  "v_exp_f32 v1, v1\n\t" \
  "v_mfma_f32_16x16x32_bf16 v[108:111], a[244:247], v[116:119], v[108:111]\n\t" \
  "s_waitcnt vmcnt(0) lgkmcnt(0)\n\t" \
  "v_max3_f32 v203, v203, v104, v105\n\t" \
  "v_max3_f32 v204, v204, v106, v107\n\t" \
  "v_mfma_f32_16x16x32_bf16 v[108:111], a[240:243], v[112:115], v[108:111]\n\t" \
  "s_barrier\n\t" \
  "v_mfma_f32_16x16x32_bf16 a[0:3], a[128:131], v[128:131], 0\n\t" \
  "v_exp_f32 v2, v2\n\t" \
  "v_mfma_f32_16x16x32_bf16 a[0:3], a[132:135], v[132:135], a[0:3]\n\t" \
  "v_exp_f32 v3, v3\n\t" \
  "v_mfma_f32_16x16x32_bf16 a[4:7], a[136:139], v[128:131], 0\n\t" \
  "v_exp_f32 v4, v4\n\t" \
  "v_mfma_f32_16x16x32_bf16 a[4:7], a[140:143], v[132:135], a[4:7]\n\t" \
  "v_max3_f32 v203, v203, v108, v109\n\t" \
  "v_max3_f32 v204, v204, v110, v111\n\t" \
  "v_mfma_f32_16x16x32_bf16 a[8:11], a[144:147], v[128:131], 0\n\t" \
  "v_max_f32 v204, v204, v203\n\t" \
  "v_cmp_lt_f32 vcc, 0x42800000, v204\n\t" \
  "v_mfma_f32_16x16x32_bf16 a[8:11], a[148:151], v[132:135], a[8:11]\n\t" \
  "v_exp_f32 v5, v5\n\t" \
  "v_mfma_f32_16x16x32_bf16 a[12:15], a[152:155], v[128:131], 0\n\t" \
  "s_cbranch_vccnz .Lsf_c13\n\t" \
  ".Lsf_r13:\n\t" \
  "v_mfma_f32_16x16x32_bf16 a[12:15], a[156:159], v[132:135], a[12:15]\n\t" \
  "s_add_u32 m0, s38, 65536\n\t" \
  "v_exp_f32 v6, v6\n\t" \
  "v_mfma_f32_16x16x32_bf16 a[16:19], a[160:163], v[128:131], 0\n\t" \
  "s_add_u32 s96, s38, 49152\n\t" \
  "v_exp_f32 v7, v7\n\t" \
  "v_mfma_f32_16x16x32_bf16 a[16:19], a[164:167], v[132:135], a[16:19]\n\t" \
  "v_exp_f32 v8, v8\n\t" \
  "v_mfma_f32_16x16x32_bf16 a[20:23], a[168:171], v[128:131], 0\n\t" \
  "buffer_load_dwordx4 v224, s[60:63], s96 offen offset:0 lds\n\t" \
  "v_mfma_f32_16x16x32_bf16 a[20:23], a[172:175], v[132:135], a[20:23]\n\t" \
  "v_exp_f32 v9, v9\n\t" \
  "v_mfma_f32_16x16x32_bf16 a[24:27], a[176:179], v[128:131], 0\n\t" \
  "v_exp_f32 v10, v10\n\t" \
  "v_mfma_f32_16x16x32_bf16 a[24:27], a[180:183], v[132:135], a[24:27]\n\t" \
  "v_exp_f32 v11, v11\n\t" \
  "v_mfma_f32_16x16x32_bf16 a[28:31], a[184:187], v[128:131], 0\n\t" \
  "v_exp_f32 v12, v12\n\t" \
  "v_mfma_f32_16x16x32_bf16 a[28:31], a[188:191], v[132:135], a[28:31]\n\t" \
  "v_exp_f32 v13, v13\n\t" \
  "v_smfmac_f32_16x16x64_bf16 v[160:163], v[216:219], v[128:135], v208\n\t" \
  "ds_read_b128 a[192:195], v220 offset:0\n\t" \
  "v_cvt_pk_bf16_f32 v128, v0, v1\n\t" \
  "v_mfma_f32_16x16x32_bf16 a[32:35], a[128:131], v[136:139], 0\n\t" \
  "buffer_load_dwordx4 v225, s[60:63], s96 offen offset:1024 lds\n\t" \
  "v_mfma_f32_16x16x32_bf16 a[32:35], a[132:135], v[140:143], a[32:35]\n\t" \
  "v_exp_f32 v14, v14\n\t" \
  "v_mfma_f32_16x16x32_bf16 a[36:39], a[136:139], v[136:139], 0\n\t" \
  "v_exp_f32 v15, v15\n\t" \
  "v_mfma_f32_16x16x32_bf16 a[36:39], a[140:143], v[140:143], a[36:39]\n\t" \
  "ds_read_b128 a[196:199], v221 offset:0\n\t" \
  "v_cvt_pk_bf16_f32 v129, v2, v3\n\t" \
  "v_mfma_f32_16x16x32_bf16 a[40:43], a[144:147], v[136:139], 0\n\t" \
  "v_exp_f32 v32, v32\n\t" \
  "v_mfma_f32_16x16x32_bf16 a[40:43], a[148:151], v[140:143], a[40:43]\n\t" \
  "v_exp_f32 v33, v33\n\t" \
  "v_mfma_f32_16x16x32_bf16 a[44:47], a[152:155], v[136:139], 0\n\t" \
  "buffer_load_dwordx4 v226, s[60:63], s96 offen offset:2048 lds\n\t" \
  "v_mfma_f32_16x16x32_bf16 a[44:47], a[156:159], v[140:143], a[44:47]\n\t" \
  "ds_read_b128 a[200:203], v222 offset:0\n\t" \
  "v_cvt_pk_bf16_f32 v130, v4, v5\n\t" \
  "v_mfma_f32_16x16x32_bf16 a[48:51], a[160:163], v[136:139], 0\n\t" \
  "v_exp_f32 v34, v34\n\t" \
  "v_mfma_f32_16x16x32_bf16 a[48:51], a[164:167], v[140:143], a[48:51]\n\t" \
  "ds_read_b128 a[204:207], v223 offset:0\n\t" \
  "v_cvt_pk_bf16_f32 v131, v6, v7\n\t" \
  "v_mfma_f32_16x16x32_bf16 a[52:55], a[168:171], v[136:139], 0\n\t" \
  "v_exp_f32 v35, v35\n\t" \
  "v_mfma_f32_16x16x32_bf16 a[52:55], a[172:175], v[140:143], a[52:55]\n\t" \
  "ds_read_b128 a[208:211], v220 offset:4096\n\t" \
  "v_cvt_pk_bf16_f32 v132, v8, v9\n\t" \
  "v_mfma_f32_16x16x32_bf16 a[56:59], a[176:179], v[136:139], 0\n\t" \
  "buffer_load_dwordx4 v227, s[60:63], s96 offen offset:3072 lds\n\t" \
  "v_mfma_f32_16x16x32_bf16 a[56:59], a[180:183], v[140:143], a[56:59]\n\t" \
  "v_exp_f32 v36, v36\n\t" \
  "v_mfma_f32_16x16x32_bf16 a[60:63], a[184:187], v[136:139], 0\n\t" \
  "s_add_u32 m0, s38, 81920\n\t" \
  "ds_read_b128 a[212:215], v221 offset:4096\n\t" \
  "v_cvt_pk_bf16_f32 v133, v10, v11\n\t" \
  "v_mfma_f32_16x16x32_bf16 a[60:63], a[188:191], v[140:143], a[60:63]\n\t" \
  "s_add_u32 s96, s38, 32768\n\t" \
  "v_exp_f32 v37, v37\n\t" \
  "v_smfmac_f32_16x16x64_bf16 v[164:167], v[216:219], v[136:143], v208\n\t" \
  "ds_read_b128 a[216:219], v222 offset:4096\n\t" \
  "v_cvt_pk_bf16_f32 v134, v12, v13\n\t" \
  "v_mfma_f32_16x16x32_bf16 a[64:67], a[128:131], v[144:147], 0\n\t" \
  "v_exp_f32 v38, v38\n\t" \
  "v_mfma_f32_16x16x32_bf16 a[64:67], a[132:135], v[148:151], a[64:67]\n\t" \
  "buffer_load_dwordx4 v230, s[64:67], s96 offen offset:0 lds\n\t" \
  "v_mfma_f32_16x16x32_bf16 a[68:71], a[136:139], v[144:147], 0\n\t" \
  "ds_read_b128 a[220:223], v223 offset:4096\n\t" \
  "v_cvt_pk_bf16_f32 v135, v14, v15\n\t" \
  "v_mfma_f32_16x16x32_bf16 a[68:71], a[140:143], v[148:151], a[68:71]\n\t" \
  "v_exp_f32 v39, v39\n\t" \
  "v_mfma_f32_16x16x32_bf16 a[72:75], a[144:147], v[144:147], 0\n\t" \
  "v_exp_f32 v40, v40\n\t" \
  "v_mfma_f32_16x16x32_bf16 a[72:75], a[148:151], v[148:151], a[72:75]\n\t" \
  "ds_read_b128 a[224:227], v220 offset:8192\n\t" \
  "v_cvt_pk_bf16_f32 v136, v32, v33\n\t" \
  "v_mfma_f32_16x16x32_bf16 a[76:79], a[152:155], v[144:147], 0\n\t" \
  "ds_read_b128 a[228:231], v221 offset:8192\n\t" \
  "v_cvt_pk_bf16_f32 v137, v34, v35\n\t" \
  "v_mfma_f32_16x16x32_bf16 a[76:79], a[156:159], v[148:151], a[76:79]\n\t" \
  "v_exp_f32 v41, v41\n\t" \
  "v_mfma_f32_16x16x32_bf16 a[80:83], a[160:163], v[144:147], 0\n\t" \
  "buffer_load_dwordx4 v231, s[64:67], s96 offen offset:1024 lds\n\t" \
  "v_mfma_f32_16x16x32_bf16 a[80:83], a[164:167], v[148:151], a[80:83]\n\t" \
  "v_exp_f32 v42, v42\n\t" \
  "v_mfma_f32_16x16x32_bf16 a[84:87], a[168:171], v[144:147], 0\n\t" \
  "ds_read_b128 a[232:235], v222 offset:8192\n\t" \
  "v_cvt_pk_bf16_f32 v138, v36, v37\n\t" \
  "v_mfma_f32_16x16x32_bf16 a[84:87], a[172:175], v[148:151], a[84:87]\n\t" \
  "v_exp_f32 v43, v43\n\t" \
  "v_mfma_f32_16x16x32_bf16 a[88:91], a[176:179], v[144:147], 0\n\t" \
  "ds_read_b128 a[236:239], v223 offset:8192\n\t" \
  "v_cvt_pk_bf16_f32 v139, v38, v39\n\t" \
  "v_mfma_f32_16x16x32_bf16 a[88:91], a[180:183], v[148:151], a[88:91]\n\t" \
  "v_exp_f32 v44, v44\n\t" \
  "v_mfma_f32_16x16x32_bf16 a[92:95], a[184:187], v[144:147], 0\n\t" \
  "buffer_load_dwordx4 v230, s[64:67], s96 offen offset:2048 lds\n\t" \
  "v_mfma_f32_16x16x32_bf16 a[92:95], a[188:191], v[148:151], a[92:95]\n\t" \
  "ds_read_b128 a[240:243], v220 offset:12288\n\t" \
  "v_cvt_pk_bf16_f32 v140, v40, v41\n\t" \
  "v_smfmac_f32_16x16x64_bf16 v[168:171], v[216:219], v[144:151], v208\n\t" \
  "ds_read_b128 a[244:247], v221 offset:12288\n\t" \
  "v_cvt_pk_bf16_f32 v141, v42, v43\n\t" \
  "v_mfma_f32_16x16x32_bf16 a[96:99], a[128:131], v[152:155], 0\n\t" \
  "v_exp_f32 v45, v45\n\t" \
  "v_mfma_f32_16x16x32_bf16 a[96:99], a[132:135], v[156:159], a[96:99]\n\t" \
  "v_exp_f32 v46, v46\n\t" \
  "v_mfma_f32_16x16x32_bf16 a[100:103], a[136:139], v[152:155], 0\n\t" \
  "v_exp_f32 v47, v47\n\t" \
  "v_mfma_f32_16x16x32_bf16 a[100:103], a[140:143], v[156:159], a[100:103]\n\t" \
  "buffer_load_dwordx4 v231, s[64:67], s96 offen offset:3072 lds\n\t" \
  "v_mfma_f32_16x16x32_bf16 a[104:107], a[144:147], v[152:155], 0\n\t" \
  "ds_read_b128 a[248:251], v222 offset:12288\n\t" \
  "v_cvt_pk_bf16_f32 v142, v44, v45\n\t" \
  "v_mfma_f32_16x16x32_bf16 a[104:107], a[148:151], v[156:159], a[104:107]\n\t" \
  "ds_read_b128 a[252:255], v223 offset:12288\n\t" \
  "v_cvt_pk_bf16_f32 v143, v46, v47\n\t" \
  "v_mfma_f32_16x16x32_bf16 a[108:111], a[152:155], v[152:155], 0\n\t" \
  "v_exp_f32 v64, v64\n\t" \
  "v_mfma_f32_16x16x32_bf16 a[108:111], a[156:159], v[156:159], a[108:111]\n\t" \
  "v_exp_f32 v65, v65\n\t" \
  "v_mfma_f32_16x16x32_bf16 a[112:115], a[160:163], v[152:155], 0\n\t" \
  "v_exp_f32 v66, v66\n\t" \
  "v_mfma_f32_16x16x32_bf16 a[112:115], a[164:167], v[156:159], a[112:115]\n\t" \
  "v_exp_f32 v67, v67\n\t" \
  "v_mfma_f32_16x16x32_bf16 a[116:119], a[168:171], v[152:155], 0\n\t" \
  "v_exp_f32 v68, v68\n\t" \
  "v_mfma_f32_16x16x32_bf16 a[116:119], a[172:175], v[156:159], a[116:119]\n\t" \
  "v_exp_f32 v69, v69\n\t" \
  "v_mfma_f32_16x16x32_bf16 a[120:123], a[176:179], v[152:155], 0\n\t" \
  "v_exp_f32 v70, v70\n\t" \
  "v_mfma_f32_16x16x32_bf16 a[120:123], a[180:183], v[156:159], a[120:123]\n\t" \
  "v_exp_f32 v71, v71\n\t" \
  "v_mfma_f32_16x16x32_bf16 a[124:127], a[184:187], v[152:155], 0\n\t" \
  "v_exp_f32 v72, v72\n\t" \
  "v_mfma_f32_16x16x32_bf16 a[124:127], a[188:191], v[156:159], a[124:127]\n\t" \
  "v_exp_f32 v73, v73\n\t" \
  "v_smfmac_f32_16x16x64_bf16 v[172:175], v[216:219], v[152:159], v208\n\t" \
  "s_waitcnt lgkmcnt(12)\n\t" \
  "s_cmp_lg_u32 s99, 0\n\t" \
  "s_cbranch_scc1 .Lsf_p1\n\t" \
  ".Lsf_q1:\n\t" \
  "v_mfma_f32_16x16x32_bf16 v[0:3], a[192:195], v[16:19], v[176:179]\n\t" \
  "ds_read_b64_tr_b16 a[128:129], v210 offset:0\n\t" \
  "v_cvt_pk_bf16_f32 v144, v64, v65\n\t" \
  "v_mfma_f32_16x16x32_bf16 v[0:3], a[196:199], v[20:23], v[0:3]\n\t" \
  "v_exp_f32 v74, v74\n\t" \
  "v_mfma_f32_16x16x32_bf16 v[0:3], a[200:203], v[24:27], v[0:3]\n\t" \
  "ds_read_b64_tr_b16 a[130:131], v210 offset:4096\n\t" \
  "v_cvt_pk_bf16_f32 v145, v66, v67\n\t" \
  "v_mfma_f32_16x16x32_bf16 v[0:3], a[204:207], v[28:31], v[0:3]\n\t" \
  "s_waitcnt lgkmcnt(10)\n\t" \
  "v_exp_f32 v75, v75\n\t" \
  "v_mfma_f32_16x16x32_bf16 v[4:7], a[220:223], v[28:31], v[176:179]\n\t" \
  "ds_read_b64_tr_b16 a[132:133], v210 offset:8192\n\t" \
  "v_cvt_pk_bf16_f32 v146, v68, v69\n\t" \
  "v_mfma_f32_16x16x32_bf16 v[4:7], a[216:219], v[24:27], v[4:7]\n\t" \
  "v_exp_f32 v76, v76\n\t" \
  "v_mfma_f32_16x16x32_bf16 v[4:7], a[212:215], v[20:23], v[4:7]\n\t" \
  "ds_read_b64_tr_b16 a[134:135], v210 offset:12288\n\t" \
  "v_max3_f32 v204, v0, v1, v2\n\t" \
  "v_mfma_f32_16x16x32_bf16 v[4:7], a[208:211], v[16:19], v[4:7]\n\t" \
  "s_waitcnt lgkmcnt(8)\n\t" \
  "ds_read_b64_tr_b16 a[136:137], v211 offset:0\n\t" \
  "v_cvt_pk_bf16_f32 v147, v70, v71\n\t" \
  "v_mfma_f32_16x16x32_bf16 v[8:11], a[224:227], v[16:19], v[176:179]\n\t" \
  "v_exp_f32 v77, v77\n\t" \
  "v_mfma_f32_16x16x32_bf16 v[8:11], a[228:231], v[20:23], v[8:11]\n\t" \
  "ds_read_b64_tr_b16 a[138:139], v211 offset:4096\n\t" \
  "v_cvt_pk_bf16_f32 v148, v72, v73\n\t" \
  "v_mfma_f32_16x16x32_bf16 v[8:11], a[232:235], v[24:27], v[8:11]\n\t" \
  "v_max3_f32 v203, v3, v4, v5\n\t" \
  "v_max3_f32 v204, v204, v6, v7\n\t" \
  "v_mfma_f32_16x16x32_bf16 v[8:11], a[236:239], v[28:31], v[8:11]\n\t" \
  "s_waitcnt lgkmcnt(6)\n\t" \
  "ds_read_b64_tr_b16 a[140:141], v211 offset:8192\n\t" \
  "v_cvt_pk_bf16_f32 v149, v74, v75\n\t" \
  "v_mfma_f32_16x16x32_bf16 v[12:15], a[252:255], v[28:31], v[176:179]\n\t" \
  "v_exp_f32 v78, v78\n\t" \
  "v_mfma_f32_16x16x32_bf16 v[12:15], a[248:251], v[24:27], v[12:15]\n\t" \
  "ds_read_b64_tr_b16 a[142:143], v211 offset:12288\n\t" \
  "v_cvt_pk_bf16_f32 v150, v76, v77\n\t" \
  "v_mfma_f32_16x16x32_bf16 v[12:15], a[244:247], v[20:23], v[12:15]\n\t" \
  "v_max3_f32 v203, v203, v8, v9\n\t" \
  "v_max3_f32 v204, v204, v10, v11\n\t" \
  "v_mfma_f32_16x16x32_bf16 v[12:15], a[240:243], v[16:19], v[12:15]\n\t" \
  "v_exp_f32 v79, v79\n\t" \
  "v_mfma_f32_16x16x32_bf16 v[32:35], a[192:195], v[48:51], v[180:183]\n\t" \
  "ds_read_b64_tr_b16 a[144:145], v210 offset:256\n\t" \
  "v_cvt_pk_bf16_f32 v151, v78, v79\n\t" \
  "v_mfma_f32_16x16x32_bf16 v[32:35], a[196:199], v[52:55], v[32:35]\n\t" \
  "v_exp_f32 v96, v96\n\t" \
  "v_mfma_f32_16x16x32_bf16 v[32:35], a[200:203], v[56:59], v[32:35]\n\t" \
  "ds_read_b64_tr_b16 a[146:147], v210 offset:4352\n\t" \
  "v_max3_f32 v203, v203, v12, v13\n\t" \
  "v_mfma_f32_16x16x32_bf16 v[32:35], a[204:207], v[60:63], v[32:35]\n\t" \
  "v_max3_f32 v204, v204, v14, v15\n\t" \
  "v_max_f32 v204, v204, v203\n\t" \
  "v_mfma_f32_16x16x32_bf16 v[36:39], a[220:223], v[60:63], v[180:183]\n\t" \
  "ds_read_b64_tr_b16 a[148:149], v210 offset:8448\n\t" \
  "v_cmp_lt_f32 vcc, 0x42800000, v204\n\t" \
  "v_mfma_f32_16x16x32_bf16 v[36:39], a[216:219], v[56:59], v[36:39]\n\t" \
  "v_exp_f32 v97, v97\n\t" \
  "v_mfma_f32_16x16x32_bf16 v[36:39], a[212:215], v[52:55], v[36:39]\n\t" \
  "s_cbranch_vccnz .Lsf_c20\n\t" \
  ".Lsf_r20:\n\t" \
  "v_mfma_f32_16x16x32_bf16 v[36:39], a[208:211], v[48:51], v[36:39]\n\t" \
  "ds_read_b64_tr_b16 a[150:151], v210 offset:12544\n\t" \
  "v_max3_f32 v204, v32, v33, v34\n\t" \
  "v_mfma_f32_16x16x32_bf16 v[40:43], a[224:227], v[48:51], v[180:183]\n\t" \
  "ds_read_b64_tr_b16 a[152:153], v211 offset:256\n\t" \
  "v_cvt_pk_bf16_f32 v152, v96, v97\n\t" \
  "v_mfma_f32_16x16x32_bf16 v[40:43], a[228:231], v[52:55], v[40:43]\n\t" \
  "v_exp_f32 v98, v98\n\t" \
  "v_mfma_f32_16x16x32_bf16 v[40:43], a[232:235], v[56:59], v[40:43]\n\t" \
  "v_max3_f32 v203, v35, v36, v37\n\t" \
  "v_max3_f32 v204, v204, v38, v39\n\t" \
  "v_mfma_f32_16x16x32_bf16 v[40:43], a[236:239], v[60:63], v[40:43]\n\t" \
  "v_exp_f32 v99, v99\n\t" \
  "v_mfma_f32_16x16x32_bf16 v[44:47], a[252:255], v[60:63], v[180:183]\n\t" \
  "ds_read_b64_tr_b16 a[154:155], v211 offset:4352\n\t" \
  "v_cvt_pk_bf16_f32 v153, v98, v99\n\t" \
  "v_mfma_f32_16x16x32_bf16 v[44:47], a[248:251], v[56:59], v[44:47]\n\t" \
  "v_exp_f32 v100, v100\n\t" \
  "v_mfma_f32_16x16x32_bf16 v[44:47], a[244:247], v[52:55], v[44:47]\n\t" \
  "ds_read_b64_tr_b16 a[156:157], v211 offset:8448\n\t" \
  "v_max3_f32 v203, v203, v40, v41\n\t" \
  "v_mfma_f32_16x16x32_bf16 v[44:47], a[240:243], v[48:51], v[44:47]\n\t" \
  "ds_read_b64_tr_b16 a[158:159], v211 offset:12544\n\t" \
  "v_max3_f32 v204, v204, v42, v43\n\t" \
  "v_mfma_f32_16x16x32_bf16 v[64:67], a[192:195], v[80:83], v[184:187]\n\t" \
  "v_exp_f32 v101, v101\n\t" \
  "v_mfma_f32_16x16x32_bf16 v[64:67], a[196:199], v[84:87], v[64:67]\n\t" \
  "ds_read_b64_tr_b16 a[160:161], v210 offset:512\n\t" \
  "v_cvt_pk_bf16_f32 v154, v100, v101\n\t" \
  "v_mfma_f32_16x16x32_bf16 v[64:67], a[200:203], v[88:91], v[64:67]\n\t" \
  "v_max3_f32 v203, v203, v44, v45\n\t" \
  "v_max3_f32 v204, v204, v46, v47\n\t" \
  "v_mfma_f32_16x16x32_bf16 v[64:67], a[204:207], v[92:95], v[64:67]\n\t" \
  "v_max_f32 v204, v204, v203\n\t" \
  "v_cmp_lt_f32 vcc, 0x42800000, v204\n\t" \
  "v_mfma_f32_16x16x32_bf16 v[68:71], a[220:223], v[92:95], v[184:187]\n\t" \
  "v_exp_f32 v102, v102\n\t" \
  "v_mfma_f32_16x16x32_bf16 v[68:71], a[216:219], v[88:91], v[68:71]\n\t" \
  "s_cbranch_vccnz .Lsf_c21\n\t" \
  ".Lsf_r21:\n\t" \
  "v_mfma_f32_16x16x32_bf16 v[68:71], a[212:215], v[84:87], v[68:71]\n\t" \
  "ds_read_b64_tr_b16 a[162:163], v210 offset:4608\n\t" \
  "v_max3_f32 v204, v64, v65, v66\n\t" \
  "v_mfma_f32_16x16x32_bf16 v[68:71], a[208:211], v[80:83], v[68:71]\n\t" \
  "ds_read_b64_tr_b16 a[164:165], v210 offset:8704\n\t" \
  "v_exp_f32 v103, v103\n\t" \
  "v_mfma_f32_16x16x32_bf16 v[72:75], a[224:227], v[80:83], v[184:187]\n\t" \
  "ds_read_b64_tr_b16 a[166:167], v210 offset:12800\n\t" \
  "v_exp_f32 v104, v104\n\t" \
  "v_mfma_f32_16x16x32_bf16 v[72:75], a[228:231], v[84:87], v[72:75]\n\t" \
  "ds_read_b64_tr_b16 a[168:169], v211 offset:512\n\t" \
  "v_exp_f32 v105, v105\n\t" \
  "v_mfma_f32_16x16x32_bf16 v[72:75], a[232:235], v[88:91], v[72:75]\n\t" \
  "v_max3_f32 v203, v67, v68, v69\n\t" \
  "v_max3_f32 v204, v204, v70, v71\n\t" \
  "v_mfma_f32_16x16x32_bf16 v[72:75], a[236:239], v[92:95], v[72:75]\n\t" \
  "ds_read_b64_tr_b16 a[170:171], v211 offset:4608\n\t" \
  "v_exp_f32 v106, v106\n\t" \
  "v_mfma_f32_16x16x32_bf16 v[76:79], a[252:255], v[92:95], v[184:187]\n\t" \
  "ds_read_b64_tr_b16 a[172:173], v211 offset:8704\n\t" \
  "v_exp_f32 v107, v107\n\t" \
  "v_mfma_f32_16x16x32_bf16 v[76:79], a[248:251], v[88:91], v[76:79]\n\t" \
  "ds_read_b64_tr_b16 a[174:175], v211 offset:12800\n\t" \
  "v_cvt_pk_bf16_f32 v155, v102, v103\n\t" \
  "v_mfma_f32_16x16x32_bf16 v[76:79], a[244:247], v[84:87], v[76:79]\n\t" \
  "v_max3_f32 v203, v203, v72, v73\n\t" \
  "v_max3_f32 v204, v204, v74, v75\n\t" \
  "v_mfma_f32_16x16x32_bf16 v[76:79], a[240:243], v[80:83], v[76:79]\n\t" \
  "ds_read_b64_tr_b16 a[176:177], v210 offset:768\n\t" \
  "v_exp_f32 v108, v108\n\t" \
  "v_mfma_f32_16x16x32_bf16 v[96:99], a[192:195], v[112:115], v[188:191]\n\t" \
  "ds_read_b64_tr_b16 a[178:179], v210 offset:4864\n\t" \
  "v_exp_f32 v109, v109\n\t" \
  "v_mfma_f32_16x16x32_bf16 v[96:99], a[196:199], v[116:119], v[96:99]\n\t" \
  "ds_read_b64_tr_b16 a[180:181], v210 offset:8960\n\t" \
  "v_cvt_pk_bf16_f32 v156, v104, v105\n\t" \
  "v_mfma_f32_16x16x32_bf16 v[96:99], a[200:203], v[120:123], v[96:99]\n\t" \
  "v_max3_f32 v203, v203, v76, v77\n\t" \
  "v_max3_f32 v204, v204, v78, v79\n\t" \
  "v_mfma_f32_16x16x32_bf16 v[96:99], a[204:207], v[124:127], v[96:99]\n\t" \
  "v_max_f32 v204, v204, v203\n\t" \
  "v_cmp_lt_f32 vcc, 0x42800000, v204\n\t" \
  "v_mfma_f32_16x16x32_bf16 v[100:103], a[220:223], v[124:127], v[188:191]\n\t" \
  "ds_read_b64_tr_b16 a[182:183], v210 offset:13056\n\t" \
  "v_exp_f32 v110, v110\n\t" \
  "v_mfma_f32_16x16x32_bf16 v[100:103], a[216:219], v[120:123], v[100:103]\n\t" \
  "s_cbranch_vccnz .Lsf_c22\n\t" \
  ".Lsf_r22:\n\t" \
  "v_mfma_f32_16x16x32_bf16 v[100:103], a[212:215], v[116:119], v[100:103]\n\t" \
  "v_max3_f32 v204, v96, v97, v98\n\t" \
  "v_exp_f32 v111, v111\n\t" \
  "v_mfma_f32_16x16x32_bf16 v[100:103], a[208:211], v[112:115], v[100:103]\n\t" \
  "ds_read_b64_tr_b16 a[184:185], v211 offset:768\n\t" \
  "v_cvt_pk_bf16_f32 v157, v106, v107\n\t" \
  "v_mfma_f32_16x16x32_bf16 v[104:107], a[224:227], v[112:115], v[188:191]\n\t" \
  "ds_read_b64_tr_b16 a[186:187], v211 offset:4864\n\t" \
  "v_cvt_pk_bf16_f32 v158, v108, v109\n\t" \
  "v_mfma_f32_16x16x32_bf16 v[104:107], a[228:231], v[116:119], v[104:107]\n\t" \
  "ds_read_b64_tr_b16 a[188:189], v211 offset:8960\n\t" \
  "v_cvt_pk_bf16_f32 v159, v110, v111\n\t" \
  "v_mfma_f32_16x16x32_bf16 v[104:107], a[232:235], v[120:123], v[104:107]\n\t" \
  "v_max3_f32 v203, v99, v100, v101\n\t" \
  "v_max3_f32 v204, v204, v102, v103\n\t" \
  "v_mfma_f32_16x16x32_bf16 v[104:107], a[236:239], v[124:127], v[104:107]\n\t" \
  "ds_read_b64_tr_b16 a[190:191], v211 offset:13056\n\t" \
  "s_nop 0\n\t" \
  "v_mfma_f32_16x16x32_bf16 v[108:111], a[252:255], v[124:127], v[188:191]\n\t" \
  "v_exp_f32 v0, v0\n\t" \
  "v_mfma_f32_16x16x32_bf16 v[108:111], a[248:251], v[120:123], v[108:111]\n\t" \
  "v_exp_f32 v1, v1\n\t" \
  "v_mfma_f32_16x16x32_bf16 v[108:111], a[244:247], v[116:119], v[108:111]\n\t" \
  "s_waitcnt vmcnt(0) lgkmcnt(0)\n\t" \
  "v_max3_f32 v203, v203, v104, v105\n\t" \
  "v_max3_f32 v204, v204, v106, v107\n\t" \
  "v_mfma_f32_16x16x32_bf16 v[108:111], a[240:243], v[112:115], v[108:111]\n\t" \
  "s_barrier\n\t" \
  "v_mfma_f32_16x16x32_bf16 a[0:3], a[128:131], v[128:131], a[0:3]\n\t" \
  "v_exp_f32 v2, v2\n\t" \
  "v_mfma_f32_16x16x32_bf16 a[0:3], a[132:135], v[132:135], a[0:3]\n\t" \
  "v_exp_f32 v3, v3\n\t" \
  "v_mfma_f32_16x16x32_bf16 a[4:7], a[136:139], v[128:131], a[4:7]\n\t" \
  "v_exp_f32 v4, v4\n\t" \
  "v_mfma_f32_16x16x32_bf16 a[4:7], a[140:143], v[132:135], a[4:7]\n\t" \
  "v_max3_f32 v203, v203, v108, v109\n\t" \
  "v_max3_f32 v204, v204, v110, v111\n\t" \
  "v_mfma_f32_16x16x32_bf16 a[8:11], a[144:147], v[128:131], a[8:11]\n\t" \
  "v_max_f32 v204, v204, v203\n\t" \
  "v_cmp_lt_f32 vcc, 0x42800000, v204\n\t" \
  "v_mfma_f32_16x16x32_bf16 a[8:11], a[148:151], v[132:135], a[8:11]\n\t" \
  "v_exp_f32 v5, v5\n\t" \
  "v_mfma_f32_16x16x32_bf16 a[12:15], a[152:155], v[128:131], a[12:15]\n\t" \
  "s_cbranch_vccnz .Lsf_c23\n\t" \
  ".Lsf_r23:\n\t" \
  "v_mfma_f32_16x16x32_bf16 a[12:15], a[156:159], v[132:135], a[12:15]\n\t" \
  "s_add_u32 m0, s38, 49152\n\t" \
  "v_exp_f32 v6, v6\n\t" \
  "v_mfma_f32_16x16x32_bf16 a[16:19], a[160:163], v[128:131], a[16:19]\n\t" \
  "s_add_u32 s96, s38, 65536\n\t" \
  "v_exp_f32 v7, v7\n\t" \
  "v_mfma_f32_16x16x32_bf16 a[16:19], a[164:167], v[132:135], a[16:19]\n\t" \
  "v_exp_f32 v8, v8\n\t" \
  "v_mfma_f32_16x16x32_bf16 a[20:23], a[168:171], v[128:131], a[20:23]\n\t" \
  "buffer_load_dwordx4 v224, s[60:63], s96 offen offset:0 lds\n\t" \
  "v_mfma_f32_16x16x32_bf16 a[20:23], a[172:175], v[132:135], a[20:23]\n\t" \
  "v_exp_f32 v9, v9\n\t" \
  "v_mfma_f32_16x16x32_bf16 a[24:27], a[176:179], v[128:131], a[24:27]\n\t" \
  "v_exp_f32 v10, v10\n\t" \
  "v_mfma_f32_16x16x32_bf16 a[24:27], a[180:183], v[132:135], a[24:27]\n\t" \
  "v_exp_f32 v11, v11\n\t" \
  "v_mfma_f32_16x16x32_bf16 a[28:31], a[184:187], v[128:131], a[28:31]\n\t" \
  "v_exp_f32 v12, v12\n\t" \
  "v_mfma_f32_16x16x32_bf16 a[28:31], a[188:191], v[132:135], a[28:31]\n\t" \
  "v_exp_f32 v13, v13\n\t" \
  "v_smfmac_f32_16x16x64_bf16 v[160:163], v[216:219], v[128:135], v208\n\t" \
  "ds_read_b128 a[192:195], v220 offset:16384\n\t" \
  "v_cvt_pk_bf16_f32 v128, v0, v1\n\t" \
  "v_mfma_f32_16x16x32_bf16 a[32:35], a[128:131], v[136:139], a[32:35]\n\t" \
  "buffer_load_dwordx4 v225, s[60:63], s96 offen offset:1024 lds\n\t" \
  "v_mfma_f32_16x16x32_bf16 a[32:35], a[132:135], v[140:143], a[32:35]\n\t" \
  "v_exp_f32 v14, v14\n\t" \
  "v_mfma_f32_16x16x32_bf16 a[36:39], a[136:139], v[136:139], a[36:39]\n\t" \
  "v_exp_f32 v15, v15\n\t" \
  "v_mfma_f32_16x16x32_bf16 a[36:39], a[140:143], v[140:143], a[36:39]\n\t" \
  "ds_read_b128 a[196:199], v221 offset:16384\n\t" \
  "v_cvt_pk_bf16_f32 v129, v2, v3\n\t" \
  "v_mfma_f32_16x16x32_bf16 a[40:43], a[144:147], v[136:139], a[40:43]\n\t" \
  "v_exp_f32 v32, v32\n\t" \
  "v_mfma_f32_16x16x32_bf16 a[40:43], a[148:151], v[140:143], a[40:43]\n\t" \
  "v_exp_f32 v33, v33\n\t" \
  "v_mfma_f32_16x16x32_bf16 a[44:47], a[152:155], v[136:139], a[44:47]\n\t" \
  "buffer_load_dwordx4 v226, s[60:63], s96 offen offset:2048 lds\n\t" \
  "v_mfma_f32_16x16x32_bf16 a[44:47], a[156:159], v[140:143], a[44:47]\n\t" \
  "ds_read_b128 a[200:203], v222 offset:16384\n\t" \
  "v_cvt_pk_bf16_f32 v130, v4, v5\n\t" \
  "v_mfma_f32_16x16x32_bf16 a[48:51], a[160:163], v[136:139], a[48:51]\n\t" \
  "v_exp_f32 v34, v34\n\t" \
  "v_mfma_f32_16x16x32_bf16 a[48:51], a[164:167], v[140:143], a[48:51]\n\t" \
  "ds_read_b128 a[204:207], v223 offset:16384\n\t" \
  "v_cvt_pk_bf16_f32 v131, v6, v7\n\t" \
  "v_mfma_f32_16x16x32_bf16 a[52:55], a[168:171], v[136:139], a[52:55]\n\t" \
  "v_exp_f32 v35, v35\n\t" \
  "v_mfma_f32_16x16x32_bf16 a[52:55], a[172:175], v[140:143], a[52:55]\n\t" \
  "ds_read_b128 a[208:211], v220 offset:20480\n\t" \
  "v_cvt_pk_bf16_f32 v132, v8, v9\n\t" \
  "v_mfma_f32_16x16x32_bf16 a[56:59], a[176:179], v[136:139], a[56:59]\n\t" \
  "buffer_load_dwordx4 v227, s[60:63], s96 offen offset:3072 lds\n\t" \
  "v_mfma_f32_16x16x32_bf16 a[56:59], a[180:183], v[140:143], a[56:59]\n\t" \
  "v_exp_f32 v36, v36\n\t" \
  "v_mfma_f32_16x16x32_bf16 a[60:63], a[184:187], v[136:139], a[60:63]\n\t" \
  "s_add_u32 m0, s38, 147456\n\t" \
  "ds_read_b128 a[212:215], v221 offset:20480\n\t" \
  "v_cvt_pk_bf16_f32 v133, v10, v11\n\t" \
  "v_mfma_f32_16x16x32_bf16 a[60:63], a[188:191], v[140:143], a[60:63]\n\t" \
  "s_add_u32 s96, s38, 49152\n\t" \
  "v_exp_f32 v37, v37\n\t" \
  "v_smfmac_f32_16x16x64_bf16 v[164:167], v[216:219], v[136:143], v208\n\t" \
  "ds_read_b128 a[216:219], v222 offset:20480\n\t" \
  "v_cvt_pk_bf16_f32 v134, v12, v13\n\t" \
  "v_mfma_f32_16x16x32_bf16 a[64:67], a[128:131], v[144:147], a[64:67]\n\t" \
  "v_exp_f32 v38, v38\n\t" \
  "v_mfma_f32_16x16x32_bf16 a[64:67], a[132:135], v[148:151], a[64:67]\n\t" \
  "buffer_load_dwordx4 v230, s[64:67], s96 offen offset:0 lds\n\t" \
  "v_mfma_f32_16x16x32_bf16 a[68:71], a[136:139], v[144:147], a[68:71]\n\t" \
  "ds_read_b128 a[220:223], v223 offset:20480\n\t" \
  "v_cvt_pk_bf16_f32 v135, v14, v15\n\t" \
  "v_mfma_f32_16x16x32_bf16 a[68:71], a[140:143], v[148:151], a[68:71]\n\t" \
  "v_exp_f32 v39, v39\n\t" \
  "v_mfma_f32_16x16x32_bf16 a[72:75], a[144:147], v[144:147], a[72:75]\n\t" \
  "v_exp_f32 v40, v40\n\t" \
  "v_mfma_f32_16x16x32_bf16 a[72:75], a[148:151], v[148:151], a[72:75]\n\t" \
  "ds_read_b128 a[224:227], v220 offset:24576\n\t" \
  "v_cvt_pk_bf16_f32 v136, v32, v33\n\t" \
  "v_mfma_f32_16x16x32_bf16 a[76:79], a[152:155], v[144:147], a[76:79]\n\t" \
  "ds_read_b128 a[228:231], v221 offset:24576\n\t" \
  "v_cvt_pk_bf16_f32 v137, v34, v35\n\t" \
  "v_mfma_f32_16x16x32_bf16 a[76:79], a[156:159], v[148:151], a[76:79]\n\t" \
  "v_exp_f32 v41, v41\n\t" \
  "v_mfma_f32_16x16x32_bf16 a[80:83], a[160:163], v[144:147], a[80:83]\n\t" \
  "buffer_load_dwordx4 v231, s[64:67], s96 offen offset:1024 lds\n\t" \
  "v_mfma_f32_16x16x32_bf16 a[80:83], a[164:167], v[148:151], a[80:83]\n\t" \
  "v_exp_f32 v42, v42\n\t" \
  "v_mfma_f32_16x16x32_bf16 a[84:87], a[168:171], v[144:147], a[84:87]\n\t" \
  "ds_read_b128 a[232:235], v222 offset:24576\n\t" \
  "v_cvt_pk_bf16_f32 v138, v36, v37\n\t" \
  "v_mfma_f32_16x16x32_bf16 a[84:87], a[172:175], v[148:151], a[84:87]\n\t" \
  "v_exp_f32 v43, v43\n\t" \
  "v_mfma_f32_16x16x32_bf16 a[88:91], a[176:179], v[144:147], a[88:91]\n\t" \
  "ds_read_b128 a[236:239], v223 offset:24576\n\t" \
  "v_cvt_pk_bf16_f32 v139, v38, v39\n\t" \
  "v_mfma_f32_16x16x32_bf16 a[88:91], a[180:183], v[148:151], a[88:91]\n\t" \
  "v_exp_f32 v44, v44\n\t" \
  "v_mfma_f32_16x16x32_bf16 a[92:95], a[184:187], v[144:147], a[92:95]\n\t" \
  "buffer_load_dwordx4 v230, s[64:67], s96 offen offset:2048 lds\n\t" \
  "v_mfma_f32_16x16x32_bf16 a[92:95], a[188:191], v[148:151], a[92:95]\n\t" \
  "ds_read_b128 a[240:243], v220 offset:28672\n\t" \
  "v_cvt_pk_bf16_f32 v140, v40, v41\n\t" \
  "v_smfmac_f32_16x16x64_bf16 v[168:171], v[216:219], v[144:151], v208\n\t" \
  "ds_read_b128 a[244:247], v221 offset:28672\n\t" \
  "v_cvt_pk_bf16_f32 v141, v42, v43\n\t" \
  "v_mfma_f32_16x16x32_bf16 a[96:99], a[128:131], v[152:155], a[96:99]\n\t" \
  "v_exp_f32 v45, v45\n\t" \
  "v_mfma_f32_16x16x32_bf16 a[96:99], a[132:135], v[156:159], a[96:99]\n\t" \
  "v_exp_f32 v46, v46\n\t" \
  "v_mfma_f32_16x16x32_bf16 a[100:103], a[136:139], v[152:155], a[100:103]\n\t" \
  "v_exp_f32 v47, v47\n\t" \
  "v_mfma_f32_16x16x32_bf16 a[100:103], a[140:143], v[156:159], a[100:103]\n\t" \
  "buffer_load_dwordx4 v231, s[64:67], s96 offen offset:3072 lds\n\t" \
  "v_mfma_f32_16x16x32_bf16 a[104:107], a[144:147], v[152:155], a[104:107]\n\t" \
  "ds_read_b128 a[248:251], v222 offset:28672\n\t" \
  "v_cvt_pk_bf16_f32 v142, v44, v45\n\t" \
  "v_mfma_f32_16x16x32_bf16 a[104:107], a[148:151], v[156:159], a[104:107]\n\t" \
  "ds_read_b128 a[252:255], v223 offset:28672\n\t" \
  "v_cvt_pk_bf16_f32 v143, v46, v47\n\t" \
  "v_mfma_f32_16x16x32_bf16 a[108:111], a[152:155], v[152:155], a[108:111]\n\t" \
  "v_exp_f32 v64, v64\n\t" \
  "v_mfma_f32_16x16x32_bf16 a[108:111], a[156:159], v[156:159], a[108:111]\n\t" \
  "v_exp_f32 v65, v65\n\t" \
  "v_mfma_f32_16x16x32_bf16 a[112:115], a[160:163], v[152:155], a[112:115]\n\t" \
  "v_exp_f32 v66, v66\n\t" \
  "v_mfma_f32_16x16x32_bf16 a[112:115], a[164:167], v[156:159], a[112:115]\n\t" \
  "v_exp_f32 v67, v67\n\t" \
  "v_mfma_f32_16x16x32_bf16 a[116:119], a[168:171], v[152:155], a[116:119]\n\t" \
  "v_exp_f32 v68, v68\n\t" \
  "v_mfma_f32_16x16x32_bf16 a[116:119], a[172:175], v[156:159], a[116:119]\n\t" \
  "v_exp_f32 v69, v69\n\t" \
  "v_mfma_f32_16x16x32_bf16 a[120:123], a[176:179], v[152:155], a[120:123]\n\t" \
  "v_exp_f32 v70, v70\n\t" \
  "v_mfma_f32_16x16x32_bf16 a[120:123], a[180:183], v[156:159], a[120:123]\n\t" \
  "v_exp_f32 v71, v71\n\t" \
  "v_mfma_f32_16x16x32_bf16 a[124:127], a[184:187], v[152:155], a[124:127]\n\t" \
  "v_exp_f32 v72, v72\n\t" \
  "v_mfma_f32_16x16x32_bf16 a[124:127], a[188:191], v[156:159], a[124:127]\n\t" \
  "v_exp_f32 v73, v73\n\t" \
  "v_smfmac_f32_16x16x64_bf16 v[172:175], v[216:219], v[152:159], v208\n\t" \
  "s_waitcnt lgkmcnt(12)\n\t" \
  "s_cmp_lg_u32 s99, 0\n\t" \
  "s_cbranch_scc1 .Lsf_p2\n\t" \
  ".Lsf_q2:\n\t" \
  "v_mfma_f32_16x16x32_bf16 v[0:3], a[192:195], v[16:19], v[176:179]\n\t" \
  "ds_read_b64_tr_b16 a[128:129], v228 offset:0\n\t" \
  "v_cvt_pk_bf16_f32 v144, v64, v65\n\t" \
  "v_mfma_f32_16x16x32_bf16 v[0:3], a[196:199], v[20:23], v[0:3]\n\t" \
  "v_exp_f32 v74, v74\n\t" \
  "v_mfma_f32_16x16x32_bf16 v[0:3], a[200:203], v[24:27], v[0:3]\n\t" \
  "ds_read_b64_tr_b16 a[130:131], v228 offset:4096\n\t" \
  "v_cvt_pk_bf16_f32 v145, v66, v67\n\t" \
  "v_mfma_f32_16x16x32_bf16 v[0:3], a[204:207], v[28:31], v[0:3]\n\t" \
  "s_waitcnt lgkmcnt(10)\n\t" \
  "v_exp_f32 v75, v75\n\t" \
  "v_mfma_f32_16x16x32_bf16 v[4:7], a[220:223], v[28:31], v[176:179]\n\t" \
  "ds_read_b64_tr_b16 a[132:133], v228 offset:8192\n\t" \
  "v_cvt_pk_bf16_f32 v146, v68, v69\n\t" \
  "v_mfma_f32_16x16x32_bf16 v[4:7], a[216:219], v[24:27], v[4:7]\n\t" \
  "v_exp_f32 v76, v76\n\t" \
  "v_mfma_f32_16x16x32_bf16 v[4:7], a[212:215], v[20:23], v[4:7]\n\t" \
  "ds_read_b64_tr_b16 a[134:135], v228 offset:12288\n\t" \
  "v_max3_f32 v204, v0, v1, v2\n\t" \
  "v_mfma_f32_16x16x32_bf16 v[4:7], a[208:211], v[16:19], v[4:7]\n\t" \
  "s_waitcnt lgkmcnt(8)\n\t" \
  "ds_read_b64_tr_b16 a[136:137], v229 offset:0\n\t" \
  "v_cvt_pk_bf16_f32 v147, v70, v71\n\t" \
  "v_mfma_f32_16x16x32_bf16 v[8:11], a[224:227], v[16:19], v[176:179]\n\t" \
  "v_exp_f32 v77, v77\n\t" \
  "v_mfma_f32_16x16x32_bf16 v[8:11], a[228:231], v[20:23], v[8:11]\n\t" \
  "ds_read_b64_tr_b16 a[138:139], v229 offset:4096\n\t" \
  "v_cvt_pk_bf16_f32 v148, v72, v73\n\t" \
  "v_mfma_f32_16x16x32_bf16 v[8:11], a[232:235], v[24:27], v[8:11]\n\t" \
  "v_max3_f32 v203, v3, v4, v5\n\t" \
  "v_max3_f32 v204, v204, v6, v7\n\t" \
  "v_mfma_f32_16x16x32_bf16 v[8:11], a[236:239], v[28:31], v[8:11]\n\t" \
  "s_waitcnt lgkmcnt(6)\n\t" \
  "ds_read_b64_tr_b16 a[140:141], v229 offset:8192\n\t" \
  "v_cvt_pk_bf16_f32 v149, v74, v75\n\t" \
  "v_mfma_f32_16x16x32_bf16 v[12:15], a[252:255], v[28:31], v[176:179]\n\t" \
  "v_exp_f32 v78, v78\n\t" \
  "v_mfma_f32_16x16x32_bf16 v[12:15], a[248:251], v[24:27], v[12:15]\n\t" \
  "ds_read_b64_tr_b16 a[142:143], v229 offset:12288\n\t" \
  "v_cvt_pk_bf16_f32 v150, v76, v77\n\t" \
  "v_mfma_f32_16x16x32_bf16 v[12:15], a[244:247], v[20:23], v[12:15]\n\t" \
  "v_max3_f32 v203, v203, v8, v9\n\t" \
  "v_max3_f32 v204, v204, v10, v11\n\t" \
  "v_mfma_f32_16x16x32_bf16 v[12:15], a[240:243], v[16:19], v[12:15]\n\t" \
  "v_exp_f32 v79, v79\n\t" \
  "v_mfma_f32_16x16x32_bf16 v[32:35], a[192:195], v[48:51], v[180:183]\n\t" \
  "ds_read_b64_tr_b16 a[144:145], v228 offset:256\n\t" \
  "v_cvt_pk_bf16_f32 v151, v78, v79\n\t" \
  "v_mfma_f32_16x16x32_bf16 v[32:35], a[196:199], v[52:55], v[32:35]\n\t" \
  "v_exp_f32 v96, v96\n\t" \
  "v_mfma_f32_16x16x32_bf16 v[32:35], a[200:203], v[56:59], v[32:35]\n\t" \
  "ds_read_b64_tr_b16 a[146:147], v228 offset:4352\n\t" \
  "v_max3_f32 v203, v203, v12, v13\n\t" \
  "v_mfma_f32_16x16x32_bf16 v[32:35], a[204:207], v[60:63], v[32:35]\n\t" \
  "v_max3_f32 v204, v204, v14, v15\n\t" \
  "v_max_f32 v204, v204, v203\n\t" \
  "v_mfma_f32_16x16x32_bf16 v[36:39], a[220:223], v[60:63], v[180:183]\n\t" \
  "ds_read_b64_tr_b16 a[148:149], v228 offset:8448\n\t" \
  "v_cmp_lt_f32 vcc, 0x42800000, v204\n\t" \
  "v_mfma_f32_16x16x32_bf16 v[36:39], a[216:219], v[56:59], v[36:39]\n\t" \
  "v_exp_f32 v97, v97\n\t" \
  "v_mfma_f32_16x16x32_bf16 v[36:39], a[212:215], v[52:55], v[36:39]\n\t" \
  "s_cbranch_vccnz .Lsf_c30\n\t" \
  ".Lsf_r30:\n\t" \
  "v_mfma_f32_16x16x32_bf16 v[36:39], a[208:211], v[48:51], v[36:39]\n\t" \
  "ds_read_b64_tr_b16 a[150:151], v228 offset:12544\n\t" \
  "v_max3_f32 v204, v32, v33, v34\n\t" \
  "v_mfma_f32_16x16x32_bf16 v[40:43], a[224:227], v[48:51], v[180:183]\n\t" \
  "ds_read_b64_tr_b16 a[152:153], v229 offset:256\n\t" \
  "v_cvt_pk_bf16_f32 v152, v96, v97\n\t" \
  "v_mfma_f32_16x16x32_bf16 v[40:43], a[228:231], v[52:55], v[40:43]\n\t" \
  "v_exp_f32 v98, v98\n\t" \
  "v_mfma_f32_16x16x32_bf16 v[40:43], a[232:235], v[56:59], v[40:43]\n\t" \
  "v_max3_f32 v203, v35, v36, v37\n\t" \
  "v_max3_f32 v204, v204, v38, v39\n\t" \
  "v_mfma_f32_16x16x32_bf16 v[40:43], a[236:239], v[60:63], v[40:43]\n\t" \
  "v_exp_f32 v99, v99\n\t" \
  "v_mfma_f32_16x16x32_bf16 v[44:47], a[252:255], v[60:63], v[180:183]\n\t" \
  "ds_read_b64_tr_b16 a[154:155], v229 offset:4352\n\t" \
  "v_cvt_pk_bf16_f32 v153, v98, v99\n\t" \
  "v_mfma_f32_16x16x32_bf16 v[44:47], a[248:251], v[56:59], v[44:47]\n\t" \
  "v_exp_f32 v100, v100\n\t" \
  "v_mfma_f32_16x16x32_bf16 v[44:47], a[244:247], v[52:55], v[44:47]\n\t" \
  "ds_read_b64_tr_b16 a[156:157], v229 offset:8448\n\t" \
  "v_max3_f32 v203, v203, v40, v41\n\t" \
  "v_mfma_f32_16x16x32_bf16 v[44:47], a[240:243], v[48:51], v[44:47]\n\t" \
  "ds_read_b64_tr_b16 a[158:159], v229 offset:12544\n\t" \
  "v_max3_f32 v204, v204, v42, v43\n\t" \
  "v_mfma_f32_16x16x32_bf16 v[64:67], a[192:195], v[80:83], v[184:187]\n\t" \
  "v_exp_f32 v101, v101\n\t" \
  "v_mfma_f32_16x16x32_bf16 v[64:67], a[196:199], v[84:87], v[64:67]\n\t" \
  "ds_read_b64_tr_b16 a[160:161], v228 offset:512\n\t" \
  "v_cvt_pk_bf16_f32 v154, v100, v101\n\t" \
  "v_mfma_f32_16x16x32_bf16 v[64:67], a[200:203], v[88:91], v[64:67]\n\t" \
  "v_max3_f32 v203, v203, v44, v45\n\t" \
  "v_max3_f32 v204, v204, v46, v47\n\t" \
  "v_mfma_f32_16x16x32_bf16 v[64:67], a[204:207], v[92:95], v[64:67]\n\t" \
  "v_max_f32 v204, v204, v203\n\t" \
  "v_cmp_lt_f32 vcc, 0x42800000, v204\n\t" \
  "v_mfma_f32_16x16x32_bf16 v[68:71], a[220:223], v[92:95], v[184:187]\n\t" \
  "v_exp_f32 v102, v102\n\t" \
  "v_mfma_f32_16x16x32_bf16 v[68:71], a[216:219], v[88:91], v[68:71]\n\t" \
  "s_cbranch_vccnz .Lsf_c31\n\t" \
  ".Lsf_r31:\n\t" \
  "v_mfma_f32_16x16x32_bf16 v[68:71], a[212:215], v[84:87], v[68:71]\n\t" \
  "ds_read_b64_tr_b16 a[162:163], v228 offset:4608\n\t" \
  "v_max3_f32 v204, v64, v65, v66\n\t" \
  "v_mfma_f32_16x16x32_bf16 v[68:71], a[208:211], v[80:83], v[68:71]\n\t" \
  "ds_read_b64_tr_b16 a[164:165], v228 offset:8704\n\t" \
  "v_exp_f32 v103, v103\n\t" \
  "v_mfma_f32_16x16x32_bf16 v[72:75], a[224:227], v[80:83], v[184:187]\n\t" \
  "ds_read_b64_tr_b16 a[166:167], v228 offset:12800\n\t" \
  "v_exp_f32 v104, v104\n\t" \
  "v_mfma_f32_16x16x32_bf16 v[72:75], a[228:231], v[84:87], v[72:75]\n\t" \
  "ds_read_b64_tr_b16 a[168:169], v229 offset:512\n\t" \
  "v_exp_f32 v105, v105\n\t" \
  "v_mfma_f32_16x16x32_bf16 v[72:75], a[232:235], v[88:91], v[72:75]\n\t" \
  "v_max3_f32 v203, v67, v68, v69\n\t" \
  "v_max3_f32 v204, v204, v70, v71\n\t" \
  "v_mfma_f32_16x16x32_bf16 v[72:75], a[236:239], v[92:95], v[72:75]\n\t" \
  "ds_read_b64_tr_b16 a[170:171], v229 offset:4608\n\t" \
  "v_exp_f32 v106, v106\n\t" \
  "v_mfma_f32_16x16x32_bf16 v[76:79], a[252:255], v[92:95], v[184:187]\n\t" \
  "ds_read_b64_tr_b16 a[172:173], v229 offset:8704\n\t" \
  "v_exp_f32 v107, v107\n\t" \
  "v_mfma_f32_16x16x32_bf16 v[76:79], a[248:251], v[88:91], v[76:79]\n\t" \
  "ds_read_b64_tr_b16 a[174:175], v229 offset:12800\n\t" \
  "v_cvt_pk_bf16_f32 v155, v102, v103\n\t" \
  "v_mfma_f32_16x16x32_bf16 v[76:79], a[244:247], v[84:87], v[76:79]\n\t" \
  "v_max3_f32 v203, v203, v72, v73\n\t" \
  "v_max3_f32 v204, v204, v74, v75\n\t" \
  "v_mfma_f32_16x16x32_bf16 v[76:79], a[240:243], v[80:83], v[76:79]\n\t" \
  "ds_read_b64_tr_b16 a[176:177], v228 offset:768\n\t" \
  "v_exp_f32 v108, v108\n\t" \
  "v_mfma_f32_16x16x32_bf16 v[96:99], a[192:195], v[112:115], v[188:191]\n\t" \
  "ds_read_b64_tr_b16 a[178:179], v228 offset:4864\n\t" \
  "v_exp_f32 v109, v109\n\t" \
  "v_mfma_f32_16x16x32_bf16 v[96:99], a[196:199], v[116:119], v[96:99]\n\t" \
  "ds_read_b64_tr_b16 a[180:181], v228 offset:8960\n\t" \
  "v_cvt_pk_bf16_f32 v156, v104, v105\n\t" \
  "v_mfma_f32_16x16x32_bf16 v[96:99], a[200:203], v[120:123], v[96:99]\n\t" \
  "v_max3_f32 v203, v203, v76, v77\n\t" \
  "v_max3_f32 v204, v204, v78, v79\n\t" \
  "v_mfma_f32_16x16x32_bf16 v[96:99], a[204:207], v[124:127], v[96:99]\n\t" \
  "v_max_f32 v204, v204, v203\n\t" \
  "v_cmp_lt_f32 vcc, 0x42800000, v204\n\t" \
  "v_mfma_f32_16x16x32_bf16 v[100:103], a[220:223], v[124:127], v[188:191]\n\t" \
  "ds_read_b64_tr_b16 a[182:183], v228 offset:13056\n\t" \
  "v_exp_f32 v110, v110\n\t" \
  "v_mfma_f32_16x16x32_bf16 v[100:103], a[216:219], v[120:123], v[100:103]\n\t" \
  "s_cbranch_vccnz .Lsf_c32\n\t" \
  ".Lsf_r32:\n\t" \
  "v_mfma_f32_16x16x32_bf16 v[100:103], a[212:215], v[116:119], v[100:103]\n\t" \
  "v_max3_f32 v204, v96, v97, v98\n\t" \
  "v_exp_f32 v111, v111\n\t" \
  "v_mfma_f32_16x16x32_bf16 v[100:103], a[208:211], v[112:115], v[100:103]\n\t" \
  "ds_read_b64_tr_b16 a[184:185], v229 offset:768\n\t" \
  "v_cvt_pk_bf16_f32 v157, v106, v107\n\t" \
  "v_mfma_f32_16x16x32_bf16 v[104:107], a[224:227], v[112:115], v[188:191]\n\t" \
  "ds_read_b64_tr_b16 a[186:187], v229 offset:4864\n\t" \
  "v_cvt_pk_bf16_f32 v158, v108, v109\n\t" \
  "v_mfma_f32_16x16x32_bf16 v[104:107], a[228:231], v[116:119], v[104:107]\n\t" \
  "ds_read_b64_tr_b16 a[188:189], v229 offset:8960\n\t" \
  "v_cvt_pk_bf16_f32 v159, v110, v111\n\t" \
  "v_mfma_f32_16x16x32_bf16 v[104:107], a[232:235], v[120:123], v[104:107]\n\t" \
  "v_max3_f32 v203, v99, v100, v101\n\t" \
  "v_max3_f32 v204, v204, v102, v103\n\t" \
  "v_mfma_f32_16x16x32_bf16 v[104:107], a[236:239], v[124:127], v[104:107]\n\t" \
  "ds_read_b64_tr_b16 a[190:191], v229 offset:13056\n\t" \
  "s_nop 0\n\t" \
  "v_mfma_f32_16x16x32_bf16 v[108:111], a[252:255], v[124:127], v[188:191]\n\t" \
  "v_exp_f32 v0, v0\n\t" \
  "v_mfma_f32_16x16x32_bf16 v[108:111], a[248:251], v[120:123], v[108:111]\n\t" \
  "v_exp_f32 v1, v1\n\t" \
  "v_mfma_f32_16x16x32_bf16 v[108:111], a[244:247], v[116:119], v[108:111]\n\t" \
  "s_waitcnt vmcnt(0) lgkmcnt(0)\n\t" \
  "v_max3_f32 v203, v203, v104, v105\n\t" \
  "v_max3_f32 v204, v204, v106, v107\n\t" \
  "v_mfma_f32_16x16x32_bf16 v[108:111], a[240:243], v[112:115], v[108:111]\n\t" \
  "s_barrier\n\t" \
  "v_mfma_f32_16x16x32_bf16 a[0:3], a[128:131], v[128:131], a[0:3]\n\t" \
  "v_exp_f32 v2, v2\n\t" \
  "v_mfma_f32_16x16x32_bf16 a[0:3], a[132:135], v[132:135], a[0:3]\n\t" \
  "v_exp_f32 v3, v3\n\t" \
  "v_mfma_f32_16x16x32_bf16 a[4:7], a[136:139], v[128:131], a[4:7]\n\t" \
  "v_exp_f32 v4, v4\n\t" \
  "v_mfma_f32_16x16x32_bf16 a[4:7], a[140:143], v[132:135], a[4:7]\n\t" \
  "v_max3_f32 v203, v203, v108, v109\n\t" \
  "v_max3_f32 v204, v204, v110, v111\n\t" \
  "v_mfma_f32_16x16x32_bf16 a[8:11], a[144:147], v[128:131], a[8:11]\n\t" \
  "v_max_f32 v204, v204, v203\n\t" \
  "v_cmp_lt_f32 vcc, 0x42800000, v204\n\t" \
  "v_mfma_f32_16x16x32_bf16 a[8:11], a[148:151], v[132:135], a[8:11]\n\t" \
  "v_exp_f32 v5, v5\n\t" \
  "v_mfma_f32_16x16x32_bf16 a[12:15], a[152:155], v[128:131], a[12:15]\n\t" \
  "s_cbranch_vccnz .Lsf_c33\n\t" \
  ".Lsf_r33:\n\t" \
  "v_mfma_f32_16x16x32_bf16 a[12:15], a[156:159], v[132:135], a[12:15]\n\t" \
  "s_add_u32 m0, s38, 65536\n\t" \
  "v_exp_f32 v6, v6\n\t" \
  "v_mfma_f32_16x16x32_bf16 a[16:19], a[160:163], v[128:131], a[16:19]\n\t" \
  "s_add_u32 s96, s38, 81920\n\t" \
  "v_exp_f32 v7, v7\n\t" \
  "v_mfma_f32_16x16x32_bf16 a[16:19], a[164:167], v[132:135], a[16:19]\n\t" \
  "v_exp_f32 v8, v8\n\t" \
  "v_mfma_f32_16x16x32_bf16 a[20:23], a[168:171], v[128:131], a[20:23]\n\t" \
  "buffer_load_dwordx4 v224, s[60:63], s96 offen offset:0 lds\n\t" \
  "v_mfma_f32_16x16x32_bf16 a[20:23], a[172:175], v[132:135], a[20:23]\n\t" \
  "v_exp_f32 v9, v9\n\t" \
  "v_mfma_f32_16x16x32_bf16 a[24:27], a[176:179], v[128:131], a[24:27]\n\t" \
  "v_exp_f32 v10, v10\n\t" \
  "v_mfma_f32_16x16x32_bf16 a[24:27], a[180:183], v[132:135], a[24:27]\n\t" \
  "v_exp_f32 v11, v11\n\t" \
  "v_mfma_f32_16x16x32_bf16 a[28:31], a[184:187], v[128:131], a[28:31]\n\t" \
  "v_exp_f32 v12, v12\n\t" \
  "v_mfma_f32_16x16x32_bf16 a[28:31], a[188:191], v[132:135], a[28:31]\n\t" \
  "v_exp_f32 v13, v13\n\t" \
  "v_smfmac_f32_16x16x64_bf16 v[160:163], v[216:219], v[128:135], v208\n\t" \
  "ds_read_b128 a[192:195], v220 offset:0\n\t" \
  "v_cvt_pk_bf16_f32 v128, v0, v1\n\t" \
  "v_mfma_f32_16x16x32_bf16 a[32:35], a[128:131], v[136:139], a[32:35]\n\t" \
  "buffer_load_dwordx4 v225, s[60:63], s96 offen offset:1024 lds\n\t" \
  "v_mfma_f32_16x16x32_bf16 a[32:35], a[132:135], v[140:143], a[32:35]\n\t" \
  "v_exp_f32 v14, v14\n\t" \
  "v_mfma_f32_16x16x32_bf16 a[36:39], a[136:139], v[136:139], a[36:39]\n\t" \
  "v_exp_f32 v15, v15\n\t" \
  "v_mfma_f32_16x16x32_bf16 a[36:39], a[140:143], v[140:143], a[36:39]\n\t" \
  "ds_read_b128 a[196:199], v221 offset:0\n\t" \
  "v_cvt_pk_bf16_f32 v129, v2, v3\n\t" \
  "v_mfma_f32_16x16x32_bf16 a[40:43], a[144:147], v[136:139], a[40:43]\n\t" \
  "v_exp_f32 v32, v32\n\t" \
  "v_mfma_f32_16x16x32_bf16 a[40:43], a[148:151], v[140:143], a[40:43]\n\t" \
  "v_exp_f32 v33, v33\n\t" \
  "v_mfma_f32_16x16x32_bf16 a[44:47], a[152:155], v[136:139], a[44:47]\n\t" \
  "buffer_load_dwordx4 v226, s[60:63], s96 offen offset:2048 lds\n\t" \
  "v_mfma_f32_16x16x32_bf16 a[44:47], a[156:159], v[140:143], a[44:47]\n\t" \
  "ds_read_b128 a[200:203], v222 offset:0\n\t" \
  "v_cvt_pk_bf16_f32 v130, v4, v5\n\t" \
  "v_mfma_f32_16x16x32_bf16 a[48:51], a[160:163], v[136:139], a[48:51]\n\t" \
  "v_exp_f32 v34, v34\n\t" \
  "v_mfma_f32_16x16x32_bf16 a[48:51], a[164:167], v[140:143], a[48:51]\n\t" \
  "ds_read_b128 a[204:207], v223 offset:0\n\t" \
  "v_cvt_pk_bf16_f32 v131, v6, v7\n\t" \
  "v_mfma_f32_16x16x32_bf16 a[52:55], a[168:171], v[136:139], a[52:55]\n\t" \
  "v_exp_f32 v35, v35\n\t" \
  "v_mfma_f32_16x16x32_bf16 a[52:55], a[172:175], v[140:143], a[52:55]\n\t" \
  "ds_read_b128 a[208:211], v220 offset:4096\n\t" \
  "v_cvt_pk_bf16_f32 v132, v8, v9\n\t" \
  "v_mfma_f32_16x16x32_bf16 a[56:59], a[176:179], v[136:139], a[56:59]\n\t" \
  "buffer_load_dwordx4 v227, s[60:63], s96 offen offset:3072 lds\n\t" \
  "v_mfma_f32_16x16x32_bf16 a[56:59], a[180:183], v[140:143], a[56:59]\n\t" \
  "v_exp_f32 v36, v36\n\t" \
  "v_mfma_f32_16x16x32_bf16 a[60:63], a[184:187], v[136:139], a[60:63]\n\t" \
  "s_add_u32 m0, s38, 81920\n\t" \
  "ds_read_b128 a[212:215], v221 offset:4096\n\t" \
  "v_cvt_pk_bf16_f32 v133, v10, v11\n\t" \
  "v_mfma_f32_16x16x32_bf16 a[60:63], a[188:191], v[140:143], a[60:63]\n\t" \
  "s_add_u32 s96, s38, 65536\n\t" \
  "v_exp_f32 v37, v37\n\t" \
  "v_smfmac_f32_16x16x64_bf16 v[164:167], v[216:219], v[136:143], v208\n\t" \
  "ds_read_b128 a[216:219], v222 offset:4096\n\t" \
  "v_cvt_pk_bf16_f32 v134, v12, v13\n\t" \
  "v_mfma_f32_16x16x32_bf16 a[64:67], a[128:131], v[144:147], a[64:67]\n\t" \
  "v_exp_f32 v38, v38\n\t" \
  "v_mfma_f32_16x16x32_bf16 a[64:67], a[132:135], v[148:151], a[64:67]\n\t" \
  "buffer_load_dwordx4 v230, s[64:67], s96 offen offset:0 lds\n\t" \
  "v_mfma_f32_16x16x32_bf16 a[68:71], a[136:139], v[144:147], a[68:71]\n\t" \
  "ds_read_b128 a[220:223], v223 offset:4096\n\t" \
  "v_cvt_pk_bf16_f32 v135, v14, v15\n\t" \
  "v_mfma_f32_16x16x32_bf16 a[68:71], a[140:143], v[148:151], a[68:71]\n\t" \
  "v_exp_f32 v39, v39\n\t" \
  "v_mfma_f32_16x16x32_bf16 a[72:75], a[144:147], v[144:147], a[72:75]\n\t" \
  "v_exp_f32 v40, v40\n\t" \
  "v_mfma_f32_16x16x32_bf16 a[72:75], a[148:151], v[148:151], a[72:75]\n\t" \
  "ds_read_b128 a[224:227], v220 offset:8192\n\t" \
  "v_cvt_pk_bf16_f32 v136, v32, v33\n\t" \
  "v_mfma_f32_16x16x32_bf16 a[76:79], a[152:155], v[144:147], a[76:79]\n\t" \
  "ds_read_b128 a[228:231], v221 offset:8192\n\t" \
  "v_cvt_pk_bf16_f32 v137, v34, v35\n\t" \
  "v_mfma_f32_16x16x32_bf16 a[76:79], a[156:159], v[148:151], a[76:79]\n\t" \
  "v_exp_f32 v41, v41\n\t" \
  "v_mfma_f32_16x16x32_bf16 a[80:83], a[160:163], v[144:147], a[80:83]\n\t" \
  "buffer_load_dwordx4 v231, s[64:67], s96 offen offset:1024 lds\n\t" \
  "v_mfma_f32_16x16x32_bf16 a[80:83], a[164:167], v[148:151], a[80:83]\n\t" \
  "v_exp_f32 v42, v42\n\t" \
  "v_mfma_f32_16x16x32_bf16 a[84:87], a[168:171], v[144:147], a[84:87]\n\t" \
  "ds_read_b128 a[232:235], v222 offset:8192\n\t" \
  "v_cvt_pk_bf16_f32 v138, v36, v37\n\t" \
  "v_mfma_f32_16x16x32_bf16 a[84:87], a[172:175], v[148:151], a[84:87]\n\t" \
  "v_exp_f32 v43, v43\n\t" \
  "v_mfma_f32_16x16x32_bf16 a[88:91], a[176:179], v[144:147], a[88:91]\n\t" \
  "ds_read_b128 a[236:239], v223 offset:8192\n\t" \
  "v_cvt_pk_bf16_f32 v139, v38, v39\n\t" \
  "v_mfma_f32_16x16x32_bf16 a[88:91], a[180:183], v[148:151], a[88:91]\n\t" \
  "v_exp_f32 v44, v44\n\t" \
  "v_mfma_f32_16x16x32_bf16 a[92:95], a[184:187], v[144:147], a[92:95]\n\t" \
  "buffer_load_dwordx4 v230, s[64:67], s96 offen offset:2048 lds\n\t" \
  "v_mfma_f32_16x16x32_bf16 a[92:95], a[188:191], v[148:151], a[92:95]\n\t" \
  "ds_read_b128 a[240:243], v220 offset:12288\n\t" \
  "v_cvt_pk_bf16_f32 v140, v40, v41\n\t" \
  "v_smfmac_f32_16x16x64_bf16 v[168:171], v[216:219], v[144:151], v208\n\t" \
  "ds_read_b128 a[244:247], v221 offset:12288\n\t" \
  "v_cvt_pk_bf16_f32 v141, v42, v43\n\t" \
  "v_mfma_f32_16x16x32_bf16 a[96:99], a[128:131], v[152:155], a[96:99]\n\t" \
  "v_exp_f32 v45, v45\n\t" \
  "v_mfma_f32_16x16x32_bf16 a[96:99], a[132:135], v[156:159], a[96:99]\n\t" \
  "v_exp_f32 v46, v46\n\t" \
  "v_mfma_f32_16x16x32_bf16 a[100:103], a[136:139], v[152:155], a[100:103]\n\t" \
  "v_exp_f32 v47, v47\n\t" \
  "v_mfma_f32_16x16x32_bf16 a[100:103], a[140:143], v[156:159], a[100:103]\n\t" \
  "buffer_load_dwordx4 v231, s[64:67], s96 offen offset:3072 lds\n\t" \
  "v_mfma_f32_16x16x32_bf16 a[104:107], a[144:147], v[152:155], a[104:107]\n\t" \
  "ds_read_b128 a[248:251], v222 offset:12288\n\t" \
  "v_cvt_pk_bf16_f32 v142, v44, v45\n\t" \
  "v_mfma_f32_16x16x32_bf16 a[104:107], a[148:151], v[156:159], a[104:107]\n\t" \
  "ds_read_b128 a[252:255], v223 offset:12288\n\t" \
  "v_cvt_pk_bf16_f32 v143, v46, v47\n\t" \
  "v_mfma_f32_16x16x32_bf16 a[108:111], a[152:155], v[152:155], a[108:111]\n\t" \
  "v_exp_f32 v64, v64\n\t" \
  "v_mfma_f32_16x16x32_bf16 a[108:111], a[156:159], v[156:159], a[108:111]\n\t" \
  "v_exp_f32 v65, v65\n\t" \
  "v_mfma_f32_16x16x32_bf16 a[112:115], a[160:163], v[152:155], a[112:115]\n\t" \
  "v_exp_f32 v66, v66\n\t" \
  "v_mfma_f32_16x16x32_bf16 a[112:115], a[164:167], v[156:159], a[112:115]\n\t" \
  "v_exp_f32 v67, v67\n\t" \
  "v_mfma_f32_16x16x32_bf16 a[116:119], a[168:171], v[152:155], a[116:119]\n\t" \
  "v_exp_f32 v68, v68\n\t" \
  "v_mfma_f32_16x16x32_bf16 a[116:119], a[172:175], v[156:159], a[116:119]\n\t" \
  "v_exp_f32 v69, v69\n\t" \
  "v_mfma_f32_16x16x32_bf16 a[120:123], a[176:179], v[152:155], a[120:123]\n\t" \
  "v_exp_f32 v70, v70\n\t" \
  "v_mfma_f32_16x16x32_bf16 a[120:123], a[180:183], v[156:159], a[120:123]\n\t" \
  "v_exp_f32 v71, v71\n\t" \
  "v_mfma_f32_16x16x32_bf16 a[124:127], a[184:187], v[152:155], a[124:127]\n\t" \
  "v_exp_f32 v72, v72\n\t" \
  "v_mfma_f32_16x16x32_bf16 a[124:127], a[188:191], v[156:159], a[124:127]\n\t" \
  "v_exp_f32 v73, v73\n\t" \
  "v_smfmac_f32_16x16x64_bf16 v[172:175], v[216:219], v[152:159], v208\n\t" \
  "s_waitcnt lgkmcnt(12)\n\t" \
  "s_cmp_lg_u32 s99, 0\n\t" \
  "s_cbranch_scc1 .Lsf_p3\n\t" \
  ".Lsf_q3:\n\t" \
  "v_mfma_f32_16x16x32_bf16 v[0:3], a[192:195], v[16:19], v[176:179]\n\t" \
  "ds_read_b64_tr_b16 a[128:129], v210 offset:0\n\t" \
  "v_cvt_pk_bf16_f32 v144, v64, v65\n\t" \
  "v_mfma_f32_16x16x32_bf16 v[0:3], a[196:199], v[20:23], v[0:3]\n\t" \
  "v_exp_f32 v74, v74\n\t" \
  "v_mfma_f32_16x16x32_bf16 v[0:3], a[200:203], v[24:27], v[0:3]\n\t" \
  "ds_read_b64_tr_b16 a[130:131], v210 offset:4096\n\t" \
  "v_cvt_pk_bf16_f32 v145, v66, v67\n\t" \
  "v_mfma_f32_16x16x32_bf16 v[0:3], a[204:207], v[28:31], v[0:3]\n\t" \
  "s_waitcnt lgkmcnt(10)\n\t" \
  "v_exp_f32 v75, v75\n\t" \
  "v_mfma_f32_16x16x32_bf16 v[4:7], a[220:223], v[28:31], v[176:179]\n\t" \
  "ds_read_b64_tr_b16 a[132:133], v210 offset:8192\n\t" \
  "v_cvt_pk_bf16_f32 v146, v68, v69\n\t" \
  "v_mfma_f32_16x16x32_bf16 v[4:7], a[216:219], v[24:27], v[4:7]\n\t" \
  "v_exp_f32 v76, v76\n\t" \
  "v_mfma_f32_16x16x32_bf16 v[4:7], a[212:215], v[20:23], v[4:7]\n\t" \
  "ds_read_b64_tr_b16 a[134:135], v210 offset:12288\n\t" \
  "v_max3_f32 v204, v0, v1, v2\n\t" \
  "v_mfma_f32_16x16x32_bf16 v[4:7], a[208:211], v[16:19], v[4:7]\n\t" \
  "s_waitcnt lgkmcnt(8)\n\t" \
  "ds_read_b64_tr_b16 a[136:137], v211 offset:0\n\t" \
  "v_cvt_pk_bf16_f32 v147, v70, v71\n\t" \
  "v_mfma_f32_16x16x32_bf16 v[8:11], a[224:227], v[16:19], v[176:179]\n\t" \
  "v_exp_f32 v77, v77\n\t" \
  "v_mfma_f32_16x16x32_bf16 v[8:11], a[228:231], v[20:23], v[8:11]\n\t" \
  "ds_read_b64_tr_b16 a[138:139], v211 offset:4096\n\t" \
  "v_cvt_pk_bf16_f32 v148, v72, v73\n\t" \
  "v_mfma_f32_16x16x32_bf16 v[8:11], a[232:235], v[24:27], v[8:11]\n\t" \
  "v_max3_f32 v203, v3, v4, v5\n\t" \
  "v_max3_f32 v204, v204, v6, v7\n\t" \
  "v_mfma_f32_16x16x32_bf16 v[8:11], a[236:239], v[28:31], v[8:11]\n\t" \
  "s_waitcnt lgkmcnt(6)\n\t" \
  "ds_read_b64_tr_b16 a[140:141], v211 offset:8192\n\t" \
  "v_cvt_pk_bf16_f32 v149, v74, v75\n\t" \
  "v_mfma_f32_16x16x32_bf16 v[12:15], a[252:255], v[28:31], v[176:179]\n\t" \
  "v_exp_f32 v78, v78\n\t" \
  "v_mfma_f32_16x16x32_bf16 v[12:15], a[248:251], v[24:27], v[12:15]\n\t" \
  "ds_read_b64_tr_b16 a[142:143], v211 offset:12288\n\t" \
  "v_cvt_pk_bf16_f32 v150, v76, v77\n\t" \
  "v_mfma_f32_16x16x32_bf16 v[12:15], a[244:247], v[20:23], v[12:15]\n\t" \
  "v_max3_f32 v203, v203, v8, v9\n\t" \
  "v_max3_f32 v204, v204, v10, v11\n\t" \
  "v_mfma_f32_16x16x32_bf16 v[12:15], a[240:243], v[16:19], v[12:15]\n\t" \
  "v_exp_f32 v79, v79\n\t" \
  "v_mfma_f32_16x16x32_bf16 v[32:35], a[192:195], v[48:51], v[180:183]\n\t" \
  "ds_read_b64_tr_b16 a[144:145], v210 offset:256\n\t" \
  "v_cvt_pk_bf16_f32 v151, v78, v79\n\t" \
  "v_mfma_f32_16x16x32_bf16 v[32:35], a[196:199], v[52:55], v[32:35]\n\t" \
  "v_exp_f32 v96, v96\n\t" \
  "v_mfma_f32_16x16x32_bf16 v[32:35], a[200:203], v[56:59], v[32:35]\n\t" \
  "ds_read_b64_tr_b16 a[146:147], v210 offset:4352\n\t" \
  "v_max3_f32 v203, v203, v12, v13\n\t" \
  "v_mfma_f32_16x16x32_bf16 v[32:35], a[204:207], v[60:63], v[32:35]\n\t" \
  "v_max3_f32 v204, v204, v14, v15\n\t" \
  "v_max_f32 v204, v204, v203\n\t" \
  "v_mfma_f32_16x16x32_bf16 v[36:39], a[220:223], v[60:63], v[180:183]\n\t" \
  "ds_read_b64_tr_b16 a[148:149], v210 offset:8448\n\t" \
  "v_cmp_lt_f32 vcc, 0x42800000, v204\n\t" \
  "v_mfma_f32_16x16x32_bf16 v[36:39], a[216:219], v[56:59], v[36:39]\n\t" \
  "v_exp_f32 v97, v97\n\t" \
  "v_mfma_f32_16x16x32_bf16 v[36:39], a[212:215], v[52:55], v[36:39]\n\t" \
  "s_cbranch_vccnz .Lsf_c40\n\t" \
  ".Lsf_r40:\n\t" \
  "v_mfma_f32_16x16x32_bf16 v[36:39], a[208:211], v[48:51], v[36:39]\n\t" \
  "ds_read_b64_tr_b16 a[150:151], v210 offset:12544\n\t" \
  "v_max3_f32 v204, v32, v33, v34\n\t" \
  "v_mfma_f32_16x16x32_bf16 v[40:43], a[224:227], v[48:51], v[180:183]\n\t" \
  "ds_read_b64_tr_b16 a[152:153], v211 offset:256\n\t" \
  "v_cvt_pk_bf16_f32 v152, v96, v97\n\t" \
  "v_mfma_f32_16x16x32_bf16 v[40:43], a[228:231], v[52:55], v[40:43]\n\t" \
  "v_exp_f32 v98, v98\n\t" \
  "v_mfma_f32_16x16x32_bf16 v[40:43], a[232:235], v[56:59], v[40:43]\n\t" \
  "v_max3_f32 v203, v35, v36, v37\n\t" \
  "v_max3_f32 v204, v204, v38, v39\n\t" \
  "v_mfma_f32_16x16x32_bf16 v[40:43], a[236:239], v[60:63], v[40:43]\n\t" \
  "v_exp_f32 v99, v99\n\t" \
  "v_mfma_f32_16x16x32_bf16 v[44:47], a[252:255], v[60:63], v[180:183]\n\t" \
  "ds_read_b64_tr_b16 a[154:155], v211 offset:4352\n\t" \
  "v_cvt_pk_bf16_f32 v153, v98, v99\n\t" \
  "v_mfma_f32_16x16x32_bf16 v[44:47], a[248:251], v[56:59], v[44:47]\n\t" \
  "v_exp_f32 v100, v100\n\t" \
  "v_mfma_f32_16x16x32_bf16 v[44:47], a[244:247], v[52:55], v[44:47]\n\t" \
  "ds_read_b64_tr_b16 a[156:157], v211 offset:8448\n\t" \
  "v_max3_f32 v203, v203, v40, v41\n\t" \
  "v_mfma_f32_16x16x32_bf16 v[44:47], a[240:243], v[48:51], v[44:47]\n\t" \
  "ds_read_b64_tr_b16 a[158:159], v211 offset:12544\n\t" \
  "v_max3_f32 v204, v204, v42, v43\n\t" \
  "v_mfma_f32_16x16x32_bf16 v[64:67], a[192:195], v[80:83], v[184:187]\n\t" \
  "v_exp_f32 v101, v101\n\t" \
  "v_mfma_f32_16x16x32_bf16 v[64:67], a[196:199], v[84:87], v[64:67]\n\t" \
  "ds_read_b64_tr_b16 a[160:161], v210 offset:512\n\t" \
  "v_cvt_pk_bf16_f32 v154, v100, v101\n\t" \
  "v_mfma_f32_16x16x32_bf16 v[64:67], a[200:203], v[88:91], v[64:67]\n\t" \
  "v_max3_f32 v203, v203, v44, v45\n\t" \
  "v_max3_f32 v204, v204, v46, v47\n\t" \
  "v_mfma_f32_16x16x32_bf16 v[64:67], a[204:207], v[92:95], v[64:67]\n\t" \
  "v_max_f32 v204, v204, v203\n\t" \
  "v_cmp_lt_f32 vcc, 0x42800000, v204\n\t" \
  "v_mfma_f32_16x16x32_bf16 v[68:71], a[220:223], v[92:95], v[184:187]\n\t" \
  "v_exp_f32 v102, v102\n\t" \
  "v_mfma_f32_16x16x32_bf16 v[68:71], a[216:219], v[88:91], v[68:71]\n\t" \
  "s_cbranch_vccnz .Lsf_c41\n\t" \
  ".Lsf_r41:\n\t" \
  "v_mfma_f32_16x16x32_bf16 v[68:71], a[212:215], v[84:87], v[68:71]\n\t" \
  "ds_read_b64_tr_b16 a[162:163], v210 offset:4608\n\t" \
  "v_max3_f32 v204, v64, v65, v66\n\t" \
  "v_mfma_f32_16x16x32_bf16 v[68:71], a[208:211], v[80:83], v[68:71]\n\t" \
  "ds_read_b64_tr_b16 a[164:165], v210 offset:8704\n\t" \
  "v_exp_f32 v103, v103\n\t" \
  "v_mfma_f32_16x16x32_bf16 v[72:75], a[224:227], v[80:83], v[184:187]\n\t" \
  "ds_read_b64_tr_b16 a[166:167], v210 offset:12800\n\t" \
  "v_exp_f32 v104, v104\n\t" \
  "v_mfma_f32_16x16x32_bf16 v[72:75], a[228:231], v[84:87], v[72:75]\n\t" \
  "ds_read_b64_tr_b16 a[168:169], v211 offset:512\n\t" \
  "v_exp_f32 v105, v105\n\t" \
  "v_mfma_f32_16x16x32_bf16 v[72:75], a[232:235], v[88:91], v[72:75]\n\t" \
  "v_max3_f32 v203, v67, v68, v69\n\t" \
  "v_max3_f32 v204, v204, v70, v71\n\t" \
  "v_mfma_f32_16x16x32_bf16 v[72:75], a[236:239], v[92:95], v[72:75]\n\t" \
  "ds_read_b64_tr_b16 a[170:171], v211 offset:4608\n\t" \
  "v_exp_f32 v106, v106\n\t" \
  "v_mfma_f32_16x16x32_bf16 v[76:79], a[252:255], v[92:95], v[184:187]\n\t" \
  "ds_read_b64_tr_b16 a[172:173], v211 offset:8704\n\t" \
  "v_exp_f32 v107, v107\n\t" \
  "v_mfma_f32_16x16x32_bf16 v[76:79], a[248:251], v[88:91], v[76:79]\n\t" \
  "ds_read_b64_tr_b16 a[174:175], v211 offset:12800\n\t" \
  "v_cvt_pk_bf16_f32 v155, v102, v103\n\t" \
  "v_mfma_f32_16x16x32_bf16 v[76:79], a[244:247], v[84:87], v[76:79]\n\t" \
  "v_max3_f32 v203, v203, v72, v73\n\t" \
  "v_max3_f32 v204, v204, v74, v75\n\t" \
  "v_mfma_f32_16x16x32_bf16 v[76:79], a[240:243], v[80:83], v[76:79]\n\t" \
  "ds_read_b64_tr_b16 a[176:177], v210 offset:768\n\t" \
  "v_exp_f32 v108, v108\n\t" \
  "v_mfma_f32_16x16x32_bf16 v[96:99], a[192:195], v[112:115], v[188:191]\n\t" \
  "ds_read_b64_tr_b16 a[178:179], v210 offset:4864\n\t" \
  "v_exp_f32 v109, v109\n\t" \
  "v_mfma_f32_16x16x32_bf16 v[96:99], a[196:199], v[116:119], v[96:99]\n\t" \
  "ds_read_b64_tr_b16 a[180:181], v210 offset:8960\n\t" \
  "v_cvt_pk_bf16_f32 v156, v104, v105\n\t" \
  "v_mfma_f32_16x16x32_bf16 v[96:99], a[200:203], v[120:123], v[96:99]\n\t" \
  "v_max3_f32 v203, v203, v76, v77\n\t" \
  "v_max3_f32 v204, v204, v78, v79\n\t" \
  "v_mfma_f32_16x16x32_bf16 v[96:99], a[204:207], v[124:127], v[96:99]\n\t" \
  "v_max_f32 v204, v204, v203\n\t" \
  "v_cmp_lt_f32 vcc, 0x42800000, v204\n\t" \
  "v_mfma_f32_16x16x32_bf16 v[100:103], a[220:223], v[124:127], v[188:191]\n\t" \
  "ds_read_b64_tr_b16 a[182:183], v210 offset:13056\n\t" \
  "v_exp_f32 v110, v110\n\t" \
  "v_mfma_f32_16x16x32_bf16 v[100:103], a[216:219], v[120:123], v[100:103]\n\t" \
  "s_cbranch_vccnz .Lsf_c42\n\t" \
  ".Lsf_r42:\n\t" \
  "v_mfma_f32_16x16x32_bf16 v[100:103], a[212:215], v[116:119], v[100:103]\n\t" \
  "v_max3_f32 v204, v96, v97, v98\n\t" \
  "v_exp_f32 v111, v111\n\t" \
  "v_mfma_f32_16x16x32_bf16 v[100:103], a[208:211], v[112:115], v[100:103]\n\t" \
  "ds_read_b64_tr_b16 a[184:185], v211 offset:768\n\t" \
  "v_cvt_pk_bf16_f32 v157, v106, v107\n\t" \
  "v_mfma_f32_16x16x32_bf16 v[104:107], a[224:227], v[112:115], v[188:191]\n\t" \
  "ds_read_b64_tr_b16 a[186:187], v211 offset:4864\n\t" \
  "v_cvt_pk_bf16_f32 v158, v108, v109\n\t" \
  "v_mfma_f32_16x16x32_bf16 v[104:107], a[228:231], v[116:119], v[104:107]\n\t" \
  "ds_read_b64_tr_b16 a[188:189], v211 offset:8960\n\t" \
  "v_cvt_pk_bf16_f32 v159, v110, v111\n\t" \
  "v_mfma_f32_16x16x32_bf16 v[104:107], a[232:235], v[120:123], v[104:107]\n\t" \
  "v_max3_f32 v203, v99, v100, v101\n\t" \
  "v_max3_f32 v204, v204, v102, v103\n\t" \
  "v_mfma_f32_16x16x32_bf16 v[104:107], a[236:239], v[124:127], v[104:107]\n\t" \
  "ds_read_b64_tr_b16 a[190:191], v211 offset:13056\n\t" \
  "s_nop 0\n\t" \
  "v_mfma_f32_16x16x32_bf16 v[108:111], a[252:255], v[124:127], v[188:191]\n\t" \
  "v_exp_f32 v0, v0\n\t" \
  "v_mfma_f32_16x16x32_bf16 v[108:111], a[248:251], v[120:123], v[108:111]\n\t" \
  "v_exp_f32 v1, v1\n\t" \
  "v_mfma_f32_16x16x32_bf16 v[108:111], a[244:247], v[116:119], v[108:111]\n\t" \
  "s_waitcnt vmcnt(0) lgkmcnt(0)\n\t" \
  "v_max3_f32 v203, v203, v104, v105\n\t" \
  "v_max3_f32 v204, v204, v106, v107\n\t" \
  "v_mfma_f32_16x16x32_bf16 v[108:111], a[240:243], v[112:115], v[108:111]\n\t" \
  "s_barrier\n\t" \
  "v_mfma_f32_16x16x32_bf16 a[0:3], a[128:131], v[128:131], a[0:3]\n\t" \
  "v_exp_f32 v2, v2\n\t" \
  "v_mfma_f32_16x16x32_bf16 a[0:3], a[132:135], v[132:135], a[0:3]\n\t" \
  "v_exp_f32 v3, v3\n\t" \
  "v_mfma_f32_16x16x32_bf16 a[4:7], a[136:139], v[128:131], a[4:7]\n\t" \
  "v_exp_f32 v4, v4\n\t" \
  "v_mfma_f32_16x16x32_bf16 a[4:7], a[140:143], v[132:135], a[4:7]\n\t" \
  "v_max3_f32 v203, v203, v108, v109\n\t" \
  "v_max3_f32 v204, v204, v110, v111\n\t" \
  "v_mfma_f32_16x16x32_bf16 a[8:11], a[144:147], v[128:131], a[8:11]\n\t" \
  "v_max_f32 v204, v204, v203\n\t" \
  "v_cmp_lt_f32 vcc, 0x42800000, v204\n\t" \
  "v_mfma_f32_16x16x32_bf16 a[8:11], a[148:151], v[132:135], a[8:11]\n\t" \
  "v_exp_f32 v5, v5\n\t" \
  "v_mfma_f32_16x16x32_bf16 a[12:15], a[152:155], v[128:131], a[12:15]\n\t" \
  "s_cbranch_vccnz .Lsf_c43\n\t" \
  ".Lsf_r43:\n\t" \
  "v_mfma_f32_16x16x32_bf16 a[12:15], a[156:159], v[132:135], a[12:15]\n\t" \
  "s_add_u32 m0, s38, 49152\n\t" \
  "v_exp_f32 v6, v6\n\t" \
  "v_mfma_f32_16x16x32_bf16 a[16:19], a[160:163], v[128:131], a[16:19]\n\t" \
  "s_add_u32 s96, s38, 98304\n\t" \
  "v_exp_f32 v7, v7\n\t" \
  "v_mfma_f32_16x16x32_bf16 a[16:19], a[164:167], v[132:135], a[16:19]\n\t" \
  "v_exp_f32 v8, v8\n\t" \
  "v_mfma_f32_16x16x32_bf16 a[20:23], a[168:171], v[128:131], a[20:23]\n\t" \
  "buffer_load_dwordx4 v224, s[60:63], s96 offen offset:0 lds\n\t" \
  "v_mfma_f32_16x16x32_bf16 a[20:23], a[172:175], v[132:135], a[20:23]\n\t" \
  "v_exp_f32 v9, v9\n\t" \
  "v_mfma_f32_16x16x32_bf16 a[24:27], a[176:179], v[128:131], a[24:27]\n\t" \
  "v_exp_f32 v10, v10\n\t" \
  "v_mfma_f32_16x16x32_bf16 a[24:27], a[180:183], v[132:135], a[24:27]\n\t" \
  "v_exp_f32 v11, v11\n\t" \
  "v_mfma_f32_16x16x32_bf16 a[28:31], a[184:187], v[128:131], a[28:31]\n\t" \
  "v_exp_f32 v12, v12\n\t" \
  "v_mfma_f32_16x16x32_bf16 a[28:31], a[188:191], v[132:135], a[28:31]\n\t" \
  "v_exp_f32 v13, v13\n\t" \
  "v_smfmac_f32_16x16x64_bf16 v[160:163], v[216:219], v[128:135], v208\n\t" \
  "ds_read_b128 a[192:195], v220 offset:16384\n\t" \
  "v_cvt_pk_bf16_f32 v128, v0, v1\n\t" \
  "v_mfma_f32_16x16x32_bf16 a[32:35], a[128:131], v[136:139], a[32:35]\n\t" \
  "buffer_load_dwordx4 v225, s[60:63], s96 offen offset:1024 lds\n\t" \
  "v_mfma_f32_16x16x32_bf16 a[32:35], a[132:135], v[140:143], a[32:35]\n\t" \
  "v_exp_f32 v14, v14\n\t" \
  "v_mfma_f32_16x16x32_bf16 a[36:39], a[136:139], v[136:139], a[36:39]\n\t" \
  "v_exp_f32 v15, v15\n\t" \
  "v_mfma_f32_16x16x32_bf16 a[36:39], a[140:143], v[140:143], a[36:39]\n\t" \
  "ds_read_b128 a[196:199], v221 offset:16384\n\t" \
  "v_cvt_pk_bf16_f32 v129, v2, v3\n\t" \
  "v_mfma_f32_16x16x32_bf16 a[40:43], a[144:147], v[136:139], a[40:43]\n\t" \
  "v_exp_f32 v32, v32\n\t" \
  "v_mfma_f32_16x16x32_bf16 a[40:43], a[148:151], v[140:143], a[40:43]\n\t" \
  "v_exp_f32 v33, v33\n\t" \
  "v_mfma_f32_16x16x32_bf16 a[44:47], a[152:155], v[136:139], a[44:47]\n\t" \
  "buffer_load_dwordx4 v226, s[60:63], s96 offen offset:2048 lds\n\t" \
  "v_mfma_f32_16x16x32_bf16 a[44:47], a[156:159], v[140:143], a[44:47]\n\t" \
  "ds_read_b128 a[200:203], v222 offset:16384\n\t" \
  "v_cvt_pk_bf16_f32 v130, v4, v5\n\t" \
  "v_mfma_f32_16x16x32_bf16 a[48:51], a[160:163], v[136:139], a[48:51]\n\t" \
  "v_exp_f32 v34, v34\n\t" \
  "v_mfma_f32_16x16x32_bf16 a[48:51], a[164:167], v[140:143], a[48:51]\n\t" \
  "ds_read_b128 a[204:207], v223 offset:16384\n\t" \
  "v_cvt_pk_bf16_f32 v131, v6, v7\n\t" \
  "v_mfma_f32_16x16x32_bf16 a[52:55], a[168:171], v[136:139], a[52:55]\n\t" \
  "v_exp_f32 v35, v35\n\t" \
  "v_mfma_f32_16x16x32_bf16 a[52:55], a[172:175], v[140:143], a[52:55]\n\t" \
  "ds_read_b128 a[208:211], v220 offset:20480\n\t" \
  "v_cvt_pk_bf16_f32 v132, v8, v9\n\t" \
  "v_mfma_f32_16x16x32_bf16 a[56:59], a[176:179], v[136:139], a[56:59]\n\t" \
  "buffer_load_dwordx4 v227, s[60:63], s96 offen offset:3072 lds\n\t" \
  "v_mfma_f32_16x16x32_bf16 a[56:59], a[180:183], v[140:143], a[56:59]\n\t" \
  "v_exp_f32 v36, v36\n\t" \
  "v_mfma_f32_16x16x32_bf16 a[60:63], a[184:187], v[136:139], a[60:63]\n\t" \
  "s_add_u32 m0, s38, 147456\n\t" \
  "ds_read_b128 a[212:215], v221 offset:20480\n\t" \
  "v_cvt_pk_bf16_f32 v133, v10, v11\n\t" \
  "v_mfma_f32_16x16x32_bf16 a[60:63], a[188:191], v[140:143], a[60:63]\n\t" \
  "s_add_u32 s96, s38, 81920\n\t" \
  "v_exp_f32 v37, v37\n\t" \
  "v_smfmac_f32_16x16x64_bf16 v[164:167], v[216:219], v[136:143], v208\n\t" \
  "ds_read_b128 a[216:219], v222 offset:20480\n\t" \
  "v_cvt_pk_bf16_f32 v134, v12, v13\n\t" \
  "v_mfma_f32_16x16x32_bf16 a[64:67], a[128:131], v[144:147], a[64:67]\n\t" \
  "v_exp_f32 v38, v38\n\t" \
  "v_mfma_f32_16x16x32_bf16 a[64:67], a[132:135], v[148:151], a[64:67]\n\t" \
  "buffer_load_dwordx4 v230, s[64:67], s96 offen offset:0 lds\n\t" \
  "v_mfma_f32_16x16x32_bf16 a[68:71], a[136:139], v[144:147], a[68:71]\n\t" \
  "ds_read_b128 a[220:223], v223 offset:20480\n\t" \
  "v_cvt_pk_bf16_f32 v135, v14, v15\n\t" \
  "v_mfma_f32_16x16x32_bf16 a[68:71], a[140:143], v[148:151], a[68:71]\n\t" \
  "v_exp_f32 v39, v39\n\t" \
  "v_mfma_f32_16x16x32_bf16 a[72:75], a[144:147], v[144:147], a[72:75]\n\t" \
  "v_exp_f32 v40, v40\n\t" \
  "v_mfma_f32_16x16x32_bf16 a[72:75], a[148:151], v[148:151], a[72:75]\n\t" \
  "ds_read_b128 a[224:227], v220 offset:24576\n\t" \
  "v_cvt_pk_bf16_f32 v136, v32, v33\n\t" \
  "v_mfma_f32_16x16x32_bf16 a[76:79], a[152:155], v[144:147], a[76:79]\n\t" \
  "ds_read_b128 a[228:231], v221 offset:24576\n\t" \
  "v_cvt_pk_bf16_f32 v137, v34, v35\n\t" \
  "v_mfma_f32_16x16x32_bf16 a[76:79], a[156:159], v[148:151], a[76:79]\n\t" \
  "v_exp_f32 v41, v41\n\t" \
  "v_mfma_f32_16x16x32_bf16 a[80:83], a[160:163], v[144:147], a[80:83]\n\t" \
  "buffer_load_dwordx4 v231, s[64:67], s96 offen offset:1024 lds\n\t" \
  "v_mfma_f32_16x16x32_bf16 a[80:83], a[164:167], v[148:151], a[80:83]\n\t" \
  "v_exp_f32 v42, v42\n\t" \
  "v_mfma_f32_16x16x32_bf16 a[84:87], a[168:171], v[144:147], a[84:87]\n\t" \
  "ds_read_b128 a[232:235], v222 offset:24576\n\t" \
  "v_cvt_pk_bf16_f32 v138, v36, v37\n\t" \
  "v_mfma_f32_16x16x32_bf16 a[84:87], a[172:175], v[148:151], a[84:87]\n\t" \
  "v_exp_f32 v43, v43\n\t" \
  "v_mfma_f32_16x16x32_bf16 a[88:91], a[176:179], v[144:147], a[88:91]\n\t" \
  "ds_read_b128 a[236:239], v223 offset:24576\n\t" \
  "v_cvt_pk_bf16_f32 v139, v38, v39\n\t" \
  "v_mfma_f32_16x16x32_bf16 a[88:91], a[180:183], v[148:151], a[88:91]\n\t" \
  "v_exp_f32 v44, v44\n\t" \
  "v_mfma_f32_16x16x32_bf16 a[92:95], a[184:187], v[144:147], a[92:95]\n\t" \
  "buffer_load_dwordx4 v230, s[64:67], s96 offen offset:2048 lds\n\t" \
  "v_mfma_f32_16x16x32_bf16 a[92:95], a[188:191], v[148:151], a[92:95]\n\t" \
  "ds_read_b128 a[240:243], v220 offset:28672\n\t" \
  "v_cvt_pk_bf16_f32 v140, v40, v41\n\t" \
  "v_smfmac_f32_16x16x64_bf16 v[168:171], v[216:219], v[144:151], v208\n\t" \
  "ds_read_b128 a[244:247], v221 offset:28672\n\t" \
  "v_cvt_pk_bf16_f32 v141, v42, v43\n\t" \
  "v_mfma_f32_16x16x32_bf16 a[96:99], a[128:131], v[152:155], a[96:99]\n\t" \
  "v_exp_f32 v45, v45\n\t" \
  "v_mfma_f32_16x16x32_bf16 a[96:99], a[132:135], v[156:159], a[96:99]\n\t" \
  "v_exp_f32 v46, v46\n\t" \
  "v_mfma_f32_16x16x32_bf16 a[100:103], a[136:139], v[152:155], a[100:103]\n\t" \
  "v_exp_f32 v47, v47\n\t" \
  "v_mfma_f32_16x16x32_bf16 a[100:103], a[140:143], v[156:159], a[100:103]\n\t" \
  "buffer_load_dwordx4 v231, s[64:67], s96 offen offset:3072 lds\n\t" \
  "v_mfma_f32_16x16x32_bf16 a[104:107], a[144:147], v[152:155], a[104:107]\n\t" \
  "ds_read_b128 a[248:251], v222 offset:28672\n\t" \
  "v_cvt_pk_bf16_f32 v142, v44, v45\n\t" \
  "v_mfma_f32_16x16x32_bf16 a[104:107], a[148:151], v[156:159], a[104:107]\n\t" \
  "ds_read_b128 a[252:255], v223 offset:28672\n\t" \
  "v_cvt_pk_bf16_f32 v143, v46, v47\n\t" \
  "v_mfma_f32_16x16x32_bf16 a[108:111], a[152:155], v[152:155], a[108:111]\n\t" \
  "v_exp_f32 v64, v64\n\t" \
  "v_mfma_f32_16x16x32_bf16 a[108:111], a[156:159], v[156:159], a[108:111]\n\t" \
  "v_exp_f32 v65, v65\n\t" \
  "v_mfma_f32_16x16x32_bf16 a[112:115], a[160:163], v[152:155], a[112:115]\n\t" \
  "v_exp_f32 v66, v66\n\t" \
  "v_mfma_f32_16x16x32_bf16 a[112:115], a[164:167], v[156:159], a[112:115]\n\t" \
  "v_exp_f32 v67, v67\n\t" \
  "v_mfma_f32_16x16x32_bf16 a[116:119], a[168:171], v[152:155], a[116:119]\n\t" \
  "v_exp_f32 v68, v68\n\t" \
  "v_mfma_f32_16x16x32_bf16 a[116:119], a[172:175], v[156:159], a[116:119]\n\t" \
  "v_exp_f32 v69, v69\n\t" \
  "v_mfma_f32_16x16x32_bf16 a[120:123], a[176:179], v[152:155], a[120:123]\n\t" \
  "v_exp_f32 v70, v70\n\t" \
  "v_mfma_f32_16x16x32_bf16 a[120:123], a[180:183], v[156:159], a[120:123]\n\t" \
  "v_exp_f32 v71, v71\n\t" \
  "v_mfma_f32_16x16x32_bf16 a[124:127], a[184:187], v[152:155], a[124:127]\n\t" \
  "v_exp_f32 v72, v72\n\t" \
  "v_mfma_f32_16x16x32_bf16 a[124:127], a[188:191], v[156:159], a[124:127]\n\t" \
  "v_exp_f32 v73, v73\n\t" \
  "v_smfmac_f32_16x16x64_bf16 v[172:175], v[216:219], v[152:159], v208\n\t" \
  "s_waitcnt lgkmcnt(12)\n\t" \
  "s_cmp_lg_u32 s99, 0\n\t" \
  "s_cbranch_scc1 .Lsf_p4\n\t" \
  ".Lsf_q4:\n\t" \
  "v_mfma_f32_16x16x32_bf16 v[0:3], a[192:195], v[16:19], v[176:179]\n\t" \
  "ds_read_b64_tr_b16 a[128:129], v228 offset:0\n\t" \
  "v_cvt_pk_bf16_f32 v144, v64, v65\n\t" \
  "v_mfma_f32_16x16x32_bf16 v[0:3], a[196:199], v[20:23], v[0:3]\n\t" \
  "v_exp_f32 v74, v74\n\t" \
  "v_mfma_f32_16x16x32_bf16 v[0:3], a[200:203], v[24:27], v[0:3]\n\t" \
  "ds_read_b64_tr_b16 a[130:131], v228 offset:4096\n\t" \
  "v_cvt_pk_bf16_f32 v145, v66, v67\n\t" \
  "v_mfma_f32_16x16x32_bf16 v[0:3], a[204:207], v[28:31], v[0:3]\n\t" \
  "s_waitcnt lgkmcnt(10)\n\t" \
  "v_exp_f32 v75, v75\n\t" \
  "v_mfma_f32_16x16x32_bf16 v[4:7], a[220:223], v[28:31], v[176:179]\n\t" \
  "ds_read_b64_tr_b16 a[132:133], v228 offset:8192\n\t" \
  "v_cvt_pk_bf16_f32 v146, v68, v69\n\t" \
  "v_mfma_f32_16x16x32_bf16 v[4:7], a[216:219], v[24:27], v[4:7]\n\t" \
  "v_exp_f32 v76, v76\n\t" \
  "v_mfma_f32_16x16x32_bf16 v[4:7], a[212:215], v[20:23], v[4:7]\n\t" \
  "ds_read_b64_tr_b16 a[134:135], v228 offset:12288\n\t" \
  "v_max3_f32 v204, v0, v1, v2\n\t" \
  "v_mfma_f32_16x16x32_bf16 v[4:7], a[208:211], v[16:19], v[4:7]\n\t" \
  "s_waitcnt lgkmcnt(8)\n\t" \
  "ds_read_b64_tr_b16 a[136:137], v229 offset:0\n\t" \
  "v_cvt_pk_bf16_f32 v147, v70, v71\n\t" \
  "v_mfma_f32_16x16x32_bf16 v[8:11], a[224:227], v[16:19], v[176:179]\n\t" \
  "v_exp_f32 v77, v77\n\t" \
  "v_mfma_f32_16x16x32_bf16 v[8:11], a[228:231], v[20:23], v[8:11]\n\t" \
  "ds_read_b64_tr_b16 a[138:139], v229 offset:4096\n\t" \
  "v_cvt_pk_bf16_f32 v148, v72, v73\n\t" \
  "v_mfma_f32_16x16x32_bf16 v[8:11], a[232:235], v[24:27], v[8:11]\n\t" \
  "v_max3_f32 v203, v3, v4, v5\n\t" \
  "v_max3_f32 v204, v204, v6, v7\n\t" \
  "v_mfma_f32_16x16x32_bf16 v[8:11], a[236:239], v[28:31], v[8:11]\n\t" \
  "s_waitcnt lgkmcnt(6)\n\t" \
  "ds_read_b64_tr_b16 a[140:141], v229 offset:8192\n\t" \
  "v_cvt_pk_bf16_f32 v149, v74, v75\n\t" \
  "v_mfma_f32_16x16x32_bf16 v[12:15], a[252:255], v[28:31], v[176:179]\n\t" \
  "v_exp_f32 v78, v78\n\t" \
  "v_mfma_f32_16x16x32_bf16 v[12:15], a[248:251], v[24:27], v[12:15]\n\t" \
  "ds_read_b64_tr_b16 a[142:143], v229 offset:12288\n\t" \
  "v_cvt_pk_bf16_f32 v150, v76, v77\n\t" \
  "v_mfma_f32_16x16x32_bf16 v[12:15], a[244:247], v[20:23], v[12:15]\n\t" \
  "v_max3_f32 v203, v203, v8, v9\n\t" \
  "v_max3_f32 v204, v204, v10, v11\n\t" \
  "v_mfma_f32_16x16x32_bf16 v[12:15], a[240:243], v[16:19], v[12:15]\n\t" \
  "v_exp_f32 v79, v79\n\t" \
  "v_mfma_f32_16x16x32_bf16 v[32:35], a[192:195], v[48:51], v[180:183]\n\t" \
  "ds_read_b64_tr_b16 a[144:145], v228 offset:256\n\t" \
  "v_cvt_pk_bf16_f32 v151, v78, v79\n\t" \
  "v_mfma_f32_16x16x32_bf16 v[32:35], a[196:199], v[52:55], v[32:35]\n\t" \
  "v_exp_f32 v96, v96\n\t" \
  "v_mfma_f32_16x16x32_bf16 v[32:35], a[200:203], v[56:59], v[32:35]\n\t" \
  "ds_read_b64_tr_b16 a[146:147], v228 offset:4352\n\t" \
  "v_max3_f32 v203, v203, v12, v13\n\t" \
  "v_mfma_f32_16x16x32_bf16 v[32:35], a[204:207], v[60:63], v[32:35]\n\t" \
  "v_max3_f32 v204, v204, v14, v15\n\t" \
  "v_max_f32 v204, v204, v203\n\t" \
  "v_mfma_f32_16x16x32_bf16 v[36:39], a[220:223], v[60:63], v[180:183]\n\t" \
  "ds_read_b64_tr_b16 a[148:149], v228 offset:8448\n\t" \
  "v_cmp_lt_f32 vcc, 0x42800000, v204\n\t" \
  "v_mfma_f32_16x16x32_bf16 v[36:39], a[216:219], v[56:59], v[36:39]\n\t" \
  "v_exp_f32 v97, v97\n\t" \
  "v_mfma_f32_16x16x32_bf16 v[36:39], a[212:215], v[52:55], v[36:39]\n\t" \
  "s_cbranch_vccnz .Lsf_c50\n\t" \
  ".Lsf_r50:\n\t" \
  "v_mfma_f32_16x16x32_bf16 v[36:39], a[208:211], v[48:51], v[36:39]\n\t" \
  "ds_read_b64_tr_b16 a[150:151], v228 offset:12544\n\t" \
  "v_max3_f32 v204, v32, v33, v34\n\t" \
  "v_mfma_f32_16x16x32_bf16 v[40:43], a[224:227], v[48:51], v[180:183]\n\t" \
  "ds_read_b64_tr_b16 a[152:153], v229 offset:256\n\t" \
  "v_cvt_pk_bf16_f32 v152, v96, v97\n\t" \
  "v_mfma_f32_16x16x32_bf16 v[40:43], a[228:231], v[52:55], v[40:43]\n\t" \
  "v_exp_f32 v98, v98\n\t" \
  "v_mfma_f32_16x16x32_bf16 v[40:43], a[232:235], v[56:59], v[40:43]\n\t" \
  "v_max3_f32 v203, v35, v36, v37\n\t" \
  "v_max3_f32 v204, v204, v38, v39\n\t" \
  "v_mfma_f32_16x16x32_bf16 v[40:43], a[236:239], v[60:63], v[40:43]\n\t" \
  "v_exp_f32 v99, v99\n\t" \
  "v_mfma_f32_16x16x32_bf16 v[44:47], a[252:255], v[60:63], v[180:183]\n\t" \
  "ds_read_b64_tr_b16 a[154:155], v229 offset:4352\n\t" \
  "v_cvt_pk_bf16_f32 v153, v98, v99\n\t" \
  "v_mfma_f32_16x16x32_bf16 v[44:47], a[248:251], v[56:59], v[44:47]\n\t" \
  "v_exp_f32 v100, v100\n\t" \
  "v_mfma_f32_16x16x32_bf16 v[44:47], a[244:247], v[52:55], v[44:47]\n\t" \
  "ds_read_b64_tr_b16 a[156:157], v229 offset:8448\n\t" \
  "v_max3_f32 v203, v203, v40, v41\n\t" \
  "v_mfma_f32_16x16x32_bf16 v[44:47], a[240:243], v[48:51], v[44:47]\n\t" \
  "ds_read_b64_tr_b16 a[158:159], v229 offset:12544\n\t" \
  "v_max3_f32 v204, v204, v42, v43\n\t" \
  "v_mfma_f32_16x16x32_bf16 v[64:67], a[192:195], v[80:83], v[184:187]\n\t" \
  "v_exp_f32 v101, v101\n\t" \
  "v_mfma_f32_16x16x32_bf16 v[64:67], a[196:199], v[84:87], v[64:67]\n\t" \
  "ds_read_b64_tr_b16 a[160:161], v228 offset:512\n\t" \
  "v_cvt_pk_bf16_f32 v154, v100, v101\n\t" \
  "v_mfma_f32_16x16x32_bf16 v[64:67], a[200:203], v[88:91], v[64:67]\n\t" \
  "v_max3_f32 v203, v203, v44, v45\n\t" \
  "v_max3_f32 v204, v204, v46, v47\n\t" \
  "v_mfma_f32_16x16x32_bf16 v[64:67], a[204:207], v[92:95], v[64:67]\n\t" \
  "v_max_f32 v204, v204, v203\n\t" \
  "v_cmp_lt_f32 vcc, 0x42800000, v204\n\t" \
  "v_mfma_f32_16x16x32_bf16 v[68:71], a[220:223], v[92:95], v[184:187]\n\t" \
  "v_exp_f32 v102, v102\n\t" \
  "v_mfma_f32_16x16x32_bf16 v[68:71], a[216:219], v[88:91], v[68:71]\n\t" \
  "s_cbranch_vccnz .Lsf_c51\n\t" \
  ".Lsf_r51:\n\t" \
  "v_mfma_f32_16x16x32_bf16 v[68:71], a[212:215], v[84:87], v[68:71]\n\t" \
  "ds_read_b64_tr_b16 a[162:163], v228 offset:4608\n\t" \
  "v_max3_f32 v204, v64, v65, v66\n\t" \
  "v_mfma_f32_16x16x32_bf16 v[68:71], a[208:211], v[80:83], v[68:71]\n\t" \
  "ds_read_b64_tr_b16 a[164:165], v228 offset:8704\n\t" \
  "v_exp_f32 v103, v103\n\t" \
  "v_mfma_f32_16x16x32_bf16 v[72:75], a[224:227], v[80:83], v[184:187]\n\t" \
  "ds_read_b64_tr_b16 a[166:167], v228 offset:12800\n\t" \
  "v_exp_f32 v104, v104\n\t" \
  "v_mfma_f32_16x16x32_bf16 v[72:75], a[228:231], v[84:87], v[72:75]\n\t" \
  "ds_read_b64_tr_b16 a[168:169], v229 offset:512\n\t" \
  "v_exp_f32 v105, v105\n\t" \
  "v_mfma_f32_16x16x32_bf16 v[72:75], a[232:235], v[88:91], v[72:75]\n\t" \
  "v_max3_f32 v203, v67, v68, v69\n\t" \
  "v_max3_f32 v204, v204, v70, v71\n\t" \
  "v_mfma_f32_16x16x32_bf16 v[72:75], a[236:239], v[92:95], v[72:75]\n\t" \
  "ds_read_b64_tr_b16 a[170:171], v229 offset:4608\n\t" \
  "v_exp_f32 v106, v106\n\t" \
  "v_mfma_f32_16x16x32_bf16 v[76:79], a[252:255], v[92:95], v[184:187]\n\t" \
  "ds_read_b64_tr_b16 a[172:173], v229 offset:8704\n\t" \
  "v_exp_f32 v107, v107\n\t" \
  "v_mfma_f32_16x16x32_bf16 v[76:79], a[248:251], v[88:91], v[76:79]\n\t" \
  "ds_read_b64_tr_b16 a[174:175], v229 offset:12800\n\t" \
  "v_cvt_pk_bf16_f32 v155, v102, v103\n\t" \
  "v_mfma_f32_16x16x32_bf16 v[76:79], a[244:247], v[84:87], v[76:79]\n\t" \
  "v_max3_f32 v203, v203, v72, v73\n\t" \
  "v_max3_f32 v204, v204, v74, v75\n\t" \
  "v_mfma_f32_16x16x32_bf16 v[76:79], a[240:243], v[80:83], v[76:79]\n\t" \
  "ds_read_b64_tr_b16 a[176:177], v228 offset:768\n\t" \
  "v_exp_f32 v108, v108\n\t" \
  "v_mfma_f32_16x16x32_bf16 v[96:99], a[192:195], v[112:115], v[188:191]\n\t" \
  "ds_read_b64_tr_b16 a[178:179], v228 offset:4864\n\t" \
  "v_exp_f32 v109, v109\n\t" \
  "v_mfma_f32_16x16x32_bf16 v[96:99], a[196:199], v[116:119], v[96:99]\n\t" \
  "ds_read_b64_tr_b16 a[180:181], v228 offset:8960\n\t" \
  "v_cvt_pk_bf16_f32 v156, v104, v105\n\t" \
  "v_mfma_f32_16x16x32_bf16 v[96:99], a[200:203], v[120:123], v[96:99]\n\t" \
  "v_max3_f32 v203, v203, v76, v77\n\t" \
  "v_max3_f32 v204, v204, v78, v79\n\t" \
  "v_mfma_f32_16x16x32_bf16 v[96:99], a[204:207], v[124:127], v[96:99]\n\t" \
  "v_max_f32 v204, v204, v203\n\t" \
  "v_cmp_lt_f32 vcc, 0x42800000, v204\n\t" \
  "v_mfma_f32_16x16x32_bf16 v[100:103], a[220:223], v[124:127], v[188:191]\n\t" \
  "ds_read_b64_tr_b16 a[182:183], v228 offset:13056\n\t" \
  "v_exp_f32 v110, v110\n\t" \
  "v_mfma_f32_16x16x32_bf16 v[100:103], a[216:219], v[120:123], v[100:103]\n\t" \
  "s_cbranch_vccnz .Lsf_c52\n\t" \
  ".Lsf_r52:\n\t" \
  "v_mfma_f32_16x16x32_bf16 v[100:103], a[212:215], v[116:119], v[100:103]\n\t" \
  "v_max3_f32 v204, v96, v97, v98\n\t" \
  "v_exp_f32 v111, v111\n\t" \
  "v_mfma_f32_16x16x32_bf16 v[100:103], a[208:211], v[112:115], v[100:103]\n\t" \
  "ds_read_b64_tr_b16 a[184:185], v229 offset:768\n\t" \
  "v_cvt_pk_bf16_f32 v157, v106, v107\n\t" \
  "v_mfma_f32_16x16x32_bf16 v[104:107], a[224:227], v[112:115], v[188:191]\n\t" \
  "ds_read_b64_tr_b16 a[186:187], v229 offset:4864\n\t" \
  "v_cvt_pk_bf16_f32 v158, v108, v109\n\t" \
  "v_mfma_f32_16x16x32_bf16 v[104:107], a[228:231], v[116:119], v[104:107]\n\t" \
  "ds_read_b64_tr_b16 a[188:189], v229 offset:8960\n\t" \
  "v_cvt_pk_bf16_f32 v159, v110, v111\n\t" \
  "v_mfma_f32_16x16x32_bf16 v[104:107], a[232:235], v[120:123], v[104:107]\n\t" \
  "v_max3_f32 v203, v99, v100, v101\n\t" \
  "v_max3_f32 v204, v204, v102, v103\n\t" \
  "v_mfma_f32_16x16x32_bf16 v[104:107], a[236:239], v[124:127], v[104:107]\n\t" \
  "ds_read_b64_tr_b16 a[190:191], v229 offset:13056\n\t" \
  "s_nop 0\n\t" \
  "v_mfma_f32_16x16x32_bf16 v[108:111], a[252:255], v[124:127], v[188:191]\n\t" \
  "v_exp_f32 v0, v0\n\t" \
  "v_mfma_f32_16x16x32_bf16 v[108:111], a[248:251], v[120:123], v[108:111]\n\t" \
  "v_exp_f32 v1, v1\n\t" \
  "v_mfma_f32_16x16x32_bf16 v[108:111], a[244:247], v[116:119], v[108:111]\n\t" \
  "s_waitcnt vmcnt(0) lgkmcnt(0)\n\t" \
  "v_max3_f32 v203, v203, v104, v105\n\t" \
  "v_max3_f32 v204, v204, v106, v107\n\t" \
  "v_mfma_f32_16x16x32_bf16 v[108:111], a[240:243], v[112:115], v[108:111]\n\t" \
  "s_barrier\n\t" \
  "v_mfma_f32_16x16x32_bf16 a[0:3], a[128:131], v[128:131], a[0:3]\n\t" \
  "v_exp_f32 v2, v2\n\t" \
  "v_mfma_f32_16x16x32_bf16 a[0:3], a[132:135], v[132:135], a[0:3]\n\t" \
  "v_exp_f32 v3, v3\n\t" \
  "v_mfma_f32_16x16x32_bf16 a[4:7], a[136:139], v[128:131], a[4:7]\n\t" \
  "v_exp_f32 v4, v4\n\t" \
  "v_mfma_f32_16x16x32_bf16 a[4:7], a[140:143], v[132:135], a[4:7]\n\t" \
  "v_max3_f32 v203, v203, v108, v109\n\t" \
  "v_max3_f32 v204, v204, v110, v111\n\t" \
  "v_mfma_f32_16x16x32_bf16 a[8:11], a[144:147], v[128:131], a[8:11]\n\t" \
  "v_max_f32 v204, v204, v203\n\t" \
  "v_cmp_lt_f32 vcc, 0x42800000, v204\n\t" \
  "v_mfma_f32_16x16x32_bf16 a[8:11], a[148:151], v[132:135], a[8:11]\n\t" \
  "v_exp_f32 v5, v5\n\t" \
  "v_mfma_f32_16x16x32_bf16 a[12:15], a[152:155], v[128:131], a[12:15]\n\t" \
  "s_cbranch_vccnz .Lsf_c53\n\t" \
  ".Lsf_r53:\n\t" \
  "v_mfma_f32_16x16x32_bf16 a[12:15], a[156:159], v[132:135], a[12:15]\n\t" \
  "s_add_u32 m0, s38, 65536\n\t" \
  "v_exp_f32 v6, v6\n\t" \
  "v_mfma_f32_16x16x32_bf16 a[16:19], a[160:163], v[128:131], a[16:19]\n\t" \
  "s_add_u32 s96, s38, 114688\n\t" \
  "v_exp_f32 v7, v7\n\t" \
  "v_mfma_f32_16x16x32_bf16 a[16:19], a[164:167], v[132:135], a[16:19]\n\t" \
  "v_exp_f32 v8, v8\n\t" \
  "v_mfma_f32_16x16x32_bf16 a[20:23], a[168:171], v[128:131], a[20:23]\n\t" \
  "buffer_load_dwordx4 v224, s[60:63], s96 offen offset:0 lds\n\t" \
  "v_mfma_f32_16x16x32_bf16 a[20:23], a[172:175], v[132:135], a[20:23]\n\t" \
  "v_exp_f32 v9, v9\n\t" \
  "v_mfma_f32_16x16x32_bf16 a[24:27], a[176:179], v[128:131], a[24:27]\n\t" \
  "v_exp_f32 v10, v10\n\t" \
  "v_mfma_f32_16x16x32_bf16 a[24:27], a[180:183], v[132:135], a[24:27]\n\t" \
  "v_exp_f32 v11, v11\n\t" \
  "v_mfma_f32_16x16x32_bf16 a[28:31], a[184:187], v[128:131], a[28:31]\n\t" \
  "v_exp_f32 v12, v12\n\t" \
  "v_mfma_f32_16x16x32_bf16 a[28:31], a[188:191], v[132:135], a[28:31]\n\t" \
  "v_exp_f32 v13, v13\n\t" \
  "v_smfmac_f32_16x16x64_bf16 v[160:163], v[216:219], v[128:135], v208\n\t" \
  "ds_read_b128 a[192:195], v220 offset:0\n\t" \
  "v_cvt_pk_bf16_f32 v128, v0, v1\n\t" \
  "v_mfma_f32_16x16x32_bf16 a[32:35], a[128:131], v[136:139], a[32:35]\n\t" \
  "buffer_load_dwordx4 v225, s[60:63], s96 offen offset:1024 lds\n\t" \
  "v_mfma_f32_16x16x32_bf16 a[32:35], a[132:135], v[140:143], a[32:35]\n\t" \
  "v_exp_f32 v14, v14\n\t" \
  "v_mfma_f32_16x16x32_bf16 a[36:39], a[136:139], v[136:139], a[36:39]\n\t" \
  "ds_read_b128 a[196:199], v221 offset:0\n\t" \
  "v_cvt_pk_bf16_f32 v129, v2, v3\n\t" \
  "v_mfma_f32_16x16x32_bf16 a[36:39], a[140:143], v[140:143], a[36:39]\n\t" \
  "v_exp_f32 v15, v15\n\t" \
  "v_mfma_f32_16x16x32_bf16 a[40:43], a[144:147], v[136:139], a[40:43]\n\t" \
  "v_exp_f32 v32, v32\n\t" \
  "v_mfma_f32_16x16x32_bf16 a[40:43], a[148:151], v[140:143], a[40:43]\n\t" \
  "ds_read_b128 a[200:203], v222 offset:0\n\t" \
  "v_cvt_pk_bf16_f32 v130, v4, v5\n\t" \
  "v_mfma_f32_16x16x32_bf16 a[44:47], a[152:155], v[136:139], a[44:47]\n\t" \
  "buffer_load_dwordx4 v226, s[60:63], s96 offen offset:2048 lds\n\t" \
  "v_mfma_f32_16x16x32_bf16 a[44:47], a[156:159], v[140:143], a[44:47]\n\t" \
  "ds_read_b128 a[204:207], v223 offset:0\n\t" \
  "v_cvt_pk_bf16_f32 v131, v6, v7\n\t" \
  "v_mfma_f32_16x16x32_bf16 a[48:51], a[160:163], v[136:139], a[48:51]\n\t" \
  "v_exp_f32 v33, v33\n\t" \
  "v_mfma_f32_16x16x32_bf16 a[48:51], a[164:167], v[140:143], a[48:51]\n\t" \
  "v_exp_f32 v34, v34\n\t" \
  "v_mfma_f32_16x16x32_bf16 a[52:55], a[168:171], v[136:139], a[52:55]\n\t" \
  "ds_read_b128 a[208:211], v220 offset:4096\n\t" \
  "v_cvt_pk_bf16_f32 v132, v8, v9\n\t" \
  "v_mfma_f32_16x16x32_bf16 a[52:55], a[172:175], v[140:143], a[52:55]\n\t" \
  "v_exp_f32 v35, v35\n\t" \
  "v_mfma_f32_16x16x32_bf16 a[56:59], a[176:179], v[136:139], a[56:59]\n\t" \
  "buffer_load_dwordx4 v227, s[60:63], s96 offen offset:3072 lds\n\t" \
  "v_mfma_f32_16x16x32_bf16 a[56:59], a[180:183], v[140:143], a[56:59]\n\t" \
  "ds_read_b128 a[212:215], v221 offset:4096\n\t" \
  "v_cvt_pk_bf16_f32 v133, v10, v11\n\t" \
  "v_mfma_f32_16x16x32_bf16 a[60:63], a[184:187], v[136:139], a[60:63]\n\t" \
  "s_add_u32 m0, s38, 81920\n\t" \
  "v_exp_f32 v36, v36\n\t" \
  "v_mfma_f32_16x16x32_bf16 a[60:63], a[188:191], v[140:143], a[60:63]\n\t" \
  "s_add_u32 s96, s38, 98304\n\t" \
  "ds_read_b128 a[216:219], v222 offset:4096\n\t" \
  "v_cvt_pk_bf16_f32 v134, v12, v13\n\t" \
  "v_smfmac_f32_16x16x64_bf16 v[164:167], v[216:219], v[136:143], v208\n\t" \
  "ds_read_b128 a[220:223], v223 offset:4096\n\t" \
  "v_cvt_pk_bf16_f32 v135, v14, v15\n\t" \
  "v_mfma_f32_16x16x32_bf16 a[64:67], a[128:131], v[144:147], a[64:67]\n\t" \
  "v_exp_f32 v37, v37\n\t" \
  "v_mfma_f32_16x16x32_bf16 a[64:67], a[132:135], v[148:151], a[64:67]\n\t" \
  "buffer_load_dwordx4 v230, s[64:67], s96 offen offset:0 lds\n\t" \
  "v_mfma_f32_16x16x32_bf16 a[68:71], a[136:139], v[144:147], a[68:71]\n\t" \
  "v_exp_f32 v38, v38\n\t" \
  "v_mfma_f32_16x16x32_bf16 a[68:71], a[140:143], v[148:151], a[68:71]\n\t" \
  "ds_read_b128 a[224:227], v220 offset:8192\n\t" \
  "v_cvt_pk_bf16_f32 v136, v32, v33\n\t" \
  "v_mfma_f32_16x16x32_bf16 a[72:75], a[144:147], v[144:147], a[72:75]\n\t" \
  "v_exp_f32 v39, v39\n\t" \
  "v_mfma_f32_16x16x32_bf16 a[72:75], a[148:151], v[148:151], a[72:75]\n\t" \
  "ds_read_b128 a[228:231], v221 offset:8192\n\t" \
  "v_cvt_pk_bf16_f32 v137, v34, v35\n\t" \
  "v_mfma_f32_16x16x32_bf16 a[76:79], a[152:155], v[144:147], a[76:79]\n\t" \
  "v_exp_f32 v40, v40\n\t" \
  "v_mfma_f32_16x16x32_bf16 a[76:79], a[156:159], v[148:151], a[76:79]\n\t" \
  "v_exp_f32 v41, v41\n\t" \
  "v_mfma_f32_16x16x32_bf16 a[80:83], a[160:163], v[144:147], a[80:83]\n\t" \
  "buffer_load_dwordx4 v231, s[64:67], s96 offen offset:1024 lds\n\t" \
  "v_mfma_f32_16x16x32_bf16 a[80:83], a[164:167], v[148:151], a[80:83]\n\t" \
  "ds_read_b128 a[232:235], v222 offset:8192\n\t" \
  "v_cvt_pk_bf16_f32 v138, v36, v37\n\t" \
  "v_mfma_f32_16x16x32_bf16 a[84:87], a[168:171], v[144:147], a[84:87]\n\t" \
  "v_exp_f32 v42, v42\n\t" \
  "v_mfma_f32_16x16x32_bf16 a[84:87], a[172:175], v[148:151], a[84:87]\n\t" \
  "ds_read_b128 a[236:239], v223 offset:8192\n\t" \
  "v_cvt_pk_bf16_f32 v139, v38, v39\n\t" \
  "v_mfma_f32_16x16x32_bf16 a[88:91], a[176:179], v[144:147], a[88:91]\n\t" \
  "v_exp_f32 v43, v43\n\t" \
  "v_mfma_f32_16x16x32_bf16 a[88:91], a[180:183], v[148:151], a[88:91]\n\t" \
  "ds_read_b128 a[240:243], v220 offset:12288\n\t" \
  "v_cvt_pk_bf16_f32 v140, v40, v41\n\t" \
  "v_mfma_f32_16x16x32_bf16 a[92:95], a[184:187], v[144:147], a[92:95]\n\t" \
  "buffer_load_dwordx4 v230, s[64:67], s96 offen offset:2048 lds\n\t" \
  "v_mfma_f32_16x16x32_bf16 a[92:95], a[188:191], v[148:151], a[92:95]\n\t" \
  "v_exp_f32 v44, v44\n\t" \
  "v_smfmac_f32_16x16x64_bf16 v[168:171], v[216:219], v[144:151], v208\n\t" \
  "ds_read_b128 a[244:247], v221 offset:12288\n\t" \
  "v_cvt_pk_bf16_f32 v141, v42, v43\n\t" \
  "v_mfma_f32_16x16x32_bf16 a[96:99], a[128:131], v[152:155], a[96:99]\n\t" \
  "v_exp_f32 v45, v45\n\t" \
  "v_mfma_f32_16x16x32_bf16 a[96:99], a[132:135], v[156:159], a[96:99]\n\t" \
  "v_exp_f32 v46, v46\n\t" \
  "v_mfma_f32_16x16x32_bf16 a[100:103], a[136:139], v[152:155], a[100:103]\n\t" \
  "ds_read_b128 a[248:251], v222 offset:12288\n\t" \
  "v_cvt_pk_bf16_f32 v142, v44, v45\n\t" \
  "v_mfma_f32_16x16x32_bf16 a[100:103], a[140:143], v[156:159], a[100:103]\n\t" \
  "buffer_load_dwordx4 v231, s[64:67], s96 offen offset:3072 lds\n\t" \
  "v_mfma_f32_16x16x32_bf16 a[104:107], a[144:147], v[152:155], a[104:107]\n\t" \
  "v_exp_f32 v47, v47\n\t" \
  "v_mfma_f32_16x16x32_bf16 a[104:107], a[148:151], v[156:159], a[104:107]\n\t" \
  "ds_read_b128 a[252:255], v223 offset:12288\n\t" \
  "v_cvt_pk_bf16_f32 v143, v46, v47\n\t" \
  "v_mfma_f32_16x16x32_bf16 a[108:111], a[152:155], v[152:155], a[108:111]\n\t" \
  "v_exp_f32 v64, v64\n\t" \
  "v_mfma_f32_16x16x32_bf16 a[108:111], a[156:159], v[156:159], a[108:111]\n\t" \
  "v_exp_f32 v65, v65\n\t" \
  "v_mfma_f32_16x16x32_bf16 a[112:115], a[160:163], v[152:155], a[112:115]\n\t" \
  "v_exp_f32 v66, v66\n\t" \
  "v_mfma_f32_16x16x32_bf16 a[112:115], a[164:167], v[156:159], a[112:115]\n\t" \
  "v_exp_f32 v67, v67\n\t" \
  "v_mfma_f32_16x16x32_bf16 a[116:119], a[168:171], v[152:155], a[116:119]\n\t" \
  "v_exp_f32 v68, v68\n\t" \
  "v_mfma_f32_16x16x32_bf16 a[116:119], a[172:175], v[156:159], a[116:119]\n\t" \
  "v_exp_f32 v69, v69\n\t" \
  "v_mfma_f32_16x16x32_bf16 a[120:123], a[176:179], v[152:155], a[120:123]\n\t" \
  "v_exp_f32 v70, v70\n\t" \
  "v_mfma_f32_16x16x32_bf16 a[120:123], a[180:183], v[156:159], a[120:123]\n\t" \
  "v_exp_f32 v71, v71\n\t" \
  "v_mfma_f32_16x16x32_bf16 a[124:127], a[184:187], v[152:155], a[124:127]\n\t" \
  "v_exp_f32 v72, v72\n\t" \
  "v_mfma_f32_16x16x32_bf16 a[124:127], a[188:191], v[156:159], a[124:127]\n\t" \
  "v_exp_f32 v73, v73\n\t" \
  "v_smfmac_f32_16x16x64_bf16 v[172:175], v[216:219], v[152:159], v208\n\t" \
  "s_waitcnt lgkmcnt(12)\n\t" \
  "s_cmp_lg_u32 s99, 0\n\t" \
  "s_cbranch_scc1 .Lsf_p5\n\t" \
  ".Lsf_q5:\n\t" \
  "v_mfma_f32_16x16x32_bf16 v[0:3], a[192:195], v[16:19], v[176:179]\n\t" \
  "ds_read_b64_tr_b16 a[128:129], v210 offset:0\n\t" \
  "v_cvt_pk_bf16_f32 v144, v64, v65\n\t" \
  "v_mfma_f32_16x16x32_bf16 v[0:3], a[196:199], v[20:23], v[0:3]\n\t" \
  "v_exp_f32 v74, v74\n\t" \
  "v_mfma_f32_16x16x32_bf16 v[0:3], a[200:203], v[24:27], v[0:3]\n\t" \
  "ds_read_b64_tr_b16 a[130:131], v210 offset:4096\n\t" \
  "v_cvt_pk_bf16_f32 v145, v66, v67\n\t" \
  "v_mfma_f32_16x16x32_bf16 v[0:3], a[204:207], v[28:31], v[0:3]\n\t" \
  "s_waitcnt lgkmcnt(10)\n\t" \
  "v_exp_f32 v75, v75\n\t" \
  "v_mfma_f32_16x16x32_bf16 v[4:7], a[220:223], v[28:31], v[176:179]\n\t" \
  "ds_read_b64_tr_b16 a[132:133], v210 offset:8192\n\t" \
  "v_cvt_pk_bf16_f32 v146, v68, v69\n\t" \
  "v_mfma_f32_16x16x32_bf16 v[4:7], a[216:219], v[24:27], v[4:7]\n\t" \
  "v_exp_f32 v76, v76\n\t" \
  "v_mfma_f32_16x16x32_bf16 v[4:7], a[212:215], v[20:23], v[4:7]\n\t" \
  "ds_read_b64_tr_b16 a[134:135], v210 offset:12288\n\t" \
  "v_max3_f32 v204, v0, v1, v2\n\t" \
  "v_mfma_f32_16x16x32_bf16 v[4:7], a[208:211], v[16:19], v[4:7]\n\t" \
  "s_waitcnt lgkmcnt(8)\n\t" \
  "ds_read_b64_tr_b16 a[136:137], v211 offset:0\n\t" \
  "v_cvt_pk_bf16_f32 v147, v70, v71\n\t" \
  "v_mfma_f32_16x16x32_bf16 v[8:11], a[224:227], v[16:19], v[176:179]\n\t" \
  "v_exp_f32 v77, v77\n\t" \
  "v_mfma_f32_16x16x32_bf16 v[8:11], a[228:231], v[20:23], v[8:11]\n\t" \
  "ds_read_b64_tr_b16 a[138:139], v211 offset:4096\n\t" \
  "v_cvt_pk_bf16_f32 v148, v72, v73\n\t" \
  "v_mfma_f32_16x16x32_bf16 v[8:11], a[232:235], v[24:27], v[8:11]\n\t" \
  "v_max3_f32 v203, v3, v4, v5\n\t" \
  "v_max3_f32 v204, v204, v6, v7\n\t" \
  "v_mfma_f32_16x16x32_bf16 v[8:11], a[236:239], v[28:31], v[8:11]\n\t" \
  "s_waitcnt lgkmcnt(6)\n\t" \
  "ds_read_b64_tr_b16 a[140:141], v211 offset:8192\n\t" \
  "v_cvt_pk_bf16_f32 v149, v74, v75\n\t" \
  "v_mfma_f32_16x16x32_bf16 v[12:15], a[252:255], v[28:31], v[176:179]\n\t" \
  "v_exp_f32 v78, v78\n\t" \
  "v_mfma_f32_16x16x32_bf16 v[12:15], a[248:251], v[24:27], v[12:15]\n\t" \
  "ds_read_b64_tr_b16 a[142:143], v211 offset:12288\n\t" \
  "v_cvt_pk_bf16_f32 v150, v76, v77\n\t" \
  "v_mfma_f32_16x16x32_bf16 v[12:15], a[244:247], v[20:23], v[12:15]\n\t" \
  "v_max3_f32 v203, v203, v8, v9\n\t" \
  "v_max3_f32 v204, v204, v10, v11\n\t" \
  "v_mfma_f32_16x16x32_bf16 v[12:15], a[240:243], v[16:19], v[12:15]\n\t" \
  "v_exp_f32 v79, v79\n\t" \
  "v_mfma_f32_16x16x32_bf16 v[32:35], a[192:195], v[48:51], v[180:183]\n\t" \
  "ds_read_b64_tr_b16 a[144:145], v210 offset:256\n\t" \
  "v_cvt_pk_bf16_f32 v151, v78, v79\n\t" \
  "v_mfma_f32_16x16x32_bf16 v[32:35], a[196:199], v[52:55], v[32:35]\n\t" \
  "v_exp_f32 v96, v96\n\t" \
  "v_mfma_f32_16x16x32_bf16 v[32:35], a[200:203], v[56:59], v[32:35]\n\t" \
  "ds_read_b64_tr_b16 a[146:147], v210 offset:4352\n\t" \
  "v_max3_f32 v203, v203, v12, v13\n\t" \
  "v_mfma_f32_16x16x32_bf16 v[32:35], a[204:207], v[60:63], v[32:35]\n\t" \
  "v_max3_f32 v204, v204, v14, v15\n\t" \
  "v_max_f32 v204, v204, v203\n\t" \
  "v_mfma_f32_16x16x32_bf16 v[36:39], a[220:223], v[60:63], v[180:183]\n\t" \
  "ds_read_b64_tr_b16 a[148:149], v210 offset:8448\n\t" \
  "v_cmp_lt_f32 vcc, 0x42800000, v204\n\t" \
  "v_mfma_f32_16x16x32_bf16 v[36:39], a[216:219], v[56:59], v[36:39]\n\t" \
  "v_exp_f32 v97, v97\n\t" \
  "v_mfma_f32_16x16x32_bf16 v[36:39], a[212:215], v[52:55], v[36:39]\n\t" \
  "s_cbranch_vccnz .Lsf_c60\n\t" \
  ".Lsf_r60:\n\t" \
  "v_mfma_f32_16x16x32_bf16 v[36:39], a[208:211], v[48:51], v[36:39]\n\t" \
  "ds_read_b64_tr_b16 a[150:151], v210 offset:12544\n\t" \
  "v_max3_f32 v204, v32, v33, v34\n\t" \
  "v_mfma_f32_16x16x32_bf16 v[40:43], a[224:227], v[48:51], v[180:183]\n\t" \
  "ds_read_b64_tr_b16 a[152:153], v211 offset:256\n\t" \
  "v_cvt_pk_bf16_f32 v152, v96, v97\n\t" \
  "v_mfma_f32_16x16x32_bf16 v[40:43], a[228:231], v[52:55], v[40:43]\n\t" \
  "v_exp_f32 v98, v98\n\t" \
  "v_mfma_f32_16x16x32_bf16 v[40:43], a[232:235], v[56:59], v[40:43]\n\t" \
  "v_max3_f32 v203, v35, v36, v37\n\t" \
  "v_max3_f32 v204, v204, v38, v39\n\t" \
  "v_mfma_f32_16x16x32_bf16 v[40:43], a[236:239], v[60:63], v[40:43]\n\t" \
  "v_exp_f32 v99, v99\n\t" \
  "v_mfma_f32_16x16x32_bf16 v[44:47], a[252:255], v[60:63], v[180:183]\n\t" \
  "ds_read_b64_tr_b16 a[154:155], v211 offset:4352\n\t" \
  "v_cvt_pk_bf16_f32 v153, v98, v99\n\t" \
  "v_mfma_f32_16x16x32_bf16 v[44:47], a[248:251], v[56:59], v[44:47]\n\t" \
  "v_exp_f32 v100, v100\n\t" \
  "v_mfma_f32_16x16x32_bf16 v[44:47], a[244:247], v[52:55], v[44:47]\n\t" \
  "ds_read_b64_tr_b16 a[156:157], v211 offset:8448\n\t" \
  "v_max3_f32 v203, v203, v40, v41\n\t" \
  "v_mfma_f32_16x16x32_bf16 v[44:47], a[240:243], v[48:51], v[44:47]\n\t" \
  "ds_read_b64_tr_b16 a[158:159], v211 offset:12544\n\t" \
  "v_max3_f32 v204, v204, v42, v43\n\t" \
  "v_mfma_f32_16x16x32_bf16 v[64:67], a[192:195], v[80:83], v[184:187]\n\t" \
  "v_exp_f32 v101, v101\n\t" \
  "v_mfma_f32_16x16x32_bf16 v[64:67], a[196:199], v[84:87], v[64:67]\n\t" \
  "ds_read_b64_tr_b16 a[160:161], v210 offset:512\n\t" \
  "v_cvt_pk_bf16_f32 v154, v100, v101\n\t" \
  "v_mfma_f32_16x16x32_bf16 v[64:67], a[200:203], v[88:91], v[64:67]\n\t" \
  "v_max3_f32 v203, v203, v44, v45\n\t" \
  "v_max3_f32 v204, v204, v46, v47\n\t" \
  "v_mfma_f32_16x16x32_bf16 v[64:67], a[204:207], v[92:95], v[64:67]\n\t" \
  "v_max_f32 v204, v204, v203\n\t" \
  "v_cmp_lt_f32 vcc, 0x42800000, v204\n\t" \
  "v_mfma_f32_16x16x32_bf16 v[68:71], a[220:223], v[92:95], v[184:187]\n\t" \
  "v_exp_f32 v102, v102\n\t" \
  "v_mfma_f32_16x16x32_bf16 v[68:71], a[216:219], v[88:91], v[68:71]\n\t" \
  "s_cbranch_vccnz .Lsf_c61\n\t" \
  ".Lsf_r61:\n\t" \
  "v_mfma_f32_16x16x32_bf16 v[68:71], a[212:215], v[84:87], v[68:71]\n\t" \
  "ds_read_b64_tr_b16 a[162:163], v210 offset:4608\n\t" \
  "v_max3_f32 v204, v64, v65, v66\n\t" \
  "v_mfma_f32_16x16x32_bf16 v[68:71], a[208:211], v[80:83], v[68:71]\n\t" \
  "ds_read_b64_tr_b16 a[164:165], v210 offset:8704\n\t" \
  "v_exp_f32 v103, v103\n\t" \
  "v_mfma_f32_16x16x32_bf16 v[72:75], a[224:227], v[80:83], v[184:187]\n\t" \
  "ds_read_b64_tr_b16 a[166:167], v210 offset:12800\n\t" \
  "v_exp_f32 v104, v104\n\t" \
  "v_mfma_f32_16x16x32_bf16 v[72:75], a[228:231], v[84:87], v[72:75]\n\t" \
  "ds_read_b64_tr_b16 a[168:169], v211 offset:512\n\t" \
  "v_exp_f32 v105, v105\n\t" \
  "v_mfma_f32_16x16x32_bf16 v[72:75], a[232:235], v[88:91], v[72:75]\n\t" \
  "v_max3_f32 v203, v67, v68, v69\n\t" \
  "v_max3_f32 v204, v204, v70, v71\n\t" \
  "v_mfma_f32_16x16x32_bf16 v[72:75], a[236:239], v[92:95], v[72:75]\n\t" \
  "ds_read_b64_tr_b16 a[170:171], v211 offset:4608\n\t" \
  "v_exp_f32 v106, v106\n\t" \
  "v_mfma_f32_16x16x32_bf16 v[76:79], a[252:255], v[92:95], v[184:187]\n\t" \
  "ds_read_b64_tr_b16 a[172:173], v211 offset:8704\n\t" \
  "v_exp_f32 v107, v107\n\t" \
  "v_mfma_f32_16x16x32_bf16 v[76:79], a[248:251], v[88:91], v[76:79]\n\t" \
  "ds_read_b64_tr_b16 a[174:175], v211 offset:12800\n\t" \
  "v_cvt_pk_bf16_f32 v155, v102, v103\n\t" \
  "v_mfma_f32_16x16x32_bf16 v[76:79], a[244:247], v[84:87], v[76:79]\n\t" \
  "v_max3_f32 v203, v203, v72, v73\n\t" \
  "v_max3_f32 v204, v204, v74, v75\n\t" \
  "v_mfma_f32_16x16x32_bf16 v[76:79], a[240:243], v[80:83], v[76:79]\n\t" \
  "ds_read_b64_tr_b16 a[176:177], v210 offset:768\n\t" \
  "v_exp_f32 v108, v108\n\t" \
  "v_mfma_f32_16x16x32_bf16 v[96:99], a[192:195], v[112:115], v[188:191]\n\t" \
  "ds_read_b64_tr_b16 a[178:179], v210 offset:4864\n\t" \
  "v_exp_f32 v109, v109\n\t" \
  "v_mfma_f32_16x16x32_bf16 v[96:99], a[196:199], v[116:119], v[96:99]\n\t" \
  "ds_read_b64_tr_b16 a[180:181], v210 offset:8960\n\t" \
  "v_cvt_pk_bf16_f32 v156, v104, v105\n\t" \
  "v_mfma_f32_16x16x32_bf16 v[96:99], a[200:203], v[120:123], v[96:99]\n\t" \
  "v_max3_f32 v203, v203, v76, v77\n\t" \
  "v_max3_f32 v204, v204, v78, v79\n\t" \
  "v_mfma_f32_16x16x32_bf16 v[96:99], a[204:207], v[124:127], v[96:99]\n\t" \
  "v_max_f32 v204, v204, v203\n\t" \
  "v_cmp_lt_f32 vcc, 0x42800000, v204\n\t" \
  "v_mfma_f32_16x16x32_bf16 v[100:103], a[220:223], v[124:127], v[188:191]\n\t" \
  "ds_read_b64_tr_b16 a[182:183], v210 offset:13056\n\t" \
  "v_exp_f32 v110, v110\n\t" \
  "v_mfma_f32_16x16x32_bf16 v[100:103], a[216:219], v[120:123], v[100:103]\n\t" \
  "s_cbranch_vccnz .Lsf_c62\n\t" \
  ".Lsf_r62:\n\t" \
  "v_mfma_f32_16x16x32_bf16 v[100:103], a[212:215], v[116:119], v[100:103]\n\t" \
  "v_max3_f32 v204, v96, v97, v98\n\t" \
  "v_exp_f32 v111, v111\n\t" \
  "v_mfma_f32_16x16x32_bf16 v[100:103], a[208:211], v[112:115], v[100:103]\n\t" \
  "ds_read_b64_tr_b16 a[184:185], v211 offset:768\n\t" \
  "v_cvt_pk_bf16_f32 v157, v106, v107\n\t" \
  "v_mfma_f32_16x16x32_bf16 v[104:107], a[224:227], v[112:115], v[188:191]\n\t" \
  "ds_read_b64_tr_b16 a[186:187], v211 offset:4864\n\t" \
  "v_cvt_pk_bf16_f32 v158, v108, v109\n\t" \
  "v_mfma_f32_16x16x32_bf16 v[104:107], a[228:231], v[116:119], v[104:107]\n\t" \
  "ds_read_b64_tr_b16 a[188:189], v211 offset:8960\n\t" \
  "v_cvt_pk_bf16_f32 v159, v110, v111\n\t" \
  "v_mfma_f32_16x16x32_bf16 v[104:107], a[232:235], v[120:123], v[104:107]\n\t" \
  "v_max3_f32 v203, v99, v100, v101\n\t" \
  "v_max3_f32 v204, v204, v102, v103\n\t" \
  "v_mfma_f32_16x16x32_bf16 v[104:107], a[236:239], v[124:127], v[104:107]\n\t" \
  "ds_read_b64_tr_b16 a[190:191], v211 offset:13056\n\t" \
  "s_nop 0\n\t" \
  "v_mfma_f32_16x16x32_bf16 v[108:111], a[252:255], v[124:127], v[188:191]\n\t" \
  "v_exp_f32 v0, v0\n\t" \
  "v_mfma_f32_16x16x32_bf16 v[108:111], a[248:251], v[120:123], v[108:111]\n\t" \
  "v_exp_f32 v1, v1\n\t" \
  "v_mfma_f32_16x16x32_bf16 v[108:111], a[244:247], v[116:119], v[108:111]\n\t" \
  "s_waitcnt vmcnt(0) lgkmcnt(0)\n\t" \
  "v_max3_f32 v203, v203, v104, v105\n\t" \
  "v_max3_f32 v204, v204, v106, v107\n\t" \
  "v_mfma_f32_16x16x32_bf16 v[108:111], a[240:243], v[112:115], v[108:111]\n\t" \
  "s_barrier\n\t" \
  "v_mfma_f32_16x16x32_bf16 a[0:3], a[128:131], v[128:131], a[0:3]\n\t" \
  "v_exp_f32 v2, v2\n\t" \
  "v_mfma_f32_16x16x32_bf16 a[0:3], a[132:135], v[132:135], a[0:3]\n\t" \
  "v_exp_f32 v3, v3\n\t" \
  "v_mfma_f32_16x16x32_bf16 a[4:7], a[136:139], v[128:131], a[4:7]\n\t" \
  "v_exp_f32 v4, v4\n\t" \
  "v_mfma_f32_16x16x32_bf16 a[4:7], a[140:143], v[132:135], a[4:7]\n\t" \
  "v_max3_f32 v203, v203, v108, v109\n\t" \
  "v_max3_f32 v204, v204, v110, v111\n\t" \
  "v_mfma_f32_16x16x32_bf16 a[8:11], a[144:147], v[128:131], a[8:11]\n\t" \
  "v_max_f32 v204, v204, v203\n\t" \
  "v_cmp_lt_f32 vcc, 0x42800000, v204\n\t" \
  "v_mfma_f32_16x16x32_bf16 a[8:11], a[148:151], v[132:135], a[8:11]\n\t" \
  "v_exp_f32 v5, v5\n\t" \
  "v_mfma_f32_16x16x32_bf16 a[12:15], a[152:155], v[128:131], a[12:15]\n\t" \
  "s_cbranch_vccnz .Lsf_c63\n\t" \
  ".Lsf_r63:\n\t" \
  "v_mfma_f32_16x16x32_bf16 a[12:15], a[156:159], v[132:135], a[12:15]\n\t" \
  "s_add_u32 m0, s38, 147456\n\t" \
  "v_exp_f32 v6, v6\n\t" \
  "v_mfma_f32_16x16x32_bf16 a[16:19], a[160:163], v[128:131], a[16:19]\n\t" \
  "s_add_u32 s96, s38, 114688\n\t" \
  "v_exp_f32 v7, v7\n\t" \
  "v_mfma_f32_16x16x32_bf16 a[16:19], a[164:167], v[132:135], a[16:19]\n\t" \
  "v_exp_f32 v8, v8\n\t" \
  "v_mfma_f32_16x16x32_bf16 a[20:23], a[168:171], v[128:131], a[20:23]\n\t" \
  "buffer_load_dwordx4 v230, s[64:67], s96 offen offset:0 lds\n\t" \
  "v_mfma_f32_16x16x32_bf16 a[20:23], a[172:175], v[132:135], a[20:23]\n\t" \
  "v_exp_f32 v9, v9\n\t" \
  "v_mfma_f32_16x16x32_bf16 a[24:27], a[176:179], v[128:131], a[24:27]\n\t" \
  "v_exp_f32 v10, v10\n\t" \
  "v_mfma_f32_16x16x32_bf16 a[24:27], a[180:183], v[132:135], a[24:27]\n\t" \
  "v_exp_f32 v11, v11\n\t" \
  "v_mfma_f32_16x16x32_bf16 a[28:31], a[184:187], v[128:131], a[28:31]\n\t" \
  "v_exp_f32 v12, v12\n\t" \
  "v_mfma_f32_16x16x32_bf16 a[28:31], a[188:191], v[132:135], a[28:31]\n\t" \
  "v_exp_f32 v13, v13\n\t" \
  "v_smfmac_f32_16x16x64_bf16 v[160:163], v[216:219], v[128:135], v208\n\t" \
  "ds_read_b128 a[192:195], v220 offset:16384\n\t" \
  "v_cvt_pk_bf16_f32 v128, v0, v1\n\t" \
  "v_mfma_f32_16x16x32_bf16 a[32:35], a[128:131], v[136:139], a[32:35]\n\t" \
  "v_exp_f32 v14, v14\n\t" \
  "v_mfma_f32_16x16x32_bf16 a[32:35], a[132:135], v[140:143], a[32:35]\n\t" \
  "v_exp_f32 v15, v15\n\t" \
  "v_mfma_f32_16x16x32_bf16 a[36:39], a[136:139], v[136:139], a[36:39]\n\t" \
  "ds_read_b128 a[196:199], v221 offset:16384\n\t" \
  "v_cvt_pk_bf16_f32 v129, v2, v3\n\t" \
  "v_mfma_f32_16x16x32_bf16 a[36:39], a[140:143], v[140:143], a[36:39]\n\t" \
  "v_exp_f32 v32, v32\n\t" \
  "v_mfma_f32_16x16x32_bf16 a[40:43], a[144:147], v[136:139], a[40:43]\n\t" \
  "ds_read_b128 a[200:203], v222 offset:16384\n\t" \
  "v_cvt_pk_bf16_f32 v130, v4, v5\n\t" \
  "v_mfma_f32_16x16x32_bf16 a[40:43], a[148:151], v[140:143], a[40:43]\n\t" \
  "v_exp_f32 v33, v33\n\t" \
  "v_mfma_f32_16x16x32_bf16 a[44:47], a[152:155], v[136:139], a[44:47]\n\t" \
  "buffer_load_dwordx4 v231, s[64:67], s96 offen offset:1024 lds\n\t" \
  "v_mfma_f32_16x16x32_bf16 a[44:47], a[156:159], v[140:143], a[44:47]\n\t" \
  "ds_read_b128 a[204:207], v223 offset:16384\n\t" \
  "v_cvt_pk_bf16_f32 v131, v6, v7\n\t" \
  "v_mfma_f32_16x16x32_bf16 a[48:51], a[160:163], v[136:139], a[48:51]\n\t" \
  "v_exp_f32 v34, v34\n\t" \
  "v_mfma_f32_16x16x32_bf16 a[48:51], a[164:167], v[140:143], a[48:51]\n\t" \
  "v_exp_f32 v35, v35\n\t" \
  "v_mfma_f32_16x16x32_bf16 a[52:55], a[168:171], v[136:139], a[52:55]\n\t" \
  "ds_read_b128 a[208:211], v220 offset:20480\n\t" \
  "v_cvt_pk_bf16_f32 v132, v8, v9\n\t" \
  "v_mfma_f32_16x16x32_bf16 a[52:55], a[172:175], v[140:143], a[52:55]\n\t" \
  "v_exp_f32 v36, v36\n\t" \
  "v_mfma_f32_16x16x32_bf16 a[56:59], a[176:179], v[136:139], a[56:59]\n\t" \
  "ds_read_b128 a[212:215], v221 offset:20480\n\t" \
  "v_cvt_pk_bf16_f32 v133, v10, v11\n\t" \
  "v_mfma_f32_16x16x32_bf16 a[56:59], a[180:183], v[140:143], a[56:59]\n\t" \
  "v_exp_f32 v37, v37\n\t" \
  "v_mfma_f32_16x16x32_bf16 a[60:63], a[184:187], v[136:139], a[60:63]\n\t" \
  "ds_read_b128 a[216:219], v222 offset:20480\n\t" \
  "v_cvt_pk_bf16_f32 v134, v12, v13\n\t" \
  "v_mfma_f32_16x16x32_bf16 a[60:63], a[188:191], v[140:143], a[60:63]\n\t" \
  "v_exp_f32 v38, v38\n\t" \
  "v_smfmac_f32_16x16x64_bf16 v[164:167], v[216:219], v[136:143], v208\n\t" \
  "ds_read_b128 a[220:223], v223 offset:20480\n\t" \
  "v_cvt_pk_bf16_f32 v135, v14, v15\n\t" \
  "v_mfma_f32_16x16x32_bf16 a[64:67], a[128:131], v[144:147], a[64:67]\n\t" \
  "v_exp_f32 v39, v39\n\t" \
  "v_mfma_f32_16x16x32_bf16 a[64:67], a[132:135], v[148:151], a[64:67]\n\t" \
  "buffer_load_dwordx4 v230, s[64:67], s96 offen offset:2048 lds\n\t" \
  "v_mfma_f32_16x16x32_bf16 a[68:71], a[136:139], v[144:147], a[68:71]\n\t" \
  "ds_read_b128 a[224:227], v220 offset:24576\n\t" \
  "v_cvt_pk_bf16_f32 v136, v32, v33\n\t" \
  "v_mfma_f32_16x16x32_bf16 a[68:71], a[140:143], v[148:151], a[68:71]\n\t" \
  "v_exp_f32 v40, v40\n\t" \
  "v_mfma_f32_16x16x32_bf16 a[72:75], a[144:147], v[144:147], a[72:75]\n\t" \
  "v_exp_f32 v41, v41\n\t" \
  "v_mfma_f32_16x16x32_bf16 a[72:75], a[148:151], v[148:151], a[72:75]\n\t" \
  "ds_read_b128 a[228:231], v221 offset:24576\n\t" \
  "v_cvt_pk_bf16_f32 v137, v34, v35\n\t" \
  "v_mfma_f32_16x16x32_bf16 a[76:79], a[152:155], v[144:147], a[76:79]\n\t" \
  "v_exp_f32 v42, v42\n\t" \
  "v_mfma_f32_16x16x32_bf16 a[76:79], a[156:159], v[148:151], a[76:79]\n\t" \
  "v_exp_f32 v43, v43\n\t" \
  "v_mfma_f32_16x16x32_bf16 a[80:83], a[160:163], v[144:147], a[80:83]\n\t" \
  "ds_read_b128 a[232:235], v222 offset:24576\n\t" \
  "v_cvt_pk_bf16_f32 v138, v36, v37\n\t" \
  "v_mfma_f32_16x16x32_bf16 a[80:83], a[164:167], v[148:151], a[80:83]\n\t" \
  "v_exp_f32 v44, v44\n\t" \
  "v_mfma_f32_16x16x32_bf16 a[84:87], a[168:171], v[144:147], a[84:87]\n\t" \
  "v_exp_f32 v45, v45\n\t" \
  "v_mfma_f32_16x16x32_bf16 a[84:87], a[172:175], v[148:151], a[84:87]\n\t" \
  "ds_read_b128 a[236:239], v223 offset:24576\n\t" \
  "v_cvt_pk_bf16_f32 v139, v38, v39\n\t" \
  "v_mfma_f32_16x16x32_bf16 a[88:91], a[176:179], v[144:147], a[88:91]\n\t" \
  "v_exp_f32 v46, v46\n\t" \
  "v_mfma_f32_16x16x32_bf16 a[88:91], a[180:183], v[148:151], a[88:91]\n\t" \
  "ds_read_b128 a[240:243], v220 offset:28672\n\t" \
  "v_cvt_pk_bf16_f32 v140, v40, v41\n\t" \
  "v_mfma_f32_16x16x32_bf16 a[92:95], a[184:187], v[144:147], a[92:95]\n\t" \
  "buffer_load_dwordx4 v231, s[64:67], s96 offen offset:3072 lds\n\t" \
  "v_mfma_f32_16x16x32_bf16 a[92:95], a[188:191], v[148:151], a[92:95]\n\t" \
  "v_exp_f32 v47, v47\n\t" \
  "v_smfmac_f32_16x16x64_bf16 v[168:171], v[216:219], v[144:151], v208\n\t" \
  "ds_read_b128 a[244:247], v221 offset:28672\n\t" \
  "v_cvt_pk_bf16_f32 v141, v42, v43\n\t" \
  "v_mfma_f32_16x16x32_bf16 a[96:99], a[128:131], v[152:155], a[96:99]\n\t" \
  "v_exp_f32 v64, v64\n\t" \
  "v_mfma_f32_16x16x32_bf16 a[96:99], a[132:135], v[156:159], a[96:99]\n\t" \
  "v_exp_f32 v65, v65\n\t" \
  "v_mfma_f32_16x16x32_bf16 a[100:103], a[136:139], v[152:155], a[100:103]\n\t" \
  "ds_read_b128 a[248:251], v222 offset:28672\n\t" \
  "v_cvt_pk_bf16_f32 v142, v44, v45\n\t" \
  "v_mfma_f32_16x16x32_bf16 a[100:103], a[140:143], v[156:159], a[100:103]\n\t" \
  "v_exp_f32 v66, v66\n\t" \
  "v_mfma_f32_16x16x32_bf16 a[104:107], a[144:147], v[152:155], a[104:107]\n\t" \
  "v_exp_f32 v67, v67\n\t" \
  "v_mfma_f32_16x16x32_bf16 a[104:107], a[148:151], v[156:159], a[104:107]\n\t" \
  "ds_read_b128 a[252:255], v223 offset:28672\n\t" \
  "v_cvt_pk_bf16_f32 v143, v46, v47\n\t" \
  "v_mfma_f32_16x16x32_bf16 a[108:111], a[152:155], v[152:155], a[108:111]\n\t" \
  "v_exp_f32 v68, v68\n\t" \
  "v_mfma_f32_16x16x32_bf16 a[108:111], a[156:159], v[156:159], a[108:111]\n\t" \
  "v_exp_f32 v69, v69\n\t" \
  "v_mfma_f32_16x16x32_bf16 a[112:115], a[160:163], v[152:155], a[112:115]\n\t" \
  "v_exp_f32 v70, v70\n\t" \
  "v_mfma_f32_16x16x32_bf16 a[112:115], a[164:167], v[156:159], a[112:115]\n\t" \
  "v_exp_f32 v71, v71\n\t" \
  "v_mfma_f32_16x16x32_bf16 a[116:119], a[168:171], v[152:155], a[116:119]\n\t" \
  "v_exp_f32 v72, v72\n\t" \
  "v_mfma_f32_16x16x32_bf16 a[116:119], a[172:175], v[156:159], a[116:119]\n\t" \
  "v_exp_f32 v73, v73\n\t" \
  "v_mfma_f32_16x16x32_bf16 a[120:123], a[176:179], v[152:155], a[120:123]\n\t" \
  "v_exp_f32 v74, v74\n\t" \
  "v_mfma_f32_16x16x32_bf16 a[120:123], a[180:183], v[156:159], a[120:123]\n\t" \
  "v_exp_f32 v75, v75\n\t" \
  "v_mfma_f32_16x16x32_bf16 a[124:127], a[184:187], v[152:155], a[124:127]\n\t" \
  "v_exp_f32 v76, v76\n\t" \
  "v_mfma_f32_16x16x32_bf16 a[124:127], a[188:191], v[156:159], a[124:127]\n\t" \
  "v_exp_f32 v77, v77\n\t" \
  "v_smfmac_f32_16x16x64_bf16 v[172:175], v[216:219], v[152:159], v208\n\t" \
  "s_waitcnt lgkmcnt(12)\n\t" \
  "s_cmp_lg_u32 s99, 0\n\t" \
  "s_cbranch_scc1 .Lsf_p6\n\t" \
  ".Lsf_q6:\n\t" \
  "v_mfma_f32_16x16x32_bf16 v[0:3], a[192:195], v[16:19], v[176:179]\n\t" \
  "ds_read_b64_tr_b16 a[128:129], v228 offset:0\n\t" \
  "v_cvt_pk_bf16_f32 v144, v64, v65\n\t" \
  "v_mfma_f32_16x16x32_bf16 v[0:3], a[196:199], v[20:23], v[0:3]\n\t" \
  "v_exp_f32 v78, v78\n\t" \
  "v_mfma_f32_16x16x32_bf16 v[0:3], a[200:203], v[24:27], v[0:3]\n\t" \
  "ds_read_b64_tr_b16 a[130:131], v228 offset:4096\n\t" \
  "v_cvt_pk_bf16_f32 v145, v66, v67\n\t" \
  "v_mfma_f32_16x16x32_bf16 v[0:3], a[204:207], v[28:31], v[0:3]\n\t" \
  "s_waitcnt lgkmcnt(10)\n\t" \
  "v_exp_f32 v79, v79\n\t" \
  "v_mfma_f32_16x16x32_bf16 v[4:7], a[220:223], v[28:31], v[176:179]\n\t" \
  "ds_read_b64_tr_b16 a[132:133], v228 offset:8192\n\t" \
  "v_cvt_pk_bf16_f32 v146, v68, v69\n\t" \
  "v_mfma_f32_16x16x32_bf16 v[4:7], a[216:219], v[24:27], v[4:7]\n\t" \
  "v_exp_f32 v96, v96\n\t" \
  "v_mfma_f32_16x16x32_bf16 v[4:7], a[212:215], v[20:23], v[4:7]\n\t" \
  "ds_read_b64_tr_b16 a[134:135], v228 offset:12288\n\t" \
  "v_max3_f32 v204, v0, v1, v2\n\t" \
  "v_mfma_f32_16x16x32_bf16 v[4:7], a[208:211], v[16:19], v[4:7]\n\t" \
  "s_waitcnt lgkmcnt(8)\n\t" \
  "ds_read_b64_tr_b16 a[136:137], v229 offset:0\n\t" \
  "v_cvt_pk_bf16_f32 v147, v70, v71\n\t" \
  "v_mfma_f32_16x16x32_bf16 v[8:11], a[224:227], v[16:19], v[176:179]\n\t" \
  "v_exp_f32 v97, v97\n\t" \
  "v_mfma_f32_16x16x32_bf16 v[8:11], a[228:231], v[20:23], v[8:11]\n\t" \
  "ds_read_b64_tr_b16 a[138:139], v229 offset:4096\n\t" \
  "v_cvt_pk_bf16_f32 v148, v72, v73\n\t" \
  "v_mfma_f32_16x16x32_bf16 v[8:11], a[232:235], v[24:27], v[8:11]\n\t" \
  "v_max3_f32 v203, v3, v4, v5\n\t" \
  "v_max3_f32 v204, v204, v6, v7\n\t" \
  "v_mfma_f32_16x16x32_bf16 v[8:11], a[236:239], v[28:31], v[8:11]\n\t" \
  "s_waitcnt lgkmcnt(6)\n\t" \
  "ds_read_b64_tr_b16 a[140:141], v229 offset:8192\n\t" \
  "v_cvt_pk_bf16_f32 v149, v74, v75\n\t" \
  "v_mfma_f32_16x16x32_bf16 v[12:15], a[252:255], v[28:31], v[176:179]\n\t" \
  "v_exp_f32 v98, v98\n\t" \
  "v_mfma_f32_16x16x32_bf16 v[12:15], a[248:251], v[24:27], v[12:15]\n\t" \
  "ds_read_b64_tr_b16 a[142:143], v229 offset:12288\n\t" \
  "v_cvt_pk_bf16_f32 v150, v76, v77\n\t" \
  "v_mfma_f32_16x16x32_bf16 v[12:15], a[244:247], v[20:23], v[12:15]\n\t" \
  "v_max3_f32 v203, v203, v8, v9\n\t" \
  "v_max3_f32 v204, v204, v10, v11\n\t" \
  "v_mfma_f32_16x16x32_bf16 v[12:15], a[240:243], v[16:19], v[12:15]\n\t" \
  "ds_read_b64_tr_b16 a[144:145], v228 offset:256\n\t" \
  "v_cvt_pk_bf16_f32 v151, v78, v79\n\t" \
  "v_mfma_f32_16x16x32_bf16 v[32:35], a[192:195], v[48:51], v[180:183]\n\t" \
  "ds_read_b64_tr_b16 a[146:147], v228 offset:4352\n\t" \
  "v_cvt_pk_bf16_f32 v152, v96, v97\n\t" \
  "v_mfma_f32_16x16x32_bf16 v[32:35], a[196:199], v[52:55], v[32:35]\n\t" \
  "v_exp_f32 v99, v99\n\t" \
  "v_mfma_f32_16x16x32_bf16 v[32:35], a[200:203], v[56:59], v[32:35]\n\t" \
  "v_max3_f32 v203, v203, v12, v13\n\t" \
  "v_max3_f32 v204, v204, v14, v15\n\t" \
  "v_mfma_f32_16x16x32_bf16 v[32:35], a[204:207], v[60:63], v[32:35]\n\t" \
  "v_max_f32 v204, v204, v203\n\t" \
  "v_cmp_lt_f32 vcc, 0x42800000, v204\n\t" \
  "v_mfma_f32_16x16x32_bf16 v[36:39], a[220:223], v[60:63], v[180:183]\n\t" \
  "ds_read_b64_tr_b16 a[148:149], v228 offset:8448\n\t" \
  "v_cvt_pk_bf16_f32 v153, v98, v99\n\t" \
  "v_mfma_f32_16x16x32_bf16 v[36:39], a[216:219], v[56:59], v[36:39]\n\t" \
  "s_cbranch_vccnz .Lsf_c70\n\t" \
  ".Lsf_r70:\n\t" \
  "v_mfma_f32_16x16x32_bf16 v[36:39], a[212:215], v[52:55], v[36:39]\n\t" \
  "ds_read_b64_tr_b16 a[150:151], v228 offset:12544\n\t" \
  "v_max3_f32 v204, v32, v33, v34\n\t" \
  "v_mfma_f32_16x16x32_bf16 v[36:39], a[208:211], v[48:51], v[36:39]\n\t" \
  "v_exp_f32 v100, v100\n\t" \
  "v_mfma_f32_16x16x32_bf16 v[40:43], a[224:227], v[48:51], v[180:183]\n\t" \
  "v_exp_f32 v101, v101\n\t" \
  "v_mfma_f32_16x16x32_bf16 v[40:43], a[228:231], v[52:55], v[40:43]\n\t" \
  "ds_read_b64_tr_b16 a[152:153], v229 offset:256\n\t" \
  "v_cvt_pk_bf16_f32 v154, v100, v101\n\t" \
  "v_mfma_f32_16x16x32_bf16 v[40:43], a[232:235], v[56:59], v[40:43]\n\t" \
  "ds_read_b64_tr_b16 a[154:155], v229 offset:4352\n\t" \
  "v_max3_f32 v203, v35, v36, v37\n\t" \
  "v_mfma_f32_16x16x32_bf16 v[40:43], a[236:239], v[60:63], v[40:43]\n\t" \
  "ds_read_b64_tr_b16 a[156:157], v229 offset:8448\n\t" \
  "v_max3_f32 v204, v204, v38, v39\n\t" \
  "v_mfma_f32_16x16x32_bf16 v[44:47], a[252:255], v[60:63], v[180:183]\n\t" \
  "v_exp_f32 v102, v102\n\t" \
  "v_mfma_f32_16x16x32_bf16 v[44:47], a[248:251], v[56:59], v[44:47]\n\t" \
  "v_exp_f32 v103, v103\n\t" \
  "v_mfma_f32_16x16x32_bf16 v[44:47], a[244:247], v[52:55], v[44:47]\n\t" \
  "ds_read_b64_tr_b16 a[158:159], v229 offset:12544\n\t" \
  "v_max3_f32 v203, v203, v40, v41\n\t" \
  "v_mfma_f32_16x16x32_bf16 v[44:47], a[240:243], v[48:51], v[44:47]\n\t" \
  "ds_read_b64_tr_b16 a[160:161], v228 offset:512\n\t" \
  "v_max3_f32 v204, v204, v42, v43\n\t" \
  "v_mfma_f32_16x16x32_bf16 v[64:67], a[192:195], v[80:83], v[184:187]\n\t" \
  "ds_read_b64_tr_b16 a[162:163], v228 offset:4608\n\t" \
  "v_cvt_pk_bf16_f32 v155, v102, v103\n\t" \
  "v_mfma_f32_16x16x32_bf16 v[64:67], a[196:199], v[84:87], v[64:67]\n\t" \
  "v_exp_f32 v104, v104\n\t" \
  "v_mfma_f32_16x16x32_bf16 v[64:67], a[200:203], v[88:91], v[64:67]\n\t" \
  "v_max3_f32 v203, v203, v44, v45\n\t" \
  "v_max3_f32 v204, v204, v46, v47\n\t" \
  "v_mfma_f32_16x16x32_bf16 v[64:67], a[204:207], v[92:95], v[64:67]\n\t" \
  "v_max_f32 v204, v204, v203\n\t" \
  "v_cmp_lt_f32 vcc, 0x42800000, v204\n\t" \
  "v_mfma_f32_16x16x32_bf16 v[68:71], a[220:223], v[92:95], v[184:187]\n\t" \
  "v_exp_f32 v105, v105\n\t" \
  "v_mfma_f32_16x16x32_bf16 v[68:71], a[216:219], v[88:91], v[68:71]\n\t" \
  "s_cbranch_vccnz .Lsf_c71\n\t" \
  ".Lsf_r71:\n\t" \
  "v_mfma_f32_16x16x32_bf16 v[68:71], a[212:215], v[84:87], v[68:71]\n\t" \
  "ds_read_b64_tr_b16 a[164:165], v228 offset:8704\n\t" \
  "v_max3_f32 v204, v64, v65, v66\n\t" \
  "v_mfma_f32_16x16x32_bf16 v[68:71], a[208:211], v[80:83], v[68:71]\n\t" \
  "ds_read_b64_tr_b16 a[166:167], v228 offset:12800\n\t" \
  "v_cvt_pk_bf16_f32 v156, v104, v105\n\t" \
  "v_mfma_f32_16x16x32_bf16 v[72:75], a[224:227], v[80:83], v[184:187]\n\t" \
  "v_exp_f32 v106, v106\n\t" \
  "v_mfma_f32_16x16x32_bf16 v[72:75], a[228:231], v[84:87], v[72:75]\n\t" \
  "v_exp_f32 v107, v107\n\t" \
  "v_mfma_f32_16x16x32_bf16 v[72:75], a[232:235], v[88:91], v[72:75]\n\t" \
  "ds_read_b64_tr_b16 a[168:169], v229 offset:512\n\t" \
  "v_max3_f32 v203, v67, v68, v69\n\t" \
  "v_mfma_f32_16x16x32_bf16 v[72:75], a[236:239], v[92:95], v[72:75]\n\t" \
  "ds_read_b64_tr_b16 a[170:171], v229 offset:4608\n\t" \
  "v_max3_f32 v204, v204, v70, v71\n\t" \
  "v_mfma_f32_16x16x32_bf16 v[76:79], a[252:255], v[92:95], v[184:187]\n\t" \
  "ds_read_b64_tr_b16 a[172:173], v229 offset:8704\n\t" \
  "v_cvt_pk_bf16_f32 v157, v106, v107\n\t" \
  "v_mfma_f32_16x16x32_bf16 v[76:79], a[248:251], v[88:91], v[76:79]\n\t" \
  "v_exp_f32 v108, v108\n\t" \
  "v_mfma_f32_16x16x32_bf16 v[76:79], a[244:247], v[84:87], v[76:79]\n\t" \
  "v_max3_f32 v203, v203, v72, v73\n\t" \
  "v_max3_f32 v204, v204, v74, v75\n\t" \
  "v_mfma_f32_16x16x32_bf16 v[76:79], a[240:243], v[80:83], v[76:79]\n\t" \
  "v_exp_f32 v109, v109\n\t" \
  "v_mfma_f32_16x16x32_bf16 v[96:99], a[192:195], v[112:115], v[188:191]\n\t" \
  "ds_read_b64_tr_b16 a[174:175], v229 offset:12800\n\t" \
  "v_cvt_pk_bf16_f32 v158, v108, v109\n\t" \
  "v_mfma_f32_16x16x32_bf16 v[96:99], a[196:199], v[116:119], v[96:99]\n\t" \
  "v_exp_f32 v110, v110\n\t" \
  "v_mfma_f32_16x16x32_bf16 v[96:99], a[200:203], v[120:123], v[96:99]\n\t" \
  "ds_read_b64_tr_b16 a[176:177], v228 offset:768\n\t" \
  "v_max3_f32 v203, v203, v76, v77\n\t" \
  "v_mfma_f32_16x16x32_bf16 v[96:99], a[204:207], v[124:127], v[96:99]\n\t" \
  "v_max3_f32 v204, v204, v78, v79\n\t" \
  "v_max_f32 v204, v204, v203\n\t" \
  "v_mfma_f32_16x16x32_bf16 v[100:103], a[220:223], v[124:127], v[188:191]\n\t" \
  "ds_read_b64_tr_b16 a[178:179], v228 offset:4864\n\t" \
  "v_cmp_lt_f32 vcc, 0x42800000, v204\n\t" \
  "v_mfma_f32_16x16x32_bf16 v[100:103], a[216:219], v[120:123], v[100:103]\n\t" \
  "ds_read_b64_tr_b16 a[180:181], v228 offset:8960\n\t" \
  "v_exp_f32 v111, v111\n\t" \
  "v_mfma_f32_16x16x32_bf16 v[100:103], a[212:215], v[116:119], v[100:103]\n\t" \
  "s_cbranch_vccnz .Lsf_c72\n\t" \
  ".Lsf_r72:\n\t" \
  "v_mfma_f32_16x16x32_bf16 v[100:103], a[208:211], v[112:115], v[100:103]\n\t" \
  "ds_read_b64_tr_b16 a[182:183], v228 offset:13056\n\t" \
  "v_max3_f32 v204, v96, v97, v98\n\t" \
  "v_mfma_f32_16x16x32_bf16 v[104:107], a[224:227], v[112:115], v[188:191]\n\t" \
  "ds_read_b64_tr_b16 a[184:185], v229 offset:768\n\t" \
  "v_cvt_pk_bf16_f32 v159, v110, v111\n\t" \
  "v_mfma_f32_16x16x32_bf16 v[104:107], a[228:231], v[116:119], v[104:107]\n\t" \
  "ds_read_b64_tr_b16 a[186:187], v229 offset:4864\n\t" \
  "v_mfma_f32_16x16x32_bf16 v[104:107], a[232:235], v[120:123], v[104:107]\n\t" \
  "ds_read_b64_tr_b16 a[188:189], v229 offset:8960\n\t" \
  "v_max3_f32 v203, v99, v100, v101\n\t" \
  "v_mfma_f32_16x16x32_bf16 v[104:107], a[236:239], v[124:127], v[104:107]\n\t" \
  "ds_read_b64_tr_b16 a[190:191], v229 offset:13056\n\t" \
  "v_max3_f32 v204, v204, v102, v103\n\t" \
  "v_mfma_f32_16x16x32_bf16 v[108:111], a[252:255], v[124:127], v[188:191]\n\t" \
  "v_exp_f32 v0, v0\n\t" \
  "v_mfma_f32_16x16x32_bf16 v[108:111], a[248:251], v[120:123], v[108:111]\n\t" \
  "v_exp_f32 v1, v1\n\t" \
  "v_mfma_f32_16x16x32_bf16 v[108:111], a[244:247], v[116:119], v[108:111]\n\t" \
  "s_waitcnt vmcnt(0) lgkmcnt(0)\n\t" \
  "v_max3_f32 v203, v203, v104, v105\n\t" \
  "v_max3_f32 v204, v204, v106, v107\n\t" \
  "v_mfma_f32_16x16x32_bf16 v[108:111], a[240:243], v[112:115], v[108:111]\n\t" \
  "s_barrier\n\t" \
  "v_mfma_f32_16x16x32_bf16 a[0:3], a[128:131], v[128:131], a[0:3]\n\t" \
  "v_exp_f32 v2, v2\n\t" \
  "v_mfma_f32_16x16x32_bf16 a[0:3], a[132:135], v[132:135], a[0:3]\n\t" \
  "ds_read_b64_tr_b16 v[16:17], v210 offset:0\n\t" \
  "v_cvt_pk_bf16_f32 v0, v0, v1\n\t" \
  "v_mfma_f32_16x16x32_bf16 a[4:7], a[136:139], v[128:131], a[4:7]\n\t" \
  "ds_read_b64_tr_b16 v[18:19], v210 offset:4096\n\t" \
  "v_max3_f32 v203, v203, v108, v109\n\t" \
  "v_mfma_f32_16x16x32_bf16 a[4:7], a[140:143], v[132:135], a[4:7]\n\t" \
  "v_max3_f32 v204, v204, v110, v111\n\t" \
  "v_max_f32 v204, v204, v203\n\t" \
  "v_mfma_f32_16x16x32_bf16 a[8:11], a[144:147], v[128:131], a[8:11]\n\t" \
  "ds_read_b64_tr_b16 v[20:21], v210 offset:8192\n\t" \
  "v_cmp_lt_f32 vcc, 0x42800000, v204\n\t" \
  "v_mfma_f32_16x16x32_bf16 a[8:11], a[148:151], v[132:135], a[8:11]\n\t" \
  "v_exp_f32 v3, v3\n\t" \
  "v_mfma_f32_16x16x32_bf16 a[12:15], a[152:155], v[128:131], a[12:15]\n\t" \
  "s_cbranch_vccnz .Lsf_c73\n\t" \
  ".Lsf_r73:\n\t" \
  "v_mfma_f32_16x16x32_bf16 a[12:15], a[156:159], v[132:135], a[12:15]\n\t" \
  "ds_read_b64_tr_b16 v[22:23], v210 offset:12288\n\t" \
  "v_cvt_pk_bf16_f32 v1, v2, v3\n\t" \
  "v_mfma_f32_16x16x32_bf16 a[16:19], a[160:163], v[128:131], a[16:19]\n\t" \
  "v_exp_f32 v4, v4\n\t" \
  "v_mfma_f32_16x16x32_bf16 a[16:19], a[164:167], v[132:135], a[16:19]\n\t" \
  "v_exp_f32 v5, v5\n\t" \
  "v_mfma_f32_16x16x32_bf16 a[20:23], a[168:171], v[128:131], a[20:23]\n\t" \
  "ds_read_b64_tr_b16 v[24:25], v211 offset:0\n\t" \
  "v_cvt_pk_bf16_f32 v2, v4, v5\n\t" \
  "v_mfma_f32_16x16x32_bf16 a[20:23], a[172:175], v[132:135], a[20:23]\n\t" \
  "v_exp_f32 v6, v6\n\t" \
  "v_mfma_f32_16x16x32_bf16 a[24:27], a[176:179], v[128:131], a[24:27]\n\t" \
  "v_exp_f32 v7, v7\n\t" \
  "v_mfma_f32_16x16x32_bf16 a[24:27], a[180:183], v[132:135], a[24:27]\n\t" \
  "ds_read_b64_tr_b16 v[26:27], v211 offset:4096\n\t" \
  "v_cvt_pk_bf16_f32 v3, v6, v7\n\t" \
  "v_mfma_f32_16x16x32_bf16 a[28:31], a[184:187], v[128:131], a[28:31]\n\t" \
  "v_exp_f32 v8, v8\n\t" \
  "v_mfma_f32_16x16x32_bf16 a[28:31], a[188:191], v[132:135], a[28:31]\n\t" \
  "ds_read_b64_tr_b16 v[28:29], v211 offset:8192\n\t" \
  "v_smfmac_f32_16x16x64_bf16 v[160:163], v[216:219], v[128:135], v208\n\t" \
  "ds_read_b64_tr_b16 v[30:31], v211 offset:12288\n\t" \
  "v_mfma_f32_16x16x32_bf16 a[32:35], a[128:131], v[136:139], a[32:35]\n\t" \
  "ds_read_b64_tr_b16 v[48:49], v210 offset:256\n\t" \
  "v_mfma_f32_16x16x32_bf16 a[32:35], a[132:135], v[140:143], a[32:35]\n\t" \
  "ds_read_b64_tr_b16 v[50:51], v210 offset:4352\n\t" \
  "v_mfma_f32_16x16x32_bf16 a[36:39], a[136:139], v[136:139], a[36:39]\n\t" \
  "v_exp_f32 v9, v9\n\t" \
  "v_mfma_f32_16x16x32_bf16 a[36:39], a[140:143], v[140:143], a[36:39]\n\t" \
  "ds_read_b64_tr_b16 v[52:53], v210 offset:8448\n\t" \
  "v_cvt_pk_bf16_f32 v4, v8, v9\n\t" \
  "v_mfma_f32_16x16x32_bf16 a[40:43], a[144:147], v[136:139], a[40:43]\n\t" \
  "v_exp_f32 v10, v10\n\t" \
  "v_mfma_f32_16x16x32_bf16 a[40:43], a[148:151], v[140:143], a[40:43]\n\t" \
  "ds_read_b64_tr_b16 v[54:55], v210 offset:12544\n\t" \
  "v_mfma_f32_16x16x32_bf16 a[44:47], a[152:155], v[136:139], a[44:47]\n\t" \
  "v_exp_f32 v11, v11\n\t" \
  "v_mfma_f32_16x16x32_bf16 a[44:47], a[156:159], v[140:143], a[44:47]\n\t" \
  "ds_read_b64_tr_b16 v[56:57], v211 offset:256\n\t" \
  "v_cvt_pk_bf16_f32 v5, v10, v11\n\t" \
  "v_mfma_f32_16x16x32_bf16 a[48:51], a[160:163], v[136:139], a[48:51]\n\t" \
  "v_exp_f32 v12, v12\n\t" \
  "v_mfma_f32_16x16x32_bf16 a[48:51], a[164:167], v[140:143], a[48:51]\n\t" \
  "v_exp_f32 v13, v13\n\t" \
  "v_mfma_f32_16x16x32_bf16 a[52:55], a[168:171], v[136:139], a[52:55]\n\t" \
  "ds_read_b64_tr_b16 v[58:59], v211 offset:4352\n\t" \
  "v_cvt_pk_bf16_f32 v6, v12, v13\n\t" \
  "v_mfma_f32_16x16x32_bf16 a[52:55], a[172:175], v[140:143], a[52:55]\n\t" \
  "ds_read_b64_tr_b16 v[60:61], v211 offset:8448\n\t" \
  "v_mfma_f32_16x16x32_bf16 a[56:59], a[176:179], v[136:139], a[56:59]\n\t" \
  "v_exp_f32 v14, v14\n\t" \
  "v_mfma_f32_16x16x32_bf16 a[56:59], a[180:183], v[140:143], a[56:59]\n\t" \
  "v_exp_f32 v15, v15\n\t" \
  "v_mfma_f32_16x16x32_bf16 a[60:63], a[184:187], v[136:139], a[60:63]\n\t" \
  "ds_read_b64_tr_b16 v[62:63], v211 offset:12544\n\t" \
  "v_cvt_pk_bf16_f32 v7, v14, v15\n\t" \
  "v_mfma_f32_16x16x32_bf16 a[60:63], a[188:191], v[140:143], a[60:63]\n\t" \
  "v_exp_f32 v32, v32\n\t" \
  "v_smfmac_f32_16x16x64_bf16 v[164:167], v[216:219], v[136:143], v208\n\t" \
  "ds_read_b64_tr_b16 v[80:81], v210 offset:512\n\t" \
  "v_mfma_f32_16x16x32_bf16 a[64:67], a[128:131], v[144:147], a[64:67]\n\t" \
  "v_exp_f32 v33, v33\n\t" \
  "v_mfma_f32_16x16x32_bf16 a[64:67], a[132:135], v[148:151], a[64:67]\n\t" \
  "ds_read_b64_tr_b16 v[82:83], v210 offset:4608\n\t" \
  "v_cvt_pk_bf16_f32 v32, v32, v33\n\t" \
  "v_mfma_f32_16x16x32_bf16 a[68:71], a[136:139], v[144:147], a[68:71]\n\t" \
  "v_exp_f32 v34, v34\n\t" \
  "v_mfma_f32_16x16x32_bf16 a[68:71], a[140:143], v[148:151], a[68:71]\n\t" \
  "ds_read_b64_tr_b16 v[84:85], v210 offset:8704\n\t" \
  "v_mfma_f32_16x16x32_bf16 a[72:75], a[144:147], v[144:147], a[72:75]\n\t" \
  "v_exp_f32 v35, v35\n\t" \
  "v_mfma_f32_16x16x32_bf16 a[72:75], a[148:151], v[148:151], a[72:75]\n\t" \
  "ds_read_b64_tr_b16 v[86:87], v210 offset:12800\n\t" \
  "v_cvt_pk_bf16_f32 v33, v34, v35\n\t" \
  "v_mfma_f32_16x16x32_bf16 a[76:79], a[152:155], v[144:147], a[76:79]\n\t" \
  "v_exp_f32 v36, v36\n\t" \
  "v_mfma_f32_16x16x32_bf16 a[76:79], a[156:159], v[148:151], a[76:79]\n\t" \
  "ds_read_b64_tr_b16 v[88:89], v211 offset:512\n\t" \
  "v_mfma_f32_16x16x32_bf16 a[80:83], a[160:163], v[144:147], a[80:83]\n\t" \
  "v_exp_f32 v37, v37\n\t" \
  "v_mfma_f32_16x16x32_bf16 a[80:83], a[164:167], v[148:151], a[80:83]\n\t" \
  "ds_read_b64_tr_b16 v[90:91], v211 offset:4608\n\t" \
  "v_cvt_pk_bf16_f32 v34, v36, v37\n\t" \
  "v_mfma_f32_16x16x32_bf16 a[84:87], a[168:171], v[144:147], a[84:87]\n\t" \
  "v_exp_f32 v38, v38\n\t" \
  "v_mfma_f32_16x16x32_bf16 a[84:87], a[172:175], v[148:151], a[84:87]\n\t" \
  "v_exp_f32 v39, v39\n\t" \
  "v_mfma_f32_16x16x32_bf16 a[88:91], a[176:179], v[144:147], a[88:91]\n\t" \
  "ds_read_b64_tr_b16 v[92:93], v211 offset:8704\n\t" \
  "v_cvt_pk_bf16_f32 v35, v38, v39\n\t" \
  "v_mfma_f32_16x16x32_bf16 a[88:91], a[180:183], v[148:151], a[88:91]\n\t" \
  "v_exp_f32 v40, v40\n\t" \
  "v_mfma_f32_16x16x32_bf16 a[92:95], a[184:187], v[144:147], a[92:95]\n\t" \
  "ds_read_b64_tr_b16 v[94:95], v211 offset:12800\n\t" \
  "v_mfma_f32_16x16x32_bf16 a[92:95], a[188:191], v[148:151], a[92:95]\n\t" \
  "v_exp_f32 v41, v41\n\t" \
  "v_smfmac_f32_16x16x64_bf16 v[168:171], v[216:219], v[144:151], v208\n\t" \
  "ds_read_b64_tr_b16 v[112:113], v210 offset:768\n\t" \
  "v_cvt_pk_bf16_f32 v36, v40, v41\n\t" \
  "v_mfma_f32_16x16x32_bf16 a[96:99], a[128:131], v[152:155], a[96:99]\n\t" \
  "ds_read_b64_tr_b16 v[114:115], v210 offset:4864\n\t" \
  "v_mfma_f32_16x16x32_bf16 a[96:99], a[132:135], v[156:159], a[96:99]\n\t" \
  "v_exp_f32 v42, v42\n\t" \
  "v_mfma_f32_16x16x32_bf16 a[100:103], a[136:139], v[152:155], a[100:103]\n\t" \
  "ds_read_b64_tr_b16 v[116:117], v210 offset:8960\n\t" \
  "v_mfma_f32_16x16x32_bf16 a[100:103], a[140:143], v[156:159], a[100:103]\n\t" \
  "v_exp_f32 v43, v43\n\t" \
  "v_mfma_f32_16x16x32_bf16 a[104:107], a[144:147], v[152:155], a[104:107]\n\t" \
  "ds_read_b64_tr_b16 v[118:119], v210 offset:13056\n\t" \
  "v_cvt_pk_bf16_f32 v37, v42, v43\n\t" \
  "v_mfma_f32_16x16x32_bf16 a[104:107], a[148:151], v[156:159], a[104:107]\n\t" \
  "v_exp_f32 v44, v44\n\t" \
  "v_mfma_f32_16x16x32_bf16 a[108:111], a[152:155], v[152:155], a[108:111]\n\t" \
  "ds_read_b64_tr_b16 v[120:121], v211 offset:768\n\t" \
  "v_mfma_f32_16x16x32_bf16 a[108:111], a[156:159], v[156:159], a[108:111]\n\t" \
  "v_exp_f32 v45, v45\n\t" \
  "v_mfma_f32_16x16x32_bf16 a[112:115], a[160:163], v[152:155], a[112:115]\n\t" \
  "ds_read_b64_tr_b16 v[122:123], v211 offset:4864\n\t" \
  "v_cvt_pk_bf16_f32 v38, v44, v45\n\t" \
  "v_mfma_f32_16x16x32_bf16 a[112:115], a[164:167], v[156:159], a[112:115]\n\t" \
  "v_exp_f32 v46, v46\n\t" \
  "v_mfma_f32_16x16x32_bf16 a[116:119], a[168:171], v[152:155], a[116:119]\n\t" \
  "v_exp_f32 v47, v47\n\t" \
  "v_mfma_f32_16x16x32_bf16 a[116:119], a[172:175], v[156:159], a[116:119]\n\t" \
  "ds_read_b64_tr_b16 v[124:125], v211 offset:8960\n\t" \
  "v_cvt_pk_bf16_f32 v39, v46, v47\n\t" \
  "v_mfma_f32_16x16x32_bf16 a[120:123], a[176:179], v[152:155], a[120:123]\n\t" \
  "v_exp_f32 v64, v64\n\t" \
  "v_mfma_f32_16x16x32_bf16 a[120:123], a[180:183], v[156:159], a[120:123]\n\t" \
  "ds_read_b64_tr_b16 v[126:127], v211 offset:13056\n\t" \
  "v_mfma_f32_16x16x32_bf16 a[124:127], a[184:187], v[152:155], a[124:127]\n\t" \
  "v_exp_f32 v65, v65\n\t" \
  "v_mfma_f32_16x16x32_bf16 a[124:127], a[188:191], v[156:159], a[124:127]\n\t" \
  "v_exp_f32 v66, v66\n\t" \
  "v_smfmac_f32_16x16x64_bf16 v[172:175], v[216:219], v[152:159], v208\n\t" \
  "s_waitcnt lgkmcnt(15)\n\t" \
  "s_cmp_lg_u32 s99, 0\n\t" \
  "s_cbranch_scc1 .Lsf_p7\n\t" \
  ".Lsf_q7:\n\t" \
  "v_mfma_f32_16x16x32_bf16 a[0:3], v[16:19], v[0:3], a[0:3]\n\t" \
  "v_exp_f32 v67, v67\n\t" \
  "v_mfma_f32_16x16x32_bf16 a[0:3], v[20:23], v[4:7], a[0:3]\n\t" \
  "v_exp_f32 v68, v68\n\t" \
  "v_smfmac_f32_16x16x64_bf16 v[160:163], v[216:219], v[0:7], v208\n\t" \
  "v_cvt_pk_bf16_f32 v64, v64, v65\n\t" \
  "v_mfma_f32_16x16x32_bf16 a[4:7], v[24:27], v[0:3], a[4:7]\n\t" \
  "v_exp_f32 v69, v69\n\t" \
  "v_mfma_f32_16x16x32_bf16 a[4:7], v[28:31], v[4:7], a[4:7]\n\t" \
  "v_exp_f32 v70, v70\n\t" \
  "v_mfma_f32_16x16x32_bf16 a[8:11], v[48:51], v[0:3], a[8:11]\n\t" \
  "v_exp_f32 v71, v71\n\t" \
  "v_mfma_f32_16x16x32_bf16 a[8:11], v[52:55], v[4:7], a[8:11]\n\t" \
  "v_exp_f32 v72, v72\n\t" \
  "v_mfma_f32_16x16x32_bf16 a[12:15], v[56:59], v[0:3], a[12:15]\n\t" \
  "v_exp_f32 v73, v73\n\t" \
  "v_mfma_f32_16x16x32_bf16 a[12:15], v[60:63], v[4:7], a[12:15]\n\t" \
  "s_waitcnt lgkmcnt(8)\n\t" \
  "v_exp_f32 v74, v74\n\t" \
  "v_mfma_f32_16x16x32_bf16 a[16:19], v[80:83], v[0:3], a[16:19]\n\t" \
  "v_exp_f32 v75, v75\n\t" \
  "v_mfma_f32_16x16x32_bf16 a[16:19], v[84:87], v[4:7], a[16:19]\n\t" \
  "v_cvt_pk_bf16_f32 v65, v66, v67\n\t" \
  "v_cvt_pk_bf16_f32 v66, v68, v69\n\t" \
  "v_mfma_f32_16x16x32_bf16 a[20:23], v[88:91], v[0:3], a[20:23]\n\t" \
  "v_exp_f32 v76, v76\n\t" \
  "v_mfma_f32_16x16x32_bf16 a[20:23], v[92:95], v[4:7], a[20:23]\n\t" \
  "s_waitcnt lgkmcnt(0)\n\t" \
  "v_exp_f32 v77, v77\n\t" \
  "v_mfma_f32_16x16x32_bf16 a[24:27], v[112:115], v[0:3], a[24:27]\n\t" \
  "v_cvt_pk_bf16_f32 v67, v70, v71\n\t" \
  "v_cvt_pk_bf16_f32 v68, v72, v73\n\t" \
  "v_mfma_f32_16x16x32_bf16 a[24:27], v[116:119], v[4:7], a[24:27]\n\t" \
  "v_exp_f32 v78, v78\n\t" \
  "v_mfma_f32_16x16x32_bf16 a[28:31], v[120:123], v[0:3], a[28:31]\n\t" \
  "v_exp_f32 v79, v79\n\t" \
  "v_mfma_f32_16x16x32_bf16 a[28:31], v[124:127], v[4:7], a[28:31]\n\t" \
  "v_cvt_pk_bf16_f32 v69, v74, v75\n\t" \
  "v_cvt_pk_bf16_f32 v70, v76, v77\n\t" \
  "v_mfma_f32_16x16x32_bf16 a[32:35], v[16:19], v[32:35], a[32:35]\n\t" \
  "v_exp_f32 v96, v96\n\t" \
  "v_mfma_f32_16x16x32_bf16 a[32:35], v[20:23], v[36:39], a[32:35]\n\t" \
  "v_exp_f32 v97, v97\n\t" \
  "v_smfmac_f32_16x16x64_bf16 v[164:167], v[216:219], v[32:39], v208\n\t" \
  "v_cvt_pk_bf16_f32 v71, v78, v79\n\t" \
  "v_mfma_f32_16x16x32_bf16 a[36:39], v[24:27], v[32:35], a[36:39]\n\t" \
  "v_exp_f32 v98, v98\n\t" \
  "v_mfma_f32_16x16x32_bf16 a[36:39], v[28:31], v[36:39], a[36:39]\n\t" \
  "v_exp_f32 v99, v99\n\t" \
  "v_mfma_f32_16x16x32_bf16 a[40:43], v[48:51], v[32:35], a[40:43]\n\t" \
  "v_exp_f32 v100, v100\n\t" \
  "v_mfma_f32_16x16x32_bf16 a[40:43], v[52:55], v[36:39], a[40:43]\n\t" \
  "v_exp_f32 v101, v101\n\t" \
  "v_mfma_f32_16x16x32_bf16 a[44:47], v[56:59], v[32:35], a[44:47]\n\t" \
  "v_exp_f32 v102, v102\n\t" \
  "v_mfma_f32_16x16x32_bf16 a[44:47], v[60:63], v[36:39], a[44:47]\n\t" \
  "v_exp_f32 v103, v103\n\t" \
  "v_mfma_f32_16x16x32_bf16 a[48:51], v[80:83], v[32:35], a[48:51]\n\t" \
  "v_exp_f32 v104, v104\n\t" \
  "v_mfma_f32_16x16x32_bf16 a[48:51], v[84:87], v[36:39], a[48:51]\n\t" \
  "v_exp_f32 v105, v105\n\t" \
  "v_mfma_f32_16x16x32_bf16 a[52:55], v[88:91], v[32:35], a[52:55]\n\t" \
  "v_cvt_pk_bf16_f32 v96, v96, v97\n\t" \
  "v_cvt_pk_bf16_f32 v97, v98, v99\n\t" \
  "v_mfma_f32_16x16x32_bf16 a[52:55], v[92:95], v[36:39], a[52:55]\n\t" \
  "v_exp_f32 v106, v106\n\t" \
  "v_mfma_f32_16x16x32_bf16 a[56:59], v[112:115], v[32:35], a[56:59]\n\t" \
  "v_exp_f32 v107, v107\n\t" \
  "v_mfma_f32_16x16x32_bf16 a[56:59], v[116:119], v[36:39], a[56:59]\n\t" \
  "v_cvt_pk_bf16_f32 v98, v100, v101\n\t" \
  "v_cvt_pk_bf16_f32 v99, v102, v103\n\t" \
  "v_mfma_f32_16x16x32_bf16 a[60:63], v[120:123], v[32:35], a[60:63]\n\t" \
  "v_exp_f32 v108, v108\n\t" \
  "v_mfma_f32_16x16x32_bf16 a[60:63], v[124:127], v[36:39], a[60:63]\n\t" \
  "v_exp_f32 v109, v109\n\t" \
  "v_mfma_f32_16x16x32_bf16 a[64:67], v[16:19], v[64:67], a[64:67]\n\t" \
  "v_cvt_pk_bf16_f32 v100, v104, v105\n\t" \
  "v_cvt_pk_bf16_f32 v101, v106, v107\n\t" \
  "v_mfma_f32_16x16x32_bf16 a[64:67], v[20:23], v[68:71], a[64:67]\n\t" \
  "v_exp_f32 v110, v110\n\t" \
  "v_smfmac_f32_16x16x64_bf16 v[168:171], v[216:219], v[64:71], v208\n\t" \
  "v_cvt_pk_bf16_f32 v102, v108, v109\n\t" \
  "v_mfma_f32_16x16x32_bf16 a[68:71], v[24:27], v[64:67], a[68:71]\n\t" \
  "v_exp_f32 v111, v111\n\t" \
  "v_mfma_f32_16x16x32_bf16 a[68:71], v[28:31], v[68:71], a[68:71]\n\t" \
  "v_cvt_pk_bf16_f32 v103, v110, v111\n\t" \
  "v_mfma_f32_16x16x32_bf16 a[72:75], v[48:51], v[64:67], a[72:75]\n\t" \
  "v_mfma_f32_16x16x32_bf16 a[72:75], v[52:55], v[68:71], a[72:75]\n\t" \
  "v_mfma_f32_16x16x32_bf16 a[76:79], v[56:59], v[64:67], a[76:79]\n\t" \
  "v_mfma_f32_16x16x32_bf16 a[76:79], v[60:63], v[68:71], a[76:79]\n\t" \
  "v_mfma_f32_16x16x32_bf16 a[80:83], v[80:83], v[64:67], a[80:83]\n\t" \
  "v_mfma_f32_16x16x32_bf16 a[80:83], v[84:87], v[68:71], a[80:83]\n\t" \
  "v_mfma_f32_16x16x32_bf16 a[84:87], v[88:91], v[64:67], a[84:87]\n\t" \
  "v_mfma_f32_16x16x32_bf16 a[84:87], v[92:95], v[68:71], a[84:87]\n\t" \
  "v_mfma_f32_16x16x32_bf16 a[88:91], v[112:115], v[64:67], a[88:91]\n\t" \
  "v_mfma_f32_16x16x32_bf16 a[88:91], v[116:119], v[68:71], a[88:91]\n\t" \
  "v_mfma_f32_16x16x32_bf16 a[92:95], v[120:123], v[64:67], a[92:95]\n\t" \
  "v_mfma_f32_16x16x32_bf16 a[92:95], v[124:127], v[68:71], a[92:95]\n\t" \
  "v_mfma_f32_16x16x32_bf16 a[96:99], v[16:19], v[96:99], a[96:99]\n\t" \
  "v_mfma_f32_16x16x32_bf16 a[96:99], v[20:23], v[100:103], a[96:99]\n\t" \
  "v_smfmac_f32_16x16x64_bf16 v[172:175], v[216:219], v[96:103], v208\n\t" \
  "v_mfma_f32_16x16x32_bf16 a[100:103], v[24:27], v[96:99], a[100:103]\n\t" \
  "v_mfma_f32_16x16x32_bf16 a[100:103], v[28:31], v[100:103], a[100:103]\n\t" \
  "v_mfma_f32_16x16x32_bf16 a[104:107], v[48:51], v[96:99], a[104:107]\n\t" \
  "v_mfma_f32_16x16x32_bf16 a[104:107], v[52:55], v[100:103], a[104:107]\n\t" \
  "v_mfma_f32_16x16x32_bf16 a[108:111], v[56:59], v[96:99], a[108:111]\n\t" \
  "v_mfma_f32_16x16x32_bf16 a[108:111], v[60:63], v[100:103], a[108:111]\n\t" \
  "v_mfma_f32_16x16x32_bf16 a[112:115], v[80:83], v[96:99], a[112:115]\n\t" \
  "v_mfma_f32_16x16x32_bf16 a[112:115], v[84:87], v[100:103], a[112:115]\n\t" \
  "v_mfma_f32_16x16x32_bf16 a[116:119], v[88:91], v[96:99], a[116:119]\n\t" \
  "v_mfma_f32_16x16x32_bf16 a[116:119], v[92:95], v[100:103], a[116:119]\n\t" \
  "v_mfma_f32_16x16x32_bf16 a[120:123], v[112:115], v[96:99], a[120:123]\n\t" \
  "v_mfma_f32_16x16x32_bf16 a[120:123], v[116:119], v[100:103], a[120:123]\n\t" \
  "v_mfma_f32_16x16x32_bf16 a[124:127], v[120:123], v[96:99], a[124:127]\n\t" \
  "v_mfma_f32_16x16x32_bf16 a[124:127], v[124:127], v[100:103], a[124:127]\n\t" \
  "s_nop 7\n\t" \
  "s_waitcnt vmcnt(0)\n\t" \
  "s_barrier\n\t" \
  "s_waitcnt vmcnt(0) lgkmcnt(0)\n\t" \
  "s_barrier\n\t" \
  "s_add_u32 s77, s51, 131072\n\t" \
  "s_mov_b32 s89, 0\n\t" \
  "s_brev_b32 s78, 1\n\t" \
  "s_mov_b32 s79, 0x0f800000\n\t" \
  "s_waitcnt vmcnt(0) lgkmcnt(0)\n\t" \
  "s_barrier\n\t" \
  "s_waitcnt vmcnt(0) lgkmcnt(0)\n\t" \
  "s_barrier\n\t" \
  "s_waitcnt vmcnt(0) lgkmcnt(0)\n\t" \
  "s_barrier\n\t" \
  "s_add_u32 m0, s38, 16384\n\t" \
  "s_mov_b32 s96, 0\n\t" \
  "buffer_load_dwordx4 v232, s[68:71], s96 offen offset:0 lds\n\t" \
  "s_mov_b32 s96, 6144\n\t" \
  "buffer_load_dwordx4 v232, s[68:71], s96 offen offset:1024 lds\n\t" \
  "s_mov_b32 s96, 12288\n\t" \
  "buffer_load_dwordx4 v232, s[68:71], s96 offen offset:2048 lds\n\t" \
  "s_mov_b32 s96, 18432\n\t" \
  "buffer_load_dwordx4 v232, s[68:71], s96 offen offset:3072 lds\n\t" \
  "s_waitcnt vmcnt(0) lgkmcnt(0)\n\t" \
  "s_barrier\n\t" \
  "s_add_u32 m0, s38, 32768\n\t" \
  "s_mov_b32 s96, 28672\n\t" \
  "buffer_load_dwordx4 v232, s[68:71], s96 offen offset:0 lds\n\t" \
  "s_mov_b32 s96, 34816\n\t" \
  "buffer_load_dwordx4 v232, s[68:71], s96 offen offset:1024 lds\n\t" \
  "s_add_u32 m0, s38, 32768\n\t" \
  "s_mov_b32 s96, 40960\n\t" \
  "buffer_load_dwordx4 v232, s[68:71], s96 offen offset:2048 lds\n\t" \
  "s_mov_b32 s96, 47104\n\t" \
  "buffer_load_dwordx4 v232, s[68:71], s96 offen offset:3072 lds\n\t" \
  "s_add_u32 m0, s38, 114688\n\t" \
  "s_mov_b32 s96, 0\n\t" \
  "buffer_load_dwordx4 v232, s[72:75], s96 offen offset:0 lds\n\t" \
  "s_mov_b32 s96, 6144\n\t" \
  "buffer_load_dwordx4 v232, s[72:75], s96 offen offset:1024 lds\n\t" \
  "s_waitcnt vmcnt(0) lgkmcnt(0)\n\t" \
  "s_barrier\n\t" \
  "s_add_u32 m0, s38, 114688\n\t" \
  "s_mov_b32 s96, 12288\n\t" \
  "buffer_load_dwordx4 v232, s[72:75], s96 offen offset:2048 lds\n\t" \
  "s_mov_b32 s96, 18432\n\t" \
  "buffer_load_dwordx4 v232, s[72:75], s96 offen offset:3072 lds\n\t" \
  "s_add_u32 m0, s38, 131072\n\t" \
  "s_mov_b32 s96, 28672\n\t" \
  "buffer_load_dwordx4 v232, s[72:75], s96 offen offset:0 lds\n\t" \
  "s_mov_b32 s96, 34816\n\t" \
  "buffer_load_dwordx4 v232, s[72:75], s96 offen offset:1024 lds\n\t" \
  "s_mov_b32 s96, 40960\n\t" \
  "buffer_load_dwordx4 v232, s[72:75], s96 offen offset:2048 lds\n\t" \
  "s_mov_b32 s96, 47104\n\t" \
  "buffer_load_dwordx4 v232, s[72:75], s96 offen offset:3072 lds\n\t" \
  "s_waitcnt vmcnt(0) lgkmcnt(0)\n\t" \
  "s_barrier\n\t" \
  "s_waitcnt vmcnt(0) lgkmcnt(0)\n\t" \
  "s_barrier\n\t" \
  "buffer_load_dword v209, v251, s[16:19], 0 offen sc1\n\t" \
  "ds_read_b128 v[192:195], v215 offset:32768\n\t" \
  "ds_read_b128 v[196:199], v215 offset:33792\n\t" \
  "s_waitcnt vmcnt(0) lgkmcnt(0)\n\t" \
  "v_and_b32 v250, 0xbfffbfff, v209\n\t" \
  "v_cmp_ne_u32_e64 s[84:85], s36, v250\n\t" \
  "s_cmp_lg_u64 s[84:85], 0\n\t" \
  "s_cbranch_scc1 .Lsf_v\n\t" \
  ".Lsf_w:\n\t" \
  "v_and_b32 v250, 1, v233\n\t" \
  "v_lshlrev_b32 v250, 4, v250\n\t" \
  "v_lshrrev_b32 v250, v250, v209\n\t" \
  "v_and_b32 v250, 0x4000, v250\n\t" \
  "v_cmp_ne_u32_e64 s[84:85], 0, v250\n\t" \
  "s_bcnt1_i32_b32 s98, s84\n\t" \
  "s_bcnt1_i32_b32 s54, s85\n\t" \
  "s_cmp_gt_u32 s98, s54\n\t" \
  "s_cselect_b32 s55, 0x2000, 0\n\t" \
  "s_cselect_b32 s98, s84, s85\n\t" \
  "s_cmp_lg_u32 s55, 0\n\t" \
  "s_cselect_b32 s84, s85, s84\n\t" \
  "s_lshr_b32 s85, s98, 16\n\t" \
  "s_bcnt1_i32_b32 s85, s85\n\t" \
  "s_lshr_b32 s84, s84, 16\n\t" \
  "s_bcnt1_i32_b32 s84, s84\n\t" \
  "s_add_u32 s84, s84, 7\n\t" \
  "s_cmp_ge_u32 s85, s84\n\t" \
  "s_cselect_b32 s54, -1, 0\n\t" \
  "s_xor_b32 s98, s42, s55\n\t" \
  "s_and_b32 s98, s98, s54\n\t" \
  "s_xor_b32 s45, s45, s98\n\t" \
  "s_xor_b32 s46, s46, s98\n\t" \
  "s_xor_b32 s98, s43, s55\n\t" \
  "s_and_b32 s98, s98, s54\n\t" \
  "s_xor_b32 s47, s47, s98\n\t" \
  "s_xor_b32 s48, s48, s55\n\t" \
  "s_xor_b32 s49, s49, s55\n\t" \
  "s_xor_b32 s98, s45, 0\n\t" \
  "s_waitcnt vmcnt(0) lgkmcnt(0)\n\t" \
  "s_barrier\n\t" \
  "ds_read_b128 v[200:203], v215 offset:34816\n\t" \
  "s_lshl_b32 s64, s39, 17\n\t" \
  "s_add_u32 s96, s40, 0\n\t" \
  "s_and_b32 s96, s96, 0x1c0000\n\t" \
  "s_add_u32 s96, s96, s51\n\t" \
  "s_add_u32 m0, s38, 0\n\t" \
  "s_nop 0\n\t" \
  "buffer_load_dwordx4 v240, s[24:27], s96 offen offset:0 lds\n\t" \
  "s_bfe_u32 s70, s46, 0x20014\n\t" \
  "s_lshl_b32 s70, s70, 14\n\t" \
  "s_xor_b32 s67, s70, 49152\n\t" \
  "buffer_load_dwordx4 v241, s[24:27], s96 offen offset:1024 lds\n\t" \
  "s_lshl1_add_u32 s60, s67, s46\n\t" \
  "s_xor_b32 s68, s70, 32768\n\t" \
  "buffer_load_dwordx4 v248, s[24:27], s96 offen offset:2048 lds\n\t" \
  "s_lshl1_add_u32 s61, s68, s46\n\t" \
  "s_xor_b32 s69, s70, 16384\n\t" \
  "s_lshl1_add_u32 s62, s69, s46\n\t" \
  "buffer_load_dwordx4 v249, s[24:27], s96 offen offset:3072 lds\n\t" \
  "s_lshl1_add_u32 s63, s70, s46\n\t" \
  "s_add_u32 s96, s40, 262144\n\t" \
  "s_and_b32 s96, s96, 0x1c0000\n\t" \
  "s_add_u32 s96, s96, s51\n\t" \
  "s_add_u32 m0, s38, 49152\n\t" \
  "s_nop 0\n\t" \
  "buffer_load_dwordx4 v240, s[24:27], s96 offen offset:0 lds\n\t" \
  "s_mul_i32 s65, s39, 508\n\t" \
  "s_add_u32 s65, s65, s49\n\t" \
  "buffer_load_dwordx4 v241, s[24:27], s96 offen offset:1024 lds\n\t" \
  "s_lshl_b32 s80, s39, 2\n\t" \
  "s_add_u32 s80, s80, s3\n\t" \
  "s_lshl_b32 s80, s80, 6\n\t" \
  "buffer_load_dwordx4 v248, s[24:27], s96 offen offset:2048 lds\n\t" \
  "s_add_u32 s76, s38, 147456\n\t" \
  "s_lshl_b32 s75, s80, 2\n\t" \
  "s_add_u32 s75, s75, 2228224\n\t" \
  "buffer_load_dwordx4 v249, s[24:27], s96 offen offset:3072 lds\n\t" \
  "s_lshr_b32 s71, s67, 7\n\t" \
  "s_add_u32 s71, s71, s48\n\t" \
  "s_lshr_b32 s72, s68, 7\n\t" \
  "s_add_u32 s72, s72, s48\n\t" \
  "s_lshr_b32 s66, s69, 7\n\t" \
  "ds_read_b128 v[204:207], v215 offset:35840\n\t" \
  "s_add_u32 s66, s66, s48\n\t" \
  "s_lshr_b32 s73, s70, 7\n\t" \
  "s_add_u32 s73, s73, s48\n\t" \
  "s_lshl3_add_u32 s67, s67, s47\n\t" \
  "s_lshl3_add_u32 s68, s68, s47\n\t" \
  "s_add_u32 s68, s68, 4096\n\t" \
  "s_lshl3_add_u32 s69, s69, s47\n\t" \
  "s_add_u32 s69, s69, 16384\n\t" \
  "s_lshl3_add_u32 s70, s70, s47\n\t" \
  "s_waitcnt vmcnt(0) lgkmcnt(0)\n\t" \
  "s_barrier\n\t" \
  "s_add_u32 s96, s40, 0\n\t" \
  "s_and_b32 s96, s96, 0x1c0000\n\t" \
  "s_add_u32 s96, s96, s77\n\t" \
  "s_add_u32 m0, s38, 98304\n\t" \
  "s_nop 0\n\t" \
  "buffer_load_dwordx4 v240, s[24:27], s96 offen offset:0 lds\n\t" \
  "buffer_load_dwordx4 v241, s[24:27], s96 offen offset:1024 lds\n\t" \
  "buffer_load_dwordx4 v248, s[24:27], s96 offen offset:2048 lds\n\t" \
  "buffer_load_dwordx4 v249, s[24:27], s96 offen offset:3072 lds\n\t" \
  "s_add_u32 s96, s40, 524288\n\t" \
  "s_and_b32 s96, s96, 0x1c0000\n\t" \
  "s_add_u32 s96, s96, s51\n\t" \
  "s_add_u32 m0, s38, 131072\n\t" \
  "s_waitcnt vmcnt(0) lgkmcnt(0)\n\t" \
  "buffer_load_dwordx4 v240, s[24:27], s96 offen offset:0 lds\n\t" \
  "buffer_load_dwordx4 v241, s[24:27], s96 offen offset:1024 lds\n\t" \
  "buffer_load_dwordx4 v248, s[24:27], s96 offen offset:2048 lds\n\t" \
  "buffer_load_dwordx4 v249, s[24:27], s96 offen offset:3072 lds\n\t" \
  "s_add_u32 s96, s40, 262144\n\t" \
  "s_and_b32 s96, s96, 0x1c0000\n\t" \
  "s_add_u32 s96, s96, s77\n\t" \
  "s_add_u32 m0, s38, 65536\n\t" \
  "s_nop 0\n\t" \
  "buffer_load_dwordx4 v240, s[24:27], s96 offen offset:0 lds\n\t" \
  "buffer_load_dwordx4 v241, s[24:27], s96 offen offset:1024 lds\n\t" \
  "buffer_load_dwordx4 v248, s[24:27], s96 offen offset:2048 lds\n\t" \
  "buffer_load_dwordx4 v249, s[24:27], s96 offen offset:3072 lds\n\t" \
  "s_add_u32 s96, s40, 524288\n\t" \
  "s_and_b32 s96, s96, 0x1c0000\n\t" \
  "s_add_u32 s96, s96, s77\n\t" \
  "s_add_u32 m0, s38, 81920\n\t" \
  "s_waitcnt vmcnt(0) lgkmcnt(0)\n\t" \
  "s_barrier\n\t" \
  "buffer_load_dwordx4 v240, s[24:27], s96 offen offset:0 lds\n\t" \
  "ds_read_b128 v[176:179], v215 offset:16384\n\t" \
  "ds_read_b128 v[180:183], v215 offset:17408\n\t" \
  "ds_read_b128 v[184:187], v215 offset:18432\n\t" \
  "buffer_load_dwordx4 v241, s[24:27], s96 offen offset:1024 lds\n\t" \
  "ds_read_b128 v[188:191], v215 offset:19456\n\t" \
  "buffer_load_dwordx4 v248, s[24:27], s96 offen offset:2048 lds\n\t" \
  "buffer_load_dwordx4 v249, s[24:27], s96 offen offset:3072 lds\n\t" \
  "s_add_u32 s96, s40, 786432\n\t" \
  "s_and_b32 s96, s96, 0x1c0000\n\t" \
  "s_add_u32 s96, s96, s51\n\t" \
  "s_add_u32 m0, s38, 114688\n\t" \
  "s_waitcnt vmcnt(0) lgkmcnt(0)\n\t" \
  "buffer_load_dwordx4 v240, s[24:27], s96 offen offset:0 lds\n\t" \
  "buffer_load_dwordx4 v241, s[24:27], s96 offen offset:1024 lds\n\t" \
  "buffer_load_dwordx4 v248, s[24:27], s96 offen offset:2048 lds\n\t" \
  "buffer_load_dwordx4 v249, s[24:27], s96 offen offset:3072 lds\n\t" \
  "v_add_u32 v230, 0x10000, v242\n\t" \
  "v_add_u32 v232, 0x20000, v242\n\t" \
  "ds_read_b64_tr_b16 a[192:193], v234 offset:0\n\t" \
  "v_add_f32 v225, v160, v161\n\t" \
  "v_rcp_f32 v224, v225\n\t" \
  "v_accvgpr_read_b32 v128, a0\n\t" \
  "v_accvgpr_read_b32 v129, a1\n\t" \
  "v_accvgpr_read_b32 v130, a2\n\t" \
  "v_accvgpr_read_b32 v131, a3\n\t" \
  "v_accvgpr_read_b32 v132, a4\n\t" \
  "v_accvgpr_read_b32 v133, a5\n\t" \
  "v_accvgpr_read_b32 v134, a6\n\t" \
  "v_accvgpr_read_b32 v135, a7\n\t" \
  "v_pk_mul_f32 v[128:129], v[128:129], v[224:225] op_sel_hi:[1,0]\n\t" \
  "v_pk_mul_f32 v[130:131], v[130:131], v[224:225] op_sel_hi:[1,0]\n\t" \
  "v_pk_mul_f32 v[132:133], v[132:133], v[224:225] op_sel_hi:[1,0]\n\t" \
  "v_pk_mul_f32 v[134:135], v[134:135], v[224:225] op_sel_hi:[1,0]\n\t" \
  "v_cvt_pk_bf16_f32 v128, v128, v129\n\t" \
  "v_cvt_pk_bf16_f32 v129, v130, v131\n\t" \
  "v_cvt_pk_bf16_f32 v130, v132, v133\n\t" \
  "v_cvt_pk_bf16_f32 v131, v134, v135\n\t" \
  "buffer_store_dwordx4 v[128:131], v242, s[16:19], s98 offen offset:0 sc1\n\t" \
  "ds_read_b64_tr_b16 a[194:195], v234 offset:4096\n\t" \
  "v_accvgpr_read_b32 v136, a8\n\t" \
  "v_accvgpr_read_b32 v137, a9\n\t" \
  "v_accvgpr_read_b32 v138, a10\n\t" \
  "v_accvgpr_read_b32 v139, a11\n\t" \
  "v_accvgpr_read_b32 v140, a12\n\t" \
  "v_accvgpr_read_b32 v141, a13\n\t" \
  "v_accvgpr_read_b32 v142, a14\n\t" \
  "v_accvgpr_read_b32 v143, a15\n\t" \
  "v_pk_mul_f32 v[136:137], v[136:137], v[224:225] op_sel_hi:[1,0]\n\t" \
  "v_pk_mul_f32 v[138:139], v[138:139], v[224:225] op_sel_hi:[1,0]\n\t" \
  "v_pk_mul_f32 v[140:141], v[140:141], v[224:225] op_sel_hi:[1,0]\n\t" \
  "v_pk_mul_f32 v[142:143], v[142:143], v[224:225] op_sel_hi:[1,0]\n\t" \
  "v_cvt_pk_bf16_f32 v136, v136, v137\n\t" \
  "v_cvt_pk_bf16_f32 v137, v138, v139\n\t" \
  "v_cvt_pk_bf16_f32 v138, v140, v141\n\t" \
  "v_cvt_pk_bf16_f32 v139, v142, v143\n\t" \
  "buffer_store_dwordx4 v[136:139], v242, s[16:19], s98 offen offset:1024 sc1\n\t" \
  "ds_read_b64_tr_b16 a[196:197], v235 offset:0\n\t" \
  "s_add_u32 s96, s40, 786432\n\t" \
  "ds_read_b64_tr_b16 a[198:199], v235 offset:4096\n\t" \
  "s_and_b32 s96, s96, 0x1c0000\n\t" \
  "ds_read_b64_tr_b16 a[200:201], v234 offset:8192\n\t" \
  "s_add_u32 s96, s96, s77\n\t" \
  "s_add_u32 m0, s38, 147456\n\t" \
  "s_waitcnt vmcnt(0) lgkmcnt(0)\n\t" \
  "s_barrier\n\t" \
  "v_accvgpr_read_b32 v0, a16\n\t" \
  "v_accvgpr_read_b32 v1, a17\n\t" \
  "v_accvgpr_read_b32 v2, a18\n\t" \
  "v_accvgpr_read_b32 v3, a19\n\t" \
  "v_accvgpr_read_b32 v4, a20\n\t" \
  "v_accvgpr_read_b32 v5, a21\n\t" \
  "v_accvgpr_read_b32 v6, a22\n\t" \
  "v_accvgpr_read_b32 v7, a23\n\t" \
  "v_pk_mul_f32 v[0:1], v[0:1], v[224:225] op_sel_hi:[1,0]\n\t" \
  "v_pk_mul_f32 v[2:3], v[2:3], v[224:225] op_sel_hi:[1,0]\n\t" \
  "v_pk_mul_f32 v[4:5], v[4:5], v[224:225] op_sel_hi:[1,0]\n\t" \
  "v_pk_mul_f32 v[6:7], v[6:7], v[224:225] op_sel_hi:[1,0]\n\t" \
  "v_cvt_pk_bf16_f32 v0, v0, v1\n\t" \
  "v_cvt_pk_bf16_f32 v1, v2, v3\n\t" \
  "v_cvt_pk_bf16_f32 v2, v4, v5\n\t" \
  "v_cvt_pk_bf16_f32 v3, v6, v7\n\t" \
  "buffer_store_dwordx4 v[0:3], v242, s[16:19], s98 offen offset:2048 sc1\n\t" \
  "v_accvgpr_read_b32 v8, a24\n\t" \
  "v_accvgpr_read_b32 v9, a25\n\t" \
  "v_accvgpr_read_b32 v10, a26\n\t" \
  "v_accvgpr_read_b32 v11, a27\n\t" \
  "v_accvgpr_read_b32 v12, a28\n\t" \
  "v_accvgpr_read_b32 v13, a29\n\t" \
  "v_accvgpr_read_b32 v14, a30\n\t" \
  "v_accvgpr_read_b32 v15, a31\n\t" \
  "v_pk_mul_f32 v[8:9], v[8:9], v[224:225] op_sel_hi:[1,0]\n\t" \
  "v_pk_mul_f32 v[10:11], v[10:11], v[224:225] op_sel_hi:[1,0]\n\t" \
  "v_pk_mul_f32 v[12:13], v[12:13], v[224:225] op_sel_hi:[1,0]\n\t" \
  "v_pk_mul_f32 v[14:15], v[14:15], v[224:225] op_sel_hi:[1,0]\n\t" \
  "v_cvt_pk_bf16_f32 v8, v8, v9\n\t" \
  "v_cvt_pk_bf16_f32 v9, v10, v11\n\t" \
  "v_cvt_pk_bf16_f32 v10, v12, v13\n\t" \
  "v_cvt_pk_bf16_f32 v11, v14, v15\n\t" \
  "buffer_store_dwordx4 v[8:11], v242, s[16:19], s98 offen offset:3072 sc1\n\t" \
  "buffer_load_dwordx4 v240, s[24:27], s96 offen offset:0 lds\n\t" \
  "s_xor_b32 s98, s45, 32768\n\t" \
  "ds_read_b64_tr_b16 a[202:203], v234 offset:12288\n\t" \
  "ds_read_b64_tr_b16 a[204:205], v235 offset:8192\n\t" \
  "ds_read_b128 a[224:227], v213 offset:16384\n\t" \
  "ds_read_b64_tr_b16 a[206:207], v235 offset:12288\n\t" \
  "ds_read_b128 a[228:231], v213 offset:17408\n\t" \
  "ds_read_b64_tr_b16 a[208:209], v236 offset:32768\n\t" \
  "ds_read_b128 a[232:235], v213 offset:18432\n\t" \
  "buffer_load_dwordx4 v241, s[24:27], s96 offen offset:1024 lds\n\t" \
  "ds_read_b128 a[236:239], v213 offset:19456\n\t" \
  "ds_write_b128 v213, v[128:131] offset:16384\n\t" \
  "buffer_load_dwordx4 v248, s[24:27], s96 offen offset:2048 lds\n\t" \
  "ds_write_b128 v213, v[136:139] offset:17408\n\t" \
  "ds_write_b128 v213, v[0:3] offset:18432\n\t" \
  "ds_write_b128 v213, v[8:11] offset:19456\n\t" \
  "buffer_load_dwordx4 v249, s[24:27], s96 offen offset:3072 lds\n\t" \
  "ds_read_b64_tr_b16 a[210:211], v236 offset:36864\n\t" \
  "ds_read_b64_tr_b16 a[212:213], v237 offset:32768\n\t" \
  "ds_read_b64_tr_b16 a[214:215], v237 offset:36864\n\t" \
  "v_add_f32 v227, v164, v165\n\t" \
  "v_rcp_f32 v226, v227\n\t" \
  "ds_read_b64_tr_b16 a[216:217], v236 offset:40960\n\t" \
  "v_accvgpr_read_b32 v0, a32\n\t" \
  "v_accvgpr_read_b32 v1, a33\n\t" \
  "v_accvgpr_read_b32 v2, a34\n\t" \
  "v_accvgpr_read_b32 v3, a35\n\t" \
  "v_accvgpr_read_b32 v4, a36\n\t" \
  "v_accvgpr_read_b32 v5, a37\n\t" \
  "v_accvgpr_read_b32 v6, a38\n\t" \
  "v_accvgpr_read_b32 v7, a39\n\t" \
  "v_mul_f32 v0, v0, v226\n\t" \
  "v_mul_f32 v1, v1, v226\n\t" \
  "v_mul_f32 v2, v2, v226\n\t" \
  "v_mul_f32 v3, v3, v226\n\t" \
  "v_mul_f32 v4, v4, v226\n\t" \
  "v_mul_f32 v5, v5, v226\n\t" \
  "v_mul_f32 v6, v6, v226\n\t" \
  "ds_read_b64_tr_b16 a[218:219], v236 offset:45056\n\t" \
  "v_mul_f32 v7, v7, v226\n\t" \
  "v_cvt_pk_bf16_f32 v32, v0, v1\n\t" \
  "v_cvt_pk_bf16_f32 v33, v2, v3\n\t" \
  "v_cvt_pk_bf16_f32 v34, v4, v5\n\t" \
  "v_cvt_pk_bf16_f32 v35, v6, v7\n\t" \
  "v_accvgpr_read_b32 v8, a40\n\t" \
  "v_accvgpr_read_b32 v9, a41\n\t" \
  "v_accvgpr_read_b32 v10, a42\n\t" \
  "v_accvgpr_read_b32 v11, a43\n\t" \
  "v_accvgpr_read_b32 v12, a44\n\t" \
  "v_accvgpr_read_b32 v13, a45\n\t" \
  "v_accvgpr_read_b32 v14, a46\n\t" \
  "v_accvgpr_read_b32 v15, a47\n\t" \
  "v_mul_f32 v8, v8, v226\n\t" \
  "v_mul_f32 v9, v9, v226\n\t" \
  "s_waitcnt vmcnt(0) lgkmcnt(0)\n\t" \
  "s_barrier\n\t" \
  "buffer_store_dwordx4 v[32:35], v242, s[16:19], s98 offen offset:0 sc1\n\t" \
  "v_mul_f32 v10, v10, v226\n\t" \
  "v_mul_f32 v11, v11, v226\n\t" \
  "v_mul_f32 v12, v12, v226\n\t" \
  "v_mul_f32 v13, v13, v226\n\t" \
  "v_mul_f32 v14, v14, v226\n\t" \
  "v_mul_f32 v15, v15, v226\n\t" \
  "v_cvt_pk_bf16_f32 v36, v8, v9\n\t" \
  "v_cvt_pk_bf16_f32 v37, v10, v11\n\t" \
  "v_cvt_pk_bf16_f32 v38, v12, v13\n\t" \
  "v_cvt_pk_bf16_f32 v39, v14, v15\n\t" \
  "v_accvgpr_read_b32 v0, a48\n\t" \
  "v_accvgpr_read_b32 v1, a49\n\t" \
  "v_accvgpr_read_b32 v2, a50\n\t" \
  "v_accvgpr_read_b32 v3, a51\n\t" \
  "v_accvgpr_read_b32 v4, a52\n\t" \
  "v_accvgpr_read_b32 v5, a53\n\t" \
  "v_accvgpr_read_b32 v6, a54\n\t" \
  "v_accvgpr_read_b32 v7, a55\n\t" \
  "v_mul_f32 v0, v0, v226\n\t" \
  "v_mul_f32 v1, v1, v226\n\t" \
  "v_mul_f32 v2, v2, v226\n\t" \
  "v_mul_f32 v3, v3, v226\n\t" \
  "v_mul_f32 v4, v4, v226\n\t" \
  "v_mul_f32 v5, v5, v226\n\t" \
  "v_mul_f32 v6, v6, v226\n\t" \
  "v_mul_f32 v7, v7, v226\n\t" \
  "v_cvt_pk_bf16_f32 v40, v0, v1\n\t" \
  "v_cvt_pk_bf16_f32 v41, v2, v3\n\t" \
  "v_cvt_pk_bf16_f32 v42, v4, v5\n\t" \
  "v_cvt_pk_bf16_f32 v43, v6, v7\n\t" \
  "buffer_store_dwordx4 v[36:39], v242, s[16:19], s98 offen offset:1024 sc1\n\t" \
  "v_accvgpr_read_b32 v8, a56\n\t" \
  "v_accvgpr_read_b32 v9, a57\n\t" \
  "v_accvgpr_read_b32 v10, a58\n\t" \
  "v_accvgpr_read_b32 v11, a59\n\t" \
  "v_accvgpr_read_b32 v12, a60\n\t" \
  "v_accvgpr_read_b32 v13, a61\n\t" \
  "v_accvgpr_read_b32 v14, a62\n\t" \
  "v_accvgpr_read_b32 v15, a63\n\t" \
  "v_mul_f32 v8, v8, v226\n\t" \
  "v_mul_f32 v9, v9, v226\n\t" \
  "v_mul_f32 v10, v10, v226\n\t" \
  "v_mul_f32 v11, v11, v226\n\t" \
  "v_mul_f32 v12, v12, v226\n\t" \
  "v_mul_f32 v13, v13, v226\n\t" \
  "v_mul_f32 v14, v14, v226\n\t" \
  "s_waitcnt vmcnt(0) lgkmcnt(0)\n\t" \
  "buffer_store_dword v243, v254, s[16:19], s49 offen offset:0 sc1\n\t" \
  "ds_read_b64_tr_b16 a[220:221], v237 offset:40960\n\t" \
  "ds_read_b64_tr_b16 a[222:223], v237 offset:45056\n\t" \
  "ds_read_b64_tr_b16 a[128:129], v234 offset:49152\n\t" \
  "v_mul_f32 v15, v15, v226\n\t" \
  "buffer_store_dwordx4 v[40:43], v242, s[16:19], s98 offen offset:2048 sc1\n\t" \
  "v_add_f32 v221, v168, v169\n\t" \
  "v_rcp_f32 v220, v221\n\t" \
  "v_cvt_pk_bf16_f32 v44, v8, v9\n\t" \
  "v_cvt_pk_bf16_f32 v45, v10, v11\n\t" \
  "ds_read_b64_tr_b16 a[130:131], v234 offset:53248\n\t" \
  "v_cvt_pk_bf16_f32 v46, v12, v13\n\t" \
  "v_cvt_pk_bf16_f32 v47, v14, v15\n\t" \
  "ds_read_b64_tr_b16 a[132:133], v235 offset:49152\n\t" \
  "ds_read_b128 a[240:243], v213 offset:32768\n\t" \
  "s_add_u32 s96, s40, 1048576\n\t" \
  "ds_read_b128 a[244:247], v213 offset:33792\n\t" \
  "s_and_b32 s96, s96, 0x1c0000\n\t" \
  "buffer_store_dwordx4 v[44:47], v242, s[16:19], s98 offen offset:3072 sc1\n\t" \
  "ds_read_b128 a[248:251], v213 offset:34816\n\t" \
  "ds_read_b128 a[252:255], v213 offset:35840\n\t" \
  "ds_read_b64_tr_b16 a[134:135], v235 offset:53248\n\t" \
  "ds_write_b128 v213, v[32:35] offset:32768\n\t" \
  "ds_write_b128 v213, v[36:39] offset:33792\n\t" \
  "s_add_u32 s96, s96, s51\n\t" \
  "s_add_u32 m0, s38, 98304\n\t" \
  "v_accvgpr_read_b32 v64, a64\n\t" \
  "v_accvgpr_read_b32 v65, a65\n\t" \
  "v_accvgpr_read_b32 v66, a66\n\t" \
  "v_accvgpr_read_b32 v67, a67\n\t" \
  "s_waitcnt vmcnt(0) lgkmcnt(0)\n\t" \
  "s_barrier\n\t" \
  "buffer_load_dwordx4 v240, s[24:27], s96 offen offset:0 lds\n\t" \
  "v_accvgpr_read_b32 v68, a68\n\t" \
  "v_accvgpr_read_b32 v69, a69\n\t" \
  "s_xor_b32 s98, s45, 65536\n\t" \
  "ds_read_b128 v[128:131], v242 offset:16384\n\t" \
  "v_accvgpr_read_b32 v70, a70\n\t" \
  "v_accvgpr_read_b32 v71, a71\n\t" \
  "ds_read_b128 v[132:135], v242 offset:17408\n\t" \
  "v_mul_f32 v64, v64, v220\n\t" \
  "v_add_f32 v223, v172, v173\n\t" \
  "v_rcp_f32 v222, v223\n\t" \
  "buffer_load_dwordx4 v241, s[24:27], s96 offen offset:1024 lds\n\t" \
  "ds_read_b128 v[136:139], v242 offset:18432\n\t" \
  "buffer_load_dwordx4 v248, s[24:27], s96 offen offset:2048 lds\n\t" \
  "v_mul_f32 v65, v65, v220\n\t" \
  "ds_write_b128 v213, v[40:43] offset:34816\n\t" \
  "buffer_load_dwordx4 v249, s[24:27], s96 offen offset:3072 lds\n\t" \
  "ds_read_b128 v[140:143], v242 offset:19456\n\t" \
  "v_mul_f32 v66, v66, v220\n\t" \
  "ds_read_b64_tr_b16 a[136:137], v234 offset:57344\n\t" \
  "ds_read_b128 v[144:147], v242 offset:20480\n\t" \
  "v_mul_f32 v67, v67, v220\n\t" \
  "ds_read_b128 v[148:151], v242 offset:21504\n\t" \
  "v_mul_f32 v68, v68, v220\n\t" \
  "ds_write_b128 v213, v[44:47] offset:35840\n\t" \
  "ds_read_b64_tr_b16 a[138:139], v234 offset:61440\n\t" \
  "ds_read_b128 v[152:155], v242 offset:22528\n\t" \
  "v_mul_f32 v69, v69, v220\n\t" \
  "v_mul_f32 v70, v70, v220\n\t" \
  "ds_read_b64_tr_b16 a[140:141], v235 offset:57344\n\t" \
  "ds_read_b128 v[156:159], v242 offset:23552\n\t" \
  "v_mul_f32 v71, v71, v220\n\t" \
  "s_waitcnt vmcnt(0) lgkmcnt(0)\n\t" \
  "s_nop 1\n\t" \
  "s_branch .Lat_join\n\t" \
  ".Lsf_c00:\n\t" \
  "s_mov_b32 vcc_lo, 0\n\t" \
  "s_branch .Lsf_s0\n\t" \
  ".Lsf_c10:\n\t" \
  "s_mov_b32 vcc_lo, 1\n\t" \
  "s_branch .Lsf_s0\n\t" \
  ".Lsf_c20:\n\t" \
  "s_mov_b32 vcc_lo, 2\n\t" \
  "s_branch .Lsf_s0\n\t" \
  ".Lsf_c30:\n\t" \
  "s_mov_b32 vcc_lo, 3\n\t" \
  "s_branch .Lsf_s0\n\t" \
  ".Lsf_c40:\n\t" \
  "s_mov_b32 vcc_lo, 4\n\t" \
  "s_branch .Lsf_s0\n\t" \
  ".Lsf_c50:\n\t" \
  "s_mov_b32 vcc_lo, 5\n\t" \
  "s_branch .Lsf_s0\n\t" \
  ".Lsf_c60:\n\t" \
  "s_mov_b32 vcc_lo, 6\n\t" \
  "s_branch .Lsf_s0\n\t" \
  ".Lsf_c70:\n\t" \
  "s_mov_b32 vcc_lo, 7\n\t" \
  "s_branch .Lsf_s0\n\t" \
  ".Lsf_s0:\n\t" \
  "s_nop 7\n\t" \
  "s_nop 1\n\t" \
  "v_mov_b32 v205, v204\n\t" \
  "s_nop 1\n\t" \
  "v_permlane32_swap_b32 v204, v205\n\t" \
  "s_nop 1\n\t" \
  "v_max_f32 v204, v204, v205\n\t" \
  "s_nop 1\n\t" \
  "v_mov_b32 v205, v204\n\t" \
  "s_nop 1\n\t" \
  "v_permlane16_swap_b32 v204, v205\n\t" \
  "s_nop 1\n\t" \
  "v_max_f32 v204, v204, v205\n\t" \
  "v_max_f32 v204, 0, v204\n\t" \
  "v_sub_f32 v0, v0, v204\n\t" \
  "v_sub_f32 v1, v1, v204\n\t" \
  "v_sub_f32 v2, v2, v204\n\t" \
  "v_sub_f32 v3, v3, v204\n\t" \
  "v_sub_f32 v4, v4, v204\n\t" \
  "v_sub_f32 v5, v5, v204\n\t" \
  "v_sub_f32 v6, v6, v204\n\t" \
  "v_sub_f32 v7, v7, v204\n\t" \
  "v_sub_f32 v8, v8, v204\n\t" \
  "v_sub_f32 v9, v9, v204\n\t" \
  "v_sub_f32 v10, v10, v204\n\t" \
  "v_sub_f32 v11, v11, v204\n\t" \
  "v_sub_f32 v12, v12, v204\n\t" \
  "v_sub_f32 v13, v13, v204\n\t" \
  "v_sub_f32 v14, v14, v204\n\t" \
  "v_sub_f32 v15, v15, v204\n\t" \
  "v_sub_f32 v176, v176, v204\n\t" \
  "v_sub_f32 v177, v177, v204\n\t" \
  "v_sub_f32 v178, v178, v204\n\t" \
  "v_sub_f32 v179, v179, v204\n\t" \
  "v_exp_f32 v196, -v204\n\t" \
  "s_mov_b32 s99, 1\n\t" \
  "s_nop 1\n\t" \
  "s_cmp_eq_u32 vcc_lo, 0\n\t" \
  "s_cbranch_scc1 .Lsf_r00\n\t" \
  "s_cmp_eq_u32 vcc_lo, 1\n\t" \
  "s_cbranch_scc1 .Lsf_r10\n\t" \
  "s_cmp_eq_u32 vcc_lo, 2\n\t" \
  "s_cbranch_scc1 .Lsf_r20\n\t" \
  "s_cmp_eq_u32 vcc_lo, 3\n\t" \
  "s_cbranch_scc1 .Lsf_r30\n\t" \
  "s_cmp_eq_u32 vcc_lo, 4\n\t" \
  "s_cbranch_scc1 .Lsf_r40\n\t" \
  "s_cmp_eq_u32 vcc_lo, 5\n\t" \
  "s_cbranch_scc1 .Lsf_r50\n\t" \
  "s_cmp_eq_u32 vcc_lo, 6\n\t" \
  "s_cbranch_scc1 .Lsf_r60\n\t" \
  "s_cmp_eq_u32 vcc_lo, 7\n\t" \
  "s_cbranch_scc1 .Lsf_r70\n\t" \
  ".Lsf_c01:\n\t" \
  "s_mov_b32 vcc_lo, 0\n\t" \
  "s_branch .Lsf_s1\n\t" \
  ".Lsf_c11:\n\t" \
  "s_mov_b32 vcc_lo, 1\n\t" \
  "s_branch .Lsf_s1\n\t" \
  ".Lsf_c21:\n\t" \
  "s_mov_b32 vcc_lo, 2\n\t" \
  "s_branch .Lsf_s1\n\t" \
  ".Lsf_c31:\n\t" \
  "s_mov_b32 vcc_lo, 3\n\t" \
  "s_branch .Lsf_s1\n\t" \
  ".Lsf_c41:\n\t" \
  "s_mov_b32 vcc_lo, 4\n\t" \
  "s_branch .Lsf_s1\n\t" \
  ".Lsf_c51:\n\t" \
  "s_mov_b32 vcc_lo, 5\n\t" \
  "s_branch .Lsf_s1\n\t" \
  ".Lsf_c61:\n\t" \
  "s_mov_b32 vcc_lo, 6\n\t" \
  "s_branch .Lsf_s1\n\t" \
  ".Lsf_c71:\n\t" \
  "s_mov_b32 vcc_lo, 7\n\t" \
  "s_branch .Lsf_s1\n\t" \
  ".Lsf_s1:\n\t" \
  "s_nop 7\n\t" \
  "s_nop 1\n\t" \
  "v_mov_b32 v205, v204\n\t" \
  "s_nop 1\n\t" \
  "v_permlane32_swap_b32 v204, v205\n\t" \
  "s_nop 1\n\t" \
  "v_max_f32 v204, v204, v205\n\t" \
  "s_nop 1\n\t" \
  "v_mov_b32 v205, v204\n\t" \
  "s_nop 1\n\t" \
  "v_permlane16_swap_b32 v204, v205\n\t" \
  "s_nop 1\n\t" \
  "v_max_f32 v204, v204, v205\n\t" \
  "v_max_f32 v204, 0, v204\n\t" \
  "v_sub_f32 v32, v32, v204\n\t" \
  "v_sub_f32 v33, v33, v204\n\t" \
  "v_sub_f32 v34, v34, v204\n\t" \
  "v_sub_f32 v35, v35, v204\n\t" \
  "v_sub_f32 v36, v36, v204\n\t" \
  "v_sub_f32 v37, v37, v204\n\t" \
  "v_sub_f32 v38, v38, v204\n\t" \
  "v_sub_f32 v39, v39, v204\n\t" \
  "v_sub_f32 v40, v40, v204\n\t" \
  "v_sub_f32 v41, v41, v204\n\t" \
  "v_sub_f32 v42, v42, v204\n\t" \
  "v_sub_f32 v43, v43, v204\n\t" \
  "v_sub_f32 v44, v44, v204\n\t" \
  "v_sub_f32 v45, v45, v204\n\t" \
  "v_sub_f32 v46, v46, v204\n\t" \
  "v_sub_f32 v47, v47, v204\n\t" \
  "v_sub_f32 v180, v180, v204\n\t" \
  "v_sub_f32 v181, v181, v204\n\t" \
  "v_sub_f32 v182, v182, v204\n\t" \
  "v_sub_f32 v183, v183, v204\n\t" \
  "v_exp_f32 v197, -v204\n\t" \
  "s_mov_b32 s99, 1\n\t" \
  "s_nop 1\n\t" \
  "s_cmp_eq_u32 vcc_lo, 0\n\t" \
  "s_cbranch_scc1 .Lsf_r01\n\t" \
  "s_cmp_eq_u32 vcc_lo, 1\n\t" \
  "s_cbranch_scc1 .Lsf_r11\n\t" \
  "s_cmp_eq_u32 vcc_lo, 2\n\t" \
  "s_cbranch_scc1 .Lsf_r21\n\t" \
  "s_cmp_eq_u32 vcc_lo, 3\n\t" \
  "s_cbranch_scc1 .Lsf_r31\n\t" \
  "s_cmp_eq_u32 vcc_lo, 4\n\t" \
  "s_cbranch_scc1 .Lsf_r41\n\t" \
  "s_cmp_eq_u32 vcc_lo, 5\n\t" \
  "s_cbranch_scc1 .Lsf_r51\n\t" \
  "s_cmp_eq_u32 vcc_lo, 6\n\t" \
  "s_cbranch_scc1 .Lsf_r61\n\t" \
  "s_cmp_eq_u32 vcc_lo, 7\n\t" \
  "s_cbranch_scc1 .Lsf_r71\n\t" \
  ".Lsf_c02:\n\t" \
  "s_mov_b32 vcc_lo, 0\n\t" \
  "s_branch .Lsf_s2\n\t" \
  ".Lsf_c12:\n\t" \
  "s_mov_b32 vcc_lo, 1\n\t" \
  "s_branch .Lsf_s2\n\t" \
  ".Lsf_c22:\n\t" \
  "s_mov_b32 vcc_lo, 2\n\t" \
  "s_branch .Lsf_s2\n\t" \
  ".Lsf_c32:\n\t" \
  "s_mov_b32 vcc_lo, 3\n\t" \
  "s_branch .Lsf_s2\n\t" \
  ".Lsf_c42:\n\t" \
  "s_mov_b32 vcc_lo, 4\n\t" \
  "s_branch .Lsf_s2\n\t" \
  ".Lsf_c52:\n\t" \
  "s_mov_b32 vcc_lo, 5\n\t" \
  "s_branch .Lsf_s2\n\t" \
  ".Lsf_c62:\n\t" \
  "s_mov_b32 vcc_lo, 6\n\t" \
  "s_branch .Lsf_s2\n\t" \
  ".Lsf_c72:\n\t" \
  "s_mov_b32 vcc_lo, 7\n\t" \
  "s_branch .Lsf_s2\n\t" \
  ".Lsf_s2:\n\t" \
  "s_nop 7\n\t" \
  "s_nop 1\n\t" \
  "v_mov_b32 v205, v204\n\t" \
  "s_nop 1\n\t" \
  "v_permlane32_swap_b32 v204, v205\n\t" \
  "s_nop 1\n\t" \
  "v_max_f32 v204, v204, v205\n\t" \
  "s_nop 1\n\t" \
  "v_mov_b32 v205, v204\n\t" \
  "s_nop 1\n\t" \
  "v_permlane16_swap_b32 v204, v205\n\t" \
  "s_nop 1\n\t" \
  "v_max_f32 v204, v204, v205\n\t" \
  "v_max_f32 v204, 0, v204\n\t" \
  "v_sub_f32 v64, v64, v204\n\t" \
  "v_sub_f32 v65, v65, v204\n\t" \
  "v_sub_f32 v66, v66, v204\n\t" \
  "v_sub_f32 v67, v67, v204\n\t" \
  "v_sub_f32 v68, v68, v204\n\t" \
  "v_sub_f32 v69, v69, v204\n\t" \
  "v_sub_f32 v70, v70, v204\n\t" \
  "v_sub_f32 v71, v71, v204\n\t" \
  "v_sub_f32 v72, v72, v204\n\t" \
  "v_sub_f32 v73, v73, v204\n\t" \
  "v_sub_f32 v74, v74, v204\n\t" \
  "v_sub_f32 v75, v75, v204\n\t" \
  "v_sub_f32 v76, v76, v204\n\t" \
  "v_sub_f32 v77, v77, v204\n\t" \
  "v_sub_f32 v78, v78, v204\n\t" \
  "v_sub_f32 v79, v79, v204\n\t" \
  "v_sub_f32 v184, v184, v204\n\t" \
  "v_sub_f32 v185, v185, v204\n\t" \
  "v_sub_f32 v186, v186, v204\n\t" \
  "v_sub_f32 v187, v187, v204\n\t" \
  "v_exp_f32 v198, -v204\n\t" \
  "s_mov_b32 s99, 1\n\t" \
  "s_nop 1\n\t" \
  "s_cmp_eq_u32 vcc_lo, 0\n\t" \
  "s_cbranch_scc1 .Lsf_r02\n\t" \
  "s_cmp_eq_u32 vcc_lo, 1\n\t" \
  "s_cbranch_scc1 .Lsf_r12\n\t" \
  "s_cmp_eq_u32 vcc_lo, 2\n\t" \
  "s_cbranch_scc1 .Lsf_r22\n\t" \
  "s_cmp_eq_u32 vcc_lo, 3\n\t" \
  "s_cbranch_scc1 .Lsf_r32\n\t" \
  "s_cmp_eq_u32 vcc_lo, 4\n\t" \
  "s_cbranch_scc1 .Lsf_r42\n\t" \
  "s_cmp_eq_u32 vcc_lo, 5\n\t" \
  "s_cbranch_scc1 .Lsf_r52\n\t" \
  "s_cmp_eq_u32 vcc_lo, 6\n\t" \
  "s_cbranch_scc1 .Lsf_r62\n\t" \
  "s_cmp_eq_u32 vcc_lo, 7\n\t" \
  "s_cbranch_scc1 .Lsf_r72\n\t" \
  ".Lsf_c03:\n\t" \
  "s_mov_b32 vcc_lo, 0\n\t" \
  "s_branch .Lsf_s3\n\t" \
  ".Lsf_c13:\n\t" \
  "s_mov_b32 vcc_lo, 1\n\t" \
  "s_branch .Lsf_s3\n\t" \
  ".Lsf_c23:\n\t" \
  "s_mov_b32 vcc_lo, 2\n\t" \
  "s_branch .Lsf_s3\n\t" \
  ".Lsf_c33:\n\t" \
  "s_mov_b32 vcc_lo, 3\n\t" \
  "s_branch .Lsf_s3\n\t" \
  ".Lsf_c43:\n\t" \
  "s_mov_b32 vcc_lo, 4\n\t" \
  "s_branch .Lsf_s3\n\t" \
  ".Lsf_c53:\n\t" \
  "s_mov_b32 vcc_lo, 5\n\t" \
  "s_branch .Lsf_s3\n\t" \
  ".Lsf_c63:\n\t" \
  "s_mov_b32 vcc_lo, 6\n\t" \
  "s_branch .Lsf_s3\n\t" \
  ".Lsf_c73:\n\t" \
  "s_mov_b32 vcc_lo, 7\n\t" \
  "s_branch .Lsf_s3\n\t" \
  ".Lsf_s3:\n\t" \
  "s_nop 7\n\t" \
  "s_nop 1\n\t" \
  "v_mov_b32 v205, v204\n\t" \
  "s_nop 1\n\t" \
  "v_permlane32_swap_b32 v204, v205\n\t" \
  "s_nop 1\n\t" \
  "v_max_f32 v204, v204, v205\n\t" \
  "s_nop 1\n\t" \
  "v_mov_b32 v205, v204\n\t" \
  "s_nop 1\n\t" \
  "v_permlane16_swap_b32 v204, v205\n\t" \
  "s_nop 1\n\t" \
  "v_max_f32 v204, v204, v205\n\t" \
  "v_max_f32 v204, 0, v204\n\t" \
  "v_sub_f32 v96, v96, v204\n\t" \
  "v_sub_f32 v97, v97, v204\n\t" \
  "v_sub_f32 v98, v98, v204\n\t" \
  "v_sub_f32 v99, v99, v204\n\t" \
  "v_sub_f32 v100, v100, v204\n\t" \
  "v_sub_f32 v101, v101, v204\n\t" \
  "v_sub_f32 v102, v102, v204\n\t" \
  "v_sub_f32 v103, v103, v204\n\t" \
  "v_sub_f32 v104, v104, v204\n\t" \
  "v_sub_f32 v105, v105, v204\n\t" \
  "v_sub_f32 v106, v106, v204\n\t" \
  "v_sub_f32 v107, v107, v204\n\t" \
  "v_sub_f32 v108, v108, v204\n\t" \
  "v_sub_f32 v109, v109, v204\n\t" \
  "v_sub_f32 v110, v110, v204\n\t" \
  "v_sub_f32 v111, v111, v204\n\t" \
  "v_sub_f32 v188, v188, v204\n\t" \
  "v_sub_f32 v189, v189, v204\n\t" \
  "v_sub_f32 v190, v190, v204\n\t" \
  "v_sub_f32 v191, v191, v204\n\t" \
  "v_exp_f32 v199, -v204\n\t" \
  "s_mov_b32 s99, 1\n\t" \
  "s_nop 1\n\t" \
  "s_cmp_eq_u32 vcc_lo, 0\n\t" \
  "s_cbranch_scc1 .Lsf_r03\n\t" \
  "s_cmp_eq_u32 vcc_lo, 1\n\t" \
  "s_cbranch_scc1 .Lsf_r13\n\t" \
  "s_cmp_eq_u32 vcc_lo, 2\n\t" \
  "s_cbranch_scc1 .Lsf_r23\n\t" \
  "s_cmp_eq_u32 vcc_lo, 3\n\t" \
  "s_cbranch_scc1 .Lsf_r33\n\t" \
  "s_cmp_eq_u32 vcc_lo, 4\n\t" \
  "s_cbranch_scc1 .Lsf_r43\n\t" \
  "s_cmp_eq_u32 vcc_lo, 5\n\t" \
  "s_cbranch_scc1 .Lsf_r53\n\t" \
  "s_cmp_eq_u32 vcc_lo, 6\n\t" \
  "s_cbranch_scc1 .Lsf_r63\n\t" \
  "s_cmp_eq_u32 vcc_lo, 7\n\t" \
  "s_cbranch_scc1 .Lsf_r73\n\t" \
  ".Lsf_p0:\n\t" \
  "s_mov_b32 vcc_lo, 0\n\t" \
  "s_branch .Lsf_rs\n\t" \
  ".Lsf_p1:\n\t" \
  "s_mov_b32 vcc_lo, 1\n\t" \
  "s_branch .Lsf_rs\n\t" \
  ".Lsf_p2:\n\t" \
  "s_mov_b32 vcc_lo, 2\n\t" \
  "s_branch .Lsf_rs\n\t" \
  ".Lsf_p3:\n\t" \
  "s_mov_b32 vcc_lo, 3\n\t" \
  "s_branch .Lsf_rs\n\t" \
  ".Lsf_p4:\n\t" \
  "s_mov_b32 vcc_lo, 4\n\t" \
  "s_branch .Lsf_rs\n\t" \
  ".Lsf_p5:\n\t" \
  "s_mov_b32 vcc_lo, 5\n\t" \
  "s_branch .Lsf_rs\n\t" \
  ".Lsf_p6:\n\t" \
  "s_mov_b32 vcc_lo, 6\n\t" \
  "s_branch .Lsf_rs\n\t" \
  ".Lsf_p7:\n\t" \
  "s_mov_b32 vcc_lo, 7\n\t" \
  "s_branch .Lsf_rs\n\t" \
  ".Lsf_rs:\n\t" \
  "s_nop 7\n\t" \
  "s_nop 7\n\t" \
  "v_accvgpr_read_b32 v205, a0\n\t" \
  "v_mul_f32 v205, v196, v205\n\t" \
  "v_accvgpr_write_b32 a0, v205\n\t" \
  "v_accvgpr_read_b32 v205, a1\n\t" \
  "v_mul_f32 v205, v196, v205\n\t" \
  "v_accvgpr_write_b32 a1, v205\n\t" \
  "v_accvgpr_read_b32 v205, a2\n\t" \
  "v_mul_f32 v205, v196, v205\n\t" \
  "v_accvgpr_write_b32 a2, v205\n\t" \
  "v_accvgpr_read_b32 v205, a3\n\t" \
  "v_mul_f32 v205, v196, v205\n\t" \
  "v_accvgpr_write_b32 a3, v205\n\t" \
  "v_accvgpr_read_b32 v205, a4\n\t" \
  "v_mul_f32 v205, v196, v205\n\t" \
  "v_accvgpr_write_b32 a4, v205\n\t" \
  "v_accvgpr_read_b32 v205, a5\n\t" \
  "v_mul_f32 v205, v196, v205\n\t" \
  "v_accvgpr_write_b32 a5, v205\n\t" \
  "v_accvgpr_read_b32 v205, a6\n\t" \
  "v_mul_f32 v205, v196, v205\n\t" \
  "v_accvgpr_write_b32 a6, v205\n\t" \
  "v_accvgpr_read_b32 v205, a7\n\t" \
  "v_mul_f32 v205, v196, v205\n\t" \
  "v_accvgpr_write_b32 a7, v205\n\t" \
  "v_accvgpr_read_b32 v205, a8\n\t" \
  "v_mul_f32 v205, v196, v205\n\t" \
  "v_accvgpr_write_b32 a8, v205\n\t" \
  "v_accvgpr_read_b32 v205, a9\n\t" \
  "v_mul_f32 v205, v196, v205\n\t" \
  "v_accvgpr_write_b32 a9, v205\n\t" \
  "v_accvgpr_read_b32 v205, a10\n\t" \
  "v_mul_f32 v205, v196, v205\n\t" \
  "v_accvgpr_write_b32 a10, v205\n\t" \
  "v_accvgpr_read_b32 v205, a11\n\t" \
  "v_mul_f32 v205, v196, v205\n\t" \
  "v_accvgpr_write_b32 a11, v205\n\t" \
  "v_accvgpr_read_b32 v205, a12\n\t" \
  "v_mul_f32 v205, v196, v205\n\t" \
  "v_accvgpr_write_b32 a12, v205\n\t" \
  "v_accvgpr_read_b32 v205, a13\n\t" \
  "v_mul_f32 v205, v196, v205\n\t" \
  "v_accvgpr_write_b32 a13, v205\n\t" \
  "v_accvgpr_read_b32 v205, a14\n\t" \
  "v_mul_f32 v205, v196, v205\n\t" \
  "v_accvgpr_write_b32 a14, v205\n\t" \
  "v_accvgpr_read_b32 v205, a15\n\t" \
  "v_mul_f32 v205, v196, v205\n\t" \
  "v_accvgpr_write_b32 a15, v205\n\t" \
  "v_accvgpr_read_b32 v205, a16\n\t" \
  "v_mul_f32 v205, v196, v205\n\t" \
  "v_accvgpr_write_b32 a16, v205\n\t" \
  "v_accvgpr_read_b32 v205, a17\n\t" \
  "v_mul_f32 v205, v196, v205\n\t" \
  "v_accvgpr_write_b32 a17, v205\n\t" \
  "v_accvgpr_read_b32 v205, a18\n\t" \
  "v_mul_f32 v205, v196, v205\n\t" \
  "v_accvgpr_write_b32 a18, v205\n\t" \
  "v_accvgpr_read_b32 v205, a19\n\t" \
  "v_mul_f32 v205, v196, v205\n\t" \
  "v_accvgpr_write_b32 a19, v205\n\t" \
  "v_accvgpr_read_b32 v205, a20\n\t" \
  "v_mul_f32 v205, v196, v205\n\t" \
  "v_accvgpr_write_b32 a20, v205\n\t" \
  "v_accvgpr_read_b32 v205, a21\n\t" \
  "v_mul_f32 v205, v196, v205\n\t" \
  "v_accvgpr_write_b32 a21, v205\n\t" \
  "v_accvgpr_read_b32 v205, a22\n\t" \
  "v_mul_f32 v205, v196, v205\n\t" \
  "v_accvgpr_write_b32 a22, v205\n\t" \
  "v_accvgpr_read_b32 v205, a23\n\t" \
  "v_mul_f32 v205, v196, v205\n\t" \
  "v_accvgpr_write_b32 a23, v205\n\t" \
  "v_accvgpr_read_b32 v205, a24\n\t" \
  "v_mul_f32 v205, v196, v205\n\t" \
  "v_accvgpr_write_b32 a24, v205\n\t" \
  "v_accvgpr_read_b32 v205, a25\n\t" \
  "v_mul_f32 v205, v196, v205\n\t" \
  "v_accvgpr_write_b32 a25, v205\n\t" \
  "v_accvgpr_read_b32 v205, a26\n\t" \
  "v_mul_f32 v205, v196, v205\n\t" \
  "v_accvgpr_write_b32 a26, v205\n\t" \
  "v_accvgpr_read_b32 v205, a27\n\t" \
  "v_mul_f32 v205, v196, v205\n\t" \
  "v_accvgpr_write_b32 a27, v205\n\t" \
  "v_accvgpr_read_b32 v205, a28\n\t" \
  "v_mul_f32 v205, v196, v205\n\t" \
  "v_accvgpr_write_b32 a28, v205\n\t" \
  "v_accvgpr_read_b32 v205, a29\n\t" \
  "v_mul_f32 v205, v196, v205\n\t" \
  "v_accvgpr_write_b32 a29, v205\n\t" \
  "v_accvgpr_read_b32 v205, a30\n\t" \
  "v_mul_f32 v205, v196, v205\n\t" \
  "v_accvgpr_write_b32 a30, v205\n\t" \
  "v_accvgpr_read_b32 v205, a31\n\t" \
  "v_mul_f32 v205, v196, v205\n\t" \
  "v_accvgpr_write_b32 a31, v205\n\t" \
  "v_accvgpr_read_b32 v205, a32\n\t" \
  "v_mul_f32 v205, v197, v205\n\t" \
  "v_accvgpr_write_b32 a32, v205\n\t" \
  "v_accvgpr_read_b32 v205, a33\n\t" \
  "v_mul_f32 v205, v197, v205\n\t" \
  "v_accvgpr_write_b32 a33, v205\n\t" \
  "v_accvgpr_read_b32 v205, a34\n\t" \
  "v_mul_f32 v205, v197, v205\n\t" \
  "v_accvgpr_write_b32 a34, v205\n\t" \
  "v_accvgpr_read_b32 v205, a35\n\t" \
  "v_mul_f32 v205, v197, v205\n\t" \
  "v_accvgpr_write_b32 a35, v205\n\t" \
  "v_accvgpr_read_b32 v205, a36\n\t" \
  "v_mul_f32 v205, v197, v205\n\t" \
  "v_accvgpr_write_b32 a36, v205\n\t" \
  "v_accvgpr_read_b32 v205, a37\n\t" \
  "v_mul_f32 v205, v197, v205\n\t" \
  "v_accvgpr_write_b32 a37, v205\n\t" \
  "v_accvgpr_read_b32 v205, a38\n\t" \
  "v_mul_f32 v205, v197, v205\n\t" \
  "v_accvgpr_write_b32 a38, v205\n\t" \
  "v_accvgpr_read_b32 v205, a39\n\t" \
  "v_mul_f32 v205, v197, v205\n\t" \
  "v_accvgpr_write_b32 a39, v205\n\t" \
  "v_accvgpr_read_b32 v205, a40\n\t" \
  "v_mul_f32 v205, v197, v205\n\t" \
  "v_accvgpr_write_b32 a40, v205\n\t" \
  "v_accvgpr_read_b32 v205, a41\n\t" \
  "v_mul_f32 v205, v197, v205\n\t" \
  "v_accvgpr_write_b32 a41, v205\n\t" \
  "v_accvgpr_read_b32 v205, a42\n\t" \
  "v_mul_f32 v205, v197, v205\n\t" \
  "v_accvgpr_write_b32 a42, v205\n\t" \
  "v_accvgpr_read_b32 v205, a43\n\t" \
  "v_mul_f32 v205, v197, v205\n\t" \
  "v_accvgpr_write_b32 a43, v205\n\t" \
  "v_accvgpr_read_b32 v205, a44\n\t" \
  "v_mul_f32 v205, v197, v205\n\t" \
  "v_accvgpr_write_b32 a44, v205\n\t" \
  "v_accvgpr_read_b32 v205, a45\n\t" \
  "v_mul_f32 v205, v197, v205\n\t" \
  "v_accvgpr_write_b32 a45, v205\n\t" \
  "v_accvgpr_read_b32 v205, a46\n\t" \
  "v_mul_f32 v205, v197, v205\n\t" \
  "v_accvgpr_write_b32 a46, v205\n\t" \
  "v_accvgpr_read_b32 v205, a47\n\t" \
  "v_mul_f32 v205, v197, v205\n\t" \
  "v_accvgpr_write_b32 a47, v205\n\t" \
  "v_accvgpr_read_b32 v205, a48\n\t" \
  "v_mul_f32 v205, v197, v205\n\t" \
  "v_accvgpr_write_b32 a48, v205\n\t" \
  "v_accvgpr_read_b32 v205, a49\n\t" \
  "v_mul_f32 v205, v197, v205\n\t" \
  "v_accvgpr_write_b32 a49, v205\n\t" \
  "v_accvgpr_read_b32 v205, a50\n\t" \
  "v_mul_f32 v205, v197, v205\n\t" \
  "v_accvgpr_write_b32 a50, v205\n\t" \
  "v_accvgpr_read_b32 v205, a51\n\t" \
  "v_mul_f32 v205, v197, v205\n\t" \
  "v_accvgpr_write_b32 a51, v205\n\t" \
  "v_accvgpr_read_b32 v205, a52\n\t" \
  "v_mul_f32 v205, v197, v205\n\t" \
  "v_accvgpr_write_b32 a52, v205\n\t" \
  "v_accvgpr_read_b32 v205, a53\n\t" \
  "v_mul_f32 v205, v197, v205\n\t" \
  "v_accvgpr_write_b32 a53, v205\n\t" \
  "v_accvgpr_read_b32 v205, a54\n\t" \
  "v_mul_f32 v205, v197, v205\n\t" \
  "v_accvgpr_write_b32 a54, v205\n\t" \
  "v_accvgpr_read_b32 v205, a55\n\t" \
  "v_mul_f32 v205, v197, v205\n\t" \
  "v_accvgpr_write_b32 a55, v205\n\t" \
  "v_accvgpr_read_b32 v205, a56\n\t" \
  "v_mul_f32 v205, v197, v205\n\t" \
  "v_accvgpr_write_b32 a56, v205\n\t" \
  "v_accvgpr_read_b32 v205, a57\n\t" \
  "v_mul_f32 v205, v197, v205\n\t" \
  "v_accvgpr_write_b32 a57, v205\n\t" \
  "v_accvgpr_read_b32 v205, a58\n\t" \
  "v_mul_f32 v205, v197, v205\n\t" \
  "v_accvgpr_write_b32 a58, v205\n\t" \
  "v_accvgpr_read_b32 v205, a59\n\t" \
  "v_mul_f32 v205, v197, v205\n\t" \
  "v_accvgpr_write_b32 a59, v205\n\t" \
  "v_accvgpr_read_b32 v205, a60\n\t" \
  "v_mul_f32 v205, v197, v205\n\t" \
  "v_accvgpr_write_b32 a60, v205\n\t" \
  "v_accvgpr_read_b32 v205, a61\n\t" \
  "v_mul_f32 v205, v197, v205\n\t" \
  "v_accvgpr_write_b32 a61, v205\n\t" \
  "v_accvgpr_read_b32 v205, a62\n\t" \
  "v_mul_f32 v205, v197, v205\n\t" \
  "v_accvgpr_write_b32 a62, v205\n\t" \
  "v_accvgpr_read_b32 v205, a63\n\t" \
  "v_mul_f32 v205, v197, v205\n\t" \
  "v_accvgpr_write_b32 a63, v205\n\t" \
  "v_accvgpr_read_b32 v205, a64\n\t" \
  "v_mul_f32 v205, v198, v205\n\t" \
  "v_accvgpr_write_b32 a64, v205\n\t" \
  "v_accvgpr_read_b32 v205, a65\n\t" \
  "v_mul_f32 v205, v198, v205\n\t" \
  "v_accvgpr_write_b32 a65, v205\n\t" \
  "v_accvgpr_read_b32 v205, a66\n\t" \
  "v_mul_f32 v205, v198, v205\n\t" \
  "v_accvgpr_write_b32 a66, v205\n\t" \
  "v_accvgpr_read_b32 v205, a67\n\t" \
  "v_mul_f32 v205, v198, v205\n\t" \
  "v_accvgpr_write_b32 a67, v205\n\t" \
  "v_accvgpr_read_b32 v205, a68\n\t" \
  "v_mul_f32 v205, v198, v205\n\t" \
  "v_accvgpr_write_b32 a68, v205\n\t" \
  "v_accvgpr_read_b32 v205, a69\n\t" \
  "v_mul_f32 v205, v198, v205\n\t" \
  "v_accvgpr_write_b32 a69, v205\n\t" \
  "v_accvgpr_read_b32 v205, a70\n\t" \
  "v_mul_f32 v205, v198, v205\n\t" \
  "v_accvgpr_write_b32 a70, v205\n\t" \
  "v_accvgpr_read_b32 v205, a71\n\t" \
  "v_mul_f32 v205, v198, v205\n\t" \
  "v_accvgpr_write_b32 a71, v205\n\t" \
  "v_accvgpr_read_b32 v205, a72\n\t" \
  "v_mul_f32 v205, v198, v205\n\t" \
  "v_accvgpr_write_b32 a72, v205\n\t" \
  "v_accvgpr_read_b32 v205, a73\n\t" \
  "v_mul_f32 v205, v198, v205\n\t" \
  "v_accvgpr_write_b32 a73, v205\n\t" \
  "v_accvgpr_read_b32 v205, a74\n\t" \
  "v_mul_f32 v205, v198, v205\n\t" \
  "v_accvgpr_write_b32 a74, v205\n\t" \
  "v_accvgpr_read_b32 v205, a75\n\t" \
  "v_mul_f32 v205, v198, v205\n\t" \
  "v_accvgpr_write_b32 a75, v205\n\t" \
  "v_accvgpr_read_b32 v205, a76\n\t" \
  "v_mul_f32 v205, v198, v205\n\t" \
  "v_accvgpr_write_b32 a76, v205\n\t" \
  "v_accvgpr_read_b32 v205, a77\n\t" \
  "v_mul_f32 v205, v198, v205\n\t" \
  "v_accvgpr_write_b32 a77, v205\n\t" \
  "v_accvgpr_read_b32 v205, a78\n\t" \
  "v_mul_f32 v205, v198, v205\n\t" \
  "v_accvgpr_write_b32 a78, v205\n\t" \
  "v_accvgpr_read_b32 v205, a79\n\t" \
  "v_mul_f32 v205, v198, v205\n\t" \
  "v_accvgpr_write_b32 a79, v205\n\t" \
  "v_accvgpr_read_b32 v205, a80\n\t" \
  "v_mul_f32 v205, v198, v205\n\t" \
  "v_accvgpr_write_b32 a80, v205\n\t" \
  "v_accvgpr_read_b32 v205, a81\n\t" \
  "v_mul_f32 v205, v198, v205\n\t" \
  "v_accvgpr_write_b32 a81, v205\n\t" \
  "v_accvgpr_read_b32 v205, a82\n\t" \
  "v_mul_f32 v205, v198, v205\n\t" \
  "v_accvgpr_write_b32 a82, v205\n\t" \
  "v_accvgpr_read_b32 v205, a83\n\t" \
  "v_mul_f32 v205, v198, v205\n\t" \
  "v_accvgpr_write_b32 a83, v205\n\t" \
  "v_accvgpr_read_b32 v205, a84\n\t" \
  "v_mul_f32 v205, v198, v205\n\t" \
  "v_accvgpr_write_b32 a84, v205\n\t" \
  "v_accvgpr_read_b32 v205, a85\n\t" \
  "v_mul_f32 v205, v198, v205\n\t" \
  "v_accvgpr_write_b32 a85, v205\n\t" \
  "v_accvgpr_read_b32 v205, a86\n\t" \
  "v_mul_f32 v205, v198, v205\n\t" \
  "v_accvgpr_write_b32 a86, v205\n\t" \
  "v_accvgpr_read_b32 v205, a87\n\t" \
  "v_mul_f32 v205, v198, v205\n\t" \
  "v_accvgpr_write_b32 a87, v205\n\t" \
  "v_accvgpr_read_b32 v205, a88\n\t" \
  "v_mul_f32 v205, v198, v205\n\t" \
  "v_accvgpr_write_b32 a88, v205\n\t" \
  "v_accvgpr_read_b32 v205, a89\n\t" \
  "v_mul_f32 v205, v198, v205\n\t" \
  "v_accvgpr_write_b32 a89, v205\n\t" \
  "v_accvgpr_read_b32 v205, a90\n\t" \
  "v_mul_f32 v205, v198, v205\n\t" \
  "v_accvgpr_write_b32 a90, v205\n\t" \
  "v_accvgpr_read_b32 v205, a91\n\t" \
  "v_mul_f32 v205, v198, v205\n\t" \
  "v_accvgpr_write_b32 a91, v205\n\t" \
  "v_accvgpr_read_b32 v205, a92\n\t" \
  "v_mul_f32 v205, v198, v205\n\t" \
  "v_accvgpr_write_b32 a92, v205\n\t" \
  "v_accvgpr_read_b32 v205, a93\n\t" \
  "v_mul_f32 v205, v198, v205\n\t" \
  "v_accvgpr_write_b32 a93, v205\n\t" \
  "v_accvgpr_read_b32 v205, a94\n\t" \
  "v_mul_f32 v205, v198, v205\n\t" \
  "v_accvgpr_write_b32 a94, v205\n\t" \
  "v_accvgpr_read_b32 v205, a95\n\t" \
  "v_mul_f32 v205, v198, v205\n\t" \
  "v_accvgpr_write_b32 a95, v205\n\t" \
  "v_accvgpr_read_b32 v205, a96\n\t" \
  "v_mul_f32 v205, v199, v205\n\t" \
  "v_accvgpr_write_b32 a96, v205\n\t" \
  "v_accvgpr_read_b32 v205, a97\n\t" \
  "v_mul_f32 v205, v199, v205\n\t" \
  "v_accvgpr_write_b32 a97, v205\n\t" \
  "v_accvgpr_read_b32 v205, a98\n\t" \
  "v_mul_f32 v205, v199, v205\n\t" \
  "v_accvgpr_write_b32 a98, v205\n\t" \
  "v_accvgpr_read_b32 v205, a99\n\t" \
  "v_mul_f32 v205, v199, v205\n\t" \
  "v_accvgpr_write_b32 a99, v205\n\t" \
  "v_accvgpr_read_b32 v205, a100\n\t" \
  "v_mul_f32 v205, v199, v205\n\t" \
  "v_accvgpr_write_b32 a100, v205\n\t" \
  "v_accvgpr_read_b32 v205, a101\n\t" \
  "v_mul_f32 v205, v199, v205\n\t" \
  "v_accvgpr_write_b32 a101, v205\n\t" \
  "v_accvgpr_read_b32 v205, a102\n\t" \
  "v_mul_f32 v205, v199, v205\n\t" \
  "v_accvgpr_write_b32 a102, v205\n\t" \
  "v_accvgpr_read_b32 v205, a103\n\t" \
  "v_mul_f32 v205, v199, v205\n\t" \
  "v_accvgpr_write_b32 a103, v205\n\t" \
  "v_accvgpr_read_b32 v205, a104\n\t" \
  "v_mul_f32 v205, v199, v205\n\t" \
  "v_accvgpr_write_b32 a104, v205\n\t" \
  "v_accvgpr_read_b32 v205, a105\n\t" \
  "v_mul_f32 v205, v199, v205\n\t" \
  "v_accvgpr_write_b32 a105, v205\n\t" \
  "v_accvgpr_read_b32 v205, a106\n\t" \
  "v_mul_f32 v205, v199, v205\n\t" \
  "v_accvgpr_write_b32 a106, v205\n\t" \
  "v_accvgpr_read_b32 v205, a107\n\t" \
  "v_mul_f32 v205, v199, v205\n\t" \
  "v_accvgpr_write_b32 a107, v205\n\t" \
  "v_accvgpr_read_b32 v205, a108\n\t" \
  "v_mul_f32 v205, v199, v205\n\t" \
  "v_accvgpr_write_b32 a108, v205\n\t" \
  "v_accvgpr_read_b32 v205, a109\n\t" \
  "v_mul_f32 v205, v199, v205\n\t" \
  "v_accvgpr_write_b32 a109, v205\n\t" \
  "v_accvgpr_read_b32 v205, a110\n\t" \
  "v_mul_f32 v205, v199, v205\n\t" \
  "v_accvgpr_write_b32 a110, v205\n\t" \
  "v_accvgpr_read_b32 v205, a111\n\t" \
  "v_mul_f32 v205, v199, v205\n\t" \
  "v_accvgpr_write_b32 a111, v205\n\t" \
  "v_accvgpr_read_b32 v205, a112\n\t" \
  "v_mul_f32 v205, v199, v205\n\t" \
  "v_accvgpr_write_b32 a112, v205\n\t" \
  "v_accvgpr_read_b32 v205, a113\n\t" \
  "v_mul_f32 v205, v199, v205\n\t" \
  "v_accvgpr_write_b32 a113, v205\n\t" \
  "v_accvgpr_read_b32 v205, a114\n\t" \
  "v_mul_f32 v205, v199, v205\n\t" \
  "v_accvgpr_write_b32 a114, v205\n\t" \
  "v_accvgpr_read_b32 v205, a115\n\t" \
  "v_mul_f32 v205, v199, v205\n\t" \
  "v_accvgpr_write_b32 a115, v205\n\t" \
  "v_accvgpr_read_b32 v205, a116\n\t" \
  "v_mul_f32 v205, v199, v205\n\t" \
  "v_accvgpr_write_b32 a116, v205\n\t" \
  "v_accvgpr_read_b32 v205, a117\n\t" \
  "v_mul_f32 v205, v199, v205\n\t" \
  "v_accvgpr_write_b32 a117, v205\n\t" \
  "v_accvgpr_read_b32 v205, a118\n\t" \
  "v_mul_f32 v205, v199, v205\n\t" \
  "v_accvgpr_write_b32 a118, v205\n\t" \
  "v_accvgpr_read_b32 v205, a119\n\t" \
  "v_mul_f32 v205, v199, v205\n\t" \
  "v_accvgpr_write_b32 a119, v205\n\t" \
  "v_accvgpr_read_b32 v205, a120\n\t" \
  "v_mul_f32 v205, v199, v205\n\t" \
  "v_accvgpr_write_b32 a120, v205\n\t" \
  "v_accvgpr_read_b32 v205, a121\n\t" \
  "v_mul_f32 v205, v199, v205\n\t" \
  "v_accvgpr_write_b32 a121, v205\n\t" \
  "v_accvgpr_read_b32 v205, a122\n\t" \
  "v_mul_f32 v205, v199, v205\n\t" \
  "v_accvgpr_write_b32 a122, v205\n\t" \
  "v_accvgpr_read_b32 v205, a123\n\t" \
  "v_mul_f32 v205, v199, v205\n\t" \
  "v_accvgpr_write_b32 a123, v205\n\t" \
  "v_accvgpr_read_b32 v205, a124\n\t" \
  "v_mul_f32 v205, v199, v205\n\t" \
  "v_accvgpr_write_b32 a124, v205\n\t" \
  "v_accvgpr_read_b32 v205, a125\n\t" \
  "v_mul_f32 v205, v199, v205\n\t" \
  "v_accvgpr_write_b32 a125, v205\n\t" \
  "v_accvgpr_read_b32 v205, a126\n\t" \
  "v_mul_f32 v205, v199, v205\n\t" \
  "v_accvgpr_write_b32 a126, v205\n\t" \
  "v_accvgpr_read_b32 v205, a127\n\t" \
  "v_mul_f32 v205, v199, v205\n\t" \
  "v_accvgpr_write_b32 a127, v205\n\t" \
  "v_mul_f32 v160, v196, v160\n\t" \
  "v_mul_f32 v161, v196, v161\n\t" \
  "v_mov_b32 v196, 1.0\n\t" \
  "v_mul_f32 v164, v197, v164\n\t" \
  "v_mul_f32 v165, v197, v165\n\t" \
  "v_mov_b32 v197, 1.0\n\t" \
  "v_mul_f32 v168, v198, v168\n\t" \
  "v_mul_f32 v169, v198, v169\n\t" \
  "v_mov_b32 v198, 1.0\n\t" \
  "v_mul_f32 v172, v199, v172\n\t" \
  "v_mul_f32 v173, v199, v173\n\t" \
  "v_mov_b32 v199, 1.0\n\t" \
  "s_mov_b32 s99, 0\n\t" \
  "s_nop 1\n\t" \
  "s_cmp_eq_u32 vcc_lo, 0\n\t" \
  "s_cbranch_scc1 .Lsf_q0\n\t" \
  "s_cmp_eq_u32 vcc_lo, 1\n\t" \
  "s_cbranch_scc1 .Lsf_q1\n\t" \
  "s_cmp_eq_u32 vcc_lo, 2\n\t" \
  "s_cbranch_scc1 .Lsf_q2\n\t" \
  "s_cmp_eq_u32 vcc_lo, 3\n\t" \
  "s_cbranch_scc1 .Lsf_q3\n\t" \
  "s_cmp_eq_u32 vcc_lo, 4\n\t" \
  "s_cbranch_scc1 .Lsf_q4\n\t" \
  "s_cmp_eq_u32 vcc_lo, 5\n\t" \
  "s_cbranch_scc1 .Lsf_q5\n\t" \
  "s_cmp_eq_u32 vcc_lo, 6\n\t" \
  "s_cbranch_scc1 .Lsf_q6\n\t" \
  "s_cmp_eq_u32 vcc_lo, 7\n\t" \
  "s_cbranch_scc1 .Lsf_q7\n\t" \
  "s_endpgm\n\t" \
  ".Lsf_v:\n\t" \
  "s_mov_b32 s98, 0\n\t" \
  ".Lsf_v1:\n\t" \
  "s_sleep 4\n\t" \
  "buffer_load_dword v209, v251, s[16:19], 0 offen sc1\n\t" \
  "s_waitcnt vmcnt(0)\n\t" \
  "v_and_b32 v250, 0xbfffbfff, v209\n\t" \
  "v_cmp_ne_u32_e64 s[84:85], s36, v250\n\t" \
  "s_cmp_lg_u64 s[84:85], 0\n\t" \
  "s_cbranch_scc0 .Lsf_w\n\t" \
  "s_add_u32 s98, s98, 1\n\t" \
  "s_cmp_lt_u32 s98, " FQ_CAP "\n\t" \
  "s_cbranch_scc1 .Lsf_v1\n\t" \
  "v_mov_b32 v250, s36\n\t" \
  "buffer_store_dword v250, off, s[16:19], 0 offset:256 sc1\n\t" \
  "s_branch .Lsf_w\n\t" \
  ".subsection 0\n\t" \
  ".Lop_end:\n\t" \
  ""
#define OP_GA0 "=&{v[160:163]}"
#define OP_GA1 "=&{v[164:167]}"
#define OP_CW "=&{s82}"
#define OP_BB "=&{s83}"
#define AO_CLOB "v128", "v129", "v130", "v131", "v132", "v133", "v134", "v135", "v136", "v137", "v138", "v139", "v140", "v141", "v142", "v143", "v144", "v145", "v146", "v147", "v148", "v149", "v150", "v151", "v152", "v153", "v154", "v155", "v156", "v157", "v158", "v159", "v168", "v169", "v170", "v171", "v172", "v173", "v174", "v175", "v208", "v209", "v210", "v211", "v212", "v213", "v214", "v215", "v216", "v217", "v218", "v219", "v234", "v235", "v236", "v237", "v238", "v239", "v242", "v243", "v248", "v249", "v250", "v251", "v252", "v254", "s76", "s77", "s78", "s79", "s80", "s81", "s84", "s85", "s86", "s87", "s88", "s89", "s90", "s91", "s92", "s93", "s94", "s95", "s96", "s97", "s98", "s99", "vcc", "scc", "memory"
#define OP_DRD "s_waitcnt vmcnt(5)\n\t" "ds_read2st64_b64 v[32:35], %1 offset0:64 offset1:65\n\t" "ds_read2st64_b64 v[36:39], %1 offset0:66 offset1:67\n\t" "ds_read2st64_b64 v[40:43], %1 offset0:68 offset1:69\n\t" "ds_read2st64_b64 v[44:47], %1 offset0:70 offset1:71\n\t" "ds_read2st64_b64 v[48:51], %1 offset0:96 offset1:97\n\t" "ds_read2st64_b64 v[52:55], %1 offset0:98 offset1:99\n\t" "ds_read2st64_b64 v[56:59], %1 offset0:100 offset1:101\n\t" "ds_read2st64_b64 v[60:63], %1 offset0:102 offset1:103\n\t" "s_waitcnt lgkmcnt(0)"
#define ST_ASM "s_nop 1\n\t" "v_mfma_f32_16x16x32_bf16 v[80:83], a[176:179], v[160:163], 0\n\t" "v_mfma_f32_16x16x32_bf16 v[80:83], a[180:183], v[164:167], v[80:83]\n\t" "v_mfma_f32_16x16x32_bf16 v[80:83], a[184:187], v[168:171], v[80:83]\n\t" "v_mfma_f32_16x16x32_bf16 v[80:83], a[188:191], v[172:175], v[80:83]\n\t" "v_dot2_f32_bf16 v64, v160, v160, 0\n\t" "v_dot2_f32_bf16 v65, v164, v164, 0\n\t" "v_dot2_f32_bf16 v66, v168, v168, 0\n\t" "v_dot2_f32_bf16 v67, v172, v172, 0\n\t" "v_dot2_f32_bf16 v64, v161, v161, v64\n\t" "v_dot2_f32_bf16 v65, v165, v165, v65\n\t" "v_dot2_f32_bf16 v66, v169, v169, v66\n\t" "v_dot2_f32_bf16 v67, v173, v173, v67\n\t" "v_dot2_f32_bf16 v64, v162, v162, v64\n\t" "v_dot2_f32_bf16 v65, v166, v166, v65\n\t" "v_dot2_f32_bf16 v66, v170, v170, v66\n\t" "v_dot2_f32_bf16 v67, v174, v174, v67\n\t" "v_dot2_f32_bf16 v64, v163, v163, v64\n\t" "v_dot2_f32_bf16 v65, v167, v167, v65\n\t" "v_dot2_f32_bf16 v66, v171, v171, v66\n\t" "v_dot2_f32_bf16 v67, v175, v175, v67\n\t" "s_nop 2\n\t"
static_assert(W_MODB == 2228224 && W_VOTE == 8192);
#define AO_T0
static_assert(W_EPOCH == 2490368 && W_GKEY == 65536 && W_CANDH == 262144 && W_STAT == 524288 && W_YG == 1048576 && W_CANDX == 1966080);
#define MAIN_CLOB "v1", "v2", "v3", "v4", "v5", "v6", "v7", "v8", "v9", "v10", "v11", "v12", "v13", "v14", "v15", "v16", "v17", "v18", "v19", "v20", "v21", "v22", "v23", "v24", "v25", "v26", "v27", "v28", "v29", "v30", "v31", "v32", "v33", "v34", "v35", "v36", "v37", "v38", "v39", "v40", "v41", "v42", "v43", "v44", "v45", "v46", "v47", "v48", "v49", "v50", "v51", "v52", "v53", "v54", "v55", "v56", "v57", "v58", "v59", "v60", "v61", "v62", "v63", "v64", "v65", "v66", "v67", "v68", "v69", "v70", "v71", "v72", "v73", "v74", "v75", "v76", "v77", "v78", "v79", "v80", "v81", "v82", "v83", "v84", "v85", "v86", "v87", "v88", "v89", "v90", "v91", "v92", "v93", "v94", "v95", "v96", "v97", "v98", "v99", "v100", "v101", "v102", "v103", "v104", "v105", "v106", "v107", "v108", "v109", "v110", "v111", "v112", "v113", "v114", "v115", "v116", "v117", "v118", "v119", "v120", "v121", "v122", "v123", "v124", "v125", "v126", "v127", "v128", "v129", "v130", "v131", "v132", "v133", "v134", "v135", "v136", "v137", "v138", "v139", "v140", "v141", "v142", "v143", "v144", "v145", "v146", "v147", "v148", "v149", "v150", "v151", "v152", "v153", "v154", "v155", "v156", "v157", "v158", "v159", "v160", "v161", "v162", "v163", "v164", "v165", "v166", "v167", "v168", "v169", "v170", "v171", "v172", "v173", "v174", "v175", "v176", "v177", "v178", "v179", "v180", "v181", "v182", "v183", "v184", "v185", "v186", "v187", "v188", "v189", "v190", "v191", "v192", "v193", "v194", "v195", "v196", "v197", "v198", "v199", "v200", "v201", "v202", "v203", "v204", "v205", "v206", "v207", "v208", "v209", "v210", "v211", "v212", "v213", "v214", "v215", "v216", "v217", "v218", "v219", "v220", "v221", "v222", "v223", "v224", "v225", "v226", "v227", "v228", "v229", "v230", "v231", "v232", "v233", "v234", "v235", "v236", "v237", "v238", "v239", "v240", "v241", "v242", "v243", "v244", "v245", "v246", "v247", "v248", "v249", "v250", "v251", "v252", "v253", "v254", "s4", "s5", "s6", "s7", "s8", "s9", "s10", "s11", "s12", "s13", "s14", "s15", "s16", "s17", "s18", "s19", "s20", "s21", "s22", "s23", "s24", "s25", "s26", "s27", "s28", "s29", "s30", "s31", "s32", "s33", "s34", "s35", "s36", "s37", "s38", "s39", "s40", "s41", "s42", "s43", "s44", "s45", "s46", "s47", "s48", "s49", "s50", "s51", "s52", "s53", "s54", "s55", "s57", "s58", "s59", "s60", "s61", "s62", "s63", "s64", "s65", "s66", "s67", "s68", "s69", "s70", "s71", "s72", "s73", "s74", "s75", "s76", "s77", "s78", "s79", "s80", "s81", "s82", "s83", "s84", "s85", "s86", "s87", "s88", "s89", "s90", "s91", "s92", "s93", "s94", "s95", "s96", "s97", "s98", "s99", "vcc", "scc", "memory"
#define MAIN_STV
__global__ __launch_bounds__(256, 1) __attribute__((aligned(4096))) void k_fused(Args a) {
  Ctx k;
  STAMP_DECL;
  argp_t ap = (argp_t)__builtin_amdgcn_kernarg_segment_ptr();
  unsigned tid = threadIdx.x; k.bid = blockIdx.x;
  asm volatile(FQ_ASM AT_ASM : MAIN_STV "+{v0}"(tid), "+{s[0:1]}"(ap), "+{s2}"(k.bid), "=&{s3}"(k.wid), "=&{s56}"(k.t0) :: MAIN_CLOB, ACLOB_LO, ACLOB_HI);
  asm volatile(HOP_DEFS);
}

extern "C" void kernel_launch(void* const* d_in, const int* in_sizes, int n_in, void* d_out, int out_size, void* d_ws, size_t ws_size,
                              hipStream_t stream) {
  auto D = [](const void* p, unsigned bytes) { Desc d; d.p = p; d.bytes = bytes; d.w3 = SRD3; return d; };
  const size_t win = (size_t)(SKV - WIN) * DH * 2;
  Args a;
  a.x = D(d_in[0], S * DM * 2); a.w_mod = D(d_in[11], DM * 6 * DM * 2); a.c = D(d_in[10], DM * 2); a.ws = D(d_ws, W_END);
  a.wq = D(d_in[3], DM * QD * 2); a.wo = D(d_in[4], QD * DM * 2); a.wd = D(d_in[9], 0); a.w_r = D(d_in[6], DM * 2);
  a.kc = D((const char*)d_in[1] + win, WIN * DH * 2); a.vc = D((const char*)d_in[2] + win, WIN * DH * 2); a.wg = D(d_in[7], DM * FF * 2); a.wu = D(d_in[8], DM * FF * 2);
  a.out = D(d_out, S * DM * 2);
  (void)hipFuncSetAttribute((const void*)k_fused, hipFuncAttributeMaxDynamicSharedMemorySize, 163840);
  k_fused<<<256, 256, 163840, stream>>>(a);
}
```

```cpp
#include <hip/hip_runtime.h>
#include <stdint.h>

constexpr int S = 2048, DM = 1024, HQ = 32, DH = 128, SKV = 2048, WIN = 512, FF = 3584, QD = HQ * DH;
#define SRD3 0x00020000
struct Desc { const void* p; unsigned bytes, w3; };
struct Args { Desc x, w_mod, c, ws, wq, wo, wd, w_r, kc, vc, wg, wu, out; };
typedef const __attribute__((address_space(4))) Args* argp_t;

constexpr unsigned W_EPOCH = 0x260000, W_TMO = 256;
constexpr unsigned W_FLAGH = 0x261000;
constexpr unsigned W_VOTE = 0x2000, W_PROBE = 0x4000;
constexpr unsigned W_GKEY = 0x10000;
constexpr unsigned W_FLAGR = 0x8000;
constexpr unsigned W_FLAGO = 0x20000;
constexpr unsigned W_CANDH = 0x40000;
constexpr unsigned W_STAT = 0x80000;
constexpr unsigned W_YG = 0x100000;
constexpr unsigned W_CANDX = 0x1E0000;
constexpr unsigned W_ST1 = 0x200000;
constexpr unsigned W_MODA = 0x210000;
constexpr unsigned W_MODB = 0x220000;
constexpr unsigned W_HT = 4u << 20;
constexpr unsigned W_OT = 8u << 20;
constexpr unsigned W_XCH = 24u << 20;
constexpr unsigned W_END = 40u << 20;
constexpr unsigned TAGM = 0x40004000u, TAGC = 0x9E3739B9u;
#define SPINCAP 262144
#define FQ_STR2(x) #x
#define FQ_STR(x) FQ_STR2(x)
#define FQ_CAP FQ_STR(SPINCAP)
#define ACLOB_LO "a0","a1","a2","a3","a4","a5","a6","a7","a8","a9","a10","a11","a12","a13","a14","a15","a16","a17","a18","a19","a20","a21","a22","a23","a24","a25","a26","a27","a28","a29","a30","a31","a32","a33","a34","a35","a36","a37","a38","a39","a40","a41","a42","a43","a44","a45","a46","a47","a48","a49","a50","a51","a52","a53","a54","a55","a56","a57","a58","a59","a60","a61","a62","a63","a64","a65","a66","a67","a68","a69","a70","a71","a72","a73","a74","a75","a76","a77","a78","a79","a80","a81","a82","a83","a84","a85","a86","a87","a88","a89","a90","a91","a92","a93","a94","a95","a96","a97","a98","a99","a100","a101","a102","a103","a104","a105","a106","a107","a108","a109","a110","a111","a112","a113","a114","a115","a116","a117","a118","a119","a120","a121","a122","a123","a124","a125","a126","a127"
#define ACLOB_HI "a128","a129","a130","a131","a132","a133","a134","a135","a136","a137","a138","a139","a140","a141","a142","a143","a144","a145","a146","a147","a148","a149","a150","a151","a152","a153","a154","a155","a156","a157","a158","a159","a160","a161","a162","a163","a164","a165","a166","a167","a168","a169","a170","a171","a172","a173","a174","a175","a176","a177","a178","a179","a180","a181","a182","a183","a184","a185","a186","a187","a188","a189","a190","a191","a192","a193","a194","a195","a196","a197","a198","a199","a200","a201","a202","a203","a204","a205","a206","a207","a208","a209","a210","a211","a212","a213","a214","a215","a216","a217","a218","a219","a220","a221","a222","a223","a224","a225","a226","a227","a228","a229","a230","a231","a232","a233","a234","a235","a236","a237","a238","a239","a240","a241","a242","a243","a244","a245","a246","a247","a248","a249","a250","a251","a252","a253","a254","a255"
#define HOP_DEFS ".set .Lhc00, .Lhs0\n\t.set .Lhs0n, .Lhs4\n\t.set .Lhs4n, .Lhs8\n\t.set .Lhs8n, .Lhr0\n\t.set .Lhc01, .Lhs1\n\t.set .Lhs1n, .Lhs5\n\t.set .Lhs5n, .Lhs9\n\t.set .Lhs9n, .Lhr0\n\t.set .Lhc02, .Lhs2\n\t.set .Lhs2n, .Lhs6\n\t.set .Lhs6n, .Lhs10\n\t.set .Lhs10n, .Lhr0\n\t.set .Lhc03, .Lhs3\n\t.set .Lhs3n, .Lhs7\n\t.set .Lhs7n, .Lhr0\n\t.set .Lhc10, .Lhs11\n\t.set .Lhs11n, .Lhs15\n\t.set .Lhs15n, .Lhr1\n\t.set .Lhc11, .Lhs12\n\t.set .Lhs12n, .Lhs16\n\t.set .Lhs16n, .Lhr1\n\t.set .Lhc12, .Lhs13\n\t.set .Lhs13n, .Lhr1\n\t.set .Lhc13, .Lhs14\n\t.set .Lhs14n, .Lhr1"
#define FSTP(x)
#define FSTS(x)
#define FSTA(x)
#define FQ_ASM \
  FSTA("v_mov_b32 v255, 0\n\t") \
  FSTA("s_memrealtime s[94:95]\n\t") \
  FSTA("s_memtime s[92:93]\n\t") \
  FSTA("s_waitcnt lgkmcnt(0)\n\t") \
  FSTA("s_or_b32 s94, s94, 1\n\t") \
  FSTA("v_writelane_b32 v255, s94, 0\n\t") \
  FSTA("v_writelane_b32 v255, s92, 1\n\t") \
  "s_mov_b32 s37, 1\n\t" \
  ".Lfr_again:\n\t" \
  "s_load_dwordx16 s[4:19], s[0:1], 0x0\n\t" \
  "s_load_dwordx16 s[20:35], s[0:1], 0x40\n\t" \
  "s_load_dwordx16 s[60:75], s[0:1], 0x80\n\t" \
  "v_readfirstlane_b32 s3, v0\n\t" \
  "v_and_b32 v254, 63, v0\n\t" \
  "s_lshr_b32 s3, s3, 6\n\t" \
  "s_lshr_b32 s39, s2, 3\n\t" \
  "s_lshl_b32 s54, s3, 5\n\t" \
  "s_add_u32 s54, s54, s39\n\t" \
  "s_lshl_b32 s54, s54, 7\n\t" \
  "v_lshl_add_u32 v48, v254, 14, s54\n\t" \
  "v_min_u32 v48, .Lfz_z-.Lfz_a-4, v48\n\t" \
  "s_getpc_b64 s[96:97]\n\t" \
  ".Lfz_a:\n\t" \
  "global_load_dword v246, v48, s[96:97]\n\t" \
  "s_and_b32 s40, s2, 3\n\t" \
  "v_lshrrev_b32 v52, 2, v0\n\t" \
  "v_lshlrev_b32 v244, 4, v254\n\t" \
  "v_lshlrev_b32 v53, 3, v52\n\t" \
  "v_mul_u32_u24 v52, 49152, v52\n\t" \
  "v_and_or_b32 v52, v244, 48, v52\n\t" \
  "s_lshl_b32 s44, s40, 9\n\t" \
  "s_mul_i32 s45, s40, 3145728\n\t" \
  "s_bfe_u32 s52, s2, 0x10002\n\t" \
  "s_lshl_b32 s52, s52, 5\n\t" \
  "s_add_u32 s52, s52, s39\n\t" \
  "s_lshl_b32 s54, s52, 6\n\t" \
  "s_add_u32 s45, s45, s54\n\t" \
  "s_add_u32 s96, s45, 12288\n\t" \
  "s_add_u32 s97, s45, 24576\n\t" \
  "s_add_u32 s99, s45, 36864\n\t" \
  "v_mov_b32 v1, 2490368\n\t" \
  "s_and_b32 s98, s2, 4\n\t" \
  "s_lshr_b32 s54, s2, 6\n\t" \
  "s_or_b32 s98, s98, s54\n\t" \
  "s_lshl_b32 s41, s98, 19\n\t" \
  "s_lshl_b32 s38, s3, 12\n\t" \
  "s_lshl_b32 s77, s3, 13\n\t" \
  "s_and_b32 s54, s2, 0x38\n\t" \
  "s_lshl_b32 s54, s54, 11\n\t" \
  "s_lshl_b32 s55, s40, 17\n\t" \
  "s_or_b32 s46, s41, s38\n\t" \
  "s_or_b32 s46, s46, s54\n\t" \
  "s_or_b32 s46, s46, s55\n\t" \
  "s_xor_b32 s87, s55, 393216\n\t" \
  "v_lshrrev_b32 v245, 4, v254\n\t" \
  "v_lshrrev_b32 v49, 3, v254\n\t" \
  "v_and_b32 v50, 7, v254\n\t" \
  "v_xor_b32 v51, v50, v245\n\t" \
  "v_lshlrev_b32 v51, 4, v51\n\t" \
  "v_lshl_add_u32 v212, v49, 11, v51\n\t" \
  "v_xor_b32 v213, 64, v212\n\t" \
  "v_add_u32 v213, 15360, v213\n\t" \
  "s_bfe_u32 s42, s2, 0x20004\n\t" \
  "s_lshl_b32 s54, s3, 15\n\t" \
  "s_or_b32 s47, s41, s54\n\t" \
  "s_or_b32 s47, s47, s44\n\t" \
  "s_lshl_b32 s54, s42, 7\n\t" \
  "s_or_b32 s47, s47, s54\n\t" \
  "s_and_b32 s43, s2, 8\n\t" \
  "s_lshl_b32 s54, s43, 15\n\t" \
  "s_or_b32 s57, s47, s87\n\t" \
  "s_xor_b32 s57, s57, s54\n\t" \
  "s_xor_b32 s58, s57, 131072\n\t" \
  "s_lshl_b32 s54, s43, 9\n\t" \
  "s_add_u32 s90, s77, s54\n\t" \
  "s_lshl_b32 s78, s40, 21\n\t" \
  "s_lshl_b32 s50, s39, 14\n\t" \
  "s_lshl2_add_u32 s50, s44, s50\n\t" \
  "s_mul_i32 s54, s3, 0x1080\n\t" \
  "s_and_b32 s54, s54, 0x2080\n\t" \
  "s_or_b32 s50, s50, s54\n\t" \
  "v_lshl_add_u32 v54, v50, 8, s50\n\t" \
  "v_and_b32 v51, 3, v49\n\t" \
  "v_lshl_add_u32 v51, v51, 19, v54\n\t" \
  "s_mov_b32 m0, s90\n\t" \
  "s_waitcnt lgkmcnt(0)\n\t" \
  "buffer_load_dwordx2 v[2:3], v53, s[12:15], s44 offen\n\t" \
  "buffer_load_dwordx4 v[4:7], v52, s[8:11], s45 offen nt\n\t" \
  "buffer_load_dwordx4 v[8:11], v52, s[8:11], s96 offen nt\n\t" \
  "buffer_load_dwordx4 v[12:15], v52, s[8:11], s97 offen nt\n\t" \
  "buffer_load_dwordx4 v[16:19], v52, s[8:11], s99 offen nt\n\t" \
  "s_add_u32 s16, s16, 0x1fffff\n\t" \
  "s_addc_u32 s17, s17, 0\n\t" \
  "s_and_b32 s16, s16, 0xffe00000\n\t" \
  "buffer_load_dword v1, v1, s[16:19], 0 offen sc1\n\t" \
  "s_mov_b32 s96, s46\n\t" \
  "v_mov_b32 v253, v0\n\t" \
  "v_and_b32 v247, 15, v254\n\t" \
  "s_bfe_u32 s43, s2, 0x10003\n\t" \
  "v_and_b32 v48, 3, v254\n\t" \
  "v_lshl_add_u32 v48, v48, 2, v245\n\t" \
  "s_lshl_b32 s54, s3, 8\n\t" \
  "s_add_u32 s55, s54, 147456\n\t" \
  "v_lshl_add_u32 v55, v48, 3, s55\n\t" \
  "v_lshlrev_b32 v56, 3, v247\n\t" \
  "v_add_u32 v56, 147456, v56\n\t" \
  "v_and_b32 v48, 0xf0, v0\n\t" \
  "v_lshl_or_b32 v57, v48, 24, v244\n\t" \
  "s_lshl_b32 s52, s52, 8\n\t" \
  "s_lshl_b32 s55, s40, 14\n\t" \
  "s_add_u32 s52, s52, s55\n\t" \
  "s_add_u32 s52, s52, 2162688\n\t" \
  "v_and_b32 v210, 31, v254\n\t" \
  "v_lshrrev_b32 v48, 5, v254\n\t" \
  "v_lshlrev_b32 v58, 4, v48\n\t" \
  "v_lshl_or_b32 v58, v210, 26, v58\n\t" \
  "s_lshr_b32 s46, s46, 7\n\t" \
  "s_add_u32 s46, s46, 2097152\n\t" \
  "v_cvt_f32_u32 v251, v48\n\t" \
  "v_lshlrev_b32 v248, 4, v210\n\t" \
  "v_lshl_or_b32 v248, v48, 13, v248\n\t" \
  "v_lshlrev_b32 v210, 2, v210\n\t" \
  "v_lshlrev_b32 v249, 4, v247\n\t" \
  "s_lshl_b32 s55, s3, 17\n\t" \
  "s_lshl_b32 s49, s43, 18\n\t" \
  "s_xor_b32 s55, s55, s49\n\t" \
  "buffer_load_dwordx4 v[32:35], v244, s[4:7], s96 offen offset:0\n\t" \
  "buffer_load_dwordx4 v[36:39], v244, s[4:7], s96 offen offset:1024\n\t" \
  "buffer_load_dwordx4 v[40:43], v244, s[4:7], s96 offen offset:2048\n\t" \
  "buffer_load_dwordx4 v[44:47], v244, s[4:7], s96 offen offset:3072\n\t" \
  "s_and_b32 s49, s2, 0x38\n\t" \
  "s_lshl2_add_u32 s55, s44, s55\n\t" \
  "s_lshl_b32 s49, s49, 5\n\t" \
  "s_add_u32 s55, s55, s49\n\t" \
  "v_lshlrev_b32 v48, 5, v245\n\t" \
  "v_xor_b32 v48, v48, v249\n\t" \
  "v_lshl_add_u32 v48, v245, 13, v48\n\t" \
  "v_add_u32 v200, s55, v48\n\t" \
  "v_xor_b32 v48, 0x80, v200\n\t" \
  "v_add_u32 v201, 31744, v200\n\t" \
  "v_add_u32 v202, 63488, v48\n\t" \
  "v_add_u32 v203, 95232, v48\n\t" \
  "s_add_u32 s76, s38, 32768\n\t" \
  "s_lshl_b32 s49, s42, 19\n\t" \
  "s_or_b32 s49, s49, s78\n\t" \
  "s_xor_b32 s80, s49, 1048576\n\t" \
  "s_add_u32 s55, s42, 1\n\t" \
  "s_and_b32 s55, s55, 3\n\t" \
  "s_xor_b32 s55, s55, s42\n\t" \
  "s_lshl_b32 s79, s55, 19\n\t" \
  "s_xor_b32 s79, s79, s49\n\t" \
  "s_xor_b32 s81, s79, 1048576\n\t" \
  "s_lshl_b32 s48, s55, 7\n\t" \
  "s_xor_b32 s48, s48, s47\n\t" \
  "s_xor_b32 s88, s47, 256\n\t" \
  "s_xor_b32 s89, s48, 256\n\t" \
  "v_lshl_add_u32 v48, s43, 2, v245\n\t" \
  "v_bfe_u32 v50, v254, 1, 3\n\t" \
  "v_xor_b32 v48, v48, v50\n\t" \
  "v_lshlrev_b32 v48, 4, v48\n\t" \
  "v_lshl_add_u32 v48, v247, 7, v48\n\t" \
  "v_add_u32 v204, s77, v48\n\t" \
  "v_xor_b32 v205, 64, v204\n\t" \
  "v_add_u32 v206, 98304, v204\n\t" \
  "v_add_u32 v207, 98304, v205\n\t" \
  "v_lshlrev_b32 v208, 5, v245\n\t" \
  "v_add_u32 v208, 148480, v208\n\t" \
  "v_lshlrev_b32 v50, 3, v254\n\t" \
  "s_lshl_b32 s55, s43, 7\n\t" \
  "s_or_b32 s55, s55, 256\n\t" \
  "v_xor_b32 v250, s55, v50\n\t" \
  "s_lshl1_add_u32 s55, s54, 148480\n\t" \
  "v_add_u32 v250, s55, v250\n\t" \
  "v_lshl_add_u32 v214, v245, 10, v249\n\t" \
  "v_lshl_add_u32 v214, s43, 12, v214\n\t" \
  "s_lshr_b32 s55, s2, 2\n\t" \
  "s_xor_b32 s55, s55, s3\n\t" \
  "s_and_b32 s55, s55, 1\n\t" \
  "s_lshl1_add_u32 s55, s3, s55\n\t" \
  "s_lshl_b32 s82, s55, 13\n\t" \
  "s_lshr_b32 s55, s2, 6\n\t" \
  "s_lshl_b32 s55, s55, 20\n\t" \
  "s_add_u32 s82, s82, s55\n\t" \
  "s_add_u32 s82, s82, 4194304\n\t" \
  "s_lshr_b32 s86, s49, 3\n\t" \
  "s_add_u32 s86, s86, s82\n\t" \
  "s_lshr_b32 s55, s87, 9\n\t" \
  "s_add_u32 s86, s86, s55\n\t" \
  "s_lshl2_add_u32 s84, s98, s3\n\t" \
  "s_lshl_b32 s84, s84, 7\n\t" \
  "s_add_u32 s84, s84, 2494464\n\t" \
  "s_bfe_u32 s55, s2, 0x30003\n\t" \
  "s_lshl3_add_u32 s55, s40, s55\n\t" \
  "s_lshl2_add_u32 s85, s55, s84\n\t" \
  "s_lshl_b32 s55, s43, 13\n\t" \
  "s_sub_u32 s83, 4096, s55\n\t" \
  "v_and_b32 v50, 0xf8, v50\n\t" \
  "v_bfe_u32 v48, v254, 2, 2\n\t" \
  "v_lshl_add_u32 v50, v48, 8, v50\n\t" \
  "v_lshl_add_u32 v50, v245, 11, v50\n\t" \
  "v_add_u32 v184, 32768, v50\n\t" \
  "v_xor_b32 v185, 32, v184\n\t" \
  "v_xor_b32 v186, 64, v184\n\t" \
  "v_xor_b32 v187, 96, v184\n\t" \
  "v_xor_b32 v188, 128, v184\n\t" \
  "v_xor_b32 v189, 160, v184\n\t" \
  "v_xor_b32 v190, 192, v184\n\t" \
  "v_xor_b32 v191, 224, v184\n\t" \
  "s_lshl_b32 s51, s98, 12\n\t" \
  "s_add_u32 s51, s51, s54\n\t" \
  "s_add_u32 s51, s51, 2097152\n\t" \
  "s_lshr_b32 s55, s87, 7\n\t" \
  "s_add_u32 s51, s51, s55\n\t" \
  "s_add_u32 s55, s42, s3\n\t" \
  "s_and_b32 s55, s55, 3\n\t" \
  "s_lshl2_add_u32 s55, s40, s55\n\t" \
  "s_lshl_b32 s55, s55, 9\n\t" \
  "s_add_u32 s53, s55, 2162688\n\t" \
  "v_min_u32 v49, 2, v49\n\t" \
  "s_bfe_u32 s55, s2, 0x10002\n\t" \
  "s_lshl2_add_u32 s55, s40, s55\n\t" \
  "s_add_u32 s55, s55, 4\n\t" \
  "v_lshl_add_u32 v49, v49, 1, s55\n\t" \
  "v_lshl_add_u32 v215, v49, 19, v54\n\t" \
  "v_add_u32 v216, 3145728, v215\n\t" \
  "v_and_b32 v215, 0x7fffff, v215\n\t" \
  "v_and_b32 v216, 0x7fffff, v216\n\t" \
  "s_mov_b32 s98, 0xffff0000\n\t" \
  "s_mov_b32 s45, 0x05040100\n\t" \
  "s_mov_b32 s50, 0x07060302\n\t" \
  "s_lshr_b32 s44, s2, 6\n\t" \
  "s_lshl_b32 s44, s44, 9\n\t" \
  "v_lshlrev_b32 v53, 1, v0\n\t" \
  "s_waitcnt vmcnt(9)\n\t" \
  "v_lshlrev_b32 v20, 16, v2\n\t" \
  "v_and_b32 v21, s98, v2\n\t" \
  "v_lshlrev_b32 v22, 16, v3\n\t" \
  "v_and_b32 v23, s98, v3\n\t" \
  "v_mul_f32 v24, 0xbfb8aa3b, v20\n\t" \
  "v_mul_f32 v25, 0xbfb8aa3b, v21\n\t" \
  "v_mul_f32 v26, 0xbfb8aa3b, v22\n\t" \
  "v_mul_f32 v27, 0xbfb8aa3b, v23\n\t" \
  "v_exp_f32 v24, v24\n\t" \
  "v_exp_f32 v25, v25\n\t" \
  "v_exp_f32 v26, v26\n\t" \
  "v_exp_f32 v27, v27\n\t" \
  "v_add_f32 v24, 1.0, v24\n\t" \
  "v_add_f32 v25, 1.0, v25\n\t" \
  "v_add_f32 v26, 1.0, v26\n\t" \
  "v_add_f32 v27, 1.0, v27\n\t" \
  "v_rcp_f32 v24, v24\n\t" \
  "v_rcp_f32 v25, v25\n\t" \
  "v_rcp_f32 v26, v26\n\t" \
  "v_rcp_f32 v27, v27\n\t" \
  "v_mul_f32 v20, v24, v20\n\t" \
  "v_mul_f32 v21, v25, v21\n\t" \
  "v_mul_f32 v22, v26, v22\n\t" \
  "v_mul_f32 v23, v27, v23\n\t" \
  "v_cvt_pk_bf16_f32 v2, v20, v21\n\t" \
  "v_cvt_pk_bf16_f32 v3, v22, v23\n\t" \
  FSTP("s_memrealtime s[94:95]\n\t") \
  FSTP("s_memtime s[92:93]\n\t") \
  FSTP("s_waitcnt lgkmcnt(0)\n\t") \
  FSTP("s_or_b32 s94, s94, 1\n\t") \
  FSTP("v_writelane_b32 v255, s94, 54\n\t") \
  FSTP("v_writelane_b32 v255, s92, 55\n\t") \
  "s_waitcnt vmcnt(5)\n\t" \
  FSTA("s_memrealtime s[94:95]\n\t") \
  FSTA("s_memtime s[92:93]\n\t") \
  FSTA("s_waitcnt lgkmcnt(0)\n\t") \
  FSTA("s_or_b32 s94, s94, 1\n\t") \
  FSTA("v_writelane_b32 v255, s94, 2\n\t") \
  FSTA("v_writelane_b32 v255, s92, 3\n\t") \
  "v_perm_b32 v28, v8, v4, s45\n\t" \
  "v_perm_b32 v29, v9, v5, s45\n\t" \
  "v_perm_b32 v30, v10, v6, s45\n\t" \
  "v_perm_b32 v31, v11, v7, s45\n\t" \
  "v_perm_b32 v4, v8, v4, s50\n\t" \
  "v_perm_b32 v5, v9, v5, s50\n\t" \
  "v_perm_b32 v6, v10, v6, s50\n\t" \
  "v_perm_b32 v7, v11, v7, s50\n\t" \
  "v_dot2_f32_bf16 v20, v28, v2, 0\n\t" \
  "v_dot2_f32_bf16 v21, v29, v2, 0\n\t" \
  "v_dot2_f32_bf16 v22, v30, v2, 0\n\t" \
  "v_dot2_f32_bf16 v23, v31, v2, 0\n\t" \
  "v_dot2_f32_bf16 v24, v4, v2, 0\n\t" \
  "v_dot2_f32_bf16 v25, v5, v2, 0\n\t" \
  "v_dot2_f32_bf16 v26, v6, v2, 0\n\t" \
  "v_dot2_f32_bf16 v27, v7, v2, 0\n\t" \
  "v_perm_b32 v28, v16, v12, s45\n\t" \
  "v_perm_b32 v29, v17, v13, s45\n\t" \
  "v_perm_b32 v30, v18, v14, s45\n\t" \
  "v_perm_b32 v31, v19, v15, s45\n\t" \
  "v_perm_b32 v12, v16, v12, s50\n\t" \
  "v_perm_b32 v13, v17, v13, s50\n\t" \
  "v_perm_b32 v14, v18, v14, s50\n\t" \
  "v_perm_b32 v15, v19, v15, s50\n\t" \
  "v_dot2_f32_bf16 v20, v28, v3, v20\n\t" \
  "v_dot2_f32_bf16 v21, v29, v3, v21\n\t" \
  "v_dot2_f32_bf16 v22, v30, v3, v22\n\t" \
  "v_dot2_f32_bf16 v23, v31, v3, v23\n\t" \
  "v_dot2_f32_bf16 v24, v12, v3, v24\n\t" \
  "v_dot2_f32_bf16 v25, v13, v3, v25\n\t" \
  "v_dot2_f32_bf16 v26, v14, v3, v26\n\t" \
  "v_dot2_f32_bf16 v27, v15, v3, v27\n\t" \
  "v_permlane32_swap_b32 v20, v22\n\t" \
  "v_permlane32_swap_b32 v21, v23\n\t" \
  "v_permlane32_swap_b32 v24, v26\n\t" \
  "v_permlane32_swap_b32 v25, v27\n\t" \
  "v_add_f32 v20, v20, v22\n\t" \
  "v_add_f32 v21, v21, v23\n\t" \
  "v_add_f32 v24, v24, v26\n\t" \
  "v_add_f32 v25, v25, v27\n\t" \
  "v_permlane16_swap_b32 v20, v21\n\t" \
  "s_nop 0\n\t" \
  "v_permlane16_swap_b32 v24, v25\n\t" \
  "v_add_f32 v20, v20, v21\n\t" \
  "v_add_f32 v21, v24, v25\n\t" \
  "s_nop 0\n\t" \
  "v_add_f32_dpp v20, v20, v20 row_ror:8 row_mask:0xf bank_mask:0xf\n\t" \
  "v_add_f32_dpp v21, v21, v21 row_ror:8 row_mask:0xf bank_mask:0xf\n\t" \
  "s_nop 0\n\t" \
  "v_add_f32_dpp v20, v20, v20 row_ror:4 row_mask:0xf bank_mask:0xf\n\t" \
  "v_add_f32_dpp v21, v21, v21 row_ror:4 row_mask:0xf bank_mask:0xf\n\t" \
  "ds_write_b64 v55, v[20:21]\n\t" \
  "buffer_load_dwordx4 v212, s[4:7], s57 offen lds\n\t" \
  "buffer_load_dwordx4 v213, s[4:7], s57 offen offset:1024 lds\n\t" \
  "s_add_u32 m0, s90, 2048\n\t" \
  "s_nop 0\n\t" \
  "buffer_load_dwordx4 v212, s[4:7], s58 offen lds\n\t" \
  "buffer_load_dwordx4 v213, s[4:7], s58 offen offset:1024 lds\n\t" \
  "s_xor_b32 m0, s90, 4096\n\t" \
  "s_xor_b32 s97, s57, 262144\n\t" \
  "buffer_load_dwordx4 v212, s[4:7], s97 offen lds\n\t" \
  "buffer_load_dwordx4 v213, s[4:7], s97 offen offset:1024 lds\n\t" \
  "s_xor_b32 m0, s90, 6144\n\t" \
  "s_xor_b32 s97, s57, 393216\n\t" \
  "buffer_load_dwordx4 v212, s[4:7], s97 offen lds\n\t" \
  "buffer_load_dwordx4 v213, s[4:7], s97 offen offset:1024 lds\n\t" \
  "v_mov_b32 v4, 0\n\t" \
  "v_mov_b32 v5, 0\n\t" \
  "v_mov_b32 v6, 0\n\t" \
  "v_mov_b32 v7, 0\n\t" \
  "s_waitcnt vmcnt(12) lgkmcnt(0)\n\t" \
  "s_barrier\n\t" \
  "ds_read_b64 v[8:9], v56 offset:0\n\t" \
  "ds_read_b64 v[10:11], v56 offset:256\n\t" \
  "ds_read_b64 v[12:13], v56 offset:512\n\t" \
  "ds_read_b64 v[14:15], v56 offset:768\n\t" \
  "v_readfirstlane_b32 s36, v1\n\t" \
  "s_or_b32 s36, s36, 0x40004000\n\t" \
  "s_add_u32 s36, s36, 0x9e3739b9\n\t" \
  "s_and_b32 s36, s36, 0xbfffbfff\n\t" \
  "v_mov_b32 v211, s36\n\t" \
  "v_mov_b32 v17, s36\n\t" \
  "v_mov_b32 v19, s36\n\t" \
  "s_waitcnt lgkmcnt(0)\n\t" \
  "v_add_f32 v8, v8, v10\n\t" \
  "v_add_f32 v12, v12, v14\n\t" \
  "v_add_f32 v9, v9, v11\n\t" \
  "v_add_f32 v13, v13, v15\n\t" \
  "v_add_f32 v16, v8, v12\n\t" \
  "v_add_f32 v18, v9, v13\n\t" \
  "buffer_store_dwordx4 v[16:19], v57, s[16:19], s52 offen sc1\n\t" \
  "s_waitcnt vmcnt(9)\n\t" \
  FSTA("s_memrealtime s[94:95]\n\t") \
  FSTA("s_memtime s[92:93]\n\t") \
  FSTA("s_waitcnt lgkmcnt(0)\n\t") \
  FSTA("s_or_b32 s94, s94, 1\n\t") \
  FSTA("v_writelane_b32 v255, s94, 4\n\t") \
  FSTA("v_writelane_b32 v255, s92, 5\n\t") \
  "v_dot2c_f32_bf16 v4, 0x3f803f80, v32\n\t" \
  "v_dot2c_f32_bf16 v6, v32, v32\n\t" \
  "v_dot2c_f32_bf16 v5, 0x3f803f80, v40\n\t" \
  "v_dot2c_f32_bf16 v7, v40, v40\n\t" \
  "v_dot2c_f32_bf16 v4, 0x3f803f80, v33\n\t" \
  "v_dot2c_f32_bf16 v6, v33, v33\n\t" \
  "v_dot2c_f32_bf16 v5, 0x3f803f80, v41\n\t" \
  "v_dot2c_f32_bf16 v7, v41, v41\n\t" \
  "v_dot2c_f32_bf16 v4, 0x3f803f80, v34\n\t" \
  "v_dot2c_f32_bf16 v6, v34, v34\n\t" \
  "v_dot2c_f32_bf16 v5, 0x3f803f80, v42\n\t" \
  "v_dot2c_f32_bf16 v7, v42, v42\n\t" \
  "v_dot2c_f32_bf16 v4, 0x3f803f80, v35\n\t" \
  "v_dot2c_f32_bf16 v6, v35, v35\n\t" \
  "v_dot2c_f32_bf16 v5, 0x3f803f80, v43\n\t" \
  "v_dot2c_f32_bf16 v7, v43, v43\n\t" \
  "v_dot2c_f32_bf16 v4, 0x3f803f80, v36\n\t" \
  "v_dot2c_f32_bf16 v6, v36, v36\n\t" \
  "v_dot2c_f32_bf16 v5, 0x3f803f80, v44\n\t" \
  "v_dot2c_f32_bf16 v7, v44, v44\n\t" \
  "v_dot2c_f32_bf16 v4, 0x3f803f80, v37\n\t" \
  "v_dot2c_f32_bf16 v6, v37, v37\n\t" \
  "v_dot2c_f32_bf16 v5, 0x3f803f80, v45\n\t" \
  "v_dot2c_f32_bf16 v7, v45, v45\n\t" \
  "v_dot2c_f32_bf16 v4, 0x3f803f80, v38\n\t" \
  "v_dot2c_f32_bf16 v6, v38, v38\n\t" \
  "v_dot2c_f32_bf16 v5, 0x3f803f80, v46\n\t" \
  "v_dot2c_f32_bf16 v7, v46, v46\n\t" \
  "v_dot2c_f32_bf16 v4, 0x3f803f80, v39\n\t" \
  "v_dot2c_f32_bf16 v6, v39, v39\n\t" \
  "v_dot2c_f32_bf16 v5, 0x3f803f80, v47\n\t" \
  "v_dot2c_f32_bf16 v7, v47, v47\n\t" \
  "v_mov_b32 v21, s36\n\t" \
  "v_mov_b32 v23, s36\n\t" \
  "v_permlane32_swap_b32 v4, v5\n\t" \
  "v_permlane32_swap_b32 v6, v7\n\t" \
  "v_add_f32 v4, v4, v5\n\t" \
  "v_add_f32 v6, v6, v7\n\t" \
  "s_nop 1\n\t" \
  "v_permlane16_swap_b32 v4, v6\n\t" \
  "v_add_f32 v4, v4, v6\n\t" \
  "s_nop 1\n\t" \
  "v_add_f32_dpp v4, v4, v4 quad_perm:[1,0,3,2] row_mask:0xf bank_mask:0xf\n\t" \
  "s_nop 1\n\t" \
  "v_add_f32_dpp v4, v4, v4 quad_perm:[2,3,0,1] row_mask:0xf bank_mask:0xf\n\t" \
  "s_nop 1\n\t" \
  "v_add_f32_dpp v4, v4, v4 row_half_mirror row_mask:0xf bank_mask:0xf\n\t" \
  "s_nop 1\n\t" \
  "v_add_f32_dpp v4, v4, v4 row_mirror row_mask:0xf bank_mask:0xf\n\t" \
  "v_mul_f32 v4, 0x3a800000, v4\n\t" \
  "v_mov_b32 v5, v4\n\t" \
  "s_nop 1\n\t" \
  "v_permlane16_swap_b32 v4, v5\n\t" \
  "v_fma_f32 v5, -v4, v4, v5\n\t" \
  "v_max_f32 v5, 0, v5\n\t" \
  "v_add_f32 v5, 0x3727c5ac, v5\n\t" \
  "v_rsq_f32 v20, v5\n\t" \
  "s_nop 0\n\t" \
  "v_mul_f32_e64 v22, v20, -v4\n\t" \
  "buffer_store_dwordx4 v[20:23], v58, s[16:19], s46 offen sc1\n\t" \
  FSTA("s_memrealtime s[94:95]\n\t") \
  FSTA("s_memtime s[92:93]\n\t") \
  FSTA("s_waitcnt lgkmcnt(0)\n\t") \
  FSTA("s_or_b32 s94, s94, 1\n\t") \
  FSTA("v_writelane_b32 v255, s94, 6\n\t") \
  FSTA("v_writelane_b32 v255, s92, 7\n\t") \
  "buffer_load_ushort v218, v53, s[12:15], s44 offen\n\t" \
  "s_add_u32 m0, s76, 0\n\t" \
  "s_nop 0\n\t" \
  "buffer_load_dwordx4 v200, s[20:23], s49 offen offset:0 lds\n\t" \
  "buffer_load_dwordx4 v201, s[20:23], s49 offen offset:1024 lds\n\t" \
  "buffer_load_dwordx4 v202, s[20:23], s49 offen offset:2048 lds\n\t" \
  "buffer_load_dwordx4 v203, s[20:23], s49 offen offset:3072 lds\n\t" \
  "s_add_u32 m0, s77, 49152\n\t" \
  "s_xor_b32 s54, s87, 0\n\t" \
  "s_or_b32 s54, s54, s48\n\t" \
  "buffer_load_dwordx4 v212, s[4:7], s54 offen lds\n\t" \
  "buffer_load_dwordx4 v213, s[4:7], s54 offen offset:1024 lds\n\t" \
  "s_add_u32 m0, s77, 51200\n\t" \
  "s_xor_b32 s54, s87, 131072\n\t" \
  "s_or_b32 s54, s54, s48\n\t" \
  "buffer_load_dwordx4 v212, s[4:7], s54 offen lds\n\t" \
  "buffer_load_dwordx4 v213, s[4:7], s54 offen offset:1024 lds\n\t" \
  "s_add_u32 m0, s77, 53248\n\t" \
  "s_xor_b32 s54, s87, 262144\n\t" \
  "s_or_b32 s54, s54, s48\n\t" \
  "buffer_load_dwordx4 v212, s[4:7], s54 offen lds\n\t" \
  "buffer_load_dwordx4 v213, s[4:7], s54 offen offset:1024 lds\n\t" \
  "s_add_u32 m0, s77, 55296\n\t" \
  "s_xor_b32 s54, s87, 393216\n\t" \
  "s_or_b32 s54, s54, s48\n\t" \
  "buffer_load_dwordx4 v212, s[4:7], s54 offen lds\n\t" \
  "buffer_load_dwordx4 v213, s[4:7], s54 offen offset:1024 lds\n\t" \
  "s_add_u32 m0, s76, 49152\n\t" \
  "s_nop 0\n\t" \
  "buffer_load_dwordx4 v200, s[20:23], s79 offen offset:0 lds\n\t" \
  "buffer_load_dwordx4 v201, s[20:23], s79 offen offset:1024 lds\n\t" \
  "buffer_load_dwordx4 v202, s[20:23], s79 offen offset:2048 lds\n\t" \
  "buffer_load_dwordx4 v203, s[20:23], s79 offen offset:3072 lds\n\t" \
  "v_add_u32 v192, 65536, v184\n\t" \
  "v_add_u32 v193, 65536, v185\n\t" \
  "v_add_u32 v194, 65536, v186\n\t" \
  "v_add_u32 v195, 65536, v187\n\t" \
  "v_add_u32 v196, 65536, v188\n\t" \
  "v_add_u32 v197, 65536, v189\n\t" \
  "v_add_u32 v198, 65536, v190\n\t" \
  "v_add_u32 v199, 65536, v191\n\t" \
  "v_lshrrev_b32 v48, 3, v253\n\t" \
  "s_lshr_b32 s54, s2, 6\n\t" \
  "v_lshl_add_u32 v48, s54, 5, v48\n\t" \
  "v_mul_u32_u24 v48, 98304, v48\n\t" \
  "v_and_b32 v49, 0x70, v244\n\t" \
  "s_bfe_u32 s54, s2, 0x10002\n\t" \
  "s_lshl1_add_u32 s54, s40, s54\n\t" \
  "s_lshl_b32 s54, s54, 10\n\t" \
  "s_and_b32 s55, s2, 0x38\n\t" \
  "s_lshl4_add_u32 s54, s55, s54\n\t" \
  "s_add_u32 s54, s54, 4096\n\t" \
  "v_add3_u32 v209, v48, v49, s54\n\t" \
  "v_lshlrev_b32 v219, 2, v253\n\t" \
  "v_add_u32 v219, 151552, v219\n\t" \
  "s_and_b32 s54, s39, 15\n\t" \
  "s_lshl2_add_u32 s54, s54, s3\n\t" \
  "s_lshl_b32 s54, s54, 11\n\t" \
  "s_lshl_b32 s55, s40, 19\n\t" \
  "s_add_u32 s54, s54, s55\n\t" \
  "v_lshl_add_u32 v217, v247, 7, s54\n\t" \
  "s_mov_b64 exec, 0\n\t" \
  "s_cmp_eq_u32 s3, 0\n\t" \
  "s_cbranch_scc1 .Lhc00\n\t" \
  "s_cmp_eq_u32 s3, 1\n\t" \
  "s_cbranch_scc1 .Lhc01\n\t" \
  "s_cmp_eq_u32 s3, 2\n\t" \
  "s_cbranch_scc1 .Lhc02\n\t" \
  "s_branch .Lhc03\n\t" \
  ".Lhr0:\n\t" \
  "s_mov_b64 exec, -1\n\t" \
  "s_sleep 6\n\t" \
  "s_add_u32 s58, s53, 16384\n\t" \
  "s_add_u32 s90, s53, 32768\n\t" \
  "s_add_u32 s91, s53, 49152\n\t" \
  "s_xor_b32 s96, s51, 0\n\t" \
  "s_xor_b32 s97, s51, 1024\n\t" \
  "s_xor_b32 s99, s51, 2048\n\t" \
  "s_xor_b32 s57, s51, 3072\n\t" \
  "buffer_load_dwordx4 v[128:131], v248, s[16:19], s53 offen sc1\n\t" \
  "buffer_load_dwordx4 v[132:135], v248, s[16:19], s58 offen sc1\n\t" \
  "buffer_load_dwordx4 v[136:139], v248, s[16:19], s90 offen sc1\n\t" \
  "buffer_load_dwordx4 v[140:143], v248, s[16:19], s91 offen sc1\n\t" \
  "buffer_load_dwordx4 v[160:163], v249, s[16:19], s96 offen sc1\n\t" \
  "buffer_load_dwordx4 v[164:167], v249, s[16:19], s97 offen sc1\n\t" \
  "buffer_load_dwordx4 v[168:171], v249, s[16:19], s99 offen sc1\n\t" \
  "buffer_load_dwordx4 v[172:175], v249, s[16:19], s57 offen sc1\n\t" \
  "s_bitcmp1_b32 s2, 7\n\t" \
  "s_cselect_b64 s[12:13], s[64:65], s[60:61]\n\t" \
  FSTP("s_memrealtime s[94:95]\n\t") \
  FSTP("s_memtime s[92:93]\n\t") \
  FSTP("s_waitcnt lgkmcnt(0)\n\t") \
  FSTP("s_or_b32 s94, s94, 1\n\t") \
  FSTP("v_writelane_b32 v255, s94, 56\n\t") \
  FSTP("v_writelane_b32 v255, s92, 57\n\t") \
  "s_waitcnt vmcnt(12)\n\t" \
  "ds_read_b128 v[16:19], v204 offset:0\n\t" \
  "ds_read_b128 v[48:51], v204 offset:2048\n\t" \
  "ds_read_b128 v[80:83], v204 offset:4096\n\t" \
  "ds_read_b128 v[112:115], v204 offset:6144\n\t" \
  "ds_read_b128 v[20:23], v205 offset:0\n\t" \
  "ds_read_b128 v[52:55], v205 offset:2048\n\t" \
  "ds_read_b128 v[84:87], v205 offset:4096\n\t" \
  "ds_read_b128 v[116:119], v205 offset:6144\n\t" \
  "ds_read_b128 v[24:27], v204 offset:49152\n\t" \
  "ds_read_b128 v[56:59], v204 offset:51200\n\t" \
  "ds_read_b128 v[88:91], v204 offset:53248\n\t" \
  "ds_read_b128 v[120:123], v204 offset:55296\n\t" \
  "ds_read_b128 v[28:31], v205 offset:49152\n\t" \
  "ds_read_b128 v[60:63], v205 offset:51200\n\t" \
  "ds_read_b128 v[92:95], v205 offset:53248\n\t" \
  "ds_read_b128 v[124:127], v205 offset:55296\n\t" \
  "v_lshlrev_b32 v218, 16, v218\n\t" \
  "v_mul_f32 v145, 0xbfb8aa3b, v218\n\t" \
  "v_exp_f32 v145, v145\n\t" \
  "s_nop 0\n\t" \
  "v_add_f32 v145, 1.0, v145\n\t" \
  "v_rcp_f32 v145, v145\n\t" \
  "s_nop 0\n\t" \
  "v_mul_f32 v218, v145, v218\n\t" \
  "v_cvt_pk_bf16_f32 v218, v218, v218\n\t" \
  "v_lshlrev_b32 v218, 16, v218\n\t" \
  "s_waitcnt lgkmcnt(12)\n\t" \
  "v_lshlrev_b32 v0, 16, v16\n\t" \
  "v_and_b32 v1, s98, v16\n\t" \
  "v_lshlrev_b32 v2, 16, v17\n\t" \
  "v_and_b32 v3, s98, v17\n\t" \
  "v_lshlrev_b32 v4, 16, v18\n\t" \
  "v_and_b32 v5, s98, v18\n\t" \
  "v_lshlrev_b32 v6, 16, v19\n\t" \
  "v_and_b32 v7, s98, v19\n\t" \
  "v_lshlrev_b32 v32, 16, v48\n\t" \
  "v_and_b32 v33, s98, v48\n\t" \
  "v_lshlrev_b32 v34, 16, v49\n\t" \
  "v_and_b32 v35, s98, v49\n\t" \
  "v_lshlrev_b32 v36, 16, v50\n\t" \
  "v_and_b32 v37, s98, v50\n\t" \
  "v_lshlrev_b32 v38, 16, v51\n\t" \
  "v_and_b32 v39, s98, v51\n\t" \
  "v_lshlrev_b32 v64, 16, v80\n\t" \
  "v_and_b32 v65, s98, v80\n\t" \
  "v_lshlrev_b32 v66, 16, v81\n\t" \
  "v_and_b32 v67, s98, v81\n\t" \
  "v_lshlrev_b32 v68, 16, v82\n\t" \
  "v_and_b32 v69, s98, v82\n\t" \
  "v_lshlrev_b32 v70, 16, v83\n\t" \
  "v_and_b32 v71, s98, v83\n\t" \
  "v_lshlrev_b32 v96, 16, v112\n\t" \
  "v_and_b32 v97, s98, v112\n\t" \
  "v_lshlrev_b32 v98, 16, v113\n\t" \
  "v_and_b32 v99, s98, v113\n\t" \
  "v_lshlrev_b32 v100, 16, v114\n\t" \
  "v_and_b32 v101, s98, v114\n\t" \
  "v_lshlrev_b32 v102, 16, v115\n\t" \
  "v_and_b32 v103, s98, v115\n\t" \
  "s_waitcnt lgkmcnt(8)\n\t" \
  "v_lshlrev_b32 v8, 16, v20\n\t" \
  "v_and_b32 v9, s98, v20\n\t" \
  "v_lshlrev_b32 v10, 16, v21\n\t" \
  "v_and_b32 v11, s98, v21\n\t" \
  "v_lshlrev_b32 v12, 16, v22\n\t" \
  "v_and_b32 v13, s98, v22\n\t" \
  "v_lshlrev_b32 v14, 16, v23\n\t" \
  "v_and_b32 v15, s98, v23\n\t" \
  "v_lshlrev_b32 v40, 16, v52\n\t" \
  "v_and_b32 v41, s98, v52\n\t" \
  "v_lshlrev_b32 v42, 16, v53\n\t" \
  "v_and_b32 v43, s98, v53\n\t" \
  "v_lshlrev_b32 v44, 16, v54\n\t" \
  "v_and_b32 v45, s98, v54\n\t" \
  "v_lshlrev_b32 v46, 16, v55\n\t" \
  "v_and_b32 v47, s98, v55\n\t" \
  "v_lshlrev_b32 v72, 16, v84\n\t" \
  "v_and_b32 v73, s98, v84\n\t" \
  "v_lshlrev_b32 v74, 16, v85\n\t" \
  "v_and_b32 v75, s98, v85\n\t" \
  "v_lshlrev_b32 v76, 16, v86\n\t" \
  "v_and_b32 v77, s98, v86\n\t" \
  "v_lshlrev_b32 v78, 16, v87\n\t" \
  "v_and_b32 v79, s98, v87\n\t" \
  "v_lshlrev_b32 v104, 16, v116\n\t" \
  "v_and_b32 v105, s98, v116\n\t" \
  "v_lshlrev_b32 v106, 16, v117\n\t" \
  "v_and_b32 v107, s98, v117\n\t" \
  "v_lshlrev_b32 v108, 16, v118\n\t" \
  "v_and_b32 v109, s98, v118\n\t" \
  "v_lshlrev_b32 v110, 16, v119\n\t" \
  "v_and_b32 v111, s98, v119\n\t" \
  FSTP("s_memrealtime s[94:95]\n\t") \
  FSTP("s_memtime s[92:93]\n\t") \
  FSTP("s_waitcnt lgkmcnt(0)\n\t") \
  FSTP("s_or_b32 s94, s94, 1\n\t") \
  FSTP("v_writelane_b32 v255, s94, 58\n\t") \
  FSTP("v_writelane_b32 v255, s92, 59\n\t") \
  "s_waitcnt vmcnt(0)\n\t" \
  "v_xor_b32 v144, v211, v129\n\t" \
  "v_bitop3_b32 v144, v144, v131, v211 bitop3:0xf6\n\t" \
  "v_bitop3_b32 v144, v144, v133, v211 bitop3:0xf6\n\t" \
  "v_bitop3_b32 v144, v144, v135, v211 bitop3:0xf6\n\t" \
  "v_bitop3_b32 v144, v144, v137, v211 bitop3:0xf6\n\t" \
  "v_bitop3_b32 v144, v144, v139, v211 bitop3:0xf6\n\t" \
  "v_bitop3_b32 v144, v144, v141, v211 bitop3:0xf6\n\t" \
  "v_bitop3_b32 v144, v144, v143, v211 bitop3:0xf6\n\t" \
  "v_bitop3_b32 v144, v144, v161, v211 bitop3:0xf6\n\t" \
  "v_bitop3_b32 v144, v144, v163, v211 bitop3:0xf6\n\t" \
  "v_bitop3_b32 v144, v144, v165, v211 bitop3:0xf6\n\t" \
  "v_bitop3_b32 v144, v144, v167, v211 bitop3:0xf6\n\t" \
  "v_bitop3_b32 v144, v144, v169, v211 bitop3:0xf6\n\t" \
  "v_bitop3_b32 v144, v144, v171, v211 bitop3:0xf6\n\t" \
  "v_bitop3_b32 v144, v144, v173, v211 bitop3:0xf6\n\t" \
  "v_bitop3_b32 v144, v144, v175, v211 bitop3:0xf6\n\t" \
  "v_cmp_ne_u32 vcc, 0, v144\n\t" \
  "v_add_f32 v146, v128, v132\n\t" \
  "v_add_f32 v148, v136, v140\n\t" \
  "v_add_f32 v147, v130, v134\n\t" \
  "v_add_f32 v149, v138, v142\n\t" \
  "v_add_f32 v146, v146, v148\n\t" \
  "v_add_f32 v147, v147, v149\n\t" \
  "s_cbranch_vccnz .Lfr_s\n\t" \
  ".Lfr_k:\n\t" \
  FSTA("s_memrealtime s[94:95]\n\t") \
  FSTA("s_memtime s[92:93]\n\t") \
  FSTA("s_waitcnt lgkmcnt(0)\n\t") \
  FSTA("s_or_b32 s94, s94, 1\n\t") \
  FSTA("v_writelane_b32 v255, s94, 8\n\t") \
  FSTA("v_writelane_b32 v255, s92, 9\n\t") \
  "v_cvt_pk_bf16_f32 v147, v146, v147\n\t" \
  "v_lshlrev_b32 v146, 16, v147\n\t" \
  "v_and_b32 v147, s98, v147\n\t" \
  "v_add_f32 v146, v146, v251\n\t" \
  "v_add_f32 v147, v147, v251\n\t" \
  "v_cvt_pk_bf16_f32 v147, v146, v147\n\t" \
  "v_lshlrev_b32 v146, 16, v147\n\t" \
  "v_and_b32 v147, s98, v147\n\t" \
  "v_mul_f32 v146, 0x3e0293ee, v146\n\t" \
  "v_mul_f32 v147, 0x3e0293ee, v147\n\t" \
  "ds_write_b64 v250, v[146:147]\n\t" \
  "v_pk_fma_f32 v[0:1], v[0:1], v[160:161], v[162:163] op_sel_hi:[1,0,0]\n\t" \
  "v_pk_fma_f32 v[2:3], v[2:3], v[160:161], v[162:163] op_sel_hi:[1,0,0]\n\t" \
  "v_pk_fma_f32 v[4:5], v[4:5], v[160:161], v[162:163] op_sel_hi:[1,0,0]\n\t" \
  "v_pk_fma_f32 v[6:7], v[6:7], v[160:161], v[162:163] op_sel_hi:[1,0,0]\n\t" \
  "v_pk_fma_f32 v[32:33], v[32:33], v[164:165], v[166:167] op_sel_hi:[1,0,0]\n\t" \
  "v_pk_fma_f32 v[34:35], v[34:35], v[164:165], v[166:167] op_sel_hi:[1,0,0]\n\t" \
  "v_pk_fma_f32 v[36:37], v[36:37], v[164:165], v[166:167] op_sel_hi:[1,0,0]\n\t" \
  "v_pk_fma_f32 v[38:39], v[38:39], v[164:165], v[166:167] op_sel_hi:[1,0,0]\n\t" \
  "v_pk_fma_f32 v[64:65], v[64:65], v[168:169], v[170:171] op_sel_hi:[1,0,0]\n\t" \
  "v_pk_fma_f32 v[66:67], v[66:67], v[168:169], v[170:171] op_sel_hi:[1,0,0]\n\t" \
  "v_pk_fma_f32 v[68:69], v[68:69], v[168:169], v[170:171] op_sel_hi:[1,0,0]\n\t" \
  "v_pk_fma_f32 v[70:71], v[70:71], v[168:169], v[170:171] op_sel_hi:[1,0,0]\n\t" \
  "v_pk_fma_f32 v[96:97], v[96:97], v[172:173], v[174:175] op_sel_hi:[1,0,0]\n\t" \
  "v_pk_fma_f32 v[98:99], v[98:99], v[172:173], v[174:175] op_sel_hi:[1,0,0]\n\t" \
  "v_pk_fma_f32 v[100:101], v[100:101], v[172:173], v[174:175] op_sel_hi:[1,0,0]\n\t" \
  "v_pk_fma_f32 v[102:103], v[102:103], v[172:173], v[174:175] op_sel_hi:[1,0,0]\n\t" \
  FSTP("s_memrealtime s[94:95]\n\t") \
  FSTP("s_memtime s[92:93]\n\t") \
  FSTP("s_waitcnt lgkmcnt(0)\n\t") \
  FSTP("s_or_b32 s94, s94, 1\n\t") \
  FSTP("v_writelane_b32 v255, s94, 24\n\t") \
  FSTP("v_writelane_b32 v255, s92, 25\n\t") \
  "s_waitcnt lgkmcnt(0)\n\t" \
  "s_barrier\n\t" \
  FSTP("s_memrealtime s[94:95]\n\t") \
  FSTP("s_memtime s[92:93]\n\t") \
  FSTP("s_waitcnt lgkmcnt(0)\n\t") \
  FSTP("s_or_b32 s94, s94, 1\n\t") \
  FSTP("v_writelane_b32 v255, s94, 26\n\t") \
  FSTP("v_writelane_b32 v255, s92, 27\n\t") \
  "ds_read_b128 v[128:131], v208 offset:0\n\t" \
  "ds_read_b128 v[132:135], v208 offset:16\n\t" \
  "ds_read_b128 v[136:139], v208 offset:256\n\t" \
  "ds_read_b128 v[140:143], v208 offset:272\n\t" \
  "ds_read_b128 v[144:147], v208 offset:128\n\t" \
  "ds_read_b128 v[148:151], v208 offset:144\n\t" \
  "ds_read_b128 v[152:155], v208 offset:384\n\t" \
  "ds_read_b128 v[156:159], v208 offset:400\n\t" \
  "global_load_dword v246, v215, s[20:21]\n\t" \
  "v_pk_fma_f32 v[8:9], v[8:9], v[160:161], v[162:163] op_sel_hi:[1,0,0]\n\t" \
  "v_pk_fma_f32 v[10:11], v[10:11], v[160:161], v[162:163] op_sel_hi:[1,0,0]\n\t" \
  "v_pk_fma_f32 v[12:13], v[12:13], v[160:161], v[162:163] op_sel_hi:[1,0,0]\n\t" \
  "v_pk_fma_f32 v[14:15], v[14:15], v[160:161], v[162:163] op_sel_hi:[1,0,0]\n\t" \
  "v_pk_fma_f32 v[40:41], v[40:41], v[164:165], v[166:167] op_sel_hi:[1,0,0]\n\t" \
  "v_pk_fma_f32 v[42:43], v[42:43], v[164:165], v[166:167] op_sel_hi:[1,0,0]\n\t" \
  "v_pk_fma_f32 v[44:45], v[44:45], v[164:165], v[166:167] op_sel_hi:[1,0,0]\n\t" \
  "v_pk_fma_f32 v[46:47], v[46:47], v[164:165], v[166:167] op_sel_hi:[1,0,0]\n\t" \
  "v_pk_fma_f32 v[72:73], v[72:73], v[168:169], v[170:171] op_sel_hi:[1,0,0]\n\t" \
  "v_pk_fma_f32 v[74:75], v[74:75], v[168:169], v[170:171] op_sel_hi:[1,0,0]\n\t" \
  "v_pk_fma_f32 v[76:77], v[76:77], v[168:169], v[170:171] op_sel_hi:[1,0,0]\n\t" \
  "v_pk_fma_f32 v[78:79], v[78:79], v[168:169], v[170:171] op_sel_hi:[1,0,0]\n\t" \
  "v_pk_fma_f32 v[104:105], v[104:105], v[172:173], v[174:175] op_sel_hi:[1,0,0]\n\t" \
  "v_pk_fma_f32 v[106:107], v[106:107], v[172:173], v[174:175] op_sel_hi:[1,0,0]\n\t" \
  "v_pk_fma_f32 v[108:109], v[108:109], v[172:173], v[174:175] op_sel_hi:[1,0,0]\n\t" \
  "v_pk_fma_f32 v[110:111], v[110:111], v[172:173], v[174:175] op_sel_hi:[1,0,0]\n\t" \
  "s_xor_b32 s97, s86, 0\n\t" \
  "s_waitcnt lgkmcnt(4)\n\t" \
  "v_pk_fma_f32 v[0:1], v[0:1], v[128:129], v[136:137]\n\t" \
  "v_pk_fma_f32 v[2:3], v[2:3], v[130:131], v[138:139]\n\t" \
  "v_pk_fma_f32 v[4:5], v[4:5], v[132:133], v[140:141]\n\t" \
  "v_pk_fma_f32 v[6:7], v[6:7], v[134:135], v[142:143]\n\t" \
  "v_cvt_pk_bf16_f32 v16, v0, v1\n\t" \
  "v_cvt_pk_bf16_f32 v17, v2, v3\n\t" \
  "v_cvt_pk_bf16_f32 v18, v4, v5\n\t" \
  "v_cvt_pk_bf16_f32 v19, v6, v7\n\t" \
  "buffer_store_dwordx4 v[16:19], v214, s[16:19], s97 offen sc1\n\t" \
  "s_xor_b32 s97, s86, 256\n\t" \
  "v_pk_fma_f32 v[32:33], v[32:33], v[128:129], v[136:137]\n\t" \
  "v_pk_fma_f32 v[34:35], v[34:35], v[130:131], v[138:139]\n\t" \
  "v_pk_fma_f32 v[36:37], v[36:37], v[132:133], v[140:141]\n\t" \
  "v_pk_fma_f32 v[38:39], v[38:39], v[134:135], v[142:143]\n\t" \
  "v_cvt_pk_bf16_f32 v48, v32, v33\n\t" \
  "v_cvt_pk_bf16_f32 v49, v34, v35\n\t" \
  "v_cvt_pk_bf16_f32 v50, v36, v37\n\t" \
  "v_cvt_pk_bf16_f32 v51, v38, v39\n\t" \
  "buffer_store_dwordx4 v[48:51], v214, s[16:19], s97 offen sc1\n\t" \
  "s_xor_b32 s97, s86, 512\n\t" \
  "v_pk_fma_f32 v[64:65], v[64:65], v[128:129], v[136:137]\n\t" \
  "v_pk_fma_f32 v[66:67], v[66:67], v[130:131], v[138:139]\n\t" \
  "v_pk_fma_f32 v[68:69], v[68:69], v[132:133], v[140:141]\n\t" \
  "v_pk_fma_f32 v[70:71], v[70:71], v[134:135], v[142:143]\n\t" \
  "v_cvt_pk_bf16_f32 v80, v64, v65\n\t" \
  "v_cvt_pk_bf16_f32 v81, v66, v67\n\t" \
  "v_cvt_pk_bf16_f32 v82, v68, v69\n\t" \
  "v_cvt_pk_bf16_f32 v83, v70, v71\n\t" \
  ".Lhs0:\n\ts_cbranch_execz .Lhs0n\n\t" \
  "buffer_store_dwordx4 v[80:83], v214, s[16:19], s97 offen sc1\n\t" \
  "s_xor_b32 s97, s86, 768\n\t" \
  "v_pk_fma_f32 v[96:97], v[96:97], v[128:129], v[136:137]\n\t" \
  "v_pk_fma_f32 v[98:99], v[98:99], v[130:131], v[138:139]\n\t" \
  "v_pk_fma_f32 v[100:101], v[100:101], v[132:133], v[140:141]\n\t" \
  "v_pk_fma_f32 v[102:103], v[102:103], v[134:135], v[142:143]\n\t" \
  "v_cvt_pk_bf16_f32 v112, v96, v97\n\t" \
  "v_cvt_pk_bf16_f32 v113, v98, v99\n\t" \
  "v_cvt_pk_bf16_f32 v114, v100, v101\n\t" \
  "v_cvt_pk_bf16_f32 v115, v102, v103\n\t" \
  "buffer_store_dwordx4 v[112:115], v214, s[16:19], s97 offen sc1\n\t" \
  "ds_read_b128 v[128:131], v208 offset:512\n\t" \
  "ds_read_b128 v[132:135], v208 offset:528\n\t" \
  "ds_read_b128 v[136:139], v208 offset:768\n\t" \
  "ds_read_b128 v[140:143], v208 offset:784\n\t" \
  FSTP("s_memrealtime s[94:95]\n\t") \
  FSTP("s_memtime s[92:93]\n\t") \
  "s_add_u32 m0, s77, 98304\n\t" \
  "s_xor_b32 s96, s87, 0\n\t" \
  "s_add_u32 s96, s96, s88\n\t" \
  "s_waitcnt lgkmcnt(4)\n\t" \
  "v_pk_fma_f32 v[8:9], v[8:9], v[144:145], v[152:153]\n\t" \
  "v_pk_fma_f32 v[10:11], v[10:11], v[146:147], v[154:155]\n\t" \
  "v_pk_fma_f32 v[12:13], v[12:13], v[148:149], v[156:157]\n\t" \
  "v_pk_fma_f32 v[14:15], v[14:15], v[150:151], v[158:159]\n\t" \
  "v_cvt_pk_bf16_f32 v20, v8, v9\n\t" \
  "v_cvt_pk_bf16_f32 v21, v10, v11\n\t" \
  "v_cvt_pk_bf16_f32 v22, v12, v13\n\t" \
  "v_cvt_pk_bf16_f32 v23, v14, v15\n\t" \
  "buffer_load_dwordx4 v212, s[4:7], s96 offen lds\n\t" \
  "v_pk_fma_f32 v[40:41], v[40:41], v[144:145], v[152:153]\n\t" \
  "v_pk_fma_f32 v[42:43], v[42:43], v[146:147], v[154:155]\n\t" \
  "v_pk_fma_f32 v[44:45], v[44:45], v[148:149], v[156:157]\n\t" \
  "v_pk_fma_f32 v[46:47], v[46:47], v[150:151], v[158:159]\n\t" \
  "v_cvt_pk_bf16_f32 v52, v40, v41\n\t" \
  "v_cvt_pk_bf16_f32 v53, v42, v43\n\t" \
  "v_cvt_pk_bf16_f32 v54, v44, v45\n\t" \
  "v_cvt_pk_bf16_f32 v55, v46, v47\n\t" \
  "buffer_load_dwordx4 v213, s[4:7], s96 offen offset:1024 lds\n\t" \
  "s_add_u32 m0, s77, 100352\n\t" \
  "s_xor_b32 s96, s87, 131072\n\t" \
  "s_add_u32 s96, s96, s88\n\t" \
  "v_pk_fma_f32 v[72:73], v[72:73], v[144:145], v[152:153]\n\t" \
  "v_pk_fma_f32 v[74:75], v[74:75], v[146:147], v[154:155]\n\t" \
  "v_pk_fma_f32 v[76:77], v[76:77], v[148:149], v[156:157]\n\t" \
  "v_pk_fma_f32 v[78:79], v[78:79], v[150:151], v[158:159]\n\t" \
  "v_cvt_pk_bf16_f32 v84, v72, v73\n\t" \
  "v_cvt_pk_bf16_f32 v85, v74, v75\n\t" \
  "v_cvt_pk_bf16_f32 v86, v76, v77\n\t" \
  "v_cvt_pk_bf16_f32 v87, v78, v79\n\t" \
  "buffer_load_dwordx4 v212, s[4:7], s96 offen lds\n\t" \
  "v_pk_fma_f32 v[104:105], v[104:105], v[144:145], v[152:153]\n\t" \
  "v_pk_fma_f32 v[106:107], v[106:107], v[146:147], v[154:155]\n\t" \
  "v_pk_fma_f32 v[108:109], v[108:109], v[148:149], v[156:157]\n\t" \
  "v_pk_fma_f32 v[110:111], v[110:111], v[150:151], v[158:159]\n\t" \
  "v_cvt_pk_bf16_f32 v116, v104, v105\n\t" \
  "v_cvt_pk_bf16_f32 v117, v106, v107\n\t" \
  "v_cvt_pk_bf16_f32 v118, v108, v109\n\t" \
  "v_cvt_pk_bf16_f32 v119, v110, v111\n\t" \
  "buffer_load_dwordx4 v213, s[4:7], s96 offen offset:1024 lds\n\t" \
  "ds_read_b128 v[144:147], v208 offset:640\n\t" \
  "ds_read_b128 v[148:151], v208 offset:656\n\t" \
  "ds_read_b128 v[152:155], v208 offset:896\n\t" \
  "ds_read_b128 v[156:159], v208 offset:912\n\t" \
  FSTP("v_writelane_b32 v255, s94, 28\n\t") \
  FSTP("v_writelane_b32 v255, s92, 29\n\t") \
  FSTP("s_memrealtime s[94:95]\n\t") \
  FSTP("s_memtime s[92:93]\n\t") \
  "s_add_u32 m0, s77, 102400\n\t" \
  "s_xor_b32 s96, s87, 262144\n\t" \
  "s_add_u32 s96, s96, s88\n\t" \
  "v_lshlrev_b32 v176, 16, v24\n\t" \
  "v_and_b32 v177, s98, v24\n\t" \
  "v_lshlrev_b32 v178, 16, v25\n\t" \
  "v_and_b32 v179, s98, v25\n\t" \
  "v_lshlrev_b32 v180, 16, v26\n\t" \
  "v_and_b32 v181, s98, v26\n\t" \
  "v_lshlrev_b32 v182, 16, v27\n\t" \
  "v_and_b32 v183, s98, v27\n\t" \
  "v_pk_fma_f32 v[176:177], v[176:177], v[160:161], v[162:163] op_sel_hi:[1,0,0]\n\t" \
  "v_pk_fma_f32 v[178:179], v[178:179], v[160:161], v[162:163] op_sel_hi:[1,0,0]\n\t" \
  "v_pk_fma_f32 v[180:181], v[180:181], v[160:161], v[162:163] op_sel_hi:[1,0,0]\n\t" \
  "v_pk_fma_f32 v[182:183], v[182:183], v[160:161], v[162:163] op_sel_hi:[1,0,0]\n\t" \
  "s_waitcnt lgkmcnt(4)\n\t" \
  "v_pk_fma_f32 v[176:177], v[176:177], v[128:129], v[136:137]\n\t" \
  "v_pk_fma_f32 v[178:179], v[178:179], v[130:131], v[138:139]\n\t" \
  "v_pk_fma_f32 v[180:181], v[180:181], v[132:133], v[140:141]\n\t" \
  "v_pk_fma_f32 v[182:183], v[182:183], v[134:135], v[142:143]\n\t" \
  "v_cvt_pk_bf16_f32 v24, v176, v177\n\t" \
  "v_cvt_pk_bf16_f32 v25, v178, v179\n\t" \
  "v_cvt_pk_bf16_f32 v26, v180, v181\n\t" \
  "v_cvt_pk_bf16_f32 v27, v182, v183\n\t" \
  "buffer_load_dwordx4 v212, s[4:7], s96 offen lds\n\t" \
  "v_lshlrev_b32 v176, 16, v56\n\t" \
  "v_and_b32 v177, s98, v56\n\t" \
  "v_lshlrev_b32 v178, 16, v57\n\t" \
  "v_and_b32 v179, s98, v57\n\t" \
  "v_lshlrev_b32 v180, 16, v58\n\t" \
  "v_and_b32 v181, s98, v58\n\t" \
  "v_lshlrev_b32 v182, 16, v59\n\t" \
  "v_and_b32 v183, s98, v59\n\t" \
  "v_pk_fma_f32 v[176:177], v[176:177], v[164:165], v[166:167] op_sel_hi:[1,0,0]\n\t" \
  "v_pk_fma_f32 v[178:179], v[178:179], v[164:165], v[166:167] op_sel_hi:[1,0,0]\n\t" \
  "v_pk_fma_f32 v[180:181], v[180:181], v[164:165], v[166:167] op_sel_hi:[1,0,0]\n\t" \
  "v_pk_fma_f32 v[182:183], v[182:183], v[164:165], v[166:167] op_sel_hi:[1,0,0]\n\t" \
  "v_pk_fma_f32 v[176:177], v[176:177], v[128:129], v[136:137]\n\t" \
  "v_pk_fma_f32 v[178:179], v[178:179], v[130:131], v[138:139]\n\t" \
  "v_pk_fma_f32 v[180:181], v[180:181], v[132:133], v[140:141]\n\t" \
  "v_pk_fma_f32 v[182:183], v[182:183], v[134:135], v[142:143]\n\t" \
  "v_cvt_pk_bf16_f32 v56, v176, v177\n\t" \
  "v_cvt_pk_bf16_f32 v57, v178, v179\n\t" \
  "v_cvt_pk_bf16_f32 v58, v180, v181\n\t" \
  "v_cvt_pk_bf16_f32 v59, v182, v183\n\t" \
  "buffer_load_dwordx4 v213, s[4:7], s96 offen offset:1024 lds\n\t" \
  "s_add_u32 m0, s77, 104448\n\t" \
  "s_xor_b32 s96, s87, 393216\n\t" \
  "s_add_u32 s96, s96, s88\n\t" \
  "v_lshlrev_b32 v176, 16, v88\n\t" \
  "v_and_b32 v177, s98, v88\n\t" \
  "v_lshlrev_b32 v178, 16, v89\n\t" \
  "v_and_b32 v179, s98, v89\n\t" \
  "v_lshlrev_b32 v180, 16, v90\n\t" \
  "v_and_b32 v181, s98, v90\n\t" \
  "v_lshlrev_b32 v182, 16, v91\n\t" \
  "v_and_b32 v183, s98, v91\n\t" \
  "v_pk_fma_f32 v[176:177], v[176:177], v[168:169], v[170:171] op_sel_hi:[1,0,0]\n\t" \
  "v_pk_fma_f32 v[178:179], v[178:179], v[168:169], v[170:171] op_sel_hi:[1,0,0]\n\t" \
  "v_pk_fma_f32 v[180:181], v[180:181], v[168:169], v[170:171] op_sel_hi:[1,0,0]\n\t" \
  "v_pk_fma_f32 v[182:183], v[182:183], v[168:169], v[170:171] op_sel_hi:[1,0,0]\n\t" \
  "v_pk_fma_f32 v[176:177], v[176:177], v[128:129], v[136:137]\n\t" \
  "v_pk_fma_f32 v[178:179], v[178:179], v[130:131], v[138:139]\n\t" \
  "v_pk_fma_f32 v[180:181], v[180:181], v[132:133], v[140:141]\n\t" \
  "v_pk_fma_f32 v[182:183], v[182:183], v[134:135], v[142:143]\n\t" \
  "v_cvt_pk_bf16_f32 v88, v176, v177\n\t" \
  "v_cvt_pk_bf16_f32 v89, v178, v179\n\t" \
  "v_cvt_pk_bf16_f32 v90, v180, v181\n\t" \
  "v_cvt_pk_bf16_f32 v91, v182, v183\n\t" \
  "buffer_load_dwordx4 v212, s[4:7], s96 offen lds\n\t" \
  "v_lshlrev_b32 v176, 16, v120\n\t" \
  "v_and_b32 v177, s98, v120\n\t" \
  "v_lshlrev_b32 v178, 16, v121\n\t" \
  "v_and_b32 v179, s98, v121\n\t" \
  "v_lshlrev_b32 v180, 16, v122\n\t" \
  "v_and_b32 v181, s98, v122\n\t" \
  "v_lshlrev_b32 v182, 16, v123\n\t" \
  "v_and_b32 v183, s98, v123\n\t" \
  "v_pk_fma_f32 v[176:177], v[176:177], v[172:173], v[174:175] op_sel_hi:[1,0,0]\n\t" \
  "v_pk_fma_f32 v[178:179], v[178:179], v[172:173], v[174:175] op_sel_hi:[1,0,0]\n\t" \
  "v_pk_fma_f32 v[180:181], v[180:181], v[172:173], v[174:175] op_sel_hi:[1,0,0]\n\t" \
  "v_pk_fma_f32 v[182:183], v[182:183], v[172:173], v[174:175] op_sel_hi:[1,0,0]\n\t" \
  "v_pk_fma_f32 v[176:177], v[176:177], v[128:129], v[136:137]\n\t" \
  "v_pk_fma_f32 v[178:179], v[178:179], v[130:131], v[138:139]\n\t" \
  "v_pk_fma_f32 v[180:181], v[180:181], v[132:133], v[140:141]\n\t" \
  "v_pk_fma_f32 v[182:183], v[182:183], v[134:135], v[142:143]\n\t" \
  "v_cvt_pk_bf16_f32 v120, v176, v177\n\t" \
  "v_cvt_pk_bf16_f32 v121, v178, v179\n\t" \
  "v_cvt_pk_bf16_f32 v122, v180, v181\n\t" \
  "v_cvt_pk_bf16_f32 v123, v182, v183\n\t" \
  "buffer_load_dwordx4 v213, s[4:7], s96 offen offset:1024 lds\n\t" \
  "ds_read_b128 v[128:131], v208 offset:1024\n\t" \
  "ds_read_b128 v[132:135], v208 offset:1040\n\t" \
  "ds_read_b128 v[136:139], v208 offset:1280\n\t" \
  "ds_read_b128 v[140:143], v208 offset:1296\n\t" \
  FSTP("v_writelane_b32 v255, s94, 30\n\t") \
  FSTP("v_writelane_b32 v255, s92, 31\n\t") \
  FSTP("s_memrealtime s[94:95]\n\t") \
  FSTP("s_memtime s[92:93]\n\t") \
  "s_add_u32 m0, s76, 98304\n\t" \
  "v_lshlrev_b32 v176, 16, v28\n\t" \
  "v_and_b32 v177, s98, v28\n\t" \
  "v_lshlrev_b32 v178, 16, v29\n\t" \
  "v_and_b32 v179, s98, v29\n\t" \
  "v_lshlrev_b32 v180, 16, v30\n\t" \
  "v_and_b32 v181, s98, v30\n\t" \
  "v_lshlrev_b32 v182, 16, v31\n\t" \
  "v_and_b32 v183, s98, v31\n\t" \
  "v_pk_fma_f32 v[176:177], v[176:177], v[160:161], v[162:163] op_sel_hi:[1,0,0]\n\t" \
  "v_pk_fma_f32 v[178:179], v[178:179], v[160:161], v[162:163] op_sel_hi:[1,0,0]\n\t" \
  "v_pk_fma_f32 v[180:181], v[180:181], v[160:161], v[162:163] op_sel_hi:[1,0,0]\n\t" \
  "v_pk_fma_f32 v[182:183], v[182:183], v[160:161], v[162:163] op_sel_hi:[1,0,0]\n\t" \
  "s_waitcnt lgkmcnt(4)\n\t" \
  "v_pk_fma_f32 v[176:177], v[176:177], v[144:145], v[152:153]\n\t" \
  "v_pk_fma_f32 v[178:179], v[178:179], v[146:147], v[154:155]\n\t" \
  "v_pk_fma_f32 v[180:181], v[180:181], v[148:149], v[156:157]\n\t" \
  "v_pk_fma_f32 v[182:183], v[182:183], v[150:151], v[158:159]\n\t" \
  "v_cvt_pk_bf16_f32 v28, v176, v177\n\t" \
  "v_cvt_pk_bf16_f32 v29, v178, v179\n\t" \
  "v_cvt_pk_bf16_f32 v30, v180, v181\n\t" \
  "v_cvt_pk_bf16_f32 v31, v182, v183\n\t" \
  "buffer_load_dwordx4 v200, s[20:23], s80 offen offset:0 lds\n\t" \
  "ds_read_b64_tr_b16 a[128:129], v184 offset:0\n\t" \
  "ds_read_b64_tr_b16 a[130:131], v184 offset:1024\n\t" \
  "ds_read_b64_tr_b16 a[132:133], v185 offset:0\n\t" \
  "ds_read_b64_tr_b16 a[134:135], v185 offset:1024\n\t" \
  "v_lshlrev_b32 v176, 16, v60\n\t" \
  "v_and_b32 v177, s98, v60\n\t" \
  "v_lshlrev_b32 v178, 16, v61\n\t" \
  "v_and_b32 v179, s98, v61\n\t" \
  "v_lshlrev_b32 v180, 16, v62\n\t" \
  "v_and_b32 v181, s98, v62\n\t" \
  "v_lshlrev_b32 v182, 16, v63\n\t" \
  "v_and_b32 v183, s98, v63\n\t" \
  "v_pk_fma_f32 v[176:177], v[176:177], v[164:165], v[166:167] op_sel_hi:[1,0,0]\n\t" \
  "v_pk_fma_f32 v[178:179], v[178:179], v[164:165], v[166:167] op_sel_hi:[1,0,0]\n\t" \
  "v_pk_fma_f32 v[180:181], v[180:181], v[164:165], v[166:167] op_sel_hi:[1,0,0]\n\t" \
  "v_pk_fma_f32 v[182:183], v[182:183], v[164:165], v[166:167] op_sel_hi:[1,0,0]\n\t" \
  "v_pk_fma_f32 v[176:177], v[176:177], v[144:145], v[152:153]\n\t" \
  "v_pk_fma_f32 v[178:179], v[178:179], v[146:147], v[154:155]\n\t" \
  "v_pk_fma_f32 v[180:181], v[180:181], v[148:149], v[156:157]\n\t" \
  "v_pk_fma_f32 v[182:183], v[182:183], v[150:151], v[158:159]\n\t" \
  "v_cvt_pk_bf16_f32 v60, v176, v177\n\t" \
  "v_cvt_pk_bf16_f32 v61, v178, v179\n\t" \
  "v_cvt_pk_bf16_f32 v62, v180, v181\n\t" \
  "v_cvt_pk_bf16_f32 v63, v182, v183\n\t" \
  "buffer_load_dwordx4 v201, s[20:23], s80 offen offset:1024 lds\n\t" \
  "ds_read_b64_tr_b16 a[136:137], v186 offset:0\n\t" \
  "ds_read_b64_tr_b16 a[138:139], v186 offset:1024\n\t" \
  "ds_read_b64_tr_b16 a[140:141], v187 offset:0\n\t" \
  "ds_read_b64_tr_b16 a[142:143], v187 offset:1024\n\t" \
  "v_lshlrev_b32 v176, 16, v92\n\t" \
  "v_and_b32 v177, s98, v92\n\t" \
  "v_lshlrev_b32 v178, 16, v93\n\t" \
  "v_and_b32 v179, s98, v93\n\t" \
  "v_lshlrev_b32 v180, 16, v94\n\t" \
  "v_and_b32 v181, s98, v94\n\t" \
  "v_lshlrev_b32 v182, 16, v95\n\t" \
  "v_and_b32 v183, s98, v95\n\t" \
  "v_pk_fma_f32 v[176:177], v[176:177], v[168:169], v[170:171] op_sel_hi:[1,0,0]\n\t" \
  "v_pk_fma_f32 v[178:179], v[178:179], v[168:169], v[170:171] op_sel_hi:[1,0,0]\n\t" \
  "v_pk_fma_f32 v[180:181], v[180:181], v[168:169], v[170:171] op_sel_hi:[1,0,0]\n\t" \
  "v_pk_fma_f32 v[182:183], v[182:183], v[168:169], v[170:171] op_sel_hi:[1,0,0]\n\t" \
  "v_pk_fma_f32 v[176:177], v[176:177], v[144:145], v[152:153]\n\t" \
  "v_pk_fma_f32 v[178:179], v[178:179], v[146:147], v[154:155]\n\t" \
  "v_pk_fma_f32 v[180:181], v[180:181], v[148:149], v[156:157]\n\t" \
  "v_pk_fma_f32 v[182:183], v[182:183], v[150:151], v[158:159]\n\t" \
  "v_cvt_pk_bf16_f32 v92, v176, v177\n\t" \
  "v_cvt_pk_bf16_f32 v93, v178, v179\n\t" \
  "v_cvt_pk_bf16_f32 v94, v180, v181\n\t" \
  "v_cvt_pk_bf16_f32 v95, v182, v183\n\t" \
  "buffer_load_dwordx4 v202, s[20:23], s80 offen offset:2048 lds\n\t" \
  "ds_read_b64_tr_b16 a[144:145], v188 offset:0\n\t" \
  "ds_read_b64_tr_b16 a[146:147], v188 offset:1024\n\t" \
  "ds_read_b64_tr_b16 a[148:149], v189 offset:0\n\t" \
  "ds_read_b64_tr_b16 a[150:151], v189 offset:1024\n\t" \
  "v_lshlrev_b32 v176, 16, v124\n\t" \
  "v_and_b32 v177, s98, v124\n\t" \
  "v_lshlrev_b32 v178, 16, v125\n\t" \
  "v_and_b32 v179, s98, v125\n\t" \
  "v_lshlrev_b32 v180, 16, v126\n\t" \
  "v_and_b32 v181, s98, v126\n\t" \
  "v_lshlrev_b32 v182, 16, v127\n\t" \
  "v_and_b32 v183, s98, v127\n\t" \
  "v_pk_fma_f32 v[176:177], v[176:177], v[172:173], v[174:175] op_sel_hi:[1,0,0]\n\t" \
  "v_pk_fma_f32 v[178:179], v[178:179], v[172:173], v[174:175] op_sel_hi:[1,0,0]\n\t" \
  "v_pk_fma_f32 v[180:181], v[180:181], v[172:173], v[174:175] op_sel_hi:[1,0,0]\n\t" \
  "v_pk_fma_f32 v[182:183], v[182:183], v[172:173], v[174:175] op_sel_hi:[1,0,0]\n\t" \
  "v_pk_fma_f32 v[176:177], v[176:177], v[144:145], v[152:153]\n\t" \
  "v_pk_fma_f32 v[178:179], v[178:179], v[146:147], v[154:155]\n\t" \
  "v_pk_fma_f32 v[180:181], v[180:181], v[148:149], v[156:157]\n\t" \
  "v_pk_fma_f32 v[182:183], v[182:183], v[150:151], v[158:159]\n\t" \
  "v_cvt_pk_bf16_f32 v124, v176, v177\n\t" \
  "v_cvt_pk_bf16_f32 v125, v178, v179\n\t" \
  "v_cvt_pk_bf16_f32 v126, v180, v181\n\t" \
  "v_cvt_pk_bf16_f32 v127, v182, v183\n\t" \
  "buffer_load_dwordx4 v203, s[20:23], s80 offen offset:3072 lds\n\t" \
  "ds_read_b64_tr_b16 a[152:153], v190 offset:0\n\t" \
  "ds_read_b64_tr_b16 a[154:155], v190 offset:1024\n\t" \
  "ds_read_b64_tr_b16 a[156:157], v191 offset:0\n\t" \
  "ds_read_b64_tr_b16 a[158:159], v191 offset:1024\n\t" \
  "ds_read_b128 v[144:147], v208 offset:1152\n\t" \
  "ds_read_b128 v[148:151], v208 offset:1168\n\t" \
  "ds_read_b128 v[152:155], v208 offset:1408\n\t" \
  "ds_read_b128 v[156:159], v208 offset:1424\n\t" \
  FSTP("v_writelane_b32 v255, s94, 32\n\t") \
  FSTP("v_writelane_b32 v255, s92, 33\n\t") \
  FSTP("s_memrealtime s[94:95]\n\t") \
  FSTP("s_memtime s[92:93]\n\t") \
  "s_waitcnt vmcnt(12)\n\t" \
  "s_mov_b64 exec, 1\n\t" \
  "buffer_store_dword v211, off, s[16:19], s85 sc1\n\t" \
  "s_mov_b64 exec, -1\n\t" \
  FSTP("v_writelane_b32 v255, s94, 34\n\t") \
  FSTP("v_writelane_b32 v255, s92, 35\n\t") \
  FSTP("s_memrealtime s[94:95]\n\t") \
  FSTP("s_memtime s[92:93]\n\t") \
  "s_waitcnt lgkmcnt(4)\n\t" \
  "v_mfma_f32_16x16x32_bf16 v[0:3], a[128:131], v[16:19], 0\n\t" \
  "s_add_u32 m0, s77, 0\n\t" \
  "s_xor_b32 s96, s87, 0\n\t" \
  "s_add_u32 s96, s96, s89\n\t" \
  "v_mfma_f32_16x16x32_bf16 v[32:35], a[128:131], v[48:51], 0\n\t" \
  "buffer_load_dwordx4 v212, s[4:7], s96 offen lds\n\t" \
  "v_mfma_f32_16x16x32_bf16 v[64:67], a[128:131], v[80:83], 0\n\t" \
  "ds_read_b64_tr_b16 a[160:161], v184 offset:8192\n\t" \
  "v_mfma_f32_16x16x32_bf16 v[96:99], a[128:131], v[112:115], 0\n\t" \
  "ds_read_b64_tr_b16 a[162:163], v184 offset:9216\n\t" \
  "v_mfma_f32_16x16x32_bf16 v[100:103], a[132:135], v[112:115], 0\n\t" \
  "buffer_load_dwordx4 v213, s[4:7], s96 offen offset:1024 lds\n\t" \
  "v_mfma_f32_16x16x32_bf16 v[68:71], a[132:135], v[80:83], 0\n\t" \
  "s_add_u32 m0, s77, 2048\n\t" \
  "ds_read_b64_tr_b16 a[164:165], v185 offset:8192\n\t" \
  "s_xor_b32 s96, s87, 131072\n\t" \
  "v_mfma_f32_16x16x32_bf16 v[36:39], a[132:135], v[48:51], 0\n\t" \
  "ds_read_b64_tr_b16 a[166:167], v185 offset:9216\n\t" \
  "s_add_u32 s96, s96, s89\n\t" \
  "v_mfma_f32_16x16x32_bf16 v[4:7], a[132:135], v[16:19], 0\n\t" \
  "ds_read_b64_tr_b16 a[168:169], v186 offset:8192\n\t" \
  "v_mfma_f32_16x16x32_bf16 v[8:11], a[136:139], v[16:19], 0\n\t" \
  "buffer_load_dwordx4 v212, s[4:7], s96 offen lds\n\t" \
  "v_mfma_f32_16x16x32_bf16 v[40:43], a[136:139], v[48:51], 0\n\t" \
  "ds_read_b64_tr_b16 a[170:171], v186 offset:9216\n\t" \
  "v_mfma_f32_16x16x32_bf16 v[72:75], a[136:139], v[80:83], 0\n\t" \
  "ds_read_b64_tr_b16 a[172:173], v187 offset:8192\n\t" \
  "v_mfma_f32_16x16x32_bf16 v[104:107], a[136:139], v[112:115], 0\n\t" \
  "buffer_load_dwordx4 v213, s[4:7], s96 offen offset:1024 lds\n\t" \
  "v_mfma_f32_16x16x32_bf16 v[108:111], a[140:143], v[112:115], 0\n\t" \
  "s_add_u32 m0, s77, 4096\n\t" \
  "ds_read_b64_tr_b16 a[174:175], v187 offset:9216\n\t" \
  "s_xor_b32 s96, s87, 262144\n\t" \
  "v_mfma_f32_16x16x32_bf16 v[76:79], a[140:143], v[80:83], 0\n\t" \
  "ds_read_b64_tr_b16 a[176:177], v188 offset:8192\n\t" \
  "s_add_u32 s96, s96, s89\n\t" \
  "v_mfma_f32_16x16x32_bf16 v[44:47], a[140:143], v[48:51], 0\n\t" \
  "buffer_load_dwordx4 v212, s[4:7], s96 offen lds\n\t" \
  "v_mfma_f32_16x16x32_bf16 v[12:15], a[140:143], v[16:19], 0\n\t" \
  "ds_read_b64_tr_b16 a[178:179], v188 offset:9216\n\t" \
  "v_mfma_f32_16x16x32_bf16 a[64:67], a[144:147], v[16:19], 0\n\t" \
  "ds_read_b64_tr_b16 a[180:181], v189 offset:8192\n\t" \
  "v_mfma_f32_16x16x32_bf16 a[68:71], a[144:147], v[48:51], 0\n\t" \
  "buffer_load_dwordx4 v213, s[4:7], s96 offen offset:1024 lds\n\t" \
  "v_mfma_f32_16x16x32_bf16 a[72:75], a[144:147], v[80:83], 0\n\t" \
  "s_add_u32 m0, s77, 6144\n\t" \
  "ds_read_b64_tr_b16 a[182:183], v189 offset:9216\n\t" \
  "s_xor_b32 s96, s87, 393216\n\t" \
  "v_mfma_f32_16x16x32_bf16 a[76:79], a[144:147], v[112:115], 0\n\t" \
  "ds_read_b64_tr_b16 a[184:185], v190 offset:8192\n\t" \
  "s_add_u32 s96, s96, s89\n\t" \
  "v_mfma_f32_16x16x32_bf16 a[92:95], a[148:151], v[112:115], 0\n\t" \
  "ds_read_b64_tr_b16 a[186:187], v190 offset:9216\n\t" \
  "v_mfma_f32_16x16x32_bf16 a[88:91], a[148:151], v[80:83], 0\n\t" \
  "buffer_load_dwordx4 v212, s[4:7], s96 offen lds\n\t" \
  "v_mfma_f32_16x16x32_bf16 a[84:87], a[148:151], v[48:51], 0\n\t" \
  "ds_read_b64_tr_b16 a[188:189], v191 offset:8192\n\t" \
  "v_mfma_f32_16x16x32_bf16 a[80:83], a[148:151], v[16:19], 0\n\t" \
  "ds_read_b64_tr_b16 a[190:191], v191 offset:9216\n\t" \
  "v_mfma_f32_16x16x32_bf16 a[96:99], a[152:155], v[16:19], 0\n\t" \
  "v_mfma_f32_16x16x32_bf16 a[100:103], a[152:155], v[48:51], 0\n\t" \
  "buffer_load_dwordx4 v213, s[4:7], s96 offen offset:1024 lds\n\t" \
  "v_mfma_f32_16x16x32_bf16 a[104:107], a[152:155], v[80:83], 0\n\t" \
  "v_mfma_f32_16x16x32_bf16 a[108:111], a[152:155], v[112:115], 0\n\t" \
  "v_mfma_f32_16x16x32_bf16 a[124:127], a[156:159], v[112:115], 0\n\t" \
  "v_mfma_f32_16x16x32_bf16 a[120:123], a[156:159], v[80:83], 0\n\t" \
  "s_add_u32 m0, s76, 0\n\t" \
  "v_mfma_f32_16x16x32_bf16 a[116:119], a[156:159], v[48:51], 0\n\t" \
  "s_waitcnt lgkmcnt(0)\n\t" \
  "v_mfma_f32_16x16x32_bf16 a[112:115], a[156:159], v[16:19], 0\n\t" \
  "s_waitcnt vmcnt(13)\n\t" \
  "ds_read_b128 v[16:19], v206 offset:0\n\t" \
  "ds_read_b128 v[48:51], v206 offset:2048\n\t" \
  "s_barrier\n\t" \
  FSTP("v_writelane_b32 v255, s94, 36\n\t") \
  FSTP("v_writelane_b32 v255, s92, 37\n\t") \
  FSTP("s_memrealtime s[94:95]\n\t") \
  FSTP("s_memtime s[92:93]\n\t") \
  "v_mfma_f32_16x16x32_bf16 v[0:3], a[160:163], v[20:23], v[0:3]\n\t" \
  "buffer_load_dwordx4 v200, s[20:23], s81 offen offset:0 lds\n\t" \
  "ds_read_b128 v[80:83], v206 offset:4096\n\t" \
  "ds_read_b128 v[112:115], v206 offset:6144\n\t" \
  "v_mfma_f32_16x16x32_bf16 v[32:35], a[160:163], v[52:55], v[32:35]\n\t" \
  "ds_read_b64_tr_b16 a[128:129], v184 offset:49152\n\t" \
  "v_mfma_f32_16x16x32_bf16 v[64:67], a[160:163], v[84:87], v[64:67]\n\t" \
  "ds_read_b64_tr_b16 a[130:131], v184 offset:50176\n\t" \
  "v_mfma_f32_16x16x32_bf16 v[96:99], a[160:163], v[116:119], v[96:99]\n\t" \
  "buffer_load_dwordx4 v201, s[20:23], s81 offen offset:1024 lds\n\t" \
  "v_mfma_f32_16x16x32_bf16 v[100:103], a[164:167], v[116:119], v[100:103]\n\t" \
  "ds_read_b64_tr_b16 a[132:133], v185 offset:49152\n\t" \
  "v_mfma_f32_16x16x32_bf16 v[68:71], a[164:167], v[84:87], v[68:71]\n\t" \
  "s_waitcnt lgkmcnt(3)\n\t" \
  "v_lshlrev_b32 v176, 16, v16\n\t" \
  "v_and_b32 v177, s98, v16\n\t" \
  "v_mfma_f32_16x16x32_bf16 v[36:39], a[164:167], v[52:55], v[36:39]\n\t" \
  "ds_read_b64_tr_b16 a[134:135], v185 offset:50176\n\t" \
  "v_lshlrev_b32 v178, 16, v17\n\t" \
  "v_mfma_f32_16x16x32_bf16 v[4:7], a[164:167], v[20:23], v[4:7]\n\t" \
  "v_and_b32 v179, s98, v17\n\t" \
  "v_lshlrev_b32 v180, 16, v18\n\t" \
  "v_mfma_f32_16x16x32_bf16 v[8:11], a[168:171], v[20:23], v[8:11]\n\t" \
  "buffer_load_dwordx4 v202, s[20:23], s81 offen offset:2048 lds\n\t" \
  "v_mfma_f32_16x16x32_bf16 v[40:43], a[168:171], v[52:55], v[40:43]\n\t" \
  "ds_read_b64_tr_b16 a[136:137], v186 offset:49152\n\t" \
  "v_and_b32 v181, s98, v18\n\t" \
  "v_mfma_f32_16x16x32_bf16 v[72:75], a[168:171], v[84:87], v[72:75]\n\t" \
  "ds_read_b64_tr_b16 a[138:139], v186 offset:50176\n\t" \
  "v_lshlrev_b32 v182, 16, v19\n\t" \
  "v_mfma_f32_16x16x32_bf16 v[104:107], a[168:171], v[116:119], v[104:107]\n\t" \
  "ds_read_b64_tr_b16 a[140:141], v187 offset:49152\n\t" \
  "v_and_b32 v183, s98, v19\n\t" \
  "v_mfma_f32_16x16x32_bf16 v[108:111], a[172:175], v[116:119], v[108:111]\n\t" \
  "buffer_load_dwordx4 v203, s[20:23], s81 offen offset:3072 lds\n\t" \
  "v_mfma_f32_16x16x32_bf16 v[76:79], a[172:175], v[84:87], v[76:79]\n\t" \
  "s_add_u32 s96, s78, 2097152\n\t" \
  "s_and_b32 s96, s96, 0x780000\n\t" \
  "s_add_u32 m0, s76, 16384\n\t" \
  "v_mfma_f32_16x16x32_bf16 v[44:47], a[172:175], v[52:55], v[44:47]\n\t" \
  "ds_read_b64_tr_b16 a[142:143], v187 offset:50176\n\t" \
  "v_lshlrev_b32 v220, 16, v48\n\t" \
  "v_and_b32 v221, s98, v48\n\t" \
  "v_mfma_f32_16x16x32_bf16 v[12:15], a[172:175], v[20:23], v[12:15]\n\t" \
  "buffer_load_dwordx4 v200, s[20:23], s96 offen offset:0 lds\n\t" \
  "v_mfma_f32_16x16x32_bf16 a[64:67], a[176:179], v[20:23], a[64:67]\n\t" \
  "ds_read_b64_tr_b16 a[144:145], v188 offset:49152\n\t" \
  "v_lshlrev_b32 v222, 16, v49\n\t" \
  "v_mfma_f32_16x16x32_bf16 a[68:71], a[176:179], v[52:55], a[68:71]\n\t" \
  "ds_read_b64_tr_b16 a[146:147], v188 offset:50176\n\t" \
  "v_and_b32 v223, s98, v49\n\t" \
  "v_mfma_f32_16x16x32_bf16 a[72:75], a[176:179], v[84:87], a[72:75]\n\t" \
  "ds_read_b64_tr_b16 a[148:149], v189 offset:49152\n\t" \
  "v_lshlrev_b32 v224, 16, v50\n\t" \
  "v_mfma_f32_16x16x32_bf16 a[76:79], a[176:179], v[116:119], a[76:79]\n\t" \
  "buffer_load_dwordx4 v201, s[20:23], s96 offen offset:1024 lds\n\t" \
  "v_mfma_f32_16x16x32_bf16 a[92:95], a[180:183], v[116:119], a[92:95]\n\t" \
  "ds_read_b64_tr_b16 a[150:151], v189 offset:50176\n\t" \
  "v_and_b32 v225, s98, v50\n\t" \
  "v_mfma_f32_16x16x32_bf16 a[88:91], a[180:183], v[84:87], a[88:91]\n\t" \
  "ds_read_b64_tr_b16 a[152:153], v190 offset:49152\n\t" \
  "v_lshlrev_b32 v226, 16, v51\n\t" \
  "v_mfma_f32_16x16x32_bf16 a[84:87], a[180:183], v[52:55], a[84:87]\n\t" \
  "ds_read_b64_tr_b16 a[154:155], v190 offset:50176\n\t" \
  "v_and_b32 v227, s98, v51\n\t" \
  "v_mfma_f32_16x16x32_bf16 a[80:83], a[180:183], v[20:23], a[80:83]\n\t" \
  "buffer_load_dwordx4 v202, s[20:23], s96 offen offset:2048 lds\n\t" \
  "ds_read_b64_tr_b16 a[156:157], v191 offset:49152\n\t" \
  "ds_read_b64_tr_b16 a[158:159], v191 offset:50176\n\t" \
  "v_lshlrev_b32 v228, 16, v80\n\t" \
  "v_pk_fma_f32 v[176:177], v[176:177], v[160:161], v[162:163] op_sel_hi:[1,0,0]\n\t" \
  "v_pk_fma_f32 v[178:179], v[178:179], v[160:161], v[162:163] op_sel_hi:[1,0,0]\n\t" \
  "v_pk_fma_f32 v[180:181], v[180:181], v[160:161], v[162:163] op_sel_hi:[1,0,0]\n\t" \
  "v_pk_fma_f32 v[182:183], v[182:183], v[160:161], v[162:163] op_sel_hi:[1,0,0]\n\t" \
  "v_pk_fma_f32 v[176:177], v[176:177], v[128:129], v[136:137]\n\t" \
  "v_pk_fma_f32 v[178:179], v[178:179], v[130:131], v[138:139]\n\t" \
  "v_pk_fma_f32 v[180:181], v[180:181], v[132:133], v[140:141]\n\t" \
  "v_pk_fma_f32 v[182:183], v[182:183], v[134:135], v[142:143]\n\t" \
  "v_pk_fma_f32 v[220:221], v[220:221], v[164:165], v[166:167] op_sel_hi:[1,0,0]\n\t" \
  "v_pk_fma_f32 v[222:223], v[222:223], v[164:165], v[166:167] op_sel_hi:[1,0,0]\n\t" \
  "v_pk_fma_f32 v[224:225], v[224:225], v[164:165], v[166:167] op_sel_hi:[1,0,0]\n\t" \
  "v_pk_fma_f32 v[226:227], v[226:227], v[164:165], v[166:167] op_sel_hi:[1,0,0]\n\t" \
  "v_pk_fma_f32 v[220:221], v[220:221], v[128:129], v[136:137]\n\t" \
  "v_pk_fma_f32 v[222:223], v[222:223], v[130:131], v[138:139]\n\t" \
  "v_pk_fma_f32 v[224:225], v[224:225], v[132:133], v[140:141]\n\t" \
  "v_pk_fma_f32 v[226:227], v[226:227], v[134:135], v[142:143]\n\t" \
  "v_cvt_pk_bf16_f32 v16, v176, v177\n\t" \
  "v_mfma_f32_16x16x32_bf16 a[96:99], a[184:187], v[20:23], a[96:99]\n\t" \
  "v_cvt_pk_bf16_f32 v17, v178, v179\n\t" \
  "v_cvt_pk_bf16_f32 v18, v180, v181\n\t" \
  "v_and_b32 v229, s98, v80\n\t" \
  "v_mfma_f32_16x16x32_bf16 a[100:103], a[184:187], v[52:55], a[100:103]\n\t" \
  "v_cvt_pk_bf16_f32 v19, v182, v183\n\t" \
  "v_cvt_pk_bf16_f32 v48, v220, v221\n\t" \
  "v_lshlrev_b32 v230, 16, v81\n\t" \
  "v_and_b32 v231, s98, v81\n\t" \
  "v_mfma_f32_16x16x32_bf16 a[104:107], a[184:187], v[84:87], a[104:107]\n\t" \
  "v_lshlrev_b32 v232, 16, v82\n\t" \
  "v_and_b32 v233, s98, v82\n\t" \
  "v_mfma_f32_16x16x32_bf16 a[108:111], a[184:187], v[116:119], a[108:111]\n\t" \
  "buffer_load_dwordx4 v203, s[20:23], s96 offen offset:3072 lds\n\t" \
  "v_mfma_f32_16x16x32_bf16 a[124:127], a[188:191], v[116:119], a[124:127]\n\t" \
  "v_lshlrev_b32 v234, 16, v83\n\t" \
  "v_and_b32 v235, s98, v83\n\t" \
  "v_mfma_f32_16x16x32_bf16 a[120:123], a[188:191], v[84:87], a[120:123]\n\t" \
  "s_add_u32 s96, s78, 2621440\n\t" \
  "v_lshlrev_b32 v236, 16, v112\n\t" \
  "v_and_b32 v237, s98, v112\n\t" \
  "v_mfma_f32_16x16x32_bf16 a[116:119], a[188:191], v[52:55], a[116:119]\n\t" \
  "s_and_b32 s96, s96, 0x780000\n\t" \
  "s_add_u32 m0, s76, 32768\n\t" \
  "v_lshlrev_b32 v238, 16, v113\n\t" \
  "v_mfma_f32_16x16x32_bf16 a[112:115], a[188:191], v[20:23], a[112:115]\n\t" \
  "s_waitcnt lgkmcnt(0)\n\t" \
  "ds_read_b128 v[20:23], v207 offset:0\n\t" \
  "ds_read_b128 v[52:55], v207 offset:2048\n\t" \
  "ds_read_b128 v[84:87], v207 offset:4096\n\t" \
  "v_and_b32 v239, s98, v113\n\t" \
  FSTP("v_writelane_b32 v255, s94, 38\n\t") \
  FSTP("v_writelane_b32 v255, s92, 39\n\t") \
  FSTP("s_memrealtime s[94:95]\n\t") \
  FSTP("s_memtime s[92:93]\n\t") \
  "v_mfma_f32_16x16x32_bf16 v[0:3], a[128:131], v[24:27], v[0:3]\n\t" \
  "buffer_load_dwordx4 v200, s[20:23], s96 offen offset:0 lds\n\t" \
  "ds_read_b128 v[116:119], v207 offset:6144\n\t" \
  "v_mfma_f32_16x16x32_bf16 v[32:35], a[128:131], v[56:59], v[32:35]\n\t" \
  "v_lshlrev_b32 v240, 16, v114\n\t" \
  "v_and_b32 v241, s98, v114\n\t" \
  "v_mfma_f32_16x16x32_bf16 v[64:67], a[128:131], v[88:91], v[64:67]\n\t" \
  "v_lshlrev_b32 v242, 16, v115\n\t" \
  "v_and_b32 v243, s98, v115\n\t" \
  "v_mfma_f32_16x16x32_bf16 v[96:99], a[128:131], v[120:123], v[96:99]\n\t" \
  "ds_read_b64_tr_b16 a[160:161], v184 offset:57344\n\t" \
  "ds_read_b64_tr_b16 a[162:163], v184 offset:58368\n\t" \
  "v_cvt_pk_bf16_f32 v49, v222, v223\n\t" \
  "v_cvt_pk_bf16_f32 v50, v224, v225\n\t" \
  "v_cvt_pk_bf16_f32 v51, v226, v227\n\t" \
  "v_pk_fma_f32 v[228:229], v[228:229], v[168:169], v[170:171] op_sel_hi:[1,0,0]\n\t" \
  "v_pk_fma_f32 v[230:231], v[230:231], v[168:169], v[170:171] op_sel_hi:[1,0,0]\n\t" \
  "v_pk_fma_f32 v[232:233], v[232:233], v[168:169], v[170:171] op_sel_hi:[1,0,0]\n\t" \
  "v_pk_fma_f32 v[234:235], v[234:235], v[168:169], v[170:171] op_sel_hi:[1,0,0]\n\t" \
  "v_pk_fma_f32 v[228:229], v[228:229], v[128:129], v[136:137]\n\t" \
  "v_pk_fma_f32 v[230:231], v[230:231], v[130:131], v[138:139]\n\t" \
  "v_pk_fma_f32 v[232:233], v[232:233], v[132:133], v[140:141]\n\t" \
  "v_pk_fma_f32 v[234:235], v[234:235], v[134:135], v[142:143]\n\t" \
  "v_pk_fma_f32 v[236:237], v[236:237], v[172:173], v[174:175] op_sel_hi:[1,0,0]\n\t" \
  "v_pk_fma_f32 v[238:239], v[238:239], v[172:173], v[174:175] op_sel_hi:[1,0,0]\n\t" \
  "v_pk_fma_f32 v[240:241], v[240:241], v[172:173], v[174:175] op_sel_hi:[1,0,0]\n\t" \
  "v_pk_fma_f32 v[242:243], v[242:243], v[172:173], v[174:175] op_sel_hi:[1,0,0]\n\t" \
  "v_pk_fma_f32 v[236:237], v[236:237], v[128:129], v[136:137]\n\t" \
  "v_pk_fma_f32 v[238:239], v[238:239], v[130:131], v[138:139]\n\t" \
  "v_pk_fma_f32 v[240:241], v[240:241], v[132:133], v[140:141]\n\t" \
  "v_pk_fma_f32 v[242:243], v[242:243], v[134:135], v[142:143]\n\t" \
  "v_cvt_pk_bf16_f32 v80, v228, v229\n\t" \
  "v_mfma_f32_16x16x32_bf16 v[100:103], a[132:135], v[120:123], v[100:103]\n\t" \
  "ds_read_b64_tr_b16 a[164:165], v185 offset:57344\n\t" \
  "v_cvt_pk_bf16_f32 v81, v230, v231\n\t" \
  "v_mfma_f32_16x16x32_bf16 v[68:71], a[132:135], v[88:91], v[68:71]\n\t" \
  "s_waitcnt lgkmcnt(3)\n\t" \
  "v_lshlrev_b32 v176, 16, v20\n\t" \
  "v_and_b32 v177, s98, v20\n\t" \
  "v_mfma_f32_16x16x32_bf16 v[36:39], a[132:135], v[56:59], v[36:39]\n\t" \
  "buffer_load_dwordx4 v201, s[20:23], s96 offen offset:1024 lds\n\t" \
  "v_mfma_f32_16x16x32_bf16 v[4:7], a[132:135], v[24:27], v[4:7]\n\t" \
  "ds_read_b64_tr_b16 a[166:167], v185 offset:58368\n\t" \
  "v_lshlrev_b32 v178, 16, v21\n\t" \
  "v_mfma_f32_16x16x32_bf16 v[8:11], a[136:139], v[24:27], v[8:11]\n\t" \
  "ds_read_b64_tr_b16 a[168:169], v186 offset:57344\n\t" \
  "v_and_b32 v179, s98, v21\n\t" \
  "v_mfma_f32_16x16x32_bf16 v[40:43], a[136:139], v[56:59], v[40:43]\n\t" \
  "v_lshlrev_b32 v180, 16, v22\n\t" \
  "v_and_b32 v181, s98, v22\n\t" \
  "v_mfma_f32_16x16x32_bf16 v[72:75], a[136:139], v[88:91], v[72:75]\n\t" \
  "ds_read_b64_tr_b16 a[170:171], v186 offset:58368\n\t" \
  "v_lshlrev_b32 v182, 16, v23\n\t" \
  "v_mfma_f32_16x16x32_bf16 v[104:107], a[136:139], v[120:123], v[104:107]\n\t" \
  "ds_read_b64_tr_b16 a[172:173], v187 offset:57344\n\t" \
  "v_and_b32 v183, s98, v23\n\t" \
  "v_mfma_f32_16x16x32_bf16 v[108:111], a[140:143], v[120:123], v[108:111]\n\t" \
  "buffer_load_dwordx4 v202, s[20:23], s96 offen offset:2048 lds\n\t" \
  "v_mfma_f32_16x16x32_bf16 v[76:79], a[140:143], v[88:91], v[76:79]\n\t" \
  "ds_read_b64_tr_b16 a[174:175], v187 offset:58368\n\t" \
  "v_lshlrev_b32 v220, 16, v52\n\t" \
  "v_mfma_f32_16x16x32_bf16 v[44:47], a[140:143], v[56:59], v[44:47]\n\t" \
  "v_and_b32 v221, s98, v52\n\t" \
  "v_lshlrev_b32 v222, 16, v53\n\t" \
  "v_mfma_f32_16x16x32_bf16 v[12:15], a[140:143], v[24:27], v[12:15]\n\t" \
  "ds_read_b64_tr_b16 a[176:177], v188 offset:57344\n\t" \
  "v_and_b32 v223, s98, v53\n\t" \
  "v_mfma_f32_16x16x32_bf16 a[64:67], a[144:147], v[24:27], a[64:67]\n\t" \
  "ds_read_b64_tr_b16 a[178:179], v188 offset:58368\n\t" \
  "v_lshlrev_b32 v224, 16, v54\n\t" \
  "v_mfma_f32_16x16x32_bf16 a[68:71], a[144:147], v[56:59], a[68:71]\n\t" \
  "ds_read_b64_tr_b16 a[180:181], v189 offset:57344\n\t" \
  "v_and_b32 v225, s98, v54\n\t" \
  "v_mfma_f32_16x16x32_bf16 a[72:75], a[144:147], v[88:91], a[72:75]\n\t" \
  "buffer_load_dwordx4 v203, s[20:23], s96 offen offset:3072 lds\n\t" \
  "v_mfma_f32_16x16x32_bf16 a[76:79], a[144:147], v[120:123], a[76:79]\n\t" \
  "ds_read_b64_tr_b16 a[182:183], v189 offset:58368\n\t" \
  "v_cvt_pk_bf16_f32 v82, v232, v233\n\t" \
  "v_mfma_f32_16x16x32_bf16 a[92:95], a[148:151], v[120:123], a[92:95]\n\t" \
  "v_lshlrev_b32 v226, 16, v55\n\t" \
  "v_and_b32 v227, s98, v55\n\t" \
  "v_mfma_f32_16x16x32_bf16 a[88:91], a[148:151], v[88:91], a[88:91]\n\t" \
  "ds_read_b64_tr_b16 a[184:185], v190 offset:57344\n\t" \
  "v_cvt_pk_bf16_f32 v83, v234, v235\n\t" \
  "v_cvt_pk_bf16_f32 v112, v236, v237\n\t" \
  "v_cvt_pk_bf16_f32 v113, v238, v239\n\t" \
  "v_pk_fma_f32 v[176:177], v[176:177], v[160:161], v[162:163] op_sel_hi:[1,0,0]\n\t" \
  "v_pk_fma_f32 v[178:179], v[178:179], v[160:161], v[162:163] op_sel_hi:[1,0,0]\n\t" \
  "v_pk_fma_f32 v[180:181], v[180:181], v[160:161], v[162:163] op_sel_hi:[1,0,0]\n\t" \
  "v_pk_fma_f32 v[182:183], v[182:183], v[160:161], v[162:163] op_sel_hi:[1,0,0]\n\t" \
  "v_pk_fma_f32 v[176:177], v[176:177], v[144:145], v[152:153]\n\t" \
  "v_pk_fma_f32 v[178:179], v[178:179], v[146:147], v[154:155]\n\t" \
  "v_pk_fma_f32 v[180:181], v[180:181], v[148:149], v[156:157]\n\t" \
  "v_pk_fma_f32 v[182:183], v[182:183], v[150:151], v[158:159]\n\t" \
  "v_pk_fma_f32 v[220:221], v[220:221], v[164:165], v[166:167] op_sel_hi:[1,0,0]\n\t" \
  "v_pk_fma_f32 v[222:223], v[222:223], v[164:165], v[166:167] op_sel_hi:[1,0,0]\n\t" \
  "v_pk_fma_f32 v[224:225], v[224:225], v[164:165], v[166:167] op_sel_hi:[1,0,0]\n\t" \
  "v_pk_fma_f32 v[226:227], v[226:227], v[164:165], v[166:167] op_sel_hi:[1,0,0]\n\t" \
  "v_pk_fma_f32 v[220:221], v[220:221], v[144:145], v[152:153]\n\t" \
  "v_pk_fma_f32 v[222:223], v[222:223], v[146:147], v[154:155]\n\t" \
  "v_pk_fma_f32 v[224:225], v[224:225], v[148:149], v[156:157]\n\t" \
  "v_pk_fma_f32 v[226:227], v[226:227], v[150:151], v[158:159]\n\t" \
  "v_cvt_pk_bf16_f32 v20, v176, v177\n\t" \
  "v_mfma_f32_16x16x32_bf16 a[84:87], a[148:151], v[56:59], a[84:87]\n\t" \
  "ds_read_b64_tr_b16 a[186:187], v190 offset:58368\n\t" \
  "v_lshlrev_b32 v228, 16, v84\n\t" \
  "v_mfma_f32_16x16x32_bf16 a[80:83], a[148:151], v[24:27], a[80:83]\n\t" \
  "ds_read_b64_tr_b16 a[188:189], v191 offset:57344\n\t" \
  "v_and_b32 v229, s98, v84\n\t" \
  "v_mfma_f32_16x16x32_bf16 a[96:99], a[152:155], v[24:27], a[96:99]\n\t" \
  "global_load_dword v246, v217, s[12:13]\n\t" \
  "v_mfma_f32_16x16x32_bf16 a[100:103], a[152:155], v[56:59], a[100:103]\n\t" \
  "ds_read_b64_tr_b16 a[190:191], v191 offset:58368\n\t" \
  "v_lshlrev_b32 v230, 16, v85\n\t" \
  "v_mfma_f32_16x16x32_bf16 a[104:107], a[152:155], v[88:91], a[104:107]\n\t" \
  "v_and_b32 v231, s98, v85\n\t" \
  "v_lshlrev_b32 v232, 16, v86\n\t" \
  "v_mfma_f32_16x16x32_bf16 a[108:111], a[152:155], v[120:123], a[108:111]\n\t" \
  ".Lhs1:\n\ts_cbranch_execz .Lhs1n\n\t" \
  "v_and_b32 v233, s98, v86\n\t" \
  "v_lshlrev_b32 v234, 16, v87\n\t" \
  "v_mfma_f32_16x16x32_bf16 a[124:127], a[156:159], v[120:123], a[124:127]\n\t" \
  "v_cvt_pk_bf16_f32 v114, v240, v241\n\t" \
  "v_cvt_pk_bf16_f32 v115, v242, v243\n\t" \
  "v_mfma_f32_16x16x32_bf16 a[120:123], a[156:159], v[88:91], a[120:123]\n\t" \
  "s_add_u32 s96, s78, 3145728\n\t" \
  "v_and_b32 v235, s98, v87\n\t" \
  "v_lshlrev_b32 v236, 16, v116\n\t" \
  "v_mfma_f32_16x16x32_bf16 a[116:119], a[156:159], v[56:59], a[116:119]\n\t" \
  "s_waitcnt vmcnt(22) lgkmcnt(0)\n\t" \
  "s_and_b32 s96, s96, 0x780000\n\t" \
  "s_add_u32 m0, s76, 49152\n\t" \
  "v_mfma_f32_16x16x32_bf16 a[112:115], a[156:159], v[24:27], a[112:115]\n\t" \
  "s_barrier\n\t" \
  FSTP("v_writelane_b32 v255, s94, 40\n\t") \
  FSTP("v_writelane_b32 v255, s92, 41\n\t") \
  FSTP("s_memrealtime s[94:95]\n\t") \
  FSTP("s_memtime s[92:93]\n\t") \
  "v_mfma_f32_16x16x32_bf16 v[0:3], a[160:163], v[28:31], v[0:3]\n\t" \
  "buffer_load_dwordx4 v200, s[20:23], s96 offen offset:0 lds\n\t" \
  "ds_read_b64_tr_b16 a[128:129], v192 offset:32768\n\t" \
  "v_mfma_f32_16x16x32_bf16 v[32:35], a[160:163], v[60:63], v[32:35]\n\t" \
  "s_waitcnt vmcnt(14)\n\t" \
  "ds_read_b128 v[24:27], v204 offset:0\n\t" \
  "ds_read_b64_tr_b16 a[130:131], v192 offset:33792\n\t" \
  "v_and_b32 v237, s98, v116\n\t" \
  "v_mfma_f32_16x16x32_bf16 v[64:67], a[160:163], v[92:95], v[64:67]\n\t" \
  "ds_read_b128 v[56:59], v204 offset:2048\n\t" \
  "ds_read_b64_tr_b16 a[132:133], v193 offset:32768\n\t" \
  "v_cvt_pk_bf16_f32 v21, v178, v179\n\t" \
  "v_mfma_f32_16x16x32_bf16 v[96:99], a[160:163], v[124:127], v[96:99]\n\t" \
  "ds_read_b128 v[88:91], v204 offset:4096\n\t" \
  "v_lshlrev_b32 v238, 16, v117\n\t" \
  "v_mfma_f32_16x16x32_bf16 v[100:103], a[164:167], v[124:127], v[100:103]\n\t" \
  "ds_read_b128 v[120:123], v204 offset:6144\n\t" \
  "v_cvt_pk_bf16_f32 v22, v180, v181\n\t" \
  "v_mfma_f32_16x16x32_bf16 v[68:71], a[164:167], v[92:95], v[68:71]\n\t" \
  "buffer_load_dwordx4 v201, s[20:23], s96 offen offset:1024 lds\n\t" \
  "v_mfma_f32_16x16x32_bf16 v[36:39], a[164:167], v[60:63], v[36:39]\n\t" \
  "ds_read_b64_tr_b16 a[134:135], v193 offset:33792\n\t" \
  "v_and_b32 v239, s98, v117\n\t" \
  "v_mfma_f32_16x16x32_bf16 v[4:7], a[164:167], v[28:31], v[4:7]\n\t" \
  "ds_read_b64_tr_b16 a[136:137], v194 offset:32768\n\t" \
  "v_cvt_pk_bf16_f32 v23, v182, v183\n\t" \
  "v_mfma_f32_16x16x32_bf16 v[8:11], a[168:171], v[28:31], v[8:11]\n\t" \
  "buffer_load_dwordx4 v202, s[20:23], s96 offen offset:2048 lds\n\t" \
  "v_mfma_f32_16x16x32_bf16 v[40:43], a[168:171], v[60:63], v[40:43]\n\t" \
  "ds_read_b64_tr_b16 a[138:139], v194 offset:33792\n\t" \
  "v_lshlrev_b32 v240, 16, v118\n\t" \
  "v_mfma_f32_16x16x32_bf16 v[72:75], a[168:171], v[92:95], v[72:75]\n\t" \
  "ds_read_b64_tr_b16 a[140:141], v195 offset:32768\n\t" \
  "v_and_b32 v241, s98, v118\n\t" \
  "v_mfma_f32_16x16x32_bf16 v[104:107], a[168:171], v[124:127], v[104:107]\n\t" \
  "v_lshlrev_b32 v242, 16, v119\n\t" \
  "v_and_b32 v243, s98, v119\n\t" \
  "v_mfma_f32_16x16x32_bf16 v[108:111], a[172:175], v[124:127], v[108:111]\n\t" \
  "buffer_load_dwordx4 v203, s[20:23], s96 offen offset:3072 lds\n\t" \
  "ds_read_b64_tr_b16 a[142:143], v195 offset:33792\n\t" \
  "ds_read_b128 v[128:131], v208 offset:1536\n\t" \
  "v_cvt_pk_bf16_f32 v52, v220, v221\n\t" \
  "v_cvt_pk_bf16_f32 v53, v222, v223\n\t" \
  "v_pk_fma_f32 v[228:229], v[228:229], v[168:169], v[170:171] op_sel_hi:[1,0,0]\n\t" \
  "v_pk_fma_f32 v[230:231], v[230:231], v[168:169], v[170:171] op_sel_hi:[1,0,0]\n\t" \
  "v_pk_fma_f32 v[232:233], v[232:233], v[168:169], v[170:171] op_sel_hi:[1,0,0]\n\t" \
  "v_pk_fma_f32 v[234:235], v[234:235], v[168:169], v[170:171] op_sel_hi:[1,0,0]\n\t" \
  "v_pk_fma_f32 v[228:229], v[228:229], v[144:145], v[152:153]\n\t" \
  "v_pk_fma_f32 v[230:231], v[230:231], v[146:147], v[154:155]\n\t" \
  "v_pk_fma_f32 v[232:233], v[232:233], v[148:149], v[156:157]\n\t" \
  "v_pk_fma_f32 v[234:235], v[234:235], v[150:151], v[158:159]\n\t" \
  "v_pk_fma_f32 v[236:237], v[236:237], v[172:173], v[174:175] op_sel_hi:[1,0,0]\n\t" \
  "v_pk_fma_f32 v[238:239], v[238:239], v[172:173], v[174:175] op_sel_hi:[1,0,0]\n\t" \
  "v_pk_fma_f32 v[240:241], v[240:241], v[172:173], v[174:175] op_sel_hi:[1,0,0]\n\t" \
  "v_pk_fma_f32 v[242:243], v[242:243], v[172:173], v[174:175] op_sel_hi:[1,0,0]\n\t" \
  "v_pk_fma_f32 v[236:237], v[236:237], v[144:145], v[152:153]\n\t" \
  "v_pk_fma_f32 v[238:239], v[238:239], v[146:147], v[154:155]\n\t" \
  "v_pk_fma_f32 v[240:241], v[240:241], v[148:149], v[156:157]\n\t" \
  "v_pk_fma_f32 v[242:243], v[242:243], v[150:151], v[158:159]\n\t" \
  "v_cvt_pk_bf16_f32 v84, v228, v229\n\t" \
  "v_mfma_f32_16x16x32_bf16 v[76:79], a[172:175], v[92:95], v[76:79]\n\t" \
  "s_add_u32 s96, s78, 3670016\n\t" \
  "s_and_b32 s96, s96, 0x780000\n\t" \
  "s_add_u32 m0, s76, 65536\n\t" \
  "v_mfma_f32_16x16x32_bf16 v[44:47], a[172:175], v[60:63], v[44:47]\n\t" \
  "ds_read_b64_tr_b16 a[144:145], v196 offset:32768\n\t" \
  "v_cvt_pk_bf16_f32 v54, v224, v225\n\t" \
  "v_mfma_f32_16x16x32_bf16 v[12:15], a[172:175], v[28:31], v[12:15]\n\t" \
  "buffer_load_dwordx4 v200, s[20:23], s96 offen offset:0 lds\n\t" \
  "v_mfma_f32_16x16x32_bf16 a[64:67], a[176:179], v[28:31], a[64:67]\n\t" \
  "s_waitcnt lgkmcnt(7)\n\t" \
  "v_lshlrev_b32 v176, 16, v24\n\t" \
  "v_and_b32 v177, s98, v24\n\t" \
  "v_mfma_f32_16x16x32_bf16 a[68:71], a[176:179], v[60:63], a[68:71]\n\t" \
  "ds_read_b64_tr_b16 a[146:147], v196 offset:33792\n\t" \
  "v_lshlrev_b32 v178, 16, v25\n\t" \
  "v_mfma_f32_16x16x32_bf16 a[72:75], a[176:179], v[92:95], a[72:75]\n\t" \
  "ds_read_b64_tr_b16 a[148:149], v197 offset:32768\n\t" \
  "v_and_b32 v179, s98, v25\n\t" \
  "v_mfma_f32_16x16x32_bf16 a[76:79], a[176:179], v[124:127], a[76:79]\n\t" \
  "buffer_load_dwordx4 v201, s[20:23], s96 offen offset:1024 lds\n\t" \
  "v_mfma_f32_16x16x32_bf16 a[92:95], a[180:183], v[124:127], a[92:95]\n\t" \
  "ds_read_b64_tr_b16 a[150:151], v197 offset:33792\n\t" \
  "v_lshlrev_b32 v180, 16, v26\n\t" \
  "v_mfma_f32_16x16x32_bf16 a[88:91], a[180:183], v[92:95], a[88:91]\n\t" \
  "ds_read_b64_tr_b16 a[152:153], v198 offset:32768\n\t" \
  "v_and_b32 v181, s98, v26\n\t" \
  "v_mfma_f32_16x16x32_bf16 a[84:87], a[180:183], v[60:63], a[84:87]\n\t" \
  "ds_read_b64_tr_b16 a[154:155], v198 offset:33792\n\t" \
  "v_lshlrev_b32 v182, 16, v27\n\t" \
  "v_mfma_f32_16x16x32_bf16 a[80:83], a[180:183], v[28:31], a[80:83]\n\t" \
  "buffer_load_dwordx4 v202, s[20:23], s96 offen offset:2048 lds\n\t" \
  "v_mfma_f32_16x16x32_bf16 a[96:99], a[184:187], v[28:31], a[96:99]\n\t" \
  "ds_read_b64_tr_b16 a[156:157], v199 offset:32768\n\t" \
  "v_cvt_pk_bf16_f32 v55, v226, v227\n\t" \
  "v_mfma_f32_16x16x32_bf16 a[100:103], a[184:187], v[60:63], a[100:103]\n\t" \
  "ds_read_b64_tr_b16 a[158:159], v199 offset:33792\n\t" \
  "v_and_b32 v183, s98, v27\n\t" \
  "v_mfma_f32_16x16x32_bf16 a[104:107], a[184:187], v[92:95], a[104:107]\n\t" \
  "v_lshlrev_b32 v220, 16, v56\n\t" \
  "v_and_b32 v221, s98, v56\n\t" \
  "v_mfma_f32_16x16x32_bf16 a[108:111], a[184:187], v[124:127], a[108:111]\n\t" \
  "buffer_load_dwordx4 v203, s[20:23], s96 offen offset:3072 lds\n\t" \
  "v_mfma_f32_16x16x32_bf16 a[124:127], a[188:191], v[124:127], a[124:127]\n\t" \
  "v_lshlrev_b32 v222, 16, v57\n\t" \
  "v_and_b32 v223, s98, v57\n\t" \
  "v_mfma_f32_16x16x32_bf16 a[120:123], a[188:191], v[92:95], a[120:123]\n\t" \
  "v_lshlrev_b32 v224, 16, v58\n\t" \
  "v_cvt_pk_bf16_f32 v85, v230, v231\n\t" \
  "v_mfma_f32_16x16x32_bf16 a[116:119], a[188:191], v[60:63], a[116:119]\n\t" \
  "v_and_b32 v225, s98, v58\n\t" \
  "v_lshlrev_b32 v226, 16, v59\n\t" \
  "v_mfma_f32_16x16x32_bf16 a[112:115], a[188:191], v[28:31], a[112:115]\n\t" \
  "s_waitcnt lgkmcnt(0)\n\t" \
  "v_cvt_pk_bf16_f32 v86, v232, v233\n\t" \
  "v_cvt_pk_bf16_f32 v87, v234, v235\n\t" \
  FSTP("v_writelane_b32 v255, s94, 42\n\t") \
  FSTP("v_writelane_b32 v255, s92, 43\n\t") \
  FSTP("s_memrealtime s[94:95]\n\t") \
  FSTP("s_memtime s[92:93]\n\t") \
  "v_mfma_f32_16x16x32_bf16 v[0:3], a[128:131], v[16:19], v[0:3]\n\t" \
  "buffer_load_dword v252, v210, s[16:19], s84 offen sc1\n\t" \
  "v_mfma_f32_16x16x32_bf16 v[32:35], a[128:131], v[48:51], v[32:35]\n\t" \
  "ds_read_b128 v[28:31], v205 offset:0\n\t" \
  "v_and_b32 v227, s98, v59\n\t" \
  "v_mfma_f32_16x16x32_bf16 v[64:67], a[128:131], v[80:83], v[64:67]\n\t" \
  "s_lshr_b32 s99, s87, 9\n\t" \
  "ds_read_b128 v[60:63], v205 offset:2048\n\t" \
  "ds_read_b64_tr_b16 a[160:161], v192 offset:40960\n\t" \
  "v_lshlrev_b32 v228, 16, v88\n\t" \
  "v_mfma_f32_16x16x32_bf16 v[96:99], a[128:131], v[112:115], v[96:99]\n\t" \
  "ds_read_b128 v[92:95], v205 offset:4096\n\t" \
  "ds_read_b64_tr_b16 a[162:163], v192 offset:41984\n\t" \
  "v_xor_b32 v248, s99, v214\n\t" \
  "v_mfma_f32_16x16x32_bf16 v[100:103], a[132:135], v[112:115], v[100:103]\n\t" \
  "v_xor_b32 v249, 256, v248\n\t" \
  "v_xor_b32 v250, 512, v248\n\t" \
  "v_mfma_f32_16x16x32_bf16 v[68:71], a[132:135], v[80:83], v[68:71]\n\t" \
  "v_xor_b32 v251, 768, v248\n\t" \
  "s_add_u32 s96, s78, 4194304\n\t" \
  "v_mfma_f32_16x16x32_bf16 v[36:39], a[132:135], v[48:51], v[36:39]\n\t" \
  "s_and_b32 s96, s96, 0x780000\n\t" \
  "ds_read_b128 v[124:127], v205 offset:6144\n\t" \
  "ds_read_b64_tr_b16 a[164:165], v193 offset:40960\n\t" \
  "s_add_u32 m0, s76, 81920\n\t" \
  "v_mfma_f32_16x16x32_bf16 v[4:7], a[132:135], v[16:19], v[4:7]\n\t" \
  "buffer_load_dwordx4 v200, s[20:23], s96 offen offset:0 lds\n\t" \
  "v_mfma_f32_16x16x32_bf16 v[8:11], a[136:139], v[16:19], v[8:11]\n\t" \
  "ds_read_b64_tr_b16 a[166:167], v193 offset:41984\n\t" \
  "v_and_b32 v229, s98, v88\n\t" \
  "v_mfma_f32_16x16x32_bf16 v[40:43], a[136:139], v[48:51], v[40:43]\n\t" \
  "ds_read_b64_tr_b16 a[168:169], v194 offset:40960\n\t" \
  "v_lshlrev_b32 v230, 16, v89\n\t" \
  "v_mfma_f32_16x16x32_bf16 v[72:75], a[136:139], v[80:83], v[72:75]\n\t" \
  "buffer_load_dwordx4 v201, s[20:23], s96 offen offset:1024 lds\n\t" \
  "v_mfma_f32_16x16x32_bf16 v[104:107], a[136:139], v[112:115], v[104:107]\n\t" \
  "ds_read_b64_tr_b16 a[170:171], v194 offset:41984\n\t" \
  "v_and_b32 v231, s98, v89\n\t" \
  "v_mfma_f32_16x16x32_bf16 v[108:111], a[140:143], v[112:115], v[108:111]\n\t" \
  "ds_read_b64_tr_b16 a[172:173], v195 offset:40960\n\t" \
  "v_cvt_pk_bf16_f32 v116, v236, v237\n\t" \
  "v_mfma_f32_16x16x32_bf16 v[76:79], a[140:143], v[80:83], v[76:79]\n\t" \
  "ds_read_b64_tr_b16 a[174:175], v195 offset:41984\n\t" \
  "v_lshlrev_b32 v232, 16, v90\n\t" \
  "v_mfma_f32_16x16x32_bf16 v[44:47], a[140:143], v[48:51], v[44:47]\n\t" \
  "ds_read_b64_tr_b16 a[176:177], v196 offset:40960\n\t" \
  "v_cvt_pk_bf16_f32 v117, v238, v239\n\t" \
  "v_mfma_f32_16x16x32_bf16 v[12:15], a[140:143], v[16:19], v[12:15]\n\t" \
  "buffer_load_dwordx4 v202, s[20:23], s96 offen offset:2048 lds\n\t" \
  "v_mfma_f32_16x16x32_bf16 a[64:67], a[144:147], v[16:19], a[64:67]\n\t" \
  "ds_read_b64_tr_b16 a[178:179], v196 offset:41984\n\t" \
  "v_and_b32 v233, s98, v90\n\t" \
  "v_mfma_f32_16x16x32_bf16 a[68:71], a[144:147], v[48:51], a[68:71]\n\t" \
  "ds_read_b64_tr_b16 a[180:181], v197 offset:40960\n\t" \
  "v_cvt_pk_bf16_f32 v118, v240, v241\n\t" \
  "v_mfma_f32_16x16x32_bf16 a[72:75], a[144:147], v[80:83], a[72:75]\n\t" \
  "ds_read_b64_tr_b16 a[182:183], v197 offset:41984\n\t" \
  "v_lshlrev_b32 v234, 16, v91\n\t" \
  "v_mfma_f32_16x16x32_bf16 a[76:79], a[144:147], v[112:115], a[76:79]\n\t" \
  "ds_read_b128 v[132:135], v208 offset:1552\n\t" \
  "v_cvt_pk_bf16_f32 v119, v242, v243\n\t" \
  "v_mfma_f32_16x16x32_bf16 a[92:95], a[148:151], v[112:115], a[92:95]\n\t" \
  "buffer_load_dwordx4 v203, s[20:23], s96 offen offset:3072 lds\n\t" \
  "v_mfma_f32_16x16x32_bf16 a[88:91], a[148:151], v[80:83], a[88:91]\n\t" \
  "ds_read_b64_tr_b16 a[184:185], v198 offset:40960\n\t" \
  "v_and_b32 v235, s98, v91\n\t" \
  "v_mfma_f32_16x16x32_bf16 a[84:87], a[148:151], v[48:51], a[84:87]\n\t" \
  "ds_read_b64_tr_b16 a[186:187], v198 offset:41984\n\t" \
  "v_lshlrev_b32 v236, 16, v120\n\t" \
  "v_mfma_f32_16x16x32_bf16 a[80:83], a[148:151], v[16:19], a[80:83]\n\t" \
  "ds_read_b64_tr_b16 a[188:189], v199 offset:40960\n\t" \
  "v_and_b32 v237, s98, v120\n\t" \
  "v_mfma_f32_16x16x32_bf16 a[96:99], a[152:155], v[16:19], a[96:99]\n\t" \
  "ds_read_b128 v[136:139], v208 offset:1792\n\t" \
  "v_lshlrev_b32 v238, 16, v121\n\t" \
  "v_mfma_f32_16x16x32_bf16 a[100:103], a[152:155], v[48:51], a[100:103]\n\t" \
  "global_load_dword v246, v216, s[20:21]\n\t" \
  "v_mfma_f32_16x16x32_bf16 a[104:107], a[152:155], v[80:83], a[104:107]\n\t" \
  "ds_read_b64_tr_b16 a[190:191], v199 offset:41984\n\t" \
  "v_and_b32 v239, s98, v121\n\t" \
  "v_mfma_f32_16x16x32_bf16 a[108:111], a[152:155], v[112:115], a[108:111]\n\t" \
  "ds_read_b128 v[140:143], v208 offset:1808\n\t" \
  "v_lshlrev_b32 v240, 16, v122\n\t" \
  "v_mfma_f32_16x16x32_bf16 a[124:127], a[156:159], v[112:115], a[124:127]\n\t" \
  "ds_read_b128 v[144:147], v208 offset:1664\n\t" \
  "v_and_b32 v241, s98, v122\n\t" \
  "v_lshlrev_b32 v242, 16, v123\n\t" \
  "v_and_b32 v243, s98, v123\n\t" \
  "s_waitcnt lgkmcnt(1)\n\t" \
  "v_pk_fma_f32 v[176:177], v[176:177], v[160:161], v[162:163] op_sel_hi:[1,0,0]\n\t" \
  "v_pk_fma_f32 v[178:179], v[178:179], v[160:161], v[162:163] op_sel_hi:[1,0,0]\n\t" \
  "v_pk_fma_f32 v[180:181], v[180:181], v[160:161], v[162:163] op_sel_hi:[1,0,0]\n\t" \
  "v_pk_fma_f32 v[182:183], v[182:183], v[160:161], v[162:163] op_sel_hi:[1,0,0]\n\t" \
  "v_pk_fma_f32 v[176:177], v[176:177], v[128:129], v[136:137]\n\t" \
  "v_pk_fma_f32 v[178:179], v[178:179], v[130:131], v[138:139]\n\t" \
  "v_pk_fma_f32 v[180:181], v[180:181], v[132:133], v[140:141]\n\t" \
  "v_pk_fma_f32 v[182:183], v[182:183], v[134:135], v[142:143]\n\t" \
  "v_pk_fma_f32 v[220:221], v[220:221], v[164:165], v[166:167] op_sel_hi:[1,0,0]\n\t" \
  "v_pk_fma_f32 v[222:223], v[222:223], v[164:165], v[166:167] op_sel_hi:[1,0,0]\n\t" \
  "v_pk_fma_f32 v[224:225], v[224:225], v[164:165], v[166:167] op_sel_hi:[1,0,0]\n\t" \
  "v_pk_fma_f32 v[226:227], v[226:227], v[164:165], v[166:167] op_sel_hi:[1,0,0]\n\t" \
  "v_pk_fma_f32 v[220:221], v[220:221], v[128:129], v[136:137]\n\t" \
  "v_pk_fma_f32 v[222:223], v[222:223], v[130:131], v[138:139]\n\t" \
  "v_pk_fma_f32 v[224:225], v[224:225], v[132:133], v[140:141]\n\t" \
  "v_pk_fma_f32 v[226:227], v[226:227], v[134:135], v[142:143]\n\t" \
  "v_cvt_pk_bf16_f32 v24, v176, v177\n\t" \
  "v_mfma_f32_16x16x32_bf16 a[120:123], a[156:159], v[80:83], a[120:123]\n\t" \
  "v_cvt_pk_bf16_f32 v25, v178, v179\n\t" \
  "v_cvt_pk_bf16_f32 v26, v180, v181\n\t" \
  "v_cvt_pk_bf16_f32 v27, v182, v183\n\t" \
  "v_cvt_pk_bf16_f32 v56, v220, v221\n\t" \
  "v_cvt_pk_bf16_f32 v57, v222, v223\n\t" \
  "v_pk_fma_f32 v[228:229], v[228:229], v[168:169], v[170:171] op_sel_hi:[1,0,0]\n\t" \
  "v_pk_fma_f32 v[230:231], v[230:231], v[168:169], v[170:171] op_sel_hi:[1,0,0]\n\t" \
  "v_pk_fma_f32 v[232:233], v[232:233], v[168:169], v[170:171] op_sel_hi:[1,0,0]\n\t" \
  "v_pk_fma_f32 v[234:235], v[234:235], v[168:169], v[170:171] op_sel_hi:[1,0,0]\n\t" \
  "v_pk_fma_f32 v[228:229], v[228:229], v[128:129], v[136:137]\n\t" \
  "v_pk_fma_f32 v[230:231], v[230:231], v[130:131], v[138:139]\n\t" \
  "v_pk_fma_f32 v[232:233], v[232:233], v[132:133], v[140:141]\n\t" \
  "v_pk_fma_f32 v[234:235], v[234:235], v[134:135], v[142:143]\n\t" \
  "v_pk_fma_f32 v[236:237], v[236:237], v[172:173], v[174:175] op_sel_hi:[1,0,0]\n\t" \
  "v_pk_fma_f32 v[238:239], v[238:239], v[172:173], v[174:175] op_sel_hi:[1,0,0]\n\t" \
  "v_pk_fma_f32 v[240:241], v[240:241], v[172:173], v[174:175] op_sel_hi:[1,0,0]\n\t" \
  "v_pk_fma_f32 v[242:243], v[242:243], v[172:173], v[174:175] op_sel_hi:[1,0,0]\n\t" \
  "v_pk_fma_f32 v[236:237], v[236:237], v[128:129], v[136:137]\n\t" \
  "v_pk_fma_f32 v[238:239], v[238:239], v[130:131], v[138:139]\n\t" \
  "v_pk_fma_f32 v[240:241], v[240:241], v[132:133], v[140:141]\n\t" \
  "v_pk_fma_f32 v[242:243], v[242:243], v[134:135], v[142:143]\n\t" \
  "v_cvt_pk_bf16_f32 v88, v228, v229\n\t" \
  "v_mfma_f32_16x16x32_bf16 a[116:119], a[156:159], v[48:51], a[116:119]\n\t" \
  "s_waitcnt vmcnt(23)\n\t" \
  "v_cvt_pk_bf16_f32 v58, v224, v225\n\t" \
  "v_cvt_pk_bf16_f32 v59, v226, v227\n\t" \
  "v_mfma_f32_16x16x32_bf16 a[112:115], a[156:159], v[16:19], a[112:115]\n\t" \
  "s_barrier\n\t" \
  FSTP("v_writelane_b32 v255, s94, 44\n\t") \
  FSTP("v_writelane_b32 v255, s92, 45\n\t") \
  FSTP("s_memrealtime s[94:95]\n\t") \
  FSTP("s_memtime s[92:93]\n\t") \
  "v_mfma_f32_16x16x32_bf16 v[0:3], a[160:163], v[20:23], v[0:3]\n\t" \
  "s_waitcnt vmcnt(5)\n\t" \
  "v_cmp_ne_u32 vcc, v211, v252\n\t" \
  "v_cvt_pk_bf16_f32 v89, v230, v231\n\t" \
  "v_mfma_f32_16x16x32_bf16 v[32:35], a[160:163], v[52:55], v[32:35]\n\t" \
  "ds_read_b64_tr_b16 a[128:129], v184 offset:0\n\t" \
  "v_cvt_pk_bf16_f32 v90, v232, v233\n\t" \
  "v_mfma_f32_16x16x32_bf16 v[64:67], a[160:163], v[84:87], v[64:67]\n\t" \
  "s_cbranch_vccnz .Lfq_s\n\t" \
  ".Lfq_k:\n\t" \
  "v_mfma_f32_16x16x32_bf16 v[96:99], a[160:163], v[116:119], v[96:99]\n\t" \
  "s_add_u32 s97, s78, 2097152\n\t" \
  "s_bfe_u32 s97, s97, 0x140003\n\t" \
  "s_add_u32 s97, s97, s82\n\t" \
  "v_mfma_f32_16x16x32_bf16 v[100:103], a[164:167], v[116:119], v[100:103]\n\t" \
  "buffer_load_dwordx4 a[0:3], v248, s[16:19], s97 offen sc1\n\t" \
  "v_mfma_f32_16x16x32_bf16 v[68:71], a[164:167], v[84:87], v[68:71]\n\t" \
  "buffer_load_dwordx4 a[4:7], v249, s[16:19], s97 offen sc1\n\t" \
  "v_mfma_f32_16x16x32_bf16 v[36:39], a[164:167], v[52:55], v[36:39]\n\t" \
  "ds_read_b64_tr_b16 a[130:131], v184 offset:1024\n\t" \
  "v_cvt_pk_bf16_f32 v91, v234, v235\n\t" \
  "v_mfma_f32_16x16x32_bf16 v[4:7], a[164:167], v[20:23], v[4:7]\n\t" \
  "ds_read_b64_tr_b16 a[132:133], v185 offset:0\n\t" \
  "v_cvt_pk_bf16_f32 v120, v236, v237\n\t" \
  "v_mfma_f32_16x16x32_bf16 v[8:11], a[168:171], v[20:23], v[8:11]\n\t" \
  "buffer_load_dwordx4 a[8:11], v250, s[16:19], s97 offen sc1\n\t" \
  "v_mfma_f32_16x16x32_bf16 v[40:43], a[168:171], v[52:55], v[40:43]\n\t" \
  "ds_read_b64_tr_b16 a[134:135], v185 offset:1024\n\t" \
  "v_cvt_pk_bf16_f32 v121, v238, v239\n\t" \
  "v_mfma_f32_16x16x32_bf16 v[72:75], a[168:171], v[84:87], v[72:75]\n\t" \
  "v_lshlrev_b32 v176, 16, v28\n\t" \
  "v_and_b32 v177, s98, v28\n\t" \
  "v_mfma_f32_16x16x32_bf16 v[104:107], a[168:171], v[116:119], v[104:107]\n\t" \
  "ds_read_b64_tr_b16 a[136:137], v186 offset:0\n\t" \
  "v_lshlrev_b32 v178, 16, v29\n\t" \
  "v_mfma_f32_16x16x32_bf16 v[108:111], a[172:175], v[116:119], v[108:111]\n\t" \
  "buffer_load_dwordx4 a[12:15], v251, s[16:19], s97 offen sc1\n\t" \
  "v_mfma_f32_16x16x32_bf16 v[76:79], a[172:175], v[84:87], v[76:79]\n\t" \
  "s_add_u32 s97, s97, s83\n\t" \
  "ds_read_b128 v[148:151], v208 offset:1680\n\t" \
  "ds_read_b64_tr_b16 a[138:139], v186 offset:1024\n\t" \
  "v_and_b32 v179, s98, v29\n\t" \
  "v_mfma_f32_16x16x32_bf16 v[44:47], a[172:175], v[52:55], v[44:47]\n\t" \
  "ds_read_b128 v[152:155], v208 offset:1920\n\t" \
  "ds_read_b64_tr_b16 a[140:141], v187 offset:0\n\t" \
  "v_lshlrev_b32 v180, 16, v30\n\t" \
  "v_mfma_f32_16x16x32_bf16 v[12:15], a[172:175], v[20:23], v[12:15]\n\t" \
  "buffer_load_dwordx4 a[16:19], v248, s[16:19], s97 offen sc1\n\t" \
  "v_mfma_f32_16x16x32_bf16 a[64:67], a[176:179], v[20:23], a[64:67]\n\t" \
  "ds_read_b64_tr_b16 a[142:143], v187 offset:1024\n\t" \
  "v_and_b32 v181, s98, v30\n\t" \
  "v_mfma_f32_16x16x32_bf16 a[68:71], a[176:179], v[52:55], a[68:71]\n\t" \
  "ds_read_b64_tr_b16 a[144:145], v188 offset:0\n\t" \
  "v_lshlrev_b32 v182, 16, v31\n\t" \
  "v_mfma_f32_16x16x32_bf16 a[72:75], a[176:179], v[84:87], a[72:75]\n\t" \
  "ds_read_b64_tr_b16 a[146:147], v188 offset:1024\n\t" \
  "v_and_b32 v183, s98, v31\n\t" \
  "v_mfma_f32_16x16x32_bf16 a[76:79], a[176:179], v[116:119], a[76:79]\n\t" \
  "buffer_load_dwordx4 a[20:23], v249, s[16:19], s97 offen sc1\n\t" \
  "v_mfma_f32_16x16x32_bf16 a[92:95], a[180:183], v[116:119], a[92:95]\n\t" \
  "ds_read_b64_tr_b16 a[148:149], v189 offset:0\n\t" \
  "v_lshlrev_b32 v220, 16, v60\n\t" \
  "v_mfma_f32_16x16x32_bf16 a[88:91], a[180:183], v[84:87], a[88:91]\n\t" \
  "ds_read_b64_tr_b16 a[150:151], v189 offset:1024\n\t" \
  "v_and_b32 v221, s98, v60\n\t" \
  "v_mfma_f32_16x16x32_bf16 a[84:87], a[180:183], v[52:55], a[84:87]\n\t" \
  "ds_read_b64_tr_b16 a[152:153], v190 offset:0\n\t" \
  "v_lshlrev_b32 v222, 16, v61\n\t" \
  "v_mfma_f32_16x16x32_bf16 a[80:83], a[180:183], v[20:23], a[80:83]\n\t" \
  "buffer_load_dwordx4 a[24:27], v250, s[16:19], s97 offen sc1\n\t" \
  "v_mfma_f32_16x16x32_bf16 a[96:99], a[184:187], v[20:23], a[96:99]\n\t" \
  "ds_read_b64_tr_b16 a[154:155], v190 offset:1024\n\t" \
  "v_cvt_pk_bf16_f32 v122, v240, v241\n\t" \
  "v_mfma_f32_16x16x32_bf16 a[100:103], a[184:187], v[52:55], a[100:103]\n\t" \
  "ds_read_b64_tr_b16 a[156:157], v191 offset:0\n\t" \
  "v_and_b32 v223, s98, v61\n\t" \
  "v_mfma_f32_16x16x32_bf16 a[104:107], a[184:187], v[84:87], a[104:107]\n\t" \
  "ds_read_b64_tr_b16 a[158:159], v191 offset:1024\n\t" \
  "v_cvt_pk_bf16_f32 v123, v242, v243\n\t" \
  "v_mfma_f32_16x16x32_bf16 a[108:111], a[184:187], v[116:119], a[108:111]\n\t" \
  "buffer_load_dwordx4 a[28:31], v251, s[16:19], s97 offen sc1\n\t" \
  "v_mfma_f32_16x16x32_bf16 a[124:127], a[188:191], v[116:119], a[124:127]\n\t" \
  "ds_read_b128 v[156:159], v208 offset:1936\n\t" \
  "v_lshlrev_b32 v224, 16, v62\n\t" \
  "v_mfma_f32_16x16x32_bf16 a[120:123], a[188:191], v[84:87], a[120:123]\n\t" \
  "s_add_u32 s97, s78, 2621440\n\t" \
  "v_and_b32 v225, s98, v62\n\t" \
  "v_lshlrev_b32 v226, 16, v63\n\t" \
  "v_mfma_f32_16x16x32_bf16 a[116:119], a[188:191], v[52:55], a[116:119]\n\t" \
  "s_bfe_u32 s97, s97, 0x140003\n\t" \
  "s_add_u32 s97, s97, s82\n\t" \
  "v_and_b32 v227, s98, v63\n\t" \
  "v_mfma_f32_16x16x32_bf16 a[112:115], a[188:191], v[20:23], a[112:115]\n\t" \
  "s_waitcnt lgkmcnt(1)\n\t" \
  "v_lshlrev_b32 v228, 16, v92\n\t" \
  "v_and_b32 v229, s98, v92\n\t" \
  "v_lshlrev_b32 v230, 16, v93\n\t" \
  "v_and_b32 v231, s98, v93\n\t" \
  "s_waitcnt lgkmcnt(0)\n\t" \
  "v_pk_fma_f32 v[176:177], v[176:177], v[160:161], v[162:163] op_sel_hi:[1,0,0]\n\t" \
  "v_pk_fma_f32 v[178:179], v[178:179], v[160:161], v[162:163] op_sel_hi:[1,0,0]\n\t" \
  "v_pk_fma_f32 v[180:181], v[180:181], v[160:161], v[162:163] op_sel_hi:[1,0,0]\n\t" \
  "v_pk_fma_f32 v[182:183], v[182:183], v[160:161], v[162:163] op_sel_hi:[1,0,0]\n\t" \
  "v_pk_fma_f32 v[176:177], v[176:177], v[144:145], v[152:153]\n\t" \
  "v_pk_fma_f32 v[178:179], v[178:179], v[146:147], v[154:155]\n\t" \
  "v_pk_fma_f32 v[180:181], v[180:181], v[148:149], v[156:157]\n\t" \
  "v_pk_fma_f32 v[182:183], v[182:183], v[150:151], v[158:159]\n\t" \
  "v_pk_fma_f32 v[220:221], v[220:221], v[164:165], v[166:167] op_sel_hi:[1,0,0]\n\t" \
  "v_pk_fma_f32 v[222:223], v[222:223], v[164:165], v[166:167] op_sel_hi:[1,0,0]\n\t" \
  "v_pk_fma_f32 v[224:225], v[224:225], v[164:165], v[166:167] op_sel_hi:[1,0,0]\n\t" \
  "v_pk_fma_f32 v[226:227], v[226:227], v[164:165], v[166:167] op_sel_hi:[1,0,0]\n\t" \
  "v_pk_fma_f32 v[220:221], v[220:221], v[144:145], v[152:153]\n\t" \
  "v_pk_fma_f32 v[222:223], v[222:223], v[146:147], v[154:155]\n\t" \
  "v_pk_fma_f32 v[224:225], v[224:225], v[148:149], v[156:157]\n\t" \
  "v_pk_fma_f32 v[226:227], v[226:227], v[150:151], v[158:159]\n\t" \
  "v_cvt_pk_bf16_f32 v28, v176, v177\n\t" \
  FSTP("v_writelane_b32 v255, s94, 46\n\t") \
  FSTP("v_writelane_b32 v255, s92, 47\n\t") \
  FSTP("s_memrealtime s[94:95]\n\t") \
  FSTP("s_memtime s[92:93]\n\t") \
  "v_mfma_f32_16x16x32_bf16 v[0:3], a[128:131], v[24:27], v[0:3]\n\t" \
  "buffer_load_dwordx4 a[32:35], v248, s[16:19], s97 offen sc1\n\t" \
  "v_mfma_f32_16x16x32_bf16 v[32:35], a[128:131], v[56:59], v[32:35]\n\t" \
  "ds_read_b64_tr_b16 a[160:161], v184 offset:8192\n\t" \
  "v_lshlrev_b32 v232, 16, v94\n\t" \
  "v_mfma_f32_16x16x32_bf16 v[64:67], a[128:131], v[88:91], v[64:67]\n\t" \
  "ds_read_b64_tr_b16 a[162:163], v184 offset:9216\n\t" \
  "v_and_b32 v233, s98, v94\n\t" \
  "v_mfma_f32_16x16x32_bf16 v[96:99], a[128:131], v[120:123], v[96:99]\n\t" \
  "v_lshlrev_b32 v234, 16, v95\n\t" \
  "v_and_b32 v235, s98, v95\n\t" \
  "v_mfma_f32_16x16x32_bf16 v[100:103], a[132:135], v[120:123], v[100:103]\n\t" \
  "buffer_load_dwordx4 a[36:39], v249, s[16:19], s97 offen sc1\n\t" \
  "v_mfma_f32_16x16x32_bf16 v[68:71], a[132:135], v[88:91], v[68:71]\n\t" \
  "ds_read_b64_tr_b16 a[164:165], v185 offset:8192\n\t" \
  "v_lshlrev_b32 v236, 16, v124\n\t" \
  "v_mfma_f32_16x16x32_bf16 v[36:39], a[132:135], v[56:59], v[36:39]\n\t" \
  "ds_read_b64_tr_b16 a[166:167], v185 offset:9216\n\t" \
  "v_and_b32 v237, s98, v124\n\t" \
  "v_mfma_f32_16x16x32_bf16 v[4:7], a[132:135], v[24:27], v[4:7]\n\t" \
  "ds_read_b64_tr_b16 a[168:169], v186 offset:8192\n\t" \
  "v_cvt_pk_bf16_f32 v29, v178, v179\n\t" \
  "v_mfma_f32_16x16x32_bf16 v[8:11], a[136:139], v[24:27], v[8:11]\n\t" \
  "buffer_load_dwordx4 a[40:43], v250, s[16:19], s97 offen sc1\n\t" \
  "v_mfma_f32_16x16x32_bf16 v[40:43], a[136:139], v[56:59], v[40:43]\n\t" \
  "ds_read_b64_tr_b16 a[170:171], v186 offset:9216\n\t" \
  "v_lshlrev_b32 v238, 16, v125\n\t" \
  "v_mfma_f32_16x16x32_bf16 v[72:75], a[136:139], v[88:91], v[72:75]\n\t" \
  "ds_read_b64_tr_b16 a[172:173], v187 offset:8192\n\t" \
  "v_and_b32 v239, s98, v125\n\t" \
  "v_mfma_f32_16x16x32_bf16 v[104:107], a[136:139], v[120:123], v[104:107]\n\t" \
  "v_cvt_pk_bf16_f32 v30, v180, v181\n\t" \
  "v_cvt_pk_bf16_f32 v31, v182, v183\n\t" \
  "v_mfma_f32_16x16x32_bf16 v[108:111], a[140:143], v[120:123], v[108:111]\n\t" \
  "buffer_load_dwordx4 a[44:47], v251, s[16:19], s97 offen sc1\n\t" \
  "v_mfma_f32_16x16x32_bf16 v[76:79], a[140:143], v[88:91], v[76:79]\n\t" \
  "s_add_u32 s97, s97, s83\n\t" \
  "ds_read_b64_tr_b16 a[174:175], v187 offset:9216\n\t" \
  "v_lshlrev_b32 v240, 16, v126\n\t" \
  "v_mfma_f32_16x16x32_bf16 v[44:47], a[140:143], v[56:59], v[44:47]\n\t" \
  "ds_read_b64_tr_b16 a[176:177], v188 offset:8192\n\t" \
  "v_and_b32 v241, s98, v126\n\t" \
  "v_mfma_f32_16x16x32_bf16 v[12:15], a[140:143], v[24:27], v[12:15]\n\t" \
  "buffer_load_dwordx4 a[48:51], v248, s[16:19], s97 offen sc1\n\t" \
  "v_mfma_f32_16x16x32_bf16 a[64:67], a[144:147], v[24:27], a[64:67]\n\t" \
  "ds_read_b64_tr_b16 a[178:179], v188 offset:9216\n\t" \
  "v_cvt_pk_bf16_f32 v60, v220, v221\n\t" \
  "v_mfma_f32_16x16x32_bf16 a[68:71], a[144:147], v[56:59], a[68:71]\n\t" \
  "ds_read_b64_tr_b16 a[180:181], v189 offset:8192\n\t" \
  "v_cvt_pk_bf16_f32 v61, v222, v223\n\t" \
  "v_mfma_f32_16x16x32_bf16 a[72:75], a[144:147], v[88:91], a[72:75]\n\t" \
  "ds_read_b64_tr_b16 a[182:183], v189 offset:9216\n\t" \
  "v_lshlrev_b32 v242, 16, v127\n\t" \
  "v_mfma_f32_16x16x32_bf16 a[76:79], a[144:147], v[120:123], a[76:79]\n\t" \
  "buffer_load_dwordx4 a[52:55], v249, s[16:19], s97 offen sc1\n\t" \
  "v_mfma_f32_16x16x32_bf16 a[92:95], a[148:151], v[120:123], a[92:95]\n\t" \
  "ds_read_b64_tr_b16 a[184:185], v190 offset:8192\n\t" \
  "v_and_b32 v243, s98, v127\n\t" \
  "v_mfma_f32_16x16x32_bf16 a[88:91], a[148:151], v[88:91], a[88:91]\n\t" \
  "buffer_load_dwordx4 a[56:59], v250, s[16:19], s97 offen sc1\n\t" \
  "ds_read_b64_tr_b16 a[186:187], v190 offset:9216\n\t" \
  "ds_read_b64_tr_b16 a[188:189], v191 offset:8192\n\t" \
  "v_cvt_pk_bf16_f32 v62, v224, v225\n\t" \
  "v_cvt_pk_bf16_f32 v63, v226, v227\n\t" \
  "v_pk_fma_f32 v[228:229], v[228:229], v[168:169], v[170:171] op_sel_hi:[1,0,0]\n\t" \
  "v_pk_fma_f32 v[230:231], v[230:231], v[168:169], v[170:171] op_sel_hi:[1,0,0]\n\t" \
  "v_pk_fma_f32 v[232:233], v[232:233], v[168:169], v[170:171] op_sel_hi:[1,0,0]\n\t" \
  "v_pk_fma_f32 v[234:235], v[234:235], v[168:169], v[170:171] op_sel_hi:[1,0,0]\n\t" \
  "v_pk_fma_f32 v[228:229], v[228:229], v[144:145], v[152:153]\n\t" \
  "v_pk_fma_f32 v[230:231], v[230:231], v[146:147], v[154:155]\n\t" \
  "v_pk_fma_f32 v[232:233], v[232:233], v[148:149], v[156:157]\n\t" \
  "v_pk_fma_f32 v[234:235], v[234:235], v[150:151], v[158:159]\n\t" \
  "v_pk_fma_f32 v[236:237], v[236:237], v[172:173], v[174:175] op_sel_hi:[1,0,0]\n\t" \
  "v_pk_fma_f32 v[238:239], v[238:239], v[172:173], v[174:175] op_sel_hi:[1,0,0]\n\t" \
  "v_pk_fma_f32 v[240:241], v[240:241], v[172:173], v[174:175] op_sel_hi:[1,0,0]\n\t" \
  "v_pk_fma_f32 v[242:243], v[242:243], v[172:173], v[174:175] op_sel_hi:[1,0,0]\n\t" \
  "v_pk_fma_f32 v[236:237], v[236:237], v[144:145], v[152:153]\n\t" \
  "v_pk_fma_f32 v[238:239], v[238:239], v[146:147], v[154:155]\n\t" \
  "v_pk_fma_f32 v[240:241], v[240:241], v[148:149], v[156:157]\n\t" \
  "v_pk_fma_f32 v[242:243], v[242:243], v[150:151], v[158:159]\n\t" \
  "v_cvt_pk_bf16_f32 v92, v228, v229\n\t" \
  "v_mfma_f32_16x16x32_bf16 a[84:87], a[148:151], v[56:59], a[84:87]\n\t" \
  "v_cvt_pk_bf16_f32 v93, v230, v231\n\t" \
  "v_cvt_pk_bf16_f32 v94, v232, v233\n\t" \
  "v_mfma_f32_16x16x32_bf16 a[80:83], a[148:151], v[24:27], a[80:83]\n\t" \
  "v_cvt_pk_bf16_f32 v95, v234, v235\n\t" \
  "v_cvt_pk_bf16_f32 v124, v236, v237\n\t" \
  "v_mfma_f32_16x16x32_bf16 a[96:99], a[152:155], v[24:27], a[96:99]\n\t" \
  "v_cvt_pk_bf16_f32 v125, v238, v239\n\t" \
  "v_cvt_pk_bf16_f32 v126, v240, v241\n\t" \
  "v_mfma_f32_16x16x32_bf16 a[100:103], a[152:155], v[56:59], a[100:103]\n\t" \
  "ds_read_b64_tr_b16 a[190:191], v191 offset:9216\n\t" \
  "v_cvt_pk_bf16_f32 v127, v242, v243\n\t" \
  "v_mfma_f32_16x16x32_bf16 a[104:107], a[152:155], v[88:91], a[104:107]\n\t" \
  "buffer_load_dwordx4 a[60:63], v251, s[16:19], s97 offen sc1\n\t" \
  "v_mfma_f32_16x16x32_bf16 a[108:111], a[152:155], v[120:123], a[108:111]\n\t" \
  "v_mfma_f32_16x16x32_bf16 a[124:127], a[156:159], v[120:123], a[124:127]\n\t" \
  "v_mfma_f32_16x16x32_bf16 a[120:123], a[156:159], v[88:91], a[120:123]\n\t" \
  "s_add_u32 s97, s78, 3145728\n\t" \
  "s_bfe_u32 s97, s97, 0x140003\n\t" \
  "s_add_u32 s97, s97, s82\n\t" \
  "v_mfma_f32_16x16x32_bf16 a[116:119], a[156:159], v[56:59], a[116:119]\n\t" \
  "s_waitcnt lgkmcnt(0)\n\t" \
  "v_mfma_f32_16x16x32_bf16 a[112:115], a[156:159], v[24:27], a[112:115]\n\t" \
  "s_barrier\n\t" \
  FSTP("v_writelane_b32 v255, s94, 48\n\t") \
  FSTP("v_writelane_b32 v255, s92, 49\n\t") \
  FSTP("s_memrealtime s[94:95]\n\t") \
  FSTP("s_memtime s[92:93]\n\t") \
  "v_mfma_f32_16x16x32_bf16 v[0:3], a[160:163], v[28:31], v[0:3]\n\t" \
  "buffer_load_dwordx4 a[192:195], v248, s[16:19], s97 offen sc1\n\t" \
  "v_mfma_f32_16x16x32_bf16 v[32:35], a[160:163], v[60:63], v[32:35]\n\t" \
  "ds_read_b64_tr_b16 a[128:129], v184 offset:16384\n\t" \
  "v_mfma_f32_16x16x32_bf16 v[64:67], a[160:163], v[92:95], v[64:67]\n\t" \
  "ds_read_b64_tr_b16 a[130:131], v184 offset:17408\n\t" \
  "v_mfma_f32_16x16x32_bf16 v[96:99], a[160:163], v[124:127], v[96:99]\n\t" \
  "ds_write_b32 v219, v218\n\t" \
  "v_mfma_f32_16x16x32_bf16 v[100:103], a[164:167], v[124:127], v[100:103]\n\t" \
  "buffer_load_dwordx4 a[196:199], v249, s[16:19], s97 offen sc1\n\t" \
  "v_mfma_f32_16x16x32_bf16 v[68:71], a[164:167], v[92:95], v[68:71]\n\t" \
  "ds_read_b64_tr_b16 a[132:133], v185 offset:16384\n\t" \
  "v_and_b32 v219, 0x7fffffe0, v219\n\t" \
  "v_mfma_f32_16x16x32_bf16 v[36:39], a[164:167], v[60:63], v[36:39]\n\t" \
  "ds_read_b64_tr_b16 a[134:135], v185 offset:17408\n\t" \
  "v_mfma_f32_16x16x32_bf16 v[4:7], a[164:167], v[28:31], v[4:7]\n\t" \
  "ds_read_b64_tr_b16 a[136:137], v186 offset:16384\n\t" \
  "v_mfma_f32_16x16x32_bf16 v[8:11], a[168:171], v[28:31], v[8:11]\n\t" \
  "buffer_load_dwordx4 a[200:203], v250, s[16:19], s97 offen sc1\n\t" \
  "v_mfma_f32_16x16x32_bf16 v[40:43], a[168:171], v[60:63], v[40:43]\n\t" \
  "ds_read_b64_tr_b16 a[138:139], v186 offset:17408\n\t" \
  "v_mfma_f32_16x16x32_bf16 v[72:75], a[168:171], v[92:95], v[72:75]\n\t" \
  "ds_read_b64_tr_b16 a[140:141], v187 offset:16384\n\t" \
  "v_mfma_f32_16x16x32_bf16 v[104:107], a[168:171], v[124:127], v[104:107]\n\t" \
  "ds_read_b128 v[168:171], v219 offset:0\n\t" \
  "v_mfma_f32_16x16x32_bf16 v[108:111], a[172:175], v[124:127], v[108:111]\n\t" \
  "buffer_load_dwordx4 a[204:207], v251, s[16:19], s97 offen sc1\n\t" \
  "v_mfma_f32_16x16x32_bf16 v[76:79], a[172:175], v[92:95], v[76:79]\n\t" \
  "ds_read_b64_tr_b16 a[142:143], v187 offset:17408\n\t" \
  "s_add_u32 s97, s97, s83\n\t" \
  "v_mfma_f32_16x16x32_bf16 v[44:47], a[172:175], v[60:63], v[44:47]\n\t" \
  "ds_read_b64_tr_b16 a[144:145], v188 offset:16384\n\t" \
  "v_mfma_f32_16x16x32_bf16 v[12:15], a[172:175], v[28:31], v[12:15]\n\t" \
  "buffer_load_dwordx4 a[208:211], v248, s[16:19], s97 offen sc1\n\t" \
  "v_mfma_f32_16x16x32_bf16 a[64:67], a[176:179], v[28:31], a[64:67]\n\t" \
  "ds_read_b64_tr_b16 a[146:147], v188 offset:17408\n\t" \
  "v_mfma_f32_16x16x32_bf16 a[68:71], a[176:179], v[60:63], a[68:71]\n\t" \
  "ds_read_b64_tr_b16 a[148:149], v189 offset:16384\n\t" \
  "v_mfma_f32_16x16x32_bf16 a[72:75], a[176:179], v[92:95], a[72:75]\n\t" \
  "ds_read_b64_tr_b16 a[150:151], v189 offset:17408\n\t" \
  "v_mfma_f32_16x16x32_bf16 a[76:79], a[176:179], v[124:127], a[76:79]\n\t" \
  "buffer_load_dwordx4 a[212:215], v249, s[16:19], s97 offen sc1\n\t" \
  "v_mfma_f32_16x16x32_bf16 a[92:95], a[180:183], v[124:127], a[92:95]\n\t" \
  "ds_read_b64_tr_b16 a[152:153], v190 offset:16384\n\t" \
  "v_mfma_f32_16x16x32_bf16 a[88:91], a[180:183], v[92:95], a[88:91]\n\t" \
  "ds_read_b128 v[172:175], v219 offset:16\n\t" \
  "v_mfma_f32_16x16x32_bf16 a[84:87], a[180:183], v[60:63], a[84:87]\n\t" \
  "ds_read_b64_tr_b16 a[154:155], v190 offset:17408\n\t" \
  "v_mfma_f32_16x16x32_bf16 a[80:83], a[180:183], v[28:31], a[80:83]\n\t" \
  "buffer_load_dwordx4 a[216:219], v250, s[16:19], s97 offen sc1\n\t" \
  "v_mfma_f32_16x16x32_bf16 a[96:99], a[184:187], v[28:31], a[96:99]\n\t" \
  "ds_read_b64_tr_b16 a[156:157], v191 offset:16384\n\t" \
  "v_mfma_f32_16x16x32_bf16 a[100:103], a[184:187], v[60:63], a[100:103]\n\t" \
  "ds_read_b64_tr_b16 a[158:159], v191 offset:17408\n\t" \
  "v_mfma_f32_16x16x32_bf16 a[104:107], a[184:187], v[92:95], a[104:107]\n\t" \
  "buffer_load_dwordx4 a[220:223], v251, s[16:19], s97 offen sc1\n\t" \
  "v_mfma_f32_16x16x32_bf16 a[108:111], a[184:187], v[124:127], a[108:111]\n\t" \
  "v_mfma_f32_16x16x32_bf16 a[124:127], a[188:191], v[124:127], a[124:127]\n\t" \
  "v_mfma_f32_16x16x32_bf16 a[120:123], a[188:191], v[92:95], a[120:123]\n\t" \
  "s_add_u32 s97, s78, 3670016\n\t" \
  "s_bfe_u32 s97, s97, 0x140003\n\t" \
  "s_add_u32 s97, s97, s82\n\t" \
  "v_mfma_f32_16x16x32_bf16 a[116:119], a[188:191], v[60:63], a[116:119]\n\t" \
  "v_mfma_f32_16x16x32_bf16 a[112:115], a[188:191], v[28:31], a[112:115]\n\t" \
  "s_waitcnt vmcnt(20) lgkmcnt(0)\n\t" \
  FSTP("v_writelane_b32 v255, s94, 50\n\t") \
  FSTP("v_writelane_b32 v255, s92, 51\n\t") \
  FSTP("s_memrealtime s[94:95]\n\t") \
  FSTP("s_memtime s[92:93]\n\t") \
  FSTS("s_memrealtime s[94:95]\n\t") \
  FSTS("s_waitcnt lgkmcnt(0)\n\t") \
  FSTS("v_writelane_b32 v255, s94, 14\n\t") \
  "v_mfma_f32_16x16x32_bf16 a[64:67], a[144:147], a[0:3], a[64:67]\n\t" \
  "buffer_load_dwordx4 a[224:227], v248, s[16:19], s97 offen sc1\n\t" \
  "v_mfma_f32_16x16x32_bf16 a[68:71], a[144:147], a[4:7], a[68:71]\n\t" \
  "s_bfe_u32 s39, s2, 0x30003\n\t" \
  "ds_read_b64_tr_b16 a[160:161], v184 offset:24576\n\t" \
  "s_lshl_b32 s40, s39, 18\n\t" \
  "v_mfma_f32_16x16x32_bf16 a[72:75], a[144:147], a[8:11], a[72:75]\n\t" \
  "s_lshr_b32 s98, s2, 3\n\t" \
  "ds_read_b64_tr_b16 a[162:163], v184 offset:25600\n\t" \
  "v_accvgpr_read_b32 v16, a64\n\t" \
  "v_mfma_f32_16x16x32_bf16 a[76:79], a[144:147], a[12:15], a[76:79]\n\t" \
  "s_lshl_b32 s41, s3, 5\n\t" \
  "v_accvgpr_read_b32 v17, a65\n\t" \
  "v_accvgpr_read_b32 v18, a66\n\t" \
  "v_mfma_f32_16x16x32_bf16 a[92:95], a[148:151], a[12:15], a[92:95]\n\t" \
  ".Lhs2:\n\ts_cbranch_execz .Lhs2n\n\t" \
  "buffer_load_dwordx4 a[228:231], v249, s[16:19], s97 offen sc1\n\t" \
  "v_mfma_f32_16x16x32_bf16 a[88:91], a[148:151], a[8:11], a[88:91]\n\t" \
  "s_add_u32 s41, s41, s98\n\t" \
  "ds_read_b64_tr_b16 a[164:165], v185 offset:24576\n\t" \
  "v_accvgpr_read_b32 v19, a67\n\t" \
  "v_mfma_f32_16x16x32_bf16 a[84:87], a[148:151], a[4:7], a[84:87]\n\t" \
  "s_lshl_b32 s41, s41, 7\n\t" \
  "ds_read_b64_tr_b16 a[166:167], v185 offset:25600\n\t" \
  "v_accvgpr_read_b32 v48, a68\n\t" \
  "v_mfma_f32_16x16x32_bf16 a[80:83], a[148:151], a[0:3], a[80:83]\n\t" \
  "s_and_b32 s12, s2, 3\n\t" \
  "ds_read_b64_tr_b16 a[168:169], v186 offset:24576\n\t" \
  "v_accvgpr_read_b32 v49, a69\n\t" \
  "v_mfma_f32_16x16x32_bf16 a[96:99], a[152:155], a[0:3], a[96:99]\n\t" \
  "buffer_load_dwordx4 a[232:235], v250, s[16:19], s97 offen sc1\n\t" \
  "v_mfma_f32_16x16x32_bf16 a[100:103], a[152:155], a[4:7], a[100:103]\n\t" \
  "s_bfe_u32 s13, s2, 0x10002\n\t" \
  "ds_read_b64_tr_b16 a[170:171], v186 offset:25600\n\t" \
  "v_accvgpr_read_b32 v50, a70\n\t" \
  "v_mfma_f32_16x16x32_bf16 a[104:107], a[152:155], a[8:11], a[104:107]\n\t" \
  "s_lshl1_add_u32 s12, s12, s13\n\t" \
  "ds_read_b64_tr_b16 a[172:173], v187 offset:24576\n\t" \
  "v_accvgpr_read_b32 v51, a71\n\t" \
  "v_mfma_f32_16x16x32_bf16 a[108:111], a[152:155], a[12:15], a[108:111]\n\t" \
  "s_bfe_u32 s59, s2, 0x20003\n\t" \
  "v_accvgpr_read_b32 v80, a72\n\t" \
  "v_accvgpr_read_b32 v81, a73\n\t" \
  "v_mfma_f32_16x16x32_bf16 a[124:127], a[156:159], a[12:15], a[124:127]\n\t" \
  "buffer_load_dwordx4 a[236:239], v251, s[16:19], s97 offen sc1\n\t" \
  "v_mfma_f32_16x16x32_bf16 a[120:123], a[156:159], a[8:11], a[120:123]\n\t" \
  "s_add_u32 s96, s78, 4718592\n\t" \
  "s_and_b32 s96, s96, 0x780000\n\t" \
  "s_add_u32 m0, s76, 98304\n\t" \
  "v_mfma_f32_16x16x32_bf16 a[116:119], a[156:159], a[4:7], a[116:119]\n\t" \
  "s_or_b32 s59, s59, 24\n\t" \
  "ds_read_b64_tr_b16 a[174:175], v187 offset:25600\n\t" \
  "v_accvgpr_read_b32 v82, a74\n\t" \
  "v_mfma_f32_16x16x32_bf16 a[112:115], a[156:159], a[0:3], a[112:115]\n\t" \
  "buffer_load_dwordx4 v200, s[20:23], s96 offen offset:0 lds\n\t" \
  "v_mfma_f32_16x16x32_bf16 v[0:3], a[128:131], a[0:3], v[0:3]\n\t" \
  "ds_read_b64_tr_b16 a[176:177], v188 offset:24576\n\t" \
  "v_accvgpr_read_b32 v83, a75\n\t" \
  "v_mfma_f32_16x16x32_bf16 v[32:35], a[128:131], a[4:7], v[32:35]\n\t" \
  "ds_read_b64_tr_b16 a[178:179], v188 offset:25600\n\t" \
  "v_accvgpr_read_b32 v112, a76\n\t" \
  "v_mfma_f32_16x16x32_bf16 v[64:67], a[128:131], a[8:11], v[64:67]\n\t" \
  "ds_read_b64_tr_b16 a[180:181], v189 offset:24576\n\t" \
  "v_accvgpr_read_b32 v113, a77\n\t" \
  "v_mfma_f32_16x16x32_bf16 v[96:99], a[128:131], a[12:15], v[96:99]\n\t" \
  "buffer_load_dwordx4 v201, s[20:23], s96 offen offset:1024 lds\n\t" \
  "v_mfma_f32_16x16x32_bf16 v[100:103], a[132:135], a[12:15], v[100:103]\n\t" \
  "ds_read_b64_tr_b16 a[182:183], v189 offset:25600\n\t" \
  "v_accvgpr_read_b32 v114, a78\n\t" \
  "v_mfma_f32_16x16x32_bf16 v[68:71], a[132:135], a[8:11], v[68:71]\n\t" \
  "ds_read_b64_tr_b16 a[184:185], v190 offset:24576\n\t" \
  "v_accvgpr_read_b32 v115, a79\n\t" \
  "v_mfma_f32_16x16x32_bf16 v[36:39], a[132:135], a[4:7], v[36:39]\n\t" \
  "ds_read_b64_tr_b16 a[186:187], v190 offset:25600\n\t" \
  "v_accvgpr_read_b32 v116, a92\n\t" \
  "v_mfma_f32_16x16x32_bf16 v[4:7], a[132:135], a[0:3], v[4:7]\n\t" \
  "buffer_load_dwordx4 v202, s[20:23], s96 offen offset:2048 lds\n\t" \
  "v_mfma_f32_16x16x32_bf16 v[8:11], a[136:139], a[0:3], v[8:11]\n\t" \
  "ds_read_b64_tr_b16 a[188:189], v191 offset:24576\n\t" \
  "v_accvgpr_read_b32 v117, a93\n\t" \
  "v_mfma_f32_16x16x32_bf16 v[40:43], a[136:139], a[4:7], v[40:43]\n\t" \
  "ds_read_b64_tr_b16 a[190:191], v191 offset:25600\n\t" \
  "v_accvgpr_read_b32 v118, a94\n\t" \
  "v_mfma_f32_16x16x32_bf16 v[72:75], a[136:139], a[8:11], v[72:75]\n\t" \
  "v_accvgpr_read_b32 v119, a95\n\t" \
  "v_accvgpr_read_b32 v84, a88\n\t" \
  "v_mfma_f32_16x16x32_bf16 v[104:107], a[136:139], a[12:15], v[104:107]\n\t" \
  "buffer_load_dwordx4 v203, s[20:23], s96 offen offset:3072 lds\n\t" \
  "v_mfma_f32_16x16x32_bf16 v[108:111], a[140:143], a[12:15], v[108:111]\n\t" \
  "v_accvgpr_read_b32 v85, a89\n\t" \
  "v_accvgpr_read_b32 v86, a90\n\t" \
  "v_mfma_f32_16x16x32_bf16 v[76:79], a[140:143], a[8:11], v[76:79]\n\t" \
  "s_add_u32 s97, s97, s83\n\t" \
  "v_accvgpr_read_b32 v87, a91\n\t" \
  "v_accvgpr_read_b32 v52, a84\n\t" \
  "v_mfma_f32_16x16x32_bf16 v[44:47], a[140:143], a[4:7], v[44:47]\n\t" \
  "s_waitcnt vmcnt(24) lgkmcnt(0)\n\t" \
  "v_accvgpr_read_b32 v53, a85\n\t" \
  "v_accvgpr_read_b32 v54, a86\n\t" \
  "v_mfma_f32_16x16x32_bf16 v[12:15], a[140:143], a[0:3], v[12:15]\n\t" \
  "s_barrier\n\t" \
  "v_mfma_f32_16x16x32_bf16 a[96:99], a[184:187], a[16:19], a[96:99]\n\t" \
  "buffer_load_dwordx4 a[240:243], v248, s[16:19], s97 offen sc1\n\t" \
  "v_mfma_f32_16x16x32_bf16 a[100:103], a[184:187], a[20:23], a[100:103]\n\t" \
  "ds_read_b64_tr_b16 a[128:129], v184 offset:32768\n\t" \
  "v_accvgpr_read_b32 v55, a87\n\t" \
  "v_mfma_f32_16x16x32_bf16 a[104:107], a[184:187], a[24:27], a[104:107]\n\t" \
  "ds_read_b64_tr_b16 a[130:131], v184 offset:33792\n\t" \
  "v_accvgpr_read_b32 v20, a80\n\t" \
  "v_mfma_f32_16x16x32_bf16 a[108:111], a[184:187], a[28:31], a[108:111]\n\t" \
  "v_accvgpr_read_b32 v21, a81\n\t" \
  "v_accvgpr_read_b32 v22, a82\n\t" \
  "v_mfma_f32_16x16x32_bf16 a[124:127], a[188:191], a[28:31], a[124:127]\n\t" \
  "buffer_load_dwordx4 a[244:247], v249, s[16:19], s97 offen sc1\n\t" \
  "v_mfma_f32_16x16x32_bf16 a[120:123], a[188:191], a[24:27], a[120:123]\n\t" \
  "ds_read_b64_tr_b16 a[132:133], v185 offset:32768\n\t" \
  "v_accvgpr_read_b32 v23, a83\n\t" \
  "v_mfma_f32_16x16x32_bf16 a[116:119], a[188:191], a[20:23], a[116:119]\n\t" \
  "ds_read_b64_tr_b16 a[134:135], v185 offset:33792\n\t" \
  "v_accvgpr_read_b32 v24, a96\n\t" \
  "v_mfma_f32_16x16x32_bf16 a[112:115], a[188:191], a[16:19], a[112:115]\n\t" \
  "ds_read_b64_tr_b16 a[136:137], v186 offset:32768\n\t" \
  "v_accvgpr_read_b32 v25, a97\n\t" \
  "v_mfma_f32_16x16x32_bf16 v[0:3], a[160:163], a[16:19], v[0:3]\n\t" \
  "buffer_load_dwordx4 a[248:251], v250, s[16:19], s97 offen sc1\n\t" \
  "v_mfma_f32_16x16x32_bf16 v[32:35], a[160:163], a[20:23], v[32:35]\n\t" \
  "ds_read_b64_tr_b16 a[138:139], v186 offset:33792\n\t" \
  "v_accvgpr_read_b32 v26, a98\n\t" \
  "v_mfma_f32_16x16x32_bf16 v[64:67], a[160:163], a[24:27], v[64:67]\n\t" \
  "ds_read_b64_tr_b16 a[140:141], v187 offset:32768\n\t" \
  "v_accvgpr_read_b32 v27, a99\n\t" \
  "v_mfma_f32_16x16x32_bf16 v[96:99], a[160:163], a[28:31], v[96:99]\n\t" \
  "v_accvgpr_read_b32 v56, a100\n\t" \
  "v_accvgpr_read_b32 v57, a101\n\t" \
  "v_mfma_f32_16x16x32_bf16 v[100:103], a[164:167], a[28:31], v[100:103]\n\t" \
  "buffer_load_dwordx4 a[252:255], v251, s[16:19], s97 offen sc1\n\t" \
  "v_mfma_f32_16x16x32_bf16 v[68:71], a[164:167], a[24:27], v[68:71]\n\t" \
  "s_add_u32 s97, s78, 4194304\n\t" \
  "s_bfe_u32 s97, s97, 0x140003\n\t" \
  "s_add_u32 s97, s97, s82\n\t" \
  "v_mfma_f32_16x16x32_bf16 v[36:39], a[164:167], a[20:23], v[36:39]\n\t" \
  "ds_read_b64_tr_b16 a[142:143], v187 offset:33792\n\t" \
  "v_accvgpr_read_b32 v58, a102\n\t" \
  "v_mfma_f32_16x16x32_bf16 v[4:7], a[164:167], a[16:19], v[4:7]\n\t" \
  "buffer_load_dwordx4 a[0:3], v248, s[16:19], s97 offen sc1\n\t" \
  "v_mfma_f32_16x16x32_bf16 v[8:11], a[168:171], a[16:19], v[8:11]\n\t" \
  "ds_read_b64_tr_b16 a[144:145], v188 offset:32768\n\t" \
  "v_accvgpr_read_b32 v59, a103\n\t" \
  "v_mfma_f32_16x16x32_bf16 v[40:43], a[168:171], a[20:23], v[40:43]\n\t" \
  "ds_read_b64_tr_b16 a[146:147], v188 offset:33792\n\t" \
  "v_accvgpr_read_b32 v88, a104\n\t" \
  "v_mfma_f32_16x16x32_bf16 v[72:75], a[168:171], a[24:27], v[72:75]\n\t" \
  "ds_read_b64_tr_b16 a[148:149], v189 offset:32768\n\t" \
  "v_accvgpr_read_b32 v89, a105\n\t" \
  "v_mfma_f32_16x16x32_bf16 v[104:107], a[168:171], a[28:31], v[104:107]\n\t" \
  "buffer_load_dwordx4 a[4:7], v249, s[16:19], s97 offen sc1\n\t" \
  "v_mfma_f32_16x16x32_bf16 v[108:111], a[172:175], a[28:31], v[108:111]\n\t" \
  "ds_read_b64_tr_b16 a[150:151], v189 offset:33792\n\t" \
  "v_accvgpr_read_b32 v90, a106\n\t" \
  "v_mfma_f32_16x16x32_bf16 v[76:79], a[172:175], a[24:27], v[76:79]\n\t" \
  "ds_read_b64_tr_b16 a[152:153], v190 offset:32768\n\t" \
  "v_accvgpr_read_b32 v91, a107\n\t" \
  "v_mfma_f32_16x16x32_bf16 v[44:47], a[172:175], a[20:23], v[44:47]\n\t" \
  "ds_read_b64_tr_b16 a[154:155], v190 offset:33792\n\t" \
  "v_accvgpr_read_b32 v120, a108\n\t" \
  "v_mfma_f32_16x16x32_bf16 v[12:15], a[172:175], a[16:19], v[12:15]\n\t" \
  "buffer_load_dwordx4 a[8:11], v250, s[16:19], s97 offen sc1\n\t" \
  "v_mfma_f32_16x16x32_bf16 v[16:19], a[176:179], a[16:19], v[16:19]\n\t" \
  "ds_read_b64_tr_b16 a[156:157], v191 offset:32768\n\t" \
  "v_accvgpr_read_b32 v121, a109\n\t" \
  "v_mfma_f32_16x16x32_bf16 v[48:51], a[176:179], a[20:23], v[48:51]\n\t" \
  "ds_read_b64_tr_b16 a[158:159], v191 offset:33792\n\t" \
  "v_accvgpr_read_b32 v122, a110\n\t" \
  "v_mfma_f32_16x16x32_bf16 v[80:83], a[176:179], a[24:27], v[80:83]\n\t" \
  "v_accvgpr_read_b32 v123, a111\n\t" \
  "v_accvgpr_read_b32 v124, a124\n\t" \
  "v_mfma_f32_16x16x32_bf16 v[112:115], a[176:179], a[28:31], v[112:115]\n\t" \
  "buffer_load_dwordx4 a[12:15], v251, s[16:19], s97 offen sc1\n\t" \
  "v_mfma_f32_16x16x32_bf16 v[116:119], a[180:183], a[28:31], v[116:119]\n\t" \
  "v_mbcnt_lo_u32_b32 v217, -1, 0\n\t" \
  "v_mbcnt_hi_u32_b32 v217, -1, v217\n\t" \
  "v_mfma_f32_16x16x32_bf16 v[84:87], a[180:183], a[24:27], v[84:87]\n\t" \
  "v_cmp_gt_u32 vcc, 16, v217\n\t" \
  "v_lshlrev_b32 v217, 7, v217\n\t" \
  "v_mfma_f32_16x16x32_bf16 v[52:55], a[180:183], a[20:23], v[52:55]\n\t" \
  "v_bfrev_b32 v215, 1\n\t" \
  "v_cndmask_b32 v217, v215, v217, vcc\n\t" \
  "v_mfma_f32_16x16x32_bf16 v[20:23], a[180:183], a[16:19], v[20:23]\n\t" \
  "s_waitcnt vmcnt(28) lgkmcnt(0)\n\t" \
  "v_bfe_u32 v215, v217, 10, 1\n\t" \
  "s_mov_b32 s99, 11264\n\t" \
  "v_mfma_f32_16x16x32_bf16 v[0:3], a[128:131], a[32:35], v[0:3]\n\t" \
  "v_mad_u32_u24 v215, v215, s99, v217\n\t" \
  "s_add_u32 s97, s97, s83\n\t" \
  "v_mfma_f32_16x16x32_bf16 v[32:35], a[128:131], a[36:39], v[32:35]\n\t" \
  "buffer_load_dwordx4 a[16:19], v248, s[16:19], s97 offen sc1\n\t" \
  "v_mfma_f32_16x16x32_bf16 v[64:67], a[128:131], a[40:43], v[64:67]\n\t" \
  "ds_read_b64_tr_b16 a[160:161], v184 offset:40960\n\t" \
  "v_accvgpr_read_b32 v125, a125\n\t" \
  "v_mfma_f32_16x16x32_bf16 v[96:99], a[128:131], a[44:47], v[96:99]\n\t" \
  "ds_read_b64_tr_b16 a[162:163], v184 offset:41984\n\t" \
  "v_accvgpr_read_b32 v126, a126\n\t" \
  "v_mfma_f32_16x16x32_bf16 v[100:103], a[132:135], a[44:47], v[100:103]\n\t" \
  "buffer_load_dwordx4 a[20:23], v249, s[16:19], s97 offen sc1\n\t" \
  "v_mfma_f32_16x16x32_bf16 v[68:71], a[132:135], a[40:43], v[68:71]\n\t" \
  "ds_read_b64_tr_b16 a[164:165], v185 offset:40960\n\t" \
  "v_accvgpr_read_b32 v127, a127\n\t" \
  "v_mfma_f32_16x16x32_bf16 v[36:39], a[132:135], a[36:39], v[36:39]\n\t" \
  "ds_read_b64_tr_b16 a[166:167], v185 offset:41984\n\t" \
  "v_accvgpr_read_b32 v92, a120\n\t" \
  "v_mfma_f32_16x16x32_bf16 v[4:7], a[132:135], a[32:35], v[4:7]\n\t" \
  "ds_read_b64_tr_b16 a[168:169], v186 offset:40960\n\t" \
  "v_accvgpr_read_b32 v93, a121\n\t" \
  "v_mfma_f32_16x16x32_bf16 v[8:11], a[136:139], a[32:35], v[8:11]\n\t" \
  "buffer_load_dwordx4 a[24:27], v250, s[16:19], s97 offen sc1\n\t" \
  "v_mfma_f32_16x16x32_bf16 v[40:43], a[136:139], a[36:39], v[40:43]\n\t" \
  "ds_read_b64_tr_b16 a[170:171], v186 offset:41984\n\t" \
  "v_accvgpr_read_b32 v94, a122\n\t" \
  "v_mfma_f32_16x16x32_bf16 v[72:75], a[136:139], a[40:43], v[72:75]\n\t" \
  "ds_read_b64_tr_b16 a[172:173], v187 offset:40960\n\t" \
  "v_accvgpr_read_b32 v95, a123\n\t" \
  "v_mfma_f32_16x16x32_bf16 v[104:107], a[136:139], a[44:47], v[104:107]\n\t" \
  "v_accvgpr_read_b32 v60, a116\n\t" \
  "v_accvgpr_read_b32 v61, a117\n\t" \
  "v_mfma_f32_16x16x32_bf16 v[108:111], a[140:143], a[44:47], v[108:111]\n\t" \
  "buffer_load_dwordx4 a[28:31], v251, s[16:19], s97 offen sc1\n\t" \
  "v_mfma_f32_16x16x32_bf16 v[76:79], a[140:143], a[40:43], v[76:79]\n\t" \
  "s_add_u32 s96, s78, 5242880\n\t" \
  "s_and_b32 s96, s96, 0x780000\n\t" \
  "s_add_u32 m0, s76, 0\n\t" \
  "v_mfma_f32_16x16x32_bf16 v[44:47], a[140:143], a[36:39], v[44:47]\n\t" \
  "ds_read_b64_tr_b16 a[174:175], v187 offset:41984\n\t" \
  "v_accvgpr_read_b32 v62, a118\n\t" \
  "v_mfma_f32_16x16x32_bf16 v[12:15], a[140:143], a[32:35], v[12:15]\n\t" \
  "buffer_load_dwordx4 v200, s[20:23], s96 offen offset:0 lds\n\t" \
  "v_mfma_f32_16x16x32_bf16 v[16:19], a[144:147], a[32:35], v[16:19]\n\t" \
  "ds_read_b64_tr_b16 a[176:177], v188 offset:40960\n\t" \
  "v_accvgpr_read_b32 v63, a119\n\t" \
  "v_mfma_f32_16x16x32_bf16 v[48:51], a[144:147], a[36:39], v[48:51]\n\t" \
  "ds_read_b64_tr_b16 a[178:179], v188 offset:41984\n\t" \
  "v_accvgpr_read_b32 v28, a112\n\t" \
  "v_mfma_f32_16x16x32_bf16 v[80:83], a[144:147], a[40:43], v[80:83]\n\t" \
  "ds_read_b64_tr_b16 a[180:181], v189 offset:40960\n\t" \
  "v_accvgpr_read_b32 v29, a113\n\t" \
  "v_mfma_f32_16x16x32_bf16 v[112:115], a[144:147], a[44:47], v[112:115]\n\t" \
  "buffer_load_dwordx4 v201, s[20:23], s96 offen offset:1024 lds\n\t" \
  "v_mfma_f32_16x16x32_bf16 v[116:119], a[148:151], a[44:47], v[116:119]\n\t" \
  "ds_read_b64_tr_b16 a[182:183], v189 offset:41984\n\t" \
  "v_accvgpr_read_b32 v30, a114\n\t" \
  "v_mfma_f32_16x16x32_bf16 v[84:87], a[148:151], a[40:43], v[84:87]\n\t" \
  "ds_read_b64_tr_b16 a[184:185], v190 offset:40960\n\t" \
  "v_accvgpr_read_b32 v31, a115\n\t" \
  "v_mfma_f32_16x16x32_bf16 v[52:55], a[148:151], a[36:39], v[52:55]\n\t" \
  "ds_read_b64_tr_b16 a[186:187], v190 offset:41984\n\t" \
  "v_mfma_f32_16x16x32_bf16 v[20:23], a[148:151], a[32:35], v[20:23]\n\t" \
  "buffer_load_dwordx4 v202, s[20:23], s96 offen offset:2048 lds\n\t" \
  "v_mfma_f32_16x16x32_bf16 v[24:27], a[152:155], a[32:35], v[24:27]\n\t" \
  "ds_read_b64_tr_b16 a[188:189], v191 offset:40960\n\t" \
  "v_mfma_f32_16x16x32_bf16 v[56:59], a[152:155], a[36:39], v[56:59]\n\t" \
  "ds_read_b64_tr_b16 a[190:191], v191 offset:41984\n\t" \
  "v_mfma_f32_16x16x32_bf16 v[88:91], a[152:155], a[40:43], v[88:91]\n\t" \
  "s_cmp_lt_u32 s98, 24\n\t" \
  "s_cselect_b32 s59, s98, s59\n\t" \
  "s_mul_i32 s53, s98, 57344\n\t" \
  "v_mfma_f32_16x16x32_bf16 v[120:123], a[152:155], a[44:47], v[120:123]\n\t" \
  "buffer_load_dwordx4 v203, s[20:23], s96 offen offset:3072 lds\n\t" \
  "v_mfma_f32_16x16x32_bf16 v[124:127], a[156:159], a[44:47], v[124:127]\n\t" \
  "s_lshl_b32 s14, s59, 5\n\t" \
  "s_sub_u32 s53, s53, s14\n\t" \
  "s_lshl_b32 s52, s98, 16\n\t" \
  "v_mfma_f32_16x16x32_bf16 v[92:95], a[156:159], a[40:43], v[92:95]\n\t" \
  "s_lshl_b32 s14, s3, 14\n\t" \
  "s_add_u32 s52, s52, s14\n\t" \
  "s_lshl_b32 s14, s13, 13\n\t" \
  "v_mfma_f32_16x16x32_bf16 v[60:63], a[156:159], a[36:39], v[60:63]\n\t" \
  "s_waitcnt vmcnt(32) lgkmcnt(0)\n\t" \
  "s_mov_b32 s99, 0\n\t" \
  "s_add_u32 s52, s52, s14\n\t" \
  "v_mfma_f32_16x16x32_bf16 v[28:31], a[156:159], a[32:35], v[28:31]\n\t" \
  "s_barrier\n\t" \
  "v_mfma_f32_16x16x32_bf16 v[0:3], a[160:163], a[48:51], v[0:3]\n\t" \
  "buffer_load_dwordx4 v[136:139], v209, s[8:11], s99 offen nt\n\t" \
  "v_mfma_f32_16x16x32_bf16 v[32:35], a[160:163], a[52:55], v[32:35]\n\t" \
  "s_add_u32 s52, s52, s78\n\t" \
  "ds_read_b64_tr_b16 a[128:129], v184 offset:49152\n\t" \
  "s_lshl_b32 s51, s3, 15\n\t" \
  "v_mfma_f32_16x16x32_bf16 v[64:67], a[160:163], a[56:59], v[64:67]\n\t" \
  "s_add_u32 s51, s51, s78\n\t" \
  "ds_read_b64_tr_b16 a[130:131], v184 offset:50176\n\t" \
  "s_lshl_b32 s14, s39, 8\n\t" \
  "v_mfma_f32_16x16x32_bf16 v[96:99], a[160:163], a[60:63], v[96:99]\n\t" \
  "s_add_u32 s51, s51, s14\n\t" \
  "ds_read_b64_tr_b16 a[64:65], v192 offset:8192\n\t" \
  "s_lshr_b32 s98, s98, 2\n\t" \
  "v_mfma_f32_16x16x32_bf16 v[100:103], a[164:167], a[60:63], v[100:103]\n\t" \
  "s_mov_b32 s99, 12288\n\t" \
  "ds_read_b64_tr_b16 a[132:133], v185 offset:49152\n\t" \
  "s_add_u32 s13, s13, 6\n\t" \
  "v_mfma_f32_16x16x32_bf16 v[68:71], a[164:167], a[56:59], v[68:71]\n\t" \
  "ds_read_b64_tr_b16 a[134:135], v185 offset:50176\n\t" \
  "v_mfma_f32_16x16x32_bf16 v[36:39], a[164:167], a[52:55], v[36:39]\n\t" \
  "ds_read_b64_tr_b16 a[66:67], v192 offset:9216\n\t" \
  "v_mfma_f32_16x16x32_bf16 v[4:7], a[164:167], a[48:51], v[4:7]\n\t" \
  "ds_read_b64_tr_b16 a[136:137], v186 offset:49152\n\t" \
  "v_mfma_f32_16x16x32_bf16 v[8:11], a[168:171], a[48:51], v[8:11]\n\t" \
  "buffer_load_dwordx4 v[140:143], v209, s[8:11], s99 offen nt\n\t" \
  "v_mfma_f32_16x16x32_bf16 v[40:43], a[168:171], a[52:55], v[40:43]\n\t" \
  "ds_read_b64_tr_b16 a[138:139], v186 offset:50176\n\t" \
  "s_mov_b32 s99, 24576\n\t" \
  "v_mfma_f32_16x16x32_bf16 v[72:75], a[168:171], a[56:59], v[72:75]\n\t" \
  "ds_read_b64_tr_b16 a[140:141], v187 offset:49152\n\t" \
  "v_mfma_f32_16x16x32_bf16 v[104:107], a[168:171], a[60:63], v[104:107]\n\t" \
  "ds_read_b64_tr_b16 a[68:69], v193 offset:8192\n\t" \
  "v_mfma_f32_16x16x32_bf16 v[108:111], a[172:175], a[60:63], v[108:111]\n\t" \
  "ds_read_b64_tr_b16 a[142:143], v187 offset:50176\n\t" \
  "v_mfma_f32_16x16x32_bf16 v[76:79], a[172:175], a[56:59], v[76:79]\n\t" \
  "ds_read_b64_tr_b16 a[144:145], v188 offset:49152\n\t" \
  "v_mfma_f32_16x16x32_bf16 v[44:47], a[172:175], a[52:55], v[44:47]\n\t" \
  "ds_read_b64_tr_b16 a[70:71], v193 offset:9216\n\t" \
  "v_mfma_f32_16x16x32_bf16 v[12:15], a[172:175], a[48:51], v[12:15]\n\t" \
  "buffer_load_dwordx4 v[144:147], v209, s[8:11], s99 offen nt\n\t" \
  "v_mfma_f32_16x16x32_bf16 v[16:19], a[176:179], a[48:51], v[16:19]\n\t" \
  "ds_read_b64_tr_b16 a[146:147], v188 offset:50176\n\t" \
  "s_mov_b32 s99, 36864\n\t" \
  "v_mfma_f32_16x16x32_bf16 v[48:51], a[176:179], a[52:55], v[48:51]\n\t" \
  "ds_read_b64_tr_b16 a[148:149], v189 offset:49152\n\t" \
  "v_mfma_f32_16x16x32_bf16 v[80:83], a[176:179], a[56:59], v[80:83]\n\t" \
  "ds_read_b64_tr_b16 a[150:151], v189 offset:50176\n\t" \
  "v_mfma_f32_16x16x32_bf16 v[112:115], a[176:179], a[60:63], v[112:115]\n\t" \
  "ds_read_b64_tr_b16 a[72:73], v194 offset:8192\n\t" \
  "v_mfma_f32_16x16x32_bf16 v[116:119], a[180:183], a[60:63], v[116:119]\n\t" \
  "ds_read_b64_tr_b16 a[152:153], v190 offset:49152\n\t" \
  "v_mfma_f32_16x16x32_bf16 v[84:87], a[180:183], a[56:59], v[84:87]\n\t" \
  "ds_read_b64_tr_b16 a[74:75], v194 offset:9216\n\t" \
  "v_mfma_f32_16x16x32_bf16 v[52:55], a[180:183], a[52:55], v[52:55]\n\t" \
  "ds_read_b64_tr_b16 a[154:155], v190 offset:50176\n\t" \
  "v_mfma_f32_16x16x32_bf16 v[20:23], a[180:183], a[48:51], v[20:23]\n\t" \
  "buffer_load_dwordx4 v[148:151], v209, s[8:11], s99 offen nt\n\t" \
  "v_mfma_f32_16x16x32_bf16 v[24:27], a[184:187], a[48:51], v[24:27]\n\t" \
  "ds_read_b64_tr_b16 a[156:157], v191 offset:49152\n\t" \
  "v_mfma_f32_16x16x32_bf16 v[56:59], a[184:187], a[52:55], v[56:59]\n\t" \
  "ds_read_b64_tr_b16 a[158:159], v191 offset:50176\n\t" \
  "v_mfma_f32_16x16x32_bf16 v[88:91], a[184:187], a[56:59], v[88:91]\n\t" \
  "ds_read_b64_tr_b16 a[76:77], v195 offset:8192\n\t" \
  "v_mfma_f32_16x16x32_bf16 v[120:123], a[184:187], a[60:63], v[120:123]\n\t" \
  "s_cmp_eq_u32 s98, s13\n\t" \
  "s_cselect_b32 s57, 0x80000000, 0\n\t" \
  "s_cselect_b32 s30, 0, 0x8000\n\t" \
  "v_mfma_f32_16x16x32_bf16 v[124:127], a[188:191], a[60:63], v[124:127]\n\t" \
  "s_mul_i32 s14, s12, 28\n\t" \
  "ds_read_b64_tr_b16 a[78:79], v195 offset:9216\n\t" \
  "s_add_u32 s59, s59, s14\n\t" \
  "v_mfma_f32_16x16x32_bf16 v[92:95], a[188:191], a[56:59], v[92:95]\n\t" \
  "s_lshl_b32 s14, s59, 15\n\t" \
  "s_add_u32 s28, s28, s14\n\t" \
  "s_addc_u32 s29, s29, 0\n\t" \
  "v_mfma_f32_16x16x32_bf16 v[60:63], a[188:191], a[52:55], v[60:63]\n\t" \
  "s_mul_i32 s14, s3, 1835008\n\t" \
  "s_lshl_b32 s59, s59, 5\n\t" \
  "s_add_u32 s14, s14, s59\n\t" \
  "v_mfma_f32_16x16x32_bf16 v[28:31], a[188:191], a[48:51], v[28:31]\n\t" \
  "s_waitcnt vmcnt(32) lgkmcnt(2)\n\t" \
  "s_add_u32 s68, s68, s14\n\t" \
  "s_addc_u32 s69, s69, 0\n\t" \
  "v_mfma_f32_16x16x32_bf16 v[0:3], a[128:131], a[192:195], v[0:3]\n\t" \
  "ds_read_b64_tr_b16 a[160:161], v184 offset:57344\n\t" \
  "v_mfma_f32_16x16x32_bf16 v[32:35], a[128:131], a[196:199], v[32:35]\n\t" \
  "ds_read_b64_tr_b16 a[80:81], v196 offset:8192\n\t" \
  "v_mfma_f32_16x16x32_bf16 v[64:67], a[128:131], a[200:203], v[64:67]\n\t" \
  "ds_read_b64_tr_b16 a[162:163], v184 offset:58368\n\t" \
  "v_mfma_f32_16x16x32_bf16 v[96:99], a[128:131], a[204:207], v[96:99]\n\t" \
  "s_add_u32 s72, s72, s14\n\t" \
  "s_addc_u32 s73, s73, 0\n\t" \
  "s_sub_u32 s70, s70, s14\n\t" \
  "v_mfma_f32_16x16x32_bf16 v[100:103], a[132:135], a[204:207], v[100:103]\n\t" \
  "s_sub_u32 s74, s74, s14\n\t" \
  "ds_read_b64_tr_b16 a[164:165], v185 offset:57344\n\t" \
  "s_lshr_b32 s14, s78, 2\n\t" \
  "v_mfma_f32_16x16x32_bf16 v[68:71], a[132:135], a[200:203], v[68:71]\n\t" \
  "ds_read_b64_tr_b16 a[166:167], v185 offset:58368\n\t" \
  "v_mfma_f32_16x16x32_bf16 v[36:39], a[132:135], a[196:199], v[36:39]\n\t" \
  "ds_read_b64_tr_b16 a[82:83], v196 offset:9216\n\t" \
  "v_mfma_f32_16x16x32_bf16 v[4:7], a[132:135], a[192:195], v[4:7]\n\t" \
  "ds_read_b64_tr_b16 a[168:169], v186 offset:57344\n\t" \
  "v_mfma_f32_16x16x32_bf16 v[8:11], a[136:139], a[192:195], v[8:11]\n\t" \
  "ds_read_b64_tr_b16 a[84:85], v197 offset:8192\n\t" \
  "v_mfma_f32_16x16x32_bf16 v[40:43], a[136:139], a[196:199], v[40:43]\n\t" \
  "ds_read_b64_tr_b16 a[170:171], v186 offset:58368\n\t" \
  "v_mfma_f32_16x16x32_bf16 v[72:75], a[136:139], a[200:203], v[72:75]\n\t" \
  "ds_read_b64_tr_b16 a[172:173], v187 offset:57344\n\t" \
  "v_mfma_f32_16x16x32_bf16 v[104:107], a[136:139], a[204:207], v[104:107]\n\t" \
  "s_add_u32 s60, s60, s14\n\t" \
  "s_addc_u32 s61, s61, 0\n\t" \
  "v_mfma_f32_16x16x32_bf16 v[108:111], a[140:143], a[204:207], v[108:111]\n\t" \
  "ds_read_b64_tr_b16 a[174:175], v187 offset:58368\n\t" \
  "v_mfma_f32_16x16x32_bf16 v[76:79], a[140:143], a[200:203], v[76:79]\n\t" \
  "ds_read_b64_tr_b16 a[176:177], v188 offset:57344\n\t" \
  "v_mfma_f32_16x16x32_bf16 v[44:47], a[140:143], a[196:199], v[44:47]\n\t" \
  "ds_read_b64_tr_b16 a[86:87], v197 offset:9216\n\t" \
  "v_mfma_f32_16x16x32_bf16 v[12:15], a[140:143], a[192:195], v[12:15]\n\t" \
  "ds_read_b64_tr_b16 a[178:179], v188 offset:58368\n\t" \
  "v_mfma_f32_16x16x32_bf16 v[16:19], a[144:147], a[192:195], v[16:19]\n\t" \
  "ds_read_b64_tr_b16 a[88:89], v198 offset:8192\n\t" \
  "v_mfma_f32_16x16x32_bf16 v[48:51], a[144:147], a[196:199], v[48:51]\n\t" \
  "ds_read_b64_tr_b16 a[180:181], v189 offset:57344\n\t" \
  "v_mfma_f32_16x16x32_bf16 v[80:83], a[144:147], a[200:203], v[80:83]\n\t" \
  "ds_read_b64_tr_b16 a[182:183], v189 offset:58368\n\t" \
  "v_mfma_f32_16x16x32_bf16 v[112:115], a[144:147], a[204:207], v[112:115]\n\t" \
  "s_add_u32 s64, s64, s14\n\t" \
  "s_addc_u32 s65, s65, 0\n\t" \
  "s_lshl2_add_u32 s58, s2, s3\n\t" \
  "v_mfma_f32_16x16x32_bf16 v[116:119], a[148:151], a[204:207], v[116:119]\n\t" \
  "s_lshl_b32 s58, s58, 11\n\t" \
  "ds_read_b64_tr_b16 a[184:185], v190 offset:57344\n\t" \
  "s_bfe_u32 s13, s2, 0x10004\n\t" \
  "v_mfma_f32_16x16x32_bf16 v[84:87], a[148:151], a[200:203], v[84:87]\n\t" \
  "s_lshl1_add_u32 s12, s12, s13\n\t" \
  "ds_read_b64_tr_b16 a[90:91], v198 offset:9216\n\t" \
  "s_lshl_b32 s13, s13, 13\n\t" \
  "v_mfma_f32_16x16x32_bf16 v[52:55], a[148:151], a[196:199], v[52:55]\n\t" \
  "s_lshl_b32 s14, s12, 7\n\t" \
  "ds_read_b64_tr_b16 a[186:187], v190 offset:58368\n\t" \
  "s_add_u32 s13, s13, s14\n\t" \
  "v_mfma_f32_16x16x32_bf16 v[20:23], a[148:151], a[192:195], v[20:23]\n\t" \
  "s_add_u32 s13, s13, 16384\n\t" \
  "ds_read_b64_tr_b16 a[188:189], v191 offset:57344\n\t" \
  "s_lshl_b32 s12, s12, 21\n\t" \
  "v_mfma_f32_16x16x32_bf16 v[24:27], a[152:155], a[192:195], v[24:27]\n\t" \
  "ds_read_b64_tr_b16 a[92:93], v199 offset:8192\n\t" \
  "s_add_u32 s12, s12, 8388608\n\t" \
  "v_mfma_f32_16x16x32_bf16 v[56:59], a[152:155], a[196:199], v[56:59]\n\t" \
  "ds_read_b64_tr_b16 a[190:191], v191 offset:58368\n\t" \
  "v_mfma_f32_16x16x32_bf16 v[88:91], a[152:155], a[200:203], v[88:91]\n\t" \
  "s_bitcmp1_b32 s2, 3\n\t" \
  "s_cselect_b32 s91, s12, s13\n\t" \
  "s_lshr_b32 s12, s57, s3\n\t" \
  "v_mfma_f32_16x16x32_bf16 v[120:123], a[152:155], a[204:207], v[120:123]\n\t" \
  "s_cmp_lt_i32 s12, 0\n\t" \
  "s_cselect_b32 s91, s91, 0x80000000\n\t" \
  "s_lshl_b32 s90, s3, 7\n\t" \
  "v_mfma_f32_16x16x32_bf16 v[124:127], a[156:159], a[204:207], v[124:127]\n\t" \
  "s_add_u32 s90, s90, 0x220000\n\t" \
  "ds_read_b64_tr_b16 a[94:95], v199 offset:9216\n\t" \
  "s_and_b32 s98, s2, 0xc0\n\t" \
  "v_mfma_f32_16x16x32_bf16 v[92:95], a[156:159], a[200:203], v[92:95]\n\t" \
  "s_lshl_b32 s98, s98, 9\n\t" \
  "s_add_u32 s90, s90, s98\n\t" \
  "s_and_b32 s98, s2, 0x3\n\t" \
  "v_mfma_f32_16x16x32_bf16 v[60:63], a[156:159], a[196:199], v[60:63]\n\t" \
  "s_waitcnt vmcnt(28) lgkmcnt(1)\n\t" \
  "s_add_u32 s97, s78, 4718592\n\t" \
  "s_bfe_u32 s97, s97, 0x140003\n\t" \
  "v_mfma_f32_16x16x32_bf16 v[28:31], a[156:159], a[192:195], v[28:31]\n\t" \
  "s_barrier\n\t" \
  "v_mfma_f32_16x16x32_bf16 v[0:3], a[160:163], a[208:211], v[0:3]\n\t" \
  "s_add_u32 s97, s97, s82\n\t" \
  "ds_read_b64_tr_b16 a[128:129], v192 offset:0\n\t" \
  "v_mbcnt_lo_u32_b32 v233, -1, 0\n\t" \
  "v_mfma_f32_16x16x32_bf16 v[32:35], a[160:163], a[212:215], v[32:35]\n\t" \
  "buffer_load_dwordx4 a[32:35], v248, s[16:19], s97 offen sc1\n\t" \
  "v_mfma_f32_16x16x32_bf16 v[64:67], a[160:163], a[216:219], v[64:67]\n\t" \
  "s_lshl_b32 s98, s98, 13\n\t" \
  "ds_read_b64_tr_b16 a[130:131], v192 offset:1024\n\t" \
  "v_mbcnt_hi_u32_b32 v233, -1, v233\n\t" \
  "v_mfma_f32_16x16x32_bf16 v[96:99], a[160:163], a[220:223], v[96:99]\n\t" \
  "s_add_u32 s90, s90, s98\n\t" \
  "v_lshrrev_b32 v129, 4, v233\n\t" \
  "v_and_b32 v130, 15, v233\n\t" \
  "v_mfma_f32_16x16x32_bf16 v[100:103], a[164:167], a[220:223], v[100:103]\n\t" \
  "buffer_load_dwordx4 a[36:39], v249, s[16:19], s97 offen sc1\n\t" \
  "v_mfma_f32_16x16x32_bf16 v[68:71], a[164:167], a[216:219], v[68:71]\n\t" \
  "s_and_b32 s98, s2, 0x4\n\t" \
  "ds_read_b64_tr_b16 a[132:133], v193 offset:0\n\t" \
  "v_bfe_u32 v131, v233, 2, 2\n\t" \
  "v_mfma_f32_16x16x32_bf16 v[36:39], a[164:167], a[212:215], v[36:39]\n\t" \
  "s_lshl_b32 s98, s98, 10\n\t" \
  "ds_read_b64_tr_b16 a[134:135], v193 offset:1024\n\t" \
  "v_and_b32 v132, 3, v233\n\t" \
  "v_mfma_f32_16x16x32_bf16 v[4:7], a[164:167], a[208:211], v[4:7]\n\t" \
  "s_add_u32 s90, s90, s98\n\t" \
  "ds_read_b64_tr_b16 a[136:137], v194 offset:0\n\t" \
  "v_cmp_lt_u32 vcc, 6, v233\n\t" \
  "v_mfma_f32_16x16x32_bf16 v[8:11], a[168:171], a[208:211], v[8:11]\n\t" \
  "buffer_load_dwordx4 a[40:43], v250, s[16:19], s97 offen sc1\n\t" \
  "v_mfma_f32_16x16x32_bf16 v[40:43], a[168:171], a[212:215], v[40:43]\n\t" \
  "s_and_b32 s98, s2, 0x38\n\t" \
  "ds_read_b64_tr_b16 a[138:139], v194 offset:1024\n\t" \
  "v_mov_b32 v133, 6272\n\t" \
  "v_mfma_f32_16x16x32_bf16 v[72:75], a[168:171], a[216:219], v[72:75]\n\t" \
  "s_lshl_b32 s98, s98, 6\n\t" \
  "ds_read_b64_tr_b16 a[140:141], v195 offset:0\n\t" \
  "v_cndmask_b32 v133, 0, v133, vcc\n\t" \
  "v_mfma_f32_16x16x32_bf16 v[104:107], a[168:171], a[220:223], v[104:107]\n\t" \
  "s_add_u32 s90, s90, s98\n\t" \
  "v_lshl_add_u32 v133, v233, 7, v133\n\t" \
  "v_cmp_gt_u32 vcc, 14, v233\n\t" \
  "v_mfma_f32_16x16x32_bf16 v[108:111], a[172:175], a[220:223], v[108:111]\n\t" \
  "buffer_load_dwordx4 a[44:47], v251, s[16:19], s97 offen sc1\n\t" \
  "v_mfma_f32_16x16x32_bf16 v[76:79], a[172:175], a[216:219], v[76:79]\n\t" \
  "s_add_u32 s96, s78, 5767168\n\t" \
  "s_and_b32 s96, s96, 0x780000\n\t" \
  "s_add_u32 m0, s76, 16384\n\t" \
  "v_mfma_f32_16x16x32_bf16 v[44:47], a[172:175], a[212:215], v[44:47]\n\t" \
  "s_lshr_b32 s98, s2, 2\n\t" \
  "ds_read_b64_tr_b16 a[142:143], v195 offset:1024\n\t" \
  "v_bfrev_b32 v247, 1\n\t" \
  "v_mfma_f32_16x16x32_bf16 v[12:15], a[172:175], a[208:211], v[12:15]\n\t" \
  "buffer_load_dwordx4 v200, s[20:23], s96 offen offset:0 lds\n\t" \
  "v_mfma_f32_16x16x32_bf16 v[16:19], a[176:179], a[208:211], v[16:19]\n\t" \
  "s_xor_b32 s98, s98, s3\n\t" \
  "ds_read_b64_tr_b16 a[144:145], v196 offset:0\n\t" \
  "v_cndmask_b32 v245, v247, v133, vcc\n\t" \
  "v_mfma_f32_16x16x32_bf16 v[48:51], a[176:179], a[212:215], v[48:51]\n\t" \
  "s_lshl_b32 s98, s98, 13\n\t" \
  "ds_read_b64_tr_b16 a[146:147], v196 offset:1024\n\t" \
  "v_writelane_b32 v247, s91, 0\n\t" \
  "v_mfma_f32_16x16x32_bf16 v[80:83], a[176:179], a[216:219], v[80:83]\n\t" \
  "s_and_b32 s42, s98, 0x2000\n\t" \
  "ds_read_b64_tr_b16 a[148:149], v197 offset:0\n\t" \
  "v_lshrrev_b32 v133, 1, v129\n\t" \
  "v_mfma_f32_16x16x32_bf16 v[112:115], a[176:179], a[220:223], v[112:115]\n\t" \
  "buffer_load_dwordx4 v201, s[20:23], s96 offen offset:1024 lds\n\t" \
  "v_mfma_f32_16x16x32_bf16 v[116:119], a[180:183], a[220:223], v[116:119]\n\t" \
  "s_lshr_b32 s98, s2, 6\n\t" \
  "ds_read_b64_tr_b16 a[150:151], v197 offset:1024\n\t" \
  "v_and_b32 v134, 1, v129\n\t" \
  "v_mfma_f32_16x16x32_bf16 v[84:87], a[180:183], a[216:219], v[84:87]\n\t" \
  "s_lshl_b32 s98, s98, 22\n\t" \
  "ds_read_b64_tr_b16 a[152:153], v198 offset:0\n\t" \
  "v_lshl_or_b32 v133, v134, 1, v133\n\t" \
  "v_mfma_f32_16x16x32_bf16 v[52:55], a[180:183], a[212:215], v[52:55]\n\t" \
  "s_add_u32 s42, s42, s98\n\t" \
  "ds_read_b64_tr_b16 a[154:155], v198 offset:1024\n\t" \
  "v_xor_b32 v133, v133, v130\n\t" \
  "v_mfma_f32_16x16x32_bf16 v[20:23], a[180:183], a[208:211], v[20:23]\n\t" \
  "buffer_load_dwordx4 v202, s[20:23], s96 offen offset:2048 lds\n\t" \
  "v_mfma_f32_16x16x32_bf16 v[24:27], a[184:187], a[208:211], v[24:27]\n\t" \
  "s_lshl_b32 s47, s3, 15\n\t" \
  "ds_read_b64_tr_b16 a[156:157], v199 offset:0\n\t" \
  "v_lshlrev_b32 v133, 4, v133\n\t" \
  "v_mfma_f32_16x16x32_bf16 v[56:59], a[184:187], a[212:215], v[56:59]\n\t" \
  "s_add_u32 s47, s47, 0x1800000\n\t" \
  "ds_read_b64_tr_b16 a[158:159], v199 offset:1024\n\t" \
  "v_lshl_add_u32 v133, v130, 8, v133\n\t" \
  "v_mfma_f32_16x16x32_bf16 v[88:91], a[184:187], a[216:219], v[88:91]\n\t" \
  "s_add_u32 s47, s47, s42\n\t" \
  "v_add_u32 v220, 49152, v133\n\t" \
  "v_xor_b32 v221, 64, v220\n\t" \
  "v_mfma_f32_16x16x32_bf16 v[120:123], a[184:187], a[220:223], v[120:123]\n\t" \
  "buffer_load_dwordx4 v203, s[20:23], s96 offen offset:3072 lds\n\t" \
  "v_mfma_f32_16x16x32_bf16 v[124:127], a[188:191], a[220:223], v[124:127]\n\t" \
  "s_and_b32 s98, s2, 0x38\n\t" \
  "v_xor_b32 v222, 128, v220\n\t" \
  "v_xor_b32 v223, 192, v220\n\t" \
  "v_mfma_f32_16x16x32_bf16 v[92:95], a[188:191], a[216:219], v[92:95]\n\t" \
  "s_lshl_b32 s98, s98, 16\n\t" \
  "v_xor_b32 v133, v130, v129\n\t" \
  "v_lshlrev_b32 v133, 4, v133\n\t" \
  "v_mfma_f32_16x16x32_bf16 v[60:63], a[188:191], a[212:215], v[60:63]\n\t" \
  "s_waitcnt vmcnt(24) lgkmcnt(0)\n\t" \
  "s_add_u32 s97, s97, s83\n\t" \
  "v_lshl_add_u32 v224, v129, 8, v133\n\t" \
  "v_mfma_f32_16x16x32_bf16 v[28:31], a[188:191], a[208:211], v[28:31]\n\t" \
  "s_barrier\n\t" \
  "v_mfma_f32_16x16x32_bf16 v[0:3], a[128:131], a[224:227], v[0:3]\n\t" \
  "buffer_load_dwordx4 a[48:51], v248, s[16:19], s97 offen sc1\n\t" \
  "v_mfma_f32_16x16x32_bf16 v[0:3], a[64:67], a[240:243], v[0:3]\n\t" \
  "s_add_u32 s47, s47, s98\n\t" \
  "ds_read_b64_tr_b16 a[160:161], v192 offset:16384\n\t" \
  "v_xor_b32 v225, 64, v224\n\t" \
  "v_mfma_f32_16x16x32_bf16 v[32:35], a[64:67], a[244:247], v[32:35]\n\t" \
  "s_add_u32 s46, s42, 0x800000\n\t" \
  "ds_read_b64_tr_b16 a[162:163], v192 offset:17408\n\t" \
  "v_xor_b32 v226, 128, v224\n\t" \
  "v_mfma_f32_16x16x32_bf16 v[32:35], a[128:131], a[228:231], v[32:35]\n\t" \
  "s_and_b32 s98, s2, 0x3\n\t" \
  "v_xor_b32 v227, 192, v224\n\t" \
  "v_lshlrev_b32 v133, 3, v132\n\t" \
  "v_mfma_f32_16x16x32_bf16 v[64:67], a[128:131], a[232:235], v[64:67]\n\t" \
  "buffer_load_dwordx4 a[52:55], v249, s[16:19], s97 offen sc1\n\t" \
  ".Lhs3:\n\ts_cbranch_execz .Lhs3n\n\t" \
  "v_mfma_f32_16x16x32_bf16 v[64:67], a[64:67], a[248:251], v[64:67]\n\t" \
  "s_lshl_b32 s98, s98, 20\n\t" \
  "ds_read_b64_tr_b16 a[164:165], v193 offset:16384\n\t" \
  "v_lshl_add_u32 v133, v129, 10, v133\n\t" \
  "v_mfma_f32_16x16x32_bf16 v[96:99], a[64:67], a[252:255], v[96:99]\n\t" \
  "s_add_u32 s46, s46, s98\n\t" \
  "ds_read_b64_tr_b16 a[166:167], v193 offset:17408\n\t" \
  "v_lshl_add_u32 v228, v131, 6, v133\n\t" \
  "v_mfma_f32_16x16x32_bf16 v[96:99], a[128:131], a[236:239], v[96:99]\n\t" \
  "s_and_b32 s98, s3, 2\n\t" \
  "ds_read_b64_tr_b16 a[168:169], v194 offset:16384\n\t" \
  "v_lshl_add_u32 v228, v134, 5, v228\n\t" \
  "v_mfma_f32_16x16x32_bf16 v[4:7], a[132:135], a[224:227], v[4:7]\n\t" \
  "buffer_load_dwordx4 a[56:59], v250, s[16:19], s97 offen sc1\n\t" \
  "v_mfma_f32_16x16x32_bf16 v[4:7], a[68:71], a[240:243], v[4:7]\n\t" \
  "s_lshl_b32 s98, s98, 13\n\t" \
  "ds_read_b64_tr_b16 a[170:171], v194 offset:17408\n\t" \
  "v_add_u32 v228, 81920, v228\n\t" \
  "v_mfma_f32_16x16x32_bf16 v[36:39], a[68:71], a[244:247], v[36:39]\n\t" \
  "s_add_u32 s46, s46, s98\n\t" \
  "ds_read_b64_tr_b16 a[172:173], v195 offset:16384\n\t" \
  "v_xor_b32 v229, 32, v228\n\t" \
  "v_mfma_f32_16x16x32_bf16 v[36:39], a[132:135], a[228:231], v[36:39]\n\t" \
  "s_and_b32 s98, s3, 1\n\t" \
  "v_lshl_add_u32 v244, v131, 8, v133\n\t" \
  "v_lshl_add_u32 v244, v131, 5, v244\n\t" \
  "v_mfma_f32_16x16x32_bf16 v[68:71], a[132:135], a[232:235], v[68:71]\n\t" \
  "buffer_load_dwordx4 a[60:63], v251, s[16:19], s97 offen sc1\n\t" \
  "v_mfma_f32_16x16x32_bf16 v[68:71], a[68:71], a[248:251], v[68:71]\n\t" \
  "s_add_u32 s97, s78, 5242880\n\t" \
  "s_bfe_u32 s97, s97, 0x140003\n\t" \
  "s_add_u32 s97, s97, s82\n\t" \
  "v_mfma_f32_16x16x32_bf16 v[100:103], a[68:71], a[252:255], v[100:103]\n\t" \
  "s_lshl_b32 s98, s98, 12\n\t" \
  "ds_read_b64_tr_b16 a[174:175], v195 offset:17408\n\t" \
  "v_lshl_add_u32 v244, v134, 7, v244\n\t" \
  "v_mfma_f32_16x16x32_bf16 v[100:103], a[132:135], a[236:239], v[100:103]\n\t" \
  "buffer_load_dwordx4 a[192:195], v248, s[16:19], s97 offen sc1\n\t" \
  "v_mfma_f32_16x16x32_bf16 v[8:11], a[136:139], a[224:227], v[8:11]\n\t" \
  "s_add_u32 s46, s46, s98\n\t" \
  "ds_read_b64_tr_b16 a[176:177], v196 offset:16384\n\t" \
  "v_lshlrev_b32 v133, 4, v132\n\t" \
  "v_mfma_f32_16x16x32_bf16 v[8:11], a[72:75], a[240:243], v[8:11]\n\t" \
  "s_lshr_b32 s45, s87, 2\n\t" \
  "ds_read_b64_tr_b16 a[178:179], v196 offset:17408\n\t" \
  "v_lshl_add_u32 v133, v129, 6, v133\n\t" \
  "v_mfma_f32_16x16x32_bf16 v[40:43], a[72:75], a[244:247], v[40:43]\n\t" \
  "s_add_u32 s45, s45, s46\n\t" \
  "ds_read_b64_tr_b16 a[180:181], v197 offset:16384\n\t" \
  "v_lshl_add_u32 v230, v131, 8, v133\n\t" \
  "v_mfma_f32_16x16x32_bf16 v[40:43], a[136:139], a[228:231], v[40:43]\n\t" \
  "buffer_load_dwordx4 a[196:199], v249, s[16:19], s97 offen sc1\n\t" \
  "v_mfma_f32_16x16x32_bf16 v[72:75], a[136:139], a[232:235], v[72:75]\n\t" \
  "s_and_b32 s98, s2, 0x38\n\t" \
  "ds_read_b64_tr_b16 a[182:183], v197 offset:17408\n\t" \
  "v_xor_b32 v231, 32, v230\n\t" \
  "v_mfma_f32_16x16x32_bf16 v[72:75], a[72:75], a[248:251], v[72:75]\n\t" \
  "s_lshl_b32 s98, s98, 14\n\t" \
  "ds_read_b64_tr_b16 a[184:185], v198 offset:16384\n\t" \
  "v_lshlrev_b32 v133, 1, v129\n\t" \
  "v_mfma_f32_16x16x32_bf16 v[104:107], a[72:75], a[252:255], v[104:107]\n\t" \
  "s_add_u32 s45, s45, s98\n\t" \
  "ds_read_b64_tr_b16 a[186:187], v198 offset:17408\n\t" \
  "v_xor_b32 v133, v133, v130\n\t" \
  "v_mfma_f32_16x16x32_bf16 v[104:107], a[136:139], a[236:239], v[104:107]\n\t" \
  "buffer_load_dwordx4 a[200:203], v250, s[16:19], s97 offen sc1\n\t" \
  "v_mfma_f32_16x16x32_bf16 v[12:15], a[140:143], a[224:227], v[12:15]\n\t" \
  "s_lshr_b32 s98, s2, 1\n\t" \
  "ds_read_b64_tr_b16 a[188:189], v199 offset:16384\n\t" \
  "v_lshlrev_b32 v133, 4, v133\n\t" \
  "v_mfma_f32_16x16x32_bf16 v[12:15], a[76:79], a[240:243], v[12:15]\n\t" \
  "s_xor_b32 s98, s98, s3\n\t" \
  "ds_read_b64_tr_b16 a[190:191], v199 offset:17408\n\t" \
  "v_lshl_add_u32 v240, v129, 11, v133\n\t" \
  "v_mfma_f32_16x16x32_bf16 v[44:47], a[76:79], a[244:247], v[44:47]\n\t" \
  "s_lshl_b32 s98, s98, 13\n\t" \
  "ds_read_b64_tr_b16 a[96:97], v192 offset:24576\n\t" \
  "v_xor_b32 v241, 0x80, v240\n\t" \
  "v_mfma_f32_16x16x32_bf16 v[44:47], a[140:143], a[228:231], v[44:47]\n\t" \
  "buffer_load_dwordx4 a[204:207], v251, s[16:19], s97 offen sc1\n\t" \
  "v_mfma_f32_16x16x32_bf16 v[76:79], a[140:143], a[232:235], v[76:79]\n\t" \
  "s_and_b32 s42, s98, 0x2000\n\t" \
  "ds_read_b64_tr_b16 a[98:99], v192 offset:25600\n\t" \
  "v_lshrrev_b32 v133, 1, v233\n\t" \
  "v_mfma_f32_16x16x32_bf16 v[76:79], a[76:79], a[248:251], v[76:79]\n\t" \
  "s_lshr_b32 s98, s2, 5\n\t" \
  "v_mul_u32_u24 v133, 57344, v133\n\t" \
  "v_and_b32 v134, 1, v233\n\t" \
  "v_mfma_f32_16x16x32_bf16 v[108:111], a[76:79], a[252:255], v[108:111]\n\t" \
  "s_add_u32 s97, s97, s83\n\t" \
  "ds_read_b64_tr_b16 a[100:101], v193 offset:24576\n\t" \
  "v_lshl_add_u32 v133, v134, 4, v133\n\t" \
  "v_mfma_f32_16x16x32_bf16 v[108:111], a[140:143], a[236:239], v[108:111]\n\t" \
  "s_xor_b32 s98, s98, s3\n\t" \
  "ds_read_b64_tr_b16 a[102:103], v193 offset:25600\n\t" \
  "v_or_b32 v232, s57, v133\n\t" \
  "v_mfma_f32_16x16x32_bf16 v[16:19], a[144:147], a[224:227], v[16:19]\n\t" \
  "buffer_load_dwordx4 a[208:211], v248, s[16:19], s97 offen sc1\n\t" \
  "v_mfma_f32_16x16x32_bf16 v[16:19], a[80:83], a[240:243], v[16:19]\n\t" \
  "s_lshl_b32 s98, s98, 13\n\t" \
  "ds_read_b64_tr_b16 a[104:105], v194 offset:24576\n\t" \
  "s_and_b32 s43, s98, 0x2000\n\t" \
  "v_mfma_f32_16x16x32_bf16 v[48:51], a[80:83], a[244:247], v[48:51]\n\t" \
  "s_waitcnt vmcnt(20)\n\t" \
  "v_lshlrev_b32 v128, 16, v136\n\t" \
  "v_and_b32 v136, 0xffff0000, v136\n\t" \
  "v_mfma_f32_16x16x32_bf16 v[48:51], a[144:147], a[228:231], v[48:51]\n\t" \
  "s_lshl_b32 s48, s3, 2\n\t" \
  "ds_read_b64_tr_b16 a[106:107], v194 offset:25600\n\t" \
  "v_mul_f32 v176, v128, v168\n\t" \
  "v_mfma_f32_16x16x32_bf16 v[80:83], a[144:147], a[232:235], v[80:83]\n\t" \
  "buffer_load_dwordx4 a[212:215], v249, s[16:19], s97 offen sc1\n\t" \
  "v_mfma_f32_16x16x32_bf16 v[80:83], a[80:83], a[248:251], v[80:83]\n\t" \
  "s_add_u32 s48, s48, 0x8000\n\t" \
  "ds_read_b64_tr_b16 a[108:109], v195 offset:24576\n\t" \
  "v_mul_f32 v180, v136, v168\n\t" \
  "v_mfma_f32_16x16x32_bf16 v[112:115], a[80:83], a[252:255], v[112:115]\n\t" \
  "s_and_b32 s98, s2, 0x80\n\t" \
  "ds_read_b64_tr_b16 a[110:111], v195 offset:25600\n\t" \
  "v_lshlrev_b32 v128, 16, v137\n\t" \
  "v_mfma_f32_16x16x32_bf16 v[112:115], a[144:147], a[236:239], v[112:115]\n\t" \
  "s_lshl_b32 s98, s98, 7\n\t" \
  "ds_read_b64_tr_b16 a[112:113], v196 offset:24576\n\t" \
  "v_and_b32 v137, 0xffff0000, v137\n\t" \
  "v_mfma_f32_16x16x32_bf16 v[20:23], a[148:151], a[224:227], v[20:23]\n\t" \
  "buffer_load_dwordx4 a[216:219], v250, s[16:19], s97 offen sc1\n\t" \
  "v_mfma_f32_16x16x32_bf16 v[20:23], a[84:87], a[240:243], v[20:23]\n\t" \
  "s_add_u32 s48, s48, s98\n\t" \
  "ds_read_b64_tr_b16 a[114:115], v196 offset:25600\n\t" \
  "v_mul_f32 v177, v128, v168\n\t" \
  "v_mfma_f32_16x16x32_bf16 v[52:55], a[84:87], a[244:247], v[52:55]\n\t" \
  "s_and_b32 s98, s2, 0x4\n\t" \
  "v_mul_f32 v181, v137, v168\n\t" \
  "v_lshlrev_b32 v128, 16, v138\n\t" \
  "v_mfma_f32_16x16x32_bf16 v[52:55], a[148:151], a[228:231], v[52:55]\n\t" \
  "s_lshl_b32 s98, s98, 11\n\t" \
  "ds_read_b64_tr_b16 a[116:117], v197 offset:24576\n\t" \
  "v_and_b32 v138, 0xffff0000, v138\n\t" \
  "v_mfma_f32_16x16x32_bf16 v[84:87], a[148:151], a[232:235], v[84:87]\n\t" \
  "buffer_load_dwordx4 a[220:223], v251, s[16:19], s97 offen sc1\n\t" \
  "v_mfma_f32_16x16x32_bf16 v[84:87], a[84:87], a[248:251], v[84:87]\n\t" \
  "s_add_u32 s96, s78, 6291456\n\t" \
  "s_and_b32 s96, s96, 0x780000\n\t" \
  "s_add_u32 m0, s76, 32768\n\t" \
  "v_mfma_f32_16x16x32_bf16 v[116:119], a[84:87], a[252:255], v[116:119]\n\t" \
  "s_add_u32 s48, s48, s98\n\t" \
  "ds_read_b64_tr_b16 a[118:119], v197 offset:25600\n\t" \
  "v_mul_f32 v178, v128, v168\n\t" \
  "v_mfma_f32_16x16x32_bf16 v[116:119], a[148:151], a[236:239], v[116:119]\n\t" \
  "buffer_load_dwordx4 v200, s[20:23], s96 offen offset:0 lds\n\t" \
  "v_mfma_f32_16x16x32_bf16 v[24:27], a[152:155], a[224:227], v[24:27]\n\t" \
  "s_and_b32 s98, s2, 0x78\n\t" \
  "ds_read_b64_tr_b16 a[120:121], v198 offset:24576\n\t" \
  "v_mul_f32 v182, v138, v168\n\t" \
  "v_mfma_f32_16x16x32_bf16 v[24:27], a[88:91], a[240:243], v[24:27]\n\t" \
  "s_lshl_b32 s98, s98, 6\n\t" \
  "ds_read_b64_tr_b16 a[122:123], v198 offset:25600\n\t" \
  "v_lshlrev_b32 v128, 16, v139\n\t" \
  "v_mfma_f32_16x16x32_bf16 v[56:59], a[88:91], a[244:247], v[56:59]\n\t" \
  "s_add_u32 s48, s48, s98\n\t" \
  "ds_read_b64_tr_b16 a[124:125], v199 offset:24576\n\t" \
  "v_and_b32 v139, 0xffff0000, v139\n\t" \
  "v_mfma_f32_16x16x32_bf16 v[56:59], a[152:155], a[228:231], v[56:59]\n\t" \
  "buffer_load_dwordx4 v201, s[20:23], s96 offen offset:1024 lds\n\t" \
  "v_mfma_f32_16x16x32_bf16 v[88:91], a[152:155], a[232:235], v[88:91]\n\t" \
  "s_lshl_b32 s49, s3, 7\n\t" \
  "ds_read_b64_tr_b16 a[126:127], v199 offset:25600\n\t" \
  "v_mul_f32 v179, v128, v168\n\t" \
  "v_mfma_f32_16x16x32_bf16 v[88:91], a[88:91], a[248:251], v[88:91]\n\t" \
  "s_add_u32 s49, s49, 0x20000\n\t" \
  "s_and_b32 s98, s2, 0xc0\n\t" \
  "v_mul_f32 v183, v139, v168\n\t" \
  "v_mfma_f32_16x16x32_bf16 v[120:123], a[88:91], a[252:255], v[120:123]\n\t" \
  "s_waitcnt vmcnt(24)\n\t" \
  "v_lshlrev_b32 v128, 16, v140\n\t" \
  "v_and_b32 v140, 0xffff0000, v140\n\t" \
  "v_mfma_f32_16x16x32_bf16 v[120:123], a[152:155], a[236:239], v[120:123]\n\t" \
  "buffer_load_dwordx4 v202, s[20:23], s96 offen offset:2048 lds\n\t" \
  "v_mfma_f32_16x16x32_bf16 v[28:31], a[156:159], a[224:227], v[28:31]\n\t" \
  "s_lshl_b32 s98, s98, 9\n\t" \
  "v_fma_f32 v176, v128, v169, v176\n\t" \
  "v_fma_f32 v180, v140, v169, v180\n\t" \
  "v_mfma_f32_16x16x32_bf16 v[28:31], a[92:95], a[240:243], v[28:31]\n\t" \
  "s_add_u32 s49, s49, s98\n\t" \
  "v_lshlrev_b32 v128, 16, v141\n\t" \
  "v_and_b32 v141, 0xffff0000, v141\n\t" \
  "v_mfma_f32_16x16x32_bf16 v[60:63], a[92:95], a[244:247], v[60:63]\n\t" \
  "s_and_b32 s98, s2, 0x2\n\t" \
  "v_fma_f32 v177, v128, v169, v177\n\t" \
  "v_fma_f32 v181, v141, v169, v181\n\t" \
  "v_mfma_f32_16x16x32_bf16 v[60:63], a[156:159], a[228:231], v[60:63]\n\t" \
  "buffer_load_dwordx4 v203, s[20:23], s96 offen offset:3072 lds\n\t" \
  "v_mfma_f32_16x16x32_bf16 v[92:95], a[156:159], a[232:235], v[92:95]\n\t" \
  "s_lshl_b32 s98, s98, 13\n\t" \
  "v_lshlrev_b32 v128, 16, v142\n\t" \
  "v_and_b32 v142, 0xffff0000, v142\n\t" \
  "v_mfma_f32_16x16x32_bf16 v[92:95], a[92:95], a[248:251], v[92:95]\n\t" \
  "s_add_u32 s49, s49, s98\n\t" \
  "v_fma_f32 v178, v128, v169, v178\n\t" \
  "v_fma_f32 v182, v142, v169, v182\n\t" \
  "v_mfma_f32_16x16x32_bf16 v[124:127], a[92:95], a[252:255], v[124:127]\n\t" \
  "s_waitcnt lgkmcnt(0)\n\t" \
  "s_add_u32 s97, s78, 5767168\n\t" \
  "s_bfe_u32 s97, s97, 0x140003\n\t" \
  "v_mfma_f32_16x16x32_bf16 v[124:127], a[156:159], a[236:239], v[124:127]\n\t" \
  "s_barrier\n\t" \
  "v_mfma_f32_16x16x32_bf16 v[0:3], a[160:163], a[0:3], v[0:3]\n\t" \
  "s_add_u32 s97, s97, s82\n\t" \
  "ds_read_b64_tr_b16 a[128:129], v192 offset:32768\n\t" \
  "v_lshlrev_b32 v128, 16, v143\n\t" \
  "v_mfma_f32_16x16x32_bf16 v[0:3], a[96:99], a[16:19], v[0:3]\n\t" \
  "buffer_load_dwordx4 a[224:227], v248, s[16:19], s97 offen sc1\n\t" \
  "v_mfma_f32_16x16x32_bf16 v[32:35], a[96:99], a[20:23], v[32:35]\n\t" \
  "s_and_b32 s98, s2, 0x4\n\t" \
  "ds_read_b64_tr_b16 a[130:131], v192 offset:33792\n\t" \
  "v_and_b32 v143, 0xffff0000, v143\n\t" \
  "v_mfma_f32_16x16x32_bf16 v[32:35], a[160:163], a[4:7], v[32:35]\n\t" \
  "s_lshl_b32 s98, s98, 11\n\t" \
  "v_fma_f32 v179, v128, v169, v179\n\t" \
  "v_fma_f32 v183, v143, v169, v183\n\t" \
  "v_mfma_f32_16x16x32_bf16 v[64:67], a[160:163], a[8:11], v[64:67]\n\t" \
  "buffer_load_dwordx4 a[228:231], v249, s[16:19], s97 offen sc1\n\t" \
  "v_mfma_f32_16x16x32_bf16 v[64:67], a[96:99], a[24:27], v[64:67]\n\t" \
  "s_add_u32 s49, s49, s98\n\t" \
  "ds_read_b64_tr_b16 a[132:133], v193 offset:32768\n\t" \
  "s_and_b32 s98, s2, 0x1\n\t" \
  "v_mfma_f32_16x16x32_bf16 v[96:99], a[96:99], a[28:31], v[96:99]\n\t" \
  "s_lshl_b32 s98, s98, 12\n\t" \
  "ds_read_b64_tr_b16 a[134:135], v193 offset:33792\n\t" \
  "s_add_u32 s49, s49, s98\n\t" \
  "v_mfma_f32_16x16x32_bf16 v[96:99], a[160:163], a[12:15], v[96:99]\n\t" \
  "s_lshl2_add_u32 s49, s39, s49\n\t" \
  "ds_read_b64_tr_b16 a[136:137], v194 offset:32768\n\t" \
  "s_lshl_b32 s50, s3, 15\n\t" \
  "v_mfma_f32_16x16x32_bf16 v[4:7], a[164:167], a[0:3], v[4:7]\n\t" \
  "buffer_load_dwordx4 a[232:235], v250, s[16:19], s97 offen sc1\n\t" \
  "v_mfma_f32_16x16x32_bf16 v[4:7], a[100:103], a[16:19], v[4:7]\n\t" \
  "s_and_b32 s98, s2, 0x4\n\t" \
  "ds_read_b64_tr_b16 a[138:139], v194 offset:33792\n\t" \
  "s_lshl_b32 s98, s98, 19\n\t" \
  "v_mfma_f32_16x16x32_bf16 v[36:39], a[100:103], a[20:23], v[36:39]\n\t" \
  "s_add_u32 s50, s50, s98\n\t" \
  "ds_read_b64_tr_b16 a[140:141], v195 offset:32768\n\t" \
  "s_and_b32 s98, s2, 0xc0\n\t" \
  "v_mfma_f32_16x16x32_bf16 v[36:39], a[164:167], a[4:7], v[36:39]\n\t" \
  "s_waitcnt vmcnt(28)\n\t" \
  "v_lshlrev_b32 v128, 16, v144\n\t" \
  "v_and_b32 v144, 0xffff0000, v144\n\t" \
  "v_mfma_f32_16x16x32_bf16 v[68:71], a[164:167], a[8:11], v[68:71]\n\t" \
  "buffer_load_dwordx4 a[236:239], v251, s[16:19], s97 offen sc1\n\t" \
  "v_mfma_f32_16x16x32_bf16 v[68:71], a[100:103], a[24:27], v[68:71]\n\t" \
  "s_add_u32 s96, s78, 6815744\n\t" \
  "s_and_b32 s96, s96, 0x780000\n\t" \
  "s_add_u32 m0, s76, 65536\n\t" \
  "v_mfma_f32_16x16x32_bf16 v[100:103], a[100:103], a[28:31], v[100:103]\n\t" \
  "s_lshl_b32 s98, s98, 13\n\t" \
  "ds_read_b64_tr_b16 a[142:143], v195 offset:33792\n\t" \
  "v_fma_f32 v176, v128, v170, v176\n\t" \
  "v_mfma_f32_16x16x32_bf16 v[100:103], a[164:167], a[12:15], v[100:103]\n\t" \
  "buffer_load_dwordx4 v200, s[20:23], s96 offen offset:0 lds\n\t" \
  "v_mfma_f32_16x16x32_bf16 v[8:11], a[168:171], a[0:3], v[8:11]\n\t" \
  "s_add_u32 s50, s50, s98\n\t" \
  "ds_read_b64_tr_b16 a[144:145], v196 offset:32768\n\t" \
  "v_fma_f32 v180, v144, v170, v180\n\t" \
  "v_mfma_f32_16x16x32_bf16 v[8:11], a[104:107], a[16:19], v[8:11]\n\t" \
  "s_and_b32 s98, s2, 0x3\n\t" \
  "ds_read_b64_tr_b16 a[146:147], v196 offset:33792\n\t" \
  "v_lshlrev_b32 v128, 16, v145\n\t" \
  "v_mfma_f32_16x16x32_bf16 v[40:43], a[104:107], a[20:23], v[40:43]\n\t" \
  "s_lshl_b32 s98, s98, 17\n\t" \
  "ds_read_b64_tr_b16 a[148:149], v197 offset:32768\n\t" \
  "v_and_b32 v145, 0xffff0000, v145\n\t" \
  "v_mfma_f32_16x16x32_bf16 v[40:43], a[168:171], a[4:7], v[40:43]\n\t" \
  "buffer_load_dwordx4 v201, s[20:23], s96 offen offset:1024 lds\n\t" \
  "v_mfma_f32_16x16x32_bf16 v[72:75], a[168:171], a[8:11], v[72:75]\n\t" \
  "s_add_u32 s50, s50, s98\n\t" \
  "ds_read_b64_tr_b16 a[150:151], v197 offset:33792\n\t" \
  "v_fma_f32 v177, v128, v170, v177\n\t" \
  "v_mfma_f32_16x16x32_bf16 v[72:75], a[104:107], a[24:27], v[72:75]\n\t" \
  "s_and_b32 s98, s2, 0x38\n\t" \
  "ds_read_b64_tr_b16 a[152:153], v198 offset:32768\n\t" \
  "v_fma_f32 v181, v145, v170, v181\n\t" \
  "v_mfma_f32_16x16x32_bf16 v[104:107], a[104:107], a[28:31], v[104:107]\n\t" \
  "s_lshl_b32 s98, s98, 5\n\t" \
  "ds_read_b64_tr_b16 a[154:155], v198 offset:33792\n\t" \
  "v_lshlrev_b32 v128, 16, v146\n\t" \
  "v_mfma_f32_16x16x32_bf16 v[104:107], a[168:171], a[12:15], v[104:107]\n\t" \
  "buffer_load_dwordx4 v202, s[20:23], s96 offen offset:2048 lds\n\t" \
  "v_mfma_f32_16x16x32_bf16 v[12:15], a[172:175], a[0:3], v[12:15]\n\t" \
  "s_add_u32 s50, s50, s98\n\t" \
  "ds_read_b64_tr_b16 a[156:157], v199 offset:32768\n\t" \
  "v_and_b32 v146, 0xffff0000, v146\n\t" \
  "v_mfma_f32_16x16x32_bf16 v[12:15], a[108:111], a[16:19], v[12:15]\n\t" \
  "s_lshr_b32 s98, s2, 2\n\t" \
  "ds_read_b64_tr_b16 a[158:159], v199 offset:33792\n\t" \
  "v_fma_f32 v178, v128, v170, v178\n\t" \
  "v_mfma_f32_16x16x32_bf16 v[44:47], a[108:111], a[20:23], v[44:47]\n\t" \
  "s_xor_b32 s98, s98, s2\n\t" \
  "ds_read_b64_tr_b16 a[64:65], v192 offset:40960\n\t" \
  "v_fma_f32 v182, v146, v170, v182\n\t" \
  "v_mfma_f32_16x16x32_bf16 v[44:47], a[172:175], a[4:7], v[44:47]\n\t" \
  "buffer_load_dwordx4 v203, s[20:23], s96 offen offset:3072 lds\n\t" \
  "v_mfma_f32_16x16x32_bf16 v[76:79], a[172:175], a[8:11], v[76:79]\n\t" \
  "s_and_b32 s98, s98, 4\n\t" \
  "ds_read_b64_tr_b16 a[66:67], v192 offset:41984\n\t" \
  "v_lshlrev_b32 v128, 16, v147\n\t" \
  "v_mfma_f32_16x16x32_bf16 v[76:79], a[108:111], a[24:27], v[76:79]\n\t" \
  "s_lshl_b32 s44, s98, 4\n\t" \
  "v_and_b32 v147, 0xffff0000, v147\n\t" \
  "v_fma_f32 v179, v128, v170, v179\n\t" \
  "v_mfma_f32_16x16x32_bf16 v[108:111], a[108:111], a[28:31], v[108:111]\n\t" \
  "s_add_u32 s97, s97, s83\n\t" \
  "ds_read_b64_tr_b16 a[68:69], v193 offset:40960\n\t" \
  "v_fma_f32 v183, v147, v170, v183\n\t" \
  "v_mfma_f32_16x16x32_bf16 v[108:111], a[172:175], a[12:15], v[108:111]\n\t" \
  "s_and_b32 s98, s2, 0x8\n\t" \
  "ds_read_b64_tr_b16 a[70:71], v193 offset:41984\n\t" \
  "s_lshl_b32 s98, s98, 2\n\t" \
  "v_mfma_f32_16x16x32_bf16 v[16:19], a[176:179], a[0:3], v[16:19]\n\t" \
  "buffer_load_dwordx4 a[240:243], v248, s[16:19], s97 offen sc1\n\t" \
  "v_mfma_f32_16x16x32_bf16 v[16:19], a[112:115], a[16:19], v[16:19]\n\t" \
  "s_add_u32 s44, s44, s98\n\t" \
  "ds_read_b64_tr_b16 a[72:73], v194 offset:40960\n\t" \
  "s_and_b32 s98, s2, 0x3\n\t" \
  "v_mfma_f32_16x16x32_bf16 v[48:51], a[112:115], a[20:23], v[48:51]\n\t" \
  "s_waitcnt vmcnt(33)\n\t" \
  "v_lshlrev_b32 v128, 16, v148\n\t" \
  "v_and_b32 v148, 0xffff0000, v148\n\t" \
  "v_mfma_f32_16x16x32_bf16 v[48:51], a[176:179], a[4:7], v[48:51]\n\t" \
  "s_lshl_b32 s98, s98, 3\n\t" \
  "ds_read_b64_tr_b16 a[74:75], v194 offset:41984\n\t" \
  "v_fma_f32 v176, v128, v171, v176\n\t" \
  "v_mfma_f32_16x16x32_bf16 v[80:83], a[176:179], a[8:11], v[80:83]\n\t" \
  "buffer_load_dwordx4 a[244:247], v249, s[16:19], s97 offen sc1\n\t" \
  "v_mfma_f32_16x16x32_bf16 v[80:83], a[112:115], a[24:27], v[80:83]\n\t" \
  "s_add_u32 s44, s44, s98\n\t" \
  "ds_read_b64_tr_b16 a[76:77], v195 offset:40960\n\t" \
  "v_fma_f32 v180, v148, v171, v180\n\t" \
  "v_mfma_f32_16x16x32_bf16 v[112:115], a[112:115], a[28:31], v[112:115]\n\t" \
  "s_and_b32 s98, s2, 0x4\n\t" \
  "ds_read_b64_tr_b16 a[78:79], v195 offset:41984\n\t" \
  "v_lshlrev_b32 v128, 16, v149\n\t" \
  "v_mfma_f32_16x16x32_bf16 v[112:115], a[176:179], a[12:15], v[112:115]\n\t" \
  "s_lshl_b32 s98, s98, 0\n\t" \
  "ds_read_b64_tr_b16 a[80:81], v196 offset:40960\n\t" \
  "v_and_b32 v149, 0xffff0000, v149\n\t" \
  "v_mfma_f32_16x16x32_bf16 v[20:23], a[180:183], a[0:3], v[20:23]\n\t" \
  "buffer_load_dwordx4 a[248:251], v250, s[16:19], s97 offen sc1\n\t" \
  "v_mfma_f32_16x16x32_bf16 v[20:23], a[116:119], a[16:19], v[20:23]\n\t" \
  "s_add_u32 s44, s44, s98\n\t" \
  "ds_read_b64_tr_b16 a[82:83], v196 offset:41984\n\t" \
  "v_fma_f32 v177, v128, v171, v177\n\t" \
  "v_mfma_f32_16x16x32_bf16 v[52:55], a[116:119], a[20:23], v[52:55]\n\t" \
  "s_add_u32 s44, s44, 0x2000\n\t" \
  "v_fma_f32 v181, v149, v171, v181\n\t" \
  "v_lshlrev_b32 v128, 16, v150\n\t" \
  "v_mfma_f32_16x16x32_bf16 v[52:55], a[180:183], a[4:7], v[52:55]\n\t" \
  "ds_read_b64_tr_b16 a[84:85], v197 offset:40960\n\t" \
  "v_and_b32 v150, 0xffff0000, v150\n\t" \
  "v_mfma_f32_16x16x32_bf16 v[84:87], a[180:183], a[8:11], v[84:87]\n\t" \
  "buffer_load_dwordx4 a[252:255], v251, s[16:19], s97 offen sc1\n\t" \
  "v_mfma_f32_16x16x32_bf16 v[84:87], a[116:119], a[24:27], v[84:87]\n\t" \
  "s_add_u32 s96, s78, 7340032\n\t" \
  "s_and_b32 s96, s96, 0x780000\n\t" \
  "s_add_u32 m0, s76, 81920\n\t" \
  "v_mfma_f32_16x16x32_bf16 v[116:119], a[116:119], a[28:31], v[116:119]\n\t" \
  "ds_read_b64_tr_b16 a[86:87], v197 offset:41984\n\t" \
  "v_fma_f32 v178, v128, v171, v178\n\t" \
  "v_mfma_f32_16x16x32_bf16 v[116:119], a[180:183], a[12:15], v[116:119]\n\t" \
  "buffer_load_dwordx4 v200, s[20:23], s96 offen offset:0 lds\n\t" \
  "v_mfma_f32_16x16x32_bf16 v[24:27], a[184:187], a[0:3], v[24:27]\n\t" \
  "ds_read_b64_tr_b16 a[88:89], v198 offset:40960\n\t" \
  "v_fma_f32 v182, v150, v171, v182\n\t" \
  "v_mfma_f32_16x16x32_bf16 v[24:27], a[120:123], a[16:19], v[24:27]\n\t" \
  "ds_read_b64_tr_b16 a[90:91], v198 offset:41984\n\t" \
  "v_lshlrev_b32 v128, 16, v151\n\t" \
  "v_mfma_f32_16x16x32_bf16 v[56:59], a[120:123], a[20:23], v[56:59]\n\t" \
  "ds_read_b64_tr_b16 a[92:93], v199 offset:40960\n\t" \
  "v_and_b32 v151, 0xffff0000, v151\n\t" \
  "v_mfma_f32_16x16x32_bf16 v[56:59], a[184:187], a[4:7], v[56:59]\n\t" \
  "buffer_load_dwordx4 v201, s[20:23], s96 offen offset:1024 lds\n\t" \
  "v_mfma_f32_16x16x32_bf16 v[88:91], a[184:187], a[8:11], v[88:91]\n\t" \
  "ds_read_b64_tr_b16 a[94:95], v199 offset:41984\n\t" \
  "v_fma_f32 v179, v128, v171, v179\n\t" \
  "v_mfma_f32_16x16x32_bf16 v[88:91], a[120:123], a[24:27], v[88:91]\n\t" \
  "s_cmp_lt_i32 s91, 0\n\t" \
  "s_cselect_b32 s44, s91, s44\n\t" \
  "v_fma_f32 v183, v151, v171, v183\n\t" \
  "v_mfma_f32_16x16x32_bf16 v[120:123], a[120:123], a[28:31], v[120:123]\n\t" \
  "buffer_load_dwordx4 v202, s[20:23], s96 offen offset:2048 lds\n\t" \
  "v_mfma_f32_16x16x32_bf16 v[120:123], a[184:187], a[12:15], v[120:123]\n\t" \
  "v_mfma_f32_16x16x32_bf16 v[28:31], a[188:191], a[0:3], v[28:31]\n\t" \
  "v_mfma_f32_16x16x32_bf16 v[28:31], a[124:127], a[16:19], v[28:31]\n\t" \
  "buffer_load_dwordx4 v203, s[20:23], s96 offen offset:3072 lds\n\t" \
  "v_mfma_f32_16x16x32_bf16 v[60:63], a[124:127], a[20:23], v[60:63]\n\t" \
  "v_mfma_f32_16x16x32_bf16 v[60:63], a[188:191], a[4:7], v[60:63]\n\t" \
  "v_mfma_f32_16x16x32_bf16 v[92:95], a[188:191], a[8:11], v[92:95]\n\t" \
  "v_mfma_f32_16x16x32_bf16 v[92:95], a[124:127], a[24:27], v[92:95]\n\t" \
  "s_add_u32 s97, s78, 6291456\n\t" \
  "s_bfe_u32 s97, s97, 0x140003\n\t" \
  "s_add_u32 s97, s97, s82\n\t" \
  "v_mfma_f32_16x16x32_bf16 v[124:127], a[124:127], a[28:31], v[124:127]\n\t" \
  "s_waitcnt vmcnt(28) lgkmcnt(0)\n\t" \
  "v_mfma_f32_16x16x32_bf16 v[124:127], a[188:191], a[12:15], v[124:127]\n\t" \
  "s_barrier\n\t" \
  "v_mfma_f32_16x16x32_bf16 v[0:3], a[128:131], a[32:35], v[0:3]\n\t" \
  "buffer_load_dwordx4 a[0:3], v248, s[16:19], s97 offen sc1\n\t" \
  "v_mfma_f32_16x16x32_bf16 v[0:3], a[64:67], a[48:51], v[0:3]\n\t" \
  "ds_read_b64_tr_b16 a[160:161], v184 offset:0\n\t" \
  "v_mfma_f32_16x16x32_bf16 v[32:35], a[64:67], a[52:55], v[32:35]\n\t" \
  "ds_read_b64_tr_b16 a[162:163], v184 offset:1024\n\t" \
  "v_mfma_f32_16x16x32_bf16 v[32:35], a[128:131], a[36:39], v[32:35]\n\t" \
  "buffer_load_dwordx4 a[4:7], v249, s[16:19], s97 offen sc1\n\t" \
  "v_mfma_f32_16x16x32_bf16 v[64:67], a[128:131], a[40:43], v[64:67]\n\t" \
  "ds_read_b64_tr_b16 a[164:165], v185 offset:0\n\t" \
  "v_mfma_f32_16x16x32_bf16 v[64:67], a[64:67], a[56:59], v[64:67]\n\t" \
  "ds_read_b64_tr_b16 a[166:167], v185 offset:1024\n\t" \
  "v_mfma_f32_16x16x32_bf16 v[96:99], a[64:67], a[60:63], v[96:99]\n\t" \
  "buffer_load_dwordx4 a[8:11], v250, s[16:19], s97 offen sc1\n\t" \
  "v_mfma_f32_16x16x32_bf16 v[96:99], a[128:131], a[44:47], v[96:99]\n\t" \
  "ds_read_b64_tr_b16 a[168:169], v186 offset:0\n\t" \
  "v_mfma_f32_16x16x32_bf16 v[4:7], a[132:135], a[32:35], v[4:7]\n\t" \
  "ds_read_b64_tr_b16 a[170:171], v186 offset:1024\n\t" \
  "v_mfma_f32_16x16x32_bf16 v[4:7], a[68:71], a[48:51], v[4:7]\n\t" \
  "ds_read_b64_tr_b16 a[172:173], v187 offset:0\n\t" \
  "v_mfma_f32_16x16x32_bf16 v[36:39], a[68:71], a[52:55], v[36:39]\n\t" \
  "buffer_load_dwordx4 a[12:15], v251, s[16:19], s97 offen sc1\n\t" \
  "v_mfma_f32_16x16x32_bf16 v[36:39], a[132:135], a[36:39], v[36:39]\n\t" \
  "ds_read_b64_tr_b16 a[174:175], v187 offset:1024\n\t" \
  "s_add_u32 s97, s97, s83\n\t" \
  "v_mfma_f32_16x16x32_bf16 v[68:71], a[132:135], a[40:43], v[68:71]\n\t" \
  "ds_read_b64_tr_b16 a[176:177], v188 offset:0\n\t" \
  "v_mfma_f32_16x16x32_bf16 v[68:71], a[68:71], a[56:59], v[68:71]\n\t" \
  "ds_read_b64_tr_b16 a[178:179], v188 offset:1024\n\t" \
  "v_mfma_f32_16x16x32_bf16 v[100:103], a[68:71], a[60:63], v[100:103]\n\t" \
  "buffer_load_dwordx4 a[16:19], v248, s[16:19], s97 offen sc1\n\t" \
  "v_mfma_f32_16x16x32_bf16 v[100:103], a[132:135], a[44:47], v[100:103]\n\t" \
  "ds_read_b64_tr_b16 a[180:181], v189 offset:0\n\t" \
  "v_mfma_f32_16x16x32_bf16 v[8:11], a[136:139], a[32:35], v[8:11]\n\t" \
  "ds_read_b64_tr_b16 a[182:183], v189 offset:1024\n\t" \
  "v_mfma_f32_16x16x32_bf16 v[8:11], a[72:75], a[48:51], v[8:11]\n\t" \
  "buffer_load_dwordx4 a[20:23], v249, s[16:19], s97 offen sc1\n\t" \
  "v_mfma_f32_16x16x32_bf16 v[40:43], a[72:75], a[52:55], v[40:43]\n\t" \
  "ds_read_b64_tr_b16 a[184:185], v190 offset:0\n\t" \
  "v_mfma_f32_16x16x32_bf16 v[40:43], a[136:139], a[36:39], v[40:43]\n\t" \
  "ds_read_b64_tr_b16 a[186:187], v190 offset:1024\n\t" \
  "v_mfma_f32_16x16x32_bf16 v[72:75], a[136:139], a[40:43], v[72:75]\n\t" \
  "ds_read_b64_tr_b16 a[188:189], v191 offset:0\n\t" \
  "v_mfma_f32_16x16x32_bf16 v[72:75], a[72:75], a[56:59], v[72:75]\n\t" \
  "buffer_load_dwordx4 a[24:27], v250, s[16:19], s97 offen sc1\n\t" \
  "v_mfma_f32_16x16x32_bf16 v[104:107], a[72:75], a[60:63], v[104:107]\n\t" \
  "ds_read_b64_tr_b16 a[190:191], v191 offset:1024\n\t" \
  "v_mfma_f32_16x16x32_bf16 v[104:107], a[136:139], a[44:47], v[104:107]\n\t" \
  "ds_read_b64_tr_b16 a[96:97], v184 offset:8192\n\t" \
  "v_mfma_f32_16x16x32_bf16 v[12:15], a[140:143], a[32:35], v[12:15]\n\t" \
  "ds_read_b64_tr_b16 a[98:99], v184 offset:9216\n\t" \
  "v_mfma_f32_16x16x32_bf16 v[12:15], a[76:79], a[48:51], v[12:15]\n\t" \
  "buffer_load_dwordx4 a[28:31], v251, s[16:19], s97 offen sc1\n\t" \
  "v_mfma_f32_16x16x32_bf16 v[44:47], a[76:79], a[52:55], v[44:47]\n\t" \
  "ds_read_b64_tr_b16 a[100:101], v185 offset:8192\n\t" \
  "v_mfma_f32_16x16x32_bf16 v[44:47], a[140:143], a[36:39], v[44:47]\n\t" \
  "ds_read_b64_tr_b16 a[102:103], v185 offset:9216\n\t" \
  "v_mfma_f32_16x16x32_bf16 v[76:79], a[140:143], a[40:43], v[76:79]\n\t" \
  "ds_read_b64_tr_b16 a[104:105], v186 offset:8192\n\t" \
  "v_mfma_f32_16x16x32_bf16 v[76:79], a[76:79], a[56:59], v[76:79]\n\t" \
  "ds_read_b64_tr_b16 a[106:107], v186 offset:9216\n\t" \
  "s_mov_b32 s99, 49152\n\t" \
  "v_mfma_f32_16x16x32_bf16 v[108:111], a[76:79], a[60:63], v[108:111]\n\t" \
  "ds_read_b64_tr_b16 a[108:109], v187 offset:8192\n\t" \
  "v_mfma_f32_16x16x32_bf16 v[108:111], a[140:143], a[44:47], v[108:111]\n\t" \
  "buffer_load_dwordx4 v[152:155], v209, s[8:11], s99 offen nt\n\t" \
  "v_mfma_f32_16x16x32_bf16 v[16:19], a[144:147], a[32:35], v[16:19]\n\t" \
  "ds_read_b64_tr_b16 a[110:111], v187 offset:9216\n\t" \
  "s_mov_b32 s99, 61440\n\t" \
  "v_mfma_f32_16x16x32_bf16 v[16:19], a[80:83], a[48:51], v[16:19]\n\t" \
  "ds_read_b64_tr_b16 a[112:113], v188 offset:8192\n\t" \
  "v_mfma_f32_16x16x32_bf16 v[48:51], a[80:83], a[52:55], v[48:51]\n\t" \
  "ds_read_b64_tr_b16 a[114:115], v188 offset:9216\n\t" \
  "v_mfma_f32_16x16x32_bf16 v[48:51], a[144:147], a[36:39], v[48:51]\n\t" \
  "ds_read_b64_tr_b16 a[116:117], v189 offset:8192\n\t" \
  "v_mfma_f32_16x16x32_bf16 v[80:83], a[144:147], a[40:43], v[80:83]\n\t" \
  "ds_read_b64_tr_b16 a[118:119], v189 offset:9216\n\t" \
  "v_mfma_f32_16x16x32_bf16 v[80:83], a[80:83], a[56:59], v[80:83]\n\t" \
  "buffer_load_dwordx4 v[156:159], v209, s[8:11], s99 offen nt\n\t" \
  "v_mfma_f32_16x16x32_bf16 v[112:115], a[80:83], a[60:63], v[112:115]\n\t" \
  "ds_read_b64_tr_b16 a[120:121], v190 offset:8192\n\t" \
  "s_mov_b32 s99, 73728\n\t" \
  "v_mfma_f32_16x16x32_bf16 v[112:115], a[144:147], a[44:47], v[112:115]\n\t" \
  "ds_read_b64_tr_b16 a[122:123], v190 offset:9216\n\t" \
  "v_mfma_f32_16x16x32_bf16 v[20:23], a[148:151], a[32:35], v[20:23]\n\t" \
  "ds_read_b64_tr_b16 a[124:125], v191 offset:8192\n\t" \
  "v_mfma_f32_16x16x32_bf16 v[20:23], a[84:87], a[48:51], v[20:23]\n\t" \
  "ds_read_b64_tr_b16 a[126:127], v191 offset:9216\n\t" \
  "v_mfma_f32_16x16x32_bf16 v[52:55], a[84:87], a[52:55], v[52:55]\n\t" \
  "v_mfma_f32_16x16x32_bf16 v[52:55], a[148:151], a[36:39], v[52:55]\n\t" \
  "buffer_load_dwordx4 v[160:163], v209, s[8:11], s99 offen nt\n\t" \
  "v_mfma_f32_16x16x32_bf16 v[84:87], a[148:151], a[40:43], v[84:87]\n\t" \
  "s_mov_b32 s99, 86016\n\t" \
  "v_mfma_f32_16x16x32_bf16 v[84:87], a[84:87], a[56:59], v[84:87]\n\t" \
  "v_mfma_f32_16x16x32_bf16 v[116:119], a[84:87], a[60:63], v[116:119]\n\t" \
  "v_mfma_f32_16x16x32_bf16 v[116:119], a[148:151], a[44:47], v[116:119]\n\t" \
  "v_mfma_f32_16x16x32_bf16 v[24:27], a[152:155], a[32:35], v[24:27]\n\t" \
  "v_mfma_f32_16x16x32_bf16 v[24:27], a[88:91], a[48:51], v[24:27]\n\t" \
  "buffer_load_dwordx4 v[164:167], v209, s[8:11], s99 offen nt\n\t" \
  "v_mfma_f32_16x16x32_bf16 v[56:59], a[88:91], a[52:55], v[56:59]\n\t" \
  "s_add_u32 s99, s53, 0\n\t" \
  "v_mfma_f32_16x16x32_bf16 v[56:59], a[152:155], a[36:39], v[56:59]\n\t" \
  "v_mfma_f32_16x16x32_bf16 v[88:91], a[152:155], a[40:43], v[88:91]\n\t" \
  "v_mfma_f32_16x16x32_bf16 v[88:91], a[88:91], a[56:59], v[88:91]\n\t" \
  "v_mfma_f32_16x16x32_bf16 v[120:123], a[88:91], a[60:63], v[120:123]\n\t" \
  "v_mfma_f32_16x16x32_bf16 v[120:123], a[152:155], a[44:47], v[120:123]\n\t" \
  "buffer_load_dword v246, v245, s[68:71], s99 offen\n\t" \
  "v_mfma_f32_16x16x32_bf16 v[28:31], a[156:159], a[32:35], v[28:31]\n\t" \
  "v_mfma_f32_16x16x32_bf16 v[28:31], a[92:95], a[48:51], v[28:31]\n\t" \
  "v_mfma_f32_16x16x32_bf16 v[60:63], a[92:95], a[52:55], v[60:63]\n\t" \
  "v_mfma_f32_16x16x32_bf16 v[60:63], a[156:159], a[36:39], v[60:63]\n\t" \
  "v_mfma_f32_16x16x32_bf16 v[92:95], a[156:159], a[40:43], v[92:95]\n\t" \
  "v_mfma_f32_16x16x32_bf16 v[92:95], a[92:95], a[56:59], v[92:95]\n\t" \
  "v_mfma_f32_16x16x32_bf16 v[124:127], a[92:95], a[60:63], v[124:127]\n\t" \
  "s_waitcnt vmcnt(33) lgkmcnt(0)\n\t" \
  "v_mfma_f32_16x16x32_bf16 v[124:127], a[156:159], a[44:47], v[124:127]\n\t" \
  "s_barrier\n\t" \
  "v_mfma_f32_16x16x32_bf16 v[0:3], a[160:163], a[192:195], v[0:3]\n\t" \
  "ds_read_b64_tr_b16 a[128:129], v184 offset:16384\n\t" \
  "v_mfma_f32_16x16x32_bf16 v[0:3], a[96:99], a[208:211], v[0:3]\n\t" \
  "ds_read_b64_tr_b16 a[130:131], v184 offset:17408\n\t" \
  "v_mfma_f32_16x16x32_bf16 v[32:35], a[96:99], a[212:215], v[32:35]\n\t" \
  "ds_read_b64_tr_b16 a[132:133], v185 offset:16384\n\t" \
  "v_mfma_f32_16x16x32_bf16 v[32:35], a[160:163], a[196:199], v[32:35]\n\t" \
  "ds_read_b64_tr_b16 a[134:135], v185 offset:17408\n\t" \
  "v_mfma_f32_16x16x32_bf16 v[64:67], a[160:163], a[200:203], v[64:67]\n\t" \
  "ds_read_b64_tr_b16 a[136:137], v186 offset:16384\n\t" \
  "v_mfma_f32_16x16x32_bf16 v[64:67], a[96:99], a[216:219], v[64:67]\n\t" \
  "ds_read_b64_tr_b16 a[138:139], v186 offset:17408\n\t" \
  "v_mfma_f32_16x16x32_bf16 v[96:99], a[96:99], a[220:223], v[96:99]\n\t" \
  "ds_read_b64_tr_b16 a[140:141], v187 offset:16384\n\t" \
  "v_mfma_f32_16x16x32_bf16 v[96:99], a[160:163], a[204:207], v[96:99]\n\t" \
  "ds_read_b64_tr_b16 a[142:143], v187 offset:17408\n\t" \
  "v_mfma_f32_16x16x32_bf16 v[4:7], a[164:167], a[192:195], v[4:7]\n\t" \
  "ds_read_b64_tr_b16 a[144:145], v188 offset:16384\n\t" \
  "v_mfma_f32_16x16x32_bf16 v[4:7], a[100:103], a[208:211], v[4:7]\n\t" \
  "ds_read_b64_tr_b16 a[146:147], v188 offset:17408\n\t" \
  "v_mfma_f32_16x16x32_bf16 v[36:39], a[100:103], a[212:215], v[36:39]\n\t" \
  "ds_read_b64_tr_b16 a[148:149], v189 offset:16384\n\t" \
  "v_mfma_f32_16x16x32_bf16 v[36:39], a[164:167], a[196:199], v[36:39]\n\t" \
  "ds_read_b64_tr_b16 a[150:151], v189 offset:17408\n\t" \
  "v_mfma_f32_16x16x32_bf16 v[68:71], a[164:167], a[200:203], v[68:71]\n\t" \
  "ds_read_b64_tr_b16 a[152:153], v190 offset:16384\n\t" \
  "v_mfma_f32_16x16x32_bf16 v[68:71], a[100:103], a[216:219], v[68:71]\n\t" \
  "ds_read_b64_tr_b16 a[154:155], v190 offset:17408\n\t" \
  "v_mfma_f32_16x16x32_bf16 v[100:103], a[100:103], a[220:223], v[100:103]\n\t" \
  "ds_read_b64_tr_b16 a[156:157], v191 offset:16384\n\t" \
  "v_mfma_f32_16x16x32_bf16 v[100:103], a[164:167], a[204:207], v[100:103]\n\t" \
  "ds_read_b64_tr_b16 a[158:159], v191 offset:17408\n\t" \
  "v_mfma_f32_16x16x32_bf16 v[8:11], a[168:171], a[192:195], v[8:11]\n\t" \
  "ds_read_b64_tr_b16 a[64:65], v184 offset:24576\n\t" \
  "v_mfma_f32_16x16x32_bf16 v[8:11], a[104:107], a[208:211], v[8:11]\n\t" \
  "v_mfma_f32_16x16x32_bf16 v[40:43], a[104:107], a[212:215], v[40:43]\n\t" \
  "ds_read_b64_tr_b16 a[66:67], v184 offset:25600\n\t" \
  "v_mfma_f32_16x16x32_bf16 v[40:43], a[168:171], a[196:199], v[40:43]\n\t" \
  "v_mfma_f32_16x16x32_bf16 v[72:75], a[168:171], a[200:203], v[72:75]\n\t" \
  "ds_read_b64_tr_b16 a[68:69], v185 offset:24576\n\t" \
  "v_mfma_f32_16x16x32_bf16 v[72:75], a[104:107], a[216:219], v[72:75]\n\t" \
  "ds_read_b64_tr_b16 a[70:71], v185 offset:25600\n\t" \
  "v_mfma_f32_16x16x32_bf16 v[104:107], a[104:107], a[220:223], v[104:107]\n\t" \
  "v_mfma_f32_16x16x32_bf16 v[104:107], a[168:171], a[204:207], v[104:107]\n\t" \
  "ds_read_b64_tr_b16 a[72:73], v186 offset:24576\n\t" \
  "v_mfma_f32_16x16x32_bf16 v[12:15], a[172:175], a[192:195], v[12:15]\n\t" \
  "v_mfma_f32_16x16x32_bf16 v[12:15], a[108:111], a[208:211], v[12:15]\n\t" \
  "ds_read_b64_tr_b16 a[74:75], v186 offset:25600\n\t" \
  "v_mfma_f32_16x16x32_bf16 v[44:47], a[108:111], a[212:215], v[44:47]\n\t" \
  "ds_read_b64_tr_b16 a[76:77], v187 offset:24576\n\t" \
  "v_mfma_f32_16x16x32_bf16 v[44:47], a[172:175], a[196:199], v[44:47]\n\t" \
  ".Lhs4:\n\ts_cbranch_execz .Lhs4n\n\t" \
  "v_mfma_f32_16x16x32_bf16 v[76:79], a[172:175], a[200:203], v[76:79]\n\t" \
  "ds_read_b64_tr_b16 a[78:79], v187 offset:25600\n\t" \
  "v_mfma_f32_16x16x32_bf16 v[76:79], a[108:111], a[216:219], v[76:79]\n\t" \
  "s_add_u32 s97, s78, 6815744\n\t" \
  "ds_read_b64_tr_b16 a[80:81], v188 offset:24576\n\t" \
  "s_bfe_u32 s97, s97, 0x140003\n\t" \
  "v_mfma_f32_16x16x32_bf16 v[108:111], a[108:111], a[220:223], v[108:111]\n\t" \
  "s_add_u32 s97, s97, s82\n\t" \
  "v_mfma_f32_16x16x32_bf16 v[108:111], a[172:175], a[204:207], v[108:111]\n\t" \
  "buffer_load_dwordx4 a[32:35], v248, s[16:19], s97 offen sc1\n\t" \
  "v_mfma_f32_16x16x32_bf16 v[16:19], a[176:179], a[192:195], v[16:19]\n\t" \
  "ds_read_b64_tr_b16 a[82:83], v188 offset:25600\n\t" \
  "v_mfma_f32_16x16x32_bf16 v[16:19], a[112:115], a[208:211], v[16:19]\n\t" \
  "ds_read_b64_tr_b16 a[84:85], v189 offset:24576\n\t" \
  "v_mfma_f32_16x16x32_bf16 v[48:51], a[112:115], a[212:215], v[48:51]\n\t" \
  "buffer_load_dwordx4 a[36:39], v249, s[16:19], s97 offen sc1\n\t" \
  "v_mfma_f32_16x16x32_bf16 v[48:51], a[176:179], a[196:199], v[48:51]\n\t" \
  "ds_read_b64_tr_b16 a[86:87], v189 offset:25600\n\t" \
  "v_mfma_f32_16x16x32_bf16 v[80:83], a[176:179], a[200:203], v[80:83]\n\t" \
  "ds_read_b64_tr_b16 a[88:89], v190 offset:24576\n\t" \
  "v_mfma_f32_16x16x32_bf16 v[80:83], a[112:115], a[216:219], v[80:83]\n\t" \
  "v_mfma_f32_16x16x32_bf16 v[112:115], a[112:115], a[220:223], v[112:115]\n\t" \
  "buffer_load_dwordx4 a[40:43], v250, s[16:19], s97 offen sc1\n\t" \
  "v_mfma_f32_16x16x32_bf16 v[112:115], a[176:179], a[204:207], v[112:115]\n\t" \
  "ds_read_b64_tr_b16 a[90:91], v190 offset:25600\n\t" \
  "v_mfma_f32_16x16x32_bf16 v[20:23], a[180:183], a[192:195], v[20:23]\n\t" \
  "ds_read_b64_tr_b16 a[92:93], v191 offset:24576\n\t" \
  "v_mfma_f32_16x16x32_bf16 v[20:23], a[116:119], a[208:211], v[20:23]\n\t" \
  "ds_read_b64_tr_b16 a[94:95], v191 offset:25600\n\t" \
  "v_mfma_f32_16x16x32_bf16 v[52:55], a[116:119], a[212:215], v[52:55]\n\t" \
  "buffer_load_dwordx4 a[44:47], v251, s[16:19], s97 offen sc1\n\t" \
  "v_mfma_f32_16x16x32_bf16 v[52:55], a[180:183], a[196:199], v[52:55]\n\t" \
  "s_add_u32 s96, s78, 7864320\n\t" \
  "s_and_b32 s96, s96, 0x780000\n\t" \
  "s_add_u32 m0, s76, 98304\n\t" \
  "v_mfma_f32_16x16x32_bf16 v[84:87], a[180:183], a[200:203], v[84:87]\n\t" \
  "v_mfma_f32_16x16x32_bf16 v[84:87], a[116:119], a[216:219], v[84:87]\n\t" \
  "v_mfma_f32_16x16x32_bf16 v[116:119], a[116:119], a[220:223], v[116:119]\n\t" \
  "buffer_load_dwordx4 v200, s[20:23], s96 offen offset:0 lds\n\t" \
  "v_mfma_f32_16x16x32_bf16 v[116:119], a[180:183], a[204:207], v[116:119]\n\t" \
  "v_mfma_f32_16x16x32_bf16 v[24:27], a[184:187], a[192:195], v[24:27]\n\t" \
  "v_mfma_f32_16x16x32_bf16 v[24:27], a[120:123], a[208:211], v[24:27]\n\t" \
  "buffer_load_dwordx4 v201, s[20:23], s96 offen offset:1024 lds\n\t" \
  "v_mfma_f32_16x16x32_bf16 v[56:59], a[120:123], a[212:215], v[56:59]\n\t" \
  "v_mfma_f32_16x16x32_bf16 v[56:59], a[184:187], a[196:199], v[56:59]\n\t" \
  "v_mfma_f32_16x16x32_bf16 v[88:91], a[184:187], a[200:203], v[88:91]\n\t" \
  "v_mfma_f32_16x16x32_bf16 v[88:91], a[120:123], a[216:219], v[88:91]\n\t" \
  "buffer_load_dwordx4 v202, s[20:23], s96 offen offset:2048 lds\n\t" \
  "v_mfma_f32_16x16x32_bf16 v[120:123], a[120:123], a[220:223], v[120:123]\n\t" \
  "v_mfma_f32_16x16x32_bf16 v[120:123], a[184:187], a[204:207], v[120:123]\n\t" \
  "v_mfma_f32_16x16x32_bf16 v[28:31], a[188:191], a[192:195], v[28:31]\n\t" \
  "v_mfma_f32_16x16x32_bf16 v[28:31], a[124:127], a[208:211], v[28:31]\n\t" \
  "buffer_load_dwordx4 v203, s[20:23], s96 offen offset:3072 lds\n\t" \
  "v_mfma_f32_16x16x32_bf16 v[60:63], a[124:127], a[212:215], v[60:63]\n\t" \
  "v_mfma_f32_16x16x32_bf16 v[60:63], a[188:191], a[196:199], v[60:63]\n\t" \
  "v_mfma_f32_16x16x32_bf16 v[92:95], a[188:191], a[200:203], v[92:95]\n\t" \
  "v_mfma_f32_16x16x32_bf16 v[92:95], a[124:127], a[216:219], v[92:95]\n\t" \
  "s_add_u32 s97, s97, s83\n\t" \
  "v_mfma_f32_16x16x32_bf16 v[124:127], a[124:127], a[220:223], v[124:127]\n\t" \
  "s_waitcnt vmcnt(25) lgkmcnt(0)\n\t" \
  "v_mfma_f32_16x16x32_bf16 v[124:127], a[188:191], a[204:207], v[124:127]\n\t" \
  "s_barrier\n\t" \
  "v_mfma_f32_16x16x32_bf16 v[0:3], a[128:131], a[224:227], v[0:3]\n\t" \
  "buffer_load_dwordx4 a[48:51], v248, s[16:19], s97 offen sc1\n\t" \
  "v_mfma_f32_16x16x32_bf16 v[0:3], a[64:67], a[240:243], v[0:3]\n\t" \
  "ds_read_b64_tr_b16 a[160:161], v184 offset:32768\n\t" \
  "v_mfma_f32_16x16x32_bf16 v[32:35], a[64:67], a[244:247], v[32:35]\n\t" \
  "ds_read_b64_tr_b16 a[162:163], v184 offset:33792\n\t" \
  "v_mfma_f32_16x16x32_bf16 v[32:35], a[128:131], a[228:231], v[32:35]\n\t" \
  "buffer_load_dwordx4 a[52:55], v249, s[16:19], s97 offen sc1\n\t" \
  "v_mfma_f32_16x16x32_bf16 v[64:67], a[128:131], a[232:235], v[64:67]\n\t" \
  "s_waitcnt vmcnt(14)\n\t" \
  "v_lshlrev_b32 v128, 16, v152\n\t" \
  "v_and_b32 v152, 0xffff0000, v152\n\t" \
  "v_mfma_f32_16x16x32_bf16 v[64:67], a[64:67], a[248:251], v[64:67]\n\t" \
  "ds_read_b64_tr_b16 a[164:165], v185 offset:32768\n\t" \
  "v_fma_f32 v176, v128, v172, v176\n\t" \
  "v_mfma_f32_16x16x32_bf16 v[96:99], a[64:67], a[252:255], v[96:99]\n\t" \
  "ds_read_b64_tr_b16 a[166:167], v185 offset:33792\n\t" \
  "v_fma_f32 v180, v152, v172, v180\n\t" \
  "v_mfma_f32_16x16x32_bf16 v[96:99], a[128:131], a[236:239], v[96:99]\n\t" \
  "ds_read_b64_tr_b16 a[168:169], v186 offset:32768\n\t" \
  "v_lshlrev_b32 v128, 16, v153\n\t" \
  "v_mfma_f32_16x16x32_bf16 v[4:7], a[132:135], a[224:227], v[4:7]\n\t" \
  "buffer_load_dwordx4 a[56:59], v250, s[16:19], s97 offen sc1\n\t" \
  "v_mfma_f32_16x16x32_bf16 v[4:7], a[68:71], a[240:243], v[4:7]\n\t" \
  "ds_read_b64_tr_b16 a[170:171], v186 offset:33792\n\t" \
  "v_and_b32 v153, 0xffff0000, v153\n\t" \
  "v_mfma_f32_16x16x32_bf16 v[36:39], a[68:71], a[244:247], v[36:39]\n\t" \
  "ds_read_b64_tr_b16 a[172:173], v187 offset:32768\n\t" \
  "v_fma_f32 v177, v128, v172, v177\n\t" \
  "v_mfma_f32_16x16x32_bf16 v[36:39], a[132:135], a[228:231], v[36:39]\n\t" \
  "v_fma_f32 v181, v153, v172, v181\n\t" \
  "v_lshlrev_b32 v128, 16, v154\n\t" \
  "v_mfma_f32_16x16x32_bf16 v[68:71], a[132:135], a[232:235], v[68:71]\n\t" \
  "buffer_load_dwordx4 a[60:63], v251, s[16:19], s97 offen sc1\n\t" \
  "v_mfma_f32_16x16x32_bf16 v[68:71], a[68:71], a[248:251], v[68:71]\n\t" \
  "s_add_u32 s97, s78, 7340032\n\t" \
  "s_bfe_u32 s97, s97, 0x140003\n\t" \
  "s_add_u32 s97, s97, s82\n\t" \
  "v_mfma_f32_16x16x32_bf16 v[100:103], a[68:71], a[252:255], v[100:103]\n\t" \
  "ds_read_b64_tr_b16 a[174:175], v187 offset:33792\n\t" \
  "v_and_b32 v154, 0xffff0000, v154\n\t" \
  "v_mfma_f32_16x16x32_bf16 v[100:103], a[132:135], a[236:239], v[100:103]\n\t" \
  "buffer_load_dwordx4 a[192:195], v248, s[16:19], s97 offen sc1\n\t" \
  "v_mfma_f32_16x16x32_bf16 v[8:11], a[136:139], a[224:227], v[8:11]\n\t" \
  "ds_read_b64_tr_b16 a[176:177], v188 offset:32768\n\t" \
  "v_fma_f32 v178, v128, v172, v178\n\t" \
  "v_mfma_f32_16x16x32_bf16 v[8:11], a[72:75], a[240:243], v[8:11]\n\t" \
  "ds_read_b64_tr_b16 a[178:179], v188 offset:33792\n\t" \
  "v_fma_f32 v182, v154, v172, v182\n\t" \
  "v_mfma_f32_16x16x32_bf16 v[40:43], a[72:75], a[244:247], v[40:43]\n\t" \
  "ds_read_b64_tr_b16 a[180:181], v189 offset:32768\n\t" \
  "v_lshlrev_b32 v128, 16, v155\n\t" \
  "v_mfma_f32_16x16x32_bf16 v[40:43], a[136:139], a[228:231], v[40:43]\n\t" \
  "buffer_load_dwordx4 a[196:199], v249, s[16:19], s97 offen sc1\n\t" \
  "v_mfma_f32_16x16x32_bf16 v[72:75], a[136:139], a[232:235], v[72:75]\n\t" \
  "ds_read_b64_tr_b16 a[182:183], v189 offset:33792\n\t" \
  "v_and_b32 v155, 0xffff0000, v155\n\t" \
  "v_mfma_f32_16x16x32_bf16 v[72:75], a[72:75], a[248:251], v[72:75]\n\t" \
  "ds_read_b64_tr_b16 a[184:185], v190 offset:32768\n\t" \
  "v_fma_f32 v179, v128, v172, v179\n\t" \
  "v_mfma_f32_16x16x32_bf16 v[104:107], a[72:75], a[252:255], v[104:107]\n\t" \
  "ds_read_b64_tr_b16 a[186:187], v190 offset:33792\n\t" \
  "v_fma_f32 v183, v155, v172, v183\n\t" \
  "v_mfma_f32_16x16x32_bf16 v[104:107], a[136:139], a[236:239], v[104:107]\n\t" \
  "buffer_load_dwordx4 a[200:203], v250, s[16:19], s97 offen sc1\n\t" \
  "v_mfma_f32_16x16x32_bf16 v[12:15], a[140:143], a[224:227], v[12:15]\n\t" \
  "s_waitcnt vmcnt(18)\n\t" \
  "v_lshlrev_b32 v128, 16, v156\n\t" \
  "v_and_b32 v156, 0xffff0000, v156\n\t" \
  "v_mfma_f32_16x16x32_bf16 v[12:15], a[76:79], a[240:243], v[12:15]\n\t" \
  "ds_read_b64_tr_b16 a[188:189], v191 offset:32768\n\t" \
  "v_fma_f32 v176, v128, v173, v176\n\t" \
  "v_mfma_f32_16x16x32_bf16 v[44:47], a[76:79], a[244:247], v[44:47]\n\t" \
  "ds_read_b64_tr_b16 a[190:191], v191 offset:33792\n\t" \
  "v_fma_f32 v180, v156, v173, v180\n\t" \
  "v_mfma_f32_16x16x32_bf16 v[44:47], a[140:143], a[228:231], v[44:47]\n\t" \
  "buffer_load_dwordx4 a[204:207], v251, s[16:19], s97 offen sc1\n\t" \
  "v_mfma_f32_16x16x32_bf16 v[76:79], a[140:143], a[232:235], v[76:79]\n\t" \
  "ds_read_b64_tr_b16 a[96:97], v184 offset:40960\n\t" \
  "v_lshlrev_b32 v128, 16, v157\n\t" \
  "v_mfma_f32_16x16x32_bf16 v[76:79], a[76:79], a[248:251], v[76:79]\n\t" \
  "s_add_u32 s97, s97, s83\n\t" \
  "ds_read_b64_tr_b16 a[98:99], v184 offset:41984\n\t" \
  "v_and_b32 v157, 0xffff0000, v157\n\t" \
  "v_mfma_f32_16x16x32_bf16 v[108:111], a[76:79], a[252:255], v[108:111]\n\t" \
  "ds_read_b64_tr_b16 a[100:101], v185 offset:40960\n\t" \
  "v_fma_f32 v177, v128, v173, v177\n\t" \
  "v_mfma_f32_16x16x32_bf16 v[108:111], a[140:143], a[236:239], v[108:111]\n\t" \
  "ds_read_b64_tr_b16 a[102:103], v185 offset:41984\n\t" \
  "v_fma_f32 v181, v157, v173, v181\n\t" \
  "v_mfma_f32_16x16x32_bf16 v[16:19], a[144:147], a[224:227], v[16:19]\n\t" \
  "buffer_load_dwordx4 a[208:211], v248, s[16:19], s97 offen sc1\n\t" \
  "v_mfma_f32_16x16x32_bf16 v[16:19], a[80:83], a[240:243], v[16:19]\n\t" \
  "ds_read_b64_tr_b16 a[104:105], v186 offset:40960\n\t" \
  "v_lshlrev_b32 v128, 16, v158\n\t" \
  "v_mfma_f32_16x16x32_bf16 v[48:51], a[80:83], a[244:247], v[48:51]\n\t" \
  "v_and_b32 v158, 0xffff0000, v158\n\t" \
  "v_fma_f32 v178, v128, v173, v178\n\t" \
  "v_mfma_f32_16x16x32_bf16 v[48:51], a[144:147], a[228:231], v[48:51]\n\t" \
  "ds_read_b64_tr_b16 a[106:107], v186 offset:41984\n\t" \
  "v_fma_f32 v182, v158, v173, v182\n\t" \
  "v_mfma_f32_16x16x32_bf16 v[80:83], a[144:147], a[232:235], v[80:83]\n\t" \
  "ds_read_b64_tr_b16 a[108:109], v187 offset:40960\n\t" \
  "v_lshlrev_b32 v128, 16, v159\n\t" \
  "v_mfma_f32_16x16x32_bf16 v[80:83], a[80:83], a[248:251], v[80:83]\n\t" \
  "v_and_b32 v159, 0xffff0000, v159\n\t" \
  "v_fma_f32 v179, v128, v173, v179\n\t" \
  "v_mfma_f32_16x16x32_bf16 v[112:115], a[80:83], a[252:255], v[112:115]\n\t" \
  "ds_read_b64_tr_b16 a[110:111], v187 offset:41984\n\t" \
  "v_fma_f32 v183, v159, v173, v183\n\t" \
  "v_mfma_f32_16x16x32_bf16 v[112:115], a[144:147], a[236:239], v[112:115]\n\t" \
  "s_waitcnt vmcnt(19)\n\t" \
  "v_lshlrev_b32 v128, 16, v160\n\t" \
  "v_and_b32 v160, 0xffff0000, v160\n\t" \
  "v_mfma_f32_16x16x32_bf16 v[20:23], a[148:151], a[224:227], v[20:23]\n\t" \
  "buffer_load_dwordx4 a[212:215], v249, s[16:19], s97 offen sc1\n\t" \
  "v_mfma_f32_16x16x32_bf16 v[20:23], a[84:87], a[240:243], v[20:23]\n\t" \
  "ds_read_b64_tr_b16 a[112:113], v188 offset:40960\n\t" \
  "v_fma_f32 v176, v128, v174, v176\n\t" \
  "v_mfma_f32_16x16x32_bf16 v[52:55], a[84:87], a[244:247], v[52:55]\n\t" \
  "ds_read_b64_tr_b16 a[114:115], v188 offset:41984\n\t" \
  "v_fma_f32 v180, v160, v174, v180\n\t" \
  "v_mfma_f32_16x16x32_bf16 v[52:55], a[148:151], a[228:231], v[52:55]\n\t" \
  "ds_read_b64_tr_b16 a[116:117], v189 offset:40960\n\t" \
  "v_lshlrev_b32 v128, 16, v161\n\t" \
  "v_mfma_f32_16x16x32_bf16 v[84:87], a[148:151], a[232:235], v[84:87]\n\t" \
  "ds_read_b64_tr_b16 a[118:119], v189 offset:41984\n\t" \
  "v_and_b32 v161, 0xffff0000, v161\n\t" \
  "v_mfma_f32_16x16x32_bf16 v[84:87], a[84:87], a[248:251], v[84:87]\n\t" \
  "v_fma_f32 v177, v128, v174, v177\n\t" \
  "v_fma_f32 v181, v161, v174, v181\n\t" \
  "v_mfma_f32_16x16x32_bf16 v[116:119], a[84:87], a[252:255], v[116:119]\n\t" \
  "ds_read_b64_tr_b16 a[120:121], v190 offset:40960\n\t" \
  "v_lshlrev_b32 v128, 16, v162\n\t" \
  "v_mfma_f32_16x16x32_bf16 v[116:119], a[148:151], a[236:239], v[116:119]\n\t" \
  "buffer_load_dwordx4 a[216:219], v250, s[16:19], s97 offen sc1\n\t" \
  "v_mfma_f32_16x16x32_bf16 v[24:27], a[152:155], a[224:227], v[24:27]\n\t" \
  "ds_read_b64_tr_b16 a[122:123], v190 offset:41984\n\t" \
  "v_and_b32 v162, 0xffff0000, v162\n\t" \
  "v_mfma_f32_16x16x32_bf16 v[24:27], a[88:91], a[240:243], v[24:27]\n\t" \
  "ds_read_b64_tr_b16 a[124:125], v191 offset:40960\n\t" \
  "v_fma_f32 v178, v128, v174, v178\n\t" \
  "v_mfma_f32_16x16x32_bf16 v[56:59], a[88:91], a[244:247], v[56:59]\n\t" \
  "v_fma_f32 v182, v162, v174, v182\n\t" \
  "v_lshlrev_b32 v128, 16, v163\n\t" \
  "v_mfma_f32_16x16x32_bf16 v[56:59], a[152:155], a[228:231], v[56:59]\n\t" \
  "ds_read_b64_tr_b16 a[126:127], v191 offset:41984\n\t" \
  "v_and_b32 v163, 0xffff0000, v163\n\t" \
  "v_mfma_f32_16x16x32_bf16 v[88:91], a[152:155], a[232:235], v[88:91]\n\t" \
  "v_fma_f32 v179, v128, v174, v179\n\t" \
  "v_fma_f32 v183, v163, v174, v183\n\t" \
  "v_mfma_f32_16x16x32_bf16 v[88:91], a[88:91], a[248:251], v[88:91]\n\t" \
  "s_waitcnt vmcnt(20)\n\t" \
  "v_lshlrev_b32 v128, 16, v164\n\t" \
  "v_and_b32 v164, 0xffff0000, v164\n\t" \
  "v_mfma_f32_16x16x32_bf16 v[120:123], a[88:91], a[252:255], v[120:123]\n\t" \
  "v_fma_f32 v176, v128, v175, v176\n\t" \
  "v_fma_f32 v180, v164, v175, v180\n\t" \
  "v_mfma_f32_16x16x32_bf16 v[120:123], a[152:155], a[236:239], v[120:123]\n\t" \
  "buffer_load_dwordx4 a[220:223], v251, s[16:19], s97 offen sc1\n\t" \
  "v_mfma_f32_16x16x32_bf16 v[28:31], a[156:159], a[224:227], v[28:31]\n\t" \
  "v_lshlrev_b32 v128, 16, v165\n\t" \
  "v_and_b32 v165, 0xffff0000, v165\n\t" \
  "v_mfma_f32_16x16x32_bf16 v[28:31], a[92:95], a[240:243], v[28:31]\n\t" \
  "v_fma_f32 v177, v128, v175, v177\n\t" \
  "v_fma_f32 v181, v165, v175, v181\n\t" \
  "v_mfma_f32_16x16x32_bf16 v[60:63], a[92:95], a[244:247], v[60:63]\n\t" \
  "v_lshlrev_b32 v128, 16, v166\n\t" \
  "v_and_b32 v166, 0xffff0000, v166\n\t" \
  "v_mfma_f32_16x16x32_bf16 v[60:63], a[156:159], a[228:231], v[60:63]\n\t" \
  "v_fma_f32 v178, v128, v175, v178\n\t" \
  "v_fma_f32 v182, v166, v175, v182\n\t" \
  "v_mfma_f32_16x16x32_bf16 v[92:95], a[156:159], a[232:235], v[92:95]\n\t" \
  "v_lshlrev_b32 v128, 16, v167\n\t" \
  "v_and_b32 v167, 0xffff0000, v167\n\t" \
  "v_mfma_f32_16x16x32_bf16 v[92:95], a[92:95], a[248:251], v[92:95]\n\t" \
  "s_add_u32 s97, s78, 7864320\n\t" \
  "v_fma_f32 v179, v128, v175, v179\n\t" \
  "v_fma_f32 v183, v167, v175, v183\n\t" \
  "v_mfma_f32_16x16x32_bf16 v[124:127], a[92:95], a[252:255], v[124:127]\n\t" \
  "s_waitcnt lgkmcnt(0)\n\t" \
  "s_bfe_u32 s97, s97, 0x140003\n\t" \
  "s_add_u32 s97, s97, s82\n\t" \
  "v_mfma_f32_16x16x32_bf16 v[124:127], a[156:159], a[236:239], v[124:127]\n\t" \
  "s_barrier\n\t" \
  "v_mfma_f32_16x16x32_bf16 v[0:3], a[160:163], a[0:3], v[0:3]\n\t" \
  "buffer_load_dwordx4 a[224:227], v248, s[16:19], s97 offen sc1\n\t" \
  "v_mfma_f32_16x16x32_bf16 v[0:3], a[96:99], a[16:19], v[0:3]\n\t" \
  "ds_read_b64_tr_b16 a[128:129], v192 offset:0\n\t" \
  "v_and_b32 v138, 7, v233\n\t" \
  "v_mfma_f32_16x16x32_bf16 v[32:35], a[96:99], a[20:23], v[32:35]\n\t" \
  "ds_read_b64_tr_b16 a[130:131], v192 offset:1024\n\t" \
  "v_lshlrev_b32 v138, 5, v138\n\t" \
  "v_mfma_f32_16x16x32_bf16 v[32:35], a[160:163], a[4:7], v[32:35]\n\t" \
  "v_permlane32_swap_b32 v176, v178\n\t" \
  "v_mfma_f32_16x16x32_bf16 v[64:67], a[160:163], a[8:11], v[64:67]\n\t" \
  "buffer_load_dwordx4 a[228:231], v249, s[16:19], s97 offen sc1\n\t" \
  "v_mfma_f32_16x16x32_bf16 v[64:67], a[96:99], a[24:27], v[64:67]\n\t" \
  "v_permlane32_swap_b32 v180, v182\n\t" \
  "v_mfma_f32_16x16x32_bf16 v[96:99], a[96:99], a[28:31], v[96:99]\n\t" \
  "v_permlane32_swap_b32 v177, v179\n\t" \
  "v_mfma_f32_16x16x32_bf16 v[96:99], a[160:163], a[12:15], v[96:99]\n\t" \
  "v_permlane32_swap_b32 v181, v183\n\t" \
  "v_mfma_f32_16x16x32_bf16 v[4:7], a[164:167], a[0:3], v[4:7]\n\t" \
  "buffer_load_dwordx4 a[232:235], v250, s[16:19], s97 offen sc1\n\t" \
  "v_mfma_f32_16x16x32_bf16 v[4:7], a[100:103], a[16:19], v[4:7]\n\t" \
  "ds_read_b64_tr_b16 a[132:133], v193 offset:0\n\t" \
  "v_add_f32 v176, v176, v178\n\t" \
  "v_mfma_f32_16x16x32_bf16 v[36:39], a[100:103], a[20:23], v[36:39]\n\t" \
  "ds_read_b64_tr_b16 a[134:135], v193 offset:1024\n\t" \
  "v_add_f32 v177, v177, v179\n\t" \
  "v_mfma_f32_16x16x32_bf16 v[36:39], a[164:167], a[4:7], v[36:39]\n\t" \
  "ds_read_b64_tr_b16 a[136:137], v194 offset:0\n\t" \
  "v_add_f32 v180, v180, v182\n\t" \
  "v_mfma_f32_16x16x32_bf16 v[68:71], a[164:167], a[8:11], v[68:71]\n\t" \
  "buffer_load_dwordx4 a[236:239], v251, s[16:19], s97 offen sc1\n\t" \
  "v_mfma_f32_16x16x32_bf16 v[68:71], a[100:103], a[24:27], v[68:71]\n\t" \
  "s_add_u32 s97, s97, s83\n\t" \
  "ds_read_b64_tr_b16 a[138:139], v194 offset:1024\n\t" \
  "v_add_f32 v181, v181, v183\n\t" \
  "v_mfma_f32_16x16x32_bf16 v[100:103], a[100:103], a[28:31], v[100:103]\n\t" \
  "ds_read_b64_tr_b16 a[140:141], v195 offset:0\n\t" \
  "v_lshrrev_b32 v139, 4, v233\n\t" \
  "v_mfma_f32_16x16x32_bf16 v[100:103], a[164:167], a[12:15], v[100:103]\n\t" \
  "buffer_load_dwordx4 a[240:243], v248, s[16:19], s97 offen sc1\n\t" \
  "v_mfma_f32_16x16x32_bf16 v[8:11], a[168:171], a[0:3], v[8:11]\n\t" \
  "ds_read_b64_tr_b16 a[142:143], v195 offset:1024\n\t" \
  "v_mfma_f32_16x16x32_bf16 v[8:11], a[104:107], a[16:19], v[8:11]\n\t" \
  "v_permlane16_swap_b32 v176, v177\n\t" \
  "v_mfma_f32_16x16x32_bf16 v[40:43], a[104:107], a[20:23], v[40:43]\n\t" \
  "ds_read_b64_tr_b16 a[144:145], v196 offset:0\n\t" \
  "v_mfma_f32_16x16x32_bf16 v[40:43], a[168:171], a[4:7], v[40:43]\n\t" \
  "buffer_load_dwordx4 a[244:247], v249, s[16:19], s97 offen sc1\n\t" \
  "v_mfma_f32_16x16x32_bf16 v[72:75], a[168:171], a[8:11], v[72:75]\n\t" \
  "ds_read_b64_tr_b16 a[146:147], v196 offset:1024\n\t" \
  "v_mfma_f32_16x16x32_bf16 v[72:75], a[104:107], a[24:27], v[72:75]\n\t" \
  "ds_read_b64_tr_b16 a[148:149], v197 offset:0\n\t" \
  "v_mfma_f32_16x16x32_bf16 v[104:107], a[104:107], a[28:31], v[104:107]\n\t" \
  "v_permlane16_swap_b32 v180, v181\n\t" \
  "v_mfma_f32_16x16x32_bf16 v[104:107], a[168:171], a[12:15], v[104:107]\n\t" \
  "buffer_load_dwordx4 a[248:251], v250, s[16:19], s97 offen sc1\n\t" \
  "v_mfma_f32_16x16x32_bf16 v[12:15], a[172:175], a[0:3], v[12:15]\n\t" \
  "ds_read_b64_tr_b16 a[150:151], v197 offset:1024\n\t" \
  "v_add_f32 v176, v176, v177\n\t" \
  "v_mfma_f32_16x16x32_bf16 v[12:15], a[108:111], a[16:19], v[12:15]\n\t" \
  "ds_read_b64_tr_b16 a[152:153], v198 offset:0\n\t" \
  "v_add_f32 v180, v180, v181\n\t" \
  "v_mfma_f32_16x16x32_bf16 v[44:47], a[108:111], a[20:23], v[44:47]\n\t" \
  "ds_read_b64_tr_b16 a[154:155], v198 offset:1024\n\t" \
  "v_lshl_add_u32 v138, v139, 3, v138\n\t" \
  "v_mfma_f32_16x16x32_bf16 v[44:47], a[172:175], a[4:7], v[44:47]\n\t" \
  "buffer_load_dwordx4 a[252:255], v251, s[16:19], s97 offen sc1\n\t" \
  "v_mfma_f32_16x16x32_bf16 v[76:79], a[172:175], a[8:11], v[76:79]\n\t" \
  "ds_read_b64_tr_b16 a[156:157], v199 offset:0\n\t" \
  "v_and_b32 v139, 0x7fffff00, v219\n\t" \
  "v_mfma_f32_16x16x32_bf16 v[76:79], a[108:111], a[24:27], v[76:79]\n\t" \
  "s_add_u32 m0, s76, 16384\n\t" \
  "ds_read_b64_tr_b16 a[158:159], v199 offset:1024\n\t" \
  "v_add_f32_dpp v136, v176, v176 row_ror:8 row_mask:0xf bank_mask:0xf\n\t" \
  "v_mfma_f32_16x16x32_bf16 v[108:111], a[108:111], a[28:31], v[108:111]\n\t" \
  "ds_read_b64_tr_b16 a[64:65], v192 offset:8192\n\t" \
  "v_add_f32_dpp v137, v180, v180 row_ror:8 row_mask:0xf bank_mask:0xf\n\t" \
  "v_mfma_f32_16x16x32_bf16 v[108:111], a[172:175], a[12:15], v[108:111]\n\t" \
  "ds_read_b64_tr_b16 a[66:67], v192 offset:9216\n\t" \
  "v_add_u32 v138, v139, v138\n\t" \
  "v_mfma_f32_16x16x32_bf16 v[16:19], a[176:179], a[0:3], v[16:19]\n\t" \
  "buffer_load_dwordx4 v224, s[60:63], s38 offen offset:0 lds\n\t" \
  "v_mfma_f32_16x16x32_bf16 v[16:19], a[112:115], a[16:19], v[16:19]\n\t" \
  "ds_write_b64 v138, v[136:137] offset:1024\n\t" \
  "v_mfma_f32_16x16x32_bf16 v[48:51], a[112:115], a[20:23], v[48:51]\n\t" \
  "ds_read_b64_tr_b16 a[68:69], v193 offset:8192\n\t" \
  "v_and_b32 v140, 3, v233\n\t" \
  "v_mfma_f32_16x16x32_bf16 v[48:51], a[176:179], a[4:7], v[48:51]\n\t" \
  "ds_read_b64_tr_b16 a[70:71], v193 offset:9216\n\t" \
  "v_bfe_u32 v141, v233, 4, 1\n\t" \
  "v_mfma_f32_16x16x32_bf16 v[80:83], a[176:179], a[8:11], v[80:83]\n\t" \
  "ds_read_b64_tr_b16 a[72:73], v194 offset:8192\n\t" \
  "v_lshl_add_u32 v140, v141, 2, v140\n\t" \
  "v_mfma_f32_16x16x32_bf16 v[80:83], a[112:115], a[24:27], v[80:83]\n\t" \
  "buffer_load_dwordx4 v225, s[60:63], s38 offen offset:1024 lds\n\t" \
  "v_mfma_f32_16x16x32_bf16 v[112:115], a[112:115], a[28:31], v[112:115]\n\t" \
  "ds_read_b64_tr_b16 a[74:75], v194 offset:9216\n\t" \
  "v_bfe_u32 v141, v233, 2, 2\n\t" \
  "v_mfma_f32_16x16x32_bf16 v[112:115], a[176:179], a[12:15], v[112:115]\n\t" \
  "ds_read_b64_tr_b16 a[76:77], v195 offset:8192\n\t" \
  "v_lshlrev_b32 v141, 8, v141\n\t" \
  "v_mfma_f32_16x16x32_bf16 v[20:23], a[180:183], a[0:3], v[20:23]\n\t" \
  "ds_read_b64_tr_b16 a[78:79], v195 offset:9216\n\t" \
  "v_lshl_add_u32 v141, v140, 3, v141\n\t" \
  "v_mfma_f32_16x16x32_bf16 v[20:23], a[116:119], a[16:19], v[20:23]\n\t" \
  "ds_read_b64_tr_b16 a[80:81], v196 offset:8192\n\t" \
  "v_lshl_add_u32 v141, s3, 6, v141\n\t" \
  "v_mfma_f32_16x16x32_bf16 v[52:55], a[116:119], a[20:23], v[52:55]\n\t" \
  "buffer_load_dwordx4 v226, s[60:63], s38 offen offset:2048 lds\n\t" \
  "v_mfma_f32_16x16x32_bf16 v[52:55], a[180:183], a[4:7], v[52:55]\n\t" \
  "ds_read_b64_tr_b16 a[82:83], v196 offset:9216\n\t" \
  "v_lshlrev_b32 v140, 4, v140\n\t" \
  "v_mfma_f32_16x16x32_bf16 v[84:87], a[180:183], a[8:11], v[84:87]\n\t" \
  "ds_read_b64_tr_b16 a[84:85], v197 offset:8192\n\t" \
  "v_and_b32 v142, 0x2c, v233\n\t" \
  "v_mfma_f32_16x16x32_bf16 v[84:87], a[116:119], a[24:27], v[84:87]\n\t" \
  "ds_read_b64_tr_b16 a[86:87], v197 offset:9216\n\t" \
  "v_lshl_or_b32 v140, v142, 26, v140\n\t" \
  "v_mfma_f32_16x16x32_bf16 v[116:119], a[116:119], a[28:31], v[116:119]\n\t" \
  "ds_read_b64_tr_b16 a[88:89], v198 offset:8192\n\t" \
  "v_add_u32 v141, 151552, v141\n\t" \
  "v_mfma_f32_16x16x32_bf16 v[116:119], a[180:183], a[12:15], v[116:119]\n\t" \
  "buffer_load_dwordx4 v227, s[60:63], s38 offen offset:3072 lds\n\t" \
  "v_mfma_f32_16x16x32_bf16 v[24:27], a[184:187], a[0:3], v[24:27]\n\t" \
  "s_add_u32 m0, s76, 49152\n\t" \
  "ds_read_b64_tr_b16 a[90:91], v198 offset:9216\n\t" \
  "v_mov_b32 v145, v211\n\t" \
  "v_mfma_f32_16x16x32_bf16 v[24:27], a[120:123], a[16:19], v[24:27]\n\t" \
  "ds_read_b64_tr_b16 a[92:93], v199 offset:8192\n\t" \
  "v_mov_b32 v147, v211\n\t" \
  "v_mfma_f32_16x16x32_bf16 v[56:59], a[120:123], a[20:23], v[56:59]\n\t" \
  "ds_read_b64_tr_b16 a[94:95], v199 offset:9216\n\t" \
  "v_mfma_f32_16x16x32_bf16 v[56:59], a[184:187], a[4:7], v[56:59]\n\t" \
  "buffer_load_dwordx4 v230, s[64:67], s38 offen offset:0 lds\n\t" \
  "v_mfma_f32_16x16x32_bf16 v[88:91], a[184:187], a[8:11], v[88:91]\n\t" \
  "v_mfma_f32_16x16x32_bf16 v[88:91], a[120:123], a[24:27], v[88:91]\n\t" \
  "v_mfma_f32_16x16x32_bf16 v[120:123], a[120:123], a[28:31], v[120:123]\n\t" \
  "v_mfma_f32_16x16x32_bf16 v[120:123], a[184:187], a[12:15], v[120:123]\n\t" \
  "v_mfma_f32_16x16x32_bf16 v[28:31], a[188:191], a[0:3], v[28:31]\n\t" \
  "buffer_load_dwordx4 v231, s[64:67], s38 offen offset:1024 lds\n\t" \
  "v_mfma_f32_16x16x32_bf16 v[28:31], a[124:127], a[16:19], v[28:31]\n\t" \
  "v_mfma_f32_16x16x32_bf16 v[60:63], a[124:127], a[20:23], v[60:63]\n\t" \
  "v_mfma_f32_16x16x32_bf16 v[60:63], a[188:191], a[4:7], v[60:63]\n\t" \
  "v_mfma_f32_16x16x32_bf16 v[92:95], a[188:191], a[8:11], v[92:95]\n\t" \
  "v_mfma_f32_16x16x32_bf16 v[92:95], a[124:127], a[24:27], v[92:95]\n\t" \
  "v_mfma_f32_16x16x32_bf16 v[124:127], a[124:127], a[28:31], v[124:127]\n\t" \
  "s_waitcnt vmcnt(22) lgkmcnt(0)\n\t" \
  "v_mfma_f32_16x16x32_bf16 v[124:127], a[188:191], a[12:15], v[124:127]\n\t" \
  "s_barrier\n\t" \
  "v_mfma_f32_16x16x32_bf16 v[0:3], a[128:131], a[32:35], v[0:3]\n\t" \
  "buffer_load_dwordx4 v230, s[64:67], s38 offen offset:2048 lds\n\t" \
  "v_mfma_f32_16x16x32_bf16 v[0:3], a[64:67], a[48:51], v[0:3]\n\t" \
  "ds_read_b64_tr_b16 a[160:161], v192 offset:16384\n\t" \
  "v_mfma_f32_16x16x32_bf16 v[32:35], a[64:67], a[52:55], v[32:35]\n\t" \
  "ds_read_b64_tr_b16 a[162:163], v192 offset:17408\n\t" \
  "v_mfma_f32_16x16x32_bf16 v[32:35], a[128:131], a[36:39], v[32:35]\n\t" \
  "ds_read_b64 v[142:143], v141 offset:1024\n\t" \
  "v_mfma_f32_16x16x32_bf16 v[64:67], a[128:131], a[40:43], v[64:67]\n\t" \
  "ds_read_b64_tr_b16 a[164:165], v193 offset:16384\n\t" \
  "v_mfma_f32_16x16x32_bf16 v[64:67], a[64:67], a[56:59], v[64:67]\n\t" \
  "buffer_load_dwordx4 v231, s[64:67], s38 offen offset:3072 lds\n\t" \
  "v_mfma_f32_16x16x32_bf16 v[96:99], a[64:67], a[60:63], v[96:99]\n\t" \
  "s_add_u32 m0, s76, 32768\n\t" \
  "ds_read_b64_tr_b16 a[166:167], v193 offset:17408\n\t" \
  "s_add_u32 s96, s38, 16384\n\t" \
  "v_mfma_f32_16x16x32_bf16 v[96:99], a[128:131], a[44:47], v[96:99]\n\t" \
  "ds_read_b64_tr_b16 a[168:169], v194 offset:16384\n\t" \
  "v_mfma_f32_16x16x32_bf16 v[4:7], a[132:135], a[32:35], v[4:7]\n\t" \
  "buffer_load_dwordx4 v224, s[60:63], s96 offen offset:0 lds\n\t" \
  "v_mfma_f32_16x16x32_bf16 v[4:7], a[68:71], a[48:51], v[4:7]\n\t" \
  "s_waitcnt lgkmcnt(3)\n\t" \
  "v_add_f32_dpp v142, v142, v142 row_ror:4 row_mask:0xf bank_mask:0xf\n\t" \
  "v_add_f32_dpp v143, v143, v143 row_ror:4 row_mask:0xf bank_mask:0xf\n\t" \
  "v_mfma_f32_16x16x32_bf16 v[36:39], a[68:71], a[52:55], v[36:39]\n\t" \
  "ds_read_b64_tr_b16 a[170:171], v194 offset:17408\n\t" \
  "v_add_f32_dpp v144, v142, v142 row_ror:8 row_mask:0xf bank_mask:0xf\n\t" \
  "v_mfma_f32_16x16x32_bf16 v[36:39], a[132:135], a[36:39], v[36:39]\n\t" \
  "ds_read_b64_tr_b16 a[172:173], v195 offset:16384\n\t" \
  "v_add_f32_dpp v146, v143, v143 row_ror:8 row_mask:0xf bank_mask:0xf\n\t" \
  "v_mfma_f32_16x16x32_bf16 v[68:71], a[132:135], a[40:43], v[68:71]\n\t" \
  "buffer_store_dwordx4 v[144:147], v140, s[16:19], s90 offen sc1\n\t" \
  "ds_read_b64_tr_b16 a[174:175], v195 offset:17408\n\t" \
  "ds_read_b64_tr_b16 a[176:177], v196 offset:16384\n\t" \
  "ds_read_b64_tr_b16 a[178:179], v196 offset:17408\n\t" \
  "v_mfma_f32_16x16x32_bf16 v[68:71], a[68:71], a[56:59], v[68:71]\n\t" \
  "buffer_load_dwordx4 v225, s[60:63], s96 offen offset:1024 lds\n\t" \
  "v_mfma_f32_16x16x32_bf16 v[100:103], a[68:71], a[60:63], v[100:103]\n\t" \
  "ds_read_b64_tr_b16 a[180:181], v197 offset:16384\n\t" \
  "v_mfma_f32_16x16x32_bf16 v[100:103], a[132:135], a[44:47], v[100:103]\n\t" \
  "ds_read_b64_tr_b16 a[182:183], v197 offset:17408\n\t" \
  "v_mfma_f32_16x16x32_bf16 v[8:11], a[136:139], a[32:35], v[8:11]\n\t" \
  "buffer_load_dwordx4 v226, s[60:63], s96 offen offset:2048 lds\n\t" \
  "v_mfma_f32_16x16x32_bf16 v[8:11], a[72:75], a[48:51], v[8:11]\n\t" \
  "ds_read_b64_tr_b16 a[184:185], v198 offset:16384\n\t" \
  "v_mfma_f32_16x16x32_bf16 v[40:43], a[72:75], a[52:55], v[40:43]\n\t" \
  "ds_read_b64_tr_b16 a[186:187], v198 offset:17408\n\t" \
  "v_mfma_f32_16x16x32_bf16 v[40:43], a[136:139], a[36:39], v[40:43]\n\t" \
  "ds_read_b64_tr_b16 a[188:189], v199 offset:16384\n\t" \
  "v_mfma_f32_16x16x32_bf16 v[72:75], a[136:139], a[40:43], v[72:75]\n\t" \
  "buffer_load_dwordx4 v227, s[60:63], s96 offen offset:3072 lds\n\t" \
  "v_mfma_f32_16x16x32_bf16 v[72:75], a[72:75], a[56:59], v[72:75]\n\t" \
  "ds_read_b64_tr_b16 a[190:191], v199 offset:17408\n\t" \
  "s_add_u32 s99, s53, 14336\n\t" \
  "v_mfma_f32_16x16x32_bf16 v[104:107], a[72:75], a[60:63], v[104:107]\n\t" \
  "ds_read_b64_tr_b16 a[96:97], v192 offset:24576\n\t" \
  "v_mfma_f32_16x16x32_bf16 v[104:107], a[136:139], a[44:47], v[104:107]\n\t" \
  "ds_read_b64_tr_b16 a[98:99], v192 offset:25600\n\t" \
  "v_mfma_f32_16x16x32_bf16 v[12:15], a[140:143], a[32:35], v[12:15]\n\t" \
  "buffer_load_dword v246, v245, s[68:71], s99 offen\n\t" \
  "v_mfma_f32_16x16x32_bf16 v[12:15], a[76:79], a[48:51], v[12:15]\n\t" \
  "ds_read_b64_tr_b16 a[100:101], v193 offset:24576\n\t" \
  "v_mfma_f32_16x16x32_bf16 v[44:47], a[76:79], a[52:55], v[44:47]\n\t" \
  "ds_read_b64_tr_b16 a[102:103], v193 offset:25600\n\t" \
  "v_mfma_f32_16x16x32_bf16 v[44:47], a[140:143], a[36:39], v[44:47]\n\t" \
  "ds_read_b64_tr_b16 a[104:105], v194 offset:24576\n\t" \
  "v_mfma_f32_16x16x32_bf16 v[76:79], a[140:143], a[40:43], v[76:79]\n\t" \
  "ds_read_b64_tr_b16 a[106:107], v194 offset:25600\n\t" \
  "v_mfma_f32_16x16x32_bf16 v[76:79], a[76:79], a[56:59], v[76:79]\n\t" \
  "ds_read_b64_tr_b16 a[108:109], v195 offset:24576\n\t" \
  "v_mfma_f32_16x16x32_bf16 v[108:111], a[76:79], a[60:63], v[108:111]\n\t" \
  "ds_read_b64_tr_b16 a[110:111], v195 offset:25600\n\t" \
  "v_mfma_f32_16x16x32_bf16 v[108:111], a[140:143], a[44:47], v[108:111]\n\t" \
  "buffer_load_dword v246, v217, s[16:19], s58 offen sc1\n\t" \
  "v_mfma_f32_16x16x32_bf16 v[16:19], a[144:147], a[32:35], v[16:19]\n\t" \
  "s_mov_b32 s98, 0x3f803f80\n\t" \
  "v_mov_b32 v212, v217\n\t" \
  "v_add_u32 v210, 65536, v228\n\t" \
  "v_add_u32 v211, 65536, v229\n\t" \
  "v_mfma_f32_16x16x32_bf16 v[16:19], a[80:83], a[48:51], v[16:19]\n\t" \
  "v_mov_b32 v243, s36\n\t" \
  "v_and_b32 v208, 1, v233\n\t" \
  "v_mul_u32_u24 v208, 0xaaaa, v208\n\t" \
  "v_mfma_f32_16x16x32_bf16 v[48:51], a[80:83], a[52:55], v[48:51]\n\t" \
  "v_or_b32 v208, 0x4444, v208\n\t" \
  "v_lshl_or_b32 v208, v208, 16, v208\n\t" \
  "v_mfma_f32_16x16x32_bf16 v[48:51], a[144:147], a[36:39], v[48:51]\n\t" \
  "s_add_u32 s99, s53, 28672\n\t" \
  "v_mov_b32 v216, s98\n\t" \
  "v_mov_b32 v217, s98\n\t" \
  "v_mfma_f32_16x16x32_bf16 v[80:83], a[144:147], a[40:43], v[80:83]\n\t" \
  "v_mov_b32 v218, s98\n\t" \
  "v_mov_b32 v219, s98\n\t" \
  "v_mfma_f32_16x16x32_bf16 v[80:83], a[80:83], a[56:59], v[80:83]\n\t" \
  "v_mov_b32 v160, 0\n\t" \
  "v_mov_b32 v161, 0\n\t" \
  "v_mfma_f32_16x16x32_bf16 v[112:115], a[80:83], a[60:63], v[112:115]\n\t" \
  "v_mov_b32 v164, 0\n\t" \
  "v_mov_b32 v165, 0\n\t" \
  "v_mfma_f32_16x16x32_bf16 v[112:115], a[144:147], a[44:47], v[112:115]\n\t" \
  "ds_read_b64_tr_b16 a[112:113], v196 offset:24576\n\t" \
  "v_mov_b32 v168, 0\n\t" \
  "v_mov_b32 v169, 0\n\t" \
  "v_mfma_f32_16x16x32_bf16 v[20:23], a[148:151], a[32:35], v[20:23]\n\t" \
  "v_mov_b32 v172, 0\n\t" \
  "v_mov_b32 v173, 0\n\t" \
  "v_mfma_f32_16x16x32_bf16 v[20:23], a[84:87], a[48:51], v[20:23]\n\t" \
  "v_add_u32 v248, 14336, v240\n\t" \
  "v_add_u32 v249, 21504, v241\n\t" \
  "v_mfma_f32_16x16x32_bf16 v[52:55], a[84:87], a[52:55], v[52:55]\n\t" \
  "v_add_u32 v241, 7168, v241\n\t" \
  "v_lshlrev_b32_e64 v234, 6, s3\n\t" \
  "v_mfma_f32_16x16x32_bf16 v[52:55], a[148:151], a[36:39], v[52:55]\n\t" \
  "v_xor_b32 v234, v234, v244\n\t" \
  "v_xor_b32 v235, 32, v234\n\t" \
  "v_mfma_f32_16x16x32_bf16 v[84:87], a[148:151], a[40:43], v[84:87]\n\t" \
  "v_add_u32 v236, 65536, v234\n\t" \
  "v_add_u32 v237, 65536, v235\n\t" \
  "v_mfma_f32_16x16x32_bf16 v[84:87], a[84:87], a[56:59], v[84:87]\n\t" \
  "v_add_u32 v238, 131072, v234\n\t" \
  "v_add_u32 v239, 131072, v235\n\t" \
  "v_mfma_f32_16x16x32_bf16 v[116:119], a[84:87], a[60:63], v[116:119]\n\t" \
  "ds_read_b64_tr_b16 a[114:115], v196 offset:25600\n\t" \
  "v_lshlrev_b32 v242, 4, v233\n\t" \
  "v_mfma_f32_16x16x32_bf16 v[116:119], a[148:151], a[44:47], v[116:119]\n\t" \
  "buffer_load_dword v246, v245, s[68:71], s99 offen\n\t" \
  "v_mfma_f32_16x16x32_bf16 v[24:27], a[152:155], a[32:35], v[24:27]\n\t" \
  "ds_read_b64_tr_b16 a[116:117], v197 offset:24576\n\t" \
  "v_add_u32 v213, s38, v242\n\t" \
  "v_mfma_f32_16x16x32_bf16 v[24:27], a[88:91], a[48:51], v[24:27]\n\t" \
  "ds_read_b64_tr_b16 a[118:119], v197 offset:25600\n\t" \
  "v_add_u32 v215, 98304, v213\n\t" \
  "v_lshlrev_b32 v254, 9, v233\n\t" \
  "v_mfma_f32_16x16x32_bf16 v[56:59], a[88:91], a[52:55], v[56:59]\n\t" \
  "ds_read_b64_tr_b16 a[120:121], v198 offset:24576\n\t" \
  "v_and_b32 v244, 0x38, v233\n\t" \
  "v_lshl_or_b32 v254, v244, 26, v254\n\t" \
  "v_mfma_f32_16x16x32_bf16 v[56:59], a[152:155], a[36:39], v[56:59]\n\t" \
  "ds_read_b64_tr_b16 a[122:123], v198 offset:25600\n\t" \
  "v_bfrev_b32 v244, 1\n\t" \
  "v_cmp_eq_u32 vcc, s39, v233\n\t" \
  "v_mfma_f32_16x16x32_bf16 v[88:91], a[152:155], a[40:43], v[88:91]\n\t" \
  "v_add_u32 v253, s39, v233\n\t" \
  "v_bfi_b32 v253, 7, v253, v233\n\t" \
  "v_mfma_f32_16x16x32_bf16 v[88:91], a[88:91], a[56:59], v[88:91]\n\t" \
  "ds_read_b64_tr_b16 a[124:125], v199 offset:24576\n\t" \
  "v_cndmask_b32 v254, v254, v244, vcc\n\t" \
  "v_mfma_f32_16x16x32_bf16 v[120:123], a[88:91], a[60:63], v[120:123]\n\t" \
  "v_and_b32 v253, 31, v253\n\t" \
  "v_lshlrev_b32 v253, 2, v253\n\t" \
  "v_mfma_f32_16x16x32_bf16 v[120:123], a[152:155], a[44:47], v[120:123]\n\t" \
  "ds_read_b64_tr_b16 a[126:127], v199 offset:25600\n\t" \
  "v_mov_b32 v250, v244\n\t" \
  "v_mfma_f32_16x16x32_bf16 v[28:31], a[156:159], a[32:35], v[28:31]\n\t" \
  ".Lhs5:\n\ts_cbranch_execz .Lhs5n\n\t" \
  "v_writelane_b32 v244, 0, 0\n\t" \
  "v_writelane_b32 v250, s44, 0\n\t" \
  "v_mfma_f32_16x16x32_bf16 v[28:31], a[92:95], a[48:51], v[28:31]\n\t" \
  "v_lshrrev_b32 v251, 1, v233\n\t" \
  "v_lshlrev_b32 v251, 2, v251\n\t" \
  "v_mfma_f32_16x16x32_bf16 v[60:63], a[92:95], a[52:55], v[60:63]\n\t" \
  "v_add_u32 v251, 8192, v251\n\t" \
  "v_lshlrev_b32 v209, 10, v233\n\t" \
  "v_mfma_f32_16x16x32_bf16 v[60:63], a[156:159], a[36:39], v[60:63]\n\t" \
  "v_and_b32 v214, 0x38, v233\n\t" \
  "v_lshl_or_b32 v207, v214, 26, v209\n\t" \
  "v_mfma_f32_16x16x32_bf16 v[92:95], a[156:159], a[40:43], v[92:95]\n\t" \
  "v_lshrrev_b32 v252, 5, v233\n\t" \
  "v_mul_u32_u24 v252, 1536, v252\n\t" \
  "v_mfma_f32_16x16x32_bf16 v[92:95], a[92:95], a[56:59], v[92:95]\n\t" \
  "v_lshl_add_u32 v252, s3, 9, v252\n\t" \
  "v_add_u32 v252, v252, v242\n\t" \
  "v_mfma_f32_16x16x32_bf16 v[124:127], a[92:95], a[60:63], v[124:127]\n\t" \
  "v_mfma_f32_16x16x32_bf16 v[124:127], a[156:159], a[44:47], v[124:127]\n\t" \
  "s_waitcnt vmcnt(16) lgkmcnt(0)\n\t" \
  FSTP("s_waitcnt lgkmcnt(0)\n\t") \
  FSTP("v_writelane_b32 v255, s94, 52\n\t") \
  FSTP("v_writelane_b32 v255, s92, 53\n\t") \
  FSTA("s_memrealtime s[94:95]\n\t") \
  FSTA("s_memtime s[92:93]\n\t") \
  FSTA("s_waitcnt lgkmcnt(0)\n\t") \
  FSTA("v_writelane_b32 v255, s94, 16\n\t") \
  FSTA("v_writelane_b32 v255, s92, 17\n\t") \
  ".subsection 1\n\t" \
  ".Lfr_s:\n\t" \
  "s_mov_b32 s54, 0\n\t" \
  ".Lfr_r:\n\t" \
  "s_sleep 1\n\t" \
  "buffer_load_dwordx4 v[128:131], v248, s[16:19], s53 offen sc1\n\t" \
  "buffer_load_dwordx4 v[132:135], v248, s[16:19], s58 offen sc1\n\t" \
  "buffer_load_dwordx4 v[136:139], v248, s[16:19], s90 offen sc1\n\t" \
  "buffer_load_dwordx4 v[140:143], v248, s[16:19], s91 offen sc1\n\t" \
  "buffer_load_dwordx4 v[160:163], v249, s[16:19], s96 offen sc1\n\t" \
  "buffer_load_dwordx4 v[164:167], v249, s[16:19], s97 offen sc1\n\t" \
  "buffer_load_dwordx4 v[168:171], v249, s[16:19], s99 offen sc1\n\t" \
  "buffer_load_dwordx4 v[172:175], v249, s[16:19], s57 offen sc1\n\t" \
  "s_waitcnt vmcnt(0)\n\t" \
  "v_xor_b32 v144, v211, v129\n\t" \
  "v_bitop3_b32 v144, v144, v131, v211 bitop3:0xf6\n\t" \
  "v_bitop3_b32 v144, v144, v133, v211 bitop3:0xf6\n\t" \
  "v_bitop3_b32 v144, v144, v135, v211 bitop3:0xf6\n\t" \
  "v_bitop3_b32 v144, v144, v137, v211 bitop3:0xf6\n\t" \
  "v_bitop3_b32 v144, v144, v139, v211 bitop3:0xf6\n\t" \
  "v_bitop3_b32 v144, v144, v141, v211 bitop3:0xf6\n\t" \
  "v_bitop3_b32 v144, v144, v143, v211 bitop3:0xf6\n\t" \
  "v_bitop3_b32 v144, v144, v161, v211 bitop3:0xf6\n\t" \
  "v_bitop3_b32 v144, v144, v163, v211 bitop3:0xf6\n\t" \
  "v_bitop3_b32 v144, v144, v165, v211 bitop3:0xf6\n\t" \
  "v_bitop3_b32 v144, v144, v167, v211 bitop3:0xf6\n\t" \
  "v_bitop3_b32 v144, v144, v169, v211 bitop3:0xf6\n\t" \
  "v_bitop3_b32 v144, v144, v171, v211 bitop3:0xf6\n\t" \
  "v_bitop3_b32 v144, v144, v173, v211 bitop3:0xf6\n\t" \
  "v_bitop3_b32 v144, v144, v175, v211 bitop3:0xf6\n\t" \
  "v_cmp_ne_u32 vcc, 0, v144\n\t" \
  "v_add_f32 v146, v128, v132\n\t" \
  "v_add_f32 v148, v136, v140\n\t" \
  "v_add_f32 v147, v130, v134\n\t" \
  "v_add_f32 v149, v138, v142\n\t" \
  "v_add_f32 v146, v146, v148\n\t" \
  "v_add_f32 v147, v147, v149\n\t" \
  "s_cbranch_vccz .Lfr_k\n\t" \
  "s_add_u32 s54, s54, 1\n\t" \
  "s_cmp_lt_u32 s54, " FQ_CAP "\n\t" \
  "s_cbranch_scc1 .Lfr_r\n\t" \
  "buffer_store_dword v211, off, s[16:19], 0 offset:256 sc1\n\t" \
  "s_branch .Lfr_k\n\t" \
  ".Lfq_s:\n\t" \
  "s_mov_b32 s97, 0\n\t" \
  ".Lfq_r:\n\t" \
  "buffer_load_dword v252, v210, s[16:19], s84 offen sc1\n\t" \
  "s_waitcnt vmcnt(0)\n\t" \
  "v_cmp_ne_u32 vcc, v211, v252\n\t" \
  "s_cbranch_vccz .Lfq_k\n\t" \
  "s_add_u32 s97, s97, 1\n\t" \
  "s_cmp_lt_u32 s97, " FQ_CAP "\n\t" \
  "s_cbranch_scc1 .Lfq_r\n\t" \
  "buffer_store_dword v211, off, s[16:19], 0 offset:256 sc1\n\t" \
  "s_branch .Lfq_k\n\t" \
  ".subsection 0\n\t"
static_assert(W_VOTE == 8192 && W_FLAGR == 32768 && W_FLAGO == 131072 && W_MODB == 2228224 && W_OT == 8388608 && W_XCH == 25165824 && W_EPOCH == 2490368 && W_FLAGH == 2494464 && W_ST1 == 2097152 && W_MODA == 2162688 && W_HT == 4194304 && W_PROBE == 16384);
#define STS(x)
#define AO_STV
#define STA(x)
#define STP(x)
#define AT_ASM \
  "s_cmp_eq_u32 s37, 0\n\t" \
  "s_cbranch_scc1 .Lsf_go\n\t" \
  "s_barrier\n\t" \
  "v_mfma_f32_16x16x32_bf16 v[28:31], a[188:191], a[192:195], v[28:31]\n\t" \
  "s_add_u32 s77, s51, 131072\n\t" \
  "ds_read_b64_tr_b16 a[156:157], v199 offset:32768\n\t" \
  "s_mov_b32 s89, 0\n\t" \
  "v_mfma_f32_16x16x32_bf16 v[28:31], a[124:127], a[208:211], v[28:31]\n\t" \
  "s_brev_b32 s78, 1\n\t" \
  "ds_read_b64_tr_b16 a[158:159], v199 offset:33792\n\t" \
  "s_mov_b32 s79, 0x0f800000\n\t" \
  "v_mfma_f32_16x16x32_bf16 v[24:27], a[184:187], a[192:195], v[24:27]\n\t" \
  "ds_read_b64_tr_b16 a[92:93], v199 offset:40960\n\t" \
  "v_mfma_f32_16x16x32_bf16 v[24:27], a[120:123], a[208:211], v[24:27]\n\t" \
  "ds_read_b64_tr_b16 a[94:95], v199 offset:41984\n\t" \
  "v_mfma_f32_16x16x32_bf16 v[20:23], a[180:183], a[192:195], v[20:23]\n\t" \
  "ds_read_b64_tr_b16 a[152:153], v198 offset:32768\n\t" \
  "v_mfma_f32_16x16x32_bf16 v[20:23], a[116:119], a[208:211], v[20:23]\n\t" \
  "ds_read_b64_tr_b16 a[154:155], v198 offset:33792\n\t" \
  "v_mfma_f32_16x16x32_bf16 v[16:19], a[176:179], a[192:195], v[16:19]\n\t" \
  "ds_read_b64_tr_b16 a[88:89], v198 offset:40960\n\t" \
  "v_mfma_f32_16x16x32_bf16 v[16:19], a[112:115], a[208:211], v[16:19]\n\t" \
  "ds_read_b64_tr_b16 a[90:91], v198 offset:41984\n\t" \
  "v_mfma_f32_16x16x32_bf16 v[12:15], a[172:175], a[192:195], v[12:15]\n\t" \
  "ds_read_b64_tr_b16 a[148:149], v197 offset:32768\n\t" \
  "v_mfma_f32_16x16x32_bf16 v[12:15], a[108:111], a[208:211], v[12:15]\n\t" \
  "ds_read_b64_tr_b16 a[150:151], v197 offset:33792\n\t" \
  "v_mfma_f32_16x16x32_bf16 v[8:11], a[168:171], a[192:195], v[8:11]\n\t" \
  "ds_read_b64_tr_b16 a[84:85], v197 offset:40960\n\t" \
  "s_mov_b32 s54, 0\n\t" \
  "v_mfma_f32_16x16x32_bf16 v[8:11], a[104:107], a[208:211], v[8:11]\n\t" \
  "ds_read_b64_tr_b16 a[86:87], v197 offset:41984\n\t" \
  "v_mfma_f32_16x16x32_bf16 v[4:7], a[164:167], a[192:195], v[4:7]\n\t" \
  "ds_read_b64_tr_b16 a[144:145], v196 offset:32768\n\t" \
  "v_mfma_f32_16x16x32_bf16 v[4:7], a[100:103], a[208:211], v[4:7]\n\t" \
  "ds_read_b64_tr_b16 a[146:147], v196 offset:33792\n\t" \
  "v_mfma_f32_16x16x32_bf16 v[0:3], a[160:163], a[192:195], v[0:3]\n\t" \
  "ds_read_b64_tr_b16 a[80:81], v196 offset:40960\n\t" \
  "v_mfma_f32_16x16x32_bf16 v[0:3], a[96:99], a[208:211], v[0:3]\n\t" \
  "ds_read_b64_tr_b16 a[82:83], v196 offset:41984\n\t" \
  "v_mfma_f32_16x16x32_bf16 v[60:63], a[188:191], a[196:199], v[60:63]\n\t" \
  "ds_read_b64_tr_b16 a[140:141], v195 offset:32768\n\t" \
  "v_mfma_f32_16x16x32_bf16 v[60:63], a[124:127], a[212:215], v[60:63]\n\t" \
  "ds_read_b64_tr_b16 a[142:143], v195 offset:33792\n\t" \
  "v_mfma_f32_16x16x32_bf16 v[56:59], a[184:187], a[196:199], v[56:59]\n\t" \
  "ds_read_b64_tr_b16 a[76:77], v195 offset:40960\n\t" \
  "v_mfma_f32_16x16x32_bf16 v[56:59], a[120:123], a[212:215], v[56:59]\n\t" \
  "ds_read_b64_tr_b16 a[78:79], v195 offset:41984\n\t" \
  "v_mfma_f32_16x16x32_bf16 v[52:55], a[180:183], a[196:199], v[52:55]\n\t" \
  "ds_read_b64_tr_b16 a[136:137], v194 offset:32768\n\t" \
  "v_mfma_f32_16x16x32_bf16 v[52:55], a[116:119], a[212:215], v[52:55]\n\t" \
  "ds_read_b64_tr_b16 a[138:139], v194 offset:33792\n\t" \
  "v_mfma_f32_16x16x32_bf16 v[48:51], a[176:179], a[196:199], v[48:51]\n\t" \
  "ds_read_b64_tr_b16 a[72:73], v194 offset:40960\n\t" \
  "v_mfma_f32_16x16x32_bf16 v[48:51], a[112:115], a[212:215], v[48:51]\n\t" \
  "ds_read_b64_tr_b16 a[74:75], v194 offset:41984\n\t" \
  "v_mfma_f32_16x16x32_bf16 v[44:47], a[172:175], a[196:199], v[44:47]\n\t" \
  "ds_read_b64_tr_b16 a[132:133], v193 offset:32768\n\t" \
  "v_mfma_f32_16x16x32_bf16 v[44:47], a[108:111], a[212:215], v[44:47]\n\t" \
  "ds_read_b64_tr_b16 a[134:135], v193 offset:33792\n\t" \
  "v_mfma_f32_16x16x32_bf16 v[40:43], a[168:171], a[196:199], v[40:43]\n\t" \
  "ds_read_b64_tr_b16 a[68:69], v193 offset:40960\n\t" \
  "v_mfma_f32_16x16x32_bf16 v[40:43], a[104:107], a[212:215], v[40:43]\n\t" \
  "ds_read_b64_tr_b16 a[70:71], v193 offset:41984\n\t" \
  "v_mfma_f32_16x16x32_bf16 v[36:39], a[164:167], a[196:199], v[36:39]\n\t" \
  "ds_read_b64_tr_b16 a[128:129], v192 offset:32768\n\t" \
  "v_mfma_f32_16x16x32_bf16 v[36:39], a[100:103], a[212:215], v[36:39]\n\t" \
  "ds_read_b64_tr_b16 a[130:131], v192 offset:33792\n\t" \
  "v_mfma_f32_16x16x32_bf16 v[32:35], a[160:163], a[196:199], v[32:35]\n\t" \
  "ds_read_b64_tr_b16 a[64:65], v192 offset:40960\n\t" \
  "v_mfma_f32_16x16x32_bf16 v[32:35], a[96:99], a[212:215], v[32:35]\n\t" \
  "s_waitcnt lgkmcnt(15)\n\t" \
  "ds_read_b64_tr_b16 a[66:67], v192 offset:41984\n\t" \
  "v_mfma_f32_16x16x32_bf16 v[28:31], a[156:159], a[224:227], v[28:31]\n\t" \
  "v_mfma_f32_16x16x32_bf16 v[28:31], a[92:95], a[240:243], v[28:31]\n\t" \
  "v_mfma_f32_16x16x32_bf16 v[24:27], a[152:155], a[224:227], v[24:27]\n\t" \
  "s_waitcnt vmcnt(3)\n\t" \
  "s_nop 1\n\t" \
  "v_mfma_f32_16x16x32_bf16 v[24:27], a[88:91], a[240:243], v[24:27]\n\t" \
  "s_barrier\n\t" \
  "v_mfma_f32_16x16x32_bf16 v[20:23], a[148:151], a[224:227], v[20:23]\n\t" \
  "ds_read_b128 a[0:3], v220 offset:0\n\t" \
  "v_cvt_pk_bf16_f32 v31, v30, v31\n\t" \
  "v_mfma_f32_16x16x32_bf16 v[20:23], a[84:87], a[240:243], v[20:23]\n\t" \
  "ds_read_b128 a[4:7], v221 offset:0\n\t" \
  "v_cvt_pk_bf16_f32 v30, v28, v29\n\t" \
  "v_mfma_f32_16x16x32_bf16 v[16:19], a[144:147], a[224:227], v[16:19]\n\t" \
  "v_cvt_pk_bf16_f32 v28, v24, v25\n\t" \
  "v_cvt_pk_bf16_f32 v29, v26, v27\n\t" \
  "v_mfma_f32_16x16x32_bf16 v[16:19], a[80:83], a[240:243], v[16:19]\n\t" \
  "s_waitcnt lgkmcnt(10)\n\t" \
  "v_permlane16_swap_b32 v28, v30\n\t" \
  "v_mfma_f32_16x16x32_bf16 v[12:15], a[140:143], a[224:227], v[12:15]\n\t" \
  "ds_read_b128 a[8:11], v222 offset:0\n\t" \
  "v_cvt_pk_bf16_f32 v27, v22, v23\n\t" \
  "v_mfma_f32_16x16x32_bf16 v[12:15], a[76:79], a[240:243], v[12:15]\n\t" \
  "v_permlane16_swap_b32 v29, v31\n\t" \
  "v_mfma_f32_16x16x32_bf16 v[8:11], a[136:139], a[224:227], v[8:11]\n\t" \
  "ds_read_b128 a[12:15], v223 offset:0\n\t" \
  "v_cvt_pk_bf16_f32 v26, v20, v21\n\t" \
  "v_mfma_f32_16x16x32_bf16 v[8:11], a[72:75], a[240:243], v[8:11]\n\t" \
  "s_waitcnt lgkmcnt(4)\n\t" \
  "v_cvt_pk_bf16_f32 v24, v16, v17\n\t" \
  "v_cvt_pk_bf16_f32 v25, v18, v19\n\t" \
  "v_mfma_f32_16x16x32_bf16 v[4:7], a[132:135], a[224:227], v[4:7]\n\t" \
  "ds_read_b128 a[16:19], v220 offset:4096\n\t" \
  "v_cvt_pk_bf16_f32 v23, v14, v15\n\t" \
  "v_mfma_f32_16x16x32_bf16 v[4:7], a[68:71], a[240:243], v[4:7]\n\t" \
  "v_cvt_pk_bf16_f32 v22, v12, v13\n\t" \
  "v_cvt_pk_bf16_f32 v20, v8, v9\n\t" \
  "v_mfma_f32_16x16x32_bf16 v[0:3], a[128:131], a[224:227], v[0:3]\n\t" \
  "ds_read_b128 a[20:23], v221 offset:4096\n\t" \
  "v_cvt_pk_bf16_f32 v21, v10, v11\n\t" \
  "v_mfma_f32_16x16x32_bf16 v[0:3], a[64:67], a[240:243], v[0:3]\n\t" \
  "v_permlane16_swap_b32 v24, v26\n\t" \
  "v_mfma_f32_16x16x32_bf16 v[60:63], a[156:159], a[228:231], v[60:63]\n\t" \
  "ds_read_b128 a[24:27], v222 offset:4096\n\t" \
  "v_cvt_pk_bf16_f32 v19, v6, v7\n\t" \
  "v_mfma_f32_16x16x32_bf16 v[60:63], a[92:95], a[244:247], v[60:63]\n\t" \
  "v_cvt_pk_bf16_f32 v18, v4, v5\n\t" \
  "v_permlane16_swap_b32 v25, v27\n\t" \
  "v_mfma_f32_16x16x32_bf16 v[56:59], a[152:155], a[228:231], v[56:59]\n\t" \
  "ds_read_b128 a[28:31], v223 offset:4096\n\t" \
  "v_cvt_pk_bf16_f32 v16, v0, v1\n\t" \
  "v_mfma_f32_16x16x32_bf16 v[56:59], a[88:91], a[244:247], v[56:59]\n\t" \
  "ds_read_b128 a[32:35], v220 offset:8192\n\t" \
  "v_permlane16_swap_b32 v20, v22\n\t" \
  "v_mfma_f32_16x16x32_bf16 v[52:55], a[148:151], a[228:231], v[52:55]\n\t" \
  "v_permlane16_swap_b32 v21, v23\n\t" \
  "v_mfma_f32_16x16x32_bf16 v[52:55], a[84:87], a[244:247], v[52:55]\n\t" \
  "ds_read_b128 a[36:39], v221 offset:8192\n\t" \
  "v_cvt_pk_bf16_f32 v17, v2, v3\n\t" \
  "v_mfma_f32_16x16x32_bf16 v[48:51], a[144:147], a[228:231], v[48:51]\n\t" \
  "v_permlane16_swap_b32 v16, v18\n\t" \
  "v_mfma_f32_16x16x32_bf16 v[48:51], a[80:83], a[244:247], v[48:51]\n\t" \
  "ds_read_b128 a[40:43], v222 offset:8192\n\t" \
  "v_cvt_pk_bf16_f32 v63, v62, v63\n\t" \
  "v_mfma_f32_16x16x32_bf16 v[44:47], a[140:143], a[228:231], v[44:47]\n\t" \
  "v_permlane16_swap_b32 v17, v19\n\t" \
  "v_mfma_f32_16x16x32_bf16 v[44:47], a[76:79], a[244:247], v[44:47]\n\t" \
  "ds_read_b128 a[44:47], v223 offset:8192\n\t" \
  "v_cvt_pk_bf16_f32 v62, v60, v61\n\t" \
  "v_mfma_f32_16x16x32_bf16 v[40:43], a[136:139], a[228:231], v[40:43]\n\t" \
  "v_cvt_pk_bf16_f32 v60, v56, v57\n\t" \
  "v_cvt_pk_bf16_f32 v61, v58, v59\n\t" \
  "v_mfma_f32_16x16x32_bf16 v[40:43], a[72:75], a[244:247], v[40:43]\n\t" \
  "ds_read_b128 a[48:51], v220 offset:12288\n\t" \
  "v_cvt_pk_bf16_f32 v59, v54, v55\n\t" \
  "v_mfma_f32_16x16x32_bf16 v[36:39], a[132:135], a[228:231], v[36:39]\n\t" \
  "buffer_load_dword v246, v247, s[16:19], 0 offen sc1\n\t" \
  "v_mfma_f32_16x16x32_bf16 v[36:39], a[68:71], a[244:247], v[36:39]\n\t" \
  "ds_read_b128 a[52:55], v221 offset:12288\n\t" \
  "v_cvt_pk_bf16_f32 v58, v52, v53\n\t" \
  "v_mfma_f32_16x16x32_bf16 v[32:35], a[128:131], a[228:231], v[32:35]\n\t" \
  "ds_read_b128 a[56:59], v222 offset:12288\n\t" \
  "v_cvt_pk_bf16_f32 v56, v48, v49\n\t" \
  "v_mfma_f32_16x16x32_bf16 v[32:35], a[64:67], a[244:247], v[32:35]\n\t" \
  "s_waitcnt lgkmcnt(11)\n\t" \
  "ds_read_b128 a[60:63], v223 offset:12288\n\t" \
  "v_cvt_pk_bf16_f32 v57, v50, v51\n\t" \
  "v_mfma_f32_16x16x32_bf16 v[0:3], a[0:3], v[16:19], 0\n\t" \
  "v_cvt_pk_bf16_f32 v55, v46, v47\n\t" \
  "v_cvt_pk_bf16_f32 v54, v44, v45\n\t" \
  "v_mfma_f32_16x16x32_bf16 v[0:3], a[4:7], v[20:23], v[0:3]\n\t" \
  "v_permlane16_swap_b32 v60, v62\n\t" \
  "v_mfma_f32_16x16x32_bf16 v[0:3], a[8:11], v[24:27], v[0:3]\n\t" \
  "v_cvt_pk_bf16_f32 v52, v40, v41\n\t" \
  "v_cvt_pk_bf16_f32 v53, v42, v43\n\t" \
  "v_mfma_f32_16x16x32_bf16 v[0:3], a[12:15], v[28:31], v[0:3]\n\t" \
  "s_waitcnt lgkmcnt(8)\n\t" \
  "v_permlane16_swap_b32 v61, v63\n\t" \
  "v_mfma_f32_16x16x32_bf16 v[4:7], a[16:19], v[16:19], 0\n\t" \
  "s_add_u32 m0, s38, 49152\n\t" \
  "v_permlane16_swap_b32 v56, v58\n\t" \
  "v_mfma_f32_16x16x32_bf16 v[4:7], a[20:23], v[20:23], v[4:7]\n\t" \
  "s_add_u32 s96, s38, 32768\n\t" \
  "v_cvt_pk_bf16_f32 v51, v38, v39\n\t" \
  "v_cvt_pk_bf16_f32 v50, v36, v37\n\t" \
  "v_mfma_f32_16x16x32_bf16 v[4:7], a[24:27], v[24:27], v[4:7]\n\t" \
  "s_waitcnt lgkmcnt(0)\n\t" \
  "v_permlane16_swap_b32 v57, v59\n\t" \
  "v_mfma_f32_16x16x32_bf16 v[4:7], a[28:31], v[28:31], v[4:7]\n\t" \
  "s_barrier\n\t" \
  "v_mfma_f32_16x16x32_bf16 v[92:95], a[188:191], a[200:203], v[92:95]\n\t" \
  "buffer_load_dwordx4 v224, s[60:63], s96 offen offset:0 lds\n\t" \
  "v_mfma_f32_16x16x32_bf16 v[92:95], a[124:127], a[216:219], v[92:95]\n\t" \
  "v_cvt_pk_bf16_f32 v48, v32, v33\n\t" \
  "v_max3_f32 v200, v0, v1, v2\n\t" \
  "v_mfma_f32_16x16x32_bf16 v[88:91], a[184:187], a[200:203], v[88:91]\n\t" \
  "v_cvt_pk_bf16_f32 v49, v34, v35\n\t" \
  "v_max3_f32 v200, v200, v3, v4\n\t" \
  "v_mfma_f32_16x16x32_bf16 v[88:91], a[120:123], a[216:219], v[88:91]\n\t" \
  "v_max3_f32 v200, v200, v5, v6\n\t" \
  "v_max_f32 v200, v200, v7\n\t" \
  "v_mfma_f32_16x16x32_bf16 v[84:87], a[180:183], a[200:203], v[84:87]\n\t" \
  "v_mov_b32 v201, v200\n\t" \
  "s_nop 0\n\t" \
  "v_mfma_f32_16x16x32_bf16 v[84:87], a[116:119], a[216:219], v[84:87]\n\t" \
  "v_permlane32_swap_b32 v200, v201\n\t" \
  "v_mfma_f32_16x16x32_bf16 v[80:83], a[176:179], a[200:203], v[80:83]\n\t" \
  "v_max_f32 v200, v200, v201\n\t" \
  "v_mov_b32 v201, v200\n\t" \
  "v_mfma_f32_16x16x32_bf16 v[80:83], a[112:115], a[216:219], v[80:83]\n\t" \
  "buffer_load_dwordx4 v225, s[60:63], s96 offen offset:1024 lds\n\t" \
  "v_mfma_f32_16x16x32_bf16 v[76:79], a[172:175], a[200:203], v[76:79]\n\t" \
  "v_permlane16_swap_b32 v200, v201\n\t" \
  "v_mfma_f32_16x16x32_bf16 v[76:79], a[108:111], a[216:219], v[76:79]\n\t" \
  "v_max_f32 v200, v200, v201\n\t" \
  "v_sub_f32 v176, 0xc2c00000, v200\n\t" \
  "v_mfma_f32_16x16x32_bf16 v[72:75], a[168:171], a[200:203], v[72:75]\n\t" \
  "v_sub_f32 v177, 0xc2c00000, v200\n\t" \
  "v_sub_f32 v178, 0xc2c00000, v200\n\t" \
  "v_mfma_f32_16x16x32_bf16 v[72:75], a[104:107], a[216:219], v[72:75]\n\t" \
  "buffer_load_dwordx4 v226, s[60:63], s96 offen offset:2048 lds\n\t" \
  "v_mfma_f32_16x16x32_bf16 v[68:71], a[164:167], a[200:203], v[68:71]\n\t" \
  "v_sub_f32 v179, 0xc2c00000, v200\n\t" \
  "v_permlane16_swap_b32 v52, v54\n\t" \
  "v_mfma_f32_16x16x32_bf16 v[68:71], a[100:103], a[216:219], v[68:71]\n\t" \
  "v_add_f32 v0, v0, v176\n\t" \
  "v_permlane16_swap_b32 v53, v55\n\t" \
  "v_mfma_f32_16x16x32_bf16 v[64:67], a[160:163], a[200:203], v[64:67]\n\t" \
  "v_add_f32 v1, v1, v176\n\t" \
  "v_add_f32 v2, v2, v176\n\t" \
  "v_mfma_f32_16x16x32_bf16 v[64:67], a[96:99], a[216:219], v[64:67]\n\t" \
  "buffer_load_dwordx4 v227, s[60:63], s96 offen offset:3072 lds\n\t" \
  "v_mfma_f32_16x16x32_bf16 v[8:11], a[32:35], v[16:19], v[176:179]\n\t" \
  "s_add_u32 m0, s38, 147456\n\t" \
  "v_add_f32 v3, v3, v176\n\t" \
  "v_permlane16_swap_b32 v48, v50\n\t" \
  "v_mfma_f32_16x16x32_bf16 v[8:11], a[36:39], v[20:23], v[8:11]\n\t" \
  "s_add_u32 s96, s38, 16384\n\t" \
  "v_add_f32 v4, v4, v176\n\t" \
  "v_permlane16_swap_b32 v49, v51\n\t" \
  "v_mfma_f32_16x16x32_bf16 v[8:11], a[40:43], v[24:27], v[8:11]\n\t" \
  "v_exp_f32 v0, v0\n\t" \
  "v_mfma_f32_16x16x32_bf16 v[8:11], a[44:47], v[28:31], v[8:11]\n\t" \
  "v_exp_f32 v1, v1\n\t" \
  "v_mfma_f32_16x16x32_bf16 v[12:15], a[48:51], v[16:19], v[176:179]\n\t" \
  "buffer_load_dwordx4 v230, s[64:67], s96 offen offset:0 lds\n\t" \
  "v_mfma_f32_16x16x32_bf16 v[12:15], a[52:55], v[20:23], v[12:15]\n\t" \
  "v_add_f32 v5, v5, v176\n\t" \
  "v_add_f32 v6, v6, v176\n\t" \
  "v_mfma_f32_16x16x32_bf16 v[12:15], a[56:59], v[24:27], v[12:15]\n\t" \
  "v_exp_f32 v2, v2\n\t" \
  "v_mfma_f32_16x16x32_bf16 v[12:15], a[60:63], v[28:31], v[12:15]\n\t" \
  "v_exp_f32 v3, v3\n\t" \
  "v_mfma_f32_16x16x32_bf16 v[32:35], a[0:3], v[48:51], 0\n\t" \
  "v_add_f32 v7, v7, v176\n\t" \
  "v_cvt_pk_bf16_f32 v128, v0, v1\n\t" \
  "v_mfma_f32_16x16x32_bf16 v[32:35], a[4:7], v[52:55], v[32:35]\n\t" \
  "buffer_load_dwordx4 v231, s[64:67], s96 offen offset:1024 lds\n\t" \
  "v_mfma_f32_16x16x32_bf16 v[32:35], a[8:11], v[56:59], v[32:35]\n\t" \
  "v_exp_f32 v4, v4\n\t" \
  "v_mfma_f32_16x16x32_bf16 v[32:35], a[12:15], v[60:63], v[32:35]\n\t" \
  "v_exp_f32 v5, v5\n\t" \
  "v_mfma_f32_16x16x32_bf16 v[36:39], a[16:19], v[48:51], 0\n\t" \
  "v_exp_f32 v6, v6\n\t" \
  "v_mfma_f32_16x16x32_bf16 v[36:39], a[20:23], v[52:55], v[36:39]\n\t" \
  "v_exp_f32 v7, v7\n\t" \
  "v_mfma_f32_16x16x32_bf16 v[36:39], a[24:27], v[56:59], v[36:39]\n\t" \
  "buffer_load_dwordx4 v230, s[64:67], s96 offen offset:2048 lds\n\t" \
  "v_mfma_f32_16x16x32_bf16 v[36:39], a[28:31], v[60:63], v[36:39]\n\t" \
  "v_max3_f32 v200, v32, v33, v34\n\t" \
  "v_cvt_pk_bf16_f32 v129, v2, v3\n\t" \
  "v_mfma_f32_16x16x32_bf16 v[92:95], a[156:159], a[232:235], v[92:95]\n\t" \
  "v_exp_f32 v8, v8\n\t" \
  "v_mfma_f32_16x16x32_bf16 v[92:95], a[92:95], a[248:251], v[92:95]\n\t" \
  "v_exp_f32 v9, v9\n\t" \
  "v_mfma_f32_16x16x32_bf16 v[88:91], a[152:155], a[232:235], v[88:91]\n\t" \
  "v_exp_f32 v10, v10\n\t" \
  "v_max3_f32 v200, v200, v35, v36\n\t" \
  "v_max3_f32 v200, v200, v37, v38\n\t" \
  "v_mfma_f32_16x16x32_bf16 v[88:91], a[88:91], a[248:251], v[88:91]\n\t" \
  "v_max_f32 v200, v200, v39\n\t" \
  "v_mov_b32 v201, v200\n\t" \
  "v_cvt_pk_bf16_f32 v95, v94, v95\n\t" \
  "v_cvt_pk_bf16_f32 v94, v92, v93\n\t" \
  "v_mfma_f32_16x16x32_bf16 v[84:87], a[148:151], a[232:235], v[84:87]\n\t" \
  "buffer_load_dwordx4 v231, s[64:67], s96 offen offset:3072 lds\n\t" \
  "v_mfma_f32_16x16x32_bf16 v[84:87], a[84:87], a[248:251], v[84:87]\n\t" \
  "v_permlane32_swap_b32 v200, v201\n\t" \
  "v_mfma_f32_16x16x32_bf16 v[80:83], a[144:147], a[232:235], v[80:83]\n\t" \
  "v_max_f32 v200, v200, v201\n\t" \
  "v_mov_b32 v201, v200\n\t" \
  "v_mfma_f32_16x16x32_bf16 v[80:83], a[80:83], a[248:251], v[80:83]\n\t" \
  "s_add_u32 s99, s53, 43008\n\t" \
  "v_permlane16_swap_b32 v200, v201\n\t" \
  "v_cvt_pk_bf16_f32 v92, v88, v89\n\t" \
  "v_cvt_pk_bf16_f32 v93, v90, v91\n\t" \
  "v_mfma_f32_16x16x32_bf16 v[76:79], a[140:143], a[232:235], v[76:79]\n\t" \
  "v_max_f32 v200, v200, v201\n\t" \
  "v_sub_f32 v180, 0xc2c00000, v200\n\t" \
  "v_mfma_f32_16x16x32_bf16 v[76:79], a[76:79], a[248:251], v[76:79]\n\t" \
  "v_sub_f32 v181, 0xc2c00000, v200\n\t" \
  "v_sub_f32 v182, 0xc2c00000, v200\n\t" \
  "v_mfma_f32_16x16x32_bf16 v[72:75], a[136:139], a[232:235], v[72:75]\n\t" \
  "v_cvt_pk_bf16_f32 v91, v86, v87\n\t" \
  "v_sub_f32 v183, 0xc2c00000, v200\n\t" \
  "v_mfma_f32_16x16x32_bf16 v[72:75], a[72:75], a[248:251], v[72:75]\n\t" \
  "buffer_load_dword v246, v245, s[68:71], s99 offen\n\t" \
  "v_mfma_f32_16x16x32_bf16 v[68:71], a[132:135], a[232:235], v[68:71]\n\t" \
  "v_cvt_pk_bf16_f32 v90, v84, v85\n\t" \
  "v_cvt_pk_bf16_f32 v88, v80, v81\n\t" \
  "v_mfma_f32_16x16x32_bf16 v[68:71], a[68:71], a[248:251], v[68:71]\n\t" \
  "v_cvt_pk_bf16_f32 v89, v82, v83\n\t" \
  "v_permlane16_swap_b32 v92, v94\n\t" \
  "v_mfma_f32_16x16x32_bf16 v[64:67], a[128:131], a[232:235], v[64:67]\n\t" \
  "v_cvt_pk_bf16_f32 v87, v78, v79\n\t" \
  "v_permlane16_swap_b32 v93, v95\n\t" \
  "v_mfma_f32_16x16x32_bf16 v[64:67], a[64:67], a[248:251], v[64:67]\n\t" \
  "v_cvt_pk_bf16_f32 v86, v76, v77\n\t" \
  "v_exp_f32 v11, v11\n\t" \
  "v_mfma_f32_16x16x32_bf16 v[124:127], a[188:191], a[204:207], v[124:127]\n\t" \
  "v_cvt_pk_bf16_f32 v84, v72, v73\n\t" \
  "v_cvt_pk_bf16_f32 v85, v74, v75\n\t" \
  "v_mfma_f32_16x16x32_bf16 v[124:127], a[124:127], a[220:223], v[124:127]\n\t" \
  "v_cvt_pk_bf16_f32 v83, v70, v71\n\t" \
  "v_permlane16_swap_b32 v88, v90\n\t" \
  "v_mfma_f32_16x16x32_bf16 v[120:123], a[184:187], a[204:207], v[120:123]\n\t" \
  "v_cvt_pk_bf16_f32 v82, v68, v69\n\t" \
  "v_exp_f32 v12, v12\n\t" \
  "v_mfma_f32_16x16x32_bf16 v[120:123], a[120:123], a[220:223], v[120:123]\n\t" \
  "v_permlane16_swap_b32 v89, v91\n\t" \
  "v_mfma_f32_16x16x32_bf16 v[116:119], a[180:183], a[204:207], v[116:119]\n\t" \
  "v_cvt_pk_bf16_f32 v80, v64, v65\n\t" \
  "v_add_f32 v32, v32, v180\n\t" \
  "v_mfma_f32_16x16x32_bf16 v[116:119], a[116:119], a[220:223], v[116:119]\n\t" \
  "s_add_u32 s99, s53, 0\n\t" \
  "v_exp_f32 v13, v13\n\t" \
  "v_mfma_f32_16x16x32_bf16 v[112:115], a[176:179], a[204:207], v[112:115]\n\t" \
  "v_permlane16_swap_b32 v84, v86\n\t" \
  "v_mfma_f32_16x16x32_bf16 v[112:115], a[112:115], a[220:223], v[112:115]\n\t" \
  "v_cvt_pk_bf16_f32 v81, v66, v67\n\t" \
  "v_add_f32 v33, v33, v180\n\t" \
  "v_mfma_f32_16x16x32_bf16 v[108:111], a[172:175], a[204:207], v[108:111]\n\t" \
  "buffer_load_dword v246, v245, s[72:75], s99 offen\n\t" \
  "v_mfma_f32_16x16x32_bf16 v[108:111], a[108:111], a[220:223], v[108:111]\n\t" \
  "v_add_f32 v34, v34, v180\n\t" \
  "v_exp_f32 v14, v14\n\t" \
  "v_mfma_f32_16x16x32_bf16 v[104:107], a[168:171], a[204:207], v[104:107]\n\t" \
  "v_permlane16_swap_b32 v85, v87\n\t" \
  "v_mfma_f32_16x16x32_bf16 v[104:107], a[104:107], a[220:223], v[104:107]\n\t" \
  "v_exp_f32 v15, v15\n\t" \
  "v_mfma_f32_16x16x32_bf16 v[100:103], a[164:167], a[204:207], v[100:103]\n\t" \
  "v_permlane16_swap_b32 v80, v82\n\t" \
  "v_mfma_f32_16x16x32_bf16 v[100:103], a[100:103], a[220:223], v[100:103]\n\t" \
  "v_add_f32 v35, v35, v180\n\t" \
  "v_add_f32 v36, v36, v180\n\t" \
  "v_mfma_f32_16x16x32_bf16 v[96:99], a[160:163], a[204:207], v[96:99]\n\t" \
  "v_add_f32 v37, v37, v180\n\t" \
  "v_exp_f32 v32, v32\n\t" \
  "v_mfma_f32_16x16x32_bf16 v[96:99], a[96:99], a[220:223], v[96:99]\n\t" \
  "v_permlane16_swap_b32 v81, v83\n\t" \
  "v_mfma_f32_16x16x32_bf16 v[124:127], a[156:159], a[236:239], v[124:127]\n\t" \
  "v_exp_f32 v33, v33\n\t" \
  "v_mfma_f32_16x16x32_bf16 v[124:127], a[92:95], a[252:255], v[124:127]\n\t" \
  "v_exp_f32 v34, v34\n\t" \
  "v_mfma_f32_16x16x32_bf16 v[120:123], a[152:155], a[236:239], v[120:123]\n\t" \
  "v_add_f32 v38, v38, v180\n\t" \
  "v_add_f32 v39, v39, v180\n\t" \
  "v_mfma_f32_16x16x32_bf16 v[120:123], a[88:91], a[252:255], v[120:123]\n\t" \
  "v_exp_f32 v35, v35\n\t" \
  "v_mfma_f32_16x16x32_bf16 v[116:119], a[148:151], a[236:239], v[116:119]\n\t" \
  "v_cvt_pk_bf16_f32 v130, v4, v5\n\t" \
  "v_cvt_pk_bf16_f32 v127, v126, v127\n\t" \
  "v_mfma_f32_16x16x32_bf16 v[116:119], a[84:87], a[252:255], v[116:119]\n\t" \
  "v_cvt_pk_bf16_f32 v126, v124, v125\n\t" \
  "v_cvt_pk_bf16_f32 v131, v6, v7\n\t" \
  "v_mfma_f32_16x16x32_bf16 v[112:115], a[144:147], a[236:239], v[112:115]\n\t" \
  "v_cvt_pk_bf16_f32 v124, v120, v121\n\t" \
  "v_cvt_pk_bf16_f32 v125, v122, v123\n\t" \
  "v_mfma_f32_16x16x32_bf16 v[112:115], a[80:83], a[252:255], v[112:115]\n\t" \
  "v_permlane16_swap_b32 v124, v126\n\t" \
  "v_mfma_f32_16x16x32_bf16 v[108:111], a[140:143], a[236:239], v[108:111]\n\t" \
  "v_cvt_pk_bf16_f32 v123, v118, v119\n\t" \
  "v_permlane16_swap_b32 v125, v127\n\t" \
  "v_mfma_f32_16x16x32_bf16 v[108:111], a[76:79], a[252:255], v[108:111]\n\t" \
  "v_exp_f32 v36, v36\n\t" \
  "v_cvt_pk_bf16_f32 v122, v116, v117\n\t" \
  "v_mfma_f32_16x16x32_bf16 v[104:107], a[136:139], a[236:239], v[104:107]\n\t" \
  "v_exp_f32 v37, v37\n\t" \
  "v_cvt_pk_bf16_f32 v120, v112, v113\n\t" \
  "v_cvt_pk_bf16_f32 v121, v114, v115\n\t" \
  "v_mfma_f32_16x16x32_bf16 v[104:107], a[72:75], a[252:255], v[104:107]\n\t" \
  "s_nop 0\n\t" \
  "v_cvt_pk_bf16_f32 v119, v110, v111\n\t" \
  "v_permlane16_swap_b32 v120, v122\n\t" \
  "v_cvt_pk_bf16_f32 v118, v108, v109\n\t" \
  "v_mfma_f32_16x16x32_bf16 v[100:103], a[132:135], a[236:239], v[100:103]\n\t" \
  "v_permlane16_swap_b32 v121, v123\n\t" \
  "v_mfma_f32_16x16x32_bf16 v[100:103], a[68:71], a[252:255], v[100:103]\n\t" \
  "v_exp_f32 v38, v38\n\t" \
  "v_mfma_f32_16x16x32_bf16 v[96:99], a[128:131], a[236:239], v[96:99]\n\t" \
  "v_cvt_pk_bf16_f32 v116, v104, v105\n\t" \
  "v_cvt_pk_bf16_f32 v117, v106, v107\n\t" \
  "v_mfma_f32_16x16x32_bf16 v[96:99], a[64:67], a[252:255], v[96:99]\n\t" \
  "v_permlane16_swap_b32 v116, v118\n\t" \
  STS("s_memrealtime s[94:95]\n\t") \
  STS("s_memtime s[92:93]\n\t") \
  "v_mfma_f32_16x16x32_bf16 v[64:67], a[0:3], v[80:83], 0\n\t" \
  "ds_read_b128 a[192:195], v220 offset:16384\n\t" \
  "v_cvt_pk_bf16_f32 v115, v102, v103\n\t" \
  "v_mfma_f32_16x16x32_bf16 v[64:67], a[4:7], v[84:87], v[64:67]\n\t" \
  "ds_read_b128 a[196:199], v221 offset:16384\n\t" \
  "v_permlane16_swap_b32 v117, v119\n\t" \
  "v_mfma_f32_16x16x32_bf16 v[64:67], a[8:11], v[88:91], v[64:67]\n\t" \
  "ds_read_b128 a[200:203], v222 offset:16384\n\t" \
  "v_cvt_pk_bf16_f32 v114, v100, v101\n\t" \
  "v_mfma_f32_16x16x32_bf16 v[64:67], a[12:15], v[92:95], v[64:67]\n\t" \
  "v_cvt_pk_bf16_f32 v112, v96, v97\n\t" \
  "v_exp_f32 v39, v39\n\t" \
  "v_mfma_f32_16x16x32_bf16 v[68:71], a[16:19], v[80:83], 0\n\t" \
  "v_cvt_pk_bf16_f32 v113, v98, v99\n\t" \
  "v_cvt_pk_bf16_f32 v132, v8, v9\n\t" \
  "v_mfma_f32_16x16x32_bf16 v[68:71], a[20:23], v[84:87], v[68:71]\n\t" \
  "v_permlane16_swap_b32 v112, v114\n\t" \
  "v_mfma_f32_16x16x32_bf16 v[68:71], a[24:27], v[88:91], v[68:71]\n\t" \
  "ds_read_b128 a[204:207], v223 offset:16384\n\t" \
  "v_max3_f32 v200, v64, v65, v66\n\t" \
  "v_mfma_f32_16x16x32_bf16 v[68:71], a[28:31], v[92:95], v[68:71]\n\t" \
  "v_permlane16_swap_b32 v113, v115\n\t" \
  "v_mfma_f32_16x16x32_bf16 v[40:43], a[32:35], v[48:51], v[180:183]\n\t" \
  "ds_read_b128 a[208:211], v220 offset:20480\n\t" \
  "v_cvt_pk_bf16_f32 v133, v10, v11\n\t" \
  "v_mfma_f32_16x16x32_bf16 v[40:43], a[36:39], v[52:55], v[40:43]\n\t" \
  "ds_read_b128 a[212:215], v221 offset:20480\n\t" \
  "v_cvt_pk_bf16_f32 v134, v12, v13\n\t" \
  "s_nop 0\n\t" \
  "v_max3_f32 v200, v200, v67, v68\n\t" \
  "v_max3_f32 v200, v200, v69, v70\n\t" \
  "v_mfma_f32_16x16x32_bf16 v[40:43], a[40:43], v[56:59], v[40:43]\n\t" \
  "v_max_f32 v200, v200, v71\n\t" \
  "v_mov_b32 v201, v200\n\t" \
  "v_mfma_f32_16x16x32_bf16 v[40:43], a[44:47], v[60:63], v[40:43]\n\t" \
  "ds_read_b128 a[216:219], v222 offset:20480\n\t" \
  "v_permlane32_swap_b32 v200, v201\n\t" \
  "v_max_f32 v200, v200, v201\n\t" \
  "v_mov_b32 v201, v200\n\t" \
  "v_mfma_f32_16x16x32_bf16 v[44:47], a[48:51], v[48:51], v[180:183]\n\t" \
  STS("v_writelane_b32 v255, s94, 18\n\t") \
  STS("v_writelane_b32 v255, s92, 19\n\t") \
  "s_nop 0\n\t" \
  "v_permlane16_swap_b32 v200, v201\n\t" \
  "v_max_f32 v200, v200, v201\n\t" \
  "v_mfma_f32_16x16x32_bf16 v[44:47], a[52:55], v[52:55], v[44:47]\n\t" \
  "v_sub_f32 v184, 0xc2c00000, v200\n\t" \
  "v_sub_f32 v185, 0xc2c00000, v200\n\t" \
  "v_exp_f32 v40, v40\n\t" \
  "v_mfma_f32_16x16x32_bf16 v[44:47], a[56:59], v[56:59], v[44:47]\n\t" \
  "v_sub_f32 v186, 0xc2c00000, v200\n\t" \
  "v_sub_f32 v187, 0xc2c00000, v200\n\t" \
  "v_mfma_f32_16x16x32_bf16 v[44:47], a[60:63], v[60:63], v[44:47]\n\t" \
  "ds_read_b128 a[220:223], v223 offset:20480\n\t" \
  "v_add_f32 v64, v64, v184\n\t" \
  "v_exp_f32 v41, v41\n\t" \
  "v_mfma_f32_16x16x32_bf16 v[96:99], a[0:3], v[112:115], 0\n\t" \
  "ds_read_b128 a[224:227], v220 offset:24576\n\t" \
  "v_exp_f32 v42, v42\n\t" \
  "v_mfma_f32_16x16x32_bf16 v[96:99], a[4:7], v[116:119], v[96:99]\n\t" \
  "ds_read_b128 a[228:231], v221 offset:24576\n\t" \
  "v_exp_f32 v43, v43\n\t" \
  "v_mfma_f32_16x16x32_bf16 v[96:99], a[8:11], v[120:123], v[96:99]\n\t" \
  "ds_read_b128 a[232:235], v222 offset:24576\n\t" \
  "v_exp_f32 v44, v44\n\t" \
  "v_mfma_f32_16x16x32_bf16 v[96:99], a[12:15], v[124:127], v[96:99]\n\t" \
  "v_add_f32 v65, v65, v184\n\t" \
  "v_exp_f32 v45, v45\n\t" \
  "v_mfma_f32_16x16x32_bf16 v[100:103], a[16:19], v[112:115], 0\n\t" \
  "ds_read_b128 a[236:239], v223 offset:24576\n\t" \
  "v_exp_f32 v46, v46\n\t" \
  "v_mfma_f32_16x16x32_bf16 v[100:103], a[20:23], v[116:119], v[100:103]\n\t" \
  "ds_read_b128 a[240:243], v220 offset:28672\n\t" \
  "v_add_f32 v66, v66, v184\n\t" \
  "v_mfma_f32_16x16x32_bf16 v[100:103], a[24:27], v[120:123], v[100:103]\n\t" \
  "v_exp_f32 v47, v47\n\t" \
  "v_max3_f32 v200, v96, v97, v98\n\t" \
  "v_mfma_f32_16x16x32_bf16 v[100:103], a[28:31], v[124:127], v[100:103]\n\t" \
  "ds_read_b128 a[244:247], v221 offset:28672\n\t" \
  "v_add_f32 v67, v67, v184\n\t" \
  "v_mfma_f32_16x16x32_bf16 v[72:75], a[32:35], v[80:83], v[184:187]\n\t" \
  "v_exp_f32 v64, v64\n\t" \
  "v_mfma_f32_16x16x32_bf16 v[72:75], a[36:39], v[84:87], v[72:75]\n\t" \
  "ds_read_b128 a[248:251], v222 offset:28672\n\t" \
  "v_add_f32 v68, v68, v184\n\t" \
  "s_nop 0\n\t" \
  "v_max3_f32 v200, v200, v99, v100\n\t" \
  "v_max3_f32 v200, v200, v101, v102\n\t" \
  "v_mfma_f32_16x16x32_bf16 v[72:75], a[40:43], v[88:91], v[72:75]\n\t" \
  "v_exp_f32 v65, v65\n\t" \
  "v_max_f32 v200, v200, v103\n\t" \
  "v_mov_b32 v201, v200\n\t" \
  "v_mfma_f32_16x16x32_bf16 v[72:75], a[44:47], v[92:95], v[72:75]\n\t" \
  "ds_read_b128 a[252:255], v223 offset:28672\n\t" \
  "v_permlane32_swap_b32 v200, v201\n\t" \
  "v_max_f32 v200, v200, v201\n\t" \
  "v_mov_b32 v201, v200\n\t" \
  "v_mfma_f32_16x16x32_bf16 v[76:79], a[48:51], v[80:83], v[184:187]\n\t" \
  "v_exp_f32 v66, v66\n\t" \
  "v_permlane16_swap_b32 v200, v201\n\t" \
  "v_mfma_f32_16x16x32_bf16 v[76:79], a[52:55], v[84:87], v[76:79]\n\t" \
  "v_max_f32 v200, v200, v201\n\t" \
  "v_sub_f32 v188, 0xc2c00000, v200\n\t" \
  "v_mfma_f32_16x16x32_bf16 v[76:79], a[56:59], v[88:91], v[76:79]\n\t" \
  "v_sub_f32 v189, 0xc2c00000, v200\n\t" \
  "v_sub_f32 v190, 0xc2c00000, v200\n\t" \
  "v_mfma_f32_16x16x32_bf16 v[76:79], a[60:63], v[92:95], v[76:79]\n\t" \
  "v_add_f32 v69, v69, v184\n\t" \
  "v_exp_f32 v67, v67\n\t" \
  "v_sub_f32 v191, 0xc2c00000, v200\n\t" \
  "s_nop 1\n\t" \
  "v_mfma_f32_16x16x32_bf16 v[104:107], a[32:35], v[112:115], v[188:191]\n\t" \
  "v_add_f32 v70, v70, v184\n\t" \
  "v_add_f32 v71, v71, v184\n\t" \
  "v_mfma_f32_16x16x32_bf16 v[104:107], a[36:39], v[116:119], v[104:107]\n\t" \
  "v_cvt_pk_bf16_f32 v135, v14, v15\n\t" \
  "v_exp_f32 v68, v68\n\t" \
  "v_mfma_f32_16x16x32_bf16 v[104:107], a[40:43], v[120:123], v[104:107]\n\t" \
  "v_cvt_pk_bf16_f32 v136, v32, v33\n\t" \
  "v_exp_f32 v69, v69\n\t" \
  "v_mfma_f32_16x16x32_bf16 v[104:107], a[44:47], v[124:127], v[104:107]\n\t" \
  "s_add_u32 s99, s53, 14336\n\t" \
  "v_exp_f32 v70, v70\n\t" \
  "v_mfma_f32_16x16x32_bf16 v[108:111], a[48:51], v[112:115], v[188:191]\n\t" \
  "v_exp_f32 v71, v71\n\t" \
  "v_mfma_f32_16x16x32_bf16 v[108:111], a[52:55], v[116:119], v[108:111]\n\t" \
  "v_exp_f32 v72, v72\n\t" \
  "v_mfma_f32_16x16x32_bf16 v[108:111], a[56:59], v[120:123], v[108:111]\n\t" \
  "buffer_load_dword v246, v245, s[72:75], s99 offen\n\t" \
  "v_mfma_f32_16x16x32_bf16 v[108:111], a[60:63], v[124:127], v[108:111]\n\t" \
  "s_waitcnt lgkmcnt(0)\n\t" \
  "v_exp_f32 v73, v73\n\t" \
  STA("s_memrealtime s[94:95]\n\t") \
  STA("s_memtime s[92:93]\n\t") \
  "v_mfma_f32_16x16x32_bf16 v[0:3], a[192:195], v[16:19], v[176:179]\n\t" \
  "ds_read_b64_tr_b16 a[128:129], v228 offset:0\n\t" \
  "v_cvt_pk_bf16_f32 v137, v34, v35\n\t" \
  "v_mfma_f32_16x16x32_bf16 v[0:3], a[196:199], v[20:23], v[0:3]\n\t" \
  "ds_read_b64_tr_b16 a[130:131], v228 offset:4096\n\t" \
  "v_cvt_pk_bf16_f32 v138, v36, v37\n\t" \
  "v_mfma_f32_16x16x32_bf16 v[0:3], a[200:203], v[24:27], v[0:3]\n\t" \
  "ds_read_b64_tr_b16 a[132:133], v228 offset:8192\n\t" \
  "v_cvt_pk_bf16_f32 v139, v38, v39\n\t" \
  "v_mfma_f32_16x16x32_bf16 v[0:3], a[204:207], v[28:31], v[0:3]\n\t" \
  "ds_read_b64_tr_b16 a[134:135], v228 offset:12288\n\t" \
  "v_exp_f32 v74, v74\n\t" \
  "v_mfma_f32_16x16x32_bf16 v[4:7], a[220:223], v[28:31], v[176:179]\n\t" \
  "v_exp_f32 v75, v75\n\t" \
  "v_mfma_f32_16x16x32_bf16 v[4:7], a[216:219], v[24:27], v[4:7]\n\t" \
  "ds_read_b64_tr_b16 a[136:137], v229 offset:0\n\t" \
  "v_add_f32 v96, v96, v188\n\t" \
  "v_mfma_f32_16x16x32_bf16 v[4:7], a[212:215], v[20:23], v[4:7]\n\t" \
  "ds_read_b64_tr_b16 a[138:139], v229 offset:4096\n\t" \
  "v_cvt_pk_bf16_f32 v140, v40, v41\n\t" \
  "v_mfma_f32_16x16x32_bf16 v[4:7], a[208:211], v[16:19], v[4:7]\n\t" \
  "ds_read_b64_tr_b16 a[140:141], v229 offset:8192\n\t" \
  "v_exp_f32 v76, v76\n\t" \
  "v_mfma_f32_16x16x32_bf16 v[8:11], a[224:227], v[16:19], v[176:179]\n\t" \
  "v_exp_f32 v77, v77\n\t" \
  "v_mfma_f32_16x16x32_bf16 v[8:11], a[228:231], v[20:23], v[8:11]\n\t" \
  "v_add_f32 v97, v97, v188\n\t" \
  "v_cvt_pk_bf16_f32 v141, v42, v43\n\t" \
  "v_mfma_f32_16x16x32_bf16 v[8:11], a[232:235], v[24:27], v[8:11]\n\t" \
  "ds_read_b64_tr_b16 a[142:143], v229 offset:12288\n\t" \
  "v_add_f32 v98, v98, v188\n\t" \
  "v_mfma_f32_16x16x32_bf16 v[8:11], a[236:239], v[28:31], v[8:11]\n\t" \
  "ds_read_b64_tr_b16 a[144:145], v228 offset:256\n\t" \
  "v_lshl_add_u32 v214, v233, 14, s41\n\t" \
  "v_mfma_f32_16x16x32_bf16 v[12:15], a[252:255], v[28:31], v[176:179]\n\t" \
  STA("v_writelane_b32 v255, s94, 26\n\t") \
  STA("v_writelane_b32 v255, s92, 27\n\t") \
  "v_exp_f32 v78, v78\n\t" \
  "v_mfma_f32_16x16x32_bf16 v[12:15], a[248:251], v[24:27], v[12:15]\n\t" \
  "v_cvt_pk_bf16_f32 v142, v44, v45\n\t" \
  "v_min_u32 v214, .Lfz_z-.Lat_t0-4, v214\n\t" \
  "v_mfma_f32_16x16x32_bf16 v[12:15], a[244:247], v[20:23], v[12:15]\n\t" \
  "s_getpc_b64 s[84:85]\n\t" \
  ".Lat_t0:\n\t" \
  "v_exp_f32 v79, v79\n\t" \
  "v_mfma_f32_16x16x32_bf16 v[12:15], a[240:243], v[16:19], v[12:15]\n\t" \
  "global_load_dword v246, v214, s[84:85]\n\t" \
  "v_mfma_f32_16x16x32_bf16 v[32:35], a[192:195], v[48:51], v[180:183]\n\t" \
  "ds_read_b64_tr_b16 a[146:147], v228 offset:4352\n\t" \
  "v_add_f32 v99, v99, v188\n\t" \
  "v_mfma_f32_16x16x32_bf16 v[32:35], a[196:199], v[52:55], v[32:35]\n\t" \
  ".Lhs6:\n\ts_cbranch_execz .Lhs6n\n\t" \
  "v_exp_f32 v96, v96\n\t" \
  "v_mfma_f32_16x16x32_bf16 v[32:35], a[200:203], v[56:59], v[32:35]\n\t" \
  "ds_read_b64_tr_b16 a[148:149], v228 offset:8448\n\t" \
  "v_cvt_pk_bf16_f32 v143, v46, v47\n\t" \
  "v_mfma_f32_16x16x32_bf16 v[32:35], a[204:207], v[60:63], v[32:35]\n\t" \
  "v_add_f32 v100, v100, v188\n\t" \
  "v_cvt_pk_bf16_f32 v144, v64, v65\n\t" \
  "v_mfma_f32_16x16x32_bf16 v[36:39], a[220:223], v[60:63], v[180:183]\n\t" \
  "ds_read_b64_tr_b16 a[150:151], v228 offset:12544\n\t" \
  "v_add_f32 v101, v101, v188\n\t" \
  "v_mfma_f32_16x16x32_bf16 v[36:39], a[216:219], v[56:59], v[36:39]\n\t" \
  "ds_read_b64_tr_b16 a[152:153], v229 offset:256\n\t" \
  "v_cvt_pk_bf16_f32 v145, v66, v67\n\t" \
  "v_mfma_f32_16x16x32_bf16 v[36:39], a[212:215], v[52:55], v[36:39]\n\t" \
  "v_add_f32 v102, v102, v188\n\t" \
  "v_exp_f32 v97, v97\n\t" \
  "v_mfma_f32_16x16x32_bf16 v[36:39], a[208:211], v[48:51], v[36:39]\n\t" \
  "ds_read_b64_tr_b16 a[154:155], v229 offset:4352\n\t" \
  "v_cvt_pk_bf16_f32 v146, v68, v69\n\t" \
  "v_mfma_f32_16x16x32_bf16 v[40:43], a[224:227], v[48:51], v[180:183]\n\t" \
  "v_exp_f32 v98, v98\n\t" \
  "v_mfma_f32_16x16x32_bf16 v[40:43], a[228:231], v[52:55], v[40:43]\n\t" \
  "ds_read_b64_tr_b16 a[156:157], v229 offset:8448\n\t" \
  "v_add_f32 v103, v103, v188\n\t" \
  "v_mfma_f32_16x16x32_bf16 v[40:43], a[232:235], v[56:59], v[40:43]\n\t" \
  "v_exp_f32 v99, v99\n\t" \
  "v_mfma_f32_16x16x32_bf16 v[40:43], a[236:239], v[60:63], v[40:43]\n\t" \
  "ds_read_b64_tr_b16 a[158:159], v229 offset:12544\n\t" \
  "v_cvt_pk_bf16_f32 v147, v70, v71\n\t" \
  "v_mfma_f32_16x16x32_bf16 v[44:47], a[252:255], v[60:63], v[180:183]\n\t" \
  "v_exp_f32 v100, v100\n\t" \
  "v_mfma_f32_16x16x32_bf16 v[44:47], a[248:251], v[56:59], v[44:47]\n\t" \
  "ds_read_b64_tr_b16 a[160:161], v228 offset:512\n\t" \
  "v_cvt_pk_bf16_f32 v148, v72, v73\n\t" \
  "v_mfma_f32_16x16x32_bf16 v[44:47], a[244:247], v[52:55], v[44:47]\n\t" \
  "ds_read_b64_tr_b16 a[162:163], v228 offset:4608\n\t" \
  "v_cvt_pk_bf16_f32 v149, v74, v75\n\t" \
  "v_mfma_f32_16x16x32_bf16 v[44:47], a[240:243], v[48:51], v[44:47]\n\t" \
  "v_cvt_pk_bf16_f32 v150, v76, v77\n\t" \
  "v_exp_f32 v101, v101\n\t" \
  "v_mfma_f32_16x16x32_bf16 v[64:67], a[192:195], v[80:83], v[184:187]\n\t" \
  "ds_read_b64_tr_b16 a[164:165], v228 offset:8704\n\t" \
  "v_cvt_pk_bf16_f32 v151, v78, v79\n\t" \
  "v_mfma_f32_16x16x32_bf16 v[64:67], a[196:199], v[84:87], v[64:67]\n\t" \
  "v_exp_f32 v102, v102\n\t" \
  "v_mfma_f32_16x16x32_bf16 v[64:67], a[200:203], v[88:91], v[64:67]\n\t" \
  "v_exp_f32 v103, v103\n\t" \
  "v_mfma_f32_16x16x32_bf16 v[64:67], a[204:207], v[92:95], v[64:67]\n\t" \
  "ds_read_b64_tr_b16 a[166:167], v228 offset:12800\n\t" \
  "v_cvt_pk_bf16_f32 v152, v96, v97\n\t" \
  "v_mfma_f32_16x16x32_bf16 v[68:71], a[220:223], v[92:95], v[184:187]\n\t" \
  "ds_read_b64_tr_b16 a[168:169], v229 offset:512\n\t" \
  "v_cvt_pk_bf16_f32 v153, v98, v99\n\t" \
  "v_mfma_f32_16x16x32_bf16 v[68:71], a[216:219], v[88:91], v[68:71]\n\t" \
  "v_exp_f32 v104, v104\n\t" \
  "v_mfma_f32_16x16x32_bf16 v[68:71], a[212:215], v[84:87], v[68:71]\n\t" \
  "ds_read_b64_tr_b16 a[170:171], v229 offset:4608\n\t" \
  "v_cvt_pk_bf16_f32 v154, v100, v101\n\t" \
  "v_mfma_f32_16x16x32_bf16 v[68:71], a[208:211], v[80:83], v[68:71]\n\t" \
  "ds_read_b64_tr_b16 a[172:173], v229 offset:8704\n\t" \
  "v_exp_f32 v105, v105\n\t" \
  "v_mfma_f32_16x16x32_bf16 v[72:75], a[224:227], v[80:83], v[184:187]\n\t" \
  "v_exp_f32 v106, v106\n\t" \
  "v_mfma_f32_16x16x32_bf16 v[72:75], a[228:231], v[84:87], v[72:75]\n\t" \
  "ds_read_b64_tr_b16 a[174:175], v229 offset:12800\n\t" \
  "v_cvt_pk_bf16_f32 v155, v102, v103\n\t" \
  "v_mfma_f32_16x16x32_bf16 v[72:75], a[232:235], v[88:91], v[72:75]\n\t" \
  "v_exp_f32 v107, v107\n\t" \
  "v_mfma_f32_16x16x32_bf16 v[72:75], a[236:239], v[92:95], v[72:75]\n\t" \
  "ds_read_b64_tr_b16 a[176:177], v228 offset:768\n\t" \
  "v_cvt_pk_bf16_f32 v156, v104, v105\n\t" \
  "v_mfma_f32_16x16x32_bf16 v[76:79], a[252:255], v[92:95], v[184:187]\n\t" \
  "v_exp_f32 v108, v108\n\t" \
  "v_mfma_f32_16x16x32_bf16 v[76:79], a[248:251], v[88:91], v[76:79]\n\t" \
  "ds_read_b64_tr_b16 a[178:179], v228 offset:4864\n\t" \
  "v_cvt_pk_bf16_f32 v157, v106, v107\n\t" \
  "v_mfma_f32_16x16x32_bf16 v[76:79], a[244:247], v[84:87], v[76:79]\n\t" \
  "v_exp_f32 v109, v109\n\t" \
  "v_mfma_f32_16x16x32_bf16 v[76:79], a[240:243], v[80:83], v[76:79]\n\t" \
  "ds_read_b64_tr_b16 a[180:181], v228 offset:8960\n\t" \
  "v_cvt_pk_bf16_f32 v158, v108, v109\n\t" \
  "v_mfma_f32_16x16x32_bf16 v[96:99], a[192:195], v[112:115], v[188:191]\n\t" \
  "v_exp_f32 v110, v110\n\t" \
  "v_mfma_f32_16x16x32_bf16 v[96:99], a[196:199], v[116:119], v[96:99]\n\t" \
  "v_exp_f32 v111, v111\n\t" \
  "v_mfma_f32_16x16x32_bf16 v[96:99], a[200:203], v[120:123], v[96:99]\n\t" \
  "ds_read_b64_tr_b16 a[182:183], v228 offset:13056\n\t" \
  "v_cvt_pk_bf16_f32 v159, v110, v111\n\t" \
  "v_mfma_f32_16x16x32_bf16 v[96:99], a[204:207], v[124:127], v[96:99]\n\t" \
  "v_exp_f32 v0, v0\n\t" \
  "v_mfma_f32_16x16x32_bf16 v[100:103], a[220:223], v[124:127], v[188:191]\n\t" \
  "ds_read_b64_tr_b16 a[184:185], v229 offset:768\n\t" \
  "v_mfma_f32_16x16x32_bf16 v[100:103], a[216:219], v[120:123], v[100:103]\n\t" \
  "v_exp_f32 v1, v1\n\t" \
  "v_mfma_f32_16x16x32_bf16 v[100:103], a[212:215], v[116:119], v[100:103]\n\t" \
  "ds_read_b64_tr_b16 a[186:187], v229 offset:4864\n\t" \
  "v_mfma_f32_16x16x32_bf16 v[100:103], a[208:211], v[112:115], v[100:103]\n\t" \
  "s_add_u32 s99, s53, 28672\n\t" \
  "v_exp_f32 v2, v2\n\t" \
  "v_mfma_f32_16x16x32_bf16 v[104:107], a[224:227], v[112:115], v[188:191]\n\t" \
  "ds_read_b64_tr_b16 a[188:189], v229 offset:8960\n\t" \
  "v_mfma_f32_16x16x32_bf16 v[104:107], a[228:231], v[116:119], v[104:107]\n\t" \
  "ds_read_b64_tr_b16 a[190:191], v229 offset:13056\n\t" \
  "v_mfma_f32_16x16x32_bf16 v[104:107], a[232:235], v[120:123], v[104:107]\n\t" \
  "buffer_load_dword v246, v245, s[72:75], s99 offen\n\t" \
  "v_mfma_f32_16x16x32_bf16 v[104:107], a[236:239], v[124:127], v[104:107]\n\t" \
  "v_exp_f32 v3, v3\n\t" \
  "v_mfma_f32_16x16x32_bf16 v[108:111], a[252:255], v[124:127], v[188:191]\n\t" \
  "v_exp_f32 v4, v4\n\t" \
  "v_mfma_f32_16x16x32_bf16 v[108:111], a[248:251], v[120:123], v[108:111]\n\t" \
  "v_exp_f32 v5, v5\n\t" \
  "v_mfma_f32_16x16x32_bf16 v[108:111], a[244:247], v[116:119], v[108:111]\n\t" \
  "s_waitcnt vmcnt(5) lgkmcnt(0)\n\t" \
  "v_exp_f32 v6, v6\n\t" \
  "v_mfma_f32_16x16x32_bf16 v[108:111], a[240:243], v[112:115], v[108:111]\n\t" \
  "s_barrier\n\t" \
  STA("s_memrealtime s[94:95]\n\t") \
  STA("s_memtime s[92:93]\n\t") \
  "v_mfma_f32_16x16x32_bf16 a[0:3], a[128:131], v[128:131], 0\n\t" \
  "v_exp_f32 v7, v7\n\t" \
  "v_mfma_f32_16x16x32_bf16 a[0:3], a[132:135], v[132:135], a[0:3]\n\t" \
  "v_exp_f32 v8, v8\n\t" \
  "v_mfma_f32_16x16x32_bf16 a[4:7], a[136:139], v[128:131], 0\n\t" \
  "v_exp_f32 v9, v9\n\t" \
  "v_mfma_f32_16x16x32_bf16 a[4:7], a[140:143], v[132:135], a[4:7]\n\t" \
  "v_exp_f32 v10, v10\n\t" \
  "v_mfma_f32_16x16x32_bf16 a[8:11], a[144:147], v[128:131], 0\n\t" \
  "v_exp_f32 v11, v11\n\t" \
  "v_mfma_f32_16x16x32_bf16 a[8:11], a[148:151], v[132:135], a[8:11]\n\t" \
  "v_exp_f32 v12, v12\n\t" \
  "v_mfma_f32_16x16x32_bf16 a[12:15], a[152:155], v[128:131], 0\n\t" \
  "v_exp_f32 v13, v13\n\t" \
  "v_mfma_f32_16x16x32_bf16 a[12:15], a[156:159], v[132:135], a[12:15]\n\t" \
  "v_exp_f32 v14, v14\n\t" \
  "v_mfma_f32_16x16x32_bf16 a[16:19], a[160:163], v[128:131], 0\n\t" \
  "v_exp_f32 v15, v15\n\t" \
  "v_mfma_f32_16x16x32_bf16 a[16:19], a[164:167], v[132:135], a[16:19]\n\t" \
  "v_exp_f32 v32, v32\n\t" \
  "v_mfma_f32_16x16x32_bf16 a[20:23], a[168:171], v[128:131], 0\n\t" \
  "v_exp_f32 v33, v33\n\t" \
  "v_mfma_f32_16x16x32_bf16 a[20:23], a[172:175], v[132:135], a[20:23]\n\t" \
  "v_exp_f32 v34, v34\n\t" \
  "v_mfma_f32_16x16x32_bf16 a[24:27], a[176:179], v[128:131], 0\n\t" \
  STA("v_writelane_b32 v255, s94, 28\n\t") \
  STA("v_writelane_b32 v255, s92, 29\n\t") \
  "v_exp_f32 v35, v35\n\t" \
  "v_mfma_f32_16x16x32_bf16 a[24:27], a[180:183], v[132:135], a[24:27]\n\t" \
  "v_exp_f32 v36, v36\n\t" \
  "v_mfma_f32_16x16x32_bf16 a[28:31], a[184:187], v[128:131], 0\n\t" \
  "v_exp_f32 v37, v37\n\t" \
  "v_mfma_f32_16x16x32_bf16 a[28:31], a[188:191], v[132:135], a[28:31]\n\t" \
  "v_exp_f32 v38, v38\n\t" \
  "v_smfmac_f32_16x16x64_bf16 v[160:163], v[216:219], v[128:135], v208\n\t" \
  "s_add_u32 m0, s38, 65536\n\t" \
  "ds_read_b128 a[192:195], v220 offset:0\n\t" \
  "v_cvt_pk_bf16_f32 v128, v0, v1\n\t" \
  "v_mfma_f32_16x16x32_bf16 a[32:35], a[128:131], v[136:139], 0\n\t" \
  "s_add_u32 s96, s38, 49152\n\t" \
  "ds_read_b128 a[196:199], v221 offset:0\n\t" \
  "v_cvt_pk_bf16_f32 v129, v2, v3\n\t" \
  "v_mfma_f32_16x16x32_bf16 a[32:35], a[132:135], v[140:143], a[32:35]\n\t" \
  "v_exp_f32 v39, v39\n\t" \
  "v_mfma_f32_16x16x32_bf16 a[36:39], a[136:139], v[136:139], 0\n\t" \
  "v_exp_f32 v40, v40\n\t" \
  "v_mfma_f32_16x16x32_bf16 a[36:39], a[140:143], v[140:143], a[36:39]\n\t" \
  "buffer_load_dwordx4 v224, s[60:63], s96 offen offset:0 lds\n\t" \
  "v_mfma_f32_16x16x32_bf16 a[40:43], a[144:147], v[136:139], 0\n\t" \
  "ds_read_b128 a[200:203], v222 offset:0\n\t" \
  "v_cvt_pk_bf16_f32 v130, v4, v5\n\t" \
  "v_mfma_f32_16x16x32_bf16 a[40:43], a[148:151], v[140:143], a[40:43]\n\t" \
  "v_exp_f32 v41, v41\n\t" \
  "v_mfma_f32_16x16x32_bf16 a[44:47], a[152:155], v[136:139], 0\n\t" \
  "ds_read_b128 a[204:207], v223 offset:0\n\t" \
  "v_cvt_pk_bf16_f32 v131, v6, v7\n\t" \
  "v_mfma_f32_16x16x32_bf16 a[44:47], a[156:159], v[140:143], a[44:47]\n\t" \
  "v_exp_f32 v42, v42\n\t" \
  "v_mfma_f32_16x16x32_bf16 a[48:51], a[160:163], v[136:139], 0\n\t" \
  "ds_read_b128 a[208:211], v220 offset:4096\n\t" \
  "v_cvt_pk_bf16_f32 v132, v8, v9\n\t" \
  "v_mfma_f32_16x16x32_bf16 a[48:51], a[164:167], v[140:143], a[48:51]\n\t" \
  "buffer_load_dwordx4 v225, s[60:63], s96 offen offset:1024 lds\n\t" \
  "v_mfma_f32_16x16x32_bf16 a[52:55], a[168:171], v[136:139], 0\n\t" \
  "v_exp_f32 v43, v43\n\t" \
  "v_mfma_f32_16x16x32_bf16 a[52:55], a[172:175], v[140:143], a[52:55]\n\t" \
  "ds_read_b128 a[212:215], v221 offset:4096\n\t" \
  "v_cvt_pk_bf16_f32 v133, v10, v11\n\t" \
  "v_mfma_f32_16x16x32_bf16 a[56:59], a[176:179], v[136:139], 0\n\t" \
  "v_exp_f32 v44, v44\n\t" \
  "v_mfma_f32_16x16x32_bf16 a[56:59], a[180:183], v[140:143], a[56:59]\n\t" \
  "v_exp_f32 v45, v45\n\t" \
  "v_mfma_f32_16x16x32_bf16 a[60:63], a[184:187], v[136:139], 0\n\t" \
  "buffer_load_dwordx4 v226, s[60:63], s96 offen offset:2048 lds\n\t" \
  "v_mfma_f32_16x16x32_bf16 a[60:63], a[188:191], v[140:143], a[60:63]\n\t" \
  "ds_read_b128 a[216:219], v222 offset:4096\n\t" \
  "v_cvt_pk_bf16_f32 v134, v12, v13\n\t" \
  "v_smfmac_f32_16x16x64_bf16 v[164:167], v[216:219], v[136:143], v208\n\t" \
  "ds_read_b128 a[220:223], v223 offset:4096\n\t" \
  "v_cvt_pk_bf16_f32 v135, v14, v15\n\t" \
  "v_mfma_f32_16x16x32_bf16 a[64:67], a[128:131], v[144:147], 0\n\t" \
  "v_exp_f32 v46, v46\n\t" \
  "v_mfma_f32_16x16x32_bf16 a[64:67], a[132:135], v[148:151], a[64:67]\n\t" \
  "v_exp_f32 v47, v47\n\t" \
  "v_mfma_f32_16x16x32_bf16 a[68:71], a[136:139], v[144:147], 0\n\t" \
  "ds_read_b128 a[224:227], v220 offset:8192\n\t" \
  "v_cvt_pk_bf16_f32 v136, v32, v33\n\t" \
  "v_mfma_f32_16x16x32_bf16 a[68:71], a[140:143], v[148:151], a[68:71]\n\t" \
  "buffer_load_dwordx4 v227, s[60:63], s96 offen offset:3072 lds\n\t" \
  "v_mfma_f32_16x16x32_bf16 a[72:75], a[144:147], v[144:147], 0\n\t" \
  "s_add_u32 m0, s38, 81920\n\t" \
  "v_exp_f32 v64, v64\n\t" \
  "v_mfma_f32_16x16x32_bf16 a[72:75], a[148:151], v[148:151], a[72:75]\n\t" \
  "s_add_u32 s96, s38, 32768\n\t" \
  "ds_read_b128 a[228:231], v221 offset:8192\n\t" \
  "v_cvt_pk_bf16_f32 v137, v34, v35\n\t" \
  "v_mfma_f32_16x16x32_bf16 a[76:79], a[152:155], v[144:147], 0\n\t" \
  "v_exp_f32 v65, v65\n\t" \
  "v_mfma_f32_16x16x32_bf16 a[76:79], a[156:159], v[148:151], a[76:79]\n\t" \
  "s_add_u32 s99, s53, 43008\n\t" \
  "v_exp_f32 v66, v66\n\t" \
  "v_mfma_f32_16x16x32_bf16 a[80:83], a[160:163], v[144:147], 0\n\t" \
  "buffer_load_dwordx4 v230, s[64:67], s96 offen offset:0 lds\n\t" \
  "v_mfma_f32_16x16x32_bf16 a[80:83], a[164:167], v[148:151], a[80:83]\n\t" \
  "ds_read_b128 a[232:235], v222 offset:8192\n\t" \
  "v_cvt_pk_bf16_f32 v138, v36, v37\n\t" \
  "v_mfma_f32_16x16x32_bf16 a[84:87], a[168:171], v[144:147], 0\n\t" \
  "buffer_load_dword v246, v245, s[72:75], s99 offen\n\t" \
  "v_mfma_f32_16x16x32_bf16 a[84:87], a[172:175], v[148:151], a[84:87]\n\t" \
  "ds_read_b128 a[236:239], v223 offset:8192\n\t" \
  "v_cvt_pk_bf16_f32 v139, v38, v39\n\t" \
  "v_mfma_f32_16x16x32_bf16 a[88:91], a[176:179], v[144:147], 0\n\t" \
  "v_exp_f32 v67, v67\n\t" \
  "v_mfma_f32_16x16x32_bf16 a[88:91], a[180:183], v[148:151], a[88:91]\n\t" \
  "ds_read_b128 a[240:243], v220 offset:12288\n\t" \
  "v_cvt_pk_bf16_f32 v140, v40, v41\n\t" \
  "v_mfma_f32_16x16x32_bf16 a[92:95], a[184:187], v[144:147], 0\n\t" \
  "buffer_load_dwordx4 v231, s[64:67], s96 offen offset:1024 lds\n\t" \
  "v_mfma_f32_16x16x32_bf16 a[92:95], a[188:191], v[148:151], a[92:95]\n\t" \
  "v_exp_f32 v68, v68\n\t" \
  "v_smfmac_f32_16x16x64_bf16 v[168:171], v[216:219], v[144:151], v208\n\t" \
  "ds_read_b128 a[244:247], v221 offset:12288\n\t" \
  "v_cvt_pk_bf16_f32 v141, v42, v43\n\t" \
  "v_mfma_f32_16x16x32_bf16 a[96:99], a[128:131], v[152:155], 0\n\t" \
  "v_exp_f32 v69, v69\n\t" \
  "v_mfma_f32_16x16x32_bf16 a[96:99], a[132:135], v[156:159], a[96:99]\n\t" \
  "v_exp_f32 v70, v70\n\t" \
  "v_mfma_f32_16x16x32_bf16 a[100:103], a[136:139], v[152:155], 0\n\t" \
  "buffer_load_dwordx4 v230, s[64:67], s96 offen offset:2048 lds\n\t" \
  "v_mfma_f32_16x16x32_bf16 a[100:103], a[140:143], v[156:159], a[100:103]\n\t" \
  "ds_read_b128 a[248:251], v222 offset:12288\n\t" \
  "v_cvt_pk_bf16_f32 v142, v44, v45\n\t" \
  "v_mfma_f32_16x16x32_bf16 a[104:107], a[144:147], v[152:155], 0\n\t" \
  "v_exp_f32 v71, v71\n\t" \
  "v_mfma_f32_16x16x32_bf16 a[104:107], a[148:151], v[156:159], a[104:107]\n\t" \
  "ds_read_b128 a[252:255], v223 offset:12288\n\t" \
  "v_cvt_pk_bf16_f32 v143, v46, v47\n\t" \
  "v_mfma_f32_16x16x32_bf16 a[108:111], a[152:155], v[152:155], 0\n\t" \
  "v_exp_f32 v72, v72\n\t" \
  "v_mfma_f32_16x16x32_bf16 a[108:111], a[156:159], v[156:159], a[108:111]\n\t" \
  "v_exp_f32 v73, v73\n\t" \
  "v_mfma_f32_16x16x32_bf16 a[112:115], a[160:163], v[152:155], 0\n\t" \
  "buffer_load_dwordx4 v231, s[64:67], s96 offen offset:3072 lds\n\t" \
  "v_mfma_f32_16x16x32_bf16 a[112:115], a[164:167], v[156:159], a[112:115]\n\t" \
  "v_exp_f32 v74, v74\n\t" \
  "v_mfma_f32_16x16x32_bf16 a[116:119], a[168:171], v[152:155], 0\n\t" \
  "v_exp_f32 v75, v75\n\t" \
  "v_mfma_f32_16x16x32_bf16 a[116:119], a[172:175], v[156:159], a[116:119]\n\t" \
  "v_exp_f32 v76, v76\n\t" \
  "v_mfma_f32_16x16x32_bf16 a[120:123], a[176:179], v[152:155], 0\n\t" \
  "v_exp_f32 v77, v77\n\t" \
  "v_mfma_f32_16x16x32_bf16 a[120:123], a[180:183], v[156:159], a[120:123]\n\t" \
  "v_exp_f32 v78, v78\n\t" \
  "v_mfma_f32_16x16x32_bf16 a[124:127], a[184:187], v[152:155], 0\n\t" \
  "v_exp_f32 v79, v79\n\t" \
  "v_mfma_f32_16x16x32_bf16 a[124:127], a[188:191], v[156:159], a[124:127]\n\t" \
  "v_exp_f32 v96, v96\n\t" \
  "v_smfmac_f32_16x16x64_bf16 v[172:175], v[216:219], v[152:159], v208\n\t" \
  "s_waitcnt lgkmcnt(12)\n\t" \
  "v_cvt_pk_bf16_f32 v144, v64, v65\n\t" \
  STA("s_memrealtime s[94:95]\n\t") \
  STA("s_memtime s[92:93]\n\t") \
  "v_mfma_f32_16x16x32_bf16 v[0:3], a[192:195], v[16:19], v[176:179]\n\t" \
  "ds_read_b64_tr_b16 a[128:129], v210 offset:0\n\t" \
  "v_cvt_pk_bf16_f32 v145, v66, v67\n\t" \
  "v_mfma_f32_16x16x32_bf16 v[0:3], a[196:199], v[20:23], v[0:3]\n\t" \
  "v_exp_f32 v97, v97\n\t" \
  "v_mfma_f32_16x16x32_bf16 v[0:3], a[200:203], v[24:27], v[0:3]\n\t" \
  "s_add_u32 m0, s38, 16384\n\t" \
  "ds_read_b64_tr_b16 a[130:131], v210 offset:4096\n\t" \
  "v_cvt_pk_bf16_f32 v146, v68, v69\n\t" \
  "v_mfma_f32_16x16x32_bf16 v[0:3], a[204:207], v[28:31], v[0:3]\n\t" \
  "s_waitcnt lgkmcnt(10)\n\t" \
  "v_exp_f32 v98, v98\n\t" \
  "v_mfma_f32_16x16x32_bf16 v[4:7], a[220:223], v[28:31], v[176:179]\n\t" \
  "s_mov_b32 s96, 0\n\t" \
  "ds_read_b64_tr_b16 a[132:133], v210 offset:8192\n\t" \
  "v_cvt_pk_bf16_f32 v147, v70, v71\n\t" \
  "v_mfma_f32_16x16x32_bf16 v[4:7], a[216:219], v[24:27], v[4:7]\n\t" \
  "v_exp_f32 v99, v99\n\t" \
  "v_mfma_f32_16x16x32_bf16 v[4:7], a[212:215], v[20:23], v[4:7]\n\t" \
  "buffer_load_dwordx4 v232, s[68:71], s96 offen offset:0 lds\n\t" \
  "v_mfma_f32_16x16x32_bf16 v[4:7], a[208:211], v[16:19], v[4:7]\n\t" \
  "s_waitcnt lgkmcnt(7)\n\t" \
  "ds_read_b64_tr_b16 a[134:135], v210 offset:12288\n\t" \
  "v_cvt_pk_bf16_f32 v148, v72, v73\n\t" \
  "v_mfma_f32_16x16x32_bf16 v[8:11], a[224:227], v[16:19], v[176:179]\n\t" \
  "ds_read_b64_tr_b16 a[136:137], v211 offset:0\n\t" \
  "v_cvt_pk_bf16_f32 v149, v74, v75\n\t" \
  "v_mfma_f32_16x16x32_bf16 v[8:11], a[228:231], v[20:23], v[8:11]\n\t" \
  "ds_read_b64_tr_b16 a[138:139], v211 offset:4096\n\t" \
  "v_cvt_pk_bf16_f32 v150, v76, v77\n\t" \
  "v_mfma_f32_16x16x32_bf16 v[8:11], a[232:235], v[24:27], v[8:11]\n\t" \
  "v_exp_f32 v100, v100\n\t" \
  "v_mfma_f32_16x16x32_bf16 v[8:11], a[236:239], v[28:31], v[8:11]\n\t" \
  "s_waitcnt lgkmcnt(6)\n\t" \
  "ds_read_b64_tr_b16 a[140:141], v211 offset:8192\n\t" \
  "v_cvt_pk_bf16_f32 v151, v78, v79\n\t" \
  "v_mfma_f32_16x16x32_bf16 v[12:15], a[252:255], v[28:31], v[176:179]\n\t" \
  STA("v_writelane_b32 v255, s94, 30\n\t") \
  STA("v_writelane_b32 v255, s92, 31\n\t") \
  "v_exp_f32 v101, v101\n\t" \
  "v_mfma_f32_16x16x32_bf16 v[12:15], a[248:251], v[24:27], v[12:15]\n\t" \
  "ds_read_b64_tr_b16 a[142:143], v211 offset:12288\n\t" \
  "v_cvt_pk_bf16_f32 v152, v96, v97\n\t" \
  "v_mfma_f32_16x16x32_bf16 v[12:15], a[244:247], v[20:23], v[12:15]\n\t" \
  "ds_read_b64_tr_b16 a[144:145], v210 offset:256\n\t" \
  "v_cvt_pk_bf16_f32 v153, v98, v99\n\t" \
  "v_mfma_f32_16x16x32_bf16 v[12:15], a[240:243], v[16:19], v[12:15]\n\t" \
  "v_exp_f32 v102, v102\n\t" \
  "v_mfma_f32_16x16x32_bf16 v[32:35], a[192:195], v[48:51], v[180:183]\n\t" \
  "s_mov_b32 s96, 6144\n\t" \
  "ds_read_b64_tr_b16 a[146:147], v210 offset:4352\n\t" \
  "v_cvt_pk_bf16_f32 v154, v100, v101\n\t" \
  "v_mfma_f32_16x16x32_bf16 v[32:35], a[196:199], v[52:55], v[32:35]\n\t" \
  "v_exp_f32 v103, v103\n\t" \
  "v_mfma_f32_16x16x32_bf16 v[32:35], a[200:203], v[56:59], v[32:35]\n\t" \
  "ds_read_b64_tr_b16 a[148:149], v210 offset:8448\n\t" \
  "v_cvt_pk_bf16_f32 v155, v102, v103\n\t" \
  "v_mfma_f32_16x16x32_bf16 v[32:35], a[204:207], v[60:63], v[32:35]\n\t" \
  "v_exp_f32 v104, v104\n\t" \
  "v_mfma_f32_16x16x32_bf16 v[36:39], a[220:223], v[60:63], v[180:183]\n\t" \
  "buffer_load_dwordx4 v232, s[68:71], s96 offen offset:1024 lds\n\t" \
  "v_mfma_f32_16x16x32_bf16 v[36:39], a[216:219], v[56:59], v[36:39]\n\t" \
  "v_exp_f32 v105, v105\n\t" \
  "v_mfma_f32_16x16x32_bf16 v[36:39], a[212:215], v[52:55], v[36:39]\n\t" \
  "ds_read_b64_tr_b16 a[150:151], v210 offset:12544\n\t" \
  "v_cvt_pk_bf16_f32 v156, v104, v105\n\t" \
  "v_mfma_f32_16x16x32_bf16 v[36:39], a[208:211], v[48:51], v[36:39]\n\t" \
  "v_exp_f32 v106, v106\n\t" \
  "v_mfma_f32_16x16x32_bf16 v[40:43], a[224:227], v[48:51], v[180:183]\n\t" \
  "v_exp_f32 v107, v107\n\t" \
  "v_mfma_f32_16x16x32_bf16 v[40:43], a[228:231], v[52:55], v[40:43]\n\t" \
  "ds_read_b64_tr_b16 a[152:153], v211 offset:256\n\t" \
  "v_cvt_pk_bf16_f32 v157, v106, v107\n\t" \
  "v_mfma_f32_16x16x32_bf16 v[40:43], a[232:235], v[56:59], v[40:43]\n\t" \
  "v_exp_f32 v108, v108\n\t" \
  "v_mfma_f32_16x16x32_bf16 v[40:43], a[236:239], v[60:63], v[40:43]\n\t" \
  "v_exp_f32 v109, v109\n\t" \
  "v_mfma_f32_16x16x32_bf16 v[44:47], a[252:255], v[60:63], v[180:183]\n\t" \
  "ds_read_b64_tr_b16 a[154:155], v211 offset:4352\n\t" \
  "v_cvt_pk_bf16_f32 v158, v108, v109\n\t" \
  "v_mfma_f32_16x16x32_bf16 v[44:47], a[248:251], v[56:59], v[44:47]\n\t" \
  "v_exp_f32 v110, v110\n\t" \
  "v_mfma_f32_16x16x32_bf16 v[44:47], a[244:247], v[52:55], v[44:47]\n\t" \
  "ds_read_b64_tr_b16 a[156:157], v211 offset:8448\n\t" \
  "s_mov_b32 s96, 12288\n\t" \
  "v_mfma_f32_16x16x32_bf16 v[44:47], a[240:243], v[48:51], v[44:47]\n\t" \
  "v_exp_f32 v111, v111\n\t" \
  "v_mfma_f32_16x16x32_bf16 v[64:67], a[192:195], v[80:83], v[184:187]\n\t" \
  "ds_read_b64_tr_b16 a[158:159], v211 offset:12544\n\t" \
  "v_cvt_pk_bf16_f32 v159, v110, v111\n\t" \
  "v_mfma_f32_16x16x32_bf16 v[64:67], a[196:199], v[84:87], v[64:67]\n\t" \
  "ds_read_b64_tr_b16 a[160:161], v210 offset:512\n\t" \
  "v_mfma_f32_16x16x32_bf16 v[64:67], a[200:203], v[88:91], v[64:67]\n\t" \
  "buffer_load_dwordx4 v232, s[68:71], s96 offen offset:2048 lds\n\t" \
  "v_mfma_f32_16x16x32_bf16 v[64:67], a[204:207], v[92:95], v[64:67]\n\t" \
  "ds_read_b64_tr_b16 a[162:163], v210 offset:4608\n\t" \
  "v_mfma_f32_16x16x32_bf16 v[68:71], a[220:223], v[92:95], v[184:187]\n\t" \
  "ds_read_b64_tr_b16 a[164:165], v210 offset:8704\n\t" \
  "v_mfma_f32_16x16x32_bf16 v[68:71], a[216:219], v[88:91], v[68:71]\n\t" \
  "ds_read_b64_tr_b16 a[166:167], v210 offset:12800\n\t" \
  "v_mfma_f32_16x16x32_bf16 v[68:71], a[212:215], v[84:87], v[68:71]\n\t" \
  "v_exp_f32 v0, v0\n\t" \
  "v_mfma_f32_16x16x32_bf16 v[68:71], a[208:211], v[80:83], v[68:71]\n\t" \
  "ds_read_b64_tr_b16 a[168:169], v211 offset:512\n\t" \
  "v_mfma_f32_16x16x32_bf16 v[72:75], a[224:227], v[80:83], v[184:187]\n\t" \
  "v_exp_f32 v1, v1\n\t" \
  "v_mfma_f32_16x16x32_bf16 v[72:75], a[228:231], v[84:87], v[72:75]\n\t" \
  "ds_read_b64_tr_b16 a[170:171], v211 offset:4608\n\t" \
  "v_mfma_f32_16x16x32_bf16 v[72:75], a[232:235], v[88:91], v[72:75]\n\t" \
  "ds_read_b64_tr_b16 a[172:173], v211 offset:8704\n\t" \
  "v_mfma_f32_16x16x32_bf16 v[72:75], a[236:239], v[92:95], v[72:75]\n\t" \
  "v_exp_f32 v2, v2\n\t" \
  "v_mfma_f32_16x16x32_bf16 v[76:79], a[252:255], v[92:95], v[184:187]\n\t" \
  "ds_read_b64_tr_b16 a[174:175], v211 offset:12800\n\t" \
  "s_mov_b32 s96, 18432\n\t" \
  "v_mfma_f32_16x16x32_bf16 v[76:79], a[248:251], v[88:91], v[76:79]\n\t" \
  "ds_read_b64_tr_b16 a[176:177], v210 offset:768\n\t" \
  "v_mfma_f32_16x16x32_bf16 v[76:79], a[244:247], v[84:87], v[76:79]\n\t" \
  "v_exp_f32 v3, v3\n\t" \
  "v_mfma_f32_16x16x32_bf16 v[76:79], a[240:243], v[80:83], v[76:79]\n\t" \
  "ds_read_b64_tr_b16 a[178:179], v210 offset:4864\n\t" \
  "v_mfma_f32_16x16x32_bf16 v[96:99], a[192:195], v[112:115], v[188:191]\n\t" \
  "buffer_load_dwordx4 v232, s[68:71], s96 offen offset:3072 lds\n\t" \
  "v_mfma_f32_16x16x32_bf16 v[96:99], a[196:199], v[116:119], v[96:99]\n\t" \
  "ds_read_b64_tr_b16 a[180:181], v210 offset:8960\n\t" \
  "v_mfma_f32_16x16x32_bf16 v[96:99], a[200:203], v[120:123], v[96:99]\n\t" \
  "ds_read_b64_tr_b16 a[182:183], v210 offset:13056\n\t" \
  "v_mfma_f32_16x16x32_bf16 v[96:99], a[204:207], v[124:127], v[96:99]\n\t" \
  "v_exp_f32 v4, v4\n\t" \
  "v_mfma_f32_16x16x32_bf16 v[100:103], a[220:223], v[124:127], v[188:191]\n\t" \
  "ds_read_b64_tr_b16 a[184:185], v211 offset:768\n\t" \
  "v_mfma_f32_16x16x32_bf16 v[100:103], a[216:219], v[120:123], v[100:103]\n\t" \
  "v_exp_f32 v5, v5\n\t" \
  "v_mfma_f32_16x16x32_bf16 v[100:103], a[212:215], v[116:119], v[100:103]\n\t" \
  "ds_read_b64_tr_b16 a[186:187], v211 offset:4864\n\t" \
  "v_mfma_f32_16x16x32_bf16 v[100:103], a[208:211], v[112:115], v[100:103]\n\t" \
  "ds_read_b64_tr_b16 a[188:189], v211 offset:8960\n\t" \
  "v_mfma_f32_16x16x32_bf16 v[104:107], a[224:227], v[112:115], v[188:191]\n\t" \
  "v_exp_f32 v6, v6\n\t" \
  "v_mfma_f32_16x16x32_bf16 v[104:107], a[228:231], v[116:119], v[104:107]\n\t" \
  "ds_read_b64_tr_b16 a[190:191], v211 offset:13056\n\t" \
  "v_mfma_f32_16x16x32_bf16 v[104:107], a[232:235], v[120:123], v[104:107]\n\t" \
  "v_exp_f32 v7, v7\n\t" \
  "v_mfma_f32_16x16x32_bf16 v[104:107], a[236:239], v[124:127], v[104:107]\n\t" \
  "v_exp_f32 v8, v8\n\t" \
  "v_mfma_f32_16x16x32_bf16 v[108:111], a[252:255], v[124:127], v[188:191]\n\t" \
  "v_exp_f32 v9, v9\n\t" \
  "v_mfma_f32_16x16x32_bf16 v[108:111], a[248:251], v[120:123], v[108:111]\n\t" \
  "v_exp_f32 v10, v10\n\t" \
  "v_mfma_f32_16x16x32_bf16 v[108:111], a[244:247], v[116:119], v[108:111]\n\t" \
  "s_waitcnt vmcnt(4) lgkmcnt(0)\n\t" \
  "v_exp_f32 v11, v11\n\t" \
  "v_mfma_f32_16x16x32_bf16 v[108:111], a[240:243], v[112:115], v[108:111]\n\t" \
  "s_barrier\n\t" \
  STA("s_memrealtime s[94:95]\n\t") \
  STA("s_memtime s[92:93]\n\t") \
  "v_mfma_f32_16x16x32_bf16 a[0:3], a[128:131], v[128:131], a[0:3]\n\t" \
  "buffer_store_dword v233, v247, s[16:19], 0 offen sc1\n\t" \
  "v_mfma_f32_16x16x32_bf16 a[0:3], a[132:135], v[132:135], a[0:3]\n\t" \
  "v_exp_f32 v12, v12\n\t" \
  "v_mfma_f32_16x16x32_bf16 a[4:7], a[136:139], v[128:131], a[4:7]\n\t" \
  "v_exp_f32 v13, v13\n\t" \
  "v_mfma_f32_16x16x32_bf16 a[4:7], a[140:143], v[132:135], a[4:7]\n\t" \
  "v_exp_f32 v14, v14\n\t" \
  "v_mfma_f32_16x16x32_bf16 a[8:11], a[144:147], v[128:131], a[8:11]\n\t" \
  "v_exp_f32 v15, v15\n\t" \
  "v_mfma_f32_16x16x32_bf16 a[8:11], a[148:151], v[132:135], a[8:11]\n\t" \
  "v_exp_f32 v32, v32\n\t" \
  "v_mfma_f32_16x16x32_bf16 a[12:15], a[152:155], v[128:131], a[12:15]\n\t" \
  "v_exp_f32 v33, v33\n\t" \
  "v_mfma_f32_16x16x32_bf16 a[12:15], a[156:159], v[132:135], a[12:15]\n\t" \
  "v_exp_f32 v34, v34\n\t" \
  "v_mfma_f32_16x16x32_bf16 a[16:19], a[160:163], v[128:131], a[16:19]\n\t" \
  "v_exp_f32 v35, v35\n\t" \
  "v_mfma_f32_16x16x32_bf16 a[16:19], a[164:167], v[132:135], a[16:19]\n\t" \
  "v_exp_f32 v36, v36\n\t" \
  "v_mfma_f32_16x16x32_bf16 a[20:23], a[168:171], v[128:131], a[20:23]\n\t" \
  "v_exp_f32 v37, v37\n\t" \
  "v_mfma_f32_16x16x32_bf16 a[20:23], a[172:175], v[132:135], a[20:23]\n\t" \
  "v_exp_f32 v38, v38\n\t" \
  "v_mfma_f32_16x16x32_bf16 a[24:27], a[176:179], v[128:131], a[24:27]\n\t" \
  STA("v_writelane_b32 v255, s94, 32\n\t") \
  STA("v_writelane_b32 v255, s92, 33\n\t") \
  "v_exp_f32 v39, v39\n\t" \
  "v_mfma_f32_16x16x32_bf16 a[24:27], a[180:183], v[132:135], a[24:27]\n\t" \
  "v_exp_f32 v40, v40\n\t" \
  "v_mfma_f32_16x16x32_bf16 a[28:31], a[184:187], v[128:131], a[28:31]\n\t" \
  "v_exp_f32 v41, v41\n\t" \
  "v_mfma_f32_16x16x32_bf16 a[28:31], a[188:191], v[132:135], a[28:31]\n\t" \
  "v_exp_f32 v42, v42\n\t" \
  "v_smfmac_f32_16x16x64_bf16 v[160:163], v[216:219], v[128:135], v208\n\t" \
  "ds_read_b128 a[192:195], v220 offset:16384\n\t" \
  "v_cvt_pk_bf16_f32 v128, v0, v1\n\t" \
  "v_mfma_f32_16x16x32_bf16 a[32:35], a[128:131], v[136:139], a[32:35]\n\t" \
  "ds_read_b128 a[196:199], v221 offset:16384\n\t" \
  "v_cvt_pk_bf16_f32 v129, v2, v3\n\t" \
  "v_mfma_f32_16x16x32_bf16 a[32:35], a[132:135], v[140:143], a[32:35]\n\t" \
  "s_add_u32 m0, s38, 32768\n\t" \
  "v_exp_f32 v43, v43\n\t" \
  "v_mfma_f32_16x16x32_bf16 a[36:39], a[136:139], v[136:139], a[36:39]\n\t" \
  "s_mov_b32 s96, 28672\n\t" \
  "v_exp_f32 v44, v44\n\t" \
  "v_mfma_f32_16x16x32_bf16 a[36:39], a[140:143], v[140:143], a[36:39]\n\t" \
  "ds_read_b128 a[200:203], v222 offset:16384\n\t" \
  "v_cvt_pk_bf16_f32 v130, v4, v5\n\t" \
  "v_mfma_f32_16x16x32_bf16 a[40:43], a[144:147], v[136:139], a[40:43]\n\t" \
  "v_exp_f32 v45, v45\n\t" \
  "v_mfma_f32_16x16x32_bf16 a[40:43], a[148:151], v[140:143], a[40:43]\n\t" \
  "buffer_load_dwordx4 v232, s[68:71], s96 offen offset:0 lds\n\t" \
  "v_mfma_f32_16x16x32_bf16 a[44:47], a[152:155], v[136:139], a[44:47]\n\t" \
  "ds_read_b128 a[204:207], v223 offset:16384\n\t" \
  "v_cvt_pk_bf16_f32 v131, v6, v7\n\t" \
  "v_mfma_f32_16x16x32_bf16 a[44:47], a[156:159], v[140:143], a[44:47]\n\t" \
  "v_exp_f32 v46, v46\n\t" \
  "v_mfma_f32_16x16x32_bf16 a[48:51], a[160:163], v[136:139], a[48:51]\n\t" \
  "ds_read_b128 a[208:211], v220 offset:20480\n\t" \
  "v_cvt_pk_bf16_f32 v132, v8, v9\n\t" \
  "v_mfma_f32_16x16x32_bf16 a[48:51], a[164:167], v[140:143], a[48:51]\n\t" \
  "v_exp_f32 v47, v47\n\t" \
  "v_mfma_f32_16x16x32_bf16 a[52:55], a[168:171], v[136:139], a[52:55]\n\t" \
  "v_exp_f32 v64, v64\n\t" \
  "v_mfma_f32_16x16x32_bf16 a[52:55], a[172:175], v[140:143], a[52:55]\n\t" \
  "ds_read_b128 a[212:215], v221 offset:20480\n\t" \
  "v_cvt_pk_bf16_f32 v133, v10, v11\n\t" \
  "v_mfma_f32_16x16x32_bf16 a[56:59], a[176:179], v[136:139], a[56:59]\n\t" \
  "v_exp_f32 v65, v65\n\t" \
  "v_mfma_f32_16x16x32_bf16 a[56:59], a[180:183], v[140:143], a[56:59]\n\t" \
  "s_mov_b32 s96, 34816\n\t" \
  "v_exp_f32 v66, v66\n\t" \
  "v_mfma_f32_16x16x32_bf16 a[60:63], a[184:187], v[136:139], a[60:63]\n\t" \
  "ds_read_b128 a[216:219], v222 offset:20480\n\t" \
  "v_cvt_pk_bf16_f32 v134, v12, v13\n\t" \
  "v_mfma_f32_16x16x32_bf16 a[60:63], a[188:191], v[140:143], a[60:63]\n\t" \
  "v_exp_f32 v67, v67\n\t" \
  "v_smfmac_f32_16x16x64_bf16 v[164:167], v[216:219], v[136:143], v208\n\t" \
  "ds_read_b128 a[220:223], v223 offset:20480\n\t" \
  "v_cvt_pk_bf16_f32 v135, v14, v15\n\t" \
  "v_mfma_f32_16x16x32_bf16 a[64:67], a[128:131], v[144:147], a[64:67]\n\t" \
  "buffer_load_dwordx4 v232, s[68:71], s96 offen offset:1024 lds\n\t" \
  "v_mfma_f32_16x16x32_bf16 a[64:67], a[132:135], v[148:151], a[64:67]\n\t" \
  "v_exp_f32 v68, v68\n\t" \
  "v_mfma_f32_16x16x32_bf16 a[68:71], a[136:139], v[144:147], a[68:71]\n\t" \
  "ds_read_b128 a[224:227], v220 offset:24576\n\t" \
  "v_cvt_pk_bf16_f32 v136, v32, v33\n\t" \
  "v_mfma_f32_16x16x32_bf16 a[68:71], a[140:143], v[148:151], a[68:71]\n\t" \
  "v_exp_f32 v69, v69\n\t" \
  "v_mfma_f32_16x16x32_bf16 a[72:75], a[144:147], v[144:147], a[72:75]\n\t" \
  "s_add_u32 m0, s38, 49152\n\t" \
  "v_exp_f32 v70, v70\n\t" \
  "v_mfma_f32_16x16x32_bf16 a[72:75], a[148:151], v[148:151], a[72:75]\n\t" \
  "s_add_u32 s96, s38, 65536\n\t" \
  "ds_read_b128 a[228:231], v221 offset:24576\n\t" \
  "v_cvt_pk_bf16_f32 v137, v34, v35\n\t" \
  "v_mfma_f32_16x16x32_bf16 a[76:79], a[152:155], v[144:147], a[76:79]\n\t" \
  "v_exp_f32 v71, v71\n\t" \
  "v_mfma_f32_16x16x32_bf16 a[76:79], a[156:159], v[148:151], a[76:79]\n\t" \
  "v_exp_f32 v72, v72\n\t" \
  "v_mfma_f32_16x16x32_bf16 a[80:83], a[160:163], v[144:147], a[80:83]\n\t" \
  "buffer_load_dwordx4 v224, s[60:63], s96 offen offset:0 lds\n\t" \
  "v_mfma_f32_16x16x32_bf16 a[80:83], a[164:167], v[148:151], a[80:83]\n\t" \
  "ds_read_b128 a[232:235], v222 offset:24576\n\t" \
  "v_cvt_pk_bf16_f32 v138, v36, v37\n\t" \
  "v_mfma_f32_16x16x32_bf16 a[84:87], a[168:171], v[144:147], a[84:87]\n\t" \
  "v_exp_f32 v73, v73\n\t" \
  "v_mfma_f32_16x16x32_bf16 a[84:87], a[172:175], v[148:151], a[84:87]\n\t" \
  "buffer_load_dwordx4 v225, s[60:63], s96 offen offset:1024 lds\n\t" \
  "v_mfma_f32_16x16x32_bf16 a[88:91], a[176:179], v[144:147], a[88:91]\n\t" \
  "ds_read_b128 a[236:239], v223 offset:24576\n\t" \
  "v_cvt_pk_bf16_f32 v139, v38, v39\n\t" \
  "v_mfma_f32_16x16x32_bf16 a[88:91], a[180:183], v[148:151], a[88:91]\n\t" \
  "ds_read_b128 a[240:243], v220 offset:28672\n\t" \
  "v_cvt_pk_bf16_f32 v140, v40, v41\n\t" \
  "v_mfma_f32_16x16x32_bf16 a[92:95], a[184:187], v[144:147], a[92:95]\n\t" \
  "buffer_load_dwordx4 v226, s[60:63], s96 offen offset:2048 lds\n\t" \
  "v_mfma_f32_16x16x32_bf16 a[92:95], a[188:191], v[148:151], a[92:95]\n\t" \
  "v_exp_f32 v74, v74\n\t" \
  "v_smfmac_f32_16x16x64_bf16 v[168:171], v[216:219], v[144:151], v208\n\t" \
  "ds_read_b128 a[244:247], v221 offset:28672\n\t" \
  "v_cvt_pk_bf16_f32 v141, v42, v43\n\t" \
  "v_mfma_f32_16x16x32_bf16 a[96:99], a[128:131], v[152:155], a[96:99]\n\t" \
  "buffer_load_dwordx4 v227, s[60:63], s96 offen offset:3072 lds\n\t" \
  "v_mfma_f32_16x16x32_bf16 a[96:99], a[132:135], v[156:159], a[96:99]\n\t" \
  "s_add_u32 m0, s38, 147456\n\t" \
  "s_add_u32 s96, s38, 49152\n\t" \
  "v_cvt_pk_bf16_f32 v142, v44, v45\n\t" \
  "v_mfma_f32_16x16x32_bf16 a[100:103], a[136:139], v[152:155], a[100:103]\n\t" \
  "buffer_load_dwordx4 v230, s[64:67], s96 offen offset:0 lds\n\t" \
  "v_mfma_f32_16x16x32_bf16 a[100:103], a[140:143], v[156:159], a[100:103]\n\t" \
  "ds_read_b128 a[248:251], v222 offset:28672\n\t" \
  "v_cvt_pk_bf16_f32 v143, v46, v47\n\t" \
  "v_mfma_f32_16x16x32_bf16 a[104:107], a[144:147], v[152:155], a[104:107]\n\t" \
  "v_exp_f32 v75, v75\n\t" \
  "v_mfma_f32_16x16x32_bf16 a[104:107], a[148:151], v[156:159], a[104:107]\n\t" \
  "buffer_load_dwordx4 v231, s[64:67], s96 offen offset:1024 lds\n\t" \
  "v_mfma_f32_16x16x32_bf16 a[108:111], a[152:155], v[152:155], a[108:111]\n\t" \
  "ds_read_b128 a[252:255], v223 offset:28672\n\t" \
  "v_cvt_pk_bf16_f32 v144, v64, v65\n\t" \
  "v_mfma_f32_16x16x32_bf16 a[108:111], a[156:159], v[156:159], a[108:111]\n\t" \
  "v_exp_f32 v76, v76\n\t" \
  "v_mfma_f32_16x16x32_bf16 a[112:115], a[160:163], v[152:155], a[112:115]\n\t" \
  "buffer_load_dwordx4 v230, s[64:67], s96 offen offset:2048 lds\n\t" \
  "v_mfma_f32_16x16x32_bf16 a[112:115], a[164:167], v[156:159], a[112:115]\n\t" \
  "v_exp_f32 v77, v77\n\t" \
  "v_mfma_f32_16x16x32_bf16 a[116:119], a[168:171], v[152:155], a[116:119]\n\t" \
  "v_exp_f32 v78, v78\n\t" \
  "v_mfma_f32_16x16x32_bf16 a[116:119], a[172:175], v[156:159], a[116:119]\n\t" \
  "buffer_load_dwordx4 v231, s[64:67], s96 offen offset:3072 lds\n\t" \
  "v_mfma_f32_16x16x32_bf16 a[120:123], a[176:179], v[152:155], a[120:123]\n\t" \
  "v_exp_f32 v79, v79\n\t" \
  "v_mfma_f32_16x16x32_bf16 a[120:123], a[180:183], v[156:159], a[120:123]\n\t" \
  "v_exp_f32 v96, v96\n\t" \
  "v_mfma_f32_16x16x32_bf16 a[124:127], a[184:187], v[152:155], a[124:127]\n\t" \
  "v_exp_f32 v97, v97\n\t" \
  "v_mfma_f32_16x16x32_bf16 a[124:127], a[188:191], v[156:159], a[124:127]\n\t" \
  "v_cvt_pk_bf16_f32 v145, v66, v67\n\t" \
  "v_cvt_pk_bf16_f32 v146, v68, v69\n\t" \
  "v_smfmac_f32_16x16x64_bf16 v[172:175], v[216:219], v[152:159], v208\n\t" \
  ".Lhs7:\n\ts_cbranch_execz .Lhs7n\n\t" \
  "s_waitcnt lgkmcnt(12)\n\t" \
  "v_cvt_pk_bf16_f32 v147, v70, v71\n\t" \
  STA("s_memrealtime s[94:95]\n\t") \
  STA("s_memtime s[92:93]\n\t") \
  "v_mfma_f32_16x16x32_bf16 v[0:3], a[192:195], v[16:19], v[176:179]\n\t" \
  "ds_read_b64_tr_b16 a[128:129], v228 offset:0\n\t" \
  "v_cvt_pk_bf16_f32 v148, v72, v73\n\t" \
  "v_mfma_f32_16x16x32_bf16 v[0:3], a[196:199], v[20:23], v[0:3]\n\t" \
  "v_exp_f32 v98, v98\n\t" \
  "v_mfma_f32_16x16x32_bf16 v[0:3], a[200:203], v[24:27], v[0:3]\n\t" \
  "s_add_u32 m0, s38, 32768\n\t" \
  "ds_read_b64_tr_b16 a[130:131], v228 offset:4096\n\t" \
  "v_cvt_pk_bf16_f32 v149, v74, v75\n\t" \
  "v_mfma_f32_16x16x32_bf16 v[0:3], a[204:207], v[28:31], v[0:3]\n\t" \
  "s_waitcnt lgkmcnt(10)\n\t" \
  "v_exp_f32 v99, v99\n\t" \
  "v_mfma_f32_16x16x32_bf16 v[4:7], a[220:223], v[28:31], v[176:179]\n\t" \
  "s_mov_b32 s96, 40960\n\t" \
  "ds_read_b64_tr_b16 a[132:133], v228 offset:8192\n\t" \
  "v_cvt_pk_bf16_f32 v150, v76, v77\n\t" \
  "v_mfma_f32_16x16x32_bf16 v[4:7], a[216:219], v[24:27], v[4:7]\n\t" \
  "v_exp_f32 v100, v100\n\t" \
  "v_mfma_f32_16x16x32_bf16 v[4:7], a[212:215], v[20:23], v[4:7]\n\t" \
  "buffer_load_dwordx4 v232, s[68:71], s96 offen offset:2048 lds\n\t" \
  "v_mfma_f32_16x16x32_bf16 v[4:7], a[208:211], v[16:19], v[4:7]\n\t" \
  "s_waitcnt lgkmcnt(7)\n\t" \
  "ds_read_b64_tr_b16 a[134:135], v228 offset:12288\n\t" \
  "v_cvt_pk_bf16_f32 v151, v78, v79\n\t" \
  "v_mfma_f32_16x16x32_bf16 v[8:11], a[224:227], v[16:19], v[176:179]\n\t" \
  "ds_read_b64_tr_b16 a[136:137], v229 offset:0\n\t" \
  "v_cvt_pk_bf16_f32 v152, v96, v97\n\t" \
  "v_mfma_f32_16x16x32_bf16 v[8:11], a[228:231], v[20:23], v[8:11]\n\t" \
  "ds_read_b64_tr_b16 a[138:139], v229 offset:4096\n\t" \
  "v_cvt_pk_bf16_f32 v153, v98, v99\n\t" \
  "v_mfma_f32_16x16x32_bf16 v[8:11], a[232:235], v[24:27], v[8:11]\n\t" \
  "v_exp_f32 v101, v101\n\t" \
  "v_mfma_f32_16x16x32_bf16 v[8:11], a[236:239], v[28:31], v[8:11]\n\t" \
  "s_waitcnt lgkmcnt(6)\n\t" \
  "ds_read_b64_tr_b16 a[140:141], v229 offset:8192\n\t" \
  "v_cvt_pk_bf16_f32 v154, v100, v101\n\t" \
  "v_mfma_f32_16x16x32_bf16 v[12:15], a[252:255], v[28:31], v[176:179]\n\t" \
  STA("v_writelane_b32 v255, s94, 34\n\t") \
  STA("v_writelane_b32 v255, s92, 35\n\t") \
  "v_exp_f32 v102, v102\n\t" \
  "v_mfma_f32_16x16x32_bf16 v[12:15], a[248:251], v[24:27], v[12:15]\n\t" \
  "v_exp_f32 v103, v103\n\t" \
  "v_mfma_f32_16x16x32_bf16 v[12:15], a[244:247], v[20:23], v[12:15]\n\t" \
  "ds_read_b64_tr_b16 a[142:143], v229 offset:12288\n\t" \
  "v_cvt_pk_bf16_f32 v155, v102, v103\n\t" \
  "v_mfma_f32_16x16x32_bf16 v[12:15], a[240:243], v[16:19], v[12:15]\n\t" \
  "v_exp_f32 v104, v104\n\t" \
  "v_mfma_f32_16x16x32_bf16 v[32:35], a[192:195], v[48:51], v[180:183]\n\t" \
  "s_mov_b32 s96, 47104\n\t" \
  "v_exp_f32 v105, v105\n\t" \
  "v_mfma_f32_16x16x32_bf16 v[32:35], a[196:199], v[52:55], v[32:35]\n\t" \
  "ds_read_b64_tr_b16 a[144:145], v228 offset:256\n\t" \
  "v_cvt_pk_bf16_f32 v156, v104, v105\n\t" \
  "v_mfma_f32_16x16x32_bf16 v[32:35], a[200:203], v[56:59], v[32:35]\n\t" \
  "v_exp_f32 v106, v106\n\t" \
  "v_mfma_f32_16x16x32_bf16 v[32:35], a[204:207], v[60:63], v[32:35]\n\t" \
  "v_exp_f32 v107, v107\n\t" \
  "v_mfma_f32_16x16x32_bf16 v[36:39], a[220:223], v[60:63], v[180:183]\n\t" \
  "buffer_load_dwordx4 v232, s[68:71], s96 offen offset:3072 lds\n\t" \
  "v_mfma_f32_16x16x32_bf16 v[36:39], a[216:219], v[56:59], v[36:39]\n\t" \
  "ds_read_b64_tr_b16 a[146:147], v228 offset:4352\n\t" \
  "v_cvt_pk_bf16_f32 v157, v106, v107\n\t" \
  "v_mfma_f32_16x16x32_bf16 v[36:39], a[212:215], v[52:55], v[36:39]\n\t" \
  "v_exp_f32 v108, v108\n\t" \
  "v_mfma_f32_16x16x32_bf16 v[36:39], a[208:211], v[48:51], v[36:39]\n\t" \
  "ds_read_b64_tr_b16 a[148:149], v228 offset:8448\n\t" \
  "v_mfma_f32_16x16x32_bf16 v[40:43], a[224:227], v[48:51], v[180:183]\n\t" \
  "ds_read_b64_tr_b16 a[150:151], v228 offset:12544\n\t" \
  "v_mfma_f32_16x16x32_bf16 v[40:43], a[228:231], v[52:55], v[40:43]\n\t" \
  "v_exp_f32 v109, v109\n\t" \
  "v_mfma_f32_16x16x32_bf16 v[40:43], a[232:235], v[56:59], v[40:43]\n\t" \
  "ds_read_b64_tr_b16 a[152:153], v229 offset:256\n\t" \
  "v_cvt_pk_bf16_f32 v158, v108, v109\n\t" \
  "v_mfma_f32_16x16x32_bf16 v[40:43], a[236:239], v[60:63], v[40:43]\n\t" \
  "v_exp_f32 v110, v110\n\t" \
  "v_mfma_f32_16x16x32_bf16 v[44:47], a[252:255], v[60:63], v[180:183]\n\t" \
  "ds_read_b64_tr_b16 a[154:155], v229 offset:4352\n\t" \
  "v_mfma_f32_16x16x32_bf16 v[44:47], a[248:251], v[56:59], v[44:47]\n\t" \
  "v_exp_f32 v111, v111\n\t" \
  "v_mfma_f32_16x16x32_bf16 v[44:47], a[244:247], v[52:55], v[44:47]\n\t" \
  "s_add_u32 m0, s38, 114688\n\t" \
  "ds_read_b64_tr_b16 a[156:157], v229 offset:8448\n\t" \
  "v_cvt_pk_bf16_f32 v159, v110, v111\n\t" \
  "v_mfma_f32_16x16x32_bf16 v[44:47], a[240:243], v[48:51], v[44:47]\n\t" \
  "ds_read_b64_tr_b16 a[158:159], v229 offset:12544\n\t" \
  "s_mov_b32 s96, 0\n\t" \
  "v_mfma_f32_16x16x32_bf16 v[64:67], a[192:195], v[80:83], v[184:187]\n\t" \
  "ds_read_b64_tr_b16 a[160:161], v228 offset:512\n\t" \
  "v_mfma_f32_16x16x32_bf16 v[64:67], a[196:199], v[84:87], v[64:67]\n\t" \
  "v_exp_f32 v0, v0\n\t" \
  "v_mfma_f32_16x16x32_bf16 v[64:67], a[200:203], v[88:91], v[64:67]\n\t" \
  "buffer_load_dwordx4 v232, s[72:75], s96 offen offset:0 lds\n\t" \
  "v_mfma_f32_16x16x32_bf16 v[64:67], a[204:207], v[92:95], v[64:67]\n\t" \
  "ds_read_b64_tr_b16 a[162:163], v228 offset:4608\n\t" \
  "v_mfma_f32_16x16x32_bf16 v[68:71], a[220:223], v[92:95], v[184:187]\n\t" \
  "ds_read_b64_tr_b16 a[164:165], v228 offset:8704\n\t" \
  "v_mfma_f32_16x16x32_bf16 v[68:71], a[216:219], v[88:91], v[68:71]\n\t" \
  "ds_read_b64_tr_b16 a[166:167], v228 offset:12800\n\t" \
  "v_mfma_f32_16x16x32_bf16 v[68:71], a[212:215], v[84:87], v[68:71]\n\t" \
  "v_exp_f32 v1, v1\n\t" \
  "v_mfma_f32_16x16x32_bf16 v[68:71], a[208:211], v[80:83], v[68:71]\n\t" \
  "ds_read_b64_tr_b16 a[168:169], v229 offset:512\n\t" \
  "v_mfma_f32_16x16x32_bf16 v[72:75], a[224:227], v[80:83], v[184:187]\n\t" \
  "v_exp_f32 v2, v2\n\t" \
  "v_mfma_f32_16x16x32_bf16 v[72:75], a[228:231], v[84:87], v[72:75]\n\t" \
  "ds_read_b64_tr_b16 a[170:171], v229 offset:4608\n\t" \
  "v_mfma_f32_16x16x32_bf16 v[72:75], a[232:235], v[88:91], v[72:75]\n\t" \
  "ds_read_b64_tr_b16 a[172:173], v229 offset:8704\n\t" \
  "v_mfma_f32_16x16x32_bf16 v[72:75], a[236:239], v[92:95], v[72:75]\n\t" \
  "ds_read_b64_tr_b16 a[174:175], v229 offset:12800\n\t" \
  "buffer_load_dword v246, v247, s[16:19], 0 offen sc1\n\t" \
  "v_mfma_f32_16x16x32_bf16 v[76:79], a[252:255], v[92:95], v[184:187]\n\t" \
  "s_mov_b32 s96, 6144\n\t" \
  "v_exp_f32 v3, v3\n\t" \
  "v_mfma_f32_16x16x32_bf16 v[76:79], a[248:251], v[88:91], v[76:79]\n\t" \
  "v_exp_f32 v4, v4\n\t" \
  "v_mfma_f32_16x16x32_bf16 v[76:79], a[244:247], v[84:87], v[76:79]\n\t" \
  "ds_read_b64_tr_b16 a[176:177], v228 offset:768\n\t" \
  "v_mfma_f32_16x16x32_bf16 v[76:79], a[240:243], v[80:83], v[76:79]\n\t" \
  "ds_read_b64_tr_b16 a[178:179], v228 offset:4864\n\t" \
  "v_mfma_f32_16x16x32_bf16 v[96:99], a[192:195], v[112:115], v[188:191]\n\t" \
  "buffer_load_dwordx4 v232, s[72:75], s96 offen offset:1024 lds\n\t" \
  "v_mfma_f32_16x16x32_bf16 v[96:99], a[196:199], v[116:119], v[96:99]\n\t" \
  "ds_read_b64_tr_b16 a[180:181], v228 offset:8960\n\t" \
  "v_mfma_f32_16x16x32_bf16 v[96:99], a[200:203], v[120:123], v[96:99]\n\t" \
  "v_exp_f32 v5, v5\n\t" \
  "v_mfma_f32_16x16x32_bf16 v[96:99], a[204:207], v[124:127], v[96:99]\n\t" \
  "ds_read_b64_tr_b16 a[182:183], v228 offset:13056\n\t" \
  "v_mfma_f32_16x16x32_bf16 v[100:103], a[220:223], v[124:127], v[188:191]\n\t" \
  "ds_read_b64_tr_b16 a[184:185], v229 offset:768\n\t" \
  "v_mfma_f32_16x16x32_bf16 v[100:103], a[216:219], v[120:123], v[100:103]\n\t" \
  "v_exp_f32 v6, v6\n\t" \
  "v_mfma_f32_16x16x32_bf16 v[100:103], a[212:215], v[116:119], v[100:103]\n\t" \
  "ds_read_b64_tr_b16 a[186:187], v229 offset:4864\n\t" \
  "v_mfma_f32_16x16x32_bf16 v[100:103], a[208:211], v[112:115], v[100:103]\n\t" \
  "ds_read_b64_tr_b16 a[188:189], v229 offset:8960\n\t" \
  "v_mfma_f32_16x16x32_bf16 v[104:107], a[224:227], v[112:115], v[188:191]\n\t" \
  "v_exp_f32 v7, v7\n\t" \
  "v_mfma_f32_16x16x32_bf16 v[104:107], a[228:231], v[116:119], v[104:107]\n\t" \
  "ds_read_b64_tr_b16 a[190:191], v229 offset:13056\n\t" \
  "v_mfma_f32_16x16x32_bf16 v[104:107], a[232:235], v[120:123], v[104:107]\n\t" \
  "v_exp_f32 v8, v8\n\t" \
  "v_mfma_f32_16x16x32_bf16 v[104:107], a[236:239], v[124:127], v[104:107]\n\t" \
  "v_exp_f32 v9, v9\n\t" \
  "v_mfma_f32_16x16x32_bf16 v[108:111], a[252:255], v[124:127], v[188:191]\n\t" \
  "v_exp_f32 v10, v10\n\t" \
  "v_mfma_f32_16x16x32_bf16 v[108:111], a[248:251], v[120:123], v[108:111]\n\t" \
  "v_exp_f32 v11, v11\n\t" \
  "v_mfma_f32_16x16x32_bf16 v[108:111], a[244:247], v[116:119], v[108:111]\n\t" \
  "s_waitcnt vmcnt(5) lgkmcnt(0)\n\t" \
  "v_exp_f32 v12, v12\n\t" \
  "v_mfma_f32_16x16x32_bf16 v[108:111], a[240:243], v[112:115], v[108:111]\n\t" \
  "s_barrier\n\t" \
  STA("s_memrealtime s[94:95]\n\t") \
  STA("s_memtime s[92:93]\n\t") \
  "v_mfma_f32_16x16x32_bf16 a[0:3], a[128:131], v[128:131], a[0:3]\n\t" \
  "v_exp_f32 v13, v13\n\t" \
  "v_mfma_f32_16x16x32_bf16 a[0:3], a[132:135], v[132:135], a[0:3]\n\t" \
  "v_exp_f32 v14, v14\n\t" \
  "v_mfma_f32_16x16x32_bf16 a[4:7], a[136:139], v[128:131], a[4:7]\n\t" \
  "v_exp_f32 v15, v15\n\t" \
  "v_mfma_f32_16x16x32_bf16 a[4:7], a[140:143], v[132:135], a[4:7]\n\t" \
  "v_exp_f32 v32, v32\n\t" \
  "v_mfma_f32_16x16x32_bf16 a[8:11], a[144:147], v[128:131], a[8:11]\n\t" \
  "v_exp_f32 v33, v33\n\t" \
  "v_mfma_f32_16x16x32_bf16 a[8:11], a[148:151], v[132:135], a[8:11]\n\t" \
  "v_exp_f32 v34, v34\n\t" \
  "v_mfma_f32_16x16x32_bf16 a[12:15], a[152:155], v[128:131], a[12:15]\n\t" \
  "s_getreg_b32 s98, hwreg(HW_REG_IB_STS, 0, 4)\n\t" \
  "s_cmp_lg_u32 s98, 0\n\t" \
  "s_addc_u32 s54, s54, 0\n\t" \
  "v_mfma_f32_16x16x32_bf16 a[12:15], a[156:159], v[132:135], a[12:15]\n\t" \
  "v_exp_f32 v35, v35\n\t" \
  "v_mfma_f32_16x16x32_bf16 a[16:19], a[160:163], v[128:131], a[16:19]\n\t" \
  "s_getreg_b32 s98, hwreg(HW_REG_IB_STS, 0, 4)\n\t" \
  "s_cmp_lg_u32 s98, 0\n\t" \
  "s_addc_u32 s54, s54, 0\n\t" \
  "v_mfma_f32_16x16x32_bf16 a[16:19], a[164:167], v[132:135], a[16:19]\n\t" \
  "v_exp_f32 v36, v36\n\t" \
  "v_mfma_f32_16x16x32_bf16 a[20:23], a[168:171], v[128:131], a[20:23]\n\t" \
  "s_getreg_b32 s98, hwreg(HW_REG_IB_STS, 0, 4)\n\t" \
  "s_cmp_lg_u32 s98, 0\n\t" \
  "s_addc_u32 s54, s54, 0\n\t" \
  "v_mfma_f32_16x16x32_bf16 a[20:23], a[172:175], v[132:135], a[20:23]\n\t" \
  "v_exp_f32 v37, v37\n\t" \
  "v_mfma_f32_16x16x32_bf16 a[24:27], a[176:179], v[128:131], a[24:27]\n\t" \
  STA("v_writelane_b32 v255, s94, 36\n\t") \
  STA("v_writelane_b32 v255, s92, 37\n\t") \
  "s_getreg_b32 s98, hwreg(HW_REG_IB_STS, 0, 4)\n\t" \
  "s_cmp_lg_u32 s98, 0\n\t" \
  "s_addc_u32 s54, s54, 0\n\t" \
  "v_mfma_f32_16x16x32_bf16 a[24:27], a[180:183], v[132:135], a[24:27]\n\t" \
  "v_exp_f32 v38, v38\n\t" \
  "v_mfma_f32_16x16x32_bf16 a[28:31], a[184:187], v[128:131], a[28:31]\n\t" \
  "v_exp_f32 v39, v39\n\t" \
  "v_mfma_f32_16x16x32_bf16 a[28:31], a[188:191], v[132:135], a[28:31]\n\t" \
  "v_exp_f32 v40, v40\n\t" \
  "v_smfmac_f32_16x16x64_bf16 v[160:163], v[216:219], v[128:135], v208\n\t" \
  "ds_read_b128 a[192:195], v220 offset:0\n\t" \
  "v_cvt_pk_bf16_f32 v128, v0, v1\n\t" \
  "v_mfma_f32_16x16x32_bf16 a[32:35], a[128:131], v[136:139], a[32:35]\n\t" \
  "ds_read_b128 a[196:199], v221 offset:0\n\t" \
  "v_cvt_pk_bf16_f32 v129, v2, v3\n\t" \
  "v_mfma_f32_16x16x32_bf16 a[32:35], a[132:135], v[140:143], a[32:35]\n\t" \
  "s_add_u32 m0, s38, 114688\n\t" \
  "v_exp_f32 v41, v41\n\t" \
  "v_mfma_f32_16x16x32_bf16 a[36:39], a[136:139], v[136:139], a[36:39]\n\t" \
  "s_mov_b32 s96, 12288\n\t" \
  "v_exp_f32 v42, v42\n\t" \
  "v_mfma_f32_16x16x32_bf16 a[36:39], a[140:143], v[140:143], a[36:39]\n\t" \
  "ds_read_b128 a[200:203], v222 offset:0\n\t" \
  "v_cvt_pk_bf16_f32 v130, v4, v5\n\t" \
  "v_mfma_f32_16x16x32_bf16 a[40:43], a[144:147], v[136:139], a[40:43]\n\t" \
  "v_exp_f32 v43, v43\n\t" \
  "v_mfma_f32_16x16x32_bf16 a[40:43], a[148:151], v[140:143], a[40:43]\n\t" \
  "buffer_load_dwordx4 v232, s[72:75], s96 offen offset:2048 lds\n\t" \
  "v_mfma_f32_16x16x32_bf16 a[44:47], a[152:155], v[136:139], a[44:47]\n\t" \
  "ds_read_b128 a[204:207], v223 offset:0\n\t" \
  "v_cvt_pk_bf16_f32 v131, v6, v7\n\t" \
  "v_mfma_f32_16x16x32_bf16 a[44:47], a[156:159], v[140:143], a[44:47]\n\t" \
  "v_exp_f32 v44, v44\n\t" \
  "v_mfma_f32_16x16x32_bf16 a[48:51], a[160:163], v[136:139], a[48:51]\n\t" \
  "ds_read_b128 a[208:211], v220 offset:4096\n\t" \
  "v_cvt_pk_bf16_f32 v132, v8, v9\n\t" \
  "v_mfma_f32_16x16x32_bf16 a[48:51], a[164:167], v[140:143], a[48:51]\n\t" \
  STS("v_writelane_b32 v255, s54, 58\n\t") \
  "s_cmp_ge_u32 s54, 2\n\t" \
  "s_cselect_b32 s98, 0x4000, 0\n\t" \
  "s_or_b32 s98, s98, s36\n\t" \
  "v_mfma_f32_16x16x32_bf16 a[52:55], a[168:171], v[136:139], a[52:55]\n\t" \
  "s_cmp_ge_u32 s54, 3\n\t" \
  "s_cselect_b32 s54, 2.0, 0\n\t" \
  "s_or_b32 s98, s98, s54\n\t" \
  "v_mfma_f32_16x16x32_bf16 a[52:55], a[172:175], v[140:143], a[52:55]\n\t" \
  "ds_read_b128 a[212:215], v221 offset:4096\n\t" \
  "v_mov_b32 v209, s98\n\t" \
  "v_mfma_f32_16x16x32_bf16 a[56:59], a[176:179], v[136:139], a[56:59]\n\t" \
  "v_exp_f32 v45, v45\n\t" \
  "v_mfma_f32_16x16x32_bf16 a[56:59], a[180:183], v[140:143], a[56:59]\n\t" \
  "s_mov_b32 s96, 18432\n\t" \
  "v_exp_f32 v46, v46\n\t" \
  "v_mfma_f32_16x16x32_bf16 a[60:63], a[184:187], v[136:139], a[60:63]\n\t" \
  "ds_read_b128 a[216:219], v222 offset:4096\n\t" \
  "v_cvt_pk_bf16_f32 v133, v10, v11\n\t" \
  "v_mfma_f32_16x16x32_bf16 a[60:63], a[188:191], v[140:143], a[60:63]\n\t" \
  "buffer_store_dword v209, v250, s[16:19], 0 offen sc1\n\t" \
  "ds_read_b128 a[220:223], v223 offset:4096\n\t" \
  "v_smfmac_f32_16x16x64_bf16 v[164:167], v[216:219], v[136:143], v208\n\t" \
  "ds_read_b128 a[224:227], v220 offset:8192\n\t" \
  "v_cvt_pk_bf16_f32 v134, v12, v13\n\t" \
  "v_mfma_f32_16x16x32_bf16 a[64:67], a[128:131], v[144:147], a[64:67]\n\t" \
  "buffer_load_dwordx4 v232, s[72:75], s96 offen offset:3072 lds\n\t" \
  "v_mfma_f32_16x16x32_bf16 a[64:67], a[132:135], v[148:151], a[64:67]\n\t" \
  "v_exp_f32 v47, v47\n\t" \
  "v_mfma_f32_16x16x32_bf16 a[68:71], a[136:139], v[144:147], a[68:71]\n\t" \
  "v_exp_f32 v64, v64\n\t" \
  "v_mfma_f32_16x16x32_bf16 a[68:71], a[140:143], v[148:151], a[68:71]\n\t" \
  "v_exp_f32 v65, v65\n\t" \
  "v_mfma_f32_16x16x32_bf16 a[72:75], a[144:147], v[144:147], a[72:75]\n\t" \
  "s_add_u32 m0, s38, 65536\n\t" \
  "v_exp_f32 v66, v66\n\t" \
  "v_mfma_f32_16x16x32_bf16 a[72:75], a[148:151], v[148:151], a[72:75]\n\t" \
  "s_add_u32 s96, s38, 81920\n\t" \
  "ds_read_b128 a[228:231], v221 offset:8192\n\t" \
  "v_cvt_pk_bf16_f32 v135, v14, v15\n\t" \
  "v_mfma_f32_16x16x32_bf16 a[76:79], a[152:155], v[144:147], a[76:79]\n\t" \
  "v_exp_f32 v67, v67\n\t" \
  "v_mfma_f32_16x16x32_bf16 a[76:79], a[156:159], v[148:151], a[76:79]\n\t" \
  "v_exp_f32 v68, v68\n\t" \
  "v_mfma_f32_16x16x32_bf16 a[80:83], a[160:163], v[144:147], a[80:83]\n\t" \
  "buffer_load_dwordx4 v224, s[60:63], s96 offen offset:0 lds\n\t" \
  "v_mfma_f32_16x16x32_bf16 a[80:83], a[164:167], v[148:151], a[80:83]\n\t" \
  "ds_read_b128 a[232:235], v222 offset:8192\n\t" \
  "v_cvt_pk_bf16_f32 v136, v32, v33\n\t" \
  "v_mfma_f32_16x16x32_bf16 a[84:87], a[168:171], v[144:147], a[84:87]\n\t" \
  "v_exp_f32 v69, v69\n\t" \
  "v_mfma_f32_16x16x32_bf16 a[84:87], a[172:175], v[148:151], a[84:87]\n\t" \
  "buffer_load_dwordx4 v225, s[60:63], s96 offen offset:1024 lds\n\t" \
  "v_mfma_f32_16x16x32_bf16 a[88:91], a[176:179], v[144:147], a[88:91]\n\t" \
  "ds_read_b128 a[236:239], v223 offset:8192\n\t" \
  "v_cvt_pk_bf16_f32 v137, v34, v35\n\t" \
  "v_mfma_f32_16x16x32_bf16 a[88:91], a[180:183], v[148:151], a[88:91]\n\t" \
  "ds_read_b128 a[240:243], v220 offset:12288\n\t" \
  "v_cvt_pk_bf16_f32 v138, v36, v37\n\t" \
  "v_mfma_f32_16x16x32_bf16 a[92:95], a[184:187], v[144:147], a[92:95]\n\t" \
  "buffer_load_dwordx4 v226, s[60:63], s96 offen offset:2048 lds\n\t" \
  "v_mfma_f32_16x16x32_bf16 a[92:95], a[188:191], v[148:151], a[92:95]\n\t" \
  "v_exp_f32 v70, v70\n\t" \
  "v_smfmac_f32_16x16x64_bf16 v[168:171], v[216:219], v[144:151], v208\n\t" \
  "ds_read_b128 a[244:247], v221 offset:12288\n\t" \
  "v_cvt_pk_bf16_f32 v139, v38, v39\n\t" \
  "v_mfma_f32_16x16x32_bf16 a[96:99], a[128:131], v[152:155], a[96:99]\n\t" \
  "buffer_load_dwordx4 v227, s[60:63], s96 offen offset:3072 lds\n\t" \
  "v_mfma_f32_16x16x32_bf16 a[96:99], a[132:135], v[156:159], a[96:99]\n\t" \
  "s_add_u32 m0, s38, 81920\n\t" \
  "v_cvt_pk_bf16_f32 v140, v40, v41\n\t" \
  "s_add_u32 s96, s38, 65536\n\t" \
  "v_mfma_f32_16x16x32_bf16 a[100:103], a[136:139], v[152:155], a[100:103]\n\t" \
  "buffer_load_dwordx4 v230, s[64:67], s96 offen offset:0 lds\n\t" \
  "v_mfma_f32_16x16x32_bf16 a[100:103], a[140:143], v[156:159], a[100:103]\n\t" \
  "ds_read_b128 a[248:251], v222 offset:12288\n\t" \
  "v_cvt_pk_bf16_f32 v141, v42, v43\n\t" \
  "v_mfma_f32_16x16x32_bf16 a[104:107], a[144:147], v[152:155], a[104:107]\n\t" \
  "v_exp_f32 v71, v71\n\t" \
  "v_mfma_f32_16x16x32_bf16 a[104:107], a[148:151], v[156:159], a[104:107]\n\t" \
  "buffer_load_dwordx4 v231, s[64:67], s96 offen offset:1024 lds\n\t" \
  "v_mfma_f32_16x16x32_bf16 a[108:111], a[152:155], v[152:155], a[108:111]\n\t" \
  "ds_read_b128 a[252:255], v223 offset:12288\n\t" \
  "v_cvt_pk_bf16_f32 v142, v44, v45\n\t" \
  "v_mfma_f32_16x16x32_bf16 a[108:111], a[156:159], v[156:159], a[108:111]\n\t" \
  "v_exp_f32 v72, v72\n\t" \
  "v_mfma_f32_16x16x32_bf16 a[112:115], a[160:163], v[152:155], a[112:115]\n\t" \
  "buffer_load_dwordx4 v230, s[64:67], s96 offen offset:2048 lds\n\t" \
  "v_mfma_f32_16x16x32_bf16 a[112:115], a[164:167], v[156:159], a[112:115]\n\t" \
  "v_exp_f32 v73, v73\n\t" \
  "v_mfma_f32_16x16x32_bf16 a[116:119], a[168:171], v[152:155], a[116:119]\n\t" \
  "v_exp_f32 v74, v74\n\t" \
  "v_mfma_f32_16x16x32_bf16 a[116:119], a[172:175], v[156:159], a[116:119]\n\t" \
  "buffer_load_dwordx4 v231, s[64:67], s96 offen offset:3072 lds\n\t" \
  "v_mfma_f32_16x16x32_bf16 a[120:123], a[176:179], v[152:155], a[120:123]\n\t" \
  "v_exp_f32 v75, v75\n\t" \
  "v_mfma_f32_16x16x32_bf16 a[120:123], a[180:183], v[156:159], a[120:123]\n\t" \
  "v_exp_f32 v76, v76\n\t" \
  "v_mfma_f32_16x16x32_bf16 a[124:127], a[184:187], v[152:155], a[124:127]\n\t" \
  "v_exp_f32 v77, v77\n\t" \
  "v_mfma_f32_16x16x32_bf16 a[124:127], a[188:191], v[156:159], a[124:127]\n\t" \
  "v_exp_f32 v78, v78\n\t" \
  "v_smfmac_f32_16x16x64_bf16 v[172:175], v[216:219], v[152:159], v208\n\t" \
  "s_waitcnt lgkmcnt(12)\n\t" \
  "v_cvt_pk_bf16_f32 v143, v46, v47\n\t" \
  STA("s_memrealtime s[94:95]\n\t") \
  STA("s_memtime s[92:93]\n\t") \
  "v_mfma_f32_16x16x32_bf16 v[0:3], a[192:195], v[16:19], v[176:179]\n\t" \
  "ds_read_b64_tr_b16 a[128:129], v210 offset:0\n\t" \
  "v_cvt_pk_bf16_f32 v144, v64, v65\n\t" \
  "v_mfma_f32_16x16x32_bf16 v[0:3], a[196:199], v[20:23], v[0:3]\n\t" \
  "v_exp_f32 v79, v79\n\t" \
  "v_mfma_f32_16x16x32_bf16 v[0:3], a[200:203], v[24:27], v[0:3]\n\t" \
  "s_add_u32 m0, s38, 131072\n\t" \
  "ds_read_b64_tr_b16 a[130:131], v210 offset:4096\n\t" \
  "v_cvt_pk_bf16_f32 v145, v66, v67\n\t" \
  "v_mfma_f32_16x16x32_bf16 v[0:3], a[204:207], v[28:31], v[0:3]\n\t" \
  "s_waitcnt lgkmcnt(10)\n\t" \
  "v_exp_f32 v96, v96\n\t" \
  "v_mfma_f32_16x16x32_bf16 v[4:7], a[220:223], v[28:31], v[176:179]\n\t" \
  "s_mov_b32 s96, 28672\n\t" \
  "ds_read_b64_tr_b16 a[132:133], v210 offset:8192\n\t" \
  "v_cvt_pk_bf16_f32 v146, v68, v69\n\t" \
  "v_mfma_f32_16x16x32_bf16 v[4:7], a[216:219], v[24:27], v[4:7]\n\t" \
  "v_exp_f32 v97, v97\n\t" \
  "v_mfma_f32_16x16x32_bf16 v[4:7], a[212:215], v[20:23], v[4:7]\n\t" \
  "buffer_load_dwordx4 v232, s[72:75], s96 offen offset:0 lds\n\t" \
  "v_mfma_f32_16x16x32_bf16 v[4:7], a[208:211], v[16:19], v[4:7]\n\t" \
  "s_waitcnt lgkmcnt(7)\n\t" \
  "ds_read_b64_tr_b16 a[134:135], v210 offset:12288\n\t" \
  "v_cvt_pk_bf16_f32 v147, v70, v71\n\t" \
  "v_mfma_f32_16x16x32_bf16 v[8:11], a[224:227], v[16:19], v[176:179]\n\t" \
  "ds_read_b64_tr_b16 a[136:137], v211 offset:0\n\t" \
  "v_cvt_pk_bf16_f32 v148, v72, v73\n\t" \
  "v_mfma_f32_16x16x32_bf16 v[8:11], a[228:231], v[20:23], v[8:11]\n\t" \
  "ds_read_b64_tr_b16 a[138:139], v211 offset:4096\n\t" \
  "v_cvt_pk_bf16_f32 v149, v74, v75\n\t" \
  "v_mfma_f32_16x16x32_bf16 v[8:11], a[232:235], v[24:27], v[8:11]\n\t" \
  "v_exp_f32 v98, v98\n\t" \
  "v_mfma_f32_16x16x32_bf16 v[8:11], a[236:239], v[28:31], v[8:11]\n\t" \
  "s_waitcnt lgkmcnt(6)\n\t" \
  "ds_read_b64_tr_b16 a[140:141], v211 offset:8192\n\t" \
  "v_cvt_pk_bf16_f32 v150, v76, v77\n\t" \
  "v_mfma_f32_16x16x32_bf16 v[12:15], a[252:255], v[28:31], v[176:179]\n\t" \
  STA("v_writelane_b32 v255, s94, 38\n\t") \
  STA("v_writelane_b32 v255, s92, 39\n\t") \
  "v_exp_f32 v99, v99\n\t" \
  "v_mfma_f32_16x16x32_bf16 v[12:15], a[248:251], v[24:27], v[12:15]\n\t" \
  "ds_read_b64_tr_b16 a[142:143], v211 offset:12288\n\t" \
  "v_cvt_pk_bf16_f32 v151, v78, v79\n\t" \
  "v_mfma_f32_16x16x32_bf16 v[12:15], a[244:247], v[20:23], v[12:15]\n\t" \
  "ds_read_b64_tr_b16 a[144:145], v210 offset:256\n\t" \
  "v_cvt_pk_bf16_f32 v152, v96, v97\n\t" \
  "v_mfma_f32_16x16x32_bf16 v[12:15], a[240:243], v[16:19], v[12:15]\n\t" \
  "v_exp_f32 v100, v100\n\t" \
  "v_mfma_f32_16x16x32_bf16 v[32:35], a[192:195], v[48:51], v[180:183]\n\t" \
  "s_mov_b32 s96, 34816\n\t" \
  "ds_read_b64_tr_b16 a[146:147], v210 offset:4352\n\t" \
  "v_cvt_pk_bf16_f32 v153, v98, v99\n\t" \
  "v_mfma_f32_16x16x32_bf16 v[32:35], a[196:199], v[52:55], v[32:35]\n\t" \
  "v_exp_f32 v101, v101\n\t" \
  "v_mfma_f32_16x16x32_bf16 v[32:35], a[200:203], v[56:59], v[32:35]\n\t" \
  "ds_read_b64_tr_b16 a[148:149], v210 offset:8448\n\t" \
  "v_cvt_pk_bf16_f32 v154, v100, v101\n\t" \
  "v_mfma_f32_16x16x32_bf16 v[32:35], a[204:207], v[60:63], v[32:35]\n\t" \
  "v_exp_f32 v102, v102\n\t" \
  "v_mfma_f32_16x16x32_bf16 v[36:39], a[220:223], v[60:63], v[180:183]\n\t" \
  "buffer_load_dwordx4 v232, s[72:75], s96 offen offset:1024 lds\n\t" \
  "v_mfma_f32_16x16x32_bf16 v[36:39], a[216:219], v[56:59], v[36:39]\n\t" \
  "v_exp_f32 v103, v103\n\t" \
  "v_mfma_f32_16x16x32_bf16 v[36:39], a[212:215], v[52:55], v[36:39]\n\t" \
  "ds_read_b64_tr_b16 a[150:151], v210 offset:12544\n\t" \
  "v_cvt_pk_bf16_f32 v155, v102, v103\n\t" \
  "v_mfma_f32_16x16x32_bf16 v[36:39], a[208:211], v[48:51], v[36:39]\n\t" \
  "v_exp_f32 v104, v104\n\t" \
  "v_mfma_f32_16x16x32_bf16 v[40:43], a[224:227], v[48:51], v[180:183]\n\t" \
  "v_exp_f32 v105, v105\n\t" \
  "v_mfma_f32_16x16x32_bf16 v[40:43], a[228:231], v[52:55], v[40:43]\n\t" \
  "ds_read_b64_tr_b16 a[152:153], v211 offset:256\n\t" \
  "v_cvt_pk_bf16_f32 v156, v104, v105\n\t" \
  "v_mfma_f32_16x16x32_bf16 v[40:43], a[232:235], v[56:59], v[40:43]\n\t" \
  "v_exp_f32 v106, v106\n\t" \
  "v_mfma_f32_16x16x32_bf16 v[40:43], a[236:239], v[60:63], v[40:43]\n\t" \
  "v_exp_f32 v107, v107\n\t" \
  "v_mfma_f32_16x16x32_bf16 v[44:47], a[252:255], v[60:63], v[180:183]\n\t" \
  "ds_read_b64_tr_b16 a[154:155], v211 offset:4352\n\t" \
  "v_cvt_pk_bf16_f32 v157, v106, v107\n\t" \
  "v_mfma_f32_16x16x32_bf16 v[44:47], a[248:251], v[56:59], v[44:47]\n\t" \
  "v_exp_f32 v108, v108\n\t" \
  "v_mfma_f32_16x16x32_bf16 v[44:47], a[244:247], v[52:55], v[44:47]\n\t" \
  "ds_read_b64_tr_b16 a[156:157], v211 offset:8448\n\t" \
  "s_mov_b32 s96, 40960\n\t" \
  "v_mfma_f32_16x16x32_bf16 v[44:47], a[240:243], v[48:51], v[44:47]\n\t" \
  "v_exp_f32 v109, v109\n\t" \
  "v_mfma_f32_16x16x32_bf16 v[64:67], a[192:195], v[80:83], v[184:187]\n\t" \
  "ds_read_b64_tr_b16 a[158:159], v211 offset:12544\n\t" \
  "v_cvt_pk_bf16_f32 v158, v108, v109\n\t" \
  "v_mfma_f32_16x16x32_bf16 v[64:67], a[196:199], v[84:87], v[64:67]\n\t" \
  "v_exp_f32 v110, v110\n\t" \
  "v_mfma_f32_16x16x32_bf16 v[64:67], a[200:203], v[88:91], v[64:67]\n\t" \
  "buffer_load_dwordx4 v232, s[72:75], s96 offen offset:2048 lds\n\t" \
  "v_mfma_f32_16x16x32_bf16 v[64:67], a[204:207], v[92:95], v[64:67]\n\t" \
  "ds_read_b64_tr_b16 a[160:161], v210 offset:512\n\t" \
  "v_mfma_f32_16x16x32_bf16 v[68:71], a[220:223], v[92:95], v[184:187]\n\t" \
  "ds_read_b64_tr_b16 a[162:163], v210 offset:4608\n\t" \
  "v_mfma_f32_16x16x32_bf16 v[68:71], a[216:219], v[88:91], v[68:71]\n\t" \
  "v_exp_f32 v111, v111\n\t" \
  "v_mfma_f32_16x16x32_bf16 v[68:71], a[212:215], v[84:87], v[68:71]\n\t" \
  "ds_read_b64_tr_b16 a[164:165], v210 offset:8704\n\t" \
  "v_cvt_pk_bf16_f32 v159, v110, v111\n\t" \
  "v_mfma_f32_16x16x32_bf16 v[68:71], a[208:211], v[80:83], v[68:71]\n\t" \
  "ds_read_b64_tr_b16 a[166:167], v210 offset:12800\n\t" \
  "v_mfma_f32_16x16x32_bf16 v[72:75], a[224:227], v[80:83], v[184:187]\n\t" \
  "ds_read_b64_tr_b16 a[168:169], v211 offset:512\n\t" \
  "v_mfma_f32_16x16x32_bf16 v[72:75], a[228:231], v[84:87], v[72:75]\n\t" \
  "ds_read_b64_tr_b16 a[170:171], v211 offset:4608\n\t" \
  "v_mfma_f32_16x16x32_bf16 v[72:75], a[232:235], v[88:91], v[72:75]\n\t" \
  "ds_read_b64_tr_b16 a[172:173], v211 offset:8704\n\t" \
  "v_mfma_f32_16x16x32_bf16 v[72:75], a[236:239], v[92:95], v[72:75]\n\t" \
  "ds_read_b64_tr_b16 a[174:175], v211 offset:12800\n\t" \
  "v_mfma_f32_16x16x32_bf16 v[76:79], a[252:255], v[92:95], v[184:187]\n\t" \
  "s_mov_b32 s96, 47104\n\t" \
  "v_exp_f32 v0, v0\n\t" \
  "v_mfma_f32_16x16x32_bf16 v[76:79], a[248:251], v[88:91], v[76:79]\n\t" \
  "ds_read_b64_tr_b16 a[176:177], v210 offset:768\n\t" \
  "v_mfma_f32_16x16x32_bf16 v[76:79], a[244:247], v[84:87], v[76:79]\n\t" \
  "v_exp_f32 v1, v1\n\t" \
  "v_mfma_f32_16x16x32_bf16 v[76:79], a[240:243], v[80:83], v[76:79]\n\t" \
  "ds_read_b64_tr_b16 a[178:179], v210 offset:4864\n\t" \
  "v_mfma_f32_16x16x32_bf16 v[96:99], a[192:195], v[112:115], v[188:191]\n\t" \
  "buffer_load_dwordx4 v232, s[72:75], s96 offen offset:3072 lds\n\t" \
  "v_mfma_f32_16x16x32_bf16 v[96:99], a[196:199], v[116:119], v[96:99]\n\t" \
  "ds_read_b64_tr_b16 a[180:181], v210 offset:8960\n\t" \
  "v_mfma_f32_16x16x32_bf16 v[96:99], a[200:203], v[120:123], v[96:99]\n\t" \
  "ds_read_b64_tr_b16 a[182:183], v210 offset:13056\n\t" \
  "v_mfma_f32_16x16x32_bf16 v[96:99], a[204:207], v[124:127], v[96:99]\n\t" \
  "s_add_u32 s99, s52, 0\n\t" \
  "v_exp_f32 v2, v2\n\t" \
  "v_mfma_f32_16x16x32_bf16 v[100:103], a[220:223], v[124:127], v[188:191]\n\t" \
  "ds_read_b64_tr_b16 a[184:185], v211 offset:768\n\t" \
  "v_mfma_f32_16x16x32_bf16 v[100:103], a[216:219], v[120:123], v[100:103]\n\t" \
  "ds_read_b64_tr_b16 a[186:187], v211 offset:4864\n\t" \
  "v_mfma_f32_16x16x32_bf16 v[100:103], a[212:215], v[116:119], v[100:103]\n\t" \
  "buffer_load_dword v246, v212, s[24:27], s99 offen\n\t" \
  "v_mfma_f32_16x16x32_bf16 v[100:103], a[208:211], v[112:115], v[100:103]\n\t" \
  "ds_read_b64_tr_b16 a[188:189], v211 offset:8960\n\t" \
  "v_mfma_f32_16x16x32_bf16 v[104:107], a[224:227], v[112:115], v[188:191]\n\t" \
  "v_exp_f32 v3, v3\n\t" \
  "v_mfma_f32_16x16x32_bf16 v[104:107], a[228:231], v[116:119], v[104:107]\n\t" \
  "ds_read_b64_tr_b16 a[190:191], v211 offset:13056\n\t" \
  "v_mfma_f32_16x16x32_bf16 v[104:107], a[232:235], v[120:123], v[104:107]\n\t" \
  "v_exp_f32 v4, v4\n\t" \
  "v_mfma_f32_16x16x32_bf16 v[104:107], a[236:239], v[124:127], v[104:107]\n\t" \
  "v_exp_f32 v5, v5\n\t" \
  "v_mfma_f32_16x16x32_bf16 v[108:111], a[252:255], v[124:127], v[188:191]\n\t" \
  "v_exp_f32 v6, v6\n\t" \
  "v_mfma_f32_16x16x32_bf16 v[108:111], a[248:251], v[120:123], v[108:111]\n\t" \
  "v_exp_f32 v7, v7\n\t" \
  "v_mfma_f32_16x16x32_bf16 v[108:111], a[244:247], v[116:119], v[108:111]\n\t" \
  "s_waitcnt vmcnt(5) lgkmcnt(0)\n\t" \
  "v_exp_f32 v8, v8\n\t" \
  "v_mfma_f32_16x16x32_bf16 v[108:111], a[240:243], v[112:115], v[108:111]\n\t" \
  "s_barrier\n\t" \
  STA("s_memrealtime s[94:95]\n\t") \
  STA("s_memtime s[92:93]\n\t") \
  "v_mfma_f32_16x16x32_bf16 a[0:3], a[128:131], v[128:131], a[0:3]\n\t" \
  "v_exp_f32 v9, v9\n\t" \
  "v_mfma_f32_16x16x32_bf16 a[0:3], a[132:135], v[132:135], a[0:3]\n\t" \
  "v_exp_f32 v10, v10\n\t" \
  "v_mfma_f32_16x16x32_bf16 a[4:7], a[136:139], v[128:131], a[4:7]\n\t" \
  "v_exp_f32 v11, v11\n\t" \
  "v_mfma_f32_16x16x32_bf16 a[4:7], a[140:143], v[132:135], a[4:7]\n\t" \
  "v_exp_f32 v12, v12\n\t" \
  "v_mfma_f32_16x16x32_bf16 a[8:11], a[144:147], v[128:131], a[8:11]\n\t" \
  "v_exp_f32 v13, v13\n\t" \
  "v_mfma_f32_16x16x32_bf16 a[8:11], a[148:151], v[132:135], a[8:11]\n\t" \
  "v_exp_f32 v14, v14\n\t" \
  "v_mfma_f32_16x16x32_bf16 a[12:15], a[152:155], v[128:131], a[12:15]\n\t" \
  "v_exp_f32 v15, v15\n\t" \
  "v_mfma_f32_16x16x32_bf16 a[12:15], a[156:159], v[132:135], a[12:15]\n\t" \
  "v_exp_f32 v32, v32\n\t" \
  "v_mfma_f32_16x16x32_bf16 a[16:19], a[160:163], v[128:131], a[16:19]\n\t" \
  "v_exp_f32 v33, v33\n\t" \
  "v_mfma_f32_16x16x32_bf16 a[16:19], a[164:167], v[132:135], a[16:19]\n\t" \
  "v_exp_f32 v34, v34\n\t" \
  "v_mfma_f32_16x16x32_bf16 a[20:23], a[168:171], v[128:131], a[20:23]\n\t" \
  "v_exp_f32 v35, v35\n\t" \
  "v_mfma_f32_16x16x32_bf16 a[20:23], a[172:175], v[132:135], a[20:23]\n\t" \
  "v_exp_f32 v36, v36\n\t" \
  "v_mfma_f32_16x16x32_bf16 a[24:27], a[176:179], v[128:131], a[24:27]\n\t" \
  STA("v_writelane_b32 v255, s94, 40\n\t") \
  STA("v_writelane_b32 v255, s92, 41\n\t") \
  "v_exp_f32 v37, v37\n\t" \
  "v_mfma_f32_16x16x32_bf16 a[24:27], a[180:183], v[132:135], a[24:27]\n\t" \
  "v_exp_f32 v38, v38\n\t" \
  "v_mfma_f32_16x16x32_bf16 a[28:31], a[184:187], v[128:131], a[28:31]\n\t" \
  "v_exp_f32 v39, v39\n\t" \
  "v_mfma_f32_16x16x32_bf16 a[28:31], a[188:191], v[132:135], a[28:31]\n\t" \
  "v_exp_f32 v40, v40\n\t" \
  "v_smfmac_f32_16x16x64_bf16 v[160:163], v[216:219], v[128:135], v208\n\t" \
  "s_add_u32 m0, s38, 49152\n\t" \
  "ds_read_b128 a[192:195], v220 offset:16384\n\t" \
  "v_cvt_pk_bf16_f32 v128, v0, v1\n\t" \
  "v_mfma_f32_16x16x32_bf16 a[32:35], a[128:131], v[136:139], a[32:35]\n\t" \
  "s_add_u32 s96, s38, 98304\n\t" \
  "ds_read_b128 a[196:199], v221 offset:16384\n\t" \
  "v_cvt_pk_bf16_f32 v129, v2, v3\n\t" \
  "v_mfma_f32_16x16x32_bf16 a[32:35], a[132:135], v[140:143], a[32:35]\n\t" \
  "v_exp_f32 v41, v41\n\t" \
  "v_mfma_f32_16x16x32_bf16 a[36:39], a[136:139], v[136:139], a[36:39]\n\t" \
  "v_exp_f32 v42, v42\n\t" \
  "v_mfma_f32_16x16x32_bf16 a[36:39], a[140:143], v[140:143], a[36:39]\n\t" \
  "buffer_load_dwordx4 v224, s[60:63], s96 offen offset:0 lds\n\t" \
  "v_mfma_f32_16x16x32_bf16 a[40:43], a[144:147], v[136:139], a[40:43]\n\t" \
  "ds_read_b128 a[200:203], v222 offset:16384\n\t" \
  "v_cvt_pk_bf16_f32 v130, v4, v5\n\t" \
  "v_mfma_f32_16x16x32_bf16 a[40:43], a[148:151], v[140:143], a[40:43]\n\t" \
  "v_exp_f32 v43, v43\n\t" \
  "v_mfma_f32_16x16x32_bf16 a[44:47], a[152:155], v[136:139], a[44:47]\n\t" \
  "ds_read_b128 a[204:207], v223 offset:16384\n\t" \
  "v_cvt_pk_bf16_f32 v131, v6, v7\n\t" \
  "v_mfma_f32_16x16x32_bf16 a[44:47], a[156:159], v[140:143], a[44:47]\n\t" \
  "v_exp_f32 v44, v44\n\t" \
  "v_mfma_f32_16x16x32_bf16 a[48:51], a[160:163], v[136:139], a[48:51]\n\t" \
  "ds_read_b128 a[208:211], v220 offset:20480\n\t" \
  "v_cvt_pk_bf16_f32 v132, v8, v9\n\t" \
  "v_mfma_f32_16x16x32_bf16 a[48:51], a[164:167], v[140:143], a[48:51]\n\t" \
  "buffer_load_dwordx4 v225, s[60:63], s96 offen offset:1024 lds\n\t" \
  "v_mfma_f32_16x16x32_bf16 a[52:55], a[168:171], v[136:139], a[52:55]\n\t" \
  "v_exp_f32 v45, v45\n\t" \
  "v_mfma_f32_16x16x32_bf16 a[52:55], a[172:175], v[140:143], a[52:55]\n\t" \
  "ds_read_b128 a[212:215], v221 offset:20480\n\t" \
  "v_cvt_pk_bf16_f32 v133, v10, v11\n\t" \
  "v_mfma_f32_16x16x32_bf16 a[56:59], a[176:179], v[136:139], a[56:59]\n\t" \
  "v_exp_f32 v46, v46\n\t" \
  "v_mfma_f32_16x16x32_bf16 a[56:59], a[180:183], v[140:143], a[56:59]\n\t" \
  "v_exp_f32 v47, v47\n\t" \
  "v_mfma_f32_16x16x32_bf16 a[60:63], a[184:187], v[136:139], a[60:63]\n\t" \
  "buffer_load_dwordx4 v226, s[60:63], s96 offen offset:2048 lds\n\t" \
  "v_mfma_f32_16x16x32_bf16 a[60:63], a[188:191], v[140:143], a[60:63]\n\t" \
  "ds_read_b128 a[216:219], v222 offset:20480\n\t" \
  "v_cvt_pk_bf16_f32 v134, v12, v13\n\t" \
  "v_smfmac_f32_16x16x64_bf16 v[164:167], v[216:219], v[136:143], v208\n\t" \
  "ds_read_b128 a[220:223], v223 offset:20480\n\t" \
  "v_cvt_pk_bf16_f32 v135, v14, v15\n\t" \
  "v_mfma_f32_16x16x32_bf16 a[64:67], a[128:131], v[144:147], a[64:67]\n\t" \
  "v_exp_f32 v64, v64\n\t" \
  "v_mfma_f32_16x16x32_bf16 a[64:67], a[132:135], v[148:151], a[64:67]\n\t" \
  "v_exp_f32 v65, v65\n\t" \
  "v_mfma_f32_16x16x32_bf16 a[68:71], a[136:139], v[144:147], a[68:71]\n\t" \
  "ds_read_b128 a[224:227], v220 offset:24576\n\t" \
  "v_cvt_pk_bf16_f32 v136, v32, v33\n\t" \
  "v_mfma_f32_16x16x32_bf16 a[68:71], a[140:143], v[148:151], a[68:71]\n\t" \
  "buffer_load_dwordx4 v227, s[60:63], s96 offen offset:3072 lds\n\t" \
  "v_mfma_f32_16x16x32_bf16 a[72:75], a[144:147], v[144:147], a[72:75]\n\t" \
  "s_add_u32 m0, s38, 147456\n\t" \
  "v_exp_f32 v66, v66\n\t" \
  "v_mfma_f32_16x16x32_bf16 a[72:75], a[148:151], v[148:151], a[72:75]\n\t" \
  "s_add_u32 s96, s38, 81920\n\t" \
  "ds_read_b128 a[228:231], v221 offset:24576\n\t" \
  "v_cvt_pk_bf16_f32 v137, v34, v35\n\t" \
  "v_mfma_f32_16x16x32_bf16 a[76:79], a[152:155], v[144:147], a[76:79]\n\t" \
  "v_exp_f32 v67, v67\n\t" \
  "v_mfma_f32_16x16x32_bf16 a[76:79], a[156:159], v[148:151], a[76:79]\n\t" \
  "v_exp_f32 v68, v68\n\t" \
  "v_mfma_f32_16x16x32_bf16 a[80:83], a[160:163], v[144:147], a[80:83]\n\t" \
  "buffer_load_dwordx4 v230, s[64:67], s96 offen offset:0 lds\n\t" \
  "v_mfma_f32_16x16x32_bf16 a[80:83], a[164:167], v[148:151], a[80:83]\n\t" \
  "ds_read_b128 a[232:235], v222 offset:24576\n\t" \
  "v_cvt_pk_bf16_f32 v138, v36, v37\n\t" \
  "v_mfma_f32_16x16x32_bf16 a[84:87], a[168:171], v[144:147], a[84:87]\n\t" \
  ".Lhs8:\n\ts_cbranch_execz .Lhs8n\n\t" \
  "v_exp_f32 v69, v69\n\t" \
  "v_mfma_f32_16x16x32_bf16 a[84:87], a[172:175], v[148:151], a[84:87]\n\t" \
  "ds_read_b128 a[236:239], v223 offset:24576\n\t" \
  "v_cvt_pk_bf16_f32 v139, v38, v39\n\t" \
  "v_mfma_f32_16x16x32_bf16 a[88:91], a[176:179], v[144:147], a[88:91]\n\t" \
  "v_exp_f32 v70, v70\n\t" \
  "v_mfma_f32_16x16x32_bf16 a[88:91], a[180:183], v[148:151], a[88:91]\n\t" \
  "ds_read_b128 a[240:243], v220 offset:28672\n\t" \
  "v_cvt_pk_bf16_f32 v140, v40, v41\n\t" \
  "v_mfma_f32_16x16x32_bf16 a[92:95], a[184:187], v[144:147], a[92:95]\n\t" \
  "buffer_load_dwordx4 v231, s[64:67], s96 offen offset:1024 lds\n\t" \
  "v_mfma_f32_16x16x32_bf16 a[92:95], a[188:191], v[148:151], a[92:95]\n\t" \
  "v_exp_f32 v71, v71\n\t" \
  "v_smfmac_f32_16x16x64_bf16 v[168:171], v[216:219], v[144:151], v208\n\t" \
  "ds_read_b128 a[244:247], v221 offset:28672\n\t" \
  "v_cvt_pk_bf16_f32 v141, v42, v43\n\t" \
  "v_mfma_f32_16x16x32_bf16 a[96:99], a[128:131], v[152:155], a[96:99]\n\t" \
  "v_exp_f32 v72, v72\n\t" \
  "v_mfma_f32_16x16x32_bf16 a[96:99], a[132:135], v[156:159], a[96:99]\n\t" \
  "v_exp_f32 v73, v73\n\t" \
  "v_mfma_f32_16x16x32_bf16 a[100:103], a[136:139], v[152:155], a[100:103]\n\t" \
  "buffer_load_dwordx4 v230, s[64:67], s96 offen offset:2048 lds\n\t" \
  "v_mfma_f32_16x16x32_bf16 a[100:103], a[140:143], v[156:159], a[100:103]\n\t" \
  "ds_read_b128 a[248:251], v222 offset:28672\n\t" \
  "v_cvt_pk_bf16_f32 v142, v44, v45\n\t" \
  "v_mfma_f32_16x16x32_bf16 a[104:107], a[144:147], v[152:155], a[104:107]\n\t" \
  "v_exp_f32 v74, v74\n\t" \
  "v_mfma_f32_16x16x32_bf16 a[104:107], a[148:151], v[156:159], a[104:107]\n\t" \
  "ds_read_b128 a[252:255], v223 offset:28672\n\t" \
  "v_cvt_pk_bf16_f32 v143, v46, v47\n\t" \
  "v_mfma_f32_16x16x32_bf16 a[108:111], a[152:155], v[152:155], a[108:111]\n\t" \
  "v_exp_f32 v75, v75\n\t" \
  "v_mfma_f32_16x16x32_bf16 a[108:111], a[156:159], v[156:159], a[108:111]\n\t" \
  "v_exp_f32 v76, v76\n\t" \
  "v_mfma_f32_16x16x32_bf16 a[112:115], a[160:163], v[152:155], a[112:115]\n\t" \
  "buffer_load_dwordx4 v231, s[64:67], s96 offen offset:3072 lds\n\t" \
  "v_mfma_f32_16x16x32_bf16 a[112:115], a[164:167], v[156:159], a[112:115]\n\t" \
  "v_exp_f32 v77, v77\n\t" \
  "v_mfma_f32_16x16x32_bf16 a[116:119], a[168:171], v[152:155], a[116:119]\n\t" \
  "v_exp_f32 v78, v78\n\t" \
  "v_mfma_f32_16x16x32_bf16 a[116:119], a[172:175], v[156:159], a[116:119]\n\t" \
  "v_exp_f32 v79, v79\n\t" \
  "v_mfma_f32_16x16x32_bf16 a[120:123], a[176:179], v[152:155], a[120:123]\n\t" \
  "s_add_u32 s99, s52, 2048\n\t" \
  "v_exp_f32 v96, v96\n\t" \
  "v_mfma_f32_16x16x32_bf16 a[120:123], a[180:183], v[156:159], a[120:123]\n\t" \
  "v_cvt_pk_bf16_f32 v144, v64, v65\n\t" \
  "v_cvt_pk_bf16_f32 v145, v66, v67\n\t" \
  "v_mfma_f32_16x16x32_bf16 a[124:127], a[184:187], v[152:155], a[124:127]\n\t" \
  "v_exp_f32 v97, v97\n\t" \
  "v_mfma_f32_16x16x32_bf16 a[124:127], a[188:191], v[156:159], a[124:127]\n\t" \
  "buffer_load_dword v246, v212, s[24:27], s99 offen\n\t" \
  "v_smfmac_f32_16x16x64_bf16 v[172:175], v[216:219], v[152:159], v208\n\t" \
  "s_waitcnt lgkmcnt(12)\n\t" \
  "v_cvt_pk_bf16_f32 v146, v68, v69\n\t" \
  STA("s_memrealtime s[94:95]\n\t") \
  STA("s_memtime s[92:93]\n\t") \
  "v_mfma_f32_16x16x32_bf16 v[0:3], a[192:195], v[16:19], v[176:179]\n\t" \
  "ds_read_b64_tr_b16 a[128:129], v228 offset:0\n\t" \
  "v_cvt_pk_bf16_f32 v147, v70, v71\n\t" \
  "v_mfma_f32_16x16x32_bf16 v[0:3], a[196:199], v[20:23], v[0:3]\n\t" \
  "v_exp_f32 v98, v98\n\t" \
  "v_mfma_f32_16x16x32_bf16 v[0:3], a[200:203], v[24:27], v[0:3]\n\t" \
  "ds_read_b64_tr_b16 a[130:131], v228 offset:4096\n\t" \
  "v_cvt_pk_bf16_f32 v148, v72, v73\n\t" \
  "v_mfma_f32_16x16x32_bf16 v[0:3], a[204:207], v[28:31], v[0:3]\n\t" \
  "s_waitcnt lgkmcnt(10)\n\t" \
  "v_exp_f32 v99, v99\n\t" \
  "v_mfma_f32_16x16x32_bf16 v[4:7], a[220:223], v[28:31], v[176:179]\n\t" \
  "ds_read_b64_tr_b16 a[132:133], v228 offset:8192\n\t" \
  "v_cvt_pk_bf16_f32 v149, v74, v75\n\t" \
  "v_mfma_f32_16x16x32_bf16 v[4:7], a[216:219], v[24:27], v[4:7]\n\t" \
  "v_exp_f32 v100, v100\n\t" \
  "v_mfma_f32_16x16x32_bf16 v[4:7], a[212:215], v[20:23], v[4:7]\n\t" \
  "ds_read_b64_tr_b16 a[134:135], v228 offset:12288\n\t" \
  "v_cvt_pk_bf16_f32 v150, v76, v77\n\t" \
  "v_mfma_f32_16x16x32_bf16 v[4:7], a[208:211], v[16:19], v[4:7]\n\t" \
  "s_waitcnt lgkmcnt(8)\n\t" \
  "ds_read_b64_tr_b16 a[136:137], v229 offset:0\n\t" \
  "v_cvt_pk_bf16_f32 v151, v78, v79\n\t" \
  "v_mfma_f32_16x16x32_bf16 v[8:11], a[224:227], v[16:19], v[176:179]\n\t" \
  "v_exp_f32 v101, v101\n\t" \
  "v_mfma_f32_16x16x32_bf16 v[8:11], a[228:231], v[20:23], v[8:11]\n\t" \
  "s_add_u32 s99, s52, 4096\n\t" \
  "ds_read_b64_tr_b16 a[138:139], v229 offset:4096\n\t" \
  "v_cvt_pk_bf16_f32 v152, v96, v97\n\t" \
  "v_mfma_f32_16x16x32_bf16 v[8:11], a[232:235], v[24:27], v[8:11]\n\t" \
  "v_exp_f32 v102, v102\n\t" \
  "v_mfma_f32_16x16x32_bf16 v[8:11], a[236:239], v[28:31], v[8:11]\n\t" \
  "s_waitcnt lgkmcnt(6)\n\t" \
  "ds_read_b64_tr_b16 a[140:141], v229 offset:8192\n\t" \
  "v_cvt_pk_bf16_f32 v153, v98, v99\n\t" \
  "v_mfma_f32_16x16x32_bf16 v[12:15], a[252:255], v[28:31], v[176:179]\n\t" \
  STA("v_writelane_b32 v255, s94, 42\n\t") \
  STA("v_writelane_b32 v255, s92, 43\n\t") \
  "buffer_load_dword v246, v212, s[24:27], s99 offen\n\t" \
  "v_mfma_f32_16x16x32_bf16 v[12:15], a[248:251], v[24:27], v[12:15]\n\t" \
  "ds_read_b64_tr_b16 a[142:143], v229 offset:12288\n\t" \
  "v_cvt_pk_bf16_f32 v154, v100, v101\n\t" \
  "v_mfma_f32_16x16x32_bf16 v[12:15], a[244:247], v[20:23], v[12:15]\n\t" \
  "v_exp_f32 v103, v103\n\t" \
  "v_mfma_f32_16x16x32_bf16 v[12:15], a[240:243], v[16:19], v[12:15]\n\t" \
  "ds_read_b64_tr_b16 a[144:145], v228 offset:256\n\t" \
  "v_cvt_pk_bf16_f32 v155, v102, v103\n\t" \
  "v_mfma_f32_16x16x32_bf16 v[32:35], a[192:195], v[48:51], v[180:183]\n\t" \
  "v_exp_f32 v104, v104\n\t" \
  "v_mfma_f32_16x16x32_bf16 v[32:35], a[196:199], v[52:55], v[32:35]\n\t" \
  "v_exp_f32 v105, v105\n\t" \
  "v_mfma_f32_16x16x32_bf16 v[32:35], a[200:203], v[56:59], v[32:35]\n\t" \
  "ds_read_b64_tr_b16 a[146:147], v228 offset:4352\n\t" \
  "v_cvt_pk_bf16_f32 v156, v104, v105\n\t" \
  "v_mfma_f32_16x16x32_bf16 v[32:35], a[204:207], v[60:63], v[32:35]\n\t" \
  "v_exp_f32 v106, v106\n\t" \
  "v_mfma_f32_16x16x32_bf16 v[36:39], a[220:223], v[60:63], v[180:183]\n\t" \
  "v_exp_f32 v107, v107\n\t" \
  "v_mfma_f32_16x16x32_bf16 v[36:39], a[216:219], v[56:59], v[36:39]\n\t" \
  "ds_read_b64_tr_b16 a[148:149], v228 offset:8448\n\t" \
  "v_cvt_pk_bf16_f32 v157, v106, v107\n\t" \
  "v_mfma_f32_16x16x32_bf16 v[36:39], a[212:215], v[52:55], v[36:39]\n\t" \
  "v_exp_f32 v108, v108\n\t" \
  "v_mfma_f32_16x16x32_bf16 v[36:39], a[208:211], v[48:51], v[36:39]\n\t" \
  "v_exp_f32 v109, v109\n\t" \
  "v_mfma_f32_16x16x32_bf16 v[40:43], a[224:227], v[48:51], v[180:183]\n\t" \
  "ds_read_b64_tr_b16 a[150:151], v228 offset:12544\n\t" \
  "v_cvt_pk_bf16_f32 v158, v108, v109\n\t" \
  "v_mfma_f32_16x16x32_bf16 v[40:43], a[228:231], v[52:55], v[40:43]\n\t" \
  "v_exp_f32 v110, v110\n\t" \
  "v_mfma_f32_16x16x32_bf16 v[40:43], a[232:235], v[56:59], v[40:43]\n\t" \
  "ds_read_b64_tr_b16 a[152:153], v229 offset:256\n\t" \
  "v_mfma_f32_16x16x32_bf16 v[40:43], a[236:239], v[60:63], v[40:43]\n\t" \
  "v_exp_f32 v111, v111\n\t" \
  "v_mfma_f32_16x16x32_bf16 v[44:47], a[252:255], v[60:63], v[180:183]\n\t" \
  "ds_read_b64_tr_b16 a[154:155], v229 offset:4352\n\t" \
  "v_cvt_pk_bf16_f32 v159, v110, v111\n\t" \
  "v_mfma_f32_16x16x32_bf16 v[44:47], a[248:251], v[56:59], v[44:47]\n\t" \
  "ds_read_b64_tr_b16 a[156:157], v229 offset:8448\n\t" \
  "v_mfma_f32_16x16x32_bf16 v[44:47], a[244:247], v[52:55], v[44:47]\n\t" \
  "ds_read_b64_tr_b16 a[158:159], v229 offset:12544\n\t" \
  "v_mfma_f32_16x16x32_bf16 v[44:47], a[240:243], v[48:51], v[44:47]\n\t" \
  "v_exp_f32 v0, v0\n\t" \
  "v_mfma_f32_16x16x32_bf16 v[64:67], a[192:195], v[80:83], v[184:187]\n\t" \
  "ds_read_b64_tr_b16 a[160:161], v228 offset:512\n\t" \
  "v_mfma_f32_16x16x32_bf16 v[64:67], a[196:199], v[84:87], v[64:67]\n\t" \
  "ds_read_b64_tr_b16 a[162:163], v228 offset:4608\n\t" \
  "v_mfma_f32_16x16x32_bf16 v[64:67], a[200:203], v[88:91], v[64:67]\n\t" \
  "v_exp_f32 v1, v1\n\t" \
  "v_mfma_f32_16x16x32_bf16 v[64:67], a[204:207], v[92:95], v[64:67]\n\t" \
  "ds_read_b64_tr_b16 a[164:165], v228 offset:8704\n\t" \
  "v_mfma_f32_16x16x32_bf16 v[68:71], a[220:223], v[92:95], v[184:187]\n\t" \
  "v_exp_f32 v2, v2\n\t" \
  "v_mfma_f32_16x16x32_bf16 v[68:71], a[216:219], v[88:91], v[68:71]\n\t" \
  "ds_read_b64_tr_b16 a[166:167], v228 offset:12800\n\t" \
  "v_mfma_f32_16x16x32_bf16 v[68:71], a[212:215], v[84:87], v[68:71]\n\t" \
  "ds_read_b64_tr_b16 a[168:169], v229 offset:512\n\t" \
  "v_mfma_f32_16x16x32_bf16 v[68:71], a[208:211], v[80:83], v[68:71]\n\t" \
  "v_exp_f32 v3, v3\n\t" \
  "v_mfma_f32_16x16x32_bf16 v[72:75], a[224:227], v[80:83], v[184:187]\n\t" \
  "ds_read_b64_tr_b16 a[170:171], v229 offset:4608\n\t" \
  "v_mfma_f32_16x16x32_bf16 v[72:75], a[228:231], v[84:87], v[72:75]\n\t" \
  "v_exp_f32 v4, v4\n\t" \
  "v_mfma_f32_16x16x32_bf16 v[72:75], a[232:235], v[88:91], v[72:75]\n\t" \
  "ds_read_b64_tr_b16 a[172:173], v229 offset:8704\n\t" \
  "v_mfma_f32_16x16x32_bf16 v[72:75], a[236:239], v[92:95], v[72:75]\n\t" \
  "ds_read_b64_tr_b16 a[174:175], v229 offset:12800\n\t" \
  "v_mfma_f32_16x16x32_bf16 v[76:79], a[252:255], v[92:95], v[184:187]\n\t" \
  "v_exp_f32 v5, v5\n\t" \
  "v_mfma_f32_16x16x32_bf16 v[76:79], a[248:251], v[88:91], v[76:79]\n\t" \
  "ds_read_b64_tr_b16 a[176:177], v228 offset:768\n\t" \
  "v_mfma_f32_16x16x32_bf16 v[76:79], a[244:247], v[84:87], v[76:79]\n\t" \
  "ds_read_b64_tr_b16 a[178:179], v228 offset:4864\n\t" \
  "v_mfma_f32_16x16x32_bf16 v[76:79], a[240:243], v[80:83], v[76:79]\n\t" \
  "v_exp_f32 v6, v6\n\t" \
  "v_mfma_f32_16x16x32_bf16 v[96:99], a[192:195], v[112:115], v[188:191]\n\t" \
  "ds_read_b64_tr_b16 a[180:181], v228 offset:8960\n\t" \
  "v_mfma_f32_16x16x32_bf16 v[96:99], a[196:199], v[116:119], v[96:99]\n\t" \
  "v_exp_f32 v7, v7\n\t" \
  "v_mfma_f32_16x16x32_bf16 v[96:99], a[200:203], v[120:123], v[96:99]\n\t" \
  "ds_read_b64_tr_b16 a[182:183], v228 offset:13056\n\t" \
  "v_mfma_f32_16x16x32_bf16 v[96:99], a[204:207], v[124:127], v[96:99]\n\t" \
  "ds_read_b64_tr_b16 a[184:185], v229 offset:768\n\t" \
  "v_mfma_f32_16x16x32_bf16 v[100:103], a[220:223], v[124:127], v[188:191]\n\t" \
  "v_exp_f32 v8, v8\n\t" \
  "v_mfma_f32_16x16x32_bf16 v[100:103], a[216:219], v[120:123], v[100:103]\n\t" \
  "ds_read_b64_tr_b16 a[186:187], v229 offset:4864\n\t" \
  "v_mfma_f32_16x16x32_bf16 v[100:103], a[212:215], v[116:119], v[100:103]\n\t" \
  "ds_read_b64_tr_b16 a[188:189], v229 offset:8960\n\t" \
  "v_mfma_f32_16x16x32_bf16 v[100:103], a[208:211], v[112:115], v[100:103]\n\t" \
  "v_exp_f32 v9, v9\n\t" \
  "v_mfma_f32_16x16x32_bf16 v[104:107], a[224:227], v[112:115], v[188:191]\n\t" \
  "ds_read_b64_tr_b16 a[190:191], v229 offset:13056\n\t" \
  "v_mfma_f32_16x16x32_bf16 v[104:107], a[228:231], v[116:119], v[104:107]\n\t" \
  "v_exp_f32 v10, v10\n\t" \
  "v_mfma_f32_16x16x32_bf16 v[104:107], a[232:235], v[120:123], v[104:107]\n\t" \
  "v_exp_f32 v11, v11\n\t" \
  "v_mfma_f32_16x16x32_bf16 v[104:107], a[236:239], v[124:127], v[104:107]\n\t" \
  "v_exp_f32 v12, v12\n\t" \
  "v_mfma_f32_16x16x32_bf16 v[108:111], a[252:255], v[124:127], v[188:191]\n\t" \
  "v_exp_f32 v13, v13\n\t" \
  "v_mfma_f32_16x16x32_bf16 v[108:111], a[248:251], v[120:123], v[108:111]\n\t" \
  "v_exp_f32 v14, v14\n\t" \
  "v_mfma_f32_16x16x32_bf16 v[108:111], a[244:247], v[116:119], v[108:111]\n\t" \
  "s_waitcnt vmcnt(2) lgkmcnt(0)\n\t" \
  "v_exp_f32 v15, v15\n\t" \
  "v_mfma_f32_16x16x32_bf16 v[108:111], a[240:243], v[112:115], v[108:111]\n\t" \
  "s_barrier\n\t" \
  STA("s_memrealtime s[94:95]\n\t") \
  STA("s_memtime s[92:93]\n\t") \
  "v_mfma_f32_16x16x32_bf16 a[0:3], a[128:131], v[128:131], a[0:3]\n\t" \
  "v_exp_f32 v32, v32\n\t" \
  "v_mfma_f32_16x16x32_bf16 a[0:3], a[132:135], v[132:135], a[0:3]\n\t" \
  "v_exp_f32 v33, v33\n\t" \
  "v_mfma_f32_16x16x32_bf16 a[4:7], a[136:139], v[128:131], a[4:7]\n\t" \
  "v_exp_f32 v34, v34\n\t" \
  "v_mfma_f32_16x16x32_bf16 a[4:7], a[140:143], v[132:135], a[4:7]\n\t" \
  "v_exp_f32 v35, v35\n\t" \
  "v_mfma_f32_16x16x32_bf16 a[8:11], a[144:147], v[128:131], a[8:11]\n\t" \
  "v_exp_f32 v36, v36\n\t" \
  "v_mfma_f32_16x16x32_bf16 a[8:11], a[148:151], v[132:135], a[8:11]\n\t" \
  "v_exp_f32 v37, v37\n\t" \
  "v_mfma_f32_16x16x32_bf16 a[12:15], a[152:155], v[128:131], a[12:15]\n\t" \
  "v_exp_f32 v38, v38\n\t" \
  "v_mfma_f32_16x16x32_bf16 a[12:15], a[156:159], v[132:135], a[12:15]\n\t" \
  "v_exp_f32 v39, v39\n\t" \
  "v_mfma_f32_16x16x32_bf16 a[16:19], a[160:163], v[128:131], a[16:19]\n\t" \
  "buffer_load_dword v209, v251, s[16:19], 0 offen sc1\n\t" \
  "v_mfma_f32_16x16x32_bf16 a[16:19], a[164:167], v[132:135], a[16:19]\n\t" \
  "v_exp_f32 v40, v40\n\t" \
  "v_mfma_f32_16x16x32_bf16 a[20:23], a[168:171], v[128:131], a[20:23]\n\t" \
  "v_exp_f32 v41, v41\n\t" \
  "v_mfma_f32_16x16x32_bf16 a[20:23], a[172:175], v[132:135], a[20:23]\n\t" \
  "v_exp_f32 v42, v42\n\t" \
  "v_mfma_f32_16x16x32_bf16 a[24:27], a[176:179], v[128:131], a[24:27]\n\t" \
  STA("v_writelane_b32 v255, s94, 44\n\t") \
  STA("v_writelane_b32 v255, s92, 45\n\t") \
  "v_exp_f32 v43, v43\n\t" \
  "v_mfma_f32_16x16x32_bf16 a[24:27], a[180:183], v[132:135], a[24:27]\n\t" \
  "v_exp_f32 v44, v44\n\t" \
  "v_mfma_f32_16x16x32_bf16 a[28:31], a[184:187], v[128:131], a[28:31]\n\t" \
  "v_exp_f32 v45, v45\n\t" \
  "v_mfma_f32_16x16x32_bf16 a[28:31], a[188:191], v[132:135], a[28:31]\n\t" \
  "v_exp_f32 v46, v46\n\t" \
  "v_smfmac_f32_16x16x64_bf16 v[160:163], v[216:219], v[128:135], v208\n\t" \
  "s_add_u32 m0, s38, 65536\n\t" \
  "ds_read_b128 a[192:195], v220 offset:0\n\t" \
  "v_cvt_pk_bf16_f32 v128, v0, v1\n\t" \
  "v_mfma_f32_16x16x32_bf16 a[32:35], a[128:131], v[136:139], a[32:35]\n\t" \
  "s_add_u32 s96, s38, 114688\n\t" \
  "ds_read_b128 a[196:199], v221 offset:0\n\t" \
  "v_cvt_pk_bf16_f32 v129, v2, v3\n\t" \
  "v_mfma_f32_16x16x32_bf16 a[32:35], a[132:135], v[140:143], a[32:35]\n\t" \
  "v_exp_f32 v47, v47\n\t" \
  "v_mfma_f32_16x16x32_bf16 a[36:39], a[136:139], v[136:139], a[36:39]\n\t" \
  "v_exp_f32 v64, v64\n\t" \
  "v_mfma_f32_16x16x32_bf16 a[36:39], a[140:143], v[140:143], a[36:39]\n\t" \
  "buffer_load_dwordx4 v224, s[60:63], s96 offen offset:0 lds\n\t" \
  "v_mfma_f32_16x16x32_bf16 a[40:43], a[144:147], v[136:139], a[40:43]\n\t" \
  "ds_read_b128 a[200:203], v222 offset:0\n\t" \
  "v_cvt_pk_bf16_f32 v130, v4, v5\n\t" \
  "v_mfma_f32_16x16x32_bf16 a[40:43], a[148:151], v[140:143], a[40:43]\n\t" \
  "v_exp_f32 v65, v65\n\t" \
  "v_mfma_f32_16x16x32_bf16 a[44:47], a[152:155], v[136:139], a[44:47]\n\t" \
  "ds_read_b128 a[204:207], v223 offset:0\n\t" \
  "v_cvt_pk_bf16_f32 v131, v6, v7\n\t" \
  "v_mfma_f32_16x16x32_bf16 a[44:47], a[156:159], v[140:143], a[44:47]\n\t" \
  "v_exp_f32 v66, v66\n\t" \
  "v_mfma_f32_16x16x32_bf16 a[48:51], a[160:163], v[136:139], a[48:51]\n\t" \
  "ds_read_b128 a[208:211], v220 offset:4096\n\t" \
  "v_cvt_pk_bf16_f32 v132, v8, v9\n\t" \
  "v_mfma_f32_16x16x32_bf16 a[48:51], a[164:167], v[140:143], a[48:51]\n\t" \
  "buffer_load_dwordx4 v225, s[60:63], s96 offen offset:1024 lds\n\t" \
  "v_mfma_f32_16x16x32_bf16 a[52:55], a[168:171], v[136:139], a[52:55]\n\t" \
  "v_exp_f32 v67, v67\n\t" \
  "v_mfma_f32_16x16x32_bf16 a[52:55], a[172:175], v[140:143], a[52:55]\n\t" \
  "ds_read_b128 a[212:215], v221 offset:4096\n\t" \
  "v_cvt_pk_bf16_f32 v133, v10, v11\n\t" \
  "v_mfma_f32_16x16x32_bf16 a[56:59], a[176:179], v[136:139], a[56:59]\n\t" \
  "v_exp_f32 v68, v68\n\t" \
  "v_mfma_f32_16x16x32_bf16 a[56:59], a[180:183], v[140:143], a[56:59]\n\t" \
  "v_exp_f32 v69, v69\n\t" \
  "v_mfma_f32_16x16x32_bf16 a[60:63], a[184:187], v[136:139], a[60:63]\n\t" \
  "buffer_load_dwordx4 v226, s[60:63], s96 offen offset:2048 lds\n\t" \
  "v_mfma_f32_16x16x32_bf16 a[60:63], a[188:191], v[140:143], a[60:63]\n\t" \
  "ds_read_b128 a[216:219], v222 offset:4096\n\t" \
  "v_cvt_pk_bf16_f32 v134, v12, v13\n\t" \
  "v_smfmac_f32_16x16x64_bf16 v[164:167], v[216:219], v[136:143], v208\n\t" \
  "ds_read_b128 v[192:195], v215 offset:32768\n\t" \
  "v_cvt_pk_bf16_f32 v135, v14, v15\n\t" \
  "v_mfma_f32_16x16x32_bf16 a[64:67], a[128:131], v[144:147], a[64:67]\n\t" \
  "ds_read_b128 a[220:223], v223 offset:4096\n\t" \
  "v_cvt_pk_bf16_f32 v136, v32, v33\n\t" \
  "v_mfma_f32_16x16x32_bf16 a[64:67], a[132:135], v[148:151], a[64:67]\n\t" \
  "v_exp_f32 v70, v70\n\t" \
  "v_mfma_f32_16x16x32_bf16 a[68:71], a[136:139], v[144:147], a[68:71]\n\t" \
  "ds_read_b128 a[224:227], v220 offset:8192\n\t" \
  "v_cvt_pk_bf16_f32 v137, v34, v35\n\t" \
  "v_mfma_f32_16x16x32_bf16 a[68:71], a[140:143], v[148:151], a[68:71]\n\t" \
  "buffer_load_dwordx4 v227, s[60:63], s96 offen offset:3072 lds\n\t" \
  "v_mfma_f32_16x16x32_bf16 a[72:75], a[144:147], v[144:147], a[72:75]\n\t" \
  "s_add_u32 m0, s38, 81920\n\t" \
  "v_exp_f32 v71, v71\n\t" \
  "v_mfma_f32_16x16x32_bf16 a[72:75], a[148:151], v[148:151], a[72:75]\n\t" \
  "s_add_u32 s96, s38, 98304\n\t" \
  "ds_read_b128 a[228:231], v221 offset:8192\n\t" \
  "v_cvt_pk_bf16_f32 v138, v36, v37\n\t" \
  "v_mfma_f32_16x16x32_bf16 a[76:79], a[152:155], v[144:147], a[76:79]\n\t" \
  "v_exp_f32 v72, v72\n\t" \
  "v_mfma_f32_16x16x32_bf16 a[76:79], a[156:159], v[148:151], a[76:79]\n\t" \
  "v_exp_f32 v73, v73\n\t" \
  "v_mfma_f32_16x16x32_bf16 a[80:83], a[160:163], v[144:147], a[80:83]\n\t" \
  "buffer_load_dwordx4 v230, s[64:67], s96 offen offset:0 lds\n\t" \
  "v_mfma_f32_16x16x32_bf16 a[80:83], a[164:167], v[148:151], a[80:83]\n\t" \
  "ds_read_b128 a[232:235], v222 offset:8192\n\t" \
  "v_cvt_pk_bf16_f32 v139, v38, v39\n\t" \
  "v_mfma_f32_16x16x32_bf16 a[84:87], a[168:171], v[144:147], a[84:87]\n\t" \
  "v_exp_f32 v74, v74\n\t" \
  "v_mfma_f32_16x16x32_bf16 a[84:87], a[172:175], v[148:151], a[84:87]\n\t" \
  "ds_read_b128 a[236:239], v223 offset:8192\n\t" \
  "v_cvt_pk_bf16_f32 v140, v40, v41\n\t" \
  "v_mfma_f32_16x16x32_bf16 a[88:91], a[176:179], v[144:147], a[88:91]\n\t" \
  "v_exp_f32 v75, v75\n\t" \
  "v_mfma_f32_16x16x32_bf16 a[88:91], a[180:183], v[148:151], a[88:91]\n\t" \
  "ds_read_b128 a[240:243], v220 offset:12288\n\t" \
  "v_cvt_pk_bf16_f32 v141, v42, v43\n\t" \
  "v_mfma_f32_16x16x32_bf16 a[92:95], a[184:187], v[144:147], a[92:95]\n\t" \
  "buffer_load_dwordx4 v231, s[64:67], s96 offen offset:1024 lds\n\t" \
  "v_mfma_f32_16x16x32_bf16 a[92:95], a[188:191], v[148:151], a[92:95]\n\t" \
  "v_exp_f32 v76, v76\n\t" \
  "v_smfmac_f32_16x16x64_bf16 v[168:171], v[216:219], v[144:151], v208\n\t" \
  "ds_read_b128 a[244:247], v221 offset:12288\n\t" \
  "v_cvt_pk_bf16_f32 v142, v44, v45\n\t" \
  "v_mfma_f32_16x16x32_bf16 a[96:99], a[128:131], v[152:155], a[96:99]\n\t" \
  "v_exp_f32 v77, v77\n\t" \
  "v_mfma_f32_16x16x32_bf16 a[96:99], a[132:135], v[156:159], a[96:99]\n\t" \
  "v_exp_f32 v78, v78\n\t" \
  "v_mfma_f32_16x16x32_bf16 a[100:103], a[136:139], v[152:155], a[100:103]\n\t" \
  "buffer_load_dwordx4 v230, s[64:67], s96 offen offset:2048 lds\n\t" \
  "v_mfma_f32_16x16x32_bf16 a[100:103], a[140:143], v[156:159], a[100:103]\n\t" \
  "ds_read_b128 a[248:251], v222 offset:12288\n\t" \
  "v_cvt_pk_bf16_f32 v143, v46, v47\n\t" \
  "v_mfma_f32_16x16x32_bf16 a[104:107], a[144:147], v[152:155], a[104:107]\n\t" \
  "v_exp_f32 v79, v79\n\t" \
  "v_mfma_f32_16x16x32_bf16 a[104:107], a[148:151], v[156:159], a[104:107]\n\t" \
  "ds_read_b128 a[252:255], v223 offset:12288\n\t" \
  "v_cvt_pk_bf16_f32 v144, v64, v65\n\t" \
  "v_mfma_f32_16x16x32_bf16 a[108:111], a[152:155], v[152:155], a[108:111]\n\t" \
  "v_exp_f32 v96, v96\n\t" \
  "v_mfma_f32_16x16x32_bf16 a[108:111], a[156:159], v[156:159], a[108:111]\n\t" \
  "v_exp_f32 v97, v97\n\t" \
  "v_mfma_f32_16x16x32_bf16 a[112:115], a[160:163], v[152:155], a[112:115]\n\t" \
  "buffer_load_dwordx4 v231, s[64:67], s96 offen offset:3072 lds\n\t" \
  "v_mfma_f32_16x16x32_bf16 a[112:115], a[164:167], v[156:159], a[112:115]\n\t" \
  "v_cvt_pk_bf16_f32 v145, v66, v67\n\t" \
  "v_cvt_pk_bf16_f32 v146, v68, v69\n\t" \
  "v_mfma_f32_16x16x32_bf16 a[116:119], a[168:171], v[152:155], a[116:119]\n\t" \
  "v_exp_f32 v98, v98\n\t" \
  "v_mfma_f32_16x16x32_bf16 a[116:119], a[172:175], v[156:159], a[116:119]\n\t" \
  "v_exp_f32 v99, v99\n\t" \
  "v_mfma_f32_16x16x32_bf16 a[120:123], a[176:179], v[152:155], a[120:123]\n\t" \
  "s_add_u32 s99, s52, 6144\n\t" \
  "v_cvt_pk_bf16_f32 v147, v70, v71\n\t" \
  "v_cvt_pk_bf16_f32 v148, v72, v73\n\t" \
  "v_mfma_f32_16x16x32_bf16 a[120:123], a[180:183], v[156:159], a[120:123]\n\t" \
  "v_exp_f32 v100, v100\n\t" \
  "v_mfma_f32_16x16x32_bf16 a[124:127], a[184:187], v[152:155], a[124:127]\n\t" \
  "v_exp_f32 v101, v101\n\t" \
  "v_mfma_f32_16x16x32_bf16 a[124:127], a[188:191], v[156:159], a[124:127]\n\t" \
  "buffer_load_dword v246, v212, s[24:27], s99 offen\n\t" \
  "v_smfmac_f32_16x16x64_bf16 v[172:175], v[216:219], v[152:159], v208\n\t" \
  "s_waitcnt lgkmcnt(13)\n\t" \
  "ds_read_b128 v[196:199], v215 offset:33792\n\t" \
  "v_lshl_add_u32 v214, v233, 14, s41\n\t" \
  STA("s_memrealtime s[94:95]\n\t") \
  STA("s_memtime s[92:93]\n\t") \
  "v_mfma_f32_16x16x32_bf16 v[0:3], a[192:195], v[16:19], v[176:179]\n\t" \
  "ds_read_b64_tr_b16 a[128:129], v210 offset:0\n\t" \
  "v_min_u32 v214, .Lfz_z-.Lat_t1-4, v214\n\t" \
  "v_mfma_f32_16x16x32_bf16 v[0:3], a[196:199], v[20:23], v[0:3]\n\t" \
  "s_getpc_b64 s[84:85]\n\t" \
  ".Lat_t1:\n\t" \
  "v_exp_f32 v102, v102\n\t" \
  "v_mfma_f32_16x16x32_bf16 v[0:3], a[200:203], v[24:27], v[0:3]\n\t" \
  "global_load_dword v246, v214, s[84:85]\n\t" \
  "v_mfma_f32_16x16x32_bf16 v[0:3], a[204:207], v[28:31], v[0:3]\n\t" \
  "s_waitcnt lgkmcnt(10)\n\t" \
  "ds_read_b64_tr_b16 a[130:131], v210 offset:4096\n\t" \
  "v_cvt_pk_bf16_f32 v149, v74, v75\n\t" \
  "v_mfma_f32_16x16x32_bf16 v[4:7], a[220:223], v[28:31], v[176:179]\n\t" \
  "ds_read_b64_tr_b16 a[132:133], v210 offset:8192\n\t" \
  "v_cvt_pk_bf16_f32 v150, v76, v77\n\t" \
  "v_mfma_f32_16x16x32_bf16 v[4:7], a[216:219], v[24:27], v[4:7]\n\t" \
  "v_exp_f32 v103, v103\n\t" \
  "v_mfma_f32_16x16x32_bf16 v[4:7], a[212:215], v[20:23], v[4:7]\n\t" \
  "ds_read_b64_tr_b16 a[134:135], v210 offset:12288\n\t" \
  "v_cvt_pk_bf16_f32 v151, v78, v79\n\t" \
  "v_mfma_f32_16x16x32_bf16 v[4:7], a[208:211], v[16:19], v[4:7]\n\t" \
  "s_waitcnt lgkmcnt(9)\n\t" \
  "ds_read_b64_tr_b16 a[136:137], v211 offset:0\n\t" \
  "v_cvt_pk_bf16_f32 v152, v96, v97\n\t" \
  "v_mfma_f32_16x16x32_bf16 v[8:11], a[224:227], v[16:19], v[176:179]\n\t" \
  "v_exp_f32 v104, v104\n\t" \
  "v_mfma_f32_16x16x32_bf16 v[8:11], a[228:231], v[20:23], v[8:11]\n\t" \
  "ds_read_b64_tr_b16 a[138:139], v211 offset:4096\n\t" \
  "v_cvt_pk_bf16_f32 v153, v98, v99\n\t" \
  "v_mfma_f32_16x16x32_bf16 v[8:11], a[232:235], v[24:27], v[8:11]\n\t" \
  "v_exp_f32 v105, v105\n\t" \
  "v_mfma_f32_16x16x32_bf16 v[8:11], a[236:239], v[28:31], v[8:11]\n\t" \
  "s_waitcnt lgkmcnt(7)\n\t" \
  "ds_read_b64_tr_b16 a[140:141], v211 offset:8192\n\t" \
  "v_cvt_pk_bf16_f32 v154, v100, v101\n\t" \
  "v_mfma_f32_16x16x32_bf16 v[12:15], a[252:255], v[28:31], v[176:179]\n\t" \
  STA("v_writelane_b32 v255, s94, 46\n\t") \
  STA("v_writelane_b32 v255, s92, 47\n\t") \
  "v_exp_f32 v106, v106\n\t" \
  "v_mfma_f32_16x16x32_bf16 v[12:15], a[248:251], v[24:27], v[12:15]\n\t" \
  "ds_read_b64_tr_b16 a[142:143], v211 offset:12288\n\t" \
  "v_cvt_pk_bf16_f32 v155, v102, v103\n\t" \
  "v_mfma_f32_16x16x32_bf16 v[12:15], a[244:247], v[20:23], v[12:15]\n\t" \
  "ds_read_b64_tr_b16 a[144:145], v210 offset:256\n\t" \
  "v_cvt_pk_bf16_f32 v156, v104, v105\n\t" \
  "v_mfma_f32_16x16x32_bf16 v[12:15], a[240:243], v[16:19], v[12:15]\n\t" \
  "v_exp_f32 v107, v107\n\t" \
  "v_mfma_f32_16x16x32_bf16 v[32:35], a[192:195], v[48:51], v[180:183]\n\t" \
  "s_waitcnt vmcnt(10)\n\t" \
  "v_and_b32 v250, 0xbfffbfff, v209\n\t" \
  "v_cmp_ne_u32_e64 s[84:85], s36, v250\n\t" \
  "v_mfma_f32_16x16x32_bf16 v[32:35], a[196:199], v[52:55], v[32:35]\n\t" \
  "s_cmp_lg_u64 s[84:85], 0\n\t" \
  "s_cbranch_scc1 .Lat_v\n\t" \
  ".Lat_w:\n\t" \
  "v_mfma_f32_16x16x32_bf16 v[32:35], a[200:203], v[56:59], v[32:35]\n\t" \
  "v_and_b32 v250, 1, v233\n\t" \
  "v_lshlrev_b32 v250, 4, v250\n\t" \
  "v_mfma_f32_16x16x32_bf16 v[32:35], a[204:207], v[60:63], v[32:35]\n\t" \
  "v_lshrrev_b32 v250, v250, v209\n\t" \
  "v_and_b32 v250, 0x4000, v250\n\t" \
  "v_mfma_f32_16x16x32_bf16 v[36:39], a[220:223], v[60:63], v[180:183]\n\t" \
  "ds_read_b64_tr_b16 a[146:147], v210 offset:4352\n\t" \
  "v_cmp_ne_u32_e64 s[84:85], 0, v250\n\t" \
  STS("s_nop 4\n\t") \
  STS("v_writelane_b32 v255, s84, 60\n\t") \
  STS("v_writelane_b32 v255, s85, 61\n\t") \
  "v_mfma_f32_16x16x32_bf16 v[36:39], a[216:219], v[56:59], v[36:39]\n\t" \
  "s_bcnt1_i32_b32 s98, s84\n\t" \
  "ds_read_b64_tr_b16 a[148:149], v210 offset:8448\n\t" \
  "s_bcnt1_i32_b32 s54, s85\n\t" \
  "v_mfma_f32_16x16x32_bf16 v[36:39], a[212:215], v[52:55], v[36:39]\n\t" \
  "s_cmp_gt_u32 s98, s54\n\t" \
  "s_cselect_b32 s55, 0x2000, 0\n\t" \
  "s_cselect_b32 s98, s84, s85\n\t" \
  "v_mfma_f32_16x16x32_bf16 v[36:39], a[208:211], v[48:51], v[36:39]\n\t" \
  "s_cmp_lg_u32 s55, 0\n\t" \
  "s_cselect_b32 s84, s85, s84\n\t" \
  "s_lshr_b32 s85, s98, 16\n\t" \
  "v_mfma_f32_16x16x32_bf16 v[40:43], a[224:227], v[48:51], v[180:183]\n\t" \
  "s_bcnt1_i32_b32 s85, s85\n\t" \
  "s_lshr_b32 s84, s84, 16\n\t" \
  "s_bcnt1_i32_b32 s84, s84\n\t" \
  "v_mfma_f32_16x16x32_bf16 v[40:43], a[228:231], v[52:55], v[40:43]\n\t" \
  "s_add_u32 s84, s84, 7\n\t" \
  "s_cmp_ge_u32 s85, s84\n\t" \
  "s_cselect_b32 s54, -1, 0\n\t" \
  "v_mfma_f32_16x16x32_bf16 v[40:43], a[232:235], v[56:59], v[40:43]\n\t" \
  "s_xor_b32 s98, s42, s55\n\t" \
  "s_and_b32 s98, s98, s54\n\t" \
  "s_xor_b32 s45, s45, s98\n\t" \
  "v_mfma_f32_16x16x32_bf16 v[40:43], a[236:239], v[60:63], v[40:43]\n\t" \
  "s_xor_b32 s46, s46, s98\n\t" \
  STS("v_writelane_b32 v255, s54, 57\n\t") \
  STS("v_writelane_b32 v255, s55, 59\n\t") \
  "s_xor_b32 s98, s43, s55\n\t" \
  "s_and_b32 s98, s98, s54\n\t" \
  "v_mfma_f32_16x16x32_bf16 v[44:47], a[252:255], v[60:63], v[180:183]\n\t" \
  "s_xor_b32 s47, s47, s98\n\t" \
  "s_xor_b32 s48, s48, s55\n\t" \
  "s_xor_b32 s49, s49, s55\n\t" \
  "v_mfma_f32_16x16x32_bf16 v[44:47], a[248:251], v[56:59], v[44:47]\n\t" \
  "s_xor_b32 s98, s45, 0\n\t" \
  "ds_read_b64_tr_b16 a[150:151], v210 offset:12544\n\t" \
  "v_cvt_pk_bf16_f32 v157, v106, v107\n\t" \
  "v_mfma_f32_16x16x32_bf16 v[44:47], a[244:247], v[52:55], v[44:47]\n\t" \
  "v_exp_f32 v108, v108\n\t" \
  "v_mfma_f32_16x16x32_bf16 v[44:47], a[240:243], v[48:51], v[44:47]\n\t" \
  "ds_read_b64_tr_b16 a[152:153], v211 offset:256\n\t" \
  "v_mfma_f32_16x16x32_bf16 v[64:67], a[192:195], v[80:83], v[184:187]\n\t" \
  "v_exp_f32 v109, v109\n\t" \
  "v_mfma_f32_16x16x32_bf16 v[64:67], a[196:199], v[84:87], v[64:67]\n\t" \
  "ds_read_b64_tr_b16 a[154:155], v211 offset:4352\n\t" \
  "v_cvt_pk_bf16_f32 v158, v108, v109\n\t" \
  "v_mfma_f32_16x16x32_bf16 v[64:67], a[200:203], v[88:91], v[64:67]\n\t" \
  "ds_read_b64_tr_b16 a[156:157], v211 offset:8448\n\t" \
  "v_mfma_f32_16x16x32_bf16 v[64:67], a[204:207], v[92:95], v[64:67]\n\t" \
  "v_exp_f32 v110, v110\n\t" \
  "v_mfma_f32_16x16x32_bf16 v[68:71], a[220:223], v[92:95], v[184:187]\n\t" \
  "ds_read_b64_tr_b16 a[158:159], v211 offset:12544\n\t" \
  "v_mfma_f32_16x16x32_bf16 v[68:71], a[216:219], v[88:91], v[68:71]\n\t" \
  "v_exp_f32 v111, v111\n\t" \
  "v_mfma_f32_16x16x32_bf16 v[68:71], a[212:215], v[84:87], v[68:71]\n\t" \
  "ds_read_b64_tr_b16 a[160:161], v210 offset:512\n\t" \
  "v_cvt_pk_bf16_f32 v159, v110, v111\n\t" \
  "v_mfma_f32_16x16x32_bf16 v[68:71], a[208:211], v[80:83], v[68:71]\n\t" \
  "ds_read_b64_tr_b16 a[162:163], v210 offset:4608\n\t" \
  "v_exp_f32 v0, v0\n\t" \
  "v_mfma_f32_16x16x32_bf16 v[72:75], a[224:227], v[80:83], v[184:187]\n\t" \
  "v_exp_f32 v1, v1\n\t" \
  "v_mfma_f32_16x16x32_bf16 v[72:75], a[228:231], v[84:87], v[72:75]\n\t" \
  "ds_read_b64_tr_b16 a[164:165], v210 offset:8704\n\t" \
  "v_mfma_f32_16x16x32_bf16 v[72:75], a[232:235], v[88:91], v[72:75]\n\t" \
  "v_exp_f32 v2, v2\n\t" \
  "v_mfma_f32_16x16x32_bf16 v[72:75], a[236:239], v[92:95], v[72:75]\n\t" \
  "ds_read_b64_tr_b16 a[166:167], v210 offset:12800\n\t" \
  "v_mfma_f32_16x16x32_bf16 v[76:79], a[252:255], v[92:95], v[184:187]\n\t" \
  "ds_read_b64_tr_b16 a[168:169], v211 offset:512\n\t" \
  "v_exp_f32 v3, v3\n\t" \
  "v_mfma_f32_16x16x32_bf16 v[76:79], a[248:251], v[88:91], v[76:79]\n\t" \
  "v_exp_f32 v4, v4\n\t" \
  "v_mfma_f32_16x16x32_bf16 v[76:79], a[244:247], v[84:87], v[76:79]\n\t" \
  "ds_read_b64_tr_b16 a[170:171], v211 offset:4608\n\t" \
  "v_mfma_f32_16x16x32_bf16 v[76:79], a[240:243], v[80:83], v[76:79]\n\t" \
  "ds_read_b64_tr_b16 a[172:173], v211 offset:8704\n\t" \
  "v_exp_f32 v5, v5\n\t" \
  "v_mfma_f32_16x16x32_bf16 v[96:99], a[192:195], v[112:115], v[188:191]\n\t" \
  "v_exp_f32 v6, v6\n\t" \
  "v_mfma_f32_16x16x32_bf16 v[96:99], a[196:199], v[116:119], v[96:99]\n\t" \
  "ds_read_b64_tr_b16 a[174:175], v211 offset:12800\n\t" \
  "v_mfma_f32_16x16x32_bf16 v[96:99], a[200:203], v[120:123], v[96:99]\n\t" \
  "ds_read_b64_tr_b16 a[176:177], v210 offset:768\n\t" \
  "v_exp_f32 v7, v7\n\t" \
  "v_mfma_f32_16x16x32_bf16 v[96:99], a[204:207], v[124:127], v[96:99]\n\t" \
  "v_exp_f32 v8, v8\n\t" \
  "v_mfma_f32_16x16x32_bf16 v[100:103], a[220:223], v[124:127], v[188:191]\n\t" \
  "v_exp_f32 v9, v9\n\t" \
  "v_mfma_f32_16x16x32_bf16 v[100:103], a[216:219], v[120:123], v[100:103]\n\t" \
  "ds_read_b64_tr_b16 a[178:179], v210 offset:4864\n\t" \
  "v_mfma_f32_16x16x32_bf16 v[100:103], a[212:215], v[116:119], v[100:103]\n\t" \
  "ds_read_b64_tr_b16 a[180:181], v210 offset:8960\n\t" \
  "v_mfma_f32_16x16x32_bf16 v[100:103], a[208:211], v[112:115], v[100:103]\n\t" \
  "ds_read_b64_tr_b16 a[182:183], v210 offset:13056\n\t" \
  "v_exp_f32 v10, v10\n\t" \
  "v_mfma_f32_16x16x32_bf16 v[104:107], a[224:227], v[112:115], v[188:191]\n\t" \
  "ds_read_b64_tr_b16 a[184:185], v211 offset:768\n\t" \
  "v_exp_f32 v11, v11\n\t" \
  "v_mfma_f32_16x16x32_bf16 v[104:107], a[228:231], v[116:119], v[104:107]\n\t" \
  "ds_read_b64_tr_b16 a[186:187], v211 offset:4864\n\t" \
  "v_exp_f32 v12, v12\n\t" \
  "v_mfma_f32_16x16x32_bf16 v[104:107], a[232:235], v[120:123], v[104:107]\n\t" \
  "v_exp_f32 v13, v13\n\t" \
  "v_mfma_f32_16x16x32_bf16 v[104:107], a[236:239], v[124:127], v[104:107]\n\t" \
  "v_exp_f32 v14, v14\n\t" \
  "v_mfma_f32_16x16x32_bf16 v[108:111], a[252:255], v[124:127], v[188:191]\n\t" \
  "ds_read_b64_tr_b16 a[188:189], v211 offset:8960\n\t" \
  "v_mfma_f32_16x16x32_bf16 v[108:111], a[248:251], v[120:123], v[108:111]\n\t" \
  "ds_read_b64_tr_b16 a[190:191], v211 offset:13056\n\t" \
  "v_exp_f32 v15, v15\n\t" \
  "v_mfma_f32_16x16x32_bf16 v[108:111], a[244:247], v[116:119], v[108:111]\n\t" \
  "s_waitcnt vmcnt(2) lgkmcnt(0)\n\t" \
  "v_exp_f32 v32, v32\n\t" \
  "v_mfma_f32_16x16x32_bf16 v[108:111], a[240:243], v[112:115], v[108:111]\n\t" \
  "s_barrier\n\t" \
  STA("s_memrealtime s[94:95]\n\t") \
  STA("s_memtime s[92:93]\n\t") \
  "v_mfma_f32_16x16x32_bf16 a[0:3], a[128:131], v[128:131], a[0:3]\n\t" \
  "v_exp_f32 v33, v33\n\t" \
  "v_mfma_f32_16x16x32_bf16 a[0:3], a[132:135], v[132:135], a[0:3]\n\t" \
  "v_exp_f32 v34, v34\n\t" \
  "v_mfma_f32_16x16x32_bf16 a[4:7], a[136:139], v[128:131], a[4:7]\n\t" \
  "v_exp_f32 v35, v35\n\t" \
  "v_mfma_f32_16x16x32_bf16 a[4:7], a[140:143], v[132:135], a[4:7]\n\t" \
  "v_exp_f32 v36, v36\n\t" \
  "v_mfma_f32_16x16x32_bf16 a[8:11], a[144:147], v[128:131], a[8:11]\n\t" \
  "v_exp_f32 v37, v37\n\t" \
  "v_mfma_f32_16x16x32_bf16 a[8:11], a[148:151], v[132:135], a[8:11]\n\t" \
  "v_exp_f32 v38, v38\n\t" \
  "v_mfma_f32_16x16x32_bf16 a[12:15], a[152:155], v[128:131], a[12:15]\n\t" \
  "v_exp_f32 v39, v39\n\t" \
  "v_mfma_f32_16x16x32_bf16 a[12:15], a[156:159], v[132:135], a[12:15]\n\t" \
  "v_exp_f32 v40, v40\n\t" \
  "v_mfma_f32_16x16x32_bf16 a[16:19], a[160:163], v[128:131], a[16:19]\n\t" \
  "v_exp_f32 v41, v41\n\t" \
  "v_mfma_f32_16x16x32_bf16 a[16:19], a[164:167], v[132:135], a[16:19]\n\t" \
  "v_exp_f32 v42, v42\n\t" \
  "v_mfma_f32_16x16x32_bf16 a[20:23], a[168:171], v[128:131], a[20:23]\n\t" \
  "v_exp_f32 v43, v43\n\t" \
  "v_mfma_f32_16x16x32_bf16 a[20:23], a[172:175], v[132:135], a[20:23]\n\t" \
  "v_exp_f32 v44, v44\n\t" \
  "v_mfma_f32_16x16x32_bf16 a[24:27], a[176:179], v[128:131], a[24:27]\n\t" \
  STA("v_writelane_b32 v255, s94, 48\n\t") \
  STA("v_writelane_b32 v255, s92, 49\n\t") \
  "v_exp_f32 v45, v45\n\t" \
  "v_mfma_f32_16x16x32_bf16 a[24:27], a[180:183], v[132:135], a[24:27]\n\t" \
  "v_exp_f32 v46, v46\n\t" \
  "v_mfma_f32_16x16x32_bf16 a[28:31], a[184:187], v[128:131], a[28:31]\n\t" \
  "ds_read_b128 a[192:195], v220 offset:16384\n\t" \
  "v_exp_f32 v47, v47\n\t" \
  "v_mfma_f32_16x16x32_bf16 a[28:31], a[188:191], v[132:135], a[28:31]\n\t" \
  "v_exp_f32 v64, v64\n\t" \
  "v_smfmac_f32_16x16x64_bf16 v[160:163], v[216:219], v[128:135], v208\n\t" \
  "s_add_u32 m0, s38, 147456\n\t" \
  "ds_read_b128 v[200:203], v215 offset:34816\n\t" \
  "v_cvt_pk_bf16_f32 v128, v0, v1\n\t" \
  "v_mfma_f32_16x16x32_bf16 a[32:35], a[128:131], v[136:139], a[32:35]\n\t" \
  "s_add_u32 s96, s38, 114688\n\t" \
  "ds_read_b128 a[196:199], v221 offset:16384\n\t" \
  "v_exp_f32 v65, v65\n\t" \
  "v_mfma_f32_16x16x32_bf16 a[32:35], a[132:135], v[140:143], a[32:35]\n\t" \
  "v_exp_f32 v66, v66\n\t" \
  "v_mfma_f32_16x16x32_bf16 a[36:39], a[136:139], v[136:139], a[36:39]\n\t" \
  "v_exp_f32 v67, v67\n\t" \
  "v_mfma_f32_16x16x32_bf16 a[36:39], a[140:143], v[140:143], a[36:39]\n\t" \
  "buffer_load_dwordx4 v230, s[64:67], s96 offen offset:0 lds\n\t" \
  "v_mfma_f32_16x16x32_bf16 a[40:43], a[144:147], v[136:139], a[40:43]\n\t" \
  "ds_read_b128 a[200:203], v222 offset:16384\n\t" \
  "v_exp_f32 v68, v68\n\t" \
  "v_mfma_f32_16x16x32_bf16 a[40:43], a[148:151], v[140:143], a[40:43]\n\t" \
  "v_exp_f32 v69, v69\n\t" \
  "v_mfma_f32_16x16x32_bf16 a[44:47], a[152:155], v[136:139], a[44:47]\n\t" \
  "v_exp_f32 v70, v70\n\t" \
  "v_mfma_f32_16x16x32_bf16 a[44:47], a[156:159], v[140:143], a[44:47]\n\t" \
  "buffer_load_dwordx4 v231, s[64:67], s96 offen offset:1024 lds\n\t" \
  "v_mfma_f32_16x16x32_bf16 a[48:51], a[160:163], v[136:139], a[48:51]\n\t" \
  "ds_read_b128 a[204:207], v223 offset:16384\n\t" \
  "v_exp_f32 v71, v71\n\t" \
  "v_mfma_f32_16x16x32_bf16 a[48:51], a[164:167], v[140:143], a[48:51]\n\t" \
  ".Lhs9:\n\ts_cbranch_execz .Lhs9n\n\t" \
  "v_exp_f32 v72, v72\n\t" \
  "v_mfma_f32_16x16x32_bf16 a[52:55], a[168:171], v[136:139], a[52:55]\n\t" \
  "v_exp_f32 v73, v73\n\t" \
  "v_mfma_f32_16x16x32_bf16 a[52:55], a[172:175], v[140:143], a[52:55]\n\t" \
  "buffer_load_dwordx4 v230, s[64:67], s96 offen offset:2048 lds\n\t" \
  "v_mfma_f32_16x16x32_bf16 a[56:59], a[176:179], v[136:139], a[56:59]\n\t" \
  "v_exp_f32 v74, v74\n\t" \
  "v_mfma_f32_16x16x32_bf16 a[56:59], a[180:183], v[140:143], a[56:59]\n\t" \
  "ds_read_b128 a[208:211], v220 offset:20480\n\t" \
  "v_cvt_pk_bf16_f32 v129, v2, v3\n\t" \
  "v_mfma_f32_16x16x32_bf16 a[60:63], a[184:187], v[136:139], a[60:63]\n\t" \
  "buffer_load_dwordx4 v231, s[64:67], s96 offen offset:3072 lds\n\t" \
  "v_mfma_f32_16x16x32_bf16 a[60:63], a[188:191], v[140:143], a[60:63]\n\t" \
  "s_lshl_b32 s64, s39, 17\n\t" \
  "ds_read_b128 a[212:215], v221 offset:20480\n\t" \
  "v_cvt_pk_bf16_f32 v130, v4, v5\n\t" \
  "v_smfmac_f32_16x16x64_bf16 v[164:167], v[216:219], v[136:143], v208\n\t" \
  "s_add_u32 s96, s40, 0\n\t" \
  "s_and_b32 s96, s96, 0x1c0000\n\t" \
  "s_add_u32 s96, s96, s51\n\t" \
  "v_mfma_f32_16x16x32_bf16 a[64:67], a[128:131], v[144:147], a[64:67]\n\t" \
  "s_add_u32 m0, s38, 0\n\t" \
  "ds_read_b128 a[216:219], v222 offset:20480\n\t" \
  "v_exp_f32 v75, v75\n\t" \
  "v_mfma_f32_16x16x32_bf16 a[64:67], a[132:135], v[148:151], a[64:67]\n\t" \
  "buffer_load_dwordx4 v240, s[24:27], s96 offen offset:0 lds\n\t" \
  "v_mfma_f32_16x16x32_bf16 a[68:71], a[136:139], v[144:147], a[68:71]\n\t" \
  "s_bfe_u32 s70, s46, 0x20014\n\t" \
  "ds_read_b128 a[220:223], v223 offset:20480\n\t" \
  "v_exp_f32 v76, v76\n\t" \
  "v_mfma_f32_16x16x32_bf16 a[68:71], a[140:143], v[148:151], a[68:71]\n\t" \
  "s_lshl_b32 s70, s70, 14\n\t" \
  "ds_read_b128 a[224:227], v220 offset:24576\n\t" \
  "v_exp_f32 v77, v77\n\t" \
  "v_mfma_f32_16x16x32_bf16 a[72:75], a[144:147], v[144:147], a[72:75]\n\t" \
  "s_xor_b32 s67, s70, 49152\n\t" \
  "v_exp_f32 v78, v78\n\t" \
  "v_mfma_f32_16x16x32_bf16 a[72:75], a[148:151], v[148:151], a[72:75]\n\t" \
  "buffer_load_dwordx4 v241, s[24:27], s96 offen offset:1024 lds\n\t" \
  "v_mfma_f32_16x16x32_bf16 a[76:79], a[152:155], v[144:147], a[76:79]\n\t" \
  "s_lshl1_add_u32 s60, s67, s46\n\t" \
  "v_exp_f32 v79, v79\n\t" \
  "v_mfma_f32_16x16x32_bf16 a[76:79], a[156:159], v[148:151], a[76:79]\n\t" \
  "s_xor_b32 s68, s70, 32768\n\t" \
  "v_exp_f32 v96, v96\n\t" \
  "v_mfma_f32_16x16x32_bf16 a[80:83], a[160:163], v[144:147], a[80:83]\n\t" \
  "buffer_load_dwordx4 v248, s[24:27], s96 offen offset:2048 lds\n\t" \
  "v_mfma_f32_16x16x32_bf16 a[80:83], a[164:167], v[148:151], a[80:83]\n\t" \
  "s_lshl1_add_u32 s61, s68, s46\n\t" \
  "ds_read_b128 a[228:231], v221 offset:24576\n\t" \
  "v_exp_f32 v97, v97\n\t" \
  "v_mfma_f32_16x16x32_bf16 a[84:87], a[168:171], v[144:147], a[84:87]\n\t" \
  "s_xor_b32 s69, s70, 16384\n\t" \
  "ds_read_b128 a[232:235], v222 offset:24576\n\t" \
  "v_cvt_pk_bf16_f32 v131, v6, v7\n\t" \
  "v_mfma_f32_16x16x32_bf16 a[84:87], a[172:175], v[148:151], a[84:87]\n\t" \
  "s_lshl1_add_u32 s62, s69, s46\n\t" \
  "v_exp_f32 v98, v98\n\t" \
  "v_mfma_f32_16x16x32_bf16 a[88:91], a[176:179], v[144:147], a[88:91]\n\t" \
  "buffer_load_dwordx4 v249, s[24:27], s96 offen offset:3072 lds\n\t" \
  "v_mfma_f32_16x16x32_bf16 a[88:91], a[180:183], v[148:151], a[88:91]\n\t" \
  "s_lshl1_add_u32 s63, s70, s46\n\t" \
  "ds_read_b128 a[236:239], v223 offset:24576\n\t" \
  "v_exp_f32 v99, v99\n\t" \
  "v_mfma_f32_16x16x32_bf16 a[92:95], a[184:187], v[144:147], a[92:95]\n\t" \
  "s_add_u32 s96, s40, 262144\n\t" \
  "s_and_b32 s96, s96, 0x1c0000\n\t" \
  "s_add_u32 s96, s96, s51\n\t" \
  "v_mfma_f32_16x16x32_bf16 a[92:95], a[188:191], v[148:151], a[92:95]\n\t" \
  "s_add_u32 m0, s38, 49152\n\t" \
  "ds_read_b128 a[240:243], v220 offset:28672\n\t" \
  "v_cvt_pk_bf16_f32 v132, v8, v9\n\t" \
  "v_smfmac_f32_16x16x64_bf16 v[168:171], v[216:219], v[144:151], v208\n\t" \
  "buffer_load_dwordx4 v240, s[24:27], s96 offen offset:0 lds\n\t" \
  "v_mfma_f32_16x16x32_bf16 a[96:99], a[128:131], v[152:155], a[96:99]\n\t" \
  "s_mul_i32 s65, s39, 508\n\t" \
  "ds_read_b128 a[244:247], v221 offset:28672\n\t" \
  "v_exp_f32 v100, v100\n\t" \
  "v_mfma_f32_16x16x32_bf16 a[96:99], a[132:135], v[156:159], a[96:99]\n\t" \
  "s_add_u32 s65, s65, s49\n\t" \
  "v_exp_f32 v101, v101\n\t" \
  "v_mfma_f32_16x16x32_bf16 a[100:103], a[136:139], v[152:155], a[100:103]\n\t" \
  "buffer_load_dwordx4 v241, s[24:27], s96 offen offset:1024 lds\n\t" \
  "v_mfma_f32_16x16x32_bf16 a[100:103], a[140:143], v[156:159], a[100:103]\n\t" \
  "s_lshl_b32 s80, s39, 2\n\t" \
  "ds_read_b128 a[248:251], v222 offset:28672\n\t" \
  "v_cvt_pk_bf16_f32 v133, v10, v11\n\t" \
  "v_mfma_f32_16x16x32_bf16 a[104:107], a[144:147], v[152:155], a[104:107]\n\t" \
  "s_add_u32 s80, s80, s3\n\t" \
  "v_exp_f32 v102, v102\n\t" \
  "v_mfma_f32_16x16x32_bf16 a[104:107], a[148:151], v[156:159], a[104:107]\n\t" \
  "s_lshl_b32 s80, s80, 6\n\t" \
  "ds_read_b128 a[252:255], v223 offset:28672\n\t" \
  "v_cvt_pk_bf16_f32 v134, v12, v13\n\t" \
  "v_mfma_f32_16x16x32_bf16 a[108:111], a[152:155], v[152:155], a[108:111]\n\t" \
  "buffer_load_dwordx4 v248, s[24:27], s96 offen offset:2048 lds\n\t" \
  "v_mfma_f32_16x16x32_bf16 a[108:111], a[156:159], v[156:159], a[108:111]\n\t" \
  "s_add_u32 s76, s38, 147456\n\t" \
  "v_cvt_pk_bf16_f32 v135, v14, v15\n\t" \
  "v_exp_f32 v103, v103\n\t" \
  "v_mfma_f32_16x16x32_bf16 a[112:115], a[160:163], v[152:155], a[112:115]\n\t" \
  "s_lshl_b32 s75, s80, 2\n\t" \
  "v_exp_f32 v104, v104\n\t" \
  "v_mfma_f32_16x16x32_bf16 a[112:115], a[164:167], v[156:159], a[112:115]\n\t" \
  "s_add_u32 s75, s75, 2228224\n\t" \
  "v_exp_f32 v105, v105\n\t" \
  "v_mfma_f32_16x16x32_bf16 a[116:119], a[168:171], v[152:155], a[116:119]\n\t" \
  "buffer_load_dwordx4 v249, s[24:27], s96 offen offset:3072 lds\n\t" \
  "v_mfma_f32_16x16x32_bf16 a[116:119], a[172:175], v[156:159], a[116:119]\n\t" \
  "s_lshr_b32 s71, s67, 7\n\t" \
  "v_cvt_pk_bf16_f32 v136, v32, v33\n\t" \
  "v_cvt_pk_bf16_f32 v137, v34, v35\n\t" \
  "v_mfma_f32_16x16x32_bf16 a[120:123], a[176:179], v[152:155], a[120:123]\n\t" \
  "s_add_u32 s71, s71, s48\n\t" \
  "v_cvt_pk_bf16_f32 v138, v36, v37\n\t" \
  "v_exp_f32 v106, v106\n\t" \
  "v_mfma_f32_16x16x32_bf16 a[120:123], a[180:183], v[156:159], a[120:123]\n\t" \
  "s_lshr_b32 s72, s68, 7\n\t" \
  "v_exp_f32 v107, v107\n\t" \
  "v_mfma_f32_16x16x32_bf16 a[124:127], a[184:187], v[152:155], a[124:127]\n\t" \
  "s_add_u32 s72, s72, s48\n\t" \
  "v_exp_f32 v108, v108\n\t" \
  "v_mfma_f32_16x16x32_bf16 a[124:127], a[188:191], v[156:159], a[124:127]\n\t" \
  "s_lshr_b32 s66, s69, 7\n\t" \
  "v_cvt_pk_bf16_f32 v139, v38, v39\n\t" \
  "v_cvt_pk_bf16_f32 v140, v40, v41\n\t" \
  "v_smfmac_f32_16x16x64_bf16 v[172:175], v[216:219], v[152:159], v208\n\t" \
  "s_waitcnt lgkmcnt(12)\n\t" \
  "ds_read_b128 v[204:207], v215 offset:35840\n\t" \
  "v_cvt_pk_bf16_f32 v141, v42, v43\n\t" \
  STA("s_memrealtime s[94:95]\n\t") \
  STA("s_memtime s[92:93]\n\t") \
  "v_mfma_f32_16x16x32_bf16 v[0:3], a[192:195], v[16:19], v[176:179]\n\t" \
  "ds_read_b64_tr_b16 a[128:129], v228 offset:0\n\t" \
  "ds_read_b64_tr_b16 a[130:131], v228 offset:4096\n\t" \
  "ds_read_b64_tr_b16 a[132:133], v228 offset:8192\n\t" \
  "ds_read_b64_tr_b16 a[134:135], v228 offset:12288\n\t" \
  "v_mfma_f32_16x16x32_bf16 v[0:3], a[196:199], v[20:23], v[0:3]\n\t" \
  "s_add_u32 s66, s66, s48\n\t" \
  "ds_read_b64_tr_b16 a[136:137], v229 offset:0\n\t" \
  "v_cvt_pk_bf16_f32 v142, v44, v45\n\t" \
  "v_mfma_f32_16x16x32_bf16 v[0:3], a[200:203], v[24:27], v[0:3]\n\t" \
  "s_lshr_b32 s73, s70, 7\n\t" \
  "ds_read_b64_tr_b16 a[138:139], v229 offset:4096\n\t" \
  "v_exp_f32 v109, v109\n\t" \
  "v_mfma_f32_16x16x32_bf16 v[0:3], a[204:207], v[28:31], v[0:3]\n\t" \
  "s_waitcnt lgkmcnt(15)\n\t" \
  "ds_read_b64_tr_b16 a[140:141], v229 offset:8192\n\t" \
  "v_cvt_pk_bf16_f32 v143, v46, v47\n\t" \
  "v_mfma_f32_16x16x32_bf16 v[4:7], a[220:223], v[28:31], v[176:179]\n\t" \
  "s_add_u32 s73, s73, s48\n\t" \
  "ds_read_b64_tr_b16 a[142:143], v229 offset:12288\n\t" \
  "v_cvt_pk_bf16_f32 v144, v64, v65\n\t" \
  "v_mfma_f32_16x16x32_bf16 v[4:7], a[216:219], v[24:27], v[4:7]\n\t" \
  "s_lshl3_add_u32 s67, s67, s47\n\t" \
  "ds_read_b64_tr_b16 a[144:145], v228 offset:256\n\t" \
  "v_exp_f32 v110, v110\n\t" \
  "v_mfma_f32_16x16x32_bf16 v[4:7], a[212:215], v[20:23], v[4:7]\n\t" \
  "s_lshl3_add_u32 s68, s68, s47\n\t" \
  "ds_read_b64_tr_b16 a[146:147], v228 offset:4352\n\t" \
  "v_exp_f32 v111, v111\n\t" \
  "v_mfma_f32_16x16x32_bf16 v[4:7], a[208:211], v[16:19], v[4:7]\n\t" \
  "s_waitcnt lgkmcnt(15)\n\t" \
  "ds_read_b64_tr_b16 a[148:149], v228 offset:8448\n\t" \
  "v_cvt_pk_bf16_f32 v145, v66, v67\n\t" \
  "v_mfma_f32_16x16x32_bf16 v[8:11], a[224:227], v[16:19], v[176:179]\n\t" \
  "s_add_u32 s68, s68, 4096\n\t" \
  "v_exp_f32 v0, v0\n\t" \
  "v_mfma_f32_16x16x32_bf16 v[8:11], a[228:231], v[20:23], v[8:11]\n\t" \
  "s_lshl3_add_u32 s69, s69, s47\n\t" \
  "ds_read_b64_tr_b16 a[150:151], v228 offset:12544\n\t" \
  "v_cvt_pk_bf16_f32 v146, v68, v69\n\t" \
  "v_mfma_f32_16x16x32_bf16 v[8:11], a[232:235], v[24:27], v[8:11]\n\t" \
  "s_add_u32 s69, s69, 16384\n\t" \
  "ds_read_b64_tr_b16 a[152:153], v229 offset:256\n\t" \
  "v_cvt_pk_bf16_f32 v147, v70, v71\n\t" \
  "v_mfma_f32_16x16x32_bf16 v[8:11], a[236:239], v[28:31], v[8:11]\n\t" \
  "s_waitcnt lgkmcnt(14)\n\t" \
  "v_cvt_pk_bf16_f32 v148, v72, v73\n\t" \
  "v_exp_f32 v1, v1\n\t" \
  "v_mfma_f32_16x16x32_bf16 v[12:15], a[252:255], v[28:31], v[176:179]\n\t" \
  STA("v_writelane_b32 v255, s94, 50\n\t") \
  STA("v_writelane_b32 v255, s92, 51\n\t") \
  "s_lshl3_add_u32 s70, s70, s47\n\t" \
  "v_exp_f32 v2, v2\n\t" \
  "v_mfma_f32_16x16x32_bf16 v[12:15], a[248:251], v[24:27], v[12:15]\n\t" \
  "s_barrier\n\t" \
  "v_mfma_f32_16x16x32_bf16 v[12:15], a[244:247], v[20:23], v[12:15]\n\t" \
  "v_exp_f32 v3, v3\n\t" \
  "v_mfma_f32_16x16x32_bf16 v[12:15], a[240:243], v[16:19], v[12:15]\n\t" \
  "ds_read_b64_tr_b16 a[154:155], v229 offset:4352\n\t" \
  "v_cvt_pk_bf16_f32 v149, v74, v75\n\t" \
  "v_mfma_f32_16x16x32_bf16 v[32:35], a[192:195], v[48:51], v[180:183]\n\t" \
  "s_add_u32 s96, s40, 0\n\t" \
  "v_exp_f32 v4, v4\n\t" \
  "v_mfma_f32_16x16x32_bf16 v[32:35], a[196:199], v[52:55], v[32:35]\n\t" \
  "s_and_b32 s96, s96, 0x1c0000\n\t" \
  "ds_read_b64_tr_b16 a[156:157], v229 offset:8448\n\t" \
  "v_cvt_pk_bf16_f32 v150, v76, v77\n\t" \
  "v_mfma_f32_16x16x32_bf16 v[32:35], a[200:203], v[56:59], v[32:35]\n\t" \
  "s_add_u32 s96, s96, s77\n\t" \
  "v_cvt_pk_bf16_f32 v151, v78, v79\n\t" \
  "s_add_u32 m0, s38, 98304\n\t" \
  "v_mfma_f32_16x16x32_bf16 v[32:35], a[204:207], v[60:63], v[32:35]\n\t" \
  "ds_read_b64_tr_b16 a[158:159], v229 offset:12544\n\t" \
  "v_cvt_pk_bf16_f32 v152, v96, v97\n\t" \
  "v_mfma_f32_16x16x32_bf16 v[36:39], a[220:223], v[60:63], v[180:183]\n\t" \
  "buffer_load_dwordx4 v240, s[24:27], s96 offen offset:0 lds\n\t" \
  "v_mfma_f32_16x16x32_bf16 v[36:39], a[216:219], v[56:59], v[36:39]\n\t" \
  "v_exp_f32 v5, v5\n\t" \
  "v_mfma_f32_16x16x32_bf16 v[36:39], a[212:215], v[52:55], v[36:39]\n\t" \
  "v_exp_f32 v6, v6\n\t" \
  "v_mfma_f32_16x16x32_bf16 v[36:39], a[208:211], v[48:51], v[36:39]\n\t" \
  "ds_read_b64_tr_b16 a[160:161], v228 offset:512\n\t" \
  "v_cvt_pk_bf16_f32 v153, v98, v99\n\t" \
  "v_mfma_f32_16x16x32_bf16 v[40:43], a[224:227], v[48:51], v[180:183]\n\t" \
  "buffer_load_dwordx4 v241, s[24:27], s96 offen offset:1024 lds\n\t" \
  "v_mfma_f32_16x16x32_bf16 v[40:43], a[228:231], v[52:55], v[40:43]\n\t" \
  "v_exp_f32 v7, v7\n\t" \
  "v_mfma_f32_16x16x32_bf16 v[40:43], a[232:235], v[56:59], v[40:43]\n\t" \
  "v_exp_f32 v8, v8\n\t" \
  "v_mfma_f32_16x16x32_bf16 v[40:43], a[236:239], v[60:63], v[40:43]\n\t" \
  "buffer_load_dwordx4 v248, s[24:27], s96 offen offset:2048 lds\n\t" \
  "v_mfma_f32_16x16x32_bf16 v[44:47], a[252:255], v[60:63], v[180:183]\n\t" \
  "v_exp_f32 v9, v9\n\t" \
  "v_mfma_f32_16x16x32_bf16 v[44:47], a[248:251], v[56:59], v[44:47]\n\t" \
  "ds_read_b64_tr_b16 a[162:163], v228 offset:4608\n\t" \
  "v_cvt_pk_bf16_f32 v154, v100, v101\n\t" \
  "v_mfma_f32_16x16x32_bf16 v[44:47], a[244:247], v[52:55], v[44:47]\n\t" \
  "buffer_load_dwordx4 v249, s[24:27], s96 offen offset:3072 lds\n\t" \
  "v_mfma_f32_16x16x32_bf16 v[44:47], a[240:243], v[48:51], v[44:47]\n\t" \
  "s_add_u32 s96, s40, 524288\n\t" \
  "ds_read_b64_tr_b16 a[164:165], v228 offset:8704\n\t" \
  "v_cvt_pk_bf16_f32 v155, v102, v103\n\t" \
  "v_mfma_f32_16x16x32_bf16 v[64:67], a[192:195], v[80:83], v[184:187]\n\t" \
  "s_and_b32 s96, s96, 0x1c0000\n\t" \
  "s_add_u32 s96, s96, s51\n\t" \
  "s_add_u32 m0, s38, 131072\n\t" \
  "v_mfma_f32_16x16x32_bf16 v[64:67], a[196:199], v[84:87], v[64:67]\n\t" \
  "ds_read_b64_tr_b16 a[166:167], v228 offset:12800\n\t" \
  "v_cvt_pk_bf16_f32 v156, v104, v105\n\t" \
  "v_mfma_f32_16x16x32_bf16 v[64:67], a[200:203], v[88:91], v[64:67]\n\t" \
  "s_waitcnt lgkmcnt(15)\n\t" \
  "buffer_load_dwordx4 v240, s[24:27], s96 offen offset:0 lds\n\t" \
  "v_mfma_f32_16x16x32_bf16 v[64:67], a[204:207], v[92:95], v[64:67]\n\t" \
  "ds_read_b64_tr_b16 a[168:169], v229 offset:512\n\t" \
  "v_cvt_pk_bf16_f32 v157, v106, v107\n\t" \
  "v_mfma_f32_16x16x32_bf16 v[68:71], a[220:223], v[92:95], v[184:187]\n\t" \
  "v_exp_f32 v10, v10\n\t" \
  "v_mfma_f32_16x16x32_bf16 v[68:71], a[216:219], v[88:91], v[68:71]\n\t" \
  "buffer_load_dwordx4 v241, s[24:27], s96 offen offset:1024 lds\n\t" \
  "v_mfma_f32_16x16x32_bf16 v[68:71], a[212:215], v[84:87], v[68:71]\n\t" \
  "ds_read_b64_tr_b16 a[170:171], v229 offset:4608\n\t" \
  "v_cvt_pk_bf16_f32 v158, v108, v109\n\t" \
  "v_mfma_f32_16x16x32_bf16 v[68:71], a[208:211], v[80:83], v[68:71]\n\t" \
  "ds_read_b64_tr_b16 a[172:173], v229 offset:8704\n\t" \
  "v_cvt_pk_bf16_f32 v159, v110, v111\n\t" \
  "v_mfma_f32_16x16x32_bf16 v[72:75], a[224:227], v[80:83], v[184:187]\n\t" \
  "buffer_load_dwordx4 v248, s[24:27], s96 offen offset:2048 lds\n\t" \
  "v_mfma_f32_16x16x32_bf16 v[72:75], a[228:231], v[84:87], v[72:75]\n\t" \
  "v_exp_f32 v11, v11\n\t" \
  "v_mfma_f32_16x16x32_bf16 v[72:75], a[232:235], v[88:91], v[72:75]\n\t" \
  "ds_read_b64_tr_b16 a[174:175], v229 offset:12800\n\t" \
  "v_cvt_pk_bf16_f32 v0, v0, v1\n\t" \
  "v_mfma_f32_16x16x32_bf16 v[72:75], a[236:239], v[92:95], v[72:75]\n\t" \
  "v_exp_f32 v12, v12\n\t" \
  "v_mfma_f32_16x16x32_bf16 v[76:79], a[252:255], v[92:95], v[184:187]\n\t" \
  "buffer_load_dwordx4 v249, s[24:27], s96 offen offset:3072 lds\n\t" \
  "v_mfma_f32_16x16x32_bf16 v[76:79], a[248:251], v[88:91], v[76:79]\n\t" \
  "s_add_u32 s96, s40, 262144\n\t" \
  "s_and_b32 s96, s96, 0x1c0000\n\t" \
  "s_add_u32 s96, s96, s77\n\t" \
  "v_mfma_f32_16x16x32_bf16 v[76:79], a[244:247], v[84:87], v[76:79]\n\t" \
  "s_add_u32 m0, s38, 65536\n\t" \
  "ds_read_b64_tr_b16 a[176:177], v228 offset:768\n\t" \
  "v_cvt_pk_bf16_f32 v1, v2, v3\n\t" \
  "v_mfma_f32_16x16x32_bf16 v[76:79], a[240:243], v[80:83], v[76:79]\n\t" \
  "buffer_load_dwordx4 v240, s[24:27], s96 offen offset:0 lds\n\t" \
  "v_mfma_f32_16x16x32_bf16 v[96:99], a[192:195], v[112:115], v[188:191]\n\t" \
  "ds_read_b64_tr_b16 a[178:179], v228 offset:4864\n\t" \
  "v_cvt_pk_bf16_f32 v2, v4, v5\n\t" \
  "v_mfma_f32_16x16x32_bf16 v[96:99], a[196:199], v[116:119], v[96:99]\n\t" \
  "ds_read_b64_tr_b16 a[180:181], v228 offset:8960\n\t" \
  "v_exp_f32 v13, v13\n\t" \
  "v_mfma_f32_16x16x32_bf16 v[96:99], a[200:203], v[120:123], v[96:99]\n\t" \
  "buffer_load_dwordx4 v241, s[24:27], s96 offen offset:1024 lds\n\t" \
  "v_mfma_f32_16x16x32_bf16 v[96:99], a[204:207], v[124:127], v[96:99]\n\t" \
  "ds_read_b64_tr_b16 a[182:183], v228 offset:13056\n\t" \
  "v_exp_f32 v14, v14\n\t" \
  "v_mfma_f32_16x16x32_bf16 v[100:103], a[220:223], v[124:127], v[188:191]\n\t" \
  "ds_read_b64_tr_b16 a[184:185], v229 offset:768\n\t" \
  "v_cvt_pk_bf16_f32 v3, v6, v7\n\t" \
  "v_mfma_f32_16x16x32_bf16 v[100:103], a[216:219], v[120:123], v[100:103]\n\t" \
  "ds_read_b64_tr_b16 a[186:187], v229 offset:4864\n\t" \
  "v_exp_f32 v15, v15\n\t" \
  "v_mfma_f32_16x16x32_bf16 v[100:103], a[212:215], v[116:119], v[100:103]\n\t" \
  "buffer_load_dwordx4 v248, s[24:27], s96 offen offset:2048 lds\n\t" \
  "v_mfma_f32_16x16x32_bf16 v[100:103], a[208:211], v[112:115], v[100:103]\n\t" \
  "ds_read_b64_tr_b16 a[188:189], v229 offset:8960\n\t" \
  "v_exp_f32 v32, v32\n\t" \
  "v_mfma_f32_16x16x32_bf16 v[104:107], a[224:227], v[112:115], v[188:191]\n\t" \
  "ds_read_b64_tr_b16 a[190:191], v229 offset:13056\n\t" \
  "v_exp_f32 v33, v33\n\t" \
  "v_mfma_f32_16x16x32_bf16 v[104:107], a[228:231], v[116:119], v[104:107]\n\t" \
  "buffer_load_dwordx4 v249, s[24:27], s96 offen offset:3072 lds\n\t" \
  "v_mfma_f32_16x16x32_bf16 v[104:107], a[232:235], v[120:123], v[104:107]\n\t" \
  "s_add_u32 s96, s40, 524288\n\t" \
  "v_exp_f32 v34, v34\n\t" \
  "v_mfma_f32_16x16x32_bf16 v[104:107], a[236:239], v[124:127], v[104:107]\n\t" \
  "s_and_b32 s96, s96, 0x1c0000\n\t" \
  "v_exp_f32 v35, v35\n\t" \
  "v_mfma_f32_16x16x32_bf16 v[108:111], a[252:255], v[124:127], v[188:191]\n\t" \
  "s_add_u32 s96, s96, s77\n\t" \
  "v_exp_f32 v36, v36\n\t" \
  "v_mfma_f32_16x16x32_bf16 v[108:111], a[248:251], v[120:123], v[108:111]\n\t" \
  "s_add_u32 m0, s38, 81920\n\t" \
  "v_exp_f32 v37, v37\n\t" \
  "v_mfma_f32_16x16x32_bf16 v[108:111], a[244:247], v[116:119], v[108:111]\n\t" \
  "s_waitcnt vmcnt(8) lgkmcnt(0)\n\t" \
  "v_exp_f32 v38, v38\n\t" \
  "v_mfma_f32_16x16x32_bf16 v[108:111], a[240:243], v[112:115], v[108:111]\n\t" \
  "s_barrier\n\t" \
  STS("s_memrealtime s[94:95]\n\t") \
  STS("s_memtime s[92:93]\n\t") \
  "v_mfma_f32_16x16x32_bf16 a[0:3], a[128:131], v[128:131], a[0:3]\n\t" \
  "buffer_load_dwordx4 v240, s[24:27], s96 offen offset:0 lds\n\t" \
  "ds_read_b64_tr_b16 v[16:17], v210 offset:0\n\t" \
  "ds_read_b128 v[176:179], v215 offset:16384\n\t" \
  "ds_read_b128 v[180:183], v215 offset:17408\n\t" \
  "v_mfma_f32_16x16x32_bf16 a[0:3], a[132:135], v[132:135], a[0:3]\n\t" \
  "ds_read_b64_tr_b16 v[18:19], v210 offset:4096\n\t" \
  "ds_read_b64_tr_b16 v[20:21], v210 offset:8192\n\t" \
  "ds_read_b64_tr_b16 v[22:23], v210 offset:12288\n\t" \
  "ds_read_b64_tr_b16 v[24:25], v211 offset:0\n\t" \
  "v_mfma_f32_16x16x32_bf16 a[4:7], a[136:139], v[128:131], a[4:7]\n\t" \
  "ds_read_b64_tr_b16 v[26:27], v211 offset:4096\n\t" \
  "ds_read_b64_tr_b16 v[28:29], v211 offset:8192\n\t" \
  "ds_read_b64_tr_b16 v[30:31], v211 offset:12288\n\t" \
  "ds_read_b128 v[184:187], v215 offset:18432\n\t" \
  "v_mfma_f32_16x16x32_bf16 a[4:7], a[140:143], v[132:135], a[4:7]\n\t" \
  "buffer_load_dwordx4 v241, s[24:27], s96 offen offset:1024 lds\n\t" \
  "ds_read_b64_tr_b16 v[48:49], v210 offset:256\n\t" \
  "ds_read_b64_tr_b16 v[50:51], v210 offset:4352\n\t" \
  "ds_read_b64_tr_b16 v[52:53], v210 offset:8448\n\t" \
  "v_mfma_f32_16x16x32_bf16 a[8:11], a[144:147], v[128:131], a[8:11]\n\t" \
  "ds_read_b128 v[188:191], v215 offset:19456\n\t" \
  "ds_read_b64_tr_b16 v[54:55], v210 offset:12544\n\t" \
  "v_cvt_pk_bf16_f32 v4, v8, v9\n\t" \
  "v_mfma_f32_16x16x32_bf16 a[8:11], a[148:151], v[132:135], a[8:11]\n\t" \
  "v_exp_f32 v39, v39\n\t" \
  "v_mfma_f32_16x16x32_bf16 a[12:15], a[152:155], v[128:131], a[12:15]\n\t" \
  "buffer_load_dwordx4 v248, s[24:27], s96 offen offset:2048 lds\n\t" \
  "v_mfma_f32_16x16x32_bf16 a[12:15], a[156:159], v[132:135], a[12:15]\n\t" \
  "ds_read_b64_tr_b16 v[56:57], v211 offset:256\n\t" \
  "v_cvt_pk_bf16_f32 v5, v10, v11\n\t" \
  "v_mfma_f32_16x16x32_bf16 a[16:19], a[160:163], v[128:131], a[16:19]\n\t" \
  "v_exp_f32 v40, v40\n\t" \
  "v_mfma_f32_16x16x32_bf16 a[16:19], a[164:167], v[132:135], a[16:19]\n\t" \
  "buffer_load_dwordx4 v249, s[24:27], s96 offen offset:3072 lds\n\t" \
  "v_mfma_f32_16x16x32_bf16 a[20:23], a[168:171], v[128:131], a[20:23]\n\t" \
  "s_add_u32 s96, s40, 786432\n\t" \
  "ds_read_b64_tr_b16 v[58:59], v211 offset:4352\n\t" \
  "v_cvt_pk_bf16_f32 v6, v12, v13\n\t" \
  "v_mfma_f32_16x16x32_bf16 a[20:23], a[172:175], v[132:135], a[20:23]\n\t" \
  "s_and_b32 s96, s96, 0x1c0000\n\t" \
  "ds_read_b64_tr_b16 v[60:61], v211 offset:8448\n\t" \
  "v_cvt_pk_bf16_f32 v7, v14, v15\n\t" \
  "v_mfma_f32_16x16x32_bf16 a[24:27], a[176:179], v[128:131], a[24:27]\n\t" \
  STS("v_writelane_b32 v255, s94, 20\n\t") \
  STS("v_writelane_b32 v255, s92, 21\n\t") \
  "s_add_u32 s96, s96, s51\n\t" \
  "s_add_u32 m0, s38, 114688\n\t" \
  "v_cvt_pk_bf16_f32 v32, v32, v33\n\t" \
  "v_mfma_f32_16x16x32_bf16 a[24:27], a[180:183], v[132:135], a[24:27]\n\t" \
  "v_exp_f32 v41, v41\n\t" \
  "v_mfma_f32_16x16x32_bf16 a[28:31], a[184:187], v[128:131], a[28:31]\n\t" \
  "s_waitcnt lgkmcnt(4)\n\t" \
  "buffer_load_dwordx4 v240, s[24:27], s96 offen offset:0 lds\n\t" \
  "v_mfma_f32_16x16x32_bf16 a[28:31], a[188:191], v[132:135], a[28:31]\n\t" \
  "ds_read_b64_tr_b16 v[62:63], v211 offset:12544\n\t" \
  "v_cvt_pk_bf16_f32 v33, v34, v35\n\t" \
  "v_smfmac_f32_16x16x64_bf16 v[160:163], v[216:219], v[128:135], v208\n\t" \
  "ds_read_b64_tr_b16 v[80:81], v210 offset:512\n\t" \
  "v_cvt_pk_bf16_f32 v34, v36, v37\n\t" \
  "v_mfma_f32_16x16x32_bf16 a[32:35], a[128:131], v[136:139], a[32:35]\n\t" \
  "buffer_load_dwordx4 v241, s[24:27], s96 offen offset:1024 lds\n\t" \
  "v_mfma_f32_16x16x32_bf16 a[32:35], a[132:135], v[140:143], a[32:35]\n\t" \
  "v_exp_f32 v42, v42\n\t" \
  "v_mfma_f32_16x16x32_bf16 a[36:39], a[136:139], v[136:139], a[36:39]\n\t" \
  "ds_read_b64_tr_b16 v[82:83], v210 offset:4608\n\t" \
  "v_cvt_pk_bf16_f32 v35, v38, v39\n\t" \
  "v_mfma_f32_16x16x32_bf16 a[36:39], a[140:143], v[140:143], a[36:39]\n\t" \
  "buffer_load_dwordx4 v248, s[24:27], s96 offen offset:2048 lds\n\t" \
  "v_mfma_f32_16x16x32_bf16 a[40:43], a[144:147], v[136:139], a[40:43]\n\t" \
  "ds_read_b64_tr_b16 v[84:85], v210 offset:8704\n\t" \
  "v_cvt_pk_bf16_f32 v36, v40, v41\n\t" \
  "v_mfma_f32_16x16x32_bf16 a[40:43], a[148:151], v[140:143], a[40:43]\n\t" \
  "ds_read_b64_tr_b16 v[86:87], v210 offset:12800\n\t" \
  "v_exp_f32 v43, v43\n\t" \
  "v_mfma_f32_16x16x32_bf16 a[44:47], a[152:155], v[136:139], a[44:47]\n\t" \
  "buffer_load_dwordx4 v249, s[24:27], s96 offen offset:3072 lds\n\t" \
  "v_mfma_f32_16x16x32_bf16 a[44:47], a[156:159], v[140:143], a[44:47]\n\t" \
  "ds_read_b64_tr_b16 v[88:89], v211 offset:512\n\t" \
  "v_exp_f32 v44, v44\n\t" \
  "v_mfma_f32_16x16x32_bf16 a[48:51], a[160:163], v[136:139], a[48:51]\n\t" \
  "ds_read_b64_tr_b16 v[90:91], v211 offset:4608\n\t" \
  "v_exp_f32 v45, v45\n\t" \
  "v_mfma_f32_16x16x32_bf16 a[48:51], a[164:167], v[140:143], a[48:51]\n\t" \
  "ds_read_b64_tr_b16 v[92:93], v211 offset:8704\n\t" \
  "v_add_u32 v230, 0x10000, v242\n\t" \
  "v_mfma_f32_16x16x32_bf16 a[52:55], a[168:171], v[136:139], a[52:55]\n\t" \
  "ds_read_b64_tr_b16 v[94:95], v211 offset:12800\n\t" \
  "v_exp_f32 v46, v46\n\t" \
  "v_mfma_f32_16x16x32_bf16 a[52:55], a[172:175], v[140:143], a[52:55]\n\t" \
  "v_exp_f32 v47, v47\n\t" \
  "v_mfma_f32_16x16x32_bf16 a[56:59], a[176:179], v[136:139], a[56:59]\n\t" \
  "ds_read_b64_tr_b16 v[112:113], v210 offset:768\n\t" \
  "v_cvt_pk_bf16_f32 v37, v42, v43\n\t" \
  "v_mfma_f32_16x16x32_bf16 a[56:59], a[180:183], v[140:143], a[56:59]\n\t" \
  "ds_read_b64_tr_b16 v[114:115], v210 offset:4864\n\t" \
  "v_add_u32 v232, 0x20000, v242\n\t" \
  "v_mfma_f32_16x16x32_bf16 a[60:63], a[184:187], v[136:139], a[60:63]\n\t" \
  "ds_read_b64_tr_b16 v[116:117], v210 offset:8960\n\t" \
  "v_exp_f32 v64, v64\n\t" \
  "v_mfma_f32_16x16x32_bf16 a[60:63], a[188:191], v[140:143], a[60:63]\n\t" \
  "ds_read_b64_tr_b16 v[118:119], v210 offset:13056\n\t" \
  "v_cvt_pk_bf16_f32 v38, v44, v45\n\t" \
  "v_smfmac_f32_16x16x64_bf16 v[164:167], v[216:219], v[136:143], v208\n\t" \
  "ds_read_b64_tr_b16 a[192:193], v234 offset:0\n\t" \
  "v_cvt_pk_bf16_f32 v39, v46, v47\n\t" \
  "v_mfma_f32_16x16x32_bf16 a[0:3], v[16:19], v[0:3], a[0:3]\n\t" \
  "ds_read_b64_tr_b16 v[120:121], v211 offset:768\n\t" \
  "v_exp_f32 v65, v65\n\t" \
  "v_mfma_f32_16x16x32_bf16 a[0:3], v[20:23], v[4:7], a[0:3]\n\t" \
  "ds_read_b64_tr_b16 v[122:123], v211 offset:4864\n\t" \
  "v_exp_f32 v66, v66\n\t" \
  "v_smfmac_f32_16x16x64_bf16 v[160:163], v[216:219], v[0:7], v208\n\t" \
  "ds_read_b64_tr_b16 v[124:125], v211 offset:8960\n\t" \
  "v_cvt_pk_bf16_f32 v64, v64, v65\n\t" \
  "v_mfma_f32_16x16x32_bf16 a[4:7], v[24:27], v[0:3], a[4:7]\n\t" \
  "v_exp_f32 v67, v67\n\t" \
  "v_mfma_f32_16x16x32_bf16 a[4:7], v[28:31], v[4:7], a[4:7]\n\t" \
  "s_waitcnt lgkmcnt(15)\n\t" \
  "v_exp_f32 v68, v68\n\t" \
  "v_mfma_f32_16x16x32_bf16 a[8:11], v[48:51], v[0:3], a[8:11]\n\t" \
  "ds_read_b64_tr_b16 v[126:127], v211 offset:13056\n\t" \
  "v_add_f32 v225, v160, v161\n\t" \
  "v_mfma_f32_16x16x32_bf16 a[8:11], v[52:55], v[4:7], a[8:11]\n\t" \
  "v_rcp_f32 v224, v225\n\t" \
  "v_mfma_f32_16x16x32_bf16 a[12:15], v[56:59], v[0:3], a[12:15]\n\t" \
  "v_accvgpr_read_b32 v128, a0\n\t" \
  "v_accvgpr_read_b32 v129, a1\n\t" \
  "v_accvgpr_read_b32 v130, a2\n\t" \
  "v_accvgpr_read_b32 v131, a3\n\t" \
  "v_accvgpr_read_b32 v132, a4\n\t" \
  "v_accvgpr_read_b32 v133, a5\n\t" \
  "v_accvgpr_read_b32 v134, a6\n\t" \
  "v_accvgpr_read_b32 v135, a7\n\t" \
  "v_pk_mul_f32 v[128:129], v[128:129], v[224:225] op_sel_hi:[1,0]\n\t" \
  "v_pk_mul_f32 v[130:131], v[130:131], v[224:225] op_sel_hi:[1,0]\n\t" \
  "v_pk_mul_f32 v[132:133], v[132:133], v[224:225] op_sel_hi:[1,0]\n\t" \
  "v_pk_mul_f32 v[134:135], v[134:135], v[224:225] op_sel_hi:[1,0]\n\t" \
  "v_cvt_pk_bf16_f32 v128, v128, v129\n\t" \
  "v_cvt_pk_bf16_f32 v129, v130, v131\n\t" \
  "v_cvt_pk_bf16_f32 v130, v132, v133\n\t" \
  "v_cvt_pk_bf16_f32 v131, v134, v135\n\t" \
  "buffer_store_dwordx4 v[128:131], v242, s[16:19], s98 offen offset:0 sc1\n\t" \
  "v_mfma_f32_16x16x32_bf16 a[12:15], v[60:63], v[4:7], a[12:15]\n\t" \
  "s_waitcnt lgkmcnt(9)\n\t" \
  "v_cmp_nge_f32 vcc, 0x5f800000, v225\n\t" \
  "v_exp_f32 v69, v69\n\t" \
  "v_mfma_f32_16x16x32_bf16 a[16:19], v[80:83], v[0:3], a[16:19]\n\t" \
  "s_cmp_lg_u64 vcc, 0\n\t" \
  "s_cselect_b32 s89, s78, s89\n\t" \
  "v_or_b32 v254, s89, v254\n\t" \
  "v_mfma_f32_16x16x32_bf16 a[16:19], v[84:87], v[4:7], a[16:19]\n\t" \
  "ds_read_b64_tr_b16 a[194:195], v234 offset:4096\n\t" \
  "v_exp_f32 v70, v70\n\t" \
  "v_mfma_f32_16x16x32_bf16 a[20:23], v[88:91], v[0:3], a[20:23]\n\t" \
  "v_accvgpr_read_b32 v136, a8\n\t" \
  "v_accvgpr_read_b32 v137, a9\n\t" \
  "v_accvgpr_read_b32 v138, a10\n\t" \
  "v_accvgpr_read_b32 v139, a11\n\t" \
  "v_accvgpr_read_b32 v140, a12\n\t" \
  "v_accvgpr_read_b32 v141, a13\n\t" \
  "v_accvgpr_read_b32 v142, a14\n\t" \
  "v_accvgpr_read_b32 v143, a15\n\t" \
  "v_pk_mul_f32 v[136:137], v[136:137], v[224:225] op_sel_hi:[1,0]\n\t" \
  "v_pk_mul_f32 v[138:139], v[138:139], v[224:225] op_sel_hi:[1,0]\n\t" \
  "v_pk_mul_f32 v[140:141], v[140:141], v[224:225] op_sel_hi:[1,0]\n\t" \
  "v_pk_mul_f32 v[142:143], v[142:143], v[224:225] op_sel_hi:[1,0]\n\t" \
  "v_cvt_pk_bf16_f32 v136, v136, v137\n\t" \
  "v_cvt_pk_bf16_f32 v137, v138, v139\n\t" \
  "v_cvt_pk_bf16_f32 v138, v140, v141\n\t" \
  "v_cvt_pk_bf16_f32 v139, v142, v143\n\t" \
  "buffer_store_dwordx4 v[136:139], v242, s[16:19], s98 offen offset:1024 sc1\n\t" \
  "v_mfma_f32_16x16x32_bf16 a[20:23], v[92:95], v[4:7], a[20:23]\n\t" \
  "s_waitcnt lgkmcnt(1)\n\t" \
  "ds_read_b64_tr_b16 a[196:197], v235 offset:0\n\t" \
  "v_exp_f32 v71, v71\n\t" \
  "v_mfma_f32_16x16x32_bf16 a[24:27], v[112:115], v[0:3], a[24:27]\n\t" \
  "s_add_u32 s96, s40, 786432\n\t" \
  "ds_read_b64_tr_b16 a[198:199], v235 offset:4096\n\t" \
  "v_exp_f32 v72, v72\n\t" \
  "v_mfma_f32_16x16x32_bf16 a[24:27], v[116:119], v[4:7], a[24:27]\n\t" \
  "s_and_b32 s96, s96, 0x1c0000\n\t" \
  "ds_read_b64_tr_b16 a[200:201], v234 offset:8192\n\t" \
  "v_cvt_pk_bf16_f32 v65, v66, v67\n\t" \
  "v_mfma_f32_16x16x32_bf16 a[28:31], v[120:123], v[0:3], a[28:31]\n\t" \
  "s_add_u32 s96, s96, s77\n\t" \
  "v_exp_f32 v73, v73\n\t" \
  "v_mfma_f32_16x16x32_bf16 a[28:31], v[124:127], v[4:7], a[28:31]\n\t" \
  "s_add_u32 m0, s38, 147456\n\t" \
  "v_exp_f32 v74, v74\n\t" \
  "v_mfma_f32_16x16x32_bf16 a[64:67], a[128:131], v[144:147], a[64:67]\n\t" \
  "s_barrier\n\t" \
  "v_mfma_f32_16x16x32_bf16 a[64:67], a[132:135], v[148:151], a[64:67]\n\t" \
  "v_exp_f32 v75, v75\n\t" \
  "v_mfma_f32_16x16x32_bf16 a[68:71], a[136:139], v[144:147], a[68:71]\n\t" \
  "v_accvgpr_read_b32 v0, a16\n\t" \
  "v_accvgpr_read_b32 v1, a17\n\t" \
  "v_accvgpr_read_b32 v2, a18\n\t" \
  "v_accvgpr_read_b32 v3, a19\n\t" \
  "v_accvgpr_read_b32 v4, a20\n\t" \
  "v_accvgpr_read_b32 v5, a21\n\t" \
  "v_accvgpr_read_b32 v6, a22\n\t" \
  "v_accvgpr_read_b32 v7, a23\n\t" \
  "v_pk_mul_f32 v[0:1], v[0:1], v[224:225] op_sel_hi:[1,0]\n\t" \
  "v_pk_mul_f32 v[2:3], v[2:3], v[224:225] op_sel_hi:[1,0]\n\t" \
  "v_pk_mul_f32 v[4:5], v[4:5], v[224:225] op_sel_hi:[1,0]\n\t" \
  "v_pk_mul_f32 v[6:7], v[6:7], v[224:225] op_sel_hi:[1,0]\n\t" \
  "v_cvt_pk_bf16_f32 v0, v0, v1\n\t" \
  "v_cvt_pk_bf16_f32 v1, v2, v3\n\t" \
  "v_cvt_pk_bf16_f32 v2, v4, v5\n\t" \
  "v_cvt_pk_bf16_f32 v3, v6, v7\n\t" \
  "buffer_store_dwordx4 v[0:3], v242, s[16:19], s98 offen offset:2048 sc1\n\t" \
  "v_mfma_f32_16x16x32_bf16 a[68:71], a[140:143], v[148:151], a[68:71]\n\t" \
  "v_accvgpr_read_b32 v8, a24\n\t" \
  "v_accvgpr_read_b32 v9, a25\n\t" \
  "v_accvgpr_read_b32 v10, a26\n\t" \
  "v_accvgpr_read_b32 v11, a27\n\t" \
  "v_accvgpr_read_b32 v12, a28\n\t" \
  "v_accvgpr_read_b32 v13, a29\n\t" \
  "v_accvgpr_read_b32 v14, a30\n\t" \
  "v_accvgpr_read_b32 v15, a31\n\t" \
  "v_pk_mul_f32 v[8:9], v[8:9], v[224:225] op_sel_hi:[1,0]\n\t" \
  "v_pk_mul_f32 v[10:11], v[10:11], v[224:225] op_sel_hi:[1,0]\n\t" \
  "v_pk_mul_f32 v[12:13], v[12:13], v[224:225] op_sel_hi:[1,0]\n\t" \
  "v_pk_mul_f32 v[14:15], v[14:15], v[224:225] op_sel_hi:[1,0]\n\t" \
  "v_cvt_pk_bf16_f32 v8, v8, v9\n\t" \
  "v_cvt_pk_bf16_f32 v9, v10, v11\n\t" \
  "v_cvt_pk_bf16_f32 v10, v12, v13\n\t" \
  "v_cvt_pk_bf16_f32 v11, v14, v15\n\t" \
  "buffer_store_dwordx4 v[8:11], v242, s[16:19], s98 offen offset:3072 sc1\n\t" \
  "v_mfma_f32_16x16x32_bf16 a[72:75], a[144:147], v[144:147], a[72:75]\n\t" \
  "buffer_load_dwordx4 v240, s[24:27], s96 offen offset:0 lds\n\t" \
  "v_mfma_f32_16x16x32_bf16 a[72:75], a[148:151], v[148:151], a[72:75]\n\t" \
  "s_xor_b32 s98, s45, 32768\n\t" \
  "ds_read_b64_tr_b16 a[202:203], v234 offset:12288\n\t" \
  "v_exp_f32 v76, v76\n\t" \
  "v_mfma_f32_16x16x32_bf16 a[76:79], a[152:155], v[144:147], a[76:79]\n\t" \
  "ds_read_b64_tr_b16 a[204:205], v235 offset:8192\n\t" \
  "ds_read_b128 a[224:227], v213 offset:16384\n\t" \
  "v_exp_f32 v77, v77\n\t" \
  "v_mfma_f32_16x16x32_bf16 a[76:79], a[156:159], v[148:151], a[76:79]\n\t" \
  "ds_read_b64_tr_b16 a[206:207], v235 offset:12288\n\t" \
  "ds_read_b128 a[228:231], v213 offset:17408\n\t" \
  "v_cvt_pk_bf16_f32 v66, v68, v69\n\t" \
  "v_mfma_f32_16x16x32_bf16 a[80:83], a[160:163], v[144:147], a[80:83]\n\t" \
  "ds_read_b64_tr_b16 a[208:209], v236 offset:32768\n\t" \
  "ds_read_b128 a[232:235], v213 offset:18432\n\t" \
  "v_cvt_pk_bf16_f32 v67, v70, v71\n\t" \
  "v_mfma_f32_16x16x32_bf16 a[80:83], a[164:167], v[148:151], a[80:83]\n\t" \
  "v_exp_f32 v78, v78\n\t" \
  "v_mfma_f32_16x16x32_bf16 a[84:87], a[168:171], v[144:147], a[84:87]\n\t" \
  "buffer_load_dwordx4 v241, s[24:27], s96 offen offset:1024 lds\n\t" \
  "v_mfma_f32_16x16x32_bf16 a[84:87], a[172:175], v[148:151], a[84:87]\n\t" \
  "ds_read_b128 a[236:239], v213 offset:19456\n\t" \
  "v_cvt_pk_bf16_f32 v68, v72, v73\n\t" \
  "v_mfma_f32_16x16x32_bf16 a[88:91], a[176:179], v[144:147], a[88:91]\n\t" \
  ".Lhs10:\n\ts_cbranch_execz .Lhs10n\n\t" \
  "ds_write_b128 v213, v[128:131] offset:16384\n\t" \
  "v_mfma_f32_16x16x32_bf16 a[88:91], a[180:183], v[148:151], a[88:91]\n\t" \
  "buffer_load_dwordx4 v248, s[24:27], s96 offen offset:2048 lds\n\t" \
  "ds_write_b128 v213, v[136:139] offset:17408\n\t" \
  "v_mfma_f32_16x16x32_bf16 a[92:95], a[184:187], v[144:147], a[92:95]\n\t" \
  "ds_write_b128 v213, v[0:3] offset:18432\n\t" \
  "v_mfma_f32_16x16x32_bf16 a[92:95], a[188:191], v[148:151], a[92:95]\n\t" \
  "ds_write_b128 v213, v[8:11] offset:19456\n\t" \
  "v_smfmac_f32_16x16x64_bf16 v[168:171], v[216:219], v[144:151], v208\n\t" \
  "buffer_load_dwordx4 v249, s[24:27], s96 offen offset:3072 lds\n\t" \
  "v_mfma_f32_16x16x32_bf16 a[32:35], v[16:19], v[32:35], a[32:35]\n\t" \
  "ds_read_b64_tr_b16 a[210:211], v236 offset:36864\n\t" \
  "v_cvt_pk_bf16_f32 v69, v74, v75\n\t" \
  "v_mfma_f32_16x16x32_bf16 a[32:35], v[20:23], v[36:39], a[32:35]\n\t" \
  "ds_read_b64_tr_b16 a[212:213], v237 offset:32768\n\t" \
  "v_exp_f32 v79, v79\n\t" \
  "v_smfmac_f32_16x16x64_bf16 v[164:167], v[216:219], v[32:39], v208\n\t" \
  "ds_read_b64_tr_b16 a[214:215], v237 offset:36864\n\t" \
  "v_cvt_pk_bf16_f32 v70, v76, v77\n\t" \
  "v_mfma_f32_16x16x32_bf16 a[36:39], v[24:27], v[32:35], a[36:39]\n\t" \
  "v_exp_f32 v96, v96\n\t" \
  "v_mfma_f32_16x16x32_bf16 a[36:39], v[28:31], v[36:39], a[36:39]\n\t" \
  "s_nop 2\n\t" \
  "v_add_f32 v227, v164, v165\n\t" \
  "v_rcp_f32 v226, v227\n\t" \
  "v_mfma_f32_16x16x32_bf16 a[40:43], v[48:51], v[32:35], a[40:43]\n\t" \
  "ds_read_b64_tr_b16 a[216:217], v236 offset:40960\n\t" \
  "v_cmp_nge_f32 vcc, 0x5f800000, v227\n\t" \
  "v_accvgpr_read_b32 v0, a32\n\t" \
  "v_accvgpr_read_b32 v1, a33\n\t" \
  "v_mfma_f32_16x16x32_bf16 a[40:43], v[52:55], v[36:39], a[40:43]\n\t" \
  "s_cmp_lg_u64 vcc, 0\n\t" \
  "s_cselect_b32 s89, s78, s89\n\t" \
  "v_or_b32 v254, s89, v254\n\t" \
  "v_accvgpr_read_b32 v2, a34\n\t" \
  "v_accvgpr_read_b32 v3, a35\n\t" \
  "v_mfma_f32_16x16x32_bf16 a[44:47], v[56:59], v[32:35], a[44:47]\n\t" \
  "v_exp_f32 v97, v97\n\t" \
  "v_accvgpr_read_b32 v4, a36\n\t" \
  "v_accvgpr_read_b32 v5, a37\n\t" \
  "v_mfma_f32_16x16x32_bf16 a[44:47], v[60:63], v[36:39], a[44:47]\n\t" \
  "v_accvgpr_read_b32 v6, a38\n\t" \
  "v_accvgpr_read_b32 v7, a39\n\t" \
  "v_mfma_f32_16x16x32_bf16 a[48:51], v[80:83], v[32:35], a[48:51]\n\t" \
  "v_mul_f32 v0, v0, v226\n\t" \
  "v_mul_f32 v1, v1, v226\n\t" \
  "v_mfma_f32_16x16x32_bf16 a[48:51], v[84:87], v[36:39], a[48:51]\n\t" \
  "v_mul_f32 v2, v2, v226\n\t" \
  "v_exp_f32 v98, v98\n\t" \
  "v_mfma_f32_16x16x32_bf16 a[52:55], v[88:91], v[32:35], a[52:55]\n\t" \
  "v_mul_f32 v3, v3, v226\n\t" \
  "v_exp_f32 v99, v99\n\t" \
  "v_mfma_f32_16x16x32_bf16 a[52:55], v[92:95], v[36:39], a[52:55]\n\t" \
  "v_mul_f32 v4, v4, v226\n\t" \
  "v_mul_f32 v5, v5, v226\n\t" \
  "v_mfma_f32_16x16x32_bf16 a[56:59], v[112:115], v[32:35], a[56:59]\n\t" \
  "v_mul_f32 v6, v6, v226\n\t" \
  "v_exp_f32 v100, v100\n\t" \
  "v_mfma_f32_16x16x32_bf16 a[56:59], v[116:119], v[36:39], a[56:59]\n\t" \
  "ds_read_b64_tr_b16 a[218:219], v236 offset:45056\n\t" \
  "v_mul_f32 v7, v7, v226\n\t" \
  "v_mfma_f32_16x16x32_bf16 a[60:63], v[120:123], v[32:35], a[60:63]\n\t" \
  "v_exp_f32 v101, v101\n\t" \
  "v_mfma_f32_16x16x32_bf16 a[60:63], v[124:127], v[36:39], a[60:63]\n\t" \
  "v_cvt_pk_bf16_f32 v32, v0, v1\n\t" \
  "v_cvt_pk_bf16_f32 v33, v2, v3\n\t" \
  "v_cvt_pk_bf16_f32 v34, v4, v5\n\t" \
  "v_cvt_pk_bf16_f32 v35, v6, v7\n\t" \
  "v_mfma_f32_16x16x32_bf16 a[96:99], a[128:131], v[152:155], a[96:99]\n\t" \
  "v_accvgpr_read_b32 v8, a40\n\t" \
  "v_accvgpr_read_b32 v9, a41\n\t" \
  "v_accvgpr_read_b32 v10, a42\n\t" \
  "v_accvgpr_read_b32 v11, a43\n\t" \
  "v_mfma_f32_16x16x32_bf16 a[96:99], a[132:135], v[156:159], a[96:99]\n\t" \
  "s_waitcnt vmcnt(8) lgkmcnt(13)\n\t" \
  "v_accvgpr_read_b32 v12, a44\n\t" \
  "v_accvgpr_read_b32 v13, a45\n\t" \
  "v_accvgpr_read_b32 v14, a46\n\t" \
  "v_accvgpr_read_b32 v15, a47\n\t" \
  "v_mfma_f32_16x16x32_bf16 a[100:103], a[136:139], v[152:155], a[100:103]\n\t" \
  "v_mul_f32 v8, v8, v226\n\t" \
  "v_mul_f32 v9, v9, v226\n\t" \
  "s_barrier\n\t" \
  "v_mfma_f32_16x16x32_bf16 a[100:103], a[140:143], v[156:159], a[100:103]\n\t" \
  "buffer_store_dwordx4 v[32:35], v242, s[16:19], s98 offen offset:0 sc1\n\t" \
  "v_mul_f32 v10, v10, v226\n\t" \
  "v_mul_f32 v11, v11, v226\n\t" \
  "v_mul_f32 v12, v12, v226\n\t" \
  "v_mfma_f32_16x16x32_bf16 a[104:107], a[144:147], v[152:155], a[104:107]\n\t" \
  "v_mul_f32 v13, v13, v226\n\t" \
  "v_mul_f32 v14, v14, v226\n\t" \
  "v_mul_f32 v15, v15, v226\n\t" \
  "v_cvt_pk_bf16_f32 v36, v8, v9\n\t" \
  "v_mfma_f32_16x16x32_bf16 a[104:107], a[148:151], v[156:159], a[104:107]\n\t" \
  "v_cvt_pk_bf16_f32 v37, v10, v11\n\t" \
  "v_cvt_pk_bf16_f32 v38, v12, v13\n\t" \
  "v_cvt_pk_bf16_f32 v39, v14, v15\n\t" \
  "v_accvgpr_read_b32 v0, a48\n\t" \
  "v_mfma_f32_16x16x32_bf16 a[108:111], a[152:155], v[152:155], a[108:111]\n\t" \
  "v_accvgpr_read_b32 v1, a49\n\t" \
  "v_accvgpr_read_b32 v2, a50\n\t" \
  "v_accvgpr_read_b32 v3, a51\n\t" \
  "v_exp_f32 v102, v102\n\t" \
  "v_mfma_f32_16x16x32_bf16 a[108:111], a[156:159], v[156:159], a[108:111]\n\t" \
  "v_accvgpr_read_b32 v4, a52\n\t" \
  "v_accvgpr_read_b32 v5, a53\n\t" \
  "v_accvgpr_read_b32 v6, a54\n\t" \
  "v_accvgpr_read_b32 v7, a55\n\t" \
  "v_mfma_f32_16x16x32_bf16 a[112:115], a[160:163], v[152:155], a[112:115]\n\t" \
  "v_mul_f32 v0, v0, v226\n\t" \
  "v_mul_f32 v1, v1, v226\n\t" \
  "v_mul_f32 v2, v2, v226\n\t" \
  "v_exp_f32 v103, v103\n\t" \
  "v_mfma_f32_16x16x32_bf16 a[112:115], a[164:167], v[156:159], a[112:115]\n\t" \
  "v_cvt_pk_bf16_f32 v71, v78, v79\n\t" \
  "v_mul_f32 v3, v3, v226\n\t" \
  "v_mul_f32 v4, v4, v226\n\t" \
  "v_mul_f32 v5, v5, v226\n\t" \
  "v_mfma_f32_16x16x32_bf16 a[116:119], a[168:171], v[152:155], a[116:119]\n\t" \
  "v_mul_f32 v6, v6, v226\n\t" \
  "v_mul_f32 v7, v7, v226\n\t" \
  "v_cvt_pk_bf16_f32 v40, v0, v1\n\t" \
  "v_exp_f32 v104, v104\n\t" \
  "v_mfma_f32_16x16x32_bf16 a[116:119], a[172:175], v[156:159], a[116:119]\n\t" \
  "v_cvt_pk_bf16_f32 v96, v96, v97\n\t" \
  "v_cvt_pk_bf16_f32 v41, v2, v3\n\t" \
  "v_cvt_pk_bf16_f32 v42, v4, v5\n\t" \
  "v_cvt_pk_bf16_f32 v43, v6, v7\n\t" \
  "v_mfma_f32_16x16x32_bf16 a[120:123], a[176:179], v[152:155], a[120:123]\n\t" \
  "buffer_store_dwordx4 v[36:39], v242, s[16:19], s98 offen offset:1024 sc1\n\t" \
  "v_accvgpr_read_b32 v8, a56\n\t" \
  "v_accvgpr_read_b32 v9, a57\n\t" \
  "v_accvgpr_read_b32 v10, a58\n\t" \
  "v_mfma_f32_16x16x32_bf16 a[120:123], a[180:183], v[156:159], a[120:123]\n\t" \
  "v_accvgpr_read_b32 v11, a59\n\t" \
  "v_accvgpr_read_b32 v12, a60\n\t" \
  "v_accvgpr_read_b32 v13, a61\n\t" \
  "v_exp_f32 v105, v105\n\t" \
  "v_mfma_f32_16x16x32_bf16 a[124:127], a[184:187], v[152:155], a[124:127]\n\t" \
  "v_cvt_pk_bf16_f32 v97, v98, v99\n\t" \
  "v_accvgpr_read_b32 v14, a62\n\t" \
  "v_accvgpr_read_b32 v15, a63\n\t" \
  "v_mul_f32 v8, v8, v226\n\t" \
  "v_mfma_f32_16x16x32_bf16 a[124:127], a[188:191], v[156:159], a[124:127]\n\t" \
  "v_mul_f32 v9, v9, v226\n\t" \
  "v_mul_f32 v10, v10, v226\n\t" \
  "v_mul_f32 v11, v11, v226\n\t" \
  "v_exp_f32 v106, v106\n\t" \
  "v_smfmac_f32_16x16x64_bf16 v[172:175], v[216:219], v[152:159], v208\n\t" \
  "v_cvt_pk_bf16_f32 v98, v100, v101\n\t" \
  "v_mul_f32 v12, v12, v226\n\t" \
  "v_mul_f32 v13, v13, v226\n\t" \
  "v_mul_f32 v14, v14, v226\n\t" \
  "v_mfma_f32_16x16x32_bf16 a[64:67], v[16:19], v[64:67], a[64:67]\n\t" \
  "s_waitcnt vmcnt(6)\n\t" \
  "buffer_store_dword v243, v254, s[16:19], s49 offen offset:0 sc1\n\t" \
  "ds_read_b64_tr_b16 a[220:221], v237 offset:40960\n\t" \
  "v_mfma_f32_16x16x32_bf16 a[64:67], v[20:23], v[68:71], a[64:67]\n\t" \
  "ds_read_b64_tr_b16 a[222:223], v237 offset:45056\n\t" \
  "v_cvt_pk_bf16_f32 v99, v102, v103\n\t" \
  "v_exp_f32 v107, v107\n\t" \
  "v_smfmac_f32_16x16x64_bf16 v[168:171], v[216:219], v[64:71], v208\n\t" \
  "ds_read_b64_tr_b16 a[128:129], v234 offset:49152\n\t" \
  "v_exp_f32 v108, v108\n\t" \
  "v_mfma_f32_16x16x32_bf16 a[68:71], v[24:27], v[64:67], a[68:71]\n\t" \
  "v_cvt_pk_bf16_f32 v100, v104, v105\n\t" \
  "v_exp_f32 v109, v109\n\t" \
  "v_mul_f32 v15, v15, v226\n\t" \
  "v_mfma_f32_16x16x32_bf16 a[68:71], v[28:31], v[68:71], a[68:71]\n\t" \
  "buffer_store_dwordx4 v[40:43], v242, s[16:19], s98 offen offset:2048 sc1\n\t" \
  "v_add_f32 v221, v168, v169\n\t" \
  "v_rcp_f32 v220, v221\n\t" \
  "v_cmp_nge_f32 vcc, 0x5f800000, v221\n\t" \
  "v_mfma_f32_16x16x32_bf16 a[72:75], v[48:51], v[64:67], a[72:75]\n\t" \
  "s_cmp_lg_u64 vcc, 0\n\t" \
  "s_cselect_b32 s89, s78, s89\n\t" \
  "v_or_b32 v254, s89, v254\n\t" \
  "v_mfma_f32_16x16x32_bf16 a[72:75], v[52:55], v[68:71], a[72:75]\n\t" \
  "v_cvt_pk_bf16_f32 v44, v8, v9\n\t" \
  "v_cvt_pk_bf16_f32 v45, v10, v11\n\t" \
  "v_mfma_f32_16x16x32_bf16 a[76:79], v[56:59], v[64:67], a[76:79]\n\t" \
  "v_cvt_pk_bf16_f32 v101, v106, v107\n\t" \
  "v_exp_f32 v110, v110\n\t" \
  "v_mfma_f32_16x16x32_bf16 a[76:79], v[60:63], v[68:71], a[76:79]\n\t" \
  "ds_read_b64_tr_b16 a[130:131], v234 offset:53248\n\t" \
  "v_cvt_pk_bf16_f32 v46, v12, v13\n\t" \
  "v_mfma_f32_16x16x32_bf16 a[80:83], v[80:83], v[64:67], a[80:83]\n\t" \
  "v_cvt_pk_bf16_f32 v102, v108, v109\n\t" \
  "v_cvt_pk_bf16_f32 v47, v14, v15\n\t" \
  "v_mfma_f32_16x16x32_bf16 a[80:83], v[84:87], v[68:71], a[80:83]\n\t" \
  "ds_read_b64_tr_b16 a[132:133], v235 offset:49152\n\t" \
  "v_exp_f32 v111, v111\n\t" \
  "v_mfma_f32_16x16x32_bf16 a[84:87], v[88:91], v[64:67], a[84:87]\n\t" \
  "ds_read_b128 a[240:243], v213 offset:32768\n\t" \
  "v_cvt_pk_bf16_f32 v103, v110, v111\n\t" \
  "v_mfma_f32_16x16x32_bf16 a[84:87], v[92:95], v[68:71], a[84:87]\n\t" \
  "s_add_u32 s96, s40, 1048576\n\t" \
  "ds_read_b128 a[244:247], v213 offset:33792\n\t" \
  "s_and_b32 s96, s96, 0x1c0000\n\t" \
  "v_mfma_f32_16x16x32_bf16 a[88:91], v[112:115], v[64:67], a[88:91]\n\t" \
  "buffer_store_dwordx4 v[44:47], v242, s[16:19], s98 offen offset:3072 sc1\n\t" \
  "ds_read_b128 a[248:251], v213 offset:34816\n\t" \
  "ds_read_b128 a[252:255], v213 offset:35840\n\t" \
  "ds_read_b64_tr_b16 a[134:135], v235 offset:53248\n\t" \
  "v_mfma_f32_16x16x32_bf16 a[88:91], v[116:119], v[68:71], a[88:91]\n\t" \
  "ds_write_b128 v213, v[32:35] offset:32768\n\t" \
  "v_mfma_f32_16x16x32_bf16 a[92:95], v[120:123], v[64:67], a[92:95]\n\t" \
  "ds_write_b128 v213, v[36:39] offset:33792\n\t" \
  "v_mfma_f32_16x16x32_bf16 a[92:95], v[124:127], v[68:71], a[92:95]\n\t" \
  "s_waitcnt vmcnt(5) lgkmcnt(10)\n\t" \
  "s_add_u32 s96, s96, s51\n\t" \
  "s_add_u32 m0, s38, 98304\n\t" \
  "v_accvgpr_read_b32 v64, a64\n\t" \
  "v_accvgpr_read_b32 v65, a65\n\t" \
  "v_mfma_f32_16x16x32_bf16 a[96:99], v[16:19], v[96:99], a[96:99]\n\t" \
  "v_accvgpr_read_b32 v66, a66\n\t" \
  "v_accvgpr_read_b32 v67, a67\n\t" \
  "s_barrier\n\t" \
  "v_mfma_f32_16x16x32_bf16 a[96:99], v[20:23], v[100:103], a[96:99]\n\t" \
  "buffer_load_dwordx4 v240, s[24:27], s96 offen offset:0 lds\n\t" \
  "v_accvgpr_read_b32 v68, a68\n\t" \
  "v_accvgpr_read_b32 v69, a69\n\t" \
  "v_smfmac_f32_16x16x64_bf16 v[172:175], v[216:219], v[96:103], v208\n\t" \
  "s_xor_b32 s98, s45, 65536\n\t" \
  "ds_read_b128 v[128:131], v242 offset:16384\n\t" \
  "v_accvgpr_read_b32 v70, a70\n\t" \
  "v_accvgpr_read_b32 v71, a71\n\t" \
  "v_mfma_f32_16x16x32_bf16 a[100:103], v[24:27], v[96:99], a[100:103]\n\t" \
  "ds_read_b128 v[132:135], v242 offset:17408\n\t" \
  "v_mul_f32 v64, v64, v220\n\t" \
  "v_mfma_f32_16x16x32_bf16 a[100:103], v[28:31], v[100:103], a[100:103]\n\t" \
  "v_add_f32 v223, v172, v173\n\t" \
  "v_rcp_f32 v222, v223\n\t" \
  "v_mfma_f32_16x16x32_bf16 a[104:107], v[48:51], v[96:99], a[104:107]\n\t" \
  "buffer_load_dwordx4 v241, s[24:27], s96 offen offset:1024 lds\n\t" \
  "v_mfma_f32_16x16x32_bf16 a[104:107], v[52:55], v[100:103], a[104:107]\n\t" \
  "ds_read_b128 v[136:139], v242 offset:18432\n\t" \
  "v_cmp_nge_f32 vcc, 0x5f800000, v223\n\t" \
  "v_mfma_f32_16x16x32_bf16 a[108:111], v[56:59], v[96:99], a[108:111]\n\t" \
  "s_cmp_lg_u64 vcc, 0\n\t" \
  "s_cselect_b32 s89, s78, s89\n\t" \
  "v_or_b32 v254, s89, v254\n\t" \
  "v_mfma_f32_16x16x32_bf16 a[108:111], v[60:63], v[100:103], a[108:111]\n\t" \
  "buffer_load_dwordx4 v248, s[24:27], s96 offen offset:2048 lds\n\t" \
  "v_mul_f32 v65, v65, v220\n\t" \
  "v_mfma_f32_16x16x32_bf16 a[112:115], v[80:83], v[96:99], a[112:115]\n\t" \
  "s_cmp_lg_u32 s89, 0\n\t" \
  "s_cbranch_scc1 .Lat_x\n\t" \
  ".Lat_y:\n\t" \
  "v_mfma_f32_16x16x32_bf16 a[112:115], v[84:87], v[100:103], a[112:115]\n\t" \
  "ds_write_b128 v213, v[40:43] offset:34816\n\t" \
  "v_mfma_f32_16x16x32_bf16 a[116:119], v[88:91], v[96:99], a[116:119]\n\t" \
  "buffer_load_dwordx4 v249, s[24:27], s96 offen offset:3072 lds\n\t" \
  "ds_read_b128 v[140:143], v242 offset:19456\n\t" \
  "v_mul_f32 v66, v66, v220\n\t" \
  "v_mfma_f32_16x16x32_bf16 a[116:119], v[92:95], v[100:103], a[116:119]\n\t" \
  "ds_read_b64_tr_b16 a[136:137], v234 offset:57344\n\t" \
  "ds_read_b128 v[144:147], v242 offset:20480\n\t" \
  "v_mul_f32 v67, v67, v220\n\t" \
  "v_mfma_f32_16x16x32_bf16 a[120:123], v[112:115], v[96:99], a[120:123]\n\t" \
  "ds_read_b128 v[148:151], v242 offset:21504\n\t" \
  "v_mul_f32 v68, v68, v220\n\t" \
  "v_mfma_f32_16x16x32_bf16 a[120:123], v[116:119], v[100:103], a[120:123]\n\t" \
  "ds_write_b128 v213, v[44:47] offset:35840\n\t" \
  "v_mfma_f32_16x16x32_bf16 a[124:127], v[120:123], v[96:99], a[124:127]\n\t" \
  "ds_read_b64_tr_b16 a[138:139], v234 offset:61440\n\t" \
  "ds_read_b128 v[152:155], v242 offset:22528\n\t" \
  "v_mul_f32 v69, v69, v220\n\t" \
  "v_mul_f32 v70, v70, v220\n\t" \
  "v_mfma_f32_16x16x32_bf16 a[124:127], v[124:127], v[100:103], a[124:127]\n\t" \
  "s_waitcnt lgkmcnt(6)\n\t" \
  "ds_read_b64_tr_b16 a[140:141], v235 offset:57344\n\t" \
  "ds_read_b128 v[156:159], v242 offset:23552\n\t" \
  "v_mul_f32 v71, v71, v220\n\t" \
  ".Lat_join:\n\t" \
  STS("s_memrealtime s[94:95]\n\t") \
  STS("s_memtime s[92:93]\n\t") \
  "v_mfma_f32_16x16x32_bf16 v[0:3], a[192:195], v[128:131], 0\n\t" \
  "v_cvt_pk_bf16_f32 v112, v64, v65\n\t" \
  "v_cvt_pk_bf16_f32 v113, v66, v67\n\t" \
  "v_cvt_pk_bf16_f32 v114, v68, v69\n\t" \
  "v_cvt_pk_bf16_f32 v115, v70, v71\n\t" \
  "v_mfma_f32_16x16x32_bf16 v[0:3], a[200:203], v[132:135], v[0:3]\n\t" \
  "buffer_load_dword v246, v253, s[16:19], s65 offen sc1\n\t" \
  "v_accvgpr_read_b32 v72, a72\n\t" \
  "v_accvgpr_read_b32 v73, a73\n\t" \
  "v_accvgpr_read_b32 v74, a74\n\t" \
  "v_mfma_f32_16x16x32_bf16 v[0:3], a[208:211], v[136:139], v[0:3]\n\t" \
  "v_accvgpr_read_b32 v75, a75\n\t" \
  "v_accvgpr_read_b32 v76, a76\n\t" \
  "v_accvgpr_read_b32 v77, a77\n\t" \
  "v_accvgpr_read_b32 v78, a78\n\t" \
  "v_mfma_f32_16x16x32_bf16 v[0:3], a[216:219], v[140:143], v[0:3]\n\t" \
  "ds_read_b128 v[160:163], v242 offset:24576\n\t" \
  "v_accvgpr_read_b32 v79, a79\n\t" \
  "v_mul_f32 v72, v72, v220\n\t" \
  "v_mul_f32 v73, v73, v220\n\t" \
  "v_mfma_f32_16x16x32_bf16 v[16:19], a[220:223], v[140:143], 0\n\t" \
  "ds_read_b128 v[164:167], v242 offset:25600\n\t" \
  "v_mul_f32 v74, v74, v220\n\t" \
  "v_mul_f32 v75, v75, v220\n\t" \
  "v_mul_f32 v76, v76, v220\n\t" \
  "v_mfma_f32_16x16x32_bf16 v[16:19], a[212:215], v[136:139], v[16:19]\n\t" \
  "ds_read_b128 v[168:171], v242 offset:26624\n\t" \
  "v_mul_f32 v77, v77, v220\n\t" \
  "v_mul_f32 v78, v78, v220\n\t" \
  "v_mul_f32 v79, v79, v220\n\t" \
  "v_mfma_f32_16x16x32_bf16 v[16:19], a[204:207], v[132:135], v[16:19]\n\t" \
  "ds_read_b128 v[172:175], v242 offset:27648\n\t" \
  "v_cvt_pk_bf16_f32 v116, v72, v73\n\t" \
  "v_cvt_pk_bf16_f32 v117, v74, v75\n\t" \
  "v_cvt_pk_bf16_f32 v118, v76, v77\n\t" \
  "v_mfma_f32_16x16x32_bf16 v[16:19], a[196:199], v[128:131], v[16:19]\n\t" \
  "s_waitcnt lgkmcnt(4)\n\t" \
  "ds_read_b64_tr_b16 a[142:143], v235 offset:61440\n\t" \
  "v_cvt_pk_bf16_f32 v119, v78, v79\n\t" \
  "v_accvgpr_read_b32 v64, a80\n\t" \
  "v_accvgpr_read_b32 v65, a81\n\t" \
  "v_mfma_f32_16x16x32_bf16 v[4:7], a[192:195], v[144:147], 0\n\t" \
  "buffer_store_dwordx4 v[112:115], v242, s[16:19], s98 offen offset:0 sc1\n\t" \
  "v_accvgpr_read_b32 v66, a82\n\t" \
  "ds_read_b128 v[128:131], v242 offset:28672\n\t" \
  "v_accvgpr_read_b32 v67, a83\n\t" \
  "v_mfma_f32_16x16x32_bf16 v[4:7], a[200:203], v[148:151], v[4:7]\n\t" \
  "v_accvgpr_read_b32 v68, a84\n\t" \
  "v_accvgpr_read_b32 v69, a85\n\t" \
  "v_accvgpr_read_b32 v70, a86\n\t" \
  "v_accvgpr_read_b32 v71, a87\n\t" \
  "v_mfma_f32_16x16x32_bf16 v[4:7], a[208:211], v[152:155], v[4:7]\n\t" \
  "ds_read_b64_tr_b16 a[144:145], v236 offset:0\n\t" \
  "ds_read_b128 v[132:135], v242 offset:29696\n\t" \
  "v_mul_f32 v64, v64, v220\n\t" \
  "v_mul_f32 v65, v65, v220\n\t" \
  "v_mfma_f32_16x16x32_bf16 v[4:7], a[216:219], v[156:159], v[4:7]\n\t" \
  "v_mul_f32 v66, v66, v220\n\t" \
  "v_mul_f32 v67, v67, v220\n\t" \
  "v_mul_f32 v68, v68, v220\n\t" \
  "v_mul_f32 v69, v69, v220\n\t" \
  "v_mfma_f32_16x16x32_bf16 v[20:23], a[220:223], v[156:159], 0\n\t" \
  STS("v_writelane_b32 v255, s94, 22\n\t") \
  STS("v_writelane_b32 v255, s92, 23\n\t") \
  "ds_read_b64_tr_b16 a[146:147], v236 offset:4096\n\t" \
  "ds_read_b128 v[136:139], v242 offset:30720\n\t" \
  "v_mul_f32 v70, v70, v220\n\t" \
  "v_mul_f32 v71, v71, v220\n\t" \
  "v_mfma_f32_16x16x32_bf16 v[20:23], a[212:215], v[152:155], v[20:23]\n\t" \
  "v_cvt_pk_bf16_f32 v120, v64, v65\n\t" \
  "v_cvt_pk_bf16_f32 v121, v66, v67\n\t" \
  "v_cvt_pk_bf16_f32 v122, v68, v69\n\t" \
  "v_cvt_pk_bf16_f32 v123, v70, v71\n\t" \
  "v_mfma_f32_16x16x32_bf16 v[20:23], a[204:207], v[148:151], v[20:23]\n\t" \
  "s_waitcnt lgkmcnt(5)\n\t" \
  "ds_read_b64_tr_b16 a[148:149], v237 offset:0\n\t" \
  "ds_read_b128 v[140:143], v242 offset:31744\n\t" \
  "v_accvgpr_read_b32 v72, a88\n\t" \
  "v_accvgpr_read_b32 v73, a89\n\t" \
  "v_mfma_f32_16x16x32_bf16 v[20:23], a[196:199], v[144:147], v[20:23]\n\t" \
  "v_accvgpr_read_b32 v74, a90\n\t" \
  "v_accvgpr_read_b32 v75, a91\n\t" \
  "s_barrier\n\t" \
  "v_mfma_f32_16x16x32_bf16 v[8:11], a[192:195], v[160:163], 0\n\t" \
  "ds_read_b128 v[144:147], v242 offset:32768\n\t" \
  "v_accvgpr_read_b32 v76, a92\n\t" \
  "v_accvgpr_read_b32 v77, a93\n\t" \
  "v_accvgpr_read_b32 v78, a94\n\t" \
  "v_mfma_f32_16x16x32_bf16 v[8:11], a[200:203], v[164:167], v[8:11]\n\t" \
  "ds_read_b32 v209, v213 offset:0\n\t" \
  "ds_read_b64_tr_b16 a[150:151], v237 offset:4096\n\t" \
  "ds_read_b128 v[148:151], v242 offset:33792\n\t" \
  "v_accvgpr_read_b32 v79, a95\n\t" \
  "v_mfma_f32_16x16x32_bf16 v[8:11], a[208:211], v[168:171], v[8:11]\n\t" \
  "buffer_store_dwordx4 v[116:119], v242, s[16:19], s98 offen offset:1024 sc1\n\t" \
  "ds_read_b64_tr_b16 a[152:153], v236 offset:8192\n\t" \
  "ds_read_b128 v[152:155], v242 offset:34816\n\t" \
  "ds_read_b64_tr_b16 a[154:155], v236 offset:12288\n\t" \
  "v_mfma_f32_16x16x32_bf16 v[8:11], a[216:219], v[172:175], v[8:11]\n\t" \
  "ds_read_b64_tr_b16 a[156:157], v237 offset:8192\n\t" \
  "v_mul_f32 v72, v72, v220\n\t" \
  "v_mfma_f32_16x16x32_bf16 v[24:27], a[220:223], v[172:175], 0\n\t" \
  "v_mul_f32 v73, v73, v220\n\t" \
  "v_mul_f32 v74, v74, v220\n\t" \
  "v_mfma_f32_16x16x32_bf16 v[24:27], a[212:215], v[168:171], v[24:27]\n\t" \
  "ds_read_b128 v[156:159], v242 offset:35840\n\t" \
  "v_mul_f32 v75, v75, v220\n\t" \
  "v_mfma_f32_16x16x32_bf16 v[24:27], a[204:207], v[164:167], v[24:27]\n\t" \
  "s_waitcnt lgkmcnt(7)\n\t" \
  "v_cmp_eq_u32 vcc, s36, v209\n\t" \
  "v_mul_f32 v76, v76, v220\n\t" \
  "v_mfma_f32_16x16x32_bf16 v[24:27], a[196:199], v[160:163], v[24:27]\n\t" \
  "s_and_b32 vcc_lo, vcc_lo, s37\n\t" \
  "s_cbranch_scc1 .Lat_rd\n\t" \
  "v_mfma_f32_16x16x32_bf16 v[12:15], a[192:195], v[128:131], 0\n\t" \
  "s_waitcnt vmcnt(7)\n\t" \
  "buffer_store_dword v243, v254, s[16:19], s49 offen offset:32 sc1\n\t" \
  "ds_read_b128 v[160:163], v242 offset:36864\n\t" \
  "v_mfma_f32_16x16x32_bf16 v[12:15], a[200:203], v[132:135], v[12:15]\n\t" \
  "ds_write_b128 v213, v[112:115] offset:0\n\t" \
  "v_mfma_f32_16x16x32_bf16 v[12:15], a[208:211], v[136:139], v[12:15]\n\t" \
  "ds_read_b64_tr_b16 a[158:159], v237 offset:12288\n\t" \
  "ds_read_b128 v[164:167], v242 offset:37888\n\t" \
  "v_mul_f32 v77, v77, v220\n\t" \
  "v_mfma_f32_16x16x32_bf16 v[12:15], a[216:219], v[140:143], v[12:15]\n\t" \
  "ds_read_b64_tr_b16 a[160:161], v238 offset:0\n\t" \
  "ds_read_b128 v[168:171], v242 offset:38912\n\t" \
  "v_mul_f32 v78, v78, v220\n\t" \
  "v_mfma_f32_16x16x32_bf16 v[28:31], a[220:223], v[140:143], 0\n\t" \
  "ds_write_b128 v213, v[116:119] offset:1024\n\t" \
  "v_mfma_f32_16x16x32_bf16 v[28:31], a[212:215], v[136:139], v[28:31]\n\t" \
  "ds_write_b128 v213, v[120:123] offset:2048\n\t" \
  "v_mfma_f32_16x16x32_bf16 v[28:31], a[204:207], v[132:135], v[28:31]\n\t" \
  "s_add_u32 s97, s64, 131072\n\t" \
  "ds_read_b64_tr_b16 a[162:163], v238 offset:4096\n\t" \
  "ds_read_b128 v[172:175], v242 offset:39936\n\t" \
  "v_mul_f32 v79, v79, v220\n\t" \
  "v_mfma_f32_16x16x32_bf16 v[28:31], a[196:199], v[128:131], v[28:31]\n\t" \
  "s_waitcnt lgkmcnt(10)\n\t" \
  "s_waitcnt vmcnt(3)\n\t" \
  "v_cmp_ne_u32 vcc, s36, v246\n\t" \
  "v_cvt_pk_bf16_f32 v124, v72, v73\n\t" \
  STP("s_memrealtime s[94:95]\n\t") \
  STP("s_memtime s[92:93]\n\t") \
  "v_mfma_f32_16x16x32_bf16 v[32:35], a[192:195], v[144:147], 0\n\t" \
  "s_and_b32 s97, s97, 0xe0000\n\t" \
  "s_add_u32 s97, s97, s60\n\t" \
  "s_add_u32 m0, s38, 49152\n\t" \
  "v_mfma_f32_16x16x32_bf16 v[32:35], a[200:203], v[148:151], v[32:35]\n\t" \
  "s_and_b32 vcc_lo, vcc_lo, 0xe\n\t" \
  "s_cbranch_scc1 .Lat_s0\n\t" \
  ".Lat_k0:\n\t" \
  "ds_read_b128 v[128:131], v242 offset:40960\n\t" \
  "v_cvt_pk_bf16_f32 v125, v74, v75\n\t" \
  "v_mfma_f32_16x16x32_bf16 v[32:35], a[208:211], v[152:155], v[32:35]\n\t" \
  "buffer_load_dwordx4 v242, s[16:19], s97 offen offset:0 sc1 lds\n\t" \
  "v_mfma_f32_16x16x32_bf16 v[32:35], a[216:219], v[156:159], v[32:35]\n\t" \
  "ds_read_b64_tr_b16 a[164:165], v239 offset:0\n\t" \
  "ds_read_b128 v[132:135], v242 offset:41984\n\t" \
  "v_cvt_pk_bf16_f32 v126, v76, v77\n\t" \
  "v_mfma_f32_16x16x32_bf16 v[48:51], a[220:223], v[156:159], 0\n\t" \
  "ds_read_b128 v[136:139], v242 offset:43008\n\t" \
  "v_cvt_pk_bf16_f32 v127, v78, v79\n\t" \
  "v_mfma_f32_16x16x32_bf16 v[48:51], a[212:215], v[152:155], v[48:51]\n\t" \
  "buffer_load_dwordx4 v242, s[16:19], s97 offen offset:1024 sc1 lds\n\t" \
  "ds_write_b128 v213, v[124:127] offset:3072\n\t" \
  "v_mfma_f32_16x16x32_bf16 v[48:51], a[204:207], v[148:151], v[48:51]\n\t" \
  "ds_read_b64_tr_b16 a[166:167], v239 offset:4096\n\t" \
  "v_mfma_f32_16x16x32_bf16 v[48:51], a[196:199], v[144:147], v[48:51]\n\t" \
  "s_waitcnt lgkmcnt(6)\n\t" \
  "ds_read_b64_tr_b16 a[168:169], v238 offset:8192\n\t" \
  "ds_read_b128 v[140:143], v242 offset:44032\n\t" \
  "v_mfma_f32_16x16x32_bf16 v[36:39], a[192:195], v[160:163], 0\n\t" \
  "buffer_load_dwordx4 v242, s[16:19], s97 offen offset:2048 sc1 lds\n\t" \
  "v_mfma_f32_16x16x32_bf16 v[36:39], a[200:203], v[164:167], v[36:39]\n\t" \
  "ds_read_b64_tr_b16 a[170:171], v238 offset:12288\n\t" \
  "ds_read_b128 v[144:147], v242 offset:45056\n\t" \
  "v_mfma_f32_16x16x32_bf16 v[36:39], a[208:211], v[168:171], v[36:39]\n\t" \
  "ds_read_b64_tr_b16 a[172:173], v239 offset:8192\n\t" \
  "ds_read_b128 v[148:151], v242 offset:46080\n\t" \
  "v_mfma_f32_16x16x32_bf16 v[36:39], a[216:219], v[172:175], v[36:39]\n\t" \
  "buffer_load_dwordx4 v242, s[16:19], s97 offen offset:3072 sc1 lds\n\t" \
  "v_mfma_f32_16x16x32_bf16 v[52:55], a[220:223], v[172:175], 0\n\t" \
  STP("v_writelane_b32 v255, s94, 24\n\t") \
  STP("v_writelane_b32 v255, s92, 25\n\t") \
  "ds_read_b128 v[152:155], v242 offset:47104\n\t" \
  "v_mfma_f32_16x16x32_bf16 v[52:55], a[212:215], v[168:171], v[52:55]\n\t" \
  "ds_read_b64_tr_b16 a[174:175], v239 offset:12288\n\t" \
  "v_mfma_f32_16x16x32_bf16 v[52:55], a[204:207], v[164:167], v[52:55]\n\t" \
  "s_waitcnt lgkmcnt(6)\n\t" \
  "buffer_store_dwordx4 v[120:123], v242, s[16:19], s98 offen offset:2048 sc1\n\t" \
  "ds_read_b64_tr_b16 a[176:177], v236 offset:16384\n\t" \
  "ds_read_b64_tr_b16 a[178:179], v236 offset:20480\n\t" \
  "ds_read_b128 v[156:159], v242 offset:48128\n\t" \
  "v_mfma_f32_16x16x32_bf16 v[52:55], a[196:199], v[160:163], v[52:55]\n\t" \
  "s_barrier\n\t" \
  "v_mfma_f32_16x16x32_bf16 v[40:43], a[192:195], v[128:131], 0\n\t" \
  "ds_read_b128 v[160:163], v242 offset:0\n\t" \
  "v_mfma_f32_16x16x32_bf16 v[40:43], a[200:203], v[132:135], v[40:43]\n\t" \
  "ds_read_b64_tr_b16 a[180:181], v237 offset:16384\n\t" \
  "v_mfma_f32_16x16x32_bf16 v[40:43], a[208:211], v[136:139], v[40:43]\n\t" \
  "ds_read_b128 v[164:167], v242 offset:1024\n\t" \
  "v_mfma_f32_16x16x32_bf16 v[40:43], a[216:219], v[140:143], v[40:43]\n\t" \
  "ds_read_b64_tr_b16 a[182:183], v237 offset:20480\n\t" \
  "v_mfma_f32_16x16x32_bf16 v[56:59], a[220:223], v[140:143], 0\n\t" \
  "ds_read_b128 v[168:171], v242 offset:2048\n\t" \
  "v_mfma_f32_16x16x32_bf16 v[56:59], a[212:215], v[136:139], v[56:59]\n\t" \
  "ds_read_b128 v[172:175], v242 offset:3072\n\t" \
  "v_mfma_f32_16x16x32_bf16 v[56:59], a[204:207], v[132:135], v[56:59]\n\t" \
  "ds_read_b64_tr_b16 a[184:185], v236 offset:24576\n\t" \
  "v_mfma_f32_16x16x32_bf16 v[56:59], a[196:199], v[128:131], v[56:59]\n\t" \
  "s_waitcnt lgkmcnt(7)\n\t" \
  "ds_read_b64_tr_b16 a[186:187], v236 offset:28672\n\t" \
  "v_mfma_f32_16x16x32_bf16 v[44:47], a[192:195], v[144:147], 0\n\t" \
  "ds_read_b128 v[128:131], v242 offset:4096\n\t" \
  "v_mfma_f32_16x16x32_bf16 v[44:47], a[200:203], v[148:151], v[44:47]\n\t" \
  "ds_read_b128 v[132:135], v242 offset:5120\n\t" \
  "v_mfma_f32_16x16x32_bf16 v[44:47], a[208:211], v[152:155], v[44:47]\n\t" \
  "ds_read_b128 v[136:139], v242 offset:6144\n\t" \
  "v_mfma_f32_16x16x32_bf16 v[44:47], a[216:219], v[156:159], v[44:47]\n\t" \
  "ds_read_b64_tr_b16 a[188:189], v237 offset:24576\n\t" \
  "v_mfma_f32_16x16x32_bf16 v[60:63], a[220:223], v[156:159], 0\n\t" \
  "ds_read_b128 v[140:143], v242 offset:7168\n\t" \
  "v_mfma_f32_16x16x32_bf16 v[60:63], a[212:215], v[152:155], v[60:63]\n\t" \
  "ds_read_b64_tr_b16 a[190:191], v237 offset:28672\n\t" \
  "v_mfma_f32_16x16x32_bf16 v[60:63], a[204:207], v[148:151], v[60:63]\n\t" \
  "ds_read_b64_tr_b16 a[0:1], v236 offset:49152\n\t" \
  "v_mfma_f32_16x16x32_bf16 v[60:63], a[196:199], v[144:147], v[60:63]\n\t" \
  "s_waitcnt lgkmcnt(9)\n\t" \
  "ds_read_b64_tr_b16 a[2:3], v236 offset:53248\n\t" \
  STP("s_memrealtime s[94:95]\n\t") \
  STP("s_memtime s[92:93]\n\t") \
  "v_mfma_f32_16x16x32_bf16 v[64:67], a[192:195], v[160:163], 0\n\t" \
  "ds_read_b128 v[144:147], v242 offset:8192\n\t" \
  "v_mfma_f32_16x16x32_bf16 v[64:67], a[200:203], v[164:167], v[64:67]\n\t" \
  "s_add_u32 s97, s64, 262144\n\t" \
  "ds_read_b128 v[148:151], v242 offset:9216\n\t" \
  "s_and_b32 s97, s97, 0xe0000\n\t" \
  "v_mfma_f32_16x16x32_bf16 v[64:67], a[208:211], v[168:171], v[64:67]\n\t" \
  "s_add_u32 s97, s97, s60\n\t" \
  "ds_read_b128 v[152:155], v242 offset:10240\n\t" \
  "s_add_u32 m0, s38, 65536\n\t" \
  "v_mfma_f32_16x16x32_bf16 v[64:67], a[216:219], v[172:175], v[64:67]\n\t" \
  "ds_read_b128 v[156:159], v242 offset:11264\n\t" \
  "v_mfma_f32_16x16x32_bf16 v[80:83], a[220:223], v[172:175], 0\n\t" \
  "ds_read_b64_tr_b16 a[4:5], v237 offset:49152\n\t" \
  "v_mfma_f32_16x16x32_bf16 v[80:83], a[212:215], v[168:171], v[80:83]\n\t" \
  "buffer_load_dword v246, v253, s[16:19], s65 offen sc1\n\t" \
  "v_mfma_f32_16x16x32_bf16 v[80:83], a[204:207], v[164:167], v[80:83]\n\t" \
  "ds_read_b64_tr_b16 a[6:7], v237 offset:53248\n\t" \
  "v_mfma_f32_16x16x32_bf16 v[80:83], a[196:199], v[160:163], v[80:83]\n\t" \
  "s_waitcnt lgkmcnt(9)\n\t" \
  "buffer_load_dwordx4 v242, s[16:19], s97 offen offset:0 sc1 lds\n\t" \
  "v_mfma_f32_16x16x32_bf16 v[68:71], a[192:195], v[128:131], 0\n\t" \
  "s_mov_b64 exec, 0\n\t" \
  "s_cmp_eq_u32 s3, 0\n\t" \
  "s_cbranch_scc1 .Lhc10\n\t" \
  "s_cmp_eq_u32 s3, 1\n\t" \
  "s_cbranch_scc1 .Lhc11\n\t" \
  "s_cmp_eq_u32 s3, 2\n\t" \
  "s_cbranch_scc1 .Lhc12\n\t" \
  "s_branch .Lhc13\n\t" \
  ".Lhr1:\n\t" \
  "s_mov_b64 exec, -1\n\t" \
  "v_mfma_f32_16x16x32_bf16 v[68:71], a[200:203], v[132:135], v[68:71]\n\t" \
  "ds_read_b128 v[160:163], v242 offset:12288\n\t" \
  "v_mfma_f32_16x16x32_bf16 v[68:71], a[208:211], v[136:139], v[68:71]\n\t" \
  "buffer_load_dwordx4 v242, s[16:19], s97 offen offset:1024 sc1 lds\n\t" \
  "v_mfma_f32_16x16x32_bf16 v[68:71], a[216:219], v[140:143], v[68:71]\n\t" \
  "ds_read_b64_tr_b16 a[8:9], v236 offset:57344\n\t" \
  "ds_read_b128 v[164:167], v242 offset:13312\n\t" \
  "v_mfma_f32_16x16x32_bf16 v[84:87], a[220:223], v[140:143], 0\n\t" \
  STP("v_writelane_b32 v255, s94, 26\n\t") \
  STP("v_writelane_b32 v255, s92, 27\n\t") \
  "ds_read_b128 v[168:171], v242 offset:14336\n\t" \
  "v_mfma_f32_16x16x32_bf16 v[84:87], a[212:215], v[136:139], v[84:87]\n\t" \
  "buffer_load_dwordx4 v242, s[16:19], s97 offen offset:2048 sc1 lds\n\t" \
  "v_mfma_f32_16x16x32_bf16 v[84:87], a[204:207], v[132:135], v[84:87]\n\t" \
  "s_waitcnt vmcnt(5) lgkmcnt(6)\n\t" \
  "ds_read_b128 v[172:175], v242 offset:15360\n\t" \
  "v_mfma_f32_16x16x32_bf16 v[84:87], a[196:199], v[128:131], v[84:87]\n\t" \
  "s_barrier\n\t" \
  "v_mfma_f32_16x16x32_bf16 v[72:75], a[192:195], v[144:147], 0\n\t" \
  "buffer_load_dwordx4 v242, s[16:19], s97 offen offset:3072 sc1 lds\n\t" \
  "v_mfma_f32_16x16x32_bf16 v[72:75], a[200:203], v[148:151], v[72:75]\n\t" \
  "s_add_u32 s97, s64, 393216\n\t" \
  "s_and_b32 s97, s97, 0xe0000\n\t" \
  "s_add_u32 s97, s97, s60\n\t" \
  "v_mfma_f32_16x16x32_bf16 v[72:75], a[208:211], v[152:155], v[72:75]\n\t" \
  "s_add_u32 m0, s38, 81920\n\t" \
  "ds_read_b64_tr_b16 a[10:11], v236 offset:61440\n\t" \
  "ds_read_b128 v[128:131], v242 offset:49152\n\t" \
  "v_mfma_f32_16x16x32_bf16 v[72:75], a[216:219], v[156:159], v[72:75]\n\t" \
  "buffer_load_dwordx4 v242, s[16:19], s97 offen offset:0 sc1 lds\n\t" \
  "v_mfma_f32_16x16x32_bf16 v[88:91], a[220:223], v[156:159], 0\n\t" \
  "ds_read_b64_tr_b16 a[12:13], v237 offset:57344\n\t" \
  "ds_read_b128 v[132:135], v242 offset:50176\n\t" \
  "v_mfma_f32_16x16x32_bf16 v[88:91], a[212:215], v[152:155], v[88:91]\n\t" \
  "ds_read_b64_tr_b16 a[14:15], v237 offset:61440\n\t" \
  "ds_read_b128 v[136:139], v242 offset:51200\n\t" \
  "v_mfma_f32_16x16x32_bf16 v[88:91], a[204:207], v[148:151], v[88:91]\n\t" \
  "buffer_load_dwordx4 v242, s[16:19], s97 offen offset:1024 sc1 lds\n\t" \
  "v_mfma_f32_16x16x32_bf16 v[88:91], a[196:199], v[144:147], v[88:91]\n\t" \
  "s_waitcnt lgkmcnt(6)\n\t" \
  "ds_read_b64_tr_b16 a[16:17], v238 offset:16384\n\t" \
  "ds_read_b128 v[140:143], v242 offset:52224\n\t" \
  "v_mfma_f32_16x16x32_bf16 v[76:79], a[192:195], v[160:163], 0\n\t" \
  "ds_read_b128 v[144:147], v242 offset:53248\n\t" \
  "v_mfma_f32_16x16x32_bf16 v[76:79], a[200:203], v[164:167], v[76:79]\n\t" \
  "buffer_load_dwordx4 v242, s[16:19], s97 offen offset:2048 sc1 lds\n\t" \
  "v_mfma_f32_16x16x32_bf16 v[76:79], a[208:211], v[168:171], v[76:79]\n\t" \
  "ds_read_b128 v[148:151], v242 offset:54272\n\t" \
  "v_mfma_f32_16x16x32_bf16 v[76:79], a[216:219], v[172:175], v[76:79]\n\t" \
  "ds_read_b128 v[152:155], v242 offset:55296\n\t" \
  "v_mfma_f32_16x16x32_bf16 v[92:95], a[220:223], v[172:175], 0\n\t" \
  "buffer_load_dwordx4 v242, s[16:19], s97 offen offset:3072 sc1 lds\n\t" \
  "v_mfma_f32_16x16x32_bf16 v[92:95], a[212:215], v[168:171], v[92:95]\n\t" \
  "s_add_u32 s96, s40, 1048576\n\t" \
  "s_and_b32 s96, s96, 0x1c0000\n\t" \
  "s_add_u32 s96, s96, s77\n\t" \
  "v_mfma_f32_16x16x32_bf16 v[92:95], a[204:207], v[164:167], v[92:95]\n\t" \
  "s_add_u32 m0, s38, 131072\n\t" \
  "ds_read_b64_tr_b16 a[18:19], v238 offset:20480\n\t" \
  "ds_read_b128 v[156:159], v242 offset:56320\n\t" \
  ".Lhs11:\n\ts_cbranch_execz .Lhs11n\n\t" \
  "v_mfma_f32_16x16x32_bf16 v[92:95], a[196:199], v[160:163], v[92:95]\n\t" \
  "s_waitcnt lgkmcnt(5)\n\t" \
  "buffer_load_dwordx4 v240, s[24:27], s96 offen offset:0 lds\n\t" \
  STP("s_memrealtime s[94:95]\n\t") \
  STP("s_memtime s[92:93]\n\t") \
  "v_mfma_f32_16x16x32_bf16 v[0:3], a[128:131], v[128:131], v[0:3]\n\t" \
  "ds_read_b64_tr_b16 a[20:21], v239 offset:16384\n\t" \
  "ds_read_b128 v[160:163], v242 offset:57344\n\t" \
  "v_accvgpr_read_b32 v96, a96\n\t" \
  "v_mfma_f32_16x16x32_bf16 v[0:3], a[136:139], v[132:135], v[0:3]\n\t" \
  "ds_read_b64_tr_b16 a[22:23], v239 offset:20480\n\t" \
  "ds_read_b128 v[164:167], v242 offset:58368\n\t" \
  "v_accvgpr_read_b32 v97, a97\n\t" \
  "v_mfma_f32_16x16x32_bf16 v[0:3], a[144:147], v[136:139], v[0:3]\n\t" \
  "buffer_load_dwordx4 v241, s[24:27], s96 offen offset:1024 lds\n\t" \
  "v_mfma_f32_16x16x32_bf16 v[0:3], a[152:155], v[140:143], v[0:3]\n\t" \
  "ds_read_b64_tr_b16 a[24:25], v238 offset:24576\n\t" \
  "ds_read_b128 v[168:171], v242 offset:59392\n\t" \
  "v_accvgpr_read_b32 v98, a98\n\t" \
  "v_mfma_f32_16x16x32_bf16 v[16:19], a[156:159], v[140:143], v[16:19]\n\t" \
  "s_waitcnt vmcnt(10)\n\t" \
  "v_cmp_ne_u32 vcc, s36, v246\n\t" \
  "v_accvgpr_read_b32 v99, a99\n\t" \
  "v_mfma_f32_16x16x32_bf16 v[16:19], a[148:151], v[136:139], v[16:19]\n\t" \
  "buffer_load_dwordx4 v248, s[24:27], s96 offen offset:2048 lds\n\t" \
  "v_mfma_f32_16x16x32_bf16 v[16:19], a[140:143], v[132:135], v[16:19]\n\t" \
  "s_and_b32 vcc_lo, vcc_lo, 0xfefe\n\t" \
  "s_cbranch_scc1 .Lat_s1\n\t" \
  ".Lat_k1:\n\t" \
  "v_mfma_f32_16x16x32_bf16 v[16:19], a[132:135], v[128:131], v[16:19]\n\t" \
  "s_waitcnt lgkmcnt(6)\n\t" \
  "ds_read_b64_tr_b16 a[26:27], v238 offset:28672\n\t" \
  "ds_read_b128 v[172:175], v242 offset:60416\n\t" \
  "v_accvgpr_read_b32 v100, a100\n\t" \
  "v_mfma_f32_16x16x32_bf16 v[4:7], a[128:131], v[144:147], v[4:7]\n\t" \
  "buffer_load_dwordx4 v249, s[24:27], s96 offen offset:3072 lds\n\t" \
  "v_mfma_f32_16x16x32_bf16 v[4:7], a[136:139], v[148:151], v[4:7]\n\t" \
  "s_add_u32 s97, s64, 131072\n\t" \
  "s_and_b32 s97, s97, 0xe0000\n\t" \
  "ds_read_b128 v[128:131], v242 offset:61440\n\t" \
  "s_add_u32 s97, s97, s61\n\t" \
  "v_mfma_f32_16x16x32_bf16 v[4:7], a[144:147], v[152:155], v[4:7]\n\t" \
  "s_add_u32 m0, s38, 32768\n\t" \
  "ds_read_b64_tr_b16 a[28:29], v239 offset:24576\n\t" \
  "ds_read_b128 v[132:135], v242 offset:62464\n\t" \
  "v_accvgpr_read_b32 v101, a101\n\t" \
  "v_mfma_f32_16x16x32_bf16 v[4:7], a[152:155], v[156:159], v[4:7]\n\t" \
  "buffer_load_dwordx4 v242, s[16:19], s97 offen offset:0 sc1 lds\n\t" \
  "v_mfma_f32_16x16x32_bf16 v[20:23], a[156:159], v[156:159], v[20:23]\n\t" \
  STP("v_writelane_b32 v255, s94, 28\n\t") \
  STP("v_writelane_b32 v255, s92, 29\n\t") \
  "ds_read_b128 v[136:139], v242 offset:63488\n\t" \
  "v_accvgpr_read_b32 v102, a102\n\t" \
  "v_mfma_f32_16x16x32_bf16 v[20:23], a[148:151], v[152:155], v[20:23]\n\t" \
  "ds_read_b64_tr_b16 a[30:31], v239 offset:28672\n\t" \
  "v_accvgpr_read_b32 v103, a103\n\t" \
  "v_mfma_f32_16x16x32_bf16 v[20:23], a[140:143], v[148:151], v[20:23]\n\t" \
  "s_waitcnt vmcnt(9) lgkmcnt(5)\n\t" \
  "buffer_load_dwordx4 v242, s[16:19], s97 offen offset:1024 sc1 lds\n\t" \
  "v_mfma_f32_16x16x32_bf16 v[20:23], a[132:135], v[144:147], v[20:23]\n\t" \
  "s_barrier\n\t" \
  "v_mfma_f32_16x16x32_bf16 v[8:11], a[128:131], v[160:163], v[8:11]\n\t" \
  "ds_read_b128 v[140:143], v242 offset:64512\n\t" \
  "v_mul_f32 v96, v96, v222\n\t" \
  "v_mfma_f32_16x16x32_bf16 v[8:11], a[136:139], v[164:167], v[8:11]\n\t" \
  "buffer_load_dwordx4 v242, s[16:19], s97 offen offset:2048 sc1 lds\n\t" \
  "v_mfma_f32_16x16x32_bf16 v[8:11], a[144:147], v[168:171], v[8:11]\n\t" \
  "ds_read_b128 v[144:147], v230 offset:0\n\t" \
  "v_mul_f32 v97, v97, v222\n\t" \
  "v_mfma_f32_16x16x32_bf16 v[8:11], a[152:155], v[172:175], v[8:11]\n\t" \
  "ds_read_b128 v[148:151], v230 offset:1024\n\t" \
  "v_mul_f32 v98, v98, v222\n\t" \
  "v_mfma_f32_16x16x32_bf16 v[24:27], a[156:159], v[172:175], v[24:27]\n\t" \
  "buffer_load_dwordx4 v242, s[16:19], s97 offen offset:3072 sc1 lds\n\t" \
  "v_mfma_f32_16x16x32_bf16 v[24:27], a[148:151], v[168:171], v[24:27]\n\t" \
  "ds_read_b128 v[152:155], v230 offset:2048\n\t" \
  "v_mul_f32 v99, v99, v222\n\t" \
  "v_mfma_f32_16x16x32_bf16 v[24:27], a[140:143], v[164:167], v[24:27]\n\t" \
  "ds_read_b64_tr_b16 a[32:33], v236 offset:32768\n\t" \
  "v_mul_f32 v100, v100, v222\n\t" \
  "v_mfma_f32_16x16x32_bf16 v[24:27], a[132:135], v[160:163], v[24:27]\n\t" \
  "s_waitcnt lgkmcnt(4)\n\t" \
  "buffer_store_dwordx4 v[124:127], v242, s[16:19], s98 offen offset:3072 sc1\n\t" \
  "ds_read_b64_tr_b16 a[34:35], v236 offset:36864\n\t" \
  "ds_read_b64_tr_b16 a[36:37], v237 offset:32768\n\t" \
  "ds_read_b128 v[156:159], v230 offset:3072\n\t" \
  "v_mfma_f32_16x16x32_bf16 v[12:15], a[128:131], v[128:131], v[12:15]\n\t" \
  "s_xor_b32 s98, s45, 98304\n\t" \
  "ds_read_b128 v[160:163], v230 offset:4096\n\t" \
  "v_mul_f32 v101, v101, v222\n\t" \
  "v_mfma_f32_16x16x32_bf16 v[12:15], a[136:139], v[132:135], v[12:15]\n\t" \
  "ds_read_b64_tr_b16 a[38:39], v237 offset:36864\n\t" \
  "v_mul_f32 v102, v102, v222\n\t" \
  "v_mfma_f32_16x16x32_bf16 v[12:15], a[144:147], v[136:139], v[12:15]\n\t" \
  "s_add_u32 s97, s64, 524288\n\t" \
  "ds_read_b128 v[164:167], v230 offset:5120\n\t" \
  "v_mul_f32 v103, v103, v222\n\t" \
  "v_mfma_f32_16x16x32_bf16 v[12:15], a[152:155], v[140:143], v[12:15]\n\t" \
  "s_and_b32 s97, s97, 0xe0000\n\t" \
  "ds_read_b64_tr_b16 a[40:41], v236 offset:40960\n\t" \
  "v_cvt_pk_bf16_f32 v96, v96, v97\n\t" \
  "v_mfma_f32_16x16x32_bf16 v[28:31], a[156:159], v[140:143], v[28:31]\n\t" \
  "s_add_u32 s97, s97, s60\n\t" \
  "ds_read_b128 v[168:171], v230 offset:6144\n\t" \
  "v_cvt_pk_bf16_f32 v97, v98, v99\n\t" \
  "v_mfma_f32_16x16x32_bf16 v[28:31], a[148:151], v[136:139], v[28:31]\n\t" \
  "s_add_u32 m0, s38, 0\n\t" \
  "ds_read_b64_tr_b16 a[42:43], v236 offset:45056\n\t" \
  "v_cvt_pk_bf16_f32 v98, v100, v101\n\t" \
  "v_mfma_f32_16x16x32_bf16 v[28:31], a[140:143], v[132:135], v[28:31]\n\t" \
  "ds_read_b128 v[172:175], v230 offset:7168\n\t" \
  "v_cvt_pk_bf16_f32 v99, v102, v103\n\t" \
  "v_mfma_f32_16x16x32_bf16 v[28:31], a[132:135], v[128:131], v[28:31]\n\t" \
  "s_waitcnt lgkmcnt(7)\n\t" \
  "ds_write_b128 v213, v[96:99] offset:16384\n\t" \
  STP("s_memrealtime s[94:95]\n\t") \
  STP("s_memtime s[92:93]\n\t") \
  "v_mfma_f32_16x16x32_bf16 v[0:3], a[160:163], v[144:147], v[0:3]\n\t" \
  "buffer_load_dwordx4 v242, s[16:19], s97 offen offset:0 sc1 lds\n\t" \
  "v_mfma_f32_16x16x32_bf16 v[0:3], a[168:171], v[148:151], v[0:3]\n\t" \
  "ds_read_b128 v[128:131], v230 offset:8192\n\t" \
  "v_accvgpr_read_b32 v104, a104\n\t" \
  "v_mfma_f32_16x16x32_bf16 v[0:3], a[176:179], v[152:155], v[0:3]\n\t" \
  "ds_read_b64_tr_b16 a[44:45], v237 offset:40960\n\t" \
  "v_accvgpr_read_b32 v105, a105\n\t" \
  "v_mfma_f32_16x16x32_bf16 v[0:3], a[184:187], v[156:159], v[0:3]\n\t" \
  "buffer_load_dwordx4 v242, s[16:19], s97 offen offset:1024 sc1 lds\n\t" \
  "v_mfma_f32_16x16x32_bf16 v[16:19], a[188:191], v[156:159], v[16:19]\n\t" \
  "ds_read_b128 v[132:135], v230 offset:9216\n\t" \
  "v_accvgpr_read_b32 v106, a106\n\t" \
  "v_mfma_f32_16x16x32_bf16 v[16:19], a[180:183], v[152:155], v[16:19]\n\t" \
  "ds_read_b128 v[136:139], v230 offset:10240\n\t" \
  "v_accvgpr_read_b32 v107, a107\n\t" \
  "v_mfma_f32_16x16x32_bf16 v[16:19], a[172:175], v[148:151], v[16:19]\n\t" \
  "ds_read_b64_tr_b16 a[46:47], v237 offset:45056\n\t" \
  "v_accvgpr_read_b32 v108, a108\n\t" \
  "v_mfma_f32_16x16x32_bf16 v[16:19], a[164:167], v[144:147], v[16:19]\n\t" \
  "s_waitcnt lgkmcnt(6)\n\t" \
  "ds_read_b128 v[140:143], v230 offset:11264\n\t" \
  "v_accvgpr_read_b32 v109, a109\n\t" \
  "v_mfma_f32_16x16x32_bf16 v[4:7], a[160:163], v[160:163], v[4:7]\n\t" \
  "ds_read_b128 v[144:147], v230 offset:12288\n\t" \
  "v_accvgpr_read_b32 v110, a110\n\t" \
  "v_mfma_f32_16x16x32_bf16 v[4:7], a[168:171], v[164:167], v[4:7]\n\t" \
  "v_accvgpr_read_b32 v111, a111\n\t" \
  "v_mul_f32 v104, v104, v222\n\t" \
  "v_mfma_f32_16x16x32_bf16 v[4:7], a[176:179], v[168:171], v[4:7]\n\t" \
  "ds_read_b128 v[148:151], v230 offset:13312\n\t" \
  "v_mul_f32 v105, v105, v222\n\t" \
  "v_mfma_f32_16x16x32_bf16 v[4:7], a[184:187], v[172:175], v[4:7]\n\t" \
  "buffer_load_dwordx4 v242, s[16:19], s97 offen offset:2048 sc1 lds\n\t" \
  "v_mfma_f32_16x16x32_bf16 v[20:23], a[188:191], v[172:175], v[20:23]\n\t" \
  STP("v_writelane_b32 v255, s94, 30\n\t") \
  STP("v_writelane_b32 v255, s92, 31\n\t") \
  "ds_read_b128 v[152:155], v230 offset:14336\n\t" \
  "v_mul_f32 v106, v106, v222\n\t" \
  "v_mfma_f32_16x16x32_bf16 v[20:23], a[180:183], v[168:171], v[20:23]\n\t" \
  "v_mul_f32 v107, v107, v222\n\t" \
  "v_mul_f32 v108, v108, v222\n\t" \
  "v_mfma_f32_16x16x32_bf16 v[20:23], a[172:175], v[164:167], v[20:23]\n\t" \
  "s_waitcnt vmcnt(12) lgkmcnt(3)\n\t" \
  "buffer_load_dwordx4 v242, s[16:19], s97 offen offset:3072 sc1 lds\n\t" \
  "v_mfma_f32_16x16x32_bf16 v[20:23], a[164:167], v[160:163], v[20:23]\n\t" \
  "s_barrier\n\t" \
  "v_mfma_f32_16x16x32_bf16 v[8:11], a[160:163], v[128:131], v[8:11]\n\t" \
  "ds_read_b128 v[156:159], v230 offset:15360\n\t" \
  "v_mul_f32 v109, v109, v222\n\t" \
  "v_mfma_f32_16x16x32_bf16 v[8:11], a[168:171], v[132:135], v[8:11]\n\t" \
  "buffer_store_dwordx4 v[96:99], v242, s[16:19], s98 offen offset:0 sc1\n\t" \
  "v_mul_f32 v110, v110, v222\n\t" \
  "ds_read_b128 v[160:163], v230 offset:16384\n\t" \
  "v_mul_f32 v111, v111, v222\n\t" \
  "v_mfma_f32_16x16x32_bf16 v[8:11], a[176:179], v[136:139], v[8:11]\n\t" \
  "s_add_u32 s96, s40, 1310720\n\t" \
  "ds_read_b128 v[164:167], v230 offset:17408\n\t" \
  "v_cvt_pk_bf16_f32 v104, v104, v105\n\t" \
  "v_mfma_f32_16x16x32_bf16 v[8:11], a[184:187], v[140:143], v[8:11]\n\t" \
  "s_and_b32 s96, s96, 0x1c0000\n\t" \
  "v_cvt_pk_bf16_f32 v105, v106, v107\n\t" \
  "v_cvt_pk_bf16_f32 v106, v108, v109\n\t" \
  "v_mfma_f32_16x16x32_bf16 v[24:27], a[188:191], v[140:143], v[24:27]\n\t" \
  "s_add_u32 s96, s96, s51\n\t" \
  "ds_read_b128 v[168:171], v230 offset:18432\n\t" \
  "v_cvt_pk_bf16_f32 v107, v110, v111\n\t" \
  "v_mfma_f32_16x16x32_bf16 v[24:27], a[180:183], v[136:139], v[24:27]\n\t" \
  "ds_write_b128 v213, v[104:107] offset:17408\n\t" \
  "v_mfma_f32_16x16x32_bf16 v[24:27], a[172:175], v[132:135], v[24:27]\n\t" \
  "s_add_u32 m0, s38, 147456\n\t" \
  "ds_read_b128 v[172:175], v230 offset:19456\n\t" \
  "v_accvgpr_read_b32 v96, a112\n\t" \
  "v_mfma_f32_16x16x32_bf16 v[24:27], a[164:167], v[128:131], v[24:27]\n\t" \
  "s_waitcnt lgkmcnt(5)\n\t" \
  "v_accvgpr_read_b32 v97, a113\n\t" \
  "v_accvgpr_read_b32 v98, a114\n\t" \
  "v_mfma_f32_16x16x32_bf16 v[12:15], a[160:163], v[144:147], v[12:15]\n\t" \
  "buffer_load_dwordx4 v240, s[24:27], s96 offen offset:0 lds\n\t" \
  "v_mfma_f32_16x16x32_bf16 v[12:15], a[168:171], v[148:151], v[12:15]\n\t" \
  "ds_read_b128 v[128:131], v230 offset:20480\n\t" \
  "v_accvgpr_read_b32 v99, a115\n\t" \
  "v_mfma_f32_16x16x32_bf16 v[12:15], a[176:179], v[152:155], v[12:15]\n\t" \
  "ds_read_b128 v[132:135], v230 offset:21504\n\t" \
  "v_accvgpr_read_b32 v100, a116\n\t" \
  "v_mfma_f32_16x16x32_bf16 v[12:15], a[184:187], v[156:159], v[12:15]\n\t" \
  "v_accvgpr_read_b32 v101, a117\n\t" \
  "v_accvgpr_read_b32 v102, a118\n\t" \
  "v_mfma_f32_16x16x32_bf16 v[28:31], a[188:191], v[156:159], v[28:31]\n\t" \
  "buffer_load_dwordx4 v241, s[24:27], s96 offen offset:1024 lds\n\t" \
  "v_mfma_f32_16x16x32_bf16 v[28:31], a[180:183], v[152:155], v[28:31]\n\t" \
  "ds_read_b128 v[136:139], v230 offset:22528\n\t" \
  "v_accvgpr_read_b32 v103, a119\n\t" \
  "v_mfma_f32_16x16x32_bf16 v[28:31], a[172:175], v[148:151], v[28:31]\n\t" \
  "ds_read_b128 v[140:143], v230 offset:23552\n\t" \
  "v_mul_f32 v96, v96, v222\n\t" \
  "v_mfma_f32_16x16x32_bf16 v[28:31], a[164:167], v[144:147], v[28:31]\n\t" \
  "s_waitcnt lgkmcnt(4)\n\t" \
  "buffer_load_dwordx4 v248, s[24:27], s96 offen offset:2048 lds\n\t" \
  "v_mfma_f32_16x16x32_bf16 v[0:3], a[0:3], v[160:163], v[0:3]\n\t" \
  "ds_read_b128 v[144:147], v230 offset:24576\n\t" \
  "v_mul_f32 v97, v97, v222\n\t" \
  "v_mfma_f32_16x16x32_bf16 v[0:3], a[8:11], v[164:167], v[0:3]\n\t" \
  "v_mul_f32 v98, v98, v222\n\t" \
  "v_mul_f32 v99, v99, v222\n\t" \
  "v_mfma_f32_16x16x32_bf16 v[0:3], a[16:19], v[168:171], v[0:3]\n\t" \
  "buffer_load_dwordx4 v249, s[24:27], s96 offen offset:3072 lds\n\t" \
  "v_mfma_f32_16x16x32_bf16 v[0:3], a[24:27], v[172:175], v[0:3]\n\t" \
  "s_add_u32 s97, s64, 262144\n\t" \
  "s_and_b32 s97, s97, 0xe0000\n\t" \
  "s_add_u32 s97, s97, s61\n\t" \
  "v_mfma_f32_16x16x32_bf16 v[16:19], a[28:31], v[172:175], v[16:19]\n\t" \
  "s_add_u32 m0, s38, 49152\n\t" \
  "ds_read_b128 v[148:151], v230 offset:25600\n\t" \
  "v_mul_f32 v100, v100, v222\n\t" \
  "v_mfma_f32_16x16x32_bf16 v[16:19], a[20:23], v[168:171], v[16:19]\n\t" \
  "buffer_load_dwordx4 v242, s[16:19], s97 offen offset:0 sc1 lds\n\t" \
  "v_mfma_f32_16x16x32_bf16 v[16:19], a[12:15], v[164:167], v[16:19]\n\t" \
  "ds_read_b128 v[152:155], v230 offset:26624\n\t" \
  "v_mul_f32 v101, v101, v222\n\t" \
  "v_mfma_f32_16x16x32_bf16 v[16:19], a[4:7], v[160:163], v[16:19]\n\t" \
  "s_waitcnt lgkmcnt(3)\n\t" \
  "ds_read_b128 v[156:159], v230 offset:27648\n\t" \
  "v_mul_f32 v102, v102, v222\n\t" \
  "v_mfma_f32_16x16x32_bf16 v[4:7], a[0:3], v[128:131], v[4:7]\n\t" \
  "ds_read_b128 v[160:163], v230 offset:28672\n\t" \
  "v_mul_f32 v103, v103, v222\n\t" \
  "v_mfma_f32_16x16x32_bf16 v[4:7], a[8:11], v[132:135], v[4:7]\n\t" \
  "v_cvt_pk_bf16_f32 v96, v96, v97\n\t" \
  "v_cvt_pk_bf16_f32 v97, v98, v99\n\t" \
  "v_mfma_f32_16x16x32_bf16 v[4:7], a[16:19], v[136:139], v[4:7]\n\t" \
  "ds_read_b128 v[164:167], v230 offset:29696\n\t" \
  "v_cvt_pk_bf16_f32 v98, v100, v101\n\t" \
  "v_mfma_f32_16x16x32_bf16 v[4:7], a[24:27], v[140:143], v[4:7]\n\t" \
  "buffer_load_dwordx4 v242, s[16:19], s97 offen offset:1024 sc1 lds\n\t" \
  "v_mfma_f32_16x16x32_bf16 v[20:23], a[28:31], v[140:143], v[20:23]\n\t" \
  "ds_read_b128 v[168:171], v230 offset:30720\n\t" \
  "v_cvt_pk_bf16_f32 v99, v102, v103\n\t" \
  "v_mfma_f32_16x16x32_bf16 v[20:23], a[20:23], v[136:139], v[20:23]\n\t" \
  "ds_write_b128 v213, v[96:99] offset:18432\n\t" \
  "v_mfma_f32_16x16x32_bf16 v[20:23], a[12:15], v[132:135], v[20:23]\n\t" \
  "s_waitcnt vmcnt(12) lgkmcnt(4)\n\t" \
  "buffer_load_dwordx4 v242, s[16:19], s97 offen offset:2048 sc1 lds\n\t" \
  "v_mfma_f32_16x16x32_bf16 v[20:23], a[4:7], v[128:131], v[20:23]\n\t" \
  "s_barrier\n\t" \
  "v_mfma_f32_16x16x32_bf16 v[8:11], a[0:3], v[144:147], v[8:11]\n\t" \
  "ds_read_b128 v[172:175], v230 offset:31744\n\t" \
  "v_mfma_f32_16x16x32_bf16 v[8:11], a[8:11], v[148:151], v[8:11]\n\t" \
  "buffer_load_dwordx4 v242, s[16:19], s97 offen offset:3072 sc1 lds\n\t" \
  "v_mfma_f32_16x16x32_bf16 v[8:11], a[16:19], v[152:155], v[8:11]\n\t" \
  "ds_read_b128 v[128:131], v242 offset:32768\n\t" \
  "v_mfma_f32_16x16x32_bf16 v[8:11], a[24:27], v[156:159], v[8:11]\n\t" \
  "ds_read_b128 v[132:135], v242 offset:33792\n\t" \
  "v_mfma_f32_16x16x32_bf16 v[24:27], a[28:31], v[156:159], v[24:27]\n\t" \
  "buffer_store_dwordx4 v[104:107], v242, s[16:19], s98 offen offset:1024 sc1\n\t" \
  "ds_read_b64_tr_b16 a[48:49], v238 offset:0\n\t" \
  "ds_read_b64_tr_b16 a[50:51], v238 offset:4096\n\t" \
  "ds_read_b128 v[136:139], v242 offset:34816\n\t" \
  "v_mfma_f32_16x16x32_bf16 v[24:27], a[20:23], v[152:155], v[24:27]\n\t" \
  "v_accvgpr_read_b32 v104, a120\n\t" \
  "v_accvgpr_read_b32 v105, a121\n\t" \
  "v_mfma_f32_16x16x32_bf16 v[24:27], a[12:15], v[148:151], v[24:27]\n\t" \
  "ds_read_b128 v[140:143], v242 offset:35840\n\t" \
  "v_accvgpr_read_b32 v106, a122\n\t" \
  "v_mfma_f32_16x16x32_bf16 v[24:27], a[4:7], v[144:147], v[24:27]\n\t" \
  "s_waitcnt lgkmcnt(6)\n\t" \
  "ds_read_b64_tr_b16 a[52:53], v239 offset:0\n\t" \
  "v_accvgpr_read_b32 v107, a123\n\t" \
  "v_mfma_f32_16x16x32_bf16 v[12:15], a[0:3], v[160:163], v[12:15]\n\t" \
  "s_add_u32 s96, s40, 1310720\n\t" \
  "ds_read_b128 v[144:147], v242 offset:36864\n\t" \
  "v_accvgpr_read_b32 v108, a124\n\t" \
  "v_mfma_f32_16x16x32_bf16 v[12:15], a[8:11], v[164:167], v[12:15]\n\t" \
  "s_and_b32 s96, s96, 0x1c0000\n\t" \
  "ds_read_b64_tr_b16 a[54:55], v239 offset:4096\n\t" \
  "v_accvgpr_read_b32 v109, a125\n\t" \
  "v_mfma_f32_16x16x32_bf16 v[12:15], a[16:19], v[168:171], v[12:15]\n\t" \
  "s_add_u32 s96, s96, s77\n\t" \
  "ds_read_b128 v[148:151], v242 offset:37888\n\t" \
  "v_accvgpr_read_b32 v110, a126\n\t" \
  "v_mfma_f32_16x16x32_bf16 v[12:15], a[24:27], v[172:175], v[12:15]\n\t" \
  "s_add_u32 m0, s38, 65536\n\t" \
  "ds_read_b64_tr_b16 a[56:57], v238 offset:8192\n\t" \
  "v_accvgpr_read_b32 v111, a127\n\t" \
  "v_mfma_f32_16x16x32_bf16 v[28:31], a[28:31], v[172:175], v[28:31]\n\t" \
  "ds_read_b128 v[152:155], v242 offset:38912\n\t" \
  "v_mul_f32 v104, v104, v222\n\t" \
  "v_mfma_f32_16x16x32_bf16 v[28:31], a[20:23], v[168:171], v[28:31]\n\t" \
  "buffer_load_dwordx4 v240, s[24:27], s96 offen offset:0 lds\n\t" \
  "v_mfma_f32_16x16x32_bf16 v[28:31], a[12:15], v[164:167], v[28:31]\n\t" \
  "ds_read_b128 v[156:159], v242 offset:39936\n\t" \
  "v_mul_f32 v105, v105, v222\n\t" \
  "v_mfma_f32_16x16x32_bf16 v[28:31], a[4:7], v[160:163], v[28:31]\n\t" \
  "s_waitcnt lgkmcnt(7)\n\t" \
  "ds_read_b64_tr_b16 a[58:59], v238 offset:12288\n\t" \
  "v_mul_f32 v106, v106, v222\n\t" \
  "v_mfma_f32_16x16x32_bf16 v[32:35], a[128:131], v[128:131], v[32:35]\n\t" \
  "ds_read_b128 v[160:163], v242 offset:40960\n\t" \
  "v_mul_f32 v107, v107, v222\n\t" \
  "v_mfma_f32_16x16x32_bf16 v[32:35], a[136:139], v[132:135], v[32:35]\n\t" \
  "ds_read_b64_tr_b16 a[60:61], v239 offset:8192\n\t" \
  "v_mul_f32 v108, v108, v222\n\t" \
  "v_mfma_f32_16x16x32_bf16 v[32:35], a[144:147], v[136:139], v[32:35]\n\t" \
  "ds_read_b128 v[164:167], v242 offset:41984\n\t" \
  "v_mul_f32 v109, v109, v222\n\t" \
  "v_mfma_f32_16x16x32_bf16 v[32:35], a[152:155], v[140:143], v[32:35]\n\t" \
  "buffer_load_dwordx4 v241, s[24:27], s96 offen offset:1024 lds\n\t" \
  "v_mfma_f32_16x16x32_bf16 v[48:51], a[156:159], v[140:143], v[48:51]\n\t" \
  "ds_read_b128 v[168:171], v242 offset:43008\n\t" \
  "v_mul_f32 v110, v110, v222\n\t" \
  "v_mfma_f32_16x16x32_bf16 v[48:51], a[148:151], v[136:139], v[48:51]\n\t" \
  "ds_read_b64_tr_b16 a[62:63], v239 offset:12288\n\t" \
  "v_mul_f32 v111, v111, v222\n\t" \
  "v_mfma_f32_16x16x32_bf16 v[48:51], a[140:143], v[132:135], v[48:51]\n\t" \
  "ds_read_b128 v[172:175], v242 offset:44032\n\t" \
  "v_cvt_pk_bf16_f32 v104, v104, v105\n\t" \
  "v_mfma_f32_16x16x32_bf16 v[48:51], a[132:135], v[128:131], v[48:51]\n\t" \
  "s_waitcnt lgkmcnt(7)\n\t" \
  "v_cvt_pk_bf16_f32 v105, v106, v107\n\t" \
  "v_cvt_pk_bf16_f32 v106, v108, v109\n\t" \
  "v_mfma_f32_16x16x32_bf16 v[36:39], a[128:131], v[144:147], v[36:39]\n\t" \
  "ds_read_b128 v[128:131], v242 offset:45056\n\t" \
  "v_cvt_pk_bf16_f32 v107, v110, v111\n\t" \
  "v_mfma_f32_16x16x32_bf16 v[36:39], a[136:139], v[148:151], v[36:39]\n\t" \
  "ds_write_b128 v213, v[104:107] offset:19456\n\t" \
  "v_mfma_f32_16x16x32_bf16 v[36:39], a[144:147], v[152:155], v[36:39]\n\t" \
  "ds_read_b128 v[132:135], v242 offset:46080\n\t" \
  "v_mfma_f32_16x16x32_bf16 v[36:39], a[152:155], v[156:159], v[36:39]\n\t" \
  "ds_read_b128 v[136:139], v242 offset:47104\n\t" \
  "v_mfma_f32_16x16x32_bf16 v[52:55], a[156:159], v[156:159], v[52:55]\n\t" \
  "ds_read_b128 v[140:143], v242 offset:48128\n\t" \
  "v_mfma_f32_16x16x32_bf16 v[52:55], a[148:151], v[152:155], v[52:55]\n\t" \
  "v_mfma_f32_16x16x32_bf16 v[52:55], a[140:143], v[148:151], v[52:55]\n\t" \
  "s_waitcnt vmcnt(12) lgkmcnt(5)\n\t" \
  "buffer_load_dwordx4 v248, s[24:27], s96 offen offset:2048 lds\n\t" \
  "v_mfma_f32_16x16x32_bf16 v[52:55], a[132:135], v[144:147], v[52:55]\n\t" \
  "s_barrier\n\t" \
  "v_mfma_f32_16x16x32_bf16 v[40:43], a[128:131], v[160:163], v[40:43]\n\t" \
  "ds_read_b128 v[144:147], v242 offset:0\n\t" \
  "v_mfma_f32_16x16x32_bf16 v[40:43], a[136:139], v[164:167], v[40:43]\n\t" \
  "buffer_load_dwordx4 v249, s[24:27], s96 offen offset:3072 lds\n\t" \
  "v_mfma_f32_16x16x32_bf16 v[40:43], a[144:147], v[168:171], v[40:43]\n\t" \
  "s_add_u32 s96, s40, 1572864\n\t" \
  "ds_read_b128 v[148:151], v242 offset:1024\n\t" \
  "s_and_b32 s96, s96, 0x1c0000\n\t" \
  "v_mfma_f32_16x16x32_bf16 v[40:43], a[152:155], v[172:175], v[40:43]\n\t" \
  "s_add_u32 s96, s96, s51\n\t" \
  "ds_read_b128 v[152:155], v242 offset:2048\n\t" \
  "s_add_u32 m0, s38, 81920\n\t" \
  "v_mfma_f32_16x16x32_bf16 v[56:59], a[156:159], v[172:175], v[56:59]\n\t" \
  "buffer_store_dwordx4 v[96:99], v242, s[16:19], s98 offen offset:2048 sc1\n\t" \
  "ds_read_b128 v[156:159], v242 offset:3072\n\t" \
  "v_mfma_f32_16x16x32_bf16 v[56:59], a[148:151], v[168:171], v[56:59]\n\t" \
  "v_mfma_f32_16x16x32_bf16 v[56:59], a[140:143], v[164:167], v[56:59]\n\t" \
  "v_mfma_f32_16x16x32_bf16 v[56:59], a[132:135], v[160:163], v[56:59]\n\t" \
  "s_waitcnt lgkmcnt(4)\n\t" \
  "buffer_load_dwordx4 v240, s[24:27], s96 offen offset:0 lds\n\t" \
  "v_mfma_f32_16x16x32_bf16 v[44:47], a[128:131], v[128:131], v[44:47]\n\t" \
  "ds_read_b128 v[160:163], v242 offset:4096\n\t" \
  "v_mfma_f32_16x16x32_bf16 v[44:47], a[136:139], v[132:135], v[44:47]\n\t" \
  "ds_read_b128 v[164:167], v242 offset:5120\n\t" \
  "v_mfma_f32_16x16x32_bf16 v[44:47], a[144:147], v[136:139], v[44:47]\n\t" \
  "buffer_load_dwordx4 v241, s[24:27], s96 offen offset:1024 lds\n\t" \
  "v_mfma_f32_16x16x32_bf16 v[44:47], a[152:155], v[140:143], v[44:47]\n\t" \
  "ds_read_b128 v[168:171], v242 offset:6144\n\t" \
  "v_mfma_f32_16x16x32_bf16 v[60:63], a[156:159], v[140:143], v[60:63]\n\t" \
  "ds_read_b128 v[172:175], v242 offset:7168\n\t" \
  "v_mfma_f32_16x16x32_bf16 v[60:63], a[148:151], v[136:139], v[60:63]\n\t" \
  "buffer_load_dwordx4 v248, s[24:27], s96 offen offset:2048 lds\n\t" \
  "v_mfma_f32_16x16x32_bf16 v[60:63], a[140:143], v[132:135], v[60:63]\n\t" \
  "v_mfma_f32_16x16x32_bf16 v[60:63], a[132:135], v[128:131], v[60:63]\n\t" \
  "s_waitcnt lgkmcnt(4)\n\t" \
  "v_mfma_f32_16x16x32_bf16 v[0:3], a[32:35], v[144:147], v[0:3]\n\t" \
  "buffer_load_dwordx4 v249, s[24:27], s96 offen offset:3072 lds\n\t" \
  "v_mfma_f32_16x16x32_bf16 v[0:3], a[40:43], v[148:151], v[0:3]\n\t" \
  "ds_read_b128 v[128:131], v242 offset:8192\n\t" \
  "v_mfma_f32_16x16x32_bf16 v[0:3], a[48:51], v[152:155], v[0:3]\n\t" \
  "ds_read_b128 v[132:135], v242 offset:9216\n\t" \
  "v_mfma_f32_16x16x32_bf16 v[0:3], a[56:59], v[156:159], v[0:3]\n\t" \
  "buffer_store_dwordx4 v[104:107], v242, s[16:19], s98 offen offset:3072 sc1\n\t" \
  "ds_read_b128 v[136:139], v242 offset:10240\n\t" \
  "ds_read_b128 v[140:143], v242 offset:11264\n\t" \
  "v_mfma_f32_16x16x32_bf16 v[16:19], a[60:63], v[156:159], v[16:19]\n\t" \
  "v_mfma_f32_16x16x32_bf16 v[16:19], a[52:55], v[152:155], v[16:19]\n\t" \
  "v_mfma_f32_16x16x32_bf16 v[16:19], a[44:47], v[148:151], v[16:19]\n\t" \
  "v_mfma_f32_16x16x32_bf16 v[16:19], a[36:39], v[144:147], v[16:19]\n\t" \
  "s_waitcnt lgkmcnt(4)\n\t" \
  "v_mfma_f32_16x16x32_bf16 v[4:7], a[32:35], v[160:163], v[4:7]\n\t" \
  "ds_read_b128 v[144:147], v242 offset:12288\n\t" \
  "v_mfma_f32_16x16x32_bf16 v[4:7], a[40:43], v[164:167], v[4:7]\n\t" \
  "ds_read_b128 v[148:151], v242 offset:13312\n\t" \
  "v_mfma_f32_16x16x32_bf16 v[4:7], a[48:51], v[168:171], v[4:7]\n\t" \
  "s_add_u32 s97, s64, 655360\n\t" \
  "ds_read_b128 v[152:155], v242 offset:14336\n\t" \
  "s_and_b32 s97, s97, 0xe0000\n\t" \
  "v_mfma_f32_16x16x32_bf16 v[4:7], a[56:59], v[172:175], v[4:7]\n\t" \
  "s_add_u32 s97, s97, s60\n\t" \
  "ds_read_b128 v[156:159], v242 offset:15360\n\t" \
  "s_add_u32 m0, s38, 131072\n\t" \
  "v_mfma_f32_16x16x32_bf16 v[20:23], a[60:63], v[172:175], v[20:23]\n\t" \
  "v_mfma_f32_16x16x32_bf16 v[20:23], a[52:55], v[168:171], v[20:23]\n\t" \
  "v_mfma_f32_16x16x32_bf16 v[20:23], a[44:47], v[164:167], v[20:23]\n\t" \
  "s_waitcnt vmcnt(11) lgkmcnt(4)\n\t" \
  "v_mfma_f32_16x16x32_bf16 v[20:23], a[36:39], v[160:163], v[20:23]\n\t" \
  "s_barrier\n\t" \
  "v_mfma_f32_16x16x32_bf16 v[8:11], a[32:35], v[128:131], v[8:11]\n\t" \
  "buffer_load_dwordx4 v242, s[16:19], s97 offen offset:0 sc1 lds\n\t" \
  "v_mfma_f32_16x16x32_bf16 v[8:11], a[40:43], v[132:135], v[8:11]\n\t" \
  "ds_read_b128 v[160:163], v242 offset:49152\n\t" \
  "v_mfma_f32_16x16x32_bf16 v[8:11], a[48:51], v[136:139], v[8:11]\n\t" \
  "ds_read_b128 v[164:167], v242 offset:50176\n\t" \
  "v_mfma_f32_16x16x32_bf16 v[8:11], a[56:59], v[140:143], v[8:11]\n\t" \
  "buffer_load_dwordx4 v242, s[16:19], s97 offen offset:1024 sc1 lds\n\t" \
  "v_mfma_f32_16x16x32_bf16 v[24:27], a[60:63], v[140:143], v[24:27]\n\t" \
  "ds_read_b128 v[168:171], v242 offset:51200\n\t" \
  "v_mfma_f32_16x16x32_bf16 v[24:27], a[52:55], v[136:139], v[24:27]\n\t" \
  "ds_read_b64_tr_b16 a[64:65], v238 offset:16384\n\t" \
  "v_mfma_f32_16x16x32_bf16 v[24:27], a[44:47], v[132:135], v[24:27]\n\t" \
  "buffer_load_dwordx4 v242, s[16:19], s97 offen offset:2048 sc1 lds\n\t" \
  "v_mfma_f32_16x16x32_bf16 v[24:27], a[36:39], v[128:131], v[24:27]\n\t" \
  "s_waitcnt lgkmcnt(4)\n\t" \
  "ds_read_b64_tr_b16 a[66:67], v238 offset:20480\n\t" \
  "ds_read_b128 v[172:175], v242 offset:52224\n\t" \
  "v_mfma_f32_16x16x32_bf16 v[12:15], a[32:35], v[144:147], v[12:15]\n\t" \
  "ds_read_b128 v[128:131], v242 offset:53248\n\t" \
  "v_mfma_f32_16x16x32_bf16 v[12:15], a[40:43], v[148:151], v[12:15]\n\t" \
  "ds_read_b64_tr_b16 a[68:69], v239 offset:16384\n\t" \
  "v_mfma_f32_16x16x32_bf16 v[12:15], a[48:51], v[152:155], v[12:15]\n\t" \
  "buffer_load_dwordx4 v242, s[16:19], s97 offen offset:3072 sc1 lds\n\t" \
  "v_mfma_f32_16x16x32_bf16 v[12:15], a[56:59], v[156:159], v[12:15]\n\t" \
  "s_add_u32 s96, s40, 1572864\n\t" \
  "s_and_b32 s96, s96, 0x1c0000\n\t" \
  "s_add_u32 s96, s96, s77\n\t" \
  "v_mfma_f32_16x16x32_bf16 v[28:31], a[60:63], v[156:159], v[28:31]\n\t" \
  "s_add_u32 m0, s38, 32768\n\t" \
  "ds_read_b128 v[132:135], v242 offset:54272\n\t" \
  "v_mfma_f32_16x16x32_bf16 v[28:31], a[52:55], v[152:155], v[28:31]\n\t" \
  "buffer_load_dwordx4 v240, s[24:27], s96 offen offset:0 lds\n\t" \
  "v_mfma_f32_16x16x32_bf16 v[28:31], a[44:47], v[148:151], v[28:31]\n\t" \
  "ds_read_b128 v[136:139], v242 offset:55296\n\t" \
  "v_mfma_f32_16x16x32_bf16 v[28:31], a[36:39], v[144:147], v[28:31]\n\t" \
  "s_waitcnt lgkmcnt(4)\n\t" \
  "ds_read_b128 v[140:143], v242 offset:56320\n\t" \
  "v_mfma_f32_16x16x32_bf16 v[32:35], a[160:163], v[160:163], v[32:35]\n\t" \
  "ds_read_b64_tr_b16 a[70:71], v239 offset:20480\n\t" \
  "v_mfma_f32_16x16x32_bf16 v[32:35], a[168:171], v[164:167], v[32:35]\n\t" \
  "buffer_load_dwordx4 v241, s[24:27], s96 offen offset:1024 lds\n\t" \
  "v_mfma_f32_16x16x32_bf16 v[32:35], a[176:179], v[168:171], v[32:35]\n\t" \
  "ds_read_b128 v[144:147], v242 offset:57344\n\t" \
  "v_mfma_f32_16x16x32_bf16 v[32:35], a[184:187], v[172:175], v[32:35]\n\t" \
  "ds_read_b64_tr_b16 a[72:73], v238 offset:24576\n\t" \
  "v_mfma_f32_16x16x32_bf16 v[48:51], a[188:191], v[172:175], v[48:51]\n\t" \
  "ds_read_b128 v[148:151], v242 offset:58368\n\t" \
  "v_mfma_f32_16x16x32_bf16 v[48:51], a[180:183], v[168:171], v[48:51]\n\t" \
  "ds_read_b128 v[152:155], v242 offset:59392\n\t" \
  "v_mfma_f32_16x16x32_bf16 v[48:51], a[172:175], v[164:167], v[48:51]\n\t" \
  "ds_read_b128 v[156:159], v242 offset:60416\n\t" \
  "ds_read_b64_tr_b16 a[74:75], v238 offset:28672\n\t" \
  "v_mfma_f32_16x16x32_bf16 v[48:51], a[164:167], v[160:163], v[48:51]\n\t" \
  "s_waitcnt lgkmcnt(7)\n\t" \
  "ds_read_b64_tr_b16 a[76:77], v239 offset:24576\n\t" \
  "v_mfma_f32_16x16x32_bf16 v[36:39], a[160:163], v[128:131], v[36:39]\n\t" \
  "ds_read_b64_tr_b16 a[78:79], v239 offset:28672\n\t" \
  "v_mfma_f32_16x16x32_bf16 v[36:39], a[168:171], v[132:135], v[36:39]\n\t" \
  "ds_read_b128 v[160:163], v242 offset:61440\n\t" \
  "v_mfma_f32_16x16x32_bf16 v[36:39], a[176:179], v[136:139], v[36:39]\n\t" \
  "ds_read_b128 v[164:167], v242 offset:62464\n\t" \
  "v_mfma_f32_16x16x32_bf16 v[36:39], a[184:187], v[140:143], v[36:39]\n\t" \
  "ds_read_b128 v[168:171], v242 offset:63488\n\t" \
  "v_mfma_f32_16x16x32_bf16 v[52:55], a[188:191], v[140:143], v[52:55]\n\t" \
  "ds_read_b128 v[172:175], v242 offset:64512\n\t" \
  "v_mfma_f32_16x16x32_bf16 v[52:55], a[180:183], v[136:139], v[52:55]\n\t" \
  "buffer_load_dwordx4 v248, s[24:27], s96 offen offset:2048 lds\n\t" \
  "v_mfma_f32_16x16x32_bf16 v[52:55], a[172:175], v[132:135], v[52:55]\n\t" \
  "s_waitcnt vmcnt(13) lgkmcnt(4)\n\t" \
  "v_mfma_f32_16x16x32_bf16 v[52:55], a[164:167], v[128:131], v[52:55]\n\t" \
  "s_barrier\n\t" \
  "v_mfma_f32_16x16x32_bf16 v[40:43], a[160:163], v[144:147], v[40:43]\n\t" \
  "buffer_load_dwordx4 v249, s[24:27], s96 offen offset:3072 lds\n\t" \
  "v_mfma_f32_16x16x32_bf16 v[40:43], a[168:171], v[148:151], v[40:43]\n\t" \
  "s_add_u32 s97, s64, 393216\n\t" \
  "ds_read_b128 v[128:131], v242 offset:16384\n\t" \
  "s_and_b32 s97, s97, 0xe0000\n\t" \
  "v_mfma_f32_16x16x32_bf16 v[40:43], a[176:179], v[152:155], v[40:43]\n\t" \
  "s_add_u32 s97, s97, s61\n\t" \
  "ds_read_b128 v[132:135], v242 offset:17408\n\t" \
  "s_add_u32 m0, s38, 0\n\t" \
  "v_mfma_f32_16x16x32_bf16 v[40:43], a[184:187], v[156:159], v[40:43]\n\t" \
  "ds_read_b64_tr_b16 a[80:81], v236 offset:0\n\t" \
  "v_mfma_f32_16x16x32_bf16 v[56:59], a[188:191], v[156:159], v[56:59]\n\t" \
  "buffer_load_dwordx4 v242, s[16:19], s97 offen offset:0 sc1 lds\n\t" \
  "v_mfma_f32_16x16x32_bf16 v[56:59], a[180:183], v[152:155], v[56:59]\n\t" \
  "ds_read_b64_tr_b16 a[82:83], v236 offset:4096\n\t" \
  "ds_read_b128 v[136:139], v242 offset:18432\n\t" \
  "v_mfma_f32_16x16x32_bf16 v[56:59], a[172:175], v[148:151], v[56:59]\n\t" \
  "ds_read_b64_tr_b16 a[84:85], v237 offset:0\n\t" \
  "ds_read_b128 v[140:143], v242 offset:19456\n\t" \
  "v_mfma_f32_16x16x32_bf16 v[56:59], a[164:167], v[144:147], v[56:59]\n\t" \
  "s_waitcnt lgkmcnt(7)\n\t" \
  "buffer_load_dwordx4 v242, s[16:19], s97 offen offset:1024 sc1 lds\n\t" \
  "v_mfma_f32_16x16x32_bf16 v[44:47], a[160:163], v[160:163], v[44:47]\n\t" \
  "ds_read_b128 v[144:147], v242 offset:20480\n\t" \
  "v_mfma_f32_16x16x32_bf16 v[44:47], a[168:171], v[164:167], v[44:47]\n\t" \
  "ds_read_b128 v[148:151], v242 offset:21504\n\t" \
  "v_mfma_f32_16x16x32_bf16 v[44:47], a[176:179], v[168:171], v[44:47]\n\t" \
  "ds_read_b64_tr_b16 a[86:87], v237 offset:4096\n\t" \
  "v_mfma_f32_16x16x32_bf16 v[44:47], a[184:187], v[172:175], v[44:47]\n\t" \
  "ds_read_b128 v[152:155], v242 offset:22528\n\t" \
  "v_mfma_f32_16x16x32_bf16 v[60:63], a[188:191], v[172:175], v[60:63]\n\t" \
  "buffer_load_dwordx4 v242, s[16:19], s97 offen offset:2048 sc1 lds\n\t" \
  "v_mfma_f32_16x16x32_bf16 v[60:63], a[180:183], v[168:171], v[60:63]\n\t" \
  "ds_read_b64_tr_b16 a[88:89], v236 offset:8192\n\t" \
  "v_mfma_f32_16x16x32_bf16 v[60:63], a[172:175], v[164:167], v[60:63]\n\t" \
  "ds_read_b128 v[156:159], v242 offset:23552\n\t" \
  "v_mfma_f32_16x16x32_bf16 v[60:63], a[164:167], v[160:163], v[60:63]\n\t" \
  "s_waitcnt lgkmcnt(6)\n\t" \
  "buffer_load_dwordx4 v242, s[16:19], s97 offen offset:3072 sc1 lds\n\t" \
  "v_mfma_f32_16x16x32_bf16 v[96:99], a[192:195], v[128:131], 0\n\t" \
  "s_add_u32 s96, s40, 1835008\n\t" \
  "ds_read_b128 v[160:163], v242 offset:24576\n\t" \
  "s_and_b32 s96, s96, 0x1c0000\n\t" \
  "v_mfma_f32_16x16x32_bf16 v[96:99], a[200:203], v[132:135], v[96:99]\n\t" \
  "s_add_u32 s96, s96, s51\n\t" \
  "ds_read_b64_tr_b16 a[90:91], v236 offset:12288\n\t" \
  "s_add_u32 m0, s38, 147456\n\t" \
  "v_mfma_f32_16x16x32_bf16 v[96:99], a[208:211], v[136:139], v[96:99]\n\t" \
  "s_waitcnt vmcnt(12)\n\t" \
  "buffer_store_dword v243, v254, s[16:19], s49 offen offset:64 sc1\n\t" \
  "ds_read_b128 v[164:167], v242 offset:25600\n\t" \
  "v_mfma_f32_16x16x32_bf16 v[96:99], a[216:219], v[140:143], v[96:99]\n\t" \
  "ds_read_b64_tr_b16 a[92:93], v237 offset:8192\n\t" \
  "v_mfma_f32_16x16x32_bf16 v[112:115], a[220:223], v[140:143], 0\n\t" \
  "buffer_load_dwordx4 v240, s[24:27], s96 offen offset:0 lds\n\t" \
  "v_mfma_f32_16x16x32_bf16 v[112:115], a[212:215], v[136:139], v[112:115]\n\t" \
  "ds_read_b128 v[168:171], v242 offset:26624\n\t" \
  "v_mfma_f32_16x16x32_bf16 v[112:115], a[204:207], v[132:135], v[112:115]\n\t" \
  "ds_read_b128 v[172:175], v242 offset:27648\n\t" \
  "v_mfma_f32_16x16x32_bf16 v[112:115], a[196:199], v[128:131], v[112:115]\n\t" \
  "s_waitcnt lgkmcnt(6)\n\t" \
  "buffer_load_dwordx4 v241, s[24:27], s96 offen offset:1024 lds\n\t" \
  "v_mfma_f32_16x16x32_bf16 v[100:103], a[192:195], v[144:147], 0\n\t" \
  ".Lhs12:\n\ts_cbranch_execz .Lhs12n\n\t" \
  "ds_read_b64_tr_b16 a[94:95], v237 offset:12288\n\t" \
  "v_mfma_f32_16x16x32_bf16 v[100:103], a[200:203], v[148:151], v[100:103]\n\t" \
  "ds_read_b128 v[128:131], v242 offset:28672\n\t" \
  "v_mfma_f32_16x16x32_bf16 v[100:103], a[208:211], v[152:155], v[100:103]\n\t" \
  "ds_read_b128 v[132:135], v242 offset:29696\n\t" \
  "v_mfma_f32_16x16x32_bf16 v[100:103], a[216:219], v[156:159], v[100:103]\n\t" \
  "ds_read_b128 v[136:139], v242 offset:30720\n\t" \
  "v_mfma_f32_16x16x32_bf16 v[116:119], a[220:223], v[156:159], 0\n\t" \
  "ds_read_b128 v[140:143], v242 offset:31744\n\t" \
  "v_mfma_f32_16x16x32_bf16 v[116:119], a[212:215], v[152:155], v[116:119]\n\t" \
  "v_mfma_f32_16x16x32_bf16 v[116:119], a[204:207], v[148:151], v[116:119]\n\t" \
  "s_waitcnt vmcnt(11) lgkmcnt(4)\n\t" \
  "v_mfma_f32_16x16x32_bf16 v[116:119], a[196:199], v[144:147], v[116:119]\n\t" \
  "s_barrier\n\t" \
  "v_mfma_f32_16x16x32_bf16 v[104:107], a[192:195], v[160:163], 0\n\t" \
  "ds_read_b128 v[144:147], v232 offset:0\n\t" \
  "v_mfma_f32_16x16x32_bf16 v[104:107], a[200:203], v[164:167], v[104:107]\n\t" \
  "ds_read_b64_tr_b16 a[96:97], v236 offset:16384\n\t" \
  "v_mfma_f32_16x16x32_bf16 v[104:107], a[208:211], v[168:171], v[104:107]\n\t" \
  "ds_read_b128 v[148:151], v232 offset:1024\n\t" \
  "v_mfma_f32_16x16x32_bf16 v[104:107], a[216:219], v[172:175], v[104:107]\n\t" \
  "ds_read_b128 v[152:155], v232 offset:2048\n\t" \
  "v_mfma_f32_16x16x32_bf16 v[120:123], a[220:223], v[172:175], 0\n\t" \
  "ds_read_b64_tr_b16 a[98:99], v236 offset:20480\n\t" \
  "v_mfma_f32_16x16x32_bf16 v[120:123], a[212:215], v[168:171], v[120:123]\n\t" \
  "ds_read_b128 v[156:159], v232 offset:3072\n\t" \
  "v_mfma_f32_16x16x32_bf16 v[120:123], a[204:207], v[164:167], v[120:123]\n\t" \
  "buffer_load_dwordx4 v248, s[24:27], s96 offen offset:2048 lds\n\t" \
  "v_mfma_f32_16x16x32_bf16 v[120:123], a[196:199], v[160:163], v[120:123]\n\t" \
  "s_waitcnt lgkmcnt(6)\n\t" \
  "ds_read_b64_tr_b16 a[100:101], v237 offset:16384\n\t" \
  "v_mfma_f32_16x16x32_bf16 v[108:111], a[192:195], v[128:131], 0\n\t" \
  "ds_read_b128 v[160:163], v232 offset:4096\n\t" \
  "v_mfma_f32_16x16x32_bf16 v[108:111], a[200:203], v[132:135], v[108:111]\n\t" \
  "ds_read_b64_tr_b16 a[102:103], v237 offset:20480\n\t" \
  "v_mfma_f32_16x16x32_bf16 v[108:111], a[208:211], v[136:139], v[108:111]\n\t" \
  "buffer_load_dwordx4 v249, s[24:27], s96 offen offset:3072 lds\n\t" \
  "v_mfma_f32_16x16x32_bf16 v[108:111], a[216:219], v[140:143], v[108:111]\n\t" \
  "s_add_u32 s97, s64, 786432\n\t" \
  "s_and_b32 s97, s97, 0xe0000\n\t" \
  "s_add_u32 s97, s97, s60\n\t" \
  "v_mfma_f32_16x16x32_bf16 v[124:127], a[220:223], v[140:143], 0\n\t" \
  "s_add_u32 m0, s38, 65536\n\t" \
  "ds_read_b64_tr_b16 a[104:105], v236 offset:24576\n\t" \
  "ds_read_b128 v[164:167], v232 offset:5120\n\t" \
  "v_mfma_f32_16x16x32_bf16 v[124:127], a[212:215], v[136:139], v[124:127]\n\t" \
  "buffer_load_dwordx4 v242, s[16:19], s97 offen offset:0 sc1 lds\n\t" \
  "v_mfma_f32_16x16x32_bf16 v[124:127], a[204:207], v[132:135], v[124:127]\n\t" \
  "ds_read_b64_tr_b16 a[106:107], v236 offset:28672\n\t" \
  "ds_read_b128 v[168:171], v232 offset:6144\n\t" \
  "v_mfma_f32_16x16x32_bf16 v[124:127], a[196:199], v[128:131], v[124:127]\n\t" \
  "s_waitcnt lgkmcnt(7)\n\t" \
  "ds_read_b128 v[172:175], v232 offset:7168\n\t" \
  "v_mfma_f32_16x16x32_bf16 v[0:3], a[64:67], v[144:147], v[0:3]\n\t" \
  "ds_read_b128 v[128:131], v232 offset:8192\n\t" \
  "v_lshrrev_b32 v247, 1, v242\n\t" \
  "v_mfma_f32_16x16x32_bf16 v[0:3], a[72:75], v[148:151], v[0:3]\n\t" \
  "buffer_load_dwordx4 v242, s[16:19], s97 offen offset:1024 sc1 lds\n\t" \
  "v_mfma_f32_16x16x32_bf16 v[0:3], a[80:83], v[152:155], v[0:3]\n\t" \
  "ds_read_b128 v[132:135], v232 offset:9216\n\t" \
  "v_add_u32 v247, s38, v247\n\t" \
  "v_mfma_f32_16x16x32_bf16 v[0:3], a[88:91], v[156:159], v[0:3]\n\t" \
  "ds_read_b64_tr_b16 a[108:109], v237 offset:24576\n\t" \
  "v_mfma_f32_16x16x32_bf16 v[16:19], a[92:95], v[156:159], v[16:19]\n\t" \
  "buffer_load_dwordx4 v242, s[16:19], s97 offen offset:2048 sc1 lds\n\t" \
  "v_mfma_f32_16x16x32_bf16 v[16:19], a[84:87], v[152:155], v[16:19]\n\t" \
  "ds_read_b128 v[136:139], v232 offset:10240\n\t" \
  "v_mfma_f32_16x16x32_bf16 v[16:19], a[76:79], v[148:151], v[16:19]\n\t" \
  "ds_read_b128 v[140:143], v232 offset:11264\n\t" \
  "v_mfma_f32_16x16x32_bf16 v[16:19], a[68:71], v[144:147], v[16:19]\n\t" \
  "s_waitcnt lgkmcnt(5)\n\t" \
  "buffer_load_dwordx4 v242, s[16:19], s97 offen offset:3072 sc1 lds\n\t" \
  "v_mfma_f32_16x16x32_bf16 v[4:7], a[64:67], v[160:163], v[4:7]\n\t" \
  "s_add_u32 s96, s40, 1835008\n\t" \
  "s_and_b32 s96, s96, 0x1c0000\n\t" \
  "s_add_u32 s96, s96, s77\n\t" \
  "v_mfma_f32_16x16x32_bf16 v[4:7], a[72:75], v[164:167], v[4:7]\n\t" \
  "s_add_u32 m0, s38, 49152\n\t" \
  "ds_read_b128 v[144:147], v232 offset:12288\n\t" \
  "v_mfma_f32_16x16x32_bf16 v[4:7], a[80:83], v[168:171], v[4:7]\n\t" \
  "buffer_load_dwordx4 v240, s[24:27], s96 offen offset:0 lds\n\t" \
  "v_mfma_f32_16x16x32_bf16 v[4:7], a[88:91], v[172:175], v[4:7]\n\t" \
  "ds_read_b128 v[148:151], v232 offset:13312\n\t" \
  "ds_read_b64_tr_b16 a[110:111], v237 offset:28672\n\t" \
  "v_mfma_f32_16x16x32_bf16 v[20:23], a[92:95], v[172:175], v[20:23]\n\t" \
  "ds_read_b128 v[152:155], v232 offset:14336\n\t" \
  "v_mfma_f32_16x16x32_bf16 v[20:23], a[84:87], v[168:171], v[20:23]\n\t" \
  "buffer_load_dwordx4 v241, s[24:27], s96 offen offset:1024 lds\n\t" \
  "v_mfma_f32_16x16x32_bf16 v[20:23], a[76:79], v[164:167], v[20:23]\n\t" \
  "s_waitcnt vmcnt(11) lgkmcnt(1)\n\t" \
  "ds_read_b128 v[156:159], v232 offset:15360\n\t" \
  "v_mfma_f32_16x16x32_bf16 v[20:23], a[68:71], v[160:163], v[20:23]\n\t" \
  "s_barrier\n\t" \
  "v_mfma_f32_16x16x32_bf16 v[8:11], a[64:67], v[128:131], v[8:11]\n\t" \
  "buffer_load_dwordx4 v248, s[24:27], s96 offen offset:2048 lds\n\t" \
  "v_mfma_f32_16x16x32_bf16 v[8:11], a[72:75], v[132:135], v[8:11]\n\t" \
  "ds_read_b128 v[160:163], v242 offset:0\n\t" \
  "v_mfma_f32_16x16x32_bf16 v[8:11], a[80:83], v[136:139], v[8:11]\n\t" \
  "ds_read_b128 v[164:167], v242 offset:1024\n\t" \
  "v_mfma_f32_16x16x32_bf16 v[8:11], a[88:91], v[140:143], v[8:11]\n\t" \
  "ds_read_b64_tr_b16 a[112:113], v234 offset:32768\n\t" \
  "v_mfma_f32_16x16x32_bf16 v[24:27], a[92:95], v[140:143], v[24:27]\n\t" \
  "ds_read_b128 v[168:171], v242 offset:2048\n\t" \
  "v_mfma_f32_16x16x32_bf16 v[24:27], a[84:87], v[136:139], v[24:27]\n\t" \
  "buffer_load_dwordx4 v249, s[24:27], s96 offen offset:3072 lds\n\t" \
  "v_mfma_f32_16x16x32_bf16 v[24:27], a[76:79], v[132:135], v[24:27]\n\t" \
  "s_add_u32 s97, s64, 524288\n\t" \
  "s_and_b32 s97, s97, 0xe0000\n\t" \
  "s_add_u32 s97, s97, s61\n\t" \
  "v_mfma_f32_16x16x32_bf16 v[24:27], a[68:71], v[128:131], v[24:27]\n\t" \
  "s_waitcnt lgkmcnt(4)\n\t" \
  "ds_read_b128 v[172:175], v242 offset:3072\n\t" \
  "s_add_u32 m0, s38, 81920\n\t" \
  "v_mfma_f32_16x16x32_bf16 v[12:15], a[64:67], v[144:147], v[12:15]\n\t" \
  "buffer_load_dwordx4 v242, s[16:19], s97 offen offset:0 sc1 lds\n\t" \
  "v_mfma_f32_16x16x32_bf16 v[12:15], a[72:75], v[148:151], v[12:15]\n\t" \
  "ds_read_b128 v[128:131], v242 offset:4096\n\t" \
  "v_mfma_f32_16x16x32_bf16 v[12:15], a[80:83], v[152:155], v[12:15]\n\t" \
  "ds_read_b64_tr_b16 a[114:115], v234 offset:36864\n\t" \
  "v_mfma_f32_16x16x32_bf16 v[12:15], a[88:91], v[156:159], v[12:15]\n\t" \
  "ds_read_b128 v[132:135], v242 offset:5120\n\t" \
  "v_mfma_f32_16x16x32_bf16 v[28:31], a[92:95], v[156:159], v[28:31]\n\t" \
  "ds_read_b64_tr_b16 a[116:117], v235 offset:32768\n\t" \
  "v_mfma_f32_16x16x32_bf16 v[28:31], a[84:87], v[152:155], v[28:31]\n\t" \
  "ds_read_b128 v[136:139], v242 offset:6144\n\t" \
  "v_mfma_f32_16x16x32_bf16 v[28:31], a[76:79], v[148:151], v[28:31]\n\t" \
  "ds_read_b128 v[140:143], v242 offset:7168\n\t" \
  "v_mfma_f32_16x16x32_bf16 v[28:31], a[68:71], v[144:147], v[28:31]\n\t" \
  "s_waitcnt lgkmcnt(6)\n\t" \
  "ds_read_b64_tr_b16 a[118:119], v235 offset:36864\n\t" \
  STP("s_memrealtime s[94:95]\n\t") \
  STP("s_memtime s[92:93]\n\t") \
  "v_mfma_f32_16x16x32_bf16 v[32:35], a[0:3], v[160:163], v[32:35]\n\t" \
  "ds_read_b128 v[144:147], v242 offset:8192\n\t" \
  "v_mfma_f32_16x16x32_bf16 v[32:35], a[8:11], v[164:167], v[32:35]\n\t" \
  "ds_read_b64_tr_b16 a[120:121], v234 offset:40960\n\t" \
  "v_mfma_f32_16x16x32_bf16 v[32:35], a[16:19], v[168:171], v[32:35]\n\t" \
  "ds_read_b128 v[148:151], v242 offset:9216\n\t" \
  "v_mfma_f32_16x16x32_bf16 v[32:35], a[24:27], v[172:175], v[32:35]\n\t" \
  "ds_read_b64_tr_b16 a[122:123], v234 offset:45056\n\t" \
  "v_mfma_f32_16x16x32_bf16 v[48:51], a[28:31], v[172:175], v[48:51]\n\t" \
  "ds_read_b128 v[152:155], v242 offset:10240\n\t" \
  "v_mfma_f32_16x16x32_bf16 v[48:51], a[20:23], v[168:171], v[48:51]\n\t" \
  "ds_read_b64_tr_b16 a[124:125], v235 offset:40960\n\t" \
  "v_mfma_f32_16x16x32_bf16 v[48:51], a[12:15], v[164:167], v[48:51]\n\t" \
  "buffer_load_dwordx4 v242, s[16:19], s97 offen offset:1024 sc1 lds\n\t" \
  "v_mfma_f32_16x16x32_bf16 v[48:51], a[4:7], v[160:163], v[48:51]\n\t" \
  "s_waitcnt lgkmcnt(7)\n\t" \
  "ds_read_b128 v[156:159], v242 offset:11264\n\t" \
  "v_mfma_f32_16x16x32_bf16 v[36:39], a[0:3], v[128:131], v[36:39]\n\t" \
  "ds_read_b64_tr_b16 a[126:127], v235 offset:45056\n\t" \
  "v_mfma_f32_16x16x32_bf16 v[36:39], a[8:11], v[132:135], v[36:39]\n\t" \
  "ds_read_b128 v[160:163], v242 offset:12288\n\t" \
  "v_mfma_f32_16x16x32_bf16 v[36:39], a[16:19], v[136:139], v[36:39]\n\t" \
  "buffer_load_dwordx4 v242, s[16:19], s97 offen offset:2048 sc1 lds\n\t" \
  "v_mfma_f32_16x16x32_bf16 v[36:39], a[24:27], v[140:143], v[36:39]\n\t" \
  "ds_read_b128 v[164:167], v242 offset:13312\n\t" \
  "v_mfma_f32_16x16x32_bf16 v[52:55], a[28:31], v[140:143], v[52:55]\n\t" \
  STP("v_writelane_b32 v255, s94, 32\n\t") \
  STP("v_writelane_b32 v255, s92, 33\n\t") \
  "ds_read_b128 v[168:171], v242 offset:14336\n\t" \
  "v_mfma_f32_16x16x32_bf16 v[52:55], a[20:23], v[136:139], v[52:55]\n\t" \
  "buffer_load_dwordx4 v242, s[16:19], s97 offen offset:3072 sc1 lds\n\t" \
  "v_mfma_f32_16x16x32_bf16 v[52:55], a[12:15], v[132:135], v[52:55]\n\t" \
  "s_waitcnt vmcnt(8) lgkmcnt(3)\n\t" \
  "s_add_u32 s97, s64, 917504\n\t" \
  "s_and_b32 s97, s97, 0xe0000\n\t" \
  "v_mfma_f32_16x16x32_bf16 v[52:55], a[4:7], v[128:131], v[52:55]\n\t" \
  "s_barrier\n\t" \
  "v_mfma_f32_16x16x32_bf16 v[40:43], a[0:3], v[144:147], v[40:43]\n\t" \
  "s_add_u32 s97, s97, s60\n\t" \
  "ds_read_b128 v[172:175], v242 offset:15360\n\t" \
  "s_add_u32 m0, s38, 131072\n\t" \
  "v_mfma_f32_16x16x32_bf16 v[40:43], a[8:11], v[148:151], v[40:43]\n\t" \
  "buffer_load_dwordx4 v242, s[16:19], s97 offen offset:0 sc1 lds\n\t" \
  "v_mfma_f32_16x16x32_bf16 v[40:43], a[16:19], v[152:155], v[40:43]\n\t" \
  "ds_read_b128 v[128:131], v230 offset:0\n\t" \
  "v_mfma_f32_16x16x32_bf16 v[40:43], a[24:27], v[156:159], v[40:43]\n\t" \
  "buffer_load_dwordx4 v242, s[16:19], s97 offen offset:1024 sc1 lds\n\t" \
  "v_mfma_f32_16x16x32_bf16 v[56:59], a[28:31], v[156:159], v[56:59]\n\t" \
  "ds_read_b128 v[132:135], v230 offset:1024\n\t" \
  "v_mfma_f32_16x16x32_bf16 v[56:59], a[20:23], v[152:155], v[56:59]\n\t" \
  "ds_read_b128 v[136:139], v230 offset:2048\n\t" \
  "v_mfma_f32_16x16x32_bf16 v[56:59], a[12:15], v[148:151], v[56:59]\n\t" \
  "buffer_load_dwordx4 v242, s[16:19], s97 offen offset:2048 sc1 lds\n\t" \
  "v_mfma_f32_16x16x32_bf16 v[56:59], a[4:7], v[144:147], v[56:59]\n\t" \
  "s_waitcnt lgkmcnt(3)\n\t" \
  "ds_read_b128 v[140:143], v230 offset:3072\n\t" \
  "v_mfma_f32_16x16x32_bf16 v[44:47], a[0:3], v[160:163], v[44:47]\n\t" \
  "ds_read_b128 v[144:147], v230 offset:4096\n\t" \
  "v_mfma_f32_16x16x32_bf16 v[44:47], a[8:11], v[164:167], v[44:47]\n\t" \
  "ds_read_b128 v[148:151], v230 offset:5120\n\t" \
  "v_mfma_f32_16x16x32_bf16 v[44:47], a[16:19], v[168:171], v[44:47]\n\t" \
  "ds_read_b128 v[152:155], v230 offset:6144\n\t" \
  "v_mfma_f32_16x16x32_bf16 v[44:47], a[24:27], v[172:175], v[44:47]\n\t" \
  "ds_read_b128 v[156:159], v230 offset:7168\n\t" \
  "v_mfma_f32_16x16x32_bf16 v[60:63], a[28:31], v[172:175], v[60:63]\n\t" \
  "buffer_load_dwordx4 v242, s[16:19], s97 offen offset:3072 sc1 lds\n\t" \
  "v_mfma_f32_16x16x32_bf16 v[60:63], a[20:23], v[168:171], v[60:63]\n\t" \
  "s_add_u32 s97, s64, 655360\n\t" \
  "s_and_b32 s97, s97, 0xe0000\n\t" \
  "s_add_u32 s97, s97, s61\n\t" \
  "v_mfma_f32_16x16x32_bf16 v[60:63], a[12:15], v[164:167], v[60:63]\n\t" \
  "s_add_u32 m0, s38, 16384\n\t" \
  "v_mfma_f32_16x16x32_bf16 v[60:63], a[4:7], v[160:163], v[60:63]\n\t" \
  "s_waitcnt lgkmcnt(4)\n\t" \
  "buffer_load_dword v246, v253, s[16:19], s65 offen sc1\n\t" \
  "v_mfma_f32_16x16x32_bf16 v[0:3], a[96:99], v[128:131], v[0:3]\n\t" \
  "ds_read_b128 v[160:163], v230 offset:8192\n\t" \
  "v_mfma_f32_16x16x32_bf16 v[0:3], a[104:107], v[132:135], v[0:3]\n\t" \
  "ds_read_b128 v[164:167], v230 offset:9216\n\t" \
  "v_mfma_f32_16x16x32_bf16 v[0:3], a[112:115], v[136:139], v[0:3]\n\t" \
  "ds_read_b128 v[168:171], v230 offset:10240\n\t" \
  "v_mfma_f32_16x16x32_bf16 v[0:3], a[120:123], v[140:143], v[0:3]\n\t" \
  "buffer_load_dwordx4 v242, s[16:19], s97 offen offset:0 sc1 lds\n\t" \
  "v_mfma_f32_16x16x32_bf16 v[16:19], a[124:127], v[140:143], v[16:19]\n\t" \
  "ds_read_b128 v[172:175], v230 offset:11264\n\t" \
  "v_mfma_f32_16x16x32_bf16 v[16:19], a[116:119], v[136:139], v[16:19]\n\t" \
  "v_mfma_f32_16x16x32_bf16 v[16:19], a[108:111], v[132:135], v[16:19]\n\t" \
  "v_mfma_f32_16x16x32_bf16 v[16:19], a[100:103], v[128:131], v[16:19]\n\t" \
  "s_waitcnt lgkmcnt(4)\n\t" \
  "buffer_load_dwordx4 v242, s[16:19], s97 offen offset:1024 sc1 lds\n\t" \
  "v_mfma_f32_16x16x32_bf16 v[4:7], a[96:99], v[144:147], v[4:7]\n\t" \
  "ds_read_b128 v[128:131], v230 offset:12288\n\t" \
  "v_mfma_f32_16x16x32_bf16 v[4:7], a[104:107], v[148:151], v[4:7]\n\t" \
  "ds_read_b128 v[132:135], v230 offset:13312\n\t" \
  "v_mfma_f32_16x16x32_bf16 v[4:7], a[112:115], v[152:155], v[4:7]\n\t" \
  "buffer_load_dwordx4 v242, s[16:19], s97 offen offset:2048 sc1 lds\n\t" \
  "v_mfma_f32_16x16x32_bf16 v[4:7], a[120:123], v[156:159], v[4:7]\n\t" \
  "ds_read_b128 v[136:139], v230 offset:14336\n\t" \
  "v_mfma_f32_16x16x32_bf16 v[20:23], a[124:127], v[156:159], v[20:23]\n\t" \
  "ds_read_b128 v[140:143], v230 offset:15360\n\t" \
  "v_mfma_f32_16x16x32_bf16 v[20:23], a[116:119], v[152:155], v[20:23]\n\t" \
  "buffer_load_dwordx4 v242, s[16:19], s97 offen offset:3072 sc1 lds\n\t" \
  "v_mfma_f32_16x16x32_bf16 v[20:23], a[108:111], v[148:151], v[20:23]\n\t" \
  "s_waitcnt vmcnt(9) lgkmcnt(4)\n\t" \
  "s_add_u32 s97, s64, 786432\n\t" \
  "s_and_b32 s97, s97, 0xe0000\n\t" \
  "v_mfma_f32_16x16x32_bf16 v[20:23], a[100:103], v[144:147], v[20:23]\n\t" \
  "s_barrier\n\t" \
  "v_mfma_f32_16x16x32_bf16 v[8:11], a[96:99], v[160:163], v[8:11]\n\t" \
  "s_add_u32 s97, s97, s61\n\t" \
  "ds_read_b128 v[144:147], v230 offset:16384\n\t" \
  "ds_read_b128 v[148:151], v230 offset:17408\n\t" \
  "s_add_u32 m0, s38, 0\n\t" \
  "v_mfma_f32_16x16x32_bf16 v[8:11], a[104:107], v[164:167], v[8:11]\n\t" \
  "buffer_load_dwordx4 v242, s[16:19], s97 offen offset:0 sc1 lds\n\t" \
  "ds_read_b64_tr_b16 a[192:193], v238 offset:16384\n\t" \
  "v_mfma_f32_16x16x32_bf16 v[8:11], a[112:115], v[168:171], v[8:11]\n\t" \
  "ds_read_b64_tr_b16 a[194:195], v238 offset:20480\n\t" \
  "ds_read_b128 v[152:155], v230 offset:18432\n\t" \
  "v_mfma_f32_16x16x32_bf16 v[8:11], a[120:123], v[172:175], v[8:11]\n\t" \
  "buffer_load_dwordx4 v242, s[16:19], s97 offen offset:1024 sc1 lds\n\t" \
  "v_mfma_f32_16x16x32_bf16 v[24:27], a[124:127], v[172:175], v[24:27]\n\t" \
  "ds_read_b128 v[156:159], v230 offset:19456\n\t" \
  "v_mfma_f32_16x16x32_bf16 v[24:27], a[116:119], v[168:171], v[24:27]\n\t" \
  "ds_read_b64_tr_b16 a[196:197], v239 offset:16384\n\t" \
  "v_mfma_f32_16x16x32_bf16 v[24:27], a[108:111], v[164:167], v[24:27]\n\t" \
  "buffer_load_dwordx4 v242, s[16:19], s97 offen offset:2048 sc1 lds\n\t" \
  "v_mfma_f32_16x16x32_bf16 v[24:27], a[100:103], v[160:163], v[24:27]\n\t" \
  "s_waitcnt lgkmcnt(7)\n\t" \
  "ds_read_b64_tr_b16 a[198:199], v239 offset:20480\n\t" \
  "v_mfma_f32_16x16x32_bf16 v[12:15], a[96:99], v[128:131], v[12:15]\n\t" \
  "ds_read_b128 v[160:163], v230 offset:20480\n\t" \
  "ds_read_b128 v[164:167], v230 offset:21504\n\t" \
  "v_mfma_f32_16x16x32_bf16 v[12:15], a[104:107], v[132:135], v[12:15]\n\t" \
  "buffer_load_dwordx4 v242, s[16:19], s97 offen offset:3072 sc1 lds\n\t" \
  "v_mfma_f32_16x16x32_bf16 v[12:15], a[112:115], v[136:139], v[12:15]\n\t" \
  "s_add_u32 s97, s64, 917504\n\t" \
  "s_and_b32 s97, s97, 0xe0000\n\t" \
  "ds_read_b128 v[168:171], v230 offset:22528\n\t" \
  "s_add_u32 s97, s97, s61\n\t" \
  "v_mfma_f32_16x16x32_bf16 v[12:15], a[120:123], v[140:143], v[12:15]\n\t" \
  "s_add_u32 m0, s38, 32768\n\t" \
  "ds_read_b128 v[172:175], v230 offset:23552\n\t" \
  "ds_read_b64_tr_b16 a[200:201], v238 offset:24576\n\t" \
  "v_mfma_f32_16x16x32_bf16 v[28:31], a[124:127], v[140:143], v[28:31]\n\t" \
  "buffer_load_dwordx4 v242, s[16:19], s97 offen offset:0 sc1 lds\n\t" \
  "v_mfma_f32_16x16x32_bf16 v[28:31], a[116:119], v[136:139], v[28:31]\n\t" \
  "ds_read_b64_tr_b16 a[202:203], v238 offset:28672\n\t" \
  "v_mfma_f32_16x16x32_bf16 v[28:31], a[108:111], v[132:135], v[28:31]\n\t" \
  "ds_read_b64_tr_b16 a[204:205], v239 offset:24576\n\t" \
  "v_mfma_f32_16x16x32_bf16 v[28:31], a[100:103], v[128:131], v[28:31]\n\t" \
  "s_waitcnt lgkmcnt(9)\n\t" \
  "buffer_load_dwordx4 v242, s[16:19], s97 offen offset:1024 sc1 lds\n\t" \
  "v_mfma_f32_16x16x32_bf16 v[32:35], a[32:35], v[144:147], v[32:35]\n\t" \
  "ds_read_b128 v[128:131], v230 offset:24576\n\t" \
  "ds_read_b128 v[132:135], v230 offset:25600\n\t" \
  "v_mfma_f32_16x16x32_bf16 v[32:35], a[40:43], v[148:151], v[32:35]\n\t" \
  "ds_read_b64_tr_b16 a[206:207], v239 offset:28672\n\t" \
  "ds_read_b128 v[136:139], v230 offset:26624\n\t" \
  "v_mfma_f32_16x16x32_bf16 v[32:35], a[48:51], v[152:155], v[32:35]\n\t" \
  "buffer_load_dwordx4 v242, s[16:19], s97 offen offset:2048 sc1 lds\n\t" \
  "v_mfma_f32_16x16x32_bf16 v[32:35], a[56:59], v[156:159], v[32:35]\n\t" \
  "s_waitcnt vmcnt(11)\n\t" \
  "v_cmp_ne_u32 vcc, s36, v246\n\t" \
  "v_mfma_f32_16x16x32_bf16 v[48:51], a[60:63], v[156:159], v[48:51]\n\t" \
  "s_and_b32 vcc_lo, vcc_lo, 0xfe0000\n\t" \
  "s_cbranch_scc1 .Lat_s2\n\t" \
  ".Lat_k2:\n\t" \
  "v_mfma_f32_16x16x32_bf16 v[48:51], a[52:55], v[152:155], v[48:51]\n\t" \
  "buffer_load_dwordx4 v242, s[16:19], s97 offen offset:3072 sc1 lds\n\t" \
  "v_mfma_f32_16x16x32_bf16 v[48:51], a[44:47], v[148:151], v[48:51]\n\t" \
  "s_add_u32 s97, s64, 131072\n\t" \
  "s_and_b32 s97, s97, 0xe0000\n\t" \
  "ds_read_b128 v[140:143], v230 offset:27648\n\t" \
  "s_add_u32 s97, s97, s62\n\t" \
  "v_mfma_f32_16x16x32_bf16 v[48:51], a[36:39], v[144:147], v[48:51]\n\t" \
  "s_waitcnt lgkmcnt(8)\n\t" \
  "ds_read_b64_tr_b16 a[208:209], v234 offset:49152\n\t" \
  "s_add_u32 m0, s38, 98304\n\t" \
  "v_mfma_f32_16x16x32_bf16 v[36:39], a[32:35], v[160:163], v[36:39]\n\t" \
  "buffer_load_dwordx4 v242, s[16:19], s97 offen offset:0 sc1 lds\n\t" \
  "ds_read_b128 v[144:147], v230 offset:28672\n\t" \
  "ds_read_b128 v[148:151], v230 offset:29696\n\t" \
  "v_mfma_f32_16x16x32_bf16 v[36:39], a[40:43], v[164:167], v[36:39]\n\t" \
  "ds_read_b64_tr_b16 a[210:211], v234 offset:53248\n\t" \
  "ds_read_b128 v[152:155], v230 offset:30720\n\t" \
  "v_mfma_f32_16x16x32_bf16 v[36:39], a[48:51], v[168:171], v[36:39]\n\t" \
  "ds_read_b128 v[156:159], v230 offset:31744\n\t" \
  "v_mfma_f32_16x16x32_bf16 v[36:39], a[56:59], v[172:175], v[36:39]\n\t" \
  "buffer_load_dwordx4 v242, s[16:19], s97 offen offset:1024 sc1 lds\n\t" \
  "v_mfma_f32_16x16x32_bf16 v[52:55], a[60:63], v[172:175], v[52:55]\n\t" \
  "ds_read_b64_tr_b16 a[212:213], v235 offset:49152\n\t" \
  "v_mfma_f32_16x16x32_bf16 v[52:55], a[52:55], v[168:171], v[52:55]\n\t" \
  "ds_read_b64_tr_b16 a[214:215], v235 offset:53248\n\t" \
  "v_mfma_f32_16x16x32_bf16 v[52:55], a[44:47], v[164:167], v[52:55]\n\t" \
  "s_waitcnt vmcnt(10) lgkmcnt(8)\n\t" \
  "buffer_load_dwordx4 v242, s[16:19], s97 offen offset:2048 sc1 lds\n\t" \
  "v_mfma_f32_16x16x32_bf16 v[52:55], a[36:39], v[160:163], v[52:55]\n\t" \
  "s_barrier\n\t" \
  "v_mfma_f32_16x16x32_bf16 v[40:43], a[32:35], v[128:131], v[40:43]\n\t" \
  "ds_read_b128 v[160:163], v232 offset:0\n\t" \
  "v_mfma_f32_16x16x32_bf16 v[40:43], a[40:43], v[132:135], v[40:43]\n\t" \
  "ds_read_b64_tr_b16 a[216:217], v234 offset:57344\n\t" \
  "v_mfma_f32_16x16x32_bf16 v[40:43], a[48:51], v[136:139], v[40:43]\n\t" \
  "buffer_load_dwordx4 v242, s[16:19], s97 offen offset:3072 sc1 lds\n\t" \
  "v_mfma_f32_16x16x32_bf16 v[40:43], a[56:59], v[140:143], v[40:43]\n\t" \
  "s_add_u32 s97, s64, 262144\n\t" \
  "ds_read_b64_tr_b16 a[218:219], v234 offset:61440\n\t" \
  "ds_read_b128 v[164:167], v232 offset:1024\n\t" \
  "s_and_b32 s97, s97, 0xe0000\n\t" \
  "v_mfma_f32_16x16x32_bf16 v[56:59], a[60:63], v[140:143], v[56:59]\n\t" \
  "s_add_u32 s97, s97, s62\n\t" \
  "ds_read_b128 v[168:171], v232 offset:2048\n\t" \
  "s_add_u32 m0, s38, 65536\n\t" \
  "v_mfma_f32_16x16x32_bf16 v[56:59], a[52:55], v[136:139], v[56:59]\n\t" \
  "ds_read_b128 v[172:175], v232 offset:3072\n\t" \
  "v_mfma_f32_16x16x32_bf16 v[56:59], a[44:47], v[132:135], v[56:59]\n\t" \
  "buffer_load_dwordx4 v242, s[16:19], s97 offen offset:0 sc1 lds\n\t" \
  "v_mfma_f32_16x16x32_bf16 v[56:59], a[36:39], v[128:131], v[56:59]\n\t" \
  "s_waitcnt lgkmcnt(8)\n\t" \
  "ds_read_b64_tr_b16 a[220:221], v235 offset:57344\n\t" \
  "v_mfma_f32_16x16x32_bf16 v[44:47], a[32:35], v[144:147], v[44:47]\n\t" \
  "ds_read_b128 v[128:131], v232 offset:4096\n\t" \
  "v_mfma_f32_16x16x32_bf16 v[44:47], a[40:43], v[148:151], v[44:47]\n\t" \
  "buffer_load_dwordx4 v242, s[16:19], s97 offen offset:1024 sc1 lds\n\t" \
  "v_mfma_f32_16x16x32_bf16 v[44:47], a[48:51], v[152:155], v[44:47]\n\t" \
  "ds_read_b64_tr_b16 a[222:223], v235 offset:61440\n\t" \
  "v_mfma_f32_16x16x32_bf16 v[44:47], a[56:59], v[156:159], v[44:47]\n\t" \
  "ds_read_b128 v[132:135], v232 offset:5120\n\t" \
  "v_mfma_f32_16x16x32_bf16 v[60:63], a[60:63], v[156:159], v[60:63]\n\t" \
  "ds_read_b128 v[136:139], v232 offset:6144\n\t" \
  "v_mfma_f32_16x16x32_bf16 v[60:63], a[52:55], v[152:155], v[60:63]\n\t" \
  "buffer_load_dwordx4 v242, s[16:19], s97 offen offset:2048 sc1 lds\n\t" \
  "v_mfma_f32_16x16x32_bf16 v[60:63], a[44:47], v[148:151], v[60:63]\n\t" \
  "ds_read_b128 v[140:143], v232 offset:7168\n\t" \
  "v_mfma_f32_16x16x32_bf16 v[60:63], a[36:39], v[144:147], v[60:63]\n\t" \
  "s_waitcnt lgkmcnt(3)\n\t" \
  "v_mfma_f32_16x16x32_bf16 v[0:3], a[192:195], v[160:163], v[0:3]\n\t" \
  "buffer_load_dwordx4 v242, s[16:19], s97 offen offset:3072 sc1 lds\n\t" \
  "v_mfma_f32_16x16x32_bf16 v[0:3], a[200:203], v[164:167], v[0:3]\n\t" \
  "s_add_u32 s97, s64, 393216\n\t" \
  "ds_read_b128 v[144:147], v232 offset:8192\n\t" \
  "s_and_b32 s97, s97, 0xe0000\n\t" \
  "v_mfma_f32_16x16x32_bf16 v[0:3], a[208:211], v[168:171], v[0:3]\n\t" \
  "s_add_u32 s97, s97, s62\n\t" \
  "ds_read_b128 v[148:151], v232 offset:9216\n\t" \
  "s_add_u32 m0, s38, 114688\n\t" \
  "v_mfma_f32_16x16x32_bf16 v[0:3], a[216:219], v[172:175], v[0:3]\n\t" \
  "ds_read_b128 v[152:155], v232 offset:10240\n\t" \
  "v_mfma_f32_16x16x32_bf16 v[16:19], a[220:223], v[172:175], v[16:19]\n\t" \
  "buffer_load_dwordx4 v242, s[16:19], s97 offen offset:0 sc1 lds\n\t" \
  "v_mfma_f32_16x16x32_bf16 v[16:19], a[212:215], v[168:171], v[16:19]\n\t" \
  "ds_read_b128 v[156:159], v232 offset:11264\n\t" \
  "v_mfma_f32_16x16x32_bf16 v[16:19], a[204:207], v[164:167], v[16:19]\n\t" \
  "s_nop 2\n\t" \
  "v_mfma_f32_16x16x32_bf16 v[16:19], a[196:199], v[160:163], v[16:19]\n\t" \
  "s_waitcnt lgkmcnt(4)\n\t" \
  "buffer_load_dwordx4 v242, s[16:19], s97 offen offset:1024 sc1 lds\n\t" \
  "v_mfma_f32_16x16x32_bf16 v[4:7], a[192:195], v[128:131], v[4:7]\n\t" \
  "ds_read_b128 v[160:163], v232 offset:12288\n\t" \
  "v_cvt_pk_bf16_f32 v208, v0, v1\n\t" \
  "v_mfma_f32_16x16x32_bf16 v[4:7], a[200:203], v[132:135], v[4:7]\n\t" \
  "ds_read_b128 v[164:167], v232 offset:13312\n\t" \
  "v_cvt_pk_bf16_f32 v209, v2, v3\n\t" \
  "v_mfma_f32_16x16x32_bf16 v[4:7], a[208:211], v[136:139], v[4:7]\n\t" \
  "v_cvt_pk_bf16_f32 v210, v16, v17\n\t" \
  "v_cvt_pk_bf16_f32 v211, v18, v19\n\t" \
  "v_mfma_f32_16x16x32_bf16 v[4:7], a[216:219], v[140:143], v[4:7]\n\t" \
  "buffer_load_dwordx4 v242, s[16:19], s97 offen offset:2048 sc1 lds\n\t" \
  "v_mfma_f32_16x16x32_bf16 v[20:23], a[220:223], v[140:143], v[20:23]\n\t" \
  "ds_read_b128 v[168:171], v232 offset:14336\n\t" \
  "v_mfma_f32_16x16x32_bf16 v[20:23], a[212:215], v[136:139], v[20:23]\n\t" \
  "ds_read_b128 v[172:175], v232 offset:15360\n\t" \
  "v_mfma_f32_16x16x32_bf16 v[20:23], a[204:207], v[132:135], v[20:23]\n\t" \
  "s_nop 2\n\t" \
  "v_mfma_f32_16x16x32_bf16 v[20:23], a[196:199], v[128:131], v[20:23]\n\t" \
  "s_waitcnt lgkmcnt(4)\n\t" \
  "v_cvt_pk_bf16_f32 v212, v4, v5\n\t" \
  "v_cvt_pk_bf16_f32 v213, v6, v7\n\t" \
  "v_mfma_f32_16x16x32_bf16 v[8:11], a[192:195], v[144:147], v[8:11]\n\t" \
  "ds_read_b128 v[128:131], v242 offset:16384\n\t" \
  "s_nop 0\n\t" \
  "v_mfma_f32_16x16x32_bf16 v[8:11], a[200:203], v[148:151], v[8:11]\n\t" \
  "ds_read_b128 v[132:135], v242 offset:17408\n\t" \
  "v_cvt_pk_bf16_f32 v214, v20, v21\n\t" \
  "v_mfma_f32_16x16x32_bf16 v[8:11], a[208:211], v[152:155], v[8:11]\n\t" \
  "ds_read_b128 v[136:139], v242 offset:18432\n\t" \
  "v_cvt_pk_bf16_f32 v215, v22, v23\n\t" \
  "v_mfma_f32_16x16x32_bf16 v[8:11], a[216:219], v[156:159], v[8:11]\n\t" \
  "ds_read_b128 v[140:143], v242 offset:19456\n\t" \
  "v_mfma_f32_16x16x32_bf16 v[24:27], a[220:223], v[156:159], v[24:27]\n\t" \
  "v_mfma_f32_16x16x32_bf16 v[24:27], a[212:215], v[152:155], v[24:27]\n\t" \
  "v_mfma_f32_16x16x32_bf16 v[24:27], a[204:207], v[148:151], v[24:27]\n\t" \
  "s_nop 2\n\t" \
  "v_mfma_f32_16x16x32_bf16 v[24:27], a[196:199], v[144:147], v[24:27]\n\t" \
  "s_waitcnt lgkmcnt(4)\n\t" \
  "v_cvt_pk_bf16_f32 v216, v8, v9\n\t" \
  "v_cvt_pk_bf16_f32 v217, v10, v11\n\t" \
  "v_mfma_f32_16x16x32_bf16 v[12:15], a[192:195], v[160:163], v[12:15]\n\t" \
  "ds_read_b128 v[144:147], v242 offset:20480\n\t" \
  "s_nop 0\n\t" \
  "v_mfma_f32_16x16x32_bf16 v[12:15], a[200:203], v[164:167], v[12:15]\n\t" \
  "ds_read_b128 v[148:151], v242 offset:21504\n\t" \
  "v_cvt_pk_bf16_f32 v218, v24, v25\n\t" \
  "v_mfma_f32_16x16x32_bf16 v[12:15], a[208:211], v[168:171], v[12:15]\n\t" \
  "ds_read_b128 v[152:155], v242 offset:22528\n\t" \
  "v_cvt_pk_bf16_f32 v219, v26, v27\n\t" \
  "v_mfma_f32_16x16x32_bf16 v[12:15], a[216:219], v[172:175], v[12:15]\n\t" \
  "buffer_store_dwordx4 v[208:211], v242, s[16:19], s67 offen offset:0 sc1\n\t" \
  "ds_read_b128 v[156:159], v242 offset:23552\n\t" \
  "v_mfma_f32_16x16x32_bf16 v[28:31], a[220:223], v[172:175], v[28:31]\n\t" \
  "v_mfma_f32_16x16x32_bf16 v[28:31], a[212:215], v[168:171], v[28:31]\n\t" \
  "v_mfma_f32_16x16x32_bf16 v[28:31], a[204:207], v[164:167], v[28:31]\n\t" \
  "s_nop 2\n\t" \
  "v_mfma_f32_16x16x32_bf16 v[28:31], a[196:199], v[160:163], v[28:31]\n\t" \
  "s_waitcnt lgkmcnt(4)\n\t" \
  "v_cvt_pk_bf16_f32 v220, v12, v13\n\t" \
  "v_cvt_pk_bf16_f32 v221, v14, v15\n\t" \
  "v_mfma_f32_16x16x32_bf16 v[32:35], a[64:67], v[128:131], v[32:35]\n\t" \
  "buffer_store_dwordx4 v[212:215], v242, s[16:19], s67 offen offset:1024 sc1\n\t" \
  "ds_read_b128 v[160:163], v242 offset:24576\n\t" \
  "ds_read_b128 v[164:167], v242 offset:25600\n\t" \
  "ds_read_b128 v[168:171], v242 offset:26624\n\t" \
  "v_mfma_f32_16x16x32_bf16 v[32:35], a[72:75], v[132:135], v[32:35]\n\t" \
  "v_cvt_pk_bf16_f32 v222, v28, v29\n\t" \
  "v_cvt_pk_bf16_f32 v223, v30, v31\n\t" \
  "v_mfma_f32_16x16x32_bf16 v[32:35], a[80:83], v[136:139], v[32:35]\n\t" \
  "ds_read_b128 v[172:175], v242 offset:27648\n\t" \
  "v_mfma_f32_16x16x32_bf16 v[32:35], a[88:91], v[140:143], v[32:35]\n\t" \
  "v_mfma_f32_16x16x32_bf16 v[48:51], a[92:95], v[140:143], v[48:51]\n\t" \
  "v_mfma_f32_16x16x32_bf16 v[48:51], a[84:87], v[136:139], v[48:51]\n\t" \
  "v_mfma_f32_16x16x32_bf16 v[48:51], a[76:79], v[132:135], v[48:51]\n\t" \
  "v_mfma_f32_16x16x32_bf16 v[48:51], a[68:71], v[128:131], v[48:51]\n\t" \
  "s_waitcnt lgkmcnt(4)\n\t" \
  "v_mfma_f32_16x16x32_bf16 v[36:39], a[64:67], v[144:147], v[36:39]\n\t" \
  "ds_read_b128 v[128:131], v242 offset:28672\n\t" \
  "v_mfma_f32_16x16x32_bf16 v[36:39], a[72:75], v[148:151], v[36:39]\n\t" \
  "ds_read_b128 v[132:135], v242 offset:29696\n\t" \
  "v_mfma_f32_16x16x32_bf16 v[36:39], a[80:83], v[152:155], v[36:39]\n\t" \
  "buffer_store_dwordx4 v[216:219], v242, s[16:19], s67 offen offset:2048 sc1\n\t" \
  "ds_read_b128 v[136:139], v242 offset:30720\n\t" \
  "ds_read_b128 v[140:143], v242 offset:31744\n\t" \
  "v_mfma_f32_16x16x32_bf16 v[36:39], a[88:91], v[156:159], v[36:39]\n\t" \
  "v_mfma_f32_16x16x32_bf16 v[52:55], a[92:95], v[156:159], v[52:55]\n\t" \
  "v_mfma_f32_16x16x32_bf16 v[52:55], a[84:87], v[152:155], v[52:55]\n\t" \
  "v_mfma_f32_16x16x32_bf16 v[52:55], a[76:79], v[148:151], v[52:55]\n\t" \
  "s_waitcnt vmcnt(14) lgkmcnt(4)\n\t" \
  "v_mfma_f32_16x16x32_bf16 v[52:55], a[68:71], v[144:147], v[52:55]\n\t" \
  "s_barrier\n\t" \
  "v_mfma_f32_16x16x32_bf16 v[40:43], a[64:67], v[160:163], v[40:43]\n\t" \
  "ds_read_b128 v[144:147], v242 offset:0\n\t" \
  "v_mfma_f32_16x16x32_bf16 v[40:43], a[72:75], v[164:167], v[40:43]\n\t" \
  "ds_read_b128 v[148:151], v242 offset:1024\n\t" \
  "v_mfma_f32_16x16x32_bf16 v[40:43], a[80:83], v[168:171], v[40:43]\n\t" \
  "ds_read_b128 v[152:155], v242 offset:2048\n\t" \
  "v_mfma_f32_16x16x32_bf16 v[40:43], a[88:91], v[172:175], v[40:43]\n\t" \
  "ds_read_b128 v[156:159], v242 offset:3072\n\t" \
  "v_mfma_f32_16x16x32_bf16 v[56:59], a[92:95], v[172:175], v[56:59]\n\t" \
  "v_mfma_f32_16x16x32_bf16 v[56:59], a[84:87], v[168:171], v[56:59]\n\t" \
  "v_mfma_f32_16x16x32_bf16 v[56:59], a[76:79], v[164:167], v[56:59]\n\t" \
  "v_mfma_f32_16x16x32_bf16 v[56:59], a[68:71], v[160:163], v[56:59]\n\t" \
  "s_waitcnt lgkmcnt(4)\n\t" \
  "buffer_store_dwordx4 v[220:223], v242, s[16:19], s67 offen offset:3072 sc1\n\t" \
  "v_mfma_f32_16x16x32_bf16 v[44:47], a[64:67], v[128:131], v[44:47]\n\t" \
  "ds_read_b128 v[160:163], v242 offset:4096\n\t" \
  "v_mfma_f32_16x16x32_bf16 v[44:47], a[72:75], v[132:135], v[44:47]\n\t" \
  "ds_read_b128 v[164:167], v242 offset:5120\n\t" \
  "v_mfma_f32_16x16x32_bf16 v[44:47], a[80:83], v[136:139], v[44:47]\n\t" \
  "ds_read_b128 v[168:171], v242 offset:6144\n\t" \
  "v_mfma_f32_16x16x32_bf16 v[44:47], a[88:91], v[140:143], v[44:47]\n\t" \
  "ds_read_b128 v[172:175], v242 offset:7168\n\t" \
  "v_mfma_f32_16x16x32_bf16 v[60:63], a[92:95], v[140:143], v[60:63]\n\t" \
  "v_mfma_f32_16x16x32_bf16 v[60:63], a[84:87], v[136:139], v[60:63]\n\t" \
  "v_mfma_f32_16x16x32_bf16 v[60:63], a[76:79], v[132:135], v[60:63]\n\t" \
  "v_mfma_f32_16x16x32_bf16 v[60:63], a[68:71], v[128:131], v[60:63]\n\t" \
  "s_waitcnt lgkmcnt(4)\n\t" \
  "v_mfma_f32_16x16x32_bf16 v[32:35], a[96:99], v[144:147], v[32:35]\n\t" \
  "ds_read_b128 v[128:131], v242 offset:8192\n\t" \
  "v_mfma_f32_16x16x32_bf16 v[32:35], a[104:107], v[148:151], v[32:35]\n\t" \
  "ds_read_b128 v[132:135], v242 offset:9216\n\t" \
  "v_mfma_f32_16x16x32_bf16 v[32:35], a[112:115], v[152:155], v[32:35]\n\t" \
  "ds_read_b128 v[136:139], v242 offset:10240\n\t" \
  "v_mfma_f32_16x16x32_bf16 v[32:35], a[120:123], v[156:159], v[32:35]\n\t" \
  "buffer_load_dwordx4 v242, s[16:19], s97 offen offset:3072 sc1 lds\n\t" \
  "v_mfma_f32_16x16x32_bf16 v[48:51], a[124:127], v[156:159], v[48:51]\n\t" \
  "s_add_u32 s97, s64, 524288\n\t" \
  "ds_read_b128 v[140:143], v242 offset:11264\n\t" \
  "s_and_b32 s97, s97, 0xe0000\n\t" \
  "v_mfma_f32_16x16x32_bf16 v[48:51], a[116:119], v[152:155], v[48:51]\n\t" \
  "s_add_u32 s97, s97, s62\n\t" \
  "s_add_u32 m0, s38, 81920\n\t" \
  "v_mfma_f32_16x16x32_bf16 v[48:51], a[108:111], v[148:151], v[48:51]\n\t" \
  "v_mfma_f32_16x16x32_bf16 v[48:51], a[100:103], v[144:147], v[48:51]\n\t" \
  "s_waitcnt lgkmcnt(4)\n\t" \
  "buffer_load_dwordx4 v242, s[16:19], s97 offen offset:0 sc1 lds\n\t" \
  "v_mfma_f32_16x16x32_bf16 v[36:39], a[96:99], v[160:163], v[36:39]\n\t" \
  "ds_read_b128 v[144:147], v242 offset:12288\n\t" \
  "v_mfma_f32_16x16x32_bf16 v[36:39], a[104:107], v[164:167], v[36:39]\n\t" \
  "ds_read_b128 v[148:151], v242 offset:13312\n\t" \
  "v_mfma_f32_16x16x32_bf16 v[36:39], a[112:115], v[168:171], v[36:39]\n\t" \
  "ds_read_b128 v[152:155], v242 offset:14336\n\t" \
  "v_mfma_f32_16x16x32_bf16 v[36:39], a[120:123], v[172:175], v[36:39]\n\t" \
  "buffer_load_dwordx4 v242, s[16:19], s97 offen offset:1024 sc1 lds\n\t" \
  "v_mfma_f32_16x16x32_bf16 v[52:55], a[124:127], v[172:175], v[52:55]\n\t" \
  "ds_read_b128 v[156:159], v242 offset:15360\n\t" \
  "v_mfma_f32_16x16x32_bf16 v[52:55], a[116:119], v[168:171], v[52:55]\n\t" \
  "v_mfma_f32_16x16x32_bf16 v[52:55], a[108:111], v[164:167], v[52:55]\n\t" \
  "buffer_load_dwordx4 v242, s[16:19], s97 offen offset:2048 sc1 lds\n\t" \
  "v_mfma_f32_16x16x32_bf16 v[52:55], a[100:103], v[160:163], v[52:55]\n\t" \
  "s_waitcnt lgkmcnt(4)\n\t" \
  "v_mfma_f32_16x16x32_bf16 v[40:43], a[96:99], v[128:131], v[40:43]\n\t" \
  "ds_read_b128 v[160:163], v242 offset:32768\n\t" \
  "v_mfma_f32_16x16x32_bf16 v[40:43], a[104:107], v[132:135], v[40:43]\n\t" \
  ".Lhs13:\n\ts_cbranch_execz .Lhs13n\n\t" \
  "ds_read_b128 v[164:167], v242 offset:33792\n\t" \
  "v_mfma_f32_16x16x32_bf16 v[40:43], a[112:115], v[136:139], v[40:43]\n\t" \
  "buffer_load_dwordx4 v242, s[16:19], s97 offen offset:3072 sc1 lds\n\t" \
  "v_mfma_f32_16x16x32_bf16 v[40:43], a[120:123], v[140:143], v[40:43]\n\t" \
  "s_add_u32 s97, s64, 655360\n\t" \
  "s_and_b32 s97, s97, 0xe0000\n\t" \
  "s_add_u32 s97, s97, s62\n\t" \
  "v_mfma_f32_16x16x32_bf16 v[56:59], a[124:127], v[140:143], v[56:59]\n\t" \
  "s_add_u32 m0, s38, 131072\n\t" \
  "ds_read_b128 v[168:171], v242 offset:34816\n\t" \
  "v_mfma_f32_16x16x32_bf16 v[56:59], a[116:119], v[136:139], v[56:59]\n\t" \
  "buffer_load_dwordx4 v242, s[16:19], s97 offen offset:0 sc1 lds\n\t" \
  "v_mfma_f32_16x16x32_bf16 v[56:59], a[108:111], v[132:135], v[56:59]\n\t" \
  "ds_read_b128 v[172:175], v242 offset:35840\n\t" \
  "v_mfma_f32_16x16x32_bf16 v[56:59], a[100:103], v[128:131], v[56:59]\n\t" \
  "s_waitcnt lgkmcnt(4)\n\t" \
  "v_mfma_f32_16x16x32_bf16 v[44:47], a[96:99], v[144:147], v[44:47]\n\t" \
  "ds_read_b128 v[128:131], v242 offset:36864\n\t" \
  "v_mfma_f32_16x16x32_bf16 v[44:47], a[104:107], v[148:151], v[44:47]\n\t" \
  "buffer_load_dwordx4 v242, s[16:19], s97 offen offset:1024 sc1 lds\n\t" \
  "v_mfma_f32_16x16x32_bf16 v[44:47], a[112:115], v[152:155], v[44:47]\n\t" \
  "ds_read_b128 v[132:135], v242 offset:37888\n\t" \
  "v_mfma_f32_16x16x32_bf16 v[44:47], a[120:123], v[156:159], v[44:47]\n\t" \
  "ds_read_b128 v[136:139], v242 offset:38912\n\t" \
  "v_mfma_f32_16x16x32_bf16 v[60:63], a[124:127], v[156:159], v[60:63]\n\t" \
  "buffer_load_dwordx4 v242, s[16:19], s97 offen offset:2048 sc1 lds\n\t" \
  "v_mfma_f32_16x16x32_bf16 v[60:63], a[116:119], v[152:155], v[60:63]\n\t" \
  "ds_read_b128 v[140:143], v242 offset:39936\n\t" \
  "v_mfma_f32_16x16x32_bf16 v[60:63], a[108:111], v[148:151], v[60:63]\n\t" \
  "v_mfma_f32_16x16x32_bf16 v[60:63], a[100:103], v[144:147], v[60:63]\n\t" \
  "s_waitcnt lgkmcnt(4)\n\t" \
  "v_mfma_f32_16x16x32_bf16 v[32:35], a[192:195], v[160:163], v[32:35]\n\t" \
  "buffer_load_dwordx4 v242, s[16:19], s97 offen offset:3072 sc1 lds\n\t" \
  "v_mfma_f32_16x16x32_bf16 v[32:35], a[200:203], v[164:167], v[32:35]\n\t" \
  "s_add_u32 s97, s64, 786432\n\t" \
  "ds_read_b128 v[144:147], v242 offset:40960\n\t" \
  "s_and_b32 s97, s97, 0xe0000\n\t" \
  "v_mfma_f32_16x16x32_bf16 v[32:35], a[208:211], v[168:171], v[32:35]\n\t" \
  "s_add_u32 s97, s97, s62\n\t" \
  "ds_read_b128 v[148:151], v242 offset:41984\n\t" \
  "s_add_u32 m0, s38, 49152\n\t" \
  "v_mfma_f32_16x16x32_bf16 v[32:35], a[216:219], v[172:175], v[32:35]\n\t" \
  "ds_read_b128 v[152:155], v242 offset:43008\n\t" \
  "v_mfma_f32_16x16x32_bf16 v[48:51], a[220:223], v[172:175], v[48:51]\n\t" \
  "ds_read_b128 v[156:159], v242 offset:44032\n\t" \
  "v_mfma_f32_16x16x32_bf16 v[48:51], a[212:215], v[168:171], v[48:51]\n\t" \
  "v_mfma_f32_16x16x32_bf16 v[48:51], a[204:207], v[164:167], v[48:51]\n\t" \
  "buffer_load_dwordx4 v242, s[16:19], s97 offen offset:0 sc1 lds\n\t" \
  "v_mfma_f32_16x16x32_bf16 v[48:51], a[196:199], v[160:163], v[48:51]\n\t" \
  "s_waitcnt lgkmcnt(4)\n\t" \
  "v_cvt_pk_bf16_f32 v0, v32, v33\n\t" \
  "v_cvt_pk_bf16_f32 v1, v34, v35\n\t" \
  "v_mfma_f32_16x16x32_bf16 v[36:39], a[192:195], v[128:131], v[36:39]\n\t" \
  "ds_read_b128 v[160:163], v242 offset:45056\n\t" \
  "s_nop 0\n\t" \
  "v_mfma_f32_16x16x32_bf16 v[36:39], a[200:203], v[132:135], v[36:39]\n\t" \
  "ds_read_b128 v[164:167], v242 offset:46080\n\t" \
  "v_cvt_pk_bf16_f32 v2, v48, v49\n\t" \
  "v_mfma_f32_16x16x32_bf16 v[36:39], a[208:211], v[136:139], v[36:39]\n\t" \
  "ds_read_b128 v[168:171], v242 offset:47104\n\t" \
  "v_cvt_pk_bf16_f32 v3, v50, v51\n\t" \
  "v_mfma_f32_16x16x32_bf16 v[36:39], a[216:219], v[140:143], v[36:39]\n\t" \
  "ds_read_b128 v[172:175], v242 offset:48128\n\t" \
  "v_mfma_f32_16x16x32_bf16 v[52:55], a[220:223], v[140:143], v[52:55]\n\t" \
  "v_mfma_f32_16x16x32_bf16 v[52:55], a[212:215], v[136:139], v[52:55]\n\t" \
  "v_mfma_f32_16x16x32_bf16 v[52:55], a[204:207], v[132:135], v[52:55]\n\t" \
  "s_waitcnt vmcnt(17) lgkmcnt(4)\n\t" \
  "s_nop 1\n\t" \
  "v_mfma_f32_16x16x32_bf16 v[52:55], a[196:199], v[128:131], v[52:55]\n\t" \
  "s_barrier\n\t" \
  "v_mfma_f32_16x16x32_bf16 v[40:43], a[192:195], v[144:147], v[40:43]\n\t" \
  "ds_read_b128 v[128:131], v230 offset:32768\n\t" \
  "v_cvt_pk_bf16_f32 v4, v36, v37\n\t" \
  "v_mfma_f32_16x16x32_bf16 v[40:43], a[200:203], v[148:151], v[40:43]\n\t" \
  "v_cvt_pk_bf16_f32 v5, v38, v39\n\t" \
  "v_and_b32 v235, 15, v233\n\t" \
  "v_mfma_f32_16x16x32_bf16 v[40:43], a[208:211], v[152:155], v[40:43]\n\t" \
  "v_cvt_pk_bf16_f32 v6, v52, v53\n\t" \
  "v_cvt_pk_bf16_f32 v7, v54, v55\n\t" \
  "v_mfma_f32_16x16x32_bf16 v[40:43], a[216:219], v[156:159], v[40:43]\n\t" \
  "buffer_store_dwordx4 v[0:3], v242, s[16:19], s68 offen offset:0 sc1\n\t" \
  "ds_read_b128 v[132:135], v230 offset:33792\n\t" \
  "v_lshrrev_b32 v236, 4, v233\n\t" \
  "v_lshlrev_b32 v234, 13, v236\n\t" \
  "v_mfma_f32_16x16x32_bf16 v[56:59], a[220:223], v[156:159], v[56:59]\n\t" \
  "ds_read_b128 v[136:139], v230 offset:34816\n\t" \
  "v_lshl_add_u32 v234, v235, 4, v234\n\t" \
  "v_mfma_f32_16x16x32_bf16 v[56:59], a[212:215], v[152:155], v[56:59]\n\t" \
  "buffer_load_dwordx4 v242, s[16:19], s97 offen offset:1024 sc1 lds\n\t" \
  "v_mfma_f32_16x16x32_bf16 v[56:59], a[204:207], v[148:151], v[56:59]\n\t" \
  "ds_read_b128 v[140:143], v230 offset:35840\n\t" \
  "v_lshlrev_b32 v240, 7, v236\n\t" \
  "v_mfma_f32_16x16x32_bf16 v[56:59], a[196:199], v[144:147], v[56:59]\n\t" \
  "s_waitcnt lgkmcnt(4)\n\t" \
  "v_cvt_pk_bf16_f32 v8, v40, v41\n\t" \
  "v_cvt_pk_bf16_f32 v9, v42, v43\n\t" \
  "v_mfma_f32_16x16x32_bf16 v[44:47], a[192:195], v[160:163], v[44:47]\n\t" \
  "s_waitcnt vmcnt(12)\n\t" \
  "buffer_store_dword v243, v244, s[16:19], s71 offen offset:0 sc1\n\t" \
  "ds_read_b128 v[144:147], v230 offset:36864\n\t" \
  "v_mfma_f32_16x16x32_bf16 v[44:47], a[200:203], v[164:167], v[44:47]\n\t" \
  "v_cvt_pk_bf16_f32 v10, v56, v57\n\t" \
  "v_cvt_pk_bf16_f32 v11, v58, v59\n\t" \
  "v_mfma_f32_16x16x32_bf16 v[44:47], a[208:211], v[168:171], v[44:47]\n\t" \
  "buffer_store_dwordx4 v[4:7], v242, s[16:19], s68 offen offset:1024 sc1\n\t" \
  "ds_read_b128 v[148:151], v230 offset:37888\n\t" \
  "v_lshl_add_u32 v240, v235, 3, v240\n\t" \
  "v_add_u32 v240, s76, v240\n\t" \
  "v_mfma_f32_16x16x32_bf16 v[44:47], a[216:219], v[172:175], v[44:47]\n\t" \
  "ds_read_b128 v[152:155], v230 offset:38912\n\t" \
  "v_lshlrev_b32 v235, 2, v235\n\t" \
  "v_mfma_f32_16x16x32_bf16 v[60:63], a[220:223], v[172:175], v[60:63]\n\t" \
  "ds_read_b128 v[156:159], v230 offset:39936\n\t" \
  "v_mfma_f32_16x16x32_bf16 v[60:63], a[212:215], v[168:171], v[60:63]\n\t" \
  "v_mfma_f32_16x16x32_bf16 v[60:63], a[204:207], v[164:167], v[60:63]\n\t" \
  "s_nop 2\n\t" \
  "v_mfma_f32_16x16x32_bf16 v[60:63], a[196:199], v[160:163], v[60:63]\n\t" \
  "s_waitcnt lgkmcnt(4)\n\t" \
  "v_cvt_pk_bf16_f32 v12, v44, v45\n\t" \
  "v_cvt_pk_bf16_f32 v13, v46, v47\n\t" \
  STP("s_memrealtime s[94:95]\n\t") \
  STP("s_memtime s[92:93]\n\t") \
  "v_mfma_f32_16x16x32_bf16 v[64:67], a[128:131], v[128:131], v[64:67]\n\t" \
  "ds_read_b128 v[160:163], v230 offset:40960\n\t" \
  "s_nop 0\n\t" \
  "v_mfma_f32_16x16x32_bf16 v[64:67], a[136:139], v[132:135], v[64:67]\n\t" \
  "ds_read_b128 v[164:167], v230 offset:41984\n\t" \
  "v_cvt_pk_bf16_f32 v14, v60, v61\n\t" \
  "v_mfma_f32_16x16x32_bf16 v[64:67], a[144:147], v[136:139], v[64:67]\n\t" \
  "ds_read_b128 v[168:171], v230 offset:43008\n\t" \
  "v_cvt_pk_bf16_f32 v15, v62, v63\n\t" \
  "v_mfma_f32_16x16x32_bf16 v[64:67], a[152:155], v[140:143], v[64:67]\n\t" \
  "ds_read_b128 v[172:175], v230 offset:44032\n\t" \
  "s_add_u32 s78, s75, 0\n\t" \
  "v_mfma_f32_16x16x32_bf16 v[80:83], a[156:159], v[140:143], v[80:83]\n\t" \
  "v_mfma_f32_16x16x32_bf16 v[80:83], a[148:151], v[136:139], v[80:83]\n\t" \
  "v_mfma_f32_16x16x32_bf16 v[80:83], a[140:143], v[132:135], v[80:83]\n\t" \
  "v_mfma_f32_16x16x32_bf16 v[80:83], a[132:135], v[128:131], v[80:83]\n\t" \
  "s_waitcnt lgkmcnt(4)\n\t" \
  "buffer_load_dwordx4 v[208:211], v234, s[16:19], s78 offen sc1\n\t" \
  "v_mfma_f32_16x16x32_bf16 v[68:71], a[128:131], v[144:147], v[68:71]\n\t" \
  "ds_read_b128 v[128:131], v230 offset:45056\n\t" \
  "s_add_u32 s78, s75, 32768\n\t" \
  "v_mfma_f32_16x16x32_bf16 v[68:71], a[136:139], v[148:151], v[68:71]\n\t" \
  "ds_read_b128 v[132:135], v230 offset:46080\n\t" \
  "v_mfma_f32_16x16x32_bf16 v[68:71], a[144:147], v[152:155], v[68:71]\n\t" \
  "ds_read_b128 v[136:139], v230 offset:47104\n\t" \
  "v_mfma_f32_16x16x32_bf16 v[68:71], a[152:155], v[156:159], v[68:71]\n\t" \
  "ds_read_b128 v[140:143], v230 offset:48128\n\t" \
  "v_mfma_f32_16x16x32_bf16 v[84:87], a[156:159], v[156:159], v[84:87]\n\t" \
  STP("v_writelane_b32 v255, s94, 34\n\t") \
  STP("v_writelane_b32 v255, s92, 35\n\t") \
  "buffer_load_dwordx4 v[212:215], v234, s[16:19], s78 offen sc1\n\t" \
  "v_mfma_f32_16x16x32_bf16 v[84:87], a[148:151], v[152:155], v[84:87]\n\t" \
  "s_add_u32 s78, s75, 65536\n\t" \
  "v_mfma_f32_16x16x32_bf16 v[84:87], a[140:143], v[148:151], v[84:87]\n\t" \
  "v_mfma_f32_16x16x32_bf16 v[84:87], a[132:135], v[144:147], v[84:87]\n\t" \
  "s_waitcnt lgkmcnt(4)\n\t" \
  "v_mfma_f32_16x16x32_bf16 v[72:75], a[128:131], v[160:163], v[72:75]\n\t" \
  "ds_read_b128 v[144:147], v230 offset:0\n\t" \
  "v_mfma_f32_16x16x32_bf16 v[72:75], a[136:139], v[164:167], v[72:75]\n\t" \
  "buffer_load_dwordx4 v[216:219], v234, s[16:19], s78 offen sc1\n\t" \
  "v_mfma_f32_16x16x32_bf16 v[72:75], a[144:147], v[168:171], v[72:75]\n\t" \
  "ds_read_b128 v[148:151], v230 offset:1024\n\t" \
  "s_add_u32 s78, s75, 98304\n\t" \
  "v_mfma_f32_16x16x32_bf16 v[72:75], a[152:155], v[172:175], v[72:75]\n\t" \
  "ds_read_b128 v[152:155], v230 offset:2048\n\t" \
  "v_mfma_f32_16x16x32_bf16 v[88:91], a[156:159], v[172:175], v[88:91]\n\t" \
  "ds_read_b128 v[156:159], v230 offset:3072\n\t" \
  "v_mfma_f32_16x16x32_bf16 v[88:91], a[148:151], v[168:171], v[88:91]\n\t" \
  "v_mfma_f32_16x16x32_bf16 v[88:91], a[140:143], v[164:167], v[88:91]\n\t" \
  "buffer_load_dwordx4 v[220:223], v234, s[16:19], s78 offen sc1\n\t" \
  "v_mfma_f32_16x16x32_bf16 v[88:91], a[132:135], v[160:163], v[88:91]\n\t" \
  "s_waitcnt lgkmcnt(4)\n\t" \
  "buffer_load_dword v224, v235, s[32:35], s80 offen\n\t" \
  "v_mfma_f32_16x16x32_bf16 v[76:79], a[128:131], v[128:131], v[76:79]\n\t" \
  "ds_read_b128 v[160:163], v230 offset:4096\n\t" \
  "v_lshrrev_b32 v250, 4, v233\n\t" \
  "v_mfma_f32_16x16x32_bf16 v[76:79], a[136:139], v[132:135], v[76:79]\n\t" \
  "v_and_b32 v238, 15, v233\n\t" \
  "v_xor_b32 v26, v238, v250\n\t" \
  "v_mfma_f32_16x16x32_bf16 v[76:79], a[144:147], v[136:139], v[76:79]\n\t" \
  "ds_read_b128 v[164:167], v230 offset:5120\n\t" \
  "v_lshlrev_b32 v26, 4, v26\n\t" \
  "v_mfma_f32_16x16x32_bf16 v[76:79], a[152:155], v[140:143], v[76:79]\n\t" \
  "v_lshl_add_u32 v26, v250, 11, v26\n\t" \
  "v_xor_b32 v27, 64, v26\n\t" \
  "v_mfma_f32_16x16x32_bf16 v[92:95], a[156:159], v[140:143], v[92:95]\n\t" \
  "ds_read_b128 v[168:171], v230 offset:6144\n\t" \
  "v_xor_b32 v28, 0x80, v26\n\t" \
  "v_mfma_f32_16x16x32_bf16 v[92:95], a[148:151], v[136:139], v[92:95]\n\t" \
  "v_xor_b32 v29, 0xc0, v26\n\t" \
  "v_lshrrev_b32 v30, 5, v233\n\t" \
  "v_mfma_f32_16x16x32_bf16 v[92:95], a[140:143], v[132:135], v[92:95]\n\t" \
  "ds_read_b128 v[172:175], v230 offset:7168\n\t" \
  "v_lshl_add_u32 v30, s3, 2, v30\n\t" \
  "v_mfma_f32_16x16x32_bf16 v[92:95], a[132:135], v[128:131], v[92:95]\n\t" \
  "s_waitcnt lgkmcnt(4)\n\t" \
  "v_xor_b32 v30, v30, v238\n\t" \
  "v_lshlrev_b32 v30, 4, v30\n\t" \
  "v_mfma_f32_16x16x32_bf16 v[64:67], a[160:163], v[144:147], v[64:67]\n\t" \
  "buffer_store_dwordx4 v[8:11], v242, s[16:19], s68 offen offset:2048 sc1\n\t" \
  "ds_read_b128 v[128:131], v230 offset:8192\n\t" \
  "v_lshl_add_u32 v30, v238, 8, v30\n\t" \
  "v_and_b32 v250, 1, v250\n\t" \
  "v_mfma_f32_16x16x32_bf16 v[64:67], a[168:171], v[148:151], v[64:67]\n\t" \
  "v_lshl_add_u32 v30, v250, 3, v30\n\t" \
  "v_add_u32 v238, 0, v30\n\t" \
  "v_mfma_f32_16x16x32_bf16 v[64:67], a[176:179], v[152:155], v[64:67]\n\t" \
  "ds_read_b128 v[132:135], v230 offset:9216\n\t" \
  "v_xor_b32 v239, 32, v238\n\t" \
  "v_mfma_f32_16x16x32_bf16 v[64:67], a[184:187], v[156:159], v[64:67]\n\t" \
  "v_bfe_u32 v237, v233, 1, 2\n\t" \
  "v_bfe_u32 v16, v233, 3, 1\n\t" \
  "v_mfma_f32_16x16x32_bf16 v[80:83], a[188:191], v[156:159], v[80:83]\n\t" \
  "ds_read_b128 v[136:139], v230 offset:10240\n\t" \
  "v_lshlrev_b32 v237, 4, v237\n\t" \
  "v_mfma_f32_16x16x32_bf16 v[80:83], a[180:183], v[152:155], v[80:83]\n\t" \
  "v_lshl_add_u32 v237, v16, 3, v237\n\t" \
  "v_and_b32 v16, 1, v233\n\t" \
  "v_mfma_f32_16x16x32_bf16 v[80:83], a[172:175], v[148:151], v[80:83]\n\t" \
  "ds_read_b128 v[140:143], v230 offset:11264\n\t" \
  "v_lshl_add_u32 v237, v16, 2, v237\n\t" \
  "v_mfma_f32_16x16x32_bf16 v[80:83], a[164:167], v[144:147], v[80:83]\n\t" \
  "s_waitcnt lgkmcnt(4)\n\t" \
  "v_add_u32 v237, s76, v237\n\t" \
  "v_cmp_eq_u32 vcc, 2, v236\n\t" \
  "v_mfma_f32_16x16x32_bf16 v[68:71], a[160:163], v[160:163], v[68:71]\n\t" \
  "ds_read_b128 v[144:147], v230 offset:12288\n\t" \
  "v_mov_b32 v16, 0\n\t" \
  "v_mfma_f32_16x16x32_bf16 v[68:71], a[168:171], v[164:167], v[68:71]\n\t" \
  "v_mov_b32 v17, 0\n\t" \
  "v_mov_b32 v18, 0\n\t" \
  "v_mfma_f32_16x16x32_bf16 v[68:71], a[176:179], v[168:171], v[68:71]\n\t" \
  "ds_read_b128 v[148:151], v230 offset:13312\n\t" \
  "v_mov_b32 v19, 0\n\t" \
  "v_mfma_f32_16x16x32_bf16 v[68:71], a[184:187], v[172:175], v[68:71]\n\t" \
  "v_cndmask_b32 v236, 0, 1.0, vcc\n\t" \
  "v_mov_b32 v20, 0x3f803f80\n\t" \
  "v_mfma_f32_16x16x32_bf16 v[84:87], a[188:191], v[172:175], v[84:87]\n\t" \
  "ds_read_b128 v[152:155], v230 offset:14336\n\t" \
  "v_add_u32 v21, s76, v242\n\t" \
  "v_mfma_f32_16x16x32_bf16 v[84:87], a[180:183], v[168:171], v[84:87]\n\t" \
  "buffer_load_dwordx4 v242, s[16:19], s97 offen offset:2048 sc1 lds\n\t" \
  "v_mfma_f32_16x16x32_bf16 v[84:87], a[172:175], v[164:167], v[84:87]\n\t" \
  "s_waitcnt vmcnt(16) lgkmcnt(3)\n\t" \
  "ds_write_b128 v21, v[16:19] offset:0\n\t" \
  "v_mfma_f32_16x16x32_bf16 v[84:87], a[164:167], v[160:163], v[84:87]\n\t" \
  "ds_write_b128 v21, v[16:19] offset:1024\n\t" \
  "s_barrier\n\t" \
  "v_mfma_f32_16x16x32_bf16 v[72:75], a[160:163], v[128:131], v[72:75]\n\t" \
  "buffer_load_dwordx4 v242, s[16:19], s97 offen offset:3072 sc1 lds\n\t" \
  "v_mfma_f32_16x16x32_bf16 v[72:75], a[168:171], v[132:135], v[72:75]\n\t" \
  "ds_read_b128 v[156:159], v230 offset:15360\n\t" \
  "v_mfma_f32_16x16x32_bf16 v[72:75], a[176:179], v[136:139], v[72:75]\n\t" \
  "ds_read_b128 v[160:163], v230 offset:49152\n\t" \
  "v_mfma_f32_16x16x32_bf16 v[72:75], a[184:187], v[140:143], v[72:75]\n\t" \
  "ds_write_b128 v21, v[16:19] offset:1536\n\t" \
  "v_mfma_f32_16x16x32_bf16 v[88:91], a[188:191], v[140:143], v[88:91]\n\t" \
  "buffer_store_dwordx4 v[12:15], v242, s[16:19], s68 offen offset:3072 sc1\n\t" \
  "ds_read_b128 v[164:167], v230 offset:50176\n\t" \
  "ds_read_b128 v[168:171], v230 offset:51200\n\t" \
  "ds_read_b128 v[172:175], v230 offset:52224\n\t" \
  "v_mfma_f32_16x16x32_bf16 v[88:91], a[180:183], v[136:139], v[88:91]\n\t" \
  "s_mov_b64 exec, 0xffff\n\t" \
  "ds_write_b32 v237, v20 offset:960\n\t" \
  "s_mov_b64 exec, -1\n\t" \
  "v_mfma_f32_16x16x32_bf16 v[88:91], a[172:175], v[132:135], v[88:91]\n\t" \
  "v_mfma_f32_16x16x32_bf16 v[88:91], a[164:167], v[128:131], v[88:91]\n\t" \
  "s_waitcnt lgkmcnt(6)\n\t" \
  "v_mfma_f32_16x16x32_bf16 v[76:79], a[160:163], v[144:147], v[76:79]\n\t" \
  "ds_read_b128 v[128:131], v230 offset:53248\n\t" \
  "v_mfma_f32_16x16x32_bf16 v[76:79], a[168:171], v[148:151], v[76:79]\n\t" \
  "ds_read_b128 v[132:135], v230 offset:54272\n\t" \
  "v_mfma_f32_16x16x32_bf16 v[76:79], a[176:179], v[152:155], v[76:79]\n\t" \
  "s_add_u32 m0, s38, 0\n\t" \
  "ds_read_b128 v[136:139], v230 offset:55296\n\t" \
  "s_add_u32 s96, s50, 0\n\t" \
  "v_mfma_f32_16x16x32_bf16 v[76:79], a[184:187], v[156:159], v[76:79]\n\t" \
  "ds_read_b128 v[140:143], v230 offset:56320\n\t" \
  "v_mfma_f32_16x16x32_bf16 v[92:95], a[188:191], v[156:159], v[92:95]\n\t" \
  "v_mfma_f32_16x16x32_bf16 v[92:95], a[180:183], v[152:155], v[92:95]\n\t" \
  "v_mfma_f32_16x16x32_bf16 v[92:95], a[172:175], v[148:151], v[92:95]\n\t" \
  "v_mfma_f32_16x16x32_bf16 v[92:95], a[164:167], v[144:147], v[92:95]\n\t" \
  "s_waitcnt lgkmcnt(5)\n\t" \
  "v_mfma_f32_16x16x32_bf16 v[64:67], a[0:3], v[160:163], v[64:67]\n\t" \
  "buffer_load_dwordx4 v26, s[4:7], s96 offen offset:0 lds\n\t" \
  "v_mfma_f32_16x16x32_bf16 v[64:67], a[8:11], v[164:167], v[64:67]\n\t" \
  "s_add_u32 s96, s50, 7168\n\t" \
  "ds_read_b128 v[144:147], v230 offset:57344\n\t" \
  "v_mfma_f32_16x16x32_bf16 v[64:67], a[16:19], v[168:171], v[64:67]\n\t" \
  "ds_read_b128 v[148:151], v230 offset:58368\n\t" \
  "v_mfma_f32_16x16x32_bf16 v[64:67], a[24:27], v[172:175], v[64:67]\n\t" \
  "ds_read_b128 v[152:155], v230 offset:59392\n\t" \
  "v_mfma_f32_16x16x32_bf16 v[80:83], a[28:31], v[172:175], v[80:83]\n\t" \
  "ds_read_b128 v[156:159], v230 offset:60416\n\t" \
  "v_mfma_f32_16x16x32_bf16 v[80:83], a[20:23], v[168:171], v[80:83]\n\t" \
  "buffer_load_dwordx4 v27, s[4:7], s96 offen offset:1024 lds\n\t" \
  "v_mfma_f32_16x16x32_bf16 v[80:83], a[12:15], v[164:167], v[80:83]\n\t" \
  "s_add_u32 s96, s50, 14336\n\t" \
  "v_mfma_f32_16x16x32_bf16 v[80:83], a[4:7], v[160:163], v[80:83]\n\t" \
  "s_waitcnt lgkmcnt(4)\n\t" \
  "v_mfma_f32_16x16x32_bf16 v[68:71], a[0:3], v[128:131], v[68:71]\n\t" \
  "ds_read_b128 v[160:163], v230 offset:61440\n\t" \
  "v_mfma_f32_16x16x32_bf16 v[68:71], a[8:11], v[132:135], v[68:71]\n\t" \
  "ds_read_b128 v[164:167], v230 offset:62464\n\t" \
  "v_mfma_f32_16x16x32_bf16 v[68:71], a[16:19], v[136:139], v[68:71]\n\t" \
  "buffer_load_dwordx4 v28, s[4:7], s96 offen offset:2048 lds\n\t" \
  "v_mfma_f32_16x16x32_bf16 v[68:71], a[24:27], v[140:143], v[68:71]\n\t" \
  "s_add_u32 s96, s50, 21504\n\t" \
  "ds_read_b128 v[168:171], v230 offset:63488\n\t" \
  "v_mfma_f32_16x16x32_bf16 v[84:87], a[28:31], v[140:143], v[84:87]\n\t" \
  "ds_read_b128 v[172:175], v230 offset:64512\n\t" \
  "v_mfma_f32_16x16x32_bf16 v[84:87], a[20:23], v[136:139], v[84:87]\n\t" \
  "v_mfma_f32_16x16x32_bf16 v[84:87], a[12:15], v[132:135], v[84:87]\n\t" \
  "v_mfma_f32_16x16x32_bf16 v[84:87], a[4:7], v[128:131], v[84:87]\n\t" \
  "s_waitcnt lgkmcnt(4)\n\t" \
  "buffer_load_dwordx4 v29, s[4:7], s96 offen offset:3072 lds\n\t" \
  "v_mfma_f32_16x16x32_bf16 v[72:75], a[0:3], v[144:147], v[72:75]\n\t" \
  "s_mov_b32 s40, 0x05040100\n\t" \
  "ds_read_b128 v[128:131], v230 offset:16384\n\t" \
  "s_mov_b32 s41, 0x07060302\n\t" \
  "v_mfma_f32_16x16x32_bf16 v[72:75], a[8:11], v[148:151], v[72:75]\n\t" \
  "s_load_dwordx4 s[4:7], s[0:1], 0xc0\n\t" \
  "ds_read_b128 v[132:135], v230 offset:17408\n\t" \
  "s_and_b32 s42, s2, 4\n\t" \
  "v_mfma_f32_16x16x32_bf16 v[72:75], a[16:19], v[152:155], v[72:75]\n\t" \
  "s_add_u32 s97, s64, 917504\n\t" \
  "s_and_b32 s97, s97, 0xe0000\n\t" \
  "s_add_u32 s97, s97, s62\n\t" \
  "v_mfma_f32_16x16x32_bf16 v[72:75], a[24:27], v[156:159], v[72:75]\n\t" \
  "s_add_u32 m0, s38, 16384\n\t" \
  "ds_read_b128 v[136:139], v230 offset:18432\n\t" \
  "s_lshl_b32 s9, s42, 11\n\t" \
  "v_mfma_f32_16x16x32_bf16 v[88:91], a[28:31], v[156:159], v[88:91]\n\t" \
  "buffer_load_dwordx4 v242, s[16:19], s97 offen offset:0 sc1 lds\n\t" \
  "v_mfma_f32_16x16x32_bf16 v[88:91], a[20:23], v[152:155], v[88:91]\n\t" \
  "s_xor_b32 s9, s9, s55\n\t" \
  "ds_read_b128 v[140:143], v230 offset:19456\n\t" \
  "s_lshr_b32 s43, s2, 6\n\t" \
  "v_mfma_f32_16x16x32_bf16 v[88:91], a[12:15], v[148:151], v[88:91]\n\t" \
  "s_add_u32 s42, s42, s43\n\t" \
  "s_and_b32 s43, s2, 3\n\t" \
  "s_lshl2_add_u32 s8, s42, s43\n\t" \
  "v_mfma_f32_16x16x32_bf16 v[88:91], a[4:7], v[144:147], v[88:91]\n\t" \
  "s_waitcnt lgkmcnt(4)\n\t" \
  "s_and_b32 s11, s8, 15\n\t" \
  "s_lshl_b32 s11, s11, 15\n\t" \
  "v_mfma_f32_16x16x32_bf16 v[76:79], a[0:3], v[160:163], v[76:79]\n\t" \
  "buffer_load_dwordx4 v242, s[16:19], s97 offen offset:1024 sc1 lds\n\t" \
  "v_mfma_f32_16x16x32_bf16 v[76:79], a[8:11], v[164:167], v[76:79]\n\t" \
  "s_add_u32 s11, s11, s9\n\t" \
  "ds_read_b128 v[144:147], v230 offset:20480\n\t" \
  "s_add_u32 s11, s11, 524288\n\t" \
  "v_mfma_f32_16x16x32_bf16 v[76:79], a[16:19], v[168:171], v[76:79]\n\t" \
  "s_waitcnt vmcnt(10)\n\t" \
  "v_xor_b32 v246, v243, v209\n\t" \
  "v_bitop3_b32 v246, v246, v211, v243 bitop3:0xf6\n\t" \
  "v_mfma_f32_16x16x32_bf16 v[76:79], a[24:27], v[172:175], v[76:79]\n\t" \
  "s_and_b32 s42, s2, 0x20\n\t" \
  "v_bitop3_b32 v246, v246, v213, v243 bitop3:0xf6\n\t" \
  "v_bitop3_b32 v246, v246, v215, v243 bitop3:0xf6\n\t" \
  "v_mfma_f32_16x16x32_bf16 v[92:95], a[28:31], v[172:175], v[92:95]\n\t" \
  "s_lshl_b32 s42, s42, 9\n\t" \
  "v_bitop3_b32 v246, v246, v217, v243 bitop3:0xf6\n\t" \
  "v_bitop3_b32 v246, v246, v219, v243 bitop3:0xf6\n\t" \
  "v_mfma_f32_16x16x32_bf16 v[92:95], a[20:23], v[168:171], v[92:95]\n\t" \
  "s_add_u32 s10, s11, s42\n\t" \
  "v_bitop3_b32 v246, v246, v221, v243 bitop3:0xf6\n\t" \
  "v_bitop3_b32 v246, v246, v223, v243 bitop3:0xf6\n\t" \
  "v_mfma_f32_16x16x32_bf16 v[92:95], a[12:15], v[164:167], v[92:95]\n\t" \
  "buffer_load_dwordx4 v242, s[16:19], s97 offen offset:2048 sc1 lds\n\t" \
  "v_mfma_f32_16x16x32_bf16 v[92:95], a[4:7], v[160:163], v[92:95]\n\t" \
  "s_waitcnt lgkmcnt(1)\n\t" \
  "ds_read_b128 v[148:151], v230 offset:21504\n\t" \
  "v_cmp_ne_u32 vcc, 0, v246\n\t" \
  "v_mfma_f32_16x16x32_bf16 v[64:67], a[32:35], v[128:131], v[64:67]\n\t" \
  "buffer_load_dwordx4 v242, s[16:19], s97 offen offset:3072 sc1 lds\n\t" \
  "ds_read_b128 v[152:155], v230 offset:22528\n\t" \
  "s_and_b32 s42, s2, 0x18\n\t" \
  "v_mfma_f32_16x16x32_bf16 v[64:67], a[40:43], v[132:135], v[64:67]\n\t" \
  "s_add_u32 s97, s64, 131072\n\t" \
  "s_and_b32 s97, s97, 0xe0000\n\t" \
  "s_add_u32 s97, s97, s63\n\t" \
  "v_mfma_f32_16x16x32_bf16 v[64:67], a[48:51], v[136:139], v[64:67]\n\t" \
  "s_cbranch_vccnz .Lat_s3\n\t" \
  ".Lat_k3:\n\t" \
  "ds_read_b128 v[156:159], v230 offset:23552\n\t" \
  "s_add_u32 m0, s38, 32768\n\t" \
  "v_mfma_f32_16x16x32_bf16 v[64:67], a[56:59], v[140:143], v[64:67]\n\t" \
  "buffer_load_dwordx4 v242, s[16:19], s97 offen offset:0 sc1 lds\n\t" \
  "v_mfma_f32_16x16x32_bf16 v[80:83], a[60:63], v[140:143], v[80:83]\n\t" \
  "s_lshl_b32 s42, s42, 8\n\t" \
  "ds_read_b128 v[160:163], v230 offset:24576\n\t" \
  "v_add_f32 v208, v208, v212\n\t" \
  "v_mfma_f32_16x16x32_bf16 v[80:83], a[52:55], v[136:139], v[80:83]\n\t" \
  "s_add_u32 s10, s10, s42\n\t" \
  "ds_read_b128 v[164:167], v230 offset:25600\n\t" \
  "v_add_f32 v216, v216, v220\n\t" \
  "v_mfma_f32_16x16x32_bf16 v[80:83], a[44:47], v[132:135], v[80:83]\n\t" \
  "s_mul_i32 s42, s3, 0x3000\n\t" \
  "ds_read_b128 v[168:171], v230 offset:26624\n\t" \
  "v_add_f32 v210, v210, v214\n\t" \
  "v_mfma_f32_16x16x32_bf16 v[80:83], a[36:39], v[128:131], v[80:83]\n\t" \
  "s_waitcnt lgkmcnt(3)\n\t" \
  "ds_read_b128 v[172:175], v230 offset:27648\n\t" \
  "v_add_f32 v218, v218, v222\n\t" \
  "v_mfma_f32_16x16x32_bf16 v[68:71], a[32:35], v[144:147], v[68:71]\n\t" \
  "buffer_load_dwordx4 v242, s[16:19], s97 offen offset:1024 sc1 lds\n\t" \
  "v_mfma_f32_16x16x32_bf16 v[68:71], a[40:43], v[148:151], v[68:71]\n\t" \
  "s_and_b32 s42, s42, 0x1000\n\t" \
  "ds_read_b128 v[128:131], v230 offset:28672\n\t" \
  "v_add_f32 v208, v208, v216\n\t" \
  "v_mfma_f32_16x16x32_bf16 v[68:71], a[48:51], v[152:155], v[68:71]\n\t" \
  "s_add_u32 s11, s11, s42\n\t" \
  "ds_read_b128 v[132:135], v230 offset:29696\n\t" \
  "v_add_f32 v210, v210, v218\n\t" \
  "v_mfma_f32_16x16x32_bf16 v[68:71], a[56:59], v[156:159], v[68:71]\n\t" \
  "s_and_b32 s42, s3, 2\n\t" \
  "v_lshlrev_b32 v19, 16, v224\n\t" \
  "v_and_b32 v20, 0xffff0000, v224\n\t" \
  "v_mfma_f32_16x16x32_bf16 v[84:87], a[60:63], v[156:159], v[84:87]\n\t" \
  "s_lshl_b32 s42, s42, 13\n\t" \
  "ds_read_b128 v[136:139], v230 offset:30720\n\t" \
  "v_cvt_pk_bf16_f32 v18, v208, v210\n\t" \
  "v_mfma_f32_16x16x32_bf16 v[84:87], a[52:55], v[152:155], v[84:87]\n\t" \
  "s_add_u32 s11, s11, s42\n\t" \
  "v_lshlrev_b32 v16, 16, v18\n\t" \
  "v_and_b32 v17, 0xffff0000, v18\n\t" \
  "v_mfma_f32_16x16x32_bf16 v[84:87], a[44:47], v[148:151], v[84:87]\n\t" \
  "s_waitcnt vmcnt(11) lgkmcnt(3)\n\t" \
  "ds_read_b128 v[140:143], v230 offset:31744\n\t" \
  "v_add_f32 v16, v16, v236\n\t" \
  "v_mfma_f32_16x16x32_bf16 v[84:87], a[36:39], v[144:147], v[84:87]\n\t" \
  "s_barrier\n\t" \
  "v_mfma_f32_16x16x32_bf16 v[72:75], a[32:35], v[160:163], v[72:75]\n\t" \
  "buffer_load_dwordx4 v242, s[16:19], s97 offen offset:2048 sc1 lds\n\t" \
  "v_mfma_f32_16x16x32_bf16 v[72:75], a[40:43], v[164:167], v[72:75]\n\t" \
  "s_add_u32 s12, s11, 2048\n\t" \
  "ds_read_b128 v[144:147], v232 offset:0\n\t" \
  "v_add_f32 v17, v17, v236\n\t" \
  "v_mfma_f32_16x16x32_bf16 v[72:75], a[48:51], v[168:171], v[72:75]\n\t" \
  "s_sub_u32 s39, s10, s11\n\t" \
  "ds_read_b128 v[148:151], v232 offset:1024\n\t" \
  "v_cvt_pk_bf16_f32 v18, v16, v17\n\t" \
  "v_mfma_f32_16x16x32_bf16 v[72:75], a[56:59], v[172:175], v[72:75]\n\t" \
  "v_lshlrev_b32 v16, 16, v18\n\t" \
  "v_and_b32 v17, 0xffff0000, v18\n\t" \
  "v_mfma_f32_16x16x32_bf16 v[88:91], a[60:63], v[172:175], v[88:91]\n\t" \
  "ds_read_b128 v[152:155], v232 offset:2048\n\t" \
  "v_mfma_f32_16x16x32_bf16 v[88:91], a[52:55], v[168:171], v[88:91]\n\t" \
  "s_waitcnt vmcnt(11)\n\t" \
  "buffer_store_dword v243, v244, s[16:19], s72 offen offset:16 sc1\n\t" \
  "ds_read_b128 v[156:159], v232 offset:3072\n\t" \
  "v_mfma_f32_16x16x32_bf16 v[88:91], a[44:47], v[164:167], v[88:91]\n\t" \
  "ds_write_b64 v240, v[16:17] offset:2560\n\t" \
  "v_mfma_f32_16x16x32_bf16 v[88:91], a[36:39], v[160:163], v[88:91]\n\t" \
  "s_waitcnt lgkmcnt(5)\n\t" \
  "v_mul_f32 v22, v16, v19\n\t" \
  "v_mul_f32 v23, v17, v20\n\t" \
  "v_mfma_f32_16x16x32_bf16 v[76:79], a[32:35], v[128:131], v[76:79]\n\t" \
  "ds_read_b128 v[160:163], v232 offset:4096\n\t" \
  "v_cvt_pk_bf16_f32 v24, v22, v23\n\t" \
  "v_mfma_f32_16x16x32_bf16 v[76:79], a[40:43], v[132:135], v[76:79]\n\t" \
  "v_add_f32 v18, v22, v23\n\t" \
  "v_lshlrev_b32 v19, 16, v24\n\t" \
  "v_mfma_f32_16x16x32_bf16 v[76:79], a[48:51], v[136:139], v[76:79]\n\t" \
  "ds_read_b128 v[164:167], v232 offset:5120\n\t" \
  "v_and_b32 v20, 0xffff0000, v24\n\t" \
  "v_mfma_f32_16x16x32_bf16 v[76:79], a[56:59], v[140:143], v[76:79]\n\t" \
  "v_add_f32_dpp v18, v18, v18 row_ror:8 row_mask:0xf bank_mask:0xf\n\t" \
  "v_sub_f32 v22, v22, v19\n\t" \
  "v_mfma_f32_16x16x32_bf16 v[92:95], a[60:63], v[140:143], v[92:95]\n\t" \
  "ds_read_b128 v[168:171], v232 offset:6144\n\t" \
  "v_sub_f32 v23, v23, v20\n\t" \
  "v_mfma_f32_16x16x32_bf16 v[92:95], a[52:55], v[136:139], v[92:95]\n\t" \
  "v_add_f32_dpp v18, v18, v18 row_ror:4 row_mask:0xf bank_mask:0xf\n\t" \
  "v_cvt_pk_bf16_f32 v25, v22, v23\n\t" \
  "v_mfma_f32_16x16x32_bf16 v[92:95], a[44:47], v[132:135], v[92:95]\n\t" \
  "s_mov_b32 s84, 0\n\t" \
  "ds_read_b128 v[172:175], v232 offset:7168\n\t" \
  "v_add_f32_dpp v18, v18, v18 row_ror:2 row_mask:0xf bank_mask:0xf\n\t" \
  "v_mfma_f32_16x16x32_bf16 v[92:95], a[36:39], v[128:131], v[92:95]\n\t" \
  "s_waitcnt lgkmcnt(5)\n\t" \
  "buffer_load_dwordx4 v242, s[16:19], s97 offen offset:3072 sc1 lds\n\t" \
  "v_mfma_f32_16x16x32_bf16 v[64:67], a[64:67], v[144:147], v[64:67]\n\t" \
  "s_mov_b32 s85, 0xffff\n\t" \
  "ds_read_b128 v[128:131], v232 offset:8192\n\t" \
  "v_add_f32_dpp v18, v18, v18 row_ror:1 row_mask:0xf bank_mask:0xf\n\t" \
  "v_mfma_f32_16x16x32_bf16 v[64:67], a[72:75], v[148:151], v[64:67]\n\t" \
  "s_mov_b64 exec, s[84:85]\n\t" \
  "ds_write_b32 v237, v24 offset:1040\n\t" \
  "ds_write_b32 v237, v25 offset:1120\n\t" \
  "s_mov_b64 exec, -1\n\t" \
  "v_mfma_f32_16x16x32_bf16 v[64:67], a[80:83], v[152:155], v[64:67]\n\t" \
  "s_add_u32 s97, s64, 262144\n\t" \
  "s_and_b32 s97, s97, 0xe0000\n\t" \
  "s_add_u32 s97, s97, s63\n\t" \
  "v_mfma_f32_16x16x32_bf16 v[64:67], a[88:91], v[156:159], v[64:67]\n\t" \
  "s_add_u32 m0, s38, 65536\n\t" \
  "ds_read_b128 v[132:135], v232 offset:9216\n\t" \
  "v_readlane_b32 s82, v18, 32\n\t" \
  "v_mfma_f32_16x16x32_bf16 v[80:83], a[92:95], v[156:159], v[80:83]\n\t" \
  "buffer_load_dwordx4 v242, s[16:19], s97 offen offset:0 sc1 lds\n\t" \
  "v_mfma_f32_16x16x32_bf16 v[80:83], a[84:87], v[152:155], v[80:83]\n\t" \
  "ds_read_b128 v[136:139], v232 offset:10240\n\t" \
  "v_readlane_b32 s83, v18, 16\n\t" \
  "v_mfma_f32_16x16x32_bf16 v[80:83], a[76:79], v[148:151], v[80:83]\n\t" \
  "ds_read_b128 v[140:143], v232 offset:11264\n\t" \
  "v_and_b32 v235, 16, v242\n\t" \
  "v_mfma_f32_16x16x32_bf16 v[80:83], a[68:71], v[144:147], v[80:83]\n\t" \
  "s_waitcnt lgkmcnt(6)\n\t" \
  "s_cmp_eq_u32 s39, 0\n\t" \
  "s_cselect_b32 s11, s12, s11\n\t" \
  "v_mfma_f32_16x16x32_bf16 v[68:71], a[64:67], v[160:163], v[68:71]\n\t" \
  "ds_read_b128 v[144:147], v232 offset:12288\n\t" \
  "v_mfma_f32_16x16x32_bf16 v[68:71], a[72:75], v[164:167], v[68:71]\n\t" \
  "buffer_load_dwordx4 v242, s[16:19], s97 offen offset:1024 sc1 lds\n\t" \
  "v_mfma_f32_16x16x32_bf16 v[68:71], a[80:83], v[168:171], v[68:71]\n\t" \
  "ds_read_b128 v[148:151], v232 offset:13312\n\t" \
  "v_mfma_f32_16x16x32_bf16 v[68:71], a[88:91], v[172:175], v[68:71]\n\t" \
  "s_cmp_eq_u32 s39, 2048\n\t" \
  "s_cselect_b32 s12, s11, s12\n\t" \
  "s_lshl_b32 s42, s3, 10\n\t" \
  "v_mfma_f32_16x16x32_bf16 v[84:87], a[92:95], v[172:175], v[84:87]\n\t" \
  "buffer_load_dwordx4 v242, s[16:19], s97 offen offset:2048 sc1 lds\n\t" \
  "v_mfma_f32_16x16x32_bf16 v[84:87], a[84:87], v[168:171], v[84:87]\n\t" \
  "s_add_u32 s10, s10, s42\n\t" \
  "ds_read_b128 v[152:155], v232 offset:14336\n\t" \
  "s_lshr_b32 s42, s2, 1\n\t" \
  "v_mfma_f32_16x16x32_bf16 v[84:87], a[76:79], v[164:167], v[84:87]\n\t" \
  "s_and_b32 s42, s42, 12\n\t" \
  "ds_read_b128 v[156:159], v232 offset:15360\n\t" \
  "s_or_b32 s42, s42, s43\n\t" \
  "v_mfma_f32_16x16x32_bf16 v[84:87], a[68:71], v[160:163], v[84:87]\n\t" \
  "s_waitcnt lgkmcnt(4)\n\t" \
  "buffer_load_dwordx4 v242, s[16:19], s97 offen offset:3072 sc1 lds\n\t" \
  "v_mfma_f32_16x16x32_bf16 v[72:75], a[64:67], v[128:131], v[72:75]\n\t" \
  "s_add_u32 s97, s64, 393216\n\t" \
  "s_and_b32 s97, s97, 0xe0000\n\t" \
  "s_add_u32 s97, s97, s63\n\t" \
  "v_mfma_f32_16x16x32_bf16 v[72:75], a[72:75], v[132:135], v[72:75]\n\t" \
  "s_add_u32 m0, s38, 98304\n\t" \
  "ds_read_b128 v[160:163], v242 offset:49152\n\t" \
  "s_lshl_b32 s13, s42, 8\n\t" \
  "v_mfma_f32_16x16x32_bf16 v[72:75], a[80:83], v[136:139], v[72:75]\n\t" \
  "buffer_load_dwordx4 v242, s[16:19], s97 offen offset:0 sc1 lds\n\t" \
  "v_mfma_f32_16x16x32_bf16 v[72:75], a[88:91], v[140:143], v[72:75]\n\t" \
  "s_add_u32 s13, s13, s9\n\t" \
  "ds_read_b128 v[164:167], v242 offset:50176\n\t" \
  "s_add_u32 s13, s13, 65536\n\t" \
  "v_mfma_f32_16x16x32_bf16 v[88:91], a[92:95], v[140:143], v[88:91]\n\t" \
  "s_bfe_u32 s44, s2, 0x30003\n\t" \
  "ds_read_b128 v[168:171], v242 offset:51200\n\t" \
  "s_lshl_b32 s42, s3, 13\n\t" \
  "v_mfma_f32_16x16x32_bf16 v[88:91], a[84:87], v[136:139], v[88:91]\n\t" \
  "buffer_load_dwordx4 v242, s[16:19], s97 offen offset:1024 sc1 lds\n\t" \
  "v_mfma_f32_16x16x32_bf16 v[88:91], a[76:79], v[132:135], v[88:91]\n\t" \
  "s_and_b32 s42, s42, 0x2000\n\t" \
  "ds_read_b128 v[172:175], v242 offset:52224\n\t" \
  "s_xor_b32 s14, s42, s55\n\t" \
  "v_mfma_f32_16x16x32_bf16 v[88:91], a[68:71], v[128:131], v[88:91]\n\t" \
  "s_waitcnt lgkmcnt(4)\n\t" \
  "s_lshr_b32 s42, s3, 1\n\t" \
  "s_lshl1_add_u32 s42, s44, s42\n\t" \
  "v_mfma_f32_16x16x32_bf16 v[76:79], a[64:67], v[144:147], v[76:79]\n\t" \
  "s_lshl_b32 s42, s42, 8\n\t" \
  "ds_read_b128 v[128:131], v242 offset:53248\n\t" \
  "s_add_u32 s14, s14, s42\n\t" \
  "v_mfma_f32_16x16x32_bf16 v[76:79], a[72:75], v[148:151], v[76:79]\n\t" \
  "s_lshl3_add_u32 s14, s8, s14\n\t" \
  "ds_read_b128 v[132:135], v242 offset:54272\n\t" \
  "s_add_u32 s14, s14, 65536\n\t" \
  "v_mfma_f32_16x16x32_bf16 v[76:79], a[80:83], v[152:155], v[76:79]\n\t" \
  "s_add_u32 s15, s9, 262144\n\t" \
  "ds_read_b128 v[136:139], v242 offset:55296\n\t" \
  "s_lshl_b32 s42, s8, 13\n\t" \
  "v_mfma_f32_16x16x32_bf16 v[76:79], a[88:91], v[156:159], v[76:79]\n\t" \
  "s_and_b32 s42, s42, 0x3c000\n\t" \
  "ds_read_b128 v[140:143], v242 offset:56320\n\t" \
  "s_add_u32 s20, s15, s42\n\t" \
  "v_mfma_f32_16x16x32_bf16 v[92:95], a[92:95], v[156:159], v[92:95]\n\t" \
  "buffer_load_dwordx4 v242, s[16:19], s97 offen offset:2048 sc1 lds\n\t" \
  "v_mfma_f32_16x16x32_bf16 v[92:95], a[84:87], v[152:155], v[92:95]\n\t" \
  ".Lhs14:\n\ts_cbranch_execz .Lhs14n\n\t" \
  "s_and_b32 s42, s8, 1\n\t" \
  "s_lshl_b32 s42, s42, 12\n\t" \
  "s_add_u32 s20, s20, s42\n\t" \
  "v_mfma_f32_16x16x32_bf16 v[92:95], a[76:79], v[148:151], v[92:95]\n\t" \
  "s_lshl2_add_u32 s42, s44, s3\n\t" \
  "s_lshl_b32 s42, s42, 7\n\t" \
  "s_add_u32 s20, s20, s42\n\t" \
  "v_mfma_f32_16x16x32_bf16 v[92:95], a[68:71], v[144:147], v[92:95]\n\t" \
  "s_waitcnt lgkmcnt(4)\n\t" \
  "s_and_b32 s21, s42, 0x7ff\n\t" \
  "s_lshl_b32 s22, s8, 17\n\t" \
  "v_mfma_f32_16x16x32_bf16 v[64:67], a[96:99], v[160:163], v[64:67]\n\t" \
  "s_lshr_b32 s42, s42, 1\n\t" \
  "ds_read_b128 v[144:147], v242 offset:57344\n\t" \
  "s_add_u32 s22, s22, s42\n\t" \
  "v_mfma_f32_16x16x32_bf16 v[64:67], a[104:107], v[164:167], v[64:67]\n\t" \
  "buffer_load_dwordx4 v242, s[16:19], s97 offen offset:3072 sc1 lds\n\t" \
  "v_mfma_f32_16x16x32_bf16 v[64:67], a[112:115], v[168:171], v[64:67]\n\t" \
  "s_and_b32 s42, s2, 0x20\n\t" \
  "ds_read_b128 v[148:151], v242 offset:58368\n\t" \
  "s_lshl_b32 s42, s42, 8\n\t" \
  "v_mfma_f32_16x16x32_bf16 v[64:67], a[120:123], v[172:175], v[64:67]\n\t" \
  "s_xor_b32 s42, s42, s55\n\t" \
  "ds_read_b128 v[152:155], v242 offset:59392\n\t" \
  "s_add_u32 s21, s21, s42\n\t" \
  "v_mfma_f32_16x16x32_bf16 v[80:83], a[124:127], v[172:175], v[80:83]\n\t" \
  "s_add_u32 s97, s64, 524288\n\t" \
  "s_and_b32 s97, s97, 0xe0000\n\t" \
  "s_add_u32 s97, s97, s63\n\t" \
  "v_mfma_f32_16x16x32_bf16 v[80:83], a[116:119], v[168:171], v[80:83]\n\t" \
  "s_add_u32 m0, s38, 114688\n\t" \
  "ds_read_b128 v[156:159], v242 offset:60416\n\t" \
  "s_lshl_b32 s42, s43, 11\n\t" \
  "v_mfma_f32_16x16x32_bf16 v[80:83], a[108:111], v[164:167], v[80:83]\n\t" \
  "buffer_load_dwordx4 v242, s[16:19], s97 offen offset:0 sc1 lds\n\t" \
  "v_mfma_f32_16x16x32_bf16 v[80:83], a[100:103], v[160:163], v[80:83]\n\t" \
  "s_waitcnt lgkmcnt(4)\n\t" \
  "s_add_u32 s21, s21, s42\n\t" \
  "s_lshr_b32 s42, s8, 2\n\t" \
  "v_mfma_f32_16x16x32_bf16 v[68:71], a[96:99], v[128:131], v[68:71]\n\t" \
  "s_lshl_b32 s42, s42, 14\n\t" \
  "ds_read_b128 v[160:163], v242 offset:61440\n\t" \
  "s_add_u32 s21, s21, s42\n\t" \
  "v_mfma_f32_16x16x32_bf16 v[68:71], a[104:107], v[132:135], v[68:71]\n\t" \
  "buffer_load_dwordx4 v242, s[16:19], s97 offen offset:1024 sc1 lds\n\t" \
  "v_mfma_f32_16x16x32_bf16 v[68:71], a[112:115], v[136:139], v[68:71]\n\t" \
  "s_add_u32 s21, s21, 1966080\n\t" \
  "ds_read_b128 v[164:167], v242 offset:62464\n\t" \
  "s_lshl_b32 s42, s59, 7\n\t" \
  "v_mfma_f32_16x16x32_bf16 v[68:71], a[120:123], v[140:143], v[68:71]\n\t" \
  "s_and_b32 s23, s42, 0xffffc000\n\t" \
  "ds_read_b128 v[168:171], v242 offset:63488\n\t" \
  "s_lshl_b32 s42, s59, 6\n\t" \
  "v_mfma_f32_16x16x32_bf16 v[84:87], a[124:127], v[140:143], v[84:87]\n\t" \
  "s_and_b32 s42, s42, 0x1800\n\t" \
  "ds_read_b128 v[172:175], v242 offset:64512\n\t" \
  "s_add_u32 s23, s23, s42\n\t" \
  "v_mfma_f32_16x16x32_bf16 v[84:87], a[116:119], v[136:139], v[84:87]\n\t" \
  "s_mul_i32 s42, s3, 0x1400\n\t" \
  "s_and_b32 s42, s42, 0x2400\n\t" \
  "s_xor_b32 s42, s42, s55\n\t" \
  "v_mfma_f32_16x16x32_bf16 v[84:87], a[108:111], v[132:135], v[84:87]\n\t" \
  "s_waitcnt vmcnt(10) lgkmcnt(4)\n\t" \
  "buffer_load_dwordx4 v242, s[16:19], s97 offen offset:2048 sc1 lds\n\t" \
  "v_mfma_f32_16x16x32_bf16 v[84:87], a[100:103], v[128:131], v[84:87]\n\t" \
  "s_barrier\n\t" \
  "v_mfma_f32_16x16x32_bf16 v[72:75], a[96:99], v[144:147], v[72:75]\n\t" \
  "s_add_u32 s23, s23, s42\n\t" \
  "ds_read_b128 v[128:131], v242 offset:16384\n\t" \
  "s_add_u32 s23, s23, 1048576\n\t" \
  "v_mfma_f32_16x16x32_bf16 v[72:75], a[104:107], v[148:151], v[72:75]\n\t" \
  "buffer_load_dwordx4 v242, s[16:19], s97 offen offset:3072 sc1 lds\n\t" \
  "v_mfma_f32_16x16x32_bf16 v[72:75], a[112:115], v[152:155], v[72:75]\n\t" \
  "s_add_u32 s97, s64, 655360\n\t" \
  "s_and_b32 s97, s97, 0xe0000\n\t" \
  "s_add_u32 s97, s97, s63\n\t" \
  "v_mfma_f32_16x16x32_bf16 v[72:75], a[120:123], v[156:159], v[72:75]\n\t" \
  "s_add_u32 m0, s38, 81920\n\t" \
  "ds_read_b128 v[132:135], v242 offset:17408\n\t" \
  "v_mfma_f32_16x16x32_bf16 v[88:91], a[124:127], v[156:159], v[88:91]\n\t" \
  "buffer_load_dwordx4 v242, s[16:19], s97 offen offset:0 sc1 lds\n\t" \
  "v_mfma_f32_16x16x32_bf16 v[88:91], a[116:119], v[152:155], v[88:91]\n\t" \
  "ds_read_b128 v[136:139], v242 offset:18432\n\t" \
  "v_mfma_f32_16x16x32_bf16 v[88:91], a[108:111], v[148:151], v[88:91]\n\t" \
  "ds_read_b128 v[140:143], v242 offset:19456\n\t" \
  "v_mfma_f32_16x16x32_bf16 v[88:91], a[100:103], v[144:147], v[88:91]\n\t" \
  "s_waitcnt lgkmcnt(4)\n\t" \
  "buffer_load_dwordx4 v242, s[16:19], s97 offen offset:1024 sc1 lds\n\t" \
  "v_mfma_f32_16x16x32_bf16 v[76:79], a[96:99], v[160:163], v[76:79]\n\t" \
  "ds_read_b128 v[144:147], v242 offset:20480\n\t" \
  "v_mfma_f32_16x16x32_bf16 v[76:79], a[104:107], v[164:167], v[76:79]\n\t" \
  "ds_read_b128 v[148:151], v242 offset:21504\n\t" \
  "v_mfma_f32_16x16x32_bf16 v[76:79], a[112:115], v[168:171], v[76:79]\n\t" \
  "buffer_load_dwordx4 v242, s[16:19], s97 offen offset:2048 sc1 lds\n\t" \
  "v_mfma_f32_16x16x32_bf16 v[76:79], a[120:123], v[172:175], v[76:79]\n\t" \
  "ds_read_b128 v[152:155], v242 offset:22528\n\t" \
  "v_mfma_f32_16x16x32_bf16 v[92:95], a[124:127], v[172:175], v[92:95]\n\t" \
  "ds_read_b128 v[156:159], v242 offset:23552\n\t" \
  "v_mfma_f32_16x16x32_bf16 v[92:95], a[116:119], v[168:171], v[92:95]\n\t" \
  "buffer_load_dwordx4 v242, s[16:19], s97 offen offset:3072 sc1 lds\n\t" \
  "v_mfma_f32_16x16x32_bf16 v[92:95], a[108:111], v[164:167], v[92:95]\n\t" \
  "s_add_u32 s97, s64, 786432\n\t" \
  "s_and_b32 s97, s97, 0xe0000\n\t" \
  "s_add_u32 s97, s97, s63\n\t" \
  "v_mfma_f32_16x16x32_bf16 v[92:95], a[100:103], v[160:163], v[92:95]\n\t" \
  "s_waitcnt lgkmcnt(4)\n\t" \
  "s_add_u32 m0, s38, 131072\n\t" \
  "v_mfma_f32_16x16x32_bf16 v[64:67], a[192:195], v[128:131], v[64:67]\n\t" \
  "buffer_load_dwordx4 v242, s[16:19], s97 offen offset:0 sc1 lds\n\t" \
  "v_mfma_f32_16x16x32_bf16 v[64:67], a[200:203], v[132:135], v[64:67]\n\t" \
  "ds_read_b128 v[160:163], v242 offset:24576\n\t" \
  "v_mfma_f32_16x16x32_bf16 v[64:67], a[208:211], v[136:139], v[64:67]\n\t" \
  "ds_read_b128 v[164:167], v242 offset:25600\n\t" \
  "v_mfma_f32_16x16x32_bf16 v[64:67], a[216:219], v[140:143], v[64:67]\n\t" \
  "buffer_load_dwordx4 v242, s[16:19], s97 offen offset:1024 sc1 lds\n\t" \
  "v_mfma_f32_16x16x32_bf16 v[80:83], a[220:223], v[140:143], v[80:83]\n\t" \
  "ds_read_b128 v[168:171], v242 offset:26624\n\t" \
  "v_mfma_f32_16x16x32_bf16 v[80:83], a[212:215], v[136:139], v[80:83]\n\t" \
  "ds_read_b128 v[172:175], v242 offset:27648\n\t" \
  "v_mfma_f32_16x16x32_bf16 v[80:83], a[204:207], v[132:135], v[80:83]\n\t" \
  "buffer_load_dwordx4 v242, s[16:19], s97 offen offset:2048 sc1 lds\n\t" \
  "v_mfma_f32_16x16x32_bf16 v[80:83], a[196:199], v[128:131], v[80:83]\n\t" \
  "s_waitcnt lgkmcnt(4)\n\t" \
  "v_cvt_pk_bf16_f32 v0, v64, v65\n\t" \
  "v_cvt_pk_bf16_f32 v1, v66, v67\n\t" \
  "v_mfma_f32_16x16x32_bf16 v[68:71], a[192:195], v[144:147], v[68:71]\n\t" \
  "buffer_load_dword v246, v235, s[16:19], s73 offen sc1\n\t" \
  "v_mfma_f32_16x16x32_bf16 v[68:71], a[200:203], v[148:151], v[68:71]\n\t" \
  "buffer_load_dwordx4 v242, s[16:19], s97 offen offset:3072 sc1 lds\n\t" \
  "v_mfma_f32_16x16x32_bf16 v[68:71], a[208:211], v[152:155], v[68:71]\n\t" \
  "ds_read_b128 v[128:131], v242 offset:28672\n\t" \
  "v_cvt_pk_bf16_f32 v2, v80, v81\n\t" \
  "v_mfma_f32_16x16x32_bf16 v[68:71], a[216:219], v[156:159], v[68:71]\n\t" \
  "ds_read_b128 v[132:135], v242 offset:29696\n\t" \
  "v_cvt_pk_bf16_f32 v3, v82, v83\n\t" \
  "v_mfma_f32_16x16x32_bf16 v[84:87], a[220:223], v[156:159], v[84:87]\n\t" \
  "ds_read_b128 v[136:139], v242 offset:30720\n\t" \
  "v_mov_b32 v250, 0x3f80\n\t" \
  "v_mfma_f32_16x16x32_bf16 v[84:87], a[212:215], v[152:155], v[84:87]\n\t" \
  "ds_read_b128 v[140:143], v242 offset:31744\n\t" \
  "v_lshlrev_b32 v250, v235, v250\n\t" \
  "v_mfma_f32_16x16x32_bf16 v[84:87], a[204:207], v[148:151], v[84:87]\n\t" \
  "s_mov_b32 s84, 0x300003\n\t" \
  "s_mov_b32 s85, 0x30000300\n\t" \
  "s_mov_b32 s86, 0xc0000c\n\t" \
  "v_mfma_f32_16x16x32_bf16 v[84:87], a[196:199], v[144:147], v[84:87]\n\t" \
  "s_waitcnt lgkmcnt(4)\n\t" \
  "v_cvt_pk_bf16_f32 v4, v68, v69\n\t" \
  "v_cvt_pk_bf16_f32 v5, v70, v71\n\t" \
  "v_mfma_f32_16x16x32_bf16 v[72:75], a[192:195], v[160:163], v[72:75]\n\t" \
  "buffer_store_dwordx4 v[0:3], v242, s[16:19], s69 offen offset:0 sc1\n\t" \
  "ds_read_b128 v[144:147], v242 offset:32768\n\t" \
  "ds_read_b128 v[148:151], v242 offset:33792\n\t" \
  "ds_read_b128 v[152:155], v242 offset:34816\n\t" \
  "v_mfma_f32_16x16x32_bf16 v[72:75], a[200:203], v[164:167], v[72:75]\n\t" \
  "s_mov_b32 s87, 0xc0000c00\n\t" \
  "v_cvt_pk_bf16_f32 v6, v84, v85\n\t" \
  "v_cvt_pk_bf16_f32 v7, v86, v87\n\t" \
  "v_mfma_f32_16x16x32_bf16 v[72:75], a[208:211], v[168:171], v[72:75]\n\t" \
  "v_cndmask_b32 v248, 0, v250, s[84:85]\n\t" \
  "v_cndmask_b32 v249, 0, v250, s[86:87]\n\t" \
  "v_mfma_f32_16x16x32_bf16 v[72:75], a[216:219], v[172:175], v[72:75]\n\t" \
  "v_and_b32 v236, 15, v233\n\t" \
  "v_lshrrev_b32 v250, 4, v233\n\t" \
  "v_mfma_f32_16x16x32_bf16 v[88:91], a[220:223], v[172:175], v[88:91]\n\t" \
  "v_mul_u32_u24 v236, 80, v236\n\t" \
  "v_lshlrev_b32 v237, 4, v250\n\t" \
  "v_mfma_f32_16x16x32_bf16 v[88:91], a[212:215], v[168:171], v[88:91]\n\t" \
  "v_add_u32 v237, s76, v237\n\t" \
  "v_add_u32 v236, v237, v236\n\t" \
  "v_mfma_f32_16x16x32_bf16 v[88:91], a[204:207], v[164:167], v[88:91]\n\t" \
  "s_add_u32 s78, s70, 4096\n\t" \
  "ds_read_b128 v[156:159], v242 offset:35840\n\t" \
  "s_add_u32 s79, s70, 16384\n\t" \
  "v_mfma_f32_16x16x32_bf16 v[88:91], a[196:199], v[160:163], v[88:91]\n\t" \
  "s_waitcnt lgkmcnt(4)\n\t" \
  "v_cvt_pk_bf16_f32 v8, v72, v73\n\t" \
  "v_cvt_pk_bf16_f32 v9, v74, v75\n\t" \
  "v_mfma_f32_16x16x32_bf16 v[76:79], a[192:195], v[128:131], v[76:79]\n\t" \
  "buffer_store_dwordx4 v[4:7], v242, s[16:19], s69 offen offset:1024 sc1\n\t" \
  "ds_read_b128 v[160:163], v242 offset:36864\n\t" \
  "ds_read_b128 v[164:167], v242 offset:37888\n\t" \
  "ds_read_b128 v[168:171], v242 offset:38912\n\t" \
  "v_mfma_f32_16x16x32_bf16 v[76:79], a[200:203], v[132:135], v[76:79]\n\t" \
  "v_cvt_pk_bf16_f32 v10, v88, v89\n\t" \
  "v_cvt_pk_bf16_f32 v11, v90, v91\n\t" \
  "v_mfma_f32_16x16x32_bf16 v[76:79], a[208:211], v[136:139], v[76:79]\n\t" \
  "ds_read_b128 v[172:175], v242 offset:39936\n\t" \
  "v_mfma_f32_16x16x32_bf16 v[76:79], a[216:219], v[140:143], v[76:79]\n\t" \
  "v_mfma_f32_16x16x32_bf16 v[92:95], a[220:223], v[140:143], v[92:95]\n\t" \
  "v_mfma_f32_16x16x32_bf16 v[92:95], a[212:215], v[136:139], v[92:95]\n\t" \
  "v_mfma_f32_16x16x32_bf16 v[92:95], a[204:207], v[132:135], v[92:95]\n\t" \
  "s_nop 2\n\t" \
  "v_mfma_f32_16x16x32_bf16 v[92:95], a[196:199], v[128:131], v[92:95]\n\t" \
  "s_waitcnt lgkmcnt(4)\n\t" \
  "v_cvt_pk_bf16_f32 v12, v76, v77\n\t" \
  "v_cvt_pk_bf16_f32 v13, v78, v79\n\t" \
  STP("s_memrealtime s[94:95]\n\t") \
  STP("s_memtime s[92:93]\n\t") \
  "v_mfma_f32_16x16x32_bf16 v[96:99], a[128:131], v[144:147], v[96:99]\n\t" \
  "buffer_store_dwordx4 v[8:11], v242, s[16:19], s69 offen offset:2048 sc1\n\t" \
  "ds_read_b128 v[128:131], v242 offset:40960\n\t" \
  "ds_read_b128 v[132:135], v242 offset:41984\n\t" \
  "ds_read_b64 v[208:209], v238 offset:0\n\t" \
  "v_mfma_f32_16x16x32_bf16 v[96:99], a[136:139], v[148:151], v[96:99]\n\t" \
  "v_cvt_pk_bf16_f32 v14, v92, v93\n\t" \
  "v_cvt_pk_bf16_f32 v15, v94, v95\n\t" \
  "v_mfma_f32_16x16x32_bf16 v[96:99], a[144:147], v[152:155], v[96:99]\n\t" \
  "ds_read_b64 v[210:211], v238 offset:4096\n\t" \
  "v_mfma_f32_16x16x32_bf16 v[96:99], a[152:155], v[156:159], v[96:99]\n\t" \
  "ds_read_b128 v[136:139], v242 offset:43008\n\t" \
  "v_mfma_f32_16x16x32_bf16 v[112:115], a[156:159], v[156:159], v[112:115]\n\t" \
  "ds_read_b64 v[212:213], v238 offset:8192\n\t" \
  "v_mfma_f32_16x16x32_bf16 v[112:115], a[148:151], v[152:155], v[112:115]\n\t" \
  "ds_read_b128 v[140:143], v242 offset:44032\n\t" \
  "v_mfma_f32_16x16x32_bf16 v[112:115], a[140:143], v[148:151], v[112:115]\n\t" \
  "ds_read_b64 v[214:215], v238 offset:12288\n\t" \
  "v_mfma_f32_16x16x32_bf16 v[112:115], a[132:135], v[144:147], v[112:115]\n\t" \
  "s_waitcnt lgkmcnt(8)\n\t" \
  "ds_read_b64 v[216:217], v239 offset:0\n\t" \
  "v_mfma_f32_16x16x32_bf16 v[100:103], a[128:131], v[160:163], v[100:103]\n\t" \
  "buffer_store_dwordx4 v[12:15], v242, s[16:19], s69 offen offset:3072 sc1\n\t" \
  "ds_read_b128 v[144:147], v242 offset:45056\n\t" \
  "ds_read_b128 v[148:151], v242 offset:46080\n\t" \
  "ds_read_b128 v[152:155], v242 offset:47104\n\t" \
  "v_mfma_f32_16x16x32_bf16 v[100:103], a[136:139], v[164:167], v[100:103]\n\t" \
  "ds_read_b128 v[156:159], v242 offset:48128\n\t" \
  "v_mfma_f32_16x16x32_bf16 v[100:103], a[144:147], v[168:171], v[100:103]\n\t" \
  "ds_read_b64 v[218:219], v239 offset:4096\n\t" \
  "v_mfma_f32_16x16x32_bf16 v[100:103], a[152:155], v[172:175], v[100:103]\n\t" \
  "ds_read_b64 v[220:221], v239 offset:8192\n\t" \
  "v_mfma_f32_16x16x32_bf16 v[116:119], a[156:159], v[172:175], v[116:119]\n\t" \
  STP("v_writelane_b32 v255, s94, 36\n\t") \
  STP("v_writelane_b32 v255, s92, 37\n\t") \
  "ds_read_b64 v[222:223], v239 offset:12288\n\t" \
  "v_mfma_f32_16x16x32_bf16 v[116:119], a[148:151], v[168:171], v[116:119]\n\t" \
  "s_waitcnt lgkmcnt(0)\n\t" \
  "v_lshlrev_b32 v0, 16, v208\n\t" \
  "v_lshl_add_u32 v224, s3, 10, v242\n\t" \
  "v_mfma_f32_16x16x32_bf16 v[116:119], a[140:143], v[164:167], v[116:119]\n\t" \
  "s_waitcnt vmcnt(17)\n\t" \
  "v_and_b32 v225, 31, v233\n\t" \
  "v_lshlrev_b32 v245, 2, v225\n\t" \
  "v_mfma_f32_16x16x32_bf16 v[116:119], a[132:135], v[160:163], v[116:119]\n\t" \
  "s_barrier\n\t" \
  "v_mfma_f32_16x16x32_bf16 v[104:107], a[128:131], v[128:131], v[104:107]\n\t" \
  "ds_read_b128 v[160:163], v230 offset:0\n\t" \
  "v_lshlrev_b32 v225, 3, v225\n\t" \
  "v_mfma_f32_16x16x32_bf16 v[104:107], a[136:139], v[132:135], v[104:107]\n\t" \
  "v_and_b32 v226, 0xffffffe0, v224\n\t" \
  "v_lshl_add_u32 v227, s8, 6, v233\n\t" \
  "v_mfma_f32_16x16x32_bf16 v[104:107], a[144:147], v[136:139], v[104:107]\n\t" \
  "ds_read_b128 v[164:167], v230 offset:1024\n\t" \
  "v_sub_u32 v227, 0x7ff, v227\n\t" \
  "v_mfma_f32_16x16x32_bf16 v[104:107], a[152:155], v[140:143], v[104:107]\n\t" \
  "v_and_b32 v228, 0x30, v233\n\t" \
  "v_and_b32 v239, 15, v233\n\t" \
  "v_mfma_f32_16x16x32_bf16 v[120:123], a[156:159], v[140:143], v[120:123]\n\t" \
  "s_add_u32 s97, s64, 917504\n\t" \
  "ds_read_b128 v[168:171], v230 offset:2048\n\t" \
  "v_and_b32 v241, 16, v233\n\t" \
  "v_mfma_f32_16x16x32_bf16 v[120:123], a[148:151], v[136:139], v[120:123]\n\t" \
  "s_and_b32 s97, s97, 0xe0000\n\t" \
  "v_lshlrev_b32 v231, 11, v239\n\t" \
  "v_lshl_add_u32 v231, v241, 1, v231\n\t" \
  "v_mfma_f32_16x16x32_bf16 v[120:123], a[140:143], v[132:135], v[120:123]\n\t" \
  "s_add_u32 s97, s97, s63\n\t" \
  "ds_read_b128 v[172:175], v230 offset:3072\n\t" \
  "v_lshrrev_b32 v239, 5, v233\n\t" \
  "v_mfma_f32_16x16x32_bf16 v[120:123], a[132:135], v[128:131], v[120:123]\n\t" \
  "s_add_u32 m0, s38, 16384\n\t" \
  "v_lshl_add_u32 v231, v239, 4, v231\n\t" \
  "v_lshl_add_u32 v253, v239, 6, v241\n\t" \
  "v_mfma_f32_16x16x32_bf16 v[108:111], a[128:131], v[144:147], v[108:111]\n\t" \
  "ds_read_b128 v[128:131], v230 offset:4096\n\t" \
  "v_and_b32 v239, 1, v233\n\t" \
  "v_mfma_f32_16x16x32_bf16 v[108:111], a[136:139], v[148:151], v[108:111]\n\t" \
  "v_lshl_add_u32 v253, v239, 5, v253\n\t" \
  "v_lshl_add_u32 v253, s3, 7, v253\n\t" \
  "v_mfma_f32_16x16x32_bf16 v[108:111], a[144:147], v[152:155], v[108:111]\n\t" \
  "buffer_load_dwordx4 v242, s[16:19], s97 offen offset:0 sc1 lds\n\t" \
  "v_mfma_f32_16x16x32_bf16 v[108:111], a[152:155], v[156:159], v[108:111]\n\t" \
  "ds_read_b128 v[132:135], v230 offset:5120\n\t" \
  "v_and_b32 v239, 0x800, v224\n\t" \
  "v_mfma_f32_16x16x32_bf16 v[124:127], a[156:159], v[156:159], v[124:127]\n\t" \
  "ds_read_b128 v[136:139], v230 offset:6144\n\t" \
  "v_lshl_or_b32 v240, v239, 20, v242\n\t" \
  "v_mfma_f32_16x16x32_bf16 v[124:127], a[148:151], v[152:155], v[124:127]\n\t" \
  "v_mov_b32 v238, s82\n\t" \
  "v_writelane_b32 v238, s83, 1\n\t" \
  "v_mfma_f32_16x16x32_bf16 v[124:127], a[140:143], v[148:151], v[124:127]\n\t" \
  "buffer_load_dwordx4 v242, s[16:19], s97 offen offset:1024 sc1 lds\n\t" \
  "v_mfma_f32_16x16x32_bf16 v[124:127], a[132:135], v[144:147], v[124:127]\n\t" \
  "s_waitcnt lgkmcnt(3)\n\t" \
  "ds_read_b128 v[140:143], v230 offset:7168\n\t" \
  "v_perm_b32 v239, v180, v176, s40\n\t" \
  "v_mfma_f32_16x16x32_bf16 v[96:99], a[160:163], v[160:163], v[96:99]\n\t" \
  "ds_read_b128 v[144:147], v230 offset:8192\n\t" \
  "v_perm_b32 v180, v180, v176, s41\n\t" \
  "v_mfma_f32_16x16x32_bf16 v[96:99], a[168:171], v[164:167], v[96:99]\n\t" \
  "buffer_load_dwordx4 v242, s[16:19], s97 offen offset:2048 sc1 lds\n\t" \
  "v_mfma_f32_16x16x32_bf16 v[96:99], a[176:179], v[168:171], v[96:99]\n\t" \
  "ds_read_b128 v[148:151], v230 offset:9216\n\t" \
  "v_mov_b32 v176, v239\n\t" \
  "v_mfma_f32_16x16x32_bf16 v[96:99], a[184:187], v[172:175], v[96:99]\n\t" \
  "v_perm_b32 v239, v181, v177, s40\n\t" \
  "v_perm_b32 v181, v181, v177, s41\n\t" \
  "v_mfma_f32_16x16x32_bf16 v[112:115], a[188:191], v[172:175], v[112:115]\n\t" \
  "ds_read_b128 v[152:155], v230 offset:10240\n\t" \
  "v_mov_b32 v177, v239\n\t" \
  "v_mfma_f32_16x16x32_bf16 v[112:115], a[180:183], v[168:171], v[112:115]\n\t" \
  "buffer_load_dwordx4 v242, s[16:19], s97 offen offset:3072 sc1 lds\n\t" \
  "v_mfma_f32_16x16x32_bf16 v[112:115], a[172:175], v[164:167], v[112:115]\n\t" \
  "ds_read_b128 v[156:159], v230 offset:11264\n\t" \
  "v_perm_b32 v239, v182, v178, s40\n\t" \
  "v_mfma_f32_16x16x32_bf16 v[112:115], a[164:167], v[160:163], v[112:115]\n\t" \
  "s_waitcnt lgkmcnt(4)\n\t" \
  "v_perm_b32 v182, v182, v178, s41\n\t" \
  "v_mov_b32 v178, v239\n\t" \
  "v_mfma_f32_16x16x32_bf16 v[100:103], a[160:163], v[128:131], v[100:103]\n\t" \
  "ds_read_b128 v[160:163], v230 offset:12288\n\t" \
  "v_perm_b32 v239, v183, v179, s40\n\t" \
  "v_mfma_f32_16x16x32_bf16 v[100:103], a[168:171], v[132:135], v[100:103]\n\t" \
  "v_perm_b32 v183, v183, v179, s41\n\t" \
  "v_mov_b32 v179, v239\n\t" \
  "v_mfma_f32_16x16x32_bf16 v[100:103], a[176:179], v[136:139], v[100:103]\n\t" \
  "ds_read_b128 v[164:167], v230 offset:13312\n\t" \
  "v_perm_b32 v239, v188, v184, s40\n\t" \
  "v_mfma_f32_16x16x32_bf16 v[100:103], a[184:187], v[140:143], v[100:103]\n\t" \
  "v_perm_b32 v188, v188, v184, s41\n\t" \
  "v_mov_b32 v184, v239\n\t" \
  "v_mfma_f32_16x16x32_bf16 v[116:119], a[188:191], v[140:143], v[116:119]\n\t" \
  "ds_read_b128 v[168:171], v230 offset:14336\n\t" \
  "v_perm_b32 v239, v189, v185, s40\n\t" \
  "v_mfma_f32_16x16x32_bf16 v[116:119], a[180:183], v[136:139], v[116:119]\n\t" \
  "v_perm_b32 v189, v189, v185, s41\n\t" \
  "v_mov_b32 v185, v239\n\t" \
  "v_mfma_f32_16x16x32_bf16 v[116:119], a[172:175], v[132:135], v[116:119]\n\t" \
  "ds_read_b128 v[172:175], v230 offset:15360\n\t" \
  "v_and_b32 v1, 0xffff0000, v208\n\t" \
  "v_mfma_f32_16x16x32_bf16 v[116:119], a[164:167], v[128:131], v[116:119]\n\t" \
  "s_waitcnt lgkmcnt(4)\n\t" \
  "v_lshlrev_b32 v2, 16, v209\n\t" \
  "v_perm_b32 v239, v190, v186, s40\n\t" \
  "v_mfma_f32_16x16x32_bf16 v[104:107], a[160:163], v[144:147], v[104:107]\n\t" \
  "s_waitcnt vmcnt(9)\n\t" \
  "v_cmp_ne_u32 vcc, s36, v246\n\t" \
  "v_perm_b32 v190, v190, v186, s41\n\t" \
  "v_mfma_f32_16x16x32_bf16 v[104:107], a[168:171], v[148:151], v[104:107]\n\t" \
  "ds_read_b128 v[128:131], v230 offset:32768\n\t" \
  "v_and_b32 v3, 0xffff0000, v209\n\t" \
  "v_mfma_f32_16x16x32_bf16 v[104:107], a[176:179], v[152:155], v[104:107]\n\t" \
  "s_cbranch_vccnz .Lat_s4\n\t" \
  ".Lat_k4:\n\t" \
  "v_mfma_f32_16x16x32_bf16 v[104:107], a[184:187], v[156:159], v[104:107]\n\t" \
  "buffer_load_dwordx4 v[32:35], v242, s[16:19], s70 offen offset:0 sc1\n\t" \
  "v_mfma_f32_16x16x32_bf16 v[120:123], a[188:191], v[156:159], v[120:123]\n\t" \
  "ds_read_b128 v[132:135], v230 offset:33792\n\t" \
  "v_lshlrev_b32 v4, 16, v210\n\t" \
  "v_mfma_f32_16x16x32_bf16 v[120:123], a[180:183], v[152:155], v[120:123]\n\t" \
  "ds_read_b128 v[136:139], v230 offset:34816\n\t" \
  "v_mov_b32 v186, v239\n\t" \
  "v_mfma_f32_16x16x32_bf16 v[120:123], a[172:175], v[148:151], v[120:123]\n\t" \
  "ds_read_b128 v[140:143], v230 offset:35840\n\t" \
  "v_perm_b32 v239, v191, v187, s40\n\t" \
  "v_mfma_f32_16x16x32_bf16 v[120:123], a[164:167], v[144:147], v[120:123]\n\t" \
  "s_waitcnt lgkmcnt(4)\n\t" \
  "v_and_b32 v5, 0xffff0000, v210\n\t" \
  "v_perm_b32 v191, v191, v187, s41\n\t" \
  "v_mfma_f32_16x16x32_bf16 v[108:111], a[160:163], v[160:163], v[108:111]\n\t" \
  "ds_read_b128 v[144:147], v230 offset:36864\n\t" \
  "v_lshlrev_b32 v6, 16, v211\n\t" \
  "v_mfma_f32_16x16x32_bf16 v[108:111], a[168:171], v[164:167], v[108:111]\n\t" \
  "buffer_load_dwordx4 v[36:39], v242, s[16:19], s70 offen offset:1024 sc1\n\t" \
  "v_mfma_f32_16x16x32_bf16 v[108:111], a[176:179], v[168:171], v[108:111]\n\t" \
  "ds_read_b128 v[148:151], v230 offset:37888\n\t" \
  "v_and_b32 v7, 0xffff0000, v211\n\t" \
  "v_mfma_f32_16x16x32_bf16 v[108:111], a[184:187], v[172:175], v[108:111]\n\t" \
  "v_mov_b32 v187, v239\n\t" \
  "v_perm_b32 v239, v196, v192, s40\n\t" \
  "v_mfma_f32_16x16x32_bf16 v[124:127], a[188:191], v[172:175], v[124:127]\n\t" \
  "ds_read_b128 v[152:155], v230 offset:38912\n\t" \
  "v_lshlrev_b32 v8, 16, v212\n\t" \
  "v_mfma_f32_16x16x32_bf16 v[124:127], a[180:183], v[168:171], v[124:127]\n\t" \
  "v_and_b32 v9, 0xffff0000, v212\n\t" \
  "v_perm_b32 v196, v196, v192, s41\n\t" \
  "v_mfma_f32_16x16x32_bf16 v[124:127], a[172:175], v[164:167], v[124:127]\n\t" \
  "ds_read_b128 v[156:159], v230 offset:39936\n\t" \
  "v_mov_b32 v192, v239\n\t" \
  "v_mfma_f32_16x16x32_bf16 v[124:127], a[164:167], v[160:163], v[124:127]\n\t" \
  "s_waitcnt lgkmcnt(4)\n\t" \
  "buffer_load_dwordx4 v[40:43], v242, s[16:19], s70 offen offset:2048 sc1\n\t" \
  "v_mfma_f32_16x16x32_bf16 v[96:99], a[0:3], v[128:131], v[96:99]\n\t" \
  "ds_read_b128 v[160:163], v230 offset:40960\n\t" \
  "v_lshlrev_b32 v10, 16, v213\n\t" \
  "v_mfma_f32_16x16x32_bf16 v[96:99], a[8:11], v[132:135], v[96:99]\n\t" \
  "v_and_b32 v11, 0xffff0000, v213\n\t" \
  "v_perm_b32 v239, v197, v193, s40\n\t" \
  "v_mfma_f32_16x16x32_bf16 v[96:99], a[16:19], v[136:139], v[96:99]\n\t" \
  "ds_read_b128 v[164:167], v230 offset:41984\n\t" \
  "v_perm_b32 v197, v197, v193, s41\n\t" \
  "v_mfma_f32_16x16x32_bf16 v[96:99], a[24:27], v[140:143], v[96:99]\n\t" \
  "v_lshlrev_b32 v12, 16, v214\n\t" \
  "v_mov_b32 v193, v239\n\t" \
  "v_mfma_f32_16x16x32_bf16 v[112:115], a[28:31], v[140:143], v[112:115]\n\t" \
  "s_waitcnt vmcnt(7)\n\t" \
  "buffer_store_dword v243, v244, s[16:19], s66 offen offset:32 sc1\n\t" \
  "ds_read_b128 v[168:171], v230 offset:43008\n\t" \
  "v_mfma_f32_16x16x32_bf16 v[112:115], a[20:23], v[136:139], v[112:115]\n\t" \
  "v_and_b32 v13, 0xffff0000, v214\n\t" \
  "v_perm_b32 v239, v198, v194, s40\n\t" \
  "v_mfma_f32_16x16x32_bf16 v[112:115], a[12:15], v[132:135], v[112:115]\n\t" \
  "buffer_load_dwordx4 v[44:47], v242, s[16:19], s70 offen offset:3072 sc1\n\t" \
  "v_mfma_f32_16x16x32_bf16 v[112:115], a[4:7], v[128:131], v[112:115]\n\t" \
  "s_waitcnt lgkmcnt(3)\n\t" \
  "ds_read_b128 v[172:175], v230 offset:44032\n\t" \
  "v_lshlrev_b32 v14, 16, v215\n\t" \
  "v_mfma_f32_16x16x32_bf16 v[100:103], a[0:3], v[144:147], v[100:103]\n\t" \
  "ds_read_b128 v[128:131], v230 offset:45056\n\t" \
  "v_perm_b32 v198, v198, v194, s41\n\t" \
  "v_mfma_f32_16x16x32_bf16 v[100:103], a[8:11], v[148:151], v[100:103]\n\t" \
  "v_and_b32 v15, 0xffff0000, v215\n\t" \
  "v_mov_b32 v194, v239\n\t" \
  "v_mfma_f32_16x16x32_bf16 v[100:103], a[16:19], v[152:155], v[100:103]\n\t" \
  "ds_read_b128 v[132:135], v230 offset:46080\n\t" \
  "v_lshlrev_b32 v16, 16, v216\n\t" \
  "v_mfma_f32_16x16x32_bf16 v[100:103], a[24:27], v[156:159], v[100:103]\n\t" \
  "buffer_load_dwordx4 v[48:51], v242, s[16:19], s78 offen offset:0 sc1\n\t" \
  "v_mfma_f32_16x16x32_bf16 v[116:119], a[28:31], v[156:159], v[116:119]\n\t" \
  "ds_read_b128 v[136:139], v230 offset:47104\n\t" \
  "v_perm_b32 v239, v199, v195, s40\n\t" \
  "v_mfma_f32_16x16x32_bf16 v[116:119], a[20:23], v[152:155], v[116:119]\n\t" \
  "v_and_b32 v17, 0xffff0000, v216\n\t" \
  "v_lshlrev_b32 v18, 16, v217\n\t" \
  "v_mfma_f32_16x16x32_bf16 v[116:119], a[12:15], v[148:151], v[116:119]\n\t" \
  "s_waitcnt lgkmcnt(3)\n\t" \
  "ds_read_b128 v[140:143], v230 offset:48128\n\t" \
  "v_perm_b32 v199, v199, v195, s41\n\t" \
  "v_mfma_f32_16x16x32_bf16 v[116:119], a[4:7], v[144:147], v[116:119]\n\t" \
  "s_barrier\n\t" \
  "v_mfma_f32_16x16x32_bf16 v[104:107], a[0:3], v[160:163], v[104:107]\n\t" \
  "ds_read_b128 v[144:147], v230 offset:49152\n\t" \
  "v_mov_b32 v195, v239\n\t" \
  "v_mfma_f32_16x16x32_bf16 v[104:107], a[8:11], v[164:167], v[104:107]\n\t" \
  "buffer_load_dwordx4 v[52:55], v242, s[16:19], s78 offen offset:1024 sc1\n\t" \
  "v_mfma_f32_16x16x32_bf16 v[104:107], a[16:19], v[168:171], v[104:107]\n\t" \
  "ds_read_b128 v[148:151], v230 offset:50176\n\t" \
  "v_and_b32 v19, 0xffff0000, v217\n\t" \
  "v_mfma_f32_16x16x32_bf16 v[104:107], a[24:27], v[172:175], v[104:107]\n\t" \
  "v_lshlrev_b32 v20, 16, v218\n\t" \
  "v_perm_b32 v239, v204, v200, s40\n\t" \
  "v_mfma_f32_16x16x32_bf16 v[120:123], a[28:31], v[172:175], v[120:123]\n\t" \
  "ds_read_b128 v[152:155], v230 offset:51200\n\t" \
  "v_and_b32 v21, 0xffff0000, v218\n\t" \
  "v_mfma_f32_16x16x32_bf16 v[120:123], a[20:23], v[168:171], v[120:123]\n\t" \
  "v_lshlrev_b32 v22, 16, v219\n\t" \
  "v_perm_b32 v204, v204, v200, s41\n\t" \
  "v_mfma_f32_16x16x32_bf16 v[120:123], a[12:15], v[164:167], v[120:123]\n\t" \
  "ds_read_b128 v[156:159], v230 offset:52224\n\t" \
  "v_mov_b32 v200, v239\n\t" \
  "v_mfma_f32_16x16x32_bf16 v[120:123], a[4:7], v[160:163], v[120:123]\n\t" \
  "s_waitcnt lgkmcnt(4)\n\t" \
  "buffer_load_dwordx4 v[56:59], v242, s[16:19], s78 offen offset:2048 sc1\n\t" \
  "v_mfma_f32_16x16x32_bf16 v[108:111], a[0:3], v[128:131], v[108:111]\n\t" \
  "ds_read_b128 v[160:163], v230 offset:53248\n\t" \
  "v_and_b32 v23, 0xffff0000, v219\n\t" \
  "v_mfma_f32_16x16x32_bf16 v[108:111], a[8:11], v[132:135], v[108:111]\n\t" \
  "v_lshlrev_b32 v24, 16, v220\n\t" \
  "v_perm_b32 v239, v205, v201, s40\n\t" \
  "v_mfma_f32_16x16x32_bf16 v[108:111], a[16:19], v[136:139], v[108:111]\n\t" \
  "ds_read_b128 v[164:167], v230 offset:54272\n\t" \
  "v_perm_b32 v205, v205, v201, s41\n\t" \
  "v_mfma_f32_16x16x32_bf16 v[108:111], a[24:27], v[140:143], v[108:111]\n\t" \
  "v_and_b32 v25, 0xffff0000, v220\n\t" \
  "v_mov_b32 v201, v239\n\t" \
  "v_mfma_f32_16x16x32_bf16 v[124:127], a[28:31], v[140:143], v[124:127]\n\t" \
  "ds_read_b128 v[168:171], v230 offset:55296\n\t" \
  "v_lshlrev_b32 v26, 16, v221\n\t" \
  "v_mfma_f32_16x16x32_bf16 v[124:127], a[20:23], v[136:139], v[124:127]\n\t" \
  "buffer_load_dwordx4 v[60:63], v242, s[16:19], s78 offen offset:3072 sc1\n\t" \
  "v_mfma_f32_16x16x32_bf16 v[124:127], a[12:15], v[132:135], v[124:127]\n\t" \
  "ds_read_b128 v[172:175], v230 offset:56320\n\t" \
  "v_accvgpr_write_b32 a128, 0\n\t" \
  "v_mfma_f32_16x16x32_bf16 v[124:127], a[4:7], v[128:131], v[124:127]\n\t" \
  "s_waitcnt lgkmcnt(4)\n\t" \
  "v_accvgpr_write_b32 a129, 0\n\t" \
  "v_accvgpr_write_b32 a130, v248\n\t" \
  "v_mfma_f32_16x16x32_bf16 v[96:99], a[32:35], v[144:147], v[96:99]\n\t" \
  "ds_read_b128 v[128:131], v230 offset:57344\n\t" \
  "v_accvgpr_write_b32 a131, v249\n\t" \
  "v_mfma_f32_16x16x32_bf16 v[96:99], a[40:43], v[148:151], v[96:99]\n\t" \
  "v_accvgpr_write_b32 a132, 0\n\t" \
  "v_accvgpr_write_b32 a133, 0\n\t" \
  "v_mfma_f32_16x16x32_bf16 v[96:99], a[48:51], v[152:155], v[96:99]\n\t" \
  "ds_read_b128 v[132:135], v230 offset:58368\n\t" \
  "v_perm_b32 v239, v206, v202, s40\n\t" \
  "v_mfma_f32_16x16x32_bf16 v[96:99], a[56:59], v[156:159], v[96:99]\n\t" \
  "ds_read_b128 a[176:179], v236 offset:960\n\t" \
  "v_and_b32 v27, 0xffff0000, v221\n\t" \
  "v_mfma_f32_16x16x32_bf16 v[112:115], a[60:63], v[156:159], v[112:115]\n\t" \
  "ds_read_b128 v[136:139], v230 offset:59392\n\t" \
  "v_perm_b32 v206, v206, v202, s41\n\t" \
  "v_mfma_f32_16x16x32_bf16 v[112:115], a[52:55], v[152:155], v[112:115]\n\t" \
  "ds_read_b128 a[180:183], v236 offset:640\n\t" \
  "v_lshlrev_b32 v28, 16, v222\n\t" \
  "v_mfma_f32_16x16x32_bf16 v[112:115], a[44:47], v[148:151], v[112:115]\n\t" \
  "ds_read_b128 v[140:143], v230 offset:60416\n\t" \
  "v_mov_b32 v202, v239\n\t" \
  "v_mfma_f32_16x16x32_bf16 v[112:115], a[36:39], v[144:147], v[112:115]\n\t" \
  "s_waitcnt lgkmcnt(6)\n\t" \
  "ds_read_b128 a[184:187], v236 offset:320\n\t" \
  "v_and_b32 v29, 0xffff0000, v222\n\t" \
  "v_mfma_f32_16x16x32_bf16 v[100:103], a[32:35], v[160:163], v[100:103]\n\t" \
  "ds_read_b128 v[144:147], v230 offset:61440\n\t" \
  "v_perm_b32 v239, v207, v203, s40\n\t" \
  "v_mfma_f32_16x16x32_bf16 v[100:103], a[40:43], v[164:167], v[100:103]\n\t" \
  "ds_read_b128 a[188:191], v236 offset:0\n\t" \
  "v_lshlrev_b32 v30, 16, v223\n\t" \
  "v_mfma_f32_16x16x32_bf16 v[100:103], a[48:51], v[168:171], v[100:103]\n\t" \
  "ds_read_b128 v[148:151], v230 offset:62464\n\t" \
  "v_perm_b32 v207, v207, v203, s41\n\t" \
  "v_mfma_f32_16x16x32_bf16 v[100:103], a[56:59], v[172:175], v[100:103]\n\t" \
  "v_and_b32 v31, 0xffff0000, v223\n\t" \
  "v_mov_b32 v203, v239\n\t" \
  "v_mfma_f32_16x16x32_bf16 v[116:119], a[60:63], v[172:175], v[116:119]\n\t" \
  "ds_read_b128 v[152:155], v230 offset:63488\n\t" \
  "v_accvgpr_read_b32 v239, a224\n\t" \
  "v_mfma_f32_16x16x32_bf16 v[116:119], a[52:55], v[168:171], v[116:119]\n\t" \
  "v_accvgpr_read_b32 v84, a228\n\t" \
  "v_perm_b32 v80, v84, v239, s40\n\t" \
  "v_mfma_f32_16x16x32_bf16 v[116:119], a[44:47], v[164:167], v[116:119]\n\t" \
  "ds_read_b128 v[156:159], v230 offset:64512\n\t" \
  "v_perm_b32 v84, v84, v239, s41\n\t" \
  "v_mfma_f32_16x16x32_bf16 v[116:119], a[36:39], v[160:163], v[116:119]\n\t" \
  "s_waitcnt lgkmcnt(6)\n\t" \
  "v_accvgpr_read_b32 v239, a225\n\t" \
  "v_accvgpr_read_b32 v85, a229\n\t" \
  "v_mfma_f32_16x16x32_bf16 v[104:107], a[32:35], v[128:131], v[104:107]\n\t" \
  "ds_read_b128 v[160:163], v230 offset:16384\n\t" \
  "v_perm_b32 v81, v85, v239, s40\n\t" \
  "v_mfma_f32_16x16x32_bf16 v[104:107], a[40:43], v[132:135], v[104:107]\n\t" \
  "v_perm_b32 v85, v85, v239, s41\n\t" \
  "v_accvgpr_read_b32 v239, a226\n\t" \
  "v_mfma_f32_16x16x32_bf16 v[104:107], a[48:51], v[136:139], v[104:107]\n\t" \
  "ds_read_b128 v[164:167], v230 offset:17408\n\t" \
  "v_accvgpr_read_b32 v86, a230\n\t" \
  "v_mfma_f32_16x16x32_bf16 v[104:107], a[56:59], v[140:143], v[104:107]\n\t" \
  "v_perm_b32 v82, v86, v239, s40\n\t" \
  "v_perm_b32 v86, v86, v239, s41\n\t" \
  "v_mfma_f32_16x16x32_bf16 v[120:123], a[60:63], v[140:143], v[120:123]\n\t" \
  "buffer_load_dword v246, off, s[16:19], s73 offset:32 sc1\n\t" \
  "v_mfma_f32_16x16x32_bf16 v[120:123], a[52:55], v[136:139], v[120:123]\n\t" \
  "ds_read_b128 v[168:171], v230 offset:18432\n\t" \
  "v_accvgpr_read_b32 v239, a227\n\t" \
  "v_mfma_f32_16x16x32_bf16 v[120:123], a[44:47], v[132:135], v[120:123]\n\t" \
  "ds_read_b128 v[172:175], v230 offset:19456\n\t" \
  "v_accvgpr_read_b32 v87, a231\n\t" \
  "v_mfma_f32_16x16x32_bf16 v[120:123], a[36:39], v[128:131], v[120:123]\n\t" \
  "s_waitcnt lgkmcnt(4)\n\t" \
  "v_perm_b32 v83, v87, v239, s40\n\t" \
  "v_perm_b32 v87, v87, v239, s41\n\t" \
  "v_mfma_f32_16x16x32_bf16 v[108:111], a[32:35], v[144:147], v[108:111]\n\t" \
  "ds_read_b128 v[128:131], v230 offset:20480\n\t" \
  "v_accvgpr_read_b32 v239, a232\n\t" \
  "v_mfma_f32_16x16x32_bf16 v[108:111], a[40:43], v[148:151], v[108:111]\n\t" \
  "v_accvgpr_read_b32 v92, a236\n\t" \
  "v_perm_b32 v88, v92, v239, s40\n\t" \
  "v_mfma_f32_16x16x32_bf16 v[108:111], a[48:51], v[152:155], v[108:111]\n\t" \
  "ds_read_b128 v[132:135], v230 offset:21504\n\t" \
  "v_perm_b32 v92, v92, v239, s41\n\t" \
  "v_mfma_f32_16x16x32_bf16 v[108:111], a[56:59], v[156:159], v[108:111]\n\t" \
  ".Lhs15:\n\ts_cbranch_execz .Lhs15n\n\t" \
  "v_accvgpr_read_b32 v239, a233\n\t" \
  "v_accvgpr_read_b32 v93, a237\n\t" \
  "v_mfma_f32_16x16x32_bf16 v[124:127], a[60:63], v[156:159], v[124:127]\n\t" \
  "ds_read_b128 v[136:139], v230 offset:22528\n\t" \
  "v_perm_b32 v89, v93, v239, s40\n\t" \
  "v_mfma_f32_16x16x32_bf16 v[124:127], a[52:55], v[152:155], v[124:127]\n\t" \
  "v_perm_b32 v93, v93, v239, s41\n\t" \
  "v_accvgpr_read_b32 v239, a234\n\t" \
  "v_mfma_f32_16x16x32_bf16 v[124:127], a[44:47], v[148:151], v[124:127]\n\t" \
  "ds_read_b128 v[140:143], v230 offset:23552\n\t" \
  "v_accvgpr_read_b32 v94, a238\n\t" \
  "v_mfma_f32_16x16x32_bf16 v[124:127], a[36:39], v[144:147], v[124:127]\n\t" \
  "s_waitcnt lgkmcnt(4)\n\t" \
  "v_perm_b32 v90, v94, v239, s40\n\t" \
  "v_perm_b32 v94, v94, v239, s41\n\t" \
  "v_mfma_f32_16x16x32_bf16 v[96:99], a[64:67], v[160:163], v[96:99]\n\t" \
  "ds_read_b128 v[144:147], v230 offset:24576\n\t" \
  "v_accvgpr_read_b32 v239, a235\n\t" \
  "v_mfma_f32_16x16x32_bf16 v[96:99], a[72:75], v[164:167], v[96:99]\n\t" \
  "v_accvgpr_read_b32 v95, a239\n\t" \
  "v_perm_b32 v91, v95, v239, s40\n\t" \
  "v_mfma_f32_16x16x32_bf16 v[96:99], a[80:83], v[168:171], v[96:99]\n\t" \
  "ds_read_b128 v[148:151], v230 offset:25600\n\t" \
  "v_perm_b32 v95, v95, v239, s41\n\t" \
  "v_mfma_f32_16x16x32_bf16 v[96:99], a[88:91], v[172:175], v[96:99]\n\t" \
  "v_accvgpr_read_b32 v239, a240\n\t" \
  "v_accvgpr_read_b32 v212, a244\n\t" \
  "v_mfma_f32_16x16x32_bf16 v[112:115], a[92:95], v[172:175], v[112:115]\n\t" \
  "ds_read_b128 v[152:155], v230 offset:26624\n\t" \
  "v_perm_b32 v208, v212, v239, s40\n\t" \
  "v_mfma_f32_16x16x32_bf16 v[112:115], a[84:87], v[168:171], v[112:115]\n\t" \
  "v_perm_b32 v212, v212, v239, s41\n\t" \
  "v_accvgpr_read_b32 v239, a241\n\t" \
  "v_mfma_f32_16x16x32_bf16 v[112:115], a[76:79], v[164:167], v[112:115]\n\t" \
  "ds_read_b128 v[156:159], v230 offset:27648\n\t" \
  "v_accvgpr_read_b32 v213, a245\n\t" \
  "v_mfma_f32_16x16x32_bf16 v[112:115], a[68:71], v[160:163], v[112:115]\n\t" \
  "s_waitcnt lgkmcnt(4)\n\t" \
  "v_perm_b32 v209, v213, v239, s40\n\t" \
  "v_perm_b32 v213, v213, v239, s41\n\t" \
  "v_mfma_f32_16x16x32_bf16 v[100:103], a[64:67], v[128:131], v[100:103]\n\t" \
  "ds_read_b128 v[160:163], v230 offset:28672\n\t" \
  "v_accvgpr_read_b32 v239, a242\n\t" \
  "v_mfma_f32_16x16x32_bf16 v[100:103], a[72:75], v[132:135], v[100:103]\n\t" \
  "v_accvgpr_read_b32 v214, a246\n\t" \
  "v_perm_b32 v210, v214, v239, s40\n\t" \
  "v_mfma_f32_16x16x32_bf16 v[100:103], a[80:83], v[136:139], v[100:103]\n\t" \
  "ds_read_b128 v[164:167], v230 offset:29696\n\t" \
  "v_perm_b32 v214, v214, v239, s41\n\t" \
  "v_mfma_f32_16x16x32_bf16 v[100:103], a[88:91], v[140:143], v[100:103]\n\t" \
  "v_accvgpr_read_b32 v239, a243\n\t" \
  "v_accvgpr_read_b32 v215, a247\n\t" \
  "v_mfma_f32_16x16x32_bf16 v[116:119], a[92:95], v[140:143], v[116:119]\n\t" \
  "ds_read_b128 v[168:171], v230 offset:30720\n\t" \
  "v_perm_b32 v211, v215, v239, s40\n\t" \
  "v_mfma_f32_16x16x32_bf16 v[116:119], a[84:87], v[136:139], v[116:119]\n\t" \
  "v_perm_b32 v215, v215, v239, s41\n\t" \
  "v_accvgpr_read_b32 v239, a248\n\t" \
  "v_mfma_f32_16x16x32_bf16 v[116:119], a[76:79], v[132:135], v[116:119]\n\t" \
  "s_waitcnt vmcnt(10) lgkmcnt(3)\n\t" \
  "ds_read_b128 v[172:175], v230 offset:31744\n\t" \
  "v_accvgpr_read_b32 v220, a252\n\t" \
  "v_mfma_f32_16x16x32_bf16 v[116:119], a[68:71], v[128:131], v[116:119]\n\t" \
  "s_barrier\n\t" \
  "v_mfma_f32_16x16x32_bf16 v[104:107], a[64:67], v[144:147], v[104:107]\n\t" \
  "ds_read_b128 v[128:131], v232 offset:0\n\t" \
  "v_perm_b32 v216, v220, v239, s40\n\t" \
  "v_mfma_f32_16x16x32_bf16 v[104:107], a[72:75], v[148:151], v[104:107]\n\t" \
  "v_perm_b32 v220, v220, v239, s41\n\t" \
  "v_accvgpr_read_b32 v239, a249\n\t" \
  "v_mfma_f32_16x16x32_bf16 v[104:107], a[80:83], v[152:155], v[104:107]\n\t" \
  "ds_read_b128 v[132:135], v232 offset:1024\n\t" \
  "v_accvgpr_read_b32 v221, a253\n\t" \
  "v_mfma_f32_16x16x32_bf16 v[104:107], a[88:91], v[156:159], v[104:107]\n\t" \
  "buffer_load_dword v251, off, s[16:19], s73 offset:32 sc1\n\t" \
  "v_mfma_f32_16x16x32_bf16 v[120:123], a[92:95], v[156:159], v[120:123]\n\t" \
  "ds_read_b128 v[136:139], v232 offset:2048\n\t" \
  "v_perm_b32 v217, v221, v239, s40\n\t" \
  "v_mfma_f32_16x16x32_bf16 v[120:123], a[84:87], v[152:155], v[120:123]\n\t" \
  "v_perm_b32 v221, v221, v239, s41\n\t" \
  "v_accvgpr_read_b32 v239, a250\n\t" \
  "v_mfma_f32_16x16x32_bf16 v[120:123], a[76:79], v[148:151], v[120:123]\n\t" \
  "ds_read_b128 v[140:143], v232 offset:3072\n\t" \
  "v_accvgpr_read_b32 v222, a254\n\t" \
  "v_mfma_f32_16x16x32_bf16 v[120:123], a[68:71], v[144:147], v[120:123]\n\t" \
  "s_waitcnt lgkmcnt(4)\n\t" \
  "v_perm_b32 v218, v222, v239, s40\n\t" \
  "v_perm_b32 v222, v222, v239, s41\n\t" \
  "v_mfma_f32_16x16x32_bf16 v[108:111], a[64:67], v[160:163], v[108:111]\n\t" \
  "s_waitcnt vmcnt(1)\n\t" \
  "v_cmp_ne_u32 vcc, s36, v246\n\t" \
  "v_accvgpr_read_b32 v239, a251\n\t" \
  "v_mfma_f32_16x16x32_bf16 v[108:111], a[72:75], v[164:167], v[108:111]\n\t" \
  "ds_read_b128 v[144:147], v232 offset:4096\n\t" \
  "v_accvgpr_read_b32 v223, a255\n\t" \
  "v_mfma_f32_16x16x32_bf16 v[108:111], a[80:83], v[168:171], v[108:111]\n\t" \
  "s_mov_b32 s88, vcc_lo\n\t" \
  "s_cbranch_vccnz .Lat_n2\n\t" \
  "buffer_load_dwordx4 v[64:67], v242, s[16:19], s79 offen offset:0 sc1\n\t" \
  "buffer_load_dwordx4 v[68:71], v242, s[16:19], s79 offen offset:1024 sc1\n\t" \
  "buffer_load_dwordx4 v[72:75], v242, s[16:19], s79 offen offset:2048 sc1\n\t" \
  "buffer_load_dwordx4 v[76:79], v242, s[16:19], s79 offen offset:3072 sc1\n\t" \
  ".Lat_n2:\n\t" \
  "ds_read_b128 v[148:151], v232 offset:5120\n\t" \
  "v_perm_b32 v219, v223, v239, s40\n\t" \
  "v_mfma_f32_16x16x32_bf16 v[108:111], a[88:91], v[172:175], v[108:111]\n\t" \
  "ds_read_b128 v[152:155], v232 offset:6144\n\t" \
  "v_perm_b32 v223, v223, v239, s41\n\t" \
  "v_mfma_f32_16x16x32_bf16 v[124:127], a[92:95], v[172:175], v[124:127]\n\t" \
  "ds_read_b128 v[156:159], v232 offset:7168\n\t" \
  "v_mfma_f32_16x16x32_bf16 v[124:127], a[84:87], v[168:171], v[124:127]\n\t" \
  "v_mfma_f32_16x16x32_bf16 v[124:127], a[76:79], v[164:167], v[124:127]\n\t" \
  "v_mfma_f32_16x16x32_bf16 v[124:127], a[68:71], v[160:163], v[124:127]\n\t" \
  "s_waitcnt lgkmcnt(4)\n\t" \
  "v_mfma_f32_16x16x32_bf16 v[96:99], a[96:99], v[128:131], v[96:99]\n\t" \
  "ds_read_b128 v[160:163], v232 offset:8192\n\t" \
  "v_mfma_f32_16x16x32_bf16 v[96:99], a[104:107], v[132:135], v[96:99]\n\t" \
  "ds_read_b128 v[164:167], v232 offset:9216\n\t" \
  "v_mfma_f32_16x16x32_bf16 v[96:99], a[112:115], v[136:139], v[96:99]\n\t" \
  "ds_read_b128 v[168:171], v232 offset:10240\n\t" \
  "v_mfma_f32_16x16x32_bf16 v[96:99], a[120:123], v[140:143], v[96:99]\n\t" \
  "ds_read_b128 v[172:175], v232 offset:11264\n\t" \
  "v_mfma_f32_16x16x32_bf16 v[112:115], a[124:127], v[140:143], v[112:115]\n\t" \
  "v_mfma_f32_16x16x32_bf16 v[112:115], a[116:119], v[136:139], v[112:115]\n\t" \
  "v_mfma_f32_16x16x32_bf16 v[112:115], a[108:111], v[132:135], v[112:115]\n\t" \
  "v_mfma_f32_16x16x32_bf16 v[112:115], a[100:103], v[128:131], v[112:115]\n\t" \
  "s_waitcnt lgkmcnt(4)\n\t" \
  "v_mfma_f32_16x16x32_bf16 v[100:103], a[96:99], v[144:147], v[100:103]\n\t" \
  "ds_read_b128 v[128:131], v232 offset:12288\n\t" \
  "v_mfma_f32_16x16x32_bf16 v[100:103], a[104:107], v[148:151], v[100:103]\n\t" \
  "ds_read_b128 v[132:135], v232 offset:13312\n\t" \
  "v_mfma_f32_16x16x32_bf16 v[100:103], a[112:115], v[152:155], v[100:103]\n\t" \
  "ds_read_b128 v[136:139], v232 offset:14336\n\t" \
  "v_mfma_f32_16x16x32_bf16 v[100:103], a[120:123], v[156:159], v[100:103]\n\t" \
  "ds_read_b128 v[140:143], v232 offset:15360\n\t" \
  "v_mfma_f32_16x16x32_bf16 v[116:119], a[124:127], v[156:159], v[116:119]\n\t" \
  "v_mfma_f32_16x16x32_bf16 v[116:119], a[116:119], v[152:155], v[116:119]\n\t" \
  "v_mfma_f32_16x16x32_bf16 v[116:119], a[108:111], v[148:151], v[116:119]\n\t" \
  "v_mfma_f32_16x16x32_bf16 v[116:119], a[100:103], v[144:147], v[116:119]\n\t" \
  "s_waitcnt lgkmcnt(4)\n\t" \
  "v_mfma_f32_16x16x32_bf16 v[104:107], a[96:99], v[160:163], v[104:107]\n\t" \
  "ds_read_b128 v[144:147], v242 offset:16384\n\t" \
  "v_mfma_f32_16x16x32_bf16 v[104:107], a[104:107], v[164:167], v[104:107]\n\t" \
  "ds_read_b128 v[148:151], v242 offset:17408\n\t" \
  "v_mfma_f32_16x16x32_bf16 v[104:107], a[112:115], v[168:171], v[104:107]\n\t" \
  "ds_read_b128 v[152:155], v242 offset:18432\n\t" \
  "v_mfma_f32_16x16x32_bf16 v[104:107], a[120:123], v[172:175], v[104:107]\n\t" \
  "ds_read_b128 v[156:159], v242 offset:19456\n\t" \
  "v_mfma_f32_16x16x32_bf16 v[120:123], a[124:127], v[172:175], v[120:123]\n\t" \
  "v_mfma_f32_16x16x32_bf16 v[120:123], a[116:119], v[168:171], v[120:123]\n\t" \
  "v_mfma_f32_16x16x32_bf16 v[120:123], a[108:111], v[164:167], v[120:123]\n\t" \
  "v_mfma_f32_16x16x32_bf16 v[120:123], a[100:103], v[160:163], v[120:123]\n\t" \
  "s_waitcnt lgkmcnt(4)\n\t" \
  "v_mfma_f32_16x16x32_bf16 v[108:111], a[96:99], v[128:131], v[108:111]\n\t" \
  "s_cmp_lg_u32 s88, 0\n\t" \
  "s_cbranch_scc1 .Lat_m3\n\t" \
  ".Lat_n3:\n\t" \
  "v_mfma_f32_16x16x32_bf16 v[108:111], a[104:107], v[132:135], v[108:111]\n\t" \
  "s_add_u32 m0, s38, 32768\n\t" \
  "ds_read_b128 v[160:163], v242 offset:20480\n\t" \
  "s_mov_b32 s96, 0\n\t" \
  "v_mfma_f32_16x16x32_bf16 v[108:111], a[112:115], v[136:139], v[108:111]\n\t" \
  "buffer_load_dwordx4 v252, s[28:31], s96 offen offset:0 nt lds\n\t" \
  "v_mfma_f32_16x16x32_bf16 v[108:111], a[120:123], v[140:143], v[108:111]\n\t" \
  "ds_read_b128 v[164:167], v242 offset:21504\n\t" \
  "s_mov_b32 s96, 3072\n\t" \
  "v_mfma_f32_16x16x32_bf16 v[124:127], a[124:127], v[140:143], v[124:127]\n\t" \
  "buffer_load_dwordx4 v252, s[28:31], s96 offen offset:1024 nt lds\n\t" \
  "v_mfma_f32_16x16x32_bf16 v[124:127], a[116:119], v[136:139], v[124:127]\n\t" \
  "ds_read_b128 v[168:171], v242 offset:22528\n\t" \
  "s_mov_b32 s96, 6144\n\t" \
  "v_mfma_f32_16x16x32_bf16 v[124:127], a[108:111], v[132:135], v[124:127]\n\t" \
  "buffer_load_dwordx4 v252, s[28:31], s96 offen offset:2048 nt lds\n\t" \
  "v_mfma_f32_16x16x32_bf16 v[124:127], a[100:103], v[128:131], v[124:127]\n\t" \
  "ds_read_b128 v[172:175], v242 offset:23552\n\t" \
  "s_mov_b32 s96, 9216\n\t" \
  "v_mfma_f32_16x16x32_bf16 v[96:99], a[130:133], v[32:35], v[96:99]\n\t" \
  "buffer_load_dwordx4 v252, s[28:31], s96 offen offset:3072 nt lds\n\t" \
  "v_mfma_f32_16x16x32_bf16 v[100:103], a[130:133], v[36:39], v[100:103]\n\t" \
  "s_add_u32 m0, s38, 49152\n\t" \
  "s_mov_b32 s96, 16384\n\t" \
  "v_mfma_f32_16x16x32_bf16 v[104:107], a[130:133], v[40:43], v[104:107]\n\t" \
  "buffer_load_dwordx4 v252, s[28:31], s96 offen offset:0 nt lds\n\t" \
  "v_mfma_f32_16x16x32_bf16 v[108:111], a[130:133], v[44:47], v[108:111]\n\t" \
  "s_mov_b32 s96, 19456\n\t" \
  "v_mfma_f32_16x16x32_bf16 v[112:115], a[128:131], v[32:35], v[112:115]\n\t" \
  "buffer_load_dwordx4 v252, s[28:31], s96 offen offset:1024 nt lds\n\t" \
  "v_mfma_f32_16x16x32_bf16 v[116:119], a[128:131], v[36:39], v[116:119]\n\t" \
  "s_mov_b32 s96, 22528\n\t" \
  "v_mfma_f32_16x16x32_bf16 v[120:123], a[128:131], v[40:43], v[120:123]\n\t" \
  "buffer_load_dwordx4 v252, s[28:31], s96 offen offset:2048 nt lds\n\t" \
  "v_mfma_f32_16x16x32_bf16 v[124:127], a[128:131], v[44:47], v[124:127]\n\t" \
  "s_waitcnt lgkmcnt(4)\n\t" \
  "s_mov_b32 s96, 25600\n\t" \
  "v_mfma_f32_16x16x32_bf16 v[96:99], a[192:195], v[144:147], v[96:99]\n\t" \
  "buffer_load_dwordx4 v252, s[28:31], s96 offen offset:3072 nt lds\n\t" \
  "v_mfma_f32_16x16x32_bf16 v[96:99], a[200:203], v[148:151], v[96:99]\n\t" \
  "ds_read_b128 v[128:131], v242 offset:24576\n\t" \
  "v_mfma_f32_16x16x32_bf16 v[96:99], a[208:211], v[152:155], v[96:99]\n\t" \
  "ds_read_b128 v[132:135], v242 offset:25600\n\t" \
  "v_mfma_f32_16x16x32_bf16 v[96:99], a[216:219], v[156:159], v[96:99]\n\t" \
  "ds_read_b128 v[136:139], v242 offset:26624\n\t" \
  "v_mfma_f32_16x16x32_bf16 v[112:115], a[220:223], v[156:159], v[112:115]\n\t" \
  "ds_read_b128 v[140:143], v242 offset:27648\n\t" \
  "v_mfma_f32_16x16x32_bf16 v[112:115], a[212:215], v[152:155], v[112:115]\n\t" \
  "v_mfma_f32_16x16x32_bf16 v[112:115], a[204:207], v[148:151], v[112:115]\n\t" \
  "v_mfma_f32_16x16x32_bf16 v[112:115], a[196:199], v[144:147], v[112:115]\n\t" \
  "v_mfma_f32_16x16x32_bf16 v[96:99], a[130:133], v[48:51], v[96:99]\n\t" \
  "v_mfma_f32_16x16x32_bf16 v[100:103], a[130:133], v[52:55], v[100:103]\n\t" \
  "v_mfma_f32_16x16x32_bf16 v[104:107], a[130:133], v[56:59], v[104:107]\n\t" \
  "v_mfma_f32_16x16x32_bf16 v[108:111], a[130:133], v[60:63], v[108:111]\n\t" \
  "v_mfma_f32_16x16x32_bf16 v[112:115], a[128:131], v[48:51], v[112:115]\n\t" \
  "v_mfma_f32_16x16x32_bf16 v[116:119], a[128:131], v[52:55], v[116:119]\n\t" \
  "v_mfma_f32_16x16x32_bf16 v[120:123], a[128:131], v[56:59], v[120:123]\n\t" \
  "v_mfma_f32_16x16x32_bf16 v[124:127], a[128:131], v[60:63], v[124:127]\n\t" \
  "s_waitcnt lgkmcnt(4)\n\t" \
  "v_mfma_f32_16x16x32_bf16 v[100:103], a[192:195], v[160:163], v[100:103]\n\t" \
  "ds_read_b128 v[144:147], v242 offset:28672\n\t" \
  "v_mfma_f32_16x16x32_bf16 v[100:103], a[200:203], v[164:167], v[100:103]\n\t" \
  "ds_read_b128 v[148:151], v242 offset:29696\n\t" \
  "v_mfma_f32_16x16x32_bf16 v[100:103], a[208:211], v[168:171], v[100:103]\n\t" \
  "ds_read_b128 v[152:155], v242 offset:30720\n\t" \
  "v_mfma_f32_16x16x32_bf16 v[100:103], a[216:219], v[172:175], v[100:103]\n\t" \
  "ds_read_b128 v[156:159], v242 offset:31744\n\t" \
  "v_mfma_f32_16x16x32_bf16 v[116:119], a[220:223], v[172:175], v[116:119]\n\t" \
  "v_mfma_f32_16x16x32_bf16 v[116:119], a[212:215], v[168:171], v[116:119]\n\t" \
  "v_mfma_f32_16x16x32_bf16 v[116:119], a[204:207], v[164:167], v[116:119]\n\t" \
  "v_mfma_f32_16x16x32_bf16 v[116:119], a[196:199], v[160:163], v[116:119]\n\t" \
  "s_waitcnt lgkmcnt(4)\n\t" \
  "v_mfma_f32_16x16x32_bf16 v[104:107], a[192:195], v[128:131], v[104:107]\n\t" \
  "ds_read_b128 v[160:163], v237 offset:2560\n\t" \
  "v_mfma_f32_16x16x32_bf16 v[104:107], a[200:203], v[132:135], v[104:107]\n\t" \
  "ds_read_b128 v[164:167], v237 offset:2624\n\t" \
  "v_mfma_f32_16x16x32_bf16 v[104:107], a[208:211], v[136:139], v[104:107]\n\t" \
  "v_mfma_f32_16x16x32_bf16 v[104:107], a[216:219], v[140:143], v[104:107]\n\t" \
  "v_mfma_f32_16x16x32_bf16 v[120:123], a[220:223], v[140:143], v[120:123]\n\t" \
  "v_mfma_f32_16x16x32_bf16 v[120:123], a[212:215], v[136:139], v[120:123]\n\t" \
  "v_mfma_f32_16x16x32_bf16 v[120:123], a[204:207], v[132:135], v[120:123]\n\t" \
  "v_mfma_f32_16x16x32_bf16 v[120:123], a[196:199], v[128:131], v[120:123]\n\t" \
  "s_waitcnt lgkmcnt(2)\n\t" \
  "v_mfma_f32_16x16x32_bf16 v[108:111], a[192:195], v[144:147], v[108:111]\n\t" \
  "v_mfma_f32_16x16x32_bf16 v[108:111], a[200:203], v[148:151], v[108:111]\n\t" \
  "v_mfma_f32_16x16x32_bf16 v[108:111], a[208:211], v[152:155], v[108:111]\n\t" \
  "v_mfma_f32_16x16x32_bf16 v[108:111], a[216:219], v[156:159], v[108:111]\n\t" \
  "v_mfma_f32_16x16x32_bf16 v[124:127], a[220:223], v[156:159], v[124:127]\n\t" \
  "v_mfma_f32_16x16x32_bf16 v[124:127], a[212:215], v[152:155], v[124:127]\n\t" \
  "v_mfma_f32_16x16x32_bf16 v[124:127], a[204:207], v[148:151], v[124:127]\n\t" \
  "v_mfma_f32_16x16x32_bf16 v[124:127], a[196:199], v[144:147], v[124:127]\n\t" \
  STP("s_memrealtime s[94:95]\n\t") \
  STP("s_memtime s[92:93]\n\t") \
  "s_cmp_lg_u32 s88, 0\n\t" \
  "s_cbranch_scc1 .Lat_s99\n\t" \
  ".Lat_d2:\n\t" \
  "s_waitcnt vmcnt(8)\n\t" \
  "v_mfma_f32_16x16x32_bf16 v[96:99], a[130:133], v[64:67], v[96:99]\n\t" \
  "v_mfma_f32_16x16x32_bf16 v[100:103], a[130:133], v[68:71], v[100:103]\n\t" \
  "v_mfma_f32_16x16x32_bf16 v[104:107], a[130:133], v[72:75], v[104:107]\n\t" \
  "v_mfma_f32_16x16x32_bf16 v[108:111], a[130:133], v[76:79], v[108:111]\n\t" \
  "v_mfma_f32_16x16x32_bf16 v[112:115], a[128:131], v[64:67], v[112:115]\n\t" \
  "v_mfma_f32_16x16x32_bf16 v[116:119], a[128:131], v[68:71], v[116:119]\n\t" \
  "v_mfma_f32_16x16x32_bf16 v[120:123], a[128:131], v[72:75], v[120:123]\n\t" \
  "v_mfma_f32_16x16x32_bf16 v[124:127], a[128:131], v[76:79], v[124:127]\n\t" \
  "s_waitcnt lgkmcnt(0)\n\t" \
  STP("v_writelane_b32 v255, s94, 38\n\t") \
  STP("v_writelane_b32 v255, s92, 39\n\t") \
  "v_pk_fma_f32 v[0:1], v[160:161], v[96:97], v[0:1]\n\t" \
  "v_pk_fma_f32 v[4:5], v[160:161], v[100:101], v[4:5]\n\t" \
  "v_pk_fma_f32 v[8:9], v[160:161], v[104:105], v[8:9]\n\t" \
  "v_pk_fma_f32 v[12:13], v[160:161], v[108:109], v[12:13]\n\t" \
  "v_pk_fma_f32 v[2:3], v[162:163], v[98:99], v[2:3]\n\t" \
  "v_pk_fma_f32 v[6:7], v[162:163], v[102:103], v[6:7]\n\t" \
  "v_pk_fma_f32 v[10:11], v[162:163], v[106:107], v[10:11]\n\t" \
  "v_pk_fma_f32 v[14:15], v[162:163], v[110:111], v[14:15]\n\t" \
  "v_pk_fma_f32 v[16:17], v[164:165], v[112:113], v[16:17]\n\t" \
  "v_pk_fma_f32 v[20:21], v[164:165], v[116:117], v[20:21]\n\t" \
  "v_pk_fma_f32 v[24:25], v[164:165], v[120:121], v[24:25]\n\t" \
  "v_pk_fma_f32 v[28:29], v[164:165], v[124:125], v[28:29]\n\t" \
  "v_pk_fma_f32 v[18:19], v[166:167], v[114:115], v[18:19]\n\t" \
  "v_pk_fma_f32 v[22:23], v[166:167], v[118:119], v[22:23]\n\t" \
  "v_pk_fma_f32 v[26:27], v[166:167], v[122:123], v[26:27]\n\t" \
  "v_pk_fma_f32 v[30:31], v[166:167], v[126:127], v[30:31]\n\t" \
  "v_cvt_pk_bf16_f32 v128, v0, v1\n\t" \
  "v_cvt_pk_bf16_f32 v132, v4, v5\n\t" \
  "v_cvt_pk_bf16_f32 v136, v8, v9\n\t" \
  "v_cvt_pk_bf16_f32 v140, v12, v13\n\t" \
  "v_cvt_pk_bf16_f32 v129, v2, v3\n\t" \
  "v_cvt_pk_bf16_f32 v133, v6, v7\n\t" \
  "v_cvt_pk_bf16_f32 v137, v10, v11\n\t" \
  "v_cvt_pk_bf16_f32 v141, v14, v15\n\t" \
  "v_cvt_pk_bf16_f32 v130, v16, v17\n\t" \
  "v_cvt_pk_bf16_f32 v134, v20, v21\n\t" \
  "v_cvt_pk_bf16_f32 v138, v24, v25\n\t" \
  "v_cvt_pk_bf16_f32 v142, v28, v29\n\t" \
  "v_cvt_pk_bf16_f32 v131, v18, v19\n\t" \
  "v_cvt_pk_bf16_f32 v135, v22, v23\n\t" \
  "v_cvt_pk_bf16_f32 v139, v26, v27\n\t" \
  "v_cvt_pk_bf16_f32 v143, v30, v31\n\t" \
  "v_dot2_f32_bf16 v64, v128, v128, 0\n\t" \
  "v_dot2_f32_bf16 v65, v132, v132, 0\n\t" \
  "v_dot2_f32_bf16 v66, v136, v136, 0\n\t" \
  "v_dot2_f32_bf16 v67, v140, v140, 0\n\t" \
  "v_dot2_f32_bf16 v64, v129, v129, v64\n\t" \
  "v_dot2_f32_bf16 v65, v133, v133, v65\n\t" \
  "v_dot2_f32_bf16 v66, v137, v137, v66\n\t" \
  "v_dot2_f32_bf16 v67, v141, v141, v67\n\t" \
  "v_dot2_f32_bf16 v64, v130, v130, v64\n\t" \
  "v_dot2_f32_bf16 v65, v134, v134, v65\n\t" \
  "v_dot2_f32_bf16 v66, v138, v138, v66\n\t" \
  "v_dot2_f32_bf16 v67, v142, v142, v67\n\t" \
  "v_dot2_f32_bf16 v64, v131, v131, v64\n\t" \
  "v_dot2_f32_bf16 v65, v135, v135, v65\n\t" \
  "v_dot2_f32_bf16 v66, v139, v139, v66\n\t" \
  "v_dot2_f32_bf16 v67, v143, v143, v67\n\t" \
  "v_mfma_f32_16x16x32_bf16 v[68:71], a[176:179], v[128:131], 0\n\t" \
  "v_mfma_f32_16x16x32_bf16 v[68:71], a[180:183], v[132:135], v[68:71]\n\t" \
  "v_permlane32_swap_b32 v64, v66\n\t" \
  "v_mfma_f32_16x16x32_bf16 v[68:71], a[184:187], v[136:139], v[68:71]\n\t" \
  "v_permlane32_swap_b32 v65, v67\n\t" \
  "v_mfma_f32_16x16x32_bf16 v[68:71], a[188:191], v[140:143], v[68:71]\n\t" \
  "v_add_f32 v64, v64, v66\n\t" \
  "v_add_f32 v65, v65, v67\n\t" \
  "v_mov_b32 v73, s36\n\t" \
  "v_mov_b32 v75, s36\n\t" \
  "v_permlane16_swap_b32 v64, v65\n\t" \
  "s_mov_b32 s81, 0\n\t" \
  "s_nop 1\n\t" \
  "v_add_f32 v70, v69, v70\n\t" \
  "v_add_f32 v69, v64, v65\n\t" \
  "v_mov_b32 v71, v238\n\t" \
  "ds_write_b128 v224, v[68:71]\n\t" \
  "s_waitcnt lgkmcnt(0)\n\t" \
  "s_barrier\n\t" \
  "ds_read_b128 v[96:99], v242 offset:0\n\t" \
  "ds_read_b128 v[100:103], v242 offset:1024\n\t" \
  "ds_read_b128 v[104:107], v242 offset:2048\n\t" \
  "ds_read_b128 v[108:111], v242 offset:3072\n\t" \
  "s_cmp_eq_u32 s3, 0\n\t" \
  "s_cselect_b64 vcc, -1, 0\n\t" \
  "s_waitcnt lgkmcnt(2)\n\t" \
  "v_pk_add_f32 v[96:97], v[96:97], v[100:101]\n\t" \
  "v_pk_add_f32 v[98:99], v[98:99], v[102:103]\n\t" \
  "s_waitcnt lgkmcnt(0)\n\t" \
  "v_pk_add_f32 v[104:105], v[104:105], v[108:109]\n\t" \
  "v_pk_add_f32 v[106:107], v[106:107], v[110:111]\n\t" \
  "v_pk_add_f32 v[64:65], v[96:97], v[104:105]\n\t" \
  "v_pk_add_f32 v[66:67], v[98:99], v[106:107]\n\t" \
  "v_cndmask_b32 v72, v66, v64, vcc\n\t" \
  "v_cndmask_b32 v74, v67, v65, vcc\n\t" \
  "buffer_store_dwordx4 v[72:75], v240, s[16:19], s10 offen sc1\n\t" \
  STP("s_memrealtime s[94:95]\n\t") \
  STP("s_memtime s[92:93]\n\t") \
  STP("s_waitcnt lgkmcnt(0)\n\t") \
  STP("v_writelane_b32 v255, s94, 40\n\t") \
  STP("v_writelane_b32 v255, s92, 41\n\t") \
  "buffer_load_dwordx4 v[96:99], v242, s[16:19], s11 offen offset:0 sc1\n\t" \
  "buffer_load_dwordx4 v[100:103], v242, s[16:19], s11 offen offset:1024 sc1\n\t" \
  "buffer_load_dwordx4 v[104:107], v242, s[16:19], s12 offen offset:0 sc1\n\t" \
  "buffer_load_dwordx4 v[108:111], v242, s[16:19], s12 offen offset:1024 sc1\n\t" \
  "ds_read_b128 v[144:147], v237 offset:2688\n\t" \
  "ds_read_b128 v[148:151], v237 offset:2752\n\t" \
  "ds_read_b128 v[152:155], v237 offset:2816\n\t" \
  "ds_read_b128 v[156:159], v237 offset:2880\n\t" \
  "ds_read_b128 v[168:171], v237 offset:2944\n\t" \
  "ds_read_b128 v[172:175], v237 offset:3008\n\t" \
  "v_lshlrev_b32 v0, 16, v128\n\t" \
  "v_and_b32 v1, 0xffff0000, v128\n\t" \
  "v_lshlrev_b32 v2, 16, v129\n\t" \
  "v_and_b32 v3, 0xffff0000, v129\n\t" \
  "v_lshlrev_b32 v4, 16, v130\n\t" \
  "v_and_b32 v5, 0xffff0000, v130\n\t" \
  "v_lshlrev_b32 v6, 16, v131\n\t" \
  "v_and_b32 v7, 0xffff0000, v131\n\t" \
  "v_lshlrev_b32 v8, 16, v132\n\t" \
  "v_and_b32 v9, 0xffff0000, v132\n\t" \
  "v_lshlrev_b32 v10, 16, v133\n\t" \
  "v_and_b32 v11, 0xffff0000, v133\n\t" \
  "v_lshlrev_b32 v12, 16, v134\n\t" \
  "v_and_b32 v13, 0xffff0000, v134\n\t" \
  "v_lshlrev_b32 v14, 16, v135\n\t" \
  "v_and_b32 v15, 0xffff0000, v135\n\t" \
  "v_lshlrev_b32 v16, 16, v136\n\t" \
  "v_and_b32 v17, 0xffff0000, v136\n\t" \
  "v_lshlrev_b32 v18, 16, v137\n\t" \
  "v_and_b32 v19, 0xffff0000, v137\n\t" \
  "v_lshlrev_b32 v20, 16, v138\n\t" \
  "v_and_b32 v21, 0xffff0000, v138\n\t" \
  "v_lshlrev_b32 v22, 16, v139\n\t" \
  "v_and_b32 v23, 0xffff0000, v139\n\t" \
  "v_lshlrev_b32 v24, 16, v140\n\t" \
  "v_and_b32 v25, 0xffff0000, v140\n\t" \
  "v_lshlrev_b32 v26, 16, v141\n\t" \
  "v_and_b32 v27, 0xffff0000, v141\n\t" \
  "v_lshlrev_b32 v28, 16, v142\n\t" \
  "v_and_b32 v29, 0xffff0000, v142\n\t" \
  "v_lshlrev_b32 v30, 16, v143\n\t" \
  "v_and_b32 v31, 0xffff0000, v143\n\t" \
  "v_lshrrev_b32 v72, 9, v224\n\t" \
  "v_lshlrev_b32 v246, 14, v72\n\t" \
  "v_bfe_u32 v72, v224, 7, 2\n\t" \
  "v_lshl_add_u32 v246, v72, 11, v246\n\t" \
  "v_and_b32 v72, 0x70, v242\n\t" \
  "v_add_u32 v246, v246, v72\n\t" \
  "s_and_b32 s44, s2, 3\n\t" \
  "s_and_b32 s42, s2, 0x38\n\t" \
  "s_lshl_b32 s42, s42, 6\n\t" \
  "s_lshl_b32 s44, s44, 7\n\t" \
  "s_add_u32 s91, s44, s42\n\t" \
  "s_and_b32 s84, s91, 0x7ff\n\t" \
  "s_add_u32 s84, s84, s9\n\t" \
  "s_add_u32 s84, s84, 1048576\n\t" \
  "s_add_u32 s85, s84, 131072\n\t" \
  "s_add_u32 s86, s84, 262144\n\t" \
  "s_add_u32 s87, s84, 393216\n\t" \
  "s_add_u32 s88, s84, 524288\n\t" \
  "s_add_u32 s89, s84, 655360\n\t" \
  "s_add_u32 s90, s84, 786432\n\t" \
  "s_waitcnt vmcnt(5)\n\t" \
  "ds_read2st64_b64 v[32:35], v247 offset0:64 offset1:65\n\t" \
  "ds_read2st64_b64 v[36:39], v247 offset0:66 offset1:67\n\t" \
  "ds_read2st64_b64 v[40:43], v247 offset0:68 offset1:69\n\t" \
  "ds_read2st64_b64 v[44:47], v247 offset0:70 offset1:71\n\t" \
  "ds_read2st64_b64 v[48:51], v247 offset0:96 offset1:97\n\t" \
  "ds_read2st64_b64 v[52:55], v247 offset0:98 offset1:99\n\t" \
  "ds_read2st64_b64 v[56:59], v247 offset0:100 offset1:101\n\t" \
  "ds_read2st64_b64 v[60:63], v247 offset0:102 offset1:103\n\t" \
  "s_waitcnt lgkmcnt(0)\n\t" \
  "v_perm_b32 v112, v34, v32, s40\n\t" \
  "v_perm_b32 v34, v34, v32, s41\n\t" \
  "v_perm_b32 v113, v35, v33, s40\n\t" \
  "v_perm_b32 v35, v35, v33, s41\n\t" \
  "v_perm_b32 v114, v38, v36, s40\n\t" \
  "v_perm_b32 v38, v38, v36, s41\n\t" \
  "v_perm_b32 v115, v39, v37, s40\n\t" \
  "v_perm_b32 v39, v39, v37, s41\n\t" \
  "v_perm_b32 v116, v42, v40, s40\n\t" \
  "v_perm_b32 v42, v42, v40, s41\n\t" \
  "v_perm_b32 v117, v43, v41, s40\n\t" \
  "v_perm_b32 v43, v43, v41, s41\n\t" \
  "v_perm_b32 v118, v46, v44, s40\n\t" \
  "v_perm_b32 v46, v46, v44, s41\n\t" \
  "v_perm_b32 v119, v47, v45, s40\n\t" \
  "v_perm_b32 v47, v47, v45, s41\n\t" \
  "v_perm_b32 v120, v50, v48, s40\n\t" \
  "v_perm_b32 v50, v50, v48, s41\n\t" \
  "v_perm_b32 v121, v51, v49, s40\n\t" \
  "v_perm_b32 v51, v51, v49, s41\n\t" \
  "v_perm_b32 v122, v54, v52, s40\n\t" \
  "v_perm_b32 v54, v54, v52, s41\n\t" \
  "v_perm_b32 v123, v55, v53, s40\n\t" \
  "v_perm_b32 v55, v55, v53, s41\n\t" \
  "v_perm_b32 v124, v58, v56, s40\n\t" \
  "v_perm_b32 v58, v58, v56, s41\n\t" \
  "v_perm_b32 v125, v59, v57, s40\n\t" \
  "v_perm_b32 v59, v59, v57, s41\n\t" \
  "v_perm_b32 v126, v62, v60, s40\n\t" \
  "v_perm_b32 v62, v62, v60, s41\n\t" \
  "v_perm_b32 v127, v63, v61, s40\n\t" \
  "v_perm_b32 v63, v63, v61, s41\n\t" \
  "s_cmp_lt_u32 s39, 4096\n\t" \
  "s_cselect_b64 s[52:53], -1, 0\n\t" \
  "s_waitcnt vmcnt(0)\n\t" \
  "s_branch .Ltl_w1\n\t" \
  ".Ltl_r1:\n\t" \
  "s_sleep 1\n\t" \
  "buffer_load_dwordx4 v[96:99], v242, s[16:19], s11 offen offset:0 sc1\n\t" \
  "buffer_load_dwordx4 v[100:103], v242, s[16:19], s11 offen offset:1024 sc1\n\t" \
  "buffer_load_dwordx4 v[104:107], v242, s[16:19], s12 offen offset:0 sc1\n\t" \
  "buffer_load_dwordx4 v[108:111], v242, s[16:19], s12 offen offset:1024 sc1\n\t" \
  "s_waitcnt vmcnt(0)\n\t" \
  ".Ltl_w1:\n\t" \
  "v_xor_b32 v72, s36, v97\n\t" \
  "v_bitop3_b32 v72, v72, v99, s36 bitop3:0xf6\n\t" \
  "v_bitop3_b32 v72, v72, v101, s36 bitop3:0xf6\n\t" \
  "v_bitop3_b32 v72, v72, v103, s36 bitop3:0xf6\n\t" \
  "v_bitop3_b32 v72, v72, v105, s36 bitop3:0xf6\n\t" \
  "v_bitop3_b32 v72, v72, v107, s36 bitop3:0xf6\n\t" \
  "v_bitop3_b32 v72, v72, v109, s36 bitop3:0xf6\n\t" \
  "v_bitop3_b32 v72, v72, v111, s36 bitop3:0xf6\n\t" \
  "v_cmp_ne_u32 vcc, 0, v72\n\t" \
  "v_cndmask_b32 v96, v96, v64, s[52:53]\n\t" \
  "v_cndmask_b32 v98, v98, v65, s[52:53]\n\t" \
  "v_cndmask_b32 v100, v100, v66, s[52:53]\n\t" \
  "v_cndmask_b32 v102, v102, v67, s[52:53]\n\t" \
  "v_add_f32 v68, v96, v104\n\t" \
  "v_add_f32 v69, v98, v106\n\t" \
  "v_add_f32 v70, v100, v108\n\t" \
  "v_add_f32 v71, v102, v110\n\t" \
  "s_cbranch_vccz .Ltl_k1\n\t" \
  "s_add_u32 s81, s81, 1\n\t" \
  "s_cmp_lt_u32 s81, " FQ_CAP "\n\t" \
  "s_cbranch_scc1 .Ltl_r1\n\t" \
  "buffer_store_dword v243, off, s[16:19], 0 offset:256 sc1\n\t" \
  ".Ltl_k1:\n\t" \
  "ds_write_b128 v224, v[68:71] offset:4096\n\t" \
  "s_waitcnt lgkmcnt(0)\n\t" \
  "s_barrier\n\t" \
  "ds_read_b128 v[96:99], v242 offset:4096\n\t" \
  "ds_read_b128 v[100:103], v242 offset:5120\n\t" \
  "ds_read_b128 v[104:107], v242 offset:6144\n\t" \
  "ds_read_b128 v[108:111], v242 offset:7168\n\t" \
  "buffer_load_dwordx2 v[248:249], v225, s[16:19], s13 offen sc1\n\t" \
  STP("s_memrealtime s[94:95]\n\t") \
  STP("s_memtime s[92:93]\n\t") \
  STP("s_waitcnt lgkmcnt(0)\n\t") \
  STP("v_writelane_b32 v255, s94, 42\n\t") \
  STP("v_writelane_b32 v255, s92, 43\n\t") \
  "s_waitcnt lgkmcnt(2)\n\t" \
  "v_pk_add_f32 v[96:97], v[96:97], v[100:101]\n\t" \
  "v_pk_add_f32 v[98:99], v[98:99], v[102:103]\n\t" \
  "s_waitcnt lgkmcnt(0)\n\t" \
  "v_pk_add_f32 v[104:105], v[104:105], v[108:109]\n\t" \
  "v_pk_add_f32 v[106:107], v[106:107], v[110:111]\n\t" \
  "v_pk_add_f32 v[64:65], v[96:97], v[104:105]\n\t" \
  "v_pk_add_f32 v[66:67], v[98:99], v[106:107]\n\t" \
  "v_mul_f32 v64, 0x3a800000, v64\n\t" \
  "v_mul_f32 v65, 0x3a800000, v65\n\t" \
  "v_readlane_b32 s42, v67, 0\n\t" \
  "v_readlane_b32 s43, v67, 1\n\t" \
  "v_readlane_b32 s53, v67, 2\n\t" \
  "v_fma_f32 v65, -v64, v64, v65\n\t" \
  "v_max_f32 v65, 0, v65\n\t" \
  "v_add_f32 v65, 0x3727c5ac, v65\n\t" \
  "v_rsq_f32 v68, v65\n\t" \
  "v_fma_f32 v66, -v64, s42, v66\n\t" \
  "v_fma_f32 v66, v68, v66, s43\n\t" \
  "v_cvt_pk_bf16_f32 v66, v66, v66\n\t" \
  "v_ashrrev_i32 v67, 31, v66\n\t" \
  "v_or_b32 v67, 0x80000000, v67\n\t" \
  "v_xor_b32 v66, v66, v67\n\t" \
  "v_and_b32 v66, 0xffff0000, v66\n\t" \
  "v_or_b32 v66, v66, v227\n\t" \
  "s_nop 1\n\t" \
  "v_max_u32_dpp v66, v66, v66 quad_perm:[1,0,3,2] row_mask:0xf bank_mask:0xf\n\t" \
  "v_mul_f32_e64 v70, v68, -v64\n\t" \
  "v_mov_b32 v65, s36\n\t" \
  "v_max_u32_dpp v66, v66, v66 quad_perm:[2,3,0,1] row_mask:0xf bank_mask:0xf\n\t" \
  "v_mov_b32 v97, s36\n\t" \
  "v_mov_b32 v99, s36\n\t" \
  "v_max_u32_dpp v66, v66, v66 row_half_mirror row_mask:0xf bank_mask:0xf\n\t" \
  "v_mov_b32 v101, s36\n\t" \
  "v_mov_b32 v103, s36\n\t" \
  "v_max_u32_dpp v66, v66, v66 row_mirror row_mask:0xf bank_mask:0xf\n\t" \
  "v_mov_b32 v105, s36\n\t" \
  "v_mov_b32 v107, s36\n\t" \
  "v_readlane_b32 s46, v66, 0\n\t" \
  "v_readlane_b32 s42, v66, 16\n\t" \
  "v_readlane_b32 s43, v66, 32\n\t" \
  "v_readlane_b32 s44, v66, 48\n\t" \
  "s_max_u32 s46, s46, s42\n\t" \
  "s_max_u32 s43, s43, s44\n\t" \
  "s_max_u32 s46, s46, s43\n\t" \
  "v_mov_b32 v64, s46\n\t" \
  "buffer_store_dwordx2 v[64:65], v244, s[16:19], s14 offen sc1\n\t" \
  STP("s_memrealtime s[94:95]\n\t") \
  STP("s_memtime s[92:93]\n\t") \
  STP("s_waitcnt lgkmcnt(0)\n\t") \
  STP("v_writelane_b32 v255, s94, 44\n\t") \
  STP("v_writelane_b32 v255, s92, 45\n\t") \
  "s_not_b32 s48, s46\n\t" \
  "s_and_b32 s48, s48, 63\n\t" \
  "s_and_b32 s44, s48, 15\n\t" \
  "s_lshl_b32 s50, 0x10001, s44\n\t" \
  "s_mov_b32 s51, s50\n\t" \
  "v_readlane_b32 s52, v68, s48\n\t" \
  "v_readlane_b32 s54, v70, s48\n\t" \
  "s_lshr_b32 s48, s48, 4\n\t" \
  "s_lshl_b32 s44, s48, 2\n\t" \
  "v_mov_b32 v109, s36\n\t" \
  "v_mov_b32 v111, s36\n\t" \
  "s_set_gpr_idx_on s44, gpr_idx(SRC0)\n\t" \
  "v_mov_b32 v104, v128\n\t" \
  "v_mov_b32 v106, v129\n\t" \
  "v_mov_b32 v108, v130\n\t" \
  "v_mov_b32 v110, v131\n\t" \
  "s_set_gpr_idx_off\n\t" \
  "v_mov_b32 v66, s54\n\t" \
  "v_lshlrev_b32 v160, 16, v104\n\t" \
  "v_and_b32 v161, 0xffff0000, v104\n\t" \
  "v_lshlrev_b32 v162, 16, v106\n\t" \
  "v_and_b32 v163, 0xffff0000, v106\n\t" \
  "v_lshlrev_b32 v164, 16, v108\n\t" \
  "v_and_b32 v165, 0xffff0000, v108\n\t" \
  "v_lshlrev_b32 v166, 16, v110\n\t" \
  "v_and_b32 v167, 0xffff0000, v110\n\t" \
  "v_fma_f32 v160, v160, s52, v66\n\t" \
  "v_fma_f32 v161, v161, s52, v66\n\t" \
  "v_fma_f32 v162, v162, s52, v66\n\t" \
  "v_fma_f32 v163, v163, s52, v66\n\t" \
  "v_fma_f32 v164, v164, s52, v66\n\t" \
  "v_fma_f32 v165, v165, s52, v66\n\t" \
  "v_fma_f32 v166, v166, s52, v66\n\t" \
  "v_fma_f32 v167, v167, s52, v66\n\t" \
  "v_pk_fma_f32 v[160:161], v[160:161], v[152:153], v[144:145]\n\t" \
  "v_pk_fma_f32 v[162:163], v[162:163], v[154:155], v[146:147]\n\t" \
  "v_pk_fma_f32 v[164:165], v[164:165], v[156:157], v[148:149]\n\t" \
  "v_pk_fma_f32 v[166:167], v[166:167], v[158:159], v[150:151]\n\t" \
  "v_cvt_pk_bf16_f32 v96, v160, v161\n\t" \
  "v_cvt_pk_bf16_f32 v98, v162, v163\n\t" \
  ".Lhs16:\n\ts_cbranch_execz .Lhs16n\n\t" \
  "v_cvt_pk_bf16_f32 v100, v164, v165\n\t" \
  "v_cvt_pk_bf16_f32 v102, v166, v167\n\t" \
  "s_waitcnt vmcnt(1)\n\t" \
  "v_cmp_eq_u32 vcc, s36, v249\n\t" \
  "v_mov_b32 v69, v68\n\t" \
  "v_mov_b32 v71, v70\n\t" \
  "v_cndmask_b32 v64, 0, v248, vcc\n\t" \
  "s_nop 1\n\t" \
  "v_max_u32_dpp v64, v64, v64 quad_perm:[1,0,3,2] row_mask:0xf bank_mask:0xf\n\t" \
  "v_permlane32_swap_b32 v68, v69\n\t" \
  "v_permlane32_swap_b32 v70, v71\n\t" \
  "v_max_u32_dpp v64, v64, v64 quad_perm:[2,3,0,1] row_mask:0xf bank_mask:0xf\n\t" \
  "v_mov_b32 v160, v68\n\t" \
  "v_mov_b32 v161, v69\n\t" \
  "v_max_u32_dpp v64, v64, v64 row_half_mirror row_mask:0xf bank_mask:0xf\n\t" \
  "v_mov_b32 v162, v70\n\t" \
  "v_mov_b32 v163, v71\n\t" \
  "v_max_u32_dpp v64, v64, v64 row_mirror row_mask:0xf bank_mask:0xf\n\t" \
  "s_nop 0\n\t" \
  "v_readlane_b32 s45, v64, 0\n\t" \
  "v_readlane_b32 s42, v64, 16\n\t" \
  "s_max_u32 s45, s45, s42\n\t" \
  "s_cmp_ge_u32 s46, s45\n\t" \
  "s_cselect_b32 s49, 1, 0\n\t" \
  "s_max_u32 s45, s45, s46\n\t" \
  "s_not_b32 s42, s45\n\t" \
  "s_bfe_u32 s47, s42, 0x50006\n\t" \
  "s_lshl_b32 s43, s42, 7\n\t" \
  "s_and_b32 s43, s43, 0x3c000\n\t" \
  "s_lshl_b32 s42, s42, 6\n\t" \
  "s_and_b32 s42, s42, 0x1000\n\t" \
  "s_add_u32 s42, s42, s43\n\t" \
  "s_add_u32 s42, s42, s15\n\t" \
  "buffer_load_dwordx2 v[248:249], v225, s[16:19], s13 offen sc1\n\t" \
  "buffer_load_dwordx4 v[72:75], v226, s[16:19], s42 offen offset:0 sc1\n\t" \
  "buffer_load_dwordx4 v[76:79], v226, s[16:19], s42 offen offset:16 sc1\n\t" \
  "s_cmp_eq_u32 s49, 0\n\t" \
  "s_cbranch_scc1 .Ltl_nc\n\t" \
  "s_xor_b32 s43, s20, 8192\n\t" \
  "s_mov_b64 exec, s[50:51]\n\t" \
  "buffer_store_dwordx4 v[96:99], v228, s[16:19], s20 offen offset:0 sc1\n\t" \
  "buffer_store_dwordx4 v[100:103], v228, s[16:19], s20 offen offset:64 sc1\n\t" \
  "buffer_store_dwordx4 v[96:99], v228, s[16:19], s43 offen offset:0 sc1\n\t" \
  "buffer_store_dwordx4 v[100:103], v228, s[16:19], s43 offen offset:64 sc1\n\t" \
  "buffer_store_dwordx4 v[104:107], v228, s[16:19], s21 offen offset:0 sc1\n\t" \
  "buffer_store_dwordx4 v[108:111], v228, s[16:19], s21 offen offset:64 sc1\n\t" \
  "s_mov_b64 exec, -1\n\t" \
  ".Ltl_nc:\n\t" \
  "v_permlane16_swap_b32 v68, v160\n\t" \
  "v_permlane16_swap_b32 v69, v161\n\t" \
  "v_permlane16_swap_b32 v70, v162\n\t" \
  "v_permlane16_swap_b32 v71, v163\n\t" \
  "v_pk_fma_f32 v[96:97], v[0:1], v[68:69], v[70:71] op_sel:[0,0,0] op_sel_hi:[1,0,0]\n\t" \
  "v_pk_fma_f32 v[98:99], v[2:3], v[68:69], v[70:71] op_sel:[0,0,0] op_sel_hi:[1,0,0]\n\t" \
  "v_pk_fma_f32 v[100:101], v[4:5], v[68:69], v[70:71] op_sel:[0,0,0] op_sel_hi:[1,0,0]\n\t" \
  "v_pk_fma_f32 v[102:103], v[6:7], v[68:69], v[70:71] op_sel:[0,0,0] op_sel_hi:[1,0,0]\n\t" \
  "v_pk_fma_f32 v[96:97], v[96:97], v[152:153], v[144:145]\n\t" \
  "v_pk_fma_f32 v[98:99], v[98:99], v[154:155], v[146:147]\n\t" \
  "v_pk_fma_f32 v[100:101], v[100:101], v[156:157], v[148:149]\n\t" \
  "v_pk_fma_f32 v[102:103], v[102:103], v[158:159], v[150:151]\n\t" \
  "v_cvt_pk_bf16_f32 v97, v96, v97\n\t" \
  "v_cvt_pk_bf16_f32 v99, v98, v99\n\t" \
  "v_cvt_pk_bf16_f32 v101, v100, v101\n\t" \
  "v_cvt_pk_bf16_f32 v103, v102, v103\n\t" \
  "v_lshlrev_b32 v96, 16, v97\n\t" \
  "v_and_b32 v97, 0xffff0000, v97\n\t" \
  "v_lshlrev_b32 v98, 16, v99\n\t" \
  "v_and_b32 v99, 0xffff0000, v99\n\t" \
  "v_lshlrev_b32 v100, 16, v101\n\t" \
  "v_and_b32 v101, 0xffff0000, v101\n\t" \
  "v_lshlrev_b32 v102, 16, v103\n\t" \
  "v_and_b32 v103, 0xffff0000, v103\n\t" \
  "v_pk_fma_f32 v[96:97], v[168:169], v[96:97], v[0:1]\n\t" \
  "v_pk_fma_f32 v[98:99], v[170:171], v[98:99], v[2:3]\n\t" \
  "v_pk_fma_f32 v[100:101], v[172:173], v[100:101], v[4:5]\n\t" \
  "v_pk_fma_f32 v[102:103], v[174:175], v[102:103], v[6:7]\n\t" \
  "v_cvt_pk_bf16_f32 v128, v96, v97\n\t" \
  "v_cvt_pk_bf16_f32 v129, v98, v99\n\t" \
  "v_cvt_pk_bf16_f32 v130, v100, v101\n\t" \
  "v_cvt_pk_bf16_f32 v131, v102, v103\n\t" \
  "v_pk_fma_f32 v[104:105], v[8:9], v[160:161], v[162:163] op_sel:[0,0,0] op_sel_hi:[1,0,0]\n\t" \
  "v_pk_fma_f32 v[106:107], v[10:11], v[160:161], v[162:163] op_sel:[0,0,0] op_sel_hi:[1,0,0]\n\t" \
  "v_pk_fma_f32 v[108:109], v[12:13], v[160:161], v[162:163] op_sel:[0,0,0] op_sel_hi:[1,0,0]\n\t" \
  "v_pk_fma_f32 v[110:111], v[14:15], v[160:161], v[162:163] op_sel:[0,0,0] op_sel_hi:[1,0,0]\n\t" \
  "v_pk_fma_f32 v[104:105], v[104:105], v[152:153], v[144:145]\n\t" \
  "v_pk_fma_f32 v[106:107], v[106:107], v[154:155], v[146:147]\n\t" \
  "v_pk_fma_f32 v[108:109], v[108:109], v[156:157], v[148:149]\n\t" \
  "v_pk_fma_f32 v[110:111], v[110:111], v[158:159], v[150:151]\n\t" \
  "v_cvt_pk_bf16_f32 v105, v104, v105\n\t" \
  "v_cvt_pk_bf16_f32 v107, v106, v107\n\t" \
  "v_cvt_pk_bf16_f32 v109, v108, v109\n\t" \
  "v_cvt_pk_bf16_f32 v111, v110, v111\n\t" \
  "v_lshlrev_b32 v104, 16, v105\n\t" \
  "v_and_b32 v105, 0xffff0000, v105\n\t" \
  "v_lshlrev_b32 v106, 16, v107\n\t" \
  "v_and_b32 v107, 0xffff0000, v107\n\t" \
  "v_lshlrev_b32 v108, 16, v109\n\t" \
  "v_and_b32 v109, 0xffff0000, v109\n\t" \
  "v_lshlrev_b32 v110, 16, v111\n\t" \
  "v_and_b32 v111, 0xffff0000, v111\n\t" \
  "v_pk_fma_f32 v[104:105], v[168:169], v[104:105], v[8:9]\n\t" \
  "v_pk_fma_f32 v[106:107], v[170:171], v[106:107], v[10:11]\n\t" \
  "v_pk_fma_f32 v[108:109], v[172:173], v[108:109], v[12:13]\n\t" \
  "v_pk_fma_f32 v[110:111], v[174:175], v[110:111], v[14:15]\n\t" \
  "v_cvt_pk_bf16_f32 v132, v104, v105\n\t" \
  "v_cvt_pk_bf16_f32 v133, v106, v107\n\t" \
  "v_cvt_pk_bf16_f32 v134, v108, v109\n\t" \
  "v_cvt_pk_bf16_f32 v135, v110, v111\n\t" \
  "v_pk_fma_f32 v[96:97], v[16:17], v[68:69], v[70:71] op_sel:[0,1,1] op_sel_hi:[1,1,1]\n\t" \
  "v_pk_fma_f32 v[98:99], v[18:19], v[68:69], v[70:71] op_sel:[0,1,1] op_sel_hi:[1,1,1]\n\t" \
  "v_pk_fma_f32 v[100:101], v[20:21], v[68:69], v[70:71] op_sel:[0,1,1] op_sel_hi:[1,1,1]\n\t" \
  "v_pk_fma_f32 v[102:103], v[22:23], v[68:69], v[70:71] op_sel:[0,1,1] op_sel_hi:[1,1,1]\n\t" \
  "v_pk_fma_f32 v[96:97], v[96:97], v[152:153], v[144:145]\n\t" \
  "v_pk_fma_f32 v[98:99], v[98:99], v[154:155], v[146:147]\n\t" \
  "v_pk_fma_f32 v[100:101], v[100:101], v[156:157], v[148:149]\n\t" \
  "v_pk_fma_f32 v[102:103], v[102:103], v[158:159], v[150:151]\n\t" \
  "v_cvt_pk_bf16_f32 v97, v96, v97\n\t" \
  "v_cvt_pk_bf16_f32 v99, v98, v99\n\t" \
  "v_cvt_pk_bf16_f32 v101, v100, v101\n\t" \
  "v_cvt_pk_bf16_f32 v103, v102, v103\n\t" \
  "v_lshlrev_b32 v96, 16, v97\n\t" \
  "v_and_b32 v97, 0xffff0000, v97\n\t" \
  "v_lshlrev_b32 v98, 16, v99\n\t" \
  "v_and_b32 v99, 0xffff0000, v99\n\t" \
  "v_lshlrev_b32 v100, 16, v101\n\t" \
  "v_and_b32 v101, 0xffff0000, v101\n\t" \
  "v_lshlrev_b32 v102, 16, v103\n\t" \
  "v_and_b32 v103, 0xffff0000, v103\n\t" \
  "v_pk_fma_f32 v[96:97], v[168:169], v[96:97], v[16:17]\n\t" \
  "v_pk_fma_f32 v[98:99], v[170:171], v[98:99], v[18:19]\n\t" \
  "v_pk_fma_f32 v[100:101], v[172:173], v[100:101], v[20:21]\n\t" \
  "v_pk_fma_f32 v[102:103], v[174:175], v[102:103], v[22:23]\n\t" \
  "v_cvt_pk_bf16_f32 v136, v96, v97\n\t" \
  "v_cvt_pk_bf16_f32 v137, v98, v99\n\t" \
  "v_cvt_pk_bf16_f32 v138, v100, v101\n\t" \
  "v_cvt_pk_bf16_f32 v139, v102, v103\n\t" \
  "v_pk_fma_f32 v[104:105], v[24:25], v[160:161], v[162:163] op_sel:[0,1,1] op_sel_hi:[1,1,1]\n\t" \
  "v_pk_fma_f32 v[106:107], v[26:27], v[160:161], v[162:163] op_sel:[0,1,1] op_sel_hi:[1,1,1]\n\t" \
  "v_pk_fma_f32 v[108:109], v[28:29], v[160:161], v[162:163] op_sel:[0,1,1] op_sel_hi:[1,1,1]\n\t" \
  "v_pk_fma_f32 v[110:111], v[30:31], v[160:161], v[162:163] op_sel:[0,1,1] op_sel_hi:[1,1,1]\n\t" \
  "v_pk_fma_f32 v[104:105], v[104:105], v[152:153], v[144:145]\n\t" \
  "v_pk_fma_f32 v[106:107], v[106:107], v[154:155], v[146:147]\n\t" \
  "v_pk_fma_f32 v[108:109], v[108:109], v[156:157], v[148:149]\n\t" \
  "v_pk_fma_f32 v[110:111], v[110:111], v[158:159], v[150:151]\n\t" \
  "v_cvt_pk_bf16_f32 v105, v104, v105\n\t" \
  "v_cvt_pk_bf16_f32 v107, v106, v107\n\t" \
  "v_cvt_pk_bf16_f32 v109, v108, v109\n\t" \
  "v_cvt_pk_bf16_f32 v111, v110, v111\n\t" \
  "v_lshlrev_b32 v104, 16, v105\n\t" \
  "v_and_b32 v105, 0xffff0000, v105\n\t" \
  "v_lshlrev_b32 v106, 16, v107\n\t" \
  "v_and_b32 v107, 0xffff0000, v107\n\t" \
  "v_lshlrev_b32 v108, 16, v109\n\t" \
  "v_and_b32 v109, 0xffff0000, v109\n\t" \
  "v_lshlrev_b32 v110, 16, v111\n\t" \
  "v_and_b32 v111, 0xffff0000, v111\n\t" \
  "v_pk_fma_f32 v[104:105], v[168:169], v[104:105], v[24:25]\n\t" \
  "v_pk_fma_f32 v[106:107], v[170:171], v[106:107], v[26:27]\n\t" \
  "v_pk_fma_f32 v[108:109], v[172:173], v[108:109], v[28:29]\n\t" \
  "v_pk_fma_f32 v[110:111], v[174:175], v[110:111], v[30:31]\n\t" \
  "v_cvt_pk_bf16_f32 v140, v104, v105\n\t" \
  "v_cvt_pk_bf16_f32 v141, v106, v107\n\t" \
  "v_cvt_pk_bf16_f32 v142, v108, v109\n\t" \
  "v_cvt_pk_bf16_f32 v143, v110, v111\n\t" \
  "v_permlane16_swap_b32 v128, v130\n\t" \
  "v_permlane16_swap_b32 v129, v131\n\t" \
  "v_permlane16_swap_b32 v132, v134\n\t" \
  "v_permlane16_swap_b32 v133, v135\n\t" \
  "v_permlane16_swap_b32 v136, v138\n\t" \
  "v_permlane16_swap_b32 v137, v139\n\t" \
  "v_permlane16_swap_b32 v140, v142\n\t" \
  "v_permlane16_swap_b32 v141, v143\n\t" \
  STP("s_memrealtime s[94:95]\n\t") \
  STP("s_memtime s[92:93]\n\t") \
  STP("s_waitcnt lgkmcnt(0)\n\t") \
  STP("v_writelane_b32 v255, s94, 46\n\t") \
  STP("v_writelane_b32 v255, s92, 47\n\t") \
  "s_mov_b32 s98, 0\n\t" \
  "s_mov_b32 s81, 0\n\t" \
  "s_lshl_b32 s99, 1, s8\n\t" \
  "s_cmp_eq_u32 s49, 0\n\t" \
  "s_cbranch_scc1 .Ltl_w2\n\t" \
  "s_waitcnt vmcnt(6)\n\t" \
  "s_branch .Ltl_t2\n\t" \
  ".Ltl_s2:\n\t" \
  "s_bcnt1_i32_b32 s54, s54\n\t" \
  "s_cmp_ge_u32 s54, 24\n\t" \
  "s_cselect_b32 s54, 1, s98\n\t" \
  "s_cmp_lg_u32 s54, 0\n\t" \
  "s_cbranch_scc1 .Ltl_q2\n\t" \
  "s_mov_b32 s98, 1\n\t" \
  "s_mov_b64 s[96:97], -1\n\t" \
  "s_cmp_eq_u32 s49, 0\n\t" \
  "s_cselect_b64 s[52:53], 0, s[50:51]\n\t" \
  "s_not_b64 s[52:53], s[52:53]\n\t" \
  "s_cmp_eq_u32 s48, 0\n\t" \
  "s_cselect_b64 exec, s[52:53], s[96:97]\n\t" \
  "s_add_u32 s44, s22, 0\n\t" \
  "buffer_store_dwordx4 v[128:131], v231, s[4:7], s44 offen sc0 sc1\n\t" \
  "s_cmp_eq_u32 s48, 1\n\t" \
  "s_cselect_b64 exec, s[52:53], s[96:97]\n\t" \
  "s_add_u32 s44, s22, 32768\n\t" \
  "buffer_store_dwordx4 v[132:135], v231, s[4:7], s44 offen sc0 sc1\n\t" \
  "s_cmp_eq_u32 s48, 2\n\t" \
  "s_cselect_b64 exec, s[52:53], s[96:97]\n\t" \
  "s_add_u32 s44, s22, 65536\n\t" \
  "buffer_store_dwordx4 v[136:139], v231, s[4:7], s44 offen sc0 sc1\n\t" \
  "s_cmp_eq_u32 s48, 3\n\t" \
  "s_cselect_b64 exec, s[52:53], s[96:97]\n\t" \
  "s_add_u32 s44, s22, 98304\n\t" \
  "buffer_store_dwordx4 v[140:143], v231, s[4:7], s44 offen sc0 sc1\n\t" \
  "s_mov_b64 exec, -1\n\t" \
  ".Ltl_q2:\n\t" \
  "s_add_u32 s81, s81, 1\n\t" \
  "s_cmp_lt_u32 s81, " FQ_CAP "\n\t" \
  "s_cbranch_scc0 .Ltl_g2\n\t" \
  "s_sleep 1\n\t" \
  "s_not_b32 s42, s45\n\t" \
  "s_bfe_u32 s47, s42, 0x50006\n\t" \
  "s_lshl_b32 s43, s42, 7\n\t" \
  "s_and_b32 s43, s43, 0x3c000\n\t" \
  "s_lshl_b32 s42, s42, 6\n\t" \
  "s_and_b32 s42, s42, 0x1000\n\t" \
  "s_add_u32 s42, s42, s43\n\t" \
  "s_add_u32 s42, s42, s15\n\t" \
  "buffer_load_dwordx2 v[248:249], v225, s[16:19], s13 offen sc1\n\t" \
  "buffer_load_dwordx4 v[72:75], v226, s[16:19], s42 offen offset:0 sc1\n\t" \
  "buffer_load_dwordx4 v[76:79], v226, s[16:19], s42 offen offset:16 sc1\n\t" \
  ".Ltl_w2:\n\t" \
  "s_waitcnt vmcnt(0)\n\t" \
  ".Ltl_t2:\n\t" \
  "v_cmp_eq_u32 vcc, s36, v249\n\t" \
  "v_xor_b32 v65, s36, v73\n\t" \
  "v_bitop3_b32 v65, v65, v75, s36 bitop3:0xf6\n\t" \
  "v_cndmask_b32 v64, 0, v248, vcc\n\t" \
  "s_or_b32 s54, vcc_lo, s99\n\t" \
  "s_nop 0\n\t" \
  "v_max_u32_dpp v64, v64, v64 quad_perm:[1,0,3,2] row_mask:0xf bank_mask:0xf\n\t" \
  "v_bitop3_b32 v65, v65, v77, s36 bitop3:0xf6\n\t" \
  "v_bitop3_b32 v65, v65, v79, s36 bitop3:0xf6\n\t" \
  "v_max_u32_dpp v64, v64, v64 quad_perm:[2,3,0,1] row_mask:0xf bank_mask:0xf\n\t" \
  "v_cmp_ne_u32 vcc, 0, v65\n\t" \
  "s_nop 0\n\t" \
  "v_max_u32_dpp v64, v64, v64 row_half_mirror row_mask:0xf bank_mask:0xf\n\t" \
  "s_nop 1\n\t" \
  "v_max_u32_dpp v64, v64, v64 row_mirror row_mask:0xf bank_mask:0xf\n\t" \
  "s_nop 0\n\t" \
  "v_readlane_b32 s45, v64, 0\n\t" \
  "v_readlane_b32 s42, v64, 16\n\t" \
  "s_max_u32 s45, s45, s42\n\t" \
  "s_max_u32 s45, s45, s46\n\t" \
  "s_not_b32 s42, s45\n\t" \
  "s_bfe_u32 s42, s42, 0x50006\n\t" \
  "s_xor_b32 s42, s42, s47\n\t" \
  "s_orn2_b32 s42, s42, s54\n\t" \
  "s_or_b32 s42, s42, vcc_lo\n\t" \
  "s_or_b32 s42, s42, vcc_hi\n\t" \
  "s_cbranch_scc1 .Ltl_s2\n\t" \
  ".Ltl_k2:\n\t" \
  STP("s_memrealtime s[94:95]\n\t") \
  STP("s_memtime s[92:93]\n\t") \
  STP("s_waitcnt lgkmcnt(0)\n\t") \
  STP("v_writelane_b32 v255, s94, 48\n\t") \
  STP("v_writelane_b32 v255, s92, 49\n\t") \
  "s_cmp_lt_i32 s57, 0\n\t" \
  "s_cbranch_scc1 .Ltl_nf\n\t" \
  "v_dot2_f32_bf16 v96, v80, v72, 0\n\t" \
  "v_dot2_f32_bf16 v97, v84, v72, 0\n\t" \
  "v_dot2_f32_bf16 v98, v81, v72, 0\n\t" \
  "v_dot2_f32_bf16 v99, v85, v72, 0\n\t" \
  "v_dot2_f32_bf16 v100, v82, v72, 0\n\t" \
  "v_dot2_f32_bf16 v101, v86, v72, 0\n\t" \
  "v_dot2_f32_bf16 v102, v83, v72, 0\n\t" \
  "v_dot2_f32_bf16 v103, v87, v72, 0\n\t" \
  "v_dot2_f32_bf16 v104, v176, v72, 0\n\t" \
  "v_dot2_f32_bf16 v105, v180, v72, 0\n\t" \
  "v_dot2_f32_bf16 v106, v177, v72, 0\n\t" \
  "v_dot2_f32_bf16 v107, v181, v72, 0\n\t" \
  "v_dot2_f32_bf16 v108, v178, v72, 0\n\t" \
  "v_dot2_f32_bf16 v109, v182, v72, 0\n\t" \
  "v_dot2_f32_bf16 v110, v179, v72, 0\n\t" \
  "v_dot2_f32_bf16 v111, v183, v72, 0\n\t" \
  "v_dot2_f32_bf16 v96, v88, v74, v96\n\t" \
  "v_dot2_f32_bf16 v97, v92, v74, v97\n\t" \
  "v_dot2_f32_bf16 v98, v89, v74, v98\n\t" \
  "v_dot2_f32_bf16 v99, v93, v74, v99\n\t" \
  "v_dot2_f32_bf16 v100, v90, v74, v100\n\t" \
  "v_dot2_f32_bf16 v101, v94, v74, v101\n\t" \
  "v_dot2_f32_bf16 v102, v91, v74, v102\n\t" \
  "v_dot2_f32_bf16 v103, v95, v74, v103\n\t" \
  "v_dot2_f32_bf16 v104, v184, v74, v104\n\t" \
  "v_dot2_f32_bf16 v105, v188, v74, v105\n\t" \
  "v_dot2_f32_bf16 v106, v185, v74, v106\n\t" \
  "v_dot2_f32_bf16 v107, v189, v74, v107\n\t" \
  "v_dot2_f32_bf16 v108, v186, v74, v108\n\t" \
  "v_dot2_f32_bf16 v109, v190, v74, v109\n\t" \
  "v_dot2_f32_bf16 v110, v187, v74, v110\n\t" \
  "v_dot2_f32_bf16 v111, v191, v74, v111\n\t" \
  "v_dot2_f32_bf16 v96, v208, v76, v96\n\t" \
  "v_dot2_f32_bf16 v97, v212, v76, v97\n\t" \
  "v_dot2_f32_bf16 v98, v209, v76, v98\n\t" \
  "v_dot2_f32_bf16 v99, v213, v76, v99\n\t" \
  "v_dot2_f32_bf16 v100, v210, v76, v100\n\t" \
  "v_dot2_f32_bf16 v101, v214, v76, v101\n\t" \
  "v_dot2_f32_bf16 v102, v211, v76, v102\n\t" \
  "v_dot2_f32_bf16 v103, v215, v76, v103\n\t" \
  "v_dot2_f32_bf16 v104, v192, v76, v104\n\t" \
  "v_dot2_f32_bf16 v105, v196, v76, v105\n\t" \
  "v_dot2_f32_bf16 v106, v193, v76, v106\n\t" \
  "v_dot2_f32_bf16 v107, v197, v76, v107\n\t" \
  "v_dot2_f32_bf16 v108, v194, v76, v108\n\t" \
  "v_dot2_f32_bf16 v109, v198, v76, v109\n\t" \
  "v_dot2_f32_bf16 v110, v195, v76, v110\n\t" \
  "v_dot2_f32_bf16 v111, v199, v76, v111\n\t" \
  "v_dot2_f32_bf16 v96, v216, v78, v96\n\t" \
  "v_dot2_f32_bf16 v97, v220, v78, v97\n\t" \
  "v_dot2_f32_bf16 v98, v217, v78, v98\n\t" \
  "v_dot2_f32_bf16 v99, v221, v78, v99\n\t" \
  "v_dot2_f32_bf16 v100, v218, v78, v100\n\t" \
  "v_dot2_f32_bf16 v101, v222, v78, v101\n\t" \
  "v_dot2_f32_bf16 v102, v219, v78, v102\n\t" \
  "v_dot2_f32_bf16 v103, v223, v78, v103\n\t" \
  "v_dot2_f32_bf16 v104, v200, v78, v104\n\t" \
  "v_dot2_f32_bf16 v105, v204, v78, v105\n\t" \
  "v_dot2_f32_bf16 v106, v201, v78, v106\n\t" \
  "v_dot2_f32_bf16 v107, v205, v78, v107\n\t" \
  "v_dot2_f32_bf16 v108, v202, v78, v108\n\t" \
  "v_dot2_f32_bf16 v109, v206, v78, v109\n\t" \
  "v_dot2_f32_bf16 v110, v203, v78, v110\n\t" \
  "v_dot2_f32_bf16 v111, v207, v78, v111\n\t" \
  "v_permlane32_swap_b32 v96, v104\n\t" \
  "v_permlane32_swap_b32 v97, v105\n\t" \
  "v_permlane32_swap_b32 v98, v106\n\t" \
  "v_permlane32_swap_b32 v99, v107\n\t" \
  "v_permlane32_swap_b32 v100, v108\n\t" \
  "v_permlane32_swap_b32 v101, v109\n\t" \
  "v_permlane32_swap_b32 v102, v110\n\t" \
  "v_permlane32_swap_b32 v103, v111\n\t" \
  "v_add_f32 v96, v96, v104\n\t" \
  "v_add_f32 v97, v97, v105\n\t" \
  "v_add_f32 v98, v98, v106\n\t" \
  "v_add_f32 v99, v99, v107\n\t" \
  "v_add_f32 v100, v100, v108\n\t" \
  "v_add_f32 v101, v101, v109\n\t" \
  "v_add_f32 v102, v102, v110\n\t" \
  "v_add_f32 v103, v103, v111\n\t" \
  "v_permlane16_swap_b32 v96, v100\n\t" \
  "v_permlane16_swap_b32 v97, v101\n\t" \
  "v_permlane16_swap_b32 v98, v102\n\t" \
  "v_permlane16_swap_b32 v99, v103\n\t" \
  "v_add_f32 v96, v96, v100\n\t" \
  "v_add_f32 v97, v97, v101\n\t" \
  "v_add_f32 v98, v98, v102\n\t" \
  "v_add_f32 v99, v99, v103\n\t" \
  "v_add_f32_dpp v96, v96, v96 row_ror:8 row_mask:0xf bank_mask:0xf\n\t" \
  "v_add_f32_dpp v97, v97, v97 row_ror:8 row_mask:0xf bank_mask:0xf\n\t" \
  "v_add_f32_dpp v98, v98, v98 row_ror:8 row_mask:0xf bank_mask:0xf\n\t" \
  "v_add_f32_dpp v99, v99, v99 row_ror:8 row_mask:0xf bank_mask:0xf\n\t" \
  "v_add_f32_dpp v96, v96, v96 row_ror:4 row_mask:0xf bank_mask:0xf\n\t" \
  "v_add_f32_dpp v97, v97, v97 row_ror:4 row_mask:0xf bank_mask:0xf\n\t" \
  "v_add_f32_dpp v98, v98, v98 row_ror:4 row_mask:0xf bank_mask:0xf\n\t" \
  "v_add_f32_dpp v99, v99, v99 row_ror:4 row_mask:0xf bank_mask:0xf\n\t" \
  "v_add_f32_dpp v96, v96, v96 row_ror:2 row_mask:0xf bank_mask:0xf\n\t" \
  "v_add_f32_dpp v97, v97, v97 row_ror:2 row_mask:0xf bank_mask:0xf\n\t" \
  "v_add_f32_dpp v98, v98, v98 row_ror:2 row_mask:0xf bank_mask:0xf\n\t" \
  "v_add_f32_dpp v99, v99, v99 row_ror:2 row_mask:0xf bank_mask:0xf\n\t" \
  "s_mov_b32 exec_lo, 0x00030003\n\t" \
  "s_mov_b32 exec_hi, 0x00030003\n\t" \
  "ds_write_b128 v253, v[96:99] offset:8192\n\t" \
  "s_mov_b64 exec, -1\n\t" \
  "v_mov_b32 v101, s36\n\t" \
  "v_mov_b32 v103, s36\n\t" \
  "s_waitcnt lgkmcnt(0)\n\t" \
  "s_barrier\n\t" \
  "v_add_u32 v104, 8192, v245\n\t" \
  "ds_read2_b32 v[96:97], v104 offset1:64\n\t" \
  "ds_read2_b32 v[98:99], v104 offset0:32 offset1:96\n\t" \
  "s_waitcnt lgkmcnt(0)\n\t" \
  "v_pk_add_f32 v[96:97], v[96:97], v[98:99]\n\t" \
  "v_add_f32 v96, v96, v97\n\t" \
  "v_cvt_pk_bf16_f32 v96, v96, v96\n\t" \
  "v_lshlrev_b32 v96, 16, v96\n\t" \
  "v_mov_b32 v97, v96\n\t" \
  "s_nop 1\n\t" \
  "v_permlane16_swap_b32 v96, v97\n\t" \
  "v_mul_f32 v98, 0xbfb8aa3b, v96\n\t" \
  "v_exp_f32 v98, v98\n\t" \
  "s_nop 0\n\t" \
  "v_add_f32 v98, 1.0, v98\n\t" \
  "v_rcp_f32 v98, v98\n\t" \
  "s_nop 0\n\t" \
  "v_mul_f32 v96, v98, v96\n\t" \
  "v_cvt_pk_bf16_f32 v96, v96, v96\n\t" \
  "v_lshlrev_b32 v96, 16, v96\n\t" \
  "v_mul_f32 v96, v97, v96\n\t" \
  "s_nop 1\n\t" \
  "v_mov_b32_dpp v97, v96 quad_perm:[1,0,3,2] row_mask:0xf bank_mask:0xf\n\t" \
  "v_cvt_pk_bf16_f32 v96, v96, v97\n\t" \
  "s_nop 0\n\t" \
  "v_readlane_b32 s60, v96, 0\n\t" \
  "v_readlane_b32 s61, v96, 2\n\t" \
  "v_readlane_b32 s62, v96, 4\n\t" \
  "v_readlane_b32 s63, v96, 6\n\t" \
  "v_readlane_b32 s64, v96, 8\n\t" \
  "v_readlane_b32 s65, v96, 10\n\t" \
  "v_readlane_b32 s66, v96, 12\n\t" \
  "v_readlane_b32 s67, v96, 14\n\t" \
  "v_dot2_f32_bf16 v104, v112, s60, 0\n\t" \
  "v_dot2_f32_bf16 v105, v34, s60, 0\n\t" \
  "v_dot2_f32_bf16 v106, v113, s60, 0\n\t" \
  "v_dot2_f32_bf16 v107, v35, s60, 0\n\t" \
  "v_dot2_f32_bf16 v104, v114, s61, v104\n\t" \
  "v_dot2_f32_bf16 v105, v38, s61, v105\n\t" \
  "v_dot2_f32_bf16 v106, v115, s61, v106\n\t" \
  "v_dot2_f32_bf16 v107, v39, s61, v107\n\t" \
  "v_dot2_f32_bf16 v104, v116, s62, v104\n\t" \
  "v_dot2_f32_bf16 v105, v42, s62, v105\n\t" \
  "v_dot2_f32_bf16 v106, v117, s62, v106\n\t" \
  "v_dot2_f32_bf16 v107, v43, s62, v107\n\t" \
  "v_dot2_f32_bf16 v104, v118, s63, v104\n\t" \
  "v_dot2_f32_bf16 v105, v46, s63, v105\n\t" \
  "v_dot2_f32_bf16 v106, v119, s63, v106\n\t" \
  "v_dot2_f32_bf16 v107, v47, s63, v107\n\t" \
  "v_dot2_f32_bf16 v104, v120, s64, v104\n\t" \
  "v_dot2_f32_bf16 v105, v50, s64, v105\n\t" \
  "v_dot2_f32_bf16 v106, v121, s64, v106\n\t" \
  "v_dot2_f32_bf16 v107, v51, s64, v107\n\t" \
  "v_dot2_f32_bf16 v104, v122, s65, v104\n\t" \
  "v_dot2_f32_bf16 v105, v54, s65, v105\n\t" \
  "v_dot2_f32_bf16 v106, v123, s65, v106\n\t" \
  "v_dot2_f32_bf16 v107, v55, s65, v107\n\t" \
  "v_dot2_f32_bf16 v104, v124, s66, v104\n\t" \
  "v_dot2_f32_bf16 v105, v58, s66, v105\n\t" \
  "v_dot2_f32_bf16 v106, v125, s66, v106\n\t" \
  "v_dot2_f32_bf16 v107, v59, s66, v107\n\t" \
  "v_dot2_f32_bf16 v104, v126, s67, v104\n\t" \
  "v_dot2_f32_bf16 v105, v62, s67, v105\n\t" \
  "v_dot2_f32_bf16 v106, v127, s67, v106\n\t" \
  "v_dot2_f32_bf16 v107, v63, s67, v107\n\t" \
  "s_nop 0\n\t" \
  "v_cvt_pk_bf16_f32 v100, v104, v105\n\t" \
  "s_nop 0\n\t" \
  "v_cvt_pk_bf16_f32 v102, v106, v107\n\t" \
  "buffer_store_dwordx4 v[100:103], v242, s[16:19], s23 offen sc1\n\t" \
  ".Ltl_nf:\n\t" \
  STP("s_memrealtime s[94:95]\n\t") \
  STP("s_memtime s[92:93]\n\t") \
  STP("s_waitcnt lgkmcnt(0)\n\t") \
  STP("v_writelane_b32 v255, s94, 50\n\t") \
  STP("v_writelane_b32 v255, s92, 51\n\t") \
  "s_cmp_eq_u32 s47, s8\n\t" \
  "s_cselect_b64 s[52:53], s[50:51], 0\n\t" \
  "s_cselect_b32 s54, 0, s49\n\t" \
  "s_not_b64 s[52:53], s[52:53]\n\t" \
  "s_mov_b64 s[96:97], -1\n\t" \
  "s_cmp_eq_u32 s98, 0\n\t" \
  "s_cbranch_scc1 .Ltl_so\n\t" \
  "s_cmp_eq_u32 s54, 0\n\t" \
  "s_cbranch_scc1 .Ltl_ns\n\t" \
  "s_mov_b64 s[52:53], s[50:51]\n\t" \
  "s_mov_b64 s[96:97], 0\n\t" \
  ".Ltl_so:\n\t" \
  "s_cmp_eq_u32 s48, 0\n\t" \
  "s_cselect_b64 exec, s[52:53], s[96:97]\n\t" \
  "s_add_u32 s44, s22, 0\n\t" \
  "buffer_store_dwordx4 v[128:131], v231, s[4:7], s44 offen sc0 sc1\n\t" \
  "s_cmp_eq_u32 s48, 1\n\t" \
  "s_cselect_b64 exec, s[52:53], s[96:97]\n\t" \
  "s_add_u32 s44, s22, 32768\n\t" \
  "buffer_store_dwordx4 v[132:135], v231, s[4:7], s44 offen sc0 sc1\n\t" \
  "s_cmp_eq_u32 s48, 2\n\t" \
  "s_cselect_b64 exec, s[52:53], s[96:97]\n\t" \
  "s_add_u32 s44, s22, 65536\n\t" \
  "buffer_store_dwordx4 v[136:139], v231, s[4:7], s44 offen sc0 sc1\n\t" \
  "s_cmp_eq_u32 s48, 3\n\t" \
  "s_cselect_b64 exec, s[52:53], s[96:97]\n\t" \
  "s_add_u32 s44, s22, 98304\n\t" \
  "buffer_store_dwordx4 v[140:143], v231, s[4:7], s44 offen sc0 sc1\n\t" \
  "s_mov_b64 exec, -1\n\t" \
  ".Ltl_ns:\n\t" \
  "s_or_b32 s42, s2, s3\n\t" \
  "s_cbranch_scc1 .Ltl_ne\n\t" \
  "s_mov_b32 s42, 2490368\n\t" \
  "buffer_store_dword v243, v244, s[16:19], s42 offen sc1\n\t" \
  ".Ltl_ne:\n\t" \
  "s_cmp_lt_i32 s57, 0\n\t" \
  "s_cbranch_scc0 .Ltl_end\n\t" \
  "v_and_b32 v1, 15, v233\n\t" \
  "v_lshlrev_b32 v1, 3, v1\n\t" \
  "s_not_b32 s42, s45\n\t" \
  "s_bfe_u32 s47, s42, 0x50006\n\t" \
  "s_lshl_b32 s43, s42, 7\n\t" \
  "s_and_b32 s43, s43, 0x3c000\n\t" \
  "s_lshl_b32 s42, s42, 6\n\t" \
  "s_and_b32 s42, s42, 0x1000\n\t" \
  "s_add_u32 s42, s42, s43\n\t" \
  "s_add_u32 s42, s42, s15\n\t" \
  "s_add_u32 s69, s42, s91\n\t" \
  "s_lshl_b32 s42, s47, 12\n\t" \
  "s_and_b32 s42, s42, 0x1c000\n\t" \
  "s_add_u32 s70, s84, s42\n\t" \
  "s_and_b32 s42, s47, 3\n\t" \
  "s_lshl_b32 s42, s42, 11\n\t" \
  "s_add_u32 s70, s70, s42\n\t" \
  "s_add_u32 s70, s70, 917504\n\t" \
  "s_and_b32 s44, s2, 3\n\t" \
  "s_lshl_b32 s42, s44, 12\n\t" \
  "s_add_u32 s42, s42, 150400\n\t" \
  "v_add_u32 v6, s42, v1\n\t" \
  "ds_read_b64 v[6:7], v6\n\t" \
  "v_and_b32 v5, 0x70, v242\n\t" \
  "v_lshrrev_b32 v3, 2, v228\n\t" \
  "v_add_u32 v3, v3, v5\n\t" \
  "v_lshl_add_u32 v3, s3, 7, v3\n\t" \
  "s_not_b32 s42, s45\n\t" \
  "s_and_b32 s42, s42, 0x7ff\n\t" \
  "s_lshl_b32 s42, s42, 11\n\t" \
  "s_lshr_b32 s71, s91, 1\n\t" \
  "s_add_u32 s71, s71, s42\n\t" \
  "s_ashr_i32 s42, s45, 31\n\t" \
  "s_orn2_b32 s42, 0x80000000, s42\n\t" \
  "s_xor_b32 s42, s42, s45\n\t" \
  "s_and_b32 s42, s42, 0xffff0000\n\t" \
  "v_mov_b32 v2, s42\n\t" \
  "v_mul_f32 v2, 0xbfb8aa3b, v2\n\t" \
  "v_exp_f32 v2, v2\n\t" \
  "s_nop 0\n\t" \
  "v_add_f32 v2, 1.0, v2\n\t" \
  "v_rcp_f32 v2, v2\n\t" \
  "s_nop 0\n\t" \
  "v_cvt_pk_bf16_f32 v2, v2, v2\n\t" \
  "v_lshlrev_b32 v2, 16, v2\n\t" \
  "s_mov_b32 s81, 0\n\t" \
  "s_sleep 16\n\t" \
  ".Ltl_r3:\n\t" \
  "s_sleep 1\n\t" \
  "buffer_load_dwordx4 v[12:15], v246, s[16:19], s84 offen sc1\n\t" \
  "buffer_load_dwordx4 v[16:19], v246, s[16:19], s85 offen sc1\n\t" \
  "buffer_load_dwordx4 v[20:23], v246, s[16:19], s86 offen sc1\n\t" \
  "buffer_load_dwordx4 v[24:27], v246, s[16:19], s87 offen sc1\n\t" \
  "buffer_load_dwordx4 v[28:31], v246, s[16:19], s88 offen sc1\n\t" \
  "buffer_load_dwordx4 v[32:35], v246, s[16:19], s89 offen sc1\n\t" \
  "buffer_load_dwordx4 v[36:39], v246, s[16:19], s90 offen sc1\n\t" \
  "buffer_load_dwordx2 v[8:9], v1, s[16:19], s69 offen sc1\n\t" \
  "buffer_load_dwordx2 v[10:11], v1, s[16:19], s70 offen sc1\n\t" \
  "s_waitcnt vmcnt(0) lgkmcnt(0)\n\t" \
  "v_xor_b32 v4, s36, v9\n\t" \
  "v_bitop3_b32 v4, v4, v11, s36 bitop3:0xf6\n\t" \
  "v_bitop3_b32 v4, v4, v13, s36 bitop3:0xf6\n\t" \
  "v_bitop3_b32 v4, v4, v15, s36 bitop3:0xf6\n\t" \
  "v_bitop3_b32 v4, v4, v17, s36 bitop3:0xf6\n\t" \
  "v_bitop3_b32 v4, v4, v19, s36 bitop3:0xf6\n\t" \
  "v_bitop3_b32 v4, v4, v21, s36 bitop3:0xf6\n\t" \
  "v_bitop3_b32 v4, v4, v23, s36 bitop3:0xf6\n\t" \
  "v_bitop3_b32 v4, v4, v25, s36 bitop3:0xf6\n\t" \
  "v_bitop3_b32 v4, v4, v27, s36 bitop3:0xf6\n\t" \
  "v_bitop3_b32 v4, v4, v29, s36 bitop3:0xf6\n\t" \
  "v_bitop3_b32 v4, v4, v31, s36 bitop3:0xf6\n\t" \
  "v_bitop3_b32 v4, v4, v33, s36 bitop3:0xf6\n\t" \
  "v_bitop3_b32 v4, v4, v35, s36 bitop3:0xf6\n\t" \
  "v_bitop3_b32 v4, v4, v37, s36 bitop3:0xf6\n\t" \
  "v_bitop3_b32 v4, v4, v39, s36 bitop3:0xf6\n\t" \
  "v_cmp_ne_u32 vcc, 0, v4\n\t" \
  "v_lshlrev_b32 v40, 16, v12\n\t" \
  "v_and_b32 v41, 0xffff0000, v12\n\t" \
  "v_lshlrev_b32 v42, 16, v14\n\t" \
  "v_and_b32 v43, 0xffff0000, v14\n\t" \
  "v_lshlrev_b32 v44, 16, v16\n\t" \
  "v_and_b32 v45, 0xffff0000, v16\n\t" \
  "v_lshlrev_b32 v46, 16, v18\n\t" \
  "v_and_b32 v47, 0xffff0000, v18\n\t" \
  "s_cbranch_vccz .Ltl_k3\n\t" \
  "s_add_u32 s81, s81, 1\n\t" \
  "s_cmp_lt_u32 s81, " FQ_CAP "\n\t" \
  "s_cbranch_scc1 .Ltl_r3\n\t" \
  "buffer_store_dword v243, off, s[16:19], 0 offset:256 sc1\n\t" \
  ".Ltl_k3:\n\t" \
  "v_pk_add_f32 v[40:41], v[40:41], v[44:45]\n\t" \
  "v_pk_add_f32 v[42:43], v[42:43], v[46:47]\n\t" \
  "v_lshlrev_b32 v44, 16, v20\n\t" \
  "v_and_b32 v45, 0xffff0000, v20\n\t" \
  "v_lshlrev_b32 v46, 16, v22\n\t" \
  "v_and_b32 v47, 0xffff0000, v22\n\t" \
  "v_pk_add_f32 v[40:41], v[40:41], v[44:45]\n\t" \
  "v_pk_add_f32 v[42:43], v[42:43], v[46:47]\n\t" \
  "v_lshlrev_b32 v44, 16, v24\n\t" \
  "v_and_b32 v45, 0xffff0000, v24\n\t" \
  "v_lshlrev_b32 v46, 16, v26\n\t" \
  "v_and_b32 v47, 0xffff0000, v26\n\t" \
  "v_pk_add_f32 v[40:41], v[40:41], v[44:45]\n\t" \
  "v_pk_add_f32 v[42:43], v[42:43], v[46:47]\n\t" \
  "v_lshlrev_b32 v44, 16, v28\n\t" \
  "v_and_b32 v45, 0xffff0000, v28\n\t" \
  "v_lshlrev_b32 v46, 16, v30\n\t" \
  "v_and_b32 v47, 0xffff0000, v30\n\t" \
  "v_pk_add_f32 v[40:41], v[40:41], v[44:45]\n\t" \
  "v_pk_add_f32 v[42:43], v[42:43], v[46:47]\n\t" \
  "v_lshlrev_b32 v44, 16, v32\n\t" \
  "v_and_b32 v45, 0xffff0000, v32\n\t" \
  "v_lshlrev_b32 v46, 16, v34\n\t" \
  "v_and_b32 v47, 0xffff0000, v34\n\t" \
  "v_pk_add_f32 v[40:41], v[40:41], v[44:45]\n\t" \
  "v_pk_add_f32 v[42:43], v[42:43], v[46:47]\n\t" \
  "v_lshlrev_b32 v44, 16, v36\n\t" \
  "v_and_b32 v45, 0xffff0000, v36\n\t" \
  "v_lshlrev_b32 v46, 16, v38\n\t" \
  "v_and_b32 v47, 0xffff0000, v38\n\t" \
  "v_pk_add_f32 v[40:41], v[40:41], v[44:45]\n\t" \
  "v_pk_add_f32 v[42:43], v[42:43], v[46:47]\n\t" \
  "s_nop 1\n\t" \
  "v_permlane32_swap_b32 v40, v42\n\t" \
  "v_permlane32_swap_b32 v41, v43\n\t" \
  "v_add_f32 v40, v40, v42\n\t" \
  "v_add_f32 v41, v41, v43\n\t" \
  "s_nop 1\n\t" \
  "v_permlane16_swap_b32 v40, v41\n\t" \
  "v_add_f32 v40, v40, v41\n\t" \
  "s_nop 1\n\t" \
  "v_add_f32_dpp v40, v40, v40 row_ror:8 row_mask:0xf bank_mask:0xf\n\t" \
  "ds_write_b32 v3, v40 offset:12288\n\t" \
  "v_lshlrev_b32 v44, 16, v8\n\t" \
  "v_and_b32 v45, 0xffff0000, v8\n\t" \
  "v_lshlrev_b32 v46, 16, v10\n\t" \
  "v_and_b32 v47, 0xffff0000, v10\n\t" \
  "s_waitcnt lgkmcnt(0)\n\t" \
  "s_barrier\n\t" \
  "s_cmp_lg_u32 s3, 0\n\t" \
  "s_cbranch_scc1 .Ltl_end\n\t" \
  "v_add_u32 v4, 12288, v1\n\t" \
  "ds_read2_b64 v[12:15], v4 offset1:16\n\t" \
  "ds_read2_b64 v[16:19], v4 offset0:32 offset1:48\n\t" \
  "v_lshrrev_b32 v1, 1, v1\n\t" \
  "s_waitcnt lgkmcnt(0)\n\t" \
  "v_pk_add_f32 v[12:13], v[12:13], v[14:15]\n\t" \
  "v_pk_add_f32 v[16:17], v[16:17], v[18:19]\n\t" \
  "v_pk_add_f32 v[12:13], v[12:13], v[16:17]\n\t" \
  "v_cvt_pk_bf16_f32 v13, v12, v13\n\t" \
  "v_lshlrev_b32 v12, 16, v13\n\t" \
  "v_and_b32 v13, 0xffff0000, v13\n\t" \
  "v_mul_f32 v12, v12, v2\n\t" \
  "v_mul_f32 v13, v13, v2\n\t" \
  "v_cvt_pk_bf16_f32 v13, v12, v13\n\t" \
  "v_lshlrev_b32 v12, 16, v13\n\t" \
  "v_and_b32 v13, 0xffff0000, v13\n\t" \
  "v_pk_add_f32 v[12:13], v[12:13], v[44:45]\n\t" \
  "v_cvt_pk_bf16_f32 v13, v12, v13\n\t" \
  "v_lshlrev_b32 v12, 16, v13\n\t" \
  "v_and_b32 v13, 0xffff0000, v13\n\t" \
  "v_pk_mul_f32 v[12:13], v[12:13], v[6:7]\n\t" \
  "v_cvt_pk_bf16_f32 v13, v12, v13\n\t" \
  "v_lshlrev_b32 v12, 16, v13\n\t" \
  "v_and_b32 v13, 0xffff0000, v13\n\t" \
  "v_pk_add_f32 v[12:13], v[12:13], v[46:47]\n\t" \
  "v_cvt_pk_bf16_f32 v12, v12, v13\n\t" \
  "s_mov_b64 exec, 0xffff\n\t" \
  "buffer_store_dword v12, v1, s[4:7], s71 offen sc0 sc1\n\t" \
  "s_mov_b64 exec, -1\n\t" \
  ".Ltl_end:\n\t" \
  STP("s_memrealtime s[94:95]\n\t") \
  STP("s_memtime s[92:93]\n\t") \
  STP("s_waitcnt lgkmcnt(0)\n\t") \
  STP("v_writelane_b32 v255, s94, 52\n\t") \
  STP("v_writelane_b32 v255, s92, 53\n\t") \
  ".subsection 1\n\t" \
  ".Lat_x:\n\t" \
  "v_lshlrev_b32 v209, 12, v233\n\t" \
  "v_add_u32 v209, 0, v209\n\t" \
  "s_mov_b64 exec, 15\n\t" \
  "ds_write_b32 v209, v243\n\t" \
  "s_mov_b64 exec, -1\n\t" \
  "s_waitcnt lgkmcnt(0)\n\t" \
  "s_branch .Lat_y\n\t" \
  ".Lat_rd:\n\t" \
  "s_waitcnt vmcnt(0) lgkmcnt(0)\n\t" \
  "s_barrier\n\t" \
  "s_nop 7\n\t" \
  "v_lshl_add_u32 v0, s3, 6, v233\n\t" \
  "s_mov_b32 s37, 0\n\t" \
  "s_branch .Lfr_again\n\t" \
  ".Lat_s0:\n\t" \
  "s_mov_b32 s81, 0\n\t" \
  ".Lat_r0:\n\t" \
  "s_sleep 4\n\t" \
  "buffer_load_dword v246, v253, s[16:19], s65 offen sc1\n\t" \
  "s_waitcnt vmcnt(0)\n\t" \
  "v_cmp_ne_u32 vcc, s36, v246\n\t" \
  "s_and_b32 vcc_lo, vcc_lo, 0xe\n\t" \
  "s_cbranch_scc0 .Lat_k0\n\t" \
  "s_add_u32 s81, s81, 1\n\t" \
  "s_cmp_lt_u32 s81, " FQ_CAP "\n\t" \
  "s_cbranch_scc1 .Lat_r0\n\t" \
  "buffer_store_dword v243, off, s[16:19], 0 offset:256 sc1\n\t" \
  "s_branch .Lat_k0\n\t" \
  ".Lat_s1:\n\t" \
  "s_mov_b32 s81, 0\n\t" \
  ".Lat_r1:\n\t" \
  "s_sleep 4\n\t" \
  "buffer_load_dword v246, v253, s[16:19], s65 offen sc1\n\t" \
  "s_waitcnt vmcnt(0)\n\t" \
  "v_cmp_ne_u32 vcc, s36, v246\n\t" \
  "s_and_b32 vcc_lo, vcc_lo, 0xfefe\n\t" \
  "s_cbranch_scc0 .Lat_k1\n\t" \
  "s_add_u32 s81, s81, 1\n\t" \
  "s_cmp_lt_u32 s81, " FQ_CAP "\n\t" \
  "s_cbranch_scc1 .Lat_r1\n\t" \
  "buffer_store_dword v243, off, s[16:19], 0 offset:256 sc1\n\t" \
  "s_branch .Lat_k1\n\t" \
  ".Lat_s2:\n\t" \
  "s_mov_b32 s81, 0\n\t" \
  ".Lat_r2:\n\t" \
  "s_sleep 4\n\t" \
  "buffer_load_dword v246, v253, s[16:19], s65 offen sc1\n\t" \
  "s_waitcnt vmcnt(0)\n\t" \
  "v_cmp_ne_u32 vcc, s36, v246\n\t" \
  "s_and_b32 vcc_lo, vcc_lo, 0xfe0000\n\t" \
  "s_cbranch_scc0 .Lat_k2\n\t" \
  "s_add_u32 s81, s81, 1\n\t" \
  "s_cmp_lt_u32 s81, " FQ_CAP "\n\t" \
  "s_cbranch_scc1 .Lat_r2\n\t" \
  "buffer_store_dword v243, off, s[16:19], 0 offset:256 sc1\n\t" \
  "s_branch .Lat_k2\n\t" \
  ".Lat_s3:\n\t" \
  "s_mov_b32 s81, 0\n\t" \
  ".Lat_r3:\n\t" \
  "s_sleep 4\n\t" \
  "s_add_u32 s78, s75, 0\n\t" \
  "buffer_load_dwordx4 v[208:211], v234, s[16:19], s78 offen sc1\n\t" \
  "s_add_u32 s78, s75, 32768\n\t" \
  "buffer_load_dwordx4 v[212:215], v234, s[16:19], s78 offen sc1\n\t" \
  "s_add_u32 s78, s75, 65536\n\t" \
  "buffer_load_dwordx4 v[216:219], v234, s[16:19], s78 offen sc1\n\t" \
  "s_add_u32 s78, s75, 98304\n\t" \
  "buffer_load_dwordx4 v[220:223], v234, s[16:19], s78 offen sc1\n\t" \
  "s_waitcnt vmcnt(0)\n\t" \
  "v_xor_b32 v246, v243, v209\n\t" \
  "v_bitop3_b32 v246, v246, v211, v243 bitop3:0xf6\n\t" \
  "v_bitop3_b32 v246, v246, v213, v243 bitop3:0xf6\n\t" \
  "v_bitop3_b32 v246, v246, v215, v243 bitop3:0xf6\n\t" \
  "v_bitop3_b32 v246, v246, v217, v243 bitop3:0xf6\n\t" \
  "v_bitop3_b32 v246, v246, v219, v243 bitop3:0xf6\n\t" \
  "v_bitop3_b32 v246, v246, v221, v243 bitop3:0xf6\n\t" \
  "v_bitop3_b32 v246, v246, v223, v243 bitop3:0xf6\n\t" \
  "v_cmp_ne_u32 vcc, 0, v246\n\t" \
  "s_cbranch_vccz .Lat_k3\n\t" \
  "s_add_u32 s81, s81, 1\n\t" \
  "s_cmp_lt_u32 s81, " FQ_CAP "\n\t" \
  "s_cbranch_scc1 .Lat_r3\n\t" \
  "buffer_store_dword v243, off, s[16:19], 0 offset:256 sc1\n\t" \
  "s_branch .Lat_k3\n\t" \
  ".Lat_s4:\n\t" \
  "s_mov_b32 s81, 0\n\t" \
  ".Lat_r4:\n\t" \
  "s_sleep 4\n\t" \
  "buffer_load_dword v246, v235, s[16:19], s73 offen sc1\n\t" \
  "s_waitcnt vmcnt(0)\n\t" \
  "v_cmp_ne_u32 vcc, s36, v246\n\t" \
  "s_cbranch_vccz .Lat_k4\n\t" \
  "s_add_u32 s81, s81, 1\n\t" \
  "s_cmp_lt_u32 s81, " FQ_CAP "\n\t" \
  "s_cbranch_scc1 .Lat_r4\n\t" \
  "buffer_store_dword v243, off, s[16:19], 0 offset:256 sc1\n\t" \
  "s_branch .Lat_k4\n\t" \
  ".Lat_m3:\n\t" \
  "s_waitcnt vmcnt(0)\n\t" \
  "v_cmp_ne_u32 vcc, s36, v251\n\t" \
  "s_mov_b32 s88, vcc_lo\n\t" \
  "s_cbranch_vccnz .Lat_n3\n\t" \
  "buffer_load_dwordx4 v[64:67], v242, s[16:19], s79 offen offset:0 sc1\n\t" \
  "buffer_load_dwordx4 v[68:71], v242, s[16:19], s79 offen offset:1024 sc1\n\t" \
  "buffer_load_dwordx4 v[72:75], v242, s[16:19], s79 offen offset:2048 sc1\n\t" \
  "buffer_load_dwordx4 v[76:79], v242, s[16:19], s79 offen offset:3072 sc1\n\t" \
  "s_branch .Lat_n3\n\t" \
  ".Lat_v:\n\t" \
  "s_mov_b32 s98, 0\n\t" \
  ".Lat_v1:\n\t" \
  "s_sleep 4\n\t" \
  "buffer_load_dword v209, v251, s[16:19], 0 offen sc1\n\t" \
  "s_waitcnt vmcnt(0)\n\t" \
  "v_and_b32 v250, 0xbfffbfff, v209\n\t" \
  "v_cmp_ne_u32_e64 s[84:85], s36, v250\n\t" \
  "s_cmp_lg_u64 s[84:85], 0\n\t" \
  "s_cbranch_scc0 .Lat_w\n\t" \
  "s_add_u32 s98, s98, 1\n\t" \
  "s_cmp_lt_u32 s98, " FQ_CAP "\n\t" \
  "s_cbranch_scc1 .Lat_v1\n\t" \
  "v_mov_b32 v250, s36\n\t" \
  "buffer_store_dword v250, off, s[16:19], 0 offset:256 sc1\n\t" \
  "s_branch .Lat_w\n\t" \
  ".Lat_s99:\n\t" \
  "s_mov_b32 s81, 0\n\t" \
  ".Lat_r99:\n\t" \
  "buffer_load_dword v246, off, s[16:19], s73 offset:32 sc1\n\t" \
  "s_waitcnt vmcnt(0)\n\t" \
  "v_cmp_ne_u32 vcc, s36, v246\n\t" \
  "s_cbranch_vccz .Lat_k99\n\t" \
  "s_add_u32 s81, s81, 1\n\t" \
  "s_cmp_lt_u32 s81, " FQ_CAP "\n\t" \
  "s_cbranch_scc1 .Lat_r99\n\t" \
  "buffer_store_dword v243, off, s[16:19], 0 offset:256 sc1\n\t" \
  ".Lat_k99:\n\t" \
  "buffer_load_dwordx4 v[64:67], v242, s[16:19], s79 offen offset:0 sc1\n\t" \
  "buffer_load_dwordx4 v[68:71], v242, s[16:19], s79 offen offset:1024 sc1\n\t" \
  "buffer_load_dwordx4 v[72:75], v242, s[16:19], s79 offen offset:2048 sc1\n\t" \
  "buffer_load_dwordx4 v[76:79], v242, s[16:19], s79 offen offset:3072 sc1\n\t" \
  "s_waitcnt vmcnt(0)\n\t" \
  "s_branch .Lat_d2\n\t" \
  ".Ltl_g2:\n\t" \
  "buffer_store_dword v243, off, s[16:19], 0 offset:256 sc1\n\t" \
  "s_branch .Ltl_k2\n\t" \
  "s_endpgm\n\t" \
  ".Lfz_z:\n\t" \
  ".fill 128, 4, 0xBF800000\n\t" \
  ".subsection 2\n\t" \
  ".Lsf_go:\n\t" \
  "s_barrier\n\t" \
  "v_mfma_f32_16x16x32_bf16 v[28:31], a[188:191], a[192:195], v[28:31]\n\t" \
  "ds_read_b64_tr_b16 a[156:157], v199 offset:32768\n\t" \
  "v_mfma_f32_16x16x32_bf16 v[28:31], a[124:127], a[208:211], v[28:31]\n\t" \
  "ds_read_b64_tr_b16 a[158:159], v199 offset:33792\n\t" \
  "v_mfma_f32_16x16x32_bf16 v[24:27], a[184:187], a[192:195], v[24:27]\n\t" \
  "ds_read_b64_tr_b16 a[92:93], v199 offset:40960\n\t" \
  "v_mfma_f32_16x16x32_bf16 v[24:27], a[120:123], a[208:211], v[24:27]\n\t" \
  "ds_read_b64_tr_b16 a[94:95], v199 offset:41984\n\t" \
  "v_mfma_f32_16x16x32_bf16 v[20:23], a[180:183], a[192:195], v[20:23]\n\t" \
  "ds_read_b64_tr_b16 a[152:153], v198 offset:32768\n\t" \
  "v_mfma_f32_16x16x32_bf16 v[20:23], a[116:119], a[208:211], v[20:23]\n\t" \
  "ds_read_b64_tr_b16 a[154:155], v198 offset:33792\n\t" \
  "v_mfma_f32_16x16x32_bf16 v[16:19], a[176:179], a[192:195], v[16:19]\n\t" \
  "ds_read_b64_tr_b16 a[88:89], v198 offset:40960\n\t" \
  "v_mfma_f32_16x16x32_bf16 v[16:19], a[112:115], a[208:211], v[16:19]\n\t" \
  "ds_read_b64_tr_b16 a[90:91], v198 offset:41984\n\t" \
  "v_mfma_f32_16x16x32_bf16 v[12:15], a[172:175], a[192:195], v[12:15]\n\t" \
  "ds_read_b64_tr_b16 a[148:149], v197 offset:32768\n\t" \
  "v_mfma_f32_16x16x32_bf16 v[12:15], a[108:111], a[208:211], v[12:15]\n\t" \
  "ds_read_b64_tr_b16 a[150:151], v197 offset:33792\n\t" \
  "v_mfma_f32_16x16x32_bf16 v[8:11], a[168:171], a[192:195], v[8:11]\n\t" \
  "ds_read_b64_tr_b16 a[84:85], v197 offset:40960\n\t" \
  "v_mfma_f32_16x16x32_bf16 v[8:11], a[104:107], a[208:211], v[8:11]\n\t" \
  "ds_read_b64_tr_b16 a[86:87], v197 offset:41984\n\t" \
  "v_mfma_f32_16x16x32_bf16 v[4:7], a[164:167], a[192:195], v[4:7]\n\t" \
  "ds_read_b64_tr_b16 a[144:145], v196 offset:32768\n\t" \
  "v_mfma_f32_16x16x32_bf16 v[4:7], a[100:103], a[208:211], v[4:7]\n\t" \
  "ds_read_b64_tr_b16 a[146:147], v196 offset:33792\n\t" \
  "v_mfma_f32_16x16x32_bf16 v[0:3], a[160:163], a[192:195], v[0:3]\n\t" \
  "ds_read_b64_tr_b16 a[80:81], v196 offset:40960\n\t" \
  "v_mfma_f32_16x16x32_bf16 v[0:3], a[96:99], a[208:211], v[0:3]\n\t" \
  "ds_read_b64_tr_b16 a[82:83], v196 offset:41984\n\t" \
  "v_mfma_f32_16x16x32_bf16 v[60:63], a[188:191], a[196:199], v[60:63]\n\t" \
  "ds_read_b64_tr_b16 a[140:141], v195 offset:32768\n\t" \
  "v_mfma_f32_16x16x32_bf16 v[60:63], a[124:127], a[212:215], v[60:63]\n\t" \
  "ds_read_b64_tr_b16 a[142:143], v195 offset:33792\n\t" \
  "v_mfma_f32_16x16x32_bf16 v[56:59], a[184:187], a[196:199], v[56:59]\n\t" \
  "ds_read_b64_tr_b16 a[76:77], v195 offset:40960\n\t" \
  "v_mfma_f32_16x16x32_bf16 v[56:59], a[120:123], a[212:215], v[56:59]\n\t" \
  "ds_read_b64_tr_b16 a[78:79], v195 offset:41984\n\t" \
  "v_mfma_f32_16x16x32_bf16 v[52:55], a[180:183], a[196:199], v[52:55]\n\t" \
  "ds_read_b64_tr_b16 a[136:137], v194 offset:32768\n\t" \
  "v_mfma_f32_16x16x32_bf16 v[52:55], a[116:119], a[212:215], v[52:55]\n\t" \
  "ds_read_b64_tr_b16 a[138:139], v194 offset:33792\n\t" \
  "v_mfma_f32_16x16x32_bf16 v[48:51], a[176:179], a[196:199], v[48:51]\n\t" \
  "ds_read_b64_tr_b16 a[72:73], v194 offset:40960\n\t" \
  "v_mfma_f32_16x16x32_bf16 v[48:51], a[112:115], a[212:215], v[48:51]\n\t" \
  "ds_read_b64_tr_b16 a[74:75], v194 offset:41984\n\t" \
  "v_mfma_f32_16x16x32_bf16 v[44:47], a[172:175], a[196:199], v[44:47]\n\t" \
  "ds_read_b64_tr_b16 a[132:133], v193 offset:32768\n\t" \
  "v_mfma_f32_16x16x32_bf16 v[44:47], a[108:111], a[212:215], v[44:47]\n\t" \
  "ds_read_b64_tr_b16 a[134:135], v193 offset:33792\n\t" \
  "v_mfma_f32_16x16x32_bf16 v[40:43], a[168:171], a[196:199], v[40:43]\n\t" \
  "ds_read_b64_tr_b16 a[68:69], v193 offset:40960\n\t" \
  "v_mfma_f32_16x16x32_bf16 v[40:43], a[104:107], a[212:215], v[40:43]\n\t" \
  "ds_read_b64_tr_b16 a[70:71], v193 offset:41984\n\t" \
  "v_mfma_f32_16x16x32_bf16 v[36:39], a[164:167], a[196:199], v[36:39]\n\t" \
  "ds_read_b64_tr_b16 a[128:129], v192 offset:32768\n\t" \
  "v_mfma_f32_16x16x32_bf16 v[36:39], a[100:103], a[212:215], v[36:39]\n\t" \
  "ds_read_b64_tr_b16 a[130:131], v192 offset:33792\n\t" \
  "v_mfma_f32_16x16x32_bf16 v[32:35], a[160:163], a[196:199], v[32:35]\n\t" \
  "ds_read_b64_tr_b16 a[64:65], v192 offset:40960\n\t" \
  "v_mfma_f32_16x16x32_bf16 v[32:35], a[96:99], a[212:215], v[32:35]\n\t" \
  "s_waitcnt lgkmcnt(15)\n\t" \
  "ds_read_b64_tr_b16 a[66:67], v192 offset:41984\n\t" \
  "v_mfma_f32_16x16x32_bf16 v[28:31], a[156:159], a[224:227], v[28:31]\n\t" \
  "v_mfma_f32_16x16x32_bf16 v[28:31], a[92:95], a[240:243], v[28:31]\n\t" \
  "v_mfma_f32_16x16x32_bf16 v[24:27], a[152:155], a[224:227], v[24:27]\n\t" \
  "s_waitcnt vmcnt(3)\n\t" \
  "s_nop 1\n\t" \
  "v_mfma_f32_16x16x32_bf16 v[24:27], a[88:91], a[240:243], v[24:27]\n\t" \
  "s_barrier\n\t" \
  "v_mfma_f32_16x16x32_bf16 v[20:23], a[148:151], a[224:227], v[20:23]\n\t" \
  "ds_read_b128 a[0:3], v220 offset:0\n\t" \
  "v_cvt_pk_bf16_f32 v31, v30, v31\n\t" \
  "v_mfma_f32_16x16x32_bf16 v[20:23], a[84:87], a[240:243], v[20:23]\n\t" \
  "ds_read_b128 a[4:7], v221 offset:0\n\t" \
  "v_cvt_pk_bf16_f32 v30, v28, v29\n\t" \
  "v_mfma_f32_16x16x32_bf16 v[16:19], a[144:147], a[224:227], v[16:19]\n\t" \
  "v_cvt_pk_bf16_f32 v28, v24, v25\n\t" \
  "v_cvt_pk_bf16_f32 v29, v26, v27\n\t" \
  "v_mfma_f32_16x16x32_bf16 v[16:19], a[80:83], a[240:243], v[16:19]\n\t" \
  "s_waitcnt lgkmcnt(10)\n\t" \
  "v_permlane16_swap_b32 v28, v30\n\t" \
  "v_mfma_f32_16x16x32_bf16 v[12:15], a[140:143], a[224:227], v[12:15]\n\t" \
  "ds_read_b128 a[8:11], v222 offset:0\n\t" \
  "v_cvt_pk_bf16_f32 v27, v22, v23\n\t" \
  "v_mfma_f32_16x16x32_bf16 v[12:15], a[76:79], a[240:243], v[12:15]\n\t" \
  "v_permlane16_swap_b32 v29, v31\n\t" \
  "v_mfma_f32_16x16x32_bf16 v[8:11], a[136:139], a[224:227], v[8:11]\n\t" \
  "ds_read_b128 a[12:15], v223 offset:0\n\t" \
  "v_cvt_pk_bf16_f32 v26, v20, v21\n\t" \
  "v_mfma_f32_16x16x32_bf16 v[8:11], a[72:75], a[240:243], v[8:11]\n\t" \
  "s_waitcnt lgkmcnt(4)\n\t" \
  "v_cvt_pk_bf16_f32 v24, v16, v17\n\t" \
  "v_cvt_pk_bf16_f32 v25, v18, v19\n\t" \
  "v_mfma_f32_16x16x32_bf16 v[4:7], a[132:135], a[224:227], v[4:7]\n\t" \
  "ds_read_b128 a[16:19], v220 offset:4096\n\t" \
  "v_cvt_pk_bf16_f32 v23, v14, v15\n\t" \
  "v_mfma_f32_16x16x32_bf16 v[4:7], a[68:71], a[240:243], v[4:7]\n\t" \
  "v_cvt_pk_bf16_f32 v22, v12, v13\n\t" \
  "v_cvt_pk_bf16_f32 v20, v8, v9\n\t" \
  "v_mfma_f32_16x16x32_bf16 v[0:3], a[128:131], a[224:227], v[0:3]\n\t" \
  "ds_read_b128 a[20:23], v221 offset:4096\n\t" \
  "v_mov_b32 v196, 1.0\n\t" \
  "v_mfma_f32_16x16x32_bf16 v[0:3], a[64:67], a[240:243], v[0:3]\n\t" \
  "ds_read_b128 a[24:27], v222 offset:4096\n\t" \
  "v_permlane16_swap_b32 v24, v26\n\t" \
  "v_mfma_f32_16x16x32_bf16 v[60:63], a[156:159], a[228:231], v[60:63]\n\t" \
  "v_mov_b32 v197, 1.0\n\t" \
  "v_cvt_pk_bf16_f32 v21, v10, v11\n\t" \
  "v_mfma_f32_16x16x32_bf16 v[60:63], a[92:95], a[244:247], v[60:63]\n\t" \
  "v_mov_b32 v198, 1.0\n\t" \
  "v_permlane16_swap_b32 v25, v27\n\t" \
  "v_mfma_f32_16x16x32_bf16 v[56:59], a[152:155], a[228:231], v[56:59]\n\t" \
  "ds_read_b128 a[28:31], v223 offset:4096\n\t" \
  "v_mov_b32 v199, 1.0\n\t" \
  "v_mfma_f32_16x16x32_bf16 v[56:59], a[88:91], a[244:247], v[56:59]\n\t" \
  "s_mov_b32 s99, 0\n\t" \
  "ds_read_b128 a[32:35], v220 offset:8192\n\t" \
  "v_cvt_pk_bf16_f32 v19, v6, v7\n\t" \
  "v_mfma_f32_16x16x32_bf16 v[52:55], a[148:151], a[228:231], v[52:55]\n\t" \
  "v_cvt_pk_bf16_f32 v18, v4, v5\n\t" \
  "v_permlane16_swap_b32 v20, v22\n\t" \
  "v_mfma_f32_16x16x32_bf16 v[52:55], a[84:87], a[244:247], v[52:55]\n\t" \
  "ds_read_b128 a[36:39], v221 offset:8192\n\t" \
  "v_cvt_pk_bf16_f32 v16, v0, v1\n\t" \
  "v_mfma_f32_16x16x32_bf16 v[48:51], a[144:147], a[228:231], v[48:51]\n\t" \
  "v_cvt_pk_bf16_f32 v17, v2, v3\n\t" \
  "v_permlane16_swap_b32 v21, v23\n\t" \
  "v_mfma_f32_16x16x32_bf16 v[48:51], a[80:83], a[244:247], v[48:51]\n\t" \
  "ds_read_b128 a[40:43], v222 offset:8192\n\t" \
  "v_cvt_pk_bf16_f32 v63, v62, v63\n\t" \
  "v_mfma_f32_16x16x32_bf16 v[44:47], a[140:143], a[228:231], v[44:47]\n\t" \
  "v_permlane16_swap_b32 v16, v18\n\t" \
  "v_mfma_f32_16x16x32_bf16 v[44:47], a[76:79], a[244:247], v[44:47]\n\t" \
  "ds_read_b128 a[44:47], v223 offset:8192\n\t" \
  "v_permlane16_swap_b32 v17, v19\n\t" \
  "v_mfma_f32_16x16x32_bf16 v[40:43], a[136:139], a[228:231], v[40:43]\n\t" \
  "v_cvt_pk_bf16_f32 v62, v60, v61\n\t" \
  "v_cvt_pk_bf16_f32 v60, v56, v57\n\t" \
  "v_mfma_f32_16x16x32_bf16 v[40:43], a[72:75], a[244:247], v[40:43]\n\t" \
  "ds_read_b128 a[48:51], v220 offset:12288\n\t" \
  "v_cvt_pk_bf16_f32 v61, v58, v59\n\t" \
  "v_mfma_f32_16x16x32_bf16 v[36:39], a[132:135], a[228:231], v[36:39]\n\t" \
  "v_cvt_pk_bf16_f32 v59, v54, v55\n\t" \
  "v_cvt_pk_bf16_f32 v58, v52, v53\n\t" \
  "v_mfma_f32_16x16x32_bf16 v[36:39], a[68:71], a[244:247], v[36:39]\n\t" \
  "ds_read_b128 a[52:55], v221 offset:12288\n\t" \
  "v_cvt_pk_bf16_f32 v56, v48, v49\n\t" \
  "v_mfma_f32_16x16x32_bf16 v[32:35], a[128:131], a[228:231], v[32:35]\n\t" \
  "ds_read_b128 a[56:59], v222 offset:12288\n\t" \
  "v_cvt_pk_bf16_f32 v57, v50, v51\n\t" \
  "v_mfma_f32_16x16x32_bf16 v[32:35], a[64:67], a[244:247], v[32:35]\n\t" \
  "s_waitcnt lgkmcnt(11)\n\t" \
  "ds_read_b128 a[60:63], v223 offset:12288\n\t" \
  "v_cvt_pk_bf16_f32 v55, v46, v47\n\t" \
  "v_mfma_f32_16x16x32_bf16 v[0:3], a[0:3], v[16:19], 0\n\t" \
  "v_cvt_pk_bf16_f32 v54, v44, v45\n\t" \
  "v_cvt_pk_bf16_f32 v52, v40, v41\n\t" \
  "v_mfma_f32_16x16x32_bf16 v[0:3], a[4:7], v[20:23], v[0:3]\n\t" \
  "v_permlane16_swap_b32 v60, v62\n\t" \
  "v_mfma_f32_16x16x32_bf16 v[0:3], a[8:11], v[24:27], v[0:3]\n\t" \
  "v_permlane16_swap_b32 v61, v63\n\t" \
  "v_mfma_f32_16x16x32_bf16 v[0:3], a[12:15], v[28:31], v[0:3]\n\t" \
  "s_waitcnt lgkmcnt(8)\n\t" \
  "v_permlane16_swap_b32 v56, v58\n\t" \
  "v_mfma_f32_16x16x32_bf16 v[4:7], a[16:19], v[16:19], 0\n\t" \
  "s_add_u32 m0, s38, 49152\n\t" \
  "v_cvt_pk_bf16_f32 v53, v42, v43\n\t" \
  "v_cvt_pk_bf16_f32 v51, v38, v39\n\t" \
  "v_mfma_f32_16x16x32_bf16 v[4:7], a[20:23], v[20:23], v[4:7]\n\t" \
  "s_add_u32 s96, s38, 32768\n\t" \
  "v_permlane16_swap_b32 v57, v59\n\t" \
  "v_mfma_f32_16x16x32_bf16 v[4:7], a[24:27], v[24:27], v[4:7]\n\t" \
  "s_waitcnt lgkmcnt(0)\n\t" \
  "v_cvt_pk_bf16_f32 v50, v36, v37\n\t" \
  "v_cvt_pk_bf16_f32 v48, v32, v33\n\t" \
  "v_mfma_f32_16x16x32_bf16 v[4:7], a[28:31], v[28:31], v[4:7]\n\t" \
  "s_barrier\n\t" \
  "v_mfma_f32_16x16x32_bf16 v[92:95], a[188:191], a[200:203], v[92:95]\n\t" \
  "buffer_load_dwordx4 v224, s[60:63], s96 offen offset:0 lds\n\t" \
  "v_mfma_f32_16x16x32_bf16 v[92:95], a[124:127], a[216:219], v[92:95]\n\t" \
  "v_cvt_pk_bf16_f32 v49, v34, v35\n\t" \
  "v_max3_f32 v200, v0, v1, v2\n\t" \
  "v_mfma_f32_16x16x32_bf16 v[88:91], a[184:187], a[200:203], v[88:91]\n\t" \
  "v_permlane16_swap_b32 v52, v54\n\t" \
  "v_mfma_f32_16x16x32_bf16 v[88:91], a[120:123], a[216:219], v[88:91]\n\t" \
  "v_max3_f32 v200, v200, v3, v4\n\t" \
  "v_max3_f32 v200, v200, v5, v6\n\t" \
  "v_mfma_f32_16x16x32_bf16 v[84:87], a[180:183], a[200:203], v[84:87]\n\t" \
  "v_max_f32 v200, v200, v7\n\t" \
  "v_mov_b32 v201, v200\n\t" \
  "v_mfma_f32_16x16x32_bf16 v[84:87], a[116:119], a[216:219], v[84:87]\n\t" \
  "v_permlane16_swap_b32 v53, v55\n\t" \
  "v_mfma_f32_16x16x32_bf16 v[80:83], a[176:179], a[200:203], v[80:83]\n\t" \
  "v_permlane32_swap_b32 v200, v201\n\t" \
  "v_mfma_f32_16x16x32_bf16 v[80:83], a[112:115], a[216:219], v[80:83]\n\t" \
  "v_max_f32 v200, v200, v201\n\t" \
  "v_mov_b32 v201, v200\n\t" \
  "v_mfma_f32_16x16x32_bf16 v[76:79], a[172:175], a[200:203], v[76:79]\n\t" \
  "v_permlane16_swap_b32 v48, v50\n\t" \
  "v_mfma_f32_16x16x32_bf16 v[76:79], a[108:111], a[216:219], v[76:79]\n\t" \
  "v_permlane16_swap_b32 v200, v201\n\t" \
  "v_mfma_f32_16x16x32_bf16 v[72:75], a[168:171], a[200:203], v[72:75]\n\t" \
  "v_max_f32 v200, v200, v201\n\t" \
  "v_sub_f32 v176, 0xc2c00000, v200\n\t" \
  "v_mfma_f32_16x16x32_bf16 v[72:75], a[104:107], a[216:219], v[72:75]\n\t" \
  "v_sub_f32 v177, 0xc2c00000, v200\n\t" \
  "v_sub_f32 v178, 0xc2c00000, v200\n\t" \
  "v_mfma_f32_16x16x32_bf16 v[68:71], a[164:167], a[200:203], v[68:71]\n\t" \
  "v_sub_f32 v179, 0xc2c00000, v200\n\t" \
  "v_mfma_f32_16x16x32_bf16 v[68:71], a[100:103], a[216:219], v[68:71]\n\t" \
  "v_permlane16_swap_b32 v49, v51\n\t" \
  "v_mfma_f32_16x16x32_bf16 v[64:67], a[160:163], a[200:203], v[64:67]\n\t" \
  "v_add_f32 v0, v0, v176\n\t" \
  "v_add_f32 v1, v1, v176\n\t" \
  "v_mfma_f32_16x16x32_bf16 v[64:67], a[96:99], a[216:219], v[64:67]\n\t" \
  "v_add_f32 v2, v2, v176\n\t" \
  "v_add_f32 v3, v3, v176\n\t" \
  "v_mfma_f32_16x16x32_bf16 v[8:11], a[32:35], v[16:19], v[176:179]\n\t" \
  "buffer_load_dwordx4 v225, s[60:63], s96 offen offset:1024 lds\n\t" \
  "v_mfma_f32_16x16x32_bf16 v[8:11], a[36:39], v[20:23], v[8:11]\n\t" \
  "v_add_f32 v4, v4, v176\n\t" \
  "v_add_f32 v5, v5, v176\n\t" \
  "v_mfma_f32_16x16x32_bf16 v[8:11], a[40:43], v[24:27], v[8:11]\n\t" \
  "v_add_f32 v6, v6, v176\n\t" \
  "v_add_f32 v7, v7, v176\n\t" \
  "v_mfma_f32_16x16x32_bf16 v[8:11], a[44:47], v[28:31], v[8:11]\n\t" \
  "s_nop 2\n\t" \
  "v_mfma_f32_16x16x32_bf16 v[12:15], a[48:51], v[16:19], v[176:179]\n\t" \
  "s_nop 2\n\t" \
  "v_mfma_f32_16x16x32_bf16 v[12:15], a[52:55], v[20:23], v[12:15]\n\t" \
  "v_max3_f32 v204, v8, v9, v10\n\t" \
  "v_mfma_f32_16x16x32_bf16 v[12:15], a[56:59], v[24:27], v[12:15]\n\t" \
  "s_nop 2\n\t" \
  "v_mfma_f32_16x16x32_bf16 v[12:15], a[60:63], v[28:31], v[12:15]\n\t" \
  "s_nop 2\n\t" \
  "v_mfma_f32_16x16x32_bf16 v[32:35], a[0:3], v[48:51], 0\n\t" \
  "s_nop 2\n\t" \
  "v_mfma_f32_16x16x32_bf16 v[32:35], a[4:7], v[52:55], v[32:35]\n\t" \
  "v_max3_f32 v203, v11, v12, v13\n\t" \
  "v_max3_f32 v204, v204, v14, v15\n\t" \
  "v_mfma_f32_16x16x32_bf16 v[32:35], a[8:11], v[56:59], v[32:35]\n\t" \
  "v_max_f32 v204, v204, v203\n\t" \
  "v_cmp_lt_f32 vcc, 0x42800000, v204\n\t" \
  "v_mfma_f32_16x16x32_bf16 v[32:35], a[12:15], v[60:63], v[32:35]\n\t" \
  "s_nop 2\n\t" \
  "v_mfma_f32_16x16x32_bf16 v[36:39], a[16:19], v[48:51], 0\n\t" \
  "s_cbranch_vccnz .Lsf_c00\n\t" \
  ".Lsf_r00:\n\t" \
  "v_mfma_f32_16x16x32_bf16 v[36:39], a[20:23], v[52:55], v[36:39]\n\t" \
  "v_exp_f32 v0, v0\n\t" \
  "v_mfma_f32_16x16x32_bf16 v[36:39], a[24:27], v[56:59], v[36:39]\n\t" \
  "v_exp_f32 v1, v1\n\t" \
  "v_mfma_f32_16x16x32_bf16 v[36:39], a[28:31], v[60:63], v[36:39]\n\t" \
  "v_max3_f32 v200, v32, v33, v34\n\t" \
  "v_cvt_pk_bf16_f32 v128, v0, v1\n\t" \
  "v_mfma_f32_16x16x32_bf16 v[92:95], a[156:159], a[232:235], v[92:95]\n\t" \
  "buffer_load_dwordx4 v226, s[60:63], s96 offen offset:2048 lds\n\t" \
  "v_mfma_f32_16x16x32_bf16 v[92:95], a[92:95], a[248:251], v[92:95]\n\t" \
  "v_exp_f32 v2, v2\n\t" \
  "v_mfma_f32_16x16x32_bf16 v[88:91], a[152:155], a[232:235], v[88:91]\n\t" \
  "v_exp_f32 v3, v3\n\t" \
  "v_mfma_f32_16x16x32_bf16 v[88:91], a[88:91], a[248:251], v[88:91]\n\t" \
  "v_max3_f32 v200, v200, v35, v36\n\t" \
  "v_max3_f32 v200, v200, v37, v38\n\t" \
  "v_mfma_f32_16x16x32_bf16 v[84:87], a[148:151], a[232:235], v[84:87]\n\t" \
  "v_max_f32 v200, v200, v39\n\t" \
  "v_mov_b32 v201, v200\n\t" \
  "v_mfma_f32_16x16x32_bf16 v[84:87], a[84:87], a[248:251], v[84:87]\n\t" \
  "v_cvt_pk_bf16_f32 v95, v94, v95\n\t" \
  "v_cvt_pk_bf16_f32 v94, v92, v93\n\t" \
  "v_mfma_f32_16x16x32_bf16 v[80:83], a[144:147], a[232:235], v[80:83]\n\t" \
  "v_cvt_pk_bf16_f32 v92, v88, v89\n\t" \
  "v_permlane32_swap_b32 v200, v201\n\t" \
  "v_mfma_f32_16x16x32_bf16 v[80:83], a[80:83], a[248:251], v[80:83]\n\t" \
  "v_max_f32 v200, v200, v201\n\t" \
  "v_mov_b32 v201, v200\n\t" \
  "v_mfma_f32_16x16x32_bf16 v[76:79], a[140:143], a[232:235], v[76:79]\n\t" \
  "v_cvt_pk_bf16_f32 v93, v90, v91\n\t" \
  "v_cvt_pk_bf16_f32 v91, v86, v87\n\t" \
  "v_mfma_f32_16x16x32_bf16 v[76:79], a[76:79], a[248:251], v[76:79]\n\t" \
  "v_permlane16_swap_b32 v200, v201\n\t" \
  "v_mfma_f32_16x16x32_bf16 v[72:75], a[136:139], a[232:235], v[72:75]\n\t" \
  "v_max_f32 v200, v200, v201\n\t" \
  "v_sub_f32 v180, 0xc2c00000, v200\n\t" \
  "v_mfma_f32_16x16x32_bf16 v[72:75], a[72:75], a[248:251], v[72:75]\n\t" \
  "v_sub_f32 v181, 0xc2c00000, v200\n\t" \
  "v_sub_f32 v182, 0xc2c00000, v200\n\t" \
  "v_mfma_f32_16x16x32_bf16 v[68:71], a[132:135], a[232:235], v[68:71]\n\t" \
  "v_cvt_pk_bf16_f32 v90, v84, v85\n\t" \
  "v_sub_f32 v183, 0xc2c00000, v200\n\t" \
  "v_mfma_f32_16x16x32_bf16 v[68:71], a[68:71], a[248:251], v[68:71]\n\t" \
  "v_cvt_pk_bf16_f32 v88, v80, v81\n\t" \
  "v_cvt_pk_bf16_f32 v89, v82, v83\n\t" \
  "v_mfma_f32_16x16x32_bf16 v[64:67], a[128:131], a[232:235], v[64:67]\n\t" \
  "v_cvt_pk_bf16_f32 v87, v78, v79\n\t" \
  "v_permlane16_swap_b32 v92, v94\n\t" \
  "v_mfma_f32_16x16x32_bf16 v[64:67], a[64:67], a[248:251], v[64:67]\n\t" \
  "v_cvt_pk_bf16_f32 v86, v76, v77\n\t" \
  "v_permlane16_swap_b32 v93, v95\n\t" \
  "v_mfma_f32_16x16x32_bf16 v[124:127], a[188:191], a[204:207], v[124:127]\n\t" \
  "buffer_load_dwordx4 v227, s[60:63], s96 offen offset:3072 lds\n\t" \
  "v_mfma_f32_16x16x32_bf16 v[124:127], a[124:127], a[220:223], v[124:127]\n\t" \
  "v_cvt_pk_bf16_f32 v84, v72, v73\n\t" \
  "v_permlane16_swap_b32 v88, v90\n\t" \
  "v_mfma_f32_16x16x32_bf16 v[120:123], a[184:187], a[204:207], v[120:123]\n\t" \
  "v_cvt_pk_bf16_f32 v85, v74, v75\n\t" \
  "v_cvt_pk_bf16_f32 v83, v70, v71\n\t" \
  "v_mfma_f32_16x16x32_bf16 v[120:123], a[120:123], a[220:223], v[120:123]\n\t" \
  "v_cvt_pk_bf16_f32 v82, v68, v69\n\t" \
  "v_permlane16_swap_b32 v89, v91\n\t" \
  "v_mfma_f32_16x16x32_bf16 v[116:119], a[180:183], a[204:207], v[116:119]\n\t" \
  "v_cvt_pk_bf16_f32 v80, v64, v65\n\t" \
  "v_permlane16_swap_b32 v84, v86\n\t" \
  "v_mfma_f32_16x16x32_bf16 v[116:119], a[116:119], a[220:223], v[116:119]\n\t" \
  "v_exp_f32 v4, v4\n\t" \
  "v_mfma_f32_16x16x32_bf16 v[112:115], a[176:179], a[204:207], v[112:115]\n\t" \
  "v_permlane16_swap_b32 v85, v87\n\t" \
  "v_mfma_f32_16x16x32_bf16 v[112:115], a[112:115], a[220:223], v[112:115]\n\t" \
  "v_cvt_pk_bf16_f32 v81, v66, v67\n\t" \
  "v_add_f32 v32, v32, v180\n\t" \
  "v_mfma_f32_16x16x32_bf16 v[108:111], a[172:175], a[204:207], v[108:111]\n\t" \
  "v_exp_f32 v5, v5\n\t" \
  "v_mfma_f32_16x16x32_bf16 v[108:111], a[108:111], a[220:223], v[108:111]\n\t" \
  "v_permlane16_swap_b32 v80, v82\n\t" \
  "v_mfma_f32_16x16x32_bf16 v[104:107], a[168:171], a[204:207], v[104:107]\n\t" \
  "v_exp_f32 v6, v6\n\t" \
  "v_mfma_f32_16x16x32_bf16 v[104:107], a[104:107], a[220:223], v[104:107]\n\t" \
  "v_add_f32 v33, v33, v180\n\t" \
  "v_add_f32 v34, v34, v180\n\t" \
  "v_mfma_f32_16x16x32_bf16 v[100:103], a[164:167], a[204:207], v[100:103]\n\t" \
  "s_add_u32 m0, s38, 147456\n\t" \
  "v_permlane16_swap_b32 v81, v83\n\t" \
  "v_mfma_f32_16x16x32_bf16 v[100:103], a[100:103], a[220:223], v[100:103]\n\t" \
  "s_add_u32 s96, s38, 16384\n\t" \
  "v_exp_f32 v7, v7\n\t" \
  "v_mfma_f32_16x16x32_bf16 v[96:99], a[160:163], a[204:207], v[96:99]\n\t" \
  "v_exp_f32 v8, v8\n\t" \
  "v_mfma_f32_16x16x32_bf16 v[96:99], a[96:99], a[220:223], v[96:99]\n\t" \
  "v_add_f32 v35, v35, v180\n\t" \
  "v_add_f32 v36, v36, v180\n\t" \
  "v_mfma_f32_16x16x32_bf16 v[124:127], a[156:159], a[236:239], v[124:127]\n\t" \
  "buffer_load_dwordx4 v230, s[64:67], s96 offen offset:0 lds\n\t" \
  "v_mfma_f32_16x16x32_bf16 v[124:127], a[92:95], a[252:255], v[124:127]\n\t" \
  "v_exp_f32 v9, v9\n\t" \
  "v_mfma_f32_16x16x32_bf16 v[120:123], a[152:155], a[236:239], v[120:123]\n\t" \
  "v_exp_f32 v10, v10\n\t" \
  "v_mfma_f32_16x16x32_bf16 v[120:123], a[88:91], a[252:255], v[120:123]\n\t" \
  "v_add_f32 v37, v37, v180\n\t" \
  "v_add_f32 v38, v38, v180\n\t" \
  "v_mfma_f32_16x16x32_bf16 v[116:119], a[148:151], a[236:239], v[116:119]\n\t" \
  "v_add_f32 v39, v39, v180\n\t" \
  "v_cvt_pk_bf16_f32 v127, v126, v127\n\t" \
  "v_mfma_f32_16x16x32_bf16 v[116:119], a[84:87], a[252:255], v[116:119]\n\t" \
  "v_cvt_pk_bf16_f32 v126, v124, v125\n\t" \
  "v_cvt_pk_bf16_f32 v129, v2, v3\n\t" \
  "v_mfma_f32_16x16x32_bf16 v[112:115], a[144:147], a[236:239], v[112:115]\n\t" \
  "v_cvt_pk_bf16_f32 v124, v120, v121\n\t" \
  "v_cvt_pk_bf16_f32 v125, v122, v123\n\t" \
  "v_mfma_f32_16x16x32_bf16 v[112:115], a[80:83], a[252:255], v[112:115]\n\t" \
  "v_permlane16_swap_b32 v124, v126\n\t" \
  "v_mfma_f32_16x16x32_bf16 v[108:111], a[140:143], a[236:239], v[108:111]\n\t" \
  "v_cvt_pk_bf16_f32 v123, v118, v119\n\t" \
  "v_permlane16_swap_b32 v125, v127\n\t" \
  "v_mfma_f32_16x16x32_bf16 v[108:111], a[76:79], a[252:255], v[108:111]\n\t" \
  "v_exp_f32 v11, v11\n\t" \
  "v_cvt_pk_bf16_f32 v122, v116, v117\n\t" \
  "v_mfma_f32_16x16x32_bf16 v[104:107], a[136:139], a[236:239], v[104:107]\n\t" \
  "v_exp_f32 v12, v12\n\t" \
  "v_cvt_pk_bf16_f32 v120, v112, v113\n\t" \
  "v_cvt_pk_bf16_f32 v121, v114, v115\n\t" \
  "v_mfma_f32_16x16x32_bf16 v[104:107], a[72:75], a[252:255], v[104:107]\n\t" \
  "s_nop 0\n\t" \
  "v_cvt_pk_bf16_f32 v119, v110, v111\n\t" \
  "v_permlane16_swap_b32 v120, v122\n\t" \
  "v_cvt_pk_bf16_f32 v118, v108, v109\n\t" \
  "v_mfma_f32_16x16x32_bf16 v[100:103], a[132:135], a[236:239], v[100:103]\n\t" \
  "v_permlane16_swap_b32 v121, v123\n\t" \
  "v_mfma_f32_16x16x32_bf16 v[100:103], a[68:71], a[252:255], v[100:103]\n\t" \
  "v_cvt_pk_bf16_f32 v130, v4, v5\n\t" \
  "v_cvt_pk_bf16_f32 v116, v104, v105\n\t" \
  "v_mfma_f32_16x16x32_bf16 v[96:99], a[128:131], a[236:239], v[96:99]\n\t" \
  "v_cvt_pk_bf16_f32 v117, v106, v107\n\t" \
  "v_cvt_pk_bf16_f32 v131, v6, v7\n\t" \
  "v_mfma_f32_16x16x32_bf16 v[96:99], a[64:67], a[252:255], v[96:99]\n\t" \
  "v_cvt_pk_bf16_f32 v132, v8, v9\n\t" \
  "v_permlane16_swap_b32 v116, v118\n\t" \
  "v_cvt_pk_bf16_f32 v115, v102, v103\n\t" \
  "v_mfma_f32_16x16x32_bf16 v[64:67], a[0:3], v[80:83], 0\n\t" \
  "buffer_load_dwordx4 v231, s[64:67], s96 offen offset:1024 lds\n\t" \
  "v_mfma_f32_16x16x32_bf16 v[64:67], a[4:7], v[84:87], v[64:67]\n\t" \
  "ds_read_b128 a[192:195], v220 offset:16384\n\t" \
  "v_permlane16_swap_b32 v117, v119\n\t" \
  "v_mfma_f32_16x16x32_bf16 v[64:67], a[8:11], v[88:91], v[64:67]\n\t" \
  "ds_read_b128 a[196:199], v221 offset:16384\n\t" \
  "v_exp_f32 v13, v13\n\t" \
  "v_mfma_f32_16x16x32_bf16 v[64:67], a[12:15], v[92:95], v[64:67]\n\t" \
  "ds_read_b128 a[200:203], v222 offset:16384\n\t" \
  "v_cvt_pk_bf16_f32 v114, v100, v101\n\t" \
  "v_mfma_f32_16x16x32_bf16 v[68:71], a[16:19], v[80:83], 0\n\t" \
  "ds_read_b128 a[204:207], v223 offset:16384\n\t" \
  "v_cvt_pk_bf16_f32 v112, v96, v97\n\t" \
  "v_mfma_f32_16x16x32_bf16 v[68:71], a[20:23], v[84:87], v[68:71]\n\t" \
  "ds_read_b128 a[208:211], v220 offset:20480\n\t" \
  "v_exp_f32 v14, v14\n\t" \
  "v_max3_f32 v200, v64, v65, v66\n\t" \
  "v_mfma_f32_16x16x32_bf16 v[68:71], a[24:27], v[88:91], v[68:71]\n\t" \
  "ds_read_b128 a[212:215], v221 offset:20480\n\t" \
  "v_cvt_pk_bf16_f32 v113, v98, v99\n\t" \
  "v_mfma_f32_16x16x32_bf16 v[68:71], a[28:31], v[92:95], v[68:71]\n\t" \
  "v_permlane16_swap_b32 v112, v114\n\t" \
  "v_mfma_f32_16x16x32_bf16 v[40:43], a[32:35], v[48:51], v[180:183]\n\t" \
  "v_permlane16_swap_b32 v113, v115\n\t" \
  "v_mfma_f32_16x16x32_bf16 v[40:43], a[36:39], v[52:55], v[40:43]\n\t" \
  "s_nop 3\n\t" \
  "v_max3_f32 v200, v200, v67, v68\n\t" \
  "v_mfma_f32_16x16x32_bf16 v[40:43], a[40:43], v[56:59], v[40:43]\n\t" \
  "v_max3_f32 v200, v200, v69, v70\n\t" \
  "v_exp_f32 v15, v15\n\t" \
  "v_max_f32 v200, v200, v71\n\t" \
  "v_mov_b32 v201, v200\n\t" \
  "v_mfma_f32_16x16x32_bf16 v[40:43], a[44:47], v[60:63], v[40:43]\n\t" \
  "ds_read_b128 a[216:219], v222 offset:20480\n\t" \
  "v_permlane32_swap_b32 v200, v201\n\t" \
  "v_max_f32 v200, v200, v201\n\t" \
  "v_mov_b32 v201, v200\n\t" \
  "v_mfma_f32_16x16x32_bf16 v[44:47], a[48:51], v[48:51], v[180:183]\n\t" \
  "s_nop 0\n\t" \
  "v_permlane16_swap_b32 v200, v201\n\t" \
  "v_max_f32 v200, v200, v201\n\t" \
  "v_sub_f32 v184, 0xc2c00000, v200\n\t" \
  "v_sub_f32 v185, 0xc2c00000, v200\n\t" \
  "v_mfma_f32_16x16x32_bf16 v[44:47], a[52:55], v[52:55], v[44:47]\n\t" \
  "ds_read_b128 a[220:223], v223 offset:20480\n\t" \
  "v_max3_f32 v204, v40, v41, v42\n\t" \
  "v_sub_f32 v186, 0xc2c00000, v200\n\t" \
  "v_sub_f32 v187, 0xc2c00000, v200\n\t" \
  "v_mfma_f32_16x16x32_bf16 v[44:47], a[56:59], v[56:59], v[44:47]\n\t" \
  "ds_read_b128 a[224:227], v220 offset:24576\n\t" \
  "v_add_f32 v64, v64, v184\n\t" \
  "v_add_f32 v65, v65, v184\n\t" \
  "v_mfma_f32_16x16x32_bf16 v[44:47], a[60:63], v[60:63], v[44:47]\n\t" \
  "v_add_f32 v66, v66, v184\n\t" \
  "v_add_f32 v67, v67, v184\n\t" \
  "v_mfma_f32_16x16x32_bf16 v[96:99], a[0:3], v[112:115], 0\n\t" \
  "buffer_load_dwordx4 v230, s[64:67], s96 offen offset:2048 lds\n\t" \
  "v_mfma_f32_16x16x32_bf16 v[96:99], a[4:7], v[116:119], v[96:99]\n\t" \
  "ds_read_b128 a[228:231], v221 offset:24576\n\t" \
  "v_add_f32 v68, v68, v184\n\t" \
  "s_nop 0\n\t" \
  "v_max3_f32 v203, v43, v44, v45\n\t" \
  "v_max3_f32 v204, v204, v46, v47\n\t" \
  "v_mfma_f32_16x16x32_bf16 v[96:99], a[8:11], v[120:123], v[96:99]\n\t" \
  "ds_read_b128 a[232:235], v222 offset:24576\n\t" \
  "v_add_f32 v69, v69, v184\n\t" \
  "v_max_f32 v204, v204, v203\n\t" \
  "v_cmp_lt_f32 vcc, 0x42800000, v204\n\t" \
  "v_mfma_f32_16x16x32_bf16 v[96:99], a[12:15], v[124:127], v[96:99]\n\t" \
  "v_add_f32 v70, v70, v184\n\t" \
  "v_add_f32 v71, v71, v184\n\t" \
  "v_mfma_f32_16x16x32_bf16 v[100:103], a[16:19], v[112:115], 0\n\t" \
  "s_cbranch_vccnz .Lsf_c01\n\t" \
  ".Lsf_r01:\n\t" \
  "v_mfma_f32_16x16x32_bf16 v[100:103], a[20:23], v[116:119], v[100:103]\n\t" \
  "ds_read_b128 a[236:239], v223 offset:24576\n\t" \
  "v_exp_f32 v32, v32\n\t" \
  "v_exp_f32 v33, v33\n\t" \
  "v_mfma_f32_16x16x32_bf16 v[100:103], a[24:27], v[120:123], v[100:103]\n\t" \
  "v_max3_f32 v200, v96, v97, v98\n\t" \
  "v_exp_f32 v34, v34\n\t" \
  "v_exp_f32 v35, v35\n\t" \
  "v_mfma_f32_16x16x32_bf16 v[100:103], a[28:31], v[124:127], v[100:103]\n\t" \
  "ds_read_b128 a[240:243], v220 offset:28672\n\t" \
  "v_exp_f32 v36, v36\n\t" \
  "v_exp_f32 v37, v37\n\t" \
  "v_mfma_f32_16x16x32_bf16 v[72:75], a[32:35], v[80:83], v[184:187]\n\t" \
  "ds_read_b128 a[244:247], v221 offset:28672\n\t" \
  "v_exp_f32 v38, v38\n\t" \
  "v_exp_f32 v39, v39\n\t" \
  "v_mfma_f32_16x16x32_bf16 v[72:75], a[36:39], v[84:87], v[72:75]\n\t" \
  "v_max3_f32 v200, v200, v99, v100\n\t" \
  "v_max3_f32 v200, v200, v101, v102\n\t" \
  "v_max_f32 v200, v200, v103\n\t" \
  "v_mov_b32 v201, v200\n\t" \
  "v_mfma_f32_16x16x32_bf16 v[72:75], a[40:43], v[88:91], v[72:75]\n\t" \
  "ds_read_b128 a[248:251], v222 offset:28672\n\t" \
  "v_permlane32_swap_b32 v200, v201\n\t" \
  "v_max_f32 v200, v200, v201\n\t" \
  "v_mov_b32 v201, v200\n\t" \
  "v_mfma_f32_16x16x32_bf16 v[72:75], a[44:47], v[92:95], v[72:75]\n\t" \
  "s_nop 0\n\t" \
  "v_permlane16_swap_b32 v200, v201\n\t" \
  "v_max_f32 v200, v200, v201\n\t" \
  "v_sub_f32 v188, 0xc2c00000, v200\n\t" \
  "v_sub_f32 v189, 0xc2c00000, v200\n\t" \
  "v_mfma_f32_16x16x32_bf16 v[76:79], a[48:51], v[80:83], v[184:187]\n\t" \
  "ds_read_b128 a[252:255], v223 offset:28672\n\t" \
  "v_sub_f32 v190, 0xc2c00000, v200\n\t" \
  "v_exp_f32 v40, v40\n\t" \
  "v_sub_f32 v191, 0xc2c00000, v200\n\t" \
  "v_mfma_f32_16x16x32_bf16 v[76:79], a[52:55], v[84:87], v[76:79]\n\t" \
  "v_max3_f32 v204, v72, v73, v74\n\t" \
  "v_exp_f32 v41, v41\n\t" \
  "v_exp_f32 v42, v42\n\t" \
  "v_mfma_f32_16x16x32_bf16 v[76:79], a[56:59], v[88:91], v[76:79]\n\t" \
  "v_add_f32 v96, v96, v188\n\t" \
  "v_exp_f32 v43, v43\n\t" \
  "v_exp_f32 v44, v44\n\t" \
  "v_mfma_f32_16x16x32_bf16 v[76:79], a[60:63], v[92:95], v[76:79]\n\t" \
  "v_add_f32 v97, v97, v188\n\t" \
  "v_exp_f32 v45, v45\n\t" \
  "v_exp_f32 v46, v46\n\t" \
  "v_mfma_f32_16x16x32_bf16 v[104:107], a[32:35], v[112:115], v[188:191]\n\t" \
  "buffer_load_dwordx4 v231, s[64:67], s96 offen offset:3072 lds\n\t" \
  "v_exp_f32 v47, v47\n\t" \
  "v_mfma_f32_16x16x32_bf16 v[104:107], a[36:39], v[116:119], v[104:107]\n\t" \
  "s_nop 0\n\t" \
  "v_max3_f32 v203, v75, v76, v77\n\t" \
  "v_max3_f32 v204, v204, v78, v79\n\t" \
  "v_max_f32 v204, v204, v203\n\t" \
  "v_cmp_lt_f32 vcc, 0x42800000, v204\n\t" \
  "v_mfma_f32_16x16x32_bf16 v[104:107], a[40:43], v[120:123], v[104:107]\n\t" \
  "v_cvt_pk_bf16_f32 v133, v10, v11\n\t" \
  "v_cvt_pk_bf16_f32 v134, v12, v13\n\t" \
  "v_add_f32 v98, v98, v188\n\t" \
  "v_mfma_f32_16x16x32_bf16 v[104:107], a[44:47], v[124:127], v[104:107]\n\t" \
  "v_add_f32 v99, v99, v188\n\t" \
  "v_add_f32 v100, v100, v188\n\t" \
  "s_cbranch_vccnz .Lsf_c02\n\t" \
  ".Lsf_r02:\n\t" \
  "v_mfma_f32_16x16x32_bf16 v[108:111], a[48:51], v[112:115], v[188:191]\n\t" \
  "v_add_f32 v101, v101, v188\n\t" \
  "v_exp_f32 v64, v64\n\t" \
  "v_exp_f32 v65, v65\n\t" \
  "v_mfma_f32_16x16x32_bf16 v[108:111], a[52:55], v[116:119], v[108:111]\n\t" \
  "v_max3_f32 v204, v104, v105, v106\n\t" \
  "v_exp_f32 v66, v66\n\t" \
  "v_exp_f32 v67, v67\n\t" \
  "v_mfma_f32_16x16x32_bf16 v[108:111], a[56:59], v[120:123], v[108:111]\n\t" \
  "v_add_f32 v102, v102, v188\n\t" \
  "v_exp_f32 v68, v68\n\t" \
  "v_exp_f32 v69, v69\n\t" \
  "v_mfma_f32_16x16x32_bf16 v[108:111], a[60:63], v[124:127], v[108:111]\n\t" \
  "s_waitcnt lgkmcnt(0)\n\t" \
  "v_add_f32 v103, v103, v188\n\t" \
  "v_cvt_pk_bf16_f32 v135, v14, v15\n\t" \
  "v_exp_f32 v70, v70\n\t" \
  "v_mfma_f32_16x16x32_bf16 v[0:3], a[192:195], v[16:19], v[176:179]\n\t" \
  "ds_read_b64_tr_b16 a[128:129], v228 offset:0\n\t" \
  "v_exp_f32 v71, v71\n\t" \
  "v_mfma_f32_16x16x32_bf16 v[0:3], a[196:199], v[20:23], v[0:3]\n\t" \
  "ds_read_b64_tr_b16 a[130:131], v228 offset:4096\n\t" \
  "v_exp_f32 v72, v72\n\t" \
  "v_max3_f32 v203, v107, v108, v109\n\t" \
  "v_mfma_f32_16x16x32_bf16 v[0:3], a[200:203], v[24:27], v[0:3]\n\t" \
  "v_max3_f32 v204, v204, v110, v111\n\t" \
  "ds_read_b64_tr_b16 a[132:133], v228 offset:8192\n\t" \
  "v_max_f32 v204, v204, v203\n\t" \
  "v_mfma_f32_16x16x32_bf16 v[0:3], a[204:207], v[28:31], v[0:3]\n\t" \
  "ds_read_b64_tr_b16 a[134:135], v228 offset:12288\n\t" \
  "v_cmp_lt_f32 vcc, 0x42800000, v204\n\t" \
  "v_mfma_f32_16x16x32_bf16 v[4:7], a[220:223], v[28:31], v[176:179]\n\t" \
  "ds_read_b64_tr_b16 a[136:137], v229 offset:0\n\t" \
  "v_exp_f32 v73, v73\n\t" \
  "v_mfma_f32_16x16x32_bf16 v[4:7], a[216:219], v[24:27], v[4:7]\n\t" \
  "s_cbranch_vccnz .Lsf_c03\n\t" \
  ".Lsf_r03:\n\t" \
  "v_mfma_f32_16x16x32_bf16 v[4:7], a[212:215], v[20:23], v[4:7]\n\t" \
  "s_cmp_lg_u32 s99, 0\n\t" \
  "s_cbranch_scc1 .Lsf_p0\n\t" \
  ".Lsf_q0:\n\t" \
  "v_mfma_f32_16x16x32_bf16 v[4:7], a[208:211], v[16:19], v[4:7]\n\t" \
  "ds_read_b64_tr_b16 a[138:139], v229 offset:4096\n\t" \
  "v_max3_f32 v204, v0, v1, v2\n\t" \
  "v_cvt_pk_bf16_f32 v136, v32, v33\n\t" \
  "v_mfma_f32_16x16x32_bf16 v[8:11], a[224:227], v[16:19], v[176:179]\n\t" \
  "v_cvt_pk_bf16_f32 v137, v34, v35\n\t" \
  "v_exp_f32 v74, v74\n\t" \
  "v_mfma_f32_16x16x32_bf16 v[8:11], a[228:231], v[20:23], v[8:11]\n\t" \
  "ds_read_b64_tr_b16 a[140:141], v229 offset:8192\n\t" \
  "v_exp_f32 v75, v75\n\t" \
  "v_mfma_f32_16x16x32_bf16 v[8:11], a[232:235], v[24:27], v[8:11]\n\t" \
  "v_max3_f32 v203, v3, v4, v5\n\t" \
  "v_max3_f32 v204, v204, v6, v7\n\t" \
  "v_mfma_f32_16x16x32_bf16 v[8:11], a[236:239], v[28:31], v[8:11]\n\t" \
  "ds_read_b64_tr_b16 a[142:143], v229 offset:12288\n\t" \
  "v_exp_f32 v76, v76\n\t" \
  "v_mfma_f32_16x16x32_bf16 v[12:15], a[252:255], v[28:31], v[176:179]\n\t" \
  "v_cvt_pk_bf16_f32 v138, v36, v37\n\t" \
  "v_cvt_pk_bf16_f32 v139, v38, v39\n\t" \
  "v_mfma_f32_16x16x32_bf16 v[12:15], a[248:251], v[24:27], v[12:15]\n\t" \
  "v_exp_f32 v77, v77\n\t" \
  "v_mfma_f32_16x16x32_bf16 v[12:15], a[244:247], v[20:23], v[12:15]\n\t" \
  "v_max3_f32 v203, v203, v8, v9\n\t" \
  "v_max3_f32 v204, v204, v10, v11\n\t" \
  "v_mfma_f32_16x16x32_bf16 v[12:15], a[240:243], v[16:19], v[12:15]\n\t" \
  "ds_read_b64_tr_b16 a[144:145], v228 offset:256\n\t" \
  "v_cvt_pk_bf16_f32 v140, v40, v41\n\t" \
  "v_mfma_f32_16x16x32_bf16 v[32:35], a[192:195], v[48:51], v[180:183]\n\t" \
  "ds_read_b64_tr_b16 a[146:147], v228 offset:4352\n\t" \
  "v_cvt_pk_bf16_f32 v141, v42, v43\n\t" \
  "v_mfma_f32_16x16x32_bf16 v[32:35], a[196:199], v[52:55], v[32:35]\n\t" \
  "v_cvt_pk_bf16_f32 v142, v44, v45\n\t" \
  "v_exp_f32 v78, v78\n\t" \
  "v_mfma_f32_16x16x32_bf16 v[32:35], a[200:203], v[56:59], v[32:35]\n\t" \
  "v_max3_f32 v203, v203, v12, v13\n\t" \
  "v_max3_f32 v204, v204, v14, v15\n\t" \
  "v_mfma_f32_16x16x32_bf16 v[32:35], a[204:207], v[60:63], v[32:35]\n\t" \
  "v_max_f32 v204, v204, v203\n\t" \
  "v_cmp_lt_f32 vcc, 0x42800000, v204\n\t" \
  "v_mfma_f32_16x16x32_bf16 v[36:39], a[220:223], v[60:63], v[180:183]\n\t" \
  "ds_read_b64_tr_b16 a[148:149], v228 offset:8448\n\t" \
  "v_exp_f32 v79, v79\n\t" \
  "v_mfma_f32_16x16x32_bf16 v[36:39], a[216:219], v[56:59], v[36:39]\n\t" \
  "s_cbranch_vccnz .Lsf_c10\n\t" \
  ".Lsf_r10:\n\t" \
  "v_mfma_f32_16x16x32_bf16 v[36:39], a[212:215], v[52:55], v[36:39]\n\t" \
  "ds_read_b64_tr_b16 a[150:151], v228 offset:12544\n\t" \
  "v_max3_f32 v204, v32, v33, v34\n\t" \
  "v_mfma_f32_16x16x32_bf16 v[36:39], a[208:211], v[48:51], v[36:39]\n\t" \
  "ds_read_b64_tr_b16 a[152:153], v229 offset:256\n\t" \
  "v_exp_f32 v96, v96\n\t" \
  "v_mfma_f32_16x16x32_bf16 v[40:43], a[224:227], v[48:51], v[180:183]\n\t" \
  "ds_read_b64_tr_b16 a[154:155], v229 offset:4352\n\t" \
  "v_exp_f32 v97, v97\n\t" \
  "v_mfma_f32_16x16x32_bf16 v[40:43], a[228:231], v[52:55], v[40:43]\n\t" \
  "v_cvt_pk_bf16_f32 v143, v46, v47\n\t" \
  "v_cvt_pk_bf16_f32 v144, v64, v65\n\t" \
  "v_mfma_f32_16x16x32_bf16 v[40:43], a[232:235], v[56:59], v[40:43]\n\t" \
  "v_max3_f32 v203, v35, v36, v37\n\t" \
  "v_max3_f32 v204, v204, v38, v39\n\t" \
  "v_mfma_f32_16x16x32_bf16 v[40:43], a[236:239], v[60:63], v[40:43]\n\t" \
  "ds_read_b64_tr_b16 a[156:157], v229 offset:8448\n\t" \
  "v_exp_f32 v98, v98\n\t" \
  "v_mfma_f32_16x16x32_bf16 v[44:47], a[252:255], v[60:63], v[180:183]\n\t" \
  "ds_read_b64_tr_b16 a[158:159], v229 offset:12544\n\t" \
  "v_exp_f32 v99, v99\n\t" \
  "v_mfma_f32_16x16x32_bf16 v[44:47], a[248:251], v[56:59], v[44:47]\n\t" \
  "v_cvt_pk_bf16_f32 v145, v66, v67\n\t" \
  "v_cvt_pk_bf16_f32 v146, v68, v69\n\t" \
  "v_mfma_f32_16x16x32_bf16 v[44:47], a[244:247], v[52:55], v[44:47]\n\t" \
  "v_max3_f32 v203, v203, v40, v41\n\t" \
  "v_max3_f32 v204, v204, v42, v43\n\t" \
  "v_mfma_f32_16x16x32_bf16 v[44:47], a[240:243], v[48:51], v[44:47]\n\t" \
  "ds_read_b64_tr_b16 a[160:161], v228 offset:512\n\t" \
  "v_exp_f32 v100, v100\n\t" \
  "v_mfma_f32_16x16x32_bf16 v[64:67], a[192:195], v[80:83], v[184:187]\n\t" \
  "ds_read_b64_tr_b16 a[162:163], v228 offset:4608\n\t" \
  "v_exp_f32 v101, v101\n\t" \
  "v_mfma_f32_16x16x32_bf16 v[64:67], a[196:199], v[84:87], v[64:67]\n\t" \
  "ds_read_b64_tr_b16 a[164:165], v228 offset:8704\n\t" \
  "v_cvt_pk_bf16_f32 v147, v70, v71\n\t" \
  "v_mfma_f32_16x16x32_bf16 v[64:67], a[200:203], v[88:91], v[64:67]\n\t" \
  "v_max3_f32 v203, v203, v44, v45\n\t" \
  "v_max3_f32 v204, v204, v46, v47\n\t" \
  "v_mfma_f32_16x16x32_bf16 v[64:67], a[204:207], v[92:95], v[64:67]\n\t" \
  "v_max_f32 v204, v204, v203\n\t" \
  "v_cmp_lt_f32 vcc, 0x42800000, v204\n\t" \
  "v_mfma_f32_16x16x32_bf16 v[68:71], a[220:223], v[92:95], v[184:187]\n\t" \
  "ds_read_b64_tr_b16 a[166:167], v228 offset:12800\n\t" \
  "v_exp_f32 v102, v102\n\t" \
  "v_mfma_f32_16x16x32_bf16 v[68:71], a[216:219], v[88:91], v[68:71]\n\t" \
  "s_cbranch_vccnz .Lsf_c11\n\t" \
  ".Lsf_r11:\n\t" \
  "v_mfma_f32_16x16x32_bf16 v[68:71], a[212:215], v[84:87], v[68:71]\n\t" \
  "v_max3_f32 v204, v64, v65, v66\n\t" \
  "v_exp_f32 v103, v103\n\t" \
  "v_cvt_pk_bf16_f32 v148, v72, v73\n\t" \
  "v_mfma_f32_16x16x32_bf16 v[68:71], a[208:211], v[80:83], v[68:71]\n\t" \
  "ds_read_b64_tr_b16 a[168:169], v229 offset:512\n\t" \
  "v_exp_f32 v104, v104\n\t" \
  "v_cvt_pk_bf16_f32 v149, v74, v75\n\t" \
  "v_mfma_f32_16x16x32_bf16 v[72:75], a[224:227], v[80:83], v[184:187]\n\t" \
  "ds_read_b64_tr_b16 a[170:171], v229 offset:4608\n\t" \
  "v_exp_f32 v105, v105\n\t" \
  "v_mfma_f32_16x16x32_bf16 v[72:75], a[228:231], v[84:87], v[72:75]\n\t" \
  "ds_read_b64_tr_b16 a[172:173], v229 offset:8704\n\t" \
  "v_max3_f32 v203, v67, v68, v69\n\t" \
  "v_mfma_f32_16x16x32_bf16 v[72:75], a[232:235], v[88:91], v[72:75]\n\t" \
  "v_max3_f32 v204, v204, v70, v71\n\t" \
  "v_exp_f32 v106, v106\n\t" \
  "v_mfma_f32_16x16x32_bf16 v[72:75], a[236:239], v[92:95], v[72:75]\n\t" \
  "ds_read_b64_tr_b16 a[174:175], v229 offset:12800\n\t" \
  "v_cvt_pk_bf16_f32 v150, v76, v77\n\t" \
  "v_cvt_pk_bf16_f32 v151, v78, v79\n\t" \
  "v_mfma_f32_16x16x32_bf16 v[76:79], a[252:255], v[92:95], v[184:187]\n\t" \
  "ds_read_b64_tr_b16 a[176:177], v228 offset:768\n\t" \
  "v_exp_f32 v107, v107\n\t" \
  "v_mfma_f32_16x16x32_bf16 v[76:79], a[248:251], v[88:91], v[76:79]\n\t" \
  "v_cvt_pk_bf16_f32 v152, v96, v97\n\t" \
  "v_max3_f32 v203, v203, v72, v73\n\t" \
  "v_mfma_f32_16x16x32_bf16 v[76:79], a[244:247], v[84:87], v[76:79]\n\t" \
  "v_max3_f32 v204, v204, v74, v75\n\t" \
  "v_exp_f32 v108, v108\n\t" \
  "v_mfma_f32_16x16x32_bf16 v[76:79], a[240:243], v[80:83], v[76:79]\n\t" \
  "ds_read_b64_tr_b16 a[178:179], v228 offset:4864\n\t" \
  "v_cvt_pk_bf16_f32 v153, v98, v99\n\t" \
  "v_mfma_f32_16x16x32_bf16 v[96:99], a[192:195], v[112:115], v[188:191]\n\t" \
  "ds_read_b64_tr_b16 a[180:181], v228 offset:8960\n\t" \
  "v_exp_f32 v109, v109\n\t" \
  "v_mfma_f32_16x16x32_bf16 v[96:99], a[196:199], v[116:119], v[96:99]\n\t" \
  "v_cvt_pk_bf16_f32 v154, v100, v101\n\t" \
  "v_cvt_pk_bf16_f32 v155, v102, v103\n\t" \
  "v_mfma_f32_16x16x32_bf16 v[96:99], a[200:203], v[120:123], v[96:99]\n\t" \
  "v_max3_f32 v203, v203, v76, v77\n\t" \
  "v_max3_f32 v204, v204, v78, v79\n\t" \
  "v_mfma_f32_16x16x32_bf16 v[96:99], a[204:207], v[124:127], v[96:99]\n\t" \
  "v_max_f32 v204, v204, v203\n\t" \
  "v_cmp_lt_f32 vcc, 0x42800000, v204\n\t" \
  "v_mfma_f32_16x16x32_bf16 v[100:103], a[220:223], v[124:127], v[188:191]\n\t" \
  "ds_read_b64_tr_b16 a[182:183], v228 offset:13056\n\t" \
  "v_exp_f32 v110, v110\n\t" \
  "v_mfma_f32_16x16x32_bf16 v[100:103], a[216:219], v[120:123], v[100:103]\n\t" \
  "s_cbranch_vccnz .Lsf_c12\n\t" \
  ".Lsf_r12:\n\t" \
  "v_mfma_f32_16x16x32_bf16 v[100:103], a[212:215], v[116:119], v[100:103]\n\t" \
  "v_max3_f32 v204, v96, v97, v98\n\t" \
  "v_exp_f32 v111, v111\n\t" \
  "v_cvt_pk_bf16_f32 v156, v104, v105\n\t" \
  "v_mfma_f32_16x16x32_bf16 v[100:103], a[208:211], v[112:115], v[100:103]\n\t" \
  "ds_read_b64_tr_b16 a[184:185], v229 offset:768\n\t" \
  "v_cvt_pk_bf16_f32 v157, v106, v107\n\t" \
  "v_mfma_f32_16x16x32_bf16 v[104:107], a[224:227], v[112:115], v[188:191]\n\t" \
  "ds_read_b64_tr_b16 a[186:187], v229 offset:4864\n\t" \
  "v_cvt_pk_bf16_f32 v158, v108, v109\n\t" \
  "v_mfma_f32_16x16x32_bf16 v[104:107], a[228:231], v[116:119], v[104:107]\n\t" \
  "ds_read_b64_tr_b16 a[188:189], v229 offset:8960\n\t" \
  "v_cvt_pk_bf16_f32 v159, v110, v111\n\t" \
  "v_mfma_f32_16x16x32_bf16 v[104:107], a[232:235], v[120:123], v[104:107]\n\t" \
  "v_max3_f32 v203, v99, v100, v101\n\t" \
  "v_max3_f32 v204, v204, v102, v103\n\t" \
  "v_mfma_f32_16x16x32_bf16 v[104:107], a[236:239], v[124:127], v[104:107]\n\t" \
  "ds_read_b64_tr_b16 a[190:191], v229 offset:13056\n\t" \
  "s_nop 0\n\t" \
  "v_mfma_f32_16x16x32_bf16 v[108:111], a[252:255], v[124:127], v[188:191]\n\t" \
  "v_exp_f32 v0, v0\n\t" \
  "v_mfma_f32_16x16x32_bf16 v[108:111], a[248:251], v[120:123], v[108:111]\n\t" \
  "v_exp_f32 v1, v1\n\t" \
  "v_mfma_f32_16x16x32_bf16 v[108:111], a[244:247], v[116:119], v[108:111]\n\t" \
  "s_waitcnt vmcnt(0) lgkmcnt(0)\n\t" \
  "v_max3_f32 v203, v203, v104, v105\n\t" \
  "v_max3_f32 v204, v204, v106, v107\n\t" \
  "v_mfma_f32_16x16x32_bf16 v[108:111], a[240:243], v[112:115], v[108:111]\n\t" \
  "s_barrier\n\t" \
  "v_mfma_f32_16x16x32_bf16 a[0:3], a[128:131], v[128:131], 0\n\t" \
  "v_exp_f32 v2, v2\n\t" \
  "v_mfma_f32_16x16x32_bf16 a[0:3], a[132:135], v[132:135], a[0:3]\n\t" \
  "v_exp_f32 v3, v3\n\t" \
  "v_mfma_f32_16x16x32_bf16 a[4:7], a[136:139], v[128:131], 0\n\t" \
  "v_exp_f32 v4, v4\n\t" \
  "v_mfma_f32_16x16x32_bf16 a[4:7], a[140:143], v[132:135], a[4:7]\n\t" \
  "v_max3_f32 v203, v203, v108, v109\n\t" \
  "v_max3_f32 v204, v204, v110, v111\n\t" \
  "v_mfma_f32_16x16x32_bf16 a[8:11], a[144:147], v[128:131], 0\n\t" \
  "v_max_f32 v204, v204, v203\n\t" \
  "v_cmp_lt_f32 vcc, 0x42800000, v204\n\t" \
  "v_mfma_f32_16x16x32_bf16 a[8:11], a[148:151], v[132:135], a[8:11]\n\t" \
  "v_exp_f32 v5, v5\n\t" \
  "v_mfma_f32_16x16x32_bf16 a[12:15], a[152:155], v[128:131], 0\n\t" \
  "s_cbranch_vccnz .Lsf_c13\n\t" \
  ".Lsf_r13:\n\t" \
  "v_mfma_f32_16x16x32_bf16 a[12:15], a[156:159], v[132:135], a[12:15]\n\t" \
  "s_add_u32 m0, s38, 65536\n\t" \
  "v_exp_f32 v6, v6\n\t" \
  "v_mfma_f32_16x16x32_bf16 a[16:19], a[160:163], v[128:131], 0\n\t" \
  "s_add_u32 s96, s38, 49152\n\t" \
  "v_exp_f32 v7, v7\n\t" \
  "v_mfma_f32_16x16x32_bf16 a[16:19], a[164:167], v[132:135], a[16:19]\n\t" \
  "v_exp_f32 v8, v8\n\t" \
  "v_mfma_f32_16x16x32_bf16 a[20:23], a[168:171], v[128:131], 0\n\t" \
  "buffer_load_dwordx4 v224, s[60:63], s96 offen offset:0 lds\n\t" \
  "v_mfma_f32_16x16x32_bf16 a[20:23], a[172:175], v[132:135], a[20:23]\n\t" \
  "v_exp_f32 v9, v9\n\t" \
  "v_mfma_f32_16x16x32_bf16 a[24:27], a[176:179], v[128:131], 0\n\t" \
  "v_exp_f32 v10, v10\n\t" \
  "v_mfma_f32_16x16x32_bf16 a[24:27], a[180:183], v[132:135], a[24:27]\n\t" \
  "v_exp_f32 v11, v11\n\t" \
  "v_mfma_f32_16x16x32_bf16 a[28:31], a[184:187], v[128:131], 0\n\t" \
  "v_exp_f32 v12, v12\n\t" \
  "v_mfma_f32_16x16x32_bf16 a[28:31], a[188:191], v[132:135], a[28:31]\n\t" \
  "v_exp_f32 v13, v13\n\t" \
  "v_smfmac_f32_16x16x64_bf16 v[160:163], v[216:219], v[128:135], v208\n\t" \
  "ds_read_b128 a[192:195], v220 offset:0\n\t" \
  "v_cvt_pk_bf16_f32 v128, v0, v1\n\t" \
  "v_mfma_f32_16x16x32_bf16 a[32:35], a[128:131], v[136:139], 0\n\t" \
  "buffer_load_dwordx4 v225, s[60:63], s96 offen offset:1024 lds\n\t" \
  "v_mfma_f32_16x16x32_bf16 a[32:35], a[132:135], v[140:143], a[32:35]\n\t" \
  "v_exp_f32 v14, v14\n\t" \
  "v_mfma_f32_16x16x32_bf16 a[36:39], a[136:139], v[136:139], 0\n\t" \
  "v_exp_f32 v15, v15\n\t" \
  "v_mfma_f32_16x16x32_bf16 a[36:39], a[140:143], v[140:143], a[36:39]\n\t" \
  "ds_read_b128 a[196:199], v221 offset:0\n\t" \
  "v_cvt_pk_bf16_f32 v129, v2, v3\n\t" \
  "v_mfma_f32_16x16x32_bf16 a[40:43], a[144:147], v[136:139], 0\n\t" \
  "v_exp_f32 v32, v32\n\t" \
  "v_mfma_f32_16x16x32_bf16 a[40:43], a[148:151], v[140:143], a[40:43]\n\t" \
  "v_exp_f32 v33, v33\n\t" \
  "v_mfma_f32_16x16x32_bf16 a[44:47], a[152:155], v[136:139], 0\n\t" \
  "buffer_load_dwordx4 v226, s[60:63], s96 offen offset:2048 lds\n\t" \
  "v_mfma_f32_16x16x32_bf16 a[44:47], a[156:159], v[140:143], a[44:47]\n\t" \
  "ds_read_b128 a[200:203], v222 offset:0\n\t" \
  "v_cvt_pk_bf16_f32 v130, v4, v5\n\t" \
  "v_mfma_f32_16x16x32_bf16 a[48:51], a[160:163], v[136:139], 0\n\t" \
  "v_exp_f32 v34, v34\n\t" \
  "v_mfma_f32_16x16x32_bf16 a[48:51], a[164:167], v[140:143], a[48:51]\n\t" \
  "ds_read_b128 a[204:207], v223 offset:0\n\t" \
  "v_cvt_pk_bf16_f32 v131, v6, v7\n\t" \
  "v_mfma_f32_16x16x32_bf16 a[52:55], a[168:171], v[136:139], 0\n\t" \
  "v_exp_f32 v35, v35\n\t" \
  "v_mfma_f32_16x16x32_bf16 a[52:55], a[172:175], v[140:143], a[52:55]\n\t" \
  "ds_read_b128 a[208:211], v220 offset:4096\n\t" \
  "v_cvt_pk_bf16_f32 v132, v8, v9\n\t" \
  "v_mfma_f32_16x16x32_bf16 a[56:59], a[176:179], v[136:139], 0\n\t" \
  "buffer_load_dwordx4 v227, s[60:63], s96 offen offset:3072 lds\n\t" \
  "v_mfma_f32_16x16x32_bf16 a[56:59], a[180:183], v[140:143], a[56:59]\n\t" \
  "v_exp_f32 v36, v36\n\t" \
  "v_mfma_f32_16x16x32_bf16 a[60:63], a[184:187], v[136:139], 0\n\t" \
  "s_add_u32 m0, s38, 81920\n\t" \
  "ds_read_b128 a[212:215], v221 offset:4096\n\t" \
  "v_cvt_pk_bf16_f32 v133, v10, v11\n\t" \
  "v_mfma_f32_16x16x32_bf16 a[60:63], a[188:191], v[140:143], a[60:63]\n\t" \
  "s_add_u32 s96, s38, 32768\n\t" \
  "v_exp_f32 v37, v37\n\t" \
  "v_smfmac_f32_16x16x64_bf16 v[164:167], v[216:219], v[136:143], v208\n\t" \
  "ds_read_b128 a[216:219], v222 offset:4096\n\t" \
  "v_cvt_pk_bf16_f32 v134, v12, v13\n\t" \
  "v_mfma_f32_16x16x32_bf16 a[64:67], a[128:131], v[144:147], 0\n\t" \
  "v_exp_f32 v38, v38\n\t" \
  "v_mfma_f32_16x16x32_bf16 a[64:67], a[132:135], v[148:151], a[64:67]\n\t" \
  "buffer_load_dwordx4 v230, s[64:67], s96 offen offset:0 lds\n\t" \
  "v_mfma_f32_16x16x32_bf16 a[68:71], a[136:139], v[144:147], 0\n\t" \
  "ds_read_b128 a[220:223], v223 offset:4096\n\t" \
  "v_cvt_pk_bf16_f32 v135, v14, v15\n\t" \
  "v_mfma_f32_16x16x32_bf16 a[68:71], a[140:143], v[148:151], a[68:71]\n\t" \
  "v_exp_f32 v39, v39\n\t" \
  "v_mfma_f32_16x16x32_bf16 a[72:75], a[144:147], v[144:147], 0\n\t" \
  "v_exp_f32 v40, v40\n\t" \
  "v_mfma_f32_16x16x32_bf16 a[72:75], a[148:151], v[148:151], a[72:75]\n\t" \
  "ds_read_b128 a[224:227], v220 offset:8192\n\t" \
  "v_cvt_pk_bf16_f32 v136, v32, v33\n\t" \
  "v_mfma_f32_16x16x32_bf16 a[76:79], a[152:155], v[144:147], 0\n\t" \
  "ds_read_b128 a[228:231], v221 offset:8192\n\t" \
  "v_cvt_pk_bf16_f32 v137, v34, v35\n\t" \
  "v_mfma_f32_16x16x32_bf16 a[76:79], a[156:159], v[148:151], a[76:79]\n\t" \
  "v_exp_f32 v41, v41\n\t" \
  "v_mfma_f32_16x16x32_bf16 a[80:83], a[160:163], v[144:147], 0\n\t" \
  "buffer_load_dwordx4 v231, s[64:67], s96 offen offset:1024 lds\n\t" \
  "v_mfma_f32_16x16x32_bf16 a[80:83], a[164:167], v[148:151], a[80:83]\n\t" \
  "v_exp_f32 v42, v42\n\t" \
  "v_mfma_f32_16x16x32_bf16 a[84:87], a[168:171], v[144:147], 0\n\t" \
  "ds_read_b128 a[232:235], v222 offset:8192\n\t" \
  "v_cvt_pk_bf16_f32 v138, v36, v37\n\t" \
  "v_mfma_f32_16x16x32_bf16 a[84:87], a[172:175], v[148:151], a[84:87]\n\t" \
  "v_exp_f32 v43, v43\n\t" \
  "v_mfma_f32_16x16x32_bf16 a[88:91], a[176:179], v[144:147], 0\n\t" \
  "ds_read_b128 a[236:239], v223 offset:8192\n\t" \
  "v_cvt_pk_bf16_f32 v139, v38, v39\n\t" \
  "v_mfma_f32_16x16x32_bf16 a[88:91], a[180:183], v[148:151], a[88:91]\n\t" \
  "v_exp_f32 v44, v44\n\t" \
  "v_mfma_f32_16x16x32_bf16 a[92:95], a[184:187], v[144:147], 0\n\t" \
  "buffer_load_dwordx4 v230, s[64:67], s96 offen offset:2048 lds\n\t" \
  "v_mfma_f32_16x16x32_bf16 a[92:95], a[188:191], v[148:151], a[92:95]\n\t" \
  "ds_read_b128 a[240:243], v220 offset:12288\n\t" \
  "v_cvt_pk_bf16_f32 v140, v40, v41\n\t" \
  "v_smfmac_f32_16x16x64_bf16 v[168:171], v[216:219], v[144:151], v208\n\t" \
  "ds_read_b128 a[244:247], v221 offset:12288\n\t" \
  "v_cvt_pk_bf16_f32 v141, v42, v43\n\t" \
  "v_mfma_f32_16x16x32_bf16 a[96:99], a[128:131], v[152:155], 0\n\t" \
  "v_exp_f32 v45, v45\n\t" \
  "v_mfma_f32_16x16x32_bf16 a[96:99], a[132:135], v[156:159], a[96:99]\n\t" \
  "v_exp_f32 v46, v46\n\t" \
  "v_mfma_f32_16x16x32_bf16 a[100:103], a[136:139], v[152:155], 0\n\t" \
  "v_exp_f32 v47, v47\n\t" \
  "v_mfma_f32_16x16x32_bf16 a[100:103], a[140:143], v[156:159], a[100:103]\n\t" \
  "buffer_load_dwordx4 v231, s[64:67], s96 offen offset:3072 lds\n\t" \
  "v_mfma_f32_16x16x32_bf16 a[104:107], a[144:147], v[152:155], 0\n\t" \
  "ds_read_b128 a[248:251], v222 offset:12288\n\t" \
  "v_cvt_pk_bf16_f32 v142, v44, v45\n\t" \
  "v_mfma_f32_16x16x32_bf16 a[104:107], a[148:151], v[156:159], a[104:107]\n\t" \
  "ds_read_b128 a[252:255], v223 offset:12288\n\t" \
  "v_cvt_pk_bf16_f32 v143, v46, v47\n\t" \
  "v_mfma_f32_16x16x32_bf16 a[108:111], a[152:155], v[152:155], 0\n\t" \
  "v_exp_f32 v64, v64\n\t" \
  "v_mfma_f32_16x16x32_bf16 a[108:111], a[156:159], v[156:159], a[108:111]\n\t" \
  "v_exp_f32 v65, v65\n\t" \
  "v_mfma_f32_16x16x32_bf16 a[112:115], a[160:163], v[152:155], 0\n\t" \
  "v_exp_f32 v66, v66\n\t" \
  "v_mfma_f32_16x16x32_bf16 a[112:115], a[164:167], v[156:159], a[112:115]\n\t" \
  "v_exp_f32 v67, v67\n\t" \
  "v_mfma_f32_16x16x32_bf16 a[116:119], a[168:171], v[152:155], 0\n\t" \
  "v_exp_f32 v68, v68\n\t" \
  "v_mfma_f32_16x16x32_bf16 a[116:119], a[172:175], v[156:159], a[116:119]\n\t" \
  "v_exp_f32 v69, v69\n\t" \
  "v_mfma_f32_16x16x32_bf16 a[120:123], a[176:179], v[152:155], 0\n\t" \
  "v_exp_f32 v70, v70\n\t" \
  "v_mfma_f32_16x16x32_bf16 a[120:123], a[180:183], v[156:159], a[120:123]\n\t" \
  "v_exp_f32 v71, v71\n\t" \
  "v_mfma_f32_16x16x32_bf16 a[124:127], a[184:187], v[152:155], 0\n\t" \
  "v_exp_f32 v72, v72\n\t" \
  "v_mfma_f32_16x16x32_bf16 a[124:127], a[188:191], v[156:159], a[124:127]\n\t" \
  "v_exp_f32 v73, v73\n\t" \
  "v_smfmac_f32_16x16x64_bf16 v[172:175], v[216:219], v[152:159], v208\n\t" \
  "s_waitcnt lgkmcnt(12)\n\t" \
  "s_cmp_lg_u32 s99, 0\n\t" \
  "s_cbranch_scc1 .Lsf_p1\n\t" \
  ".Lsf_q1:\n\t" \
  "v_mfma_f32_16x16x32_bf16 v[0:3], a[192:195], v[16:19], v[176:179]\n\t" \
  "ds_read_b64_tr_b16 a[128:129], v210 offset:0\n\t" \
  "v_cvt_pk_bf16_f32 v144, v64, v65\n\t" \
  "v_mfma_f32_16x16x32_bf16 v[0:3], a[196:199], v[20:23], v[0:3]\n\t" \
  "v_exp_f32 v74, v74\n\t" \
  "v_mfma_f32_16x16x32_bf16 v[0:3], a[200:203], v[24:27], v[0:3]\n\t" \
  "ds_read_b64_tr_b16 a[130:131], v210 offset:4096\n\t" \
  "v_cvt_pk_bf16_f32 v145, v66, v67\n\t" \
  "v_mfma_f32_16x16x32_bf16 v[0:3], a[204:207], v[28:31], v[0:3]\n\t" \
  "s_waitcnt lgkmcnt(10)\n\t" \
  "v_exp_f32 v75, v75\n\t" \
  "v_mfma_f32_16x16x32_bf16 v[4:7], a[220:223], v[28:31], v[176:179]\n\t" \
  "ds_read_b64_tr_b16 a[132:133], v210 offset:8192\n\t" \
  "v_cvt_pk_bf16_f32 v146, v68, v69\n\t" \
  "v_mfma_f32_16x16x32_bf16 v[4:7], a[216:219], v[24:27], v[4:7]\n\t" \
  "v_exp_f32 v76, v76\n\t" \
  "v_mfma_f32_16x16x32_bf16 v[4:7], a[212:215], v[20:23], v[4:7]\n\t" \
  "ds_read_b64_tr_b16 a[134:135], v210 offset:12288\n\t" \
  "v_max3_f32 v204, v0, v1, v2\n\t" \
  "v_mfma_f32_16x16x32_bf16 v[4:7], a[208:211], v[16:19], v[4:7]\n\t" \
  "s_waitcnt lgkmcnt(8)\n\t" \
  "ds_read_b64_tr_b16 a[136:137], v211 offset:0\n\t" \
  "v_cvt_pk_bf16_f32 v147, v70, v71\n\t" \
  "v_mfma_f32_16x16x32_bf16 v[8:11], a[224:227], v[16:19], v[176:179]\n\t" \
  "v_exp_f32 v77, v77\n\t" \
  "v_mfma_f32_16x16x32_bf16 v[8:11], a[228:231], v[20:23], v[8:11]\n\t" \
  "ds_read_b64_tr_b16 a[138:139], v211 offset:4096\n\t" \
  "v_cvt_pk_bf16_f32 v148, v72, v73\n\t" \
  "v_mfma_f32_16x16x32_bf16 v[8:11], a[232:235], v[24:27], v[8:11]\n\t" \
  "v_max3_f32 v203, v3, v4, v5\n\t" \
  "v_max3_f32 v204, v204, v6, v7\n\t" \
  "v_mfma_f32_16x16x32_bf16 v[8:11], a[236:239], v[28:31], v[8:11]\n\t" \
  "s_waitcnt lgkmcnt(6)\n\t" \
  "ds_read_b64_tr_b16 a[140:141], v211 offset:8192\n\t" \
  "v_cvt_pk_bf16_f32 v149, v74, v75\n\t" \
  "v_mfma_f32_16x16x32_bf16 v[12:15], a[252:255], v[28:31], v[176:179]\n\t" \
  "v_exp_f32 v78, v78\n\t" \
  "v_mfma_f32_16x16x32_bf16 v[12:15], a[248:251], v[24:27], v[12:15]\n\t" \
  "ds_read_b64_tr_b16 a[142:143], v211 offset:12288\n\t" \
  "v_cvt_pk_bf16_f32 v150, v76, v77\n\t" \
  "v_mfma_f32_16x16x32_bf16 v[12:15], a[244:247], v[20:23], v[12:15]\n\t" \
  "v_max3_f32 v203, v203, v8, v9\n\t" \
  "v_max3_f32 v204, v204, v10, v11\n\t" \
  "v_mfma_f32_16x16x32_bf16 v[12:15], a[240:243], v[16:19], v[12:15]\n\t" \
  "v_exp_f32 v79, v79\n\t" \
  "v_mfma_f32_16x16x32_bf16 v[32:35], a[192:195], v[48:51], v[180:183]\n\t" \
  "ds_read_b64_tr_b16 a[144:145], v210 offset:256\n\t" \
  "v_cvt_pk_bf16_f32 v151, v78, v79\n\t" \
  "v_mfma_f32_16x16x32_bf16 v[32:35], a[196:199], v[52:55], v[32:35]\n\t" \
  "v_exp_f32 v96, v96\n\t" \
  "v_mfma_f32_16x16x32_bf16 v[32:35], a[200:203], v[56:59], v[32:35]\n\t" \
  "ds_read_b64_tr_b16 a[146:147], v210 offset:4352\n\t" \
  "v_max3_f32 v203, v203, v12, v13\n\t" \
  "v_mfma_f32_16x16x32_bf16 v[32:35], a[204:207], v[60:63], v[32:35]\n\t" \
  "v_max3_f32 v204, v204, v14, v15\n\t" \
  "v_max_f32 v204, v204, v203\n\t" \
  "v_mfma_f32_16x16x32_bf16 v[36:39], a[220:223], v[60:63], v[180:183]\n\t" \
  "ds_read_b64_tr_b16 a[148:149], v210 offset:8448\n\t" \
  "v_cmp_lt_f32 vcc, 0x42800000, v204\n\t" \
  "v_mfma_f32_16x16x32_bf16 v[36:39], a[216:219], v[56:59], v[36:39]\n\t" \
  "v_exp_f32 v97, v97\n\t" \
  "v_mfma_f32_16x16x32_bf16 v[36:39], a[212:215], v[52:55], v[36:39]\n\t" \
  "s_cbranch_vccnz .Lsf_c20\n\t" \
  ".Lsf_r20:\n\t" \
  "v_mfma_f32_16x16x32_bf16 v[36:39], a[208:211], v[48:51], v[36:39]\n\t" \
  "ds_read_b64_tr_b16 a[150:151], v210 offset:12544\n\t" \
  "v_max3_f32 v204, v32, v33, v34\n\t" \
  "v_mfma_f32_16x16x32_bf16 v[40:43], a[224:227], v[48:51], v[180:183]\n\t" \
  "ds_read_b64_tr_b16 a[152:153], v211 offset:256\n\t" \
  "v_cvt_pk_bf16_f32 v152, v96, v97\n\t" \
  "v_mfma_f32_16x16x32_bf16 v[40:43], a[228:231], v[52:55], v[40:43]\n\t" \
  "v_exp_f32 v98, v98\n\t" \
  "v_mfma_f32_16x16x32_bf16 v[40:43], a[232:235], v[56:59], v[40:43]\n\t" \
  "v_max3_f32 v203, v35, v36, v37\n\t" \
  "v_max3_f32 v204, v204, v38, v39\n\t" \
  "v_mfma_f32_16x16x32_bf16 v[40:43], a[236:239], v[60:63], v[40:43]\n\t" \
  "v_exp_f32 v99, v99\n\t" \
  "v_mfma_f32_16x16x32_bf16 v[44:47], a[252:255], v[60:63], v[180:183]\n\t" \
  "ds_read_b64_tr_b16 a[154:155], v211 offset:4352\n\t" \
  "v_cvt_pk_bf16_f32 v153, v98, v99\n\t" \
  "v_mfma_f32_16x16x32_bf16 v[44:47], a[248:251], v[56:59], v[44:47]\n\t" \
  "v_exp_f32 v100, v100\n\t" \
  "v_mfma_f32_16x16x32_bf16 v[44:47], a[244:247], v[52:55], v[44:47]\n\t" \
  "ds_read_b64_tr_b16 a[156:157], v211 offset:8448\n\t" \
  "v_max3_f32 v203, v203, v40, v41\n\t" \
  "v_mfma_f32_16x16x32_bf16 v[44:47], a[240:243], v[48:51], v[44:47]\n\t" \
  "ds_read_b64_tr_b16 a[158:159], v211 offset:12544\n\t" \
  "v_max3_f32 v204, v204, v42, v43\n\t" \
  "v_mfma_f32_16x16x32_bf16 v[64:67], a[192:195], v[80:83], v[184:187]\n\t" \
  "v_exp_f32 v101, v101\n\t" \
  "v_mfma_f32_16x16x32_bf16 v[64:67], a[196:199], v[84:87], v[64:67]\n\t" \
  "ds_read_b64_tr_b16 a[160:161], v210 offset:512\n\t" \
  "v_cvt_pk_bf16_f32 v154, v100, v101\n\t" \
  "v_mfma_f32_16x16x32_bf16 v[64:67], a[200:203], v[88:91], v[64:67]\n\t" \
  "v_max3_f32 v203, v203, v44, v45\n\t" \
  "v_max3_f32 v204, v204, v46, v47\n\t" \
  "v_mfma_f32_16x16x32_bf16 v[64:67], a[204:207], v[92:95], v[64:67]\n\t" \
  "v_max_f32 v204, v204, v203\n\t" \
  "v_cmp_lt_f32 vcc, 0x42800000, v204\n\t" \
  "v_mfma_f32_16x16x32_bf16 v[68:71], a[220:223], v[92:95], v[184:187]\n\t" \
  "v_exp_f32 v102, v102\n\t" \
  "v_mfma_f32_16x16x32_bf16 v[68:71], a[216:219], v[88:91], v[68:71]\n\t" \
  "s_cbranch_vccnz .Lsf_c21\n\t" \
  ".Lsf_r21:\n\t" \
  "v_mfma_f32_16x16x32_bf16 v[68:71], a[212:215], v[84:87], v[68:71]\n\t" \
  "ds_read_b64_tr_b16 a[162:163], v210 offset:4608\n\t" \
  "v_max3_f32 v204, v64, v65, v66\n\t" \
  "v_mfma_f32_16x16x32_bf16 v[68:71], a[208:211], v[80:83], v[68:71]\n\t" \
  "ds_read_b64_tr_b16 a[164:165], v210 offset:8704\n\t" \
  "v_exp_f32 v103, v103\n\t" \
  "v_mfma_f32_16x16x32_bf16 v[72:75], a[224:227], v[80:83], v[184:187]\n\t" \
  "ds_read_b64_tr_b16 a[166:167], v210 offset:12800\n\t" \
  "v_exp_f32 v104, v104\n\t" \
  "v_mfma_f32_16x16x32_bf16 v[72:75], a[228:231], v[84:87], v[72:75]\n\t" \
  "ds_read_b64_tr_b16 a[168:169], v211 offset:512\n\t" \
  "v_exp_f32 v105, v105\n\t" \
  "v_mfma_f32_16x16x32_bf16 v[72:75], a[232:235], v[88:91], v[72:75]\n\t" \
  "v_max3_f32 v203, v67, v68, v69\n\t" \
  "v_max3_f32 v204, v204, v70, v71\n\t" \
  "v_mfma_f32_16x16x32_bf16 v[72:75], a[236:239], v[92:95], v[72:75]\n\t" \
  "ds_read_b64_tr_b16 a[170:171], v211 offset:4608\n\t" \
  "v_exp_f32 v106, v106\n\t" \
  "v_mfma_f32_16x16x32_bf16 v[76:79], a[252:255], v[92:95], v[184:187]\n\t" \
  "ds_read_b64_tr_b16 a[172:173], v211 offset:8704\n\t" \
  "v_exp_f32 v107, v107\n\t" \
  "v_mfma_f32_16x16x32_bf16 v[76:79], a[248:251], v[88:91], v[76:79]\n\t" \
  "ds_read_b64_tr_b16 a[174:175], v211 offset:12800\n\t" \
  "v_cvt_pk_bf16_f32 v155, v102, v103\n\t" \
  "v_mfma_f32_16x16x32_bf16 v[76:79], a[244:247], v[84:87], v[76:79]\n\t" \
  "v_max3_f32 v203, v203, v72, v73\n\t" \
  "v_max3_f32 v204, v204, v74, v75\n\t" \
  "v_mfma_f32_16x16x32_bf16 v[76:79], a[240:243], v[80:83], v[76:79]\n\t" \
  "ds_read_b64_tr_b16 a[176:177], v210 offset:768\n\t" \
  "v_exp_f32 v108, v108\n\t" \
  "v_mfma_f32_16x16x32_bf16 v[96:99], a[192:195], v[112:115], v[188:191]\n\t" \
  "ds_read_b64_tr_b16 a[178:179], v210 offset:4864\n\t" \
  "v_exp_f32 v109, v109\n\t" \
  "v_mfma_f32_16x16x32_bf16 v[96:99], a[196:199], v[116:119], v[96:99]\n\t" \
  "ds_read_b64_tr_b16 a[180:181], v210 offset:8960\n\t" \
  "v_cvt_pk_bf16_f32 v156, v104, v105\n\t" \
  "v_mfma_f32_16x16x32_bf16 v[96:99], a[200:203], v[120:123], v[96:99]\n\t" \
  "v_max3_f32 v203, v203, v76, v77\n\t" \
  "v_max3_f32 v204, v204, v78, v79\n\t" \
  "v_mfma_f32_16x16x32_bf16 v[96:99], a[204:207], v[124:127], v[96:99]\n\t" \
  "v_max_f32 v204, v204, v203\n\t" \
  "v_cmp_lt_f32 vcc, 0x42800000, v204\n\t" \
  "v_mfma_f32_16x16x32_bf16 v[100:103], a[220:223], v[124:127], v[188:191]\n\t" \
  "ds_read_b64_tr_b16 a[182:183], v210 offset:13056\n\t" \
  "v_exp_f32 v110, v110\n\t" \
  "v_mfma_f32_16x16x32_bf16 v[100:103], a[216:219], v[120:123], v[100:103]\n\t" \
  "s_cbranch_vccnz .Lsf_c22\n\t" \
  ".Lsf_r22:\n\t" \
  "v_mfma_f32_16x16x32_bf16 v[100:103], a[212:215], v[116:119], v[100:103]\n\t" \
  "v_max3_f32 v204, v96, v97, v98\n\t" \
  "v_exp_f32 v111, v111\n\t" \
  "v_mfma_f32_16x16x32_bf16 v[100:103], a[208:211], v[112:115], v[100:103]\n\t" \
  "ds_read_b64_tr_b16 a[184:185], v211 offset:768\n\t" \
  "v_cvt_pk_bf16_f32 v157, v106, v107\n\t" \
  "v_mfma_f32_16x16x32_bf16 v[104:107], a[224:227], v[112:115], v[188:191]\n\t" \
  "ds_read_b64_tr_b16 a[186:187], v211 offset:4864\n\t" \
  "v_cvt_pk_bf16_f32 v158, v108, v109\n\t" \
  "v_mfma_f32_16x16x32_bf16 v[104:107], a[228:231], v[116:119], v[104:107]\n\t" \
  "ds_read_b64_tr_b16 a[188:189], v211 offset:8960\n\t" \
  "v_cvt_pk_bf16_f32 v159, v110, v111\n\t" \
  "v_mfma_f32_16x16x32_bf16 v[104:107], a[232:235], v[120:123], v[104:107]\n\t" \
  "v_max3_f32 v203, v99, v100, v101\n\t" \
  "v_max3_f32 v204, v204, v102, v103\n\t" \
  "v_mfma_f32_16x16x32_bf16 v[104:107], a[236:239], v[124:127], v[104:107]\n\t" \
  "ds_read_b64_tr_b16 a[190:191], v211 offset:13056\n\t" \
  "s_nop 0\n\t" \
  "v_mfma_f32_16x16x32_bf16 v[108:111], a[252:255], v[124:127], v[188:191]\n\t" \
  "v_exp_f32 v0, v0\n\t" \
  "v_mfma_f32_16x16x32_bf16 v[108:111], a[248:251], v[120:123], v[108:111]\n\t" \
  "v_exp_f32 v1, v1\n\t" \
  "v_mfma_f32_16x16x32_bf16 v[108:111], a[244:247], v[116:119], v[108:111]\n\t" \
  "s_waitcnt vmcnt(0) lgkmcnt(0)\n\t" \
  "v_max3_f32 v203, v203, v104, v105\n\t" \
  "v_max3_f32 v204, v204, v106, v107\n\t" \
  "v_mfma_f32_16x16x32_bf16 v[108:111], a[240:243], v[112:115], v[108:111]\n\t" \
  "s_barrier\n\t" \
  "v_mfma_f32_16x16x32_bf16 a[0:3], a[128:131], v[128:131], a[0:3]\n\t" \
  "v_exp_f32 v2, v2\n\t" \
  "v_mfma_f32_16x16x32_bf16 a[0:3], a[132:135], v[132:135], a[0:3]\n\t" \
  "v_exp_f32 v3, v3\n\t" \
  "v_mfma_f32_16x16x32_bf16 a[4:7], a[136:139], v[128:131], a[4:7]\n\t" \
  "v_exp_f32 v4, v4\n\t" \
  "v_mfma_f32_16x16x32_bf16 a[4:7], a[140:143], v[132:135], a[4:7]\n\t" \
  "v_max3_f32 v203, v203, v108, v109\n\t" \
  "v_max3_f32 v204, v204, v110, v111\n\t" \
  "v_mfma_f32_16x16x32_bf16 a[8:11], a[144:147], v[128:131], a[8:11]\n\t" \
  "v_max_f32 v204, v204, v203\n\t" \
  "v_cmp_lt_f32 vcc, 0x42800000, v204\n\t" \
  "v_mfma_f32_16x16x32_bf16 a[8:11], a[148:151], v[132:135], a[8:11]\n\t" \
  "v_exp_f32 v5, v5\n\t" \
  "v_mfma_f32_16x16x32_bf16 a[12:15], a[152:155], v[128:131], a[12:15]\n\t" \
  "s_cbranch_vccnz .Lsf_c23\n\t" \
  ".Lsf_r23:\n\t" \
  "v_mfma_f32_16x16x32_bf16 a[12:15], a[156:159], v[132:135], a[12:15]\n\t" \
  "s_add_u32 m0, s38, 49152\n\t" \
  "v_exp_f32 v6, v6\n\t" \
  "v_mfma_f32_16x16x32_bf16 a[16:19], a[160:163], v[128:131], a[16:19]\n\t" \
  "s_add_u32 s96, s38, 65536\n\t" \
  "v_exp_f32 v7, v7\n\t" \
  "v_mfma_f32_16x16x32_bf16 a[16:19], a[164:167], v[132:135], a[16:19]\n\t" \
  "v_exp_f32 v8, v8\n\t" \
  "v_mfma_f32_16x16x32_bf16 a[20:23], a[168:171], v[128:131], a[20:23]\n\t" \
  "buffer_load_dwordx4 v224, s[60:63], s96 offen offset:0 lds\n\t" \
  "v_mfma_f32_16x16x32_bf16 a[20:23], a[172:175], v[132:135], a[20:23]\n\t" \
  "v_exp_f32 v9, v9\n\t" \
  "v_mfma_f32_16x16x32_bf16 a[24:27], a[176:179], v[128:131], a[24:27]\n\t" \
  "v_exp_f32 v10, v10\n\t" \
  "v_mfma_f32_16x16x32_bf16 a[24:27], a[180:183], v[132:135], a[24:27]\n\t" \
  "v_exp_f32 v11, v11\n\t" \
  "v_mfma_f32_16x16x32_bf16 a[28:31], a[184:187], v[128:131], a[28:31]\n\t" \
  "v_exp_f32 v12, v12\n\t" \
  "v_mfma_f32_16x16x32_bf16 a[28:31], a[188:191], v[132:135], a[28:31]\n\t" \
  "v_exp_f32 v13, v13\n\t" \
  "v_smfmac_f32_16x16x64_bf16 v[160:163], v[216:219], v[128:135], v208\n\t" \
  "ds_read_b128 a[192:195], v220 offset:16384\n\t" \
  "v_cvt_pk_bf16_f32 v128, v0, v1\n\t" \
  "v_mfma_f32_16x16x32_bf16 a[32:35], a[128:131], v[136:139], a[32:35]\n\t" \
  "buffer_load_dwordx4 v225, s[60:63], s96 offen offset:1024 lds\n\t" \
  "v_mfma_f32_16x16x32_bf16 a[32:35], a[132:135], v[140:143], a[32:35]\n\t" \
  "v_exp_f32 v14, v14\n\t" \
  "v_mfma_f32_16x16x32_bf16 a[36:39], a[136:139], v[136:139], a[36:39]\n\t" \
  "v_exp_f32 v15, v15\n\t" \
  "v_mfma_f32_16x16x32_bf16 a[36:39], a[140:143], v[140:143], a[36:39]\n\t" \
  "ds_read_b128 a[196:199], v221 offset:16384\n\t" \
  "v_cvt_pk_bf16_f32 v129, v2, v3\n\t" \
  "v_mfma_f32_16x16x32_bf16 a[40:43], a[144:147], v[136:139], a[40:43]\n\t" \
  "v_exp_f32 v32, v32\n\t" \
  "v_mfma_f32_16x16x32_bf16 a[40:43], a[148:151], v[140:143], a[40:43]\n\t" \
  "v_exp_f32 v33, v33\n\t" \
  "v_mfma_f32_16x16x32_bf16 a[44:47], a[152:155], v[136:139], a[44:47]\n\t" \
  "buffer_load_dwordx4 v226, s[60:63], s96 offen offset:2048 lds\n\t" \
  "v_mfma_f32_16x16x32_bf16 a[44:47], a[156:159], v[140:143], a[44:47]\n\t" \
  "ds_read_b128 a[200:203], v222 offset:16384\n\t" \
  "v_cvt_pk_bf16_f32 v130, v4, v5\n\t" \
  "v_mfma_f32_16x16x32_bf16 a[48:51], a[160:163], v[136:139], a[48:51]\n\t" \
  "v_exp_f32 v34, v34\n\t" \
  "v_mfma_f32_16x16x32_bf16 a[48:51], a[164:167], v[140:143], a[48:51]\n\t" \
  "ds_read_b128 a[204:207], v223 offset:16384\n\t" \
  "v_cvt_pk_bf16_f32 v131, v6, v7\n\t" \
  "v_mfma_f32_16x16x32_bf16 a[52:55], a[168:171], v[136:139], a[52:55]\n\t" \
  "v_exp_f32 v35, v35\n\t" \
  "v_mfma_f32_16x16x32_bf16 a[52:55], a[172:175], v[140:143], a[52:55]\n\t" \
  "ds_read_b128 a[208:211], v220 offset:20480\n\t" \
  "v_cvt_pk_bf16_f32 v132, v8, v9\n\t" \
  "v_mfma_f32_16x16x32_bf16 a[56:59], a[176:179], v[136:139], a[56:59]\n\t" \
  "buffer_load_dwordx4 v227, s[60:63], s96 offen offset:3072 lds\n\t" \
  "v_mfma_f32_16x16x32_bf16 a[56:59], a[180:183], v[140:143], a[56:59]\n\t" \
  "v_exp_f32 v36, v36\n\t" \
  "v_mfma_f32_16x16x32_bf16 a[60:63], a[184:187], v[136:139], a[60:63]\n\t" \
  "s_add_u32 m0, s38, 147456\n\t" \
  "ds_read_b128 a[212:215], v221 offset:20480\n\t" \
  "v_cvt_pk_bf16_f32 v133, v10, v11\n\t" \
  "v_mfma_f32_16x16x32_bf16 a[60:63], a[188:191], v[140:143], a[60:63]\n\t" \
  "s_add_u32 s96, s38, 49152\n\t" \
  "v_exp_f32 v37, v37\n\t" \
  "v_smfmac_f32_16x16x64_bf16 v[164:167], v[216:219], v[136:143], v208\n\t" \
  "ds_read_b128 a[216:219], v222 offset:20480\n\t" \
  "v_cvt_pk_bf16_f32 v134, v12, v13\n\t" \
  "v_mfma_f32_16x16x32_bf16 a[64:67], a[128:131], v[144:147], a[64:67]\n\t" \
  "v_exp_f32 v38, v38\n\t" \
  "v_mfma_f32_16x16x32_bf16 a[64:67], a[132:135], v[148:151], a[64:67]\n\t" \
  "buffer_load_dwordx4 v230, s[64:67], s96 offen offset:0 lds\n\t" \
  "v_mfma_f32_16x16x32_bf16 a[68:71], a[136:139], v[144:147], a[68:71]\n\t" \
  "ds_read_b128 a[220:223], v223 offset:20480\n\t" \
  "v_cvt_pk_bf16_f32 v135, v14, v15\n\t" \
  "v_mfma_f32_16x16x32_bf16 a[68:71], a[140:143], v[148:151], a[68:71]\n\t" \
  "v_exp_f32 v39, v39\n\t" \
  "v_mfma_f32_16x16x32_bf16 a[72:75], a[144:147], v[144:147], a[72:75]\n\t" \
  "v_exp_f32 v40, v40\n\t" \
  "v_mfma_f32_16x16x32_bf16 a[72:75], a[148:151], v[148:151], a[72:75]\n\t" \
  "ds_read_b128 a[224:227], v220 offset:24576\n\t" \
  "v_cvt_pk_bf16_f32 v136, v32, v33\n\t" \
  "v_mfma_f32_16x16x32_bf16 a[76:79], a[152:155], v[144:147], a[76:79]\n\t" \
  "ds_read_b128 a[228:231], v221 offset:24576\n\t" \
  "v_cvt_pk_bf16_f32 v137, v34, v35\n\t" \
  "v_mfma_f32_16x16x32_bf16 a[76:79], a[156:159], v[148:151], a[76:79]\n\t" \
  "v_exp_f32 v41, v41\n\t" \
  "v_mfma_f32_16x16x32_bf16 a[80:83], a[160:163], v[144:147], a[80:83]\n\t" \
  "buffer_load_dwordx4 v231, s[64:67], s96 offen offset:1024 lds\n\t" \
  "v_mfma_f32_16x16x32_bf16 a[80:83], a[164:167], v[148:151], a[80:83]\n\t" \
  "v_exp_f32 v42, v42\n\t" \
  "v_mfma_f32_16x16x32_bf16 a[84:87], a[168:171], v[144:147], a[84:87]\n\t" \
  "ds_read_b128 a[232:235], v222 offset:24576\n\t" \
  "v_cvt_pk_bf16_f32 v138, v36, v37\n\t" \
  "v_mfma_f32_16x16x32_bf16 a[84:87], a[172:175], v[148:151], a[84:87]\n\t" \
  "v_exp_f32 v43, v43\n\t" \
  "v_mfma_f32_16x16x32_bf16 a[88:91], a[176:179], v[144:147], a[88:91]\n\t" \
  "ds_read_b128 a[236:239], v223 offset:24576\n\t" \
  "v_cvt_pk_bf16_f32 v139, v38, v39\n\t" \
  "v_mfma_f32_16x16x32_bf16 a[88:91], a[180:183], v[148:151], a[88:91]\n\t" \
  "v_exp_f32 v44, v44\n\t" \
  "v_mfma_f32_16x16x32_bf16 a[92:95], a[184:187], v[144:147], a[92:95]\n\t" \
  "buffer_load_dwordx4 v230, s[64:67], s96 offen offset:2048 lds\n\t" \
  "v_mfma_f32_16x16x32_bf16 a[92:95], a[188:191], v[148:151], a[92:95]\n\t" \
  "ds_read_b128 a[240:243], v220 offset:28672\n\t" \
  "v_cvt_pk_bf16_f32 v140, v40, v41\n\t" \
  "v_smfmac_f32_16x16x64_bf16 v[168:171], v[216:219], v[144:151], v208\n\t" \
  "ds_read_b128 a[244:247], v221 offset:28672\n\t" \
  "v_cvt_pk_bf16_f32 v141, v42, v43\n\t" \
  "v_mfma_f32_16x16x32_bf16 a[96:99], a[128:131], v[152:155], a[96:99]\n\t" \
  "v_exp_f32 v45, v45\n\t" \
  "v_mfma_f32_16x16x32_bf16 a[96:99], a[132:135], v[156:159], a[96:99]\n\t" \
  "v_exp_f32 v46, v46\n\t" \
  "v_mfma_f32_16x16x32_bf16 a[100:103], a[136:139], v[152:155], a[100:103]\n\t" \
  "v_exp_f32 v47, v47\n\t" \
  "v_mfma_f32_16x16x32_bf16 a[100:103], a[140:143], v[156:159], a[100:103]\n\t" \
  "buffer_load_dwordx4 v231, s[64:67], s96 offen offset:3072 lds\n\t" \
  "v_mfma_f32_16x16x32_bf16 a[104:107], a[144:147], v[152:155], a[104:107]\n\t" \
  "ds_read_b128 a[248:251], v222 offset:28672\n\t" \
  "v_cvt_pk_bf16_f32 v142, v44, v45\n\t" \
  "v_mfma_f32_16x16x32_bf16 a[104:107], a[148:151], v[156:159], a[104:107]\n\t" \
  "ds_read_b128 a[252:255], v223 offset:28672\n\t" \
  "v_cvt_pk_bf16_f32 v143, v46, v47\n\t" \
  "v_mfma_f32_16x16x32_bf16 a[108:111], a[152:155], v[152:155], a[108:111]\n\t" \
  "v_exp_f32 v64, v64\n\t" \
  "v_mfma_f32_16x16x32_bf16 a[108:111], a[156:159], v[156:159], a[108:111]\n\t" \
  "v_exp_f32 v65, v65\n\t" \
  "v_mfma_f32_16x16x32_bf16 a[112:115], a[160:163], v[152:155], a[112:115]\n\t" \
  "v_exp_f32 v66, v66\n\t" \
  "v_mfma_f32_16x16x32_bf16 a[112:115], a[164:167], v[156:159], a[112:115]\n\t" \
  "v_exp_f32 v67, v67\n\t" \
  "v_mfma_f32_16x16x32_bf16 a[116:119], a[168:171], v[152:155], a[116:119]\n\t" \
  "v_exp_f32 v68, v68\n\t" \
  "v_mfma_f32_16x16x32_bf16 a[116:119], a[172:175], v[156:159], a[116:119]\n\t" \
  "v_exp_f32 v69, v69\n\t" \
  "v_mfma_f32_16x16x32_bf16 a[120:123], a[176:179], v[152:155], a[120:123]\n\t" \
  "v_exp_f32 v70, v70\n\t" \
  "v_mfma_f32_16x16x32_bf16 a[120:123], a[180:183], v[156:159], a[120:123]\n\t" \
  "v_exp_f32 v71, v71\n\t" \
  "v_mfma_f32_16x16x32_bf16 a[124:127], a[184:187], v[152:155], a[124:127]\n\t" \
  "v_exp_f32 v72, v72\n\t" \
  "v_mfma_f32_16x16x32_bf16 a[124:127], a[188:191], v[156:159], a[124:127]\n\t" \
  "v_exp_f32 v73, v73\n\t" \
  "v_smfmac_f32_16x16x64_bf16 v[172:175], v[216:219], v[152:159], v208\n\t" \
  "s_waitcnt lgkmcnt(12)\n\t" \
  "s_cmp_lg_u32 s99, 0\n\t" \
  "s_cbranch_scc1 .Lsf_p2\n\t" \
  ".Lsf_q2:\n\t" \
  "v_mfma_f32_16x16x32_bf16 v[0:3], a[192:195], v[16:19], v[176:179]\n\t" \
  "ds_read_b64_tr_b16 a[128:129], v228 offset:0\n\t" \
  "v_cvt_pk_bf16_f32 v144, v64, v65\n\t" \
  "v_mfma_f32_16x16x32_bf16 v[0:3], a[196:199], v[20:23], v[0:3]\n\t" \
  "v_exp_f32 v74, v74\n\t" \
  "v_mfma_f32_16x16x32_bf16 v[0:3], a[200:203], v[24:27], v[0:3]\n\t" \
  "ds_read_b64_tr_b16 a[130:131], v228 offset:4096\n\t" \
  "v_cvt_pk_bf16_f32 v145, v66, v67\n\t" \
  "v_mfma_f32_16x16x32_bf16 v[0:3], a[204:207], v[28:31], v[0:3]\n\t" \
  "s_waitcnt lgkmcnt(10)\n\t" \
  "v_exp_f32 v75, v75\n\t" \
  "v_mfma_f32_16x16x32_bf16 v[4:7], a[220:223], v[28:31], v[176:179]\n\t" \
  "ds_read_b64_tr_b16 a[132:133], v228 offset:8192\n\t" \
  "v_cvt_pk_bf16_f32 v146, v68, v69\n\t" \
  "v_mfma_f32_16x16x32_bf16 v[4:7], a[216:219], v[24:27], v[4:7]\n\t" \
  "v_exp_f32 v76, v76\n\t" \
  "v_mfma_f32_16x16x32_bf16 v[4:7], a[212:215], v[20:23], v[4:7]\n\t" \
  "ds_read_b64_tr_b16 a[134:135], v228 offset:12288\n\t" \
  "v_max3_f32 v204, v0, v1, v2\n\t" \
  "v_mfma_f32_16x16x32_bf16 v[4:7], a[208:211], v[16:19], v[4:7]\n\t" \
  "s_waitcnt lgkmcnt(8)\n\t" \
  "ds_read_b64_tr_b16 a[136:137], v229 offset:0\n\t" \
  "v_cvt_pk_bf16_f32 v147, v70, v71\n\t" \
  "v_mfma_f32_16x16x32_bf16 v[8:11], a[224:227], v[16:19], v[176:179]\n\t" \
  "v_exp_f32 v77, v77\n\t" \
  "v_mfma_f32_16x16x32_bf16 v[8:11], a[228:231], v[20:23], v[8:11]\n\t" \
  "ds_read_b64_tr_b16 a[138:139], v229 offset:4096\n\t" \
  "v_cvt_pk_bf16_f32 v148, v72, v73\n\t" \
  "v_mfma_f32_16x16x32_bf16 v[8:11], a[232:235], v[24:27], v[8:11]\n\t" \
  "v_max3_f32 v203, v3, v4, v5\n\t" \
  "v_max3_f32 v204, v204, v6, v7\n\t" \
  "v_mfma_f32_16x16x32_bf16 v[8:11], a[236:239], v[28:31], v[8:11]\n\t" \
  "s_waitcnt lgkmcnt(6)\n\t" \
  "ds_read_b64_tr_b16 a[140:141], v229 offset:8192\n\t" \
  "v_cvt_pk_bf16_f32 v149, v74, v75\n\t" \
  "v_mfma_f32_16x16x32_bf16 v[12:15], a[252:255], v[28:31], v[176:179]\n\t" \
  "v_exp_f32 v78, v78\n\t" \
  "v_mfma_f32_16x16x32_bf16 v[12:15], a[248:251], v[24:27], v[12:15]\n\t" \
  "ds_read_b64_tr_b16 a[142:143], v229 offset:12288\n\t" \
  "v_cvt_pk_bf16_f32 v150, v76, v77\n\t" \
  "v_mfma_f32_16x16x32_bf16 v[12:15], a[244:247], v[20:23], v[12:15]\n\t" \
  "v_max3_f32 v203, v203, v8, v9\n\t" \
  "v_max3_f32 v204, v204, v10, v11\n\t" \
  "v_mfma_f32_16x16x32_bf16 v[12:15], a[240:243], v[16:19], v[12:15]\n\t" \
  "v_exp_f32 v79, v79\n\t" \
  "v_mfma_f32_16x16x32_bf16 v[32:35], a[192:195], v[48:51], v[180:183]\n\t" \
  "ds_read_b64_tr_b16 a[144:145], v228 offset:256\n\t" \
  "v_cvt_pk_bf16_f32 v151, v78, v79\n\t" \
  "v_mfma_f32_16x16x32_bf16 v[32:35], a[196:199], v[52:55], v[32:35]\n\t" \
  "v_exp_f32 v96, v96\n\t" \
  "v_mfma_f32_16x16x32_bf16 v[32:35], a[200:203], v[56:59], v[32:35]\n\t" \
  "ds_read_b64_tr_b16 a[146:147], v228 offset:4352\n\t" \
  "v_max3_f32 v203, v203, v12, v13\n\t" \
  "v_mfma_f32_16x16x32_bf16 v[32:35], a[204:207], v[60:63], v[32:35]\n\t" \
  "v_max3_f32 v204, v204, v14, v15\n\t" \
  "v_max_f32 v204, v204, v203\n\t" \
  "v_mfma_f32_16x16x32_bf16 v[36:39], a[220:223], v[60:63], v[180:183]\n\t" \
  "ds_read_b64_tr_b16 a[148:149], v228 offset:8448\n\t" \
  "v_cmp_lt_f32 vcc, 0x42800000, v204\n\t" \
  "v_mfma_f32_16x16x32_bf16 v[36:39], a[216:219], v[56:59], v[36:39]\n\t" \
  "v_exp_f32 v97, v97\n\t" \
  "v_mfma_f32_16x16x32_bf16 v[36:39], a[212:215], v[52:55], v[36:39]\n\t" \
  "s_cbranch_vccnz .Lsf_c30\n\t" \
  ".Lsf_r30:\n\t" \
  "v_mfma_f32_16x16x32_bf16 v[36:39], a[208:211], v[48:51], v[36:39]\n\t" \
  "ds_read_b64_tr_b16 a[150:151], v228 offset:12544\n\t" \
  "v_max3_f32 v204, v32, v33, v34\n\t" \
  "v_mfma_f32_16x16x32_bf16 v[40:43], a[224:227], v[48:51], v[180:183]\n\t" \
  "ds_read_b64_tr_b16 a[152:153], v229 offset:256\n\t" \
  "v_cvt_pk_bf16_f32 v152, v96, v97\n\t" \
  "v_mfma_f32_16x16x32_bf16 v[40:43], a[228:231], v[52:55], v[40:43]\n\t" \
  "v_exp_f32 v98, v98\n\t" \
  "v_mfma_f32_16x16x32_bf16 v[40:43], a[232:235], v[56:59], v[40:43]\n\t" \
  "v_max3_f32 v203, v35, v36, v37\n\t" \
  "v_max3_f32 v204, v204, v38, v39\n\t" \
  "v_mfma_f32_16x16x32_bf16 v[40:43], a[236:239], v[60:63], v[40:43]\n\t" \
  "v_exp_f32 v99, v99\n\t" \
  "v_mfma_f32_16x16x32_bf16 v[44:47], a[252:255], v[60:63], v[180:183]\n\t" \
  "ds_read_b64_tr_b16 a[154:155], v229 offset:4352\n\t" \
  "v_cvt_pk_bf16_f32 v153, v98, v99\n\t" \
  "v_mfma_f32_16x16x32_bf16 v[44:47], a[248:251], v[56:59], v[44:47]\n\t" \
  "v_exp_f32 v100, v100\n\t" \
  "v_mfma_f32_16x16x32_bf16 v[44:47], a[244:247], v[52:55], v[44:47]\n\t" \
  "ds_read_b64_tr_b16 a[156:157], v229 offset:8448\n\t" \
  "v_max3_f32 v203, v203, v40, v41\n\t" \
  "v_mfma_f32_16x16x32_bf16 v[44:47], a[240:243], v[48:51], v[44:47]\n\t" \
  "ds_read_b64_tr_b16 a[158:159], v229 offset:12544\n\t" \
  "v_max3_f32 v204, v204, v42, v43\n\t" \
  "v_mfma_f32_16x16x32_bf16 v[64:67], a[192:195], v[80:83], v[184:187]\n\t" \
  "v_exp_f32 v101, v101\n\t" \
  "v_mfma_f32_16x16x32_bf16 v[64:67], a[196:199], v[84:87], v[64:67]\n\t" \
  "ds_read_b64_tr_b16 a[160:161], v228 offset:512\n\t" \
  "v_cvt_pk_bf16_f32 v154, v100, v101\n\t" \
  "v_mfma_f32_16x16x32_bf16 v[64:67], a[200:203], v[88:91], v[64:67]\n\t" \
  "v_max3_f32 v203, v203, v44, v45\n\t" \
  "v_max3_f32 v204, v204, v46, v47\n\t" \
  "v_mfma_f32_16x16x32_bf16 v[64:67], a[204:207], v[92:95], v[64:67]\n\t" \
  "v_max_f32 v204, v204, v203\n\t" \
  "v_cmp_lt_f32 vcc, 0x42800000, v204\n\t" \
  "v_mfma_f32_16x16x32_bf16 v[68:71], a[220:223], v[92:95], v[184:187]\n\t" \
  "v_exp_f32 v102, v102\n\t" \
  "v_mfma_f32_16x16x32_bf16 v[68:71], a[216:219], v[88:91], v[68:71]\n\t" \
  "s_cbranch_vccnz .Lsf_c31\n\t" \
  ".Lsf_r31:\n\t" \
  "v_mfma_f32_16x16x32_bf16 v[68:71], a[212:215], v[84:87], v[68:71]\n\t" \
  "ds_read_b64_tr_b16 a[162:163], v228 offset:4608\n\t" \
  "v_max3_f32 v204, v64, v65, v66\n\t" \
  "v_mfma_f32_16x16x32_bf16 v[68:71], a[208:211], v[80:83], v[68:71]\n\t" \
  "ds_read_b64_tr_b16 a[164:165], v228 offset:8704\n\t" \
  "v_exp_f32 v103, v103\n\t" \
  "v_mfma_f32_16x16x32_bf16 v[72:75], a[224:227], v[80:83], v[184:187]\n\t" \
  "ds_read_b64_tr_b16 a[166:167], v228 offset:12800\n\t" \
  "v_exp_f32 v104, v104\n\t" \
  "v_mfma_f32_16x16x32_bf16 v[72:75], a[228:231], v[84:87], v[72:75]\n\t" \
  "ds_read_b64_tr_b16 a[168:169], v229 offset:512\n\t" \
  "v_exp_f32 v105, v105\n\t" \
  "v_mfma_f32_16x16x32_bf16 v[72:75], a[232:235], v[88:91], v[72:75]\n\t" \
  "v_max3_f32 v203, v67, v68, v69\n\t" \
  "v_max3_f32 v204, v204, v70, v71\n\t" \
  "v_mfma_f32_16x16x32_bf16 v[72:75], a[236:239], v[92:95], v[72:75]\n\t" \
  "ds_read_b64_tr_b16 a[170:171], v229 offset:4608\n\t" \
  "v_exp_f32 v106, v106\n\t" \
  "v_mfma_f32_16x16x32_bf16 v[76:79], a[252:255], v[92:95], v[184:187]\n\t" \
  "ds_read_b64_tr_b16 a[172:173], v229 offset:8704\n\t" \
  "v_exp_f32 v107, v107\n\t" \
  "v_mfma_f32_16x16x32_bf16 v[76:79], a[248:251], v[88:91], v[76:79]\n\t" \
  "ds_read_b64_tr_b16 a[174:175], v229 offset:12800\n\t" \
  "v_cvt_pk_bf16_f32 v155, v102, v103\n\t" \
  "v_mfma_f32_16x16x32_bf16 v[76:79], a[244:247], v[84:87], v[76:79]\n\t" \
  "v_max3_f32 v203, v203, v72, v73\n\t" \
  "v_max3_f32 v204, v204, v74, v75\n\t" \
  "v_mfma_f32_16x16x32_bf16 v[76:79], a[240:243], v[80:83], v[76:79]\n\t" \
  "ds_read_b64_tr_b16 a[176:177], v228 offset:768\n\t" \
  "v_exp_f32 v108, v108\n\t" \
  "v_mfma_f32_16x16x32_bf16 v[96:99], a[192:195], v[112:115], v[188:191]\n\t" \
  "ds_read_b64_tr_b16 a[178:179], v228 offset:4864\n\t" \
  "v_exp_f32 v109, v109\n\t" \
  "v_mfma_f32_16x16x32_bf16 v[96:99], a[196:199], v[116:119], v[96:99]\n\t" \
  "ds_read_b64_tr_b16 a[180:181], v228 offset:8960\n\t" \
  "v_cvt_pk_bf16_f32 v156, v104, v105\n\t" \
  "v_mfma_f32_16x16x32_bf16 v[96:99], a[200:203], v[120:123], v[96:99]\n\t" \
  "v_max3_f32 v203, v203, v76, v77\n\t" \
  "v_max3_f32 v204, v204, v78, v79\n\t" \
  "v_mfma_f32_16x16x32_bf16 v[96:99], a[204:207], v[124:127], v[96:99]\n\t" \
  "v_max_f32 v204, v204, v203\n\t" \
  "v_cmp_lt_f32 vcc, 0x42800000, v204\n\t" \
  "v_mfma_f32_16x16x32_bf16 v[100:103], a[220:223], v[124:127], v[188:191]\n\t" \
  "ds_read_b64_tr_b16 a[182:183], v228 offset:13056\n\t" \
  "v_exp_f32 v110, v110\n\t" \
  "v_mfma_f32_16x16x32_bf16 v[100:103], a[216:219], v[120:123], v[100:103]\n\t" \
  "s_cbranch_vccnz .Lsf_c32\n\t" \
  ".Lsf_r32:\n\t" \
  "v_mfma_f32_16x16x32_bf16 v[100:103], a[212:215], v[116:119], v[100:103]\n\t" \
  "v_max3_f32 v204, v96, v97, v98\n\t" \
  "v_exp_f32 v111, v111\n\t" \
  "v_mfma_f32_16x16x32_bf16 v[100:103], a[208:211], v[112:115], v[100:103]\n\t" \
  "ds_read_b64_tr_b16 a[184:185], v229 offset:768\n\t" \
  "v_cvt_pk_bf16_f32 v157, v106, v107\n\t" \
  "v_mfma_f32_16x16x32_bf16 v[104:107], a[224:227], v[112:115], v[188:191]\n\t" \
  "ds_read_b64_tr_b16 a[186:187], v229 offset:4864\n\t" \
  "v_cvt_pk_bf16_f32 v158, v108, v109\n\t" \
  "v_mfma_f32_16x16x32_bf16 v[104:107], a[228:231], v[116:119], v[104:107]\n\t" \
  "ds_read_b64_tr_b16 a[188:189], v229 offset:8960\n\t" \
  "v_cvt_pk_bf16_f32 v159, v110, v111\n\t" \
  "v_mfma_f32_16x16x32_bf16 v[104:107], a[232:235], v[120:123], v[104:107]\n\t" \
  "v_max3_f32 v203, v99, v100, v101\n\t" \
  "v_max3_f32 v204, v204, v102, v103\n\t" \
  "v_mfma_f32_16x16x32_bf16 v[104:107], a[236:239], v[124:127], v[104:107]\n\t" \
  "ds_read_b64_tr_b16 a[190:191], v229 offset:13056\n\t" \
  "s_nop 0\n\t" \
  "v_mfma_f32_16x16x32_bf16 v[108:111], a[252:255], v[124:127], v[188:191]\n\t" \
  "v_exp_f32 v0, v0\n\t" \
  "v_mfma_f32_16x16x32_bf16 v[108:111], a[248:251], v[120:123], v[108:111]\n\t" \
  "v_exp_f32 v1, v1\n\t" \
  "v_mfma_f32_16x16x32_bf16 v[108:111], a[244:247], v[116:119], v[108:111]\n\t" \
  "s_waitcnt vmcnt(0) lgkmcnt(0)\n\t" \
  "v_max3_f32 v203, v203, v104, v105\n\t" \
  "v_max3_f32 v204, v204, v106, v107\n\t" \
  "v_mfma_f32_16x16x32_bf16 v[108:111], a[240:243], v[112:115], v[108:111]\n\t" \
  "s_barrier\n\t" \
  "v_mfma_f32_16x16x32_bf16 a[0:3], a[128:131], v[128:131], a[0:3]\n\t" \
  "v_exp_f32 v2, v2\n\t" \
  "v_mfma_f32_16x16x32_bf16 a[0:3], a[132:135], v[132:135], a[0:3]\n\t" \
  "v_exp_f32 v3, v3\n\t" \
  "v_mfma_f32_16x16x32_bf16 a[4:7], a[136:139], v[128:131], a[4:7]\n\t" \
  "v_exp_f32 v4, v4\n\t" \
  "v_mfma_f32_16x16x32_bf16 a[4:7], a[140:143], v[132:135], a[4:7]\n\t" \
  "v_max3_f32 v203, v203, v108, v109\n\t" \
  "v_max3_f32 v204, v204, v110, v111\n\t" \
  "v_mfma_f32_16x16x32_bf16 a[8:11], a[144:147], v[128:131], a[8:11]\n\t" \
  "v_max_f32 v204, v204, v203\n\t" \
  "v_cmp_lt_f32 vcc, 0x42800000, v204\n\t" \
  "v_mfma_f32_16x16x32_bf16 a[8:11], a[148:151], v[132:135], a[8:11]\n\t" \
  "v_exp_f32 v5, v5\n\t" \
  "v_mfma_f32_16x16x32_bf16 a[12:15], a[152:155], v[128:131], a[12:15]\n\t" \
  "s_cbranch_vccnz .Lsf_c33\n\t" \
  ".Lsf_r33:\n\t" \
  "v_mfma_f32_16x16x32_bf16 a[12:15], a[156:159], v[132:135], a[12:15]\n\t" \
  "s_add_u32 m0, s38, 65536\n\t" \
  "v_exp_f32 v6, v6\n\t" \
  "v_mfma_f32_16x16x32_bf16 a[16:19], a[160:163], v[128:131], a[16:19]\n\t" \
  "s_add_u32 s96, s38, 81920\n\t" \
  "v_exp_f32 v7, v7\n\t" \
  "v_mfma_f32_16x16x32_bf16 a[16:19], a[164:167], v[132:135], a[16:19]\n\t" \
  "v_exp_f32 v8, v8\n\t" \
  "v_mfma_f32_16x16x32_bf16 a[20:23], a[168:171], v[128:131], a[20:23]\n\t" \
  "buffer_load_dwordx4 v224, s[60:63], s96 offen offset:0 lds\n\t" \
  "v_mfma_f32_16x16x32_bf16 a[20:23], a[172:175], v[132:135], a[20:23]\n\t" \
  "v_exp_f32 v9, v9\n\t" \
  "v_mfma_f32_16x16x32_bf16 a[24:27], a[176:179], v[128:131], a[24:27]\n\t" \
  "v_exp_f32 v10, v10\n\t" \
  "v_mfma_f32_16x16x32_bf16 a[24:27], a[180:183], v[132:135], a[24:27]\n\t" \
  "v_exp_f32 v11, v11\n\t" \
  "v_mfma_f32_16x16x32_bf16 a[28:31], a[184:187], v[128:131], a[28:31]\n\t" \
  "v_exp_f32 v12, v12\n\t" \
  "v_mfma_f32_16x16x32_bf16 a[28:31], a[188:191], v[132:135], a[28:31]\n\t" \
  "v_exp_f32 v13, v13\n\t" \
  "v_smfmac_f32_16x16x64_bf16 v[160:163], v[216:219], v[128:135], v208\n\t" \
  "ds_read_b128 a[192:195], v220 offset:0\n\t" \
  "v_cvt_pk_bf16_f32 v128, v0, v1\n\t" \
  "v_mfma_f32_16x16x32_bf16 a[32:35], a[128:131], v[136:139], a[32:35]\n\t" \
  "buffer_load_dwordx4 v225, s[60:63], s96 offen offset:1024 lds\n\t" \
  "v_mfma_f32_16x16x32_bf16 a[32:35], a[132:135], v[140:143], a[32:35]\n\t" \
  "v_exp_f32 v14, v14\n\t" \
  "v_mfma_f32_16x16x32_bf16 a[36:39], a[136:139], v[136:139], a[36:39]\n\t" \
  "v_exp_f32 v15, v15\n\t" \
  "v_mfma_f32_16x16x32_bf16 a[36:39], a[140:143], v[140:143], a[36:39]\n\t" \
  "ds_read_b128 a[196:199], v221 offset:0\n\t" \
  "v_cvt_pk_bf16_f32 v129, v2, v3\n\t" \
  "v_mfma_f32_16x16x32_bf16 a[40:43], a[144:147], v[136:139], a[40:43]\n\t" \
  "v_exp_f32 v32, v32\n\t" \
  "v_mfma_f32_16x16x32_bf16 a[40:43], a[148:151], v[140:143], a[40:43]\n\t" \
  "v_exp_f32 v33, v33\n\t" \
  "v_mfma_f32_16x16x32_bf16 a[44:47], a[152:155], v[136:139], a[44:47]\n\t" \
  "buffer_load_dwordx4 v226, s[60:63], s96 offen offset:2048 lds\n\t" \
  "v_mfma_f32_16x16x32_bf16 a[44:47], a[156:159], v[140:143], a[44:47]\n\t" \
  "ds_read_b128 a[200:203], v222 offset:0\n\t" \
  "v_cvt_pk_bf16_f32 v130, v4, v5\n\t" \
  "v_mfma_f32_16x16x32_bf16 a[48:51], a[160:163], v[136:139], a[48:51]\n\t" \
  "v_exp_f32 v34, v34\n\t" \
  "v_mfma_f32_16x16x32_bf16 a[48:51], a[164:167], v[140:143], a[48:51]\n\t" \
  "ds_read_b128 a[204:207], v223 offset:0\n\t" \
  "v_cvt_pk_bf16_f32 v131, v6, v7\n\t" \
  "v_mfma_f32_16x16x32_bf16 a[52:55], a[168:171], v[136:139], a[52:55]\n\t" \
  "v_exp_f32 v35, v35\n\t" \
  "v_mfma_f32_16x16x32_bf16 a[52:55], a[172:175], v[140:143], a[52:55]\n\t" \
  "ds_read_b128 a[208:211], v220 offset:4096\n\t" \
  "v_cvt_pk_bf16_f32 v132, v8, v9\n\t" \
  "v_mfma_f32_16x16x32_bf16 a[56:59], a[176:179], v[136:139], a[56:59]\n\t" \
  "buffer_load_dwordx4 v227, s[60:63], s96 offen offset:3072 lds\n\t" \
  "v_mfma_f32_16x16x32_bf16 a[56:59], a[180:183], v[140:143], a[56:59]\n\t" \
  "v_exp_f32 v36, v36\n\t" \
  "v_mfma_f32_16x16x32_bf16 a[60:63], a[184:187], v[136:139], a[60:63]\n\t" \
  "s_add_u32 m0, s38, 81920\n\t" \
  "ds_read_b128 a[212:215], v221 offset:4096\n\t" \
  "v_cvt_pk_bf16_f32 v133, v10, v11\n\t" \
  "v_mfma_f32_16x16x32_bf16 a[60:63], a[188:191], v[140:143], a[60:63]\n\t" \
  "s_add_u32 s96, s38, 65536\n\t" \
  "v_exp_f32 v37, v37\n\t" \
  "v_smfmac_f32_16x16x64_bf16 v[164:167], v[216:219], v[136:143], v208\n\t" \
  "ds_read_b128 a[216:219], v222 offset:4096\n\t" \
  "v_cvt_pk_bf16_f32 v134, v12, v13\n\t" \
  "v_mfma_f32_16x16x32_bf16 a[64:67], a[128:131], v[144:147], a[64:67]\n\t" \
  "v_exp_f32 v38, v38\n\t" \
  "v_mfma_f32_16x16x32_bf16 a[64:67], a[132:135], v[148:151], a[64:67]\n\t" \
  "buffer_load_dwordx4 v230, s[64:67], s96 offen offset:0 lds\n\t" \
  "v_mfma_f32_16x16x32_bf16 a[68:71], a[136:139], v[144:147], a[68:71]\n\t" \
  "ds_read_b128 a[220:223], v223 offset:4096\n\t" \
  "v_cvt_pk_bf16_f32 v135, v14, v15\n\t" \
  "v_mfma_f32_16x16x32_bf16 a[68:71], a[140:143], v[148:151], a[68:71]\n\t" \
  "v_exp_f32 v39, v39\n\t" \
  "v_mfma_f32_16x16x32_bf16 a[72:75], a[144:147], v[144:147], a[72:75]\n\t" \
  "v_exp_f32 v40, v40\n\t" \
  "v_mfma_f32_16x16x32_bf16 a[72:75], a[148:151], v[148:151], a[72:75]\n\t" \
  "ds_read_b128 a[224:227], v220 offset:8192\n\t" \
  "v_cvt_pk_bf16_f32 v136, v32, v33\n\t" \
  "v_mfma_f32_16x16x32_bf16 a[76:79], a[152:155], v[144:147], a[76:79]\n\t" \
  "ds_read_b128 a[228:231], v221 offset:8192\n\t" \
  "v_cvt_pk_bf16_f32 v137, v34, v35\n\t" \
  "v_mfma_f32_16x16x32_bf16 a[76:79], a[156:159], v[148:151], a[76:79]\n\t" \
  "v_exp_f32 v41, v41\n\t" \
  "v_mfma_f32_16x16x32_bf16 a[80:83], a[160:163], v[144:147], a[80:83]\n\t" \
  "buffer_load_dwordx4 v231, s[64:67], s96 offen offset:1024 lds\n\t" \
  "v_mfma_f32_16x16x32_bf16 a[80:83], a[164:167], v[148:151], a[80:83]\n\t" \
  "v_exp_f32 v42, v42\n\t" \
  "v_mfma_f32_16x16x32_bf16 a[84:87], a[168:171], v[144:147], a[84:87]\n\t" \
  "ds_read_b128 a[232:235], v222 offset:8192\n\t" \
  "v_cvt_pk_bf16_f32 v138, v36, v37\n\t" \
  "v_mfma_f32_16x16x32_bf16 a[84:87], a[172:175], v[148:151], a[84:87]\n\t" \
  "v_exp_f32 v43, v43\n\t" \
  "v_mfma_f32_16x16x32_bf16 a[88:91], a[176:179], v[144:147], a[88:91]\n\t" \
  "ds_read_b128 a[236:239], v223 offset:8192\n\t" \
  "v_cvt_pk_bf16_f32 v139, v38, v39\n\t" \
  "v_mfma_f32_16x16x32_bf16 a[88:91], a[180:183], v[148:151], a[88:91]\n\t" \
  "v_exp_f32 v44, v44\n\t" \
  "v_mfma_f32_16x16x32_bf16 a[92:95], a[184:187], v[144:147], a[92:95]\n\t" \
  "buffer_load_dwordx4 v230, s[64:67], s96 offen offset:2048 lds\n\t" \
  "v_mfma_f32_16x16x32_bf16 a[92:95], a[188:191], v[148:151], a[92:95]\n\t" \
  "ds_read_b128 a[240:243], v220 offset:12288\n\t" \
  "v_cvt_pk_bf16_f32 v140, v40, v41\n\t" \
  "v_smfmac_f32_16x16x64_bf16 v[168:171], v[216:219], v[144:151], v208\n\t" \
  "ds_read_b128 a[244:247], v221 offset:12288\n\t" \
  "v_cvt_pk_bf16_f32 v141, v42, v43\n\t" \
  "v_mfma_f32_16x16x32_bf16 a[96:99], a[128:131], v[152:155], a[96:99]\n\t" \
  "v_exp_f32 v45, v45\n\t" \
  "v_mfma_f32_16x16x32_bf16 a[96:99], a[132:135], v[156:159], a[96:99]\n\t" \
  "v_exp_f32 v46, v46\n\t" \
  "v_mfma_f32_16x16x32_bf16 a[100:103], a[136:139], v[152:155], a[100:103]\n\t" \
  "v_exp_f32 v47, v47\n\t" \
  "v_mfma_f32_16x16x32_bf16 a[100:103], a[140:143], v[156:159], a[100:103]\n\t" \
  "buffer_load_dwordx4 v231, s[64:67], s96 offen offset:3072 lds\n\t" \
  "v_mfma_f32_16x16x32_bf16 a[104:107], a[144:147], v[152:155], a[104:107]\n\t" \
  "ds_read_b128 a[248:251], v222 offset:12288\n\t" \
  "v_cvt_pk_bf16_f32 v142, v44, v45\n\t" \
  "v_mfma_f32_16x16x32_bf16 a[104:107], a[148:151], v[156:159], a[104:107]\n\t" \
  "ds_read_b128 a[252:255], v223 offset:12288\n\t" \
  "v_cvt_pk_bf16_f32 v143, v46, v47\n\t" \
  "v_mfma_f32_16x16x32_bf16 a[108:111], a[152:155], v[152:155], a[108:111]\n\t" \
  "v_exp_f32 v64, v64\n\t" \
  "v_mfma_f32_16x16x32_bf16 a[108:111], a[156:159], v[156:159], a[108:111]\n\t" \
  "v_exp_f32 v65, v65\n\t" \
  "v_mfma_f32_16x16x32_bf16 a[112:115], a[160:163], v[152:155], a[112:115]\n\t" \
  "v_exp_f32 v66, v66\n\t" \
  "v_mfma_f32_16x16x32_bf16 a[112:115], a[164:167], v[156:159], a[112:115]\n\t" \
  "v_exp_f32 v67, v67\n\t" \
  "v_mfma_f32_16x16x32_bf16 a[116:119], a[168:171], v[152:155], a[116:119]\n\t" \
  "v_exp_f32 v68, v68\n\t" \
  "v_mfma_f32_16x16x32_bf16 a[116:119], a[172:175], v[156:159], a[116:119]\n\t" \
  "v_exp_f32 v69, v69\n\t" \
  "v_mfma_f32_16x16x32_bf16 a[120:123], a[176:179], v[152:155], a[120:123]\n\t" \
  "v_exp_f32 v70, v70\n\t" \
  "v_mfma_f32_16x16x32_bf16 a[120:123], a[180:183], v[156:159], a[120:123]\n\t" \
  "v_exp_f32 v71, v71\n\t" \
  "v_mfma_f32_16x16x32_bf16 a[124:127], a[184:187], v[152:155], a[124:127]\n\t" \
  "v_exp_f32 v72, v72\n\t" \
  "v_mfma_f32_16x16x32_bf16 a[124:127], a[188:191], v[156:159], a[124:127]\n\t" \
  "v_exp_f32 v73, v73\n\t" \
  "v_smfmac_f32_16x16x64_bf16 v[172:175], v[216:219], v[152:159], v208\n\t" \
  "s_waitcnt lgkmcnt(12)\n\t" \
  "s_cmp_lg_u32 s99, 0\n\t" \
  "s_cbranch_scc1 .Lsf_p3\n\t" \
  ".Lsf_q3:\n\t" \
  "v_mfma_f32_16x16x32_bf16 v[0:3], a[192:195], v[16:19], v[176:179]\n\t" \
  "ds_read_b64_tr_b16 a[128:129], v210 offset:0\n\t" \
  "v_cvt_pk_bf16_f32 v144, v64, v65\n\t" \
  "v_mfma_f32_16x16x32_bf16 v[0:3], a[196:199], v[20:23], v[0:3]\n\t" \
  "v_exp_f32 v74, v74\n\t" \
  "v_mfma_f32_16x16x32_bf16 v[0:3], a[200:203], v[24:27], v[0:3]\n\t" \
  "ds_read_b64_tr_b16 a[130:131], v210 offset:4096\n\t" \
  "v_cvt_pk_bf16_f32 v145, v66, v67\n\t" \
  "v_mfma_f32_16x16x32_bf16 v[0:3], a[204:207], v[28:31], v[0:3]\n\t" \
  "s_waitcnt lgkmcnt(10)\n\t" \
  "v_exp_f32 v75, v75\n\t" \
  "v_mfma_f32_16x16x32_bf16 v[4:7], a[220:223], v[28:31], v[176:179]\n\t" \
  "ds_read_b64_tr_b16 a[132:133], v210 offset:8192\n\t" \
  "v_cvt_pk_bf16_f32 v146, v68, v69\n\t" \
  "v_mfma_f32_16x16x32_bf16 v[4:7], a[216:219], v[24:27], v[4:7]\n\t" \
  "v_exp_f32 v76, v76\n\t" \
  "v_mfma_f32_16x16x32_bf16 v[4:7], a[212:215], v[20:23], v[4:7]\n\t" \
  "ds_read_b64_tr_b16 a[134:135], v210 offset:12288\n\t" \
  "v_max3_f32 v204, v0, v1, v2\n\t" \
  "v_mfma_f32_16x16x32_bf16 v[4:7], a[208:211], v[16:19], v[4:7]\n\t" \
  "s_waitcnt lgkmcnt(8)\n\t" \
  "ds_read_b64_tr_b16 a[136:137], v211 offset:0\n\t" \
  "v_cvt_pk_bf16_f32 v147, v70, v71\n\t" \
  "v_mfma_f32_16x16x32_bf16 v[8:11], a[224:227], v[16:19], v[176:179]\n\t" \
  "v_exp_f32 v77, v77\n\t" \
  "v_mfma_f32_16x16x32_bf16 v[8:11], a[228:231], v[20:23], v[8:11]\n\t" \
  "ds_read_b64_tr_b16 a[138:139], v211 offset:4096\n\t" \
  "v_cvt_pk_bf16_f32 v148, v72, v73\n\t" \
  "v_mfma_f32_16x16x32_bf16 v[8:11], a[232:235], v[24:27], v[8:11]\n\t" \
  "v_max3_f32 v203, v3, v4, v5\n\t" \
  "v_max3_f32 v204, v204, v6, v7\n\t" \
  "v_mfma_f32_16x16x32_bf16 v[8:11], a[236:239], v[28:31], v[8:11]\n\t" \
  "s_waitcnt lgkmcnt(6)\n\t" \
  "ds_read_b64_tr_b16 a[140:141], v211 offset:8192\n\t" \
  "v_cvt_pk_bf16_f32 v149, v74, v75\n\t" \
  "v_mfma_f32_16x16x32_bf16 v[12:15], a[252:255], v[28:31], v[176:179]\n\t" \
  "v_exp_f32 v78, v78\n\t" \
  "v_mfma_f32_16x16x32_bf16 v[12:15], a[248:251], v[24:27], v[12:15]\n\t" \
  "ds_read_b64_tr_b16 a[142:143], v211 offset:12288\n\t" \
  "v_cvt_pk_bf16_f32 v150, v76, v77\n\t" \
  "v_mfma_f32_16x16x32_bf16 v[12:15], a[244:247], v[20:23], v[12:15]\n\t" \
  "v_max3_f32 v203, v203, v8, v9\n\t" \
  "v_max3_f32 v204, v204, v10, v11\n\t" \
  "v_mfma_f32_16x16x32_bf16 v[12:15], a[240:243], v[16:19], v[12:15]\n\t" \
  "v_exp_f32 v79, v79\n\t" \
  "v_mfma_f32_16x16x32_bf16 v[32:35], a[192:195], v[48:51], v[180:183]\n\t" \
  "ds_read_b64_tr_b16 a[144:145], v210 offset:256\n\t" \
  "v_cvt_pk_bf16_f32 v151, v78, v79\n\t" \
  "v_mfma_f32_16x16x32_bf16 v[32:35], a[196:199], v[52:55], v[32:35]\n\t" \
  "v_exp_f32 v96, v96\n\t" \
  "v_mfma_f32_16x16x32_bf16 v[32:35], a[200:203], v[56:59], v[32:35]\n\t" \
  "ds_read_b64_tr_b16 a[146:147], v210 offset:4352\n\t" \
  "v_max3_f32 v203, v203, v12, v13\n\t" \
  "v_mfma_f32_16x16x32_bf16 v[32:35], a[204:207], v[60:63], v[32:35]\n\t" \
  "v_max3_f32 v204, v204, v14, v15\n\t" \
  "v_max_f32 v204, v204, v203\n\t" \
  "v_mfma_f32_16x16x32_bf16 v[36:39], a[220:223], v[60:63], v[180:183]\n\t" \
  "ds_read_b64_tr_b16 a[148:149], v210 offset:8448\n\t" \
  "v_cmp_lt_f32 vcc, 0x42800000, v204\n\t" \
  "v_mfma_f32_16x16x32_bf16 v[36:39], a[216:219], v[56:59], v[36:39]\n\t" \
  "v_exp_f32 v97, v97\n\t" \
  "v_mfma_f32_16x16x32_bf16 v[36:39], a[212:215], v[52:55], v[36:39]\n\t" \
  "s_cbranch_vccnz .Lsf_c40\n\t" \
  ".Lsf_r40:\n\t" \
  "v_mfma_f32_16x16x32_bf16 v[36:39], a[208:211], v[48:51], v[36:39]\n\t" \
  "ds_read_b64_tr_b16 a[150:151], v210 offset:12544\n\t" \
  "v_max3_f32 v204, v32, v33, v34\n\t" \
  "v_mfma_f32_16x16x32_bf16 v[40:43], a[224:227], v[48:51], v[180:183]\n\t" \
  "ds_read_b64_tr_b16 a[152:153], v211 offset:256\n\t" \
  "v_cvt_pk_bf16_f32 v152, v96, v97\n\t" \
  "v_mfma_f32_16x16x32_bf16 v[40:43], a[228:231], v[52:55], v[40:43]\n\t" \
  "v_exp_f32 v98, v98\n\t" \
  "v_mfma_f32_16x16x32_bf16 v[40:43], a[232:235], v[56:59], v[40:43]\n\t" \
  "v_max3_f32 v203, v35, v36, v37\n\t" \
  "v_max3_f32 v204, v204, v38, v39\n\t" \
  "v_mfma_f32_16x16x32_bf16 v[40:43], a[236:239], v[60:63], v[40:43]\n\t" \
  "v_exp_f32 v99, v99\n\t" \
  "v_mfma_f32_16x16x32_bf16 v[44:47], a[252:255], v[60:63], v[180:183]\n\t" \
  "ds_read_b64_tr_b16 a[154:155], v211 offset:4352\n\t" \
  "v_cvt_pk_bf16_f32 v153, v98, v99\n\t" \
  "v_mfma_f32_16x16x32_bf16 v[44:47], a[248:251], v[56:59], v[44:47]\n\t" \
  "v_exp_f32 v100, v100\n\t" \
  "v_mfma_f32_16x16x32_bf16 v[44:47], a[244:247], v[52:55], v[44:47]\n\t" \
  "ds_read_b64_tr_b16 a[156:157], v211 offset:8448\n\t" \
  "v_max3_f32 v203, v203, v40, v41\n\t" \
  "v_mfma_f32_16x16x32_bf16 v[44:47], a[240:243], v[48:51], v[44:47]\n\t" \
  "ds_read_b64_tr_b16 a[158:159], v211 offset:12544\n\t" \
  "v_max3_f32 v204, v204, v42, v43\n\t" \
  "v_mfma_f32_16x16x32_bf16 v[64:67], a[192:195], v[80:83], v[184:187]\n\t" \
  "v_exp_f32 v101, v101\n\t" \
  "v_mfma_f32_16x16x32_bf16 v[64:67], a[196:199], v[84:87], v[64:67]\n\t" \
  "ds_read_b64_tr_b16 a[160:161], v210 offset:512\n\t" \
  "v_cvt_pk_bf16_f32 v154, v100, v101\n\t" \
  "v_mfma_f32_16x16x32_bf16 v[64:67], a[200:203], v[88:91], v[64:67]\n\t" \
  "v_max3_f32 v203, v203, v44, v45\n\t" \
  "v_max3_f32 v204, v204, v46, v47\n\t" \
  "v_mfma_f32_16x16x32_bf16 v[64:67], a[204:207], v[92:95], v[64:67]\n\t" \
  "v_max_f32 v204, v204, v203\n\t" \
  "v_cmp_lt_f32 vcc, 0x42800000, v204\n\t" \
  "v_mfma_f32_16x16x32_bf16 v[68:71], a[220:223], v[92:95], v[184:187]\n\t" \
  "v_exp_f32 v102, v102\n\t" \
  "v_mfma_f32_16x16x32_bf16 v[68:71], a[216:219], v[88:91], v[68:71]\n\t" \
  "s_cbranch_vccnz .Lsf_c41\n\t" \
  ".Lsf_r41:\n\t" \
  "v_mfma_f32_16x16x32_bf16 v[68:71], a[212:215], v[84:87], v[68:71]\n\t" \
  "ds_read_b64_tr_b16 a[162:163], v210 offset:4608\n\t" \
  "v_max3_f32 v204, v64, v65, v66\n\t" \
  "v_mfma_f32_16x16x32_bf16 v[68:71], a[208:211], v[80:83], v[68:71]\n\t" \
  "ds_read_b64_tr_b16 a[164:165], v210 offset:8704\n\t" \
  "v_exp_f32 v103, v103\n\t" \
  "v_mfma_f32_16x16x32_bf16 v[72:75], a[224:227], v[80:83], v[184:187]\n\t" \
  "ds_read_b64_tr_b16 a[166:167], v210 offset:12800\n\t" \
  "v_exp_f32 v104, v104\n\t" \
  "v_mfma_f32_16x16x32_bf16 v[72:75], a[228:231], v[84:87], v[72:75]\n\t" \
  "ds_read_b64_tr_b16 a[168:169], v211 offset:512\n\t" \
  "v_exp_f32 v105, v105\n\t" \
  "v_mfma_f32_16x16x32_bf16 v[72:75], a[232:235], v[88:91], v[72:75]\n\t" \
  "v_max3_f32 v203, v67, v68, v69\n\t" \
  "v_max3_f32 v204, v204, v70, v71\n\t" \
  "v_mfma_f32_16x16x32_bf16 v[72:75], a[236:239], v[92:95], v[72:75]\n\t" \
  "ds_read_b64_tr_b16 a[170:171], v211 offset:4608\n\t" \
  "v_exp_f32 v106, v106\n\t" \
  "v_mfma_f32_16x16x32_bf16 v[76:79], a[252:255], v[92:95], v[184:187]\n\t" \
  "ds_read_b64_tr_b16 a[172:173], v211 offset:8704\n\t" \
  "v_exp_f32 v107, v107\n\t" \
  "v_mfma_f32_16x16x32_bf16 v[76:79], a[248:251], v[88:91], v[76:79]\n\t" \
  "ds_read_b64_tr_b16 a[174:175], v211 offset:12800\n\t" \
  "v_cvt_pk_bf16_f32 v155, v102, v103\n\t" \
  "v_mfma_f32_16x16x32_bf16 v[76:79], a[244:247], v[84:87], v[76:79]\n\t" \
  "v_max3_f32 v203, v203, v72, v73\n\t" \
  "v_max3_f32 v204, v204, v74, v75\n\t" \
  "v_mfma_f32_16x16x32_bf16 v[76:79], a[240:243], v[80:83], v[76:79]\n\t" \
  "ds_read_b64_tr_b16 a[176:177], v210 offset:768\n\t" \
  "v_exp_f32 v108, v108\n\t" \
  "v_mfma_f32_16x16x32_bf16 v[96:99], a[192:195], v[112:115], v[188:191]\n\t" \
  "ds_read_b64_tr_b16 a[178:179], v210 offset:4864\n\t" \
  "v_exp_f32 v109, v109\n\t" \
  "v_mfma_f32_16x16x32_bf16 v[96:99], a[196:199], v[116:119], v[96:99]\n\t" \
  "ds_read_b64_tr_b16 a[180:181], v210 offset:8960\n\t" \
  "v_cvt_pk_bf16_f32 v156, v104, v105\n\t" \
  "v_mfma_f32_16x16x32_bf16 v[96:99], a[200:203], v[120:123], v[96:99]\n\t" \
  "v_max3_f32 v203, v203, v76, v77\n\t" \
  "v_max3_f32 v204, v204, v78, v79\n\t" \
  "v_mfma_f32_16x16x32_bf16 v[96:99], a[204:207], v[124:127], v[96:99]\n\t" \
  "v_max_f32 v204, v204, v203\n\t" \
  "v_cmp_lt_f32 vcc, 0x42800000, v204\n\t" \
  "v_mfma_f32_16x16x32_bf16 v[100:103], a[220:223], v[124:127], v[188:191]\n\t" \
  "ds_read_b64_tr_b16 a[182:183], v210 offset:13056\n\t" \
  "v_exp_f32 v110, v110\n\t" \
  "v_mfma_f32_16x16x32_bf16 v[100:103], a[216:219], v[120:123], v[100:103]\n\t" \
  "s_cbranch_vccnz .Lsf_c42\n\t" \
  ".Lsf_r42:\n\t" \
  "v_mfma_f32_16x16x32_bf16 v[100:103], a[212:215], v[116:119], v[100:103]\n\t" \
  "v_max3_f32 v204, v96, v97, v98\n\t" \
  "v_exp_f32 v111, v111\n\t" \
  "v_mfma_f32_16x16x32_bf16 v[100:103], a[208:211], v[112:115], v[100:103]\n\t" \
  "ds_read_b64_tr_b16 a[184:185], v211 offset:768\n\t" \
  "v_cvt_pk_bf16_f32 v157, v106, v107\n\t" \
  "v_mfma_f32_16x16x32_bf16 v[104:107], a[224:227], v[112:115], v[188:191]\n\t" \
  "ds_read_b64_tr_b16 a[186:187], v211 offset:4864\n\t" \
  "v_cvt_pk_bf16_f32 v158, v108, v109\n\t" \
  "v_mfma_f32_16x16x32_bf16 v[104:107], a[228:231], v[116:119], v[104:107]\n\t" \
  "ds_read_b64_tr_b16 a[188:189], v211 offset:8960\n\t" \
  "v_cvt_pk_bf16_f32 v159, v110, v111\n\t" \
  "v_mfma_f32_16x16x32_bf16 v[104:107], a[232:235], v[120:123], v[104:107]\n\t" \
  "v_max3_f32 v203, v99, v100, v101\n\t" \
  "v_max3_f32 v204, v204, v102, v103\n\t" \
  "v_mfma_f32_16x16x32_bf16 v[104:107], a[236:239], v[124:127], v[104:107]\n\t" \
  "ds_read_b64_tr_b16 a[190:191], v211 offset:13056\n\t" \
  "s_nop 0\n\t" \
  "v_mfma_f32_16x16x32_bf16 v[108:111], a[252:255], v[124:127], v[188:191]\n\t" \
  "v_exp_f32 v0, v0\n\t" \
  "v_mfma_f32_16x16x32_bf16 v[108:111], a[248:251], v[120:123], v[108:111]\n\t" \
  "v_exp_f32 v1, v1\n\t" \
  "v_mfma_f32_16x16x32_bf16 v[108:111], a[244:247], v[116:119], v[108:111]\n\t" \
  "s_waitcnt vmcnt(0) lgkmcnt(0)\n\t" \
  "v_max3_f32 v203, v203, v104, v105\n\t" \
  "v_max3_f32 v204, v204, v106, v107\n\t" \
  "v_mfma_f32_16x16x32_bf16 v[108:111], a[240:243], v[112:115], v[108:111]\n\t" \
  "s_barrier\n\t" \
  "v_mfma_f32_16x16x32_bf16 a[0:3], a[128:131], v[128:131], a[0:3]\n\t" \
  "v_exp_f32 v2, v2\n\t" \
  "v_mfma_f32_16x16x32_bf16 a[0:3], a[132:135], v[132:135], a[0:3]\n\t" \
  "v_exp_f32 v3, v3\n\t" \
  "v_mfma_f32_16x16x32_bf16 a[4:7], a[136:139], v[128:131], a[4:7]\n\t" \
  "v_exp_f32 v4, v4\n\t" \
  "v_mfma_f32_16x16x32_bf16 a[4:7], a[140:143], v[132:135], a[4:7]\n\t" \
  "v_max3_f32 v203, v203, v108, v109\n\t" \
  "v_max3_f32 v204, v204, v110, v111\n\t" \
  "v_mfma_f32_16x16x32_bf16 a[8:11], a[144:147], v[128:131], a[8:11]\n\t" \
  "v_max_f32 v204, v204, v203\n\t" \
  "v_cmp_lt_f32 vcc, 0x42800000, v204\n\t" \
  "v_mfma_f32_16x16x32_bf16 a[8:11], a[148:151], v[132:135], a[8:11]\n\t" \
  "v_exp_f32 v5, v5\n\t" \
  "v_mfma_f32_16x16x32_bf16 a[12:15], a[152:155], v[128:131], a[12:15]\n\t" \
  "s_cbranch_vccnz .Lsf_c43\n\t" \
  ".Lsf_r43:\n\t" \
  "v_mfma_f32_16x16x32_bf16 a[12:15], a[156:159], v[132:135], a[12:15]\n\t" \
  "s_add_u32 m0, s38, 49152\n\t" \
  "v_exp_f32 v6, v6\n\t" \
  "v_mfma_f32_16x16x32_bf16 a[16:19], a[160:163], v[128:131], a[16:19]\n\t" \
  "s_add_u32 s96, s38, 98304\n\t" \
  "v_exp_f32 v7, v7\n\t" \
  "v_mfma_f32_16x16x32_bf16 a[16:19], a[164:167], v[132:135], a[16:19]\n\t" \
  "v_exp_f32 v8, v8\n\t" \
  "v_mfma_f32_16x16x32_bf16 a[20:23], a[168:171], v[128:131], a[20:23]\n\t" \
  "buffer_load_dwordx4 v224, s[60:63], s96 offen offset:0 lds\n\t" \
  "v_mfma_f32_16x16x32_bf16 a[20:23], a[172:175], v[132:135], a[20:23]\n\t" \
  "v_exp_f32 v9, v9\n\t" \
  "v_mfma_f32_16x16x32_bf16 a[24:27], a[176:179], v[128:131], a[24:27]\n\t" \
  "v_exp_f32 v10, v10\n\t" \
  "v_mfma_f32_16x16x32_bf16 a[24:27], a[180:183], v[132:135], a[24:27]\n\t" \
  "v_exp_f32 v11, v11\n\t" \
  "v_mfma_f32_16x16x32_bf16 a[28:31], a[184:187], v[128:131], a[28:31]\n\t" \
  "v_exp_f32 v12, v12\n\t" \
  "v_mfma_f32_16x16x32_bf16 a[28:31], a[188:191], v[132:135], a[28:31]\n\t" \
  "v_exp_f32 v13, v13\n\t" \
  "v_smfmac_f32_16x16x64_bf16 v[160:163], v[216:219], v[128:135], v208\n\t" \
  "ds_read_b128 a[192:195], v220 offset:16384\n\t" \
  "v_cvt_pk_bf16_f32 v128, v0, v1\n\t" \
  "v_mfma_f32_16x16x32_bf16 a[32:35], a[128:131], v[136:139], a[32:35]\n\t" \
  "buffer_load_dwordx4 v225, s[60:63], s96 offen offset:1024 lds\n\t" \
  "v_mfma_f32_16x16x32_bf16 a[32:35], a[132:135], v[140:143], a[32:35]\n\t" \
  "v_exp_f32 v14, v14\n\t" \
  "v_mfma_f32_16x16x32_bf16 a[36:39], a[136:139], v[136:139], a[36:39]\n\t" \
  "v_exp_f32 v15, v15\n\t" \
  "v_mfma_f32_16x16x32_bf16 a[36:39], a[140:143], v[140:143], a[36:39]\n\t" \
  "ds_read_b128 a[196:199], v221 offset:16384\n\t" \
  "v_cvt_pk_bf16_f32 v129, v2, v3\n\t" \
  "v_mfma_f32_16x16x32_bf16 a[40:43], a[144:147], v[136:139], a[40:43]\n\t" \
  "v_exp_f32 v32, v32\n\t" \
  "v_mfma_f32_16x16x32_bf16 a[40:43], a[148:151], v[140:143], a[40:43]\n\t" \
  "v_exp_f32 v33, v33\n\t" \
  "v_mfma_f32_16x16x32_bf16 a[44:47], a[152:155], v[136:139], a[44:47]\n\t" \
  "buffer_load_dwordx4 v226, s[60:63], s96 offen offset:2048 lds\n\t" \
  "v_mfma_f32_16x16x32_bf16 a[44:47], a[156:159], v[140:143], a[44:47]\n\t" \
  "ds_read_b128 a[200:203], v222 offset:16384\n\t" \
  "v_cvt_pk_bf16_f32 v130, v4, v5\n\t" \
  "v_mfma_f32_16x16x32_bf16 a[48:51], a[160:163], v[136:139], a[48:51]\n\t" \
  "v_exp_f32 v34, v34\n\t" \
  "v_mfma_f32_16x16x32_bf16 a[48:51], a[164:167], v[140:143], a[48:51]\n\t" \
  "ds_read_b128 a[204:207], v223 offset:16384\n\t" \
  "v_cvt_pk_bf16_f32 v131, v6, v7\n\t" \
  "v_mfma_f32_16x16x32_bf16 a[52:55], a[168:171], v[136:139], a[52:55]\n\t" \
  "v_exp_f32 v35, v35\n\t" \
  "v_mfma_f32_16x16x32_bf16 a[52:55], a[172:175], v[140:143], a[52:55]\n\t" \
  "ds_read_b128 a[208:211], v220 offset:20480\n\t" \
  "v_cvt_pk_bf16_f32 v132, v8, v9\n\t" \
  "v_mfma_f32_16x16x32_bf16 a[56:59], a[176:179], v[136:139], a[56:59]\n\t" \
  "buffer_load_dwordx4 v227, s[60:63], s96 offen offset:3072 lds\n\t" \
  "v_mfma_f32_16x16x32_bf16 a[56:59], a[180:183], v[140:143], a[56:59]\n\t" \
  "v_exp_f32 v36, v36\n\t" \
  "v_mfma_f32_16x16x32_bf16 a[60:63], a[184:187], v[136:139], a[60:63]\n\t" \
  "s_add_u32 m0, s38, 147456\n\t" \
  "ds_read_b128 a[212:215], v221 offset:20480\n\t" \
  "v_cvt_pk_bf16_f32 v133, v10, v11\n\t" \
  "v_mfma_f32_16x16x32_bf16 a[60:63], a[188:191], v[140:143], a[60:63]\n\t" \
  "s_add_u32 s96, s38, 81920\n\t" \
  "v_exp_f32 v37, v37\n\t" \
  "v_smfmac_f32_16x16x64_bf16 v[164:167], v[216:219], v[136:143], v208\n\t" \
  "ds_read_b128 a[216:219], v222 offset:20480\n\t" \
  "v_cvt_pk_bf16_f32 v134, v12, v13\n\t" \
  "v_mfma_f32_16x16x32_bf16 a[64:67], a[128:131], v[144:147], a[64:67]\n\t" \
  "v_exp_f32 v38, v38\n\t" \
  "v_mfma_f32_16x16x32_bf16 a[64:67], a[132:135], v[148:151], a[64:67]\n\t" \
  "buffer_load_dwordx4 v230, s[64:67], s96 offen offset:0 lds\n\t" \
  "v_mfma_f32_16x16x32_bf16 a[68:71], a[136:139], v[144:147], a[68:71]\n\t" \
  "ds_read_b128 a[220:223], v223 offset:20480\n\t" \
  "v_cvt_pk_bf16_f32 v135, v14, v15\n\t" \
  "v_mfma_f32_16x16x32_bf16 a[68:71], a[140:143], v[148:151], a[68:71]\n\t" \
  "v_exp_f32 v39, v39\n\t" \
  "v_mfma_f32_16x16x32_bf16 a[72:75], a[144:147], v[144:147], a[72:75]\n\t" \
  "v_exp_f32 v40, v40\n\t" \
  "v_mfma_f32_16x16x32_bf16 a[72:75], a[148:151], v[148:151], a[72:75]\n\t" \
  "ds_read_b128 a[224:227], v220 offset:24576\n\t" \
  "v_cvt_pk_bf16_f32 v136, v32, v33\n\t" \
  "v_mfma_f32_16x16x32_bf16 a[76:79], a[152:155], v[144:147], a[76:79]\n\t" \
  "ds_read_b128 a[228:231], v221 offset:24576\n\t" \
  "v_cvt_pk_bf16_f32 v137, v34, v35\n\t" \
  "v_mfma_f32_16x16x32_bf16 a[76:79], a[156:159], v[148:151], a[76:79]\n\t" \
  "v_exp_f32 v41, v41\n\t" \
  "v_mfma_f32_16x16x32_bf16 a[80:83], a[160:163], v[144:147], a[80:83]\n\t" \
  "buffer_load_dwordx4 v231, s[64:67], s96 offen offset:1024 lds\n\t" \
  "v_mfma_f32_16x16x32_bf16 a[80:83], a[164:167], v[148:151], a[80:83]\n\t" \
  "v_exp_f32 v42, v42\n\t" \
  "v_mfma_f32_16x16x32_bf16 a[84:87], a[168:171], v[144:147], a[84:87]\n\t" \
  "ds_read_b128 a[232:235], v222 offset:24576\n\t" \
  "v_cvt_pk_bf16_f32 v138, v36, v37\n\t" \
  "v_mfma_f32_16x16x32_bf16 a[84:87], a[172:175], v[148:151], a[84:87]\n\t" \
  "v_exp_f32 v43, v43\n\t" \
  "v_mfma_f32_16x16x32_bf16 a[88:91], a[176:179], v[144:147], a[88:91]\n\t" \
  "ds_read_b128 a[236:239], v223 offset:24576\n\t" \
  "v_cvt_pk_bf16_f32 v139, v38, v39\n\t" \
  "v_mfma_f32_16x16x32_bf16 a[88:91], a[180:183], v[148:151], a[88:91]\n\t" \
  "v_exp_f32 v44, v44\n\t" \
  "v_mfma_f32_16x16x32_bf16 a[92:95], a[184:187], v[144:147], a[92:95]\n\t" \
  "buffer_load_dwordx4 v230, s[64:67], s96 offen offset:2048 lds\n\t" \
  "v_mfma_f32_16x16x32_bf16 a[92:95], a[188:191], v[148:151], a[92:95]\n\t" \
  "ds_read_b128 a[240:243], v220 offset:28672\n\t" \
  "v_cvt_pk_bf16_f32 v140, v40, v41\n\t" \
  "v_smfmac_f32_16x16x64_bf16 v[168:171], v[216:219], v[144:151], v208\n\t" \
  "ds_read_b128 a[244:247], v221 offset:28672\n\t" \
  "v_cvt_pk_bf16_f32 v141, v42, v43\n\t" \
  "v_mfma_f32_16x16x32_bf16 a[96:99], a[128:131], v[152:155], a[96:99]\n\t" \
  "v_exp_f32 v45, v45\n\t" \
  "v_mfma_f32_16x16x32_bf16 a[96:99], a[132:135], v[156:159], a[96:99]\n\t" \
  "v_exp_f32 v46, v46\n\t" \
  "v_mfma_f32_16x16x32_bf16 a[100:103], a[136:139], v[152:155], a[100:103]\n\t" \
  "v_exp_f32 v47, v47\n\t" \
  "v_mfma_f32_16x16x32_bf16 a[100:103], a[140:143], v[156:159], a[100:103]\n\t" \
  "buffer_load_dwordx4 v231, s[64:67], s96 offen offset:3072 lds\n\t" \
  "v_mfma_f32_16x16x32_bf16 a[104:107], a[144:147], v[152:155], a[104:107]\n\t" \
  "ds_read_b128 a[248:251], v222 offset:28672\n\t" \
  "v_cvt_pk_bf16_f32 v142, v44, v45\n\t" \
  "v_mfma_f32_16x16x32_bf16 a[104:107], a[148:151], v[156:159], a[104:107]\n\t" \
  "ds_read_b128 a[252:255], v223 offset:28672\n\t" \
  "v_cvt_pk_bf16_f32 v143, v46, v47\n\t" \
  "v_mfma_f32_16x16x32_bf16 a[108:111], a[152:155], v[152:155], a[108:111]\n\t" \
  "v_exp_f32 v64, v64\n\t" \
  "v_mfma_f32_16x16x32_bf16 a[108:111], a[156:159], v[156:159], a[108:111]\n\t" \
  "v_exp_f32 v65, v65\n\t" \
  "v_mfma_f32_16x16x32_bf16 a[112:115], a[160:163], v[152:155], a[112:115]\n\t" \
  "v_exp_f32 v66, v66\n\t" \
  "v_mfma_f32_16x16x32_bf16 a[112:115], a[164:167], v[156:159], a[112:115]\n\t" \
  "v_exp_f32 v67, v67\n\t" \
  "v_mfma_f32_16x16x32_bf16 a[116:119], a[168:171], v[152:155], a[116:119]\n\t" \
  "v_exp_f32 v68, v68\n\t" \
  "v_mfma_f32_16x16x32_bf16 a[116:119], a[172:175], v[156:159], a[116:119]\n\t" \
  "v_exp_f32 v69, v69\n\t" \
  "v_mfma_f32_16x16x32_bf16 a[120:123], a[176:179], v[152:155], a[120:123]\n\t" \
  "v_exp_f32 v70, v70\n\t" \
  "v_mfma_f32_16x16x32_bf16 a[120:123], a[180:183], v[156:159], a[120:123]\n\t" \
  "v_exp_f32 v71, v71\n\t" \
  "v_mfma_f32_16x16x32_bf16 a[124:127], a[184:187], v[152:155], a[124:127]\n\t" \
  "v_exp_f32 v72, v72\n\t" \
  "v_mfma_f32_16x16x32_bf16 a[124:127], a[188:191], v[156:159], a[124:127]\n\t" \
  "v_exp_f32 v73, v73\n\t" \
  "v_smfmac_f32_16x16x64_bf16 v[172:175], v[216:219], v[152:159], v208\n\t" \
  "s_waitcnt lgkmcnt(12)\n\t" \
  "s_cmp_lg_u32 s99, 0\n\t" \
  "s_cbranch_scc1 .Lsf_p4\n\t" \
  ".Lsf_q4:\n\t" \
  "v_mfma_f32_16x16x32_bf16 v[0:3], a[192:195], v[16:19], v[176:179]\n\t" \
  "ds_read_b64_tr_b16 a[128:129], v228 offset:0\n\t" \
  "v_cvt_pk_bf16_f32 v144, v64, v65\n\t" \
  "v_mfma_f32_16x16x32_bf16 v[0:3], a[196:199], v[20:23], v[0:3]\n\t" \
  "v_exp_f32 v74, v74\n\t" \
  "v_mfma_f32_16x16x32_bf16 v[0:3], a[200:203], v[24:27], v[0:3]\n\t" \
  "ds_read_b64_tr_b16 a[130:131], v228 offset:4096\n\t" \
  "v_cvt_pk_bf16_f32 v145, v66, v67\n\t" \
  "v_mfma_f32_16x16x32_bf16 v[0:3], a[204:207], v[28:31], v[0:3]\n\t" \
  "s_waitcnt lgkmcnt(10)\n\t" \
  "v_exp_f32 v75, v75\n\t" \
  "v_mfma_f32_16x16x32_bf16 v[4:7], a[220:223], v[28:31], v[176:179]\n\t" \
  "ds_read_b64_tr_b16 a[132:133], v228 offset:8192\n\t" \
  "v_cvt_pk_bf16_f32 v146, v68, v69\n\t" \
  "v_mfma_f32_16x16x32_bf16 v[4:7], a[216:219], v[24:27], v[4:7]\n\t" \
  "v_exp_f32 v76, v76\n\t" \
  "v_mfma_f32_16x16x32_bf16 v[4:7], a[212:215], v[20:23], v[4:7]\n\t" \
  "ds_read_b64_tr_b16 a[134:135], v228 offset:12288\n\t" \
  "v_max3_f32 v204, v0, v1, v2\n\t" \
  "v_mfma_f32_16x16x32_bf16 v[4:7], a[208:211], v[16:19], v[4:7]\n\t" \
  "s_waitcnt lgkmcnt(8)\n\t" \
  "ds_read_b64_tr_b16 a[136:137], v229 offset:0\n\t" \
  "v_cvt_pk_bf16_f32 v147, v70, v71\n\t" \
  "v_mfma_f32_16x16x32_bf16 v[8:11], a[224:227], v[16:19], v[176:179]\n\t" \
  "v_exp_f32 v77, v77\n\t" \
  "v_mfma_f32_16x16x32_bf16 v[8:11], a[228:231], v[20:23], v[8:11]\n\t" \
  "ds_read_b64_tr_b16 a[138:139], v229 offset:4096\n\t" \
  "v_cvt_pk_bf16_f32 v148, v72, v73\n\t" \
  "v_mfma_f32_16x16x32_bf16 v[8:11], a[232:235], v[24:27], v[8:11]\n\t" \
  "v_max3_f32 v203, v3, v4, v5\n\t" \
  "v_max3_f32 v204, v204, v6, v7\n\t" \
  "v_mfma_f32_16x16x32_bf16 v[8:11], a[236:239], v[28:31], v[8:11]\n\t" \
  "s_waitcnt lgkmcnt(6)\n\t" \
  "ds_read_b64_tr_b16 a[140:141], v229 offset:8192\n\t" \
  "v_cvt_pk_bf16_f32 v149, v74, v75\n\t" \
  "v_mfma_f32_16x16x32_bf16 v[12:15], a[252:255], v[28:31], v[176:179]\n\t" \
  "v_exp_f32 v78, v78\n\t" \
  "v_mfma_f32_16x16x32_bf16 v[12:15], a[248:251], v[24:27], v[12:15]\n\t" \
  "ds_read_b64_tr_b16 a[142:143], v229 offset:12288\n\t" \
  "v_cvt_pk_bf16_f32 v150, v76, v77\n\t" \
  "v_mfma_f32_16x16x32_bf16 v[12:15], a[244:247], v[20:23], v[12:15]\n\t" \
  "v_max3_f32 v203, v203, v8, v9\n\t" \
  "v_max3_f32 v204, v204, v10, v11\n\t" \
  "v_mfma_f32_16x16x32_bf16 v[12:15], a[240:243], v[16:19], v[12:15]\n\t" \
  "v_exp_f32 v79, v79\n\t" \
  "v_mfma_f32_16x16x32_bf16 v[32:35], a[192:195], v[48:51], v[180:183]\n\t" \
  "ds_read_b64_tr_b16 a[144:145], v228 offset:256\n\t" \
  "v_cvt_pk_bf16_f32 v151, v78, v79\n\t" \
  "v_mfma_f32_16x16x32_bf16 v[32:35], a[196:199], v[52:55], v[32:35]\n\t" \
  "v_exp_f32 v96, v96\n\t" \
  "v_mfma_f32_16x16x32_bf16 v[32:35], a[200:203], v[56:59], v[32:35]\n\t" \
  "ds_read_b64_tr_b16 a[146:147], v228 offset:4352\n\t" \
  "v_max3_f32 v203, v203, v12, v13\n\t" \
  "v_mfma_f32_16x16x32_bf16 v[32:35], a[204:207], v[60:63], v[32:35]\n\t" \
  "v_max3_f32 v204, v204, v14, v15\n\t" \
  "v_max_f32 v204, v204, v203\n\t" \
  "v_mfma_f32_16x16x32_bf16 v[36:39], a[220:223], v[60:63], v[180:183]\n\t" \
  "ds_read_b64_tr_b16 a[148:149], v228 offset:8448\n\t" \
  "v_cmp_lt_f32 vcc, 0x42800000, v204\n\t" \
  "v_mfma_f32_16x16x32_bf16 v[36:39], a[216:219], v[56:59], v[36:39]\n\t" \
  "v_exp_f32 v97, v97\n\t" \
  "v_mfma_f32_16x16x32_bf16 v[36:39], a[212:215], v[52:55], v[36:39]\n\t" \
  "s_cbranch_vccnz .Lsf_c50\n\t" \
  ".Lsf_r50:\n\t" \
  "v_mfma_f32_16x16x32_bf16 v[36:39], a[208:211], v[48:51], v[36:39]\n\t" \
  "ds_read_b64_tr_b16 a[150:151], v228 offset:12544\n\t" \
  "v_max3_f32 v204, v32, v33, v34\n\t" \
  "v_mfma_f32_16x16x32_bf16 v[40:43], a[224:227], v[48:51], v[180:183]\n\t" \
  "ds_read_b64_tr_b16 a[152:153], v229 offset:256\n\t" \
  "v_cvt_pk_bf16_f32 v152, v96, v97\n\t" \
  "v_mfma_f32_16x16x32_bf16 v[40:43], a[228:231], v[52:55], v[40:43]\n\t" \
  "v_exp_f32 v98, v98\n\t" \
  "v_mfma_f32_16x16x32_bf16 v[40:43], a[232:235], v[56:59], v[40:43]\n\t" \
  "v_max3_f32 v203, v35, v36, v37\n\t" \
  "v_max3_f32 v204, v204, v38, v39\n\t" \
  "v_mfma_f32_16x16x32_bf16 v[40:43], a[236:239], v[60:63], v[40:43]\n\t" \
  "v_exp_f32 v99, v99\n\t" \
  "v_mfma_f32_16x16x32_bf16 v[44:47], a[252:255], v[60:63], v[180:183]\n\t" \
  "ds_read_b64_tr_b16 a[154:155], v229 offset:4352\n\t" \
  "v_cvt_pk_bf16_f32 v153, v98, v99\n\t" \
  "v_mfma_f32_16x16x32_bf16 v[44:47], a[248:251], v[56:59], v[44:47]\n\t" \
  "v_exp_f32 v100, v100\n\t" \
  "v_mfma_f32_16x16x32_bf16 v[44:47], a[244:247], v[52:55], v[44:47]\n\t" \
  "ds_read_b64_tr_b16 a[156:157], v229 offset:8448\n\t" \
  "v_max3_f32 v203, v203, v40, v41\n\t" \
  "v_mfma_f32_16x16x32_bf16 v[44:47], a[240:243], v[48:51], v[44:47]\n\t" \
  "ds_read_b64_tr_b16 a[158:159], v229 offset:12544\n\t" \
  "v_max3_f32 v204, v204, v42, v43\n\t" \
  "v_mfma_f32_16x16x32_bf16 v[64:67], a[192:195], v[80:83], v[184:187]\n\t" \
  "v_exp_f32 v101, v101\n\t" \
  "v_mfma_f32_16x16x32_bf16 v[64:67], a[196:199], v[84:87], v[64:67]\n\t" \
  "ds_read_b64_tr_b16 a[160:161], v228 offset:512\n\t" \
  "v_cvt_pk_bf16_f32 v154, v100, v101\n\t" \
  "v_mfma_f32_16x16x32_bf16 v[64:67], a[200:203], v[88:91], v[64:67]\n\t" \
  "v_max3_f32 v203, v203, v44, v45\n\t" \
  "v_max3_f32 v204, v204, v46, v47\n\t" \
  "v_mfma_f32_16x16x32_bf16 v[64:67], a[204:207], v[92:95], v[64:67]\n\t" \
  "v_max_f32 v204, v204, v203\n\t" \
  "v_cmp_lt_f32 vcc, 0x42800000, v204\n\t" \
  "v_mfma_f32_16x16x32_bf16 v[68:71], a[220:223], v[92:95], v[184:187]\n\t" \
  "v_exp_f32 v102, v102\n\t" \
  "v_mfma_f32_16x16x32_bf16 v[68:71], a[216:219], v[88:91], v[68:71]\n\t" \
  "s_cbranch_vccnz .Lsf_c51\n\t" \
  ".Lsf_r51:\n\t" \
  "v_mfma_f32_16x16x32_bf16 v[68:71], a[212:215], v[84:87], v[68:71]\n\t" \
  "ds_read_b64_tr_b16 a[162:163], v228 offset:4608\n\t" \
  "v_max3_f32 v204, v64, v65, v66\n\t" \
  "v_mfma_f32_16x16x32_bf16 v[68:71], a[208:211], v[80:83], v[68:71]\n\t" \
  "ds_read_b64_tr_b16 a[164:165], v228 offset:8704\n\t" \
  "v_exp_f32 v103, v103\n\t" \
  "v_mfma_f32_16x16x32_bf16 v[72:75], a[224:227], v[80:83], v[184:187]\n\t" \
  "ds_read_b64_tr_b16 a[166:167], v228 offset:12800\n\t" \
  "v_exp_f32 v104, v104\n\t" \
  "v_mfma_f32_16x16x32_bf16 v[72:75], a[228:231], v[84:87], v[72:75]\n\t" \
  "ds_read_b64_tr_b16 a[168:169], v229 offset:512\n\t" \
  "v_exp_f32 v105, v105\n\t" \
  "v_mfma_f32_16x16x32_bf16 v[72:75], a[232:235], v[88:91], v[72:75]\n\t" \
  "v_max3_f32 v203, v67, v68, v69\n\t" \
  "v_max3_f32 v204, v204, v70, v71\n\t" \
  "v_mfma_f32_16x16x32_bf16 v[72:75], a[236:239], v[92:95], v[72:75]\n\t" \
  "ds_read_b64_tr_b16 a[170:171], v229 offset:4608\n\t" \
  "v_exp_f32 v106, v106\n\t" \
  "v_mfma_f32_16x16x32_bf16 v[76:79], a[252:255], v[92:95], v[184:187]\n\t" \
  "ds_read_b64_tr_b16 a[172:173], v229 offset:8704\n\t" \
  "v_exp_f32 v107, v107\n\t" \
  "v_mfma_f32_16x16x32_bf16 v[76:79], a[248:251], v[88:91], v[76:79]\n\t" \
  "ds_read_b64_tr_b16 a[174:175], v229 offset:12800\n\t" \
  "v_cvt_pk_bf16_f32 v155, v102, v103\n\t" \
  "v_mfma_f32_16x16x32_bf16 v[76:79], a[244:247], v[84:87], v[76:79]\n\t" \
  "v_max3_f32 v203, v203, v72, v73\n\t" \
  "v_max3_f32 v204, v204, v74, v75\n\t" \
  "v_mfma_f32_16x16x32_bf16 v[76:79], a[240:243], v[80:83], v[76:79]\n\t" \
  "ds_read_b64_tr_b16 a[176:177], v228 offset:768\n\t" \
  "v_exp_f32 v108, v108\n\t" \
  "v_mfma_f32_16x16x32_bf16 v[96:99], a[192:195], v[112:115], v[188:191]\n\t" \
  "ds_read_b64_tr_b16 a[178:179], v228 offset:4864\n\t" \
  "v_exp_f32 v109, v109\n\t" \
  "v_mfma_f32_16x16x32_bf16 v[96:99], a[196:199], v[116:119], v[96:99]\n\t" \
  "ds_read_b64_tr_b16 a[180:181], v228 offset:8960\n\t" \
  "v_cvt_pk_bf16_f32 v156, v104, v105\n\t" \
  "v_mfma_f32_16x16x32_bf16 v[96:99], a[200:203], v[120:123], v[96:99]\n\t" \
  "v_max3_f32 v203, v203, v76, v77\n\t" \
  "v_max3_f32 v204, v204, v78, v79\n\t" \
  "v_mfma_f32_16x16x32_bf16 v[96:99], a[204:207], v[124:127], v[96:99]\n\t" \
  "v_max_f32 v204, v204, v203\n\t" \
  "v_cmp_lt_f32 vcc, 0x42800000, v204\n\t" \
  "v_mfma_f32_16x16x32_bf16 v[100:103], a[220:223], v[124:127], v[188:191]\n\t" \
  "ds_read_b64_tr_b16 a[182:183], v228 offset:13056\n\t" \
  "v_exp_f32 v110, v110\n\t" \
  "v_mfma_f32_16x16x32_bf16 v[100:103], a[216:219], v[120:123], v[100:103]\n\t" \
  "s_cbranch_vccnz .Lsf_c52\n\t" \
  ".Lsf_r52:\n\t" \
  "v_mfma_f32_16x16x32_bf16 v[100:103], a[212:215], v[116:119], v[100:103]\n\t" \
  "v_max3_f32 v204, v96, v97, v98\n\t" \
  "v_exp_f32 v111, v111\n\t" \
  "v_mfma_f32_16x16x32_bf16 v[100:103], a[208:211], v[112:115], v[100:103]\n\t" \
  "ds_read_b64_tr_b16 a[184:185], v229 offset:768\n\t" \
  "v_cvt_pk_bf16_f32 v157, v106, v107\n\t" \
  "v_mfma_f32_16x16x32_bf16 v[104:107], a[224:227], v[112:115], v[188:191]\n\t" \
  "ds_read_b64_tr_b16 a[186:187], v229 offset:4864\n\t" \
  "v_cvt_pk_bf16_f32 v158, v108, v109\n\t" \
  "v_mfma_f32_16x16x32_bf16 v[104:107], a[228:231], v[116:119], v[104:107]\n\t" \
  "ds_read_b64_tr_b16 a[188:189], v229 offset:8960\n\t" \
  "v_cvt_pk_bf16_f32 v159, v110, v111\n\t" \
  "v_mfma_f32_16x16x32_bf16 v[104:107], a[232:235], v[120:123], v[104:107]\n\t" \
  "v_max3_f32 v203, v99, v100, v101\n\t" \
  "v_max3_f32 v204, v204, v102, v103\n\t" \
  "v_mfma_f32_16x16x32_bf16 v[104:107], a[236:239], v[124:127], v[104:107]\n\t" \
  "ds_read_b64_tr_b16 a[190:191], v229 offset:13056\n\t" \
  "s_nop 0\n\t" \
  "v_mfma_f32_16x16x32_bf16 v[108:111], a[252:255], v[124:127], v[188:191]\n\t" \
  "v_exp_f32 v0, v0\n\t" \
  "v_mfma_f32_16x16x32_bf16 v[108:111], a[248:251], v[120:123], v[108:111]\n\t" \
  "v_exp_f32 v1, v1\n\t" \
  "v_mfma_f32_16x16x32_bf16 v[108:111], a[244:247], v[116:119], v[108:111]\n\t" \
  "s_waitcnt vmcnt(0) lgkmcnt(0)\n\t" \
  "v_max3_f32 v203, v203, v104, v105\n\t" \
  "v_max3_f32 v204, v204, v106, v107\n\t" \
  "v_mfma_f32_16x16x32_bf16 v[108:111], a[240:243], v[112:115], v[108:111]\n\t" \
  "s_barrier\n\t" \
  "v_mfma_f32_16x16x32_bf16 a[0:3], a[128:131], v[128:131], a[0:3]\n\t" \
  "v_exp_f32 v2, v2\n\t" \
  "v_mfma_f32_16x16x32_bf16 a[0:3], a[132:135], v[132:135], a[0:3]\n\t" \
  "v_exp_f32 v3, v3\n\t" \
  "v_mfma_f32_16x16x32_bf16 a[4:7], a[136:139], v[128:131], a[4:7]\n\t" \
  "v_exp_f32 v4, v4\n\t" \
  "v_mfma_f32_16x16x32_bf16 a[4:7], a[140:143], v[132:135], a[4:7]\n\t" \
  "v_max3_f32 v203, v203, v108, v109\n\t" \
  "v_max3_f32 v204, v204, v110, v111\n\t" \
  "v_mfma_f32_16x16x32_bf16 a[8:11], a[144:147], v[128:131], a[8:11]\n\t" \
  "v_max_f32 v204, v204, v203\n\t" \
  "v_cmp_lt_f32 vcc, 0x42800000, v204\n\t" \
  "v_mfma_f32_16x16x32_bf16 a[8:11], a[148:151], v[132:135], a[8:11]\n\t" \
  "v_exp_f32 v5, v5\n\t" \
  "v_mfma_f32_16x16x32_bf16 a[12:15], a[152:155], v[128:131], a[12:15]\n\t" \
  "s_cbranch_vccnz .Lsf_c53\n\t" \
  ".Lsf_r53:\n\t" \
  "v_mfma_f32_16x16x32_bf16 a[12:15], a[156:159], v[132:135], a[12:15]\n\t" \
  "s_add_u32 m0, s38, 65536\n\t" \
  "v_exp_f32 v6, v6\n\t" \
  "v_mfma_f32_16x16x32_bf16 a[16:19], a[160:163], v[128:131], a[16:19]\n\t" \
  "s_add_u32 s96, s38, 114688\n\t" \
  "v_exp_f32 v7, v7\n\t" \
  "v_mfma_f32_16x16x32_bf16 a[16:19], a[164:167], v[132:135], a[16:19]\n\t" \
  "v_exp_f32 v8, v8\n\t" \
  "v_mfma_f32_16x16x32_bf16 a[20:23], a[168:171], v[128:131], a[20:23]\n\t" \
  "buffer_load_dwordx4 v224, s[60:63], s96 offen offset:0 lds\n\t" \
  "v_mfma_f32_16x16x32_bf16 a[20:23], a[172:175], v[132:135], a[20:23]\n\t" \
  "v_exp_f32 v9, v9\n\t" \
  "v_mfma_f32_16x16x32_bf16 a[24:27], a[176:179], v[128:131], a[24:27]\n\t" \
  "v_exp_f32 v10, v10\n\t" \
  "v_mfma_f32_16x16x32_bf16 a[24:27], a[180:183], v[132:135], a[24:27]\n\t" \
  "v_exp_f32 v11, v11\n\t" \
  "v_mfma_f32_16x16x32_bf16 a[28:31], a[184:187], v[128:131], a[28:31]\n\t" \
  "v_exp_f32 v12, v12\n\t" \
  "v_mfma_f32_16x16x32_bf16 a[28:31], a[188:191], v[132:135], a[28:31]\n\t" \
  "v_exp_f32 v13, v13\n\t" \
  "v_smfmac_f32_16x16x64_bf16 v[160:163], v[216:219], v[128:135], v208\n\t" \
  "ds_read_b128 a[192:195], v220 offset:0\n\t" \
  "v_cvt_pk_bf16_f32 v128, v0, v1\n\t" \
  "v_mfma_f32_16x16x32_bf16 a[32:35], a[128:131], v[136:139], a[32:35]\n\t" \
  "buffer_load_dwordx4 v225, s[60:63], s96 offen offset:1024 lds\n\t" \
  "v_mfma_f32_16x16x32_bf16 a[32:35], a[132:135], v[140:143], a[32:35]\n\t" \
  "v_exp_f32 v14, v14\n\t" \
  "v_mfma_f32_16x16x32_bf16 a[36:39], a[136:139], v[136:139], a[36:39]\n\t" \
  "ds_read_b128 a[196:199], v221 offset:0\n\t" \
  "v_cvt_pk_bf16_f32 v129, v2, v3\n\t" \
  "v_mfma_f32_16x16x32_bf16 a[36:39], a[140:143], v[140:143], a[36:39]\n\t" \
  "v_exp_f32 v15, v15\n\t" \
  "v_mfma_f32_16x16x32_bf16 a[40:43], a[144:147], v[136:139], a[40:43]\n\t" \
  "v_exp_f32 v32, v32\n\t" \
  "v_mfma_f32_16x16x32_bf16 a[40:43], a[148:151], v[140:143], a[40:43]\n\t" \
  "ds_read_b128 a[200:203], v222 offset:0\n\t" \
  "v_cvt_pk_bf16_f32 v130, v4, v5\n\t" \
  "v_mfma_f32_16x16x32_bf16 a[44:47], a[152:155], v[136:139], a[44:47]\n\t" \
  "buffer_load_dwordx4 v226, s[60:63], s96 offen offset:2048 lds\n\t" \
  "v_mfma_f32_16x16x32_bf16 a[44:47], a[156:159], v[140:143], a[44:47]\n\t" \
  "ds_read_b128 a[204:207], v223 offset:0\n\t" \
  "v_cvt_pk_bf16_f32 v131, v6, v7\n\t" \
  "v_mfma_f32_16x16x32_bf16 a[48:51], a[160:163], v[136:139], a[48:51]\n\t" \
  "v_exp_f32 v33, v33\n\t" \
  "v_mfma_f32_16x16x32_bf16 a[48:51], a[164:167], v[140:143], a[48:51]\n\t" \
  "v_exp_f32 v34, v34\n\t" \
  "v_mfma_f32_16x16x32_bf16 a[52:55], a[168:171], v[136:139], a[52:55]\n\t" \
  "ds_read_b128 a[208:211], v220 offset:4096\n\t" \
  "v_cvt_pk_bf16_f32 v132, v8, v9\n\t" \
  "v_mfma_f32_16x16x32_bf16 a[52:55], a[172:175], v[140:143], a[52:55]\n\t" \
  "v_exp_f32 v35, v35\n\t" \
  "v_mfma_f32_16x16x32_bf16 a[56:59], a[176:179], v[136:139], a[56:59]\n\t" \
  "buffer_load_dwordx4 v227, s[60:63], s96 offen offset:3072 lds\n\t" \
  "v_mfma_f32_16x16x32_bf16 a[56:59], a[180:183], v[140:143], a[56:59]\n\t" \
  "ds_read_b128 a[212:215], v221 offset:4096\n\t" \
  "v_cvt_pk_bf16_f32 v133, v10, v11\n\t" \
  "v_mfma_f32_16x16x32_bf16 a[60:63], a[184:187], v[136:139], a[60:63]\n\t" \
  "s_add_u32 m0, s38, 81920\n\t" \
  "v_exp_f32 v36, v36\n\t" \
  "v_mfma_f32_16x16x32_bf16 a[60:63], a[188:191], v[140:143], a[60:63]\n\t" \
  "s_add_u32 s96, s38, 98304\n\t" \
  "ds_read_b128 a[216:219], v222 offset:4096\n\t" \
  "v_cvt_pk_bf16_f32 v134, v12, v13\n\t" \
  "v_smfmac_f32_16x16x64_bf16 v[164:167], v[216:219], v[136:143], v208\n\t" \
  "ds_read_b128 a[220:223], v223 offset:4096\n\t" \
  "v_cvt_pk_bf16_f32 v135, v14, v15\n\t" \
  "v_mfma_f32_16x16x32_bf16 a[64:67], a[128:131], v[144:147], a[64:67]\n\t" \
  "v_exp_f32 v37, v37\n\t" \
  "v_mfma_f32_16x16x32_bf16 a[64:67], a[132:135], v[148:151], a[64:67]\n\t" \
  "buffer_load_dwordx4 v230, s[64:67], s96 offen offset:0 lds\n\t" \
  "v_mfma_f32_16x16x32_bf16 a[68:71], a[136:139], v[144:147], a[68:71]\n\t" \
  "v_exp_f32 v38, v38\n\t" \
  "v_mfma_f32_16x16x32_bf16 a[68:71], a[140:143], v[148:151], a[68:71]\n\t" \
  "ds_read_b128 a[224:227], v220 offset:8192\n\t" \
  "v_cvt_pk_bf16_f32 v136, v32, v33\n\t" \
  "v_mfma_f32_16x16x32_bf16 a[72:75], a[144:147], v[144:147], a[72:75]\n\t" \
  "v_exp_f32 v39, v39\n\t" \
  "v_mfma_f32_16x16x32_bf16 a[72:75], a[148:151], v[148:151], a[72:75]\n\t" \
  "ds_read_b128 a[228:231], v221 offset:8192\n\t" \
  "v_cvt_pk_bf16_f32 v137, v34, v35\n\t" \
  "v_mfma_f32_16x16x32_bf16 a[76:79], a[152:155], v[144:147], a[76:79]\n\t" \
  "v_exp_f32 v40, v40\n\t" \
  "v_mfma_f32_16x16x32_bf16 a[76:79], a[156:159], v[148:151], a[76:79]\n\t" \
  "v_exp_f32 v41, v41\n\t" \
  "v_mfma_f32_16x16x32_bf16 a[80:83], a[160:163], v[144:147], a[80:83]\n\t" \
  "buffer_load_dwordx4 v231, s[64:67], s96 offen offset:1024 lds\n\t" \
  "v_mfma_f32_16x16x32_bf16 a[80:83], a[164:167], v[148:151], a[80:83]\n\t" \
  "ds_read_b128 a[232:235], v222 offset:8192\n\t" \
  "v_cvt_pk_bf16_f32 v138, v36, v37\n\t" \
  "v_mfma_f32_16x16x32_bf16 a[84:87], a[168:171], v[144:147], a[84:87]\n\t" \
  "v_exp_f32 v42, v42\n\t" \
  "v_mfma_f32_16x16x32_bf16 a[84:87], a[172:175], v[148:151], a[84:87]\n\t" \
  "ds_read_b128 a[236:239], v223 offset:8192\n\t" \
  "v_cvt_pk_bf16_f32 v139, v38, v39\n\t" \
  "v_mfma_f32_16x16x32_bf16 a[88:91], a[176:179], v[144:147], a[88:91]\n\t" \
  "v_exp_f32 v43, v43\n\t" \
  "v_mfma_f32_16x16x32_bf16 a[88:91], a[180:183], v[148:151], a[88:91]\n\t" \
  "ds_read_b128 a[240:243], v220 offset:12288\n\t" \
  "v_cvt_pk_bf16_f32 v140, v40, v41\n\t" \
  "v_mfma_f32_16x16x32_bf16 a[92:95], a[184:187], v[144:147], a[92:95]\n\t" \
  "buffer_load_dwordx4 v230, s[64:67], s96 offen offset:2048 lds\n\t" \
  "v_mfma_f32_16x16x32_bf16 a[92:95], a[188:191], v[148:151], a[92:95]\n\t" \
  "v_exp_f32 v44, v44\n\t" \
  "v_smfmac_f32_16x16x64_bf16 v[168:171], v[216:219], v[144:151], v208\n\t" \
  "ds_read_b128 a[244:247], v221 offset:12288\n\t" \
  "v_cvt_pk_bf16_f32 v141, v42, v43\n\t" \
  "v_mfma_f32_16x16x32_bf16 a[96:99], a[128:131], v[152:155], a[96:99]\n\t" \
  "v_exp_f32 v45, v45\n\t" \
  "v_mfma_f32_16x16x32_bf16 a[96:99], a[132:135], v[156:159], a[96:99]\n\t" \
  "v_exp_f32 v46, v46\n\t" \
  "v_mfma_f32_16x16x32_bf16 a[100:103], a[136:139], v[152:155], a[100:103]\n\t" \
  "ds_read_b128 a[248:251], v222 offset:12288\n\t" \
  "v_cvt_pk_bf16_f32 v142, v44, v45\n\t" \
  "v_mfma_f32_16x16x32_bf16 a[100:103], a[140:143], v[156:159], a[100:103]\n\t" \
  "buffer_load_dwordx4 v231, s[64:67], s96 offen offset:3072 lds\n\t" \
  "v_mfma_f32_16x16x32_bf16 a[104:107], a[144:147], v[152:155], a[104:107]\n\t" \
  "v_exp_f32 v47, v47\n\t" \
  "v_mfma_f32_16x16x32_bf16 a[104:107], a[148:151], v[156:159], a[104:107]\n\t" \
  "ds_read_b128 a[252:255], v223 offset:12288\n\t" \
  "v_cvt_pk_bf16_f32 v143, v46, v47\n\t" \
  "v_mfma_f32_16x16x32_bf16 a[108:111], a[152:155], v[152:155], a[108:111]\n\t" \
  "v_exp_f32 v64, v64\n\t" \
  "v_mfma_f32_16x16x32_bf16 a[108:111], a[156:159], v[156:159], a[108:111]\n\t" \
  "v_exp_f32 v65, v65\n\t" \
  "v_mfma_f32_16x16x32_bf16 a[112:115], a[160:163], v[152:155], a[112:115]\n\t" \
  "v_exp_f32 v66, v66\n\t" \
  "v_mfma_f32_16x16x32_bf16 a[112:115], a[164:167], v[156:159], a[112:115]\n\t" \
  "v_exp_f32 v67, v67\n\t" \
  "v_mfma_f32_16x16x32_bf16 a[116:119], a[168:171], v[152:155], a[116:119]\n\t" \
  "v_exp_f32 v68, v68\n\t" \
  "v_mfma_f32_16x16x32_bf16 a[116:119], a[172:175], v[156:159], a[116:119]\n\t" \
  "v_exp_f32 v69, v69\n\t" \
  "v_mfma_f32_16x16x32_bf16 a[120:123], a[176:179], v[152:155], a[120:123]\n\t" \
  "v_exp_f32 v70, v70\n\t" \
  "v_mfma_f32_16x16x32_bf16 a[120:123], a[180:183], v[156:159], a[120:123]\n\t" \
  "v_exp_f32 v71, v71\n\t" \
  "v_mfma_f32_16x16x32_bf16 a[124:127], a[184:187], v[152:155], a[124:127]\n\t" \
  "v_exp_f32 v72, v72\n\t" \
  "v_mfma_f32_16x16x32_bf16 a[124:127], a[188:191], v[156:159], a[124:127]\n\t" \
  "v_exp_f32 v73, v73\n\t" \
  "v_smfmac_f32_16x16x64_bf16 v[172:175], v[216:219], v[152:159], v208\n\t" \
  "s_waitcnt lgkmcnt(12)\n\t" \
  "s_cmp_lg_u32 s99, 0\n\t" \
  "s_cbranch_scc1 .Lsf_p5\n\t" \
  ".Lsf_q5:\n\t" \
  "v_mfma_f32_16x16x32_bf16 v[0:3], a[192:195], v[16:19], v[176:179]\n\t" \
  "ds_read_b64_tr_b16 a[128:129], v210 offset:0\n\t" \
  "v_cvt_pk_bf16_f32 v144, v64, v65\n\t" \
  "v_mfma_f32_16x16x32_bf16 v[0:3], a[196:199], v[20:23], v[0:3]\n\t" \
  "v_exp_f32 v74, v74\n\t" \
  "v_mfma_f32_16x16x32_bf16 v[0:3], a[200:203], v[24:27], v[0:3]\n\t" \
  "ds_read_b64_tr_b16 a[130:131], v210 offset:4096\n\t" \
  "v_cvt_pk_bf16_f32 v145, v66, v67\n\t" \
  "v_mfma_f32_16x16x32_bf16 v[0:3], a[204:207], v[28:31], v[0:3]\n\t" \
  "s_waitcnt lgkmcnt(10)\n\t" \
  "v_exp_f32 v75, v75\n\t" \
  "v_mfma_f32_16x16x32_bf16 v[4:7], a[220:223], v[28:31], v[176:179]\n\t" \
  "ds_read_b64_tr_b16 a[132:133], v210 offset:8192\n\t" \
  "v_cvt_pk_bf16_f32 v146, v68, v69\n\t" \
  "v_mfma_f32_16x16x32_bf16 v[4:7], a[216:219], v[24:27], v[4:7]\n\t" \
  "v_exp_f32 v76, v76\n\t" \
  "v_mfma_f32_16x16x32_bf16 v[4:7], a[212:215], v[20:23], v[4:7]\n\t" \
  "ds_read_b64_tr_b16 a[134:135], v210 offset:12288\n\t" \
  "v_max3_f32 v204, v0, v1, v2\n\t" \
  "v_mfma_f32_16x16x32_bf16 v[4:7], a[208:211], v[16:19], v[4:7]\n\t" \
  "s_waitcnt lgkmcnt(8)\n\t" \
  "ds_read_b64_tr_b16 a[136:137], v211 offset:0\n\t" \
  "v_cvt_pk_bf16_f32 v147, v70, v71\n\t" \
  "v_mfma_f32_16x16x32_bf16 v[8:11], a[224:227], v[16:19], v[176:179]\n\t" \
  "v_exp_f32 v77, v77\n\t" \
  "v_mfma_f32_16x16x32_bf16 v[8:11], a[228:231], v[20:23], v[8:11]\n\t" \
  "ds_read_b64_tr_b16 a[138:139], v211 offset:4096\n\t" \
  "v_cvt_pk_bf16_f32 v148, v72, v73\n\t" \
  "v_mfma_f32_16x16x32_bf16 v[8:11], a[232:235], v[24:27], v[8:11]\n\t" \
  "v_max3_f32 v203, v3, v4, v5\n\t" \
  "v_max3_f32 v204, v204, v6, v7\n\t" \
  "v_mfma_f32_16x16x32_bf16 v[8:11], a[236:239], v[28:31], v[8:11]\n\t" \
  "s_waitcnt lgkmcnt(6)\n\t" \
  "ds_read_b64_tr_b16 a[140:141], v211 offset:8192\n\t" \
  "v_cvt_pk_bf16_f32 v149, v74, v75\n\t" \
  "v_mfma_f32_16x16x32_bf16 v[12:15], a[252:255], v[28:31], v[176:179]\n\t" \
  "v_exp_f32 v78, v78\n\t" \
  "v_mfma_f32_16x16x32_bf16 v[12:15], a[248:251], v[24:27], v[12:15]\n\t" \
  "ds_read_b64_tr_b16 a[142:143], v211 offset:12288\n\t" \
  "v_cvt_pk_bf16_f32 v150, v76, v77\n\t" \
  "v_mfma_f32_16x16x32_bf16 v[12:15], a[244:247], v[20:23], v[12:15]\n\t" \
  "v_max3_f32 v203, v203, v8, v9\n\t" \
  "v_max3_f32 v204, v204, v10, v11\n\t" \
  "v_mfma_f32_16x16x32_bf16 v[12:15], a[240:243], v[16:19], v[12:15]\n\t" \
  "v_exp_f32 v79, v79\n\t" \
  "v_mfma_f32_16x16x32_bf16 v[32:35], a[192:195], v[48:51], v[180:183]\n\t" \
  "ds_read_b64_tr_b16 a[144:145], v210 offset:256\n\t" \
  "v_cvt_pk_bf16_f32 v151, v78, v79\n\t" \
  "v_mfma_f32_16x16x32_bf16 v[32:35], a[196:199], v[52:55], v[32:35]\n\t" \
  "v_exp_f32 v96, v96\n\t" \
  "v_mfma_f32_16x16x32_bf16 v[32:35], a[200:203], v[56:59], v[32:35]\n\t" \
  "ds_read_b64_tr_b16 a[146:147], v210 offset:4352\n\t" \
  "v_max3_f32 v203, v203, v12, v13\n\t" \
  "v_mfma_f32_16x16x32_bf16 v[32:35], a[204:207], v[60:63], v[32:35]\n\t" \
  "v_max3_f32 v204, v204, v14, v15\n\t" \
  "v_max_f32 v204, v204, v203\n\t" \
  "v_mfma_f32_16x16x32_bf16 v[36:39], a[220:223], v[60:63], v[180:183]\n\t" \
  "ds_read_b64_tr_b16 a[148:149], v210 offset:8448\n\t" \
  "v_cmp_lt_f32 vcc, 0x42800000, v204\n\t" \
  "v_mfma_f32_16x16x32_bf16 v[36:39], a[216:219], v[56:59], v[36:39]\n\t" \
  "v_exp_f32 v97, v97\n\t" \
  "v_mfma_f32_16x16x32_bf16 v[36:39], a[212:215], v[52:55], v[36:39]\n\t" \
  "s_cbranch_vccnz .Lsf_c60\n\t" \
  ".Lsf_r60:\n\t" \
  "v_mfma_f32_16x16x32_bf16 v[36:39], a[208:211], v[48:51], v[36:39]\n\t" \
  "ds_read_b64_tr_b16 a[150:151], v210 offset:12544\n\t" \
  "v_max3_f32 v204, v32, v33, v34\n\t" \
  "v_mfma_f32_16x16x32_bf16 v[40:43], a[224:227], v[48:51], v[180:183]\n\t" \
  "ds_read_b64_tr_b16 a[152:153], v211 offset:256\n\t" \
  "v_cvt_pk_bf16_f32 v152, v96, v97\n\t" \
  "v_mfma_f32_16x16x32_bf16 v[40:43], a[228:231], v[52:55], v[40:43]\n\t" \
  "v_exp_f32 v98, v98\n\t" \
  "v_mfma_f32_16x16x32_bf16 v[40:43], a[232:235], v[56:59], v[40:43]\n\t" \
  "v_max3_f32 v203, v35, v36, v37\n\t" \
  "v_max3_f32 v204, v204, v38, v39\n\t" \
  "v_mfma_f32_16x16x32_bf16 v[40:43], a[236:239], v[60:63], v[40:43]\n\t" \
  "v_exp_f32 v99, v99\n\t" \
  "v_mfma_f32_16x16x32_bf16 v[44:47], a[252:255], v[60:63], v[180:183]\n\t" \
  "ds_read_b64_tr_b16 a[154:155], v211 offset:4352\n\t" \
  "v_cvt_pk_bf16_f32 v153, v98, v99\n\t" \
  "v_mfma_f32_16x16x32_bf16 v[44:47], a[248:251], v[56:59], v[44:47]\n\t" \
  "v_exp_f32 v100, v100\n\t" \
  "v_mfma_f32_16x16x32_bf16 v[44:47], a[244:247], v[52:55], v[44:47]\n\t" \
  "ds_read_b64_tr_b16 a[156:157], v211 offset:8448\n\t" \
  "v_max3_f32 v203, v203, v40, v41\n\t" \
  "v_mfma_f32_16x16x32_bf16 v[44:47], a[240:243], v[48:51], v[44:47]\n\t" \
  "ds_read_b64_tr_b16 a[158:159], v211 offset:12544\n\t" \
  "v_max3_f32 v204, v204, v42, v43\n\t" \
  "v_mfma_f32_16x16x32_bf16 v[64:67], a[192:195], v[80:83], v[184:187]\n\t" \
  "v_exp_f32 v101, v101\n\t" \
  "v_mfma_f32_16x16x32_bf16 v[64:67], a[196:199], v[84:87], v[64:67]\n\t" \
  "ds_read_b64_tr_b16 a[160:161], v210 offset:512\n\t" \
  "v_cvt_pk_bf16_f32 v154, v100, v101\n\t" \
  "v_mfma_f32_16x16x32_bf16 v[64:67], a[200:203], v[88:91], v[64:67]\n\t" \
  "v_max3_f32 v203, v203, v44, v45\n\t" \
  "v_max3_f32 v204, v204, v46, v47\n\t" \
  "v_mfma_f32_16x16x32_bf16 v[64:67], a[204:207], v[92:95], v[64:67]\n\t" \
  "v_max_f32 v204, v204, v203\n\t" \
  "v_cmp_lt_f32 vcc, 0x42800000, v204\n\t" \
  "v_mfma_f32_16x16x32_bf16 v[68:71], a[220:223], v[92:95], v[184:187]\n\t" \
  "v_exp_f32 v102, v102\n\t" \
  "v_mfma_f32_16x16x32_bf16 v[68:71], a[216:219], v[88:91], v[68:71]\n\t" \
  "s_cbranch_vccnz .Lsf_c61\n\t" \
  ".Lsf_r61:\n\t" \
  "v_mfma_f32_16x16x32_bf16 v[68:71], a[212:215], v[84:87], v[68:71]\n\t" \
  "ds_read_b64_tr_b16 a[162:163], v210 offset:4608\n\t" \
  "v_max3_f32 v204, v64, v65, v66\n\t" \
  "v_mfma_f32_16x16x32_bf16 v[68:71], a[208:211], v[80:83], v[68:71]\n\t" \
  "ds_read_b64_tr_b16 a[164:165], v210 offset:8704\n\t" \
  "v_exp_f32 v103, v103\n\t" \
  "v_mfma_f32_16x16x32_bf16 v[72:75], a[224:227], v[80:83], v[184:187]\n\t" \
  "ds_read_b64_tr_b16 a[166:167], v210 offset:12800\n\t" \
  "v_exp_f32 v104, v104\n\t" \
  "v_mfma_f32_16x16x32_bf16 v[72:75], a[228:231], v[84:87], v[72:75]\n\t" \
  "ds_read_b64_tr_b16 a[168:169], v211 offset:512\n\t" \
  "v_exp_f32 v105, v105\n\t" \
  "v_mfma_f32_16x16x32_bf16 v[72:75], a[232:235], v[88:91], v[72:75]\n\t" \
  "v_max3_f32 v203, v67, v68, v69\n\t" \
  "v_max3_f32 v204, v204, v70, v71\n\t" \
  "v_mfma_f32_16x16x32_bf16 v[72:75], a[236:239], v[92:95], v[72:75]\n\t" \
  "ds_read_b64_tr_b16 a[170:171], v211 offset:4608\n\t" \
  "v_exp_f32 v106, v106\n\t" \
  "v_mfma_f32_16x16x32_bf16 v[76:79], a[252:255], v[92:95], v[184:187]\n\t" \
  "ds_read_b64_tr_b16 a[172:173], v211 offset:8704\n\t" \
  "v_exp_f32 v107, v107\n\t" \
  "v_mfma_f32_16x16x32_bf16 v[76:79], a[248:251], v[88:91], v[76:79]\n\t" \
  "ds_read_b64_tr_b16 a[174:175], v211 offset:12800\n\t" \
  "v_cvt_pk_bf16_f32 v155, v102, v103\n\t" \
  "v_mfma_f32_16x16x32_bf16 v[76:79], a[244:247], v[84:87], v[76:79]\n\t" \
  "v_max3_f32 v203, v203, v72, v73\n\t" \
  "v_max3_f32 v204, v204, v74, v75\n\t" \
  "v_mfma_f32_16x16x32_bf16 v[76:79], a[240:243], v[80:83], v[76:79]\n\t" \
  "ds_read_b64_tr_b16 a[176:177], v210 offset:768\n\t" \
  "v_exp_f32 v108, v108\n\t" \
  "v_mfma_f32_16x16x32_bf16 v[96:99], a[192:195], v[112:115], v[188:191]\n\t" \
  "ds_read_b64_tr_b16 a[178:179], v210 offset:4864\n\t" \
  "v_exp_f32 v109, v109\n\t" \
  "v_mfma_f32_16x16x32_bf16 v[96:99], a[196:199], v[116:119], v[96:99]\n\t" \
  "ds_read_b64_tr_b16 a[180:181], v210 offset:8960\n\t" \
  "v_cvt_pk_bf16_f32 v156, v104, v105\n\t" \
  "v_mfma_f32_16x16x32_bf16 v[96:99], a[200:203], v[120:123], v[96:99]\n\t" \
  "v_max3_f32 v203, v203, v76, v77\n\t" \
  "v_max3_f32 v204, v204, v78, v79\n\t" \
  "v_mfma_f32_16x16x32_bf16 v[96:99], a[204:207], v[124:127], v[96:99]\n\t" \
  "v_max_f32 v204, v204, v203\n\t" \
  "v_cmp_lt_f32 vcc, 0x42800000, v204\n\t" \
  "v_mfma_f32_16x16x32_bf16 v[100:103], a[220:223], v[124:127], v[188:191]\n\t" \
  "ds_read_b64_tr_b16 a[182:183], v210 offset:13056\n\t" \
  "v_exp_f32 v110, v110\n\t" \
  "v_mfma_f32_16x16x32_bf16 v[100:103], a[216:219], v[120:123], v[100:103]\n\t" \
  "s_cbranch_vccnz .Lsf_c62\n\t" \
  ".Lsf_r62:\n\t" \
  "v_mfma_f32_16x16x32_bf16 v[100:103], a[212:215], v[116:119], v[100:103]\n\t" \
  "v_max3_f32 v204, v96, v97, v98\n\t" \
  "v_exp_f32 v111, v111\n\t" \
  "v_mfma_f32_16x16x32_bf16 v[100:103], a[208:211], v[112:115], v[100:103]\n\t" \
  "ds_read_b64_tr_b16 a[184:185], v211 offset:768\n\t" \
  "v_cvt_pk_bf16_f32 v157, v106, v107\n\t" \
  "v_mfma_f32_16x16x32_bf16 v[104:107], a[224:227], v[112:115], v[188:191]\n\t" \
  "ds_read_b64_tr_b16 a[186:187], v211 offset:4864\n\t" \
  "v_cvt_pk_bf16_f32 v158, v108, v109\n\t" \
  "v_mfma_f32_16x16x32_bf16 v[104:107], a[228:231], v[116:119], v[104:107]\n\t" \
  "ds_read_b64_tr_b16 a[188:189], v211 offset:8960\n\t" \
  "v_cvt_pk_bf16_f32 v159, v110, v111\n\t" \
  "v_mfma_f32_16x16x32_bf16 v[104:107], a[232:235], v[120:123], v[104:107]\n\t" \
  "v_max3_f32 v203, v99, v100, v101\n\t" \
  "v_max3_f32 v204, v204, v102, v103\n\t" \
  "v_mfma_f32_16x16x32_bf16 v[104:107], a[236:239], v[124:127], v[104:107]\n\t" \
  "ds_read_b64_tr_b16 a[190:191], v211 offset:13056\n\t" \
  "s_nop 0\n\t" \
  "v_mfma_f32_16x16x32_bf16 v[108:111], a[252:255], v[124:127], v[188:191]\n\t" \
  "v_exp_f32 v0, v0\n\t" \
  "v_mfma_f32_16x16x32_bf16 v[108:111], a[248:251], v[120:123], v[108:111]\n\t" \
  "v_exp_f32 v1, v1\n\t" \
  "v_mfma_f32_16x16x32_bf16 v[108:111], a[244:247], v[116:119], v[108:111]\n\t" \
  "s_waitcnt vmcnt(0) lgkmcnt(0)\n\t" \
  "v_max3_f32 v203, v203, v104, v105\n\t" \
  "v_max3_f32 v204, v204, v106, v107\n\t" \
  "v_mfma_f32_16x16x32_bf16 v[108:111], a[240:243], v[112:115], v[108:111]\n\t" \
  "s_barrier\n\t" \
  "v_mfma_f32_16x16x32_bf16 a[0:3], a[128:131], v[128:131], a[0:3]\n\t" \
  "v_exp_f32 v2, v2\n\t" \
  "v_mfma_f32_16x16x32_bf16 a[0:3], a[132:135], v[132:135], a[0:3]\n\t" \
  "v_exp_f32 v3, v3\n\t" \
  "v_mfma_f32_16x16x32_bf16 a[4:7], a[136:139], v[128:131], a[4:7]\n\t" \
  "v_exp_f32 v4, v4\n\t" \
  "v_mfma_f32_16x16x32_bf16 a[4:7], a[140:143], v[132:135], a[4:7]\n\t" \
  "v_max3_f32 v203, v203, v108, v109\n\t" \
  "v_max3_f32 v204, v204, v110, v111\n\t" \
  "v_mfma_f32_16x16x32_bf16 a[8:11], a[144:147], v[128:131], a[8:11]\n\t" \
  "v_max_f32 v204, v204, v203\n\t" \
  "v_cmp_lt_f32 vcc, 0x42800000, v204\n\t" \
  "v_mfma_f32_16x16x32_bf16 a[8:11], a[148:151], v[132:135], a[8:11]\n\t" \
  "v_exp_f32 v5, v5\n\t" \
  "v_mfma_f32_16x16x32_bf16 a[12:15], a[152:155], v[128:131], a[12:15]\n\t" \
  "s_cbranch_vccnz .Lsf_c63\n\t" \
  ".Lsf_r63:\n\t" \
  "v_mfma_f32_16x16x32_bf16 a[12:15], a[156:159], v[132:135], a[12:15]\n\t" \
  "s_add_u32 m0, s38, 147456\n\t" \
  "v_exp_f32 v6, v6\n\t" \
  "v_mfma_f32_16x16x32_bf16 a[16:19], a[160:163], v[128:131], a[16:19]\n\t" \
  "s_add_u32 s96, s38, 114688\n\t" \
  "v_exp_f32 v7, v7\n\t" \
  "v_mfma_f32_16x16x32_bf16 a[16:19], a[164:167], v[132:135], a[16:19]\n\t" \
  "v_exp_f32 v8, v8\n\t" \
  "v_mfma_f32_16x16x32_bf16 a[20:23], a[168:171], v[128:131], a[20:23]\n\t" \
  "buffer_load_dwordx4 v230, s[64:67], s96 offen offset:0 lds\n\t" \
  "v_mfma_f32_16x16x32_bf16 a[20:23], a[172:175], v[132:135], a[20:23]\n\t" \
  "v_exp_f32 v9, v9\n\t" \
  "v_mfma_f32_16x16x32_bf16 a[24:27], a[176:179], v[128:131], a[24:27]\n\t" \
  "v_exp_f32 v10, v10\n\t" \
  "v_mfma_f32_16x16x32_bf16 a[24:27], a[180:183], v[132:135], a[24:27]\n\t" \
  "v_exp_f32 v11, v11\n\t" \
  "v_mfma_f32_16x16x32_bf16 a[28:31], a[184:187], v[128:131], a[28:31]\n\t" \
  "v_exp_f32 v12, v12\n\t" \
  "v_mfma_f32_16x16x32_bf16 a[28:31], a[188:191], v[132:135], a[28:31]\n\t" \
  "v_exp_f32 v13, v13\n\t" \
  "v_smfmac_f32_16x16x64_bf16 v[160:163], v[216:219], v[128:135], v208\n\t" \
  "ds_read_b128 a[192:195], v220 offset:16384\n\t" \
  "v_cvt_pk_bf16_f32 v128, v0, v1\n\t" \
  "v_mfma_f32_16x16x32_bf16 a[32:35], a[128:131], v[136:139], a[32:35]\n\t" \
  "v_exp_f32 v14, v14\n\t" \
  "v_mfma_f32_16x16x32_bf16 a[32:35], a[132:135], v[140:143], a[32:35]\n\t" \
  "v_exp_f32 v15, v15\n\t" \
  "v_mfma_f32_16x16x32_bf16 a[36:39], a[136:139], v[136:139], a[36:39]\n\t" \
  "ds_read_b128 a[196:199], v221 offset:16384\n\t" \
  "v_cvt_pk_bf16_f32 v129, v2, v3\n\t" \
  "v_mfma_f32_16x16x32_bf16 a[36:39], a[140:143], v[140:143], a[36:39]\n\t" \
  "v_exp_f32 v32, v32\n\t" \
  "v_mfma_f32_16x16x32_bf16 a[40:43], a[144:147], v[136:139], a[40:43]\n\t" \
  "ds_read_b128 a[200:203], v222 offset:16384\n\t" \
  "v_cvt_pk_bf16_f32 v130, v4, v5\n\t" \
  "v_mfma_f32_16x16x32_bf16 a[40:43], a[148:151], v[140:143], a[40:43]\n\t" \
  "v_exp_f32 v33, v33\n\t" \
  "v_mfma_f32_16x16x32_bf16 a[44:47], a[152:155], v[136:139], a[44:47]\n\t" \
  "buffer_load_dwordx4 v231, s[64:67], s96 offen offset:1024 lds\n\t" \
  "v_mfma_f32_16x16x32_bf16 a[44:47], a[156:159], v[140:143], a[44:47]\n\t" \
  "ds_read_b128 a[204:207], v223 offset:16384\n\t" \
  "v_cvt_pk_bf16_f32 v131, v6, v7\n\t" \
  "v_mfma_f32_16x16x32_bf16 a[48:51], a[160:163], v[136:139], a[48:51]\n\t" \
  "v_exp_f32 v34, v34\n\t" \
  "v_mfma_f32_16x16x32_bf16 a[48:51], a[164:167], v[140:143], a[48:51]\n\t" \
  "v_exp_f32 v35, v35\n\t" \
  "v_mfma_f32_16x16x32_bf16 a[52:55], a[168:171], v[136:139], a[52:55]\n\t" \
  "ds_read_b128 a[208:211], v220 offset:20480\n\t" \
  "v_cvt_pk_bf16_f32 v132, v8, v9\n\t" \
  "v_mfma_f32_16x16x32_bf16 a[52:55], a[172:175], v[140:143], a[52:55]\n\t" \
  "v_exp_f32 v36, v36\n\t" \
  "v_mfma_f32_16x16x32_bf16 a[56:59], a[176:179], v[136:139], a[56:59]\n\t" \
  "ds_read_b128 a[212:215], v221 offset:20480\n\t" \
  "v_cvt_pk_bf16_f32 v133, v10, v11\n\t" \
  "v_mfma_f32_16x16x32_bf16 a[56:59], a[180:183], v[140:143], a[56:59]\n\t" \
  "v_exp_f32 v37, v37\n\t" \
  "v_mfma_f32_16x16x32_bf16 a[60:63], a[184:187], v[136:139], a[60:63]\n\t" \
  "ds_read_b128 a[216:219], v222 offset:20480\n\t" \
  "v_cvt_pk_bf16_f32 v134, v12, v13\n\t" \
  "v_mfma_f32_16x16x32_bf16 a[60:63], a[188:191], v[140:143], a[60:63]\n\t" \
  "v_exp_f32 v38, v38\n\t" \
  "v_smfmac_f32_16x16x64_bf16 v[164:167], v[216:219], v[136:143], v208\n\t" \
  "ds_read_b128 a[220:223], v223 offset:20480\n\t" \
  "v_cvt_pk_bf16_f32 v135, v14, v15\n\t" \
  "v_mfma_f32_16x16x32_bf16 a[64:67], a[128:131], v[144:147], a[64:67]\n\t" \
  "v_exp_f32 v39, v39\n\t" \
  "v_mfma_f32_16x16x32_bf16 a[64:67], a[132:135], v[148:151], a[64:67]\n\t" \
  "buffer_load_dwordx4 v230, s[64:67], s96 offen offset:2048 lds\n\t" \
  "v_mfma_f32_16x16x32_bf16 a[68:71], a[136:139], v[144:147], a[68:71]\n\t" \
  "ds_read_b128 a[224:227], v220 offset:24576\n\t" \
  "v_cvt_pk_bf16_f32 v136, v32, v33\n\t" \
  "v_mfma_f32_16x16x32_bf16 a[68:71], a[140:143], v[148:151], a[68:71]\n\t" \
  "v_exp_f32 v40, v40\n\t" \
  "v_mfma_f32_16x16x32_bf16 a[72:75], a[144:147], v[144:147], a[72:75]\n\t" \
  "v_exp_f32 v41, v41\n\t" \
  "v_mfma_f32_16x16x32_bf16 a[72:75], a[148:151], v[148:151], a[72:75]\n\t" \
  "ds_read_b128 a[228:231], v221 offset:24576\n\t" \
  "v_cvt_pk_bf16_f32 v137, v34, v35\n\t" \
  "v_mfma_f32_16x16x32_bf16 a[76:79], a[152:155], v[144:147], a[76:79]\n\t" \
  "v_exp_f32 v42, v42\n\t" \
  "v_mfma_f32_16x16x32_bf16 a[76:79], a[156:159], v[148:151], a[76:79]\n\t" \
  "v_exp_f32 v43, v43\n\t" \
  "v_mfma_f32_16x16x32_bf16 a[80:83], a[160:163], v[144:147], a[80:83]\n\t" \
  "ds_read_b128 a[232:235], v222 offset:24576\n\t" \
  "v_cvt_pk_bf16_f32 v138, v36, v37\n\t" \
  "v_mfma_f32_16x16x32_bf16 a[80:83], a[164:167], v[148:151], a[80:83]\n\t" \
  "v_exp_f32 v44, v44\n\t" \
  "v_mfma_f32_16x16x32_bf16 a[84:87], a[168:171], v[144:147], a[84:87]\n\t" \
  "v_exp_f32 v45, v45\n\t" \
  "v_mfma_f32_16x16x32_bf16 a[84:87], a[172:175], v[148:151], a[84:87]\n\t" \
  "ds_read_b128 a[236:239], v223 offset:24576\n\t" \
  "v_cvt_pk_bf16_f32 v139, v38, v39\n\t" \
  "v_mfma_f32_16x16x32_bf16 a[88:91], a[176:179], v[144:147], a[88:91]\n\t" \
  "v_exp_f32 v46, v46\n\t" \
  "v_mfma_f32_16x16x32_bf16 a[88:91], a[180:183], v[148:151], a[88:91]\n\t" \
  "ds_read_b128 a[240:243], v220 offset:28672\n\t" \
  "v_cvt_pk_bf16_f32 v140, v40, v41\n\t" \
  "v_mfma_f32_16x16x32_bf16 a[92:95], a[184:187], v[144:147], a[92:95]\n\t" \
  "buffer_load_dwordx4 v231, s[64:67], s96 offen offset:3072 lds\n\t" \
  "v_mfma_f32_16x16x32_bf16 a[92:95], a[188:191], v[148:151], a[92:95]\n\t" \
  "v_exp_f32 v47, v47\n\t" \
  "v_smfmac_f32_16x16x64_bf16 v[168:171], v[216:219], v[144:151], v208\n\t" \
  "ds_read_b128 a[244:247], v221 offset:28672\n\t" \
  "v_cvt_pk_bf16_f32 v141, v42, v43\n\t" \
  "v_mfma_f32_16x16x32_bf16 a[96:99], a[128:131], v[152:155], a[96:99]\n\t" \
  "v_exp_f32 v64, v64\n\t" \
  "v_mfma_f32_16x16x32_bf16 a[96:99], a[132:135], v[156:159], a[96:99]\n\t" \
  "v_exp_f32 v65, v65\n\t" \
  "v_mfma_f32_16x16x32_bf16 a[100:103], a[136:139], v[152:155], a[100:103]\n\t" \
  "ds_read_b128 a[248:251], v222 offset:28672\n\t" \
  "v_cvt_pk_bf16_f32 v142, v44, v45\n\t" \
  "v_mfma_f32_16x16x32_bf16 a[100:103], a[140:143], v[156:159], a[100:103]\n\t" \
  "v_exp_f32 v66, v66\n\t" \
  "v_mfma_f32_16x16x32_bf16 a[104:107], a[144:147], v[152:155], a[104:107]\n\t" \
  "v_exp_f32 v67, v67\n\t" \
  "v_mfma_f32_16x16x32_bf16 a[104:107], a[148:151], v[156:159], a[104:107]\n\t" \
  "ds_read_b128 a[252:255], v223 offset:28672\n\t" \
  "v_cvt_pk_bf16_f32 v143, v46, v47\n\t" \
  "v_mfma_f32_16x16x32_bf16 a[108:111], a[152:155], v[152:155], a[108:111]\n\t" \
  "v_exp_f32 v68, v68\n\t" \
  "v_mfma_f32_16x16x32_bf16 a[108:111], a[156:159], v[156:159], a[108:111]\n\t" \
  "v_exp_f32 v69, v69\n\t" \
  "v_mfma_f32_16x16x32_bf16 a[112:115], a[160:163], v[152:155], a[112:115]\n\t" \
  "v_exp_f32 v70, v70\n\t" \
  "v_mfma_f32_16x16x32_bf16 a[112:115], a[164:167], v[156:159], a[112:115]\n\t" \
  "v_exp_f32 v71, v71\n\t" \
  "v_mfma_f32_16x16x32_bf16 a[116:119], a[168:171], v[152:155], a[116:119]\n\t" \
  "v_exp_f32 v72, v72\n\t" \
  "v_mfma_f32_16x16x32_bf16 a[116:119], a[172:175], v[156:159], a[116:119]\n\t" \
  "v_exp_f32 v73, v73\n\t" \
  "v_mfma_f32_16x16x32_bf16 a[120:123], a[176:179], v[152:155], a[120:123]\n\t" \
  "v_exp_f32 v74, v74\n\t" \
  "v_mfma_f32_16x16x32_bf16 a[120:123], a[180:183], v[156:159], a[120:123]\n\t" \
  "v_exp_f32 v75, v75\n\t" \
  "v_mfma_f32_16x16x32_bf16 a[124:127], a[184:187], v[152:155], a[124:127]\n\t" \
  "v_exp_f32 v76, v76\n\t" \
  "v_mfma_f32_16x16x32_bf16 a[124:127], a[188:191], v[156:159], a[124:127]\n\t" \
  "v_exp_f32 v77, v77\n\t" \
  "v_smfmac_f32_16x16x64_bf16 v[172:175], v[216:219], v[152:159], v208\n\t" \
  "s_waitcnt lgkmcnt(12)\n\t" \
  "s_cmp_lg_u32 s99, 0\n\t" \
  "s_cbranch_scc1 .Lsf_p6\n\t" \
  ".Lsf_q6:\n\t" \
  "v_mfma_f32_16x16x32_bf16 v[0:3], a[192:195], v[16:19], v[176:179]\n\t" \
  "ds_read_b64_tr_b16 a[128:129], v228 offset:0\n\t" \
  "v_cvt_pk_bf16_f32 v144, v64, v65\n\t" \
  "v_mfma_f32_16x16x32_bf16 v[0:3], a[196:199], v[20:23], v[0:3]\n\t" \
  "v_exp_f32 v78, v78\n\t" \
  "v_mfma_f32_16x16x32_bf16 v[0:3], a[200:203], v[24:27], v[0:3]\n\t" \
  "ds_read_b64_tr_b16 a[130:131], v228 offset:4096\n\t" \
  "v_cvt_pk_bf16_f32 v145, v66, v67\n\t" \
  "v_mfma_f32_16x16x32_bf16 v[0:3], a[204:207], v[28:31], v[0:3]\n\t" \
  "s_waitcnt lgkmcnt(10)\n\t" \
  "v_exp_f32 v79, v79\n\t" \
  "v_mfma_f32_16x16x32_bf16 v[4:7], a[220:223], v[28:31], v[176:179]\n\t" \
  "ds_read_b64_tr_b16 a[132:133], v228 offset:8192\n\t" \
  "v_cvt_pk_bf16_f32 v146, v68, v69\n\t" \
  "v_mfma_f32_16x16x32_bf16 v[4:7], a[216:219], v[24:27], v[4:7]\n\t" \
  "v_exp_f32 v96, v96\n\t" \
  "v_mfma_f32_16x16x32_bf16 v[4:7], a[212:215], v[20:23], v[4:7]\n\t" \
  "ds_read_b64_tr_b16 a[134:135], v228 offset:12288\n\t" \
  "v_max3_f32 v204, v0, v1, v2\n\t" \
  "v_mfma_f32_16x16x32_bf16 v[4:7], a[208:211], v[16:19], v[4:7]\n\t" \
  "s_waitcnt lgkmcnt(8)\n\t" \
  "ds_read_b64_tr_b16 a[136:137], v229 offset:0\n\t" \
  "v_cvt_pk_bf16_f32 v147, v70, v71\n\t" \
  "v_mfma_f32_16x16x32_bf16 v[8:11], a[224:227], v[16:19], v[176:179]\n\t" \
  "v_exp_f32 v97, v97\n\t" \
  "v_mfma_f32_16x16x32_bf16 v[8:11], a[228:231], v[20:23], v[8:11]\n\t" \
  "ds_read_b64_tr_b16 a[138:139], v229 offset:4096\n\t" \
  "v_cvt_pk_bf16_f32 v148, v72, v73\n\t" \
  "v_mfma_f32_16x16x32_bf16 v[8:11], a[232:235], v[24:27], v[8:11]\n\t" \
  "v_max3_f32 v203, v3, v4, v5\n\t" \
  "v_max3_f32 v204, v204, v6, v7\n\t" \
  "v_mfma_f32_16x16x32_bf16 v[8:11], a[236:239], v[28:31], v[8:11]\n\t" \
  "s_waitcnt lgkmcnt(6)\n\t" \
  "ds_read_b64_tr_b16 a[140:141], v229 offset:8192\n\t" \
  "v_cvt_pk_bf16_f32 v149, v74, v75\n\t" \
  "v_mfma_f32_16x16x32_bf16 v[12:15], a[252:255], v[28:31], v[176:179]\n\t" \
  "v_exp_f32 v98, v98\n\t" \
  "v_mfma_f32_16x16x32_bf16 v[12:15], a[248:251], v[24:27], v[12:15]\n\t" \
  "ds_read_b64_tr_b16 a[142:143], v229 offset:12288\n\t" \
  "v_cvt_pk_bf16_f32 v150, v76, v77\n\t" \
  "v_mfma_f32_16x16x32_bf16 v[12:15], a[244:247], v[20:23], v[12:15]\n\t" \
  "v_max3_f32 v203, v203, v8, v9\n\t" \
  "v_max3_f32 v204, v204, v10, v11\n\t" \
  "v_mfma_f32_16x16x32_bf16 v[12:15], a[240:243], v[16:19], v[12:15]\n\t" \
  "ds_read_b64_tr_b16 a[144:145], v228 offset:256\n\t" \
  "v_cvt_pk_bf16_f32 v151, v78, v79\n\t" \
  "v_mfma_f32_16x16x32_bf16 v[32:35], a[192:195], v[48:51], v[180:183]\n\t" \
  "ds_read_b64_tr_b16 a[146:147], v228 offset:4352\n\t" \
  "v_cvt_pk_bf16_f32 v152, v96, v97\n\t" \
  "v_mfma_f32_16x16x32_bf16 v[32:35], a[196:199], v[52:55], v[32:35]\n\t" \
  "v_exp_f32 v99, v99\n\t" \
  "v_mfma_f32_16x16x32_bf16 v[32:35], a[200:203], v[56:59], v[32:35]\n\t" \
  "v_max3_f32 v203, v203, v12, v13\n\t" \
  "v_max3_f32 v204, v204, v14, v15\n\t" \
  "v_mfma_f32_16x16x32_bf16 v[32:35], a[204:207], v[60:63], v[32:35]\n\t" \
  "v_max_f32 v204, v204, v203\n\t" \
  "v_cmp_lt_f32 vcc, 0x42800000, v204\n\t" \
  "v_mfma_f32_16x16x32_bf16 v[36:39], a[220:223], v[60:63], v[180:183]\n\t" \
  "ds_read_b64_tr_b16 a[148:149], v228 offset:8448\n\t" \
  "v_cvt_pk_bf16_f32 v153, v98, v99\n\t" \
  "v_mfma_f32_16x16x32_bf16 v[36:39], a[216:219], v[56:59], v[36:39]\n\t" \
  "s_cbranch_vccnz .Lsf_c70\n\t" \
  ".Lsf_r70:\n\t" \
  "v_mfma_f32_16x16x32_bf16 v[36:39], a[212:215], v[52:55], v[36:39]\n\t" \
  "ds_read_b64_tr_b16 a[150:151], v228 offset:12544\n\t" \
  "v_max3_f32 v204, v32, v33, v34\n\t" \
  "v_mfma_f32_16x16x32_bf16 v[36:39], a[208:211], v[48:51], v[36:39]\n\t" \
  "v_exp_f32 v100, v100\n\t" \
  "v_mfma_f32_16x16x32_bf16 v[40:43], a[224:227], v[48:51], v[180:183]\n\t" \
  "v_exp_f32 v101, v101\n\t" \
  "v_mfma_f32_16x16x32_bf16 v[40:43], a[228:231], v[52:55], v[40:43]\n\t" \
  "ds_read_b64_tr_b16 a[152:153], v229 offset:256\n\t" \
  "v_cvt_pk_bf16_f32 v154, v100, v101\n\t" \
  "v_mfma_f32_16x16x32_bf16 v[40:43], a[232:235], v[56:59], v[40:43]\n\t" \
  "ds_read_b64_tr_b16 a[154:155], v229 offset:4352\n\t" \
  "v_max3_f32 v203, v35, v36, v37\n\t" \
  "v_mfma_f32_16x16x32_bf16 v[40:43], a[236:239], v[60:63], v[40:43]\n\t" \
  "ds_read_b64_tr_b16 a[156:157], v229 offset:8448\n\t" \
  "v_max3_f32 v204, v204, v38, v39\n\t" \
  "v_mfma_f32_16x16x32_bf16 v[44:47], a[252:255], v[60:63], v[180:183]\n\t" \
  "v_exp_f32 v102, v102\n\t" \
  "v_mfma_f32_16x16x32_bf16 v[44:47], a[248:251], v[56:59], v[44:47]\n\t" \
  "v_exp_f32 v103, v103\n\t" \
  "v_mfma_f32_16x16x32_bf16 v[44:47], a[244:247], v[52:55], v[44:47]\n\t" \
  "ds_read_b64_tr_b16 a[158:159], v229 offset:12544\n\t" \
  "v_max3_f32 v203, v203, v40, v41\n\t" \
  "v_mfma_f32_16x16x32_bf16 v[44:47], a[240:243], v[48:51], v[44:47]\n\t" \
  "ds_read_b64_tr_b16 a[160:161], v228 offset:512\n\t" \
  "v_max3_f32 v204, v204, v42, v43\n\t" \
  "v_mfma_f32_16x16x32_bf16 v[64:67], a[192:195], v[80:83], v[184:187]\n\t" \
  "ds_read_b64_tr_b16 a[162:163], v228 offset:4608\n\t" \
  "v_cvt_pk_bf16_f32 v155, v102, v103\n\t" \
  "v_mfma_f32_16x16x32_bf16 v[64:67], a[196:199], v[84:87], v[64:67]\n\t" \
  "v_exp_f32 v104, v104\n\t" \
  "v_mfma_f32_16x16x32_bf16 v[64:67], a[200:203], v[88:91], v[64:67]\n\t" \
  "v_max3_f32 v203, v203, v44, v45\n\t" \
  "v_max3_f32 v204, v204, v46, v47\n\t" \
  "v_mfma_f32_16x16x32_bf16 v[64:67], a[204:207], v[92:95], v[64:67]\n\t" \
  "v_max_f32 v204, v204, v203\n\t" \
  "v_cmp_lt_f32 vcc, 0x42800000, v204\n\t" \
  "v_mfma_f32_16x16x32_bf16 v[68:71], a[220:223], v[92:95], v[184:187]\n\t" \
  "v_exp_f32 v105, v105\n\t" \
  "v_mfma_f32_16x16x32_bf16 v[68:71], a[216:219], v[88:91], v[68:71]\n\t" \
  "s_cbranch_vccnz .Lsf_c71\n\t" \
  ".Lsf_r71:\n\t" \
  "v_mfma_f32_16x16x32_bf16 v[68:71], a[212:215], v[84:87], v[68:71]\n\t" \
  "ds_read_b64_tr_b16 a[164:165], v228 offset:8704\n\t" \
  "v_max3_f32 v204, v64, v65, v66\n\t" \
  "v_mfma_f32_16x16x32_bf16 v[68:71], a[208:211], v[80:83], v[68:71]\n\t" \
  "ds_read_b64_tr_b16 a[166:167], v228 offset:12800\n\t" \
  "v_cvt_pk_bf16_f32 v156, v104, v105\n\t" \
  "v_mfma_f32_16x16x32_bf16 v[72:75], a[224:227], v[80:83], v[184:187]\n\t" \
  "v_exp_f32 v106, v106\n\t" \
  "v_mfma_f32_16x16x32_bf16 v[72:75], a[228:231], v[84:87], v[72:75]\n\t" \
  "v_exp_f32 v107, v107\n\t" \
  "v_mfma_f32_16x16x32_bf16 v[72:75], a[232:235], v[88:91], v[72:75]\n\t" \
  "ds_read_b64_tr_b16 a[168:169], v229 offset:512\n\t" \
  "v_max3_f32 v203, v67, v68, v69\n\t" \
  "v_mfma_f32_16x16x32_bf16 v[72:75], a[236:239], v[92:95], v[72:75]\n\t" \
  "ds_read_b64_tr_b16 a[170:171], v229 offset:4608\n\t" \
  "v_max3_f32 v204, v204, v70, v71\n\t" \
  "v_mfma_f32_16x16x32_bf16 v[76:79], a[252:255], v[92:95], v[184:187]\n\t" \
  "ds_read_b64_tr_b16 a[172:173], v229 offset:8704\n\t" \
  "v_cvt_pk_bf16_f32 v157, v106, v107\n\t" \
  "v_mfma_f32_16x16x32_bf16 v[76:79], a[248:251], v[88:91], v[76:79]\n\t" \
  "v_exp_f32 v108, v108\n\t" \
  "v_mfma_f32_16x16x32_bf16 v[76:79], a[244:247], v[84:87], v[76:79]\n\t" \
  "v_max3_f32 v203, v203, v72, v73\n\t" \
  "v_max3_f32 v204, v204, v74, v75\n\t" \
  "v_mfma_f32_16x16x32_bf16 v[76:79], a[240:243], v[80:83], v[76:79]\n\t" \
  "v_exp_f32 v109, v109\n\t" \
  "v_mfma_f32_16x16x32_bf16 v[96:99], a[192:195], v[112:115], v[188:191]\n\t" \
  "ds_read_b64_tr_b16 a[174:175], v229 offset:12800\n\t" \
  "v_cvt_pk_bf16_f32 v158, v108, v109\n\t" \
  "v_mfma_f32_16x16x32_bf16 v[96:99], a[196:199], v[116:119], v[96:99]\n\t" \
  "v_exp_f32 v110, v110\n\t" \
  "v_mfma_f32_16x16x32_bf16 v[96:99], a[200:203], v[120:123], v[96:99]\n\t" \
  "ds_read_b64_tr_b16 a[176:177], v228 offset:768\n\t" \
  "v_max3_f32 v203, v203, v76, v77\n\t" \
  "v_mfma_f32_16x16x32_bf16 v[96:99], a[204:207], v[124:127], v[96:99]\n\t" \
  "v_max3_f32 v204, v204, v78, v79\n\t" \
  "v_max_f32 v204, v204, v203\n\t" \
  "v_mfma_f32_16x16x32_bf16 v[100:103], a[220:223], v[124:127], v[188:191]\n\t" \
  "ds_read_b64_tr_b16 a[178:179], v228 offset:4864\n\t" \
  "v_cmp_lt_f32 vcc, 0x42800000, v204\n\t" \
  "v_mfma_f32_16x16x32_bf16 v[100:103], a[216:219], v[120:123], v[100:103]\n\t" \
  "ds_read_b64_tr_b16 a[180:181], v228 offset:8960\n\t" \
  "v_exp_f32 v111, v111\n\t" \
  "v_mfma_f32_16x16x32_bf16 v[100:103], a[212:215], v[116:119], v[100:103]\n\t" \
  "s_cbranch_vccnz .Lsf_c72\n\t" \
  ".Lsf_r72:\n\t" \
  "v_mfma_f32_16x16x32_bf16 v[100:103], a[208:211], v[112:115], v[100:103]\n\t" \
  "ds_read_b64_tr_b16 a[182:183], v228 offset:13056\n\t" \
  "v_max3_f32 v204, v96, v97, v98\n\t" \
  "v_mfma_f32_16x16x32_bf16 v[104:107], a[224:227], v[112:115], v[188:191]\n\t" \
  "ds_read_b64_tr_b16 a[184:185], v229 offset:768\n\t" \
  "v_cvt_pk_bf16_f32 v159, v110, v111\n\t" \
  "v_mfma_f32_16x16x32_bf16 v[104:107], a[228:231], v[116:119], v[104:107]\n\t" \
  "ds_read_b64_tr_b16 a[186:187], v229 offset:4864\n\t" \
  "v_mfma_f32_16x16x32_bf16 v[104:107], a[232:235], v[120:123], v[104:107]\n\t" \
  "ds_read_b64_tr_b16 a[188:189], v229 offset:8960\n\t" \
  "v_max3_f32 v203, v99, v100, v101\n\t" \
  "v_mfma_f32_16x16x32_bf16 v[104:107], a[236:239], v[124:127], v[104:107]\n\t" \
  "ds_read_b64_tr_b16 a[190:191], v229 offset:13056\n\t" \
  "v_max3_f32 v204, v204, v102, v103\n\t" \
  "v_mfma_f32_16x16x32_bf16 v[108:111], a[252:255], v[124:127], v[188:191]\n\t" \
  "v_exp_f32 v0, v0\n\t" \
  "v_mfma_f32_16x16x32_bf16 v[108:111], a[248:251], v[120:123], v[108:111]\n\t" \
  "v_exp_f32 v1, v1\n\t" \
  "v_mfma_f32_16x16x32_bf16 v[108:111], a[244:247], v[116:119], v[108:111]\n\t" \
  "s_waitcnt vmcnt(0) lgkmcnt(0)\n\t" \
  "v_max3_f32 v203, v203, v104, v105\n\t" \
  "v_max3_f32 v204, v204, v106, v107\n\t" \
  "v_mfma_f32_16x16x32_bf16 v[108:111], a[240:243], v[112:115], v[108:111]\n\t" \
  "s_barrier\n\t" \
  "v_mfma_f32_16x16x32_bf16 a[0:3], a[128:131], v[128:131], a[0:3]\n\t" \
  "v_exp_f32 v2, v2\n\t" \
  "v_mfma_f32_16x16x32_bf16 a[0:3], a[132:135], v[132:135], a[0:3]\n\t" \
  "ds_read_b64_tr_b16 v[16:17], v210 offset:0\n\t" \
  "v_cvt_pk_bf16_f32 v0, v0, v1\n\t" \
  "v_mfma_f32_16x16x32_bf16 a[4:7], a[136:139], v[128:131], a[4:7]\n\t" \
  "ds_read_b64_tr_b16 v[18:19], v210 offset:4096\n\t" \
  "v_max3_f32 v203, v203, v108, v109\n\t" \
  "v_mfma_f32_16x16x32_bf16 a[4:7], a[140:143], v[132:135], a[4:7]\n\t" \
  "v_max3_f32 v204, v204, v110, v111\n\t" \
  "v_max_f32 v204, v204, v203\n\t" \
  "v_mfma_f32_16x16x32_bf16 a[8:11], a[144:147], v[128:131], a[8:11]\n\t" \
  "ds_read_b64_tr_b16 v[20:21], v210 offset:8192\n\t" \
  "v_cmp_lt_f32 vcc, 0x42800000, v204\n\t" \
  "v_mfma_f32_16x16x32_bf16 a[8:11], a[148:151], v[132:135], a[8:11]\n\t" \
  "v_exp_f32 v3, v3\n\t" \
  "v_mfma_f32_16x16x32_bf16 a[12:15], a[152:155], v[128:131], a[12:15]\n\t" \
  "s_cbranch_vccnz .Lsf_c73\n\t" \
  ".Lsf_r73:\n\t" \
  "v_mfma_f32_16x16x32_bf16 a[12:15], a[156:159], v[132:135], a[12:15]\n\t" \
  "ds_read_b64_tr_b16 v[22:23], v210 offset:12288\n\t" \
  "v_cvt_pk_bf16_f32 v1, v2, v3\n\t" \
  "v_mfma_f32_16x16x32_bf16 a[16:19], a[160:163], v[128:131], a[16:19]\n\t" \
  "v_exp_f32 v4, v4\n\t" \
  "v_mfma_f32_16x16x32_bf16 a[16:19], a[164:167], v[132:135], a[16:19]\n\t" \
  "v_exp_f32 v5, v5\n\t" \
  "v_mfma_f32_16x16x32_bf16 a[20:23], a[168:171], v[128:131], a[20:23]\n\t" \
  "ds_read_b64_tr_b16 v[24:25], v211 offset:0\n\t" \
  "v_cvt_pk_bf16_f32 v2, v4, v5\n\t" \
  "v_mfma_f32_16x16x32_bf16 a[20:23], a[172:175], v[132:135], a[20:23]\n\t" \
  "v_exp_f32 v6, v6\n\t" \
  "v_mfma_f32_16x16x32_bf16 a[24:27], a[176:179], v[128:131], a[24:27]\n\t" \
  "v_exp_f32 v7, v7\n\t" \
  "v_mfma_f32_16x16x32_bf16 a[24:27], a[180:183], v[132:135], a[24:27]\n\t" \
  "ds_read_b64_tr_b16 v[26:27], v211 offset:4096\n\t" \
  "v_cvt_pk_bf16_f32 v3, v6, v7\n\t" \
  "v_mfma_f32_16x16x32_bf16 a[28:31], a[184:187], v[128:131], a[28:31]\n\t" \
  "v_exp_f32 v8, v8\n\t" \
  "v_mfma_f32_16x16x32_bf16 a[28:31], a[188:191], v[132:135], a[28:31]\n\t" \
  "ds_read_b64_tr_b16 v[28:29], v211 offset:8192\n\t" \
  "v_smfmac_f32_16x16x64_bf16 v[160:163], v[216:219], v[128:135], v208\n\t" \
  "ds_read_b64_tr_b16 v[30:31], v211 offset:12288\n\t" \
  "v_mfma_f32_16x16x32_bf16 a[32:35], a[128:131], v[136:139], a[32:35]\n\t" \
  "ds_read_b64_tr_b16 v[48:49], v210 offset:256\n\t" \
  "v_mfma_f32_16x16x32_bf16 a[32:35], a[132:135], v[140:143], a[32:35]\n\t" \
  "ds_read_b64_tr_b16 v[50:51], v210 offset:4352\n\t" \
  "v_mfma_f32_16x16x32_bf16 a[36:39], a[136:139], v[136:139], a[36:39]\n\t" \
  "v_exp_f32 v9, v9\n\t" \
  "v_mfma_f32_16x16x32_bf16 a[36:39], a[140:143], v[140:143], a[36:39]\n\t" \
  "ds_read_b64_tr_b16 v[52:53], v210 offset:8448\n\t" \
  "v_cvt_pk_bf16_f32 v4, v8, v9\n\t" \
  "v_mfma_f32_16x16x32_bf16 a[40:43], a[144:147], v[136:139], a[40:43]\n\t" \
  "v_exp_f32 v10, v10\n\t" \
  "v_mfma_f32_16x16x32_bf16 a[40:43], a[148:151], v[140:143], a[40:43]\n\t" \
  "ds_read_b64_tr_b16 v[54:55], v210 offset:12544\n\t" \
  "v_mfma_f32_16x16x32_bf16 a[44:47], a[152:155], v[136:139], a[44:47]\n\t" \
  "v_exp_f32 v11, v11\n\t" \
  "v_mfma_f32_16x16x32_bf16 a[44:47], a[156:159], v[140:143], a[44:47]\n\t" \
  "ds_read_b64_tr_b16 v[56:57], v211 offset:256\n\t" \
  "v_cvt_pk_bf16_f32 v5, v10, v11\n\t" \
  "v_mfma_f32_16x16x32_bf16 a[48:51], a[160:163], v[136:139], a[48:51]\n\t" \
  "v_exp_f32 v12, v12\n\t" \
  "v_mfma_f32_16x16x32_bf16 a[48:51], a[164:167], v[140:143], a[48:51]\n\t" \
  "v_exp_f32 v13, v13\n\t" \
  "v_mfma_f32_16x16x32_bf16 a[52:55], a[168:171], v[136:139], a[52:55]\n\t" \
  "ds_read_b64_tr_b16 v[58:59], v211 offset:4352\n\t" \
  "v_cvt_pk_bf16_f32 v6, v12, v13\n\t" \
  "v_mfma_f32_16x16x32_bf16 a[52:55], a[172:175], v[140:143], a[52:55]\n\t" \
  "ds_read_b64_tr_b16 v[60:61], v211 offset:8448\n\t" \
  "v_mfma_f32_16x16x32_bf16 a[56:59], a[176:179], v[136:139], a[56:59]\n\t" \
  "v_exp_f32 v14, v14\n\t" \
  "v_mfma_f32_16x16x32_bf16 a[56:59], a[180:183], v[140:143], a[56:59]\n\t" \
  "v_exp_f32 v15, v15\n\t" \
  "v_mfma_f32_16x16x32_bf16 a[60:63], a[184:187], v[136:139], a[60:63]\n\t" \
  "ds_read_b64_tr_b16 v[62:63], v211 offset:12544\n\t" \
  "v_cvt_pk_bf16_f32 v7, v14, v15\n\t" \
  "v_mfma_f32_16x16x32_bf16 a[60:63], a[188:191], v[140:143], a[60:63]\n\t" \
  "v_exp_f32 v32, v32\n\t" \
  "v_smfmac_f32_16x16x64_bf16 v[164:167], v[216:219], v[136:143], v208\n\t" \
  "ds_read_b64_tr_b16 v[80:81], v210 offset:512\n\t" \
  "v_mfma_f32_16x16x32_bf16 a[64:67], a[128:131], v[144:147], a[64:67]\n\t" \
  "v_exp_f32 v33, v33\n\t" \
  "v_mfma_f32_16x16x32_bf16 a[64:67], a[132:135], v[148:151], a[64:67]\n\t" \
  "ds_read_b64_tr_b16 v[82:83], v210 offset:4608\n\t" \
  "v_cvt_pk_bf16_f32 v32, v32, v33\n\t" \
  "v_mfma_f32_16x16x32_bf16 a[68:71], a[136:139], v[144:147], a[68:71]\n\t" \
  "v_exp_f32 v34, v34\n\t" \
  "v_mfma_f32_16x16x32_bf16 a[68:71], a[140:143], v[148:151], a[68:71]\n\t" \
  "ds_read_b64_tr_b16 v[84:85], v210 offset:8704\n\t" \
  "v_mfma_f32_16x16x32_bf16 a[72:75], a[144:147], v[144:147], a[72:75]\n\t" \
  "v_exp_f32 v35, v35\n\t" \
  "v_mfma_f32_16x16x32_bf16 a[72:75], a[148:151], v[148:151], a[72:75]\n\t" \
  "ds_read_b64_tr_b16 v[86:87], v210 offset:12800\n\t" \
  "v_cvt_pk_bf16_f32 v33, v34, v35\n\t" \
  "v_mfma_f32_16x16x32_bf16 a[76:79], a[152:155], v[144:147], a[76:79]\n\t" \
  "v_exp_f32 v36, v36\n\t" \
  "v_mfma_f32_16x16x32_bf16 a[76:79], a[156:159], v[148:151], a[76:79]\n\t" \
  "ds_read_b64_tr_b16 v[88:89], v211 offset:512\n\t" \
  "v_mfma_f32_16x16x32_bf16 a[80:83], a[160:163], v[144:147], a[80:83]\n\t" \
  "v_exp_f32 v37, v37\n\t" \
  "v_mfma_f32_16x16x32_bf16 a[80:83], a[164:167], v[148:151], a[80:83]\n\t" \
  "ds_read_b64_tr_b16 v[90:91], v211 offset:4608\n\t" \
  "v_cvt_pk_bf16_f32 v34, v36, v37\n\t" \
  "v_mfma_f32_16x16x32_bf16 a[84:87], a[168:171], v[144:147], a[84:87]\n\t" \
  "v_exp_f32 v38, v38\n\t" \
  "v_mfma_f32_16x16x32_bf16 a[84:87], a[172:175], v[148:151], a[84:87]\n\t" \
  "v_exp_f32 v39, v39\n\t" \
  "v_mfma_f32_16x16x32_bf16 a[88:91], a[176:179], v[144:147], a[88:91]\n\t" \
  "ds_read_b64_tr_b16 v[92:93], v211 offset:8704\n\t" \
  "v_cvt_pk_bf16_f32 v35, v38, v39\n\t" \
  "v_mfma_f32_16x16x32_bf16 a[88:91], a[180:183], v[148:151], a[88:91]\n\t" \
  "v_exp_f32 v40, v40\n\t" \
  "v_mfma_f32_16x16x32_bf16 a[92:95], a[184:187], v[144:147], a[92:95]\n\t" \
  "ds_read_b64_tr_b16 v[94:95], v211 offset:12800\n\t" \
  "v_mfma_f32_16x16x32_bf16 a[92:95], a[188:191], v[148:151], a[92:95]\n\t" \
  "v_exp_f32 v41, v41\n\t" \
  "v_smfmac_f32_16x16x64_bf16 v[168:171], v[216:219], v[144:151], v208\n\t" \
  "ds_read_b64_tr_b16 v[112:113], v210 offset:768\n\t" \
  "v_cvt_pk_bf16_f32 v36, v40, v41\n\t" \
  "v_mfma_f32_16x16x32_bf16 a[96:99], a[128:131], v[152:155], a[96:99]\n\t" \
  "ds_read_b64_tr_b16 v[114:115], v210 offset:4864\n\t" \
  "v_mfma_f32_16x16x32_bf16 a[96:99], a[132:135], v[156:159], a[96:99]\n\t" \
  "v_exp_f32 v42, v42\n\t" \
  "v_mfma_f32_16x16x32_bf16 a[100:103], a[136:139], v[152:155], a[100:103]\n\t" \
  "ds_read_b64_tr_b16 v[116:117], v210 offset:8960\n\t" \
  "v_mfma_f32_16x16x32_bf16 a[100:103], a[140:143], v[156:159], a[100:103]\n\t" \
  "v_exp_f32 v43, v43\n\t" \
  "v_mfma_f32_16x16x32_bf16 a[104:107], a[144:147], v[152:155], a[104:107]\n\t" \
  "ds_read_b64_tr_b16 v[118:119], v210 offset:13056\n\t" \
  "v_cvt_pk_bf16_f32 v37, v42, v43\n\t" \
  "v_mfma_f32_16x16x32_bf16 a[104:107], a[148:151], v[156:159], a[104:107]\n\t" \
  "v_exp_f32 v44, v44\n\t" \
  "v_mfma_f32_16x16x32_bf16 a[108:111], a[152:155], v[152:155], a[108:111]\n\t" \
  "ds_read_b64_tr_b16 v[120:121], v211 offset:768\n\t" \
  "v_mfma_f32_16x16x32_bf16 a[108:111], a[156:159], v[156:159], a[108:111]\n\t" \
  "v_exp_f32 v45, v45\n\t" \
  "v_mfma_f32_16x16x32_bf16 a[112:115], a[160:163], v[152:155], a[112:115]\n\t" \
  "ds_read_b64_tr_b16 v[122:123], v211 offset:4864\n\t" \
  "v_cvt_pk_bf16_f32 v38, v44, v45\n\t" \
  "v_mfma_f32_16x16x32_bf16 a[112:115], a[164:167], v[156:159], a[112:115]\n\t" \
  "v_exp_f32 v46, v46\n\t" \
  "v_mfma_f32_16x16x32_bf16 a[116:119], a[168:171], v[152:155], a[116:119]\n\t" \
  "v_exp_f32 v47, v47\n\t" \
  "v_mfma_f32_16x16x32_bf16 a[116:119], a[172:175], v[156:159], a[116:119]\n\t" \
  "ds_read_b64_tr_b16 v[124:125], v211 offset:8960\n\t" \
  "v_cvt_pk_bf16_f32 v39, v46, v47\n\t" \
  "v_mfma_f32_16x16x32_bf16 a[120:123], a[176:179], v[152:155], a[120:123]\n\t" \
  "v_exp_f32 v64, v64\n\t" \
  "v_mfma_f32_16x16x32_bf16 a[120:123], a[180:183], v[156:159], a[120:123]\n\t" \
  "ds_read_b64_tr_b16 v[126:127], v211 offset:13056\n\t" \
  "v_mfma_f32_16x16x32_bf16 a[124:127], a[184:187], v[152:155], a[124:127]\n\t" \
  "v_exp_f32 v65, v65\n\t" \
  "v_mfma_f32_16x16x32_bf16 a[124:127], a[188:191], v[156:159], a[124:127]\n\t" \
  "v_exp_f32 v66, v66\n\t" \
  "v_smfmac_f32_16x16x64_bf16 v[172:175], v[216:219], v[152:159], v208\n\t" \
  "s_waitcnt lgkmcnt(15)\n\t" \
  "s_cmp_lg_u32 s99, 0\n\t" \
  "s_cbranch_scc1 .Lsf_p7\n\t" \
  ".Lsf_q7:\n\t" \
  "v_mfma_f32_16x16x32_bf16 a[0:3], v[16:19], v[0:3], a[0:3]\n\t" \
  "v_exp_f32 v67, v67\n\t" \
  "v_mfma_f32_16x16x32_bf16 a[0:3], v[20:23], v[4:7], a[0:3]\n\t" \
  "v_exp_f32 v68, v68\n\t" \
  "v_smfmac_f32_16x16x64_bf16 v[160:163], v[216:219], v[0:7], v208\n\t" \
  "v_cvt_pk_bf16_f32 v64, v64, v65\n\t" \
  "v_mfma_f32_16x16x32_bf16 a[4:7], v[24:27], v[0:3], a[4:7]\n\t" \
  "v_exp_f32 v69, v69\n\t" \
  "v_mfma_f32_16x16x32_bf16 a[4:7], v[28:31], v[4:7], a[4:7]\n\t" \
  "v_exp_f32 v70, v70\n\t" \
  "v_mfma_f32_16x16x32_bf16 a[8:11], v[48:51], v[0:3], a[8:11]\n\t" \
  "v_exp_f32 v71, v71\n\t" \
  "v_mfma_f32_16x16x32_bf16 a[8:11], v[52:55], v[4:7], a[8:11]\n\t" \
  "v_exp_f32 v72, v72\n\t" \
  "v_mfma_f32_16x16x32_bf16 a[12:15], v[56:59], v[0:3], a[12:15]\n\t" \
  "v_exp_f32 v73, v73\n\t" \
  "v_mfma_f32_16x16x32_bf16 a[12:15], v[60:63], v[4:7], a[12:15]\n\t" \
  "s_waitcnt lgkmcnt(8)\n\t" \
  "v_exp_f32 v74, v74\n\t" \
  "v_mfma_f32_16x16x32_bf16 a[16:19], v[80:83], v[0:3], a[16:19]\n\t" \
  "v_exp_f32 v75, v75\n\t" \
  "v_mfma_f32_16x16x32_bf16 a[16:19], v[84:87], v[4:7], a[16:19]\n\t" \
  "v_cvt_pk_bf16_f32 v65, v66, v67\n\t" \
  "v_cvt_pk_bf16_f32 v66, v68, v69\n\t" \
  "v_mfma_f32_16x16x32_bf16 a[20:23], v[88:91], v[0:3], a[20:23]\n\t" \
  "v_exp_f32 v76, v76\n\t" \
  "v_mfma_f32_16x16x32_bf16 a[20:23], v[92:95], v[4:7], a[20:23]\n\t" \
  "s_waitcnt lgkmcnt(0)\n\t" \
  "v_exp_f32 v77, v77\n\t" \
  "v_mfma_f32_16x16x32_bf16 a[24:27], v[112:115], v[0:3], a[24:27]\n\t" \
  "v_cvt_pk_bf16_f32 v67, v70, v71\n\t" \
  "v_cvt_pk_bf16_f32 v68, v72, v73\n\t" \
  "v_mfma_f32_16x16x32_bf16 a[24:27], v[116:119], v[4:7], a[24:27]\n\t" \
  "v_exp_f32 v78, v78\n\t" \
  "v_mfma_f32_16x16x32_bf16 a[28:31], v[120:123], v[0:3], a[28:31]\n\t" \
  "v_exp_f32 v79, v79\n\t" \
  "v_mfma_f32_16x16x32_bf16 a[28:31], v[124:127], v[4:7], a[28:31]\n\t" \
  "v_cvt_pk_bf16_f32 v69, v74, v75\n\t" \
  "v_cvt_pk_bf16_f32 v70, v76, v77\n\t" \
  "v_mfma_f32_16x16x32_bf16 a[32:35], v[16:19], v[32:35], a[32:35]\n\t" \
  "v_exp_f32 v96, v96\n\t" \
  "v_mfma_f32_16x16x32_bf16 a[32:35], v[20:23], v[36:39], a[32:35]\n\t" \
  "v_exp_f32 v97, v97\n\t" \
  "v_smfmac_f32_16x16x64_bf16 v[164:167], v[216:219], v[32:39], v208\n\t" \
  "v_cvt_pk_bf16_f32 v71, v78, v79\n\t" \
  "v_mfma_f32_16x16x32_bf16 a[36:39], v[24:27], v[32:35], a[36:39]\n\t" \
  "v_exp_f32 v98, v98\n\t" \
  "v_mfma_f32_16x16x32_bf16 a[36:39], v[28:31], v[36:39], a[36:39]\n\t" \
  "v_exp_f32 v99, v99\n\t" \
  "v_mfma_f32_16x16x32_bf16 a[40:43], v[48:51], v[32:35], a[40:43]\n\t" \
  "v_exp_f32 v100, v100\n\t" \
  "v_mfma_f32_16x16x32_bf16 a[40:43], v[52:55], v[36:39], a[40:43]\n\t" \
  "v_exp_f32 v101, v101\n\t" \
  "v_mfma_f32_16x16x32_bf16 a[44:47], v[56:59], v[32:35], a[44:47]\n\t" \
  "v_exp_f32 v102, v102\n\t" \
  "v_mfma_f32_16x16x32_bf16 a[44:47], v[60:63], v[36:39], a[44:47]\n\t" \
  "v_exp_f32 v103, v103\n\t" \
  "v_mfma_f32_16x16x32_bf16 a[48:51], v[80:83], v[32:35], a[48:51]\n\t" \
  "v_exp_f32 v104, v104\n\t" \
  "v_mfma_f32_16x16x32_bf16 a[48:51], v[84:87], v[36:39], a[48:51]\n\t" \
  "v_exp_f32 v105, v105\n\t" \
  "v_mfma_f32_16x16x32_bf16 a[52:55], v[88:91], v[32:35], a[52:55]\n\t" \
  "v_cvt_pk_bf16_f32 v96, v96, v97\n\t" \
  "v_cvt_pk_bf16_f32 v97, v98, v99\n\t" \
  "v_mfma_f32_16x16x32_bf16 a[52:55], v[92:95], v[36:39], a[52:55]\n\t" \
  "v_exp_f32 v106, v106\n\t" \
  "v_mfma_f32_16x16x32_bf16 a[56:59], v[112:115], v[32:35], a[56:59]\n\t" \
  "v_exp_f32 v107, v107\n\t" \
  "v_mfma_f32_16x16x32_bf16 a[56:59], v[116:119], v[36:39], a[56:59]\n\t" \
  "v_cvt_pk_bf16_f32 v98, v100, v101\n\t" \
  "v_cvt_pk_bf16_f32 v99, v102, v103\n\t" \
  "v_mfma_f32_16x16x32_bf16 a[60:63], v[120:123], v[32:35], a[60:63]\n\t" \
  "v_exp_f32 v108, v108\n\t" \
  "v_mfma_f32_16x16x32_bf16 a[60:63], v[124:127], v[36:39], a[60:63]\n\t" \
  "v_exp_f32 v109, v109\n\t" \
  "v_mfma_f32_16x16x32_bf16 a[64:67], v[16:19], v[64:67], a[64:67]\n\t" \
  "v_cvt_pk_bf16_f32 v100, v104, v105\n\t" \
  "v_cvt_pk_bf16_f32 v101, v106, v107\n\t" \
  "v_mfma_f32_16x16x32_bf16 a[64:67], v[20:23], v[68:71], a[64:67]\n\t" \
  "v_exp_f32 v110, v110\n\t" \
  "v_smfmac_f32_16x16x64_bf16 v[168:171], v[216:219], v[64:71], v208\n\t" \
  "v_cvt_pk_bf16_f32 v102, v108, v109\n\t" \
  "v_mfma_f32_16x16x32_bf16 a[68:71], v[24:27], v[64:67], a[68:71]\n\t" \
  "v_exp_f32 v111, v111\n\t" \
  "v_mfma_f32_16x16x32_bf16 a[68:71], v[28:31], v[68:71], a[68:71]\n\t" \
  "v_cvt_pk_bf16_f32 v103, v110, v111\n\t" \
  "v_mfma_f32_16x16x32_bf16 a[72:75], v[48:51], v[64:67], a[72:75]\n\t" \
  "v_mfma_f32_16x16x32_bf16 a[72:75], v[52:55], v[68:71], a[72:75]\n\t" \
  "v_mfma_f32_16x16x32_bf16 a[76:79], v[56:59], v[64:67], a[76:79]\n\t" \
  "v_mfma_f32_16x16x32_bf16 a[76:79], v[60:63], v[68:71], a[76:79]\n\t" \
  "v_mfma_f32_16x16x32_bf16 a[80:83], v[80:83], v[64:67], a[80:83]\n\t" \
  "v_mfma_f32_16x16x32_bf16 a[80:83], v[84:87], v[68:71], a[80:83]\n\t" \
  "v_mfma_f32_16x16x32_bf16 a[84:87], v[88:91], v[64:67], a[84:87]\n\t" \
  "v_mfma_f32_16x16x32_bf16 a[84:87], v[92:95], v[68:71], a[84:87]\n\t" \
  "v_mfma_f32_16x16x32_bf16 a[88:91], v[112:115], v[64:67], a[88:91]\n\t" \
  "v_mfma_f32_16x16x32_bf16 a[88:91], v[116:119], v[68:71], a[88:91]\n\t" \
  "v_mfma_f32_16x16x32_bf16 a[92:95], v[120:123], v[64:67], a[92:95]\n\t" \
  "v_mfma_f32_16x16x32_bf16 a[92:95], v[124:127], v[68:71], a[92:95]\n\t" \
  "v_mfma_f32_16x16x32_bf16 a[96:99], v[16:19], v[96:99], a[96:99]\n\t" \
  "v_mfma_f32_16x16x32_bf16 a[96:99], v[20:23], v[100:103], a[96:99]\n\t" \
  "v_smfmac_f32_16x16x64_bf16 v[172:175], v[216:219], v[96:103], v208\n\t" \
  "v_mfma_f32_16x16x32_bf16 a[100:103], v[24:27], v[96:99], a[100:103]\n\t" \
  "v_mfma_f32_16x16x32_bf16 a[100:103], v[28:31], v[100:103], a[100:103]\n\t" \
  "v_mfma_f32_16x16x32_bf16 a[104:107], v[48:51], v[96:99], a[104:107]\n\t" \
  "v_mfma_f32_16x16x32_bf16 a[104:107], v[52:55], v[100:103], a[104:107]\n\t" \
  "v_mfma_f32_16x16x32_bf16 a[108:111], v[56:59], v[96:99], a[108:111]\n\t" \
  "v_mfma_f32_16x16x32_bf16 a[108:111], v[60:63], v[100:103], a[108:111]\n\t" \
  "v_mfma_f32_16x16x32_bf16 a[112:115], v[80:83], v[96:99], a[112:115]\n\t" \
  "v_mfma_f32_16x16x32_bf16 a[112:115], v[84:87], v[100:103], a[112:115]\n\t" \
  "v_mfma_f32_16x16x32_bf16 a[116:119], v[88:91], v[96:99], a[116:119]\n\t" \
  "v_mfma_f32_16x16x32_bf16 a[116:119], v[92:95], v[100:103], a[116:119]\n\t" \
  "v_mfma_f32_16x16x32_bf16 a[120:123], v[112:115], v[96:99], a[120:123]\n\t" \
  "v_mfma_f32_16x16x32_bf16 a[120:123], v[116:119], v[100:103], a[120:123]\n\t" \
  "v_mfma_f32_16x16x32_bf16 a[124:127], v[120:123], v[96:99], a[124:127]\n\t" \
  "v_mfma_f32_16x16x32_bf16 a[124:127], v[124:127], v[100:103], a[124:127]\n\t" \
  "s_nop 7\n\t" \
  "s_waitcnt vmcnt(0)\n\t" \
  "s_barrier\n\t" \
  "s_waitcnt vmcnt(0) lgkmcnt(0)\n\t" \
  "s_barrier\n\t" \
  "s_add_u32 s77, s51, 131072\n\t" \
  "s_mov_b32 s89, 0\n\t" \
  "s_brev_b32 s78, 1\n\t" \
  "s_mov_b32 s79, 0x0f800000\n\t" \
  "s_waitcnt vmcnt(0) lgkmcnt(0)\n\t" \
  "s_barrier\n\t" \
  "s_waitcnt vmcnt(0) lgkmcnt(0)\n\t" \
  "s_barrier\n\t" \
  "s_waitcnt vmcnt(0) lgkmcnt(0)\n\t" \
  "s_barrier\n\t" \
  "s_add_u32 m0, s38, 16384\n\t" \
  "s_mov_b32 s96, 0\n\t" \
  "buffer_load_dwordx4 v232, s[68:71], s96 offen offset:0 lds\n\t" \
  "s_mov_b32 s96, 6144\n\t" \
  "buffer_load_dwordx4 v232, s[68:71], s96 offen offset:1024 lds\n\t" \
  "s_mov_b32 s96, 12288\n\t" \
  "buffer_load_dwordx4 v232, s[68:71], s96 offen offset:2048 lds\n\t" \
  "s_mov_b32 s96, 18432\n\t" \
  "buffer_load_dwordx4 v232, s[68:71], s96 offen offset:3072 lds\n\t" \
  "s_waitcnt vmcnt(0) lgkmcnt(0)\n\t" \
  "s_barrier\n\t" \
  "s_add_u32 m0, s38, 32768\n\t" \
  "s_mov_b32 s96, 28672\n\t" \
  "buffer_load_dwordx4 v232, s[68:71], s96 offen offset:0 lds\n\t" \
  "s_mov_b32 s96, 34816\n\t" \
  "buffer_load_dwordx4 v232, s[68:71], s96 offen offset:1024 lds\n\t" \
  "s_add_u32 m0, s38, 32768\n\t" \
  "s_mov_b32 s96, 40960\n\t" \
  "buffer_load_dwordx4 v232, s[68:71], s96 offen offset:2048 lds\n\t" \
  "s_mov_b32 s96, 47104\n\t" \
  "buffer_load_dwordx4 v232, s[68:71], s96 offen offset:3072 lds\n\t" \
  "s_add_u32 m0, s38, 114688\n\t" \
  "s_mov_b32 s96, 0\n\t" \
  "buffer_load_dwordx4 v232, s[72:75], s96 offen offset:0 lds\n\t" \
  "s_mov_b32 s96, 6144\n\t" \
  "buffer_load_dwordx4 v232, s[72:75], s96 offen offset:1024 lds\n\t" \
  "s_waitcnt vmcnt(0) lgkmcnt(0)\n\t" \
  "s_barrier\n\t" \
  "s_add_u32 m0, s38, 114688\n\t" \
  "s_mov_b32 s96, 12288\n\t" \
  "buffer_load_dwordx4 v232, s[72:75], s96 offen offset:2048 lds\n\t" \
  "s_mov_b32 s96, 18432\n\t" \
  "buffer_load_dwordx4 v232, s[72:75], s96 offen offset:3072 lds\n\t" \
  "s_add_u32 m0, s38, 131072\n\t" \
  "s_mov_b32 s96, 28672\n\t" \
  "buffer_load_dwordx4 v232, s[72:75], s96 offen offset:0 lds\n\t" \
  "s_mov_b32 s96, 34816\n\t" \
  "buffer_load_dwordx4 v232, s[72:75], s96 offen offset:1024 lds\n\t" \
  "s_mov_b32 s96, 40960\n\t" \
  "buffer_load_dwordx4 v232, s[72:75], s96 offen offset:2048 lds\n\t" \
  "s_mov_b32 s96, 47104\n\t" \
  "buffer_load_dwordx4 v232, s[72:75], s96 offen offset:3072 lds\n\t" \
  "s_waitcnt vmcnt(0) lgkmcnt(0)\n\t" \
  "s_barrier\n\t" \
  "s_waitcnt vmcnt(0) lgkmcnt(0)\n\t" \
  "s_barrier\n\t" \
  "buffer_load_dword v209, v251, s[16:19], 0 offen sc1\n\t" \
  "ds_read_b128 v[192:195], v215 offset:32768\n\t" \
  "ds_read_b128 v[196:199], v215 offset:33792\n\t" \
  "s_waitcnt vmcnt(0) lgkmcnt(0)\n\t" \
  "v_and_b32 v250, 0xbfffbfff, v209\n\t" \
  "v_cmp_ne_u32_e64 s[84:85], s36, v250\n\t" \
  "s_cmp_lg_u64 s[84:85], 0\n\t" \
  "s_cbranch_scc1 .Lsf_v\n\t" \
  ".Lsf_w:\n\t" \
  "v_and_b32 v250, 1, v233\n\t" \
  "v_lshlrev_b32 v250, 4, v250\n\t" \
  "v_lshrrev_b32 v250, v250, v209\n\t" \
  "v_and_b32 v250, 0x4000, v250\n\t" \
  "v_cmp_ne_u32_e64 s[84:85], 0, v250\n\t" \
  "s_bcnt1_i32_b32 s98, s84\n\t" \
  "s_bcnt1_i32_b32 s54, s85\n\t" \
  "s_cmp_gt_u32 s98, s54\n\t" \
  "s_cselect_b32 s55, 0x2000, 0\n\t" \
  "s_cselect_b32 s98, s84, s85\n\t" \
  "s_cmp_lg_u32 s55, 0\n\t" \
  "s_cselect_b32 s84, s85, s84\n\t" \
  "s_lshr_b32 s85, s98, 16\n\t" \
  "s_bcnt1_i32_b32 s85, s85\n\t" \
  "s_lshr_b32 s84, s84, 16\n\t" \
  "s_bcnt1_i32_b32 s84, s84\n\t" \
  "s_add_u32 s84, s84, 7\n\t" \
  "s_cmp_ge_u32 s85, s84\n\t" \
  "s_cselect_b32 s54, -1, 0\n\t" \
  "s_xor_b32 s98, s42, s55\n\t" \
  "s_and_b32 s98, s98, s54\n\t" \
  "s_xor_b32 s45, s45, s98\n\t" \
  "s_xor_b32 s46, s46, s98\n\t" \
  "s_xor_b32 s98, s43, s55\n\t" \
  "s_and_b32 s98, s98, s54\n\t" \
  "s_xor_b32 s47, s47, s98\n\t" \
  "s_xor_b32 s48, s48, s55\n\t" \
  "s_xor_b32 s49, s49, s55\n\t" \
  "s_xor_b32 s98, s45, 0\n\t" \
  "s_waitcnt vmcnt(0) lgkmcnt(0)\n\t" \
  "s_barrier\n\t" \
  "ds_read_b128 v[200:203], v215 offset:34816\n\t" \
  "s_lshl_b32 s64, s39, 17\n\t" \
  "s_add_u32 s96, s40, 0\n\t" \
  "s_and_b32 s96, s96, 0x1c0000\n\t" \
  "s_add_u32 s96, s96, s51\n\t" \
  "s_add_u32 m0, s38, 0\n\t" \
  "s_nop 0\n\t" \
  "buffer_load_dwordx4 v240, s[24:27], s96 offen offset:0 lds\n\t" \
  "s_bfe_u32 s70, s46, 0x20014\n\t" \
  "s_lshl_b32 s70, s70, 14\n\t" \
  "s_xor_b32 s67, s70, 49152\n\t" \
  "buffer_load_dwordx4 v241, s[24:27], s96 offen offset:1024 lds\n\t" \
  "s_lshl1_add_u32 s60, s67, s46\n\t" \
  "s_xor_b32 s68, s70, 32768\n\t" \
  "buffer_load_dwordx4 v248, s[24:27], s96 offen offset:2048 lds\n\t" \
  "s_lshl1_add_u32 s61, s68, s46\n\t" \
  "s_xor_b32 s69, s70, 16384\n\t" \
  "s_lshl1_add_u32 s62, s69, s46\n\t" \
  "buffer_load_dwordx4 v249, s[24:27], s96 offen offset:3072 lds\n\t" \
  "s_lshl1_add_u32 s63, s70, s46\n\t" \
  "s_add_u32 s96, s40, 262144\n\t" \
  "s_and_b32 s96, s96, 0x1c0000\n\t" \
  "s_add_u32 s96, s96, s51\n\t" \
  "s_add_u32 m0, s38, 49152\n\t" \
  "s_nop 0\n\t" \
  "buffer_load_dwordx4 v240, s[24:27], s96 offen offset:0 lds\n\t" \
  "s_mul_i32 s65, s39, 508\n\t" \
  "s_add_u32 s65, s65, s49\n\t" \
  "buffer_load_dwordx4 v241, s[24:27], s96 offen offset:1024 lds\n\t" \
  "s_lshl_b32 s80, s39, 2\n\t" \
  "s_add_u32 s80, s80, s3\n\t" \
  "s_lshl_b32 s80, s80, 6\n\t" \
  "buffer_load_dwordx4 v248, s[24:27], s96 offen offset:2048 lds\n\t" \
  "s_add_u32 s76, s38, 147456\n\t" \
  "s_lshl_b32 s75, s80, 2\n\t" \
  "s_add_u32 s75, s75, 2228224\n\t" \
  "buffer_load_dwordx4 v249, s[24:27], s96 offen offset:3072 lds\n\t" \
  "s_lshr_b32 s71, s67, 7\n\t" \
  "s_add_u32 s71, s71, s48\n\t" \
  "s_lshr_b32 s72, s68, 7\n\t" \
  "s_add_u32 s72, s72, s48\n\t" \
  "s_lshr_b32 s66, s69, 7\n\t" \
  "ds_read_b128 v[204:207], v215 offset:35840\n\t" \
  "s_add_u32 s66, s66, s48\n\t" \
  "s_lshr_b32 s73, s70, 7\n\t" \
  "s_add_u32 s73, s73, s48\n\t" \
  "s_lshl3_add_u32 s67, s67, s47\n\t" \
  "s_lshl3_add_u32 s68, s68, s47\n\t" \
  "s_add_u32 s68, s68, 4096\n\t" \
  "s_lshl3_add_u32 s69, s69, s47\n\t" \
  "s_add_u32 s69, s69, 16384\n\t" \
  "s_lshl3_add_u32 s70, s70, s47\n\t" \
  "s_waitcnt vmcnt(0) lgkmcnt(0)\n\t" \
  "s_barrier\n\t" \
  "s_add_u32 s96, s40, 0\n\t" \
  "s_and_b32 s96, s96, 0x1c0000\n\t" \
  "s_add_u32 s96, s96, s77\n\t" \
  "s_add_u32 m0, s38, 98304\n\t" \
  "s_nop 0\n\t" \
  "buffer_load_dwordx4 v240, s[24:27], s96 offen offset:0 lds\n\t" \
  "buffer_load_dwordx4 v241, s[24:27], s96 offen offset:1024 lds\n\t" \
  "buffer_load_dwordx4 v248, s[24:27], s96 offen offset:2048 lds\n\t" \
  "buffer_load_dwordx4 v249, s[24:27], s96 offen offset:3072 lds\n\t" \
  "s_add_u32 s96, s40, 524288\n\t" \
  "s_and_b32 s96, s96, 0x1c0000\n\t" \
  "s_add_u32 s96, s96, s51\n\t" \
  "s_add_u32 m0, s38, 131072\n\t" \
  "s_waitcnt vmcnt(0) lgkmcnt(0)\n\t" \
  "buffer_load_dwordx4 v240, s[24:27], s96 offen offset:0 lds\n\t" \
  "buffer_load_dwordx4 v241, s[24:27], s96 offen offset:1024 lds\n\t" \
  "buffer_load_dwordx4 v248, s[24:27], s96 offen offset:2048 lds\n\t" \
  "buffer_load_dwordx4 v249, s[24:27], s96 offen offset:3072 lds\n\t" \
  "s_add_u32 s96, s40, 262144\n\t" \
  "s_and_b32 s96, s96, 0x1c0000\n\t" \
  "s_add_u32 s96, s96, s77\n\t" \
  "s_add_u32 m0, s38, 65536\n\t" \
  "s_nop 0\n\t" \
  "buffer_load_dwordx4 v240, s[24:27], s96 offen offset:0 lds\n\t" \
  "buffer_load_dwordx4 v241, s[24:27], s96 offen offset:1024 lds\n\t" \
  "buffer_load_dwordx4 v248, s[24:27], s96 offen offset:2048 lds\n\t" \
  "buffer_load_dwordx4 v249, s[24:27], s96 offen offset:3072 lds\n\t" \
  "s_add_u32 s96, s40, 524288\n\t" \
  "s_and_b32 s96, s96, 0x1c0000\n\t" \
  "s_add_u32 s96, s96, s77\n\t" \
  "s_add_u32 m0, s38, 81920\n\t" \
  "s_waitcnt vmcnt(0) lgkmcnt(0)\n\t" \
  "s_barrier\n\t" \
  "buffer_load_dwordx4 v240, s[24:27], s96 offen offset:0 lds\n\t" \
  "ds_read_b128 v[176:179], v215 offset:16384\n\t" \
  "ds_read_b128 v[180:183], v215 offset:17408\n\t" \
  "ds_read_b128 v[184:187], v215 offset:18432\n\t" \
  "buffer_load_dwordx4 v241, s[24:27], s96 offen offset:1024 lds\n\t" \
  "ds_read_b128 v[188:191], v215 offset:19456\n\t" \
  "buffer_load_dwordx4 v248, s[24:27], s96 offen offset:2048 lds\n\t" \
  "buffer_load_dwordx4 v249, s[24:27], s96 offen offset:3072 lds\n\t" \
  "s_add_u32 s96, s40, 786432\n\t" \
  "s_and_b32 s96, s96, 0x1c0000\n\t" \
  "s_add_u32 s96, s96, s51\n\t" \
  "s_add_u32 m0, s38, 114688\n\t" \
  "s_waitcnt vmcnt(0) lgkmcnt(0)\n\t" \
  "buffer_load_dwordx4 v240, s[24:27], s96 offen offset:0 lds\n\t" \
  "buffer_load_dwordx4 v241, s[24:27], s96 offen offset:1024 lds\n\t" \
  "buffer_load_dwordx4 v248, s[24:27], s96 offen offset:2048 lds\n\t" \
  "buffer_load_dwordx4 v249, s[24:27], s96 offen offset:3072 lds\n\t" \
  "v_add_u32 v230, 0x10000, v242\n\t" \
  "v_add_u32 v232, 0x20000, v242\n\t" \
  "ds_read_b64_tr_b16 a[192:193], v234 offset:0\n\t" \
  "v_add_f32 v225, v160, v161\n\t" \
  "v_rcp_f32 v224, v225\n\t" \
  "v_accvgpr_read_b32 v128, a0\n\t" \
  "v_accvgpr_read_b32 v129, a1\n\t" \
  "v_accvgpr_read_b32 v130, a2\n\t" \
  "v_accvgpr_read_b32 v131, a3\n\t" \
  "v_accvgpr_read_b32 v132, a4\n\t" \
  "v_accvgpr_read_b32 v133, a5\n\t" \
  "v_accvgpr_read_b32 v134, a6\n\t" \
  "v_accvgpr_read_b32 v135, a7\n\t" \
  "v_pk_mul_f32 v[128:129], v[128:129], v[224:225] op_sel_hi:[1,0]\n\t" \
  "v_pk_mul_f32 v[130:131], v[130:131], v[224:225] op_sel_hi:[1,0]\n\t" \
  "v_pk_mul_f32 v[132:133], v[132:133], v[224:225] op_sel_hi:[1,0]\n\t" \
  "v_pk_mul_f32 v[134:135], v[134:135], v[224:225] op_sel_hi:[1,0]\n\t" \
  "v_cvt_pk_bf16_f32 v128, v128, v129\n\t" \
  "v_cvt_pk_bf16_f32 v129, v130, v131\n\t" \
  "v_cvt_pk_bf16_f32 v130, v132, v133\n\t" \
  "v_cvt_pk_bf16_f32 v131, v134, v135\n\t" \
  "buffer_store_dwordx4 v[128:131], v242, s[16:19], s98 offen offset:0 sc1\n\t" \
  "ds_read_b64_tr_b16 a[194:195], v234 offset:4096\n\t" \
  "v_accvgpr_read_b32 v136, a8\n\t" \
  "v_accvgpr_read_b32 v137, a9\n\t" \
  "v_accvgpr_read_b32 v138, a10\n\t" \
  "v_accvgpr_read_b32 v139, a11\n\t" \
  "v_accvgpr_read_b32 v140, a12\n\t" \
  "v_accvgpr_read_b32 v141, a13\n\t" \
  "v_accvgpr_read_b32 v142, a14\n\t" \
  "v_accvgpr_read_b32 v143, a15\n\t" \
  "v_pk_mul_f32 v[136:137], v[136:137], v[224:225] op_sel_hi:[1,0]\n\t" \
  "v_pk_mul_f32 v[138:139], v[138:139], v[224:225] op_sel_hi:[1,0]\n\t" \
  "v_pk_mul_f32 v[140:141], v[140:141], v[224:225] op_sel_hi:[1,0]\n\t" \
  "v_pk_mul_f32 v[142:143], v[142:143], v[224:225] op_sel_hi:[1,0]\n\t" \
  "v_cvt_pk_bf16_f32 v136, v136, v137\n\t" \
  "v_cvt_pk_bf16_f32 v137, v138, v139\n\t" \
  "v_cvt_pk_bf16_f32 v138, v140, v141\n\t" \
  "v_cvt_pk_bf16_f32 v139, v142, v143\n\t" \
  "buffer_store_dwordx4 v[136:139], v242, s[16:19], s98 offen offset:1024 sc1\n\t" \
  "ds_read_b64_tr_b16 a[196:197], v235 offset:0\n\t" \
  "s_add_u32 s96, s40, 786432\n\t" \
  "ds_read_b64_tr_b16 a[198:199], v235 offset:4096\n\t" \
  "s_and_b32 s96, s96, 0x1c0000\n\t" \
  "ds_read_b64_tr_b16 a[200:201], v234 offset:8192\n\t" \
  "s_add_u32 s96, s96, s77\n\t" \
  "s_add_u32 m0, s38, 147456\n\t" \
  "s_waitcnt vmcnt(0) lgkmcnt(0)\n\t" \
  "s_barrier\n\t" \
  "v_accvgpr_read_b32 v0, a16\n\t" \
  "v_accvgpr_read_b32 v1, a17\n\t" \
  "v_accvgpr_read_b32 v2, a18\n\t" \
  "v_accvgpr_read_b32 v3, a19\n\t" \
  "v_accvgpr_read_b32 v4, a20\n\t" \
  "v_accvgpr_read_b32 v5, a21\n\t" \
  "v_accvgpr_read_b32 v6, a22\n\t" \
  "v_accvgpr_read_b32 v7, a23\n\t" \
  "v_pk_mul_f32 v[0:1], v[0:1], v[224:225] op_sel_hi:[1,0]\n\t" \
  "v_pk_mul_f32 v[2:3], v[2:3], v[224:225] op_sel_hi:[1,0]\n\t" \
  "v_pk_mul_f32 v[4:5], v[4:5], v[224:225] op_sel_hi:[1,0]\n\t" \
  "v_pk_mul_f32 v[6:7], v[6:7], v[224:225] op_sel_hi:[1,0]\n\t" \
  "v_cvt_pk_bf16_f32 v0, v0, v1\n\t" \
  "v_cvt_pk_bf16_f32 v1, v2, v3\n\t" \
  "v_cvt_pk_bf16_f32 v2, v4, v5\n\t" \
  "v_cvt_pk_bf16_f32 v3, v6, v7\n\t" \
  "buffer_store_dwordx4 v[0:3], v242, s[16:19], s98 offen offset:2048 sc1\n\t" \
  "v_accvgpr_read_b32 v8, a24\n\t" \
  "v_accvgpr_read_b32 v9, a25\n\t" \
  "v_accvgpr_read_b32 v10, a26\n\t" \
  "v_accvgpr_read_b32 v11, a27\n\t" \
  "v_accvgpr_read_b32 v12, a28\n\t" \
  "v_accvgpr_read_b32 v13, a29\n\t" \
  "v_accvgpr_read_b32 v14, a30\n\t" \
  "v_accvgpr_read_b32 v15, a31\n\t" \
  "v_pk_mul_f32 v[8:9], v[8:9], v[224:225] op_sel_hi:[1,0]\n\t" \
  "v_pk_mul_f32 v[10:11], v[10:11], v[224:225] op_sel_hi:[1,0]\n\t" \
  "v_pk_mul_f32 v[12:13], v[12:13], v[224:225] op_sel_hi:[1,0]\n\t" \
  "v_pk_mul_f32 v[14:15], v[14:15], v[224:225] op_sel_hi:[1,0]\n\t" \
  "v_cvt_pk_bf16_f32 v8, v8, v9\n\t" \
  "v_cvt_pk_bf16_f32 v9, v10, v11\n\t" \
  "v_cvt_pk_bf16_f32 v10, v12, v13\n\t" \
  "v_cvt_pk_bf16_f32 v11, v14, v15\n\t" \
  "buffer_store_dwordx4 v[8:11], v242, s[16:19], s98 offen offset:3072 sc1\n\t" \
  "buffer_load_dwordx4 v240, s[24:27], s96 offen offset:0 lds\n\t" \
  "s_xor_b32 s98, s45, 32768\n\t" \
  "ds_read_b64_tr_b16 a[202:203], v234 offset:12288\n\t" \
  "ds_read_b64_tr_b16 a[204:205], v235 offset:8192\n\t" \
  "ds_read_b128 a[224:227], v213 offset:16384\n\t" \
  "ds_read_b64_tr_b16 a[206:207], v235 offset:12288\n\t" \
  "ds_read_b128 a[228:231], v213 offset:17408\n\t" \
  "ds_read_b64_tr_b16 a[208:209], v236 offset:32768\n\t" \
  "ds_read_b128 a[232:235], v213 offset:18432\n\t" \
  "buffer_load_dwordx4 v241, s[24:27], s96 offen offset:1024 lds\n\t" \
  "ds_read_b128 a[236:239], v213 offset:19456\n\t" \
  "ds_write_b128 v213, v[128:131] offset:16384\n\t" \
  "buffer_load_dwordx4 v248, s[24:27], s96 offen offset:2048 lds\n\t" \
  "ds_write_b128 v213, v[136:139] offset:17408\n\t" \
  "ds_write_b128 v213, v[0:3] offset:18432\n\t" \
  "ds_write_b128 v213, v[8:11] offset:19456\n\t" \
  "buffer_load_dwordx4 v249, s[24:27], s96 offen offset:3072 lds\n\t" \
  "ds_read_b64_tr_b16 a[210:211], v236 offset:36864\n\t" \
  "ds_read_b64_tr_b16 a[212:213], v237 offset:32768\n\t" \
  "ds_read_b64_tr_b16 a[214:215], v237 offset:36864\n\t" \
  "v_add_f32 v227, v164, v165\n\t" \
  "v_rcp_f32 v226, v227\n\t" \
  "ds_read_b64_tr_b16 a[216:217], v236 offset:40960\n\t" \
  "v_accvgpr_read_b32 v0, a32\n\t" \
  "v_accvgpr_read_b32 v1, a33\n\t" \
  "v_accvgpr_read_b32 v2, a34\n\t" \
  "v_accvgpr_read_b32 v3, a35\n\t" \
  "v_accvgpr_read_b32 v4, a36\n\t" \
  "v_accvgpr_read_b32 v5, a37\n\t" \
  "v_accvgpr_read_b32 v6, a38\n\t" \
  "v_accvgpr_read_b32 v7, a39\n\t" \
  "v_mul_f32 v0, v0, v226\n\t" \
  "v_mul_f32 v1, v1, v226\n\t" \
  "v_mul_f32 v2, v2, v226\n\t" \
  "v_mul_f32 v3, v3, v226\n\t" \
  "v_mul_f32 v4, v4, v226\n\t" \
  "v_mul_f32 v5, v5, v226\n\t" \
  "v_mul_f32 v6, v6, v226\n\t" \
  "ds_read_b64_tr_b16 a[218:219], v236 offset:45056\n\t" \
  "v_mul_f32 v7, v7, v226\n\t" \
  "v_cvt_pk_bf16_f32 v32, v0, v1\n\t" \
  "v_cvt_pk_bf16_f32 v33, v2, v3\n\t" \
  "v_cvt_pk_bf16_f32 v34, v4, v5\n\t" \
  "v_cvt_pk_bf16_f32 v35, v6, v7\n\t" \
  "v_accvgpr_read_b32 v8, a40\n\t" \
  "v_accvgpr_read_b32 v9, a41\n\t" \
  "v_accvgpr_read_b32 v10, a42\n\t" \
  "v_accvgpr_read_b32 v11, a43\n\t" \
  "v_accvgpr_read_b32 v12, a44\n\t" \
  "v_accvgpr_read_b32 v13, a45\n\t" \
  "v_accvgpr_read_b32 v14, a46\n\t" \
  "v_accvgpr_read_b32 v15, a47\n\t" \
  "v_mul_f32 v8, v8, v226\n\t" \
  "v_mul_f32 v9, v9, v226\n\t" \
  "s_waitcnt vmcnt(0) lgkmcnt(0)\n\t" \
  "s_barrier\n\t" \
  "buffer_store_dwordx4 v[32:35], v242, s[16:19], s98 offen offset:0 sc1\n\t" \
  "v_mul_f32 v10, v10, v226\n\t" \
  "v_mul_f32 v11, v11, v226\n\t" \
  "v_mul_f32 v12, v12, v226\n\t" \
  "v_mul_f32 v13, v13, v226\n\t" \
  "v_mul_f32 v14, v14, v226\n\t" \
  "v_mul_f32 v15, v15, v226\n\t" \
  "v_cvt_pk_bf16_f32 v36, v8, v9\n\t" \
  "v_cvt_pk_bf16_f32 v37, v10, v11\n\t" \
  "v_cvt_pk_bf16_f32 v38, v12, v13\n\t" \
  "v_cvt_pk_bf16_f32 v39, v14, v15\n\t" \
  "v_accvgpr_read_b32 v0, a48\n\t" \
  "v_accvgpr_read_b32 v1, a49\n\t" \
  "v_accvgpr_read_b32 v2, a50\n\t" \
  "v_accvgpr_read_b32 v3, a51\n\t" \
  "v_accvgpr_read_b32 v4, a52\n\t" \
  "v_accvgpr_read_b32 v5, a53\n\t" \
  "v_accvgpr_read_b32 v6, a54\n\t" \
  "v_accvgpr_read_b32 v7, a55\n\t" \
  "v_mul_f32 v0, v0, v226\n\t" \
  "v_mul_f32 v1, v1, v226\n\t" \
  "v_mul_f32 v2, v2, v226\n\t" \
  "v_mul_f32 v3, v3, v226\n\t" \
  "v_mul_f32 v4, v4, v226\n\t" \
  "v_mul_f32 v5, v5, v226\n\t" \
  "v_mul_f32 v6, v6, v226\n\t" \
  "v_mul_f32 v7, v7, v226\n\t" \
  "v_cvt_pk_bf16_f32 v40, v0, v1\n\t" \
  "v_cvt_pk_bf16_f32 v41, v2, v3\n\t" \
  "v_cvt_pk_bf16_f32 v42, v4, v5\n\t" \
  "v_cvt_pk_bf16_f32 v43, v6, v7\n\t" \
  "buffer_store_dwordx4 v[36:39], v242, s[16:19], s98 offen offset:1024 sc1\n\t" \
  "v_accvgpr_read_b32 v8, a56\n\t" \
  "v_accvgpr_read_b32 v9, a57\n\t" \
  "v_accvgpr_read_b32 v10, a58\n\t" \
  "v_accvgpr_read_b32 v11, a59\n\t" \
  "v_accvgpr_read_b32 v12, a60\n\t" \
  "v_accvgpr_read_b32 v13, a61\n\t" \
  "v_accvgpr_read_b32 v14, a62\n\t" \
  "v_accvgpr_read_b32 v15, a63\n\t" \
  "v_mul_f32 v8, v8, v226\n\t" \
  "v_mul_f32 v9, v9, v226\n\t" \
  "v_mul_f32 v10, v10, v226\n\t" \
  "v_mul_f32 v11, v11, v226\n\t" \
  "v_mul_f32 v12, v12, v226\n\t" \
  "v_mul_f32 v13, v13, v226\n\t" \
  "v_mul_f32 v14, v14, v226\n\t" \
  "s_waitcnt vmcnt(0) lgkmcnt(0)\n\t" \
  "buffer_store_dword v243, v254, s[16:19], s49 offen offset:0 sc1\n\t" \
  "ds_read_b64_tr_b16 a[220:221], v237 offset:40960\n\t" \
  "ds_read_b64_tr_b16 a[222:223], v237 offset:45056\n\t" \
  "ds_read_b64_tr_b16 a[128:129], v234 offset:49152\n\t" \
  "v_mul_f32 v15, v15, v226\n\t" \
  "buffer_store_dwordx4 v[40:43], v242, s[16:19], s98 offen offset:2048 sc1\n\t" \
  "v_add_f32 v221, v168, v169\n\t" \
  "v_rcp_f32 v220, v221\n\t" \
  "v_cvt_pk_bf16_f32 v44, v8, v9\n\t" \
  "v_cvt_pk_bf16_f32 v45, v10, v11\n\t" \
  "ds_read_b64_tr_b16 a[130:131], v234 offset:53248\n\t" \
  "v_cvt_pk_bf16_f32 v46, v12, v13\n\t" \
  "v_cvt_pk_bf16_f32 v47, v14, v15\n\t" \
  "ds_read_b64_tr_b16 a[132:133], v235 offset:49152\n\t" \
  "ds_read_b128 a[240:243], v213 offset:32768\n\t" \
  "s_add_u32 s96, s40, 1048576\n\t" \
  "ds_read_b128 a[244:247], v213 offset:33792\n\t" \
  "s_and_b32 s96, s96, 0x1c0000\n\t" \
  "buffer_store_dwordx4 v[44:47], v242, s[16:19], s98 offen offset:3072 sc1\n\t" \
  "ds_read_b128 a[248:251], v213 offset:34816\n\t" \
  "ds_read_b128 a[252:255], v213 offset:35840\n\t" \
  "ds_read_b64_tr_b16 a[134:135], v235 offset:53248\n\t" \
  "ds_write_b128 v213, v[32:35] offset:32768\n\t" \
  "ds_write_b128 v213, v[36:39] offset:33792\n\t" \
  "s_add_u32 s96, s96, s51\n\t" \
  "s_add_u32 m0, s38, 98304\n\t" \
  "v_accvgpr_read_b32 v64, a64\n\t" \
  "v_accvgpr_read_b32 v65, a65\n\t" \
  "v_accvgpr_read_b32 v66, a66\n\t" \
  "v_accvgpr_read_b32 v67, a67\n\t" \
  "s_waitcnt vmcnt(0) lgkmcnt(0)\n\t" \
  "s_barrier\n\t" \
  "buffer_load_dwordx4 v240, s[24:27], s96 offen offset:0 lds\n\t" \
  "v_accvgpr_read_b32 v68, a68\n\t" \
  "v_accvgpr_read_b32 v69, a69\n\t" \
  "s_xor_b32 s98, s45, 65536\n\t" \
  "ds_read_b128 v[128:131], v242 offset:16384\n\t" \
  "v_accvgpr_read_b32 v70, a70\n\t" \
  "v_accvgpr_read_b32 v71, a71\n\t" \
  "ds_read_b128 v[132:135], v242 offset:17408\n\t" \
  "v_mul_f32 v64, v64, v220\n\t" \
  "v_add_f32 v223, v172, v173\n\t" \
  "v_rcp_f32 v222, v223\n\t" \
  "buffer_load_dwordx4 v241, s[24:27], s96 offen offset:1024 lds\n\t" \
  "ds_read_b128 v[136:139], v242 offset:18432\n\t" \
  "buffer_load_dwordx4 v248, s[24:27], s96 offen offset:2048 lds\n\t" \
  "v_mul_f32 v65, v65, v220\n\t" \
  "ds_write_b128 v213, v[40:43] offset:34816\n\t" \
  "buffer_load_dwordx4 v249, s[24:27], s96 offen offset:3072 lds\n\t" \
  "ds_read_b128 v[140:143], v242 offset:19456\n\t" \
  "v_mul_f32 v66, v66, v220\n\t" \
  "ds_read_b64_tr_b16 a[136:137], v234 offset:57344\n\t" \
  "ds_read_b128 v[144:147], v242 offset:20480\n\t" \
  "v_mul_f32 v67, v67, v220\n\t" \
  "ds_read_b128 v[148:151], v242 offset:21504\n\t" \
  "v_mul_f32 v68, v68, v220\n\t" \
  "ds_write_b128 v213, v[44:47] offset:35840\n\t" \
  "ds_read_b64_tr_b16 a[138:139], v234 offset:61440\n\t" \
  "ds_read_b128 v[152:155], v242 offset:22528\n\t" \
  "v_mul_f32 v69, v69, v220\n\t" \
  "v_mul_f32 v70, v70, v220\n\t" \
  "ds_read_b64_tr_b16 a[140:141], v235 offset:57344\n\t" \
  "ds_read_b128 v[156:159], v242 offset:23552\n\t" \
  "v_mul_f32 v71, v71, v220\n\t" \
  "s_waitcnt vmcnt(0) lgkmcnt(0)\n\t" \
  "s_nop 1\n\t" \
  "s_branch .Lat_join\n\t" \
  ".Lsf_c00:\n\t" \
  "s_mov_b32 vcc_lo, 0\n\t" \
  "s_branch .Lsf_s0\n\t" \
  ".Lsf_c10:\n\t" \
  "s_mov_b32 vcc_lo, 1\n\t" \
  "s_branch .Lsf_s0\n\t" \
  ".Lsf_c20:\n\t" \
  "s_mov_b32 vcc_lo, 2\n\t" \
  "s_branch .Lsf_s0\n\t" \
  ".Lsf_c30:\n\t" \
  "s_mov_b32 vcc_lo, 3\n\t" \
  "s_branch .Lsf_s0\n\t" \
  ".Lsf_c40:\n\t" \
  "s_mov_b32 vcc_lo, 4\n\t" \
  "s_branch .Lsf_s0\n\t" \
  ".Lsf_c50:\n\t" \
  "s_mov_b32 vcc_lo, 5\n\t" \
  "s_branch .Lsf_s0\n\t" \
  ".Lsf_c60:\n\t" \
  "s_mov_b32 vcc_lo, 6\n\t" \
  "s_branch .Lsf_s0\n\t" \
  ".Lsf_c70:\n\t" \
  "s_mov_b32 vcc_lo, 7\n\t" \
  "s_branch .Lsf_s0\n\t" \
  ".Lsf_s0:\n\t" \
  "s_nop 7\n\t" \
  "s_nop 1\n\t" \
  "v_mov_b32 v205, v204\n\t" \
  "s_nop 1\n\t" \
  "v_permlane32_swap_b32 v204, v205\n\t" \
  "s_nop 1\n\t" \
  "v_max_f32 v204, v204, v205\n\t" \
  "s_nop 1\n\t" \
  "v_mov_b32 v205, v204\n\t" \
  "s_nop 1\n\t" \
  "v_permlane16_swap_b32 v204, v205\n\t" \
  "s_nop 1\n\t" \
  "v_max_f32 v204, v204, v205\n\t" \
  "v_max_f32 v204, 0, v204\n\t" \
  "v_sub_f32 v0, v0, v204\n\t" \
  "v_sub_f32 v1, v1, v204\n\t" \
  "v_sub_f32 v2, v2, v204\n\t" \
  "v_sub_f32 v3, v3, v204\n\t" \
  "v_sub_f32 v4, v4, v204\n\t" \
  "v_sub_f32 v5, v5, v204\n\t" \
  "v_sub_f32 v6, v6, v204\n\t" \
  "v_sub_f32 v7, v7, v204\n\t" \
  "v_sub_f32 v8, v8, v204\n\t" \
  "v_sub_f32 v9, v9, v204\n\t" \
  "v_sub_f32 v10, v10, v204\n\t" \
  "v_sub_f32 v11, v11, v204\n\t" \
  "v_sub_f32 v12, v12, v204\n\t" \
  "v_sub_f32 v13, v13, v204\n\t" \
  "v_sub_f32 v14, v14, v204\n\t" \
  "v_sub_f32 v15, v15, v204\n\t" \
  "v_sub_f32 v176, v176, v204\n\t" \
  "v_sub_f32 v177, v177, v204\n\t" \
  "v_sub_f32 v178, v178, v204\n\t" \
  "v_sub_f32 v179, v179, v204\n\t" \
  "v_exp_f32 v196, -v204\n\t" \
  "s_mov_b32 s99, 1\n\t" \
  "s_nop 1\n\t" \
  "s_cmp_eq_u32 vcc_lo, 0\n\t" \
  "s_cbranch_scc1 .Lsf_r00\n\t" \
  "s_cmp_eq_u32 vcc_lo, 1\n\t" \
  "s_cbranch_scc1 .Lsf_r10\n\t" \
  "s_cmp_eq_u32 vcc_lo, 2\n\t" \
  "s_cbranch_scc1 .Lsf_r20\n\t" \
  "s_cmp_eq_u32 vcc_lo, 3\n\t" \
  "s_cbranch_scc1 .Lsf_r30\n\t" \
  "s_cmp_eq_u32 vcc_lo, 4\n\t" \
  "s_cbranch_scc1 .Lsf_r40\n\t" \
  "s_cmp_eq_u32 vcc_lo, 5\n\t" \
  "s_cbranch_scc1 .Lsf_r50\n\t" \
  "s_cmp_eq_u32 vcc_lo, 6\n\t" \
  "s_cbranch_scc1 .Lsf_r60\n\t" \
  "s_cmp_eq_u32 vcc_lo, 7\n\t" \
  "s_cbranch_scc1 .Lsf_r70\n\t" \
  ".Lsf_c01:\n\t" \
  "s_mov_b32 vcc_lo, 0\n\t" \
  "s_branch .Lsf_s1\n\t" \
  ".Lsf_c11:\n\t" \
  "s_mov_b32 vcc_lo, 1\n\t" \
  "s_branch .Lsf_s1\n\t" \
  ".Lsf_c21:\n\t" \
  "s_mov_b32 vcc_lo, 2\n\t" \
  "s_branch .Lsf_s1\n\t" \
  ".Lsf_c31:\n\t" \
  "s_mov_b32 vcc_lo, 3\n\t" \
  "s_branch .Lsf_s1\n\t" \
  ".Lsf_c41:\n\t" \
  "s_mov_b32 vcc_lo, 4\n\t" \
  "s_branch .Lsf_s1\n\t" \
  ".Lsf_c51:\n\t" \
  "s_mov_b32 vcc_lo, 5\n\t" \
  "s_branch .Lsf_s1\n\t" \
  ".Lsf_c61:\n\t" \
  "s_mov_b32 vcc_lo, 6\n\t" \
  "s_branch .Lsf_s1\n\t" \
  ".Lsf_c71:\n\t" \
  "s_mov_b32 vcc_lo, 7\n\t" \
  "s_branch .Lsf_s1\n\t" \
  ".Lsf_s1:\n\t" \
  "s_nop 7\n\t" \
  "s_nop 1\n\t" \
  "v_mov_b32 v205, v204\n\t" \
  "s_nop 1\n\t" \
  "v_permlane32_swap_b32 v204, v205\n\t" \
  "s_nop 1\n\t" \
  "v_max_f32 v204, v204, v205\n\t" \
  "s_nop 1\n\t" \
  "v_mov_b32 v205, v204\n\t" \
  "s_nop 1\n\t" \
  "v_permlane16_swap_b32 v204, v205\n\t" \
  "s_nop 1\n\t" \
  "v_max_f32 v204, v204, v205\n\t" \
  "v_max_f32 v204, 0, v204\n\t" \
  "v_sub_f32 v32, v32, v204\n\t" \
  "v_sub_f32 v33, v33, v204\n\t" \
  "v_sub_f32 v34, v34, v204\n\t" \
  "v_sub_f32 v35, v35, v204\n\t" \
  "v_sub_f32 v36, v36, v204\n\t" \
  "v_sub_f32 v37, v37, v204\n\t" \
  "v_sub_f32 v38, v38, v204\n\t" \
  "v_sub_f32 v39, v39, v204\n\t" \
  "v_sub_f32 v40, v40, v204\n\t" \
  "v_sub_f32 v41, v41, v204\n\t" \
  "v_sub_f32 v42, v42, v204\n\t" \
  "v_sub_f32 v43, v43, v204\n\t" \
  "v_sub_f32 v44, v44, v204\n\t" \
  "v_sub_f32 v45, v45, v204\n\t" \
  "v_sub_f32 v46, v46, v204\n\t" \
  "v_sub_f32 v47, v47, v204\n\t" \
  "v_sub_f32 v180, v180, v204\n\t" \
  "v_sub_f32 v181, v181, v204\n\t" \
  "v_sub_f32 v182, v182, v204\n\t" \
  "v_sub_f32 v183, v183, v204\n\t" \
  "v_exp_f32 v197, -v204\n\t" \
  "s_mov_b32 s99, 1\n\t" \
  "s_nop 1\n\t" \
  "s_cmp_eq_u32 vcc_lo, 0\n\t" \
  "s_cbranch_scc1 .Lsf_r01\n\t" \
  "s_cmp_eq_u32 vcc_lo, 1\n\t" \
  "s_cbranch_scc1 .Lsf_r11\n\t" \
  "s_cmp_eq_u32 vcc_lo, 2\n\t" \
  "s_cbranch_scc1 .Lsf_r21\n\t" \
  "s_cmp_eq_u32 vcc_lo, 3\n\t" \
  "s_cbranch_scc1 .Lsf_r31\n\t" \
  "s_cmp_eq_u32 vcc_lo, 4\n\t" \
  "s_cbranch_scc1 .Lsf_r41\n\t" \
  "s_cmp_eq_u32 vcc_lo, 5\n\t" \
  "s_cbranch_scc1 .Lsf_r51\n\t" \
  "s_cmp_eq_u32 vcc_lo, 6\n\t" \
  "s_cbranch_scc1 .Lsf_r61\n\t" \
  "s_cmp_eq_u32 vcc_lo, 7\n\t" \
  "s_cbranch_scc1 .Lsf_r71\n\t" \
  ".Lsf_c02:\n\t" \
  "s_mov_b32 vcc_lo, 0\n\t" \
  "s_branch .Lsf_s2\n\t" \
  ".Lsf_c12:\n\t" \
  "s_mov_b32 vcc_lo, 1\n\t" \
  "s_branch .Lsf_s2\n\t" \
  ".Lsf_c22:\n\t" \
  "s_mov_b32 vcc_lo, 2\n\t" \
  "s_branch .Lsf_s2\n\t" \
  ".Lsf_c32:\n\t" \
  "s_mov_b32 vcc_lo, 3\n\t" \
  "s_branch .Lsf_s2\n\t" \
  ".Lsf_c42:\n\t" \
  "s_mov_b32 vcc_lo, 4\n\t" \
  "s_branch .Lsf_s2\n\t" \
  ".Lsf_c52:\n\t" \
  "s_mov_b32 vcc_lo, 5\n\t" \
  "s_branch .Lsf_s2\n\t" \
  ".Lsf_c62:\n\t" \
  "s_mov_b32 vcc_lo, 6\n\t" \
  "s_branch .Lsf_s2\n\t" \
  ".Lsf_c72:\n\t" \
  "s_mov_b32 vcc_lo, 7\n\t" \
  "s_branch .Lsf_s2\n\t" \
  ".Lsf_s2:\n\t" \
  "s_nop 7\n\t" \
  "s_nop 1\n\t" \
  "v_mov_b32 v205, v204\n\t" \
  "s_nop 1\n\t" \
  "v_permlane32_swap_b32 v204, v205\n\t" \
  "s_nop 1\n\t" \
  "v_max_f32 v204, v204, v205\n\t" \
  "s_nop 1\n\t" \
  "v_mov_b32 v205, v204\n\t" \
  "s_nop 1\n\t" \
  "v_permlane16_swap_b32 v204, v205\n\t" \
  "s_nop 1\n\t" \
  "v_max_f32 v204, v204, v205\n\t" \
  "v_max_f32 v204, 0, v204\n\t" \
  "v_sub_f32 v64, v64, v204\n\t" \
  "v_sub_f32 v65, v65, v204\n\t" \
  "v_sub_f32 v66, v66, v204\n\t" \
  "v_sub_f32 v67, v67, v204\n\t" \
  "v_sub_f32 v68, v68, v204\n\t" \
  "v_sub_f32 v69, v69, v204\n\t" \
  "v_sub_f32 v70, v70, v204\n\t" \
  "v_sub_f32 v71, v71, v204\n\t" \
  "v_sub_f32 v72, v72, v204\n\t" \
  "v_sub_f32 v73, v73, v204\n\t" \
  "v_sub_f32 v74, v74, v204\n\t" \
  "v_sub_f32 v75, v75, v204\n\t" \
  "v_sub_f32 v76, v76, v204\n\t" \
  "v_sub_f32 v77, v77, v204\n\t" \
  "v_sub_f32 v78, v78, v204\n\t" \
  "v_sub_f32 v79, v79, v204\n\t" \
  "v_sub_f32 v184, v184, v204\n\t" \
  "v_sub_f32 v185, v185, v204\n\t" \
  "v_sub_f32 v186, v186, v204\n\t" \
  "v_sub_f32 v187, v187, v204\n\t" \
  "v_exp_f32 v198, -v204\n\t" \
  "s_mov_b32 s99, 1\n\t" \
  "s_nop 1\n\t" \
  "s_cmp_eq_u32 vcc_lo, 0\n\t" \
  "s_cbranch_scc1 .Lsf_r02\n\t" \
  "s_cmp_eq_u32 vcc_lo, 1\n\t" \
  "s_cbranch_scc1 .Lsf_r12\n\t" \
  "s_cmp_eq_u32 vcc_lo, 2\n\t" \
  "s_cbranch_scc1 .Lsf_r22\n\t" \
  "s_cmp_eq_u32 vcc_lo, 3\n\t" \
  "s_cbranch_scc1 .Lsf_r32\n\t" \
  "s_cmp_eq_u32 vcc_lo, 4\n\t" \
  "s_cbranch_scc1 .Lsf_r42\n\t" \
  "s_cmp_eq_u32 vcc_lo, 5\n\t" \
  "s_cbranch_scc1 .Lsf_r52\n\t" \
  "s_cmp_eq_u32 vcc_lo, 6\n\t" \
  "s_cbranch_scc1 .Lsf_r62\n\t" \
  "s_cmp_eq_u32 vcc_lo, 7\n\t" \
  "s_cbranch_scc1 .Lsf_r72\n\t" \
  ".Lsf_c03:\n\t" \
  "s_mov_b32 vcc_lo, 0\n\t" \
  "s_branch .Lsf_s3\n\t" \
  ".Lsf_c13:\n\t" \
  "s_mov_b32 vcc_lo, 1\n\t" \
  "s_branch .Lsf_s3\n\t" \
  ".Lsf_c23:\n\t" \
  "s_mov_b32 vcc_lo, 2\n\t" \
  "s_branch .Lsf_s3\n\t" \
  ".Lsf_c33:\n\t" \
  "s_mov_b32 vcc_lo, 3\n\t" \
  "s_branch .Lsf_s3\n\t" \
  ".Lsf_c43:\n\t" \
  "s_mov_b32 vcc_lo, 4\n\t" \
  "s_branch .Lsf_s3\n\t" \
  ".Lsf_c53:\n\t" \
  "s_mov_b32 vcc_lo, 5\n\t" \
  "s_branch .Lsf_s3\n\t" \
  ".Lsf_c63:\n\t" \
  "s_mov_b32 vcc_lo, 6\n\t" \
  "s_branch .Lsf_s3\n\t" \
  ".Lsf_c73:\n\t" \
  "s_mov_b32 vcc_lo, 7\n\t" \
  "s_branch .Lsf_s3\n\t" \
  ".Lsf_s3:\n\t" \
  "s_nop 7\n\t" \
  "s_nop 1\n\t" \
  "v_mov_b32 v205, v204\n\t" \
  "s_nop 1\n\t" \
  "v_permlane32_swap_b32 v204, v205\n\t" \
  "s_nop 1\n\t" \
  "v_max_f32 v204, v204, v205\n\t" \
  "s_nop 1\n\t" \
  "v_mov_b32 v205, v204\n\t" \
  "s_nop 1\n\t" \
  "v_permlane16_swap_b32 v204, v205\n\t" \
  "s_nop 1\n\t" \
  "v_max_f32 v204, v204, v205\n\t" \
  "v_max_f32 v204, 0, v204\n\t" \
  "v_sub_f32 v96, v96, v204\n\t" \
  "v_sub_f32 v97, v97, v204\n\t" \
  "v_sub_f32 v98, v98, v204\n\t" \
  "v_sub_f32 v99, v99, v204\n\t" \
  "v_sub_f32 v100, v100, v204\n\t" \
  "v_sub_f32 v101, v101, v204\n\t" \
  "v_sub_f32 v102, v102, v204\n\t" \
  "v_sub_f32 v103, v103, v204\n\t" \
  "v_sub_f32 v104, v104, v204\n\t" \
  "v_sub_f32 v105, v105, v204\n\t" \
  "v_sub_f32 v106, v106, v204\n\t" \
  "v_sub_f32 v107, v107, v204\n\t" \
  "v_sub_f32 v108, v108, v204\n\t" \
  "v_sub_f32 v109, v109, v204\n\t" \
  "v_sub_f32 v110, v110, v204\n\t" \
  "v_sub_f32 v111, v111, v204\n\t" \
  "v_sub_f32 v188, v188, v204\n\t" \
  "v_sub_f32 v189, v189, v204\n\t" \
  "v_sub_f32 v190, v190, v204\n\t" \
  "v_sub_f32 v191, v191, v204\n\t" \
  "v_exp_f32 v199, -v204\n\t" \
  "s_mov_b32 s99, 1\n\t" \
  "s_nop 1\n\t" \
  "s_cmp_eq_u32 vcc_lo, 0\n\t" \
  "s_cbranch_scc1 .Lsf_r03\n\t" \
  "s_cmp_eq_u32 vcc_lo, 1\n\t" \
  "s_cbranch_scc1 .Lsf_r13\n\t" \
  "s_cmp_eq_u32 vcc_lo, 2\n\t" \
  "s_cbranch_scc1 .Lsf_r23\n\t" \
  "s_cmp_eq_u32 vcc_lo, 3\n\t" \
  "s_cbranch_scc1 .Lsf_r33\n\t" \
  "s_cmp_eq_u32 vcc_lo, 4\n\t" \
  "s_cbranch_scc1 .Lsf_r43\n\t" \
  "s_cmp_eq_u32 vcc_lo, 5\n\t" \
  "s_cbranch_scc1 .Lsf_r53\n\t" \
  "s_cmp_eq_u32 vcc_lo, 6\n\t" \
  "s_cbranch_scc1 .Lsf_r63\n\t" \
  "s_cmp_eq_u32 vcc_lo, 7\n\t" \
  "s_cbranch_scc1 .Lsf_r73\n\t" \
  ".Lsf_p0:\n\t" \
  "s_mov_b32 vcc_lo, 0\n\t" \
  "s_branch .Lsf_rs\n\t" \
  ".Lsf_p1:\n\t" \
  "s_mov_b32 vcc_lo, 1\n\t" \
  "s_branch .Lsf_rs\n\t" \
  ".Lsf_p2:\n\t" \
  "s_mov_b32 vcc_lo, 2\n\t" \
  "s_branch .Lsf_rs\n\t" \
  ".Lsf_p3:\n\t" \
  "s_mov_b32 vcc_lo, 3\n\t" \
  "s_branch .Lsf_rs\n\t" \
  ".Lsf_p4:\n\t" \
  "s_mov_b32 vcc_lo, 4\n\t" \
  "s_branch .Lsf_rs\n\t" \
  ".Lsf_p5:\n\t" \
  "s_mov_b32 vcc_lo, 5\n\t" \
  "s_branch .Lsf_rs\n\t" \
  ".Lsf_p6:\n\t" \
  "s_mov_b32 vcc_lo, 6\n\t" \
  "s_branch .Lsf_rs\n\t" \
  ".Lsf_p7:\n\t" \
  "s_mov_b32 vcc_lo, 7\n\t" \
  "s_branch .Lsf_rs\n\t" \
  ".Lsf_rs:\n\t" \
  "s_nop 7\n\t" \
  "s_nop 7\n\t" \
  "v_accvgpr_read_b32 v205, a0\n\t" \
  "v_mul_f32 v205, v196, v205\n\t" \
  "v_accvgpr_write_b32 a0, v205\n\t" \
  "v_accvgpr_read_b32 v205, a1\n\t" \
  "v_mul_f32 v205, v196, v205\n\t" \
  "v_accvgpr_write_b32 a1, v205\n\t" \
  "v_accvgpr_read_b32 v205, a2\n\t" \
  "v_mul_f32 v205, v196, v205\n\t" \
  "v_accvgpr_write_b32 a2, v205\n\t" \
  "v_accvgpr_read_b32 v205, a3\n\t" \
  "v_mul_f32 v205, v196, v205\n\t" \
  "v_accvgpr_write_b32 a3, v205\n\t" \
  "v_accvgpr_read_b32 v205, a4\n\t" \
  "v_mul_f32 v205, v196, v205\n\t" \
  "v_accvgpr_write_b32 a4, v205\n\t" \
  "v_accvgpr_read_b32 v205, a5\n\t" \
  "v_mul_f32 v205, v196, v205\n\t" \
  "v_accvgpr_write_b32 a5, v205\n\t" \
  "v_accvgpr_read_b32 v205, a6\n\t" \
  "v_mul_f32 v205, v196, v205\n\t" \
  "v_accvgpr_write_b32 a6, v205\n\t" \
  "v_accvgpr_read_b32 v205, a7\n\t" \
  "v_mul_f32 v205, v196, v205\n\t" \
  "v_accvgpr_write_b32 a7, v205\n\t" \
  "v_accvgpr_read_b32 v205, a8\n\t" \
  "v_mul_f32 v205, v196, v205\n\t" \
  "v_accvgpr_write_b32 a8, v205\n\t" \
  "v_accvgpr_read_b32 v205, a9\n\t" \
  "v_mul_f32 v205, v196, v205\n\t" \
  "v_accvgpr_write_b32 a9, v205\n\t" \
  "v_accvgpr_read_b32 v205, a10\n\t" \
  "v_mul_f32 v205, v196, v205\n\t" \
  "v_accvgpr_write_b32 a10, v205\n\t" \
  "v_accvgpr_read_b32 v205, a11\n\t" \
  "v_mul_f32 v205, v196, v205\n\t" \
  "v_accvgpr_write_b32 a11, v205\n\t" \
  "v_accvgpr_read_b32 v205, a12\n\t" \
  "v_mul_f32 v205, v196, v205\n\t" \
  "v_accvgpr_write_b32 a12, v205\n\t" \
  "v_accvgpr_read_b32 v205, a13\n\t" \
  "v_mul_f32 v205, v196, v205\n\t" \
  "v_accvgpr_write_b32 a13, v205\n\t" \
  "v_accvgpr_read_b32 v205, a14\n\t" \
  "v_mul_f32 v205, v196, v205\n\t" \
  "v_accvgpr_write_b32 a14, v205\n\t" \
  "v_accvgpr_read_b32 v205, a15\n\t" \
  "v_mul_f32 v205, v196, v205\n\t" \
  "v_accvgpr_write_b32 a15, v205\n\t" \
  "v_accvgpr_read_b32 v205, a16\n\t" \
  "v_mul_f32 v205, v196, v205\n\t" \
  "v_accvgpr_write_b32 a16, v205\n\t" \
  "v_accvgpr_read_b32 v205, a17\n\t" \
  "v_mul_f32 v205, v196, v205\n\t" \
  "v_accvgpr_write_b32 a17, v205\n\t" \
  "v_accvgpr_read_b32 v205, a18\n\t" \
  "v_mul_f32 v205, v196, v205\n\t" \
  "v_accvgpr_write_b32 a18, v205\n\t" \
  "v_accvgpr_read_b32 v205, a19\n\t" \
  "v_mul_f32 v205, v196, v205\n\t" \
  "v_accvgpr_write_b32 a19, v205\n\t" \
  "v_accvgpr_read_b32 v205, a20\n\t" \
  "v_mul_f32 v205, v196, v205\n\t" \
  "v_accvgpr_write_b32 a20, v205\n\t" \
  "v_accvgpr_read_b32 v205, a21\n\t" \
  "v_mul_f32 v205, v196, v205\n\t" \
  "v_accvgpr_write_b32 a21, v205\n\t" \
  "v_accvgpr_read_b32 v205, a22\n\t" \
  "v_mul_f32 v205, v196, v205\n\t" \
  "v_accvgpr_write_b32 a22, v205\n\t" \
  "v_accvgpr_read_b32 v205, a23\n\t" \
  "v_mul_f32 v205, v196, v205\n\t" \
  "v_accvgpr_write_b32 a23, v205\n\t" \
  "v_accvgpr_read_b32 v205, a24\n\t" \
  "v_mul_f32 v205, v196, v205\n\t" \
  "v_accvgpr_write_b32 a24, v205\n\t" \
  "v_accvgpr_read_b32 v205, a25\n\t" \
  "v_mul_f32 v205, v196, v205\n\t" \
  "v_accvgpr_write_b32 a25, v205\n\t" \
  "v_accvgpr_read_b32 v205, a26\n\t" \
  "v_mul_f32 v205, v196, v205\n\t" \
  "v_accvgpr_write_b32 a26, v205\n\t" \
  "v_accvgpr_read_b32 v205, a27\n\t" \
  "v_mul_f32 v205, v196, v205\n\t" \
  "v_accvgpr_write_b32 a27, v205\n\t" \
  "v_accvgpr_read_b32 v205, a28\n\t" \
  "v_mul_f32 v205, v196, v205\n\t" \
  "v_accvgpr_write_b32 a28, v205\n\t" \
  "v_accvgpr_read_b32 v205, a29\n\t" \
  "v_mul_f32 v205, v196, v205\n\t" \
  "v_accvgpr_write_b32 a29, v205\n\t" \
  "v_accvgpr_read_b32 v205, a30\n\t" \
  "v_mul_f32 v205, v196, v205\n\t" \
  "v_accvgpr_write_b32 a30, v205\n\t" \
  "v_accvgpr_read_b32 v205, a31\n\t" \
  "v_mul_f32 v205, v196, v205\n\t" \
  "v_accvgpr_write_b32 a31, v205\n\t" \
  "v_accvgpr_read_b32 v205, a32\n\t" \
  "v_mul_f32 v205, v197, v205\n\t" \
  "v_accvgpr_write_b32 a32, v205\n\t" \
  "v_accvgpr_read_b32 v205, a33\n\t" \
  "v_mul_f32 v205, v197, v205\n\t" \
  "v_accvgpr_write_b32 a33, v205\n\t" \
  "v_accvgpr_read_b32 v205, a34\n\t" \
  "v_mul_f32 v205, v197, v205\n\t" \
  "v_accvgpr_write_b32 a34, v205\n\t" \
  "v_accvgpr_read_b32 v205, a35\n\t" \
  "v_mul_f32 v205, v197, v205\n\t" \
  "v_accvgpr_write_b32 a35, v205\n\t" \
  "v_accvgpr_read_b32 v205, a36\n\t" \
  "v_mul_f32 v205, v197, v205\n\t" \
  "v_accvgpr_write_b32 a36, v205\n\t" \
  "v_accvgpr_read_b32 v205, a37\n\t" \
  "v_mul_f32 v205, v197, v205\n\t" \
  "v_accvgpr_write_b32 a37, v205\n\t" \
  "v_accvgpr_read_b32 v205, a38\n\t" \
  "v_mul_f32 v205, v197, v205\n\t" \
  "v_accvgpr_write_b32 a38, v205\n\t" \
  "v_accvgpr_read_b32 v205, a39\n\t" \
  "v_mul_f32 v205, v197, v205\n\t" \
  "v_accvgpr_write_b32 a39, v205\n\t" \
  "v_accvgpr_read_b32 v205, a40\n\t" \
  "v_mul_f32 v205, v197, v205\n\t" \
  "v_accvgpr_write_b32 a40, v205\n\t" \
  "v_accvgpr_read_b32 v205, a41\n\t" \
  "v_mul_f32 v205, v197, v205\n\t" \
  "v_accvgpr_write_b32 a41, v205\n\t" \
  "v_accvgpr_read_b32 v205, a42\n\t" \
  "v_mul_f32 v205, v197, v205\n\t" \
  "v_accvgpr_write_b32 a42, v205\n\t" \
  "v_accvgpr_read_b32 v205, a43\n\t" \
  "v_mul_f32 v205, v197, v205\n\t" \
  "v_accvgpr_write_b32 a43, v205\n\t" \
  "v_accvgpr_read_b32 v205, a44\n\t" \
  "v_mul_f32 v205, v197, v205\n\t" \
  "v_accvgpr_write_b32 a44, v205\n\t" \
  "v_accvgpr_read_b32 v205, a45\n\t" \
  "v_mul_f32 v205, v197, v205\n\t" \
  "v_accvgpr_write_b32 a45, v205\n\t" \
  "v_accvgpr_read_b32 v205, a46\n\t" \
  "v_mul_f32 v205, v197, v205\n\t" \
  "v_accvgpr_write_b32 a46, v205\n\t" \
  "v_accvgpr_read_b32 v205, a47\n\t" \
  "v_mul_f32 v205, v197, v205\n\t" \
  "v_accvgpr_write_b32 a47, v205\n\t" \
  "v_accvgpr_read_b32 v205, a48\n\t" \
  "v_mul_f32 v205, v197, v205\n\t" \
  "v_accvgpr_write_b32 a48, v205\n\t" \
  "v_accvgpr_read_b32 v205, a49\n\t" \
  "v_mul_f32 v205, v197, v205\n\t" \
  "v_accvgpr_write_b32 a49, v205\n\t" \
  "v_accvgpr_read_b32 v205, a50\n\t" \
  "v_mul_f32 v205, v197, v205\n\t" \
  "v_accvgpr_write_b32 a50, v205\n\t" \
  "v_accvgpr_read_b32 v205, a51\n\t" \
  "v_mul_f32 v205, v197, v205\n\t" \
  "v_accvgpr_write_b32 a51, v205\n\t" \
  "v_accvgpr_read_b32 v205, a52\n\t" \
  "v_mul_f32 v205, v197, v205\n\t" \
  "v_accvgpr_write_b32 a52, v205\n\t" \
  "v_accvgpr_read_b32 v205, a53\n\t" \
  "v_mul_f32 v205, v197, v205\n\t" \
  "v_accvgpr_write_b32 a53, v205\n\t" \
  "v_accvgpr_read_b32 v205, a54\n\t" \
  "v_mul_f32 v205, v197, v205\n\t" \
  "v_accvgpr_write_b32 a54, v205\n\t" \
  "v_accvgpr_read_b32 v205, a55\n\t" \
  "v_mul_f32 v205, v197, v205\n\t" \
  "v_accvgpr_write_b32 a55, v205\n\t" \
  "v_accvgpr_read_b32 v205, a56\n\t" \
  "v_mul_f32 v205, v197, v205\n\t" \
  "v_accvgpr_write_b32 a56, v205\n\t" \
  "v_accvgpr_read_b32 v205, a57\n\t" \
  "v_mul_f32 v205, v197, v205\n\t" \
  "v_accvgpr_write_b32 a57, v205\n\t" \
  "v_accvgpr_read_b32 v205, a58\n\t" \
  "v_mul_f32 v205, v197, v205\n\t" \
  "v_accvgpr_write_b32 a58, v205\n\t" \
  "v_accvgpr_read_b32 v205, a59\n\t" \
  "v_mul_f32 v205, v197, v205\n\t" \
  "v_accvgpr_write_b32 a59, v205\n\t" \
  "v_accvgpr_read_b32 v205, a60\n\t" \
  "v_mul_f32 v205, v197, v205\n\t" \
  "v_accvgpr_write_b32 a60, v205\n\t" \
  "v_accvgpr_read_b32 v205, a61\n\t" \
  "v_mul_f32 v205, v197, v205\n\t" \
  "v_accvgpr_write_b32 a61, v205\n\t" \
  "v_accvgpr_read_b32 v205, a62\n\t" \
  "v_mul_f32 v205, v197, v205\n\t" \
  "v_accvgpr_write_b32 a62, v205\n\t" \
  "v_accvgpr_read_b32 v205, a63\n\t" \
  "v_mul_f32 v205, v197, v205\n\t" \
  "v_accvgpr_write_b32 a63, v205\n\t" \
  "v_accvgpr_read_b32 v205, a64\n\t" \
  "v_mul_f32 v205, v198, v205\n\t" \
  "v_accvgpr_write_b32 a64, v205\n\t" \
  "v_accvgpr_read_b32 v205, a65\n\t" \
  "v_mul_f32 v205, v198, v205\n\t" \
  "v_accvgpr_write_b32 a65, v205\n\t" \
  "v_accvgpr_read_b32 v205, a66\n\t" \
  "v_mul_f32 v205, v198, v205\n\t" \
  "v_accvgpr_write_b32 a66, v205\n\t" \
  "v_accvgpr_read_b32 v205, a67\n\t" \
  "v_mul_f32 v205, v198, v205\n\t" \
  "v_accvgpr_write_b32 a67, v205\n\t" \
  "v_accvgpr_read_b32 v205, a68\n\t" \
  "v_mul_f32 v205, v198, v205\n\t" \
  "v_accvgpr_write_b32 a68, v205\n\t" \
  "v_accvgpr_read_b32 v205, a69\n\t" \
  "v_mul_f32 v205, v198, v205\n\t" \
  "v_accvgpr_write_b32 a69, v205\n\t" \
  "v_accvgpr_read_b32 v205, a70\n\t" \
  "v_mul_f32 v205, v198, v205\n\t" \
  "v_accvgpr_write_b32 a70, v205\n\t" \
  "v_accvgpr_read_b32 v205, a71\n\t" \
  "v_mul_f32 v205, v198, v205\n\t" \
  "v_accvgpr_write_b32 a71, v205\n\t" \
  "v_accvgpr_read_b32 v205, a72\n\t" \
  "v_mul_f32 v205, v198, v205\n\t" \
  "v_accvgpr_write_b32 a72, v205\n\t" \
  "v_accvgpr_read_b32 v205, a73\n\t" \
  "v_mul_f32 v205, v198, v205\n\t" \
  "v_accvgpr_write_b32 a73, v205\n\t" \
  "v_accvgpr_read_b32 v205, a74\n\t" \
  "v_mul_f32 v205, v198, v205\n\t" \
  "v_accvgpr_write_b32 a74, v205\n\t" \
  "v_accvgpr_read_b32 v205, a75\n\t" \
  "v_mul_f32 v205, v198, v205\n\t" \
  "v_accvgpr_write_b32 a75, v205\n\t" \
  "v_accvgpr_read_b32 v205, a76\n\t" \
  "v_mul_f32 v205, v198, v205\n\t" \
  "v_accvgpr_write_b32 a76, v205\n\t" \
  "v_accvgpr_read_b32 v205, a77\n\t" \
  "v_mul_f32 v205, v198, v205\n\t" \
  "v_accvgpr_write_b32 a77, v205\n\t" \
  "v_accvgpr_read_b32 v205, a78\n\t" \
  "v_mul_f32 v205, v198, v205\n\t" \
  "v_accvgpr_write_b32 a78, v205\n\t" \
  "v_accvgpr_read_b32 v205, a79\n\t" \
  "v_mul_f32 v205, v198, v205\n\t" \
  "v_accvgpr_write_b32 a79, v205\n\t" \
  "v_accvgpr_read_b32 v205, a80\n\t" \
  "v_mul_f32 v205, v198, v205\n\t" \
  "v_accvgpr_write_b32 a80, v205\n\t" \
  "v_accvgpr_read_b32 v205, a81\n\t" \
  "v_mul_f32 v205, v198, v205\n\t" \
  "v_accvgpr_write_b32 a81, v205\n\t" \
  "v_accvgpr_read_b32 v205, a82\n\t" \
  "v_mul_f32 v205, v198, v205\n\t" \
  "v_accvgpr_write_b32 a82, v205\n\t" \
  "v_accvgpr_read_b32 v205, a83\n\t" \
  "v_mul_f32 v205, v198, v205\n\t" \
  "v_accvgpr_write_b32 a83, v205\n\t" \
  "v_accvgpr_read_b32 v205, a84\n\t" \
  "v_mul_f32 v205, v198, v205\n\t" \
  "v_accvgpr_write_b32 a84, v205\n\t" \
  "v_accvgpr_read_b32 v205, a85\n\t" \
  "v_mul_f32 v205, v198, v205\n\t" \
  "v_accvgpr_write_b32 a85, v205\n\t" \
  "v_accvgpr_read_b32 v205, a86\n\t" \
  "v_mul_f32 v205, v198, v205\n\t" \
  "v_accvgpr_write_b32 a86, v205\n\t" \
  "v_accvgpr_read_b32 v205, a87\n\t" \
  "v_mul_f32 v205, v198, v205\n\t" \
  "v_accvgpr_write_b32 a87, v205\n\t" \
  "v_accvgpr_read_b32 v205, a88\n\t" \
  "v_mul_f32 v205, v198, v205\n\t" \
  "v_accvgpr_write_b32 a88, v205\n\t" \
  "v_accvgpr_read_b32 v205, a89\n\t" \
  "v_mul_f32 v205, v198, v205\n\t" \
  "v_accvgpr_write_b32 a89, v205\n\t" \
  "v_accvgpr_read_b32 v205, a90\n\t" \
  "v_mul_f32 v205, v198, v205\n\t" \
  "v_accvgpr_write_b32 a90, v205\n\t" \
  "v_accvgpr_read_b32 v205, a91\n\t" \
  "v_mul_f32 v205, v198, v205\n\t" \
  "v_accvgpr_write_b32 a91, v205\n\t" \
  "v_accvgpr_read_b32 v205, a92\n\t" \
  "v_mul_f32 v205, v198, v205\n\t" \
  "v_accvgpr_write_b32 a92, v205\n\t" \
  "v_accvgpr_read_b32 v205, a93\n\t" \
  "v_mul_f32 v205, v198, v205\n\t" \
  "v_accvgpr_write_b32 a93, v205\n\t" \
  "v_accvgpr_read_b32 v205, a94\n\t" \
  "v_mul_f32 v205, v198, v205\n\t" \
  "v_accvgpr_write_b32 a94, v205\n\t" \
  "v_accvgpr_read_b32 v205, a95\n\t" \
  "v_mul_f32 v205, v198, v205\n\t" \
  "v_accvgpr_write_b32 a95, v205\n\t" \
  "v_accvgpr_read_b32 v205, a96\n\t" \
  "v_mul_f32 v205, v199, v205\n\t" \
  "v_accvgpr_write_b32 a96, v205\n\t" \
  "v_accvgpr_read_b32 v205, a97\n\t" \
  "v_mul_f32 v205, v199, v205\n\t" \
  "v_accvgpr_write_b32 a97, v205\n\t" \
  "v_accvgpr_read_b32 v205, a98\n\t" \
  "v_mul_f32 v205, v199, v205\n\t" \
  "v_accvgpr_write_b32 a98, v205\n\t" \
  "v_accvgpr_read_b32 v205, a99\n\t" \
  "v_mul_f32 v205, v199, v205\n\t" \
  "v_accvgpr_write_b32 a99, v205\n\t" \
  "v_accvgpr_read_b32 v205, a100\n\t" \
  "v_mul_f32 v205, v199, v205\n\t" \
  "v_accvgpr_write_b32 a100, v205\n\t" \
  "v_accvgpr_read_b32 v205, a101\n\t" \
  "v_mul_f32 v205, v199, v205\n\t" \
  "v_accvgpr_write_b32 a101, v205\n\t" \
  "v_accvgpr_read_b32 v205, a102\n\t" \
  "v_mul_f32 v205, v199, v205\n\t" \
  "v_accvgpr_write_b32 a102, v205\n\t" \
  "v_accvgpr_read_b32 v205, a103\n\t" \
  "v_mul_f32 v205, v199, v205\n\t" \
  "v_accvgpr_write_b32 a103, v205\n\t" \
  "v_accvgpr_read_b32 v205, a104\n\t" \
  "v_mul_f32 v205, v199, v205\n\t" \
  "v_accvgpr_write_b32 a104, v205\n\t" \
  "v_accvgpr_read_b32 v205, a105\n\t" \
  "v_mul_f32 v205, v199, v205\n\t" \
  "v_accvgpr_write_b32 a105, v205\n\t" \
  "v_accvgpr_read_b32 v205, a106\n\t" \
  "v_mul_f32 v205, v199, v205\n\t" \
  "v_accvgpr_write_b32 a106, v205\n\t" \
  "v_accvgpr_read_b32 v205, a107\n\t" \
  "v_mul_f32 v205, v199, v205\n\t" \
  "v_accvgpr_write_b32 a107, v205\n\t" \
  "v_accvgpr_read_b32 v205, a108\n\t" \
  "v_mul_f32 v205, v199, v205\n\t" \
  "v_accvgpr_write_b32 a108, v205\n\t" \
  "v_accvgpr_read_b32 v205, a109\n\t" \
  "v_mul_f32 v205, v199, v205\n\t" \
  "v_accvgpr_write_b32 a109, v205\n\t" \
  "v_accvgpr_read_b32 v205, a110\n\t" \
  "v_mul_f32 v205, v199, v205\n\t" \
  "v_accvgpr_write_b32 a110, v205\n\t" \
  "v_accvgpr_read_b32 v205, a111\n\t" \
  "v_mul_f32 v205, v199, v205\n\t" \
  "v_accvgpr_write_b32 a111, v205\n\t" \
  "v_accvgpr_read_b32 v205, a112\n\t" \
  "v_mul_f32 v205, v199, v205\n\t" \
  "v_accvgpr_write_b32 a112, v205\n\t" \
  "v_accvgpr_read_b32 v205, a113\n\t" \
  "v_mul_f32 v205, v199, v205\n\t" \
  "v_accvgpr_write_b32 a113, v205\n\t" \
  "v_accvgpr_read_b32 v205, a114\n\t" \
  "v_mul_f32 v205, v199, v205\n\t" \
  "v_accvgpr_write_b32 a114, v205\n\t" \
  "v_accvgpr_read_b32 v205, a115\n\t" \
  "v_mul_f32 v205, v199, v205\n\t" \
  "v_accvgpr_write_b32 a115, v205\n\t" \
  "v_accvgpr_read_b32 v205, a116\n\t" \
  "v_mul_f32 v205, v199, v205\n\t" \
  "v_accvgpr_write_b32 a116, v205\n\t" \
  "v_accvgpr_read_b32 v205, a117\n\t" \
  "v_mul_f32 v205, v199, v205\n\t" \
  "v_accvgpr_write_b32 a117, v205\n\t" \
  "v_accvgpr_read_b32 v205, a118\n\t" \
  "v_mul_f32 v205, v199, v205\n\t" \
  "v_accvgpr_write_b32 a118, v205\n\t" \
  "v_accvgpr_read_b32 v205, a119\n\t" \
  "v_mul_f32 v205, v199, v205\n\t" \
  "v_accvgpr_write_b32 a119, v205\n\t" \
  "v_accvgpr_read_b32 v205, a120\n\t" \
  "v_mul_f32 v205, v199, v205\n\t" \
  "v_accvgpr_write_b32 a120, v205\n\t" \
  "v_accvgpr_read_b32 v205, a121\n\t" \
  "v_mul_f32 v205, v199, v205\n\t" \
  "v_accvgpr_write_b32 a121, v205\n\t" \
  "v_accvgpr_read_b32 v205, a122\n\t" \
  "v_mul_f32 v205, v199, v205\n\t" \
  "v_accvgpr_write_b32 a122, v205\n\t" \
  "v_accvgpr_read_b32 v205, a123\n\t" \
  "v_mul_f32 v205, v199, v205\n\t" \
  "v_accvgpr_write_b32 a123, v205\n\t" \
  "v_accvgpr_read_b32 v205, a124\n\t" \
  "v_mul_f32 v205, v199, v205\n\t" \
  "v_accvgpr_write_b32 a124, v205\n\t" \
  "v_accvgpr_read_b32 v205, a125\n\t" \
  "v_mul_f32 v205, v199, v205\n\t" \
  "v_accvgpr_write_b32 a125, v205\n\t" \
  "v_accvgpr_read_b32 v205, a126\n\t" \
  "v_mul_f32 v205, v199, v205\n\t" \
  "v_accvgpr_write_b32 a126, v205\n\t" \
  "v_accvgpr_read_b32 v205, a127\n\t" \
  "v_mul_f32 v205, v199, v205\n\t" \
  "v_accvgpr_write_b32 a127, v205\n\t" \
  "v_mul_f32 v160, v196, v160\n\t" \
  "v_mul_f32 v161, v196, v161\n\t" \
  "v_mov_b32 v196, 1.0\n\t" \
  "v_mul_f32 v164, v197, v164\n\t" \
  "v_mul_f32 v165, v197, v165\n\t" \
  "v_mov_b32 v197, 1.0\n\t" \
  "v_mul_f32 v168, v198, v168\n\t" \
  "v_mul_f32 v169, v198, v169\n\t" \
  "v_mov_b32 v198, 1.0\n\t" \
  "v_mul_f32 v172, v199, v172\n\t" \
  "v_mul_f32 v173, v199, v173\n\t" \
  "v_mov_b32 v199, 1.0\n\t" \
  "s_mov_b32 s99, 0\n\t" \
  "s_nop 1\n\t" \
  "s_cmp_eq_u32 vcc_lo, 0\n\t" \
  "s_cbranch_scc1 .Lsf_q0\n\t" \
  "s_cmp_eq_u32 vcc_lo, 1\n\t" \
  "s_cbranch_scc1 .Lsf_q1\n\t" \
  "s_cmp_eq_u32 vcc_lo, 2\n\t" \
  "s_cbranch_scc1 .Lsf_q2\n\t" \
  "s_cmp_eq_u32 vcc_lo, 3\n\t" \
  "s_cbranch_scc1 .Lsf_q3\n\t" \
  "s_cmp_eq_u32 vcc_lo, 4\n\t" \
  "s_cbranch_scc1 .Lsf_q4\n\t" \
  "s_cmp_eq_u32 vcc_lo, 5\n\t" \
  "s_cbranch_scc1 .Lsf_q5\n\t" \
  "s_cmp_eq_u32 vcc_lo, 6\n\t" \
  "s_cbranch_scc1 .Lsf_q6\n\t" \
  "s_cmp_eq_u32 vcc_lo, 7\n\t" \
  "s_cbranch_scc1 .Lsf_q7\n\t" \
  "s_endpgm\n\t" \
  ".Lsf_v:\n\t" \
  "s_mov_b32 s98, 0\n\t" \
  ".Lsf_v1:\n\t" \
  "s_sleep 4\n\t" \
  "buffer_load_dword v209, v251, s[16:19], 0 offen sc1\n\t" \
  "s_waitcnt vmcnt(0)\n\t" \
  "v_and_b32 v250, 0xbfffbfff, v209\n\t" \
  "v_cmp_ne_u32_e64 s[84:85], s36, v250\n\t" \
  "s_cmp_lg_u64 s[84:85], 0\n\t" \
  "s_cbranch_scc0 .Lsf_w\n\t" \
  "s_add_u32 s98, s98, 1\n\t" \
  "s_cmp_lt_u32 s98, " FQ_CAP "\n\t" \
  "s_cbranch_scc1 .Lsf_v1\n\t" \
  "v_mov_b32 v250, s36\n\t" \
  "buffer_store_dword v250, off, s[16:19], 0 offset:256 sc1\n\t" \
  "s_branch .Lsf_w\n\t" \
  ".subsection 0\n\t" \
  ".Lop_end:\n\t" \
  ""
static_assert(W_MODB == 2228224 && W_VOTE == 8192);
static_assert(W_EPOCH == 2490368 && W_GKEY == 65536 && W_CANDH == 262144 && W_STAT == 524288 && W_YG == 1048576 && W_CANDX == 1966080);
#define MAIN_CLOB "v1", "v2", "v3", "v4", "v5", "v6", "v7", "v8", "v9", "v10", "v11", "v12", "v13", "v14", "v15", "v16", "v17", "v18", "v19", "v20", "v21", "v22", "v23", "v24", "v25", "v26", "v27", "v28", "v29", "v30", "v31", "v32", "v33", "v34", "v35", "v36", "v37", "v38", "v39", "v40", "v41", "v42", "v43", "v44", "v45", "v46", "v47", "v48", "v49", "v50", "v51", "v52", "v53", "v54", "v55", "v56", "v57", "v58", "v59", "v60", "v61", "v62", "v63", "v64", "v65", "v66", "v67", "v68", "v69", "v70", "v71", "v72", "v73", "v74", "v75", "v76", "v77", "v78", "v79", "v80", "v81", "v82", "v83", "v84", "v85", "v86", "v87", "v88", "v89", "v90", "v91", "v92", "v93", "v94", "v95", "v96", "v97", "v98", "v99", "v100", "v101", "v102", "v103", "v104", "v105", "v106", "v107", "v108", "v109", "v110", "v111", "v112", "v113", "v114", "v115", "v116", "v117", "v118", "v119", "v120", "v121", "v122", "v123", "v124", "v125", "v126", "v127", "v128", "v129", "v130", "v131", "v132", "v133", "v134", "v135", "v136", "v137", "v138", "v139", "v140", "v141", "v142", "v143", "v144", "v145", "v146", "v147", "v148", "v149", "v150", "v151", "v152", "v153", "v154", "v155", "v156", "v157", "v158", "v159", "v160", "v161", "v162", "v163", "v164", "v165", "v166", "v167", "v168", "v169", "v170", "v171", "v172", "v173", "v174", "v175", "v176", "v177", "v178", "v179", "v180", "v181", "v182", "v183", "v184", "v185", "v186", "v187", "v188", "v189", "v190", "v191", "v192", "v193", "v194", "v195", "v196", "v197", "v198", "v199", "v200", "v201", "v202", "v203", "v204", "v205", "v206", "v207", "v208", "v209", "v210", "v211", "v212", "v213", "v214", "v215", "v216", "v217", "v218", "v219", "v220", "v221", "v222", "v223", "v224", "v225", "v226", "v227", "v228", "v229", "v230", "v231", "v232", "v233", "v234", "v235", "v236", "v237", "v238", "v239", "v240", "v241", "v242", "v243", "v244", "v245", "v246", "v247", "v248", "v249", "v250", "v251", "v252", "v253", "v254", "s4", "s5", "s6", "s7", "s8", "s9", "s10", "s11", "s12", "s13", "s14", "s15", "s16", "s17", "s18", "s19", "s20", "s21", "s22", "s23", "s24", "s25", "s26", "s27", "s28", "s29", "s30", "s31", "s32", "s33", "s34", "s35", "s36", "s37", "s38", "s39", "s40", "s41", "s42", "s43", "s44", "s45", "s46", "s47", "s48", "s49", "s50", "s51", "s52", "s53", "s54", "s55", "s57", "s58", "s59", "s60", "s61", "s62", "s63", "s64", "s65", "s66", "s67", "s68", "s69", "s70", "s71", "s72", "s73", "s74", "s75", "s76", "s77", "s78", "s79", "s80", "s81", "s82", "s83", "s84", "s85", "s86", "s87", "s88", "s89", "s90", "s91", "s92", "s93", "s94", "s95", "s96", "s97", "s98", "s99", "s100", "s101", "vcc", "scc", "memory"
#define MAIN_STV
__global__ __launch_bounds__(256, 1) __attribute__((aligned(4096))) void k_fused(Args a) {
  struct { unsigned bid, wid, t0; } k;
  argp_t ap = (argp_t)__builtin_amdgcn_kernarg_segment_ptr();
  unsigned tid = threadIdx.x; k.bid = blockIdx.x;
  asm volatile(FQ_ASM AT_ASM : MAIN_STV "+{v0}"(tid), "+{s[0:1]}"(ap), "+{s2}"(k.bid), "=&{s3}"(k.wid), "=&{s56}"(k.t0) :: MAIN_CLOB, ACLOB_LO, ACLOB_HI);
  asm volatile(HOP_DEFS);
}

extern "C" void kernel_launch(void* const* d_in, const int* in_sizes, int n_in, void* d_out, int out_size, void* d_ws, size_t ws_size,
                              hipStream_t stream) {
  auto D = [](const void* p, unsigned bytes) { Desc d; d.p = p; d.bytes = bytes; d.w3 = SRD3; return d; };
  const size_t win = (size_t)(SKV - WIN) * DH * 2;
  Args a;
  a.x = D(d_in[0], S * DM * 2); a.w_mod = D(d_in[11], DM * 6 * DM * 2); a.c = D(d_in[10], DM * 2); a.ws = D(d_ws, W_END);
  a.wq = D(d_in[3], DM * QD * 2); a.wo = D(d_in[4], QD * DM * 2); a.wd = D(d_in[9], 0); a.w_r = D(d_in[6], DM * 2);
  a.kc = D((const char*)d_in[1] + win, WIN * DH * 2); a.vc = D((const char*)d_in[2] + win, WIN * DH * 2); a.wg = D(d_in[7], DM * FF * 2); a.wu = D(d_in[8], DM * FF * 2);
  a.out = D(d_out, S * DM * 2);
  (void)hipFuncSetAttribute((const void*)k_fused, hipFuncAttributeMaxDynamicSharedMemorySize, 163840);
  k_fused<<<256, 256, 163840, stream>>>(a);
}
```
